```python
import torch

def _softmax(x, axis=-1):
    return torch.softmax(x, dim=axis)
def _gelu(x):
    return torch.nn.functional.gelu(x, approximate="tanh")
def _silu(x):
    return torch.nn.functional.silu(x)
def _sigmoid(x):
    return torch.sigmoid(x)
def _layernorm(x, axis=-1, eps=1e-5):
    m = x.mean(dim=axis, keepdim=True)
    v = x.var(dim=axis, keepdim=True, unbiased=False)
    return (x - m) / torch.sqrt(v + eps)
def _scatter_add(tgt, idx, src, axis=0):
    return tgt.clone().index_add_(axis, idx, src)
def _segment_sum(src, seg, n):
    out = torch.zeros((n,) + src.shape[1:], dtype=src.dtype)
    return out.index_add_(0, seg, src)
def _pad(x, pw):
    flat = tuple(v for pair in reversed(pw) for v in pair)
    return torch.nn.functional.pad(x, flat)

kernel_name = 'interleave_layers__sliding_local_dilated__dense_activation_gelu__prompt'

def setup_inputs():

    _w = (lambda a: (lambda t: t.to(torch.bfloat16) if t.is_floating_point() else t)(torch.as_tensor(a)))
    def _A_setup():
        B, S, D = 1, 2048, 1024
        return {
            "x": _w((lambda *s: torch.randn(*s, dtype=torch.bfloat16) * 0.05)(B, S, D)),
            "wqkv": _w((lambda *s: torch.randn(*s, dtype=torch.bfloat16) * 0.05)(D, 3 * D)),
            "wo": _w((lambda *s: torch.randn(*s, dtype=torch.bfloat16) * 0.05)(D, D)),
        }
    _A_in = _A_setup()

    def _B_setup():
        B, S, D, F = 1, 2048, 1024, 3584
        return {
            "x": _w((lambda *s: torch.randn(*s, dtype=torch.bfloat16) * 0.05)(B, S, D)),
            "w1": _w((lambda *s: torch.randn(*s, dtype=torch.bfloat16) * 1.6)(D, F) * (D ** -0.5)),
            "b1": _w((lambda *s: torch.randn(*s, dtype=torch.bfloat16) * 1.6)(F) * 0.01),
            "w2": _w((lambda *s: torch.randn(*s, dtype=torch.bfloat16) * 1.6)(F, D) * (F ** -0.5)),
            "b2": _w((lambda *s: torch.randn(*s, dtype=torch.bfloat16) * 1.6)(D) * 0.01),
        }
    _B_in = _B_setup()

    return {
        "A_x": _A_in["x"],
        "A_wqkv": _A_in["wqkv"],
        "A_wo": _A_in["wo"],
        "B_x": _B_in["x"],
        "B_w1": _B_in["w1"],
        "B_b1": _B_in["b1"],
        "B_w2": _B_in["w2"],
        "B_b2": _B_in["b2"],
    }

def reference(A_x, A_wqkv, A_wo, B_x, B_w1, B_b1, B_w2, B_b2):
    def _A_ref(x, wqkv, wo):
        B, S, D = x.shape
        H = 32; Dh = D // H
        W, dil = 8, 2
        qkv = (lambda x, *d: x.permute(*d))((x @ wqkv).reshape(B, S, 3, H, Dh), 2, 0, 3, 1, 4)
        q, k, v = qkv[0], qkv[1], qkv[2]
        scale = Dh ** -0.5
        pos = torch.arange(S)
        off = torch.arange(W) * dil
        idx = pos[:, None] - off[None, :]
        mask = idx >= 0
        idx_c = torch.where(mask, idx, 0).reshape(-1)
        kg = k[:, :, idx_c, :].reshape(B, H, S, W, Dh)
        vg = v[:, :, idx_c, :].reshape(B, H, S, W, Dh)
        logits = torch.einsum("bhsd,bhswd->bhsw", q, kg) * scale
        logits = torch.where(mask[None, None, :, :], logits, -1e9)
        attn = _softmax(logits, axis=-1)
        o = torch.einsum("bhsw,bhswd->bhsd", attn, vg)
        return (lambda x, *d: x.permute(*d))(o, 0, 2, 1, 3).reshape(B, S, D) @ wo

    def _B_ref(x, w1, b1, w2, b2):
        h = torch.einsum("bsd,df->bsf", x, w1) + b1
        h = _gelu(h)
        return torch.einsum("bsf,fd->bsd", h, w2) + b2

    x = A_x
    x = x + _A_ref(_layernorm(x, axis=-1), A_wqkv, A_wo)
    x = x + _B_ref(_layernorm(x, axis=-1), B_w1, B_b1, B_w2, B_b2)
    x = x + _A_ref(_layernorm(x, axis=-1), A_wqkv, A_wo)
    x = x + _B_ref(_layernorm(x, axis=-1), B_w1, B_b1, B_w2, B_b2)
    return x
```

```cpp
#include <hip/hip_runtime.h>
__global__ __launch_bounds__(256, 1) void layers(const void* x, const void* wqkv, const void* wo, const void* w1, const void* b1, const void* w2, const void* b2, void* out, void* ws) {
#if defined(__HIP_DEVICE_COMPILE__)
  int tid = threadIdx.x, bid = blockIdx.x;
  asm volatile(
".macro M d,w,a\nv_mfma_f32_16x16x32_bf16 v[\\d:\\d+3], v[\\w:\\w+3], a[\\a:\\a+3], v[\\d:\\d+3]\n.endm\n.macro N d,a,w\nv_mfma_f32_16x16x32_bf16 v[\\d:\\d+3], a[\\a:\\a+3], v[\\w:\\w+3], v[\\d:\\d+3]\n.endm\n"
".macro R d,b,o\nds_read_b64_tr_b16 v[\\d:\\d+1], v\\b offset:\\o\n.endm\n.macro G n\ns_waitcnt lgkmcnt(\\n)\n.endm\n"
"v_lshrrev_b32 v1, 6, v0\nv_and_b32 v0, 63, v0\nv_readfirstlane_b32 s40, v1\nv_lshlrev_b32 v252, 4, v0\ns_mov_b32 s6, 6291456\ns_mov_b32 s7, 0x20000\n"
"s_mov_b32 s10, 2097152\ns_mov_b32 s11, 0x20000\ns_mov_b32 s14, 7340032\ns_mov_b32 s15, 0x20000\ns_mov_b32 s18, 7340032\ns_mov_b32 s19, 0x20000\n"
"s_mov_b32 s22, 89128960\ns_mov_b32 s23, 0x20000\ns_mov_b32 s26, 4194304\ns_mov_b32 s27, 0x20000\ns_mov_b32 s30, 4194304\ns_mov_b32 s31, 0x20000\n"
"s_mov_b32 s34, 7168\ns_mov_b32 s35, 0x20000\ns_mov_b32 s38, 2048\ns_mov_b32 s39, 0x20000\ns_bfe_u32 s41, s45, 0x30003\ns_and_b32 s60, s45, 1\n"
"s_lshl3_add_u32 s41, s60, s41\ns_bfe_u32 s43, s45, 0x20001\ns_lshr_b32 s44, s45, 6\ns_lshl2_add_u32 s42, s43, s44\ns_lshl2_add_u32 s75, s44, s43\ns_and_b32 s90, s40, 1\n"
"s_lshl2_add_u32 s60, s41, s40\ns_lshl_b32 s49, s60, 16\ns_lshl_b32 s50, s40, 11\ns_add_u32 s50, s50, 32768\ns_lshl_b32 s52, s42, 7\ns_lshl_b32 s51, s40, 13\n"
"s_lshl_b32 s60, s90, 10\ns_add_u32 s70, s49, s60\ns_xor_b32 s71, s70, 0x10000\ns_mul_i32 s101, s41, 196608\ns_add_u32 s101, s101, s52\ns_lshl_b32 s53, s75, 7\n"
"s_getpc_b64 s[98:99]\nLqPC:\nLqq0:\nv_lshrrev_b32 v96, 7, v252\nv_lshlrev_b32 v96, 11, v96\nv_lshrrev_b32 v97, 8, v252\n"
"v_lshlrev_b32 v97, 4, v97\nv_and_b32 v98, 0x70, v252\nv_xor_b32 v98, v98, v97\nv_add_u32 v226, v96, v98\nv_xor_b32 v227, 64, v226\ns_add_u32 m0, s51, 0\n"
"s_add_u32 s60, s70, 0\nbuffer_load_dwordx4 v226, s[24:27], s60 offen lds\ns_add_u32 m0, s51, 1024\ns_add_u32 s60, s70, 16384\nbuffer_load_dwordx4 v227, s[24:27], s60 offen lds\ns_add_u32 m0, s51, 2048\n"
"s_add_u32 s60, s70, 32768\nbuffer_load_dwordx4 v226, s[24:27], s60 offen lds\ns_add_u32 m0, s51, 3072\ns_add_u32 s60, s70, 49152\nbuffer_load_dwordx4 v227, s[24:27], s60 offen lds\ns_add_u32 m0, s51, 4096\n"
"s_add_u32 s60, s71, 0\nbuffer_load_dwordx4 v226, s[24:27], s60 offen lds\ns_add_u32 m0, s51, 5120\ns_add_u32 s60, s71, 16384\nbuffer_load_dwordx4 v227, s[24:27], s60 offen lds\ns_add_u32 m0, s51, 6144\n"
"s_add_u32 s60, s71, 32768\nbuffer_load_dwordx4 v226, s[24:27], s60 offen lds\ns_add_u32 m0, s51, 7168\ns_add_u32 s60, s71, 49152\nbuffer_load_dwordx4 v227, s[24:27], s60 offen lds\nbuffer_load_dwordx2 v[230:231], off, s[20:23], 0 sc1\n"
"s_lshr_b32 s60, s45, 3\ns_lshl2_add_u32 s60, s60, s40\ns_lshl_b32 s60, s60, 8\nv_lshl_add_u32 v224, v252, 3, s60\nv_add_u32 v224, LqPC-LqPC, v224\ns_mov_b64 exec, 3\n"
"global_load_dword v224, v224, s[98:99]\ns_mov_b64 exec, -1\nv_bfe_u32 v96, v252, 5, 2\nv_bfe_u32 v97, v252, 8, 2\nv_xor_b32 v96, v96, v97\ns_and_b32 s60, s40, 1\n"
"s_lshl_b32 s60, s60, 4\ns_and_b32 s61, s40, 2\ns_lshl1_add_u32 s60, s61, s60\nv_bfe_u32 v97, v252, 7, 2\nv_lshrrev_b32 v98, 9, v252\nv_lshl_add_u32 v97, v98, 3, v97\n"
"v_add_u32 v97, s60, v97\nv_mul_u32_u24 v97, 6144, v97\nv_lshl_add_u32 v97, v96, 5, v97\nv_and_b32 v96, 16, v252\nv_add_u32 v220, v96, v97\nv_add_u32 v221, 3144704, v220\n"
"s_add_u32 m0, s50, 0\ns_add_u32 s63, s52, 2048\nbuffer_load_dwordx4 v220, s[4:7], s63 offen lds\nbuffer_load_dwordx4 v221, s[4:7], s63 offen offset:1024 lds\ns_add_u32 m0, s50, 8192\ns_add_u32 s63, s52, 198656\n"
"buffer_load_dwordx4 v220, s[4:7], s63 offen lds\nbuffer_load_dwordx4 v221, s[4:7], s63 offen offset:1024 lds\ns_lshl2_add_u32 s60, s41, s40\ns_mul_i32 s79, s60, 2176\ns_and_b32 s61, s60, -2\ns_mul_i32 s80, s61, 2176\n"
"s_lshl2_add_u32 s61, s61, s43\ns_mul_i32 s82, s61, 640\ns_mul_i32 s61, s43, 28672\ns_mul_i32 s73, s60, 229376\ns_add_u32 s73, s73, s61\ns_mul_i32 s62, s90, 14336\n"
"s_add_u32 s68, s73, s62\ns_add_u32 s61, s61, s62\ns_xor_b32 s62, s60, 1\ns_mul_i32 s69, s62, 229376\ns_add_u32 s69, s69, s61\ns_mul_i32 s61, s44, 7168\n"
"s_add_u32 s73, s73, s61\ns_lshl2_add_u32 s61, s60, s43\ns_mul_i32 s81, s61, 640\ns_lshl2_add_u32 s61, s60, s44\ns_mul_i32 s74, s61, 640\ns_lshl2_add_u32 s61, s41, s44\n"
"s_mul_i32 s61, s61, 48\ns_add_u32 s61, s61, s40\ns_lshl_b32 s72, s61, 12\ns_mul_i32 s55, s42, 448\ns_add_u32 s54, s55, 64\ns_and_b32 s54, s54, 0xffffff80\n"
"s_bitcmp0_b32 s42, 0\ns_cselect_b32 s61, 384, 0\ns_add_u32 s55, s55, s61\ns_lshl1_add_u32 s61, s41, s90\ns_lshl_b32 s61, s61, 9\ns_lshl_b32 s91, s42, 14\n"
"s_add_u32 s91, s91, s61\ns_add_u32 s91, s91, 83886080\ns_mul_i32 s61, s43, 1835008\ns_lshl_b32 s62, s44, 9\ns_add_u32 s61, s61, s62\ns_add_u32 s62, s43, 1\n"
"s_and_b32 s62, s62, 3\ns_lshl_b32 s62, s62, 7\ns_add_u32 s56, s61, s62\ns_add_u32 s62, s43, 2\ns_and_b32 s62, s62, 3\ns_lshl_b32 s62, s62, 7\n"
"s_add_u32 s57, s61, s62\ns_add_u32 s62, s43, 3\ns_and_b32 s62, s62, 3\ns_lshl_b32 s62, s62, 7\ns_add_u32 s58, s61, s62\ns_add_u32 s62, s43, 4\n"
"s_and_b32 s62, s62, 3\ns_lshl_b32 s62, s62, 7\ns_add_u32 s59, s61, s62\ns_mov_b32 s76, 0x3f803f80\ns_mov_b32 s77, 0xffff0000\ns_mov_b32 s48, 131072\n"
"s_cmp_eq_u32 s60, 0\ns_cselect_b32 s78, 0xf149f2ca, 0\nv_bfe_u32 v96, v252, 6, 3\nv_lshlrev_b32 v96, 7, v96\nv_lshrrev_b32 v97, 9, v252\nv_lshl_add_u32 v96, v97, 11, v96\n"
"v_bfe_u32 v97, v252, 4, 2\nv_lshl_add_u32 v96, v97, 3, v96\ns_lshl_b32 s60, s90, 10\ns_add_u32 s60, s60, 32768\nv_add_u32 v96, s60, v96\nv_bfe_u32 v97, v252, 7, 2\n"
"v_xor_b32 v98, 0, v97\nv_lshl_add_u32 v184, v98, 5, v96\nv_add_u32 v188, 0x10000, v184\nv_xor_b32 v98, 1, v97\nv_lshl_add_u32 v185, v98, 5, v96\nv_add_u32 v189, 0x10000, v185\n"
"v_xor_b32 v98, 2, v97\nv_lshl_add_u32 v186, v98, 5, v96\nv_add_u32 v190, 0x10000, v186\nv_xor_b32 v98, 3, v97\nv_lshl_add_u32 v187, v98, 5, v96\nv_add_u32 v191, 0x10000, v187\n"
"v_lshrrev_b32 v96, 2, v252\ns_xor_b32 s60, s51, 0x2000\nv_add_u32 v225, s60, v96\nv_mov_b32 v210, 0\nv_mov_b32 v211, 0\nv_bfe_u32 v96, v252, 5, 2\n"
"v_bfe_u32 v97, v252, 8, 2\nv_xor_b32 v96, v96, v97\nv_lshrrev_b32 v97, 7, v252\ns_lshl_b32 s60, s40, 3\nv_add_u32 v97, s60, v97\nv_mov_b32 v228, v97\n"
"v_and_b32 v97, 16, v252\nv_lshl_add_u32 v229, v96, 5, v97\nv_bfe_u32 v96, v252, 5, 2\nv_bfe_u32 v97, v252, 8, 2\nv_xor_b32 v96, v96, v97\nv_lshrrev_b32 v97, 7, v252\n"
"s_lshl_b32 s60, s40, 3\nv_add_u32 v97, s60, v97\nv_lshrrev_b32 v98, 1, v96\nv_lshl_add_u32 v97, v98, 5, v97\nv_and_b32 v96, 1, v96\nv_mul_u32_u24 v97, 7168, v97\n"
"v_lshl_add_u32 v97, v96, 5, v97\nv_and_b32 v96, 16, v252\nv_add_u32 v222, v96, v97\nv_add_u32 v223, 3668992, v222\nv_bfe_u32 v96, v252, 4, 4\nv_lshrrev_b32 v97, 8, v252\n"
"v_lshrrev_b32 v98, 1, v96\nv_xor_b32 v97, v97, v98\nv_lshlrev_b32 v96, 7, v96\nv_lshl_add_u32 v96, v97, 4, v96\nv_add_u32 v248, s51, v96\nv_xor_b32 v249, 64, v248\n"
"v_add_u32 v250, 122880, v248\nv_add_u32 v251, 122880, v249\ns_add_u32 m0, s51, 122880\ns_add_u32 s60, s70, 128\nbuffer_load_dwordx4 v226, s[24:27], s60 offen lds\ns_add_u32 m0, s51, 123904\n"
"s_add_u32 s60, s70, 16512\nbuffer_load_dwordx4 v227, s[24:27], s60 offen lds\ns_add_u32 m0, s51, 124928\ns_add_u32 s60, s70, 32896\nbuffer_load_dwordx4 v226, s[24:27], s60 offen lds\ns_add_u32 m0, s51, 125952\n"
"s_add_u32 s60, s70, 49280\nbuffer_load_dwordx4 v227, s[24:27], s60 offen lds\ns_add_u32 m0, s51, 126976\ns_add_u32 s60, s71, 128\nbuffer_load_dwordx4 v226, s[24:27], s60 offen lds\ns_add_u32 m0, s51, 128000\n"
"s_add_u32 s60, s71, 16512\nbuffer_load_dwordx4 v227, s[24:27], s60 offen lds\ns_add_u32 m0, s51, 129024\ns_add_u32 s60, s71, 32896\nbuffer_load_dwordx4 v226, s[24:27], s60 offen lds\ns_add_u32 m0, s51, 130048\n"
"s_add_u32 s60, s71, 49280\nbuffer_load_dwordx4 v227, s[24:27], s60 offen lds\nv_bfe_u32 v100, v252, 4, 4\nv_lshlrev_b32 v100, 11, v100\nv_lshrrev_b32 v101, 8, v252\nv_lshl_add_u32 v100, v101, 3, v100\n"
"s_add_u32 s60, s49, s53\ns_add_u32 s61, s60, 32768\nbuffer_load_dwordx2 v[192:193], v100, s[24:27], s60 offen offset:0\nbuffer_load_dwordx2 v[194:195], v100, s[24:27], s60 offen offset:32\nbuffer_load_dwordx2 v[196:197], v100, s[24:27], s60 offen offset:64\nbuffer_load_dwordx2 v[198:199], v100, s[24:27], s60 offen offset:96\n"
"buffer_load_dwordx2 v[200:201], v100, s[24:27], s61 offen offset:0\nbuffer_load_dwordx2 v[202:203], v100, s[24:27], s61 offen offset:32\nbuffer_load_dwordx2 v[204:205], v100, s[24:27], s61 offen offset:64\nbuffer_load_dwordx2 v[206:207], v100, s[24:27], s61 offen offset:96\ns_add_u32 m0, s50, 16384\ns_add_u32 s63, s52, 4096\n"
"buffer_load_dwordx4 v220, s[4:7], s63 offen lds\nbuffer_load_dwordx4 v221, s[4:7], s63 offen offset:1024 lds\ns_add_u32 m0, s50, 24576\ns_add_u32 s63, s52, 200704\nbuffer_load_dwordx4 v220, s[4:7], s63 offen lds\nbuffer_load_dwordx4 v221, s[4:7], s63 offen offset:1024 lds\n"
"s_add_u32 m0, s50, 32768\ns_add_u32 s63, s101, 2048\nbuffer_load_dwordx4 v220, s[4:7], s63 offen lds\nbuffer_load_dwordx4 v221, s[4:7], s63 offen offset:1024 lds\ns_add_u32 m0, s50, 40960\ns_add_u32 s63, s101, 4096\n"
"buffer_load_dwordx4 v220, s[4:7], s63 offen lds\nbuffer_load_dwordx4 v221, s[4:7], s63 offen offset:1024 lds\nv_mov_b32 v106, 0xf149f149\nv_mov_b32 v104, 0xffff\nv_mov_b32 v105, 0xffff0000\ns_mov_b32 s60, 0x50000\n"
"s_mov_b32 s61, 0x5550055\nv_cndmask_b32 v96, 0, v104, s[60:61]\ns_mov_b32 s60, 0xa0000\ns_mov_b32 s61, 0xaaa00aa\nv_cndmask_b32 v96, v96, v105, s[60:61]\nv_bfi_b32 v100, v96, 0, v106\n"
"s_mov_b32 s60, 0x150001\ns_mov_b32 s61, 0x15550155\nv_cndmask_b32 v97, 0, v104, s[60:61]\ns_mov_b32 s60, 0x2a0002\ns_mov_b32 s61, 0x2aaa02aa\nv_cndmask_b32 v97, v97, v105, s[60:61]\n"
"v_bfi_b32 v101, v97, 0, v106\ns_mov_b32 s60, 0x55505555\ns_mov_b32 s61, 0x50005500\nv_cndmask_b32 v98, 0, v104, s[60:61]\ns_mov_b32 s60, 0xaaa0aaaa\ns_mov_b32 s61, 0xa000aa00\n"
"v_cndmask_b32 v98, v98, v105, s[60:61]\nv_bfi_b32 v102, v98, 0, v106\ns_mov_b32 s60, 0x55405554\ns_mov_b32 s61, 0x40005400\nv_cndmask_b32 v99, 0, v104, s[60:61]\ns_mov_b32 s60, 0xaa80aaa8\n"
"s_mov_b32 s61, 0x8000a800\nv_cndmask_b32 v99, v99, v105, s[60:61]\nv_bfi_b32 v103, v99, 0, v106\nv_add_u32 v104, 161792, v252\nds_write_b128 v104, v[96:99] offset:0\nds_write_b128 v104, v[100:103] offset:1024\n"
"s_waitcnt vmcnt(29)\nv_readfirstlane_b32 s83, v230\nv_readfirstlane_b32 s60, v231\ns_mul_i32 s46, s83, 0x9e3779b1\ns_add_u32 s46, s46, 0x7f4a7c15\ns_xor_b32 s47, s83, s60\n"
"s_mul_i32 s47, s47, 0x85ebca6b\ns_add_u32 s47, s47, 0xc2b2ae35\nv_mov_b32 v144, -1\nv_mov_b32 v145, -1\nv_mov_b32 v146, -1\nv_mov_b32 v147, -1\n"
"v_mov_b32 v230, 0\nv_mov_b32 v231, 0\nv_mov_b32 v232, 0\nv_mov_b32 v233, 0\ns_waitcnt vmcnt(15)\nv_dot2c_f32_bf16 v230, s76, v192\n"
"v_dot2c_f32_bf16 v231, v192, v192\ns_waitcnt vmcnt(11)\nv_dot2c_f32_bf16 v232, s76, v200\nv_dot2c_f32_bf16 v233, v200, v200\nv_dot2c_f32_bf16 v230, s76, v193\nv_dot2c_f32_bf16 v231, v193, v193\n"
"v_dot2c_f32_bf16 v232, s76, v201\nv_dot2c_f32_bf16 v233, v201, v201\nv_dot2c_f32_bf16 v230, s76, v194\nv_dot2c_f32_bf16 v231, v194, v194\ns_waitcnt vmcnt(10)\nv_dot2c_f32_bf16 v232, s76, v202\n"
"v_dot2c_f32_bf16 v233, v202, v202\nv_dot2c_f32_bf16 v230, s76, v195\nv_dot2c_f32_bf16 v231, v195, v195\nv_dot2c_f32_bf16 v232, s76, v203\nv_dot2c_f32_bf16 v233, v203, v203\nv_dot2c_f32_bf16 v230, s76, v196\n"
"v_dot2c_f32_bf16 v231, v196, v196\ns_waitcnt vmcnt(9)\nv_dot2c_f32_bf16 v232, s76, v204\nv_dot2c_f32_bf16 v233, v204, v204\nv_dot2c_f32_bf16 v230, s76, v197\nv_dot2c_f32_bf16 v231, v197, v197\n"
"v_dot2c_f32_bf16 v232, s76, v205\nv_dot2c_f32_bf16 v233, v205, v205\nv_dot2c_f32_bf16 v230, s76, v198\nv_dot2c_f32_bf16 v231, v198, v198\ns_waitcnt vmcnt(8)\nv_dot2c_f32_bf16 v232, s76, v206\n"
"v_dot2c_f32_bf16 v233, v206, v206\nv_dot2c_f32_bf16 v230, s76, v199\nv_dot2c_f32_bf16 v231, v199, v199\nv_dot2c_f32_bf16 v232, s76, v207\nv_dot2c_f32_bf16 v233, v207, v207\nv_lshrrev_b32 v234, 1, v252\n"
"v_permlane32_swap_b32 v230, v231\ns_nop 0\nv_permlane32_swap_b32 v232, v233\nv_add_f32 v230, v230, v231\nv_add_f32 v232, v232, v233\nv_mov_b32 v231, s46\n"
"s_nop 0\nv_permlane16_swap_b32 v230, v232\nv_add_f32 v230, v230, v232\ns_lshr_b32 s61, s49, 3\ns_lshl2_add_u32 s61, s53, s61\ns_add_u32 s61, s61, 81788928\n"
"buffer_store_dwordx2 v[230:231], v234, s[20:23], s61 offen sc1\nds_read_b128 a[0:3], v248 offset:0\nds_read_b128 a[4:7], v248 offset:2048\nds_read_b128 a[8:11], v248 offset:4096\nds_read_b128 a[12:15], v248 offset:6144\nds_read_b128 a[16:19], v249 offset:0\n"
"ds_read_b128 a[20:23], v249 offset:2048\nds_read_b128 a[24:27], v249 offset:4096\nds_read_b128 a[28:31], v249 offset:6144\ns_waitcnt vmcnt(5) lgkmcnt(8)\ns_barrier\nR 160,184,0\n"
"R 162,184,4096\nR 164,184,8192\nR 166,184,12288\nR 168,185,0\nR 170,185,4096\nR 172,185,8192\n"
"R 174,185,12288\nR 176,186,0\nR 178,186,4096\nR 180,186,8192\nR 182,186,12288\nG 10\n"
"v_mfma_f32_16x16x32_bf16 v[0:3], v[160:163], a[0:3], 0\ns_add_u32 m0, s50, 49152\ns_add_u32 s63, s52, 395264\nbuffer_load_dwordx4 v220, s[4:7], s63 offen lds\nG 8\nM 0,164,16\n"
"buffer_load_dwordx4 v221, s[4:7], s63 offen offset:1024 lds\nv_mfma_f32_16x16x32_bf16 v[16:19], v[160:163], a[4:7], 0\ns_add_u32 m0, s51, 0\ns_add_u32 s60, s70, 256\nbuffer_load_dwordx4 v226, s[24:27], s60 offen lds\nM 16,164,20\n"
"s_lshl4_add_u32 s62, s41, s42\ns_lshl_b32 s62, s62, 12\ns_add_u32 s62, s62, 77594624\ns_lshl_b32 s60, s40, 10\ns_add_u32 s62, s62, s60\nbuffer_store_dwordx4 v[144:147], v252, s[20:23], s62 offen sc1\n"
"s_add_u32 m0, s50, 57344\ns_add_u32 s63, s52, 591872\nv_mfma_f32_16x16x32_bf16 v[32:35], v[160:163], a[8:11], 0\nbuffer_load_dwordx4 v220, s[4:7], s63 offen lds\nM 32,164,24\ns_add_u32 m0, s51, 1024\n"
"s_add_u32 s60, s70, 16640\nbuffer_load_dwordx4 v227, s[24:27], s60 offen lds\nv_mfma_f32_16x16x32_bf16 v[48:51], v[160:163], a[12:15], 0\ns_add_u32 m0, s50, 57344\ns_nop 0\nbuffer_load_dwordx4 v221, s[4:7], s63 offen offset:1024 lds\n"
"M 48,164,28\ns_add_u32 m0, s50, 65536\ns_add_u32 s63, s52, 397312\nbuffer_load_dwordx4 v220, s[4:7], s63 offen lds\nG 4\nv_mfma_f32_16x16x32_bf16 v[4:7], v[168:171], a[0:3], 0\n"
"R 160,187,0\nM 4,172,16\ns_add_u32 m0, s51, 2048\ns_add_u32 s60, s70, 33024\nbuffer_load_dwordx4 v226, s[24:27], s60 offen lds\nv_mfma_f32_16x16x32_bf16 v[20:23], v[168:171], a[4:7], 0\n"
"R 162,187,4096\ns_add_u32 m0, s50, 65536\ns_nop 0\nbuffer_load_dwordx4 v221, s[4:7], s63 offen offset:1024 lds\nM 20,172,20\ns_add_u32 m0, s51, 3072\n"
"s_add_u32 s60, s70, 49408\nbuffer_load_dwordx4 v227, s[24:27], s60 offen lds\nv_mfma_f32_16x16x32_bf16 v[36:39], v[168:171], a[8:11], 0\nR 164,187,8192\ns_add_u32 m0, s50, 73728\nM 36,172,24\n"
"s_lshl4_add_u32 s62, s41, s42\ns_lshl_b32 s62, s62, 12\ns_add_u32 s62, s62, 78643200\ns_lshl_b32 s60, s40, 10\ns_add_u32 s62, s62, s60\nbuffer_store_dwordx4 v[144:147], v252, s[20:23], s62 offen sc1\n"
"s_add_u32 s63, s52, 593920\nv_mfma_f32_16x16x32_bf16 v[52:55], v[168:171], a[12:15], 0\nR 166,187,12288\nbuffer_load_dwordx4 v220, s[4:7], s63 offen lds\nM 52,172,28\ns_add_u32 m0, s51, 4096\n"
"s_add_u32 s60, s71, 256\nbuffer_load_dwordx4 v226, s[24:27], s60 offen lds\nG 4\nv_mfma_f32_16x16x32_bf16 v[8:11], v[176:179], a[0:3], 0\nR 168,184,16384\nM 8,180,16\n"
"s_add_u32 m0, s51, 5120\ns_add_u32 s60, s71, 16640\nbuffer_load_dwordx4 v227, s[24:27], s60 offen lds\nv_mfma_f32_16x16x32_bf16 v[24:27], v[176:179], a[4:7], 0\nR 170,184,20480\nds_read_b128 a[32:35], v250 offset:0\n"
"s_add_u32 m0, s50, 73728\nM 24,180,20\nds_read_b128 a[36:39], v250 offset:2048\nbuffer_load_dwordx4 v221, s[4:7], s63 offen offset:1024 lds\nv_mfma_f32_16x16x32_bf16 v[40:43], v[176:179], a[8:11], 0\nR 172,184,24576\n"
"ds_read_b128 a[40:43], v250 offset:4096\nM 40,180,24\nds_read_b128 a[44:47], v250 offset:6144\ns_add_u32 m0, s51, 6144\ns_add_u32 s60, s71, 33024\nbuffer_load_dwordx4 v226, s[24:27], s60 offen lds\n"
"v_mfma_f32_16x16x32_bf16 v[56:59], v[176:179], a[12:15], 0\nR 174,184,28672\nM 56,180,28\nds_read_b128 a[48:51], v251 offset:0\nG 9\nv_mfma_f32_16x16x32_bf16 v[12:15], v[160:163], a[0:3], 0\n"
"R 176,185,16384\nds_read_b128 a[52:55], v251 offset:2048\nM 12,164,16\nds_read_b128 a[56:59], v251 offset:4096\ns_add_u32 m0, s51, 7168\ns_add_u32 s60, s71, 49408\n"
"buffer_load_dwordx4 v227, s[24:27], s60 offen lds\nv_mfma_f32_16x16x32_bf16 v[28:31], v[160:163], a[4:7], 0\nR 178,185,20480\ns_add_u32 m0, s50, 81920\nM 28,164,20\nds_read_b128 a[60:63], v251 offset:6144\n"
"s_add_u32 s63, s101, 0\nv_mfma_f32_16x16x32_bf16 v[44:47], v[160:163], a[8:11], 0\nR 180,185,24576\nM 44,164,24\nbuffer_load_dwordx4 v220, s[4:7], s63 offen lds\nv_mfma_f32_16x16x32_bf16 v[60:63], v[160:163], a[12:15], 0\n"
"R 182,185,28672\nM 60,164,28\nG 2\ns_add_u32 m0, s51, 122880\ns_add_u32 s60, s70, 384\nbuffer_load_dwordx4 v226, s[24:27], s60 offen lds\n"
"s_waitcnt vmcnt(13)\ns_barrier\nv_mfma_f32_16x16x32_bf16 v[64:67], a[0:3], v[168:171], 0\nR 160,186,16384\nN 64,16,172\ns_add_u32 m0, s51, 123904\n"
"s_add_u32 s60, s70, 16768\nbuffer_load_dwordx4 v227, s[24:27], s60 offen lds\nv_mfma_f32_16x16x32_bf16 v[68:71], a[4:7], v[168:171], 0\nR 162,186,20480\ns_add_u32 m0, s50, 81920\nN 68,20,172\n"
"buffer_load_dwordx4 v221, s[4:7], s63 offen offset:1024 lds\nv_mfma_f32_16x16x32_bf16 v[96:99], a[8:11], v[168:171], 0\nR 164,186,24576\nN 96,24,172\ns_add_u32 m0, s51, 124928\ns_add_u32 s60, s70, 33152\n"
"buffer_load_dwordx4 v226, s[24:27], s60 offen lds\nv_mfma_f32_16x16x32_bf16 v[100:103], a[12:15], v[168:171], 0\nR 166,186,28672\ns_add_u32 m0, s50, 0\nN 100,28,172\ns_add_u32 s63, s52, 788480\n"
"G 4\nv_mfma_f32_16x16x32_bf16 v[72:75], a[0:3], v[176:179], 0\nR 168,187,16384\nbuffer_load_dwordx4 v220, s[4:7], s63 offen lds\nN 72,16,180\ns_add_u32 m0, s51, 125952\n"
"s_add_u32 s60, s70, 49536\nbuffer_load_dwordx4 v227, s[24:27], s60 offen lds\nv_mov_b32 v242, s76\nv_mfma_f32_16x16x32_bf16 v[76:79], a[4:7], v[176:179], 0\nR 170,187,20480\nv_mov_b32 v243, s76\n"
"v_mov_b32 v244, s76\nN 76,20,180\ns_add_u32 m0, s51, 126976\ns_add_u32 s60, s71, 384\nbuffer_load_dwordx4 v226, s[24:27], s60 offen lds\nv_mov_b32 v245, s76\n"
"v_mfma_f32_16x16x32_bf16 v[104:107], a[8:11], v[176:179], 0\nR 172,187,24576\ns_add_u32 m0, s50, 0\ns_nop 0\nbuffer_load_dwordx4 v221, s[4:7], s63 offen offset:1024 lds\ns_and_b32 s60, s40, 2\n"
"s_lshl_b32 s60, s60, 5\nv_xor_b32 v238, s60, v184\nN 104,24,180\ns_add_u32 m0, s51, 128000\ns_add_u32 s60, s71, 16768\nbuffer_load_dwordx4 v227, s[24:27], s60 offen lds\n"
"v_mfma_f32_16x16x32_bf16 v[108:111], a[12:15], v[176:179], 0\nR 174,187,28672\nv_xor_b32 v239, 32, v238\nN 108,28,180\nG 4\nv_mfma_f32_16x16x32_bf16 v[80:83], a[0:3], v[160:163], 0\n"
"R 176,184,49152\nR 128,238,32768\nR 130,238,36864\nN 80,16,164\ns_add_u32 m0, s51, 129024\ns_add_u32 s60, s71, 33152\n"
"buffer_load_dwordx4 v226, s[24:27], s60 offen lds\nR 132,239,32768\nR 134,239,36864\nv_mfma_f32_16x16x32_bf16 v[84:87], a[4:7], v[160:163], 0\nR 178,184,53248\ns_add_u32 m0, s50, 8192\n"
"s_add_u32 s63, s52, 985088\nbuffer_load_dwordx4 v220, s[4:7], s63 offen lds\nN 84,20,164\ns_add_u32 m0, s51, 130048\ns_add_u32 s60, s71, 49536\nbuffer_load_dwordx4 v227, s[24:27], s60 offen lds\n"
"v_mfma_f32_16x16x32_bf16 v[112:115], a[8:11], v[160:163], 0\nR 180,184,57344\ns_add_u32 m0, s50, 8192\ns_nop 0\nbuffer_load_dwordx4 v221, s[4:7], s63 offen offset:1024 lds\nN 112,24,164\n"
"v_mfma_f32_16x16x32_bf16 v[116:119], a[12:15], v[160:163], 0\nR 182,184,61440\nG 5\nv_mfma_f32_16x16x32_bf16 v[136:139], v[242:245], v[128:131], 0\nN 116,28,164\nv_mfma_f32_16x16x32_bf16 v[88:91], a[0:3], v[168:171], 0\n"
"R 160,185,49152\nG 4\nv_mfma_f32_16x16x32_bf16 v[140:143], v[242:245], v[132:135], 0\nN 88,16,172\nv_mfma_f32_16x16x32_bf16 v[92:95], a[4:7], v[168:171], 0\nR 162,185,53248\n"
"N 92,20,172\ns_add_u32 m0, s50, 16384\nv_mfma_f32_16x16x32_bf16 v[120:123], a[8:11], v[168:171], 0\nR 164,185,57344\nN 120,24,172\ns_add_u32 s63, s52, 790528\n"
"v_mfma_f32_16x16x32_bf16 v[124:127], a[12:15], v[168:171], 0\nR 166,185,61440\nv_lshrrev_b32 v128, 1, v252\nv_mov_b32 v131, s46\nN 124,28,172\ns_waitcnt vmcnt(16)\n"
"s_barrier\ns_mov_b64 s[60:61], 0xffff\nv_cndmask_b32 v130, v140, v136, s[60:61]\nG 4\nM 0,176,32\nR 168,186,49152\n"
"s_lshr_b32 s61, s40, 1\ns_lshl_b32 s61, s61, 8\ns_add_u32 s61, s61, s91\ns_add_u32 s61, s61, 262144\ns_bfm_b64 exec, 32, 0\nbuffer_store_dwordx2 v[130:131], v128, s[20:23], s61 offen sc1\n"
"s_mov_b64 exec, -1\nM 0,180,48\nbuffer_load_dwordx4 v220, s[4:7], s63 offen lds\nM 16,176,36\nR 170,186,53248\ns_waitcnt vmcnt(16)\n"
"ds_read_b128 a[64:67], v248 offset:0\nM 16,180,52\nds_read_b128 a[68:71], v248 offset:2048\nM 32,176,40\nR 172,186,57344\nds_read_b128 a[72:75], v248 offset:4096\n"
"M 32,180,56\nds_read_b128 a[76:79], v248 offset:6144\nM 48,176,44\nR 174,186,61440\nM 48,180,60\nds_read_b128 a[80:83], v249 offset:0\n"
"G 9\nM 4,160,32\nR 176,187,49152\nds_read_b128 a[84:87], v249 offset:2048\nR 128,238,40960\nR 130,238,45056\n"
"M 4,164,48\nds_read_b128 a[88:91], v249 offset:4096\nR 132,239,40960\nR 134,239,45056\nM 20,160,36\nR 178,187,53248\n"
"buffer_load_dwordx4 v221, s[4:7], s63 offen offset:1024 lds\nM 20,164,52\nds_read_b128 a[92:95], v249 offset:6144\ns_add_u32 m0, s50, 24576\nM 36,160,40\nR 180,187,57344\n"
"s_add_u32 s63, s52, 987136\nM 36,164,56\nbuffer_load_dwordx4 v220, s[4:7], s63 offen lds\nM 52,160,44\nR 182,187,61440\nG 7\n"
"v_mfma_f32_16x16x32_bf16 v[136:139], v[242:245], v[128:131], 0\nM 52,164,60\nG 2\ns_add_u32 m0, s51, 0\ns_add_u32 s60, s70, 512\nbuffer_load_dwordx4 v226, s[24:27], s60 offen lds\n"
"M 8,168,32\nR 160,188,0\nv_mfma_f32_16x16x32_bf16 v[140:143], v[242:245], v[132:135], 0\ns_add_u32 m0, s50, 24576\ns_nop 0\nbuffer_load_dwordx4 v221, s[4:7], s63 offen offset:1024 lds\n"
"M 8,172,48\ns_add_u32 m0, s51, 1024\ns_add_u32 s60, s70, 16896\nbuffer_load_dwordx4 v227, s[24:27], s60 offen lds\nM 24,168,36\nR 162,188,4096\n"
"M 24,172,52\nM 40,168,40\nR 164,188,8192\nM 40,172,56\ns_add_u32 m0, s51, 2048\ns_add_u32 s60, s70, 33280\n"
"buffer_load_dwordx4 v226, s[24:27], s60 offen lds\nM 56,168,44\nR 166,188,12288\nv_lshrrev_b32 v128, 1, v252\nv_mov_b32 v131, s46\nM 56,172,60\n"
"s_mov_b64 s[60:61], 0xffff\nv_cndmask_b32 v130, v140, v136, s[60:61]\ns_lshr_b32 s61, s40, 1\ns_lshl_b32 s61, s61, 8\ns_add_u32 s61, s61, s91\ns_add_u32 s61, s61, 524288\n"
"s_bfm_b64 exec, 32, 0\nbuffer_store_dwordx2 v[130:131], v128, s[20:23], s61 offen sc1\ns_mov_b64 exec, -1\nG 4\nM 12,176,32\nR 168,189,0\n"
"M 12,180,48\ns_add_u32 m0, s51, 3072\ns_add_u32 s60, s70, 49664\nbuffer_load_dwordx4 v227, s[24:27], s60 offen lds\nM 28,176,36\nR 170,189,4096\n"
"M 28,180,52\ns_add_u32 m0, s51, 4096\ns_add_u32 s60, s71, 512\nbuffer_load_dwordx4 v226, s[24:27], s60 offen lds\nM 44,176,40\nR 172,189,8192\n"
"M 44,180,56\ns_add_u32 m0, s51, 5120\ns_add_u32 s60, s71, 16896\nbuffer_load_dwordx4 v227, s[24:27], s60 offen lds\nM 60,176,44\nR 174,189,12288\n"
"M 60,180,60\ns_waitcnt vmcnt(12)\ns_barrier\nG 4\nN 64,32,160\nR 176,190,0\n"
"N 64,48,164\ns_add_u32 m0, s51, 6144\ns_add_u32 s60, s71, 33280\nbuffer_load_dwordx4 v226, s[24:27], s60 offen lds\nN 68,36,160\nR 178,190,4096\n"
"N 68,52,164\ns_add_u32 m0, s51, 7168\ns_add_u32 s60, s71, 49664\nbuffer_load_dwordx4 v227, s[24:27], s60 offen lds\nN 96,40,160\nR 180,190,8192\n"
"s_add_u32 m0, s50, 32768\ns_add_u32 s63, s52, 1181696\nN 96,56,164\nbuffer_load_dwordx4 v220, s[4:7], s63 offen lds\nN 100,44,160\nR 182,190,12288\n"
"N 100,60,164\nG 4\nN 72,32,168\nR 160,191,0\nN 72,48,172\nbuffer_load_dwordx4 v221, s[4:7], s63 offen offset:1024 lds\n"
"N 76,36,168\nR 162,191,4096\ns_add_u32 m0, s50, 40960\ns_add_u32 s63, s52, 1378304\nN 76,52,172\nbuffer_load_dwordx4 v220, s[4:7], s63 offen lds\n"
"N 104,40,168\nR 164,191,8192\nN 104,56,172\nN 108,44,168\nR 166,191,12288\nN 108,60,172\n"
"G 4\nN 80,32,176\nR 168,184,0\nN 80,48,180\nbuffer_load_dwordx4 v221, s[4:7], s63 offen offset:1024 lds\nN 84,36,176\n"
"R 170,184,4096\nds_read_b128 a[96:99], v250 offset:0\nN 84,52,180\nds_read_b128 a[100:103], v250 offset:2048\nN 112,40,176\nR 172,184,8192\n"
"ds_read_b128 a[104:107], v250 offset:4096\nN 112,56,180\nds_read_b128 a[108:111], v250 offset:6144\nN 116,44,176\nR 174,184,12288\nN 116,60,180\n"
"ds_read_b128 a[112:115], v251 offset:0\nG 9\nN 88,32,160\nR 176,185,0\nds_read_b128 a[116:119], v251 offset:2048\nN 88,48,164\n"
"ds_read_b128 a[120:123], v251 offset:4096\nN 92,36,160\nR 178,185,4096\nN 92,52,164\nds_read_b128 a[124:127], v251 offset:6144\nN 120,40,160\n"
"R 180,185,8192\nN 120,56,164\nN 124,44,160\nR 182,185,12288\nN 124,60,164\nG 2\n"
"s_add_u32 m0, s51, 122880\ns_add_u32 s60, s70, 640\nbuffer_load_dwordx4 v226, s[24:27], s60 offen lds\ns_waitcnt vmcnt(13)\ns_barrier\nM 0,168,64\n"
"R 160,186,0\nv_add_u32 v238, 0x10000, v238\nv_add_u32 v239, 0x10000, v239\nM 0,172,80\ns_add_u32 m0, s51, 123904\ns_add_u32 s60, s70, 17024\n"
"buffer_load_dwordx4 v227, s[24:27], s60 offen lds\nM 16,168,68\nR 162,186,4096\nR 128,238,16384\nR 130,238,20480\nM 16,172,84\n"
"R 132,239,16384\nR 134,239,20480\nM 32,168,72\nR 164,186,8192\nM 32,172,88\ns_add_u32 m0, s51, 124928\n"
"s_add_u32 s60, s70, 33408\nbuffer_load_dwordx4 v226, s[24:27], s60 offen lds\nM 48,168,76\nR 166,186,12288\ns_add_u32 m0, s50, 49152\ns_add_u32 s63, s52, 1183744\n"
"buffer_load_dwordx4 v220, s[4:7], s63 offen lds\nM 48,172,92\nG 8\nM 4,176,64\nR 168,187,0\nG 5\n"
"v_mfma_f32_16x16x32_bf16 v[136:139], v[242:245], v[128:131], 0\nM 4,180,80\ns_add_u32 m0, s51, 125952\ns_add_u32 s60, s70, 49792\nbuffer_load_dwordx4 v227, s[24:27], s60 offen lds\nM 20,176,68\n"
"R 170,187,4096\nG 4\nv_mfma_f32_16x16x32_bf16 v[140:143], v[242:245], v[132:135], 0\ns_add_u32 m0, s50, 49152\ns_nop 0\nbuffer_load_dwordx4 v221, s[4:7], s63 offen offset:1024 lds\n"
"M 20,180,84\ns_add_u32 m0, s51, 126976\ns_add_u32 s60, s71, 640\nbuffer_load_dwordx4 v226, s[24:27], s60 offen lds\nM 36,176,72\nR 172,187,8192\n"
"s_add_u32 m0, s50, 57344\ns_add_u32 s63, s52, 1380352\nbuffer_load_dwordx4 v220, s[4:7], s63 offen lds\nM 36,180,88\ns_add_u32 m0, s51, 128000\ns_add_u32 s60, s71, 17024\n"
"buffer_load_dwordx4 v227, s[24:27], s60 offen lds\nM 52,176,76\nR 174,187,12288\ns_add_u32 m0, s50, 57344\nM 52,180,92\nG 4\n"
"M 8,160,64\nR 176,184,16384\nbuffer_load_dwordx4 v221, s[4:7], s63 offen offset:1024 lds\nv_lshrrev_b32 v128, 1, v252\nM 8,164,80\ns_add_u32 m0, s51, 129024\n"
"s_add_u32 s60, s71, 33408\nbuffer_load_dwordx4 v226, s[24:27], s60 offen lds\nv_mov_b32 v131, s46\nM 24,160,68\nR 178,184,20480\ns_mov_b64 s[60:61], 0xffff\n"
"v_cndmask_b32 v130, v140, v136, s[60:61]\nM 24,164,84\ns_add_u32 m0, s51, 130048\ns_add_u32 s60, s71, 49792\nbuffer_load_dwordx4 v227, s[24:27], s60 offen lds\ns_lshr_b32 s61, s40, 1\n"
"s_lshl_b32 s61, s61, 8\ns_add_u32 s61, s61, s91\ns_add_u32 s61, s61, 0\ns_bfm_b64 exec, 32, 0\nbuffer_store_dwordx2 v[130:131], v128, s[20:23], s61 offen sc1\ns_mov_b64 exec, -1\n"
"M 40,160,72\nR 180,184,24576\nM 40,164,88\nM 56,160,76\nR 182,184,28672\nM 56,164,92\n"
"G 4\nM 12,168,64\nR 160,185,16384\nM 12,172,80\ns_add_u32 m0, s50, 65536\nM 28,168,68\n"
"R 162,185,20480\nM 28,172,84\ns_add_u32 s63, s52, 1574912\nM 44,168,72\nR 164,185,24576\nv_lshrrev_b32 v230, 7, v252\n"
"M 44,172,88\nbuffer_load_dwordx4 v220, s[4:7], s63 offen lds\nv_lshlrev_b32 v230, 9, v230\nM 60,168,76\nR 166,185,28672\nv_and_b32 v231, 0x70, v252\n"
"M 60,172,92\ns_and_b32 s60, s40, 3\ns_lshl_b32 s60, s60, 7\nv_add3_u32 v230, v230, v231, s60\ns_waitcnt vmcnt(14)\ns_barrier\n"
"G 4\nN 64,64,176\nR 168,186,16384\nN 64,80,180\ns_lshl_b32 s61, s42, 14\ns_add_u32 s61, s61, 84148224\n"
"buffer_load_dwordx4 v[128:131], v230, s[20:23], s61 offen sc1\nN 68,68,176\nR 170,186,20480\nds_read_b128 a[128:131], v248 offset:0\ns_lshl_b32 s61, s42, 14\ns_add_u32 s61, s61, 84152320\n"
"buffer_load_dwordx4 v[132:135], v230, s[20:23], s61 offen sc1\nN 68,84,180\nds_read_b128 a[132:135], v248 offset:2048\ns_lshl_b32 s61, s42, 14\ns_add_u32 s61, s61, 84156416\nbuffer_load_dwordx4 v[136:139], v230, s[20:23], s61 offen sc1\n"
"N 96,72,176\nR 172,186,24576\nds_read_b128 a[136:139], v248 offset:4096\nN 96,88,180\nds_read_b128 a[140:143], v248 offset:6144\ns_lshl_b32 s61, s42, 14\n"
"s_add_u32 s61, s61, 84160512\nbuffer_load_dwordx4 v[140:143], v230, s[20:23], s61 offen sc1\nN 100,76,176\nR 174,186,28672\nv_lshrrev_b32 v234, 7, v252\nN 100,92,180\n"
"ds_read_b128 a[144:147], v249 offset:0\nG 9\nv_lshlrev_b32 v234, 9, v234\nN 72,64,160\nR 176,187,16384\nds_read_b128 a[148:151], v249 offset:2048\n"
"v_and_b32 v235, 0x70, v252\nN 72,80,164\nds_read_b128 a[152:155], v249 offset:4096\ns_and_b32 s60, s40, 3\ns_lshl_b32 s60, s60, 7\nv_add3_u32 v234, v234, v235, s60\n"
"N 76,68,160\nR 178,187,20480\nbuffer_load_dwordx4 v221, s[4:7], s63 offen offset:1024 lds\nN 76,84,164\nds_read_b128 a[156:159], v249 offset:6144\ns_lshl_b32 s61, s42, 14\n"
"s_add_u32 s61, s61, 84410368\nbuffer_load_dwordx4 v[144:147], v234, s[20:23], s61 offen sc1\nN 104,72,160\nR 180,187,24576\ns_lshl_b32 s61, s42, 14\ns_add_u32 s61, s61, 84414464\n"
"buffer_load_dwordx4 v[148:151], v234, s[20:23], s61 offen sc1\nN 104,88,164\ns_lshl_b32 s61, s42, 14\ns_add_u32 s61, s61, 84418560\nbuffer_load_dwordx4 v[152:155], v234, s[20:23], s61 offen sc1\nN 108,76,160\n"
"R 182,187,28672\ns_lshl_b32 s61, s42, 14\ns_add_u32 s61, s61, 84422656\nbuffer_load_dwordx4 v[156:159], v234, s[20:23], s61 offen sc1\nN 108,92,164\nG 2\n"
"s_add_u32 m0, s51, 0\ns_add_u32 s60, s70, 768\nbuffer_load_dwordx4 v226, s[24:27], s60 offen lds\nN 80,64,168\nR 160,184,32768\ns_add_u32 m0, s50, 73728\n"
"s_add_u32 s63, s52, 1771520\nbuffer_load_dwordx4 v220, s[4:7], s63 offen lds\nN 80,80,172\ns_add_u32 m0, s51, 1024\ns_add_u32 s60, s70, 17152\nbuffer_load_dwordx4 v227, s[24:27], s60 offen lds\n"
"N 84,68,168\nR 162,184,36864\ns_add_u32 m0, s50, 73728\ns_nop 0\nbuffer_load_dwordx4 v221, s[4:7], s63 offen offset:1024 lds\nN 84,84,172\n"
"N 112,72,168\nR 164,184,40960\nN 112,88,172\ns_add_u32 m0, s51, 2048\ns_add_u32 s60, s70, 33536\nbuffer_load_dwordx4 v226, s[24:27], s60 offen lds\n"
"N 116,76,168\nR 166,184,45056\nN 116,92,172\nG 4\nN 88,64,176\nR 168,185,32768\n"
"N 88,80,180\ns_add_u32 m0, s51, 3072\ns_add_u32 s60, s70, 49920\nbuffer_load_dwordx4 v227, s[24:27], s60 offen lds\nN 92,68,176\nR 170,185,36864\n"
"N 92,84,180\ns_add_u32 m0, s51, 4096\ns_add_u32 s60, s71, 768\nbuffer_load_dwordx4 v226, s[24:27], s60 offen lds\nN 120,72,176\nR 172,185,40960\n"
"N 120,88,180\ns_add_u32 m0, s51, 5120\ns_add_u32 s60, s71, 17152\nbuffer_load_dwordx4 v227, s[24:27], s60 offen lds\nN 124,76,176\nR 174,185,45056\n"
"N 124,92,180\ns_waitcnt vmcnt(21)\ns_barrier\nG 4\nM 0,160,96\nR 176,186,32768\n"
"M 0,164,112\ns_add_u32 m0, s51, 6144\ns_add_u32 s60, s71, 33536\nbuffer_load_dwordx4 v226, s[24:27], s60 offen lds\nM 16,160,100\nR 178,186,36864\n"
"M 16,164,116\ns_add_u32 m0, s51, 7168\ns_add_u32 s60, s71, 49920\nbuffer_load_dwordx4 v227, s[24:27], s60 offen lds\nM 32,160,104\nR 180,186,40960\n"
"s_add_u32 m0, s50, 81920\ns_add_u32 s63, s52, 1968128\nbuffer_load_dwordx4 v220, s[4:7], s63 offen lds\ns_waitcnt vmcnt(18)\nv_min3_u32 v232, v129, v131, v133\nM 32,164,120\n"
"buffer_load_dwordx4 v221, s[4:7], s63 offen offset:1024 lds\ns_waitcnt vmcnt(18)\nv_min3_u32 v232, v232, v135, v137\nM 48,160,108\nR 182,186,45056\ns_waitcnt vmcnt(17)\n"
"v_min3_u32 v232, v232, v139, v141\nv_min3_u32 v232, v232, v143, v143\nM 48,164,124\nG 4\nv_max3_u32 v233, v129, v131, v133\nv_max3_u32 v233, v233, v135, v137\n"
"v_max3_u32 v233, v233, v139, v141\nM 4,168,96\nR 160,187,32768\nv_max3_u32 v233, v233, v143, v143\nv_cmp_ne_u32 s[86:87], s46, v232\nM 4,172,112\n"
"v_cmp_ne_u32 s[88:89], s46, v233\nM 20,168,100\nR 162,187,36864\ns_or_b64 s[86:87], s[86:87], s[88:89]\ns_cbranch_scc1 Lq110\nLq111:\n"
"M 20,172,116\nv_add_f32 v128, v128, v136\nv_add_f32 v130, v130, v138\nv_add_f32 v132, v132, v140\nM 36,168,104\nR 164,187,40960\n"
"v_add_f32 v134, v134, v142\nv_add_f32 v128, v128, v132\nM 36,172,120\nv_add_f32 v130, v130, v134\nv_add_f32_dpp v128, v128, v128 row_ror:8 row_mask:0xf bank_mask:0xf\ns_nop 0\n"
"v_add_f32_dpp v130, v130, v130 row_ror:8 row_mask:0xf bank_mask:0xf\nM 52,168,108\nR 166,187,45056\nv_mov_b32 v132, v128\nv_mov_b32 v133, v130\nM 52,172,124\n"
"G 4\nv_permlane32_swap_b32 v132, v128\nv_permlane32_swap_b32 v133, v130\nv_add_f32 v128, v128, v132\nM 8,176,96\nR 168,184,49152\n"
"v_add_f32 v130, v130, v133\nv_mov_b32 v132, v128\nM 8,180,112\nv_mov_b32 v133, v130\nv_permlane16_swap_b32 v132, v128\ns_nop 0\n"
"v_permlane16_swap_b32 v133, v130\nM 24,176,100\nR 170,184,53248\nds_read_b128 a[160:163], v250 offset:0\nv_add_f32 v128, v128, v132\nM 24,180,116\n"
"ds_read_b128 a[164:167], v250 offset:2048\nv_add_f32 v130, v130, v133\nv_and_b32 v136, s77, v128\nM 40,176,104\nR 172,184,57344\nds_read_b128 a[168:171], v250 offset:4096\n"
"v_sub_f32 v137, v128, v136\nM 40,180,120\nds_read_b128 a[172:175], v250 offset:6144\nv_cvt_pk_bf16_f32 v132, v136, v137\nv_cvt_pk_bf16_f32 v133, v136, 0\nM 56,176,108\n"
"R 174,184,61440\nv_and_b32 v136, s77, v130\nv_sub_f32 v137, v130, v136\nM 56,180,124\nds_read_b128 a[176:179], v251 offset:0\nG 9\n"
"v_cvt_pk_bf16_f32 v134, v136, v137\nM 12,160,96\nR 176,185,49152\nds_read_b128 a[180:183], v251 offset:2048\nv_cvt_pk_bf16_f32 v135, v136, 0\nM 12,164,112\n"
"ds_read_b128 a[184:187], v251 offset:4096\ns_add_u32 s63, s52, 2164736\nv_and_b32 v230, 0x1ff, v230\nM 28,160,100\nR 178,185,53248\nv_add_u32 v230, 156160, v230\n"
"ds_write_b128 v230, v[132:135]\nM 28,164,116\nds_read_b128 a[188:191], v251 offset:6144\nM 44,160,104\nR 180,185,57344\nM 44,164,120\n"
"s_add_u32 m0, s50, 90112\nM 60,160,108\nR 182,185,61440\nM 60,164,124\ns_waitcnt vmcnt(8) lgkmcnt(2)\ns_barrier\n"
"buffer_load_dwordx4 v220, s[4:7], s63 offen lds\nN 64,96,168\nR 160,186,49152\nbuffer_load_dwordx4 v221, s[4:7], s63 offen offset:1024 lds\nN 64,112,172\ns_add_u32 m0, s50, 98304\n"
"s_add_u32 s63, s52, 2361344\nbuffer_load_dwordx4 v220, s[4:7], s63 offen lds\nN 68,100,168\nR 162,186,53248\nv_min3_u32 v236, v145, v147, v149\nv_min3_u32 v236, v236, v151, v153\n"
"N 68,116,172\nv_min3_u32 v236, v236, v155, v157\nv_min3_u32 v236, v236, v159, v159\nv_max3_u32 v237, v145, v147, v149\nN 96,104,168\nR 164,186,57344\n"
"v_max3_u32 v237, v237, v151, v153\nv_max3_u32 v237, v237, v155, v157\nN 96,120,172\nbuffer_load_dwordx4 v221, s[4:7], s63 offen offset:1024 lds\nv_max3_u32 v237, v237, v159, v159\nN 100,108,168\n"
"R 166,186,61440\ns_add_u32 m0, s50, 106496\nv_cmp_ne_u32 s[86:87], s46, v236\nN 100,124,172\nG 4\nv_cmp_ne_u32 s[88:89], s46, v237\n"
"N 72,96,176\nR 168,187,49152\ns_add_u32 s63, s52, 2557952\ns_or_b64 s[86:87], s[86:87], s[88:89]\ns_cbranch_scc1 Lq120\nLq121:\n"
"N 72,112,180\nbuffer_load_dwordx4 v220, s[4:7], s63 offen lds\nv_add_f32 v144, v144, v152\nN 76,100,176\nR 170,187,53248\nv_add_f32 v146, v146, v154\n"
"v_add_f32 v148, v148, v156\nN 76,116,180\nv_add_f32 v150, v150, v158\nv_add_f32 v144, v144, v148\nv_add_f32 v146, v146, v150\nN 104,104,176\n"
"R 172,187,57344\nv_add_f32_dpp v144, v144, v144 row_ror:8 row_mask:0xf bank_mask:0xf\nv_add_f32_dpp v146, v146, v146 row_ror:8 row_mask:0xf bank_mask:0xf\nN 104,120,180\nbuffer_load_dwordx4 v221, s[4:7], s63 offen offset:1024 lds\nv_mov_b32 v148, v144\n"
"N 108,108,176\nR 174,187,61440\nv_mov_b32 v149, v146\nv_permlane32_swap_b32 v148, v144\nN 108,124,180\nG 4\n"
"v_permlane32_swap_b32 v149, v146\nv_add_f32 v144, v144, v148\nv_add_f32 v146, v146, v149\nN 80,96,160\nR 176,188,0\nv_mov_b32 v148, v144\n"
"v_mov_b32 v149, v146\nN 80,112,164\nv_permlane16_swap_b32 v148, v144\nv_permlane16_swap_b32 v149, v146\nv_add_f32 v144, v144, v148\nN 84,100,160\n"
"R 178,188,4096\nv_add_f32 v146, v146, v149\nv_and_b32 v152, s77, v144\nN 84,116,164\ns_add_u32 m0, s50, 114688\nv_sub_f32 v153, v144, v152\n"
"v_cvt_pk_bf16_f32 v148, v152, v153\nN 112,104,160\nR 180,188,8192\nv_cvt_pk_bf16_f32 v149, v152, 0\nN 112,120,164\ns_add_u32 s63, s52, 2754560\n"
"v_and_b32 v152, s77, v146\nv_sub_f32 v153, v146, v152\nN 116,108,160\nR 182,188,12288\nv_cvt_pk_bf16_f32 v150, v152, v153\nN 116,124,164\n"
"G 4\nv_cvt_pk_bf16_f32 v151, v152, 0\nv_and_b32 v234, 0x1ff, v234\nN 88,96,168\nR 160,189,0\nv_add_u32 v234, 156672, v234\n"
"N 88,112,172\nbuffer_load_dwordx4 v220, s[4:7], s63 offen lds\nN 92,100,168\nR 162,189,4096\nds_write_b128 v234, v[148:151]\nN 92,116,172\n"
"s_lshr_b32 s84, s49, 3\nN 120,104,168\nR 164,189,8192\nN 120,120,172\ns_add_u32 s84, s84, 81788928\nN 124,108,168\n"
"R 166,189,12288\nN 124,124,172\ns_waitcnt vmcnt(5) lgkmcnt(2)\ns_barrier\nM 0,176,128\nR 168,190,0\n"
"M 0,180,144\nM 16,176,132\nR 170,190,4096\nds_read_b128 a[192:195], v248 offset:0\nM 16,180,148\nds_read_b128 a[196:199], v248 offset:2048\n"
"buffer_load_dwordx4 v[128:131], v252, s[20:23], s84 offen offset:0 sc1\nM 32,176,136\nR 172,190,8192\nds_read_b128 a[200:203], v248 offset:4096\nM 32,180,152\nds_read_b128 a[204:207], v248 offset:6144\n"
"buffer_load_dwordx4 v[132:135], v252, s[20:23], s84 offen offset:1024 sc1\nM 48,176,140\nR 174,190,12288\nM 48,180,156\nds_read_b128 a[208:211], v249 offset:0\nG 9\n"
"M 4,160,128\nR 176,191,0\nds_read_b128 a[212:215], v249 offset:2048\nM 4,164,144\nds_read_b128 a[216:219], v249 offset:4096\nbuffer_load_dwordx4 v[136:139], v252, s[20:23], s84 offen offset:2048 sc1\n"
"M 20,160,132\nR 178,191,4096\ns_lshr_b32 s85, s49, 3\nbuffer_load_dwordx4 v221, s[4:7], s63 offen offset:1024 lds\nM 20,164,148\nds_read_b128 a[220:223], v249 offset:6144\n"
"s_add_u32 s85, s85, 81793024\ns_add_u32 m0, s50, 0\ns_add_u32 s63, s52, 2951168\nM 36,160,136\nR 180,191,8192\nbuffer_load_dwordx4 v220, s[4:7], s63 offen lds\n"
"M 36,164,152\nbuffer_load_dwordx4 v[140:143], v252, s[20:23], s84 offen offset:3072 sc1\nM 52,160,140\nR 182,191,12288\nG 2\ns_add_u32 m0, s51, 0\n"
"s_add_u32 s60, s70, 896\nbuffer_load_dwordx4 v226, s[24:27], s60 offen lds\nM 52,164,156\ns_add_u32 m0, s51, 1024\ns_add_u32 s60, s70, 17280\nbuffer_load_dwordx4 v227, s[24:27], s60 offen lds\n"
"M 8,168,128\nR 160,188,16384\nbuffer_load_dwordx4 v[144:147], v252, s[20:23], s85 offen offset:0 sc1\nM 8,172,144\ns_add_u32 m0, s51, 2048\ns_add_u32 s60, s70, 33664\n"
"buffer_load_dwordx4 v226, s[24:27], s60 offen lds\nM 24,168,132\nR 162,188,20480\nbuffer_load_dwordx4 v[148:151], v252, s[20:23], s85 offen offset:1024 sc1\nM 24,172,148\ns_add_u32 m0, s51, 3072\n"
"s_add_u32 s60, s70, 50048\nbuffer_load_dwordx4 v227, s[24:27], s60 offen lds\nM 40,168,136\nR 164,188,24576\nbuffer_load_dwordx4 v[152:155], v252, s[20:23], s85 offen offset:2048 sc1\nM 40,172,152\n"
"s_add_u32 m0, s51, 4096\ns_add_u32 s60, s71, 896\nbuffer_load_dwordx4 v226, s[24:27], s60 offen lds\nM 56,168,140\nR 166,188,28672\ns_add_u32 m0, s50, 0\n"
"s_nop 0\nbuffer_load_dwordx4 v221, s[4:7], s63 offen offset:1024 lds\nM 56,172,156\ns_add_u32 m0, s51, 5120\ns_add_u32 s60, s71, 17280\nbuffer_load_dwordx4 v227, s[24:27], s60 offen lds\n"
"G 4\nM 12,176,128\nR 168,189,16384\nbuffer_load_dwordx4 v[156:159], v252, s[20:23], s85 offen offset:3072 sc1\nM 12,180,144\ns_add_u32 m0, s51, 6144\n"
"s_add_u32 s60, s71, 33664\nbuffer_load_dwordx4 v226, s[24:27], s60 offen lds\nM 28,176,132\nR 170,189,20480\nM 28,180,148\ns_add_u32 m0, s51, 7168\n"
"s_add_u32 s60, s71, 50048\nbuffer_load_dwordx4 v227, s[24:27], s60 offen lds\nM 44,176,136\nR 172,189,24576\ns_add_u32 m0, s50, 8192\ns_add_u32 s63, s52, 1576960\n"
"buffer_load_dwordx4 v220, s[4:7], s63 offen lds\nM 44,180,152\nbuffer_load_dwordx4 v221, s[4:7], s63 offen offset:1024 lds\nM 60,176,140\nR 174,189,28672\ns_add_u32 m0, s50, 16384\n"
"M 60,180,156\ns_waitcnt vmcnt(22)\ns_barrier\nG 4\nM 0,160,160\nR 176,190,16384\n"
"s_add_u32 s63, s52, 1773568\nM 0,164,176\nbuffer_load_dwordx4 v220, s[4:7], s63 offen lds\nM 16,160,164\nR 178,190,20480\nM 16,164,180\n"
"M 32,160,168\nR 180,190,24576\nM 32,164,184\nbuffer_load_dwordx4 v221, s[4:7], s63 offen offset:1024 lds\nM 48,160,172\nR 182,190,28672\n"
"s_add_u32 m0, s50, 24576\nM 48,164,188\nG 4\nM 4,168,160\nR 160,191,16384\ns_add_u32 s63, s52, 1970176\n"
"M 4,172,176\nbuffer_load_dwordx4 v220, s[4:7], s63 offen lds\nM 20,168,164\nR 162,191,20480\nM 20,172,180\nM 36,168,168\n"
"R 164,191,24576\nM 36,172,184\nbuffer_load_dwordx4 v221, s[4:7], s63 offen offset:1024 lds\nM 52,168,172\nR 166,191,28672\ns_add_u32 m0, s50, 32768\n"
"M 52,172,188\nG 4\nM 8,176,160\nR 168,188,32768\ns_add_u32 s63, s52, 2166784\nM 8,180,176\n"
"buffer_load_dwordx4 v220, s[4:7], s63 offen lds\nM 24,176,164\nR 170,188,36864\nM 24,180,180\nM 40,176,168\nR 172,188,40960\n"
"M 40,180,184\nbuffer_load_dwordx4 v221, s[4:7], s63 offen offset:1024 lds\nM 56,176,172\nR 174,188,45056\nM 56,180,188\nG 4\n"
"M 12,160,160\nR 176,189,32768\nM 12,164,176\nM 28,160,164\nR 178,189,36864\nM 28,164,180\n"
"s_add_u32 m0, s50, 40960\nM 44,160,168\nR 180,189,40960\nM 44,164,184\ns_add_u32 s63, s52, 2363392\nM 60,160,172\n"
"R 182,189,45056\nM 60,164,188\ns_waitcnt vmcnt(12)\ns_barrier\nG 4\nM 0,168,192\n"
"R 160,190,32768\nM 0,172,208\nbuffer_load_dwordx4 v220, s[4:7], s63 offen lds\nM 16,168,196\nR 162,190,36864\nv_min3_u32 v230, v129, v131, v133\n"
"v_min3_u32 v230, v230, v135, v137\nM 16,172,212\nv_min3_u32 v230, v230, v139, v141\nv_min3_u32 v230, v230, v143, v145\nv_min3_u32 v230, v230, v147, v149\nM 32,168,200\n"
"R 164,190,40960\nv_min3_u32 v230, v230, v151, v153\ns_waitcnt vmcnt(11)\nv_min3_u32 v230, v230, v155, v157\nM 32,172,216\nbuffer_load_dwordx4 v221, s[4:7], s63 offen offset:1024 lds\n"
"v_min3_u32 v230, v230, v159, v159\nM 48,168,204\nR 166,190,45056\ns_add_u32 s63, s52, 2560000\nv_max3_u32 v231, v129, v131, v133\nM 48,172,220\n"
"G 4\nv_max3_u32 v231, v231, v135, v137\nv_max3_u32 v231, v231, v139, v141\nv_max3_u32 v231, v231, v143, v145\nM 4,176,192\nR 168,191,32768\n"
"v_max3_u32 v231, v231, v147, v149\nv_max3_u32 v231, v231, v151, v153\nM 4,180,208\ns_add_u32 m0, s50, 49152\nv_max3_u32 v231, v231, v155, v157\nv_max3_u32 v231, v231, v159, v159\n"
"v_cmp_ne_u32 s[86:87], s46, v230\nM 20,176,196\nR 170,191,36864\nv_cmp_ne_u32 s[88:89], s46, v231\nM 20,180,212\nbuffer_load_dwordx4 v220, s[4:7], s63 offen lds\n"
"s_or_b64 s[86:87], s[86:87], s[88:89]\ns_cbranch_scc1 Lq135\nLq136:\nM 36,176,200\nR 172,191,40960\nv_add_f32 v128, v128, v144\n"
"v_add_f32 v130, v130, v146\nM 36,180,216\nv_add_f32 v132, v132, v148\nv_add_f32 v134, v134, v150\nv_add_f32 v136, v136, v152\nM 52,176,204\n"
"R 174,191,45056\nv_add_f32 v138, v138, v154\nv_add_f32 v140, v140, v156\nM 52,180,220\nG 4\nv_add_f32 v142, v142, v158\n"
"v_add_f32 v128, v128, v136\nv_add_f32 v130, v130, v138\nM 8,160,192\nR 176,188,49152\ns_waitcnt vmcnt(11)\nds_read_b128 a[224:227], v248 offset:0\n"
"v_add_f32 v132, v132, v140\nM 8,164,208\nds_read_b128 a[228:231], v248 offset:2048\nv_add_f32 v134, v134, v142\nv_add_f32 v128, v128, v132\nM 24,160,196\n"
"R 178,188,53248\nds_read_b128 a[232:235], v248 offset:4096\nv_add_f32 v130, v130, v134\nM 24,164,212\nds_read_b128 a[236:239], v248 offset:6144\nv_lshrrev_b32 v231, 1, v252\n"
"v_lshrrev_b32 v230, 2, v252\nM 40,160,200\nR 180,184,0\nLq191:\ns_lshr_b32 s60, s45, 3\ns_lshl2_add_u32 s60, s60, s40\n"
"s_lshl_b32 s60, s60, 8\nv_lshl_add_u32 v224, v252, 3, s60\nv_add_u32 v224, Lq191+4096-LqPC, v224\ns_mov_b64 exec, 3\nglobal_load_dword v224, v224, s[98:99]\ns_mov_b64 exec, -1\n"
"s_lshl_b32 s62, s40, 9\ns_add_u32 s62, s62, 159232\nv_add_u32 v231, s62, v231\nM 40,164,216\nds_read_b128 a[240:243], v249 offset:0\nds_write2_b32 v231, v128, v130 offset1:1\n"
"M 56,160,204\nR 182,184,4096\nds_read_b128 a[244:247], v249 offset:2048\ns_lshl_b32 s62, s40, 9\ns_add_u32 s62, s62, 159232\nv_add_u32 v230, s62, v230\n"
"M 56,164,220\nds_read_b128 a[248:251], v249 offset:4096\nds_read2st64_b32 v[232:233], v230 offset1:1\nG 13\nM 12,168,192\nR 160,189,49152\n"
"M 12,172,208\nds_read_b128 a[252:255], v249 offset:6144\nM 28,168,196\nR 162,189,53248\nM 28,172,212\nbuffer_load_dwordx4 v221, s[4:7], s63 offen offset:1024 lds\n"
"M 44,168,200\nR 164,185,0\ns_add_u32 s63, s52, 2756608\nM 44,172,216\ns_add_u32 m0, s50, 57344\nG 4\n"
"v_add_f32 v232, v232, v233\nv_mov_b32 v233, v232\ns_nop 1\nv_permlane32_swap_b32 v232, v233\nM 60,168,204\nR 166,185,4096\n"
"s_lshl_b32 s60, s90, 14\ns_add_u32 s70, s49, s60\ns_xor_b32 s71, s70, 0x10000\nv_mov_b32 v230, v232\nM 60,172,220\ns_barrier\n"
"v_mov_b32 v231, v233\nM 0,176,224\nR 168,190,49152\nv_permlane16_swap_b32 v230, v232\nv_permlane16_swap_b32 v231, v233\nM 0,180,240\n"
"buffer_load_dwordx4 v220, s[4:7], s63 offen lds\nv_mul_f32 v230, 0xba800000, v230\nM 16,176,228\nR 170,190,53248\nv_mul_f32 v214, v230, v230\nv_mul_f32 v231, 0x3a800000, v231\n"
"M 16,180,244\nv_sub_f32 v231, v231, v214\nv_max_f32 v231, 0, v231\nv_add_f32 v231, 0x3727c5ac, v231\nM 32,176,232\nR 172,186,0\n"
"v_and_b32 v208, s77, v230\nv_rsq_f32 v234, v231\nM 32,180,248\nbuffer_load_dwordx4 v221, s[4:7], s63 offen offset:1024 lds\nv_mul_f32 v216, v231, v234\nM 48,176,236\n"
"R 174,186,4096\ns_add_u32 s63, s52, 2953216\nv_sub_f32 v230, v230, v208\nG 7\nM 48,180,252\ns_waitcnt vmcnt(7)\n"
"s_barrier\nG 4\nv_cvt_pk_bf16_f32 v209, v230, v216\nM 4,160,224\nR 176,191,49152\nv_cvt_pk_bf16_f32 v208, v208, v208\n"
"v_mov_b32 v214, v234\nM 4,164,240\ns_add_u32 m0, s50, 65536\ns_mov_b64 s[60:61], 0xffff\nv_cndmask_b32 v208, 0, v208, s[60:61]\nv_mul_f32 v215, 0x3e8293ee, v234\n"
"M 20,160,228\nR 178,191,53248\nv_mul_f32 v232, 0xba800000, v232\nv_mul_f32 v217, v232, v232\nM 20,164,244\nbuffer_load_dwordx4 v220, s[4:7], s63 offen lds\n"
"M 36,160,232\nR 180,187,0\nv_mul_f32 v233, 0x3a800000, v233\nM 36,164,248\ns_mov_b64 s[60:61], 0xffff\nv_cndmask_b32 v209, 0, v209, s[60:61]\n"
"v_sub_f32 v233, v233, v217\nM 52,160,236\nR 182,187,4096\nv_max_f32 v233, 0, v233\nM 52,164,252\nG 4\n"
"v_add_f32 v233, 0x3727c5ac, v233\nv_and_b32 v212, s77, v232\nM 8,168,224\nR 160,184,8192\nv_sub_f32 v232, v232, v212\nM 8,172,240\n"
"buffer_load_dwordx4 v221, s[4:7], s63 offen offset:1024 lds\nM 24,168,228\nR 162,184,12288\nv_cvt_pk_bf16_f32 v212, v212, v212\nM 24,172,244\ns_add_u32 m0, s50, 73728\n"
"v_rsq_f32 v235, v233\nM 40,168,232\nR 164,184,16384\nv_mul_f32 v219, v233, v235\nM 40,172,248\ns_add_u32 s63, s52, 0\n"
"s_mov_b64 s[60:61], 0xffff\nv_cndmask_b32 v212, 0, v212, s[60:61]\nv_cvt_pk_bf16_f32 v213, v232, v219\nM 56,168,236\nR 166,184,20480\nv_mov_b32 v217, v235\n"
"M 56,172,252\ns_mov_b64 s[60:61], 0xffff\nv_cndmask_b32 v213, 0, v213, s[60:61]\nG 4\nv_mul_f32 v218, 0x3e8293ee, v235\nM 12,176,224\n"
"R 168,185,8192\nv_bfe_u32 v230, v252, 2, 6\nM 12,180,240\nbuffer_load_dwordx4 v220, s[4:7], s63 offen lds\nM 28,176,228\nR 170,185,12288\n"
"s_lshl_b32 s60, s40, 7\ns_add_u32 s60, s60, 161280\nv_add_u32 v230, s60, v230\nM 28,180,244\nds_write_b32 v230, v214 offset:0\nM 44,176,232\n"
"R 172,185,16384\nds_write_b32 v230, v217 offset:64\nM 44,180,248\nM 60,176,236\nR 174,185,20480\nM 60,180,252\n"
"G 6\nN 64,128,160\nR 176,186,8192\nN 64,144,164\ns_mov_b32 m0, s51\nN 68,132,160\n"
"R 178,186,12288\nds_write_addtid_b32 v32 offset:0\nN 68,148,164\nds_write_addtid_b32 v33 offset:256\nds_write_addtid_b32 v34 offset:512\nN 96,136,160\n"
"R 180,186,16384\nds_write_addtid_b32 v35 offset:768\nv_lshrrev_b32 v234, 7, v252\nN 96,152,164\nds_write_addtid_b32 v36 offset:1024\nv_lshlrev_b32 v234, 9, v234\n"
"N 100,140,160\nR 182,186,20480\nds_write_addtid_b32 v37 offset:1280\nN 100,156,164\nds_write_addtid_b32 v38 offset:1536\nG 11\n"
"v_and_b32 v235, 0x70, v252\nN 72,128,168\nR 160,187,8192\ns_mov_b32 s60, 156160\nv_bfe_u32 v246, v252, 4, 4\nv_lshl_add_u32 v246, v246, 3, s60\n"
"N 72,144,172\nds_read_b64 v[238:239], v246 offset:0\ns_and_b32 s60, s40, 3\ns_lshl_b32 s60, s60, 7\nv_add3_u32 v234, v234, v235, s60\nN 76,132,168\n"
"R 162,187,12288\nds_read_b64 v[240:241], v246 offset:128\ns_lshl_b32 s61, s42, 14\ns_add_u32 s61, s61, 83886080\nbuffer_load_dwordx4 v[128:131], v234, s[20:23], s61 offen sc1\nN 76,148,172\n"
"ds_read_b64 v[242:243], v246 offset:256\ns_lshl_b32 s61, s42, 14\ns_add_u32 s61, s61, 83890176\nbuffer_load_dwordx4 v[132:135], v234, s[20:23], s61 offen sc1\nN 104,136,168\nR 164,187,16384\n"
"ds_read_b64 v[244:245], v246 offset:384\nN 104,152,172\nds_read_b64 v[246:247], v246 offset:0\ns_lshl_b32 s61, s42, 14\ns_add_u32 s61, s61, 83894272\nbuffer_load_dwordx4 v[136:139], v234, s[20:23], s61 offen sc1\n"
"N 108,140,168\nR 166,187,20480\nds_write_addtid_b32 v39 offset:1792\nN 108,156,172\ns_lshl_b32 s61, s42, 14\ns_add_u32 s61, s61, 83898368\n"
"buffer_load_dwordx4 v[140:143], v234, s[20:23], s61 offen sc1\nG 12\nN 80,128,176\nR 168,184,24576\nds_write_addtid_b32 v40 offset:2048\nN 80,144,180\n"
"G 8\nv_mfma_f32_16x16x32_bf16 v[0:3], v[238:241], v[208:211], v[0:3]\nds_write_addtid_b32 v41 offset:2304\nds_write_addtid_b32 v42 offset:2560\nN 84,132,176\nR 170,184,28672\n"
"G 10\nv_mfma_f32_16x16x32_bf16 v[4:7], v[240:243], v[208:211], v[4:7]\nds_write_addtid_b32 v43 offset:2816\nN 84,148,180\nG 9\nv_mfma_f32_16x16x32_bf16 v[8:11], v[242:245], v[208:211], v[8:11]\n"
"ds_write_addtid_b32 v44 offset:3072\nds_write_addtid_b32 v45 offset:3328\nN 112,136,176\nR 172,184,32768\nG 11\nv_mfma_f32_16x16x32_bf16 v[12:15], v[244:247], v[208:211], v[12:15]\n"
"ds_write_addtid_b32 v46 offset:3584\nN 112,152,180\nv_mfma_f32_16x16x32_bf16 v[16:19], v[244:247], v[210:213], v[16:19]\nds_write_addtid_b32 v47 offset:3840\nds_write_addtid_b32 v48 offset:4096\nN 116,140,176\n"
"R 174,184,36864\nv_mfma_f32_16x16x32_bf16 v[20:23], v[238:241], v[210:213], v[20:23]\nds_write_addtid_b32 v49 offset:4352\nN 116,156,180\nv_mfma_f32_16x16x32_bf16 v[24:27], v[240:243], v[210:213], v[24:27]\nds_write_addtid_b32 v50 offset:4608\n"
"ds_write_addtid_b32 v51 offset:4864\nG 15\nN 88,128,160\nR 176,185,24576\nv_mfma_f32_16x16x32_bf16 v[28:31], v[242:245], v[210:213], v[28:31]\nds_write_addtid_b32 v52 offset:5120\n"
"N 88,144,164\nds_write_addtid_b32 v53 offset:5376\nds_write_addtid_b32 v54 offset:5632\nN 92,132,160\nR 178,185,28672\nds_write_addtid_b32 v55 offset:5888\n"
"N 92,148,164\nds_write_addtid_b32 v56 offset:6144\nds_write_addtid_b32 v57 offset:6400\nN 120,136,160\nR 180,185,32768\nds_write_addtid_b32 v58 offset:6656\n"
"N 120,152,164\nds_write_addtid_b32 v59 offset:6912\nds_write_addtid_b32 v60 offset:7168\nN 124,140,160\nR 182,185,36864\nds_write_addtid_b32 v61 offset:7424\n"
"N 124,156,164\nds_write_addtid_b32 v62 offset:7680\nds_write_addtid_b32 v63 offset:7936\nG 15\nN 64,160,168\nR 160,186,24576\n"
"s_add_u32 m0, s50, 73728\ns_nop 0\nbuffer_load_dwordx4 v221, s[4:7], s63 offen offset:1024 lds\nN 64,176,172\ns_add_u32 m0, s50, 81920\ns_add_u32 s63, s52, 196608\n"
"buffer_load_dwordx4 v220, s[4:7], s63 offen lds\nN 68,164,168\nR 162,186,28672\ns_waitcnt vmcnt(7) lgkmcnt(2)\ns_barrier\nN 68,180,172\n"
"buffer_load_dwordx4 v221, s[4:7], s63 offen offset:1024 lds\nds_read2st64_b32 v[32:33], v225 offset0:0 offset1:1\nN 96,168,168\nR 164,186,32768\ns_add_u32 m0, s50, 90112\ns_add_u32 s63, s52, 393216\n"
"ds_read2st64_b32 v[34:35], v225 offset0:2 offset1:3\nN 96,184,172\nbuffer_load_dwordx4 v220, s[4:7], s63 offen lds\nds_read2st64_b32 v[36:37], v225 offset0:4 offset1:5\nv_mov_b32 v248, -1\nv_mov_b32 v249, -1\n"
"v_mov_b32 v250, -1\nv_mov_b32 v251, -1\nN 100,172,168\nR 166,186,36864\nds_read2st64_b32 v[38:39], v225 offset0:6 offset1:7\nN 100,188,172\n"
"ds_read2st64_b32 v[40:41], v225 offset0:8 offset1:9\nds_read2st64_b32 v[42:43], v225 offset0:10 offset1:11\nN 72,160,176\nR 168,187,24576\nds_read2st64_b32 v[44:45], v225 offset0:12 offset1:13\nN 72,176,180\n"
"buffer_load_dwordx4 v221, s[4:7], s63 offen offset:1024 lds\nG 9\nv_add_f32 v0, v0, v32\nN 76,164,176\nR 170,187,28672\ns_add_u32 m0, s50, 98304\n"
"s_add_u32 s63, s52, 589824\nv_add_f32 v1, v1, v33\nN 76,180,180\nbuffer_load_dwordx4 v220, s[4:7], s63 offen lds\nG 8\nv_add_f32 v2, v2, v34\n"
"N 104,168,176\nR 172,187,32768\nv_add_f32 v3, v3, v35\nG 8\nv_add_f32 v4, v4, v36\nN 104,184,180\n"
"ds_read2st64_b32 v[46:47], v225 offset0:14 offset1:15\nv_add_f32 v5, v5, v37\nG 7\nv_add_f32 v6, v6, v38\nN 108,172,176\nR 174,187,36864\n"
"s_waitcnt vmcnt(8)\nv_min3_u32 v236, v129, v131, v133\nv_add_f32 v7, v7, v39\ns_waitcnt vmcnt(7)\nv_min3_u32 v236, v236, v135, v137\nN 108,188,180\n"
"s_waitcnt vmcnt(6)\nv_min3_u32 v236, v236, v139, v141\nv_min3_u32 v236, v236, v143, v143\nG 7\nv_add_f32 v8, v8, v40\nv_max3_u32 v237, v129, v131, v133\n"
"N 80,160,160\nR 176,184,40960\nv_max3_u32 v237, v237, v135, v137\nv_add_f32 v9, v9, v41\nv_max3_u32 v237, v237, v139, v141\nN 80,176,164\n"
"buffer_load_dwordx4 v221, s[4:7], s63 offen offset:1024 lds\nG 7\nv_add_f32 v10, v10, v42\nv_max3_u32 v237, v237, v143, v143\nN 84,164,160\nR 178,184,45056\n"
"v_cmp_ne_u32 s[86:87], s46, v236\nv_add_f32 v11, v11, v43\nv_cmp_ne_u32 s[88:89], s46, v237\nN 84,180,164\ns_add_u32 m0, s50, 106496\ns_or_b64 s[86:87], s[86:87], s[88:89]\n"
"s_cbranch_scc1 Lq150\nLq151:\nG 6\nv_add_f32 v12, v12, v44\nv_add_f32 v128, v128, v136\nN 112,168,160\n"
"R 180,184,49152\nv_add_f32 v130, v130, v138\nv_add_f32 v13, v13, v45\nv_add_f32 v132, v132, v140\nN 112,184,164\ns_add_u32 s63, s52, 786432\n"
"v_add_f32 v134, v134, v142\nv_add_f32 v128, v128, v132\nG 4\nv_add_f32 v14, v14, v46\nv_add_f32 v130, v130, v134\nN 116,172,160\n"
"R 182,184,53248\nv_add_f32_dpp v128, v128, v128 row_ror:8 row_mask:0xf bank_mask:0xf\nv_add_f32 v15, v15, v47\nv_add_f32_dpp v130, v130, v130 row_ror:8 row_mask:0xf bank_mask:0xf\nN 116,188,164\nds_read2st64_b32 v[48:49], v225 offset0:16 offset1:17\n"
"G 5\nv_mov_b32 v132, v128\nv_mov_b32 v133, v130\ns_nop 0\nv_permlane32_swap_b32 v132, v128\nN 88,160,168\n"
"R 160,185,40960\nds_read2st64_b32 v[50:51], v225 offset0:18 offset1:19\nv_permlane32_swap_b32 v133, v130\nv_add_f32 v128, v128, v132\nN 88,176,172\nbuffer_load_dwordx4 v220, s[4:7], s63 offen lds\n"
"ds_read2st64_b32 v[52:53], v225 offset0:20 offset1:21\nv_add_f32 v130, v130, v133\nN 92,164,168\nR 162,185,45056\nds_read2st64_b32 v[54:55], v225 offset0:22 offset1:23\nv_mov_b32 v132, v128\n"
"v_mov_b32 v133, v130\nN 92,180,172\nds_read2st64_b32 v[56:57], v225 offset0:24 offset1:25\nv_permlane16_swap_b32 v132, v128\nv_permlane16_swap_b32 v133, v130\nv_add_f32 v128, v128, v132\n"
"N 120,168,168\nR 164,185,49152\nds_read2st64_b32 v[58:59], v225 offset0:26 offset1:27\nv_add_f32 v130, v130, v133\nv_and_b32 v136, s77, v128\nN 120,184,172\n"
"buffer_load_dwordx4 v221, s[4:7], s63 offen offset:1024 lds\nG 8\nv_add_f32 v16, v16, v48\nv_sub_f32 v137, v128, v136\nN 124,172,168\nR 166,185,53248\n"
"s_add_u32 s63, s52, 983040\nv_add_f32 v17, v17, v49\nv_cvt_pk_bf16_f32 v132, v136, v137\nN 124,188,172\nv_cvt_pk_bf16_f32 v133, v136, 0\nG 7\n"
"v_add_f32 v18, v18, v50\nv_add_f32 v19, v19, v51\nv_and_b32 v136, s77, v130\nN 64,192,176\nR 168,186,40960\nG 7\n"
"v_add_f32 v20, v20, v52\nv_add_f32 v21, v21, v53\nv_sub_f32 v137, v130, v136\nN 64,208,180\ns_add_u32 m0, s50, 114688\nds_read2st64_b32 v[60:61], v225 offset0:28 offset1:29\n"
"G 6\nv_add_f32 v22, v22, v54\nv_cvt_pk_bf16_f32 v134, v136, v137\nN 68,196,176\nR 170,186,45056\nv_add_f32 v23, v23, v55\n"
"G 6\nv_add_f32 v24, v24, v56\nN 68,212,180\nbuffer_load_dwordx4 v220, s[4:7], s63 offen lds\nv_add_f32 v25, v25, v57\nN 96,200,176\n"
"R 172,186,49152\nG 5\nv_add_f32 v26, v26, v58\nv_add_f32 v27, v27, v59\nN 96,216,180\nds_read2st64_b32 v[62:63], v225 offset0:30 offset1:31\n"
"v_cvt_pk_bf16_f32 v135, v136, 0\nN 100,204,176\nR 174,186,53248\nv_and_b32 v234, 0x1ff, v234\nN 100,220,180\nG 6\n"
"G 4\nv_add_f32 v28, v28, v60\nv_add_f32 v29, v29, v61\nG 1\nv_add_f32 v30, v30, v62\nv_add_f32 v31, v31, v63\n"
"N 72,192,160\nR 176,187,40960\nv_mul_f32 v16, v16, v217\nv_mul_f32 v17, v17, v217\nN 72,208,164\nbuffer_load_dwordx4 v221, s[4:7], s63 offen offset:1024 lds\n"
"v_mul_f32 v18, v18, v217\nN 76,196,160\nR 178,187,45056\ns_add_u32 s63, s52, 1179648\nv_mul_f32 v19, v19, v217\nN 76,212,164\n"
"s_add_u32 m0, s50, 0\nv_mul_f32 v20, v20, v217\nv_mul_f32 v21, v21, v217\nv_mul_f32 v22, v22, v217\nN 104,200,160\nR 180,187,49152\n"
"v_mul_f32 v23, v23, v217\nN 104,216,164\nbuffer_load_dwordx4 v220, s[4:7], s63 offen lds\nN 108,204,160\nR 182,187,53248\nv_mul_f32 v24, v24, v217\n"
"N 108,220,164\nv_add_u32 v234, 155648, v234\nds_write_b128 v234, v[132:135]\nG 5\nv_mul_f32 v25, v25, v217\nN 80,192,168\n"
"R 160,184,57344\nv_mul_f32 v26, v26, v217\nN 80,208,172\nv_mul_f32 v27, v27, v217\nv_mul_f32 v28, v28, v217\nN 84,196,168\n"
"R 162,184,61440\nv_mul_f32 v29, v29, v217\nN 84,212,172\nbuffer_load_dwordx4 v221, s[4:7], s63 offen offset:1024 lds\nN 112,200,168\nR 164,188,0\n"
"s_add_u32 s63, s52, 1376256\nN 112,216,172\ns_add_u32 m0, s50, 8192\nv_mul_f32 v30, v30, v217\nv_mul_f32 v31, v31, v217\nN 116,204,168\n"
"R 166,188,4096\nv_cvt_pk_bf16_f32 v16, v16, v17\nN 116,220,172\nG 5\nv_cvt_pk_bf16_f32 v17, v18, v19\nv_cvt_pk_bf16_f32 v18, v20, v21\n"
"N 88,192,176\nR 168,185,57344\nv_cvt_pk_bf16_f32 v19, v22, v23\nN 88,208,180\nbuffer_load_dwordx4 v220, s[4:7], s63 offen lds\nN 92,196,176\n"
"R 170,185,61440\nv_cvt_pk_bf16_f32 v20, v24, v25\nN 92,212,180\nv_cvt_pk_bf16_f32 v21, v26, v27\nv_cvt_pk_bf16_f32 v22, v28, v29\nN 120,200,176\n"
"R 172,189,0\nv_cvt_pk_bf16_f32 v23, v30, v31\nN 120,216,180\nbuffer_load_dwordx4 v221, s[4:7], s63 offen offset:1024 lds\nN 124,204,176\nR 174,189,4096\n"
"v_mul_f32 v0, v0, v214\nN 124,220,180\nG 4\nv_mul_f32 v1, v1, v214\nv_mul_f32 v2, v2, v214\nN 64,224,160\n"
"R 176,186,57344\ns_waitcnt vmcnt(8)\ns_barrier\nN 64,240,164\nv_mul_f32 v3, v3, v214\nv_mul_f32 v4, v4, v214\n"
"N 68,228,160\nR 178,186,61440\nv_mul_f32 v5, v5, v214\nN 68,244,164\nv_mul_f32 v6, v6, v214\nv_mul_f32 v7, v7, v214\n"
"N 96,232,160\nR 180,190,0\ns_mov_b32 s60, 156672\nv_bfe_u32 v246, v252, 4, 4\nv_lshl_add_u32 v246, v246, 3, s60\nN 96,248,164\n"
"s_bfe_u32 s100, s58, 0x20007\ns_mul_i32 s100, s100, 49152\ns_add_u32 s100, s100, s72\ns_add_u32 s100, s100, 52461568\nbuffer_store_dwordx4 v[248:251], v252, s[20:23], s100 offen offset:0 sc1\nds_read_b64 v[238:239], v246 offset:0\n"
"N 100,236,160\nR 182,190,4096\nds_read_b64 v[240:241], v246 offset:128\nN 100,252,164\nds_read_b64 v[242:243], v246 offset:256\nG 7\n"
"N 72,224,168\nR 160,187,57344\nds_read_b64 v[244:245], v246 offset:384\nN 72,240,172\nds_read_b64 v[246:247], v246 offset:0\ns_add_u32 m0, s50, 16384\n"
"N 76,228,168\nR 162,187,61440\ns_add_u32 s63, s52, 1572864\nN 76,244,172\nbuffer_load_dwordx4 v220, s[4:7], s63 offen lds\nN 104,232,168\n"
"R 164,191,0\nv_mul_f32 v8, v8, v214\nN 104,248,172\nG 6\nv_mfma_f32_16x16x32_bf16 v[64:67], v[208:211], v[238:241], v[64:67]\nv_mul_f32 v9, v9, v214\n"
"v_mul_f32 v10, v10, v214\nN 108,236,168\nR 166,191,4096\nG 6\nv_mfma_f32_16x16x32_bf16 v[72:75], v[208:211], v[240:243], v[72:75]\nv_mul_f32 v11, v11, v214\n"
"N 108,252,172\nG 4\nv_mfma_f32_16x16x32_bf16 v[80:83], v[208:211], v[242:245], v[80:83]\nv_mul_f32 v12, v12, v214\nv_mul_f32 v13, v13, v214\nN 80,224,176\n"
"R 168,188,8192\nG 4\nv_mfma_f32_16x16x32_bf16 v[88:91], v[208:211], v[244:247], v[88:91]\nv_mul_f32 v14, v14, v214\nN 80,240,180\nv_mfma_f32_16x16x32_bf16 v[68:71], v[210:213], v[244:247], v[68:71]\n"
"buffer_load_dwordx4 v221, s[4:7], s63 offen offset:1024 lds\nN 84,228,176\nR 170,188,12288\nv_mfma_f32_16x16x32_bf16 v[76:79], v[210:213], v[238:241], v[76:79]\ns_add_u32 s63, s52, 1769472\nN 84,244,180\n"
"v_mfma_f32_16x16x32_bf16 v[84:87], v[210:213], v[240:243], v[84:87]\ns_add_u32 m0, s50, 24576\nv_mul_f32 v15, v15, v214\nv_cvt_pk_bf16_f32 v0, v0, v1\nN 112,232,176\nR 172,188,16384\n"
"v_mfma_f32_16x16x32_bf16 v[92:95], v[210:213], v[242:245], v[92:95]\nv_cvt_pk_bf16_f32 v1, v2, v3\nN 112,248,180\nbuffer_load_dwordx4 v220, s[4:7], s63 offen lds\nN 116,236,176\nR 174,188,20480\n"
"v_cvt_pk_bf16_f32 v2, v4, v5\nN 116,252,180\nG 4\nv_cvt_pk_bf16_f32 v3, v6, v7\nv_cvt_pk_bf16_f32 v4, v8, v9\nN 88,224,160\n"
"R 176,189,8192\nv_cvt_pk_bf16_f32 v5, v10, v11\nN 88,240,164\nv_cvt_pk_bf16_f32 v6, v12, v13\nv_cvt_pk_bf16_f32 v7, v14, v15\nN 92,228,160\n"
"R 178,189,12288\nN 92,244,164\nbuffer_load_dwordx4 v221, s[4:7], s63 offen offset:1024 lds\nN 120,232,160\nR 180,189,16384\nN 120,248,164\n"
"N 124,236,160\nR 182,189,20480\nN 124,252,164\nG 4\nv_mfma_f32_16x16x32_bf16 v[128:131], v[168:171], a[0:3], 0\nR 160,190,8192\n"
"M 128,172,16\ns_mov_b32 m0, s51\nv_mfma_f32_16x16x32_bf16 v[144:147], v[168:171], a[4:7], 0\nR 162,190,12288\nM 144,172,20\nds_write_addtid_b32 v96 offset:0\n"
"v_mfma_f32_16x16x32_bf16 v[32:35], v[168:171], a[8:11], 0\nR 164,190,16384\nM 32,172,24\nds_write_addtid_b32 v97 offset:256\nv_mfma_f32_16x16x32_bf16 v[48:51], v[168:171], a[12:15], 0\nR 166,190,20480\n"
"M 48,172,28\nds_write_addtid_b32 v98 offset:512\nG 7\nv_mfma_f32_16x16x32_bf16 v[132:135], v[176:179], a[0:3], 0\nR 168,191,8192\nM 132,180,16\n"
"ds_write_addtid_b32 v99 offset:768\nv_mfma_f32_16x16x32_bf16 v[148:151], v[176:179], a[4:7], 0\nR 170,191,12288\nM 148,180,20\nds_write_addtid_b32 v100 offset:1024\nv_mfma_f32_16x16x32_bf16 v[36:39], v[176:179], a[8:11], 0\n"
"R 172,191,16384\nM 36,180,24\nds_write_addtid_b32 v101 offset:1280\nv_mfma_f32_16x16x32_bf16 v[52:55], v[176:179], a[12:15], 0\nR 174,191,20480\nM 52,180,28\n"
"ds_write_addtid_b32 v102 offset:1536\nds_write_addtid_b32 v103 offset:1792\nG 10\nv_mfma_f32_16x16x32_bf16 v[136:139], v[160:163], a[0:3], 0\nR 176,188,24576\nds_write_addtid_b32 v104 offset:2048\n"
"M 136,164,16\nds_write_addtid_b32 v105 offset:2304\nds_write_addtid_b32 v106 offset:2560\nv_mfma_f32_16x16x32_bf16 v[152:155], v[160:163], a[4:7], 0\nR 178,188,28672\nds_write_addtid_b32 v107 offset:2816\n"
"M 152,164,20\nds_write_addtid_b32 v108 offset:3072\nds_write_addtid_b32 v109 offset:3328\nv_mfma_f32_16x16x32_bf16 v[40:43], v[160:163], a[8:11], 0\nR 180,188,32768\nds_write_addtid_b32 v110 offset:3584\n"
"M 40,164,24\nds_write_addtid_b32 v111 offset:3840\nds_write_addtid_b32 v112 offset:4096\nv_mfma_f32_16x16x32_bf16 v[56:59], v[160:163], a[12:15], 0\nR 182,188,36864\nds_write_addtid_b32 v113 offset:4352\n"
"M 56,164,28\nds_write_addtid_b32 v114 offset:4608\nds_write_addtid_b32 v115 offset:4864\nG 15\nv_mfma_f32_16x16x32_bf16 v[140:143], v[168:171], a[0:3], 0\nR 160,189,24576\n"
"ds_write_addtid_b32 v116 offset:5120\nM 140,172,16\nds_write_addtid_b32 v117 offset:5376\nds_write_addtid_b32 v118 offset:5632\nv_mfma_f32_16x16x32_bf16 v[156:159], v[168:171], a[4:7], 0\nR 162,189,28672\n"
"ds_write_addtid_b32 v119 offset:5888\nM 156,172,20\nds_write_addtid_b32 v120 offset:6144\nds_write_addtid_b32 v121 offset:6400\nv_mfma_f32_16x16x32_bf16 v[44:47], v[168:171], a[8:11], 0\nR 164,189,32768\n"
"ds_write_addtid_b32 v122 offset:6656\nM 44,172,24\nds_write_addtid_b32 v123 offset:6912\nds_write_addtid_b32 v124 offset:7168\nv_mfma_f32_16x16x32_bf16 v[60:63], v[168:171], a[12:15], 0\nR 166,189,36864\n"
"ds_write_addtid_b32 v125 offset:7424\nM 60,172,28\nds_write_addtid_b32 v126 offset:7680\nds_write_addtid_b32 v127 offset:7936\nG 15\nM 128,176,32\n"
"R 168,190,24576\ns_add_u32 m0, s50, 32768\ns_add_u32 s63, s52, 1966080\nM 128,180,48\nbuffer_load_dwordx4 v220, s[4:7], s63 offen lds\nM 144,176,36\n"
"R 170,190,28672\ns_waitcnt vmcnt(10) lgkmcnt(2)\ns_barrier\nM 144,180,52\nds_read2st64_b32 v[96:97], v225 offset0:0 offset1:1\nds_read2st64_b32 v[98:99], v225 offset0:2 offset1:3\n"
"v_lshrrev_b32 v226, 8, v252\ns_lshl_b32 s60, s40, 7\ns_add_u32 s60, s60, 161280\nv_lshl_add_u32 v226, v226, 4, s60\nM 32,176,40\nR 172,190,32768\n"
"ds_read2st64_b32 v[100:101], v225 offset0:4 offset1:5\nds_read_b128 v[230:233], v226 offset:0\nM 32,180,56\nbuffer_store_dwordx4 v[248:251], v252, s[20:23], s100 offen offset:1024 sc1\nds_read2st64_b32 v[102:103], v225 offset0:6 offset1:7\nds_read_b128 v[234:237], v226 offset:64\n"
"M 48,176,44\nR 174,190,36864\nds_read2st64_b32 v[104:105], v225 offset0:8 offset1:9\nM 48,180,60\nds_read2st64_b32 v[106:107], v225 offset0:10 offset1:11\nds_read2st64_b32 v[108:109], v225 offset0:12 offset1:13\n"
"M 132,160,32\nR 176,191,24576\nds_read2st64_b32 v[110:111], v225 offset0:14 offset1:15\nM 132,164,48\nds_read2st64_b32 v[112:113], v225 offset0:16 offset1:17\nG 13\n"
"v_add_f32 v64, v64, v96\nv_add_f32 v65, v65, v97\nM 148,160,36\nR 178,191,28672\nG 13\nv_add_f32 v66, v66, v98\n"
"v_add_f32 v67, v67, v99\nM 148,164,52\nbuffer_load_dwordx4 v221, s[4:7], s63 offen offset:1024 lds\nG 11\nv_add_f32 v68, v68, v100\nM 36,160,40\n"
"R 180,191,32768\ns_add_u32 s63, s52, 2162688\nv_add_f32 v69, v69, v101\nM 36,164,56\ns_add_u32 m0, s50, 40960\nds_read2st64_b32 v[114:115], v225 offset0:18 offset1:19\n"
"G 11\nv_add_f32 v70, v70, v102\nM 52,160,44\nR 182,191,36864\nv_add_f32 v71, v71, v103\nG 9\n"
"v_add_f32 v72, v72, v104\nM 52,164,60\nds_read2st64_b32 v[116:117], v225 offset0:20 offset1:21\nv_add_f32 v73, v73, v105\nM 136,168,32\nR 160,188,40960\n"
"G 10\nv_add_f32 v74, v74, v106\nv_add_f32 v75, v75, v107\nM 136,172,48\nbuffer_load_dwordx4 v220, s[4:7], s63 offen lds\nG 9\n"
"v_add_f32 v76, v76, v108\nM 152,168,36\nR 162,188,45056\nv_add_f32 v77, v77, v109\nG 8\nv_add_f32 v78, v78, v110\n"
"M 152,172,52\nds_read2st64_b32 v[118:119], v225 offset0:22 offset1:23\nv_add_f32 v79, v79, v111\nM 40,168,40\nR 164,188,49152\nG 9\n"
"v_add_f32 v80, v80, v112\nM 40,172,56\nbuffer_load_dwordx4 v221, s[4:7], s63 offen offset:1024 lds\nM 56,168,44\nR 166,188,53248\ns_add_u32 s63, s52, 2359296\n"
"M 56,172,60\nds_read2st64_b32 v[120:121], v225 offset0:24 offset1:25\nG 7\nv_add_f32 v81, v81, v113\nM 140,176,32\nR 168,189,40960\n"
"v_add_f32 v82, v82, v114\nM 140,180,48\ns_add_u32 m0, s50, 49152\nds_read2st64_b32 v[122:123], v225 offset0:26 offset1:27\nv_add_f32 v83, v83, v115\nM 156,176,36\n"
"R 170,189,45056\nG 9\nv_add_f32 v84, v84, v116\nM 156,180,52\nbuffer_load_dwordx4 v220, s[4:7], s63 offen lds\nM 44,176,40\n"
"R 172,189,49152\nv_add_f32 v85, v85, v117\nM 44,180,56\nds_read2st64_b32 v[124:125], v225 offset0:28 offset1:29\nG 8\nv_add_f32 v86, v86, v118\n"
"M 60,176,44\nR 174,189,53248\nv_add_f32 v87, v87, v119\nM 60,180,60\nds_read2st64_b32 v[126:127], v225 offset0:30 offset1:31\nG 8\n"
"G 7\nv_add_f32 v88, v88, v120\nM 128,160,64\nR 176,190,40960\nv_add_f32 v89, v89, v121\nM 128,164,80\n"
"buffer_load_dwordx4 v221, s[4:7], s63 offen offset:1024 lds\nM 144,160,68\nR 178,190,45056\ns_add_u32 s63, s52, 2555904\nM 144,164,84\ns_add_u32 m0, s50, 57344\n"
"G 7\nv_add_f32 v90, v90, v122\nv_add_f32 v91, v91, v123\nM 32,160,72\nR 180,190,49152\nG 5\n"
"v_add_f32 v92, v92, v124\nM 32,164,88\nbuffer_load_dwordx4 v220, s[4:7], s63 offen lds\nM 48,160,76\nR 182,190,53248\nv_add_f32 v93, v93, v125\n"
"M 48,164,92\nG 5\nG 4\nv_add_f32 v94, v94, v126\nv_add_f32 v95, v95, v127\nM 132,168,64\n"
"R 160,191,40960\nv_mul_f32 v64, v64, v230\nv_mul_f32 v65, v65, v231\nM 132,172,80\ns_lshl4_add_u32 s64, s41, s42\nv_mul_f32 v66, v66, v232\n"
"v_mul_f32 v67, v67, v233\nv_mul_f32 v68, v68, v234\nv_add_u32 v108, 161792, v252\nM 148,168,68\nR 162,191,45056\nds_read_b128 v[112:115], v108\n"
"v_mul_f32 v69, v69, v235\nv_mul_f32 v70, v70, v236\nM 148,172,84\nbuffer_load_dwordx4 v221, s[4:7], s63 offen offset:1024 lds\nds_read_b128 v[104:107], v108 offset:1024\nv_mul_f32 v71, v71, v237\n"
"M 36,168,72\nR 164,191,49152\nv_cvt_pk_bf16_f32 v67, v66, v67\nv_cvt_pk_bf16_f32 v66, v64, v65\nM 36,172,88\ns_add_u32 m0, s50, 65536\n"
"s_cmp_eq_u32 s40, 3\ns_cselect_b64 s[66:67], -1, 0\nv_cvt_pk_bf16_f32 v68, v68, v69\nM 52,168,76\nR 166,191,53248\ns_waitcnt vmcnt(9)\n"
"s_barrier\nv_cvt_pk_bf16_f32 v69, v70, v71\nM 52,172,92\nv_mul_f32 v72, v72, v230\nv_mul_f32 v73, v73, v231\nG 6\n"
"v_mul_f32 v74, v74, v232\nM 136,176,64\nR 168,184,0\nv_mul_f32 v75, v75, v233\nv_mul_f32 v76, v76, v234\nv_mov_b32 v124, s76\n"
"M 136,180,80\ns_add_u32 s63, s52, 2752512\nv_mul_f32 v77, v77, v235\nv_mul_f32 v78, v78, v236\nv_mul_f32 v79, v79, v237\nG 3\n"
"v_lshlrev_b32 v100, 16, v104\nM 152,176,68\nR 170,184,4096\nv_cvt_pk_bf16_f32 v72, v72, v73\nv_cvt_pk_bf16_f32 v73, v74, v75\nv_and_b32 v101, s77, v104\n"
"M 152,180,84\nbuffer_load_dwordx4 v220, s[4:7], s63 offen lds\nv_cvt_pk_bf16_f32 v74, v76, v77\nv_lshlrev_b32 v102, 16, v105\nM 40,176,72\nR 172,184,8192\n"
"v_cvt_pk_bf16_f32 v75, v78, v79\nv_mul_f32 v80, v80, v230\nv_and_b32 v103, s77, v105\nM 40,180,88\ns_lshl_b32 s64, s64, 12\nv_mul_f32 v81, v81, v231\n"
"v_mul_f32 v82, v82, v232\nv_mul_f32 v83, v83, v233\nv_lshlrev_b32 v96, 16, v106\nM 56,176,76\nR 174,184,12288\nv_mul_f32 v84, v84, v234\n"
"v_mul_f32 v85, v85, v235\nv_and_b32 v97, s77, v106\nM 56,180,92\nG 4\nv_mul_f32 v86, v86, v236\nv_mul_f32 v87, v87, v237\n"
"v_cvt_pk_bf16_f32 v78, v80, v81\nv_lshlrev_b32 v98, 16, v107\nM 140,160,64\nR 176,185,0\nv_cvt_pk_bf16_f32 v79, v82, v83\nv_cvt_pk_bf16_f32 v80, v84, v85\n"
"v_and_b32 v99, s77, v107\nM 140,164,80\ns_add_u32 s64, s64, 77594624\nv_cvt_pk_bf16_f32 v81, v86, v87\nv_mul_f32 v88, v88, v230\nv_mul_f32 v89, v89, v231\n"
"v_add_f32 v104, s78, v100\nM 156,160,68\nR 178,185,4096\nv_mul_f32 v90, v90, v232\nv_mul_f32 v91, v91, v233\nv_add_f32 v105, s78, v101\n"
"M 156,164,84\nbuffer_load_dwordx4 v221, s[4:7], s63 offen offset:1024 lds\nv_mul_f32 v92, v92, v234\nv_add_f32 v106, s78, v102\nM 44,160,72\nR 180,185,8192\n"
"v_mul_f32 v93, v93, v235\nv_mul_f32 v94, v94, v236\nM 44,164,88\nv_mul_f32 v95, v95, v237\nv_cvt_pk_bf16_f32 v84, v88, v89\nv_cvt_pk_bf16_f32 v85, v90, v91\n"
"M 60,160,76\nR 182,185,12288\nv_cvt_pk_bf16_f32 v86, v92, v93\nv_cvt_pk_bf16_f32 v87, v94, v95\nM 60,164,92\ns_mov_b64 exec, s[66:67]\n"
"buffer_store_dwordx4 v[16:19], v252, s[20:23], s64 offen offset:0 sc1\nbuffer_store_dwordx4 v[20:23], v252, s[20:23], s64 offen offset:1024 sc1\ns_mov_b64 exec, -1\nG 4\nv_lshrrev_b32 v232, 1, v252\nv_add_f32 v107, s78, v103\n"
"M 128,168,96\nR 160,186,0\ns_mov_b64 exec, s[66:67]\nbuffer_store_dwordx2 v[68:69], v232, s[20:23], s64 offen offset:2048 sc1\nbuffer_store_dwordx2 v[74:75], v232, s[20:23], s64 offen offset:2560 sc1\ns_mov_b64 exec, -1\n"
"v_mov_b32 v125, s76\nM 128,172,112\ns_mov_b64 exec, s[66:67]\nbuffer_store_dwordx2 v[80:81], v232, s[20:23], s64 offen offset:3072 sc1\nbuffer_store_dwordx2 v[86:87], v232, s[20:23], s64 offen offset:3584 sc1\ns_mov_b64 exec, -1\n"
"v_mov_b32 v126, s76\nv_mov_b32 v127, s76\nM 144,168,100\nR 162,186,4096\ns_lshl_b32 s60, s40, 12\ns_add_u32 s60, s60, 118784\n"
"v_add_u32 v230, s60, v252\nv_add_u32 v231, s60, v232\nM 144,172,116\ns_add_u32 m0, s50, 73728\ns_not_b64 exec, s[66:67]\nds_write_b128 v230, v[16:19] offset:0\n"
"ds_write_b128 v230, v[20:23] offset:1024\ns_mov_b64 exec, -1\nM 32,168,104\nR 164,186,8192\ns_add_u32 s63, s52, 2949120\nM 32,172,120\n"
"buffer_store_dwordx4 v[248:251], v252, s[20:23], s100 offen offset:2048 sc1\nM 48,168,108\nR 166,186,12288\nM 48,172,124\nG 6\nM 132,176,96\n"
"R 168,187,0\nM 132,180,112\nbuffer_load_dwordx4 v220, s[4:7], s63 offen lds\nM 148,176,100\nR 170,187,4096\nM 148,180,116\n"
"M 36,176,104\nR 172,187,8192\nM 36,180,120\nM 52,176,108\nR 174,187,12288\nM 52,180,124\n"
"G 4\nM 136,160,96\nR 176,184,16384\nM 136,164,112\nbuffer_load_dwordx4 v221, s[4:7], s63 offen offset:1024 lds\nM 152,160,100\n"
"R 178,184,20480\nM 152,164,116\ns_not_b64 exec, s[66:67]\nds_write_b64 v231, v[68:69] offset:2048\nds_write_b64 v231, v[74:75] offset:2560\ns_mov_b64 exec, -1\n"
"M 40,160,104\nR 180,184,24576\nM 40,164,120\nM 56,160,108\nR 182,184,28672\nM 56,164,124\n"
"G 6\nM 140,168,96\nR 160,185,16384\nM 140,172,112\ns_movk_i32 s63, 2048\nv_mad_u32_u24 v220, v228, s63, v229\n"
"v_add_u32 v221, 1047552, v220\ns_add_u32 m0, s50, 98304\ns_add_u32 s63, s53, 0\nbuffer_load_dwordx4 v220, s[8:11], s63 offen lds\nM 156,168,100\nR 162,185,20480\n"
"s_not_b64 exec, s[66:67]\nds_write_b64 v231, v[80:81] offset:3072\nds_write_b64 v231, v[86:87] offset:3584\ns_mov_b64 exec, -1\nM 156,172,116\nM 44,168,104\n"
"R 164,185,24576\nM 44,172,120\nM 60,168,108\nR 166,185,28672\nM 60,172,124\nG 6\n"
"M 128,176,128\nR 168,186,16384\nM 128,180,144\nbuffer_load_dwordx4 v221, s[8:11], s63 offen offset:1024 lds\nM 144,176,132\nR 170,186,20480\n"
"M 144,180,148\ns_add_u32 m0, s50, 106496\nM 32,176,136\nR 172,186,24576\ns_waitcnt vmcnt(15) lgkmcnt(5)\ns_barrier\n"
"M 32,180,152\ns_add_u32 s63, s53, 65536\nv_mov_b32 v230, 0\nv_mov_b32 v231, 0\nM 48,176,140\nR 174,186,28672\n"
"v_mov_b32 v232, 0\nM 48,180,156\nG 4\nv_mov_b32 v233, 0\nv_mov_b32 v234, 0\nM 132,160,128\n"
"R 176,187,16384\nv_mov_b32 v235, 0\nM 132,164,144\nbuffer_load_dwordx4 v220, s[8:11], s63 offen lds\nM 148,160,132\nR 178,187,20480\n"
"v_mov_b32 v236, 0\nM 148,164,148\ns_lshl4_add_u32 s65, s41, s42\nv_mov_b32 v237, 0\nv_mov_b32 v64, 0\nM 36,160,136\n"
"R 180,187,24576\nv_mov_b32 v65, 0\nM 36,164,152\ns_lshl_b32 s65, s65, 12\nv_mov_b32 v70, 0\nv_mov_b32 v71, 0\n"
"M 52,160,140\nR 182,187,28672\nv_mov_b32 v76, 0\nM 52,164,156\nG 4\nv_mov_b32 v77, 0\n"
"v_mov_b32 v82, 0\nM 136,168,128\nR 160,184,32768\nv_mov_b32 v83, 0\nM 136,172,144\nbuffer_load_dwordx4 v221, s[8:11], s63 offen offset:1024 lds\n"
"M 152,168,132\nR 162,184,36864\nv_lshrrev_b32 v227, 1, v252\nM 152,172,148\ns_add_u32 s65, s65, 77529088\nM 40,168,136\n"
"R 164,184,40960\nLq193:\ns_lshr_b32 s60, s45, 3\ns_lshl2_add_u32 s60, s60, s40\ns_lshl_b32 s60, s60, 8\nv_lshl_add_u32 v224, v252, 3, s60\n"
"v_add_u32 v224, Lq193+4096-LqPC, v224\ns_mov_b64 exec, 3\nglobal_load_dword v224, v224, s[98:99]\ns_mov_b64 exec, -1\nM 40,172,152\ns_add_u32 m0, s50, 114688\n"
"M 56,168,140\nR 166,184,45056\nM 56,172,156\nG 4\nM 140,176,128\nR 168,185,32768\n"
"M 140,180,144\ns_add_u32 s63, s53, 131072\nM 156,176,132\nR 170,185,36864\nM 156,180,148\nbuffer_load_dwordx4 v220, s[8:11], s63 offen lds\n"
"M 44,176,136\nR 172,185,40960\nM 44,180,152\nM 60,176,140\nR 174,185,45056\nM 60,180,156\n"
"G 4\nM 128,160,160\nR 176,186,32768\nM 128,164,176\nbuffer_load_dwordx4 v221, s[8:11], s63 offen offset:1024 lds\nM 144,160,164\n"
"R 178,186,36864\nM 144,164,180\ns_lshl_b32 s60, s40, 12\ns_add_u32 s60, s60, 114688\nv_add_u32 v226, s60, v252\nv_add_u32 v111, s60, v227\n"
"M 32,160,168\nR 180,186,40960\ns_mov_b32 s60, 155648\nv_bfe_u32 v246, v252, 4, 4\nv_lshl_add_u32 v246, v246, 3, s60\nM 32,164,184\n"
"buffer_store_dwordx4 v[248:251], v252, s[20:23], s100 offen offset:3072 sc1\nM 48,160,172\nR 182,186,45056\nds_read_b64 v[238:239], v246 offset:0\nM 48,164,188\ns_add_u32 s63, s53, 196608\n"
"G 5\nM 132,168,160\nR 160,187,32768\nds_read_b64 v[240:241], v246 offset:128\nM 132,172,176\ns_add_u32 m0, s50, 0\n"
"s_cmp_eq_u32 s40, 0\ns_cselect_b64 s[88:89], 0, -1\ns_cselect_b32 s60, s41, 0\ns_cmp_lg_u32 s60, 0\ns_cselect_b64 s[86:87], -1, 0\nM 148,168,164\n"
"R 162,187,36864\nds_read_b64 v[242:243], v246 offset:256\nM 148,172,180\nbuffer_load_dwordx4 v220, s[8:11], s63 offen lds\nM 36,168,168\nR 164,187,40960\n"
"ds_read_b64 v[244:245], v246 offset:384\nM 36,172,184\ns_mov_b64 exec, s[88:89]\nds_read_b128 v[230:233], v226 offset:0\nds_read_b128 v[234:237], v226 offset:1024\ns_mov_b64 exec, -1\n"
"M 52,168,172\nR 166,187,45056\nds_read_b64 v[246:247], v246 offset:0\nM 52,172,188\nG 11\nM 136,176,160\n"
"R 168,184,49152\nM 136,180,176\ns_mov_b64 exec, s[88:89]\nds_read_b64 v[64:65], v111 offset:2048\nds_read_b64 v[70:71], v111 offset:2560\ns_mov_b64 exec, -1\n"
"M 152,176,164\nR 170,184,53248\ns_waitcnt vmcnt(9) lgkmcnt(1)\ns_barrier\nM 152,180,180\nbuffer_load_dwordx4 v221, s[8:11], s63 offen offset:1024 lds\n"
"M 40,176,168\nR 172,184,57344\nv_mfma_f32_16x16x32_bf16 v[128:131], v[238:241], v[208:211], v[128:131]\ns_add_u32 m0, s50, 8192\nM 40,180,184\ns_mov_b64 exec, s[88:89]\n"
"ds_read_b64 v[76:77], v111 offset:3072\nds_read_b64 v[82:83], v111 offset:3584\ns_mov_b64 exec, -1\nM 56,176,172\nR 174,184,61440\nv_mfma_f32_16x16x32_bf16 v[132:135], v[240:243], v[208:211], v[132:135]\n"
"s_add_u32 s63, s53, 262144\nM 56,180,188\nM 140,160,160\nR 176,185,49152\nv_mfma_f32_16x16x32_bf16 v[136:139], v[242:245], v[208:211], v[136:139]\nM 140,164,176\n"
"buffer_load_dwordx4 v220, s[8:11], s63 offen lds\nM 156,160,164\nR 178,185,53248\nv_mfma_f32_16x16x32_bf16 v[140:143], v[244:247], v[208:211], v[140:143]\nM 156,164,180\nM 44,160,168\n"
"R 180,185,57344\nv_mfma_f32_16x16x32_bf16 v[144:147], v[244:247], v[210:213], v[144:147]\nM 44,164,184\ns_mov_b64 exec, s[86:87]\nbuffer_load_dwordx4 v[230:233], v252, s[20:23], s65 offen offset:0 sc1\nbuffer_load_dwordx4 v[234:237], v252, s[20:23], s65 offen offset:1024 sc1\n"
"s_mov_b64 exec, -1\nM 60,160,172\nR 182,185,61440\nv_mfma_f32_16x16x32_bf16 v[148:151], v[238:241], v[210:213], v[148:151]\nM 60,164,188\nG 4\n"
"M 32,168,200\nR 160,186,49152\nv_mfma_f32_16x16x32_bf16 v[152:155], v[240:243], v[210:213], v[152:155]\nM 32,172,216\nR 162,186,53248\nM 48,168,204\n"
"R 164,186,57344\nv_mfma_f32_16x16x32_bf16 v[156:159], v[242:245], v[210:213], v[156:159]\ns_mov_b64 exec, s[86:87]\nbuffer_load_dwordx2 v[64:65], v227, s[20:23], s65 offen offset:2048 sc1\nbuffer_load_dwordx2 v[70:71], v227, s[20:23], s65 offen offset:2560 sc1\ns_mov_b64 exec, -1\n"
"M 48,172,220\nR 166,186,61440\nG 4\nM 36,176,200\nR 168,187,49152\nM 36,180,216\n"
"R 170,187,53248\nM 52,176,204\nR 172,187,57344\nM 52,180,220\nR 174,187,61440\nG 4\n"
"M 40,160,200\nR 176,188,0\ns_mov_b64 exec, s[86:87]\nbuffer_load_dwordx2 v[76:77], v227, s[20:23], s65 offen offset:3072 sc1\nbuffer_load_dwordx2 v[82:83], v227, s[20:23], s65 offen offset:3584 sc1\ns_mov_b64 exec, -1\n"
"M 40,164,216\nR 178,188,4096\nM 56,160,204\nR 180,188,8192\nM 56,164,220\nR 182,188,12288\n"
"G 4\nM 44,168,200\nR 160,189,0\nM 44,172,216\nR 162,189,4096\nM 60,168,204\n"
"R 164,189,8192\nM 60,172,220\nR 166,189,12288\nG 4\nM 32,176,232\nR 168,190,0\n"
"M 32,180,248\nR 170,190,4096\nM 48,176,236\nR 172,190,8192\nM 48,180,252\nR 174,190,12288\n"
"G 4\nM 36,160,232\nR 176,191,0\nM 36,164,248\nR 178,191,4096\nM 52,160,236\n"
"R 180,191,8192\nM 52,164,252\nR 182,191,12288\nG 4\nM 40,168,232\nR 160,184,49152\n"
"M 40,172,248\nR 162,184,53248\nM 56,168,236\nR 164,184,57344\nM 56,172,252\nR 166,184,61440\n"
"G 4\nM 44,176,232\nR 168,185,49152\nM 44,180,248\nR 170,185,53248\nM 60,176,236\n"
"R 172,185,57344\nM 60,180,252\nR 174,185,61440\nG 4\nM 128,160,192\nR 176,186,49152\n"
"M 128,164,208\nR 178,186,53248\ns_mov_b32 m0, s51\nM 144,160,196\nR 180,186,57344\nds_write_addtid_b32 v32 offset:0\n"
"M 144,164,212\nR 182,186,61440\nds_write_addtid_b32 v33 offset:256\nG 6\nM 132,168,192\nR 160,187,49152\n"
"ds_write_addtid_b32 v34 offset:512\nM 132,172,208\nR 162,187,53248\nds_write_addtid_b32 v35 offset:768\nM 148,168,196\nR 164,187,57344\n"
"ds_write_addtid_b32 v36 offset:1024\nM 148,172,212\nR 166,187,61440\nds_write_addtid_b32 v37 offset:1280\nG 9\nM 136,176,192\n"
"R 168,188,0\nds_write_addtid_b32 v38 offset:1536\nM 136,180,208\nR 170,188,4096\nds_write_addtid_b32 v39 offset:1792\nM 152,176,196\n"
"R 172,188,8192\nds_write_addtid_b32 v40 offset:2048\nM 152,180,212\nR 174,188,12288\nds_write_addtid_b32 v41 offset:2304\nG 9\n"
"M 140,160,192\nR 176,189,0\nds_write_addtid_b32 v42 offset:2560\nM 140,164,208\nR 178,189,4096\nds_write_addtid_b32 v43 offset:2816\n"
"M 156,160,196\nR 180,189,8192\nds_write_addtid_b32 v44 offset:3072\nds_write_addtid_b32 v45 offset:3328\nds_write_addtid_b32 v46 offset:3584\nds_write_addtid_b32 v47 offset:3840\n"
"ds_write_addtid_b32 v48 offset:4096\nds_write_addtid_b32 v49 offset:4352\nds_write_addtid_b32 v50 offset:4608\nds_write_addtid_b32 v51 offset:4864\nds_write_addtid_b32 v52 offset:5120\nds_write_addtid_b32 v53 offset:5376\n"
"ds_write_addtid_b32 v54 offset:5632\nds_write_addtid_b32 v55 offset:5888\nds_write_addtid_b32 v56 offset:6144\nds_write_addtid_b32 v57 offset:6400\nds_write_addtid_b32 v58 offset:6656\nds_write_addtid_b32 v59 offset:6912\n"
"ds_write_addtid_b32 v60 offset:7168\nds_write_addtid_b32 v61 offset:7424\nds_write_addtid_b32 v62 offset:7680\nds_write_addtid_b32 v63 offset:7936\nM 156,164,212\nR 182,189,12288\n"
"s_add_u32 m0, s50, 8192\ns_nop 0\nbuffer_load_dwordx4 v221, s[8:11], s63 offen offset:1024 lds\nG 15\nM 128,168,224\nR 160,190,0\n"
"s_add_u32 m0, s50, 16384\ns_add_u32 s63, s53, 327680\nbuffer_load_dwordx4 v220, s[8:11], s63 offen lds\nM 128,172,240\nR 162,190,4096\nG 3\n"
"s_barrier\nM 144,168,228\nR 164,190,8192\nbuffer_load_dwordx4 v221, s[8:11], s63 offen offset:1024 lds\nM 144,172,244\nR 166,190,12288\n"
"G 4\nM 132,176,224\nR 168,191,0\nM 132,180,240\nR 170,191,4096\nds_read2st64_b32 v[32:33], v225 offset0:0 offset1:1\n"
"M 148,176,228\nR 172,191,8192\nds_read2st64_b32 v[34:35], v225 offset0:2 offset1:3\nM 148,180,244\nR 174,191,12288\nG 6\n"
"M 136,160,224\ns_add_u32 m0, s50, 24576\nds_read2st64_b32 v[36:37], v225 offset0:4 offset1:5\nds_read2st64_b32 v[38:39], v225 offset0:6 offset1:7\nM 136,164,240\ns_add_u32 s63, s53, 393216\n"
"ds_read2st64_b32 v[40:41], v225 offset0:8 offset1:9\nds_read2st64_b32 v[42:43], v225 offset0:10 offset1:11\nM 152,160,228\nds_read2st64_b32 v[44:45], v225 offset0:12 offset1:13\nds_read2st64_b32 v[46:47], v225 offset0:14 offset1:15\nM 152,164,244\n"
"ds_read2st64_b32 v[48:49], v225 offset0:16 offset1:17\nds_read2st64_b32 v[50:51], v225 offset0:18 offset1:19\nG 8\nM 140,168,224\nbuffer_load_dwordx4 v220, s[8:11], s63 offen lds\nM 140,172,240\n"
"ds_read2st64_b32 v[52:53], v225 offset0:20 offset1:21\nds_read2st64_b32 v[54:55], v225 offset0:22 offset1:23\nM 156,168,228\nds_read2st64_b32 v[56:57], v225 offset0:24 offset1:25\nds_read2st64_b32 v[58:59], v225 offset0:26 offset1:27\nM 156,172,244\n"
"ds_read2st64_b32 v[60:61], v225 offset0:28 offset1:29\nds_read2st64_b32 v[62:63], v225 offset0:30 offset1:31\ns_waitcnt vmcnt(7)\nv_max3_u32 v248, v233, v237, v65\ns_waitcnt vmcnt(5)\nv_max3_u32 v248, v248, v71, v77\n"
"s_waitcnt vmcnt(4)\nv_max_u32 v248, v248, v83\nv_cmp_eq_u32 vcc, -1, v248\ns_nop 1\ns_cbranch_vccnz Lq169\nLq170:\n"
"G 7\nv_pk_add_f32 v[144:145], v[144:145], v[48:49]\nG 6\nv_pk_add_f32 v[146:147], v[146:147], v[50:51]\nG 5\nv_pk_add_f32 v[148:149], v[148:149], v[52:53]\n"
"G 4\nv_pk_add_f32 v[150:151], v[150:151], v[54:55]\nv_cvt_pk_bf16_f32 v144, v144, v145\nv_cvt_pk_bf16_f32 v145, v146, v147\nv_cvt_pk_bf16_f32 v146, v148, v149\nv_cvt_pk_bf16_f32 v147, v150, v151\n"
"s_nop 1\nv_mfma_f32_16x16x32_bf16 v[52:55], v[16:19], v[144:147], v[96:99]\nv_mfma_f32_16x16x32_bf16 v[48:51], v[0:3], v[144:147], v[100:103]\nG 3\nv_pk_add_f32 v[152:153], v[152:153], v[56:57]\nG 2\n"
"v_pk_add_f32 v[154:155], v[154:155], v[58:59]\nG 1\nv_pk_add_f32 v[156:157], v[156:157], v[60:61]\nG 0\nv_pk_add_f32 v[158:159], v[158:159], v[62:63]\nv_cvt_pk_bf16_f32 v152, v152, v153\n"
"v_cvt_pk_bf16_f32 v153, v154, v155\nv_cvt_pk_bf16_f32 v154, v156, v157\nv_cvt_pk_bf16_f32 v155, v158, v159\ns_nop 1\nv_mfma_f32_16x16x32_bf16 v[60:63], v[20:23], v[152:155], v[96:99]\nv_mfma_f32_16x16x32_bf16 v[56:59], v[4:7], v[152:155], v[100:103]\n"
"v_pk_add_f32 v[128:129], v[128:129], v[32:33]\nv_pk_add_f32 v[130:131], v[130:131], v[34:35]\nv_pk_add_f32 v[132:133], v[132:133], v[36:37]\nv_pk_add_f32 v[134:135], v[134:135], v[38:39]\nv_cvt_pk_bf16_f32 v128, v128, v129\nv_cvt_pk_bf16_f32 v129, v130, v131\n"
"v_cvt_pk_bf16_f32 v130, v132, v133\nv_cvt_pk_bf16_f32 v131, v134, v135\ns_nop 1\nv_mfma_f32_16x16x32_bf16 v[36:39], v[0:3], v[128:131], v[96:99]\nv_mfma_f32_16x16x32_bf16 v[32:35], v[230:233], v[128:131], v[104:107]\nv_pk_add_f32 v[136:137], v[136:137], v[40:41]\n"
"v_pk_add_f32 v[138:139], v[138:139], v[42:43]\nv_pk_add_f32 v[140:141], v[140:141], v[44:45]\nv_pk_add_f32 v[142:143], v[142:143], v[46:47]\nv_cvt_pk_bf16_f32 v136, v136, v137\nv_cvt_pk_bf16_f32 v137, v138, v139\nv_cvt_pk_bf16_f32 v138, v140, v141\n"
"v_cvt_pk_bf16_f32 v139, v142, v143\ns_nop 1\nv_mfma_f32_16x16x32_bf16 v[44:47], v[4:7], v[136:139], v[96:99]\nv_mfma_f32_16x16x32_bf16 v[40:43], v[234:237], v[136:139], v[104:107]\nv_max3_f32 v52, v52, v53, v48\nv_max3_f32 v60, v60, v61, v56\n"
"v_max3_f32 v36, v36, v37, v32\ns_nop 4\nv_max3_f32 v44, v44, v45, v40\nv_max_f32 v52, v52, v49\nv_max_f32 v60, v60, v57\nv_max_f32 v36, v36, v33\n"
"v_max_f32 v44, v44, v41\nv_max3_f32 v54, v54, v55, v50\nv_max3_f32 v62, v62, v63, v58\nv_max3_f32 v38, v38, v39, v34\nv_max3_f32 v46, v46, v47, v42\nv_max_f32 v54, v54, v51\n"
"v_max_f32 v62, v62, v59\nv_max_f32 v38, v38, v35\nv_max_f32 v46, v46, v43\nv_max_f32 v118, v52, v54\nv_max_f32 v119, v60, v62\nv_max_f32 v116, v36, v38\n"
"v_max_f32 v117, v44, v46\nv_mov_b32 v120, v116\nv_mov_b32 v121, v117\nv_mov_b32 v122, v118\nv_mov_b32 v123, v119\nv_permlane32_swap_b32 v120, v116\n"
"v_permlane32_swap_b32 v121, v117\nv_permlane32_swap_b32 v122, v118\nv_permlane32_swap_b32 v123, v119\nv_max_f32 v116, v116, v120\nv_max_f32 v117, v117, v121\nv_max_f32 v118, v118, v122\n"
"v_max_f32 v119, v119, v123\nv_mov_b32 v120, v116\nv_mov_b32 v121, v117\nv_mov_b32 v122, v118\nv_mov_b32 v123, v119\nv_permlane16_swap_b32 v120, v116\n"
"v_permlane16_swap_b32 v121, v117\nv_permlane16_swap_b32 v122, v118\nv_permlane16_swap_b32 v123, v119\nv_max_f32 v116, v116, v120\nv_max_f32 v117, v117, v121\nv_max_f32 v118, v118, v122\n"
"v_max_f32 v119, v119, v123\nv_mul_f32 v118, v118, v218\nv_mul_f32 v119, v119, v218\nv_mul_f32 v116, v116, v215\nv_mul_f32 v117, v117, v215\nv_fma_f32 v52, v52, v218, -v118\n"
"v_fma_f32 v54, v54, v218, -v118\nv_fma_f32 v60, v60, v218, -v119\nv_fma_f32 v62, v62, v218, -v119\nv_fma_f32 v36, v36, v215, -v116\nv_fma_f32 v38, v38, v215, -v116\nv_fma_f32 v44, v44, v215, -v117\n"
"v_fma_f32 v46, v46, v215, -v117\nv_exp_f32 v52, v52\nv_exp_f32 v54, v54\nv_exp_f32 v60, v60\nv_exp_f32 v62, v62\nv_exp_f32 v36, v36\n"
"v_exp_f32 v38, v38\nv_exp_f32 v44, v44\nv_exp_f32 v46, v46\nv_cvt_pk_bf16_f32 v52, v52, v52\nv_cvt_pk_bf16_f32 v54, v54, v54\nv_and_b32 v48, v52, v112\n"
"v_and_b32 v49, v54, v113\nv_and_b32 v50, v52, v114\nv_and_b32 v51, v54, v115\ns_nop 1\nv_mfma_f32_16x16x32_bf16 v[24:27], v[124:127], v[48:51], 0\nv_mfma_f32_16x16x32_bf16 v[144:147], v[66:69], v[48:51], 0\n"
"v_mfma_f32_16x16x32_bf16 v[148:151], v[72:75], v[48:51], 0\nv_cvt_pk_bf16_f32 v60, v60, v60\nv_cvt_pk_bf16_f32 v62, v62, v62\nv_and_b32 v56, v60, v112\nv_and_b32 v57, v62, v113\nv_and_b32 v58, v60, v114\n"
"v_and_b32 v59, v62, v115\ns_nop 1\nv_mfma_f32_16x16x32_bf16 v[28:31], v[124:127], v[56:59], 0\nv_mfma_f32_16x16x32_bf16 v[152:155], v[78:81], v[56:59], 0\nv_mfma_f32_16x16x32_bf16 v[156:159], v[84:87], v[56:59], 0\nv_cvt_pk_bf16_f32 v36, v36, v36\n"
"v_cvt_pk_bf16_f32 v38, v38, v38\nv_and_b32 v32, v36, v112\nv_and_b32 v33, v38, v113\nv_and_b32 v34, v36, v114\nv_and_b32 v35, v38, v115\ns_nop 1\n"
"v_mfma_f32_16x16x32_bf16 v[8:11], v[124:127], v[32:35], 0\nv_mfma_f32_16x16x32_bf16 v[128:131], v[64:67], v[32:35], 0\nv_mfma_f32_16x16x32_bf16 v[132:135], v[70:73], v[32:35], 0\nv_cvt_pk_bf16_f32 v44, v44, v44\nv_cvt_pk_bf16_f32 v46, v46, v46\nv_and_b32 v40, v44, v112\n"
"v_and_b32 v41, v46, v113\nv_and_b32 v42, v44, v114\nv_and_b32 v43, v46, v115\ns_nop 1\nv_mfma_f32_16x16x32_bf16 v[12:15], v[124:127], v[40:43], 0\nv_mfma_f32_16x16x32_bf16 v[136:139], v[76:79], v[40:43], 0\n"
"v_mfma_f32_16x16x32_bf16 v[140:143], v[82:85], v[40:43], 0\ns_lshl_b32 s84, s42, 11\ns_add_u32 s84, s84, s49\ns_add_u32 s84, s84, 2097152\ns_add_u32 s85, s84, 32768\nv_mov_b32 v238, s46\n"
"v_mov_b32 v239, s47\nv_rcp_f32 v24, v24\nv_rcp_f32 v28, v28\nv_rcp_f32 v8, v8\nv_rcp_f32 v12, v12\nv_pk_mul_f32 v[144:145], v[144:145], v[24:25] op_sel_hi:[1,0]\n"
"v_pk_mul_f32 v[146:147], v[146:147], v[24:25] op_sel_hi:[1,0]\nv_pk_mul_f32 v[148:149], v[148:149], v[24:25] op_sel_hi:[1,0]\nv_pk_mul_f32 v[150:151], v[150:151], v[24:25] op_sel_hi:[1,0]\nv_cvt_pk_bf16_f32 v144, v144, v145\nv_cvt_pk_bf16_f32 v145, v146, v147\nv_cvt_pk_bf16_f32 v146, v148, v149\n"
"v_cvt_pk_bf16_f32 v147, v150, v151\nbuffer_store_dwordx4 v[144:147], v252, s[20:23], s85 offen offset:0 sc1\nv_pk_mul_f32 v[152:153], v[152:153], v[28:29] op_sel_hi:[1,0]\nv_pk_mul_f32 v[154:155], v[154:155], v[28:29] op_sel_hi:[1,0]\nv_pk_mul_f32 v[156:157], v[156:157], v[28:29] op_sel_hi:[1,0]\nv_pk_mul_f32 v[158:159], v[158:159], v[28:29] op_sel_hi:[1,0]\n"
"v_cvt_pk_bf16_f32 v152, v152, v153\nv_cvt_pk_bf16_f32 v153, v154, v155\nv_cvt_pk_bf16_f32 v154, v156, v157\nv_cvt_pk_bf16_f32 v155, v158, v159\nbuffer_store_dwordx4 v[152:155], v252, s[20:23], s85 offen offset:1024 sc1\nv_pk_mul_f32 v[128:129], v[128:129], v[8:9] op_sel_hi:[1,0]\n"
"v_pk_mul_f32 v[130:131], v[130:131], v[8:9] op_sel_hi:[1,0]\nv_pk_mul_f32 v[132:133], v[132:133], v[8:9] op_sel_hi:[1,0]\nv_pk_mul_f32 v[134:135], v[134:135], v[8:9] op_sel_hi:[1,0]\nv_cvt_pk_bf16_f32 v128, v128, v129\nv_cvt_pk_bf16_f32 v129, v130, v131\nv_cvt_pk_bf16_f32 v130, v132, v133\n"
"v_cvt_pk_bf16_f32 v131, v134, v135\nbuffer_store_dwordx4 v[128:131], v252, s[20:23], s84 offen offset:0 sc1\nv_pk_mul_f32 v[136:137], v[136:137], v[12:13] op_sel_hi:[1,0]\nv_pk_mul_f32 v[138:139], v[138:139], v[12:13] op_sel_hi:[1,0]\nv_pk_mul_f32 v[140:141], v[140:141], v[12:13] op_sel_hi:[1,0]\nv_pk_mul_f32 v[142:143], v[142:143], v[12:13] op_sel_hi:[1,0]\n"
"v_cvt_pk_bf16_f32 v136, v136, v137\nv_cvt_pk_bf16_f32 v137, v138, v139\nv_cvt_pk_bf16_f32 v138, v140, v141\nv_cvt_pk_bf16_f32 v139, v142, v143\nbuffer_store_dwordx4 v[136:139], v252, s[20:23], s84 offen offset:1024 sc1\ns_add_u32 s61, s79, 88080384\n"
"s_lshl_b32 s62, s42, 3\ns_add_u32 s61, s61, s62\ns_waitcnt vmcnt(0)\ns_mov_b64 exec, 1\nbuffer_atomic_swap_x2 v[238:239], off, s[20:23], s61 sc1\ns_mov_b64 exec, -1\n"
"s_barrier\ns_mul_i32 s61, s41, 229376\ns_add_u32 s61, s61, s54\ns_lshr_b32 s62, s41, 1\ns_mul_i32 s62, s62, 458752\ns_add_u32 s62, s62, s55\n"
"s_movk_i32 s60, 7168\nv_mad_u32_u24 v230, v228, s60, v229\nv_add_u32 v231, 3668992, v230\ns_add_u32 m0, s50, -32768\ns_add_u32 s60, s61, 0\nbuffer_load_dwordx4 v230, s[12:15], s60 offen offset:0 lds\n"
"buffer_load_dwordx4 v231, s[12:15], s60 offen offset:1024 lds\ns_add_u32 m0, s50, -24576\ns_add_u32 s60, s61, 128\nbuffer_load_dwordx4 v230, s[12:15], s60 offen offset:0 lds\nbuffer_load_dwordx4 v231, s[12:15], s60 offen offset:1024 lds\ns_add_u32 m0, s50, -16384\n"
"s_add_u32 s60, s61, 256\nbuffer_load_dwordx4 v230, s[12:15], s60 offen offset:0 lds\nbuffer_load_dwordx4 v231, s[12:15], s60 offen offset:1024 lds\ns_add_u32 m0, s50, -8192\ns_add_u32 s60, s62, 0\nbuffer_load_dwordx4 v222, s[12:15], s60 offen offset:0 lds\n"
"buffer_load_dwordx4 v223, s[12:15], s60 offen offset:1024 lds\nv_bfe_u32 v250, v252, 7, 1\nv_mul_u32_u24 v250, 2176, v250\nv_bfe_u32 v251, v252, 4, 3\nv_lshl_add_u32 v250, v251, 3, v250\ns_lshl_b32 s60, s90, 6\n"
"v_add_u32 v250, s60, v250\ns_add_u32 m0, s50, 24576\ns_nop 0\nbuffer_load_dwordx4 v221, s[8:11], s63 offen offset:1024 lds\ns_add_u32 m0, s50, 32768\ns_add_u32 s63, s53, 458752\n"
"buffer_load_dwordx4 v220, s[8:11], s63 offen lds\nbuffer_load_dwordx4 v221, s[8:11], s63 offen offset:1024 lds\ns_add_u32 m0, s50, 40960\ns_add_u32 s63, s53, 524288\nbuffer_load_dwordx4 v220, s[8:11], s63 offen lds\nbuffer_load_dwordx4 v221, s[8:11], s63 offen offset:1024 lds\n"
"s_add_u32 m0, s50, 49152\ns_add_u32 s63, s53, 589824\nbuffer_load_dwordx4 v220, s[8:11], s63 offen lds\nbuffer_load_dwordx4 v221, s[8:11], s63 offen offset:1024 lds\ns_add_u32 m0, s50, 57344\ns_add_u32 s63, s53, 655360\n"
"buffer_load_dwordx4 v220, s[8:11], s63 offen lds\nbuffer_load_dwordx4 v221, s[8:11], s63 offen offset:1024 lds\ns_add_u32 m0, s50, 65536\ns_add_u32 s63, s53, 720896\nbuffer_load_dwordx4 v220, s[8:11], s63 offen lds\nv_mov_b32 v248, 0\n"
"v_mov_b32 v249, 0\ns_add_u32 s61, s80, 88080384\ns_mov_b64 exec, 0xffff\nbuffer_atomic_or_x2 v[248:249], v250, s[20:23], s61 offen sc0 sc1\ns_mov_b64 exec, -1\nv_bfe_u32 v234, v252, 4, 4\n"
"v_lshrrev_b32 v235, 8, v252\nv_lshlrev_b32 v235, 2, v235\nv_sub_u32 v234, v234, v235\nv_lshlrev_b32 v235, 4, v234\ns_mov_b64 s[60:61], 0x3f80\nv_lshlrev_b64 v[208:209], v235, s[60:61]\n"
"v_cmp_gt_u32 s[60:61], 4, v234\ns_nop 1\nv_cndmask_b32 v212, 0, v208, s[60:61]\nv_cndmask_b32 v208, 0, v208, s[60:61]\nv_cndmask_b32 v213, 0, v209, s[60:61]\nv_cndmask_b32 v209, 0, v209, s[60:61]\n"
"s_nop 1\nv_mfma_f32_16x16x32_bf16 v[0:3], v[208:211], v[192:195], 0\nv_mfma_f32_16x16x32_bf16 v[4:7], v[210:213], v[192:195], 0\nv_mfma_f32_16x16x32_bf16 v[8:11], v[208:211], v[196:199], 0\nv_mfma_f32_16x16x32_bf16 v[12:15], v[210:213], v[196:199], 0\nv_mfma_f32_16x16x32_bf16 v[16:19], v[208:211], v[200:203], 0\n"
"v_mfma_f32_16x16x32_bf16 v[20:23], v[210:213], v[200:203], 0\nv_mfma_f32_16x16x32_bf16 v[24:27], v[208:211], v[204:207], 0\nv_mfma_f32_16x16x32_bf16 v[28:31], v[210:213], v[204:207], 0\ns_waitcnt vmcnt(0)\nv_cmp_eq_u64 s[86:87], s[46:47], v[248:249]\ns_nop 0\n"
"s_and_b32 s62, s86, 0xffff\ns_cmp_eq_u32 s62, 0xffff\ns_cbranch_scc0 Lq196\nLq197:\ns_add_u32 s64, s70, 2097152\nbuffer_load_dwordx4 a[0:3], v252, s[20:23], s64 offen offset:0 sc1\n"
"s_add_u32 s65, s70, 2129920\nbuffer_load_dwordx4 a[4:7], v252, s[20:23], s65 offen offset:0 sc1\ns_add_u32 s66, s71, 2097152\nbuffer_load_dwordx4 a[8:11], v252, s[20:23], s66 offen offset:0 sc1\ns_add_u32 s67, s71, 2129920\nbuffer_load_dwordx4 a[12:15], v252, s[20:23], s67 offen offset:0 sc1\n"
"buffer_load_dwordx4 a[16:19], v252, s[20:23], s64 offen offset:1024 sc1\nbuffer_load_dwordx4 a[20:23], v252, s[20:23], s65 offen offset:1024 sc1\nbuffer_load_dwordx4 a[24:27], v252, s[20:23], s66 offen offset:1024 sc1\nbuffer_load_dwordx4 a[28:31], v252, s[20:23], s67 offen offset:1024 sc1\ns_barrier\nR 160,188,32768\n"
"R 162,188,36864\nR 164,188,40960\nR 166,188,45056\nR 168,189,32768\nR 170,189,36864\nR 172,189,40960\n"
"R 174,189,45056\nR 176,190,32768\nR 178,190,36864\nR 180,190,40960\nR 182,190,45056\nbuffer_load_dwordx4 a[32:35], v252, s[20:23], s64 offen offset:2048 sc1\n"
"buffer_load_dwordx4 a[36:39], v252, s[20:23], s65 offen offset:2048 sc1\nbuffer_load_dwordx4 a[40:43], v252, s[20:23], s66 offen offset:2048 sc1\nbuffer_load_dwordx4 a[44:47], v252, s[20:23], s67 offen offset:2048 sc1\nbuffer_load_dwordx4 a[48:51], v252, s[20:23], s64 offen offset:3072 sc1\nbuffer_load_dwordx4 a[52:55], v252, s[20:23], s65 offen offset:3072 sc1\nbuffer_load_dwordx4 a[56:59], v252, s[20:23], s66 offen offset:3072 sc1\n"
"buffer_load_dwordx4 a[60:63], v252, s[20:23], s67 offen offset:3072 sc1\ns_add_u32 s64, s70, 2101248\nbuffer_load_dwordx4 a[64:67], v252, s[20:23], s64 offen offset:0 sc1\ns_add_u32 s65, s70, 2134016\nbuffer_load_dwordx4 a[68:71], v252, s[20:23], s65 offen offset:0 sc1\ns_add_u32 s66, s71, 2101248\n"
"buffer_load_dwordx4 a[72:75], v252, s[20:23], s66 offen offset:0 sc1\ns_add_u32 s67, s71, 2134016\nbuffer_load_dwordx4 a[76:79], v252, s[20:23], s67 offen offset:0 sc1\ns_waitcnt vmcnt(19) lgkmcnt(10)\nM 0,160,0\nbuffer_load_dwordx4 a[80:83], v252, s[20:23], s64 offen offset:1024 sc1\n"
"s_waitcnt vmcnt(16) lgkmcnt(8)\nM 0,164,16\nM 16,160,4\ns_waitcnt vmcnt(15)\nM 16,164,20\nv_mfma_f32_16x16x32_bf16 v[32:35], v[160:163], a[8:11], 0\n"
"buffer_load_dwordx4 a[84:87], v252, s[20:23], s65 offen offset:1024 sc1\nv_mov_b32 v242, s76\ns_waitcnt vmcnt(15)\nM 32,164,24\ns_and_b32 s60, s40, 2\ns_lshl_b32 s60, s60, 5\n"
"v_xor_b32 v238, s60, v184\nv_mfma_f32_16x16x32_bf16 v[48:51], v[160:163], a[12:15], 0\nv_xor_b32 v239, 32, v238\nv_add_u32 v238, -32768, v238\ns_waitcnt vmcnt(14)\nM 48,164,28\n"
"G 4\nv_add_u32 v239, -32768, v239\nv_mov_b32 v243, s76\nM 4,168,0\nR 160,191,32768\nv_mov_b32 v244, s76\n"
"M 4,172,16\nbuffer_load_dwordx4 a[88:91], v252, s[20:23], s66 offen offset:1024 sc1\nM 20,168,4\nR 162,191,36864\nv_mov_b32 v245, s76\nM 20,172,20\n"
"s_and_b32 s60, s41, 1\ns_lshl_b32 s60, s60, 1\ns_lshr_b32 s61, s40, 1\ns_add_u32 s60, s60, s61\ns_lshl_b32 s60, s60, 5\nv_xor_b32 v240, s60, v184\n"
"v_mfma_f32_16x16x32_bf16 v[36:39], v[168:171], a[8:11], 0\nR 164,191,40960\nv_add_u32 v240, -32768, v240\nM 36,172,24\nbuffer_load_dwordx4 a[92:95], v252, s[20:23], s67 offen offset:1024 sc1\nv_mfma_f32_16x16x32_bf16 v[52:55], v[168:171], a[12:15], 0\n"
"R 166,191,45056\nM 52,172,28\nG 4\nM 8,176,0\nR 168,188,49152\nR 64,238,0\n"
"R 66,238,4096\nM 8,180,16\nR 68,239,0\nR 70,239,4096\nM 24,176,4\nR 170,188,53248\n"
"M 24,180,20\nbuffer_load_dwordx4 a[96:99], v252, s[20:23], s64 offen offset:2048 sc1\nv_mfma_f32_16x16x32_bf16 v[40:43], v[176:179], a[8:11], 0\nR 172,184,0\nM 40,180,24\nbuffer_load_dwordx4 a[100:103], v252, s[20:23], s65 offen offset:2048 sc1\n"
"v_mfma_f32_16x16x32_bf16 v[56:59], v[176:179], a[12:15], 0\nR 174,184,4096\nG 5\nv_mfma_f32_16x16x32_bf16 v[72:75], v[242:245], v[64:67], 0\nM 56,180,28\nM 12,160,0\n"
"R 176,189,49152\nG 4\nv_mfma_f32_16x16x32_bf16 v[76:79], v[242:245], v[68:71], 0\nM 12,164,16\nbuffer_load_dwordx4 a[104:107], v252, s[20:23], s66 offen offset:2048 sc1\nM 28,160,4\n"
"R 178,189,53248\nM 28,164,20\nv_mfma_f32_16x16x32_bf16 v[44:47], v[160:163], a[8:11], 0\nR 180,185,0\nM 44,164,24\nbuffer_load_dwordx4 a[108:111], v252, s[20:23], s67 offen offset:2048 sc1\n"
"v_mfma_f32_16x16x32_bf16 v[60:63], v[160:163], a[12:15], 0\nR 182,185,4096\nv_lshrrev_b32 v64, 1, v252\nv_mov_b32 v67, s46\nM 60,164,28\ns_mov_b64 s[60:61], 0xffff\n"
"v_cndmask_b32 v66, v76, v72, s[60:61]\nG 4\ns_waitcnt vmcnt(19)\nM 0,168,32\nR 160,190,49152\ns_lshr_b32 s61, s40, 1\n"
"s_lshl_b32 s61, s61, 8\ns_add_u32 s61, s61, s91\ns_add_u32 s61, s61, 786432\ns_bfm_b64 exec, 32, 0\nbuffer_store_dwordx2 v[66:67], v64, s[20:23], s61 offen sc1\ns_mov_b64 exec, -1\n"
"s_waitcnt vmcnt(16)\nM 0,172,48\nbuffer_load_dwordx4 a[112:115], v252, s[20:23], s64 offen offset:3072 sc1\nM 16,168,36\nR 162,190,53248\ns_waitcnt vmcnt(16)\n"
"M 16,172,52\nM 32,168,40\nR 164,186,0\ns_waitcnt vmcnt(15)\nM 32,172,56\nbuffer_load_dwordx4 a[116:119], v252, s[20:23], s65 offen offset:3072 sc1\n"
"M 48,168,44\nR 166,186,4096\ns_waitcnt vmcnt(15)\nM 48,172,60\nG 4\nM 4,176,32\n"
"R 168,191,49152\nM 4,180,48\nbuffer_load_dwordx4 a[120:123], v252, s[20:23], s66 offen offset:3072 sc1\nM 20,176,36\nR 170,191,53248\nR 64,238,8192\n"
"R 66,238,12288\nM 20,180,52\nR 68,239,8192\nR 70,239,12288\nM 36,176,40\nR 172,187,0\n"
"M 36,180,56\nbuffer_load_dwordx4 a[124:127], v252, s[20:23], s67 offen offset:3072 sc1\nM 52,176,44\nR 174,187,4096\nM 52,180,60\nG 8\n"
"M 8,160,32\nR 176,184,8192\nG 5\nv_mfma_f32_16x16x32_bf16 v[72:75], v[242:245], v[64:67], 0\nM 8,164,48\ns_add_u32 s64, s70, 2105344\n"
"buffer_load_dwordx4 a[128:131], v252, s[20:23], s64 offen offset:0 sc1\nM 24,160,36\nR 178,184,12288\nG 4\nv_mfma_f32_16x16x32_bf16 v[76:79], v[242:245], v[68:71], 0\nM 24,164,52\n"
"M 40,160,40\nR 180,184,16384\nM 40,164,56\ns_add_u32 s65, s70, 2138112\nbuffer_load_dwordx4 a[132:135], v252, s[20:23], s65 offen offset:0 sc1\nM 56,160,44\n"
"R 182,184,20480\nM 56,164,60\nG 4\nM 12,168,32\nR 160,185,8192\nv_lshrrev_b32 v64, 1, v252\n"
"v_mov_b32 v67, s46\nM 12,172,48\ns_add_u32 s66, s71, 2105344\nbuffer_load_dwordx4 a[136:139], v252, s[20:23], s66 offen offset:0 sc1\ns_mov_b64 s[60:61], 0xffff\nv_cndmask_b32 v66, v76, v72, s[60:61]\n"
"M 28,168,36\nR 162,185,12288\ns_lshr_b32 s61, s40, 1\ns_lshl_b32 s61, s61, 8\ns_add_u32 s61, s61, s91\ns_add_u32 s61, s61, 1048576\n"
"s_bfm_b64 exec, 32, 0\nbuffer_store_dwordx2 v[66:67], v64, s[20:23], s61 offen sc1\ns_mov_b64 exec, -1\nM 28,172,52\nM 44,168,40\nR 164,185,16384\n"
"M 44,172,56\ns_add_u32 s67, s71, 2138112\nbuffer_load_dwordx4 a[140:143], v252, s[20:23], s67 offen offset:0 sc1\nM 60,168,44\nR 166,185,20480\nM 60,172,60\n"
"s_barrier\nG 4\ns_waitcnt vmcnt(21)\nM 0,176,64\nR 168,186,8192\ns_waitcnt vmcnt(17)\n"
"M 0,180,80\nbuffer_load_dwordx4 a[144:147], v252, s[20:23], s64 offen offset:1024 sc1\nM 16,176,68\nR 170,186,12288\ns_waitcnt vmcnt(17)\nM 16,180,84\n"
"M 32,176,72\nR 172,186,16384\nR 64,238,16384\nR 66,238,20480\ns_waitcnt vmcnt(16)\nM 32,180,88\n"
"R 68,239,16384\nR 70,239,20480\nM 48,176,76\nR 174,186,20480\ns_waitcnt vmcnt(15)\nM 48,180,92\n"
"buffer_load_dwordx4 a[148:151], v252, s[20:23], s65 offen offset:1024 sc1\nG 8\nM 4,160,64\nR 176,187,8192\nM 4,164,80\nbuffer_load_dwordx4 a[152:155], v252, s[20:23], s66 offen offset:1024 sc1\n"
"M 20,160,68\nR 178,187,12288\nG 5\nv_mfma_f32_16x16x32_bf16 v[72:75], v[242:245], v[64:67], 0\nM 20,164,84\nM 36,160,72\n"
"R 180,187,16384\nG 4\nv_mfma_f32_16x16x32_bf16 v[76:79], v[242:245], v[68:71], 0\nM 36,164,88\nbuffer_load_dwordx4 a[156:159], v252, s[20:23], s67 offen offset:1024 sc1\nM 52,160,76\n"
"R 182,187,20480\nM 52,164,92\nG 4\nM 8,168,64\nR 160,184,24576\nM 8,172,80\n"
"buffer_load_dwordx4 a[160:163], v252, s[20:23], s64 offen offset:2048 sc1\nbuffer_load_dwordx4 v221, s[8:11], s63 offen offset:1024 lds\nM 24,168,68\nR 162,184,28672\nv_lshrrev_b32 v64, 1, v252\nv_mov_b32 v67, s46\n"
"M 24,172,84\ns_mov_b64 s[60:61], 0xffff\nv_cndmask_b32 v66, v76, v72, s[60:61]\ns_lshr_b32 s61, s40, 1\ns_lshl_b32 s61, s61, 8\ns_add_u32 s61, s61, s91\n"
"s_add_u32 s61, s61, 1310720\ns_bfm_b64 exec, 32, 0\nbuffer_store_dwordx2 v[66:67], v64, s[20:23], s61 offen sc1\ns_mov_b64 exec, -1\nM 40,168,72\nR 164,184,32768\n"
"M 40,172,88\nbuffer_load_dwordx4 a[164:167], v252, s[20:23], s65 offen offset:2048 sc1\nM 56,168,76\nR 166,184,36864\nM 56,172,92\nG 4\n"
"M 12,176,64\nR 168,185,24576\nM 12,180,80\nbuffer_load_dwordx4 a[168:171], v252, s[20:23], s66 offen offset:2048 sc1\nM 28,176,68\nR 170,185,28672\n"
"s_add_u32 s63, s53, 786432\nM 28,180,84\ns_add_u32 m0, s50, 73728\nM 44,176,72\nR 172,185,32768\nM 44,180,88\n"
"buffer_load_dwordx4 a[172:175], v252, s[20:23], s67 offen offset:2048 sc1\nM 60,176,76\nR 174,185,36864\nR 64,240,24576\nR 66,240,28672\nM 60,180,92\n"
"G 6\ns_waitcnt vmcnt(23)\nM 0,160,96\nR 176,186,24576\nbuffer_load_dwordx4 v220, s[8:11], s63 offen lds\ns_waitcnt vmcnt(19)\n"
"M 0,164,112\nbuffer_load_dwordx4 a[176:179], v252, s[20:23], s64 offen offset:3072 sc1\nM 16,160,100\nR 178,186,28672\ns_waitcnt vmcnt(19)\nM 16,164,116\n"
"buffer_load_dwordx4 v221, s[8:11], s63 offen offset:1024 lds\nM 32,160,104\nR 180,186,32768\nG 3\nv_mfma_f32_16x16x32_bf16 v[72:75], v[242:245], v[64:67], 0\ns_add_u32 m0, s50, 81920\n"
"s_waitcnt vmcnt(19)\nM 32,164,120\nbuffer_load_dwordx4 a[180:183], v252, s[20:23], s65 offen offset:3072 sc1\nM 48,160,108\nR 182,186,36864\ns_add_u32 s63, s53, 851968\n"
"s_waitcnt vmcnt(19)\nM 48,164,124\nbuffer_load_dwordx4 v220, s[8:11], s63 offen lds\nM 4,168,96\nR 160,187,24576\nM 4,172,112\n"
"buffer_load_dwordx4 a[184:187], v252, s[20:23], s66 offen offset:3072 sc1\nM 20,168,100\nR 162,187,28672\nM 20,172,116\nbuffer_load_dwordx4 v221, s[8:11], s63 offen offset:1024 lds\nM 36,168,104\n"
"R 164,187,32768\ns_add_u32 m0, s50, 90112\nv_lshrrev_b32 v64, 1, v252\nM 36,172,120\nbuffer_load_dwordx4 a[188:191], v252, s[20:23], s67 offen offset:3072 sc1\nv_mov_b32 v67, s46\n"
"M 52,168,108\nR 166,187,36864\ns_add_u32 s63, s53, 917504\nv_mov_b32 v66, v72\nM 52,172,124\nbuffer_load_dwordx4 v220, s[8:11], s63 offen lds\n"
"s_lshr_b32 s61, s40, 1\ns_lshl_b32 s61, s61, 7\ns_add_u32 s61, s61, s91\ns_add_u32 s61, s61, 1572864\ns_bfm_b64 exec, 16, 0\nbuffer_store_dwordx2 v[66:67], v64, s[20:23], s61 offen sc1\n"
"s_mov_b64 exec, -1\nG 4\nM 8,176,96\nR 168,184,40960\nM 8,180,112\ns_add_u32 s64, s70, 2109440\n"
"buffer_load_dwordx4 a[192:195], v252, s[20:23], s64 offen offset:0 sc1\nM 24,176,100\nR 170,184,45056\nM 24,180,116\nbuffer_load_dwordx4 v221, s[8:11], s63 offen offset:1024 lds\nM 40,176,104\n"
"R 172,184,49152\ns_add_u32 m0, s50, 98304\nM 40,180,120\ns_add_u32 s65, s70, 2142208\nbuffer_load_dwordx4 a[196:199], v252, s[20:23], s65 offen offset:0 sc1\nM 56,176,108\n"
"R 174,184,53248\ns_add_u32 s63, s53, 983040\nM 56,180,124\nbuffer_load_dwordx4 v220, s[8:11], s63 offen lds\nG 4\nM 12,160,96\n"
"R 176,185,40960\nM 12,164,112\ns_add_u32 s66, s71, 2109440\nbuffer_load_dwordx4 a[200:203], v252, s[20:23], s66 offen offset:0 sc1\nM 28,160,100\nR 178,185,45056\n"
"M 28,164,116\nbuffer_load_dwordx4 v221, s[8:11], s63 offen offset:1024 lds\nM 44,160,104\nR 180,185,49152\nM 44,164,120\ns_add_u32 s67, s71, 2142208\n"
"buffer_load_dwordx4 a[204:207], v252, s[20:23], s67 offen offset:0 sc1\nM 60,160,108\nR 182,185,53248\nM 60,164,124\ns_waitcnt vmcnt(21)\ns_barrier\n"
"G 4\nM 0,168,128\nR 160,186,40960\nM 0,172,144\nbuffer_load_dwordx4 a[208:211], v252, s[20:23], s64 offen offset:1024 sc1\nM 16,168,132\n"
"R 162,186,45056\nM 16,172,148\nM 32,168,136\nR 164,186,49152\nM 32,172,152\nbuffer_load_dwordx4 a[212:215], v252, s[20:23], s65 offen offset:1024 sc1\n"
"M 48,168,140\nR 166,186,53248\nM 48,172,156\nG 4\nM 4,176,128\nR 168,187,40960\n"
"M 4,180,144\nbuffer_load_dwordx4 a[216:219], v252, s[20:23], s66 offen offset:1024 sc1\nM 20,176,132\nR 170,187,45056\nM 20,180,148\nM 36,176,136\n"
"R 172,187,49152\nM 36,180,152\nbuffer_load_dwordx4 a[220:223], v252, s[20:23], s67 offen offset:1024 sc1\nM 52,176,140\nR 174,187,53248\nM 52,180,156\n"
"G 4\nM 8,160,128\nR 176,184,57344\nM 8,164,144\nbuffer_load_dwordx4 a[224:227], v252, s[20:23], s64 offen offset:2048 sc1\nM 24,160,132\n"
"R 178,184,61440\nM 24,164,148\nM 40,160,136\nR 180,188,0\nM 40,164,152\nbuffer_load_dwordx4 a[228:231], v252, s[20:23], s65 offen offset:2048 sc1\n"
"M 56,160,140\nR 182,188,4096\nM 56,164,156\nG 4\nM 12,168,128\nR 160,185,57344\n"
"M 12,172,144\nbuffer_load_dwordx4 a[232:235], v252, s[20:23], s66 offen offset:2048 sc1\nM 28,168,132\nR 162,185,61440\nM 28,172,148\nM 44,168,136\n"
"R 164,189,0\nM 44,172,152\nbuffer_load_dwordx4 a[236:239], v252, s[20:23], s67 offen offset:2048 sc1\nM 60,168,140\nR 166,189,4096\nM 60,172,156\n"
"G 4\nM 0,176,160\nR 168,186,57344\ns_waitcnt vmcnt(23)\nM 0,180,176\nbuffer_load_dwordx4 a[240:243], v252, s[20:23], s64 offen offset:3072 sc1\n"
"M 16,176,164\nR 170,186,61440\ns_waitcnt vmcnt(22)\nM 16,180,180\ns_movk_i32 s63, 7168\nv_mad_u32_u24 v220, v228, s63, v229\n"
"v_add_u32 v221, 3668992, v220\ns_add_u32 m0, s50, 106496\ns_add_u32 s63, s54, 0\nbuffer_load_dwordx4 v220, s[12:15], s63 offen lds\nM 32,176,168\nR 172,190,0\n"
"s_waitcnt vmcnt(21)\nM 32,180,184\nbuffer_load_dwordx4 a[244:247], v252, s[20:23], s65 offen offset:3072 sc1\nM 48,176,172\nR 174,190,4096\ns_waitcnt vmcnt(20)\n"
"M 48,180,188\nG 4\nM 4,160,160\nR 176,187,57344\nbuffer_load_dwordx4 v221, s[12:15], s63 offen offset:1024 lds\nM 4,164,176\n"
"buffer_load_dwordx4 a[248:251], v252, s[20:23], s66 offen offset:3072 sc1\nM 20,160,164\nR 178,187,61440\ns_waitcnt vmcnt(14)\ns_barrier\ns_add_u32 m0, s50, 114688\n"
"s_add_u32 s63, s54, 229376\nM 20,164,180\nbuffer_load_dwordx4 v220, s[12:15], s63 offen lds\nM 36,160,168\nR 180,191,0\nM 36,164,184\n"
"buffer_load_dwordx4 a[252:255], v252, s[20:23], s67 offen offset:3072 sc1\nM 52,160,172\nR 182,191,4096\nM 52,164,188\nG 4\nM 8,168,160\n"
"R 160,188,8192\nbuffer_load_dwordx4 v221, s[12:15], s63 offen offset:1024 lds\nM 8,172,176\ns_add_u32 m0, s50, 0\ns_add_u32 s63, s54, 458752\nbuffer_load_dwordx4 v220, s[12:15], s63 offen lds\n"
"M 24,168,164\nR 162,188,12288\nM 24,172,180\nM 40,168,168\nR 164,188,16384\nM 40,172,184\n"
"buffer_load_dwordx4 v221, s[12:15], s63 offen offset:1024 lds\nM 56,168,172\nR 166,188,20480\ns_add_u32 m0, s50, 8192\nM 56,172,188\nG 4\n"
"M 12,176,160\nR 168,189,8192\ns_add_u32 s63, s54, 688128\nM 12,180,176\nbuffer_load_dwordx4 v220, s[12:15], s63 offen lds\nM 28,176,164\n"
"R 170,189,12288\nM 28,180,180\nM 44,176,168\nR 172,189,16384\nM 44,180,184\nbuffer_load_dwordx4 v221, s[12:15], s63 offen offset:1024 lds\n"
"M 60,176,172\nR 174,189,20480\ns_add_u32 m0, s50, 16384\nM 60,180,188\nG 4\nM 32,160,200\n"
"R 176,190,8192\ns_add_u32 s63, s54, 917504\ns_waitcnt vmcnt(17)\nM 32,164,216\nR 178,190,12288\nM 48,160,204\n"
"R 180,190,16384\ns_waitcnt vmcnt(16)\nM 48,164,220\nR 182,190,20480\nG 4\nM 36,168,200\n"
"R 160,191,8192\nbuffer_load_dwordx4 v220, s[12:15], s63 offen lds\nM 36,172,216\nR 162,191,12288\nM 52,168,204\nR 164,191,16384\n"
"M 52,172,220\nR 166,191,20480\nG 4\nM 40,176,200\nR 168,188,24576\nbuffer_load_dwordx4 v221, s[12:15], s63 offen offset:1024 lds\n"
"M 40,180,216\nR 170,188,28672\ns_add_u32 m0, s50, 24576\nM 56,176,204\nR 172,188,32768\ns_add_u32 s63, s54, 1146880\n"
"M 56,180,220\nR 174,188,36864\nG 4\nM 44,160,200\nR 176,189,24576\nbuffer_load_dwordx4 v220, s[12:15], s63 offen lds\n"
"M 44,164,216\nR 178,189,28672\nM 60,160,204\nR 180,189,32768\nM 60,164,220\nR 182,189,36864\n"
"G 4\ns_waitcnt vmcnt(16)\nM 32,168,232\nR 160,190,24576\nbuffer_load_dwordx4 v221, s[12:15], s63 offen offset:1024 lds\ns_waitcnt vmcnt(11)\n"
"M 32,172,248\nR 162,190,28672\nM 48,168,236\nR 164,190,32768\ns_waitcnt vmcnt(9)\nM 48,172,252\n"
"R 166,190,36864\nG 4\nM 36,176,232\nR 168,191,24576\nM 36,180,248\nR 170,191,28672\n"
"M 52,176,236\nR 172,191,32768\nM 52,180,252\nR 174,191,36864\nG 4\nM 40,160,232\n"
"R 176,188,8192\nM 40,164,248\nR 178,188,12288\nM 56,160,236\nR 180,188,16384\nM 56,164,252\n"
"R 182,188,20480\nG 4\nM 44,168,232\nR 160,189,8192\nM 44,172,248\nR 162,189,12288\n"
"M 60,168,236\nR 164,189,16384\nM 60,172,252\nR 166,189,20480\nG 4\nM 0,176,192\n"
"R 168,190,8192\nM 0,180,208\nR 170,190,12288\ns_mov_b32 m0, s51\nM 16,176,196\nR 172,190,16384\n"
"ds_write_addtid_b32 v32 offset:0\nM 16,180,212\nR 174,190,20480\nds_write_addtid_b32 v33 offset:256\nG 6\nM 4,160,192\n"
"R 176,191,8192\nds_write_addtid_b32 v34 offset:512\nM 4,164,208\nR 178,191,12288\nds_write_addtid_b32 v35 offset:768\nM 20,160,196\n"
"R 180,191,16384\nds_write_addtid_b32 v36 offset:1024\nM 20,164,212\nR 182,191,20480\nds_write_addtid_b32 v37 offset:1280\nG 9\n"
"M 8,168,192\nR 160,188,24576\nds_write_addtid_b32 v38 offset:1536\nM 8,172,208\nR 162,188,28672\nds_write_addtid_b32 v39 offset:1792\n"
"M 24,168,196\nR 164,188,32768\nds_write_addtid_b32 v40 offset:2048\nM 24,172,212\nR 166,188,36864\nds_write_addtid_b32 v41 offset:2304\n"
"G 9\nM 12,176,192\nR 168,189,24576\nds_write_addtid_b32 v42 offset:2560\nM 12,180,208\nR 170,189,28672\n"
"ds_write_addtid_b32 v43 offset:2816\nM 28,176,196\nR 172,189,32768\nds_write_addtid_b32 v44 offset:3072\nds_write_addtid_b32 v45 offset:3328\nds_write_addtid_b32 v46 offset:3584\n"
"ds_write_addtid_b32 v47 offset:3840\nds_write_addtid_b32 v48 offset:4096\nds_write_addtid_b32 v49 offset:4352\nds_write_addtid_b32 v50 offset:4608\nds_write_addtid_b32 v51 offset:4864\nds_write_addtid_b32 v52 offset:5120\n"
"ds_write_addtid_b32 v53 offset:5376\nds_write_addtid_b32 v54 offset:5632\nds_write_addtid_b32 v55 offset:5888\nds_write_addtid_b32 v56 offset:6144\nds_write_addtid_b32 v57 offset:6400\nds_write_addtid_b32 v58 offset:6656\n"
"ds_write_addtid_b32 v59 offset:6912\nds_write_addtid_b32 v60 offset:7168\nds_write_addtid_b32 v61 offset:7424\nds_write_addtid_b32 v62 offset:7680\nds_write_addtid_b32 v63 offset:7936\nM 28,180,212\n"
"R 174,189,36864\nG 15\nM 0,160,224\nR 176,190,24576\ns_add_u32 m0, s50, 32768\nM 0,164,240\n"
"R 178,190,28672\nG 3\ns_barrier\nM 16,160,228\nR 180,190,32768\ns_add_u32 s63, s54, 1376256\n"
"ds_read2st64_b32 v[32:33], v225 offset0:0 offset1:1\nM 16,164,244\nR 182,190,36864\nds_read2st64_b32 v[34:35], v225 offset0:2 offset1:3\nG 6\nM 4,168,224\n"
"R 160,191,24576\nbuffer_load_dwordx4 v220, s[12:15], s63 offen lds\nds_read2st64_b32 v[36:37], v225 offset0:4 offset1:5\nM 4,172,240\nR 162,191,28672\nds_read2st64_b32 v[38:39], v225 offset0:6 offset1:7\n"
"M 20,168,228\nR 164,191,32768\nds_read2st64_b32 v[40:41], v225 offset0:8 offset1:9\nM 20,172,244\nR 166,191,36864\nds_read2st64_b32 v[42:43], v225 offset0:10 offset1:11\n"
"G 9\nM 8,176,224\nbuffer_load_dwordx4 v221, s[12:15], s63 offen offset:1024 lds\nM 8,180,240\ns_add_u32 m0, s50, 40960\ns_add_u32 s63, s54, 1605632\n"
"buffer_load_dwordx4 v220, s[12:15], s63 offen lds\nM 24,176,228\nds_read2st64_b32 v[44:45], v225 offset0:12 offset1:13\nds_read2st64_b32 v[46:47], v225 offset0:14 offset1:15\nM 24,180,244\nds_read2st64_b32 v[48:49], v225 offset0:16 offset1:17\n"
"ds_read2st64_b32 v[50:51], v225 offset0:18 offset1:19\nG 5\nM 12,160,224\nds_read2st64_b32 v[52:53], v225 offset0:20 offset1:21\nds_read2st64_b32 v[54:55], v225 offset0:22 offset1:23\nM 12,164,240\n"
"buffer_load_dwordx4 v221, s[12:15], s63 offen offset:1024 lds\nM 28,160,228\nds_read2st64_b32 v[56:57], v225 offset0:24 offset1:25\nds_read2st64_b32 v[58:59], v225 offset0:26 offset1:27\nM 28,164,244\nds_read2st64_b32 v[60:61], v225 offset0:28 offset1:29\n"
"ds_read2st64_b32 v[62:63], v225 offset0:30 offset1:31\ns_lshl_b32 s84, s75, 11\ns_add_u32 s84, s84, s49\ns_add_u32 s84, s84, 10485760\ns_add_u32 s85, s84, 32768\nv_mov_b32 v238, s46\n"
"v_mov_b32 v239, s47\nv_pk_add_f32 v[0:1], v[0:1], v[32:33]\nv_pk_add_f32 v[2:3], v[2:3], v[34:35]\nv_pk_add_f32 v[4:5], v[4:5], v[36:37]\nv_pk_add_f32 v[6:7], v[6:7], v[38:39]\nv_cvt_pk_bf16_f32 v192, v0, v1\n"
"v_cvt_pk_bf16_f32 v193, v2, v3\nv_cvt_pk_bf16_f32 v194, v4, v5\nv_cvt_pk_bf16_f32 v195, v6, v7\nbuffer_store_dwordx4 v[192:195], v252, s[20:23], s84 offen offset:0 sc1\nv_pk_add_f32 v[8:9], v[8:9], v[40:41]\nG 10\n"
"v_pk_add_f32 v[10:11], v[10:11], v[42:43]\nG 9\nv_pk_add_f32 v[12:13], v[12:13], v[44:45]\nG 8\nv_pk_add_f32 v[14:15], v[14:15], v[46:47]\nv_cvt_pk_bf16_f32 v196, v8, v9\n"
"v_cvt_pk_bf16_f32 v197, v10, v11\nv_cvt_pk_bf16_f32 v198, v12, v13\nv_cvt_pk_bf16_f32 v199, v14, v15\nbuffer_store_dwordx4 v[196:199], v252, s[20:23], s84 offen offset:1024 sc1\nG 7\nv_pk_add_f32 v[16:17], v[16:17], v[48:49]\n"
"G 6\nv_pk_add_f32 v[18:19], v[18:19], v[50:51]\nG 5\nv_pk_add_f32 v[20:21], v[20:21], v[52:53]\nG 4\nv_pk_add_f32 v[22:23], v[22:23], v[54:55]\n"
"v_cvt_pk_bf16_f32 v200, v16, v17\nv_cvt_pk_bf16_f32 v201, v18, v19\nv_cvt_pk_bf16_f32 v202, v20, v21\nv_cvt_pk_bf16_f32 v203, v22, v23\nbuffer_store_dwordx4 v[200:203], v252, s[20:23], s85 offen offset:0 sc1\nG 3\n"
"v_pk_add_f32 v[24:25], v[24:25], v[56:57]\nG 2\nv_pk_add_f32 v[26:27], v[26:27], v[58:59]\nG 1\nv_pk_add_f32 v[28:29], v[28:29], v[60:61]\nG 0\n"
"v_pk_add_f32 v[30:31], v[30:31], v[62:63]\nv_cvt_pk_bf16_f32 v204, v24, v25\nv_cvt_pk_bf16_f32 v205, v26, v27\nv_cvt_pk_bf16_f32 v206, v28, v29\nv_cvt_pk_bf16_f32 v207, v30, v31\nbuffer_store_dwordx4 v[204:207], v252, s[20:23], s85 offen offset:1024 sc1\n"
"s_add_u32 s61, s79, 88219648\ns_lshl_b32 s62, s75, 3\ns_add_u32 s61, s61, s62\ns_waitcnt vmcnt(0)\ns_mov_b64 exec, 1\nbuffer_atomic_swap_x2 v[238:239], off, s[20:23], s61 sc1\n"
"s_mov_b64 exec, -1\ns_barrier\nLqw0:\nv_bfe_u32 v250, v252, 7, 1\nv_mul_u32_u24 v250, 2176, v250\nv_bfe_u32 v251, v252, 4, 3\n"
"v_lshl_add_u32 v250, v251, 3, v250\ns_lshl_b32 s60, s90, 6\nv_add_u32 v250, s60, v250\ns_add_u32 m0, s50, 49152\ns_add_u32 s63, s54, 1835008\nbuffer_load_dwordx4 v220, s[12:15], s63 offen lds\n"
"buffer_load_dwordx4 v221, s[12:15], s63 offen offset:1024 lds\ns_add_u32 m0, s50, 57344\ns_add_u32 s63, s54, 2064384\nbuffer_load_dwordx4 v220, s[12:15], s63 offen lds\nbuffer_load_dwordx4 v221, s[12:15], s63 offen offset:1024 lds\ns_add_u32 m0, s50, 65536\n"
"s_add_u32 s63, s54, 2293760\nbuffer_load_dwordx4 v220, s[12:15], s63 offen lds\nbuffer_load_dwordx4 v221, s[12:15], s63 offen offset:1024 lds\ns_add_u32 m0, s50, 73728\ns_add_u32 s63, s54, 2523136\nbuffer_load_dwordx4 v220, s[12:15], s63 offen lds\n"
"buffer_load_dwordx4 v221, s[12:15], s63 offen offset:1024 lds\ns_lshl2_add_u32 s60, s45, s40\nv_lshlrev_b32 v226, 3, v252\ns_mul_i32 s60, s60, 7168\ns_bfm_b64 exec, 56, 0\nbuffer_load_dword v226, v226, s[16:19], s60 offen\n"
"s_mov_b64 exec, -1\nv_mov_b32 v248, 0\nv_mov_b32 v249, 0\ns_add_u32 s61, s80, 88219648\ns_mov_b64 exec, 0xffff\nbuffer_atomic_or_x2 v[248:249], v250, s[20:23], s61 offen sc0 sc1\n"
"s_mov_b64 exec, -1\nv_mov_b32 v230, 0\nv_mov_b32 v231, 0\nv_mov_b32 v232, 0\nv_mov_b32 v233, 0\nv_dot2c_f32_bf16 v230, s76, v192\n"
"v_dot2c_f32_bf16 v231, v192, v192\nv_dot2c_f32_bf16 v232, s76, v200\nv_dot2c_f32_bf16 v233, v200, v200\nv_dot2c_f32_bf16 v230, s76, v193\nv_dot2c_f32_bf16 v231, v193, v193\nv_dot2c_f32_bf16 v232, s76, v201\n"
"v_dot2c_f32_bf16 v233, v201, v201\nv_dot2c_f32_bf16 v230, s76, v194\nv_dot2c_f32_bf16 v231, v194, v194\nv_dot2c_f32_bf16 v232, s76, v202\nv_dot2c_f32_bf16 v233, v202, v202\nv_dot2c_f32_bf16 v230, s76, v195\n"
"v_dot2c_f32_bf16 v231, v195, v195\nv_dot2c_f32_bf16 v232, s76, v203\nv_dot2c_f32_bf16 v233, v203, v203\nv_dot2c_f32_bf16 v230, s76, v196\nv_dot2c_f32_bf16 v231, v196, v196\nv_dot2c_f32_bf16 v232, s76, v204\n"
"v_dot2c_f32_bf16 v233, v204, v204\nv_dot2c_f32_bf16 v230, s76, v197\nv_dot2c_f32_bf16 v231, v197, v197\nv_dot2c_f32_bf16 v232, s76, v205\nv_dot2c_f32_bf16 v233, v205, v205\nv_dot2c_f32_bf16 v230, s76, v198\n"
"v_dot2c_f32_bf16 v231, v198, v198\nv_dot2c_f32_bf16 v232, s76, v206\nv_dot2c_f32_bf16 v233, v206, v206\nv_dot2c_f32_bf16 v230, s76, v199\nv_dot2c_f32_bf16 v231, v199, v199\nv_dot2c_f32_bf16 v232, s76, v207\n"
"v_dot2c_f32_bf16 v233, v207, v207\nv_lshrrev_b32 v234, 1, v252\nv_permlane32_swap_b32 v230, v231\ns_nop 0\nv_permlane32_swap_b32 v232, v233\nv_add_f32 v230, v230, v231\n"
"v_add_f32 v232, v232, v233\nv_mov_b32 v231, s46\ns_nop 0\nv_permlane16_swap_b32 v230, v232\nv_add_f32 v230, v230, v232\ns_lshr_b32 s61, s49, 3\n"
"s_lshl2_add_u32 s61, s53, s61\ns_add_u32 s61, s61, 82313216\nbuffer_store_dwordx2 v[230:231], v234, s[20:23], s61 offen sc1\ns_waitcnt vmcnt(1)\nv_cmp_eq_u64 s[86:87], s[46:47], v[248:249]\ns_nop 0\n"
"s_and_b32 s62, s86, 0xffff\ns_cmp_eq_u32 s62, 0xffff\ns_cbranch_scc0 Lq255\nLq256:\ns_add_u32 s64, s70, 10485760\nbuffer_load_dwordx4 a[0:3], v252, s[20:23], s64 offen offset:0 sc1\n"
"s_add_u32 s65, s70, 10518528\nbuffer_load_dwordx4 a[4:7], v252, s[20:23], s65 offen offset:0 sc1\ns_add_u32 s66, s71, 10485760\nbuffer_load_dwordx4 a[8:11], v252, s[20:23], s66 offen offset:0 sc1\ns_add_u32 s67, s71, 10518528\nbuffer_load_dwordx4 a[12:15], v252, s[20:23], s67 offen offset:0 sc1\n"
"buffer_load_dwordx4 a[16:19], v252, s[20:23], s64 offen offset:1024 sc1\nbuffer_load_dwordx4 a[20:23], v252, s[20:23], s65 offen offset:1024 sc1\nbuffer_load_dwordx4 a[24:27], v252, s[20:23], s66 offen offset:1024 sc1\nbuffer_load_dwordx4 a[28:31], v252, s[20:23], s67 offen offset:1024 sc1\ns_barrier\nR 160,188,40960\n"
"R 162,188,45056\nR 164,188,49152\nR 166,188,53248\nR 168,189,40960\nR 170,189,45056\nR 172,189,49152\n"
"R 174,189,53248\nR 176,190,40960\nR 178,190,45056\nR 180,190,49152\nR 182,190,53248\nbuffer_load_dwordx4 a[32:35], v252, s[20:23], s64 offen offset:2048 sc1\n"
"buffer_load_dwordx4 a[36:39], v252, s[20:23], s65 offen offset:2048 sc1\nbuffer_load_dwordx4 a[40:43], v252, s[20:23], s66 offen offset:2048 sc1\nbuffer_load_dwordx4 a[44:47], v252, s[20:23], s67 offen offset:2048 sc1\nbuffer_load_dwordx4 a[48:51], v252, s[20:23], s64 offen offset:3072 sc1\nbuffer_load_dwordx4 a[52:55], v252, s[20:23], s65 offen offset:3072 sc1\nbuffer_load_dwordx4 a[56:59], v252, s[20:23], s66 offen offset:3072 sc1\n"
"buffer_load_dwordx4 a[60:63], v252, s[20:23], s67 offen offset:3072 sc1\ns_waitcnt vmcnt(15) lgkmcnt(10)\nv_mfma_f32_16x16x32_bf16 v[32:35], v[160:163], a[0:3], 0\ns_add_u32 s64, s70, 10489856\nbuffer_load_dwordx4 a[64:67], v252, s[20:23], s64 offen offset:0 sc1\ns_waitcnt vmcnt(12) lgkmcnt(8)\n"
"M 32,164,16\nv_mfma_f32_16x16x32_bf16 v[48:51], v[160:163], a[4:7], 0\ns_waitcnt vmcnt(11)\nM 48,164,20\nv_mfma_f32_16x16x32_bf16 v[64:67], v[160:163], a[8:11], 0\ns_add_u32 s65, s70, 10522624\n"
"buffer_load_dwordx4 a[68:71], v252, s[20:23], s65 offen offset:0 sc1\ns_waitcnt vmcnt(11)\nM 64,164,24\nv_mfma_f32_16x16x32_bf16 v[80:83], v[160:163], a[12:15], 0\ns_waitcnt vmcnt(10)\nM 80,164,28\n"
"G 4\nv_mfma_f32_16x16x32_bf16 v[36:39], v[168:171], a[0:3], 0\nR 160,191,40960\nM 36,172,16\ns_add_u32 s66, s71, 10489856\nbuffer_load_dwordx4 a[72:75], v252, s[20:23], s66 offen offset:0 sc1\n"
"v_mfma_f32_16x16x32_bf16 v[52:55], v[168:171], a[4:7], 0\nR 162,191,45056\nv_lshrrev_b32 v238, 7, v252\nM 52,172,20\nv_lshlrev_b32 v238, 9, v238\nv_mfma_f32_16x16x32_bf16 v[68:71], v[168:171], a[8:11], 0\n"
"R 164,191,49152\nv_and_b32 v239, 0x70, v252\nM 68,172,24\ns_add_u32 s67, s71, 10522624\nbuffer_load_dwordx4 a[76:79], v252, s[20:23], s67 offen offset:0 sc1\ns_and_b32 s60, s40, 3\n"
"s_lshl_b32 s60, s60, 7\nv_add3_u32 v238, v238, v239, s60\nv_mfma_f32_16x16x32_bf16 v[84:87], v[168:171], a[12:15], 0\nR 166,191,53248\nM 84,172,28\ns_lshl_b32 s61, s42, 14\n"
"s_add_u32 s61, s61, 84672512\nbuffer_load_dwordx4 v[96:99], v238, s[20:23], s61 offen sc1\nG 4\nv_mfma_f32_16x16x32_bf16 v[40:43], v[176:179], a[0:3], 0\nR 168,184,0\ns_lshl_b32 s61, s42, 14\n"
"s_add_u32 s61, s61, 84676608\nbuffer_load_dwordx4 v[100:103], v238, s[20:23], s61 offen sc1\nM 40,180,16\nbuffer_load_dwordx4 a[80:83], v252, s[20:23], s64 offen offset:1024 sc1\ns_lshl_b32 s61, s42, 14\ns_add_u32 s61, s61, 84680704\n"
"buffer_load_dwordx4 v[104:107], v238, s[20:23], s61 offen sc1\nv_mfma_f32_16x16x32_bf16 v[56:59], v[176:179], a[4:7], 0\nR 170,184,4096\ns_lshl_b32 s61, s42, 14\ns_add_u32 s61, s61, 84684800\nbuffer_load_dwordx4 v[108:111], v238, s[20:23], s61 offen sc1\n"
"M 56,180,20\nv_bfe_u32 v239, v238, 2, 7\nv_mfma_f32_16x16x32_bf16 v[72:75], v[176:179], a[8:11], 0\nR 172,184,8192\nbuffer_load_dword v239, v239, s[32:35], s54 offen offset:0\nM 72,180,24\n"
"buffer_load_dwordx4 a[84:87], v252, s[20:23], s65 offen offset:1024 sc1\nv_mfma_f32_16x16x32_bf16 v[88:91], v[176:179], a[12:15], 0\nR 174,184,12288\nM 88,180,28\nG 4\nv_mfma_f32_16x16x32_bf16 v[44:47], v[160:163], a[0:3], 0\n"
"R 176,185,0\nM 44,164,16\nbuffer_load_dwordx4 a[88:91], v252, s[20:23], s66 offen offset:1024 sc1\nv_mfma_f32_16x16x32_bf16 v[60:63], v[160:163], a[4:7], 0\nR 178,185,4096\nM 60,164,20\n"
"v_mfma_f32_16x16x32_bf16 v[76:79], v[160:163], a[8:11], 0\nR 180,185,8192\nM 76,164,24\nbuffer_load_dwordx4 a[92:95], v252, s[20:23], s67 offen offset:1024 sc1\nv_mfma_f32_16x16x32_bf16 v[92:95], v[160:163], a[12:15], 0\nR 182,185,12288\n"
"M 92,164,28\nG 4\ns_waitcnt vmcnt(20)\nM 32,168,32\nR 160,186,0\ns_waitcnt vmcnt(16)\n"
"M 32,172,48\nM 48,168,36\nR 162,186,4096\ns_waitcnt vmcnt(15)\nM 48,172,52\nbuffer_load_dwordx4 a[96:99], v252, s[20:23], s64 offen offset:2048 sc1\n"
"M 64,168,40\nR 164,186,8192\ns_waitcnt vmcnt(15)\nM 64,172,56\nM 80,168,44\nR 166,186,12288\n"
"s_waitcnt vmcnt(14)\nM 80,172,60\nG 4\nM 36,176,32\nR 168,187,0\nM 36,180,48\n"
"buffer_load_dwordx4 a[100:103], v252, s[20:23], s65 offen offset:2048 sc1\nM 52,176,36\nR 170,187,4096\nM 52,180,52\nbuffer_load_dwordx4 a[104:107], v252, s[20:23], s66 offen offset:2048 sc1\nM 68,176,40\n"
"R 172,187,8192\nM 68,180,56\nM 84,176,44\nR 174,187,12288\nM 84,180,60\nG 4\n"
"M 40,160,32\nR 176,184,16384\nM 40,164,48\nbuffer_load_dwordx4 a[108:111], v252, s[20:23], s67 offen offset:2048 sc1\nM 56,160,36\nR 178,184,20480\n"
"M 56,164,52\nbuffer_load_dwordx4 a[112:115], v252, s[20:23], s64 offen offset:3072 sc1\nM 72,160,40\nR 180,184,24576\nM 72,164,56\nM 88,160,44\n"
"R 182,184,28672\nv_lshrrev_b32 v242, 7, v252\nM 88,164,60\nG 4\nv_lshlrev_b32 v242, 9, v242\nM 44,168,32\n"
"R 160,185,16384\nv_and_b32 v243, 0x70, v252\ns_waitcnt vmcnt(12)\nv_min3_u32 v240, v97, v99, v101\nM 44,172,48\nbuffer_load_dwordx4 a[116:119], v252, s[20:23], s65 offen offset:3072 sc1\n"
"s_and_b32 s60, s40, 3\ns_lshl_b32 s60, s60, 7\nv_add3_u32 v242, v242, v243, s60\ns_waitcnt vmcnt(11)\nv_min3_u32 v240, v240, v103, v105\nM 60,168,36\n"
"R 162,185,20480\ns_lshl_b32 s61, s42, 14\ns_add_u32 s61, s61, 84934656\nbuffer_load_dwordx4 v[112:115], v242, s[20:23], s61 offen sc1\ns_waitcnt vmcnt(11)\nv_min3_u32 v240, v240, v107, v109\n"
"M 60,172,52\nbuffer_load_dwordx4 a[120:123], v252, s[20:23], s66 offen offset:3072 sc1\nv_min3_u32 v240, v240, v111, v111\nM 76,168,40\nR 164,185,24576\ns_lshl_b32 s61, s42, 14\n"
"s_add_u32 s61, s61, 84938752\nbuffer_load_dwordx4 v[116:119], v242, s[20:23], s61 offen sc1\nv_max3_u32 v241, v97, v99, v101\nM 76,172,56\ns_lshl_b32 s61, s42, 14\ns_add_u32 s61, s61, 84942848\n"
"buffer_load_dwordx4 v[120:123], v242, s[20:23], s61 offen sc1\nv_max3_u32 v241, v241, v103, v105\nM 92,168,44\nR 166,185,28672\ns_lshl_b32 s61, s42, 14\ns_add_u32 s61, s61, 84946944\n"
"buffer_load_dwordx4 v[124:127], v242, s[20:23], s61 offen sc1\ns_add_u32 s63, s54, 2752512\nv_max3_u32 v241, v241, v107, v109\nM 92,172,60\ns_barrier\nG 4\n"
"v_bfe_u32 v243, v242, 2, 7\nv_max3_u32 v241, v241, v111, v111\nM 32,176,64\nR 168,186,16384\nbuffer_load_dword v243, v243, s[32:35], s54 offen offset:128\nv_cmp_ne_u32 s[86:87], s46, v240\n"
"M 32,180,80\nbuffer_load_dwordx4 a[124:127], v252, s[20:23], s67 offen offset:3072 sc1\nv_cmp_ne_u32 s[88:89], s46, v241\nM 48,176,68\nR 170,186,20480\ns_add_u32 m0, s50, 81920\n"
"s_or_b64 s[86:87], s[86:87], s[88:89]\ns_cbranch_scc1 Lq314\nLq315:\ns_waitcnt vmcnt(15)\nM 48,180,84\nbuffer_load_dwordx4 v220, s[12:15], s63 offen lds\n"
"v_add_f32 v96, v96, v104\nM 64,176,72\nR 172,186,24576\nv_add_f32 v98, v98, v106\nv_add_f32 v100, v100, v108\ns_waitcnt vmcnt(15)\n"
"M 64,180,88\ns_add_u32 s64, s70, 10493952\nbuffer_load_dwordx4 a[128:131], v252, s[20:23], s64 offen offset:0 sc1\nv_add_f32 v102, v102, v110\nM 80,176,76\nR 174,186,28672\n"
"v_add_f32 v96, v96, v100\nv_add_f32 v98, v98, v102\ns_waitcnt vmcnt(15)\nM 80,180,92\nG 4\nv_add_f32_dpp v96, v96, v96 row_ror:8 row_mask:0xf bank_mask:0xf\n"
"v_add_f32_dpp v98, v98, v98 row_ror:8 row_mask:0xf bank_mask:0xf\nv_mov_b32 v100, v96\nM 36,160,64\nR 176,187,16384\nbuffer_load_dwordx4 v221, s[12:15], s63 offen offset:1024 lds\nv_mov_b32 v101, v98\n"
"M 36,164,80\ns_add_u32 s65, s70, 10526720\nbuffer_load_dwordx4 a[132:135], v252, s[20:23], s65 offen offset:0 sc1\ns_add_u32 s63, s54, 2981888\nv_permlane32_swap_b32 v100, v96\nM 52,160,68\n"
"R 178,187,20480\ns_add_u32 m0, s50, 90112\nv_permlane32_swap_b32 v101, v98\nM 52,164,84\nbuffer_load_dwordx4 v220, s[12:15], s63 offen lds\nv_add_f32 v96, v96, v100\n"
"M 68,160,72\nR 180,187,24576\nv_add_f32 v98, v98, v101\nv_mov_b32 v100, v96\nM 68,164,88\ns_add_u32 s66, s71, 10493952\n"
"buffer_load_dwordx4 a[136:139], v252, s[20:23], s66 offen offset:0 sc1\nv_mov_b32 v101, v98\nM 84,160,76\nR 182,187,28672\nv_permlane16_swap_b32 v100, v96\nv_permlane16_swap_b32 v101, v98\n"
"M 84,164,92\nG 4\nv_add_f32 v96, v96, v100\nv_add_f32 v98, v98, v101\nv_and_b32 v104, s77, v96\nM 40,168,64\n"
"R 160,184,32768\nbuffer_load_dwordx4 v221, s[12:15], s63 offen offset:1024 lds\nv_sub_f32 v105, v96, v104\nM 40,172,80\ns_add_u32 s67, s71, 10526720\nbuffer_load_dwordx4 a[140:143], v252, s[20:23], s67 offen offset:0 sc1\n"
"v_lshlrev_b32 v106, 16, v239\nM 56,168,68\nR 162,184,36864\nv_cvt_pk_bf16_f32 v100, v104, v105\nv_cvt_pk_bf16_f32 v101, v104, v106\nM 56,172,84\n"
"v_and_b32 v104, s77, v98\nv_sub_f32 v105, v98, v104\nM 72,168,72\nR 164,184,40960\nv_and_b32 v106, s77, v239\nM 72,172,88\n"
"buffer_load_dwordx4 a[144:147], v252, s[20:23], s64 offen offset:1024 sc1\nM 88,168,76\nR 166,184,45056\nv_cvt_pk_bf16_f32 v102, v104, v105\nM 88,172,92\nG 4\n"
"v_cvt_pk_bf16_f32 v103, v104, v106\nv_and_b32 v238, 0x1ff, v238\nM 44,176,64\nR 168,185,32768\nv_add_u32 v238, 157184, v238\nM 44,180,80\n"
"buffer_load_dwordx4 a[148:151], v252, s[20:23], s65 offen offset:1024 sc1\nM 60,176,68\nR 170,185,36864\nds_write_b128 v238, v[100:103]\nM 60,180,84\nM 76,176,72\n"
"R 172,185,40960\nM 76,180,88\nbuffer_load_dwordx4 a[152:155], v252, s[20:23], s66 offen offset:1024 sc1\nM 92,176,76\nR 174,185,45056\nM 92,180,92\n"
"G 5\ns_waitcnt vmcnt(23)\nM 32,160,96\nR 176,186,32768\ns_waitcnt vmcnt(19)\nM 32,164,112\n"
"buffer_load_dwordx4 a[156:159], v252, s[20:23], s67 offen offset:1024 sc1\nM 48,160,100\nR 178,186,36864\nv_lshrrev_b32 v238, 7, v252\ns_waitcnt vmcnt(19)\nM 48,164,116\n"
"v_lshlrev_b32 v238, 9, v238\nM 64,160,104\nR 180,186,40960\nv_and_b32 v239, 0x70, v252\ns_waitcnt vmcnt(16)\nv_min3_u32 v244, v113, v115, v117\n"
"M 64,164,120\ns_and_b32 s60, s40, 3\ns_lshl_b32 s60, s60, 7\nv_add3_u32 v238, v238, v239, s60\ns_waitcnt vmcnt(15)\nv_min3_u32 v244, v244, v119, v121\n"
"M 80,160,108\nR 182,186,45056\ns_waitcnt vmcnt(14)\nv_min3_u32 v244, v244, v123, v125\nv_min3_u32 v244, v244, v127, v127\ns_waitcnt vmcnt(12)\n"
"M 80,164,124\ns_lshl_b32 s61, s42, 14\ns_add_u32 s61, s61, 85196800\nbuffer_load_dwordx4 v[128:131], v238, s[20:23], s61 offen sc1\nG 4\nv_max3_u32 v245, v113, v115, v117\n"
"M 36,168,96\nR 160,187,32768\ns_lshl_b32 s61, s42, 14\ns_add_u32 s61, s61, 85200896\nbuffer_load_dwordx4 v[132:135], v238, s[20:23], s61 offen sc1\nv_max3_u32 v245, v245, v119, v121\n"
"M 36,172,112\nbuffer_load_dwordx4 a[160:163], v252, s[20:23], s64 offen offset:2048 sc1\ns_lshl_b32 s61, s42, 14\ns_add_u32 s61, s61, 85204992\nbuffer_load_dwordx4 v[136:139], v238, s[20:23], s61 offen sc1\nv_max3_u32 v245, v245, v123, v125\n"
"M 52,168,100\nR 162,187,36864\ns_lshl_b32 s61, s42, 14\ns_add_u32 s61, s61, 85209088\nbuffer_load_dwordx4 v[140:143], v238, s[20:23], s61 offen sc1\nv_max3_u32 v245, v245, v127, v127\n"
"M 52,172,116\nbuffer_load_dwordx4 a[164:167], v252, s[20:23], s65 offen offset:2048 sc1\nv_bfe_u32 v239, v238, 2, 7\nv_cmp_ne_u32 s[86:87], s46, v244\nM 68,168,104\nR 164,187,40960\n"
"v_cmp_ne_u32 s[88:89], s46, v245\nM 68,172,120\nbuffer_load_dword v239, v239, s[32:35], s54 offen offset:256\ns_or_b64 s[86:87], s[86:87], s[88:89]\ns_cbranch_scc1 Lq326\nLq327:\n"
"M 84,168,108\nR 166,187,45056\ns_add_u32 m0, s50, 98304\nv_add_f32 v112, v112, v120\nM 84,172,124\nG 4\n"
"v_add_f32 v114, v114, v122\nv_add_f32 v116, v116, v124\nv_add_f32 v118, v118, v126\nM 40,176,96\nR 168,184,49152\ns_add_u32 s63, s54, 3211264\n"
"v_add_f32 v112, v112, v116\nM 40,180,112\nbuffer_load_dwordx4 a[168:171], v252, s[20:23], s66 offen offset:2048 sc1\nv_add_f32 v114, v114, v118\nM 56,176,100\nR 170,184,53248\n"
"v_add_f32_dpp v112, v112, v112 row_ror:8 row_mask:0xf bank_mask:0xf\nv_add_f32_dpp v114, v114, v114 row_ror:8 row_mask:0xf bank_mask:0xf\nM 56,180,116\nbuffer_load_dwordx4 a[172:175], v252, s[20:23], s67 offen offset:2048 sc1\nv_mov_b32 v116, v112\nM 72,176,104\n"
"R 172,184,57344\nbuffer_load_dwordx4 v220, s[12:15], s63 offen lds\nv_mov_b32 v117, v114\nM 72,180,120\nbuffer_load_dwordx4 v221, s[12:15], s63 offen offset:1024 lds\nv_permlane32_swap_b32 v116, v112\n"
"M 88,176,108\nR 174,184,61440\ns_add_u32 m0, s50, 106496\nv_permlane32_swap_b32 v117, v114\nM 88,180,124\nG 4\n"
"v_add_f32 v112, v112, v116\nv_add_f32 v114, v114, v117\nv_mov_b32 v116, v112\nM 44,160,96\nR 176,185,49152\ns_add_u32 s63, s54, 3440640\n"
"v_mov_b32 v117, v114\nM 44,164,112\nbuffer_load_dwordx4 a[176:179], v252, s[20:23], s64 offen offset:3072 sc1\nv_permlane16_swap_b32 v116, v112\nM 60,160,100\nR 178,185,53248\n"
"v_permlane16_swap_b32 v117, v114\nv_add_f32 v112, v112, v116\nM 60,164,116\nbuffer_load_dwordx4 a[180:183], v252, s[20:23], s65 offen offset:3072 sc1\nv_add_f32 v114, v114, v117\nM 76,160,104\n"
"R 180,185,57344\nbuffer_load_dwordx4 v220, s[12:15], s63 offen lds\nv_and_b32 v120, s77, v112\nM 76,164,120\nbuffer_load_dwordx4 v221, s[12:15], s63 offen offset:1024 lds\nv_sub_f32 v121, v112, v120\n"
"M 92,160,108\nR 182,185,61440\ns_add_u32 m0, s50, 114688\nv_lshlrev_b32 v122, 16, v243\nM 92,164,124\ns_waitcnt vmcnt(20)\n"
"s_barrier\nG 4\nv_cvt_pk_bf16_f32 v116, v120, v121\nM 32,168,128\nR 160,186,49152\ns_add_u32 s63, s54, 128\n"
"v_cvt_pk_bf16_f32 v117, v120, v122\ns_waitcnt vmcnt(18)\nM 32,172,144\nbuffer_load_dwordx4 a[184:187], v252, s[20:23], s66 offen offset:3072 sc1\nv_and_b32 v120, s77, v114\nM 48,168,132\n"
"R 162,186,53248\nv_sub_f32 v121, v114, v120\nv_and_b32 v122, s77, v243\ns_waitcnt vmcnt(18)\nM 48,172,148\nbuffer_load_dwordx4 a[188:191], v252, s[20:23], s67 offen offset:3072 sc1\n"
"v_cvt_pk_bf16_f32 v118, v120, v121\nM 64,168,136\nR 164,186,57344\nbuffer_load_dwordx4 v220, s[12:15], s63 offen lds\nv_cvt_pk_bf16_f32 v119, v120, v122\ns_waitcnt vmcnt(19)\n"
"M 64,172,152\nbuffer_load_dwordx4 v221, s[12:15], s63 offen offset:1024 lds\nv_and_b32 v242, 0x1ff, v242\nM 80,168,140\nR 166,186,61440\ns_add_u32 m0, s50, 0\n"
"v_add_u32 v242, 157696, v242\ns_waitcnt vmcnt(19)\nM 80,172,156\nds_write_b128 v242, v[116:119]\nG 5\nM 36,176,128\n"
"R 168,187,49152\ns_add_u32 s63, s54, 229504\nM 36,180,144\ns_add_u32 s64, s70, 10498048\nbuffer_load_dwordx4 a[192:195], v252, s[20:23], s64 offen offset:0 sc1\nM 52,176,132\n"
"R 170,187,53248\nM 52,180,148\nbuffer_load_dwordx4 v220, s[12:15], s63 offen lds\nM 68,176,136\nR 172,187,57344\nM 68,180,152\n"
"s_add_u32 s65, s70, 10530816\nbuffer_load_dwordx4 a[196:199], v252, s[20:23], s65 offen offset:0 sc1\nM 84,176,140\nR 174,187,61440\nv_lshrrev_b32 v242, 7, v252\nM 84,180,156\n"
"G 5\nv_lshlrev_b32 v242, 9, v242\nM 40,160,128\nR 176,188,0\nbuffer_load_dwordx4 v221, s[12:15], s63 offen offset:1024 lds\nv_and_b32 v243, 0x70, v252\n"
"s_waitcnt vmcnt(21)\nv_min3_u32 v240, v129, v131, v133\nM 40,164,144\ns_add_u32 s66, s71, 10498048\nbuffer_load_dwordx4 a[200:203], v252, s[20:23], s66 offen offset:0 sc1\ns_and_b32 s60, s40, 1\n"
"s_lshl_b32 s60, s60, 7\nv_add3_u32 v242, v242, v243, s60\ns_waitcnt vmcnt(20)\nv_min3_u32 v240, v240, v135, v137\nM 56,160,132\nR 178,188,4096\n"
"s_waitcnt vmcnt(19)\nv_min3_u32 v240, v240, v139, v141\nv_min3_u32 v240, v240, v143, v143\nM 56,164,148\ns_lshl_b32 s61, s42, 14\ns_add_u32 s61, s61, 85458944\n"
"buffer_load_dwordx4 v[144:147], v242, s[20:23], s61 offen sc1\nv_max3_u32 v241, v129, v131, v133\nM 72,160,136\nR 180,188,8192\ns_lshl_b32 s61, s42, 14\ns_add_u32 s61, s61, 85463040\n"
"buffer_load_dwordx4 v[148:151], v242, s[20:23], s61 offen sc1\nv_max3_u32 v241, v241, v135, v137\nM 72,164,152\ns_add_u32 s67, s71, 10530816\nbuffer_load_dwordx4 a[204:207], v252, s[20:23], s67 offen offset:0 sc1\ns_lshl_b32 s61, s42, 14\n"
"s_add_u32 s61, s61, 85467136\nbuffer_load_dwordx4 v[152:155], v242, s[20:23], s61 offen sc1\nv_max3_u32 v241, v241, v139, v141\nM 88,160,140\nR 182,188,12288\ns_lshl_b32 s61, s42, 14\n"
"s_add_u32 s61, s61, 85471232\nbuffer_load_dwordx4 v[156:159], v242, s[20:23], s61 offen sc1\nv_max3_u32 v241, v241, v143, v143\nM 88,164,156\nG 4\nv_bfe_u32 v243, v242, 2, 7\n"
"v_cmp_ne_u32 s[86:87], s46, v240\nv_cmp_ne_u32 s[88:89], s46, v241\nM 44,168,128\nR 160,189,0\nbuffer_load_dword v243, v243, s[32:35], s55 offen offset:0\ns_or_b64 s[86:87], s[86:87], s[88:89]\n"
"s_cbranch_scc1 Lq338\nLq339:\nM 44,172,144\nbuffer_load_dwordx4 a[208:211], v252, s[20:23], s64 offen offset:1024 sc1\nv_add_f32 v128, v128, v136\nM 60,168,132\n"
"R 162,189,4096\nv_add_f32 v130, v130, v138\nv_add_f32 v132, v132, v140\nM 60,172,148\nv_add_f32 v134, v134, v142\nv_add_f32 v128, v128, v132\n"
"v_add_f32 v130, v130, v134\nM 76,168,136\nR 164,189,8192\nv_add_f32_dpp v128, v128, v128 row_ror:8 row_mask:0xf bank_mask:0xf\nv_add_f32_dpp v130, v130, v130 row_ror:8 row_mask:0xf bank_mask:0xf\nM 76,172,152\n"
"buffer_load_dwordx4 a[212:215], v252, s[20:23], s65 offen offset:1024 sc1\nv_mov_b32 v132, v128\nM 92,168,140\nR 166,189,12288\nv_mov_b32 v133, v130\nv_permlane32_swap_b32 v132, v128\n"
"M 92,172,156\nG 4\nv_permlane32_swap_b32 v133, v130\nv_add_f32 v128, v128, v132\nv_add_f32 v130, v130, v133\nM 32,176,160\n"
"R 168,190,0\nv_mov_b32 v132, v128\nv_mov_b32 v133, v130\ns_waitcnt vmcnt(20)\nM 32,180,176\nbuffer_load_dwordx4 a[216:219], v252, s[20:23], s66 offen offset:1024 sc1\n"
"v_permlane16_swap_b32 v132, v128\nM 48,176,164\nR 170,190,4096\nv_permlane16_swap_b32 v133, v130\nv_add_f32 v128, v128, v132\ns_waitcnt vmcnt(20)\n"
"M 48,180,180\nv_add_f32 v130, v130, v133\nv_and_b32 v136, s77, v128\nv_sub_f32 v137, v128, v136\nM 64,176,168\nR 172,190,8192\n"
"v_lshlrev_b32 v138, 16, v239\nv_cvt_pk_bf16_f32 v132, v136, v137\ns_waitcnt vmcnt(17)\nM 64,180,184\nbuffer_load_dwordx4 a[220:223], v252, s[20:23], s67 offen offset:1024 sc1\nM 80,176,172\n"
"R 174,190,12288\nv_cvt_pk_bf16_f32 v133, v136, v138\ns_waitcnt vmcnt(17)\nM 80,180,188\nG 4\nv_and_b32 v136, s77, v130\n"
"v_sub_f32 v137, v130, v136\nM 36,160,160\nR 176,191,0\nv_and_b32 v138, s77, v239\nM 36,164,176\ns_lshr_b32 s84, s49, 3\n"
"v_cvt_pk_bf16_f32 v134, v136, v137\nv_cvt_pk_bf16_f32 v135, v136, v138\nM 52,160,164\nR 178,191,4096\nv_and_b32 v238, 0x1ff, v238\nM 52,164,180\n"
"buffer_load_dwordx4 a[224:227], v252, s[20:23], s64 offen offset:2048 sc1\nM 68,160,168\nR 180,191,8192\nv_add_u32 v238, 158208, v238\nM 68,164,184\ns_add_u32 s84, s84, 82313216\n"
"ds_write_b128 v238, v[132:135]\nM 84,160,172\nR 182,191,12288\nM 84,164,188\nG 5\nM 40,168,160\n"
"R 160,188,16384\nbuffer_load_dwordx4 v[0:3], v252, s[20:23], s84 offen offset:0 sc1\nM 40,172,176\nbuffer_load_dwordx4 a[228:231], v252, s[20:23], s65 offen offset:2048 sc1\nM 56,168,164\nR 162,188,20480\n"
"M 56,172,180\nbuffer_load_dwordx4 a[232:235], v252, s[20:23], s66 offen offset:2048 sc1\nM 72,168,168\nR 164,188,24576\nM 72,172,184\nbuffer_load_dwordx4 v[4:7], v252, s[20:23], s84 offen offset:1024 sc1\n"
"M 88,168,172\nR 166,188,28672\ns_lshr_b32 s85, s49, 3\nM 88,172,188\nbuffer_load_dwordx4 v[8:11], v252, s[20:23], s84 offen offset:2048 sc1\nG 4\n"
"M 44,176,160\nR 168,189,16384\ns_add_u32 s85, s85, 82317312\nM 44,180,176\nbuffer_load_dwordx4 a[236:239], v252, s[20:23], s67 offen offset:2048 sc1\nM 60,176,164\n"
"R 170,189,20480\nbuffer_load_dwordx4 v[12:15], v252, s[20:23], s84 offen offset:3072 sc1\nM 60,180,180\nbuffer_load_dwordx4 a[240:243], v252, s[20:23], s64 offen offset:3072 sc1\nM 76,176,168\nR 172,189,24576\n"
"s_waitcnt vmcnt(17)\nv_min3_u32 v244, v145, v147, v149\ns_waitcnt vmcnt(15)\nv_min3_u32 v244, v244, v151, v153\nM 76,180,184\nbuffer_load_dwordx4 v[16:19], v252, s[20:23], s85 offen offset:0 sc1\n"
"s_waitcnt vmcnt(15)\nv_min3_u32 v244, v244, v155, v157\nM 92,176,172\nR 174,189,28672\ns_add_u32 m0, s50, 8192\nv_min3_u32 v244, v244, v159, v159\n"
"M 92,180,188\nbuffer_load_dwordx4 v[20:23], v252, s[20:23], s85 offen offset:1024 sc1\ns_barrier\nG 4\nv_max3_u32 v245, v145, v147, v149\nM 32,160,192\n"
"R 176,190,16384\ns_add_u32 s63, s54, 458880\nv_max3_u32 v245, v245, v151, v153\ns_waitcnt vmcnt(14)\nM 32,164,208\nbuffer_load_dwordx4 a[244:247], v252, s[20:23], s65 offen offset:3072 sc1\n"
"v_max3_u32 v245, v245, v155, v157\nM 48,160,196\nR 178,190,20480\nbuffer_load_dwordx4 v[24:27], v252, s[20:23], s85 offen offset:2048 sc1\nv_max3_u32 v245, v245, v159, v159\ns_waitcnt vmcnt(15)\n"
"M 48,164,212\nbuffer_load_dwordx4 a[248:251], v252, s[20:23], s66 offen offset:3072 sc1\nv_cmp_ne_u32 s[86:87], s46, v244\nM 64,160,200\nR 180,190,24576\nbuffer_load_dwordx4 v220, s[12:15], s63 offen lds\n"
"v_cmp_ne_u32 s[88:89], s46, v245\ns_waitcnt vmcnt(16)\nM 64,164,216\nbuffer_load_dwordx4 v[28:31], v252, s[20:23], s85 offen offset:3072 sc1\ns_or_b64 s[86:87], s[86:87], s[88:89]\ns_cbranch_scc1 Lq350\n"
"Lq351:\nM 80,160,204\nR 182,190,28672\nv_add_f32 v144, v144, v152\nv_add_f32 v146, v146, v154\ns_waitcnt vmcnt(16)\n"
"M 80,164,220\nG 4\nv_add_f32 v148, v148, v156\nv_add_f32 v150, v150, v158\nv_add_f32 v144, v144, v148\nM 36,168,192\n"
"R 160,191,16384\nbuffer_load_dwordx4 v221, s[12:15], s63 offen offset:1024 lds\nv_add_f32 v146, v146, v150\nM 36,172,208\nbuffer_load_dwordx4 a[252:255], v252, s[20:23], s67 offen offset:3072 sc1\ns_add_u32 s63, s54, 688256\n"
"v_add_f32_dpp v144, v144, v144 row_ror:8 row_mask:0xf bank_mask:0xf\nM 52,168,196\nR 162,191,20480\ns_add_u32 m0, s50, 16384\nv_add_f32_dpp v146, v146, v146 row_ror:8 row_mask:0xf bank_mask:0xf\nM 52,172,212\n"
"buffer_load_dwordx4 v220, s[12:15], s63 offen lds\nv_mov_b32 v148, v144\nM 68,168,200\nR 164,191,24576\nv_mov_b32 v149, v146\nv_permlane32_swap_b32 v148, v144\n"
"M 68,172,216\nbuffer_load_dwordx4 v221, s[12:15], s63 offen offset:1024 lds\nv_permlane32_swap_b32 v149, v146\nM 84,168,204\nR 166,191,28672\nv_add_f32 v144, v144, v148\n"
"v_add_f32 v146, v146, v149\nM 84,172,220\nG 4\nv_mov_b32 v148, v144\nv_mov_b32 v149, v146\ns_nop 0\n"
"v_permlane16_swap_b32 v148, v144\nM 40,176,192\nR 168,188,32768\nv_permlane16_swap_b32 v149, v146\nv_add_f32 v144, v144, v148\nM 40,180,208\n"
"s_add_u32 m0, s50, 24576\nv_add_f32 v146, v146, v149\nv_and_b32 v152, s77, v144\nv_sub_f32 v153, v144, v152\nM 56,176,196\nR 170,188,36864\n"
"v_lshlrev_b32 v154, 16, v243\nv_cvt_pk_bf16_f32 v148, v152, v153\nM 56,180,212\ns_add_u32 s63, s54, 917632\nv_cvt_pk_bf16_f32 v149, v152, v154\nv_and_b32 v152, s77, v146\n"
"M 72,176,200\nR 172,188,40960\nv_sub_f32 v153, v146, v152\nM 72,180,216\nbuffer_load_dwordx4 v220, s[12:15], s63 offen lds\nM 88,176,204\n"
"R 174,188,45056\nv_and_b32 v154, s77, v243\nM 88,180,220\nG 4\nv_cvt_pk_bf16_f32 v150, v152, v153\nv_cvt_pk_bf16_f32 v151, v152, v154\n"
"M 44,160,192\nR 176,189,32768\nv_and_b32 v242, 0x1ff, v242\nM 44,164,208\nbuffer_load_dwordx4 v221, s[12:15], s63 offen offset:1024 lds\nM 60,160,196\n"
"R 178,189,36864\ns_add_u32 s63, s54, 1147008\nM 60,164,212\ns_add_u32 m0, s50, 32768\nv_add_u32 v242, 158720, v242\nds_write_b128 v242, v[148:151]\n"
"M 76,160,200\nR 180,189,40960\nLq575:\ns_lshr_b32 s60, s45, 3\ns_lshl2_add_u32 s60, s60, s40\ns_lshl_b32 s60, s60, 8\n"
"v_lshl_add_u32 v224, v252, 3, s60\nv_add_u32 v224, Lq575+4096-LqPC, v224\ns_mov_b64 exec, 3\nglobal_load_dword v224, v224, s[98:99]\ns_mov_b64 exec, -1\nM 76,164,216\n"
"buffer_load_dwordx4 v220, s[12:15], s63 offen lds\nM 92,160,204\nR 182,189,45056\nM 92,164,220\nG 5\ns_waitcnt vmcnt(23)\n"
"M 32,168,224\nR 160,190,32768\ns_waitcnt vmcnt(15)\nM 32,172,240\nbuffer_load_dwordx4 v221, s[12:15], s63 offen offset:1024 lds\nM 48,168,228\n"
"R 162,190,36864\ns_add_u32 s63, s54, 1376384\ns_waitcnt vmcnt(13)\nM 48,172,244\ns_add_u32 m0, s50, 40960\nM 64,168,232\n"
"R 164,190,40960\ns_waitcnt vmcnt(11)\nM 64,172,248\nbuffer_load_dwordx4 v220, s[12:15], s63 offen lds\nM 80,168,236\nR 166,190,45056\n"
"s_waitcnt vmcnt(8)\nM 80,172,252\nG 4\nM 36,176,224\nR 168,191,32768\nM 36,180,240\n"
"buffer_load_dwordx4 v221, s[12:15], s63 offen offset:1024 lds\nM 52,176,228\nR 170,191,36864\ns_add_u32 s63, s54, 1605760\nM 52,180,244\ns_add_u32 m0, s50, 49152\n"
"M 68,176,232\nR 172,191,40960\nv_min3_u32 v230, v1, v3, v5\nv_min3_u32 v230, v230, v7, v9\nM 68,180,248\nbuffer_load_dwordx4 v220, s[12:15], s63 offen lds\n"
"v_min3_u32 v230, v230, v11, v13\nM 84,176,236\nR 174,191,45056\nv_min3_u32 v230, v230, v15, v17\nv_min3_u32 v230, v230, v19, v21\nM 84,180,252\n"
"G 4\nv_min3_u32 v230, v230, v23, v25\nv_min3_u32 v230, v230, v27, v29\nv_min3_u32 v230, v230, v31, v31\nM 40,160,224\nR 176,188,49152\n"
"v_max3_u32 v231, v1, v3, v5\nv_max3_u32 v231, v231, v7, v9\nM 40,164,240\nbuffer_load_dwordx4 v221, s[12:15], s63 offen offset:1024 lds\nv_max3_u32 v231, v231, v11, v13\nM 56,160,228\n"
"R 178,188,53248\nv_max3_u32 v231, v231, v15, v17\nv_max3_u32 v231, v231, v19, v21\nM 56,164,244\nv_max3_u32 v231, v231, v23, v25\nv_max3_u32 v231, v231, v27, v29\n"
"v_max3_u32 v231, v231, v31, v31\nM 72,160,232\nR 180,184,0\nv_cmp_ne_u32 s[86:87], s46, v230\nv_cmp_ne_u32 s[88:89], s46, v231\nM 72,164,248\n"
"s_add_u32 m0, s50, 57344\ns_or_b64 s[86:87], s[86:87], s[88:89]\ns_cbranch_scc1 Lq312\nLq313:\nv_add_f32 v0, v0, v16\nM 88,160,236\n"
"R 182,184,4096\nv_add_f32 v2, v2, v18\nv_add_f32 v4, v4, v20\nM 88,164,252\nG 4\nv_add_f32 v6, v6, v22\n"
"v_add_f32 v8, v8, v24\nv_add_f32 v10, v10, v26\nM 44,168,224\nR 160,189,49152\nv_add_f32 v12, v12, v28\nv_add_f32 v14, v14, v30\n"
"M 44,172,240\ns_add_u32 s63, s54, 1835136\nv_add_f32 v0, v0, v8\nv_add_f32 v2, v2, v10\nv_add_f32 v4, v4, v12\nM 60,168,228\n"
"R 162,189,53248\nv_add_f32 v6, v6, v14\nv_add_f32 v0, v0, v4\nM 60,172,244\nbuffer_load_dwordx4 v220, s[12:15], s63 offen lds\nv_add_f32 v2, v2, v6\n"
"M 76,168,232\nR 164,185,0\nv_lshrrev_b32 v231, 1, v252\nv_lshrrev_b32 v230, 2, v252\nM 76,172,248\ns_lshl_b32 s62, s40, 9\n"
"s_add_u32 s62, s62, 159232\nv_add_u32 v231, s62, v231\nds_write2_b32 v231, v0, v2 offset1:1\nM 92,168,236\nR 166,185,4096\ns_lshl_b32 s62, s40, 9\n"
"s_add_u32 s62, s62, 159232\nv_add_u32 v230, s62, v230\nM 92,172,252\ns_waitcnt vmcnt(10)\ns_barrier\nds_read2st64_b32 v[232:233], v230 offset1:1\n"
"G 6\nv_mfma_f32_16x16x32_bf16 v[96:99], v[176:179], a[0:3], 0\nR 168,190,49152\nM 96,180,16\ns_mov_b32 m0, s51\nv_mfma_f32_16x16x32_bf16 v[112:115], v[176:179], a[4:7], 0\n"
"R 170,190,53248\nM 112,180,20\nds_write_addtid_b32 v64 offset:0\nv_mfma_f32_16x16x32_bf16 v[128:131], v[176:179], a[8:11], 0\nR 172,186,0\nM 128,180,24\n"
"G 4\nv_add_f32 v232, v232, v233\nv_mov_b32 v233, v232\ns_nop 1\nv_permlane32_swap_b32 v232, v233\nv_mfma_f32_16x16x32_bf16 v[144:147], v[176:179], a[12:15], 0\n"
"R 174,186,4096\nv_mov_b32 v230, v232\nv_mov_b32 v231, v233\nM 144,180,28\nv_permlane16_swap_b32 v230, v232\nv_permlane16_swap_b32 v231, v233\n"
"v_mul_f32 v230, 0xba800000, v230\nv_mfma_f32_16x16x32_bf16 v[100:103], v[160:163], a[0:3], 0\nR 176,191,49152\nv_mul_f32 v214, v230, v230\nv_mul_f32 v231, 0x3a800000, v231\nM 100,164,16\n"
"v_sub_f32 v231, v231, v214\nv_max_f32 v231, 0, v231\nv_add_f32 v231, 0x3727c5ac, v231\nv_mfma_f32_16x16x32_bf16 v[116:119], v[160:163], a[4:7], 0\nR 178,191,53248\nv_and_b32 v208, s77, v230\n"
"v_rsq_f32 v234, v231\nM 116,164,20\nds_write_addtid_b32 v65 offset:256\nv_mul_f32 v216, v231, v234\nv_sub_f32 v230, v230, v208\nv_cvt_pk_bf16_f32 v209, v230, v216\n"
"v_mfma_f32_16x16x32_bf16 v[132:135], v[160:163], a[8:11], 0\nR 180,187,0\nds_write_addtid_b32 v66 offset:512\nv_cvt_pk_bf16_f32 v208, v208, v208\nv_mul_f32 v214, 0xc0135761, v234\nM 132,164,24\n"
"s_mov_b64 s[60:61], 0xffff\nv_cndmask_b32 v208, 0, v208, s[60:61]\nds_write_addtid_b32 v67 offset:768\nv_mul_f32 v215, v234, v234\nv_mul_f32 v215, v215, v214\nv_mfma_f32_16x16x32_bf16 v[148:151], v[160:163], a[12:15], 0\n"
"R 182,187,4096\nds_write_addtid_b32 v68 offset:1024\nv_mul_f32 v215, 0x3d372713, v215\nv_mul_f32 v232, 0xba800000, v232\nM 148,164,28\ns_mov_b64 s[60:61], 0xffff\n"
"v_cndmask_b32 v209, 0, v209, s[60:61]\nds_write_addtid_b32 v69 offset:1280\nG 9\nv_mul_f32 v217, v232, v232\nv_mfma_f32_16x16x32_bf16 v[104:107], v[168:171], a[0:3], 0\nR 160,184,8192\n"
"ds_write_addtid_b32 v70 offset:1536\nv_mul_f32 v233, 0x3a800000, v233\nv_sub_f32 v233, v233, v217\nM 104,172,16\nds_write_addtid_b32 v71 offset:1792\nv_max_f32 v233, 0, v233\n"
"v_add_f32 v233, 0x3727c5ac, v233\nv_rsq_f32 v235, v233\nv_mfma_f32_16x16x32_bf16 v[120:123], v[168:171], a[4:7], 0\nR 162,184,12288\nds_write_addtid_b32 v72 offset:2048\nv_and_b32 v212, s77, v232\n"
"v_mul_f32 v219, v233, v235\nM 120,172,20\nds_write_addtid_b32 v73 offset:2304\nv_sub_f32 v232, v232, v212\nv_cvt_pk_bf16_f32 v213, v232, v219\nv_cvt_pk_bf16_f32 v212, v212, v212\n"
"v_mfma_f32_16x16x32_bf16 v[136:139], v[168:171], a[8:11], 0\nR 164,184,16384\nds_write_addtid_b32 v74 offset:2560\nv_mul_f32 v217, 0xc0135761, v235\nv_mul_f32 v218, v235, v235\nM 136,172,24\n"
"s_mov_b64 s[60:61], 0xffff\nv_cndmask_b32 v212, 0, v212, s[60:61]\nds_write_addtid_b32 v75 offset:2816\nv_mul_f32 v218, v218, v217\nv_mul_f32 v218, 0x3d372713, v218\nv_mfma_f32_16x16x32_bf16 v[152:155], v[168:171], a[12:15], 0\n"
"R 166,184,20480\nds_write_addtid_b32 v76 offset:3072\ns_mov_b64 s[60:61], 0xffff\nv_cndmask_b32 v213, 0, v213, s[60:61]\nM 152,172,28\nds_write_addtid_b32 v77 offset:3328\n"
"ds_write_addtid_b32 v78 offset:3584\nG 15\nv_mfma_f32_16x16x32_bf16 v[108:111], v[176:179], a[0:3], 0\nR 168,185,8192\nds_write_addtid_b32 v79 offset:3840\nM 108,180,16\n"
"ds_write_addtid_b32 v80 offset:4096\nds_write_addtid_b32 v81 offset:4352\nv_mfma_f32_16x16x32_bf16 v[124:127], v[176:179], a[4:7], 0\nR 170,185,12288\nds_write_addtid_b32 v82 offset:4608\nM 124,180,20\n"
"ds_write_addtid_b32 v83 offset:4864\nds_write_addtid_b32 v84 offset:5120\nv_mfma_f32_16x16x32_bf16 v[140:143], v[176:179], a[8:11], 0\nR 172,185,16384\nds_write_addtid_b32 v85 offset:5376\nM 140,180,24\n"
"ds_write_addtid_b32 v86 offset:5632\nds_write_addtid_b32 v87 offset:5888\nv_mfma_f32_16x16x32_bf16 v[156:159], v[176:179], a[12:15], 0\nR 174,185,20480\nds_write_addtid_b32 v88 offset:6144\nM 156,180,28\n"
"ds_write_addtid_b32 v89 offset:6400\nds_write_addtid_b32 v90 offset:6656\nds_write_addtid_b32 v91 offset:6912\nds_write_addtid_b32 v92 offset:7168\nds_write_addtid_b32 v93 offset:7424\nds_write_addtid_b32 v94 offset:7680\n"
"ds_write_addtid_b32 v95 offset:7936\nG 15\nM 96,160,32\nR 176,186,8192\ns_add_u32 m0, s50, 57344\ns_nop 0\n"
"buffer_load_dwordx4 v221, s[12:15], s63 offen offset:1024 lds\nM 96,164,48\ns_add_u32 m0, s50, 65536\ns_add_u32 s63, s54, 2064512\nbuffer_load_dwordx4 v220, s[12:15], s63 offen lds\nM 112,160,36\n"
"R 178,186,12288\ns_waitcnt vmcnt(3) lgkmcnt(2)\ns_barrier\nM 112,164,52\nbuffer_load_dwordx4 v221, s[12:15], s63 offen offset:1024 lds\nds_read2st64_b32 v[64:65], v225 offset0:0 offset1:1\n"
"M 128,160,40\nR 180,186,16384\ns_add_u32 s63, s54, 2293888\nds_read2st64_b32 v[66:67], v225 offset0:2 offset1:3\nM 128,164,56\ns_add_u32 m0, s50, 73728\n"
"ds_read2st64_b32 v[68:69], v225 offset0:4 offset1:5\nds_read2st64_b32 v[70:71], v225 offset0:6 offset1:7\nM 144,160,44\nR 182,186,20480\nds_read2st64_b32 v[72:73], v225 offset0:8 offset1:9\nM 144,164,60\n"
"ds_read2st64_b32 v[74:75], v225 offset0:10 offset1:11\nds_read2st64_b32 v[76:77], v225 offset0:12 offset1:13\nM 100,168,32\nR 160,187,8192\nds_read2st64_b32 v[78:79], v225 offset0:14 offset1:15\nM 100,172,48\n"
"buffer_load_dwordx4 v220, s[12:15], s63 offen lds\nG 10\nv_add_f32 v32, v32, v64\nM 116,168,36\nR 162,187,12288\nv_add_f32 v33, v33, v65\n"
"G 9\nv_add_f32 v34, v34, v66\nM 116,172,52\nbuffer_load_dwordx4 v221, s[12:15], s63 offen offset:1024 lds\nv_add_f32 v35, v35, v67\nM 132,168,40\n"
"R 164,187,16384\nv_bfe_u32 v250, v252, 5, 1\nG 9\nv_add_f32 v36, v36, v68\nv_mul_u32_u24 v250, 2560, v250\nM 132,172,56\n"
"ds_read2st64_b32 v[80:81], v225 offset0:16 offset1:17\nv_and_b32 v251, 16, v252\nv_lshl_add_u32 v250, v251, 1, v250\ns_lshl_b32 s60, s90, 6\nv_add_u32 v250, s60, v250\nM 148,168,44\n"
"R 166,187,20480\nv_add_f32 v37, v37, v69\nG 10\nv_add_f32 v38, v38, v70\nM 148,172,60\nds_read2st64_b32 v[82:83], v225 offset0:18 offset1:19\n"
"G 10\nv_add_f32 v39, v39, v71\nM 104,176,32\nR 168,184,24576\nG 10\nv_add_f32 v40, v40, v72\n"
"v_add_f32 v41, v41, v73\nM 104,180,48\ns_add_u32 m0, s50, 81920\nds_read2st64_b32 v[84:85], v225 offset0:20 offset1:21\nG 10\nv_add_f32 v42, v42, v74\n"
"M 120,176,36\nR 170,184,28672\nv_add_f32 v43, v43, v75\nG 10\nv_add_f32 v44, v44, v76\nM 120,180,52\n"
"s_add_u32 s63, s54, 2523264\nds_read2st64_b32 v[86:87], v225 offset0:22 offset1:23\nv_add_f32 v45, v45, v77\nM 136,176,40\nR 172,184,32768\nG 10\n"
"v_add_f32 v46, v46, v78\nv_add_f32 v47, v47, v79\nM 136,180,56\nbuffer_load_dwordx4 v220, s[12:15], s63 offen lds\nG 7\nv_add_f32 v48, v48, v80\n"
"M 152,176,44\nR 174,184,36864\nv_add_f32 v49, v49, v81\nG 6\nv_add_f32 v50, v50, v82\nM 152,180,60\n"
"ds_read2st64_b32 v[88:89], v225 offset0:24 offset1:25\nv_add_f32 v51, v51, v83\nM 108,160,32\nR 176,185,24576\nG 6\nv_add_f32 v52, v52, v84\n"
"v_add_f32 v53, v53, v85\nM 108,164,48\nbuffer_load_dwordx4 v221, s[12:15], s63 offen offset:1024 lds\nG 4\nv_add_f32 v54, v54, v86\nM 124,160,36\n"
"R 178,185,28672\ns_mov_b32 s60, 157184\nv_bfe_u32 v246, v252, 4, 4\nv_lshl_add_u32 v246, v246, 3, s60\nv_add_f32 v55, v55, v87\nM 124,164,52\n"
"ds_read_b64 v[238:239], v246 offset:0\ns_add_u32 m0, s50, 90112\nds_read2st64_b32 v[90:91], v225 offset0:26 offset1:27\nM 140,160,40\nR 180,185,32768\nds_read_b64 v[240:241], v246 offset:128\n"
"ds_read2st64_b32 v[92:93], v225 offset0:28 offset1:29\nM 140,164,56\nds_read_b64 v[242:243], v246 offset:256\ns_add_u32 s63, s54, 2752640\nG 8\nv_add_f32 v56, v56, v88\n"
"M 156,160,44\nR 182,185,36864\nds_read_b64 v[244:245], v246 offset:384\nv_add_f32 v57, v57, v89\nM 156,164,60\nds_read_b64 v[246:247], v246 offset:0\n"
"M 96,168,64\nR 160,186,24576\nds_read2st64_b32 v[94:95], v225 offset0:30 offset1:31\nM 96,172,80\nbuffer_load_dwordx4 v220, s[12:15], s63 offen lds\nM 112,168,68\n"
"R 162,186,28672\nG 10\nv_add_f32 v58, v58, v90\nM 112,172,84\nv_add_f32 v59, v59, v91\nG 7\n"
"v_add_f32 v60, v60, v92\nM 128,168,72\nR 164,186,32768\nv_add_f32 v61, v61, v93\nM 128,172,88\nbuffer_load_dwordx4 v221, s[12:15], s63 offen offset:1024 lds\n"
"M 144,168,76\nR 166,186,36864\ns_add_u32 m0, s50, 98304\nM 144,172,92\nG 7\nG 3\n"
"v_add_f32 v62, v62, v94\nv_add_f32 v63, v63, v95\nM 100,176,64\nR 168,187,24576\nv_mfma_f32_16x16x32_bf16 v[32:35], v[238:241], v[208:211], v[32:35]\ns_add_u32 s63, s54, 2982016\n"
"M 100,180,80\nv_mfma_f32_16x16x32_bf16 v[36:39], v[240:243], v[208:211], v[36:39]\nbuffer_load_dwordx4 v220, s[12:15], s63 offen lds\nM 116,176,68\nR 170,187,28672\nv_mfma_f32_16x16x32_bf16 v[40:43], v[242:245], v[208:211], v[40:43]\n"
"M 116,180,84\nv_mfma_f32_16x16x32_bf16 v[44:47], v[244:247], v[208:211], v[44:47]\nbuffer_load_dwordx4 v221, s[12:15], s63 offen offset:1024 lds\nM 132,176,72\nR 172,187,32768\nv_mfma_f32_16x16x32_bf16 v[48:51], v[244:247], v[210:213], v[48:51]\n"
"s_add_u32 s63, s54, 3211392\nM 132,180,88\nv_mfma_f32_16x16x32_bf16 v[52:55], v[238:241], v[210:213], v[52:55]\ns_add_u32 m0, s50, 106496\nM 148,176,76\nR 174,187,36864\n"
"v_mfma_f32_16x16x32_bf16 v[56:59], v[240:243], v[210:213], v[56:59]\nM 148,180,92\nv_mfma_f32_16x16x32_bf16 v[60:63], v[242:245], v[210:213], v[60:63]\nG 4\nM 104,160,64\nR 176,184,40960\n"
"M 104,164,80\nbuffer_load_dwordx4 v220, s[12:15], s63 offen lds\nM 120,160,68\nR 178,184,45056\nM 120,164,84\nM 136,160,72\n"
"R 180,184,49152\ns_waitcnt vmcnt(7)\ns_barrier\nM 136,164,88\nM 152,160,76\nR 182,184,53248\n"
"v_mul_f32 v230, v32, v32\nv_mul_f32 v231, v33, v33\nM 152,164,92\ns_add_u32 s84, s73, 23068672\ns_add_u32 s85, s73, 23183360\nG 4\n"
"v_mul_f32 v232, v34, v34\nM 108,168,64\nR 160,185,40960\nv_mul_f32 v233, v35, v35\nv_fma_f32 v230, v230, v215, v214\nM 108,172,80\n"
"buffer_load_dwordx4 v221, s[12:15], s63 offen offset:1024 lds\nv_fma_f32 v231, v231, v215, v214\nM 124,168,68\nR 162,185,45056\nv_fma_f32 v232, v232, v215, v214\nv_fma_f32 v233, v233, v215, v214\n"
"M 124,172,84\ns_add_u32 m0, s50, 114688\nv_mul_f32 v230, v32, v230\nv_mul_f32 v231, v33, v231\nv_mul_f32 v232, v34, v232\nM 140,168,72\n"
"R 164,185,49152\nv_mul_f32 v233, v35, v233\nv_exp_f32 v230, v230\nM 140,172,88\ns_add_u32 s63, s54, 3440768\nv_exp_f32 v231, v231\n"
"v_exp_f32 v232, v232\nM 156,168,76\nR 166,185,53248\nv_fma_f32 v230, v230, v216, v216\nv_exp_f32 v233, v233\nM 156,172,92\n"
"G 4\nv_rcp_f32 v230, v230\nv_fma_f32 v231, v231, v216, v216\nM 96,176,96\nR 168,186,40960\nv_fma_f32 v232, v232, v216, v216\n"
"v_fma_f32 v233, v233, v216, v216\nM 96,180,112\nbuffer_load_dwordx4 v220, s[12:15], s63 offen lds\nv_rcp_f32 v231, v231\nM 112,176,100\nR 170,186,45056\n"
"v_mul_f32 v230, v32, v230\nv_rcp_f32 v232, v232\nM 112,180,116\nv_rcp_f32 v233, v233\nv_mul_f32 v231, v33, v231\nM 128,176,104\n"
"R 172,186,49152\nv_mul_f32 v232, v34, v232\nv_mul_f32 v233, v35, v233\nM 128,180,120\nv_cvt_pk_bf16_f32 v32, v230, v231\nv_cvt_pk_bf16_f32 v33, v232, v233\n"
"v_mul_f32 v234, v36, v36\nM 144,176,108\nR 174,186,53248\nv_mul_f32 v235, v37, v37\nv_mul_f32 v236, v38, v38\nM 144,180,124\n"
"G 4\nv_mul_f32 v237, v39, v39\nv_fma_f32 v234, v234, v215, v214\nv_fma_f32 v235, v235, v215, v214\nM 100,160,96\nR 176,187,40960\n"
"v_fma_f32 v236, v236, v215, v214\nv_fma_f32 v237, v237, v215, v214\nM 100,164,112\nbuffer_load_dwordx4 v221, s[12:15], s63 offen offset:1024 lds\nv_mul_f32 v234, v36, v234\nM 116,160,100\n"
"R 178,187,45056\nv_mul_f32 v235, v37, v235\nv_mul_f32 v236, v38, v236\nM 116,164,116\ns_add_u32 m0, s50, 0\nv_exp_f32 v234, v234\n"
"v_mul_f32 v237, v39, v237\nM 132,160,104\nR 180,187,49152\nv_fma_f32 v234, v234, v216, v216\nv_exp_f32 v235, v235\nM 132,164,120\n"
"s_add_u32 s63, s55, 0\nv_exp_f32 v236, v236\nv_exp_f32 v237, v237\nM 148,160,108\nR 182,187,53248\nv_fma_f32 v235, v235, v216, v216\n"
"v_fma_f32 v236, v236, v216, v216\nM 148,164,124\nG 4\nv_rcp_f32 v234, v234\nv_fma_f32 v237, v237, v216, v216\nM 104,168,96\n"
"R 160,184,57344\nv_mul_f32 v234, v36, v234\nv_rcp_f32 v235, v235\nM 104,172,112\nbuffer_load_dwordx4 v222, s[12:15], s63 offen lds\nv_rcp_f32 v236, v236\n"
"M 120,168,100\nR 162,184,61440\nv_mul_f32 v235, v37, v235\nv_rcp_f32 v237, v237\nM 120,172,116\nv_mul_f32 v236, v38, v236\n"
"v_mul_f32 v237, v39, v237\nv_cvt_pk_bf16_f32 v34, v234, v235\nM 136,168,104\nR 164,188,0\nv_cvt_pk_bf16_f32 v35, v236, v237\nv_mul_f32 v230, v40, v40\n"
"M 136,172,120\nbuffer_store_dwordx4 v[32:35], v252, s[20:23], s84 offen offset:0 sc1\nv_mul_f32 v231, v41, v41\nM 152,168,108\nR 166,188,4096\nv_mul_f32 v232, v42, v42\n"
"v_mul_f32 v233, v43, v43\nM 152,172,124\nG 4\nv_fma_f32 v230, v230, v215, v214\nv_fma_f32 v231, v231, v215, v214\nv_fma_f32 v232, v232, v215, v214\n"
"M 108,176,96\nR 168,185,57344\nv_fma_f32 v233, v233, v215, v214\nv_mul_f32 v230, v40, v230\nM 108,180,112\nbuffer_load_dwordx4 v223, s[12:15], s63 offen offset:1024 lds\n"
"v_mul_f32 v231, v41, v231\nM 124,176,100\nR 170,185,61440\nv_mul_f32 v232, v42, v232\nv_mul_f32 v233, v43, v233\nM 124,180,116\n"
"s_add_u32 m0, s50, 8192\nv_exp_f32 v230, v230\nv_exp_f32 v231, v231\nM 140,176,104\nR 172,189,0\nv_fma_f32 v230, v230, v216, v216\n"
"v_exp_f32 v232, v232\nM 140,180,120\ns_add_u32 s63, s55, 458752\nv_exp_f32 v233, v233\nv_fma_f32 v231, v231, v216, v216\nM 156,176,108\n"
"R 174,189,4096\nv_fma_f32 v232, v232, v216, v216\nv_fma_f32 v233, v233, v216, v216\nM 156,180,124\nG 4\nv_rcp_f32 v230, v230\n"
"v_rcp_f32 v231, v231\nM 96,160,128\nR 176,186,57344\nv_mul_f32 v230, v40, v230\nv_rcp_f32 v232, v232\nM 96,164,144\n"
"buffer_load_dwordx4 v222, s[12:15], s63 offen lds\nv_rcp_f32 v233, v233\nM 112,160,132\nR 178,186,61440\nv_mul_f32 v231, v41, v231\nv_mul_f32 v232, v42, v232\n"
"M 112,164,148\nv_mul_f32 v233, v43, v233\nv_cvt_pk_bf16_f32 v40, v230, v231\nv_cvt_pk_bf16_f32 v41, v232, v233\nM 128,160,136\nR 180,190,0\n"
"v_mul_f32 v234, v44, v44\nv_mul_f32 v235, v45, v45\nM 128,164,152\nbuffer_load_dwordx4 v223, s[12:15], s63 offen offset:1024 lds\nv_mul_f32 v236, v46, v46\nM 144,160,140\n"
"R 182,190,4096\ns_add_u32 s63, s55, 917504\nv_mul_f32 v237, v47, v47\nM 144,164,156\nG 4\nv_fma_f32 v234, v234, v215, v214\n"
"v_fma_f32 v235, v235, v215, v214\nv_fma_f32 v236, v236, v215, v214\nM 100,168,128\nR 160,187,57344\nv_fma_f32 v237, v237, v215, v214\nv_mul_f32 v234, v44, v234\n"
"M 100,172,144\ns_add_u32 m0, s50, 16384\nv_exp_f32 v234, v234\nv_mul_f32 v235, v45, v235\nM 116,168,132\nR 162,187,61440\n"
"v_mul_f32 v236, v46, v236\nv_mul_f32 v237, v47, v237\nM 116,172,148\nbuffer_load_dwordx4 v222, s[12:15], s63 offen lds\nv_exp_f32 v235, v235\nM 132,168,136\n"
"R 164,191,0\nv_fma_f32 v234, v234, v216, v216\nv_exp_f32 v236, v236\nM 132,172,152\nv_exp_f32 v237, v237\nv_fma_f32 v235, v235, v216, v216\n"
"M 148,168,140\nR 166,191,4096\nv_fma_f32 v236, v236, v216, v216\nv_fma_f32 v237, v237, v216, v216\nM 148,172,156\nG 4\n"
"v_rcp_f32 v234, v234\nv_rcp_f32 v235, v235\nM 104,176,128\nR 168,188,8192\ns_waitcnt vmcnt(10)\ns_barrier\n"
"v_rcp_f32 v236, v236\nM 104,180,144\nbuffer_load_dwordx4 v223, s[12:15], s63 offen offset:1024 lds\nv_rcp_f32 v237, v237\nM 120,176,132\nR 170,188,12288\n"
"v_mul_f32 v234, v44, v234\nv_mul_f32 v235, v45, v235\nM 120,180,148\ns_add_u32 m0, s50, 24576\nv_mul_f32 v236, v46, v236\nv_mul_f32 v237, v47, v237\n"
"v_cvt_pk_bf16_f32 v42, v234, v235\nM 136,176,136\nR 172,188,16384\nv_cvt_pk_bf16_f32 v43, v236, v237\nv_mul_f32 v230, v48, v48\nM 136,180,152\n"
"s_add_u32 s63, s55, 1376256\nv_mul_f32 v231, v49, v49\nv_mul_f32 v232, v50, v50\nv_mul_f32 v233, v51, v51\nM 152,176,140\nR 174,188,20480\n"
"v_fma_f32 v230, v230, v218, v217\nv_fma_f32 v231, v231, v218, v217\nM 152,180,156\nG 4\nv_fma_f32 v232, v232, v218, v217\nv_fma_f32 v233, v233, v218, v217\n"
"v_mul_f32 v230, v48, v230\nM 108,160,128\nR 176,189,8192\nv_mul_f32 v231, v49, v231\nv_mul_f32 v232, v50, v232\nM 108,164,144\n"
"buffer_load_dwordx4 v222, s[12:15], s63 offen lds\nv_mul_f32 v233, v51, v233\nM 124,160,132\nR 178,189,12288\nv_mul_f32 v234, v52, v52\nv_exp_f32 v230, v230\n"
"M 124,164,148\nbuffer_store_dwordx4 v[40:43], v252, s[20:23], s84 offen offset:1024 sc1\nv_exp_f32 v231, v231\nM 140,160,136\nR 180,189,16384\nv_fma_f32 v230, v230, v219, v219\n"
"v_exp_f32 v232, v232\nM 140,164,152\nv_exp_f32 v233, v233\nv_fma_f32 v231, v231, v219, v219\nM 156,160,140\nR 182,189,20480\n"
"v_fma_f32 v232, v232, v219, v219\nv_fma_f32 v233, v233, v219, v219\nM 156,164,156\nG 4\nv_rcp_f32 v230, v230\nv_rcp_f32 v231, v231\n"
"M 96,168,160\nR 160,190,8192\nv_mul_f32 v230, v48, v230\nv_rcp_f32 v232, v232\nM 96,172,176\nbuffer_load_dwordx4 v223, s[12:15], s63 offen offset:1024 lds\n"
"v_rcp_f32 v233, v233\nM 112,168,164\nR 162,190,12288\nv_mul_f32 v231, v49, v231\nv_mul_f32 v232, v50, v232\nM 112,172,180\n"
"s_add_u32 m0, s50, 32768\nv_mul_f32 v233, v51, v233\nv_cvt_pk_bf16_f32 v48, v230, v231\nv_cvt_pk_bf16_f32 v49, v232, v233\nM 128,168,168\nR 164,190,16384\n"
"v_mul_f32 v235, v53, v53\nv_mul_f32 v236, v54, v54\nM 128,172,184\ns_add_u32 s63, s55, 1835008\nv_mul_f32 v237, v55, v55\nv_fma_f32 v234, v234, v218, v217\n"
"v_fma_f32 v235, v235, v218, v217\nM 144,168,172\nR 166,190,20480\nv_fma_f32 v236, v236, v218, v217\nv_fma_f32 v237, v237, v218, v217\nM 144,172,188\n"
"G 4\nv_mul_f32 v234, v52, v234\nv_mul_f32 v235, v53, v235\nv_mul_f32 v236, v54, v236\nM 100,176,160\nR 168,191,8192\n"
"v_mul_f32 v237, v55, v237\nv_exp_f32 v234, v234\nM 100,180,176\nbuffer_load_dwordx4 v222, s[12:15], s63 offen lds\nv_exp_f32 v235, v235\nM 116,176,164\n"
"R 170,191,12288\nv_fma_f32 v234, v234, v219, v219\nv_exp_f32 v236, v236\nM 116,180,180\nv_exp_f32 v237, v237\nv_fma_f32 v235, v235, v219, v219\n"
"M 132,176,168\nR 172,191,16384\nv_fma_f32 v236, v236, v219, v219\nv_fma_f32 v237, v237, v219, v219\nM 132,180,184\nv_rcp_f32 v234, v234\n"
"v_rcp_f32 v235, v235\nM 148,176,172\nR 174,191,20480\nv_mul_f32 v234, v52, v234\nv_rcp_f32 v236, v236\nM 148,180,188\n"
"G 4\nv_rcp_f32 v237, v237\nv_mul_f32 v235, v53, v235\nM 104,160,160\nR 176,188,24576\nv_mul_f32 v236, v54, v236\n"
"v_mul_f32 v237, v55, v237\nM 104,164,176\nbuffer_load_dwordx4 v223, s[12:15], s63 offen offset:1024 lds\nv_cvt_pk_bf16_f32 v50, v234, v235\nM 120,160,164\nR 178,188,28672\n"
"s_add_u32 s63, s55, 2293760\nv_cvt_pk_bf16_f32 v51, v236, v237\nM 120,164,180\ns_add_u32 m0, s50, 40960\nv_mul_f32 v230, v56, v56\nv_mul_f32 v231, v57, v57\n"
"v_mul_f32 v232, v58, v58\nM 136,160,168\nR 180,188,32768\nv_mul_f32 v233, v59, v59\nv_fma_f32 v230, v230, v218, v217\nM 136,164,184\n"
"buffer_load_dwordx4 v222, s[12:15], s63 offen lds\nM 152,160,172\nR 182,188,36864\nv_fma_f32 v231, v231, v218, v217\nM 152,164,188\nG 4\n"
"v_fma_f32 v232, v232, v218, v217\nv_fma_f32 v233, v233, v218, v217\nM 108,168,160\nR 160,189,24576\nv_mul_f32 v230, v56, v230\nM 108,172,176\n"
"buffer_store_dwordx4 v[48:51], v252, s[20:23], s85 offen offset:0 sc1\nM 124,168,164\nR 162,189,28672\nv_mul_f32 v231, v57, v231\nM 124,172,180\nbuffer_load_dwordx4 v223, s[12:15], s63 offen offset:1024 lds\n"
"M 140,168,168\nR 164,189,32768\ns_add_u32 s63, s55, 2752512\nM 140,172,184\ns_add_u32 m0, s50, 49152\nv_exp_f32 v230, v230\n"
"M 156,168,172\nR 166,189,36864\nv_mul_f32 v232, v58, v232\nM 156,172,188\nG 4\nv_exp_f32 v231, v231\n"
"M 96,176,192\nR 168,190,24576\nv_mul_f32 v233, v59, v233\nM 96,180,208\nbuffer_load_dwordx4 v222, s[12:15], s63 offen lds\nM 112,176,196\n"
"R 170,190,28672\ns_waitcnt vmcnt(13)\ns_barrier\nM 112,180,212\nv_exp_f32 v232, v232\nM 128,176,200\n"
"R 172,190,32768\nv_fma_f32 v230, v230, v219, v219\nM 128,180,216\nv_exp_f32 v233, v233\nM 144,176,204\nR 174,190,36864\n"
"v_fma_f32 v231, v231, v219, v219\nM 144,180,220\nG 4\nv_rcp_f32 v230, v230\nM 100,160,192\nR 176,191,24576\n"
"v_fma_f32 v232, v232, v219, v219\nM 100,164,208\nbuffer_load_dwordx4 v223, s[12:15], s63 offen offset:1024 lds\nM 116,160,196\nR 178,191,28672\ns_mov_b32 s60, 157696\n"
"v_bfe_u32 v246, v252, 4, 4\nv_lshl_add_u32 v246, v246, 3, s60\nM 116,164,212\nds_read_b64 v[238:239], v246 offset:0\ns_add_u32 m0, s50, 57344\nM 132,160,200\n"
"R 180,191,32768\nds_read_b64 v[240:241], v246 offset:128\nM 132,164,216\nds_read_b64 v[242:243], v246 offset:256\ns_add_u32 s63, s55, 3211264\nM 148,160,204\n"
"R 182,191,36864\nds_read_b64 v[244:245], v246 offset:384\nM 148,164,220\nds_read_b64 v[246:247], v246 offset:0\nG 9\nM 104,168,192\n"
"R 160,188,40960\nv_fma_f32 v233, v233, v219, v219\nM 104,172,208\nbuffer_load_dwordx4 v222, s[12:15], s63 offen lds\nM 120,168,196\nR 162,188,45056\n"
"v_mul_f32 v230, v56, v230\nM 120,172,212\nv_rcp_f32 v231, v231\nM 136,168,200\nR 164,188,49152\nv_mul_f32 v231, v57, v231\n"
"M 136,172,216\nv_rcp_f32 v232, v232\nM 152,168,204\nR 166,188,53248\nv_mul_f32 v232, v58, v232\nM 152,172,220\n"
"G 6\nv_rcp_f32 v233, v233\nM 108,176,192\nR 168,189,40960\nv_mfma_f32_16x16x32_bf16 v[96:99], v[238:241], v[208:211], v[96:99]\nv_mul_f32 v233, v59, v233\n"
"M 108,180,208\nbuffer_load_dwordx4 v223, s[12:15], s63 offen offset:1024 lds\nM 124,176,196\nR 170,189,45056\nv_mfma_f32_16x16x32_bf16 v[100:103], v[240:243], v[208:211], v[100:103]\nv_cvt_pk_bf16_f32 v56, v230, v231\n"
"M 124,180,212\ns_add_u32 m0, s50, 65536\nv_cvt_pk_bf16_f32 v57, v232, v233\nv_mul_f32 v234, v60, v60\nM 140,176,200\nR 172,189,49152\n"
"G 8\nv_mfma_f32_16x16x32_bf16 v[104:107], v[242:245], v[208:211], v[104:107]\nv_mul_f32 v235, v61, v61\nM 140,180,216\ns_add_u32 s63, s54, 256\nv_mul_f32 v236, v62, v62\n"
"v_mul_f32 v237, v63, v63\nM 156,176,204\nR 174,189,53248\nG 8\nv_mfma_f32_16x16x32_bf16 v[108:111], v[244:247], v[208:211], v[108:111]\nv_fma_f32 v234, v234, v218, v217\n"
"M 156,180,220\nG 4\nv_fma_f32 v235, v235, v218, v217\nv_fma_f32 v236, v236, v218, v217\nM 96,160,224\nR 176,190,40960\n"
"v_mfma_f32_16x16x32_bf16 v[112:115], v[244:247], v[210:213], v[112:115]\nv_fma_f32 v237, v237, v218, v217\nM 96,164,240\nbuffer_load_dwordx4 v220, s[12:15], s63 offen lds\nM 112,160,228\nR 178,190,45056\n"
"v_mfma_f32_16x16x32_bf16 v[116:119], v[238:241], v[210:213], v[116:119]\nv_mul_f32 v234, v60, v234\nM 112,164,244\nv_exp_f32 v234, v234\nM 128,160,232\nR 180,190,49152\n"
"v_mfma_f32_16x16x32_bf16 v[120:123], v[240:243], v[210:213], v[120:123]\nv_mul_f32 v235, v61, v235\nM 128,164,248\nbuffer_load_dwordx4 v221, s[12:15], s63 offen offset:1024 lds\nM 144,160,236\nR 182,190,53248\n"
"v_mfma_f32_16x16x32_bf16 v[124:127], v[242:245], v[210:213], v[124:127]\ns_add_u32 s63, s54, 229632\nM 144,164,252\nG 4\nv_exp_f32 v235, v235\nM 100,168,224\n"
"R 160,191,40960\nv_mul_f32 v236, v62, v236\nM 100,172,240\ns_add_u32 m0, s50, 73728\nv_exp_f32 v236, v236\nM 116,168,228\n"
"R 162,191,45056\nv_mul_f32 v237, v63, v237\nM 116,172,244\nbuffer_load_dwordx4 v220, s[12:15], s63 offen lds\nM 132,168,232\nR 164,191,49152\n"
"v_fma_f32 v234, v234, v219, v219\nM 132,172,248\nv_exp_f32 v237, v237\nM 148,168,236\nR 166,191,53248\nv_fma_f32 v235, v235, v219, v219\n"
"M 148,172,252\nG 4\nv_rcp_f32 v234, v234\nM 104,176,224\nR 168,184,0\nv_fma_f32 v236, v236, v219, v219\n"
"M 104,180,240\nbuffer_load_dwordx4 v221, s[12:15], s63 offen offset:1024 lds\nM 120,176,228\nR 170,184,4096\nv_fma_f32 v237, v237, v219, v219\nM 120,180,244\n"
"s_add_u32 m0, s50, 81920\nv_rcp_f32 v235, v235\nM 136,176,232\nR 172,186,0\nv_mul_f32 v234, v60, v234\nM 136,180,248\n"
"s_add_u32 s63, s54, 459008\nv_rcp_f32 v236, v236\nM 152,176,236\nR 174,186,4096\nv_mul_f32 v235, v61, v235\nM 152,180,252\n"
"G 4\nv_rcp_f32 v237, v237\nM 108,160,224\nR 176,185,0\nv_mul_f32 v236, v62, v236\nM 108,164,240\n"
"buffer_load_dwordx4 v220, s[12:15], s63 offen lds\nM 124,160,228\nR 178,185,4096\nv_mul_f32 v237, v63, v237\nM 124,164,244\nv_cvt_pk_bf16_f32 v58, v234, v235\n"
"v_cvt_pk_bf16_f32 v59, v236, v237\nM 140,160,232\nR 180,187,0\nbuffer_store_dwordx4 v[56:59], v252, s[20:23], s85 offen offset:1024 sc1\nM 140,164,248\nbuffer_load_dwordx4 v221, s[12:15], s63 offen offset:1024 lds\n"
"M 156,160,236\nR 182,187,4096\nM 156,164,252\ns_waitcnt vmcnt(16)\ns_barrier\nG 4\n"
"v_mfma_f32_16x16x32_bf16 v[0:3], v[168:171], a[0:3], 0\nR 160,184,8192\nM 0,172,16\ns_mov_b32 m0, s51\nv_mfma_f32_16x16x32_bf16 v[16:19], v[168:171], a[4:7], 0\nR 162,184,12288\n"
"M 16,172,20\nds_write_addtid_b32 v128 offset:0\nv_mfma_f32_16x16x32_bf16 v[8:11], v[168:171], a[8:11], 0\nR 164,186,8192\nM 8,172,24\nds_write_addtid_b32 v129 offset:256\n"
"v_mfma_f32_16x16x32_bf16 v[24:27], v[168:171], a[12:15], 0\nR 166,186,12288\nM 24,172,28\nds_write_addtid_b32 v130 offset:512\nG 7\nv_mfma_f32_16x16x32_bf16 v[4:7], v[176:179], a[0:3], 0\n"
"R 168,185,8192\nM 4,180,16\nds_write_addtid_b32 v131 offset:768\nv_mfma_f32_16x16x32_bf16 v[20:23], v[176:179], a[4:7], 0\nR 170,185,12288\nM 20,180,20\n"
"ds_write_addtid_b32 v132 offset:1024\nv_mfma_f32_16x16x32_bf16 v[12:15], v[176:179], a[8:11], 0\nR 172,187,8192\nM 12,180,24\nds_write_addtid_b32 v133 offset:1280\nv_mfma_f32_16x16x32_bf16 v[28:31], v[176:179], a[12:15], 0\n"
"R 174,187,12288\nM 28,180,28\nds_write_addtid_b32 v134 offset:1536\nds_write_addtid_b32 v135 offset:1792\nG 10\nM 0,160,32\n"
"R 176,184,16384\nds_write_addtid_b32 v136 offset:2048\nM 0,164,48\nds_write_addtid_b32 v137 offset:2304\nds_write_addtid_b32 v138 offset:2560\nM 16,160,36\n"
"R 178,184,20480\nds_write_addtid_b32 v139 offset:2816\nM 16,164,52\nds_write_addtid_b32 v140 offset:3072\nds_write_addtid_b32 v141 offset:3328\nM 8,160,40\n"
"R 180,186,16384\nds_write_addtid_b32 v142 offset:3584\nM 8,164,56\nds_write_addtid_b32 v143 offset:3840\nds_write_addtid_b32 v144 offset:4096\nM 24,160,44\n"
"R 182,186,20480\nds_write_addtid_b32 v145 offset:4352\nM 24,164,60\nds_write_addtid_b32 v146 offset:4608\nds_write_addtid_b32 v147 offset:4864\nG 15\n"
"M 4,168,32\nR 160,185,16384\nds_write_addtid_b32 v148 offset:5120\nM 4,172,48\nds_write_addtid_b32 v149 offset:5376\nds_write_addtid_b32 v150 offset:5632\n"
"M 20,168,36\nR 162,185,20480\nds_write_addtid_b32 v151 offset:5888\nM 20,172,52\nds_write_addtid_b32 v152 offset:6144\nds_write_addtid_b32 v153 offset:6400\n"
"M 12,168,40\nR 164,187,16384\nds_write_addtid_b32 v154 offset:6656\nM 12,172,56\nds_write_addtid_b32 v155 offset:6912\nds_write_addtid_b32 v156 offset:7168\n"
"M 28,168,44\nR 166,187,20480\nds_write_addtid_b32 v157 offset:7424\nM 28,172,60\nds_write_addtid_b32 v158 offset:7680\nds_write_addtid_b32 v159 offset:7936\n"
"G 15\nM 0,176,64\nR 168,184,24576\ns_add_u32 m0, s50, 90112\ns_add_u32 s63, s54, 688384\nM 0,180,80\n"
"buffer_load_dwordx4 v220, s[12:15], s63 offen lds\nM 16,176,68\nR 170,184,28672\ns_waitcnt vmcnt(10) lgkmcnt(2)\ns_barrier\nM 16,180,84\n"
"ds_read2st64_b32 v[128:129], v225 offset0:0 offset1:1\nM 8,176,72\nR 172,186,24576\nM 8,180,88\nds_read2st64_b32 v[130:131], v225 offset0:2 offset1:3\nds_read2st64_b32 v[132:133], v225 offset0:4 offset1:5\n"
"M 24,176,76\nR 174,186,28672\nds_read2st64_b32 v[134:135], v225 offset0:6 offset1:7\nM 24,180,92\nds_read2st64_b32 v[136:137], v225 offset0:8 offset1:9\nds_read2st64_b32 v[138:139], v225 offset0:10 offset1:11\n"
"M 4,160,64\nR 176,185,24576\nds_read2st64_b32 v[140:141], v225 offset0:12 offset1:13\nM 4,164,80\nbuffer_load_dwordx4 v221, s[12:15], s63 offen offset:1024 lds\nM 20,160,68\n"
"R 178,185,28672\nG 10\nv_add_f32 v96, v96, v128\nM 20,164,84\ns_add_u32 m0, s50, 98304\nds_read2st64_b32 v[142:143], v225 offset0:14 offset1:15\n"
"v_add_f32 v97, v97, v129\nM 12,160,72\nR 180,187,24576\nG 10\nv_add_f32 v98, v98, v130\nM 12,164,88\n"
"s_add_u32 s63, s54, 917760\nds_read2st64_b32 v[144:145], v225 offset0:16 offset1:17\nv_add_f32 v99, v99, v131\nM 28,160,76\nR 182,187,28672\nG 11\n"
"v_add_f32 v100, v100, v132\nM 28,164,92\nds_read2st64_b32 v[146:147], v225 offset0:18 offset1:19\nG 11\nv_add_f32 v101, v101, v133\nM 0,168,96\n"
"R 160,184,32768\nG 11\nv_add_f32 v102, v102, v134\nM 0,172,112\nbuffer_load_dwordx4 v220, s[12:15], s63 offen lds\nM 16,168,100\n"
"R 162,184,36864\nv_add_f32 v103, v103, v135\nM 16,172,116\nds_read2st64_b32 v[148:149], v225 offset0:20 offset1:21\nG 12\nv_add_f32 v104, v104, v136\n"
"M 8,168,104\nR 164,186,32768\nv_add_f32 v105, v105, v137\nM 8,172,120\nbuffer_load_dwordx4 v221, s[12:15], s63 offen offset:1024 lds\nM 24,168,108\n"
"R 166,186,36864\nG 13\nv_add_f32 v106, v106, v138\nM 24,172,124\nds_read2st64_b32 v[150:151], v225 offset0:22 offset1:23\nG 7\n"
"v_add_f32 v107, v107, v139\nM 4,176,96\nR 168,185,32768\nv_add_f32 v108, v108, v140\nM 4,180,112\nds_read2st64_b32 v[152:153], v225 offset0:24 offset1:25\n"
"v_add_f32 v109, v109, v141\nM 20,176,100\nR 170,185,36864\nv_add_f32 v110, v110, v142\nM 20,180,116\nds_read2st64_b32 v[154:155], v225 offset0:26 offset1:27\n"
"v_add_f32 v111, v111, v143\nM 12,176,104\nR 172,187,32768\nv_add_f32 v112, v112, v144\nM 12,180,120\nds_read2st64_b32 v[156:157], v225 offset0:28 offset1:29\n"
"v_add_f32 v113, v113, v145\nM 28,176,108\nR 174,187,36864\nG 13\nv_add_f32 v114, v114, v146\nM 28,180,124\n"
"ds_read2st64_b32 v[158:159], v225 offset0:30 offset1:31\nG 9\nv_add_f32 v115, v115, v147\nM 0,160,128\nR 176,184,40960\nv_add_f32 v116, v116, v148\n"
"M 0,164,144\nv_add_f32 v117, v117, v149\nG 9\nv_add_f32 v118, v118, v150\nM 16,160,132\nR 178,184,45056\n"
"v_add_f32 v119, v119, v151\nM 16,164,148\nG 8\nv_add_f32 v120, v120, v152\nv_add_f32 v121, v121, v153\nM 8,160,136\n"
"R 180,186,40960\nG 7\nv_add_f32 v122, v122, v154\nM 8,164,152\nv_add_f32 v123, v123, v155\nG 5\n"
"v_add_f32 v124, v124, v156\nM 24,160,140\nR 182,186,45056\nv_add_f32 v125, v125, v157\nM 24,164,156\nG 5\n"
"G 4\nv_add_f32 v126, v126, v158\nv_add_f32 v127, v127, v159\nM 4,168,128\nR 160,185,40960\nv_mul_f32 v230, v96, v96\n"
"v_mul_f32 v231, v97, v97\nM 4,172,144\ns_add_u32 s84, s73, 23070720\ns_add_u32 s85, s73, 23185408\nv_mul_f32 v232, v98, v98\nv_mul_f32 v233, v99, v99\n"
"M 20,168,132\nR 162,185,45056\ns_mov_b32 s60, 158720\nv_bfe_u32 v242, v252, 4, 4\nv_lshl_add_u32 v242, v242, 3, s60\nv_fma_f32 v230, v230, v215, v214\n"
"M 20,172,148\nds_read_b64 v[238:239], v242 offset:0\nv_fma_f32 v231, v231, v215, v214\nv_fma_f32 v232, v232, v215, v214\nM 12,168,136\nR 164,187,40960\n"
"ds_read_b64 v[240:241], v242 offset:128\nv_fma_f32 v233, v233, v215, v214\nM 12,172,152\nds_read_b64 v[242:243], v242 offset:0\nv_mul_f32 v230, v96, v230\nv_mul_f32 v231, v97, v231\n"
"M 28,168,140\nR 166,187,45056\nv_mul_f32 v232, v98, v232\nv_mul_f32 v233, v99, v233\nM 28,172,156\nG 7\n"
"v_exp_f32 v230, v230\nv_exp_f32 v231, v231\nM 0,176,160\nR 168,184,49152\nv_fma_f32 v230, v230, v216, v216\nv_exp_f32 v232, v232\n"
"M 0,180,176\nv_exp_f32 v233, v233\nv_fma_f32 v231, v231, v216, v216\nM 16,176,164\nR 170,184,53248\nv_fma_f32 v232, v232, v216, v216\n"
"v_fma_f32 v233, v233, v216, v216\nM 16,180,180\nv_rcp_f32 v230, v230\nv_rcp_f32 v231, v231\nM 8,176,168\nR 172,186,49152\n"
"v_mul_f32 v230, v96, v230\nv_rcp_f32 v232, v232\nM 8,180,184\nv_rcp_f32 v233, v233\nv_mul_f32 v231, v97, v231\nM 24,176,172\n"
"R 174,186,53248\nv_mul_f32 v232, v98, v232\nv_mul_f32 v233, v99, v233\nM 24,180,188\nG 4\nv_cvt_pk_bf16_f32 v96, v230, v231\n"
"v_cvt_pk_bf16_f32 v97, v232, v233\nv_mul_f32 v234, v100, v100\nM 4,160,160\nR 176,185,49152\nv_mfma_f32_16x16x32_bf16 v[0:3], v[238:241], v[208:211], v[0:3]\ns_waitcnt vmcnt(7)\n"
"s_barrier\nv_mul_f32 v235, v101, v101\nM 4,164,176\ns_add_u32 m0, s50, 106496\nv_mul_f32 v236, v102, v102\nv_mul_f32 v237, v103, v103\n"
"v_fma_f32 v234, v234, v215, v214\nM 20,160,164\nR 178,185,53248\nv_mfma_f32_16x16x32_bf16 v[4:7], v[240:243], v[208:211], v[4:7]\nv_fma_f32 v235, v235, v215, v214\nv_fma_f32 v236, v236, v215, v214\n"
"M 20,164,180\ns_add_u32 s63, s54, 1147136\nv_fma_f32 v237, v237, v215, v214\nv_mul_f32 v234, v100, v234\nv_mul_f32 v235, v101, v235\nM 12,160,168\n"
"R 180,187,49152\nv_mov_b32 v226, s46\nv_mfma_f32_16x16x32_bf16 v[16:19], v[240:243], v[210:213], v[16:19]\nv_mul_f32 v236, v102, v236\nM 12,164,184\nv_mov_b32 v227, s47\n"
"v_mul_f32 v237, v103, v237\nv_exp_f32 v234, v234\nM 28,160,172\nR 182,187,53248\ns_add_u32 s61, s81, 385024\ns_lshl_b32 s62, s44, 5\n"
"s_add_u32 s61, s61, s62\ns_waitcnt vmcnt(0)\ns_mov_b64 exec, 1\nbuffer_atomic_swap_x2 v[226:227], off, s[20:23], s61 sc1\ns_mov_b64 exec, -1\nv_mfma_f32_16x16x32_bf16 v[20:23], v[238:241], v[210:213], v[20:23]\n"
"v_exp_f32 v235, v235\nM 28,164,188\nbuffer_load_dwordx4 v220, s[12:15], s63 offen lds\nG 4\nv_exp_f32 v236, v236\nM 0,168,192\n"
"R 160,184,57344\nbuffer_load_dwordx4 v221, s[12:15], s63 offen offset:1024 lds\nv_exp_f32 v237, v237\nM 0,172,208\ns_add_u32 m0, s50, 114688\ns_add_u32 s63, s54, 1376512\n"
"buffer_load_dwordx4 v220, s[12:15], s63 offen lds\nv_fma_f32 v234, v234, v216, v216\nM 16,168,196\nR 162,184,61440\nv_fma_f32 v235, v235, v216, v216\nv_fma_f32 v236, v236, v216, v216\n"
"M 16,172,212\nbuffer_load_dwordx4 v221, s[12:15], s63 offen offset:1024 lds\nv_fma_f32 v237, v237, v216, v216\nM 8,168,200\nR 164,186,57344\nv_mul_f32 v230, v104, v104\n"
"v_rcp_f32 v234, v234\nM 8,172,216\nv_rcp_f32 v235, v235\nv_rcp_f32 v236, v236\nM 24,168,204\nR 166,186,61440\n"
"v_mul_f32 v234, v100, v234\nv_rcp_f32 v237, v237\nM 24,172,220\nG 4\nv_mul_f32 v235, v101, v235\nv_mul_f32 v236, v102, v236\n"
"v_mul_f32 v237, v103, v237\nM 4,176,192\nR 168,185,57344\nv_cvt_pk_bf16_f32 v98, v234, v235\nv_cvt_pk_bf16_f32 v99, v236, v237\nM 4,180,208\n"
"buffer_store_dwordx4 v[96:99], v252, s[20:23], s84 offen offset:0 sc1\nv_mul_f32 v231, v105, v105\nM 20,176,196\nR 170,185,61440\nv_mul_f32 v232, v106, v106\nv_mul_f32 v233, v107, v107\n"
"M 20,180,212\ns_add_u32 m0, s50, 0\nv_fma_f32 v230, v230, v215, v214\nv_fma_f32 v231, v231, v215, v214\nv_fma_f32 v232, v232, v215, v214\nM 12,176,200\n"
"R 172,187,57344\nLq577:\ns_lshr_b32 s60, s45, 3\ns_lshl2_add_u32 s60, s60, s40\ns_lshl_b32 s60, s60, 8\nv_lshl_add_u32 v224, v252, 3, s60\n"
"v_add_u32 v224, Lq577+4096-LqPC, v224\ns_mov_b64 exec, 3\nglobal_load_dword v224, v224, s[98:99]\ns_mov_b64 exec, -1\nv_fma_f32 v233, v233, v215, v214\nM 12,180,216\n"
"s_add_u32 s63, s54, 1605888\nv_mul_f32 v230, v104, v230\nv_mul_f32 v231, v105, v231\nv_mul_f32 v232, v106, v232\nM 28,176,204\nR 174,187,61440\n"
"v_mul_f32 v233, v107, v233\nv_exp_f32 v230, v230\nM 28,180,220\nG 4\nv_exp_f32 v231, v231\nv_exp_f32 v232, v232\n"
"M 0,160,224\nR 176,188,0\nv_fma_f32 v230, v230, v216, v216\nv_exp_f32 v233, v233\nM 0,164,240\nbuffer_load_dwordx4 v220, s[12:15], s63 offen lds\n"
"v_fma_f32 v231, v231, v216, v216\nM 16,160,228\nR 178,188,4096\nv_fma_f32 v232, v232, v216, v216\nv_fma_f32 v233, v233, v216, v216\nM 16,164,244\n"
"v_rcp_f32 v230, v230\nv_rcp_f32 v231, v231\nM 8,160,232\nR 180,188,8192\nv_mul_f32 v230, v104, v230\nv_rcp_f32 v232, v232\n"
"M 8,164,248\nbuffer_load_dwordx4 v221, s[12:15], s63 offen offset:1024 lds\nv_rcp_f32 v233, v233\nM 24,160,236\nR 182,188,12288\ns_add_u32 s63, s54, 1835264\n"
"v_mul_f32 v231, v105, v231\nM 24,164,252\nG 4\nv_mul_f32 v232, v106, v232\nv_mul_f32 v233, v107, v233\nv_cvt_pk_bf16_f32 v104, v230, v231\n"
"M 4,168,224\nR 160,189,0\nv_cvt_pk_bf16_f32 v105, v232, v233\nv_mul_f32 v234, v108, v108\nM 4,172,240\ns_add_u32 m0, s50, 8192\n"
"v_mul_f32 v235, v109, v109\nv_mul_f32 v236, v110, v110\nv_mul_f32 v237, v111, v111\nM 20,168,228\nR 162,189,4096\nv_fma_f32 v234, v234, v215, v214\n"
"v_fma_f32 v235, v235, v215, v214\nM 20,172,244\nbuffer_load_dwordx4 v220, s[12:15], s63 offen lds\nv_fma_f32 v236, v236, v215, v214\nM 12,168,232\nR 164,189,8192\n"
"v_fma_f32 v237, v237, v215, v214\nv_mul_f32 v234, v108, v234\nM 12,172,248\nv_exp_f32 v234, v234\nv_mul_f32 v235, v109, v235\nM 28,168,236\n"
"R 166,189,12288\nv_mul_f32 v236, v110, v236\nv_mul_f32 v237, v111, v237\nM 28,172,252\ns_barrier\nG 4\n"
"v_exp_f32 v235, v235\nv_mfma_f32_16x16x32_bf16 v[32:35], v[176:179], a[0:3], 0\nR 168,190,0\nv_fma_f32 v234, v234, v216, v216\nv_exp_f32 v236, v236\nM 32,180,16\n"
"s_mov_b32 m0, s51\nv_exp_f32 v237, v237\nv_fma_f32 v235, v235, v216, v216\nv_mfma_f32_16x16x32_bf16 v[48:51], v[176:179], a[4:7], 0\nR 170,190,4096\nv_fma_f32 v236, v236, v216, v216\n"
"v_fma_f32 v237, v237, v216, v216\nM 48,180,20\nv_rcp_f32 v234, v234\nv_rcp_f32 v235, v235\nv_mfma_f32_16x16x32_bf16 v[64:67], v[176:179], a[8:11], 0\nR 172,190,8192\n"
"v_mul_f32 v234, v108, v234\nv_rcp_f32 v236, v236\nM 64,180,24\nv_rcp_f32 v237, v237\nv_mul_f32 v235, v109, v235\nv_mfma_f32_16x16x32_bf16 v[80:83], v[176:179], a[12:15], 0\n"
"R 174,190,12288\nds_write_addtid_b32 v8 offset:0\nv_mul_f32 v236, v110, v236\nv_mul_f32 v237, v111, v237\nM 80,180,28\nds_write_addtid_b32 v9 offset:256\n"
"G 6\nv_cvt_pk_bf16_f32 v106, v234, v235\nv_cvt_pk_bf16_f32 v107, v236, v237\nv_mul_f32 v230, v112, v112\nv_mfma_f32_16x16x32_bf16 v[36:39], v[160:163], a[0:3], 0\nR 176,191,0\n"
"ds_write_addtid_b32 v10 offset:512\nv_mul_f32 v231, v113, v113\nv_mul_f32 v232, v114, v114\nM 36,164,16\nbuffer_store_dwordx4 v[104:107], v252, s[20:23], s84 offen offset:1024 sc1\nds_write_addtid_b32 v11 offset:768\n"
"v_mul_f32 v233, v115, v115\nv_mfma_f32_16x16x32_bf16 v[52:55], v[160:163], a[4:7], 0\nR 178,191,4096\nds_write_addtid_b32 v12 offset:1024\nv_fma_f32 v230, v230, v218, v217\nv_fma_f32 v231, v231, v218, v217\n"
"M 52,164,20\nds_write_addtid_b32 v13 offset:1280\nv_fma_f32 v232, v232, v218, v217\nv_fma_f32 v233, v233, v218, v217\nv_mul_f32 v230, v112, v230\nv_mfma_f32_16x16x32_bf16 v[68:71], v[160:163], a[8:11], 0\n"
"R 180,191,8192\nds_write_addtid_b32 v14 offset:1536\nv_mul_f32 v231, v113, v231\nv_mul_f32 v232, v114, v232\nM 68,164,24\nds_write_addtid_b32 v15 offset:1792\n"
"v_exp_f32 v230, v230\nv_mul_f32 v233, v115, v233\nv_mfma_f32_16x16x32_bf16 v[84:87], v[160:163], a[12:15], 0\nR 182,191,12288\nds_write_addtid_b32 v24 offset:4096\nv_fma_f32 v230, v230, v219, v219\n"
"v_exp_f32 v231, v231\nM 84,164,28\nds_write_addtid_b32 v25 offset:4352\nG 14\nv_exp_f32 v232, v232\nv_exp_f32 v233, v233\n"
"v_mfma_f32_16x16x32_bf16 v[40:43], v[168:171], a[0:3], 0\nR 160,188,16384\nds_write_addtid_b32 v26 offset:4608\nds_write_addtid_b32 v27 offset:4864\nds_write_addtid_b32 v28 offset:5120\nds_write_addtid_b32 v29 offset:5376\n"
"ds_write_addtid_b32 v30 offset:5632\nds_write_addtid_b32 v31 offset:5888\nv_fma_f32 v231, v231, v219, v219\nv_fma_f32 v232, v232, v219, v219\nM 40,172,16\ns_add_u32 m0, s50, 8192\n"
"s_nop 0\nbuffer_load_dwordx4 v221, s[12:15], s63 offen offset:1024 lds\nv_fma_f32 v233, v233, v219, v219\nv_mfma_f32_16x16x32_bf16 v[56:59], v[168:171], a[4:7], 0\nR 162,188,20480\nv_mul_f32 v234, v116, v116\n"
"v_rcp_f32 v230, v230\nM 56,172,20\ns_waitcnt vmcnt(7) lgkmcnt(1)\ns_barrier\nv_mul_f32 v230, v112, v230\nv_rcp_f32 v231, v231\n"
"v_mfma_f32_16x16x32_bf16 v[72:75], v[168:171], a[8:11], 0\nR 164,188,24576\nv_mov_b32 v248, 0\nv_rcp_f32 v232, v232\nM 72,172,24\ns_add_u32 m0, s50, 16384\n"
"ds_read2st64_b32 v[8:9], v225 offset0:0 offset1:1\nv_mov_b32 v249, 0\nv_mul_f32 v231, v113, v231\nv_rcp_f32 v233, v233\nv_mfma_f32_16x16x32_bf16 v[88:91], v[168:171], a[12:15], 0\nR 166,188,28672\n"
"s_add_u32 s61, s82, 385024\ns_mov_b64 exec, 0xf\nbuffer_atomic_or_x2 v[248:249], v250, s[20:23], s61 offen sc0 sc1\ns_mov_b64 exec, -1\nv_mul_f32 v232, v114, v232\nM 88,172,28\n"
"ds_read2st64_b32 v[10:11], v225 offset0:2 offset1:3\nv_mul_f32 v233, v115, v233\nv_cvt_pk_bf16_f32 v112, v230, v231\nv_cvt_pk_bf16_f32 v113, v232, v233\nv_mfma_f32_16x16x32_bf16 v[44:47], v[176:179], a[0:3], 0\nR 168,189,16384\n"
"ds_read2st64_b32 v[12:13], v225 offset0:4 offset1:5\nv_mul_f32 v235, v117, v117\nv_mul_f32 v236, v118, v118\nM 44,180,16\ns_add_u32 s63, s54, 2064640\nds_read2st64_b32 v[14:15], v225 offset0:6 offset1:7\n"
"v_mul_f32 v237, v119, v119\nv_fma_f32 v234, v234, v218, v217\nv_fma_f32 v235, v235, v218, v217\nv_mfma_f32_16x16x32_bf16 v[60:63], v[176:179], a[4:7], 0\nR 170,189,20480\nds_read2st64_b32 v[24:25], v225 offset0:16 offset1:17\n"
"v_fma_f32 v236, v236, v218, v217\nv_fma_f32 v237, v237, v218, v217\nM 60,180,20\nbuffer_load_dwordx4 v220, s[12:15], s63 offen lds\nG 7\nv_add_f32 v0, v0, v8\n"
"v_mfma_f32_16x16x32_bf16 v[76:79], v[176:179], a[8:11], 0\nR 172,189,24576\nds_read2st64_b32 v[26:27], v225 offset0:18 offset1:19\nv_add_f32 v1, v1, v9\nM 76,180,24\nds_read2st64_b32 v[28:29], v225 offset0:20 offset1:21\n"
"G 8\nv_add_f32 v2, v2, v10\nv_mfma_f32_16x16x32_bf16 v[92:95], v[176:179], a[12:15], 0\nR 174,189,28672\nv_add_f32 v3, v3, v11\nM 92,180,28\n"
"ds_read2st64_b32 v[30:31], v225 offset0:22 offset1:23\nG 8\nv_add_f32 v4, v4, v12\nM 32,160,32\nR 176,190,16384\nv_add_f32 v5, v5, v13\n"
"M 32,164,48\nbuffer_load_dwordx4 v221, s[12:15], s63 offen offset:1024 lds\nM 48,160,36\nR 178,190,20480\nG 9\nv_add_f32 v6, v6, v14\n"
"M 48,164,52\ns_add_u32 m0, s50, 24576\nv_add_f32 v7, v7, v15\nG 7\nv_add_f32 v16, v16, v24\nM 64,160,40\n"
"R 180,190,24576\nv_add_f32 v17, v17, v25\nM 64,164,56\ns_add_u32 s63, s54, 2294016\nG 6\nv_add_f32 v18, v18, v26\n"
"v_add_f32 v19, v19, v27\nM 80,160,44\nR 182,190,28672\nG 6\nv_add_f32 v20, v20, v28\nM 80,164,60\n"
"G 5\nv_add_f32 v21, v21, v29\nG 4\nv_add_f32 v22, v22, v30\nM 36,168,32\nR 160,191,16384\n"
"v_add_f32 v23, v23, v31\nM 36,172,48\nbuffer_load_dwordx4 v220, s[12:15], s63 offen lds\nM 52,168,36\nR 162,191,20480\nv_mul_f32 v234, v116, v234\n"
"M 52,172,52\nv_exp_f32 v234, v234\nM 68,168,40\nR 164,191,24576\nv_mul_f32 v235, v117, v235\nM 68,172,56\n"
"buffer_load_dwordx4 v221, s[12:15], s63 offen offset:1024 lds\nM 84,168,44\nR 166,191,28672\ns_add_u32 s63, s54, 2523392\nM 84,172,60\nG 4\n"
"v_exp_f32 v235, v235\nM 40,176,32\nR 168,188,32768\nv_mul_f32 v236, v118, v236\nM 40,180,48\ns_add_u32 m0, s50, 32768\n"
"v_exp_f32 v236, v236\nM 56,176,36\nR 170,188,36864\nv_mul_f32 v237, v119, v237\nM 56,180,52\nbuffer_load_dwordx4 v220, s[12:15], s63 offen lds\n"
"M 72,176,40\nR 172,188,40960\nv_fma_f32 v234, v234, v219, v219\nM 72,180,56\ns_add_u32 s64, s68, 23068672\nv_exp_f32 v237, v237\n"
"M 88,176,44\nR 174,188,45056\nv_fma_f32 v235, v235, v219, v219\nM 88,180,60\nG 4\nv_rcp_f32 v234, v234\n"
"M 44,160,32\nR 176,189,32768\ns_waitcnt vmcnt(5)\nv_cmp_eq_u64 s[86:87], s[46:47], v[248:249]\nM 44,164,48\nbuffer_load_dwordx4 v221, s[12:15], s63 offen offset:1024 lds\n"
"M 60,160,36\nR 178,189,36864\ns_and_b32 s62, s86, 0xf\ns_cmp_eq_u32 s62, 0xf\ns_cbranch_scc0 Lq382\nLq383:\n"
"M 60,164,52\ns_add_u32 s65, s68, 23183360\nv_rcp_f32 v235, v235\nM 76,160,40\nR 180,189,40960\nv_fma_f32 v236, v236, v219, v219\n"
"M 76,164,56\nbuffer_load_dwordx4 a[0:3], v252, s[20:23], s64 offen sc1\nM 92,160,44\nR 182,189,45056\nv_fma_f32 v237, v237, v219, v219\nM 92,164,60\n"
"G 4\nv_rcp_f32 v236, v236\nM 32,168,64\nR 160,190,32768\nv_mul_f32 v234, v116, v234\nM 32,172,80\n"
"s_add_u32 s66, s69, 23068672\nv_rcp_f32 v237, v237\nM 48,168,68\nR 162,190,36864\nv_mul_f32 v235, v117, v235\nM 48,172,84\n"
"buffer_load_dwordx4 a[4:7], v252, s[20:23], s65 offen sc1\nM 64,168,72\nR 164,190,40960\nv_mul_f32 v236, v118, v236\nM 64,172,88\nv_mul_f32 v237, v119, v237\n"
"v_cvt_pk_bf16_f32 v114, v234, v235\nM 80,168,76\nR 166,190,45056\nv_cvt_pk_bf16_f32 v115, v236, v237\nM 80,172,92\nG 4\n"
"v_mul_f32 v230, v120, v120\nv_mul_f32 v231, v121, v121\nM 36,176,64\nR 168,191,32768\nv_mul_f32 v232, v122, v122\nM 36,180,80\n"
"buffer_load_dwordx4 a[8:11], v252, s[20:23], s66 offen sc1\nM 52,176,68\nR 170,191,36864\nv_mul_f32 v233, v123, v123\nM 52,180,84\ns_add_u32 s67, s69, 23183360\n"
"v_fma_f32 v230, v230, v218, v217\nv_fma_f32 v231, v231, v218, v217\nM 68,176,72\nR 172,191,40960\ns_add_u32 m0, s50, 40960\nM 68,180,88\n"
"s_add_u32 s64, s68, 23075840\nv_fma_f32 v232, v232, v218, v217\nv_fma_f32 v233, v233, v218, v217\nM 84,176,76\nR 174,191,45056\ns_add_u32 s63, s54, 2752768\n"
"M 84,180,92\nG 4\nv_mul_f32 v230, v120, v230\nv_mul_f32 v231, v121, v231\nM 40,160,64\nR 176,188,49152\n"
"s_waitcnt vmcnt(7)\ns_barrier\nM 40,164,80\nbuffer_load_dwordx4 a[12:15], v252, s[20:23], s67 offen sc1\nbuffer_load_dwordx4 v220, s[12:15], s63 offen lds\nM 56,160,68\n"
"R 178,188,53248\nv_mul_f32 v232, v122, v232\nM 56,164,84\ns_add_u32 s65, s68, 23190528\nv_exp_f32 v230, v230\nM 72,160,72\n"
"R 180,184,0\nv_mul_f32 v233, v123, v233\nM 72,164,88\nbuffer_load_dwordx4 a[16:19], v252, s[20:23], s64 offen sc1\nbuffer_load_dwordx4 v221, s[12:15], s63 offen offset:1024 lds\nM 88,160,76\n"
"R 182,184,4096\ns_add_u32 m0, s50, 49152\nM 88,164,92\nG 4\nv_exp_f32 v231, v231\nM 44,168,64\n"
"R 160,189,49152\ns_add_u32 s63, s54, 2982144\nM 44,172,80\ns_add_u32 s66, s69, 23075840\nbuffer_load_dwordx4 v220, s[12:15], s63 offen lds\nM 60,168,68\n"
"R 162,189,53248\nv_fma_f32 v230, v230, v219, v219\nM 60,172,84\nbuffer_load_dwordx4 a[20:23], v252, s[20:23], s65 offen sc1\nM 76,168,72\nR 164,185,0\n"
"v_fma_f32 v231, v231, v219, v219\nM 76,172,88\nbuffer_load_dwordx4 v221, s[12:15], s63 offen offset:1024 lds\nM 92,168,76\nR 166,185,4096\nv_mul_f32 v234, v124, v124\n"
"M 92,172,92\nG 4\nv_exp_f32 v232, v232\nM 32,176,96\nR 168,190,49152\nv_fma_f32 v232, v232, v219, v219\n"
"M 32,180,112\nbuffer_load_dwordx4 a[24:27], v252, s[20:23], s66 offen sc1\nM 48,176,100\nR 170,190,53248\nv_mul_f32 v235, v125, v125\nM 48,180,116\n"
"s_add_u32 s67, s69, 23190528\nv_exp_f32 v233, v233\nM 64,176,104\nR 172,186,0\nv_fma_f32 v233, v233, v219, v219\nM 64,180,120\n"
"buffer_store_dwordx4 v[112:115], v252, s[20:23], s85 offen offset:0 sc1\nM 80,176,108\nR 174,186,4096\nv_mul_f32 v236, v126, v126\nM 80,180,124\nG 4\n"
"v_rcp_f32 v230, v230\nM 36,160,96\nR 176,191,49152\nv_mul_f32 v230, v120, v230\nM 36,164,112\nbuffer_load_dwordx4 a[28:31], v252, s[20:23], s67 offen sc1\n"
"M 52,160,100\nR 178,191,53248\nv_mul_f32 v237, v127, v127\nM 52,164,116\ns_add_u32 s64, s68, 23069696\nv_rcp_f32 v231, v231\n"
"M 68,160,104\nR 180,187,0\nv_mul_f32 v231, v121, v231\nM 68,164,120\ns_add_u32 s65, s68, 23184384\nv_rcp_f32 v232, v232\n"
"M 84,160,108\nR 182,187,4096\nv_mul_f32 v232, v122, v232\nM 84,164,124\nG 4\nv_rcp_f32 v233, v233\n"
"M 40,168,96\nR 160,184,8192\nv_mul_f32 v233, v123, v233\nM 40,172,112\nbuffer_load_dwordx4 a[32:35], v252, s[20:23], s64 offen sc1\nM 56,168,100\n"
"R 162,184,12288\nv_cvt_pk_bf16_f32 v120, v230, v231\nM 56,172,116\ns_add_u32 s66, s69, 23069696\nv_cvt_pk_bf16_f32 v121, v232, v233\nv_fma_f32 v234, v234, v218, v217\n"
"M 72,168,104\nR 164,184,16384\nv_fma_f32 v235, v235, v218, v217\nM 72,172,120\nbuffer_load_dwordx4 a[36:39], v252, s[20:23], s65 offen sc1\nM 88,168,108\n"
"R 166,184,20480\nv_fma_f32 v236, v236, v218, v217\nM 88,172,124\nG 4\nv_fma_f32 v237, v237, v218, v217\nv_mul_f32 v234, v124, v234\n"
"M 44,176,96\nR 168,185,8192\ns_add_u32 m0, s50, 57344\nM 44,180,112\ns_add_u32 s67, s69, 23184384\nv_exp_f32 v234, v234\n"
"M 60,176,100\nR 170,185,12288\ns_add_u32 s63, s54, 3211520\nM 60,180,116\nbuffer_load_dwordx4 a[40:43], v252, s[20:23], s66 offen sc1\nM 76,176,104\n"
"R 172,185,16384\nv_mul_f32 v235, v125, v235\nM 76,180,120\nbuffer_load_dwordx4 v220, s[12:15], s63 offen lds\nM 92,176,108\nR 174,185,20480\n"
"v_mul_f32 v236, v126, v236\nM 92,180,124\nG 4\nv_exp_f32 v235, v235\nM 32,160,128\nR 176,186,8192\n"
"v_mul_f32 v237, v127, v237\nM 32,164,144\nbuffer_load_dwordx4 a[44:47], v252, s[20:23], s67 offen sc1\nbuffer_load_dwordx4 v221, s[12:15], s63 offen offset:1024 lds\nM 48,160,132\nR 178,186,12288\n"
"s_add_u32 m0, s50, 65536\nM 48,164,148\ns_add_u32 s64, s68, 23076864\nv_exp_f32 v236, v236\nM 64,160,136\nR 180,186,16384\n"
"s_add_u32 s63, s54, 3440896\nM 64,164,152\ns_add_u32 s65, s68, 23191552\nbuffer_load_dwordx4 v220, s[12:15], s63 offen lds\nM 80,160,140\nR 182,186,20480\n"
"v_fma_f32 v234, v234, v219, v219\nM 80,164,156\nG 4\nv_exp_f32 v237, v237\nM 36,168,128\nR 160,187,8192\n"
"v_fma_f32 v235, v235, v219, v219\nM 36,172,144\nbuffer_load_dwordx4 a[48:51], v252, s[20:23], s64 offen sc1\nbuffer_load_dwordx4 v221, s[12:15], s63 offen offset:1024 lds\nM 52,168,132\nR 162,187,12288\n"
"v_fma_f32 v236, v236, v219, v219\nM 52,172,148\ns_add_u32 s66, s69, 23076864\nv_rcp_f32 v234, v234\nM 68,168,136\nR 164,187,16384\n"
"s_waitcnt vmcnt(12)\ns_barrier\nM 68,172,152\nbuffer_load_dwordx4 a[52:55], v252, s[20:23], s65 offen sc1\ns_movk_i32 s63, 2048\nv_mad_u32_u24 v220, v228, s63, v229\n"
"v_add_u32 v221, 916480, v220\ns_add_u32 m0, s50, 73728\ns_add_u32 s63, s56, 0\nbuffer_load_dwordx4 v220, s[16:19], s63 offen lds\nM 84,168,140\nR 166,187,20480\n"
"v_fma_f32 v237, v237, v219, v219\nM 84,172,156\nG 4\nv_rcp_f32 v235, v235\nM 40,176,128\nR 168,184,24576\n"
"v_mul_f32 v234, v124, v234\nM 40,180,144\ns_add_u32 s67, s69, 23191552\nbuffer_load_dwordx4 v221, s[16:19], s63 offen offset:1024 lds\nM 56,176,132\nR 170,184,28672\n"
"s_add_u32 m0, s50, 81920\nM 56,180,148\nbuffer_load_dwordx4 a[56:59], v252, s[20:23], s66 offen sc1\nM 72,176,136\nR 172,184,32768\ns_add_u32 s63, s56, 524288\n"
"M 72,180,152\nbuffer_load_dwordx4 v220, s[16:19], s63 offen lds\nM 88,176,140\nR 174,184,36864\nv_mul_f32 v235, v125, v235\nM 88,180,156\n"
"G 4\nv_rcp_f32 v236, v236\nM 44,160,128\nR 176,185,24576\nv_mul_f32 v236, v126, v236\nM 44,164,144\n"
"buffer_load_dwordx4 a[60:63], v252, s[20:23], s67 offen sc1\nbuffer_load_dwordx4 v221, s[16:19], s63 offen offset:1024 lds\nM 60,160,132\nR 178,185,28672\nv_cvt_pk_bf16_f32 v122, v234, v235\nM 60,164,148\n"
"v_rcp_f32 v237, v237\nM 76,160,136\nR 180,185,32768\nv_mul_f32 v237, v127, v237\nM 76,164,152\ns_add_u32 m0, s50, 90112\n"
"v_cvt_pk_bf16_f32 v123, v236, v237\nM 92,160,140\nR 182,185,36864\nM 92,164,156\nbuffer_store_dwordx4 v[120:123], v252, s[20:23], s85 offen offset:1024 sc1\nG 4\n"
"M 32,168,160\nR 160,186,24576\nv_mul_f32 v230, v0, v0\nv_mul_f32 v231, v1, v1\nM 32,172,176\ns_add_u32 s63, s56, 65536\n"
"s_add_u32 s84, s73, 23074816\ns_add_u32 s85, s73, 23189504\nv_mul_f32 v232, v2, v2\nM 48,168,164\nR 162,186,28672\nv_mul_f32 v233, v3, v3\n"
"v_fma_f32 v230, v230, v215, v214\nM 48,172,180\nbuffer_load_dwordx4 v220, s[16:19], s63 offen lds\nv_fma_f32 v231, v231, v215, v214\nM 64,168,168\nR 164,186,32768\n"
"v_fma_f32 v232, v232, v215, v214\nv_fma_f32 v233, v233, v215, v214\nM 64,172,184\nv_mul_f32 v230, v0, v230\nv_mul_f32 v231, v1, v231\nv_mul_f32 v232, v2, v232\n"
"M 80,168,172\nR 166,186,36864\nv_mul_f32 v233, v3, v233\nv_exp_f32 v230, v230\nM 80,172,188\nG 4\n"
"v_exp_f32 v231, v231\nv_exp_f32 v232, v232\nM 36,176,160\nR 168,187,24576\nv_fma_f32 v230, v230, v216, v216\nv_exp_f32 v233, v233\n"
"M 36,180,176\nv_rcp_f32 v230, v230\nv_fma_f32 v231, v231, v216, v216\nM 52,176,164\nR 170,187,28672\nv_fma_f32 v232, v232, v216, v216\n"
"v_fma_f32 v233, v233, v216, v216\nM 52,180,180\nv_rcp_f32 v231, v231\nv_rcp_f32 v232, v232\nM 68,176,168\nR 172,187,32768\n"
"v_mul_f32 v230, v0, v230\nv_rcp_f32 v233, v233\nM 68,180,184\nv_mul_f32 v231, v1, v231\nv_mul_f32 v232, v2, v232\nv_mul_f32 v233, v3, v233\n"
"M 84,176,172\nR 174,187,36864\nv_cvt_pk_bf16_f32 v0, v230, v231\nv_cvt_pk_bf16_f32 v1, v232, v233\nM 84,180,188\nG 4\n"
"v_mul_f32 v234, v4, v4\nv_mul_f32 v235, v5, v5\nv_mul_f32 v236, v6, v6\nM 40,160,160\nR 176,184,40960\nv_mul_f32 v237, v7, v7\n"
"v_fma_f32 v234, v234, v215, v214\nM 40,164,176\nv_fma_f32 v235, v235, v215, v214\nv_fma_f32 v236, v236, v215, v214\nv_fma_f32 v237, v237, v215, v214\nM 56,160,164\n"
"R 178,184,45056\nv_mul_f32 v234, v4, v234\nv_mul_f32 v235, v5, v235\nM 56,164,180\nv_exp_f32 v234, v234\nv_mul_f32 v236, v6, v236\n"
"M 72,160,168\nR 180,184,49152\nv_mul_f32 v237, v7, v237\nv_exp_f32 v235, v235\nM 72,164,184\nv_exp_f32 v236, v236\n"
"v_exp_f32 v237, v237\nM 88,160,172\nR 182,184,53248\nv_fma_f32 v234, v234, v216, v216\nv_fma_f32 v235, v235, v216, v216\nM 88,164,188\n"
"G 4\nv_rcp_f32 v234, v234\nv_fma_f32 v236, v236, v216, v216\nM 44,168,160\nR 160,185,40960\nv_fma_f32 v237, v237, v216, v216\n"
"v_rcp_f32 v235, v235\nM 44,172,176\nv_rcp_f32 v236, v236\nv_rcp_f32 v237, v237\nM 60,168,164\nR 162,185,45056\n"
"v_mul_f32 v234, v4, v234\nv_mul_f32 v235, v5, v235\nM 60,172,180\nv_mul_f32 v236, v6, v236\nv_mul_f32 v237, v7, v237\nv_cvt_pk_bf16_f32 v2, v234, v235\n"
"M 76,168,168\nR 164,185,49152\nv_cvt_pk_bf16_f32 v3, v236, v237\nv_mul_f32 v230, v16, v16\nM 76,172,184\nv_mul_f32 v231, v17, v17\n"
"v_mul_f32 v232, v18, v18\nv_mul_f32 v233, v19, v19\nM 92,168,172\nR 166,185,53248\nv_fma_f32 v230, v230, v218, v217\nv_fma_f32 v231, v231, v218, v217\n"
"M 92,172,188\nG 4\nv_fma_f32 v232, v232, v218, v217\nv_fma_f32 v233, v233, v218, v217\nv_mul_f32 v230, v16, v230\nM 32,176,192\n"
"R 168,186,40960\nv_mul_f32 v231, v17, v231\nv_mul_f32 v232, v18, v232\nM 32,180,208\nv_exp_f32 v230, v230\nv_mul_f32 v233, v19, v233\n"
"M 48,176,196\nR 170,186,45056\nv_fma_f32 v230, v230, v219, v219\nv_exp_f32 v231, v231\nM 48,180,212\nv_exp_f32 v232, v232\n"
"v_exp_f32 v233, v233\nM 64,176,200\nR 172,186,49152\nv_fma_f32 v231, v231, v219, v219\nv_fma_f32 v232, v232, v219, v219\nM 64,180,216\n"
"v_rcp_f32 v230, v230\nv_fma_f32 v233, v233, v219, v219\nM 80,176,204\nR 174,186,53248\nv_mul_f32 v230, v16, v230\nv_rcp_f32 v231, v231\n"
"M 80,180,220\nG 4\nv_rcp_f32 v232, v232\nv_rcp_f32 v233, v233\nM 36,160,192\nR 176,187,40960\n"
"s_waitcnt vmcnt(2)\ns_barrier\nv_mul_f32 v231, v17, v231\nM 36,164,208\nv_mul_f32 v232, v18, v232\nv_mul_f32 v233, v19, v233\n"
"v_cvt_pk_bf16_f32 v16, v230, v231\nM 52,160,196\nR 178,187,45056\ns_mov_b32 s60, 158208\nv_bfe_u32 v246, v252, 4, 4\nv_lshl_add_u32 v246, v246, 3, s60\n"
"v_cvt_pk_bf16_f32 v17, v232, v233\nM 52,164,212\nds_read_b64 v[238:239], v246 offset:0\nv_mul_f32 v234, v20, v20\nv_mul_f32 v235, v21, v21\nM 68,160,200\n"
"R 180,187,49152\nds_read_b64 v[240:241], v246 offset:128\nv_mov_b32 v226, s46\nv_mul_f32 v236, v22, v22\nM 68,164,216\nds_read_b64 v[242:243], v246 offset:256\n"
"v_mov_b32 v227, s47\nv_mul_f32 v237, v23, v23\nM 84,160,204\nR 182,187,53248\ns_add_u32 s61, s81, 385032\ns_lshl_b32 s62, s44, 5\n"
"s_add_u32 s61, s61, s62\ns_waitcnt vmcnt(0)\ns_mov_b64 exec, 1\nbuffer_atomic_swap_x2 v[226:227], off, s[20:23], s61 sc1\ns_mov_b64 exec, -1\nds_read_b64 v[244:245], v246 offset:384\n"
"v_fma_f32 v234, v234, v218, v217\nM 84,164,220\nds_read_b64 v[246:247], v246 offset:0\nG 9\nv_fma_f32 v235, v235, v218, v217\nM 40,168,192\n"
"R 160,184,57344\nbuffer_load_dwordx4 v221, s[16:19], s63 offen offset:1024 lds\nv_fma_f32 v236, v236, v218, v217\nM 40,172,208\ns_add_u32 m0, s50, 98304\ns_add_u32 s63, s56, 589824\n"
"buffer_load_dwordx4 v220, s[16:19], s63 offen lds\nv_fma_f32 v237, v237, v218, v217\nM 56,168,196\nR 162,184,61440\nv_mul_f32 v234, v20, v234\nv_mul_f32 v235, v21, v235\n"
"M 56,172,212\nbuffer_store_dwordx4 v[0:3], v252, s[20:23], s84 offen offset:0 sc1\nv_mul_f32 v236, v22, v236\nM 72,168,200\nR 164,188,0\nv_mul_f32 v237, v23, v237\n"
"v_exp_f32 v234, v234\nM 72,172,216\nbuffer_load_dwordx4 v221, s[16:19], s63 offen offset:1024 lds\nv_exp_f32 v235, v235\nM 88,168,204\nR 166,188,4096\n"
"s_add_u32 s63, s57, 0\nv_exp_f32 v236, v236\nM 88,172,220\nG 6\nv_exp_f32 v237, v237\nv_fma_f32 v234, v234, v219, v219\n"
"M 44,176,192\nR 168,185,57344\nv_mfma_f32_16x16x32_bf16 v[32:35], v[238:241], v[208:211], v[32:35]\nv_fma_f32 v235, v235, v219, v219\nv_fma_f32 v236, v236, v219, v219\nM 44,180,208\n"
"s_add_u32 m0, s50, 106496\nv_rcp_f32 v234, v234\nM 60,176,196\nR 170,185,61440\nv_mfma_f32_16x16x32_bf16 v[36:39], v[240:243], v[208:211], v[36:39]\nv_fma_f32 v237, v237, v219, v219\n"
"M 60,180,212\nbuffer_load_dwordx4 v220, s[16:19], s63 offen lds\nM 76,176,200\nR 172,189,0\nG 8\nv_mfma_f32_16x16x32_bf16 v[40:43], v[242:245], v[208:211], v[40:43]\n"
"v_mul_f32 v234, v20, v234\nM 76,180,216\nbuffer_load_dwordx4 v221, s[16:19], s63 offen offset:1024 lds\nM 92,176,204\nR 174,189,4096\nG 8\n"
"v_mfma_f32_16x16x32_bf16 v[44:47], v[244:247], v[208:211], v[44:47]\ns_add_u32 m0, s50, 114688\nM 92,180,220\nG 4\nv_rcp_f32 v235, v235\nM 32,160,224\n"
"R 176,186,57344\nv_mfma_f32_16x16x32_bf16 v[48:51], v[244:247], v[210:213], v[48:51]\ns_add_u32 s63, s57, 524288\nM 32,164,240\nbuffer_load_dwordx4 v220, s[16:19], s63 offen lds\nM 48,160,228\n"
"R 178,186,61440\nv_mfma_f32_16x16x32_bf16 v[52:55], v[238:241], v[210:213], v[52:55]\nv_mul_f32 v235, v21, v235\nM 48,164,244\nbuffer_load_dwordx4 v221, s[16:19], s63 offen offset:1024 lds\nM 64,160,232\n"
"R 180,190,0\nv_mfma_f32_16x16x32_bf16 v[56:59], v[240:243], v[210:213], v[56:59]\ns_add_u32 m0, s50, 0\ns_add_u32 s63, s57, 65536\nM 64,164,248\nbuffer_load_dwordx4 v220, s[16:19], s63 offen lds\n"
"M 80,160,236\nR 182,190,4096\nv_mfma_f32_16x16x32_bf16 v[60:63], v[242:245], v[210:213], v[60:63]\nv_cvt_pk_bf16_f32 v18, v234, v235\nM 80,164,252\nG 4\n"
"v_rcp_f32 v236, v236\nM 36,168,224\nR 160,187,57344\nv_mul_f32 v236, v22, v236\nM 36,172,240\nbuffer_load_dwordx4 v221, s[16:19], s63 offen offset:1024 lds\n"
"M 52,168,228\nR 162,187,61440\ns_add_u32 s63, s57, 589824\nM 52,172,244\ns_add_u32 m0, s50, 8192\nv_rcp_f32 v237, v237\n"
"M 68,168,232\nR 164,191,0\nv_mul_f32 v237, v23, v237\nM 68,172,248\nbuffer_load_dwordx4 v220, s[16:19], s63 offen lds\nM 84,168,236\n"
"R 166,191,4096\nv_cvt_pk_bf16_f32 v19, v236, v237\nM 84,172,252\nbuffer_store_dwordx4 v[16:19], v252, s[20:23], s85 offen offset:0 sc1\nG 4\nM 40,176,224\n"
"R 168,188,8192\nM 40,180,240\nbuffer_load_dwordx4 v221, s[16:19], s63 offen offset:1024 lds\nM 56,176,228\nR 170,188,12288\nM 56,180,244\n"
"M 72,176,232\nR 172,188,16384\nM 72,180,248\nM 88,176,236\nR 174,188,20480\nM 88,180,252\n"
"G 4\nM 44,160,224\nR 176,189,8192\nM 44,164,240\nM 60,160,228\nR 178,189,12288\n"
"v_mov_b32 v248, 0\nM 60,164,244\nv_mov_b32 v249, 0\nM 76,160,232\nR 180,189,16384\ns_add_u32 s61, s82, 385032\n"
"s_mov_b64 exec, 0xf\nbuffer_atomic_or_x2 v[248:249], v250, s[20:23], s61 offen sc0 sc1\ns_mov_b64 exec, -1\nM 76,164,248\nM 92,160,236\nR 182,189,20480\n"
"M 92,164,252\ns_waitcnt vmcnt(10)\ns_barrier\nG 4\nv_mfma_f32_16x16x32_bf16 v[96:99], v[168:171], a[0:3], 0\nR 160,190,8192\n"
"M 96,172,16\ns_mov_b32 m0, s51\nv_mfma_f32_16x16x32_bf16 v[112:115], v[168:171], a[4:7], 0\nR 162,190,12288\nM 112,172,20\nds_write_addtid_b32 v64 offset:0\n"
"v_mfma_f32_16x16x32_bf16 v[128:131], v[168:171], a[8:11], 0\nR 164,190,16384\nM 128,172,24\nds_write_addtid_b32 v65 offset:256\nv_mfma_f32_16x16x32_bf16 v[144:147], v[168:171], a[12:15], 0\nR 166,190,20480\n"
"M 144,172,28\nds_write_addtid_b32 v66 offset:512\nG 7\nv_mfma_f32_16x16x32_bf16 v[100:103], v[176:179], a[0:3], 0\nR 168,191,8192\nM 100,180,16\n"
"ds_write_addtid_b32 v67 offset:768\nv_mfma_f32_16x16x32_bf16 v[116:119], v[176:179], a[4:7], 0\nR 170,191,12288\nM 116,180,20\nds_write_addtid_b32 v68 offset:1024\nv_mfma_f32_16x16x32_bf16 v[132:135], v[176:179], a[8:11], 0\n"
"R 172,191,16384\nds_write_addtid_b32 v69 offset:1280\nM 132,180,24\nds_write_addtid_b32 v70 offset:1536\nds_write_addtid_b32 v71 offset:1792\nv_mfma_f32_16x16x32_bf16 v[148:151], v[176:179], a[12:15], 0\n"
"R 174,191,20480\nds_write_addtid_b32 v72 offset:2048\nM 148,180,28\nds_write_addtid_b32 v73 offset:2304\nds_write_addtid_b32 v74 offset:2560\nG 13\n"
"v_mfma_f32_16x16x32_bf16 v[104:107], v[160:163], a[0:3], 0\nR 176,188,24576\nds_write_addtid_b32 v75 offset:2816\nM 104,164,16\nds_write_addtid_b32 v76 offset:3072\nds_write_addtid_b32 v77 offset:3328\n"
"v_mfma_f32_16x16x32_bf16 v[120:123], v[160:163], a[4:7], 0\nR 178,188,28672\nds_write_addtid_b32 v78 offset:3584\nM 120,164,20\nds_write_addtid_b32 v79 offset:3840\nds_write_addtid_b32 v80 offset:4096\n"
"v_mfma_f32_16x16x32_bf16 v[136:139], v[160:163], a[8:11], 0\nR 180,188,32768\nds_write_addtid_b32 v81 offset:4352\nM 136,164,24\nds_write_addtid_b32 v82 offset:4608\nds_write_addtid_b32 v83 offset:4864\n"
"v_mfma_f32_16x16x32_bf16 v[152:155], v[160:163], a[12:15], 0\nR 182,188,36864\nds_write_addtid_b32 v84 offset:5120\nM 152,164,28\nds_write_addtid_b32 v85 offset:5376\nds_write_addtid_b32 v86 offset:5632\n"
"G 15\nv_mfma_f32_16x16x32_bf16 v[108:111], v[168:171], a[0:3], 0\nR 160,189,24576\nds_write_addtid_b32 v87 offset:5888\nM 108,172,16\nds_write_addtid_b32 v88 offset:6144\n"
"ds_write_addtid_b32 v89 offset:6400\nv_mfma_f32_16x16x32_bf16 v[124:127], v[168:171], a[4:7], 0\nR 162,189,28672\nds_write_addtid_b32 v90 offset:6656\nM 124,172,20\ns_add_u32 s64, s68, 23070720\n"
"ds_write_addtid_b32 v91 offset:6912\nds_write_addtid_b32 v92 offset:7168\nv_mfma_f32_16x16x32_bf16 v[140:143], v[168:171], a[8:11], 0\nR 164,189,32768\nv_mov_b32 v226, s46\nM 140,172,24\n"
"ds_write_addtid_b32 v93 offset:7424\nv_mov_b32 v227, s47\nv_mfma_f32_16x16x32_bf16 v[156:159], v[168:171], a[12:15], 0\nR 166,189,36864\ns_add_u32 s61, s81, 385048\ns_lshl_b32 s62, s44, 5\n"
"s_add_u32 s61, s61, s62\ns_waitcnt vmcnt(0)\ns_mov_b64 exec, 1\nbuffer_atomic_swap_x2 v[226:227], off, s[20:23], s61 sc1\ns_mov_b64 exec, -1\nv_cmp_eq_u64 s[86:87], s[46:47], v[248:249]\n"
"M 156,172,28\nds_write_addtid_b32 v94 offset:7680\nds_write_addtid_b32 v95 offset:7936\nG 15\nM 96,176,32\nR 168,190,24576\n"
"s_and_b32 s62, s86, 0xf\ns_cmp_eq_u32 s62, 0xf\ns_cbranch_scc0 Lq417\nLq418:\ns_add_u32 m0, s50, 16384\ns_add_u32 s63, s56, 131072\n"
"M 96,180,48\ns_add_u32 s65, s68, 23185408\nbuffer_load_dwordx4 v220, s[16:19], s63 offen lds\nM 112,176,36\nR 170,190,28672\nG 2\n"
"s_barrier\nM 112,180,52\nbuffer_load_dwordx4 a[64:67], v252, s[20:23], s64 offen sc1\nbuffer_load_dwordx4 v221, s[16:19], s63 offen offset:1024 lds\nds_read2st64_b32 v[64:65], v225 offset0:0 offset1:1\nM 128,176,40\n"
"R 172,190,32768\ns_add_u32 m0, s50, 24576\ns_add_u32 s63, s56, 655360\nbuffer_load_dwordx4 v220, s[16:19], s63 offen lds\nds_read2st64_b32 v[66:67], v225 offset0:2 offset1:3\nM 128,180,56\n"
"s_add_u32 s66, s69, 23070720\nds_read2st64_b32 v[68:69], v225 offset0:4 offset1:5\nds_read2st64_b32 v[70:71], v225 offset0:6 offset1:7\nM 144,176,44\nR 174,190,36864\nds_read2st64_b32 v[72:73], v225 offset0:8 offset1:9\n"
"M 144,180,60\nds_read2st64_b32 v[74:75], v225 offset0:10 offset1:11\nds_read2st64_b32 v[76:77], v225 offset0:12 offset1:13\nM 100,160,32\nR 176,191,24576\nbuffer_load_dwordx4 v221, s[16:19], s63 offen offset:1024 lds\n"
"ds_read2st64_b32 v[78:79], v225 offset0:14 offset1:15\nM 100,164,48\nbuffer_load_dwordx4 a[68:71], v252, s[20:23], s65 offen sc1\nG 10\nv_add_f32 v32, v32, v64\nM 116,160,36\n"
"R 178,191,28672\ns_add_u32 m0, s50, 32768\nv_add_f32 v33, v33, v65\nM 116,164,52\ns_add_u32 s67, s69, 23185408\ns_add_u32 s63, s56, 196608\n"
"G 9\nv_add_f32 v34, v34, v66\nv_add_f32 v35, v35, v67\nM 132,160,40\nR 180,191,32768\nbuffer_load_dwordx4 v220, s[16:19], s63 offen lds\n"
"G 9\nv_add_f32 v36, v36, v68\nM 132,164,56\nbuffer_load_dwordx4 a[72:75], v252, s[20:23], s66 offen sc1\nv_add_f32 v37, v37, v69\nM 148,160,44\n"
"R 182,191,36864\nG 9\nv_add_f32 v38, v38, v70\nv_add_f32 v39, v39, v71\nM 148,164,60\nds_read2st64_b32 v[80:81], v225 offset0:16 offset1:17\n"
"G 9\nG 8\nv_add_f32 v40, v40, v72\nM 104,168,32\nR 160,188,40960\nbuffer_load_dwordx4 v221, s[16:19], s63 offen offset:1024 lds\n"
"v_add_f32 v41, v41, v73\nM 104,172,48\nbuffer_load_dwordx4 a[76:79], v252, s[20:23], s67 offen sc1\nG 8\nv_add_f32 v42, v42, v74\nM 120,168,36\n"
"R 162,188,45056\nv_add_f32 v43, v43, v75\nG 8\nv_add_f32 v44, v44, v76\nM 120,172,52\ns_add_u32 s64, s68, 23077888\n"
"ds_read2st64_b32 v[82:83], v225 offset0:18 offset1:19\nv_add_f32 v45, v45, v77\nM 136,168,40\nR 164,188,49152\nG 8\nv_add_f32 v46, v46, v78\n"
"v_add_f32 v47, v47, v79\nM 136,172,56\ns_add_u32 s65, s68, 23192576\nds_read2st64_b32 v[84:85], v225 offset0:20 offset1:21\nG 5\nv_add_f32 v48, v48, v80\n"
"M 152,168,44\nR 166,188,53248\nds_read2st64_b32 v[86:87], v225 offset0:22 offset1:23\nv_add_f32 v49, v49, v81\nM 152,172,60\nds_read2st64_b32 v[88:89], v225 offset0:24 offset1:25\n"
"ds_read2st64_b32 v[90:91], v225 offset0:26 offset1:27\nM 108,176,32\nR 168,189,40960\nds_read2st64_b32 v[92:93], v225 offset0:28 offset1:29\nM 108,180,48\nbuffer_load_dwordx4 a[80:83], v252, s[20:23], s64 offen sc1\n"
"G 8\nv_add_f32 v50, v50, v82\nM 124,176,36\nR 170,189,45056\nds_read2st64_b32 v[94:95], v225 offset0:30 offset1:31\nv_add_f32 v51, v51, v83\n"
"M 124,180,52\nbuffer_load_dwordx4 a[84:87], v252, s[20:23], s65 offen sc1\nM 140,176,40\nR 172,189,49152\nG 9\nv_add_f32 v52, v52, v84\n"
"M 140,180,56\ns_add_u32 s66, s69, 23077888\nv_add_f32 v53, v53, v85\nG 7\nv_add_f32 v54, v54, v86\nM 156,176,44\n"
"R 174,189,53248\nv_add_f32 v55, v55, v87\nM 156,180,60\nG 7\nv_add_f32 v56, v56, v88\nv_add_f32 v57, v57, v89\n"
"v_mfma_f32_16x16x32_bf16 v[0:3], v[160:163], a[0:3], 0\nR 176,190,40960\nLq579:\ns_lshr_b32 s60, s45, 3\ns_lshl2_add_u32 s60, s60, s40\ns_lshl_b32 s60, s60, 8\n"
"v_lshl_add_u32 v224, v252, 3, s60\nv_add_u32 v224, Lq579+4096-LqPC, v224\ns_mov_b64 exec, 3\nglobal_load_dword v224, v224, s[98:99]\ns_mov_b64 exec, -1\nM 0,164,16\n"
"buffer_load_dwordx4 a[88:91], v252, s[20:23], s66 offen sc1\nv_mfma_f32_16x16x32_bf16 v[16:19], v[160:163], a[4:7], 0\nR 178,190,45056\nG 8\nv_add_f32 v58, v58, v90\nM 16,164,20\n"
"s_add_u32 s67, s69, 23192576\nv_add_f32 v59, v59, v91\nG 6\nv_add_f32 v60, v60, v92\nv_mfma_f32_16x16x32_bf16 a[224:227], v[160:163], a[8:11], 0\nR 180,190,49152\n"
"v_add_f32 v61, v61, v93\nv_mfma_f32_16x16x32_bf16 a[224:227], v[164:167], a[24:27], a[224:227]\nbuffer_load_dwordx4 a[92:95], v252, s[20:23], s67 offen sc1\nv_mfma_f32_16x16x32_bf16 a[240:243], v[160:163], a[12:15], 0\nR 182,190,53248\nG 6\n"
"v_add_f32 v62, v62, v94\nv_mfma_f32_16x16x32_bf16 a[240:243], v[164:167], a[28:31], a[240:243]\ns_add_u32 s63, s56, 720896\nG 4\nv_add_f32 v63, v63, v95\nv_mfma_f32_16x16x32_bf16 v[4:7], v[168:171], a[0:3], 0\n"
"R 160,191,40960\ns_add_u32 m0, s50, 40960\nv_mul_f32 v230, v32, v32\nM 4,172,16\ns_add_u32 s64, s68, 23071744\ns_add_u32 s84, s73, 23072768\n"
"s_add_u32 s85, s73, 23187456\nv_mul_f32 v231, v33, v33\nv_mfma_f32_16x16x32_bf16 v[20:23], v[168:171], a[4:7], 0\nR 162,191,45056\nv_mov_b32 v248, 0\nv_mul_f32 v232, v34, v34\n"
"M 20,172,20\nbuffer_load_dwordx4 a[96:99], v252, s[20:23], s64 offen sc1\nv_mov_b32 v249, 0\nv_mul_f32 v233, v35, v35\nv_mfma_f32_16x16x32_bf16 a[228:231], v[168:171], a[8:11], 0\nR 164,191,49152\n"
"s_add_u32 s61, s82, 385048\ns_mov_b64 exec, 0xf\nbuffer_atomic_or_x2 v[248:249], v250, s[20:23], s61 offen sc0 sc1\ns_mov_b64 exec, -1\nv_fma_f32 v230, v230, v215, v214\nv_mfma_f32_16x16x32_bf16 a[228:231], v[172:175], a[24:27], a[228:231]\n"
"s_add_u32 s65, s68, 23186432\nbuffer_load_dwordx4 v220, s[16:19], s63 offen lds\nv_fma_f32 v231, v231, v215, v214\nv_mfma_f32_16x16x32_bf16 a[244:247], v[168:171], a[12:15], 0\nR 166,191,53248\nv_fma_f32 v232, v232, v215, v214\n"
"v_fma_f32 v233, v233, v215, v214\nv_mfma_f32_16x16x32_bf16 a[244:247], v[172:175], a[28:31], a[244:247]\nG 4\nv_mul_f32 v230, v32, v230\nv_mul_f32 v231, v33, v231\nv_mul_f32 v232, v34, v232\n"
"v_mfma_f32_16x16x32_bf16 v[8:11], v[176:179], a[0:3], 0\nR 168,184,0\nv_mul_f32 v233, v35, v233\nv_exp_f32 v230, v230\nM 8,180,16\nbuffer_load_dwordx4 a[100:103], v252, s[20:23], s65 offen sc1\n"
"buffer_load_dwordx4 v221, s[16:19], s63 offen offset:1024 lds\nv_exp_f32 v231, v231\nv_mfma_f32_16x16x32_bf16 v[24:27], v[176:179], a[4:7], 0\nR 170,184,4096\ns_add_u32 s67, s69, 23186432\nv_exp_f32 v232, v232\n"
"M 24,180,20\ns_add_u32 s66, s69, 23071744\ns_add_u32 s63, s56, 393216\nv_fma_f32 v230, v230, v216, v216\nv_exp_f32 v233, v233\nv_mfma_f32_16x16x32_bf16 a[232:235], v[176:179], a[8:11], 0\n"
"R 172,184,8192\ns_add_u32 m0, s50, 49152\nv_fma_f32 v231, v231, v216, v216\nv_mfma_f32_16x16x32_bf16 a[232:235], v[180:183], a[24:27], a[232:235]\nbuffer_load_dwordx4 a[104:107], v252, s[20:23], s66 offen sc1\nbuffer_load_dwordx4 v220, s[16:19], s63 offen lds\n"
"v_fma_f32 v232, v232, v216, v216\nv_mfma_f32_16x16x32_bf16 a[248:251], v[176:179], a[12:15], 0\nR 174,184,12288\nv_fma_f32 v233, v233, v216, v216\nv_rcp_f32 v230, v230\nv_mfma_f32_16x16x32_bf16 a[248:251], v[180:183], a[28:31], a[248:251]\n"
"G 4\nv_rcp_f32 v231, v231\nv_rcp_f32 v232, v232\nv_mfma_f32_16x16x32_bf16 v[12:15], v[160:163], a[0:3], 0\nR 176,185,0\nv_mul_f32 v230, v32, v230\n"
"v_rcp_f32 v233, v233\nM 12,164,16\nbuffer_load_dwordx4 a[108:111], v252, s[20:23], s67 offen sc1\nbuffer_load_dwordx4 v221, s[16:19], s63 offen offset:1024 lds\nv_mul_f32 v231, v33, v231\nv_mfma_f32_16x16x32_bf16 v[28:31], v[160:163], a[4:7], 0\n"
"R 178,185,4096\ns_add_u32 m0, s50, 57344\nv_mul_f32 v232, v34, v232\nM 28,164,20\ns_add_u32 s64, s68, 23078912\nv_mul_f32 v233, v35, v233\n"
"v_cvt_pk_bf16_f32 v32, v230, v231\nv_cvt_pk_bf16_f32 v33, v232, v233\nv_mfma_f32_16x16x32_bf16 a[236:239], v[160:163], a[8:11], 0\nR 180,185,8192\ns_add_u32 s63, s56, 458752\nv_mul_f32 v234, v36, v36\n"
"v_mfma_f32_16x16x32_bf16 a[236:239], v[164:167], a[24:27], a[236:239]\ns_add_u32 s65, s68, 23193600\nbuffer_load_dwordx4 v220, s[16:19], s63 offen lds\nv_mul_f32 v235, v37, v37\nv_mfma_f32_16x16x32_bf16 a[252:255], v[160:163], a[12:15], 0\nR 182,185,12288\n"
"v_mul_f32 v236, v38, v38\nv_mul_f32 v237, v39, v39\nv_mfma_f32_16x16x32_bf16 a[252:255], v[164:167], a[28:31], a[252:255]\ns_waitcnt vmcnt(16)\ns_barrier\nG 4\n"
"v_fma_f32 v234, v234, v215, v214\nM 0,168,32\nR 160,186,0\nv_fma_f32 v235, v235, v215, v214\nv_fma_f32 v236, v236, v215, v214\nM 0,172,48\n"
"buffer_load_dwordx4 a[112:115], v252, s[20:23], s64 offen sc1\nbuffer_load_dwordx4 v221, s[16:19], s63 offen offset:1024 lds\nv_fma_f32 v237, v237, v215, v214\nM 16,168,36\nR 162,186,4096\nv_mul_f32 v234, v36, v234\n"
"v_mul_f32 v235, v37, v235\nM 16,172,52\nbuffer_load_dwordx4 a[116:119], v252, s[20:23], s65 offen sc1\nv_mul_f32 v236, v38, v236\nv_mfma_f32_16x16x32_bf16 a[224:227], v[168:171], a[40:43], a[224:227]\nR 164,186,8192\n"
"v_mul_f32 v237, v39, v237\nv_exp_f32 v234, v234\nv_mfma_f32_16x16x32_bf16 a[224:227], v[172:175], a[56:59], a[224:227]\ns_add_u32 s66, s69, 23078912\nv_exp_f32 v235, v235\nv_exp_f32 v236, v236\n"
"v_mfma_f32_16x16x32_bf16 a[240:243], v[168:171], a[44:47], a[240:243]\nR 166,186,12288\nv_fma_f32 v234, v234, v216, v216\nv_exp_f32 v237, v237\nv_mfma_f32_16x16x32_bf16 a[240:243], v[172:175], a[60:63], a[240:243]\nG 4\n"
"v_rcp_f32 v234, v234\nv_fma_f32 v235, v235, v216, v216\nM 4,176,32\nR 168,187,0\nv_fma_f32 v236, v236, v216, v216\nv_fma_f32 v237, v237, v216, v216\n"
"M 4,180,48\nbuffer_load_dwordx4 a[120:123], v252, s[20:23], s66 offen sc1\nv_rcp_f32 v235, v235\nM 20,176,36\nR 170,187,4096\nv_mul_f32 v234, v36, v234\n"
"v_rcp_f32 v236, v236\nM 20,180,52\ns_add_u32 s67, s69, 23193600\nv_rcp_f32 v237, v237\nv_mul_f32 v235, v37, v235\nv_mfma_f32_16x16x32_bf16 a[228:231], v[176:179], a[40:43], a[228:231]\n"
"R 172,187,8192\nv_mul_f32 v236, v38, v236\nv_mul_f32 v237, v39, v237\nv_mfma_f32_16x16x32_bf16 a[228:231], v[180:183], a[56:59], a[228:231]\nbuffer_load_dwordx4 a[124:127], v252, s[20:23], s67 offen sc1\nv_cvt_pk_bf16_f32 v34, v234, v235\n"
"v_mfma_f32_16x16x32_bf16 a[244:247], v[176:179], a[44:47], a[244:247]\nR 174,187,12288\ns_waitcnt vmcnt(13)\nv_cmp_eq_u64 s[86:87], s[46:47], v[248:249]\nv_cvt_pk_bf16_f32 v35, v236, v237\nv_mfma_f32_16x16x32_bf16 a[244:247], v[180:183], a[60:63], a[244:247]\n"
"s_add_u32 s63, s57, 131072\nG 4\nv_mul_f32 v230, v40, v40\nv_mul_f32 v231, v41, v41\nM 8,160,32\nR 176,184,16384\n"
"s_and_b32 s62, s86, 0xf\ns_cmp_eq_u32 s62, 0xf\ns_cbranch_scc0 Lq452\nLq453:\nv_mul_f32 v232, v42, v42\nM 8,164,48\n"
"s_add_u32 s64, s68, 23074816\nv_mul_f32 v233, v43, v43\nv_fma_f32 v230, v230, v215, v214\nv_fma_f32 v231, v231, v215, v214\nM 24,160,36\nR 178,184,20480\n"
"s_add_u32 m0, s50, 65536\nv_fma_f32 v232, v232, v215, v214\nM 24,164,52\nbuffer_load_dwordx4 a[128:131], v252, s[20:23], s64 offen sc1\nv_fma_f32 v233, v233, v215, v214\nv_mfma_f32_16x16x32_bf16 a[232:235], v[160:163], a[40:43], a[232:235]\n"
"R 180,184,24576\ns_add_u32 s66, s69, 23074816\nbuffer_load_dwordx4 v220, s[16:19], s63 offen lds\nv_mul_f32 v230, v40, v230\nv_mfma_f32_16x16x32_bf16 a[232:235], v[164:167], a[56:59], a[232:235]\ns_add_u32 s65, s68, 23189504\n"
"v_exp_f32 v230, v230\nv_mul_f32 v231, v41, v231\nv_mfma_f32_16x16x32_bf16 a[248:251], v[160:163], a[44:47], a[248:251]\nR 182,184,28672\nv_mul_f32 v232, v42, v232\nv_mul_f32 v233, v43, v233\n"
"v_mfma_f32_16x16x32_bf16 a[248:251], v[164:167], a[60:63], a[248:251]\nG 4\nv_exp_f32 v231, v231\nv_exp_f32 v232, v232\nM 12,168,32\nR 160,185,16384\n"
"buffer_load_dwordx4 v221, s[16:19], s63 offen offset:1024 lds\nv_exp_f32 v233, v233\nM 12,172,48\nbuffer_load_dwordx4 a[132:135], v252, s[20:23], s65 offen sc1\ns_add_u32 s63, s57, 655360\nv_fma_f32 v230, v230, v216, v216\n"
"M 28,168,36\nR 162,185,20480\ns_add_u32 m0, s50, 73728\nv_fma_f32 v231, v231, v216, v216\nM 28,172,52\nbuffer_load_dwordx4 a[136:139], v252, s[20:23], s66 offen sc1\n"
"v_fma_f32 v232, v232, v216, v216\nv_mfma_f32_16x16x32_bf16 a[236:239], v[168:171], a[40:43], a[236:239]\nR 164,185,24576\ns_add_u32 s64, s68, 23081984\nbuffer_load_dwordx4 v220, s[16:19], s63 offen lds\nv_fma_f32 v233, v233, v216, v216\n"
"v_mfma_f32_16x16x32_bf16 a[236:239], v[172:175], a[56:59], a[236:239]\ns_add_u32 s67, s69, 23189504\nv_rcp_f32 v230, v230\nv_rcp_f32 v231, v231\nv_mfma_f32_16x16x32_bf16 a[252:255], v[168:171], a[44:47], a[252:255]\nR 166,185,28672\n"
"v_mul_f32 v230, v40, v230\nv_rcp_f32 v232, v232\nv_mfma_f32_16x16x32_bf16 a[252:255], v[172:175], a[60:63], a[252:255]\nG 4\nv_rcp_f32 v233, v233\nv_mul_f32 v231, v41, v231\n"
"M 96,176,64\nR 168,186,16384\nbuffer_load_dwordx4 v221, s[16:19], s63 offen offset:1024 lds\nv_mul_f32 v232, v42, v232\nM 96,180,80\nbuffer_load_dwordx4 a[140:143], v252, s[20:23], s67 offen sc1\n"
"s_add_u32 s63, s57, 196608\nv_mul_f32 v233, v43, v233\nM 112,176,68\nR 170,186,20480\ns_add_u32 m0, s50, 81920\nv_cvt_pk_bf16_f32 v40, v230, v231\n"
"M 112,180,84\nbuffer_load_dwordx4 a[144:147], v252, s[20:23], s64 offen sc1\nv_cvt_pk_bf16_f32 v41, v232, v233\nM 128,176,72\nR 172,186,24576\ns_add_u32 s66, s69, 23081984\n"
"buffer_load_dwordx4 v220, s[16:19], s63 offen lds\nv_mul_f32 v234, v44, v44\nM 128,180,88\ns_add_u32 s65, s68, 23196672\nv_mul_f32 v235, v45, v45\nv_mul_f32 v236, v46, v46\n"
"v_mul_f32 v237, v47, v47\nM 144,176,76\nR 174,186,28672\nv_fma_f32 v234, v234, v215, v214\nv_fma_f32 v235, v235, v215, v214\nM 144,180,92\n"
"G 4\nv_fma_f32 v236, v236, v215, v214\nv_fma_f32 v237, v237, v215, v214\nv_mul_f32 v234, v44, v234\nM 100,160,64\nR 176,187,16384\n"
"buffer_load_dwordx4 v221, s[16:19], s63 offen offset:1024 lds\nv_mul_f32 v235, v45, v235\nM 100,164,80\nbuffer_load_dwordx4 a[148:151], v252, s[20:23], s65 offen sc1\ns_add_u32 s63, s57, 720896\nv_mul_f32 v236, v46, v236\n"
"M 116,160,68\nR 178,187,20480\ns_add_u32 m0, s50, 90112\nv_mul_f32 v237, v47, v237\nM 116,164,84\nbuffer_load_dwordx4 a[152:155], v252, s[20:23], s66 offen sc1\n"
"v_exp_f32 v234, v234\nM 132,160,72\nR 180,187,24576\nbuffer_load_dwordx4 v220, s[16:19], s63 offen lds\nv_exp_f32 v235, v235\nM 132,164,88\n"
"s_add_u32 s67, s69, 23196672\nv_exp_f32 v236, v236\nv_exp_f32 v237, v237\nM 148,160,76\nR 182,187,28672\nv_fma_f32 v234, v234, v216, v216\n"
"v_fma_f32 v235, v235, v216, v216\nM 148,164,92\nG 4\nv_rcp_f32 v234, v234\nv_fma_f32 v236, v236, v216, v216\nM 104,168,64\n"
"R 160,184,32768\ns_waitcnt vmcnt(17)\ns_barrier\nv_fma_f32 v237, v237, v216, v216\nM 104,172,80\nbuffer_load_dwordx4 a[156:159], v252, s[20:23], s67 offen sc1\n"
"buffer_load_dwordx4 v221, s[16:19], s63 offen offset:1024 lds\nv_rcp_f32 v235, v235\nM 120,168,68\nR 162,184,36864\nv_mul_f32 v234, v44, v234\nv_rcp_f32 v236, v236\n"
"M 120,172,84\nbuffer_store_dwordx4 v[32:35], v252, s[20:23], s84 offen offset:0 sc1\nv_rcp_f32 v237, v237\nM 136,168,72\nR 164,184,40960\nv_mul_f32 v235, v45, v235\n"
"v_mul_f32 v236, v46, v236\nM 136,172,88\ns_add_u32 m0, s50, 98304\nv_mul_f32 v237, v47, v237\nv_cvt_pk_bf16_f32 v42, v234, v235\nv_cvt_pk_bf16_f32 v43, v236, v237\n"
"M 152,168,76\nR 166,184,45056\nv_mul_f32 v230, v48, v48\nv_mul_f32 v231, v49, v49\nM 152,172,92\nG 4\n"
"v_mul_f32 v232, v50, v50\nv_mul_f32 v233, v51, v51\nv_fma_f32 v230, v230, v218, v217\nM 108,176,64\nR 168,185,32768\nv_fma_f32 v231, v231, v218, v217\n"
"v_fma_f32 v232, v232, v218, v217\nM 108,180,80\ns_add_u32 s63, s57, 393216\nv_fma_f32 v233, v233, v218, v217\nv_mul_f32 v230, v48, v230\nv_mul_f32 v231, v49, v231\n"
"M 124,176,68\nR 170,185,36864\nv_mul_f32 v232, v50, v232\nv_mul_f32 v233, v51, v233\nM 124,180,84\nbuffer_load_dwordx4 v220, s[16:19], s63 offen lds\n"
"v_exp_f32 v230, v230\nM 140,176,72\nR 172,185,40960\nv_fma_f32 v230, v230, v219, v219\nv_exp_f32 v231, v231\nM 140,180,88\n"
"buffer_store_dwordx4 v[40:43], v252, s[20:23], s84 offen offset:1024 sc1\nv_exp_f32 v232, v232\nM 156,176,76\nR 174,185,45056\nv_fma_f32 v231, v231, v219, v219\nv_exp_f32 v233, v233\n"
"M 156,180,92\nG 4\nv_rcp_f32 v230, v230\nv_fma_f32 v232, v232, v219, v219\nM 96,160,96\nR 176,186,32768\n"
"v_fma_f32 v233, v233, v219, v219\nv_rcp_f32 v231, v231\nM 96,164,112\nbuffer_load_dwordx4 v221, s[16:19], s63 offen offset:1024 lds\nv_rcp_f32 v232, v232\nM 112,160,100\n"
"R 178,186,36864\ns_add_u32 s63, s57, 458752\nv_rcp_f32 v233, v233\ns_waitcnt vmcnt(22)\nM 112,164,116\ns_add_u32 m0, s50, 106496\n"
"v_mul_f32 v230, v48, v230\nv_mul_f32 v231, v49, v231\nv_mul_f32 v232, v50, v232\nM 128,160,104\nR 180,186,40960\nv_mul_f32 v233, v51, v233\n"
"v_cvt_pk_bf16_f32 v48, v230, v231\ns_waitcnt vmcnt(21)\nM 128,164,120\nbuffer_load_dwordx4 v220, s[16:19], s63 offen lds\nv_cvt_pk_bf16_f32 v49, v232, v233\nM 144,160,108\n"
"R 182,186,45056\nv_mul_f32 v234, v52, v52\nv_mul_f32 v235, v53, v53\ns_waitcnt vmcnt(21)\nM 144,164,124\nG 4\n"
"v_mul_f32 v236, v54, v54\nv_mul_f32 v237, v55, v55\nv_fma_f32 v234, v234, v218, v217\nM 100,168,96\nR 160,187,32768\nv_fma_f32 v235, v235, v218, v217\n"
"v_fma_f32 v236, v236, v218, v217\nM 100,172,112\nv_fma_f32 v237, v237, v218, v217\nv_mul_f32 v234, v52, v234\nv_mul_f32 v235, v53, v235\nM 116,168,100\n"
"R 162,187,36864\nv_mul_f32 v236, v54, v236\nv_mul_f32 v237, v55, v237\nM 116,172,116\nbuffer_load_dwordx4 v221, s[16:19], s63 offen offset:1024 lds\nv_exp_f32 v234, v234\n"
"M 132,168,104\nR 164,187,40960\ns_add_u32 s63, s58, 0\nv_exp_f32 v235, v235\nM 132,172,120\ns_add_u32 m0, s50, 114688\n"
"v_exp_f32 v236, v236\nv_exp_f32 v237, v237\nM 148,168,108\nR 166,187,45056\nv_fma_f32 v234, v234, v219, v219\nv_fma_f32 v235, v235, v219, v219\n"
"M 148,172,124\nG 4\nv_rcp_f32 v234, v234\nv_fma_f32 v236, v236, v219, v219\nM 104,176,96\nR 168,184,49152\n"
"v_fma_f32 v237, v237, v219, v219\nv_rcp_f32 v235, v235\nM 104,180,112\nbuffer_load_dwordx4 v220, s[16:19], s63 offen lds\nv_rcp_f32 v236, v236\nM 120,176,100\n"
"R 170,184,53248\nv_mul_f32 v234, v52, v234\nv_rcp_f32 v237, v237\nM 120,180,116\nv_mul_f32 v235, v53, v235\nv_mul_f32 v236, v54, v236\n"
"v_mul_f32 v237, v55, v237\nM 136,176,104\nR 172,184,57344\nv_cvt_pk_bf16_f32 v50, v234, v235\nM 136,180,120\nbuffer_load_dwordx4 v221, s[16:19], s63 offen offset:1024 lds\n"
"M 152,176,108\nR 174,184,61440\ns_add_u32 s63, s58, 524288\nM 152,180,124\nG 4\nv_cvt_pk_bf16_f32 v51, v236, v237\n"
"v_mul_f32 v230, v56, v56\nM 108,160,96\nR 176,185,49152\nv_mul_f32 v231, v57, v57\nM 108,164,112\ns_add_u32 m0, s50, 0\n"
"v_mul_f32 v232, v58, v58\nv_mul_f32 v233, v59, v59\nM 124,160,100\nR 178,185,53248\nv_fma_f32 v230, v230, v218, v217\nM 124,164,116\n"
"buffer_load_dwordx4 v220, s[16:19], s63 offen lds\nM 140,160,104\nR 180,185,57344\nv_fma_f32 v231, v231, v218, v217\nM 140,164,120\nbuffer_store_dwordx4 v[48:51], v252, s[20:23], s85 offen offset:0 sc1\n"
"M 156,160,108\nR 182,185,61440\nv_fma_f32 v232, v232, v218, v217\nM 156,164,124\ns_waitcnt vmcnt(10)\ns_barrier\n"
"G 4\nM 96,168,128\nR 160,186,49152\nv_fma_f32 v233, v233, v218, v217\nM 96,172,144\nbuffer_load_dwordx4 v221, s[16:19], s63 offen offset:1024 lds\n"
"M 112,168,132\nR 162,186,53248\nv_mul_f32 v230, v56, v230\nM 112,172,148\ns_add_u32 m0, s50, 8192\nv_exp_f32 v230, v230\n"
"M 128,168,136\nR 164,186,57344\nv_mul_f32 v231, v57, v231\nM 128,172,152\ns_add_u32 s63, s58, 65536\nv_exp_f32 v231, v231\n"
"M 144,168,140\nR 166,186,61440\nv_mul_f32 v232, v58, v232\nM 144,172,156\nG 4\nv_exp_f32 v232, v232\n"
"M 100,176,128\nR 168,187,49152\nv_mul_f32 v233, v59, v233\nM 100,180,144\nbuffer_load_dwordx4 v220, s[16:19], s63 offen lds\nM 116,176,132\n"
"R 170,187,53248\nv_fma_f32 v230, v230, v219, v219\nM 116,180,148\nv_exp_f32 v233, v233\nM 132,176,136\nR 172,187,57344\n"
"v_fma_f32 v231, v231, v219, v219\nM 132,180,152\nv_rcp_f32 v230, v230\nM 148,176,140\nR 174,187,61440\nv_fma_f32 v232, v232, v219, v219\n"
"M 148,180,156\nG 4\nv_rcp_f32 v231, v231\nM 104,160,128\nR 176,188,0\nv_fma_f32 v233, v233, v219, v219\n"
"M 104,164,144\nbuffer_load_dwordx4 v221, s[16:19], s63 offen offset:1024 lds\nM 120,160,132\nR 178,188,4096\nv_mul_f32 v230, v56, v230\nM 120,164,148\n"
"s_add_u32 m0, s50, 16384\nv_rcp_f32 v232, v232\nM 136,160,136\nR 180,188,8192\nv_mul_f32 v231, v57, v231\nM 136,164,152\n"
"s_add_u32 s63, s58, 589824\nv_rcp_f32 v233, v233\nM 152,160,140\nR 182,188,12288\nv_mul_f32 v232, v58, v232\nM 152,164,156\n"
"G 4\nv_mul_f32 v233, v59, v233\nv_cvt_pk_bf16_f32 v56, v230, v231\nM 108,168,128\nR 160,189,0\nv_cvt_pk_bf16_f32 v57, v232, v233\n"
"M 108,172,144\nbuffer_load_dwordx4 v220, s[16:19], s63 offen lds\nM 124,168,132\nR 162,189,4096\nv_mul_f32 v234, v60, v60\nM 124,172,148\n"
"v_mul_f32 v235, v61, v61\nv_mul_f32 v236, v62, v62\nM 140,168,136\nR 164,189,8192\nv_mul_f32 v237, v63, v63\nM 140,172,152\n"
"v_fma_f32 v234, v234, v218, v217\nv_fma_f32 v235, v235, v218, v217\nM 156,168,140\nR 166,189,12288\nv_fma_f32 v236, v236, v218, v217\nM 156,172,156\n"
"G 4\nv_fma_f32 v237, v237, v218, v217\nv_mul_f32 v234, v60, v234\nM 0,176,64\nR 168,190,0\nv_mul_f32 v235, v61, v235\n"
"M 0,180,80\nbuffer_load_dwordx4 v221, s[16:19], s63 offen offset:1024 lds\nM 16,176,68\nR 170,190,4096\nv_mul_f32 v236, v62, v236\nM 16,180,84\n"
"s_add_u32 m0, s50, 24576\nv_exp_f32 v234, v234\nv_mfma_f32_16x16x32_bf16 a[224:227], v[176:179], a[72:75], a[224:227]\nR 172,190,8192\nv_mul_f32 v237, v63, v237\nv_mfma_f32_16x16x32_bf16 a[224:227], v[180:183], a[88:91], a[224:227]\n"
"s_add_u32 s63, s58, 131072\nv_exp_f32 v235, v235\nv_mfma_f32_16x16x32_bf16 a[240:243], v[176:179], a[76:79], a[240:243]\nR 174,190,12288\nv_fma_f32 v234, v234, v219, v219\nv_mfma_f32_16x16x32_bf16 a[240:243], v[180:183], a[92:95], a[240:243]\n"
"G 4\nv_exp_f32 v236, v236\nM 4,160,64\nR 176,191,0\nv_fma_f32 v235, v235, v219, v219\nM 4,164,80\n"
"buffer_load_dwordx4 v220, s[16:19], s63 offen lds\nM 20,160,68\nR 178,191,4096\nv_fma_f32 v236, v236, v219, v219\nM 20,164,84\nv_exp_f32 v237, v237\n"
"v_mfma_f32_16x16x32_bf16 a[228:231], v[160:163], a[72:75], a[228:231]\nR 180,191,8192\nv_fma_f32 v237, v237, v219, v219\nv_mfma_f32_16x16x32_bf16 a[228:231], v[164:167], a[88:91], a[228:231]\nbuffer_load_dwordx4 v221, s[16:19], s63 offen offset:1024 lds\nv_mfma_f32_16x16x32_bf16 a[244:247], v[160:163], a[76:79], a[244:247]\n"
"R 182,191,12288\ns_add_u32 s63, s58, 655360\nv_mfma_f32_16x16x32_bf16 a[244:247], v[164:167], a[92:95], a[244:247]\nG 4\nv_rcp_f32 v234, v234\nM 8,168,64\n"
"R 160,188,16384\nv_mul_f32 v234, v60, v234\nM 8,172,80\ns_add_u32 m0, s50, 32768\nv_rcp_f32 v235, v235\nM 24,168,68\n"
"R 162,188,20480\nv_mul_f32 v235, v61, v235\nM 24,172,84\nbuffer_load_dwordx4 v220, s[16:19], s63 offen lds\nv_mfma_f32_16x16x32_bf16 a[232:235], v[168:171], a[72:75], a[232:235]\nR 164,188,24576\n"
"v_cvt_pk_bf16_f32 v58, v234, v235\nv_mfma_f32_16x16x32_bf16 a[232:235], v[172:175], a[88:91], a[232:235]\nv_rcp_f32 v236, v236\nv_mfma_f32_16x16x32_bf16 a[248:251], v[168:171], a[76:79], a[248:251]\nR 166,188,28672\nv_mul_f32 v236, v62, v236\n"
"v_mfma_f32_16x16x32_bf16 a[248:251], v[172:175], a[92:95], a[248:251]\nG 4\nv_rcp_f32 v237, v237\nM 12,176,64\nR 168,189,16384\nv_mul_f32 v237, v63, v237\n"
"M 12,180,80\nbuffer_load_dwordx4 v221, s[16:19], s63 offen offset:1024 lds\nM 28,176,68\nR 170,189,20480\ns_add_u32 s63, s58, 196608\nM 28,180,84\n"
"s_add_u32 m0, s50, 40960\nv_cvt_pk_bf16_f32 v59, v236, v237\nbuffer_store_dwordx4 v[56:59], v252, s[20:23], s85 offen offset:1024 sc1\nv_mfma_f32_16x16x32_bf16 a[236:239], v[176:179], a[72:75], a[236:239]\nR 172,189,24576\nv_mfma_f32_16x16x32_bf16 a[236:239], v[180:183], a[88:91], a[236:239]\n"
"buffer_load_dwordx4 v220, s[16:19], s63 offen lds\nv_mfma_f32_16x16x32_bf16 a[252:255], v[176:179], a[76:79], a[252:255]\nR 174,189,28672\nv_mfma_f32_16x16x32_bf16 a[252:255], v[180:183], a[92:95], a[252:255]\ns_waitcnt vmcnt(10)\ns_barrier\n"
"G 4\nM 0,160,96\nR 176,190,16384\nM 0,164,112\nM 16,160,100\nR 178,190,20480\n"
"M 16,164,116\nv_mfma_f32_16x16x32_bf16 a[224:227], v[160:163], a[104:107], a[224:227]\nR 180,190,24576\nv_mfma_f32_16x16x32_bf16 a[224:227], v[164:167], a[120:123], a[224:227]\nv_mfma_f32_16x16x32_bf16 a[240:243], v[160:163], a[108:111], a[240:243]\nR 182,190,28672\n"
"v_mfma_f32_16x16x32_bf16 a[240:243], v[164:167], a[124:127], a[240:243]\nG 4\nM 4,168,96\nR 160,191,16384\nM 4,172,112\nM 20,168,100\n"
"R 162,191,20480\nM 20,172,116\nv_mfma_f32_16x16x32_bf16 a[228:231], v[168:171], a[104:107], a[228:231]\nR 164,191,24576\nv_mfma_f32_16x16x32_bf16 a[228:231], v[172:175], a[120:123], a[228:231]\nv_mfma_f32_16x16x32_bf16 a[244:247], v[168:171], a[108:111], a[244:247]\n"
"R 166,191,28672\nv_mfma_f32_16x16x32_bf16 a[244:247], v[172:175], a[124:127], a[244:247]\nG 4\nM 8,176,96\nR 168,188,32768\nM 8,180,112\n"
"M 24,176,100\nR 170,188,36864\nM 24,180,116\nv_mfma_f32_16x16x32_bf16 a[232:235], v[176:179], a[104:107], a[232:235]\nR 172,188,40960\nv_mfma_f32_16x16x32_bf16 a[232:235], v[180:183], a[120:123], a[232:235]\n"
"v_mfma_f32_16x16x32_bf16 a[248:251], v[176:179], a[108:111], a[248:251]\nR 174,188,45056\nv_mfma_f32_16x16x32_bf16 a[248:251], v[180:183], a[124:127], a[248:251]\nG 4\nM 12,160,96\nR 176,189,32768\n"
"M 12,164,112\nM 28,160,100\nR 178,189,36864\nM 28,164,116\nv_mfma_f32_16x16x32_bf16 a[236:239], v[160:163], a[104:107], a[236:239]\nR 180,189,40960\n"
"v_mfma_f32_16x16x32_bf16 a[236:239], v[164:167], a[120:123], a[236:239]\nv_mfma_f32_16x16x32_bf16 a[252:255], v[160:163], a[108:111], a[252:255]\nR 182,189,45056\nv_mfma_f32_16x16x32_bf16 a[252:255], v[164:167], a[124:127], a[252:255]\nG 4\nM 0,168,128\n"
"R 160,190,32768\nM 0,172,144\nM 16,168,132\nR 162,190,36864\nM 16,172,148\nv_mfma_f32_16x16x32_bf16 a[224:227], v[168:171], a[136:139], a[224:227]\n"
"R 164,190,40960\nv_mfma_f32_16x16x32_bf16 a[224:227], v[172:175], a[152:155], a[224:227]\nv_mfma_f32_16x16x32_bf16 a[240:243], v[168:171], a[140:143], a[240:243]\nR 166,190,45056\nv_mfma_f32_16x16x32_bf16 a[240:243], v[172:175], a[156:159], a[240:243]\nG 4\n"
"M 4,176,128\nR 168,191,32768\nM 4,180,144\nM 20,176,132\nR 170,191,36864\nv_mov_b32 v226, s46\n"
"M 20,180,148\nv_mov_b32 v227, s47\nv_mfma_f32_16x16x32_bf16 a[228:231], v[176:179], a[136:139], a[228:231]\nR 172,191,40960\ns_add_u32 s61, s81, 385040\ns_lshl_b32 s62, s44, 5\n"
"s_add_u32 s61, s61, s62\ns_waitcnt vmcnt(0)\ns_mov_b64 exec, 1\nbuffer_atomic_swap_x2 v[226:227], off, s[20:23], s61 sc1\ns_mov_b64 exec, -1\nv_mfma_f32_16x16x32_bf16 a[228:231], v[180:183], a[152:155], a[228:231]\n"
"v_mfma_f32_16x16x32_bf16 a[244:247], v[176:179], a[140:143], a[244:247]\nR 174,191,45056\nv_mfma_f32_16x16x32_bf16 a[244:247], v[180:183], a[156:159], a[244:247]\nG 4\nM 8,160,128\nR 176,188,49152\n"
"M 8,164,144\nbuffer_load_dwordx4 v221, s[16:19], s63 offen offset:1024 lds\nM 24,160,132\nR 178,188,53248\ns_add_u32 s63, s58, 720896\nM 24,164,148\n"
"s_add_u32 m0, s50, 49152\nv_mfma_f32_16x16x32_bf16 a[232:235], v[160:163], a[136:139], a[232:235]\nR 180,184,0\nv_mfma_f32_16x16x32_bf16 a[232:235], v[164:167], a[152:155], a[232:235]\nbuffer_load_dwordx4 v220, s[16:19], s63 offen lds\nv_mfma_f32_16x16x32_bf16 a[248:251], v[160:163], a[140:143], a[248:251]\n"
"R 182,184,4096\nv_mfma_f32_16x16x32_bf16 a[248:251], v[164:167], a[156:159], a[248:251]\nG 4\nM 12,168,128\nR 160,189,49152\nM 12,172,144\n"
"M 28,168,132\nR 162,189,53248\nM 28,172,148\nbuffer_load_dwordx4 v221, s[16:19], s63 offen offset:1024 lds\nv_mov_b32 v238, -1\nv_mov_b32 v239, -1\n"
"v_mov_b32 v240, -1\nv_mov_b32 v241, -1\nv_mfma_f32_16x16x32_bf16 a[236:239], v[168:171], a[136:139], a[236:239]\nR 164,185,0\ns_add_u32 s63, s58, 393216\nv_mfma_f32_16x16x32_bf16 a[236:239], v[172:175], a[152:155], a[236:239]\n"
"s_add_u32 m0, s50, 57344\nv_mfma_f32_16x16x32_bf16 a[252:255], v[168:171], a[140:143], a[252:255]\nR 166,185,4096\nv_mfma_f32_16x16x32_bf16 a[252:255], v[172:175], a[156:159], a[252:255]\ns_barrier\nG 4\n"
"v_mfma_f32_16x16x32_bf16 v[32:35], v[176:179], a[0:3], 0\nR 168,190,49152\nM 32,180,16\nbuffer_load_dwordx4 v220, s[16:19], s63 offen lds\nv_mfma_f32_16x16x32_bf16 v[48:51], v[176:179], a[4:7], 0\nR 170,190,53248\n"
"M 48,180,20\nv_mfma_f32_16x16x32_bf16 v[64:67], v[176:179], a[8:11], 0\nR 172,186,0\nM 64,180,24\nbuffer_load_dwordx4 v221, s[16:19], s63 offen offset:1024 lds\nv_mfma_f32_16x16x32_bf16 v[80:83], v[176:179], a[12:15], 0\n"
"R 174,186,4096\ns_add_u32 s63, s58, 458752\nM 80,180,28\nG 4\nv_mfma_f32_16x16x32_bf16 v[36:39], v[160:163], a[0:3], 0\nR 176,191,49152\n"
"M 36,164,16\ns_add_u32 m0, s50, 65536\nv_mfma_f32_16x16x32_bf16 v[52:55], v[160:163], a[4:7], 0\nR 178,191,53248\nM 52,164,20\nbuffer_load_dwordx4 v220, s[16:19], s63 offen lds\n"
"v_mfma_f32_16x16x32_bf16 v[68:71], v[160:163], a[8:11], 0\nR 180,187,0\nM 68,164,24\nv_mfma_f32_16x16x32_bf16 v[84:87], v[160:163], a[12:15], 0\nR 182,187,4096\nv_mov_b32 v248, 0\n"
"M 84,164,28\nG 4\nv_mov_b32 v249, 0\nv_mfma_f32_16x16x32_bf16 v[40:43], v[168:171], a[0:3], 0\nR 160,184,8192\ns_add_u32 s61, s82, 385040\n"
"s_mov_b64 exec, 0xf\nbuffer_atomic_or_x2 v[248:249], v250, s[20:23], s61 offen sc0 sc1\ns_mov_b64 exec, -1\nM 40,172,16\nv_mfma_f32_16x16x32_bf16 v[56:59], v[168:171], a[4:7], 0\nR 162,184,12288\n"
"M 56,172,20\nv_mfma_f32_16x16x32_bf16 v[72:75], v[168:171], a[8:11], 0\nR 164,184,16384\nM 72,172,24\nbuffer_load_dwordx4 v221, s[16:19], s63 offen offset:1024 lds\nv_mfma_f32_16x16x32_bf16 v[88:91], v[168:171], a[12:15], 0\n"
"R 166,184,20480\ns_add_u32 m0, s50, 73728\nM 88,172,28\nG 4\nv_mfma_f32_16x16x32_bf16 v[44:47], v[176:179], a[0:3], 0\nR 168,185,8192\n"
"s_add_u32 s63, s56, 262144\nM 44,180,16\nbuffer_load_dwordx4 v220, s[16:19], s63 offen lds\nv_mfma_f32_16x16x32_bf16 v[60:63], v[176:179], a[4:7], 0\nR 170,185,12288\nM 60,180,20\n"
"v_mfma_f32_16x16x32_bf16 v[76:79], v[176:179], a[8:11], 0\nR 172,185,16384\nM 76,180,24\nv_mfma_f32_16x16x32_bf16 v[92:95], v[176:179], a[12:15], 0\nR 174,185,20480\nM 92,180,28\n"
"G 4\nM 32,160,32\nR 176,186,8192\nM 32,164,48\nbuffer_load_dwordx4 v221, s[16:19], s63 offen offset:1024 lds\nM 48,160,36\n"
"R 178,186,12288\ns_add_u32 s63, s56, 786432\nM 48,164,52\ns_add_u32 m0, s50, 81920\nM 64,160,40\nR 180,186,16384\n"
"M 64,164,56\nbuffer_load_dwordx4 v220, s[16:19], s63 offen lds\nM 80,160,44\nR 182,186,20480\nM 80,164,60\nG 4\n"
"M 36,168,32\nR 160,187,8192\nM 36,172,48\nM 52,168,36\nR 162,187,12288\nM 52,172,52\n"
"buffer_load_dwordx4 v221, s[16:19], s63 offen offset:1024 lds\nM 68,168,40\nR 164,187,16384\nM 68,172,56\ns_add_u32 m0, s50, 90112\nM 84,168,44\n"
"R 166,187,20480\ns_add_u32 s63, s56, 327680\nM 84,172,60\nG 4\nM 40,176,32\nR 168,184,24576\n"
"M 40,180,48\ns_add_u32 s64, s68, 23072768\nM 56,176,36\nR 170,184,28672\ns_waitcnt vmcnt(5)\nv_cmp_eq_u64 s[86:87], s[46:47], v[248:249]\n"
"M 56,180,52\nbuffer_load_dwordx4 v220, s[16:19], s63 offen lds\nM 72,176,40\nR 172,184,32768\ns_and_b32 s62, s86, 0xf\ns_cmp_eq_u32 s62, 0xf\n"
"s_cbranch_scc0 Lq471\nLq472:\nM 72,180,56\ns_add_u32 s65, s68, 23187456\nM 88,176,44\nR 174,184,36864\n"
"s_barrier\nM 88,180,60\nG 4\nM 44,160,32\nR 176,185,24576\nM 44,164,48\n"
"buffer_load_dwordx4 a[160:163], v252, s[20:23], s64 offen sc1\nM 60,160,36\nR 178,185,28672\nM 60,164,52\ns_add_u32 s66, s69, 23072768\nM 76,160,40\n"
"R 180,185,32768\nM 76,164,56\nbuffer_load_dwordx4 a[164:167], v252, s[20:23], s65 offen sc1\nM 92,160,44\nR 182,185,36864\nM 92,164,60\n"
"G 4\nM 32,168,64\nR 160,186,24576\nM 32,172,80\ns_add_u32 s67, s69, 23187456\nM 48,168,68\n"
"R 162,186,28672\nM 48,172,84\nbuffer_load_dwordx4 a[168:171], v252, s[20:23], s66 offen sc1\nM 64,168,72\nR 164,186,32768\nM 64,172,88\n"
"buffer_load_dwordx4 v221, s[16:19], s63 offen offset:1024 lds\nM 80,168,76\nR 166,186,36864\ns_add_u32 m0, s50, 98304\nM 80,172,92\nG 4\n"
"M 36,176,64\nR 168,187,24576\ns_add_u32 s63, s56, 851968\nM 36,180,80\nbuffer_load_dwordx4 a[172:175], v252, s[20:23], s67 offen sc1\nM 52,176,68\n"
"R 170,187,28672\nM 52,180,84\ns_add_u32 s64, s68, 23079936\nM 68,176,72\nR 172,187,32768\nbuffer_load_dwordx4 v220, s[16:19], s63 offen lds\n"
"M 68,180,88\ns_add_u32 s65, s68, 23194624\nM 84,176,76\nR 174,187,36864\nM 84,180,92\nG 4\n"
"M 40,160,64\nR 176,184,40960\nbuffer_load_dwordx4 v221, s[16:19], s63 offen offset:1024 lds\nM 40,164,80\nbuffer_load_dwordx4 a[176:179], v252, s[20:23], s64 offen sc1\nM 56,160,68\n"
"R 178,184,45056\ns_add_u32 m0, s50, 106496\nM 56,164,84\ns_add_u32 s66, s69, 23079936\ns_add_u32 s63, s57, 262144\nM 72,160,72\n"
"R 180,184,49152\nbuffer_load_dwordx4 v220, s[16:19], s63 offen lds\nLq581:\ns_lshr_b32 s60, s45, 3\ns_lshl2_add_u32 s60, s60, s40\ns_lshl_b32 s60, s60, 8\n"
"v_lshl_add_u32 v224, v252, 3, s60\nv_add_u32 v224, Lq581+4096-LqPC, v224\ns_mov_b64 exec, 3\nglobal_load_dword v224, v224, s[98:99]\ns_mov_b64 exec, -1\nM 72,164,88\n"
"buffer_load_dwordx4 a[180:183], v252, s[20:23], s65 offen sc1\nM 88,160,76\nR 182,184,53248\nM 88,164,92\nG 4\nM 44,168,64\n"
"R 160,185,40960\nbuffer_load_dwordx4 v221, s[16:19], s63 offen offset:1024 lds\nM 44,172,80\ns_add_u32 s67, s69, 23194624\ns_add_u32 s63, s57, 786432\nM 60,168,68\n"
"R 162,185,45056\ns_add_u32 m0, s50, 114688\nM 60,172,84\nbuffer_load_dwordx4 a[184:187], v252, s[20:23], s66 offen sc1\nM 76,168,72\nR 164,185,49152\n"
"buffer_load_dwordx4 v220, s[16:19], s63 offen lds\nM 76,172,88\nbuffer_load_dwordx4 v221, s[16:19], s63 offen offset:1024 lds\nM 92,168,76\nR 166,185,53248\ns_add_u32 m0, s50, 0\n"
"M 92,172,92\nG 4\nM 32,176,96\nR 168,186,40960\ns_add_u32 s63, s57, 327680\nM 32,180,112\n"
"buffer_load_dwordx4 a[188:191], v252, s[20:23], s67 offen sc1\nM 48,176,100\nR 170,186,45056\nM 48,180,116\ns_add_u32 s64, s68, 23073792\nM 64,176,104\n"
"R 172,186,49152\nbuffer_load_dwordx4 v220, s[16:19], s63 offen lds\nM 64,180,120\nbuffer_load_dwordx4 v221, s[16:19], s63 offen offset:1024 lds\nM 80,176,108\nR 174,186,53248\n"
"s_add_u32 m0, s50, 8192\nM 80,180,124\nG 4\nM 36,160,96\nR 176,187,40960\ns_waitcnt vmcnt(19)\n"
"s_barrier\nM 36,164,112\nbuffer_load_dwordx4 a[192:195], v252, s[20:23], s64 offen sc1\nM 52,160,100\nR 178,187,45056\ns_add_u32 s63, s57, 851968\n"
"M 52,164,116\ns_add_u32 s65, s68, 23188480\nM 68,160,104\nR 180,187,49152\nbuffer_load_dwordx4 v220, s[16:19], s63 offen lds\nM 68,164,120\n"
"s_add_u32 s66, s69, 23073792\nM 84,160,108\nR 182,187,53248\nM 84,164,124\nG 4\nM 40,168,96\n"
"R 160,184,57344\nbuffer_load_dwordx4 v221, s[16:19], s63 offen offset:1024 lds\nM 40,172,112\nbuffer_load_dwordx4 a[196:199], v252, s[20:23], s65 offen sc1\nM 56,168,100\nR 162,184,61440\n"
"M 56,172,116\ns_add_u32 s67, s69, 23188480\nM 72,168,104\nR 164,188,0\nM 72,172,120\nbuffer_load_dwordx4 a[200:203], v252, s[20:23], s66 offen sc1\n"
"M 88,168,108\nR 166,188,4096\nM 88,172,124\nG 4\nM 44,176,96\nR 168,185,57344\n"
"M 44,180,112\ns_add_u32 s64, s68, 23080960\nM 60,176,100\nR 170,185,61440\nM 60,180,116\nbuffer_load_dwordx4 a[204:207], v252, s[20:23], s67 offen sc1\n"
"M 76,176,104\nR 172,189,0\nM 76,180,120\nM 92,176,108\nR 174,189,4096\nM 92,180,124\n"
"G 4\nM 32,160,128\nR 176,186,57344\nM 32,164,144\nbuffer_load_dwordx4 a[208:211], v252, s[20:23], s64 offen sc1\nM 48,160,132\n"
"R 178,186,61440\nM 48,164,148\ns_add_u32 s65, s68, 23195648\nM 64,160,136\nR 180,190,0\nM 64,164,152\n"
"s_add_u32 s66, s69, 23080960\nM 80,160,140\nR 182,190,4096\nM 80,164,156\nG 4\nM 36,168,128\n"
"R 160,187,57344\nM 36,172,144\nbuffer_load_dwordx4 a[212:215], v252, s[20:23], s65 offen sc1\nM 52,168,132\nR 162,187,61440\nM 52,172,148\n"
"s_add_u32 s67, s69, 23195648\nM 68,168,136\nR 164,191,0\nM 68,172,152\nbuffer_load_dwordx4 a[216:219], v252, s[20:23], s66 offen sc1\nM 84,168,140\n"
"R 166,191,4096\nM 84,172,156\nG 4\nM 40,176,128\nR 168,188,8192\nM 40,180,144\n"
"M 56,176,132\nR 170,188,12288\nM 56,180,148\nbuffer_load_dwordx4 a[220:223], v252, s[20:23], s67 offen sc1\nM 72,176,136\nR 172,188,16384\n"
"s_add_u32 s63, s58, 262144\nM 72,180,152\ns_add_u32 m0, s50, 16384\nM 88,176,140\nR 174,188,20480\nM 88,180,156\n"
"G 4\nM 44,160,128\nR 176,189,8192\nM 44,164,144\nbuffer_load_dwordx4 v220, s[16:19], s63 offen lds\nM 60,160,132\n"
"R 178,189,12288\nM 60,164,148\nM 76,160,136\nR 180,189,16384\nM 76,164,152\nbuffer_load_dwordx4 v221, s[16:19], s63 offen offset:1024 lds\n"
"M 92,160,140\nR 182,189,20480\nM 92,164,156\ns_waitcnt vmcnt(9)\ns_barrier\nG 4\n"
"M 96,168,160\nR 160,190,8192\nM 96,172,176\ns_add_u32 m0, s50, 24576\nM 112,168,164\nR 162,190,12288\n"
"M 112,172,180\ns_add_u32 s63, s58, 786432\nM 128,168,168\nR 164,190,16384\nv_lshrrev_b32 v230, 8, v252\nM 128,172,184\n"
"buffer_load_dwordx4 v220, s[16:19], s63 offen lds\nM 144,168,172\nR 166,190,20480\nv_lshlrev_b32 v230, 3, v230\nM 144,172,188\nG 4\n"
"M 100,176,160\nR 168,191,8192\nM 100,180,176\nbuffer_load_dwordx2 v[232:233], v230, s[36:39], s53 offen offset:0\nM 116,176,164\nR 170,191,12288\n"
"M 116,180,180\nbuffer_load_dwordx2 v[234:235], v230, s[36:39], s53 offen offset:32\nM 132,176,168\nR 172,191,16384\nM 132,180,184\nbuffer_load_dwordx4 v221, s[16:19], s63 offen offset:1024 lds\n"
"M 148,176,172\nR 174,191,20480\nM 148,180,188\nG 4\nM 104,160,160\nR 176,188,24576\n"
"M 104,164,176\ns_add_u32 m0, s50, 32768\nM 120,160,164\nR 178,188,28672\nM 120,164,180\ns_add_u32 s63, s58, 327680\n"
"M 136,160,168\nR 180,188,32768\nbuffer_load_dwordx2 v[236:237], v230, s[36:39], s53 offen offset:64\nM 136,164,184\nbuffer_load_dwordx4 v220, s[16:19], s63 offen lds\nM 152,160,172\n"
"R 182,188,36864\nM 152,164,188\nbuffer_load_dwordx2 v[238:239], v230, s[36:39], s53 offen offset:96\nG 4\nM 108,168,160\nR 160,189,24576\n"
"v_bfe_u32 v242, v252, 4, 4\nM 108,172,176\nv_lshrrev_b32 v243, 8, v252\nv_lshlrev_b32 v243, 2, v243\nM 124,168,164\nR 162,189,28672\n"
"v_sub_u32 v242, v242, v243\nM 124,172,180\nv_lshlrev_b32 v243, 4, v242\ns_mov_b64 s[60:61], 0x3f80\nv_lshlrev_b64 v[208:209], v243, s[60:61]\nM 140,168,168\n"
"R 164,189,32768\nM 140,172,184\nbuffer_load_dwordx4 v221, s[16:19], s63 offen offset:1024 lds\nM 156,168,172\nR 166,189,36864\nM 156,172,188\n"
"G 4\nM 96,176,192\nR 168,190,24576\ns_waitcnt vmcnt(13)\nM 96,180,208\ns_add_u32 m0, s50, 40960\n"
"M 112,176,196\nR 170,190,28672\ns_waitcnt vmcnt(12)\nM 112,180,212\ns_add_u32 s63, s58, 851968\nM 128,176,200\n"
"R 172,190,32768\ns_waitcnt vmcnt(11)\nM 128,180,216\nbuffer_load_dwordx4 v220, s[16:19], s63 offen lds\nM 144,176,204\nR 174,190,36864\n"
"s_waitcnt vmcnt(11)\nM 144,180,220\nG 4\nM 100,160,192\nR 176,191,24576\nM 100,164,208\n"
"v_cmp_gt_u32 s[60:61], 4, v242\ns_nop 1\nv_cndmask_b32 v212, 0, v208, s[60:61]\nv_cndmask_b32 v208, 0, v208, s[60:61]\nv_cndmask_b32 v213, 0, v209, s[60:61]\nv_cndmask_b32 v209, 0, v209, s[60:61]\n"
"M 116,160,196\nR 178,191,28672\nM 116,164,212\nM 132,160,200\nR 180,191,32768\nM 132,164,216\n"
"buffer_load_dwordx4 v221, s[16:19], s63 offen offset:1024 lds\nM 148,160,204\nR 182,191,36864\nM 148,164,220\nG 4\nM 104,168,192\n"
"R 160,188,40960\nM 104,172,208\ns_add_u32 m0, s50, 49152\nM 120,168,196\nR 162,188,45056\nM 120,172,212\n"
"s_add_u32 s63, s59, 0\nM 136,168,200\nR 164,188,49152\nM 136,172,216\nbuffer_load_dwordx4 v220, s[16:19], s63 offen lds\nM 152,168,204\n"
"R 166,188,53248\nM 152,172,220\nG 4\nM 108,176,192\nR 168,189,40960\nM 108,180,208\n"
"M 124,176,196\nR 170,189,45056\nM 124,180,212\nbuffer_load_dwordx4 v221, s[16:19], s63 offen offset:1024 lds\nM 140,176,200\nR 172,189,49152\n"
"M 140,180,216\nM 156,176,204\nR 174,189,53248\nM 156,180,220\nG 4\nM 0,160,160\n"
"R 176,190,40960\nM 0,164,176\ns_mov_b32 m0, s51\nM 16,160,164\nR 178,190,45056\nds_write_addtid_b32 v128 offset:0\n"
"M 16,164,180\nds_write_addtid_b32 v129 offset:256\nds_write_addtid_b32 v130 offset:512\nv_mfma_f32_16x16x32_bf16 a[224:227], v[160:163], a[168:171], a[224:227]\nR 180,190,49152\nds_write_addtid_b32 v131 offset:768\n"
"v_mfma_f32_16x16x32_bf16 a[224:227], v[164:167], a[184:187], a[224:227]\nds_write_addtid_b32 v132 offset:1024\nds_write_addtid_b32 v133 offset:1280\nv_mfma_f32_16x16x32_bf16 a[240:243], v[160:163], a[172:175], a[240:243]\nR 182,190,53248\nds_write_addtid_b32 v134 offset:1536\n"
"v_mfma_f32_16x16x32_bf16 a[240:243], v[164:167], a[188:191], a[240:243]\nds_write_addtid_b32 v135 offset:1792\nds_write_addtid_b32 v136 offset:2048\nG 13\nM 4,168,160\nR 160,191,40960\n"
"ds_write_addtid_b32 v137 offset:2304\nM 4,172,176\nds_write_addtid_b32 v138 offset:2560\nds_write_addtid_b32 v139 offset:2816\nM 20,168,164\nR 162,191,45056\n"
"ds_write_addtid_b32 v140 offset:3072\nM 20,172,180\nds_write_addtid_b32 v141 offset:3328\nds_write_addtid_b32 v142 offset:3584\nv_mfma_f32_16x16x32_bf16 a[228:231], v[168:171], a[168:171], a[228:231]\nR 164,191,49152\n"
"ds_write_addtid_b32 v143 offset:3840\nv_mfma_f32_16x16x32_bf16 a[228:231], v[172:175], a[184:187], a[228:231]\nds_write_addtid_b32 v144 offset:4096\nds_write_addtid_b32 v145 offset:4352\nv_mfma_f32_16x16x32_bf16 a[244:247], v[168:171], a[172:175], a[244:247]\nR 166,191,53248\n"
"ds_write_addtid_b32 v146 offset:4608\nv_mfma_f32_16x16x32_bf16 a[244:247], v[172:175], a[188:191], a[244:247]\nds_write_addtid_b32 v147 offset:4864\nds_write_addtid_b32 v148 offset:5120\nG 15\nM 8,176,160\n"
"R 168,184,0\nds_write_addtid_b32 v149 offset:5376\nM 8,180,176\nds_write_addtid_b32 v150 offset:5632\nds_write_addtid_b32 v151 offset:5888\nM 24,176,164\n"
"R 170,184,4096\nds_write_addtid_b32 v152 offset:6144\nM 24,180,180\nds_write_addtid_b32 v153 offset:6400\nds_write_addtid_b32 v154 offset:6656\nv_mfma_f32_16x16x32_bf16 a[232:235], v[176:179], a[168:171], a[232:235]\n"
"R 172,184,8192\nds_write_addtid_b32 v155 offset:6912\nv_mfma_f32_16x16x32_bf16 a[232:235], v[180:183], a[184:187], a[232:235]\nds_write_addtid_b32 v156 offset:7168\nds_write_addtid_b32 v157 offset:7424\nds_write_addtid_b32 v158 offset:7680\n"
"ds_write_addtid_b32 v159 offset:7936\nv_mfma_f32_16x16x32_bf16 a[248:251], v[176:179], a[172:175], a[248:251]\nR 174,184,12288\ns_add_u32 m0, s50, 57344\nv_mfma_f32_16x16x32_bf16 a[248:251], v[180:183], a[188:191], a[248:251]\ns_add_u32 s63, s59, 524288\n"
"G 15\nM 12,160,160\nR 176,185,0\ns_waitcnt vmcnt(8) lgkmcnt(2)\ns_barrier\nM 12,164,176\n"
"buffer_load_dwordx4 v220, s[16:19], s63 offen lds\nds_read2st64_b32 v[128:129], v225 offset0:0 offset1:1\nM 28,160,164\nR 178,185,4096\nds_read2st64_b32 v[130:131], v225 offset0:2 offset1:3\nM 28,164,180\n"
"ds_read2st64_b32 v[132:133], v225 offset0:4 offset1:5\nds_read2st64_b32 v[134:135], v225 offset0:6 offset1:7\nv_mfma_f32_16x16x32_bf16 a[236:239], v[160:163], a[168:171], a[236:239]\nR 180,185,8192\nds_read2st64_b32 v[136:137], v225 offset0:8 offset1:9\nv_mfma_f32_16x16x32_bf16 a[236:239], v[164:167], a[184:187], a[236:239]\n"
"buffer_load_dwordx4 v221, s[16:19], s63 offen offset:1024 lds\nds_read2st64_b32 v[138:139], v225 offset0:10 offset1:11\nv_mfma_f32_16x16x32_bf16 a[252:255], v[160:163], a[172:175], a[252:255]\nR 182,185,12288\ns_add_u32 s63, s59, 65536\nds_read2st64_b32 v[140:141], v225 offset0:12 offset1:13\n"
"v_mfma_f32_16x16x32_bf16 a[252:255], v[164:167], a[188:191], a[252:255]\nds_read2st64_b32 v[142:143], v225 offset0:14 offset1:15\nG 12\nG 10\nv_add_f32 v96, v96, v128\nM 0,168,192\n"
"R 160,186,0\nv_add_f32 v97, v97, v129\nG 9\nv_add_f32 v98, v98, v130\nM 0,172,208\ns_add_u32 m0, s50, 65536\n"
"ds_read2st64_b32 v[144:145], v225 offset0:16 offset1:17\nv_add_f32 v99, v99, v131\nM 16,168,196\nR 162,186,4096\nG 10\nv_add_f32 v100, v100, v132\n"
"v_add_f32 v101, v101, v133\nM 16,172,212\nbuffer_load_dwordx4 v220, s[16:19], s63 offen lds\nG 9\nv_add_f32 v102, v102, v134\nv_mfma_f32_16x16x32_bf16 a[224:227], v[168:171], a[200:203], a[224:227]\n"
"R 164,186,8192\nv_add_f32 v103, v103, v135\nG 8\nv_add_f32 v104, v104, v136\nv_mfma_f32_16x16x32_bf16 a[224:227], v[172:175], a[216:219], a[224:227]\nds_read2st64_b32 v[146:147], v225 offset0:18 offset1:19\n"
"v_add_f32 v105, v105, v137\nG 8\nv_add_f32 v106, v106, v138\nv_mfma_f32_16x16x32_bf16 a[240:243], v[168:171], a[204:207], a[240:243]\nR 166,186,12288\nv_add_f32 v107, v107, v139\n"
"G 7\nv_add_f32 v108, v108, v140\nv_mfma_f32_16x16x32_bf16 a[240:243], v[172:175], a[220:223], a[240:243]\nds_read2st64_b32 v[148:149], v225 offset0:20 offset1:21\nv_add_f32 v109, v109, v141\nM 4,176,192\n"
"R 168,187,0\nG 8\nv_add_f32 v110, v110, v142\nv_add_f32 v111, v111, v143\nM 4,180,208\nbuffer_load_dwordx4 v221, s[16:19], s63 offen offset:1024 lds\n"
"G 6\nv_add_f32 v112, v112, v144\nM 20,176,196\nR 170,187,4096\ns_add_u32 s63, s59, 589824\nv_add_f32 v113, v113, v145\n"
"M 20,180,212\ns_add_u32 m0, s50, 73728\nds_read2st64_b32 v[150:151], v225 offset0:22 offset1:23\nG 5\nv_add_f32 v114, v114, v146\nv_mfma_f32_16x16x32_bf16 a[228:231], v[176:179], a[200:203], a[228:231]\n"
"R 172,187,8192\nds_read2st64_b32 v[152:153], v225 offset0:24 offset1:25\nv_add_f32 v115, v115, v147\nv_mfma_f32_16x16x32_bf16 a[228:231], v[180:183], a[216:219], a[228:231]\nbuffer_load_dwordx4 v220, s[16:19], s63 offen lds\nG 5\n"
"v_add_f32 v116, v116, v148\nv_mfma_f32_16x16x32_bf16 a[244:247], v[176:179], a[204:207], a[244:247]\nR 174,187,12288\nds_read2st64_b32 v[154:155], v225 offset0:26 offset1:27\nv_add_f32 v117, v117, v149\nv_mfma_f32_16x16x32_bf16 a[244:247], v[180:183], a[220:223], a[244:247]\n"
"ds_read2st64_b32 v[156:157], v225 offset0:28 offset1:29\nds_read2st64_b32 v[158:159], v225 offset0:30 offset1:31\nM 8,160,192\nR 176,184,16384\nM 8,164,208\nG 7\n"
"v_add_f32 v118, v118, v150\nv_add_f32 v119, v119, v151\nM 24,160,196\nR 178,184,20480\nG 6\nv_add_f32 v120, v120, v152\n"
"v_add_f32 v121, v121, v153\nM 24,164,212\nbuffer_load_dwordx4 v221, s[16:19], s63 offen offset:1024 lds\nv_mfma_f32_16x16x32_bf16 a[232:235], v[160:163], a[200:203], a[232:235]\nR 180,184,24576\ns_add_u32 s63, s59, 131072\n"
"G 5\nv_add_f32 v122, v122, v154\nv_mfma_f32_16x16x32_bf16 a[232:235], v[164:167], a[216:219], a[232:235]\ns_add_u32 m0, s50, 81920\nv_add_f32 v123, v123, v155\nG 4\n"
"v_add_f32 v124, v124, v156\nv_add_f32 v125, v125, v157\nG 3\nv_add_f32 v126, v126, v158\nv_add_f32 v127, v127, v159\nv_mfma_f32_16x16x32_bf16 a[248:251], v[160:163], a[204:207], a[248:251]\n"
"R 182,184,28672\nv_cvt_pk_bf16_f32 v96, v96, v97\nv_cvt_pk_bf16_f32 v97, v98, v99\nv_mfma_f32_16x16x32_bf16 a[248:251], v[164:167], a[220:223], a[248:251]\nv_cvt_pk_bf16_f32 v98, v100, v101\nv_cvt_pk_bf16_f32 v99, v102, v103\n"
"v_cvt_pk_bf16_f32 v104, v104, v105\nM 12,168,192\nR 160,185,16384\nv_cvt_pk_bf16_f32 v105, v106, v107\nv_cvt_pk_bf16_f32 v106, v108, v109\nM 12,172,208\n"
"buffer_load_dwordx4 v220, s[16:19], s63 offen lds\nv_cvt_pk_bf16_f32 v107, v110, v111\nM 28,168,196\nR 162,185,20480\nv_cvt_pk_bf16_f32 v112, v112, v113\nv_cvt_pk_bf16_f32 v113, v114, v115\n"
"M 28,172,212\nv_cvt_pk_bf16_f32 v114, v116, v117\nv_cvt_pk_bf16_f32 v115, v118, v119\nv_cvt_pk_bf16_f32 v120, v120, v121\nv_mfma_f32_16x16x32_bf16 a[236:239], v[168:171], a[200:203], a[236:239]\nR 164,185,24576\n"
"v_cvt_pk_bf16_f32 v121, v122, v123\nv_cvt_pk_bf16_f32 v122, v124, v125\nv_mfma_f32_16x16x32_bf16 a[236:239], v[172:175], a[216:219], a[236:239]\nbuffer_load_dwordx4 v221, s[16:19], s63 offen offset:1024 lds\nv_cvt_pk_bf16_f32 v123, v126, v127\nv_mfma_f32_16x16x32_bf16 a[252:255], v[168:171], a[204:207], a[252:255]\n"
"R 166,185,28672\ns_add_u32 s63, s59, 655360\ns_bfe_u32 s100, s56, 0x20007\ns_mul_i32 s100, s100, 49152\ns_add_u32 s100, s100, s72\ns_add_u32 s100, s100, 52428800\n"
"v_mfma_f32_16x16x32_bf16 a[252:255], v[172:175], a[220:223], a[252:255]\ns_waitcnt vmcnt(10)\ns_barrier\nbuffer_store_dwordx4 v[96:99], v252, s[20:23], s100 offen offset:0 sc1\nG 4\nM 32,176,160\n"
"R 168,186,16384\nbuffer_store_dwordx4 v[104:107], v252, s[20:23], s100 offen offset:1024 sc1\nM 32,180,176\ns_add_u32 m0, s50, 90112\nbuffer_store_dwordx4 v[112:115], v252, s[20:23], s100 offen offset:2048 sc1\nv_xor_b32 v240, 0x2000, v225\n"
"M 48,176,164\nR 170,186,20480\nds_write2st64_b32 v240, a224, a225 offset0:0 offset1:1\nM 48,180,180\nbuffer_load_dwordx4 v220, s[16:19], s63 offen lds\nds_write2st64_b32 v240, a226, a227 offset0:2 offset1:3\n"
"M 64,176,168\nR 172,186,24576\nds_write2st64_b32 v240, a228, a229 offset0:4 offset1:5\nM 64,180,184\nbuffer_store_dwordx4 v[120:123], v252, s[20:23], s100 offen offset:3072 sc1\nds_write2st64_b32 v240, a230, a231 offset0:6 offset1:7\n"
"M 80,176,172\nR 174,186,28672\nds_write2st64_b32 v240, a232, a233 offset0:8 offset1:9\nM 80,180,188\nds_write2st64_b32 v240, a234, a235 offset0:10 offset1:11\nG 10\n"
"M 36,160,160\nR 176,187,16384\nds_write2st64_b32 v240, a236, a237 offset0:12 offset1:13\nM 36,164,176\nbuffer_load_dwordx4 v221, s[16:19], s63 offen offset:1024 lds\nds_write2st64_b32 v240, a238, a239 offset0:14 offset1:15\n"
"M 52,160,164\nR 178,187,20480\ns_add_u32 s63, s59, 196608\nds_write2st64_b32 v240, a240, a241 offset0:16 offset1:17\nM 52,164,180\ns_add_u32 m0, s50, 98304\n"
"ds_write2st64_b32 v240, a242, a243 offset0:18 offset1:19\nM 68,160,168\nR 180,187,24576\nv_mfma_f32_16x16x32_bf16 v[96:99], v[208:211], v[192:195], 0\nds_write2st64_b32 v240, a244, a245 offset0:20 offset1:21\nM 68,164,184\n"
"v_mfma_f32_16x16x32_bf16 v[100:103], v[210:213], v[192:195], 0\nbuffer_load_dwordx4 v220, s[16:19], s63 offen lds\nds_write2st64_b32 v240, a246, a247 offset0:22 offset1:23\nM 84,160,172\nR 182,187,28672\nv_mfma_f32_16x16x32_bf16 v[104:107], v[208:211], v[196:199], 0\n"
"ds_write2st64_b32 v240, a248, a249 offset0:24 offset1:25\nM 84,164,188\nv_mfma_f32_16x16x32_bf16 v[108:111], v[210:213], v[196:199], 0\nds_write2st64_b32 v240, a250, a251 offset0:26 offset1:27\nG 14\nM 40,168,160\n"
"R 160,184,32768\nv_mfma_f32_16x16x32_bf16 v[112:115], v[208:211], v[200:203], 0\nds_write2st64_b32 v240, a252, a253 offset0:28 offset1:29\nM 40,172,176\nv_mfma_f32_16x16x32_bf16 v[116:119], v[210:213], v[200:203], 0\nbuffer_load_dwordx4 v221, s[16:19], s63 offen offset:1024 lds\n"
"M 56,168,164\nR 162,184,36864\nv_mfma_f32_16x16x32_bf16 v[120:123], v[208:211], v[204:207], 0\ns_add_u32 m0, s50, 106496\ns_add_u32 s63, s59, 720896\nM 56,172,180\n"
"v_mfma_f32_16x16x32_bf16 v[124:127], v[210:213], v[204:207], 0\nbuffer_load_dwordx4 v220, s[16:19], s63 offen lds\nM 72,168,168\nR 164,184,40960\nv_mfma_f32_16x16x32_bf16 v[96:99], v[208:211], v[232:235], v[96:99]\nds_write2st64_b32 v240, a254, a255 offset0:30 offset1:31\n"
"M 72,172,184\nv_mfma_f32_16x16x32_bf16 v[100:103], v[210:213], v[232:235], v[100:103]\nM 88,168,172\nR 166,184,45056\nv_mfma_f32_16x16x32_bf16 v[104:107], v[208:211], v[236:239], v[104:107]\nM 88,172,188\n"
"v_mfma_f32_16x16x32_bf16 v[108:111], v[210:213], v[236:239], v[108:111]\nG 8\nM 44,176,160\nR 168,185,32768\nv_mfma_f32_16x16x32_bf16 v[112:115], v[208:211], v[232:235], v[112:115]\ns_waitcnt vmcnt(15) lgkmcnt(2)\n"
"s_barrier\nM 44,180,176\nv_mfma_f32_16x16x32_bf16 v[116:119], v[210:213], v[232:235], v[116:119]\nbuffer_load_dwordx4 v221, s[16:19], s63 offen offset:1024 lds\nds_read2st64_b32 v[128:129], v225 offset0:0 offset1:1\nM 60,176,164\n"
"R 170,185,36864\nv_mfma_f32_16x16x32_bf16 v[120:123], v[208:211], v[236:239], v[120:123]\ns_add_u32 s63, s59, 393216\nds_read2st64_b32 v[130:131], v225 offset0:2 offset1:3\nM 60,180,180\nv_mfma_f32_16x16x32_bf16 v[124:127], v[210:213], v[236:239], v[124:127]\n"
"s_add_u32 m0, s50, 114688\nds_read2st64_b32 v[132:133], v225 offset0:4 offset1:5\nds_read2st64_b32 v[134:135], v225 offset0:6 offset1:7\nM 76,176,168\nR 172,185,40960\nds_read2st64_b32 v[136:137], v225 offset0:8 offset1:9\n"
"M 76,180,184\nbuffer_load_dwordx4 v220, s[16:19], s63 offen lds\nds_read2st64_b32 v[138:139], v225 offset0:10 offset1:11\nM 92,176,172\nR 174,185,45056\nds_read2st64_b32 v[140:141], v225 offset0:12 offset1:13\n"
"M 92,180,188\nds_read2st64_b32 v[142:143], v225 offset0:14 offset1:15\nG 12\nG 10\nv_add_f32 v0, v0, v128\nM 32,160,192\n"
"R 176,186,32768\nv_add_f32 v1, v1, v129\nG 9\nv_add_f32 v2, v2, v130\nM 32,164,208\nds_read2st64_b32 v[144:145], v225 offset0:16 offset1:17\n"
"v_add_f32 v3, v3, v131\nG 9\nv_add_f32 v4, v4, v132\nM 48,160,196\nR 178,186,36864\nv_add_f32 v5, v5, v133\n"
"G 9\nv_add_f32 v6, v6, v134\nM 48,164,212\nbuffer_load_dwordx4 v221, s[16:19], s63 offen offset:1024 lds\nv_add_f32 v7, v7, v135\nM 64,160,200\n"
"R 180,186,40960\ns_add_u32 s63, s59, 458752\nG 8\nv_add_f32 v8, v8, v136\nM 64,164,216\ns_add_u32 m0, s50, 0\n"
"ds_read2st64_b32 v[146:147], v225 offset0:18 offset1:19\nv_add_f32 v9, v9, v137\nM 80,160,204\nR 182,186,45056\nG 9\nv_add_f32 v10, v10, v138\n"
"v_add_f32 v11, v11, v139\nM 80,164,220\nds_read2st64_b32 v[148:149], v225 offset0:20 offset1:21\nG 9\nG 8\nv_add_f32 v12, v12, v140\n"
"M 36,168,192\nR 160,187,32768\nv_add_f32 v13, v13, v141\nG 8\nv_add_f32 v14, v14, v142\nM 36,172,208\n"
"buffer_load_dwordx4 v220, s[16:19], s63 offen lds\nv_add_f32 v15, v15, v143\nM 52,168,196\nR 162,187,36864\nG 7\nv_add_f32 v16, v16, v144\n"
"v_add_f32 v17, v17, v145\nM 52,172,212\nds_read2st64_b32 v[150:151], v225 offset0:22 offset1:23\nG 5\nv_add_f32 v18, v18, v146\nv_add_f32 v19, v19, v147\n"
"M 68,168,200\nR 164,187,40960\nds_read2st64_b32 v[152:153], v225 offset0:24 offset1:25\nM 68,172,216\nbuffer_load_dwordx4 v221, s[16:19], s63 offen offset:1024 lds\nG 5\n"
"v_add_f32 v20, v20, v148\nM 84,168,204\nR 166,187,45056\ns_add_u32 s63, s59, 262144\nv_add_f32 v21, v21, v149\nM 84,172,220\n"
"ds_read2st64_b32 v[154:155], v225 offset0:26 offset1:27\nds_read2st64_b32 v[156:157], v225 offset0:28 offset1:29\nM 40,176,192\nR 168,184,49152\nds_read2st64_b32 v[158:159], v225 offset0:30 offset1:31\nM 40,180,208\n"
"s_add_u32 m0, s50, 8192\nG 7\nv_add_f32 v22, v22, v150\nv_add_f32 v23, v23, v151\nM 56,176,196\nR 170,184,53248\n"
"G 6\nv_add_f32 v24, v24, v152\nv_add_f32 v25, v25, v153\nM 56,180,212\nbuffer_load_dwordx4 v220, s[16:19], s63 offen lds\nM 72,176,200\n"
"R 172,184,57344\nM 72,180,216\nG 5\nv_add_f32 v26, v26, v154\nv_add_f32 v27, v27, v155\nG 4\n"
"v_add_f32 v28, v28, v156\nv_add_f32 v29, v29, v157\nG 2\nv_add_f32 v30, v30, v158\nv_add_f32 v31, v31, v159\nM 88,176,204\n"
"R 174,184,61440\nv_cvt_pk_bf16_f32 v0, v0, v1\nv_cvt_pk_bf16_f32 v1, v2, v3\nM 88,180,220\nv_cvt_pk_bf16_f32 v2, v4, v5\nv_cvt_pk_bf16_f32 v3, v6, v7\n"
"v_cvt_pk_bf16_f32 v8, v8, v9\nM 44,160,192\nR 176,185,49152\nv_cvt_pk_bf16_f32 v9, v10, v11\nv_cvt_pk_bf16_f32 v10, v12, v13\nM 44,164,208\n"
"buffer_load_dwordx4 v221, s[16:19], s63 offen offset:1024 lds\nv_cvt_pk_bf16_f32 v11, v14, v15\nM 60,160,196\nR 178,185,53248\ns_add_u32 s63, s59, 786432\nv_cvt_pk_bf16_f32 v16, v16, v17\n"
"M 60,164,212\ns_add_u32 m0, s50, 16384\nv_cvt_pk_bf16_f32 v17, v18, v19\nv_cvt_pk_bf16_f32 v18, v20, v21\nv_cvt_pk_bf16_f32 v19, v22, v23\nM 76,160,200\n"
"R 180,185,57344\nv_cvt_pk_bf16_f32 v24, v24, v25\nv_cvt_pk_bf16_f32 v25, v26, v27\nM 76,164,216\nbuffer_load_dwordx4 v220, s[16:19], s63 offen lds\nv_cvt_pk_bf16_f32 v26, v28, v29\n"
"M 92,160,204\nR 182,185,61440\ns_bfe_u32 s100, s57, 0x20007\ns_mul_i32 s100, s100, 49152\ns_add_u32 s100, s100, s72\ns_add_u32 s100, s100, 52445184\n"
"v_cvt_pk_bf16_f32 v27, v30, v31\nM 92,164,220\ns_waitcnt vmcnt(19)\ns_barrier\nbuffer_store_dwordx4 v[0:3], v252, s[20:23], s100 offen offset:0 sc1\nG 4\n"
"M 96,168,0\nR 160,186,49152\nbuffer_store_dwordx4 v[8:11], v252, s[20:23], s100 offen offset:1024 sc1\nM 96,172,16\ns_mov_b32 m0, s51\nbuffer_store_dwordx4 v[16:19], v252, s[20:23], s100 offen offset:2048 sc1\n"
"M 112,168,4\nR 162,186,53248\nds_write_addtid_b32 v64 offset:0\nM 112,172,20\nds_write_addtid_b32 v65 offset:256\nds_write_addtid_b32 v66 offset:512\n"
"v_mfma_f32_16x16x32_bf16 v[128:131], v[168:171], a[8:11], 0\nR 164,186,57344\nds_write_addtid_b32 v67 offset:768\nM 128,172,24\nds_write_addtid_b32 v68 offset:1024\nds_write_addtid_b32 v69 offset:1280\n"
"v_mfma_f32_16x16x32_bf16 v[144:147], v[168:171], a[12:15], 0\nR 166,186,61440\nds_write_addtid_b32 v70 offset:1536\nM 144,172,28\nds_write_addtid_b32 v71 offset:1792\nds_write_addtid_b32 v72 offset:2048\n"
"G 13\nM 100,176,0\nR 168,187,49152\nds_write_addtid_b32 v73 offset:2304\nM 100,180,16\nds_write_addtid_b32 v74 offset:2560\n"
"buffer_store_dwordx4 v[24:27], v252, s[20:23], s100 offen offset:3072 sc1\nds_write_addtid_b32 v75 offset:2816\nM 116,176,4\nR 170,187,53248\nds_write_addtid_b32 v76 offset:3072\nM 116,180,20\n"
"ds_write_addtid_b32 v77 offset:3328\nds_write_addtid_b32 v78 offset:3584\nv_mfma_f32_16x16x32_bf16 v[132:135], v[176:179], a[8:11], 0\nR 172,187,57344\nds_write_addtid_b32 v79 offset:3840\nM 132,180,24\n"
"ds_write_addtid_b32 v80 offset:4096\nds_write_addtid_b32 v81 offset:4352\nv_mfma_f32_16x16x32_bf16 v[148:151], v[176:179], a[12:15], 0\nR 174,187,61440\nds_write_addtid_b32 v82 offset:4608\nM 148,180,28\n"
"ds_write_addtid_b32 v83 offset:4864\nds_write_addtid_b32 v84 offset:5120\nG 15\nM 104,160,0\nR 176,188,0\nds_write_addtid_b32 v85 offset:5376\n"
"M 104,164,16\nds_write_addtid_b32 v86 offset:5632\nds_write_addtid_b32 v87 offset:5888\nM 120,160,4\nR 178,188,4096\nds_write_addtid_b32 v88 offset:6144\n"
"M 120,164,20\nds_write_addtid_b32 v89 offset:6400\nds_write_addtid_b32 v90 offset:6656\nv_mfma_f32_16x16x32_bf16 v[136:139], v[160:163], a[8:11], 0\nR 180,188,8192\nds_write_addtid_b32 v91 offset:6912\n"
"M 136,164,24\nds_write_addtid_b32 v92 offset:7168\nds_write_addtid_b32 v93 offset:7424\nds_write_addtid_b32 v94 offset:7680\nds_write_addtid_b32 v95 offset:7936\nv_mfma_f32_16x16x32_bf16 v[152:155], v[160:163], a[12:15], 0\n"
"R 182,188,12288\nM 152,164,28\nG 15\nM 108,168,0\nR 160,189,0\ns_waitcnt vmcnt(15) lgkmcnt(2)\n"
"s_barrier\nM 108,172,16\nds_read2st64_b32 v[64:65], v225 offset0:0 offset1:1\nds_read2st64_b32 v[66:67], v225 offset0:2 offset1:3\nM 124,168,4\nR 162,189,4096\n"
"ds_read2st64_b32 v[68:69], v225 offset0:4 offset1:5\nM 124,172,20\nds_read2st64_b32 v[70:71], v225 offset0:6 offset1:7\nds_read2st64_b32 v[72:73], v225 offset0:8 offset1:9\nv_mfma_f32_16x16x32_bf16 v[140:143], v[168:171], a[8:11], 0\nR 164,189,8192\n"
"ds_read2st64_b32 v[74:75], v225 offset0:10 offset1:11\nM 140,172,24\nds_read2st64_b32 v[76:77], v225 offset0:12 offset1:13\nds_read2st64_b32 v[78:79], v225 offset0:14 offset1:15\nv_mfma_f32_16x16x32_bf16 v[156:159], v[168:171], a[12:15], 0\nR 166,189,12288\n"
"ds_read2st64_b32 v[80:81], v225 offset0:16 offset1:17\nM 156,172,28\nds_read2st64_b32 v[82:83], v225 offset0:18 offset1:19\nG 14\nG 12\nv_add_f32 v32, v32, v64\n"
"M 96,176,32\nR 168,190,0\nv_add_f32 v33, v33, v65\nG 12\nv_add_f32 v34, v34, v66\nM 96,180,48\n"
"ds_read2st64_b32 v[84:85], v225 offset0:20 offset1:21\nv_add_f32 v35, v35, v67\nG 11\nv_add_f32 v36, v36, v68\nM 112,176,36\nR 170,190,4096\n"
"v_add_f32 v37, v37, v69\nG 11\nv_add_f32 v38, v38, v70\nM 112,180,52\nds_read2st64_b32 v[86:87], v225 offset0:22 offset1:23\nv_add_f32 v39, v39, v71\n"
"G 11\nv_add_f32 v40, v40, v72\nM 128,176,40\nR 172,190,8192\nv_add_f32 v41, v41, v73\nG 10\n"
"v_add_f32 v42, v42, v74\nM 128,180,56\nds_read2st64_b32 v[88:89], v225 offset0:24 offset1:25\nv_add_f32 v43, v43, v75\nG 10\nv_add_f32 v44, v44, v76\n"
"M 144,176,44\nR 174,190,12288\nv_add_f32 v45, v45, v77\nG 10\nv_add_f32 v46, v46, v78\nM 144,180,60\n"
"ds_read2st64_b32 v[90:91], v225 offset0:26 offset1:27\nG 10\nv_add_f32 v47, v47, v79\nM 100,160,32\nR 176,191,0\nG 10\n"
"v_add_f32 v48, v48, v80\nv_add_f32 v49, v49, v81\nM 100,164,48\nds_read2st64_b32 v[92:93], v225 offset0:28 offset1:29\nG 10\nv_add_f32 v50, v50, v82\n"
"v_add_f32 v51, v51, v83\nM 116,160,36\nR 178,191,4096\nG 9\nv_add_f32 v52, v52, v84\nM 116,164,52\n"
"ds_read2st64_b32 v[94:95], v225 offset0:30 offset1:31\nv_add_f32 v53, v53, v85\nM 132,160,40\nR 180,191,8192\nG 9\nv_add_f32 v54, v54, v86\n"
"M 132,164,56\nv_add_f32 v55, v55, v87\nG 7\nv_add_f32 v56, v56, v88\nM 148,160,44\nR 182,191,12288\n"
"v_add_f32 v57, v57, v89\nM 148,164,60\nG 7\nG 6\nv_add_f32 v58, v58, v90\nv_add_f32 v59, v59, v91\n"
"M 104,168,32\nR 160,188,16384\nG 5\nv_add_f32 v60, v60, v92\nM 104,172,48\nv_add_f32 v61, v61, v93\n"
"G 3\nv_add_f32 v62, v62, v94\nM 120,168,36\nR 162,188,20480\nv_mov_b32 v226, s46\nM 120,172,52\n"
"v_mov_b32 v227, s47\nv_add_f32 v63, v63, v95\nM 136,168,40\nR 164,188,24576\ns_bfe_u32 s62, s56, 0x20007\ns_lshl_b32 s62, s62, 5\n"
"s_add_u32 s61, s74, 548864\ns_add_u32 s61, s61, s62\ns_waitcnt vmcnt(0)\ns_mov_b64 exec, 1\nbuffer_atomic_swap_x2 v[226:227], off, s[20:23], s61 sc1\ns_mov_b64 exec, -1\n"
"M 136,172,56\ns_bfe_u32 s62, s57, 0x20007\ns_lshl_b32 s62, s62, 5\ns_add_u32 s61, s74, 548872\ns_add_u32 s61, s61, s62\ns_mov_b64 exec, 1\n"
"buffer_atomic_swap_x2 v[226:227], off, s[20:23], s61 sc1\ns_mov_b64 exec, -1\nM 152,168,44\nR 166,188,28672\nv_cvt_pk_bf16_f32 v32, v32, v33\nv_cvt_pk_bf16_f32 v33, v34, v35\n"
"M 152,172,60\nG 4\nv_cvt_pk_bf16_f32 v34, v36, v37\nv_cvt_pk_bf16_f32 v35, v38, v39\nv_cvt_pk_bf16_f32 v40, v40, v41\nM 108,176,32\n"
"R 168,189,16384\nv_cvt_pk_bf16_f32 v41, v42, v43\nv_cvt_pk_bf16_f32 v42, v44, v45\nM 108,180,48\ns_add_u32 m0, s50, 16384\nv_cvt_pk_bf16_f32 v43, v46, v47\n"
"v_cvt_pk_bf16_f32 v48, v48, v49\nv_cvt_pk_bf16_f32 v49, v50, v51\nM 124,176,36\nR 170,189,20480\nv_cvt_pk_bf16_f32 v50, v52, v53\nv_cvt_pk_bf16_f32 v51, v54, v55\n"
"M 124,180,52\nv_cvt_pk_bf16_f32 v56, v56, v57\nv_cvt_pk_bf16_f32 v57, v58, v59\nM 140,176,40\nR 172,189,24576\nv_cvt_pk_bf16_f32 v58, v60, v61\n"
"M 140,180,56\nbuffer_load_dwordx4 v221, s[16:19], s63 offen offset:1024 lds\nM 156,176,44\nR 174,189,28672\ns_add_u32 m0, s50, 24576\nM 156,180,60\n"
"s_barrier\nG 4\nM 96,160,64\nR 176,190,16384\ns_add_u32 s63, s59, 327680\nM 96,164,80\n"
"buffer_load_dwordx4 v220, s[16:19], s63 offen lds\nM 112,160,68\nR 178,190,20480\nv_cvt_pk_bf16_f32 v59, v62, v63\nM 112,164,84\ns_bfe_u32 s100, s58, 0x20007\n"
"s_mul_i32 s100, s100, 49152\ns_add_u32 s100, s100, s72\ns_add_u32 s100, s100, 52461568\nM 128,160,72\nR 180,190,24576\nbuffer_store_dwordx4 v[32:35], v252, s[20:23], s100 offen offset:0 sc1\n"
"M 128,164,88\nbuffer_store_dwordx4 v[40:43], v252, s[20:23], s100 offen offset:1024 sc1\nM 144,160,76\nR 182,190,28672\nbuffer_store_dwordx4 v[48:51], v252, s[20:23], s100 offen offset:2048 sc1\nM 144,164,92\n"
"buffer_store_dwordx4 v[56:59], v252, s[20:23], s100 offen offset:3072 sc1\nG 4\nM 100,168,64\nR 160,191,16384\nLq583:\ns_lshr_b32 s60, s45, 3\n"
"s_lshl2_add_u32 s60, s60, s40\ns_lshl_b32 s60, s60, 8\nv_lshl_add_u32 v224, v252, 3, s60\nv_add_u32 v224, Lq583+4096-LqPC, v224\ns_mov_b64 exec, 3\nglobal_load_dword v224, v224, s[98:99]\n"
"s_mov_b64 exec, -1\nM 100,172,80\nv_bfe_u32 v250, v252, 4, 2\nv_lshlrev_b32 v250, 3, v250\nM 116,168,68\nR 162,191,20480\n"
"M 116,172,84\ns_lshl_b32 s60, s43, 5\nv_add_u32 v250, s60, v250\nM 132,168,72\nR 164,191,24576\nM 132,172,88\n"
"buffer_load_dwordx4 v221, s[16:19], s63 offen offset:1024 lds\nM 148,168,76\nR 166,191,28672\ns_add_u32 m0, s50, 32768\nM 148,172,92\nG 4\n"
"M 104,176,64\nR 168,188,32768\ns_add_u32 s63, s59, 851968\nM 104,180,80\nbuffer_load_dwordx4 v220, s[16:19], s63 offen lds\nM 120,176,68\n"
"R 170,188,36864\nM 120,180,84\nM 136,176,72\nR 172,188,40960\nM 136,180,88\nM 152,176,76\n"
"R 174,188,45056\nM 152,180,92\nG 4\nM 108,160,64\nR 176,189,32768\nM 108,164,80\n"
"buffer_load_dwordx4 v221, s[16:19], s63 offen offset:1024 lds\nM 124,160,68\nR 178,189,36864\nM 124,164,84\nM 140,160,72\nR 180,189,40960\n"
"M 140,164,88\ns_movk_i32 s63, 6144\nv_mad_u32_u24 v220, v228, s63, v229\nv_add_u32 v221, 3144704, v220\ns_add_u32 m0, s50, 40960\ns_add_u32 s63, s52, 2048\n"
"buffer_load_dwordx4 v220, s[4:7], s63 offen lds\nM 156,160,76\nR 182,189,45056\nM 156,164,92\nG 4\nM 96,168,96\n"
"R 160,190,32768\nM 96,172,112\nM 112,168,100\nR 162,190,36864\nM 112,172,116\nbuffer_load_dwordx4 v221, s[4:7], s63 offen offset:1024 lds\n"
"M 128,168,104\nR 164,190,40960\ns_add_u32 s63, s52, 198656\nM 128,172,120\ns_add_u32 m0, s50, 49152\nM 144,168,108\n"
"R 166,190,45056\nM 144,172,124\nG 4\nM 100,176,96\nR 168,191,32768\nM 100,180,112\n"
"buffer_load_dwordx4 v220, s[4:7], s63 offen lds\nM 116,176,100\nR 170,191,36864\nM 116,180,116\nM 132,176,104\nR 172,191,40960\n"
"v_mov_b32 v248, 0\nM 132,180,120\nv_mov_b32 v249, 0\nM 148,176,108\nR 174,191,45056\ns_add_u32 s61, s74, 548864\n"
"s_mov_b64 exec, 3\nbuffer_atomic_or_x2 v[248:249], v250, s[20:23], s61 offen sc0 sc1\ns_mov_b64 exec, -1\nM 148,180,124\nG 4\nM 104,160,96\n"
"R 176,188,49152\ns_waitcnt vmcnt(13)\ns_barrier\nM 104,164,112\nM 120,160,100\nR 178,188,53248\n"
"M 120,164,116\nbuffer_load_dwordx4 v221, s[4:7], s63 offen offset:1024 lds\nM 136,160,104\nR 180,184,0\ns_add_u32 s63, s52, 4096\nM 136,164,120\n"
"s_add_u32 m0, s50, 57344\nM 152,160,108\nR 182,184,4096\nM 152,164,124\nG 4\nM 108,168,96\n"
"R 160,189,49152\nM 108,172,112\nbuffer_load_dwordx4 v220, s[4:7], s63 offen lds\nM 124,168,100\nR 162,189,53248\nM 124,172,116\n"
"M 140,168,104\nR 164,185,0\nM 140,172,120\nbuffer_load_dwordx4 v221, s[4:7], s63 offen offset:1024 lds\nM 156,168,108\nR 166,185,4096\n"
"M 156,172,124\nG 4\nM 96,176,128\nR 168,190,49152\nM 96,180,144\nM 112,176,132\n"
"R 170,190,53248\nM 112,180,148\ns_add_u32 m0, s50, 65536\nM 128,176,136\nR 172,186,0\ns_lshr_b32 s60, s45, 3\n"
"s_lshl2_add_u32 s60, s60, s40\ns_lshl_b32 s60, s60, 8\ns_waitcnt vmcnt(10)\nv_lshl_add_u32 v224, v252, 3, s60\nv_add_u32 v224, LqM-LqPC, v224\ns_mov_b64 exec, 3\n"
"global_load_dword v224, v224, s[98:99]\ns_mov_b64 exec, -1\nM 128,180,152\ns_add_u32 s63, s52, 200704\nM 144,176,140\nR 174,186,4096\n"
"M 144,180,156\nG 4\nM 100,160,128\nR 176,191,49152\nM 100,164,144\nbuffer_load_dwordx4 v220, s[4:7], s63 offen lds\n"
"M 116,160,132\nR 178,191,53248\nM 116,164,148\nM 132,160,136\nR 180,187,0\nM 132,164,152\n"
"s_mul_i32 s64, s43, 49152\nM 148,160,140\nR 182,187,4096\nM 148,164,156\nG 4\nM 104,168,128\n"
"R 160,184,8192\ns_waitcnt vmcnt(5)\nv_cmp_eq_u64 s[86:87], s[46:47], v[248:249]\nM 104,172,144\ns_add_u32 s64, s64, s72\nM 120,168,132\n"
"R 162,184,12288\ns_and_b32 s62, s86, 3\ns_cmp_eq_u32 s62, 3\ns_cbranch_scc0 Lq514\nLq515:\nM 120,172,148\n"
"s_add_u32 s64, s64, 52428800\nM 136,168,136\nR 164,184,16384\nM 136,172,152\nbuffer_load_dwordx4 v[0:3], v252, s[20:23], s64 offen offset:0 sc1\nM 152,168,140\n"
"R 166,184,20480\nM 152,172,156\nG 4\nM 108,176,128\nR 168,185,8192\ns_barrier\n"
"M 108,180,144\nbuffer_load_dwordx4 v[4:7], v252, s[20:23], s64 offen offset:1024 sc1\nM 124,176,132\nR 170,185,12288\ns_mul_i32 s65, s43, 49152\nM 124,180,148\n"
"buffer_load_dwordx4 v[8:11], v252, s[20:23], s64 offen offset:2048 sc1\nM 140,176,136\nR 172,185,16384\ns_add_u32 s65, s65, s72\nM 140,180,152\nbuffer_load_dwordx4 v[12:15], v252, s[20:23], s64 offen offset:3072 sc1\n"
"M 156,176,140\nR 174,185,20480\ns_add_u32 s65, s65, 52445184\nM 156,180,156\nG 4\nM 128,160,168\n"
"R 176,186,8192\nbuffer_load_dwordx4 v[16:19], v252, s[20:23], s65 offen offset:0 sc1\nM 128,164,184\nR 178,186,12288\nM 144,160,172\nR 180,186,16384\n"
"M 144,164,188\nR 182,186,20480\nG 4\nM 132,168,168\nR 160,187,8192\nbuffer_load_dwordx4 v[20:23], v252, s[20:23], s65 offen offset:1024 sc1\n"
"M 132,172,184\nR 162,187,12288\nM 148,168,172\nR 164,187,16384\nbuffer_load_dwordx4 v[24:27], v252, s[20:23], s65 offen offset:2048 sc1\nM 148,172,188\n"
"R 166,187,20480\nG 4\nM 136,176,168\nR 168,184,24576\ns_mul_i32 s66, s43, 49152\nM 136,180,184\n"
"R 170,184,28672\nbuffer_load_dwordx4 v[28:31], v252, s[20:23], s65 offen offset:3072 sc1\nM 152,176,172\nR 172,184,32768\ns_add_u32 s66, s66, s72\nM 152,180,188\n"
"R 174,184,36864\nG 4\nM 140,160,168\nR 176,185,24576\ns_add_u32 s66, s66, 52461568\nM 140,164,184\n"
"R 178,185,28672\nM 156,160,172\nR 180,185,32768\nM 156,164,188\nR 182,185,36864\nG 4\n"
"M 128,168,200\nR 160,186,24576\nbuffer_load_dwordx4 v[64:67], v252, s[20:23], s66 offen offset:0 sc1\nM 128,172,216\nR 162,186,28672\nM 144,168,204\n"
"R 164,186,32768\nbuffer_load_dwordx4 v[68:71], v252, s[20:23], s66 offen offset:1024 sc1\nM 144,172,220\nR 166,186,36864\nG 4\nM 132,176,200\n"
"R 168,187,24576\nM 132,180,216\nR 170,187,28672\nbuffer_load_dwordx4 v[72:75], v252, s[20:23], s66 offen offset:2048 sc1\nM 148,176,204\nR 172,187,32768\n"
"M 148,180,220\nR 174,187,36864\nG 4\nM 136,160,200\nR 176,184,8192\nbuffer_load_dwordx4 v[76:79], v252, s[20:23], s66 offen offset:3072 sc1\n"
"M 136,164,216\nR 178,184,12288\nbuffer_load_dwordx4 v221, s[4:7], s63 offen offset:1024 lds\nM 152,160,204\nR 180,184,16384\ns_add_u32 m0, s50, 73728\n"
"s_add_u32 s63, s52, 395264\nM 152,164,220\nR 182,184,20480\nG 4\nM 140,168,200\nR 160,185,8192\n"
"buffer_load_dwordx4 v220, s[4:7], s63 offen lds\nM 140,172,216\nR 162,185,12288\nM 156,168,204\nR 164,185,16384\nM 156,172,220\n"
"R 166,185,20480\nG 4\nM 96,176,160\nR 168,186,8192\nbuffer_load_dwordx4 v221, s[4:7], s63 offen offset:1024 lds\nM 96,180,176\n"
"R 170,186,12288\ns_mov_b32 m0, s51\nM 112,176,164\nR 172,186,16384\nds_write_addtid_b32 v128 offset:0\nM 112,180,180\n"
"R 174,186,20480\nds_write_addtid_b32 v129 offset:256\nG 6\nM 100,160,160\nR 176,187,8192\nds_write_addtid_b32 v130 offset:512\n"
"M 100,164,176\nR 178,187,12288\nds_write_addtid_b32 v131 offset:768\nM 116,160,164\nR 180,187,16384\nds_write_addtid_b32 v132 offset:1024\n"
"M 116,164,180\nR 182,187,20480\nds_write_addtid_b32 v133 offset:1280\nG 9\nM 104,168,160\nR 160,184,24576\n"
"ds_write_addtid_b32 v134 offset:1536\nM 104,172,176\nR 162,184,28672\nds_write_addtid_b32 v135 offset:1792\nM 120,168,164\nR 164,184,32768\n"
"ds_write_addtid_b32 v136 offset:2048\nM 120,172,180\nR 166,184,36864\nds_write_addtid_b32 v137 offset:2304\nG 9\nM 108,176,160\n"
"R 168,185,24576\nds_write_addtid_b32 v138 offset:2560\nM 108,180,176\nR 170,185,28672\nds_write_addtid_b32 v139 offset:2816\nM 124,176,164\n"
"R 172,185,32768\nds_write_addtid_b32 v140 offset:3072\nds_write_addtid_b32 v141 offset:3328\nds_write_addtid_b32 v142 offset:3584\nds_write_addtid_b32 v143 offset:3840\nds_write_addtid_b32 v144 offset:4096\n"
"ds_write_addtid_b32 v145 offset:4352\nds_write_addtid_b32 v146 offset:4608\nds_write_addtid_b32 v147 offset:4864\nds_write_addtid_b32 v148 offset:5120\nds_write_addtid_b32 v149 offset:5376\nds_write_addtid_b32 v150 offset:5632\n"
"ds_write_addtid_b32 v151 offset:5888\nds_write_addtid_b32 v152 offset:6144\nds_write_addtid_b32 v153 offset:6400\nds_write_addtid_b32 v154 offset:6656\nds_write_addtid_b32 v155 offset:6912\nds_write_addtid_b32 v156 offset:7168\n"
"ds_write_addtid_b32 v157 offset:7424\nds_write_addtid_b32 v158 offset:7680\nds_write_addtid_b32 v159 offset:7936\nM 124,180,180\nR 174,185,36864\ns_add_u32 m0, s50, 81920\n"
"s_add_u32 s63, s52, 591872\nbuffer_load_dwordx4 v220, s[4:7], s63 offen lds\nG 15\nM 96,160,192\nR 176,186,24576\nbuffer_load_dwordx4 v221, s[4:7], s63 offen offset:1024 lds\n"
"M 96,164,208\nR 178,186,28672\nG 3\ns_barrier\nM 112,160,196\nR 180,186,32768\n"
"s_add_u32 m0, s50, 90112\nds_read2st64_b32 v[128:129], v225 offset0:0 offset1:1\nM 112,164,212\nR 182,186,36864\nds_read2st64_b32 v[130:131], v225 offset0:2 offset1:3\nG 6\n"
"M 100,168,192\nR 160,187,24576\ns_add_u32 s63, s52, 397312\nM 100,172,208\nR 162,187,28672\nds_read2st64_b32 v[132:133], v225 offset0:4 offset1:5\n"
"M 116,168,196\nR 164,187,32768\nds_read2st64_b32 v[134:135], v225 offset0:6 offset1:7\nM 116,172,212\nR 166,187,36864\nG 7\n"
"M 104,176,192\nbuffer_load_dwordx4 v220, s[4:7], s63 offen lds\nM 104,180,208\nds_read2st64_b32 v[136:137], v225 offset0:8 offset1:9\nds_read2st64_b32 v[138:139], v225 offset0:10 offset1:11\nM 120,176,196\n"
"ds_read2st64_b32 v[140:141], v225 offset0:12 offset1:13\nds_read2st64_b32 v[142:143], v225 offset0:14 offset1:15\nM 120,180,212\nds_read2st64_b32 v[144:145], v225 offset0:16 offset1:17\nds_read2st64_b32 v[146:147], v225 offset0:18 offset1:19\nG 6\n"
"M 108,160,192\nbuffer_load_dwordx4 v221, s[4:7], s63 offen offset:1024 lds\nM 108,164,208\nds_read2st64_b32 v[148:149], v225 offset0:20 offset1:21\nds_read2st64_b32 v[150:151], v225 offset0:22 offset1:23\nM 124,160,196\n"
"ds_read2st64_b32 v[152:153], v225 offset0:24 offset1:25\nds_read2st64_b32 v[154:155], v225 offset0:26 offset1:27\nM 124,164,212\nds_read2st64_b32 v[156:157], v225 offset0:28 offset1:29\nds_read2st64_b32 v[158:159], v225 offset0:30 offset1:31\ns_lshl_b32 s84, s75, 11\n"
"s_add_u32 s84, s84, s49\ns_add_u32 s84, s84, 18874368\ns_add_u32 s85, s84, 32768\nv_mov_b32 v238, s46\nv_mov_b32 v239, s47\ns_waitcnt vmcnt(18)\n"
"v_mfma_f32_16x16x32_bf16 v[96:99], v[208:211], v[0:3], v[96:99]\nv_mfma_f32_16x16x32_bf16 v[100:103], v[210:213], v[0:3], v[100:103]\ns_waitcnt vmcnt(17)\nv_mfma_f32_16x16x32_bf16 v[104:107], v[208:211], v[4:7], v[104:107]\nv_mfma_f32_16x16x32_bf16 v[108:111], v[210:213], v[4:7], v[108:111]\ns_waitcnt vmcnt(16)\n"
"v_mfma_f32_16x16x32_bf16 v[112:115], v[208:211], v[8:11], v[112:115]\nv_mfma_f32_16x16x32_bf16 v[116:119], v[210:213], v[8:11], v[116:119]\ns_waitcnt vmcnt(15)\nv_mfma_f32_16x16x32_bf16 v[120:123], v[208:211], v[12:15], v[120:123]\nv_mfma_f32_16x16x32_bf16 v[124:127], v[210:213], v[12:15], v[124:127]\ns_waitcnt vmcnt(14)\n"
"v_mfma_f32_16x16x32_bf16 v[96:99], v[208:211], v[16:19], v[96:99]\nv_mfma_f32_16x16x32_bf16 v[100:103], v[210:213], v[16:19], v[100:103]\ns_waitcnt vmcnt(13)\nv_mfma_f32_16x16x32_bf16 v[104:107], v[208:211], v[20:23], v[104:107]\nv_mfma_f32_16x16x32_bf16 v[108:111], v[210:213], v[20:23], v[108:111]\ns_waitcnt vmcnt(12)\n"
"v_mfma_f32_16x16x32_bf16 v[112:115], v[208:211], v[24:27], v[112:115]\nv_mfma_f32_16x16x32_bf16 v[116:119], v[210:213], v[24:27], v[116:119]\ns_waitcnt vmcnt(11)\nv_mfma_f32_16x16x32_bf16 v[120:123], v[208:211], v[28:31], v[120:123]\nv_mfma_f32_16x16x32_bf16 v[124:127], v[210:213], v[28:31], v[124:127]\ns_waitcnt vmcnt(8)\n"
"v_max3_u32 v248, v67, v71, v75\ns_waitcnt vmcnt(7)\nv_max_u32 v248, v248, v79\nv_cmp_eq_u32 vcc, -1, v248\ns_nop 1\ns_cbranch_vccnz Lq537\n"
"Lq538:\nv_mfma_f32_16x16x32_bf16 v[96:99], v[208:211], v[64:67], v[96:99]\nv_mfma_f32_16x16x32_bf16 v[100:103], v[210:213], v[64:67], v[100:103]\nv_mfma_f32_16x16x32_bf16 v[104:107], v[208:211], v[68:71], v[104:107]\nv_mfma_f32_16x16x32_bf16 v[108:111], v[210:213], v[68:71], v[108:111]\nv_mfma_f32_16x16x32_bf16 v[112:115], v[208:211], v[72:75], v[112:115]\n"
"v_mfma_f32_16x16x32_bf16 v[116:119], v[210:213], v[72:75], v[116:119]\nv_mfma_f32_16x16x32_bf16 v[120:123], v[208:211], v[76:79], v[120:123]\nv_mfma_f32_16x16x32_bf16 v[124:127], v[210:213], v[76:79], v[124:127]\ns_nop 0\nv_pk_add_f32 v[96:97], v[96:97], v[128:129]\nv_pk_add_f32 v[98:99], v[98:99], v[130:131]\n"
"v_pk_add_f32 v[100:101], v[100:101], v[132:133]\nv_pk_add_f32 v[102:103], v[102:103], v[134:135]\nv_cvt_pk_bf16_f32 v192, v96, v97\nv_cvt_pk_bf16_f32 v193, v98, v99\nv_cvt_pk_bf16_f32 v194, v100, v101\nv_cvt_pk_bf16_f32 v195, v102, v103\n"
"buffer_store_dwordx4 v[192:195], v252, s[20:23], s84 offen offset:0 sc1\nG 11\nv_pk_add_f32 v[104:105], v[104:105], v[136:137]\nG 10\nv_pk_add_f32 v[106:107], v[106:107], v[138:139]\nG 9\n"
"v_pk_add_f32 v[108:109], v[108:109], v[140:141]\nG 8\nv_pk_add_f32 v[110:111], v[110:111], v[142:143]\nv_cvt_pk_bf16_f32 v196, v104, v105\nv_cvt_pk_bf16_f32 v197, v106, v107\nv_cvt_pk_bf16_f32 v198, v108, v109\n"
"v_cvt_pk_bf16_f32 v199, v110, v111\nbuffer_store_dwordx4 v[196:199], v252, s[20:23], s84 offen offset:1024 sc1\nG 7\nv_pk_add_f32 v[112:113], v[112:113], v[144:145]\nG 6\nv_pk_add_f32 v[114:115], v[114:115], v[146:147]\n"
"G 5\nv_pk_add_f32 v[116:117], v[116:117], v[148:149]\nG 4\nv_pk_add_f32 v[118:119], v[118:119], v[150:151]\nv_cvt_pk_bf16_f32 v200, v112, v113\nv_cvt_pk_bf16_f32 v201, v114, v115\n"
"v_cvt_pk_bf16_f32 v202, v116, v117\nv_cvt_pk_bf16_f32 v203, v118, v119\nbuffer_store_dwordx4 v[200:203], v252, s[20:23], s85 offen offset:0 sc1\nG 3\nv_pk_add_f32 v[120:121], v[120:121], v[152:153]\nG 2\n"
"v_pk_add_f32 v[122:123], v[122:123], v[154:155]\nG 1\nv_pk_add_f32 v[124:125], v[124:125], v[156:157]\nG 0\nv_pk_add_f32 v[126:127], v[126:127], v[158:159]\nv_cvt_pk_bf16_f32 v204, v120, v121\n"
"v_cvt_pk_bf16_f32 v205, v122, v123\nv_cvt_pk_bf16_f32 v206, v124, v125\nv_cvt_pk_bf16_f32 v207, v126, v127\nbuffer_store_dwordx4 v[204:207], v252, s[20:23], s85 offen offset:1024 sc1\ns_add_u32 s61, s79, 88358912\ns_lshl_b32 s62, s75, 3\n"
"s_add_u32 s61, s61, s62\ns_waitcnt vmcnt(0)\ns_mov_b64 exec, 1\nbuffer_atomic_swap_x2 v[238:239], off, s[20:23], s61 sc1\ns_mov_b64 exec, -1\ns_barrier\n"
"s_branch LqM\nLq110:\ns_lshl_b32 s61, s42, 14\ns_add_u32 s61, s61, 84148224\nbuffer_load_dwordx4 v[128:131], v230, s[20:23], s61 offen sc1\ns_lshl_b32 s61, s42, 14\n"
"s_add_u32 s61, s61, 84152320\nbuffer_load_dwordx4 v[132:135], v230, s[20:23], s61 offen sc1\ns_lshl_b32 s61, s42, 14\ns_add_u32 s61, s61, 84156416\nbuffer_load_dwordx4 v[136:139], v230, s[20:23], s61 offen sc1\ns_lshl_b32 s61, s42, 14\n"
"s_add_u32 s61, s61, 84160512\nbuffer_load_dwordx4 v[140:143], v230, s[20:23], s61 offen sc1\ns_waitcnt vmcnt(2)\nv_min3_u32 v232, v129, v131, v133\ns_waitcnt vmcnt(1)\nv_min3_u32 v232, v232, v135, v137\n"
"s_waitcnt vmcnt(0)\nv_min3_u32 v232, v232, v139, v141\nv_min3_u32 v232, v232, v143, v143\nv_max3_u32 v233, v129, v131, v133\nv_max3_u32 v233, v233, v135, v137\nv_max3_u32 v233, v233, v139, v141\n"
"v_max3_u32 v233, v233, v143, v143\nv_cmp_ne_u32 s[86:87], s46, v232\nv_cmp_ne_u32 s[88:89], s46, v233\ns_nop 0\ns_or_b64 s[86:87], s[86:87], s[88:89]\ns_cbranch_scc0 Lq111\n"
"s_sub_u32 s48, s48, 1\ns_cmp_gt_i32 s48, 0\ns_cbranch_scc1 Lq110\ns_branch Lq111\nLq120:\ns_lshl_b32 s61, s42, 14\n"
"s_add_u32 s61, s61, 84410368\nbuffer_load_dwordx4 v[144:147], v234, s[20:23], s61 offen sc1\ns_lshl_b32 s61, s42, 14\ns_add_u32 s61, s61, 84414464\nbuffer_load_dwordx4 v[148:151], v234, s[20:23], s61 offen sc1\ns_lshl_b32 s61, s42, 14\n"
"s_add_u32 s61, s61, 84418560\nbuffer_load_dwordx4 v[152:155], v234, s[20:23], s61 offen sc1\ns_lshl_b32 s61, s42, 14\ns_add_u32 s61, s61, 84422656\nbuffer_load_dwordx4 v[156:159], v234, s[20:23], s61 offen sc1\ns_waitcnt vmcnt(2)\n"
"v_min3_u32 v236, v145, v147, v149\ns_waitcnt vmcnt(1)\nv_min3_u32 v236, v236, v151, v153\ns_waitcnt vmcnt(0)\nv_min3_u32 v236, v236, v155, v157\nv_min3_u32 v236, v236, v159, v159\n"
"v_max3_u32 v237, v145, v147, v149\nv_max3_u32 v237, v237, v151, v153\nv_max3_u32 v237, v237, v155, v157\nv_max3_u32 v237, v237, v159, v159\nv_cmp_ne_u32 s[86:87], s46, v236\nv_cmp_ne_u32 s[88:89], s46, v237\n"
"s_nop 0\ns_or_b64 s[86:87], s[86:87], s[88:89]\ns_cbranch_scc0 Lq121\ns_sub_u32 s48, s48, 1\ns_cmp_gt_i32 s48, 0\ns_cbranch_scc1 Lq120\n"
"s_branch Lq121\nLq135:\ns_lshr_b32 s84, s49, 3\ns_add_u32 s84, s84, 81788928\nbuffer_load_dwordx4 v[128:131], v252, s[20:23], s84 offen offset:0 sc1\nbuffer_load_dwordx4 v[132:135], v252, s[20:23], s84 offen offset:1024 sc1\n"
"buffer_load_dwordx4 v[136:139], v252, s[20:23], s84 offen offset:2048 sc1\nbuffer_load_dwordx4 v[140:143], v252, s[20:23], s84 offen offset:3072 sc1\ns_lshr_b32 s85, s49, 3\ns_add_u32 s85, s85, 81793024\nbuffer_load_dwordx4 v[144:147], v252, s[20:23], s85 offen offset:0 sc1\nbuffer_load_dwordx4 v[148:151], v252, s[20:23], s85 offen offset:1024 sc1\n"
"buffer_load_dwordx4 v[152:155], v252, s[20:23], s85 offen offset:2048 sc1\nbuffer_load_dwordx4 v[156:159], v252, s[20:23], s85 offen offset:3072 sc1\ns_waitcnt vmcnt(6)\nv_min3_u32 v230, v129, v131, v133\ns_waitcnt vmcnt(5)\nv_min3_u32 v230, v230, v135, v137\n"
"s_waitcnt vmcnt(4)\nv_min3_u32 v230, v230, v139, v141\ns_waitcnt vmcnt(3)\nv_min3_u32 v230, v230, v143, v145\ns_waitcnt vmcnt(2)\nv_min3_u32 v230, v230, v147, v149\n"
"s_waitcnt vmcnt(1)\nv_min3_u32 v230, v230, v151, v153\ns_waitcnt vmcnt(0)\nv_min3_u32 v230, v230, v155, v157\nv_min3_u32 v230, v230, v159, v159\nv_max3_u32 v231, v129, v131, v133\n"
"v_max3_u32 v231, v231, v135, v137\nv_max3_u32 v231, v231, v139, v141\nv_max3_u32 v231, v231, v143, v145\nv_max3_u32 v231, v231, v147, v149\nv_max3_u32 v231, v231, v151, v153\nv_max3_u32 v231, v231, v155, v157\n"
"v_max3_u32 v231, v231, v159, v159\nv_cmp_ne_u32 s[86:87], s46, v230\nv_cmp_ne_u32 s[88:89], s46, v231\ns_nop 0\ns_or_b64 s[86:87], s[86:87], s[88:89]\ns_cbranch_scc0 Lq136\n"
"s_sub_u32 s48, s48, 1\ns_cmp_gt_i32 s48, 0\ns_cbranch_scc1 Lq135\ns_branch Lq136\nLq150:\ns_lshl_b32 s61, s42, 14\n"
"s_add_u32 s61, s61, 83886080\nbuffer_load_dwordx4 v[128:131], v234, s[20:23], s61 offen sc1\ns_lshl_b32 s61, s42, 14\ns_add_u32 s61, s61, 83890176\nbuffer_load_dwordx4 v[132:135], v234, s[20:23], s61 offen sc1\ns_lshl_b32 s61, s42, 14\n"
"s_add_u32 s61, s61, 83894272\nbuffer_load_dwordx4 v[136:139], v234, s[20:23], s61 offen sc1\ns_lshl_b32 s61, s42, 14\ns_add_u32 s61, s61, 83898368\nbuffer_load_dwordx4 v[140:143], v234, s[20:23], s61 offen sc1\ns_waitcnt vmcnt(2)\n"
"v_min3_u32 v236, v129, v131, v133\ns_waitcnt vmcnt(1)\nv_min3_u32 v236, v236, v135, v137\ns_waitcnt vmcnt(0)\nv_min3_u32 v236, v236, v139, v141\nv_min3_u32 v236, v236, v143, v143\n"
"v_max3_u32 v237, v129, v131, v133\nv_max3_u32 v237, v237, v135, v137\nv_max3_u32 v237, v237, v139, v141\nv_max3_u32 v237, v237, v143, v143\nv_cmp_ne_u32 s[86:87], s46, v236\nv_cmp_ne_u32 s[88:89], s46, v237\n"
"s_nop 0\ns_or_b64 s[86:87], s[86:87], s[88:89]\ns_cbranch_scc0 Lq151\ns_sub_u32 s48, s48, 1\ns_cmp_gt_i32 s48, 0\ns_cbranch_scc1 Lq150\n"
"s_branch Lq151\nLq169:\ns_mov_b64 exec, s[86:87]\nbuffer_load_dwordx4 v[230:233], v252, s[20:23], s65 offen offset:0 sc1\nbuffer_load_dwordx4 v[234:237], v252, s[20:23], s65 offen offset:1024 sc1\nbuffer_load_dwordx2 v[64:65], v227, s[20:23], s65 offen offset:2048 sc1\n"
"buffer_load_dwordx2 v[70:71], v227, s[20:23], s65 offen offset:2560 sc1\nbuffer_load_dwordx2 v[76:77], v227, s[20:23], s65 offen offset:3072 sc1\nbuffer_load_dwordx2 v[82:83], v227, s[20:23], s65 offen offset:3584 sc1\ns_mov_b64 exec, -1\ns_waitcnt vmcnt(0)\nv_max3_u32 v248, v233, v237, v65\n"
"v_max3_u32 v248, v248, v71, v77\nv_max_u32 v248, v248, v83\nv_cmp_eq_u32 vcc, -1, v248\ns_nop 1\ns_cbranch_vccz Lq170\ns_sub_u32 s48, s48, 1\n"
"s_cmp_gt_i32 s48, 0\ns_cbranch_scc1 Lq169\ns_branch Lq170\nLq196:\nv_mov_b32 v248, 0\nv_mov_b32 v249, 0\n"
"s_add_u32 s61, s80, 88080384\ns_mov_b64 exec, 0xffff\nbuffer_atomic_or_x2 v[248:249], v250, s[20:23], s61 offen sc0 sc1\ns_mov_b64 exec, -1\ns_waitcnt vmcnt(0)\nv_cmp_eq_u64 s[86:87], s[46:47], v[248:249]\n"
"s_nop 1\ns_and_b32 s62, s86, 0xffff\ns_cmp_eq_u32 s62, 0xffff\ns_cbranch_scc1 Lq197\ns_sleep 1\ns_sub_u32 s48, s48, 1\n"
"s_cmp_gt_i32 s48, 0\ns_cbranch_scc1 Lq196\ns_branch Lq197\nLq255:\nv_mov_b32 v248, 0\nv_mov_b32 v249, 0\n"
"s_add_u32 s61, s80, 88219648\ns_mov_b64 exec, 0xffff\nbuffer_atomic_or_x2 v[248:249], v250, s[20:23], s61 offen sc0 sc1\ns_mov_b64 exec, -1\ns_waitcnt vmcnt(0)\nv_cmp_eq_u64 s[86:87], s[46:47], v[248:249]\n"
"s_nop 1\ns_and_b32 s62, s86, 0xffff\ns_cmp_eq_u32 s62, 0xffff\ns_cbranch_scc1 Lq256\ns_sleep 1\ns_sub_u32 s48, s48, 1\n"
"s_cmp_gt_i32 s48, 0\ns_cbranch_scc1 Lq255\ns_branch Lq256\nLq312:\ns_lshr_b32 s84, s49, 3\ns_add_u32 s84, s84, 82313216\n"
"buffer_load_dwordx4 v[0:3], v252, s[20:23], s84 offen offset:0 sc1\nbuffer_load_dwordx4 v[4:7], v252, s[20:23], s84 offen offset:1024 sc1\nbuffer_load_dwordx4 v[8:11], v252, s[20:23], s84 offen offset:2048 sc1\nbuffer_load_dwordx4 v[12:15], v252, s[20:23], s84 offen offset:3072 sc1\ns_lshr_b32 s85, s49, 3\ns_add_u32 s85, s85, 82317312\n"
"buffer_load_dwordx4 v[16:19], v252, s[20:23], s85 offen offset:0 sc1\nbuffer_load_dwordx4 v[20:23], v252, s[20:23], s85 offen offset:1024 sc1\nbuffer_load_dwordx4 v[24:27], v252, s[20:23], s85 offen offset:2048 sc1\nbuffer_load_dwordx4 v[28:31], v252, s[20:23], s85 offen offset:3072 sc1\ns_waitcnt vmcnt(6)\nv_min3_u32 v230, v1, v3, v5\n"
"s_waitcnt vmcnt(5)\nv_min3_u32 v230, v230, v7, v9\ns_waitcnt vmcnt(4)\nv_min3_u32 v230, v230, v11, v13\ns_waitcnt vmcnt(3)\nv_min3_u32 v230, v230, v15, v17\n"
"s_waitcnt vmcnt(2)\nv_min3_u32 v230, v230, v19, v21\ns_waitcnt vmcnt(1)\nv_min3_u32 v230, v230, v23, v25\ns_waitcnt vmcnt(0)\nv_min3_u32 v230, v230, v27, v29\n"
"v_min3_u32 v230, v230, v31, v31\nv_max3_u32 v231, v1, v3, v5\nv_max3_u32 v231, v231, v7, v9\nv_max3_u32 v231, v231, v11, v13\nv_max3_u32 v231, v231, v15, v17\nv_max3_u32 v231, v231, v19, v21\n"
"v_max3_u32 v231, v231, v23, v25\nv_max3_u32 v231, v231, v27, v29\nv_max3_u32 v231, v231, v31, v31\nv_cmp_ne_u32 s[86:87], s46, v230\nv_cmp_ne_u32 s[88:89], s46, v231\ns_nop 0\n"
"s_or_b64 s[86:87], s[86:87], s[88:89]\ns_cbranch_scc0 Lq313\ns_sub_u32 s48, s48, 1\ns_cmp_gt_i32 s48, 0\ns_cbranch_scc1 Lq312\ns_branch Lq313\n"
"Lq314:\ns_lshl_b32 s61, s42, 14\ns_add_u32 s61, s61, 84672512\nbuffer_load_dwordx4 v[96:99], v238, s[20:23], s61 offen sc1\ns_lshl_b32 s61, s42, 14\ns_add_u32 s61, s61, 84676608\n"
"buffer_load_dwordx4 v[100:103], v238, s[20:23], s61 offen sc1\ns_lshl_b32 s61, s42, 14\ns_add_u32 s61, s61, 84680704\nbuffer_load_dwordx4 v[104:107], v238, s[20:23], s61 offen sc1\ns_lshl_b32 s61, s42, 14\ns_add_u32 s61, s61, 84684800\n"
"buffer_load_dwordx4 v[108:111], v238, s[20:23], s61 offen sc1\ns_waitcnt vmcnt(2)\nv_min3_u32 v240, v97, v99, v101\ns_waitcnt vmcnt(1)\nv_min3_u32 v240, v240, v103, v105\ns_waitcnt vmcnt(0)\n"
"v_min3_u32 v240, v240, v107, v109\nv_min3_u32 v240, v240, v111, v111\nv_max3_u32 v241, v97, v99, v101\nv_max3_u32 v241, v241, v103, v105\nv_max3_u32 v241, v241, v107, v109\nv_max3_u32 v241, v241, v111, v111\n"
"v_cmp_ne_u32 s[86:87], s46, v240\nv_cmp_ne_u32 s[88:89], s46, v241\ns_nop 0\ns_or_b64 s[86:87], s[86:87], s[88:89]\ns_cbranch_scc0 Lq315\ns_sub_u32 s48, s48, 1\n"
"s_cmp_gt_i32 s48, 0\ns_cbranch_scc1 Lq314\ns_branch Lq315\nLq326:\ns_lshl_b32 s61, s42, 14\ns_add_u32 s61, s61, 84934656\n"
"buffer_load_dwordx4 v[112:115], v242, s[20:23], s61 offen sc1\ns_lshl_b32 s61, s42, 14\ns_add_u32 s61, s61, 84938752\nbuffer_load_dwordx4 v[116:119], v242, s[20:23], s61 offen sc1\ns_lshl_b32 s61, s42, 14\ns_add_u32 s61, s61, 84942848\n"
"buffer_load_dwordx4 v[120:123], v242, s[20:23], s61 offen sc1\ns_lshl_b32 s61, s42, 14\ns_add_u32 s61, s61, 84946944\nbuffer_load_dwordx4 v[124:127], v242, s[20:23], s61 offen sc1\ns_waitcnt vmcnt(2)\nv_min3_u32 v244, v113, v115, v117\n"
"s_waitcnt vmcnt(1)\nv_min3_u32 v244, v244, v119, v121\ns_waitcnt vmcnt(0)\nv_min3_u32 v244, v244, v123, v125\nv_min3_u32 v244, v244, v127, v127\nv_max3_u32 v245, v113, v115, v117\n"
"v_max3_u32 v245, v245, v119, v121\nv_max3_u32 v245, v245, v123, v125\nv_max3_u32 v245, v245, v127, v127\nv_cmp_ne_u32 s[86:87], s46, v244\nv_cmp_ne_u32 s[88:89], s46, v245\ns_nop 0\n"
"s_or_b64 s[86:87], s[86:87], s[88:89]\ns_cbranch_scc0 Lq327\ns_sub_u32 s48, s48, 1\ns_cmp_gt_i32 s48, 0\ns_cbranch_scc1 Lq326\ns_branch Lq327\n"
"Lq338:\ns_lshl_b32 s61, s42, 14\ns_add_u32 s61, s61, 85196800\nbuffer_load_dwordx4 v[128:131], v238, s[20:23], s61 offen sc1\ns_lshl_b32 s61, s42, 14\ns_add_u32 s61, s61, 85200896\n"
"buffer_load_dwordx4 v[132:135], v238, s[20:23], s61 offen sc1\ns_lshl_b32 s61, s42, 14\ns_add_u32 s61, s61, 85204992\nbuffer_load_dwordx4 v[136:139], v238, s[20:23], s61 offen sc1\ns_lshl_b32 s61, s42, 14\ns_add_u32 s61, s61, 85209088\n"
"buffer_load_dwordx4 v[140:143], v238, s[20:23], s61 offen sc1\ns_waitcnt vmcnt(2)\nv_min3_u32 v240, v129, v131, v133\ns_waitcnt vmcnt(1)\nv_min3_u32 v240, v240, v135, v137\ns_waitcnt vmcnt(0)\n"
"v_min3_u32 v240, v240, v139, v141\nv_min3_u32 v240, v240, v143, v143\nv_max3_u32 v241, v129, v131, v133\nv_max3_u32 v241, v241, v135, v137\nv_max3_u32 v241, v241, v139, v141\nv_max3_u32 v241, v241, v143, v143\n"
"v_cmp_ne_u32 s[86:87], s46, v240\nv_cmp_ne_u32 s[88:89], s46, v241\ns_nop 0\ns_or_b64 s[86:87], s[86:87], s[88:89]\ns_cbranch_scc0 Lq339\ns_sub_u32 s48, s48, 1\n"
"s_cmp_gt_i32 s48, 0\ns_cbranch_scc1 Lq338\ns_branch Lq339\nLq350:\ns_lshl_b32 s61, s42, 14\ns_add_u32 s61, s61, 85458944\n"
"buffer_load_dwordx4 v[144:147], v242, s[20:23], s61 offen sc1\ns_lshl_b32 s61, s42, 14\ns_add_u32 s61, s61, 85463040\nbuffer_load_dwordx4 v[148:151], v242, s[20:23], s61 offen sc1\ns_lshl_b32 s61, s42, 14\ns_add_u32 s61, s61, 85467136\n"
"buffer_load_dwordx4 v[152:155], v242, s[20:23], s61 offen sc1\ns_lshl_b32 s61, s42, 14\ns_add_u32 s61, s61, 85471232\nbuffer_load_dwordx4 v[156:159], v242, s[20:23], s61 offen sc1\ns_waitcnt vmcnt(2)\nv_min3_u32 v244, v145, v147, v149\n"
"s_waitcnt vmcnt(1)\nv_min3_u32 v244, v244, v151, v153\ns_waitcnt vmcnt(0)\nv_min3_u32 v244, v244, v155, v157\nv_min3_u32 v244, v244, v159, v159\nv_max3_u32 v245, v145, v147, v149\n"
"v_max3_u32 v245, v245, v151, v153\nv_max3_u32 v245, v245, v155, v157\nv_max3_u32 v245, v245, v159, v159\nv_cmp_ne_u32 s[86:87], s46, v244\nv_cmp_ne_u32 s[88:89], s46, v245\ns_nop 0\n"
"s_or_b64 s[86:87], s[86:87], s[88:89]\ns_cbranch_scc0 Lq351\ns_sub_u32 s48, s48, 1\ns_cmp_gt_i32 s48, 0\ns_cbranch_scc1 Lq350\ns_branch Lq351\n"
"Lq382:\nv_mov_b32 v248, 0\nv_mov_b32 v249, 0\ns_add_u32 s61, s82, 385024\ns_mov_b64 exec, 0xf\nbuffer_atomic_or_x2 v[248:249], v250, s[20:23], s61 offen sc0 sc1\n"
"s_mov_b64 exec, -1\ns_waitcnt vmcnt(0)\nv_cmp_eq_u64 s[86:87], s[46:47], v[248:249]\ns_nop 1\ns_and_b32 s62, s86, 0xf\ns_cmp_eq_u32 s62, 0xf\n"
"s_cbranch_scc1 Lq383\ns_sleep 1\ns_sub_u32 s48, s48, 1\ns_cmp_gt_i32 s48, 0\ns_cbranch_scc1 Lq382\ns_branch Lq383\n"
"Lq417:\nv_mov_b32 v248, 0\nv_mov_b32 v249, 0\ns_add_u32 s61, s82, 385032\ns_mov_b64 exec, 0xf\nbuffer_atomic_or_x2 v[248:249], v250, s[20:23], s61 offen sc0 sc1\n"
"s_mov_b64 exec, -1\ns_waitcnt vmcnt(0)\nv_cmp_eq_u64 s[86:87], s[46:47], v[248:249]\ns_nop 1\ns_and_b32 s62, s86, 0xf\ns_cmp_eq_u32 s62, 0xf\n"
"s_cbranch_scc1 Lq418\ns_sleep 1\ns_sub_u32 s48, s48, 1\ns_cmp_gt_i32 s48, 0\ns_cbranch_scc1 Lq417\ns_branch Lq418\n"
"Lq452:\nv_mov_b32 v248, 0\nv_mov_b32 v249, 0\ns_add_u32 s61, s82, 385048\ns_mov_b64 exec, 0xf\nbuffer_atomic_or_x2 v[248:249], v250, s[20:23], s61 offen sc0 sc1\n"
"s_mov_b64 exec, -1\ns_waitcnt vmcnt(0)\nv_cmp_eq_u64 s[86:87], s[46:47], v[248:249]\ns_nop 1\ns_and_b32 s62, s86, 0xf\ns_cmp_eq_u32 s62, 0xf\n"
"s_cbranch_scc1 Lq453\ns_sleep 1\ns_sub_u32 s48, s48, 1\ns_cmp_gt_i32 s48, 0\ns_cbranch_scc1 Lq452\ns_branch Lq453\n"
"Lq471:\nv_mov_b32 v248, 0\nv_mov_b32 v249, 0\ns_add_u32 s61, s82, 385040\ns_mov_b64 exec, 0xf\nbuffer_atomic_or_x2 v[248:249], v250, s[20:23], s61 offen sc0 sc1\n"
"s_mov_b64 exec, -1\ns_waitcnt vmcnt(0)\nv_cmp_eq_u64 s[86:87], s[46:47], v[248:249]\ns_nop 1\ns_and_b32 s62, s86, 0xf\ns_cmp_eq_u32 s62, 0xf\n"
"s_cbranch_scc1 Lq472\ns_sleep 1\ns_sub_u32 s48, s48, 1\ns_cmp_gt_i32 s48, 0\ns_cbranch_scc1 Lq471\ns_branch Lq472\n"
"Lq514:\nv_mov_b32 v248, 0\nv_mov_b32 v249, 0\ns_add_u32 s61, s74, 548864\ns_mov_b64 exec, 3\nbuffer_atomic_or_x2 v[248:249], v250, s[20:23], s61 offen sc0 sc1\n"
"s_mov_b64 exec, -1\ns_waitcnt vmcnt(0)\nv_cmp_eq_u64 s[86:87], s[46:47], v[248:249]\ns_nop 1\ns_and_b32 s62, s86, 3\ns_cmp_eq_u32 s62, 3\n"
"s_cbranch_scc1 Lq515\ns_sleep 1\ns_sub_u32 s48, s48, 1\ns_cmp_gt_i32 s48, 0\ns_cbranch_scc1 Lq514\ns_branch Lq515\n"
"Lq537:\ns_mul_i32 s66, s43, 49152\ns_add_u32 s66, s66, s72\ns_add_u32 s66, s66, 52461568\nbuffer_load_dwordx4 v[64:67], v252, s[20:23], s66 offen offset:0 sc1\nbuffer_load_dwordx4 v[68:71], v252, s[20:23], s66 offen offset:1024 sc1\n"
"buffer_load_dwordx4 v[72:75], v252, s[20:23], s66 offen offset:2048 sc1\nbuffer_load_dwordx4 v[76:79], v252, s[20:23], s66 offen offset:3072 sc1\ns_waitcnt vmcnt(0)\nv_max3_u32 v248, v67, v71, v75\nv_max_u32 v248, v248, v79\nv_cmp_eq_u32 vcc, -1, v248\n"
"s_nop 1\ns_cbranch_vccz Lq538\ns_sub_u32 s48, s48, 1\ns_cmp_gt_i32 s48, 0\ns_cbranch_scc1 Lq537\ns_branch Lq538\n"
".p2align 6\nLqM:\nLqq1:\nv_bfe_u32 v250, v252, 7, 1\nv_mul_u32_u24 v250, 2176, v250\nv_bfe_u32 v251, v252, 4, 3\n"
"v_lshl_add_u32 v250, v251, 3, v250\ns_lshl_b32 s60, s90, 6\nv_add_u32 v250, s60, v250\ns_add_u32 m0, s50, 98304\ns_add_u32 s63, s52, 593920\nbuffer_load_dwordx4 v220, s[4:7], s63 offen lds\n"
"buffer_load_dwordx4 v221, s[4:7], s63 offen offset:1024 lds\ns_add_u32 m0, s50, 106496\ns_add_u32 s63, s52, 788480\nbuffer_load_dwordx4 v220, s[4:7], s63 offen lds\nbuffer_load_dwordx4 v221, s[4:7], s63 offen offset:1024 lds\ns_add_u32 m0, s50, 114688\n"
"s_add_u32 s63, s52, 985088\nbuffer_load_dwordx4 v220, s[4:7], s63 offen lds\nbuffer_load_dwordx4 v221, s[4:7], s63 offen offset:1024 lds\ns_add_u32 m0, s50, 0\ns_add_u32 s63, s52, 790528\nbuffer_load_dwordx4 v220, s[4:7], s63 offen lds\n"
"buffer_load_dwordx4 v221, s[4:7], s63 offen offset:1024 lds\nv_mov_b32 v248, 0\nv_mov_b32 v249, 0\ns_add_u32 s61, s80, 88358912\ns_mov_b64 exec, 0xffff\nbuffer_atomic_or_x2 v[248:249], v250, s[20:23], s61 offen sc0 sc1\n"
"s_mov_b64 exec, -1\nv_mov_b32 v230, 0\nv_mov_b32 v231, 0\nv_mov_b32 v232, 0\nv_mov_b32 v233, 0\nv_dot2c_f32_bf16 v230, s76, v192\n"
"v_dot2c_f32_bf16 v231, v192, v192\nv_dot2c_f32_bf16 v232, s76, v200\nv_dot2c_f32_bf16 v233, v200, v200\nv_dot2c_f32_bf16 v230, s76, v193\nv_dot2c_f32_bf16 v231, v193, v193\nv_dot2c_f32_bf16 v232, s76, v201\n"
"v_dot2c_f32_bf16 v233, v201, v201\nv_dot2c_f32_bf16 v230, s76, v194\nv_dot2c_f32_bf16 v231, v194, v194\nv_dot2c_f32_bf16 v232, s76, v202\nv_dot2c_f32_bf16 v233, v202, v202\nv_dot2c_f32_bf16 v230, s76, v195\n"
"v_dot2c_f32_bf16 v231, v195, v195\nv_dot2c_f32_bf16 v232, s76, v203\nv_dot2c_f32_bf16 v233, v203, v203\nv_dot2c_f32_bf16 v230, s76, v196\nv_dot2c_f32_bf16 v231, v196, v196\nv_dot2c_f32_bf16 v232, s76, v204\n"
"v_dot2c_f32_bf16 v233, v204, v204\nv_dot2c_f32_bf16 v230, s76, v197\nv_dot2c_f32_bf16 v231, v197, v197\nv_dot2c_f32_bf16 v232, s76, v205\nv_dot2c_f32_bf16 v233, v205, v205\nv_dot2c_f32_bf16 v230, s76, v198\n"
"v_dot2c_f32_bf16 v231, v198, v198\nv_dot2c_f32_bf16 v232, s76, v206\nv_dot2c_f32_bf16 v233, v206, v206\nv_dot2c_f32_bf16 v230, s76, v199\nv_dot2c_f32_bf16 v231, v199, v199\nv_dot2c_f32_bf16 v232, s76, v207\n"
"v_dot2c_f32_bf16 v233, v207, v207\nv_lshrrev_b32 v234, 1, v252\nv_permlane32_swap_b32 v230, v231\ns_nop 0\nv_permlane32_swap_b32 v232, v233\nv_add_f32 v230, v230, v231\n"
"v_add_f32 v232, v232, v233\nv_mov_b32 v231, s46\ns_nop 0\nv_permlane16_swap_b32 v230, v232\nv_add_f32 v230, v230, v232\ns_lshr_b32 s61, s49, 3\n"
"s_lshl2_add_u32 s61, s53, s61\ns_add_u32 s61, s61, 82837504\nbuffer_store_dwordx2 v[230:231], v234, s[20:23], s61 offen sc1\ns_waitcnt vmcnt(1)\nv_cmp_eq_u64 s[86:87], s[46:47], v[248:249]\ns_nop 0\n"
"s_and_b32 s62, s86, 0xffff\ns_cmp_eq_u32 s62, 0xffff\ns_cbranch_scc0 Lq588\nLq589:\ns_add_u32 s64, s70, 18874368\nbuffer_load_dwordx4 a[0:3], v252, s[20:23], s64 offen offset:0 sc1\n"
"s_add_u32 s65, s70, 18907136\nbuffer_load_dwordx4 a[4:7], v252, s[20:23], s65 offen offset:0 sc1\ns_add_u32 s66, s71, 18874368\nbuffer_load_dwordx4 a[8:11], v252, s[20:23], s66 offen offset:0 sc1\ns_add_u32 s67, s71, 18907136\nbuffer_load_dwordx4 a[12:15], v252, s[20:23], s67 offen offset:0 sc1\n"
"buffer_load_dwordx4 a[16:19], v252, s[20:23], s64 offen offset:1024 sc1\nbuffer_load_dwordx4 a[20:23], v252, s[20:23], s65 offen offset:1024 sc1\nbuffer_load_dwordx4 a[24:27], v252, s[20:23], s66 offen offset:1024 sc1\nbuffer_load_dwordx4 a[28:31], v252, s[20:23], s67 offen offset:1024 sc1\ns_barrier\nR 160,184,40960\n"
"R 162,184,45056\nR 164,184,49152\nR 166,184,53248\nR 168,185,40960\nR 170,185,45056\nR 172,185,49152\n"
"R 174,185,53248\nR 176,186,40960\nR 178,186,45056\nR 180,186,49152\nR 182,186,53248\nbuffer_load_dwordx4 a[32:35], v252, s[20:23], s64 offen offset:2048 sc1\n"
"buffer_load_dwordx4 a[36:39], v252, s[20:23], s65 offen offset:2048 sc1\nbuffer_load_dwordx4 a[40:43], v252, s[20:23], s66 offen offset:2048 sc1\nbuffer_load_dwordx4 a[44:47], v252, s[20:23], s67 offen offset:2048 sc1\nbuffer_load_dwordx4 a[48:51], v252, s[20:23], s64 offen offset:3072 sc1\nbuffer_load_dwordx4 a[52:55], v252, s[20:23], s65 offen offset:3072 sc1\nbuffer_load_dwordx4 a[56:59], v252, s[20:23], s66 offen offset:3072 sc1\n"
"buffer_load_dwordx4 a[60:63], v252, s[20:23], s67 offen offset:3072 sc1\ns_waitcnt vmcnt(15) lgkmcnt(10)\nv_mfma_f32_16x16x32_bf16 v[0:3], v[160:163], a[0:3], 0\ns_add_u32 s64, s70, 18878464\nbuffer_load_dwordx4 a[64:67], v252, s[20:23], s64 offen offset:0 sc1\ns_waitcnt vmcnt(12) lgkmcnt(8)\n"
"M 0,164,16\nv_mfma_f32_16x16x32_bf16 v[16:19], v[160:163], a[4:7], 0\ns_waitcnt vmcnt(11)\nM 16,164,20\nv_mfma_f32_16x16x32_bf16 v[32:35], v[160:163], a[8:11], 0\ns_waitcnt vmcnt(10)\n"
"M 32,164,24\ns_add_u32 s65, s70, 18911232\nbuffer_load_dwordx4 a[68:71], v252, s[20:23], s65 offen offset:0 sc1\nv_mfma_f32_16x16x32_bf16 v[48:51], v[160:163], a[12:15], 0\ns_waitcnt vmcnt(10)\nM 48,164,28\n"
"G 4\nv_mfma_f32_16x16x32_bf16 v[4:7], v[168:171], a[0:3], 0\nR 160,187,40960\nM 4,172,16\nv_mfma_f32_16x16x32_bf16 v[20:23], v[168:171], a[4:7], 0\nR 162,187,45056\n"
"M 20,172,20\ns_add_u32 s66, s71, 18878464\nbuffer_load_dwordx4 a[72:75], v252, s[20:23], s66 offen offset:0 sc1\nv_mfma_f32_16x16x32_bf16 v[36:39], v[168:171], a[8:11], 0\nR 164,187,49152\nM 36,172,24\n"
"v_mfma_f32_16x16x32_bf16 v[52:55], v[168:171], a[12:15], 0\nR 166,187,53248\nM 52,172,28\nG 4\nv_mfma_f32_16x16x32_bf16 v[8:11], v[176:179], a[0:3], 0\nR 168,184,57344\n"
"M 8,180,16\ns_add_u32 s67, s71, 18911232\nbuffer_load_dwordx4 a[76:79], v252, s[20:23], s67 offen offset:0 sc1\nv_mfma_f32_16x16x32_bf16 v[24:27], v[176:179], a[4:7], 0\nR 170,184,61440\nM 24,180,20\n"
"v_mfma_f32_16x16x32_bf16 v[40:43], v[176:179], a[8:11], 0\nR 172,188,0\nM 40,180,24\nv_mfma_f32_16x16x32_bf16 v[56:59], v[176:179], a[12:15], 0\nR 174,188,4096\nM 56,180,28\n"
"G 4\nv_mfma_f32_16x16x32_bf16 v[12:15], v[160:163], a[0:3], 0\nR 176,185,57344\nM 12,164,16\nbuffer_load_dwordx4 a[80:83], v252, s[20:23], s64 offen offset:1024 sc1\nv_mfma_f32_16x16x32_bf16 v[28:31], v[160:163], a[4:7], 0\n"
"R 178,185,61440\nM 28,164,20\nbuffer_load_dwordx4 a[84:87], v252, s[20:23], s65 offen offset:1024 sc1\nv_mfma_f32_16x16x32_bf16 v[44:47], v[160:163], a[8:11], 0\nR 180,189,0\nM 44,164,24\n"
"v_mfma_f32_16x16x32_bf16 v[60:63], v[160:163], a[12:15], 0\nR 182,189,4096\nM 60,164,28\nG 4\nv_mfma_f32_16x16x32_bf16 v[64:67], a[0:3], v[168:171], 0\nR 160,186,57344\n"
"N 64,16,172\nbuffer_load_dwordx4 a[88:91], v252, s[20:23], s66 offen offset:1024 sc1\nv_mfma_f32_16x16x32_bf16 v[68:71], a[4:7], v[168:171], 0\nR 162,186,61440\nN 68,20,172\nv_mfma_f32_16x16x32_bf16 v[96:99], a[8:11], v[168:171], 0\n"
"R 164,190,0\nN 96,24,172\nv_mfma_f32_16x16x32_bf16 v[100:103], a[12:15], v[168:171], 0\nR 166,190,4096\nN 100,28,172\nG 4\n"
"v_mfma_f32_16x16x32_bf16 v[72:75], a[0:3], v[176:179], 0\nR 168,187,57344\nN 72,16,180\nbuffer_load_dwordx4 a[92:95], v252, s[20:23], s67 offen offset:1024 sc1\nv_mfma_f32_16x16x32_bf16 v[76:79], a[4:7], v[176:179], 0\nR 170,187,61440\n"
"N 76,20,180\nv_mfma_f32_16x16x32_bf16 v[104:107], a[8:11], v[176:179], 0\nR 172,191,0\nN 104,24,180\nbuffer_load_dwordx4 a[96:99], v252, s[20:23], s64 offen offset:2048 sc1\nv_mfma_f32_16x16x32_bf16 v[108:111], a[12:15], v[176:179], 0\n"
"R 174,191,4096\nN 108,28,180\nG 4\nv_mfma_f32_16x16x32_bf16 v[80:83], a[0:3], v[160:163], 0\nR 176,188,8192\nN 80,16,164\n"
"buffer_load_dwordx4 a[100:103], v252, s[20:23], s65 offen offset:2048 sc1\nv_mfma_f32_16x16x32_bf16 v[84:87], a[4:7], v[160:163], 0\nR 178,188,12288\nN 84,20,164\nv_mfma_f32_16x16x32_bf16 v[112:115], a[8:11], v[160:163], 0\nR 180,188,16384\n"
"N 112,24,164\ns_add_u32 m0, s50, 8192\nv_mfma_f32_16x16x32_bf16 v[116:119], a[12:15], v[160:163], 0\nR 182,188,20480\ns_add_u32 s63, s52, 987136\nN 116,28,164\n"
"G 4\nv_mfma_f32_16x16x32_bf16 v[88:91], a[0:3], v[168:171], 0\nR 160,189,8192\nN 88,16,172\nbuffer_load_dwordx4 a[104:107], v252, s[20:23], s66 offen offset:2048 sc1\nv_mfma_f32_16x16x32_bf16 v[92:95], a[4:7], v[168:171], 0\n"
"R 162,189,12288\nN 92,20,172\nbuffer_load_dwordx4 v220, s[4:7], s63 offen lds\nv_mfma_f32_16x16x32_bf16 v[120:123], a[8:11], v[168:171], 0\nR 164,189,16384\nN 120,24,172\n"
"buffer_load_dwordx4 a[108:111], v252, s[20:23], s67 offen offset:2048 sc1\nv_mfma_f32_16x16x32_bf16 v[124:127], a[12:15], v[168:171], 0\nR 166,189,20480\nN 124,28,172\ns_barrier\nG 4\n"
"s_waitcnt vmcnt(20)\nM 0,176,32\nR 168,190,8192\nbuffer_load_dwordx4 v221, s[4:7], s63 offen offset:1024 lds\ns_waitcnt vmcnt(17)\nM 0,180,48\n"
"s_add_u32 m0, s50, 16384\ns_add_u32 s63, s52, 1181696\nbuffer_load_dwordx4 v220, s[4:7], s63 offen lds\nM 16,176,36\nR 170,190,12288\ns_waitcnt vmcnt(17)\n"
"M 16,180,52\nbuffer_load_dwordx4 a[112:115], v252, s[20:23], s64 offen offset:3072 sc1\nM 32,176,40\nR 172,190,16384\ns_waitcnt vmcnt(17)\nM 32,180,56\n"
"buffer_load_dwordx4 v221, s[4:7], s63 offen offset:1024 lds\nM 48,176,44\nR 174,190,20480\ns_add_u32 m0, s50, 24576\ns_waitcnt vmcnt(17)\nM 48,180,60\n"
"G 4\nM 4,160,32\nR 176,191,8192\ns_add_u32 s63, s52, 1378304\nM 4,164,48\nbuffer_load_dwordx4 a[116:119], v252, s[20:23], s65 offen offset:3072 sc1\n"
"M 20,160,36\nR 178,191,12288\nM 20,164,52\nbuffer_load_dwordx4 v220, s[4:7], s63 offen lds\nM 36,160,40\nR 180,191,16384\n"
"M 36,164,56\nbuffer_load_dwordx4 a[120:123], v252, s[20:23], s66 offen offset:3072 sc1\nM 52,160,44\nR 182,191,20480\nM 52,164,60\nG 4\n"
"M 8,168,32\nR 160,188,24576\nbuffer_load_dwordx4 v221, s[4:7], s63 offen offset:1024 lds\nM 8,172,48\nM 24,168,36\nR 162,188,28672\n"
"M 24,172,52\nbuffer_load_dwordx4 a[124:127], v252, s[20:23], s67 offen offset:3072 sc1\nM 40,168,40\nR 164,188,32768\nM 40,172,56\nM 56,168,44\n"
"R 166,188,36864\nM 56,172,60\nG 4\nM 12,176,32\nR 168,189,24576\nM 12,180,48\n"
"s_add_u32 s64, s70, 18882560\nbuffer_load_dwordx4 a[128:131], v252, s[20:23], s64 offen offset:0 sc1\nM 28,176,36\nR 170,189,28672\nM 28,180,52\nM 44,176,40\n"
"R 172,189,32768\nM 44,180,56\ns_add_u32 s65, s70, 18915328\nbuffer_load_dwordx4 a[132:135], v252, s[20:23], s65 offen offset:0 sc1\nM 60,176,44\nR 174,189,36864\n"
"M 60,180,60\nG 4\nN 64,32,160\nR 176,190,24576\nN 64,48,164\nN 68,36,160\n"
"R 178,190,28672\nN 68,52,164\ns_add_u32 s66, s71, 18882560\nbuffer_load_dwordx4 a[136:139], v252, s[20:23], s66 offen offset:0 sc1\nN 96,40,160\nR 180,190,32768\n"
"N 96,56,164\nN 100,44,160\nR 182,190,36864\nN 100,60,164\nG 4\nN 72,32,168\n"
"R 160,191,24576\ns_add_u32 s63, s52, 1183744\nN 72,48,172\ns_add_u32 s67, s71, 18915328\nbuffer_load_dwordx4 a[140:143], v252, s[20:23], s67 offen offset:0 sc1\nN 76,36,168\n"
"R 162,191,28672\nN 76,52,172\ns_add_u32 m0, s50, 32768\nN 104,40,168\nR 164,191,32768\nN 104,56,172\n"
"buffer_load_dwordx4 v220, s[4:7], s63 offen lds\nN 108,44,168\nR 166,191,36864\nN 108,60,172\nG 4\nN 80,32,176\n"
"R 168,188,40960\nN 80,48,180\nbuffer_load_dwordx4 a[144:147], v252, s[20:23], s64 offen offset:1024 sc1\nN 84,36,176\nR 170,188,45056\nN 84,52,180\n"
"buffer_load_dwordx4 a[148:151], v252, s[20:23], s65 offen offset:1024 sc1\nN 112,40,176\nR 172,188,49152\nN 112,56,180\nbuffer_load_dwordx4 v221, s[4:7], s63 offen offset:1024 lds\nN 116,44,176\n"
"R 174,188,53248\ns_add_u32 m0, s50, 40960\nN 116,60,180\nG 4\nN 88,32,160\nR 176,189,40960\n"
"s_add_u32 s63, s52, 1380352\nN 88,48,164\nbuffer_load_dwordx4 a[152:155], v252, s[20:23], s66 offen offset:1024 sc1\nN 92,36,160\nR 178,189,45056\nN 92,52,164\n"
"buffer_load_dwordx4 v220, s[4:7], s63 offen lds\nN 120,40,160\nR 180,189,49152\nN 120,56,164\nbuffer_load_dwordx4 v221, s[4:7], s63 offen offset:1024 lds\nN 124,44,160\n"
"R 182,189,53248\ns_add_u32 m0, s50, 49152\nN 124,60,164\ns_waitcnt vmcnt(12)\ns_barrier\nG 4\n"
"M 0,168,64\nR 160,190,40960\ns_add_u32 s63, s52, 1574912\nM 0,172,80\nbuffer_load_dwordx4 a[156:159], v252, s[20:23], s67 offen offset:1024 sc1\nM 16,168,68\n"
"R 162,190,45056\nM 16,172,84\nbuffer_load_dwordx4 v220, s[4:7], s63 offen lds\nM 32,168,72\nR 164,190,49152\nM 32,172,88\n"
"buffer_load_dwordx4 a[160:163], v252, s[20:23], s64 offen offset:2048 sc1\nM 48,168,76\nR 166,190,53248\nM 48,172,92\nG 4\nM 4,176,64\n"
"R 168,191,40960\nbuffer_load_dwordx4 v221, s[4:7], s63 offen offset:1024 lds\nM 4,180,80\nbuffer_load_dwordx4 a[164:167], v252, s[20:23], s65 offen offset:2048 sc1\ns_add_u32 s63, s52, 1771520\nM 20,176,68\n"
"R 170,191,45056\ns_add_u32 m0, s50, 57344\nM 20,180,84\nbuffer_load_dwordx4 v220, s[4:7], s63 offen lds\nM 36,176,72\nR 172,191,49152\n"
"M 36,180,88\nbuffer_load_dwordx4 v221, s[4:7], s63 offen offset:1024 lds\nM 52,176,76\nR 174,191,53248\nM 52,180,92\nG 4\n"
"M 8,160,64\nR 176,184,0\nM 8,164,80\nbuffer_load_dwordx4 a[168:171], v252, s[20:23], s66 offen offset:2048 sc1\nM 24,160,68\nR 178,184,4096\n"
"M 24,164,84\nM 40,160,72\nR 180,184,8192\nM 40,164,88\nbuffer_load_dwordx4 a[172:175], v252, s[20:23], s67 offen offset:2048 sc1\nM 56,160,76\n"
"R 182,184,12288\nM 56,164,92\nG 4\nM 12,168,64\nR 160,185,0\nM 12,172,80\n"
"M 28,168,68\nR 162,185,4096\nM 28,172,84\nbuffer_load_dwordx4 a[176:179], v252, s[20:23], s64 offen offset:3072 sc1\nM 44,168,72\nR 164,185,8192\n"
"M 44,172,88\nM 60,168,76\nR 166,185,12288\nM 60,172,92\nG 4\nN 64,64,176\n"
"R 168,186,0\nN 64,80,180\nbuffer_load_dwordx4 a[180:183], v252, s[20:23], s65 offen offset:3072 sc1\nN 68,68,176\nR 170,186,4096\nN 68,84,180\n"
"N 96,72,176\nR 172,186,8192\nN 96,88,180\nbuffer_load_dwordx4 a[184:187], v252, s[20:23], s66 offen offset:3072 sc1\nN 100,76,176\nR 174,186,12288\n"
"N 100,92,180\nG 4\nN 72,64,160\nR 176,187,0\nN 72,80,164\nN 76,68,160\n"
"R 178,187,4096\nN 76,84,164\nbuffer_load_dwordx4 a[188:191], v252, s[20:23], s67 offen offset:3072 sc1\nN 104,72,160\nR 180,187,8192\nN 104,88,164\n"
"s_add_u32 m0, s50, 65536\nN 108,76,160\nR 182,187,12288\ns_add_u32 s63, s52, 1968128\nN 108,92,164\nG 4\n"
"N 80,64,168\nR 160,184,16384\nN 80,80,172\ns_add_u32 s64, s70, 18886656\nbuffer_load_dwordx4 a[192:195], v252, s[20:23], s64 offen offset:0 sc1\nN 84,68,168\n"
"R 162,184,20480\nN 84,84,172\nbuffer_load_dwordx4 v220, s[4:7], s63 offen lds\nN 112,72,168\nR 164,184,24576\nN 112,88,172\n"
"s_add_u32 s65, s70, 18919424\nbuffer_load_dwordx4 a[196:199], v252, s[20:23], s65 offen offset:0 sc1\nN 116,76,168\nR 166,184,28672\nN 116,92,172\nG 4\n"
"N 88,64,176\nR 168,185,16384\nbuffer_load_dwordx4 v221, s[4:7], s63 offen offset:1024 lds\nN 88,80,180\ns_add_u32 m0, s50, 73728\ns_add_u32 s63, s52, 2164736\n"
"buffer_load_dwordx4 v220, s[4:7], s63 offen lds\nN 92,68,176\nR 170,185,20480\nN 92,84,180\ns_add_u32 s66, s71, 18886656\nbuffer_load_dwordx4 a[200:203], v252, s[20:23], s66 offen offset:0 sc1\n"
"N 120,72,176\nR 172,185,24576\nN 120,88,180\nbuffer_load_dwordx4 v221, s[4:7], s63 offen offset:1024 lds\nN 124,76,176\nR 174,185,28672\n"
"s_add_u32 m0, s50, 81920\nN 124,92,180\ns_waitcnt vmcnt(13)\ns_barrier\nG 4\nM 0,160,96\n"
"R 176,186,16384\ns_add_u32 s63, s52, 2361344\nM 0,164,112\ns_add_u32 s67, s71, 18919424\nbuffer_load_dwordx4 a[204:207], v252, s[20:23], s67 offen offset:0 sc1\nM 16,160,100\n"
"R 178,186,20480\nM 16,164,116\nbuffer_load_dwordx4 v220, s[4:7], s63 offen lds\nM 32,160,104\nR 180,186,24576\nM 32,164,120\n"
"buffer_load_dwordx4 v221, s[4:7], s63 offen offset:1024 lds\nM 48,160,108\nR 182,186,28672\ns_add_u32 m0, s50, 90112\nM 48,164,124\nG 4\n"
"M 4,168,96\nR 160,187,16384\ns_add_u32 s63, s52, 2557952\nM 4,172,112\nbuffer_load_dwordx4 a[208:211], v252, s[20:23], s64 offen offset:1024 sc1\nM 20,168,100\n"
"R 162,187,20480\nM 20,172,116\nbuffer_load_dwordx4 a[212:215], v252, s[20:23], s65 offen offset:1024 sc1\nM 36,168,104\nR 164,187,24576\nbuffer_load_dwordx4 v220, s[4:7], s63 offen lds\n"
"M 36,172,120\nbuffer_load_dwordx4 v221, s[4:7], s63 offen offset:1024 lds\nM 52,168,108\nR 166,187,28672\nM 52,172,124\nG 4\n"
"M 8,176,96\nR 168,184,32768\nM 8,180,112\nbuffer_load_dwordx4 a[216:219], v252, s[20:23], s66 offen offset:1024 sc1\nM 24,176,100\nR 170,184,36864\n"
"M 24,180,116\nM 40,176,104\nR 172,184,40960\nM 40,180,120\nM 56,176,108\nR 174,184,45056\n"
"M 56,180,124\nG 4\nM 12,160,96\nR 176,185,32768\nM 12,164,112\nbuffer_load_dwordx4 a[220:223], v252, s[20:23], s67 offen offset:1024 sc1\n"
"M 28,160,100\nR 178,185,36864\nM 28,164,116\nM 44,160,104\nR 180,185,40960\nM 44,164,120\n"
"buffer_load_dwordx4 a[224:227], v252, s[20:23], s64 offen offset:2048 sc1\nM 60,160,108\nR 182,185,45056\nM 60,164,124\nG 4\nN 64,96,168\n"
"R 160,186,32768\nN 64,112,172\nbuffer_load_dwordx4 a[228:231], v252, s[20:23], s65 offen offset:2048 sc1\nN 68,100,168\nR 162,186,36864\nN 68,116,172\n"
"N 96,104,168\nR 164,186,40960\nN 96,120,172\nN 100,108,168\nR 166,186,45056\nN 100,124,172\n"
"G 4\nN 72,96,176\nR 168,187,32768\nN 72,112,180\nbuffer_load_dwordx4 a[232:235], v252, s[20:23], s66 offen offset:2048 sc1\nN 76,100,176\n"
"R 170,187,36864\nN 76,116,180\ns_add_u32 m0, s50, 98304\nN 104,104,176\nR 172,187,40960\ns_add_u32 s63, s52, 2754560\n"
"N 104,120,180\nbuffer_load_dwordx4 a[236:239], v252, s[20:23], s67 offen offset:2048 sc1\nN 108,108,176\nR 174,187,45056\nN 108,124,180\nG 4\n"
"N 80,96,160\nR 176,184,49152\nN 80,112,164\nbuffer_load_dwordx4 v220, s[4:7], s63 offen lds\nN 84,100,160\nR 178,184,53248\n"
"N 84,116,164\nbuffer_load_dwordx4 a[240:243], v252, s[20:23], s64 offen offset:3072 sc1\nN 112,104,160\nR 180,184,57344\nN 112,120,164\nbuffer_load_dwordx4 v221, s[4:7], s63 offen offset:1024 lds\n"
"N 116,108,160\nR 182,184,61440\ns_add_u32 m0, s50, 106496\nN 116,124,164\nG 4\nN 88,96,168\n"
"R 160,185,49152\ns_add_u32 s63, s52, 2951168\nN 88,112,172\nbuffer_load_dwordx4 a[244:247], v252, s[20:23], s65 offen offset:3072 sc1\nN 92,100,168\nR 162,185,53248\n"
"N 92,116,172\nbuffer_load_dwordx4 v220, s[4:7], s63 offen lds\nN 120,104,168\nR 164,185,57344\nN 120,120,172\nbuffer_load_dwordx4 a[248:251], v252, s[20:23], s66 offen offset:3072 sc1\n"
"N 124,108,168\nR 166,185,61440\nN 124,124,172\ns_waitcnt vmcnt(12)\ns_barrier\nG 4\n"
"M 0,176,128\nR 168,186,49152\nbuffer_load_dwordx4 v221, s[4:7], s63 offen offset:1024 lds\nM 0,180,144\ns_lshr_b32 s84, s49, 3\ns_add_u32 s63, s52, 1576960\n"
"M 16,176,132\nR 170,186,53248\ns_add_u32 m0, s50, 114688\nM 16,180,148\nbuffer_load_dwordx4 a[252:255], v252, s[20:23], s67 offen offset:3072 sc1\nM 32,176,136\n"
"R 172,186,57344\nbuffer_load_dwordx4 v220, s[4:7], s63 offen lds\nM 32,180,152\ns_add_u32 s84, s84, 82837504\nM 48,176,140\nR 174,186,61440\n"
"M 48,180,156\nbuffer_load_dwordx4 v[128:131], v252, s[20:23], s84 offen offset:0 sc1\nG 4\nM 4,160,128\nR 176,187,49152\nbuffer_load_dwordx4 v221, s[4:7], s63 offen offset:1024 lds\n"
"M 4,164,144\nbuffer_load_dwordx4 v[132:135], v252, s[20:23], s84 offen offset:1024 sc1\nM 20,160,132\nR 178,187,53248\ns_add_u32 m0, s50, 0\nM 20,164,148\n"
"s_lshr_b32 s85, s49, 3\ns_add_u32 s63, s52, 1773568\nM 36,160,136\nR 180,187,57344\ns_add_u32 s85, s85, 82841600\nbuffer_load_dwordx4 v220, s[4:7], s63 offen lds\n"
"M 36,164,152\nbuffer_load_dwordx4 v[136:139], v252, s[20:23], s84 offen offset:2048 sc1\nM 52,160,140\nR 182,187,61440\nM 52,164,156\nG 4\n"
"M 8,168,128\nR 160,188,0\nbuffer_load_dwordx4 v221, s[4:7], s63 offen offset:1024 lds\nM 8,172,144\nbuffer_load_dwordx4 v[140:143], v252, s[20:23], s84 offen offset:3072 sc1\nM 24,168,132\n"
"R 162,188,4096\nM 24,172,148\nbuffer_load_dwordx4 v[144:147], v252, s[20:23], s85 offen offset:0 sc1\nM 40,168,136\nR 164,188,8192\nM 40,172,152\n"
"buffer_load_dwordx4 v[148:151], v252, s[20:23], s85 offen offset:1024 sc1\nM 56,168,140\nR 166,188,12288\nM 56,172,156\nG 4\nM 12,176,128\n"
"R 168,189,0\nM 12,180,144\nbuffer_load_dwordx4 v[152:155], v252, s[20:23], s85 offen offset:2048 sc1\nM 28,176,132\nR 170,189,4096\nM 28,180,148\n"
"buffer_load_dwordx4 v[156:159], v252, s[20:23], s85 offen offset:3072 sc1\nM 44,176,136\nR 172,189,8192\nM 44,180,152\nM 60,176,140\nR 174,189,12288\n"
"M 60,180,156\nG 4\nM 0,160,160\nR 176,190,0\nM 0,164,176\ns_add_u32 m0, s50, 8192\n"
"M 16,160,164\nR 178,190,4096\nM 16,164,180\ns_add_u32 s63, s52, 1970176\nM 32,160,168\nR 180,190,8192\n"
"M 32,164,184\nbuffer_load_dwordx4 v220, s[4:7], s63 offen lds\nM 48,160,172\nR 182,190,12288\nM 48,164,188\nG 4\n"
"M 4,168,160\nR 160,191,0\nM 4,172,176\nbuffer_load_dwordx4 v221, s[4:7], s63 offen offset:1024 lds\nM 20,168,164\nR 162,191,4096\n"
"M 20,172,180\ns_add_u32 m0, s50, 16384\nM 36,168,168\nR 164,191,8192\nM 36,172,184\ns_add_u32 s63, s52, 2166784\n"
"M 52,168,172\nR 166,191,12288\nM 52,172,188\nG 4\nM 8,176,160\nR 168,188,16384\n"
"M 8,180,176\nbuffer_load_dwordx4 v220, s[4:7], s63 offen lds\nM 24,176,164\nR 170,188,20480\nM 24,180,180\nM 40,176,168\n"
"R 172,188,24576\nM 40,180,184\nbuffer_load_dwordx4 v221, s[4:7], s63 offen offset:1024 lds\nM 56,176,172\nR 174,188,28672\ns_add_u32 s63, s52, 2363392\n"
"M 56,180,188\nG 4\nM 12,160,160\nR 176,189,16384\nM 12,164,176\ns_add_u32 m0, s50, 24576\n"
"M 28,160,164\nR 178,189,20480\nM 28,164,180\nbuffer_load_dwordx4 v220, s[4:7], s63 offen lds\nM 44,160,168\nR 180,189,24576\n"
"M 44,164,184\nM 60,160,172\nR 182,189,28672\nM 60,164,188\ns_waitcnt vmcnt(10)\ns_barrier\n"
"G 4\nM 0,168,192\nR 160,190,16384\nM 0,172,208\nbuffer_load_dwordx4 v221, s[4:7], s63 offen offset:1024 lds\nM 16,168,196\n"
"R 162,190,20480\nM 16,172,212\ns_add_u32 m0, s50, 32768\nM 32,168,200\nR 164,190,24576\nM 32,172,216\n"
"s_add_u32 s63, s52, 2560000\nM 48,168,204\nR 166,190,28672\nv_min3_u32 v230, v129, v131, v133\nv_min3_u32 v230, v230, v135, v137\nM 48,172,220\n"
"G 4\ns_waitcnt vmcnt(10)\nv_min3_u32 v230, v230, v139, v141\ns_waitcnt vmcnt(9)\nv_min3_u32 v230, v230, v143, v145\ns_waitcnt vmcnt(8)\n"
"v_min3_u32 v230, v230, v147, v149\nM 4,176,192\nR 168,191,16384\ns_waitcnt vmcnt(7)\nv_min3_u32 v230, v230, v151, v153\ns_waitcnt vmcnt(6)\n"
"v_min3_u32 v230, v230, v155, v157\nM 4,180,208\nbuffer_load_dwordx4 v220, s[4:7], s63 offen lds\nv_min3_u32 v230, v230, v159, v159\nM 20,176,196\nR 170,191,20480\n"
"v_max3_u32 v231, v129, v131, v133\nv_max3_u32 v231, v231, v135, v137\nM 20,180,212\nv_max3_u32 v231, v231, v139, v141\nv_max3_u32 v231, v231, v143, v145\nv_max3_u32 v231, v231, v147, v149\n"
"M 36,176,200\nR 172,191,24576\nv_max3_u32 v231, v231, v151, v153\nv_max3_u32 v231, v231, v155, v157\nM 36,180,216\nv_max3_u32 v231, v231, v159, v159\n"
"v_cmp_ne_u32 s[86:87], s46, v230\nv_cmp_ne_u32 s[88:89], s46, v231\nM 52,176,204\nR 174,191,28672\ns_or_b64 s[86:87], s[86:87], s[88:89]\ns_cbranch_scc1 Lq650\n"
"Lq651:\nM 52,180,220\nG 4\nv_add_f32 v128, v128, v144\nv_add_f32 v130, v130, v146\nv_add_f32 v132, v132, v148\n"
"M 8,160,192\nR 176,188,32768\nv_add_f32 v134, v134, v150\nv_add_f32 v136, v136, v152\nM 8,164,208\nbuffer_load_dwordx4 v221, s[4:7], s63 offen offset:1024 lds\n"
"v_add_f32 v138, v138, v154\nM 24,160,196\nR 178,188,36864\nv_add_f32 v140, v140, v156\nv_add_f32 v142, v142, v158\nM 24,164,212\n"
"v_add_f32 v128, v128, v136\nv_add_f32 v130, v130, v138\nv_add_f32 v132, v132, v140\nM 40,160,200\nR 180,188,40960\nLq690:\n"
"s_lshr_b32 s60, s45, 3\ns_lshl2_add_u32 s60, s60, s40\ns_lshl_b32 s60, s60, 8\nv_lshl_add_u32 v224, v252, 3, s60\nv_add_u32 v224, Lq690+4096-LqPC, v224\ns_mov_b64 exec, 3\n"
"global_load_dword v224, v224, s[98:99]\ns_mov_b64 exec, -1\nv_add_f32 v134, v134, v142\nM 40,164,216\ns_add_u32 m0, s50, 40960\nv_add_f32 v128, v128, v132\n"
"v_add_f32 v130, v130, v134\nv_lshrrev_b32 v231, 1, v252\nM 56,160,204\nR 182,188,45056\nv_lshrrev_b32 v230, 2, v252\ns_lshl_b32 s62, s40, 9\n"
"s_add_u32 s62, s62, 159232\nv_add_u32 v231, s62, v231\nM 56,164,220\nds_write2_b32 v231, v128, v130 offset1:1\nG 5\nM 12,168,192\n"
"R 160,189,32768\ns_lshl_b32 s62, s40, 9\ns_add_u32 s62, s62, 159232\nv_add_u32 v230, s62, v230\nM 12,172,208\ns_add_u32 s63, s52, 2756608\n"
"ds_read2st64_b32 v[232:233], v230 offset1:1\nM 28,168,196\nR 162,189,36864\nM 28,172,212\nbuffer_load_dwordx4 v220, s[4:7], s63 offen lds\nM 44,168,200\n"
"R 164,189,40960\nM 44,172,216\nM 60,168,204\nR 166,189,45056\nM 60,172,220\nG 6\n"
"G 3\nv_add_f32 v232, v232, v233\nv_mov_b32 v233, v232\ns_nop 1\nv_permlane32_swap_b32 v232, v233\nM 0,176,224\n"
"R 168,190,32768\nv_mov_b32 v230, v232\nv_mov_b32 v231, v233\nM 0,180,240\nbuffer_load_dwordx4 v221, s[4:7], s63 offen offset:1024 lds\nv_permlane16_swap_b32 v230, v232\n"
"M 16,176,228\nR 170,190,36864\ns_add_u32 s63, s52, 2953216\nv_permlane16_swap_b32 v231, v233\nM 16,180,244\ns_add_u32 m0, s50, 49152\n"
"v_mul_f32 v230, 0xba800000, v230\nv_mul_f32 v214, v230, v230\nv_mul_f32 v231, 0x3a800000, v231\nM 32,176,232\nR 172,190,40960\nv_sub_f32 v231, v231, v214\n"
"v_max_f32 v231, 0, v231\nM 32,180,248\nbuffer_load_dwordx4 v220, s[4:7], s63 offen lds\nv_add_f32 v231, 0x3727c5ac, v231\nM 48,176,236\nR 174,190,45056\n"
"v_and_b32 v208, s77, v230\nv_rsq_f32 v234, v231\nM 48,180,252\nG 4\nv_mul_f32 v216, v231, v234\nv_sub_f32 v230, v230, v208\n"
"v_cvt_pk_bf16_f32 v209, v230, v216\nM 4,160,224\nR 176,191,32768\nv_cvt_pk_bf16_f32 v208, v208, v208\nv_mov_b32 v214, v234\nM 4,164,240\n"
"s_mov_b64 s[60:61], 0xffff\nv_cndmask_b32 v208, 0, v208, s[60:61]\nv_mul_f32 v215, 0x3e8293ee, v234\nM 20,160,228\nR 178,191,36864\nv_mul_f32 v232, 0xba800000, v232\n"
"M 20,164,244\nbuffer_load_dwordx4 v221, s[4:7], s63 offen offset:1024 lds\nM 36,160,232\nR 180,191,40960\ns_add_u32 s63, s52, 0\nM 36,164,248\n"
"s_add_u32 m0, s50, 57344\ns_mov_b64 s[60:61], 0xffff\nv_cndmask_b32 v209, 0, v209, s[60:61]\nM 52,160,236\nR 182,191,45056\nv_mul_f32 v217, v232, v232\n"
"M 52,164,252\nG 4\nv_mul_f32 v233, 0x3a800000, v233\nv_sub_f32 v233, v233, v217\nM 8,168,224\nR 160,188,49152\n"
"v_max_f32 v233, 0, v233\nM 8,172,240\nbuffer_load_dwordx4 v220, s[4:7], s63 offen lds\nM 24,168,228\nR 162,188,53248\nv_add_f32 v233, 0x3727c5ac, v233\n"
"M 24,172,244\nv_rsq_f32 v235, v233\nM 40,168,232\nR 164,184,0\nv_and_b32 v212, s77, v232\nM 40,172,248\n"
"buffer_load_dwordx4 v221, s[4:7], s63 offen offset:1024 lds\nM 56,168,236\nR 166,184,4096\ns_add_u32 s63, s52, 196608\nM 56,172,252\nG 4\n"
"v_mul_f32 v219, v233, v235\nv_sub_f32 v232, v232, v212\nM 12,176,224\nR 168,189,49152\nv_cvt_pk_bf16_f32 v213, v232, v219\nM 12,180,240\n"
"s_add_u32 m0, s50, 65536\nv_cvt_pk_bf16_f32 v212, v212, v212\nv_mov_b32 v217, v235\nM 28,176,228\nR 170,189,53248\nv_mul_f32 v218, 0x3e8293ee, v235\n"
"M 28,180,244\nbuffer_load_dwordx4 v220, s[4:7], s63 offen lds\nM 44,176,232\nR 172,185,0\nv_bfe_u32 v230, v252, 2, 6\nM 44,180,248\n"
"s_mov_b64 s[60:61], 0xffff\nv_cndmask_b32 v212, 0, v212, s[60:61]\ns_mov_b64 s[60:61], 0xffff\nv_cndmask_b32 v213, 0, v213, s[60:61]\nM 60,176,236\nR 174,185,4096\n"
"s_lshl_b32 s60, s40, 7\ns_add_u32 s60, s60, 161280\nv_add_u32 v230, s60, v230\nM 60,180,252\ns_waitcnt vmcnt(12)\ns_barrier\n"
"G 4\nN 64,128,160\nR 176,190,49152\nN 64,144,164\ns_mov_b32 m0, s51\nds_write_b32 v230, v214 offset:0\n"
"ds_write_b32 v230, v217 offset:64\nN 68,132,160\nR 178,190,53248\nN 68,148,164\nds_write_addtid_b32 v32 offset:0\nN 96,136,160\n"
"R 180,186,0\nN 96,152,164\nds_write_addtid_b32 v33 offset:256\nN 100,140,160\nR 182,186,4096\nN 100,156,164\n"
"ds_write_addtid_b32 v34 offset:512\nG 9\nN 72,128,168\nR 160,191,49152\ns_mov_b32 s60, 156160\nv_bfe_u32 v246, v252, 4, 4\n"
"v_lshl_add_u32 v246, v246, 3, s60\nN 72,144,172\nds_read_b64 v[238:239], v246 offset:0\nN 76,132,168\nR 162,191,53248\nds_read_b64 v[240:241], v246 offset:128\n"
"N 76,148,172\nds_read_b64 v[242:243], v246 offset:256\nds_write_addtid_b32 v35 offset:768\nN 104,136,168\nR 164,187,0\nds_read_b64 v[244:245], v246 offset:384\n"
"N 104,152,172\nds_read_b64 v[246:247], v246 offset:0\nds_write_addtid_b32 v36 offset:1024\nN 108,140,168\nR 166,187,4096\nds_write_addtid_b32 v37 offset:1280\n"
"N 108,156,172\nds_write_addtid_b32 v38 offset:1536\nds_write_addtid_b32 v39 offset:1792\nG 15\nN 80,128,176\nR 168,184,8192\n"
"ds_write_addtid_b32 v40 offset:2048\nN 80,144,180\nG 12\nv_mfma_f32_16x16x32_bf16 v[0:3], v[238:241], v[208:211], v[0:3]\nds_write_addtid_b32 v41 offset:2304\nds_write_addtid_b32 v42 offset:2560\n"
"N 84,132,176\nR 170,184,12288\nG 14\nv_mfma_f32_16x16x32_bf16 v[4:7], v[240:243], v[208:211], v[4:7]\nds_write_addtid_b32 v43 offset:2816\nN 84,148,180\n"
"G 12\nv_mfma_f32_16x16x32_bf16 v[8:11], v[242:245], v[208:211], v[8:11]\nds_write_addtid_b32 v44 offset:3072\nds_write_addtid_b32 v45 offset:3328\nN 112,136,176\nR 172,184,16384\n"
"G 14\nv_mfma_f32_16x16x32_bf16 v[12:15], v[244:247], v[208:211], v[12:15]\nds_write_addtid_b32 v46 offset:3584\nN 112,152,180\nv_mfma_f32_16x16x32_bf16 v[16:19], v[244:247], v[210:213], v[16:19]\nds_write_addtid_b32 v47 offset:3840\n"
"ds_write_addtid_b32 v48 offset:4096\nN 116,140,176\nR 174,184,20480\nv_mfma_f32_16x16x32_bf16 v[20:23], v[238:241], v[210:213], v[20:23]\nds_write_addtid_b32 v49 offset:4352\nN 116,156,180\n"
"v_mfma_f32_16x16x32_bf16 v[24:27], v[240:243], v[210:213], v[24:27]\nds_write_addtid_b32 v50 offset:4608\nds_write_addtid_b32 v51 offset:4864\nG 15\nN 88,128,160\nR 176,185,8192\n"
"v_mfma_f32_16x16x32_bf16 v[28:31], v[242:245], v[210:213], v[28:31]\nds_write_addtid_b32 v52 offset:5120\nN 88,144,164\nds_write_addtid_b32 v53 offset:5376\nds_write_addtid_b32 v54 offset:5632\nN 92,132,160\n"
"R 178,185,12288\nds_write_addtid_b32 v55 offset:5888\nN 92,148,164\nds_write_addtid_b32 v56 offset:6144\nds_write_addtid_b32 v57 offset:6400\nN 120,136,160\n"
"R 180,185,16384\nds_write_addtid_b32 v58 offset:6656\nN 120,152,164\nds_write_addtid_b32 v59 offset:6912\nds_write_addtid_b32 v60 offset:7168\nN 124,140,160\n"
"R 182,185,20480\nds_write_addtid_b32 v61 offset:7424\nN 124,156,164\nds_write_addtid_b32 v62 offset:7680\nds_write_addtid_b32 v63 offset:7936\nG 15\n"
"N 64,160,168\nR 160,186,8192\ns_add_u32 m0, s50, 65536\nN 64,176,172\nbuffer_load_dwordx4 v221, s[4:7], s63 offen offset:1024 lds\nN 68,164,168\n"
"R 162,186,12288\ns_waitcnt vmcnt(4) lgkmcnt(2)\ns_barrier\ns_add_u32 m0, s50, 73728\ns_add_u32 s63, s52, 393216\nN 68,180,172\n"
"buffer_load_dwordx4 v220, s[4:7], s63 offen lds\nds_read2st64_b32 v[32:33], v225 offset0:0 offset1:1\nN 96,168,168\nR 164,186,16384\nds_read2st64_b32 v[34:35], v225 offset0:2 offset1:3\nN 96,184,172\n"
"ds_read2st64_b32 v[36:37], v225 offset0:4 offset1:5\nv_mov_b32 v248, -1\nv_mov_b32 v249, -1\nv_mov_b32 v250, -1\nv_mov_b32 v251, -1\nN 100,172,168\n"
"R 166,186,20480\nds_read2st64_b32 v[38:39], v225 offset0:6 offset1:7\nN 100,188,172\nds_read2st64_b32 v[40:41], v225 offset0:8 offset1:9\nds_read2st64_b32 v[42:43], v225 offset0:10 offset1:11\nN 72,160,176\n"
"R 168,187,8192\nds_read2st64_b32 v[44:45], v225 offset0:12 offset1:13\nN 72,176,180\nbuffer_load_dwordx4 v221, s[4:7], s63 offen offset:1024 lds\nG 9\nv_add_f32 v0, v0, v32\n"
"N 76,164,176\nR 170,187,12288\ns_add_u32 m0, s50, 81920\ns_add_u32 s63, s52, 589824\nv_add_f32 v1, v1, v33\nN 76,180,180\n"
"buffer_load_dwordx4 v220, s[4:7], s63 offen lds\nG 8\nv_add_f32 v2, v2, v34\nN 104,168,176\nR 172,187,16384\nv_add_f32 v3, v3, v35\n"
"G 8\nv_add_f32 v4, v4, v36\nN 104,184,180\nds_read2st64_b32 v[46:47], v225 offset0:14 offset1:15\nv_add_f32 v5, v5, v37\nG 7\n"
"v_add_f32 v6, v6, v38\nN 108,172,176\nR 174,187,20480\nv_add_f32 v7, v7, v39\nG 7\nv_add_f32 v8, v8, v40\n"
"N 108,188,180\nds_read2st64_b32 v[48:49], v225 offset0:16 offset1:17\nv_add_f32 v9, v9, v41\nN 80,160,160\nR 176,184,24576\nG 8\n"
"v_add_f32 v10, v10, v42\nv_add_f32 v11, v11, v43\nN 80,176,164\nbuffer_load_dwordx4 v221, s[4:7], s63 offen offset:1024 lds\nG 6\nv_add_f32 v12, v12, v44\n"
"N 84,164,160\nR 178,184,28672\nv_add_f32 v13, v13, v45\nN 84,180,164\ns_add_u32 m0, s50, 90112\nds_read2st64_b32 v[50:51], v225 offset0:18 offset1:19\n"
"G 5\nv_add_f32 v14, v14, v46\nN 112,168,160\nR 180,184,32768\nv_add_f32 v15, v15, v47\nN 112,184,164\n"
"s_add_u32 s63, s52, 786432\nds_read2st64_b32 v[52:53], v225 offset0:20 offset1:21\nG 5\nv_add_f32 v16, v16, v48\nN 116,172,160\nR 182,184,36864\n"
"v_add_f32 v17, v17, v49\nN 116,188,164\nds_read2st64_b32 v[54:55], v225 offset0:22 offset1:23\nds_read2st64_b32 v[56:57], v225 offset0:24 offset1:25\nN 88,160,168\nR 160,185,24576\n"
"ds_read2st64_b32 v[58:59], v225 offset0:26 offset1:27\nN 88,176,172\nbuffer_load_dwordx4 v220, s[4:7], s63 offen lds\nN 92,164,168\nR 162,185,28672\nG 8\n"
"v_add_f32 v18, v18, v50\nN 92,180,172\nds_read2st64_b32 v[60:61], v225 offset0:28 offset1:29\nv_add_f32 v19, v19, v51\nN 120,168,168\nR 164,185,32768\n"
"G 8\nv_add_f32 v20, v20, v52\nN 120,184,172\nbuffer_load_dwordx4 v221, s[4:7], s63 offen offset:1024 lds\nN 124,172,168\nR 166,185,36864\n"
"s_add_u32 s63, s52, 983040\nN 124,188,172\nds_read2st64_b32 v[62:63], v225 offset0:30 offset1:31\nG 9\nv_add_f32 v21, v21, v53\nN 64,192,176\n"
"R 168,186,24576\nG 9\nv_add_f32 v22, v22, v54\nN 64,208,180\ns_add_u32 m0, s50, 98304\nv_add_f32 v23, v23, v55\n"
"G 8\nv_add_f32 v24, v24, v56\nN 68,196,176\nR 170,186,28672\nv_add_f32 v25, v25, v57\nN 68,212,180\n"
"buffer_load_dwordx4 v220, s[4:7], s63 offen lds\nN 96,200,176\nR 172,186,32768\nG 8\nv_add_f32 v26, v26, v58\nN 96,216,180\n"
"v_add_f32 v27, v27, v59\nG 6\nv_add_f32 v28, v28, v60\nN 100,204,176\nR 174,186,36864\nv_add_f32 v29, v29, v61\n"
"N 100,220,180\nG 5\nG 4\nv_add_f32 v30, v30, v62\nv_add_f32 v31, v31, v63\nN 72,192,160\n"
"R 176,187,24576\nv_mul_f32 v16, v16, v217\nN 72,208,164\nbuffer_load_dwordx4 v221, s[4:7], s63 offen offset:1024 lds\nN 76,196,160\nR 178,187,28672\n"
"s_add_u32 s63, s52, 1179648\nN 76,212,164\ns_add_u32 m0, s50, 106496\nv_mul_f32 v17, v17, v217\nv_mul_f32 v18, v18, v217\nN 104,200,160\n"
"R 180,187,32768\nv_mul_f32 v19, v19, v217\nN 104,216,164\nbuffer_load_dwordx4 v220, s[4:7], s63 offen lds\nN 108,204,160\nR 182,187,36864\n"
"v_mul_f32 v20, v20, v217\nN 108,220,164\nG 4\nv_mul_f32 v21, v21, v217\nv_mul_f32 v22, v22, v217\nN 80,192,168\n"
"R 160,184,40960\nv_mul_f32 v23, v23, v217\nN 80,208,172\nv_mul_f32 v24, v24, v217\nv_mul_f32 v25, v25, v217\nN 84,196,168\n"
"R 162,184,45056\nv_mul_f32 v26, v26, v217\nN 84,212,172\nbuffer_load_dwordx4 v221, s[4:7], s63 offen offset:1024 lds\nN 112,200,168\nR 164,184,49152\n"
"s_add_u32 s63, s52, 1376256\nN 112,216,172\ns_add_u32 m0, s50, 114688\nv_mul_f32 v27, v27, v217\nv_mul_f32 v28, v28, v217\nN 116,204,168\n"
"R 166,184,53248\nv_mul_f32 v29, v29, v217\nN 116,220,172\nG 4\nv_mul_f32 v30, v30, v217\nv_mul_f32 v31, v31, v217\n"
"N 88,192,176\nR 168,185,40960\nv_cvt_pk_bf16_f32 v16, v16, v17\nN 88,208,180\nbuffer_load_dwordx4 v220, s[4:7], s63 offen lds\nN 92,196,176\n"
"R 170,185,45056\nv_cvt_pk_bf16_f32 v17, v18, v19\nN 92,212,180\nv_cvt_pk_bf16_f32 v18, v20, v21\nv_cvt_pk_bf16_f32 v19, v22, v23\nN 120,200,176\n"
"R 172,185,49152\nv_cvt_pk_bf16_f32 v20, v24, v25\nN 120,216,180\nbuffer_load_dwordx4 v221, s[4:7], s63 offen offset:1024 lds\nN 124,204,176\nR 174,185,53248\n"
"v_cvt_pk_bf16_f32 v21, v26, v27\nN 124,220,180\nG 4\nv_cvt_pk_bf16_f32 v22, v28, v29\nv_cvt_pk_bf16_f32 v23, v30, v31\nN 64,224,160\n"
"R 176,186,40960\ns_waitcnt vmcnt(8)\ns_barrier\nN 64,240,164\nv_mul_f32 v0, v0, v214\nv_mul_f32 v1, v1, v214\n"
"N 68,228,160\nR 178,186,45056\nv_mul_f32 v2, v2, v214\nN 68,244,164\nv_mul_f32 v3, v3, v214\nv_mul_f32 v4, v4, v214\n"
"N 96,232,160\nR 180,186,49152\ns_mov_b32 s60, 156672\nv_bfe_u32 v246, v252, 4, 4\nv_lshl_add_u32 v246, v246, 3, s60\nN 96,248,164\n"
"s_bfe_u32 s100, s58, 0x20007\ns_mul_i32 s100, s100, 49152\ns_add_u32 s100, s100, s72\ns_add_u32 s100, s100, 65044480\nbuffer_store_dwordx4 v[248:251], v252, s[20:23], s100 offen offset:0 sc1\nds_read_b64 v[238:239], v246 offset:0\n"
"N 100,236,160\nR 182,186,53248\nds_read_b64 v[240:241], v246 offset:128\nN 100,252,164\nds_read_b64 v[242:243], v246 offset:256\nG 7\n"
"N 72,224,168\nR 160,187,40960\nds_read_b64 v[244:245], v246 offset:384\nN 72,240,172\nds_read_b64 v[246:247], v246 offset:0\ns_add_u32 m0, s50, 0\n"
"N 76,228,168\nR 162,187,45056\ns_add_u32 s63, s52, 1572864\nN 76,244,172\nbuffer_load_dwordx4 v220, s[4:7], s63 offen lds\nN 104,232,168\n"
"R 164,187,49152\nv_mul_f32 v5, v5, v214\nN 104,248,172\nG 6\nv_mfma_f32_16x16x32_bf16 v[64:67], v[208:211], v[238:241], v[64:67]\nv_mul_f32 v6, v6, v214\n"
"v_mul_f32 v7, v7, v214\nN 108,236,168\nR 166,187,53248\nG 6\nv_mfma_f32_16x16x32_bf16 v[72:75], v[208:211], v[240:243], v[72:75]\nv_mul_f32 v8, v8, v214\n"
"N 108,252,172\nG 4\nv_mfma_f32_16x16x32_bf16 v[80:83], v[208:211], v[242:245], v[80:83]\nv_mul_f32 v9, v9, v214\nv_mul_f32 v10, v10, v214\nN 80,224,176\n"
"R 168,184,57344\nG 4\nv_mfma_f32_16x16x32_bf16 v[88:91], v[208:211], v[244:247], v[88:91]\nv_mul_f32 v11, v11, v214\nN 80,240,180\nv_mfma_f32_16x16x32_bf16 v[68:71], v[210:213], v[244:247], v[68:71]\n"
"buffer_load_dwordx4 v221, s[4:7], s63 offen offset:1024 lds\nN 84,228,176\nR 170,184,61440\nv_mfma_f32_16x16x32_bf16 v[76:79], v[210:213], v[238:241], v[76:79]\ns_add_u32 s63, s52, 1769472\nN 84,244,180\n"
"v_mfma_f32_16x16x32_bf16 v[84:87], v[210:213], v[240:243], v[84:87]\ns_add_u32 m0, s50, 8192\nv_mul_f32 v12, v12, v214\nv_mul_f32 v13, v13, v214\nN 112,232,176\nR 172,188,0\n"
"v_mfma_f32_16x16x32_bf16 v[92:95], v[210:213], v[242:245], v[92:95]\nv_mul_f32 v14, v14, v214\nN 112,248,180\nbuffer_load_dwordx4 v220, s[4:7], s63 offen lds\nN 116,236,176\nR 174,188,4096\n"
"v_mul_f32 v15, v15, v214\nN 116,252,180\nG 4\nv_cvt_pk_bf16_f32 v0, v0, v1\nv_cvt_pk_bf16_f32 v1, v2, v3\nN 88,224,160\n"
"R 176,185,57344\nv_cvt_pk_bf16_f32 v2, v4, v5\nN 88,240,164\nv_cvt_pk_bf16_f32 v3, v6, v7\nv_cvt_pk_bf16_f32 v4, v8, v9\nN 92,228,160\n"
"R 178,185,61440\nv_cvt_pk_bf16_f32 v5, v10, v11\nN 92,244,164\nbuffer_load_dwordx4 v221, s[4:7], s63 offen offset:1024 lds\nN 120,232,160\nR 180,189,0\n"
"v_cvt_pk_bf16_f32 v6, v12, v13\nN 120,248,164\nv_cvt_pk_bf16_f32 v7, v14, v15\nN 124,236,160\nR 182,189,4096\nN 124,252,164\n"
"G 4\nv_mfma_f32_16x16x32_bf16 v[128:131], v[168:171], a[0:3], 0\nR 160,186,57344\nM 128,172,16\ns_mov_b32 m0, s51\nv_mfma_f32_16x16x32_bf16 v[144:147], v[168:171], a[4:7], 0\n"
"R 162,186,61440\nM 144,172,20\nds_write_addtid_b32 v96 offset:0\nv_mfma_f32_16x16x32_bf16 v[32:35], v[168:171], a[8:11], 0\nR 164,190,0\nM 32,172,24\n"
"ds_write_addtid_b32 v97 offset:256\nv_mfma_f32_16x16x32_bf16 v[48:51], v[168:171], a[12:15], 0\nR 166,190,4096\nM 48,172,28\nds_write_addtid_b32 v98 offset:512\nG 7\n"
"v_mfma_f32_16x16x32_bf16 v[132:135], v[176:179], a[0:3], 0\nR 168,187,57344\nM 132,180,16\nds_write_addtid_b32 v99 offset:768\nv_mfma_f32_16x16x32_bf16 v[148:151], v[176:179], a[4:7], 0\nR 170,187,61440\n"
"M 148,180,20\nds_write_addtid_b32 v100 offset:1024\nv_mfma_f32_16x16x32_bf16 v[36:39], v[176:179], a[8:11], 0\nR 172,191,0\nM 36,180,24\nds_write_addtid_b32 v101 offset:1280\n"
"v_mfma_f32_16x16x32_bf16 v[52:55], v[176:179], a[12:15], 0\nR 174,191,4096\nM 52,180,28\nds_write_addtid_b32 v102 offset:1536\nds_write_addtid_b32 v103 offset:1792\nG 10\n"
"v_mfma_f32_16x16x32_bf16 v[136:139], v[160:163], a[0:3], 0\nR 176,188,8192\nds_write_addtid_b32 v104 offset:2048\nM 136,164,16\nds_write_addtid_b32 v105 offset:2304\nds_write_addtid_b32 v106 offset:2560\n"
"v_mfma_f32_16x16x32_bf16 v[152:155], v[160:163], a[4:7], 0\nR 178,188,12288\nds_write_addtid_b32 v107 offset:2816\nM 152,164,20\nds_write_addtid_b32 v108 offset:3072\nds_write_addtid_b32 v109 offset:3328\n"
"v_mfma_f32_16x16x32_bf16 v[40:43], v[160:163], a[8:11], 0\nR 180,188,16384\nds_write_addtid_b32 v110 offset:3584\nM 40,164,24\nds_write_addtid_b32 v111 offset:3840\nds_write_addtid_b32 v112 offset:4096\n"
"v_mfma_f32_16x16x32_bf16 v[56:59], v[160:163], a[12:15], 0\nR 182,188,20480\nds_write_addtid_b32 v113 offset:4352\nM 56,164,28\nds_write_addtid_b32 v114 offset:4608\nds_write_addtid_b32 v115 offset:4864\n"
"G 15\nv_mfma_f32_16x16x32_bf16 v[140:143], v[168:171], a[0:3], 0\nR 160,189,8192\nds_write_addtid_b32 v116 offset:5120\nM 140,172,16\nds_write_addtid_b32 v117 offset:5376\n"
"ds_write_addtid_b32 v118 offset:5632\nv_mfma_f32_16x16x32_bf16 v[156:159], v[168:171], a[4:7], 0\nR 162,189,12288\nds_write_addtid_b32 v119 offset:5888\nM 156,172,20\nds_write_addtid_b32 v120 offset:6144\n"
"ds_write_addtid_b32 v121 offset:6400\nv_mfma_f32_16x16x32_bf16 v[44:47], v[168:171], a[8:11], 0\nR 164,189,16384\nds_write_addtid_b32 v122 offset:6656\nM 44,172,24\nds_write_addtid_b32 v123 offset:6912\n"
"ds_write_addtid_b32 v124 offset:7168\nv_mfma_f32_16x16x32_bf16 v[60:63], v[168:171], a[12:15], 0\nR 166,189,20480\nds_write_addtid_b32 v125 offset:7424\nM 60,172,28\nds_write_addtid_b32 v126 offset:7680\n"
"ds_write_addtid_b32 v127 offset:7936\nG 15\nM 128,176,32\nR 168,190,8192\ns_add_u32 m0, s50, 16384\ns_add_u32 s63, s52, 1966080\n"
"M 128,180,48\nbuffer_load_dwordx4 v220, s[4:7], s63 offen lds\nM 144,176,36\nR 170,190,12288\ns_waitcnt vmcnt(10) lgkmcnt(2)\ns_barrier\n"
"M 144,180,52\nds_read2st64_b32 v[96:97], v225 offset0:0 offset1:1\nds_read2st64_b32 v[98:99], v225 offset0:2 offset1:3\nv_lshrrev_b32 v226, 8, v252\ns_lshl_b32 s60, s40, 7\ns_add_u32 s60, s60, 161280\n"
"v_lshl_add_u32 v226, v226, 4, s60\nM 32,176,40\nR 172,190,16384\nds_read2st64_b32 v[100:101], v225 offset0:4 offset1:5\nds_read_b128 v[230:233], v226 offset:0\nM 32,180,56\n"
"buffer_store_dwordx4 v[248:251], v252, s[20:23], s100 offen offset:1024 sc1\nds_read2st64_b32 v[102:103], v225 offset0:6 offset1:7\nds_read_b128 v[234:237], v226 offset:64\nM 48,176,44\nR 174,190,20480\nds_read2st64_b32 v[104:105], v225 offset0:8 offset1:9\n"
"M 48,180,60\nds_read2st64_b32 v[106:107], v225 offset0:10 offset1:11\nds_read2st64_b32 v[108:109], v225 offset0:12 offset1:13\nM 132,160,32\nR 176,191,8192\nds_read2st64_b32 v[110:111], v225 offset0:14 offset1:15\n"
"M 132,164,48\nds_read2st64_b32 v[112:113], v225 offset0:16 offset1:17\nG 13\nv_add_f32 v64, v64, v96\nv_add_f32 v65, v65, v97\nM 148,160,36\n"
"R 178,191,12288\nG 13\nv_add_f32 v66, v66, v98\nv_add_f32 v67, v67, v99\nM 148,164,52\nbuffer_load_dwordx4 v221, s[4:7], s63 offen offset:1024 lds\n"
"G 11\nv_add_f32 v68, v68, v100\nM 36,160,40\nR 180,191,16384\ns_add_u32 s63, s52, 2162688\nv_add_f32 v69, v69, v101\n"
"M 36,164,56\ns_add_u32 m0, s50, 24576\nds_read2st64_b32 v[114:115], v225 offset0:18 offset1:19\nG 11\nv_add_f32 v70, v70, v102\nM 52,160,44\n"
"R 182,191,20480\nv_add_f32 v71, v71, v103\nG 9\nv_add_f32 v72, v72, v104\nM 52,164,60\nds_read2st64_b32 v[116:117], v225 offset0:20 offset1:21\n"
"v_add_f32 v73, v73, v105\nM 136,168,32\nR 160,188,24576\nG 10\nv_add_f32 v74, v74, v106\nv_add_f32 v75, v75, v107\n"
"M 136,172,48\nbuffer_load_dwordx4 v220, s[4:7], s63 offen lds\nG 9\nv_add_f32 v76, v76, v108\nM 152,168,36\nR 162,188,28672\n"
"v_add_f32 v77, v77, v109\nG 8\nv_add_f32 v78, v78, v110\nM 152,172,52\nds_read2st64_b32 v[118:119], v225 offset0:22 offset1:23\nv_add_f32 v79, v79, v111\n"
"M 40,168,40\nR 164,188,32768\nG 9\nv_add_f32 v80, v80, v112\nM 40,172,56\nbuffer_load_dwordx4 v221, s[4:7], s63 offen offset:1024 lds\n"
"M 56,168,44\nR 166,188,36864\ns_add_u32 s63, s52, 2359296\nM 56,172,60\nds_read2st64_b32 v[120:121], v225 offset0:24 offset1:25\nG 7\n"
"v_add_f32 v81, v81, v113\nM 140,176,32\nR 168,189,24576\nv_add_f32 v82, v82, v114\nM 140,180,48\ns_add_u32 m0, s50, 32768\n"
"ds_read2st64_b32 v[122:123], v225 offset0:26 offset1:27\nv_add_f32 v83, v83, v115\nM 156,176,36\nR 170,189,28672\nG 9\nv_add_f32 v84, v84, v116\n"
"M 156,180,52\nbuffer_load_dwordx4 v220, s[4:7], s63 offen lds\nM 44,176,40\nR 172,189,32768\nv_add_f32 v85, v85, v117\nM 44,180,56\n"
"ds_read2st64_b32 v[124:125], v225 offset0:28 offset1:29\nG 8\nv_add_f32 v86, v86, v118\nM 60,176,44\nR 174,189,36864\nv_add_f32 v87, v87, v119\n"
"M 60,180,60\nds_read2st64_b32 v[126:127], v225 offset0:30 offset1:31\nG 8\nG 7\nv_add_f32 v88, v88, v120\nM 128,160,64\n"
"R 176,190,24576\nv_add_f32 v89, v89, v121\nM 128,164,80\nbuffer_load_dwordx4 v221, s[4:7], s63 offen offset:1024 lds\nM 144,160,68\nR 178,190,28672\n"
"s_add_u32 s63, s52, 2555904\nM 144,164,84\ns_add_u32 m0, s50, 40960\nG 7\nv_add_f32 v90, v90, v122\nv_add_f32 v91, v91, v123\n"
"M 32,160,72\nR 180,190,32768\nG 5\nv_add_f32 v92, v92, v124\nM 32,164,88\nbuffer_load_dwordx4 v220, s[4:7], s63 offen lds\n"
"M 48,160,76\nR 182,190,36864\nv_add_f32 v93, v93, v125\nM 48,164,92\nG 5\nG 4\n"
"v_add_f32 v94, v94, v126\nv_add_f32 v95, v95, v127\nM 132,168,64\nR 160,191,24576\nv_mul_f32 v64, v64, v230\nv_mul_f32 v65, v65, v231\n"
"M 132,172,80\ns_lshl4_add_u32 s64, s41, s42\nv_mul_f32 v66, v66, v232\nv_mul_f32 v67, v67, v233\nv_mul_f32 v68, v68, v234\nv_add_u32 v108, 161792, v252\n"
"M 148,168,68\nR 162,191,28672\nds_read_b128 v[112:115], v108\nv_mul_f32 v69, v69, v235\nv_mul_f32 v70, v70, v236\nM 148,172,84\n"
"buffer_load_dwordx4 v221, s[4:7], s63 offen offset:1024 lds\nds_read_b128 v[104:107], v108 offset:1024\nv_mul_f32 v71, v71, v237\nM 36,168,72\nR 164,191,32768\nv_cvt_pk_bf16_f32 v67, v66, v67\n"
"v_cvt_pk_bf16_f32 v66, v64, v65\nM 36,172,88\ns_add_u32 m0, s50, 49152\ns_cmp_eq_u32 s40, 3\ns_cselect_b64 s[66:67], -1, 0\nv_cvt_pk_bf16_f32 v68, v68, v69\n"
"M 52,168,76\nR 166,191,36864\ns_waitcnt vmcnt(9)\ns_barrier\nv_cvt_pk_bf16_f32 v69, v70, v71\nM 52,172,92\n"
"v_mul_f32 v72, v72, v230\nv_mul_f32 v73, v73, v231\nG 6\nv_mul_f32 v74, v74, v232\nM 136,176,64\nR 168,188,40960\n"
"v_mul_f32 v75, v75, v233\nv_mul_f32 v76, v76, v234\nv_mov_b32 v124, s76\nM 136,180,80\ns_add_u32 s63, s52, 2752512\nv_mul_f32 v77, v77, v235\n"
"v_mul_f32 v78, v78, v236\nv_mul_f32 v79, v79, v237\nG 3\nv_lshlrev_b32 v100, 16, v104\nM 152,176,68\nR 170,188,45056\n"
"v_cvt_pk_bf16_f32 v72, v72, v73\nv_cvt_pk_bf16_f32 v73, v74, v75\nv_and_b32 v101, s77, v104\nM 152,180,84\nbuffer_load_dwordx4 v220, s[4:7], s63 offen lds\nv_cvt_pk_bf16_f32 v74, v76, v77\n"
"v_lshlrev_b32 v102, 16, v105\nM 40,176,72\nR 172,188,49152\nv_cvt_pk_bf16_f32 v75, v78, v79\nv_mul_f32 v80, v80, v230\nv_and_b32 v103, s77, v105\n"
"M 40,180,88\ns_lshl_b32 s64, s64, 12\nv_mul_f32 v81, v81, v231\nv_mul_f32 v82, v82, v232\nv_mul_f32 v83, v83, v233\nv_lshlrev_b32 v96, 16, v106\n"
"M 56,176,76\nR 174,188,53248\nv_mul_f32 v84, v84, v234\nv_mul_f32 v85, v85, v235\nv_and_b32 v97, s77, v106\nM 56,180,92\n"
"G 4\nv_mul_f32 v86, v86, v236\nv_mul_f32 v87, v87, v237\nv_cvt_pk_bf16_f32 v78, v80, v81\nv_lshlrev_b32 v98, 16, v107\nM 140,160,64\n"
"R 176,189,40960\nv_cvt_pk_bf16_f32 v79, v82, v83\nv_cvt_pk_bf16_f32 v80, v84, v85\nv_and_b32 v99, s77, v107\nM 140,164,80\ns_add_u32 s64, s64, 78643200\n"
"v_cvt_pk_bf16_f32 v81, v86, v87\nv_mul_f32 v88, v88, v230\nv_mul_f32 v89, v89, v231\nv_add_f32 v104, s78, v100\nM 156,160,68\nR 178,189,45056\n"
"v_mul_f32 v90, v90, v232\nv_mul_f32 v91, v91, v233\nv_add_f32 v105, s78, v101\nM 156,164,84\nbuffer_load_dwordx4 v221, s[4:7], s63 offen offset:1024 lds\nv_mul_f32 v92, v92, v234\n"
"v_add_f32 v106, s78, v102\nM 44,160,72\nR 180,189,49152\nv_mul_f32 v93, v93, v235\nv_mul_f32 v94, v94, v236\nM 44,164,88\n"
"v_mul_f32 v95, v95, v237\nv_cvt_pk_bf16_f32 v84, v88, v89\nv_cvt_pk_bf16_f32 v85, v90, v91\nM 60,160,76\nR 182,189,53248\nv_cvt_pk_bf16_f32 v86, v92, v93\n"
"v_cvt_pk_bf16_f32 v87, v94, v95\nM 60,164,92\ns_mov_b64 exec, s[66:67]\nbuffer_store_dwordx4 v[16:19], v252, s[20:23], s64 offen offset:0 sc1\nbuffer_store_dwordx4 v[20:23], v252, s[20:23], s64 offen offset:1024 sc1\ns_mov_b64 exec, -1\n"
"G 4\nv_lshrrev_b32 v232, 1, v252\nv_add_f32 v107, s78, v103\nM 128,168,96\nR 160,190,40960\ns_mov_b64 exec, s[66:67]\n"
"buffer_store_dwordx2 v[68:69], v232, s[20:23], s64 offen offset:2048 sc1\nbuffer_store_dwordx2 v[74:75], v232, s[20:23], s64 offen offset:2560 sc1\ns_mov_b64 exec, -1\nv_mov_b32 v125, s76\nM 128,172,112\ns_mov_b64 exec, s[66:67]\n"
"buffer_store_dwordx2 v[80:81], v232, s[20:23], s64 offen offset:3072 sc1\nbuffer_store_dwordx2 v[86:87], v232, s[20:23], s64 offen offset:3584 sc1\ns_mov_b64 exec, -1\nv_mov_b32 v126, s76\nv_mov_b32 v127, s76\nM 144,168,100\n"
"R 162,190,45056\ns_lshl_b32 s60, s40, 12\ns_add_u32 s60, s60, 102400\nv_add_u32 v230, s60, v252\nv_add_u32 v231, s60, v232\nM 144,172,116\n"
"s_add_u32 m0, s50, 57344\ns_not_b64 exec, s[66:67]\nds_write_b128 v230, v[16:19] offset:0\nds_write_b128 v230, v[20:23] offset:1024\ns_mov_b64 exec, -1\nM 32,168,104\n"
"R 164,190,49152\ns_add_u32 s63, s52, 2949120\nM 32,172,120\nbuffer_store_dwordx4 v[248:251], v252, s[20:23], s100 offen offset:2048 sc1\nM 48,168,108\nR 166,190,53248\n"
"M 48,172,124\nG 6\nM 132,176,96\nR 168,191,40960\nM 132,180,112\nbuffer_load_dwordx4 v220, s[4:7], s63 offen lds\n"
"M 148,176,100\nR 170,191,45056\nM 148,180,116\nM 36,176,104\nR 172,191,49152\nM 36,180,120\n"
"M 52,176,108\nR 174,191,53248\nM 52,180,124\nG 4\nM 136,160,96\nR 176,184,0\n"
"M 136,164,112\nbuffer_load_dwordx4 v221, s[4:7], s63 offen offset:1024 lds\nM 152,160,100\nR 178,184,4096\nM 152,164,116\ns_not_b64 exec, s[66:67]\n"
"ds_write_b64 v231, v[68:69] offset:2048\nds_write_b64 v231, v[74:75] offset:2560\ns_mov_b64 exec, -1\nM 40,160,104\nR 180,184,8192\nM 40,164,120\n"
"M 56,160,108\nR 182,184,12288\nM 56,164,124\nG 6\nM 140,168,96\nR 160,185,0\n"
"M 140,172,112\ns_movk_i32 s63, 2048\nv_mad_u32_u24 v220, v228, s63, v229\nv_add_u32 v221, 1047552, v220\ns_add_u32 m0, s50, 81920\ns_add_u32 s63, s53, 0\n"
"buffer_load_dwordx4 v220, s[8:11], s63 offen lds\nM 156,168,100\nR 162,185,4096\ns_not_b64 exec, s[66:67]\nds_write_b64 v231, v[80:81] offset:3072\nds_write_b64 v231, v[86:87] offset:3584\n"
"s_mov_b64 exec, -1\nM 156,172,116\nM 44,168,104\nR 164,185,8192\nM 44,172,120\nM 60,168,108\n"
"R 166,185,12288\nM 60,172,124\nG 6\nM 128,176,128\nR 168,186,0\nM 128,180,144\n"
"buffer_load_dwordx4 v221, s[8:11], s63 offen offset:1024 lds\nM 144,176,132\nR 170,186,4096\nM 144,180,148\ns_add_u32 m0, s50, 90112\nM 32,176,136\n"
"R 172,186,8192\ns_waitcnt vmcnt(15) lgkmcnt(5)\ns_barrier\nM 32,180,152\ns_add_u32 s63, s53, 65536\nv_mov_b32 v230, 0\n"
"v_mov_b32 v231, 0\nM 48,176,140\nR 174,186,12288\nv_mov_b32 v232, 0\nM 48,180,156\nG 4\n"
"v_mov_b32 v233, 0\nv_mov_b32 v234, 0\nM 132,160,128\nR 176,187,0\nv_mov_b32 v235, 0\nM 132,164,144\n"
"buffer_load_dwordx4 v220, s[8:11], s63 offen lds\nM 148,160,132\nR 178,187,4096\nv_mov_b32 v236, 0\nM 148,164,148\ns_lshl4_add_u32 s65, s41, s42\n"
"v_mov_b32 v237, 0\nv_mov_b32 v64, 0\nM 36,160,136\nR 180,187,8192\nv_mov_b32 v65, 0\nM 36,164,152\n"
"s_lshl_b32 s65, s65, 12\nv_mov_b32 v70, 0\nv_mov_b32 v71, 0\nM 52,160,140\nR 182,187,12288\nv_mov_b32 v76, 0\n"
"M 52,164,156\nG 4\nv_mov_b32 v77, 0\nv_mov_b32 v82, 0\nM 136,168,128\nR 160,184,16384\n"
"v_mov_b32 v83, 0\nM 136,172,144\nbuffer_load_dwordx4 v221, s[8:11], s63 offen offset:1024 lds\nM 152,168,132\nR 162,184,20480\nv_lshrrev_b32 v227, 1, v252\n"
"M 152,172,148\ns_add_u32 s65, s65, 78577664\nM 40,168,136\nR 164,184,24576\nLq692:\ns_lshr_b32 s60, s45, 3\n"
"s_lshl2_add_u32 s60, s60, s40\ns_lshl_b32 s60, s60, 8\nv_lshl_add_u32 v224, v252, 3, s60\nv_add_u32 v224, Lq692+4096-LqPC, v224\ns_mov_b64 exec, 3\nglobal_load_dword v224, v224, s[98:99]\n"
"s_mov_b64 exec, -1\nM 40,172,152\ns_add_u32 m0, s50, 98304\nM 56,168,140\nR 166,184,28672\nM 56,172,156\n"
"G 4\nM 140,176,128\nR 168,185,16384\nM 140,180,144\ns_add_u32 s63, s53, 131072\nM 156,176,132\n"
"R 170,185,20480\nM 156,180,148\nbuffer_load_dwordx4 v220, s[8:11], s63 offen lds\nM 44,176,136\nR 172,185,24576\nM 44,180,152\n"
"M 60,176,140\nR 174,185,28672\nM 60,180,156\nG 4\nM 128,160,160\nR 176,186,16384\n"
"M 128,164,176\nbuffer_load_dwordx4 v221, s[8:11], s63 offen offset:1024 lds\nM 144,160,164\nR 178,186,20480\nM 144,164,180\ns_lshl_b32 s60, s40, 12\n"
"s_add_u32 s60, s60, 98304\nv_add_u32 v226, s60, v252\nv_add_u32 v111, s60, v227\nM 32,160,168\nR 180,186,24576\ns_mov_b32 s60, 155648\n"
"v_bfe_u32 v246, v252, 4, 4\nv_lshl_add_u32 v246, v246, 3, s60\nM 32,164,184\nbuffer_store_dwordx4 v[248:251], v252, s[20:23], s100 offen offset:3072 sc1\nM 48,160,172\nR 182,186,28672\n"
"ds_read_b64 v[238:239], v246 offset:0\nM 48,164,188\ns_add_u32 s63, s53, 196608\nG 5\nM 132,168,160\nR 160,187,16384\n"
"ds_read_b64 v[240:241], v246 offset:128\nM 132,172,176\ns_add_u32 m0, s50, 106496\ns_cmp_eq_u32 s40, 0\ns_cselect_b64 s[88:89], 0, -1\ns_cselect_b32 s60, s41, 0\n"
"s_cmp_lg_u32 s60, 0\ns_cselect_b64 s[86:87], -1, 0\nM 148,168,164\nR 162,187,20480\nds_read_b64 v[242:243], v246 offset:256\nM 148,172,180\n"
"buffer_load_dwordx4 v220, s[8:11], s63 offen lds\nM 36,168,168\nR 164,187,24576\nds_read_b64 v[244:245], v246 offset:384\nM 36,172,184\ns_mov_b64 exec, s[88:89]\n"
"ds_read_b128 v[230:233], v226 offset:0\nds_read_b128 v[234:237], v226 offset:1024\ns_mov_b64 exec, -1\nM 52,168,172\nR 166,187,28672\nds_read_b64 v[246:247], v246 offset:0\n"
"M 52,172,188\nG 11\nM 136,176,160\nR 168,184,32768\nM 136,180,176\ns_mov_b64 exec, s[88:89]\n"
"ds_read_b64 v[64:65], v111 offset:2048\nds_read_b64 v[70:71], v111 offset:2560\ns_mov_b64 exec, -1\nM 152,176,164\nR 170,184,36864\ns_waitcnt vmcnt(9) lgkmcnt(1)\n"
"s_barrier\nM 152,180,180\nbuffer_load_dwordx4 v221, s[8:11], s63 offen offset:1024 lds\nM 40,176,168\nR 172,184,40960\nv_mfma_f32_16x16x32_bf16 v[128:131], v[238:241], v[208:211], v[128:131]\n"
"s_add_u32 m0, s50, 114688\nM 40,180,184\ns_mov_b64 exec, s[88:89]\nds_read_b64 v[76:77], v111 offset:3072\nds_read_b64 v[82:83], v111 offset:3584\ns_mov_b64 exec, -1\n"
"M 56,176,172\nR 174,184,45056\nv_mfma_f32_16x16x32_bf16 v[132:135], v[240:243], v[208:211], v[132:135]\ns_add_u32 s63, s53, 262144\nM 56,180,188\nM 140,160,160\n"
"R 176,185,32768\nv_mfma_f32_16x16x32_bf16 v[136:139], v[242:245], v[208:211], v[136:139]\nM 140,164,176\nbuffer_load_dwordx4 v220, s[8:11], s63 offen lds\nM 156,160,164\nR 178,185,36864\n"
"v_mfma_f32_16x16x32_bf16 v[140:143], v[244:247], v[208:211], v[140:143]\nM 156,164,180\nM 44,160,168\nR 180,185,40960\nv_mfma_f32_16x16x32_bf16 v[144:147], v[244:247], v[210:213], v[144:147]\nM 44,164,184\n"
"s_mov_b64 exec, s[86:87]\nbuffer_load_dwordx4 v[230:233], v252, s[20:23], s65 offen offset:0 sc1\nbuffer_load_dwordx4 v[234:237], v252, s[20:23], s65 offen offset:1024 sc1\ns_mov_b64 exec, -1\nM 60,160,172\nR 182,185,45056\n"
"v_mfma_f32_16x16x32_bf16 v[148:151], v[238:241], v[210:213], v[148:151]\nM 60,164,188\nG 4\nM 32,168,200\nR 160,186,32768\nv_mfma_f32_16x16x32_bf16 v[152:155], v[240:243], v[210:213], v[152:155]\n"
"M 32,172,216\nR 162,186,36864\nM 48,168,204\nR 164,186,40960\nv_mfma_f32_16x16x32_bf16 v[156:159], v[242:245], v[210:213], v[156:159]\ns_mov_b64 exec, s[86:87]\n"
"buffer_load_dwordx2 v[64:65], v227, s[20:23], s65 offen offset:2048 sc1\nbuffer_load_dwordx2 v[70:71], v227, s[20:23], s65 offen offset:2560 sc1\ns_mov_b64 exec, -1\nM 48,172,220\nR 166,186,45056\nG 4\n"
"M 36,176,200\nR 168,187,32768\nM 36,180,216\nR 170,187,36864\nM 52,176,204\nR 172,187,40960\n"
"M 52,180,220\nR 174,187,45056\nG 4\nM 40,160,200\nR 176,184,49152\ns_mov_b64 exec, s[86:87]\n"
"buffer_load_dwordx2 v[76:77], v227, s[20:23], s65 offen offset:3072 sc1\nbuffer_load_dwordx2 v[82:83], v227, s[20:23], s65 offen offset:3584 sc1\ns_mov_b64 exec, -1\nM 40,164,216\nR 178,184,53248\nM 56,160,204\n"
"R 180,184,57344\nM 56,164,220\nR 182,184,61440\nG 4\nM 44,168,200\nR 160,185,49152\n"
"M 44,172,216\nR 162,185,53248\nM 60,168,204\nR 164,185,57344\nM 60,172,220\nR 166,185,61440\n"
"G 4\nM 32,176,232\nR 168,186,49152\nM 32,180,248\nR 170,186,53248\nM 48,176,236\n"
"R 172,186,57344\nM 48,180,252\nR 174,186,61440\nG 4\nM 36,160,232\nR 176,187,49152\n"
"M 36,164,248\nR 178,187,53248\nM 52,160,236\nR 180,187,57344\nM 52,164,252\nR 182,187,61440\n"
"G 4\nM 40,168,232\nR 160,184,32768\nM 40,172,248\nR 162,184,36864\nM 56,168,236\n"
"R 164,184,40960\nM 56,172,252\nR 166,184,45056\nG 4\nM 44,176,232\nR 168,185,32768\n"
"M 44,180,248\nR 170,185,36864\nM 60,176,236\nR 172,185,40960\nM 60,180,252\nR 174,185,45056\n"
"G 4\nM 128,160,192\nR 176,186,32768\nM 128,164,208\nR 178,186,36864\ns_mov_b32 m0, s51\n"
"M 144,160,196\nR 180,186,40960\nds_write_addtid_b32 v32 offset:0\nM 144,164,212\nR 182,186,45056\nds_write_addtid_b32 v33 offset:256\n"
"G 6\nM 132,168,192\nR 160,187,32768\nds_write_addtid_b32 v34 offset:512\nM 132,172,208\nR 162,187,36864\n"
"ds_write_addtid_b32 v35 offset:768\nM 148,168,196\nR 164,187,40960\nds_write_addtid_b32 v36 offset:1024\nM 148,172,212\nR 166,187,45056\n"
"ds_write_addtid_b32 v37 offset:1280\nG 9\nM 136,176,192\nR 168,184,49152\nds_write_addtid_b32 v38 offset:1536\nM 136,180,208\n"
"R 170,184,53248\nds_write_addtid_b32 v39 offset:1792\nM 152,176,196\nR 172,184,57344\nds_write_addtid_b32 v40 offset:2048\nM 152,180,212\n"
"R 174,184,61440\nds_write_addtid_b32 v41 offset:2304\nG 9\nM 140,160,192\nR 176,185,49152\nds_write_addtid_b32 v42 offset:2560\n"
"M 140,164,208\nR 178,185,53248\nds_write_addtid_b32 v43 offset:2816\nM 156,160,196\nR 180,185,57344\nds_write_addtid_b32 v44 offset:3072\n"
"ds_write_addtid_b32 v45 offset:3328\nds_write_addtid_b32 v46 offset:3584\nds_write_addtid_b32 v47 offset:3840\nds_write_addtid_b32 v48 offset:4096\nds_write_addtid_b32 v49 offset:4352\nds_write_addtid_b32 v50 offset:4608\n"
"ds_write_addtid_b32 v51 offset:4864\nds_write_addtid_b32 v52 offset:5120\nds_write_addtid_b32 v53 offset:5376\nds_write_addtid_b32 v54 offset:5632\nds_write_addtid_b32 v55 offset:5888\nds_write_addtid_b32 v56 offset:6144\n"
"ds_write_addtid_b32 v57 offset:6400\nds_write_addtid_b32 v58 offset:6656\nds_write_addtid_b32 v59 offset:6912\nds_write_addtid_b32 v60 offset:7168\nds_write_addtid_b32 v61 offset:7424\nds_write_addtid_b32 v62 offset:7680\n"
"ds_write_addtid_b32 v63 offset:7936\nM 156,164,212\nR 182,185,61440\ns_add_u32 m0, s50, 114688\ns_nop 0\nbuffer_load_dwordx4 v221, s[8:11], s63 offen offset:1024 lds\n"
"G 15\nM 128,168,224\nR 160,186,49152\ns_add_u32 m0, s50, 0\ns_add_u32 s63, s53, 327680\nbuffer_load_dwordx4 v220, s[8:11], s63 offen lds\n"
"M 128,172,240\nR 162,186,53248\nG 3\ns_barrier\nM 144,168,228\nR 164,186,57344\n"
"buffer_load_dwordx4 v221, s[8:11], s63 offen offset:1024 lds\nM 144,172,244\nR 166,186,61440\nG 4\nM 132,176,224\nR 168,187,49152\n"
"M 132,180,240\nR 170,187,53248\nds_read2st64_b32 v[32:33], v225 offset0:0 offset1:1\nM 148,176,228\nR 172,187,57344\nds_read2st64_b32 v[34:35], v225 offset0:2 offset1:3\n"
"M 148,180,244\nR 174,187,61440\nG 6\nM 136,160,224\ns_add_u32 m0, s50, 8192\nds_read2st64_b32 v[36:37], v225 offset0:4 offset1:5\n"
"ds_read2st64_b32 v[38:39], v225 offset0:6 offset1:7\nM 136,164,240\ns_add_u32 s63, s53, 393216\nds_read2st64_b32 v[40:41], v225 offset0:8 offset1:9\nds_read2st64_b32 v[42:43], v225 offset0:10 offset1:11\nM 152,160,228\n"
"ds_read2st64_b32 v[44:45], v225 offset0:12 offset1:13\nds_read2st64_b32 v[46:47], v225 offset0:14 offset1:15\nM 152,164,244\nds_read2st64_b32 v[48:49], v225 offset0:16 offset1:17\nds_read2st64_b32 v[50:51], v225 offset0:18 offset1:19\nG 8\n"
"M 140,168,224\nbuffer_load_dwordx4 v220, s[8:11], s63 offen lds\nM 140,172,240\nds_read2st64_b32 v[52:53], v225 offset0:20 offset1:21\nds_read2st64_b32 v[54:55], v225 offset0:22 offset1:23\nM 156,168,228\n"
"ds_read2st64_b32 v[56:57], v225 offset0:24 offset1:25\nds_read2st64_b32 v[58:59], v225 offset0:26 offset1:27\nM 156,172,244\nds_read2st64_b32 v[60:61], v225 offset0:28 offset1:29\nds_read2st64_b32 v[62:63], v225 offset0:30 offset1:31\ns_waitcnt vmcnt(7)\n"
"v_max3_u32 v248, v233, v237, v65\ns_waitcnt vmcnt(5)\nv_max3_u32 v248, v248, v71, v77\ns_waitcnt vmcnt(4)\nv_max_u32 v248, v248, v83\nv_cmp_eq_u32 vcc, -1, v248\n"
"s_nop 1\ns_cbranch_vccnz Lq674\nLq675:\nG 7\nv_pk_add_f32 v[144:145], v[144:145], v[48:49]\nG 6\n"
"v_pk_add_f32 v[146:147], v[146:147], v[50:51]\nG 5\nv_pk_add_f32 v[148:149], v[148:149], v[52:53]\nG 4\nv_pk_add_f32 v[150:151], v[150:151], v[54:55]\nv_cvt_pk_bf16_f32 v144, v144, v145\n"
"v_cvt_pk_bf16_f32 v145, v146, v147\nv_cvt_pk_bf16_f32 v146, v148, v149\nv_cvt_pk_bf16_f32 v147, v150, v151\ns_nop 1\nv_mfma_f32_16x16x32_bf16 v[52:55], v[16:19], v[144:147], v[96:99]\nv_mfma_f32_16x16x32_bf16 v[48:51], v[0:3], v[144:147], v[100:103]\n"
"G 3\nv_pk_add_f32 v[152:153], v[152:153], v[56:57]\nG 2\nv_pk_add_f32 v[154:155], v[154:155], v[58:59]\nG 1\nv_pk_add_f32 v[156:157], v[156:157], v[60:61]\n"
"G 0\nv_pk_add_f32 v[158:159], v[158:159], v[62:63]\nv_cvt_pk_bf16_f32 v152, v152, v153\nv_cvt_pk_bf16_f32 v153, v154, v155\nv_cvt_pk_bf16_f32 v154, v156, v157\nv_cvt_pk_bf16_f32 v155, v158, v159\n"
"s_nop 1\nv_mfma_f32_16x16x32_bf16 v[60:63], v[20:23], v[152:155], v[96:99]\nv_mfma_f32_16x16x32_bf16 v[56:59], v[4:7], v[152:155], v[100:103]\nv_pk_add_f32 v[128:129], v[128:129], v[32:33]\nv_pk_add_f32 v[130:131], v[130:131], v[34:35]\nv_pk_add_f32 v[132:133], v[132:133], v[36:37]\n"
"v_pk_add_f32 v[134:135], v[134:135], v[38:39]\nv_cvt_pk_bf16_f32 v128, v128, v129\nv_cvt_pk_bf16_f32 v129, v130, v131\nv_cvt_pk_bf16_f32 v130, v132, v133\nv_cvt_pk_bf16_f32 v131, v134, v135\ns_nop 1\n"
"v_mfma_f32_16x16x32_bf16 v[36:39], v[0:3], v[128:131], v[96:99]\nv_mfma_f32_16x16x32_bf16 v[32:35], v[230:233], v[128:131], v[104:107]\nv_pk_add_f32 v[136:137], v[136:137], v[40:41]\nv_pk_add_f32 v[138:139], v[138:139], v[42:43]\nv_pk_add_f32 v[140:141], v[140:141], v[44:45]\nv_pk_add_f32 v[142:143], v[142:143], v[46:47]\n"
"v_cvt_pk_bf16_f32 v136, v136, v137\nv_cvt_pk_bf16_f32 v137, v138, v139\nv_cvt_pk_bf16_f32 v138, v140, v141\nv_cvt_pk_bf16_f32 v139, v142, v143\ns_nop 1\nv_mfma_f32_16x16x32_bf16 v[44:47], v[4:7], v[136:139], v[96:99]\n"
"v_mfma_f32_16x16x32_bf16 v[40:43], v[234:237], v[136:139], v[104:107]\nv_max3_f32 v52, v52, v53, v48\nv_max3_f32 v60, v60, v61, v56\nv_max3_f32 v36, v36, v37, v32\ns_nop 4\nv_max3_f32 v44, v44, v45, v40\n"
"v_max_f32 v52, v52, v49\nv_max_f32 v60, v60, v57\nv_max_f32 v36, v36, v33\nv_max_f32 v44, v44, v41\nv_max3_f32 v54, v54, v55, v50\nv_max3_f32 v62, v62, v63, v58\n"
"v_max3_f32 v38, v38, v39, v34\nv_max3_f32 v46, v46, v47, v42\nv_max_f32 v54, v54, v51\nv_max_f32 v62, v62, v59\nv_max_f32 v38, v38, v35\nv_max_f32 v46, v46, v43\n"
"v_max_f32 v118, v52, v54\nv_max_f32 v119, v60, v62\nv_max_f32 v116, v36, v38\nv_max_f32 v117, v44, v46\nv_mov_b32 v120, v116\nv_mov_b32 v121, v117\n"
"v_mov_b32 v122, v118\nv_mov_b32 v123, v119\nv_permlane32_swap_b32 v120, v116\nv_permlane32_swap_b32 v121, v117\nv_permlane32_swap_b32 v122, v118\nv_permlane32_swap_b32 v123, v119\n"
"v_max_f32 v116, v116, v120\nv_max_f32 v117, v117, v121\nv_max_f32 v118, v118, v122\nv_max_f32 v119, v119, v123\nv_mov_b32 v120, v116\nv_mov_b32 v121, v117\n"
"v_mov_b32 v122, v118\nv_mov_b32 v123, v119\nv_permlane16_swap_b32 v120, v116\nv_permlane16_swap_b32 v121, v117\nv_permlane16_swap_b32 v122, v118\nv_permlane16_swap_b32 v123, v119\n"
"v_max_f32 v116, v116, v120\nv_max_f32 v117, v117, v121\nv_max_f32 v118, v118, v122\nv_max_f32 v119, v119, v123\nv_mul_f32 v118, v118, v218\nv_mul_f32 v119, v119, v218\n"
"v_mul_f32 v116, v116, v215\nv_mul_f32 v117, v117, v215\nv_fma_f32 v52, v52, v218, -v118\nv_fma_f32 v54, v54, v218, -v118\nv_fma_f32 v60, v60, v218, -v119\nv_fma_f32 v62, v62, v218, -v119\n"
"v_fma_f32 v36, v36, v215, -v116\nv_fma_f32 v38, v38, v215, -v116\nv_fma_f32 v44, v44, v215, -v117\nv_fma_f32 v46, v46, v215, -v117\nv_exp_f32 v52, v52\nv_exp_f32 v54, v54\n"
"v_exp_f32 v60, v60\nv_exp_f32 v62, v62\nv_exp_f32 v36, v36\nv_exp_f32 v38, v38\nv_exp_f32 v44, v44\nv_exp_f32 v46, v46\n"
"v_cvt_pk_bf16_f32 v52, v52, v52\nv_cvt_pk_bf16_f32 v54, v54, v54\nv_and_b32 v48, v52, v112\nv_and_b32 v49, v54, v113\nv_and_b32 v50, v52, v114\nv_and_b32 v51, v54, v115\n"
"s_nop 1\nv_mfma_f32_16x16x32_bf16 v[24:27], v[124:127], v[48:51], 0\nv_mfma_f32_16x16x32_bf16 v[144:147], v[66:69], v[48:51], 0\nv_mfma_f32_16x16x32_bf16 v[148:151], v[72:75], v[48:51], 0\nv_cvt_pk_bf16_f32 v60, v60, v60\nv_cvt_pk_bf16_f32 v62, v62, v62\n"
"v_and_b32 v56, v60, v112\nv_and_b32 v57, v62, v113\nv_and_b32 v58, v60, v114\nv_and_b32 v59, v62, v115\ns_nop 1\nv_mfma_f32_16x16x32_bf16 v[28:31], v[124:127], v[56:59], 0\n"
"v_mfma_f32_16x16x32_bf16 v[152:155], v[78:81], v[56:59], 0\nv_mfma_f32_16x16x32_bf16 v[156:159], v[84:87], v[56:59], 0\nv_cvt_pk_bf16_f32 v36, v36, v36\nv_cvt_pk_bf16_f32 v38, v38, v38\nv_and_b32 v32, v36, v112\nv_and_b32 v33, v38, v113\n"
"v_and_b32 v34, v36, v114\nv_and_b32 v35, v38, v115\ns_nop 1\nv_mfma_f32_16x16x32_bf16 v[8:11], v[124:127], v[32:35], 0\nv_mfma_f32_16x16x32_bf16 v[128:131], v[64:67], v[32:35], 0\nv_mfma_f32_16x16x32_bf16 v[132:135], v[70:73], v[32:35], 0\n"
"v_cvt_pk_bf16_f32 v44, v44, v44\nv_cvt_pk_bf16_f32 v46, v46, v46\nv_and_b32 v40, v44, v112\nv_and_b32 v41, v46, v113\nv_and_b32 v42, v44, v114\nv_and_b32 v43, v46, v115\n"
"s_nop 1\nv_mfma_f32_16x16x32_bf16 v[12:15], v[124:127], v[40:43], 0\nv_mfma_f32_16x16x32_bf16 v[136:139], v[76:79], v[40:43], 0\nv_mfma_f32_16x16x32_bf16 v[140:143], v[82:85], v[40:43], 0\ns_lshl_b32 s84, s42, 11\ns_add_u32 s84, s84, s49\n"
"s_add_u32 s84, s84, 6291456\ns_add_u32 s85, s84, 32768\nv_mov_b32 v238, s46\nv_mov_b32 v239, s47\nv_rcp_f32 v24, v24\nv_rcp_f32 v28, v28\n"
"v_rcp_f32 v8, v8\nv_rcp_f32 v12, v12\nv_pk_mul_f32 v[144:145], v[144:145], v[24:25] op_sel_hi:[1,0]\nv_pk_mul_f32 v[146:147], v[146:147], v[24:25] op_sel_hi:[1,0]\nv_pk_mul_f32 v[148:149], v[148:149], v[24:25] op_sel_hi:[1,0]\nv_pk_mul_f32 v[150:151], v[150:151], v[24:25] op_sel_hi:[1,0]\n"
"v_cvt_pk_bf16_f32 v144, v144, v145\nv_cvt_pk_bf16_f32 v145, v146, v147\nv_cvt_pk_bf16_f32 v146, v148, v149\nv_cvt_pk_bf16_f32 v147, v150, v151\nbuffer_store_dwordx4 v[144:147], v252, s[20:23], s85 offen offset:0 sc1\nv_pk_mul_f32 v[152:153], v[152:153], v[28:29] op_sel_hi:[1,0]\n"
"v_pk_mul_f32 v[154:155], v[154:155], v[28:29] op_sel_hi:[1,0]\nv_pk_mul_f32 v[156:157], v[156:157], v[28:29] op_sel_hi:[1,0]\nv_pk_mul_f32 v[158:159], v[158:159], v[28:29] op_sel_hi:[1,0]\nv_cvt_pk_bf16_f32 v152, v152, v153\nv_cvt_pk_bf16_f32 v153, v154, v155\nv_cvt_pk_bf16_f32 v154, v156, v157\n"
"v_cvt_pk_bf16_f32 v155, v158, v159\nbuffer_store_dwordx4 v[152:155], v252, s[20:23], s85 offen offset:1024 sc1\nv_pk_mul_f32 v[128:129], v[128:129], v[8:9] op_sel_hi:[1,0]\nv_pk_mul_f32 v[130:131], v[130:131], v[8:9] op_sel_hi:[1,0]\nv_pk_mul_f32 v[132:133], v[132:133], v[8:9] op_sel_hi:[1,0]\nv_pk_mul_f32 v[134:135], v[134:135], v[8:9] op_sel_hi:[1,0]\n"
"v_cvt_pk_bf16_f32 v128, v128, v129\nv_cvt_pk_bf16_f32 v129, v130, v131\nv_cvt_pk_bf16_f32 v130, v132, v133\nv_cvt_pk_bf16_f32 v131, v134, v135\nbuffer_store_dwordx4 v[128:131], v252, s[20:23], s84 offen offset:0 sc1\nv_pk_mul_f32 v[136:137], v[136:137], v[12:13] op_sel_hi:[1,0]\n"
"v_pk_mul_f32 v[138:139], v[138:139], v[12:13] op_sel_hi:[1,0]\nv_pk_mul_f32 v[140:141], v[140:141], v[12:13] op_sel_hi:[1,0]\nv_pk_mul_f32 v[142:143], v[142:143], v[12:13] op_sel_hi:[1,0]\nv_cvt_pk_bf16_f32 v136, v136, v137\nv_cvt_pk_bf16_f32 v137, v138, v139\nv_cvt_pk_bf16_f32 v138, v140, v141\n"
"v_cvt_pk_bf16_f32 v139, v142, v143\nbuffer_store_dwordx4 v[136:139], v252, s[20:23], s84 offen offset:1024 sc1\ns_add_u32 s61, s79, 88498176\ns_lshl_b32 s62, s42, 3\ns_add_u32 s61, s61, s62\ns_waitcnt vmcnt(0)\n"
"s_mov_b64 exec, 1\nbuffer_atomic_swap_x2 v[238:239], off, s[20:23], s61 sc1\ns_mov_b64 exec, -1\ns_barrier\nv_bfe_u32 v250, v252, 7, 1\nv_mul_u32_u24 v250, 2176, v250\n"
"v_bfe_u32 v251, v252, 4, 3\nv_lshl_add_u32 v250, v251, 3, v250\ns_lshl_b32 s60, s90, 6\nv_add_u32 v250, s60, v250\nbuffer_load_dwordx4 v221, s[8:11], s63 offen offset:1024 lds\ns_add_u32 m0, s50, 16384\n"
"s_add_u32 s63, s53, 458752\nbuffer_load_dwordx4 v220, s[8:11], s63 offen lds\nbuffer_load_dwordx4 v221, s[8:11], s63 offen offset:1024 lds\ns_add_u32 m0, s50, 24576\ns_add_u32 s63, s53, 524288\nbuffer_load_dwordx4 v220, s[8:11], s63 offen lds\n"
"buffer_load_dwordx4 v221, s[8:11], s63 offen offset:1024 lds\ns_add_u32 m0, s50, 32768\ns_add_u32 s63, s53, 589824\nbuffer_load_dwordx4 v220, s[8:11], s63 offen lds\nbuffer_load_dwordx4 v221, s[8:11], s63 offen offset:1024 lds\ns_add_u32 m0, s50, 40960\n"
"s_add_u32 s63, s53, 655360\nbuffer_load_dwordx4 v220, s[8:11], s63 offen lds\nbuffer_load_dwordx4 v221, s[8:11], s63 offen offset:1024 lds\ns_add_u32 m0, s50, 49152\ns_add_u32 s63, s53, 720896\nbuffer_load_dwordx4 v220, s[8:11], s63 offen lds\n"
"v_mov_b32 v248, 0\nv_mov_b32 v249, 0\ns_add_u32 s61, s80, 88498176\ns_mov_b64 exec, 0xffff\nbuffer_atomic_or_x2 v[248:249], v250, s[20:23], s61 offen sc0 sc1\ns_mov_b64 exec, -1\n"
"v_bfe_u32 v234, v252, 4, 4\nv_lshrrev_b32 v235, 8, v252\nv_lshlrev_b32 v235, 2, v235\nv_sub_u32 v234, v234, v235\nv_lshlrev_b32 v235, 4, v234\ns_mov_b64 s[60:61], 0x3f80\n"
"v_lshlrev_b64 v[208:209], v235, s[60:61]\nv_cmp_gt_u32 s[60:61], 4, v234\ns_nop 1\nv_cndmask_b32 v212, 0, v208, s[60:61]\nv_cndmask_b32 v208, 0, v208, s[60:61]\nv_cndmask_b32 v213, 0, v209, s[60:61]\n"
"v_cndmask_b32 v209, 0, v209, s[60:61]\ns_nop 1\nv_mfma_f32_16x16x32_bf16 v[0:3], v[208:211], v[192:195], 0\nv_mfma_f32_16x16x32_bf16 v[4:7], v[210:213], v[192:195], 0\nv_mfma_f32_16x16x32_bf16 v[8:11], v[208:211], v[196:199], 0\nv_mfma_f32_16x16x32_bf16 v[12:15], v[210:213], v[196:199], 0\n"
"v_mfma_f32_16x16x32_bf16 v[16:19], v[208:211], v[200:203], 0\nv_mfma_f32_16x16x32_bf16 v[20:23], v[210:213], v[200:203], 0\nv_mfma_f32_16x16x32_bf16 v[24:27], v[208:211], v[204:207], 0\nv_mfma_f32_16x16x32_bf16 v[28:31], v[210:213], v[204:207], 0\ns_waitcnt vmcnt(0)\nv_cmp_eq_u64 s[86:87], s[46:47], v[248:249]\n"
"s_nop 0\ns_and_b32 s62, s86, 0xffff\ns_cmp_eq_u32 s62, 0xffff\ns_cbranch_scc0 Lq695\nLq696:\ns_add_u32 s64, s70, 6291456\n"
"buffer_load_dwordx4 a[0:3], v252, s[20:23], s64 offen offset:0 sc1\ns_add_u32 s65, s70, 6324224\nbuffer_load_dwordx4 a[4:7], v252, s[20:23], s65 offen offset:0 sc1\ns_add_u32 s66, s71, 6291456\nbuffer_load_dwordx4 a[8:11], v252, s[20:23], s66 offen offset:0 sc1\ns_add_u32 s67, s71, 6324224\n"
"buffer_load_dwordx4 a[12:15], v252, s[20:23], s67 offen offset:0 sc1\nbuffer_load_dwordx4 a[16:19], v252, s[20:23], s64 offen offset:1024 sc1\nbuffer_load_dwordx4 a[20:23], v252, s[20:23], s65 offen offset:1024 sc1\nbuffer_load_dwordx4 a[24:27], v252, s[20:23], s66 offen offset:1024 sc1\nbuffer_load_dwordx4 a[28:31], v252, s[20:23], s67 offen offset:1024 sc1\ns_barrier\n"
"R 160,188,16384\nR 162,188,20480\nR 164,188,24576\nR 166,188,28672\nR 168,189,16384\nR 170,189,20480\n"
"R 172,189,24576\nR 174,189,28672\nR 176,190,16384\nR 178,190,20480\nR 180,190,24576\nR 182,190,28672\n"
"buffer_load_dwordx4 a[32:35], v252, s[20:23], s64 offen offset:2048 sc1\nbuffer_load_dwordx4 a[36:39], v252, s[20:23], s65 offen offset:2048 sc1\nbuffer_load_dwordx4 a[40:43], v252, s[20:23], s66 offen offset:2048 sc1\nbuffer_load_dwordx4 a[44:47], v252, s[20:23], s67 offen offset:2048 sc1\nbuffer_load_dwordx4 a[48:51], v252, s[20:23], s64 offen offset:3072 sc1\nbuffer_load_dwordx4 a[52:55], v252, s[20:23], s65 offen offset:3072 sc1\n"
"buffer_load_dwordx4 a[56:59], v252, s[20:23], s66 offen offset:3072 sc1\nbuffer_load_dwordx4 a[60:63], v252, s[20:23], s67 offen offset:3072 sc1\ns_add_u32 s64, s70, 6295552\nbuffer_load_dwordx4 a[64:67], v252, s[20:23], s64 offen offset:0 sc1\ns_add_u32 s65, s70, 6328320\nbuffer_load_dwordx4 a[68:71], v252, s[20:23], s65 offen offset:0 sc1\n"
"s_add_u32 s66, s71, 6295552\nbuffer_load_dwordx4 a[72:75], v252, s[20:23], s66 offen offset:0 sc1\ns_add_u32 s67, s71, 6328320\nbuffer_load_dwordx4 a[76:79], v252, s[20:23], s67 offen offset:0 sc1\ns_waitcnt vmcnt(19) lgkmcnt(10)\nM 0,160,0\n"
"buffer_load_dwordx4 a[80:83], v252, s[20:23], s64 offen offset:1024 sc1\ns_waitcnt vmcnt(16) lgkmcnt(8)\nM 0,164,16\nM 16,160,4\ns_waitcnt vmcnt(15)\nM 16,164,20\n"
"v_mfma_f32_16x16x32_bf16 v[32:35], v[160:163], a[8:11], 0\nbuffer_load_dwordx4 a[84:87], v252, s[20:23], s65 offen offset:1024 sc1\ns_waitcnt vmcnt(15)\nM 32,164,24\nv_mfma_f32_16x16x32_bf16 v[48:51], v[160:163], a[12:15], 0\ns_waitcnt vmcnt(14)\n"
"M 48,164,28\nG 4\nM 4,168,0\nR 160,191,16384\nM 4,172,16\nbuffer_load_dwordx4 a[88:91], v252, s[20:23], s66 offen offset:1024 sc1\n"
"M 20,168,4\nR 162,191,20480\nM 20,172,20\nv_mfma_f32_16x16x32_bf16 v[36:39], v[168:171], a[8:11], 0\nR 164,191,24576\nM 36,172,24\n"
"buffer_load_dwordx4 a[92:95], v252, s[20:23], s67 offen offset:1024 sc1\nv_mfma_f32_16x16x32_bf16 v[52:55], v[168:171], a[12:15], 0\nR 166,191,28672\nM 52,172,28\nG 4\nM 8,176,0\n"
"R 168,188,32768\nM 8,180,16\nbuffer_load_dwordx4 a[96:99], v252, s[20:23], s64 offen offset:2048 sc1\nM 24,176,4\nR 170,188,36864\nM 24,180,20\n"
"v_mfma_f32_16x16x32_bf16 v[40:43], v[176:179], a[8:11], 0\nR 172,188,40960\nM 40,180,24\nbuffer_load_dwordx4 a[100:103], v252, s[20:23], s65 offen offset:2048 sc1\nv_mfma_f32_16x16x32_bf16 v[56:59], v[176:179], a[12:15], 0\nR 174,188,45056\n"
"M 56,180,28\nG 4\nM 12,160,0\nR 176,189,32768\nM 12,164,16\nbuffer_load_dwordx4 a[104:107], v252, s[20:23], s66 offen offset:2048 sc1\n"
"M 28,160,4\nR 178,189,36864\nM 28,164,20\nv_mfma_f32_16x16x32_bf16 v[44:47], v[160:163], a[8:11], 0\nR 180,189,40960\nM 44,164,24\n"
"buffer_load_dwordx4 a[108:111], v252, s[20:23], s67 offen offset:2048 sc1\nv_mfma_f32_16x16x32_bf16 v[60:63], v[160:163], a[12:15], 0\nR 182,189,45056\nM 60,164,28\nG 4\ns_waitcnt vmcnt(19)\n"
"M 0,168,32\nR 160,190,32768\ns_waitcnt vmcnt(15)\nM 0,172,48\nbuffer_load_dwordx4 a[112:115], v252, s[20:23], s64 offen offset:3072 sc1\nM 16,168,36\n"
"R 162,190,36864\ns_waitcnt vmcnt(15)\nM 16,172,52\nM 32,168,40\nR 164,190,40960\ns_waitcnt vmcnt(14)\n"
"M 32,172,56\nbuffer_load_dwordx4 a[116:119], v252, s[20:23], s65 offen offset:3072 sc1\nM 48,168,44\nR 166,190,45056\ns_waitcnt vmcnt(14)\nM 48,172,60\n"
"G 4\nM 4,176,32\nR 168,191,32768\nM 4,180,48\nbuffer_load_dwordx4 a[120:123], v252, s[20:23], s66 offen offset:3072 sc1\nM 20,176,36\n"
"R 170,191,36864\nM 20,180,52\nM 36,176,40\nR 172,191,40960\nM 36,180,56\nbuffer_load_dwordx4 a[124:127], v252, s[20:23], s67 offen offset:3072 sc1\n"
"M 52,176,44\nR 174,191,45056\nM 52,180,60\nG 4\nM 8,160,32\nR 176,188,49152\n"
"M 8,164,48\ns_add_u32 s64, s70, 6299648\nbuffer_load_dwordx4 a[128:131], v252, s[20:23], s64 offen offset:0 sc1\nM 24,160,36\nR 178,188,53248\nM 24,164,52\n"
"M 40,160,40\nR 180,184,0\nM 40,164,56\ns_add_u32 s65, s70, 6332416\nbuffer_load_dwordx4 a[132:135], v252, s[20:23], s65 offen offset:0 sc1\nM 56,160,44\n"
"R 182,184,4096\nM 56,164,60\nG 4\nM 12,168,32\nR 160,189,49152\nM 12,172,48\n"
"s_add_u32 s66, s71, 6299648\nbuffer_load_dwordx4 a[136:139], v252, s[20:23], s66 offen offset:0 sc1\nM 28,168,36\nR 162,189,53248\nM 28,172,52\nM 44,168,40\n"
"R 164,185,0\nM 44,172,56\ns_add_u32 s67, s71, 6332416\nbuffer_load_dwordx4 a[140:143], v252, s[20:23], s67 offen offset:0 sc1\nM 60,168,44\nR 166,185,4096\n"
"M 60,172,60\ns_barrier\nG 4\ns_waitcnt vmcnt(19)\nM 0,176,64\nR 168,190,49152\n"
"s_waitcnt vmcnt(15)\nM 0,180,80\nbuffer_load_dwordx4 a[144:147], v252, s[20:23], s64 offen offset:1024 sc1\nM 16,176,68\nR 170,190,53248\ns_waitcnt vmcnt(15)\n"
"M 16,180,84\nM 32,176,72\nR 172,186,0\ns_waitcnt vmcnt(14)\nM 32,180,88\nbuffer_load_dwordx4 a[148:151], v252, s[20:23], s65 offen offset:1024 sc1\n"
"M 48,176,76\nR 174,186,4096\ns_waitcnt vmcnt(14)\nM 48,180,92\nG 4\nM 4,160,64\n"
"R 176,191,49152\nM 4,164,80\nbuffer_load_dwordx4 a[152:155], v252, s[20:23], s66 offen offset:1024 sc1\nM 20,160,68\nR 178,191,53248\nM 20,164,84\n"
"M 36,160,72\nR 180,187,0\nM 36,164,88\nbuffer_load_dwordx4 a[156:159], v252, s[20:23], s67 offen offset:1024 sc1\nM 52,160,76\nR 182,187,4096\n"
"M 52,164,92\nG 4\nM 8,168,64\nR 160,184,8192\nbuffer_load_dwordx4 v221, s[8:11], s63 offen offset:1024 lds\nM 8,172,80\n"
"buffer_load_dwordx4 a[160:163], v252, s[20:23], s64 offen offset:2048 sc1\nM 24,168,68\nR 162,184,12288\nM 24,172,84\nM 40,168,72\nR 164,184,16384\n"
"M 40,172,88\nbuffer_load_dwordx4 a[164:167], v252, s[20:23], s65 offen offset:2048 sc1\nM 56,168,76\nR 166,184,20480\nM 56,172,92\nG 4\n"
"M 12,176,64\nR 168,185,8192\nM 12,180,80\nbuffer_load_dwordx4 a[168:171], v252, s[20:23], s66 offen offset:2048 sc1\nM 28,176,68\nR 170,185,12288\n"
"s_add_u32 s63, s53, 786432\nM 28,180,84\ns_add_u32 m0, s50, 57344\nM 44,176,72\nR 172,185,16384\nM 44,180,88\n"
"buffer_load_dwordx4 a[172:175], v252, s[20:23], s67 offen offset:2048 sc1\nM 60,176,76\nR 174,185,20480\nM 60,180,92\nbuffer_load_dwordx4 v220, s[8:11], s63 offen lds\nG 4\n"
"s_waitcnt vmcnt(21)\nM 0,160,96\nR 176,186,8192\ns_waitcnt vmcnt(17)\nM 0,164,112\nbuffer_load_dwordx4 a[176:179], v252, s[20:23], s64 offen offset:3072 sc1\n"
"M 16,160,100\nR 178,186,12288\ns_waitcnt vmcnt(17)\nM 16,164,116\nbuffer_load_dwordx4 v221, s[8:11], s63 offen offset:1024 lds\nM 32,160,104\n"
"R 180,186,16384\ns_add_u32 m0, s50, 65536\ns_waitcnt vmcnt(17)\nM 32,164,120\nbuffer_load_dwordx4 a[180:183], v252, s[20:23], s65 offen offset:3072 sc1\nM 48,160,108\n"
"R 182,186,20480\ns_add_u32 s63, s53, 851968\ns_waitcnt vmcnt(17)\nM 48,164,124\nbuffer_load_dwordx4 v220, s[8:11], s63 offen lds\nG 4\n"
"M 4,168,96\nR 160,187,8192\nM 4,172,112\nbuffer_load_dwordx4 a[184:187], v252, s[20:23], s66 offen offset:3072 sc1\nM 20,168,100\nR 162,187,12288\n"
"M 20,172,116\nbuffer_load_dwordx4 v221, s[8:11], s63 offen offset:1024 lds\nM 36,168,104\nR 164,187,16384\ns_add_u32 m0, s50, 73728\nM 36,172,120\n"
"buffer_load_dwordx4 a[188:191], v252, s[20:23], s67 offen offset:3072 sc1\nM 52,168,108\nR 166,187,20480\ns_add_u32 s63, s53, 917504\nM 52,172,124\nbuffer_load_dwordx4 v220, s[8:11], s63 offen lds\n"
"G 4\nM 8,176,96\nR 168,184,24576\nM 8,180,112\ns_add_u32 s64, s70, 6303744\nbuffer_load_dwordx4 a[192:195], v252, s[20:23], s64 offen offset:0 sc1\n"
"M 24,176,100\nR 170,184,28672\nM 24,180,116\nbuffer_load_dwordx4 v221, s[8:11], s63 offen offset:1024 lds\nM 40,176,104\nR 172,184,32768\n"
"s_add_u32 m0, s50, 81920\nM 40,180,120\ns_add_u32 s65, s70, 6336512\nbuffer_load_dwordx4 a[196:199], v252, s[20:23], s65 offen offset:0 sc1\nM 56,176,108\nR 174,184,36864\n"
"s_add_u32 s63, s53, 983040\nM 56,180,124\nbuffer_load_dwordx4 v220, s[8:11], s63 offen lds\nG 4\nM 12,160,96\nR 176,185,24576\n"
"M 12,164,112\ns_add_u32 s66, s71, 6303744\nbuffer_load_dwordx4 a[200:203], v252, s[20:23], s66 offen offset:0 sc1\nM 28,160,100\nR 178,185,28672\nM 28,164,116\n"
"buffer_load_dwordx4 v221, s[8:11], s63 offen offset:1024 lds\nM 44,160,104\nR 180,185,32768\nM 44,164,120\ns_add_u32 s67, s71, 6336512\nbuffer_load_dwordx4 a[204:207], v252, s[20:23], s67 offen offset:0 sc1\n"
"M 60,160,108\nR 182,185,36864\nM 60,164,124\ns_waitcnt vmcnt(20)\ns_barrier\nG 4\n"
"M 0,168,128\nR 160,186,24576\nM 0,172,144\nbuffer_load_dwordx4 a[208:211], v252, s[20:23], s64 offen offset:1024 sc1\nM 16,168,132\nR 162,186,28672\n"
"M 16,172,148\nM 32,168,136\nR 164,186,32768\nM 32,172,152\nbuffer_load_dwordx4 a[212:215], v252, s[20:23], s65 offen offset:1024 sc1\nM 48,168,140\n"
"R 166,186,36864\nM 48,172,156\nG 4\nM 4,176,128\nR 168,187,24576\nM 4,180,144\n"
"buffer_load_dwordx4 a[216:219], v252, s[20:23], s66 offen offset:1024 sc1\nM 20,176,132\nR 170,187,28672\nM 20,180,148\nM 36,176,136\nR 172,187,32768\n"
"M 36,180,152\nbuffer_load_dwordx4 a[220:223], v252, s[20:23], s67 offen offset:1024 sc1\nM 52,176,140\nR 174,187,36864\nM 52,180,156\nG 4\n"
"M 8,160,128\nR 176,184,40960\nM 8,164,144\nbuffer_load_dwordx4 a[224:227], v252, s[20:23], s64 offen offset:2048 sc1\nM 24,160,132\nR 178,184,45056\n"
"M 24,164,148\nM 40,160,136\nR 180,184,49152\nM 40,164,152\nbuffer_load_dwordx4 a[228:231], v252, s[20:23], s65 offen offset:2048 sc1\nM 56,160,140\n"
"R 182,184,53248\nM 56,164,156\nG 4\nM 12,168,128\nR 160,185,40960\nM 12,172,144\n"
"buffer_load_dwordx4 a[232:235], v252, s[20:23], s66 offen offset:2048 sc1\nM 28,168,132\nR 162,185,45056\nM 28,172,148\nM 44,168,136\nR 164,185,49152\n"
"M 44,172,152\nbuffer_load_dwordx4 a[236:239], v252, s[20:23], s67 offen offset:2048 sc1\nM 60,168,140\nR 166,185,53248\nM 60,172,156\nG 4\n"
"s_waitcnt vmcnt(27)\nM 0,176,160\nR 168,186,40960\ns_waitcnt vmcnt(22)\nM 0,180,176\nbuffer_load_dwordx4 a[240:243], v252, s[20:23], s64 offen offset:3072 sc1\n"
"M 16,176,164\nR 170,186,45056\ns_waitcnt vmcnt(21)\nM 16,180,180\ns_movk_i32 s63, 7168\nv_mad_u32_u24 v220, v228, s63, v229\n"
"v_add_u32 v221, 3668992, v220\ns_add_u32 m0, s50, 90112\ns_add_u32 s63, s54, 0\nbuffer_load_dwordx4 v220, s[12:15], s63 offen lds\nM 32,176,168\nR 172,186,49152\n"
"s_waitcnt vmcnt(20)\nM 32,180,184\nbuffer_load_dwordx4 a[244:247], v252, s[20:23], s65 offen offset:3072 sc1\nM 48,176,172\nR 174,186,53248\ns_waitcnt vmcnt(19)\n"
"M 48,180,188\nG 4\nM 4,160,160\nR 176,187,40960\nbuffer_load_dwordx4 v221, s[12:15], s63 offen offset:1024 lds\nM 4,164,176\n"
"buffer_load_dwordx4 a[248:251], v252, s[20:23], s66 offen offset:3072 sc1\nM 20,160,164\nR 178,187,45056\ns_waitcnt vmcnt(14)\ns_barrier\ns_add_u32 m0, s50, 98304\n"
"s_add_u32 s63, s54, 229376\nM 20,164,180\nbuffer_load_dwordx4 v220, s[12:15], s63 offen lds\nM 36,160,168\nR 180,187,49152\nM 36,164,184\n"
"buffer_load_dwordx4 a[252:255], v252, s[20:23], s67 offen offset:3072 sc1\nM 52,160,172\nR 182,187,53248\nM 52,164,188\nG 4\nM 8,168,160\n"
"R 160,184,57344\nbuffer_load_dwordx4 v221, s[12:15], s63 offen offset:1024 lds\nM 8,172,176\ns_add_u32 m0, s50, 106496\ns_add_u32 s63, s54, 458752\nbuffer_load_dwordx4 v220, s[12:15], s63 offen lds\n"
"M 24,168,164\nR 162,184,61440\nM 24,172,180\nM 40,168,168\nR 164,188,0\nM 40,172,184\n"
"buffer_load_dwordx4 v221, s[12:15], s63 offen offset:1024 lds\nM 56,168,172\nR 166,188,4096\ns_add_u32 m0, s50, 114688\nM 56,172,188\nG 4\n"
"M 12,176,160\nR 168,185,57344\ns_add_u32 s63, s54, 688128\nM 12,180,176\nbuffer_load_dwordx4 v220, s[12:15], s63 offen lds\nM 28,176,164\n"
"R 170,185,61440\nM 28,180,180\nM 44,176,168\nR 172,189,0\nM 44,180,184\nbuffer_load_dwordx4 v221, s[12:15], s63 offen offset:1024 lds\n"
"M 60,176,172\nR 174,189,4096\ns_add_u32 m0, s50, 0\nM 60,180,188\nG 4\nM 32,160,200\n"
"R 176,186,57344\ns_add_u32 s63, s54, 917504\ns_waitcnt vmcnt(17)\nM 32,164,216\nR 178,186,61440\nM 48,160,204\n"
"R 180,190,0\ns_waitcnt vmcnt(16)\nM 48,164,220\nR 182,190,4096\nG 4\nM 36,168,200\n"
"R 160,187,57344\nbuffer_load_dwordx4 v220, s[12:15], s63 offen lds\nM 36,172,216\nR 162,187,61440\nM 52,168,204\nR 164,191,0\n"
"M 52,172,220\nR 166,191,4096\nG 4\nM 40,176,200\nR 168,188,8192\nbuffer_load_dwordx4 v221, s[12:15], s63 offen offset:1024 lds\n"
"M 40,180,216\nR 170,188,12288\ns_add_u32 m0, s50, 8192\nM 56,176,204\nR 172,188,16384\ns_add_u32 s63, s54, 1146880\n"
"M 56,180,220\nR 174,188,20480\nG 4\nM 44,160,200\nR 176,189,8192\nbuffer_load_dwordx4 v220, s[12:15], s63 offen lds\n"
"M 44,164,216\nR 178,189,12288\nM 60,160,204\nR 180,189,16384\nM 60,164,220\nR 182,189,20480\n"
"G 4\ns_waitcnt vmcnt(16)\nM 32,168,232\nR 160,190,8192\nbuffer_load_dwordx4 v221, s[12:15], s63 offen offset:1024 lds\ns_waitcnt vmcnt(11)\n"
"M 32,172,248\nR 162,190,12288\nM 48,168,236\nR 164,190,16384\ns_waitcnt vmcnt(9)\nM 48,172,252\n"
"R 166,190,20480\nG 4\nM 36,176,232\nR 168,191,8192\nM 36,180,248\nR 170,191,12288\n"
"M 52,176,236\nR 172,191,16384\nM 52,180,252\nR 174,191,20480\nG 4\nM 40,160,232\n"
"R 176,184,57344\nM 40,164,248\nR 178,184,61440\nM 56,160,236\nR 180,188,0\nM 56,164,252\n"
"R 182,188,4096\nG 4\nM 44,168,232\nR 160,185,57344\nM 44,172,248\nR 162,185,61440\n"
"M 60,168,236\nR 164,189,0\nM 60,172,252\nR 166,189,4096\nG 4\nM 0,176,192\n"
"R 168,186,57344\nM 0,180,208\nR 170,186,61440\ns_mov_b32 m0, s51\nM 16,176,196\nR 172,190,0\n"
"ds_write_addtid_b32 v32 offset:0\nM 16,180,212\nR 174,190,4096\nds_write_addtid_b32 v33 offset:256\nG 6\nM 4,160,192\n"
"R 176,187,57344\nds_write_addtid_b32 v34 offset:512\nM 4,164,208\nR 178,187,61440\nds_write_addtid_b32 v35 offset:768\nM 20,160,196\n"
"R 180,191,0\nds_write_addtid_b32 v36 offset:1024\nM 20,164,212\nR 182,191,4096\nds_write_addtid_b32 v37 offset:1280\nG 9\n"
"M 8,168,192\nR 160,188,8192\nds_write_addtid_b32 v38 offset:1536\nM 8,172,208\nR 162,188,12288\nds_write_addtid_b32 v39 offset:1792\n"
"M 24,168,196\nR 164,188,16384\nds_write_addtid_b32 v40 offset:2048\nM 24,172,212\nR 166,188,20480\nds_write_addtid_b32 v41 offset:2304\n"
"G 9\nM 12,176,192\nR 168,189,8192\nds_write_addtid_b32 v42 offset:2560\nM 12,180,208\nR 170,189,12288\n"
"ds_write_addtid_b32 v43 offset:2816\nM 28,176,196\nR 172,189,16384\nds_write_addtid_b32 v44 offset:3072\nds_write_addtid_b32 v45 offset:3328\nds_write_addtid_b32 v46 offset:3584\n"
"ds_write_addtid_b32 v47 offset:3840\nds_write_addtid_b32 v48 offset:4096\nds_write_addtid_b32 v49 offset:4352\nds_write_addtid_b32 v50 offset:4608\nds_write_addtid_b32 v51 offset:4864\nds_write_addtid_b32 v52 offset:5120\n"
"ds_write_addtid_b32 v53 offset:5376\nds_write_addtid_b32 v54 offset:5632\nds_write_addtid_b32 v55 offset:5888\nds_write_addtid_b32 v56 offset:6144\nds_write_addtid_b32 v57 offset:6400\nds_write_addtid_b32 v58 offset:6656\n"
"ds_write_addtid_b32 v59 offset:6912\nds_write_addtid_b32 v60 offset:7168\nds_write_addtid_b32 v61 offset:7424\nds_write_addtid_b32 v62 offset:7680\nds_write_addtid_b32 v63 offset:7936\nM 28,180,212\n"
"R 174,189,20480\nG 15\nM 0,160,224\nR 176,190,8192\ns_add_u32 m0, s50, 16384\nM 0,164,240\n"
"R 178,190,12288\nG 3\ns_barrier\nM 16,160,228\nR 180,190,16384\ns_add_u32 s63, s54, 1376256\n"
"ds_read2st64_b32 v[32:33], v225 offset0:0 offset1:1\nM 16,164,244\nR 182,190,20480\nds_read2st64_b32 v[34:35], v225 offset0:2 offset1:3\nG 6\nM 4,168,224\n"
"R 160,191,8192\nbuffer_load_dwordx4 v220, s[12:15], s63 offen lds\nds_read2st64_b32 v[36:37], v225 offset0:4 offset1:5\nM 4,172,240\nR 162,191,12288\nds_read2st64_b32 v[38:39], v225 offset0:6 offset1:7\n"
"M 20,168,228\nR 164,191,16384\nds_read2st64_b32 v[40:41], v225 offset0:8 offset1:9\nM 20,172,244\nR 166,191,20480\nds_read2st64_b32 v[42:43], v225 offset0:10 offset1:11\n"
"G 9\nM 8,176,224\nbuffer_load_dwordx4 v221, s[12:15], s63 offen offset:1024 lds\nM 8,180,240\ns_add_u32 m0, s50, 24576\ns_add_u32 s63, s54, 1605632\n"
"buffer_load_dwordx4 v220, s[12:15], s63 offen lds\nM 24,176,228\nds_read2st64_b32 v[44:45], v225 offset0:12 offset1:13\nds_read2st64_b32 v[46:47], v225 offset0:14 offset1:15\nM 24,180,244\nds_read2st64_b32 v[48:49], v225 offset0:16 offset1:17\n"
"ds_read2st64_b32 v[50:51], v225 offset0:18 offset1:19\nG 5\nM 12,160,224\nds_read2st64_b32 v[52:53], v225 offset0:20 offset1:21\nds_read2st64_b32 v[54:55], v225 offset0:22 offset1:23\nM 12,164,240\n"
"buffer_load_dwordx4 v221, s[12:15], s63 offen offset:1024 lds\nM 28,160,228\nds_read2st64_b32 v[56:57], v225 offset0:24 offset1:25\nds_read2st64_b32 v[58:59], v225 offset0:26 offset1:27\nM 28,164,244\nds_read2st64_b32 v[60:61], v225 offset0:28 offset1:29\n"
"ds_read2st64_b32 v[62:63], v225 offset0:30 offset1:31\ns_lshl_b32 s84, s75, 11\ns_add_u32 s84, s84, s49\ns_add_u32 s84, s84, 14680064\ns_add_u32 s85, s84, 32768\nv_mov_b32 v238, s46\n"
"v_mov_b32 v239, s47\nv_pk_add_f32 v[0:1], v[0:1], v[32:33]\nv_pk_add_f32 v[2:3], v[2:3], v[34:35]\nv_pk_add_f32 v[4:5], v[4:5], v[36:37]\nv_pk_add_f32 v[6:7], v[6:7], v[38:39]\nv_cvt_pk_bf16_f32 v192, v0, v1\n"
"v_cvt_pk_bf16_f32 v193, v2, v3\nv_cvt_pk_bf16_f32 v194, v4, v5\nv_cvt_pk_bf16_f32 v195, v6, v7\nbuffer_store_dwordx4 v[192:195], v252, s[20:23], s84 offen offset:0 sc1\nv_pk_add_f32 v[8:9], v[8:9], v[40:41]\nG 10\n"
"v_pk_add_f32 v[10:11], v[10:11], v[42:43]\nG 9\nv_pk_add_f32 v[12:13], v[12:13], v[44:45]\nG 8\nv_pk_add_f32 v[14:15], v[14:15], v[46:47]\nv_cvt_pk_bf16_f32 v196, v8, v9\n"
"v_cvt_pk_bf16_f32 v197, v10, v11\nv_cvt_pk_bf16_f32 v198, v12, v13\nv_cvt_pk_bf16_f32 v199, v14, v15\nbuffer_store_dwordx4 v[196:199], v252, s[20:23], s84 offen offset:1024 sc1\nG 7\nv_pk_add_f32 v[16:17], v[16:17], v[48:49]\n"
"G 6\nv_pk_add_f32 v[18:19], v[18:19], v[50:51]\nG 5\nv_pk_add_f32 v[20:21], v[20:21], v[52:53]\nG 4\nv_pk_add_f32 v[22:23], v[22:23], v[54:55]\n"
"v_cvt_pk_bf16_f32 v200, v16, v17\nv_cvt_pk_bf16_f32 v201, v18, v19\nv_cvt_pk_bf16_f32 v202, v20, v21\nv_cvt_pk_bf16_f32 v203, v22, v23\nbuffer_store_dwordx4 v[200:203], v252, s[20:23], s85 offen offset:0 sc1\nG 3\n"
"v_pk_add_f32 v[24:25], v[24:25], v[56:57]\nG 2\nv_pk_add_f32 v[26:27], v[26:27], v[58:59]\nG 1\nv_pk_add_f32 v[28:29], v[28:29], v[60:61]\nG 0\n"
"v_pk_add_f32 v[30:31], v[30:31], v[62:63]\nv_cvt_pk_bf16_f32 v204, v24, v25\nv_cvt_pk_bf16_f32 v205, v26, v27\nv_cvt_pk_bf16_f32 v206, v28, v29\nv_cvt_pk_bf16_f32 v207, v30, v31\nbuffer_store_dwordx4 v[204:207], v252, s[20:23], s85 offen offset:1024 sc1\n"
"s_add_u32 s61, s79, 88637440\ns_lshl_b32 s62, s75, 3\ns_add_u32 s61, s61, s62\ns_waitcnt vmcnt(0)\ns_mov_b64 exec, 1\nbuffer_atomic_swap_x2 v[238:239], off, s[20:23], s61 sc1\n"
"s_mov_b64 exec, -1\ns_barrier\nLqw1:\nv_bfe_u32 v250, v252, 7, 1\nv_mul_u32_u24 v250, 2176, v250\nv_bfe_u32 v251, v252, 4, 3\n"
"v_lshl_add_u32 v250, v251, 3, v250\ns_lshl_b32 s60, s90, 6\nv_add_u32 v250, s60, v250\ns_add_u32 m0, s50, 32768\ns_add_u32 s63, s54, 1835008\nbuffer_load_dwordx4 v220, s[12:15], s63 offen lds\n"
"buffer_load_dwordx4 v221, s[12:15], s63 offen offset:1024 lds\ns_add_u32 m0, s50, 40960\ns_add_u32 s63, s54, 2064384\nbuffer_load_dwordx4 v220, s[12:15], s63 offen lds\nbuffer_load_dwordx4 v221, s[12:15], s63 offen offset:1024 lds\ns_add_u32 m0, s50, 49152\n"
"s_add_u32 s63, s54, 2293760\nbuffer_load_dwordx4 v220, s[12:15], s63 offen lds\nbuffer_load_dwordx4 v221, s[12:15], s63 offen offset:1024 lds\ns_add_u32 m0, s50, 57344\ns_add_u32 s63, s54, 2523136\nbuffer_load_dwordx4 v220, s[12:15], s63 offen lds\n"
"buffer_load_dwordx4 v221, s[12:15], s63 offen offset:1024 lds\nv_mov_b32 v248, 0\nv_mov_b32 v249, 0\ns_add_u32 s61, s80, 88637440\ns_mov_b64 exec, 0xffff\nbuffer_atomic_or_x2 v[248:249], v250, s[20:23], s61 offen sc0 sc1\n"
"s_mov_b64 exec, -1\nv_mov_b32 v230, 0\nv_mov_b32 v231, 0\nv_mov_b32 v232, 0\nv_mov_b32 v233, 0\nv_dot2c_f32_bf16 v230, s76, v192\n"
"v_dot2c_f32_bf16 v231, v192, v192\nv_dot2c_f32_bf16 v232, s76, v200\nv_dot2c_f32_bf16 v233, v200, v200\nv_dot2c_f32_bf16 v230, s76, v193\nv_dot2c_f32_bf16 v231, v193, v193\nv_dot2c_f32_bf16 v232, s76, v201\n"
"v_dot2c_f32_bf16 v233, v201, v201\nv_dot2c_f32_bf16 v230, s76, v194\nv_dot2c_f32_bf16 v231, v194, v194\nv_dot2c_f32_bf16 v232, s76, v202\nv_dot2c_f32_bf16 v233, v202, v202\nv_dot2c_f32_bf16 v230, s76, v195\n"
"v_dot2c_f32_bf16 v231, v195, v195\nv_dot2c_f32_bf16 v232, s76, v203\nv_dot2c_f32_bf16 v233, v203, v203\nv_dot2c_f32_bf16 v230, s76, v196\nv_dot2c_f32_bf16 v231, v196, v196\nv_dot2c_f32_bf16 v232, s76, v204\n"
"v_dot2c_f32_bf16 v233, v204, v204\nv_dot2c_f32_bf16 v230, s76, v197\nv_dot2c_f32_bf16 v231, v197, v197\nv_dot2c_f32_bf16 v232, s76, v205\nv_dot2c_f32_bf16 v233, v205, v205\nv_dot2c_f32_bf16 v230, s76, v198\n"
"v_dot2c_f32_bf16 v231, v198, v198\nv_dot2c_f32_bf16 v232, s76, v206\nv_dot2c_f32_bf16 v233, v206, v206\nv_dot2c_f32_bf16 v230, s76, v199\nv_dot2c_f32_bf16 v231, v199, v199\nv_dot2c_f32_bf16 v232, s76, v207\n"
"v_dot2c_f32_bf16 v233, v207, v207\nv_lshrrev_b32 v234, 1, v252\nv_permlane32_swap_b32 v230, v231\ns_nop 0\nv_permlane32_swap_b32 v232, v233\nv_add_f32 v230, v230, v231\n"
"v_add_f32 v232, v232, v233\nv_mov_b32 v231, s46\ns_nop 0\nv_permlane16_swap_b32 v230, v232\nv_add_f32 v230, v230, v232\ns_lshr_b32 s61, s49, 3\n"
"s_lshl2_add_u32 s61, s53, s61\ns_add_u32 s61, s61, 83361792\nbuffer_store_dwordx2 v[230:231], v234, s[20:23], s61 offen sc1\ns_waitcnt vmcnt(1)\nv_cmp_eq_u64 s[86:87], s[46:47], v[248:249]\ns_nop 0\n"
"s_and_b32 s62, s86, 0xffff\ns_cmp_eq_u32 s62, 0xffff\ns_cbranch_scc0 Lq754\nLq755:\ns_add_u32 s64, s70, 14680064\nbuffer_load_dwordx4 a[0:3], v252, s[20:23], s64 offen offset:0 sc1\n"
"s_add_u32 s65, s70, 14712832\nbuffer_load_dwordx4 a[4:7], v252, s[20:23], s65 offen offset:0 sc1\ns_add_u32 s66, s71, 14680064\nbuffer_load_dwordx4 a[8:11], v252, s[20:23], s66 offen offset:0 sc1\ns_add_u32 s67, s71, 14712832\nbuffer_load_dwordx4 a[12:15], v252, s[20:23], s67 offen offset:0 sc1\n"
"buffer_load_dwordx4 a[16:19], v252, s[20:23], s64 offen offset:1024 sc1\nbuffer_load_dwordx4 a[20:23], v252, s[20:23], s65 offen offset:1024 sc1\nbuffer_load_dwordx4 a[24:27], v252, s[20:23], s66 offen offset:1024 sc1\nbuffer_load_dwordx4 a[28:31], v252, s[20:23], s67 offen offset:1024 sc1\ns_barrier\nR 160,188,24576\n"
"R 162,188,28672\nR 164,188,32768\nR 166,188,36864\nR 168,189,24576\nR 170,189,28672\nR 172,189,32768\n"
"R 174,189,36864\nR 176,190,24576\nR 178,190,28672\nR 180,190,32768\nR 182,190,36864\nbuffer_load_dwordx4 a[32:35], v252, s[20:23], s64 offen offset:2048 sc1\n"
"buffer_load_dwordx4 a[36:39], v252, s[20:23], s65 offen offset:2048 sc1\nbuffer_load_dwordx4 a[40:43], v252, s[20:23], s66 offen offset:2048 sc1\nbuffer_load_dwordx4 a[44:47], v252, s[20:23], s67 offen offset:2048 sc1\nbuffer_load_dwordx4 a[48:51], v252, s[20:23], s64 offen offset:3072 sc1\nbuffer_load_dwordx4 a[52:55], v252, s[20:23], s65 offen offset:3072 sc1\nbuffer_load_dwordx4 a[56:59], v252, s[20:23], s66 offen offset:3072 sc1\n"
"buffer_load_dwordx4 a[60:63], v252, s[20:23], s67 offen offset:3072 sc1\ns_waitcnt vmcnt(15) lgkmcnt(10)\nv_mfma_f32_16x16x32_bf16 v[32:35], v[160:163], a[0:3], 0\ns_add_u32 s64, s70, 14684160\nbuffer_load_dwordx4 a[64:67], v252, s[20:23], s64 offen offset:0 sc1\ns_waitcnt vmcnt(12) lgkmcnt(8)\n"
"M 32,164,16\nv_mfma_f32_16x16x32_bf16 v[48:51], v[160:163], a[4:7], 0\ns_waitcnt vmcnt(11)\nM 48,164,20\nv_mfma_f32_16x16x32_bf16 v[64:67], v[160:163], a[8:11], 0\ns_add_u32 s65, s70, 14716928\n"
"buffer_load_dwordx4 a[68:71], v252, s[20:23], s65 offen offset:0 sc1\ns_waitcnt vmcnt(11)\nM 64,164,24\nv_mfma_f32_16x16x32_bf16 v[80:83], v[160:163], a[12:15], 0\ns_waitcnt vmcnt(10)\nM 80,164,28\n"
"G 4\nv_mfma_f32_16x16x32_bf16 v[36:39], v[168:171], a[0:3], 0\nR 160,191,24576\nM 36,172,16\ns_add_u32 s66, s71, 14684160\nbuffer_load_dwordx4 a[72:75], v252, s[20:23], s66 offen offset:0 sc1\n"
"v_mfma_f32_16x16x32_bf16 v[52:55], v[168:171], a[4:7], 0\nR 162,191,28672\nM 52,172,20\nv_mfma_f32_16x16x32_bf16 v[68:71], v[168:171], a[8:11], 0\nR 164,191,32768\nM 68,172,24\n"
"s_add_u32 s67, s71, 14716928\nbuffer_load_dwordx4 a[76:79], v252, s[20:23], s67 offen offset:0 sc1\nv_mfma_f32_16x16x32_bf16 v[84:87], v[168:171], a[12:15], 0\nR 166,191,36864\nM 84,172,28\nG 4\n"
"v_mfma_f32_16x16x32_bf16 v[40:43], v[176:179], a[0:3], 0\nR 168,188,40960\nM 40,180,16\nbuffer_load_dwordx4 a[80:83], v252, s[20:23], s64 offen offset:1024 sc1\nv_mfma_f32_16x16x32_bf16 v[56:59], v[176:179], a[4:7], 0\nR 170,188,45056\n"
"M 56,180,20\nv_mfma_f32_16x16x32_bf16 v[72:75], v[176:179], a[8:11], 0\nR 172,188,49152\nM 72,180,24\nbuffer_load_dwordx4 a[84:87], v252, s[20:23], s65 offen offset:1024 sc1\nv_mfma_f32_16x16x32_bf16 v[88:91], v[176:179], a[12:15], 0\n"
"R 174,188,53248\nM 88,180,28\nG 4\nv_mfma_f32_16x16x32_bf16 v[44:47], v[160:163], a[0:3], 0\nR 176,189,40960\nM 44,164,16\n"
"buffer_load_dwordx4 a[88:91], v252, s[20:23], s66 offen offset:1024 sc1\nv_mfma_f32_16x16x32_bf16 v[60:63], v[160:163], a[4:7], 0\nR 178,189,45056\nM 60,164,20\nv_mfma_f32_16x16x32_bf16 v[76:79], v[160:163], a[8:11], 0\nR 180,189,49152\n"
"M 76,164,24\nbuffer_load_dwordx4 a[92:95], v252, s[20:23], s67 offen offset:1024 sc1\nv_mfma_f32_16x16x32_bf16 v[92:95], v[160:163], a[12:15], 0\nR 182,189,53248\nM 92,164,28\nG 4\n"
"s_waitcnt vmcnt(15)\nM 32,168,32\nR 160,190,40960\ns_waitcnt vmcnt(11)\nM 32,172,48\nM 48,168,36\n"
"R 162,190,45056\ns_waitcnt vmcnt(10)\nM 48,172,52\nbuffer_load_dwordx4 a[96:99], v252, s[20:23], s64 offen offset:2048 sc1\nM 64,168,40\nR 164,190,49152\n"
"s_waitcnt vmcnt(10)\nM 64,172,56\nM 80,168,44\nR 166,190,53248\ns_waitcnt vmcnt(9)\nM 80,172,60\n"
"G 4\nM 36,176,32\nR 168,191,40960\nM 36,180,48\nbuffer_load_dwordx4 a[100:103], v252, s[20:23], s65 offen offset:2048 sc1\nM 52,176,36\n"
"R 170,191,45056\nM 52,180,52\nbuffer_load_dwordx4 a[104:107], v252, s[20:23], s66 offen offset:2048 sc1\nM 68,176,40\nR 172,191,49152\nM 68,180,56\n"
"M 84,176,44\nR 174,191,53248\nM 84,180,60\nG 4\nM 40,160,32\nR 176,184,0\n"
"M 40,164,48\nbuffer_load_dwordx4 a[108:111], v252, s[20:23], s67 offen offset:2048 sc1\nM 56,160,36\nR 178,184,4096\nM 56,164,52\nbuffer_load_dwordx4 a[112:115], v252, s[20:23], s64 offen offset:3072 sc1\n"
"M 72,160,40\nR 180,184,8192\nM 72,164,56\nM 88,160,44\nR 182,184,12288\nM 88,164,60\n"
"G 4\nM 44,168,32\nR 160,185,0\nM 44,172,48\nbuffer_load_dwordx4 a[116:119], v252, s[20:23], s65 offen offset:3072 sc1\nM 60,168,36\n"
"R 162,185,4096\nM 60,172,52\nbuffer_load_dwordx4 a[120:123], v252, s[20:23], s66 offen offset:3072 sc1\nM 76,168,40\nR 164,185,8192\nM 76,172,56\n"
"s_add_u32 m0, s50, 65536\nM 92,168,44\nR 166,185,12288\ns_add_u32 s63, s54, 2752512\nM 92,172,60\ns_barrier\n"
"G 4\ns_waitcnt vmcnt(14)\nM 32,176,64\nR 168,186,0\ns_waitcnt vmcnt(10)\nM 32,180,80\n"
"buffer_load_dwordx4 a[124:127], v252, s[20:23], s67 offen offset:3072 sc1\nM 48,176,68\nR 170,186,4096\ns_waitcnt vmcnt(10)\nM 48,180,84\nbuffer_load_dwordx4 v220, s[12:15], s63 offen lds\n"
"M 64,176,72\nR 172,186,8192\ns_waitcnt vmcnt(10)\nM 64,180,88\ns_add_u32 s64, s70, 14688256\nbuffer_load_dwordx4 a[128:131], v252, s[20:23], s64 offen offset:0 sc1\n"
"M 80,176,76\nR 174,186,12288\ns_waitcnt vmcnt(10)\nM 80,180,92\nG 4\nM 36,160,64\n"
"R 176,187,0\nbuffer_load_dwordx4 v221, s[12:15], s63 offen offset:1024 lds\nM 36,164,80\ns_add_u32 s65, s70, 14721024\nbuffer_load_dwordx4 a[132:135], v252, s[20:23], s65 offen offset:0 sc1\ns_add_u32 s63, s54, 2981888\n"
"M 52,160,68\nR 178,187,4096\ns_add_u32 m0, s50, 73728\nM 52,164,84\nbuffer_load_dwordx4 v220, s[12:15], s63 offen lds\nM 68,160,72\n"
"R 180,187,8192\nM 68,164,88\ns_add_u32 s66, s71, 14688256\nbuffer_load_dwordx4 a[136:139], v252, s[20:23], s66 offen offset:0 sc1\nM 84,160,76\nR 182,187,12288\n"
"M 84,164,92\nG 4\nM 40,168,64\nR 160,184,16384\nbuffer_load_dwordx4 v221, s[12:15], s63 offen offset:1024 lds\nM 40,172,80\n"
"s_add_u32 s67, s71, 14721024\nbuffer_load_dwordx4 a[140:143], v252, s[20:23], s67 offen offset:0 sc1\nM 56,168,68\nR 162,184,20480\nM 56,172,84\nM 72,168,72\n"
"R 164,184,24576\nM 72,172,88\nbuffer_load_dwordx4 a[144:147], v252, s[20:23], s64 offen offset:1024 sc1\nM 88,168,76\nR 166,184,28672\nM 88,172,92\n"
"G 4\nM 44,176,64\nR 168,185,16384\nM 44,180,80\nbuffer_load_dwordx4 a[148:151], v252, s[20:23], s65 offen offset:1024 sc1\nM 60,176,68\n"
"R 170,185,20480\nM 60,180,84\nM 76,176,72\nR 172,185,24576\nM 76,180,88\nbuffer_load_dwordx4 a[152:155], v252, s[20:23], s66 offen offset:1024 sc1\n"
"M 92,176,76\nR 174,185,28672\nM 92,180,92\nG 4\ns_waitcnt vmcnt(18)\nM 32,160,96\n"
"R 176,186,16384\ns_waitcnt vmcnt(14)\nM 32,164,112\nbuffer_load_dwordx4 a[156:159], v252, s[20:23], s67 offen offset:1024 sc1\nM 48,160,100\nR 178,186,20480\n"
"s_waitcnt vmcnt(14)\nM 48,164,116\nM 64,160,104\nR 180,186,24576\ns_waitcnt vmcnt(13)\nM 64,164,120\n"
"M 80,160,108\nR 182,186,28672\ns_waitcnt vmcnt(12)\nM 80,164,124\nG 4\nM 36,168,96\n"
"R 160,187,16384\nM 36,172,112\nbuffer_load_dwordx4 a[160:163], v252, s[20:23], s64 offen offset:2048 sc1\nM 52,168,100\nR 162,187,20480\nM 52,172,116\n"
"buffer_load_dwordx4 a[164:167], v252, s[20:23], s65 offen offset:2048 sc1\nM 68,168,104\nR 164,187,24576\nM 68,172,120\nM 84,168,108\nR 166,187,28672\n"
"s_add_u32 m0, s50, 81920\nM 84,172,124\nG 4\nM 40,176,96\nR 168,184,32768\ns_add_u32 s63, s54, 3211264\n"
"M 40,180,112\nbuffer_load_dwordx4 a[168:171], v252, s[20:23], s66 offen offset:2048 sc1\nM 56,176,100\nR 170,184,36864\nM 56,180,116\nbuffer_load_dwordx4 a[172:175], v252, s[20:23], s67 offen offset:2048 sc1\n"
"M 72,176,104\nR 172,184,40960\nbuffer_load_dwordx4 v220, s[12:15], s63 offen lds\nM 72,180,120\nbuffer_load_dwordx4 v221, s[12:15], s63 offen offset:1024 lds\nM 88,176,108\n"
"R 174,184,45056\ns_add_u32 m0, s50, 90112\nM 88,180,124\nG 4\nM 44,160,96\nR 176,185,32768\n"
"s_add_u32 s63, s54, 3440640\nM 44,164,112\nbuffer_load_dwordx4 a[176:179], v252, s[20:23], s64 offen offset:3072 sc1\nM 60,160,100\nR 178,185,36864\nM 60,164,116\n"
"buffer_load_dwordx4 a[180:183], v252, s[20:23], s65 offen offset:3072 sc1\nM 76,160,104\nR 180,185,40960\nbuffer_load_dwordx4 v220, s[12:15], s63 offen lds\nM 76,164,120\nbuffer_load_dwordx4 v221, s[12:15], s63 offen offset:1024 lds\n"
"M 92,160,108\nR 182,185,45056\ns_add_u32 m0, s50, 98304\nM 92,164,124\ns_waitcnt vmcnt(15)\ns_barrier\n"
"G 4\nM 32,168,128\nR 160,186,32768\ns_add_u32 s63, s54, 128\ns_waitcnt vmcnt(13)\nM 32,172,144\n"
"buffer_load_dwordx4 a[184:187], v252, s[20:23], s66 offen offset:3072 sc1\nM 48,168,132\nR 162,186,36864\ns_waitcnt vmcnt(13)\nM 48,172,148\nbuffer_load_dwordx4 a[188:191], v252, s[20:23], s67 offen offset:3072 sc1\n"
"M 64,168,136\nR 164,186,40960\nbuffer_load_dwordx4 v220, s[12:15], s63 offen lds\ns_waitcnt vmcnt(14)\nM 64,172,152\nbuffer_load_dwordx4 v221, s[12:15], s63 offen offset:1024 lds\n"
"M 80,168,140\nR 166,186,45056\ns_add_u32 m0, s50, 106496\ns_waitcnt vmcnt(14)\nM 80,172,156\nG 4\n"
"M 36,176,128\nR 168,187,32768\ns_add_u32 s63, s54, 229504\nM 36,180,144\ns_add_u32 s64, s70, 14692352\nbuffer_load_dwordx4 a[192:195], v252, s[20:23], s64 offen offset:0 sc1\n"
"M 52,176,132\nR 170,187,36864\nM 52,180,148\nbuffer_load_dwordx4 v220, s[12:15], s63 offen lds\nM 68,176,136\nR 172,187,40960\n"
"M 68,180,152\ns_add_u32 s65, s70, 14725120\nbuffer_load_dwordx4 a[196:199], v252, s[20:23], s65 offen offset:0 sc1\nM 84,176,140\nR 174,187,45056\nM 84,180,156\n"
"G 4\nM 40,160,128\nR 176,184,49152\nbuffer_load_dwordx4 v221, s[12:15], s63 offen offset:1024 lds\nM 40,164,144\ns_add_u32 s66, s71, 14692352\n"
"buffer_load_dwordx4 a[200:203], v252, s[20:23], s66 offen offset:0 sc1\nM 56,160,132\nR 178,184,53248\nM 56,164,148\nM 72,160,136\nR 180,184,57344\n"
"M 72,164,152\ns_add_u32 s67, s71, 14725120\nbuffer_load_dwordx4 a[204:207], v252, s[20:23], s67 offen offset:0 sc1\nM 88,160,140\nR 182,184,61440\nM 88,164,156\n"
"G 4\nM 44,168,128\nR 160,185,49152\nM 44,172,144\nbuffer_load_dwordx4 a[208:211], v252, s[20:23], s64 offen offset:1024 sc1\nM 60,168,132\n"
"R 162,185,53248\nM 60,172,148\nM 76,168,136\nR 164,185,57344\nM 76,172,152\nbuffer_load_dwordx4 a[212:215], v252, s[20:23], s65 offen offset:1024 sc1\n"
"M 92,168,140\nR 166,185,61440\nM 92,172,156\nG 4\ns_waitcnt vmcnt(21)\nM 32,176,160\n"
"R 168,186,49152\ns_waitcnt vmcnt(15)\nM 32,180,176\nbuffer_load_dwordx4 a[216:219], v252, s[20:23], s66 offen offset:1024 sc1\nM 48,176,164\nR 170,186,53248\n"
"s_waitcnt vmcnt(15)\nM 48,180,180\nM 64,176,168\nR 172,186,57344\ns_waitcnt vmcnt(12)\nM 64,180,184\n"
"buffer_load_dwordx4 a[220:223], v252, s[20:23], s67 offen offset:1024 sc1\nM 80,176,172\nR 174,186,61440\ns_waitcnt vmcnt(12)\nM 80,180,188\nG 4\n"
"M 36,160,160\nR 176,187,49152\nM 36,164,176\ns_lshr_b32 s84, s49, 3\nM 52,160,164\nR 178,187,53248\n"
"M 52,164,180\nbuffer_load_dwordx4 a[224:227], v252, s[20:23], s64 offen offset:2048 sc1\nM 68,160,168\nR 180,187,57344\nM 68,164,184\ns_add_u32 s84, s84, 83361792\n"
"M 84,160,172\nR 182,187,61440\nM 84,164,188\nG 4\nM 40,168,160\nR 160,188,0\n"
"buffer_load_dwordx4 v[0:3], v252, s[20:23], s84 offen offset:0 sc1\nM 40,172,176\nbuffer_load_dwordx4 a[228:231], v252, s[20:23], s65 offen offset:2048 sc1\nM 56,168,164\nR 162,188,4096\nM 56,172,180\n"
"buffer_load_dwordx4 a[232:235], v252, s[20:23], s66 offen offset:2048 sc1\nM 72,168,168\nR 164,188,8192\nM 72,172,184\nbuffer_load_dwordx4 v[4:7], v252, s[20:23], s84 offen offset:1024 sc1\nM 88,168,172\n"
"R 166,188,12288\ns_lshr_b32 s85, s49, 3\nM 88,172,188\nbuffer_load_dwordx4 v[8:11], v252, s[20:23], s84 offen offset:2048 sc1\nG 4\nM 44,176,160\n"
"R 168,189,0\ns_add_u32 s85, s85, 83365888\nM 44,180,176\nbuffer_load_dwordx4 a[236:239], v252, s[20:23], s67 offen offset:2048 sc1\nM 60,176,164\nR 170,189,4096\n"
"buffer_load_dwordx4 v[12:15], v252, s[20:23], s84 offen offset:3072 sc1\nM 60,180,180\nbuffer_load_dwordx4 a[240:243], v252, s[20:23], s64 offen offset:3072 sc1\nM 76,176,168\nR 172,189,8192\nM 76,180,184\n"
"buffer_load_dwordx4 v[16:19], v252, s[20:23], s85 offen offset:0 sc1\nM 92,176,172\nR 174,189,12288\ns_add_u32 m0, s50, 114688\nM 92,180,188\nbuffer_load_dwordx4 v[20:23], v252, s[20:23], s85 offen offset:1024 sc1\n"
"s_waitcnt vmcnt(17)\ns_barrier\nG 4\nM 32,160,192\nR 176,190,0\ns_add_u32 s63, s54, 458880\n"
"s_waitcnt vmcnt(14)\nM 32,164,208\nbuffer_load_dwordx4 a[244:247], v252, s[20:23], s65 offen offset:3072 sc1\nM 48,160,196\nR 178,190,4096\nbuffer_load_dwordx4 v[24:27], v252, s[20:23], s85 offen offset:2048 sc1\n"
"s_waitcnt vmcnt(15)\nM 48,164,212\nbuffer_load_dwordx4 a[248:251], v252, s[20:23], s66 offen offset:3072 sc1\nM 64,160,200\nR 180,190,8192\nbuffer_load_dwordx4 v220, s[12:15], s63 offen lds\n"
"s_waitcnt vmcnt(16)\nM 64,164,216\nbuffer_load_dwordx4 v[28:31], v252, s[20:23], s85 offen offset:3072 sc1\nM 80,160,204\nR 182,190,12288\ns_waitcnt vmcnt(16)\n"
"M 80,164,220\nG 4\nM 36,168,192\nR 160,191,0\nbuffer_load_dwordx4 v221, s[12:15], s63 offen offset:1024 lds\nM 36,172,208\n"
"buffer_load_dwordx4 a[252:255], v252, s[20:23], s67 offen offset:3072 sc1\ns_add_u32 s63, s54, 688256\nM 52,168,196\nR 162,191,4096\ns_add_u32 m0, s50, 0\nM 52,172,212\n"
"buffer_load_dwordx4 v220, s[12:15], s63 offen lds\nM 68,168,200\nR 164,191,8192\nM 68,172,216\nbuffer_load_dwordx4 v221, s[12:15], s63 offen offset:1024 lds\nM 84,168,204\n"
"R 166,191,12288\nM 84,172,220\nG 4\nM 40,176,192\nR 168,188,16384\nM 40,180,208\n"
"s_add_u32 m0, s50, 8192\nM 56,176,196\nR 170,188,20480\nM 56,180,212\ns_add_u32 s63, s54, 917632\nM 72,176,200\n"
"R 172,188,24576\nM 72,180,216\nbuffer_load_dwordx4 v220, s[12:15], s63 offen lds\nM 88,176,204\nR 174,188,28672\nM 88,180,220\n"
"G 4\nM 44,160,192\nR 176,189,16384\nM 44,164,208\nbuffer_load_dwordx4 v221, s[12:15], s63 offen offset:1024 lds\nM 60,160,196\n"
"R 178,189,20480\ns_add_u32 s63, s54, 1147008\nM 60,164,212\ns_add_u32 m0, s50, 16384\nM 76,160,200\nR 180,189,24576\n"
"Lq1025:\ns_lshr_b32 s60, s45, 3\ns_lshl2_add_u32 s60, s60, s40\ns_lshl_b32 s60, s60, 8\nv_lshl_add_u32 v224, v252, 3, s60\nv_add_u32 v224, Lq1025+4096-LqPC, v224\n"
"s_mov_b64 exec, 3\nglobal_load_dword v224, v224, s[98:99]\ns_mov_b64 exec, -1\nM 76,164,216\nbuffer_load_dwordx4 v220, s[12:15], s63 offen lds\nM 92,160,204\n"
"R 182,189,28672\nM 92,164,220\nG 4\ns_waitcnt vmcnt(23)\nM 32,168,224\nR 160,190,16384\n"
"s_waitcnt vmcnt(15)\nM 32,172,240\nbuffer_load_dwordx4 v221, s[12:15], s63 offen offset:1024 lds\nM 48,168,228\nR 162,190,20480\ns_add_u32 s63, s54, 1376384\n"
"s_waitcnt vmcnt(13)\nM 48,172,244\ns_add_u32 m0, s50, 24576\nM 64,168,232\nR 164,190,24576\ns_waitcnt vmcnt(11)\n"
"M 64,172,248\nbuffer_load_dwordx4 v220, s[12:15], s63 offen lds\nM 80,168,236\nR 166,190,28672\ns_waitcnt vmcnt(8)\nM 80,172,252\n"
"G 4\nM 36,176,224\nR 168,191,16384\nM 36,180,240\nbuffer_load_dwordx4 v221, s[12:15], s63 offen offset:1024 lds\nM 52,176,228\n"
"R 170,191,20480\ns_add_u32 s63, s54, 1605760\nM 52,180,244\ns_add_u32 m0, s50, 32768\nM 68,176,232\nR 172,191,24576\n"
"v_min3_u32 v230, v1, v3, v5\nv_min3_u32 v230, v230, v7, v9\nM 68,180,248\nbuffer_load_dwordx4 v220, s[12:15], s63 offen lds\nv_min3_u32 v230, v230, v11, v13\nM 84,176,236\n"
"R 174,191,28672\nv_min3_u32 v230, v230, v15, v17\nv_min3_u32 v230, v230, v19, v21\nM 84,180,252\nG 4\nv_min3_u32 v230, v230, v23, v25\n"
"v_min3_u32 v230, v230, v27, v29\nv_min3_u32 v230, v230, v31, v31\nM 40,160,224\nR 176,188,32768\nv_max3_u32 v231, v1, v3, v5\nv_max3_u32 v231, v231, v7, v9\n"
"M 40,164,240\nbuffer_load_dwordx4 v221, s[12:15], s63 offen offset:1024 lds\nv_max3_u32 v231, v231, v11, v13\nM 56,160,228\nR 178,188,36864\nv_max3_u32 v231, v231, v15, v17\n"
"v_max3_u32 v231, v231, v19, v21\nM 56,164,244\nv_max3_u32 v231, v231, v23, v25\nv_max3_u32 v231, v231, v27, v29\nv_max3_u32 v231, v231, v31, v31\nM 72,160,232\n"
"R 180,188,40960\nv_cmp_ne_u32 s[86:87], s46, v230\nv_cmp_ne_u32 s[88:89], s46, v231\nM 72,164,248\ns_add_u32 m0, s50, 40960\ns_or_b64 s[86:87], s[86:87], s[88:89]\n"
"s_cbranch_scc1 Lq811\nLq812:\nv_add_f32 v0, v0, v16\nM 88,160,236\nR 182,188,45056\nv_add_f32 v2, v2, v18\n"
"v_add_f32 v4, v4, v20\nM 88,164,252\nG 4\nv_add_f32 v6, v6, v22\nv_add_f32 v8, v8, v24\nv_add_f32 v10, v10, v26\n"
"M 44,168,224\nR 160,189,32768\nv_add_f32 v12, v12, v28\nv_add_f32 v14, v14, v30\nM 44,172,240\ns_add_u32 s63, s54, 1835136\n"
"v_add_f32 v0, v0, v8\nv_add_f32 v2, v2, v10\nv_add_f32 v4, v4, v12\nM 60,168,228\nR 162,189,36864\nv_add_f32 v6, v6, v14\n"
"v_add_f32 v0, v0, v4\nM 60,172,244\nbuffer_load_dwordx4 v220, s[12:15], s63 offen lds\nv_add_f32 v2, v2, v6\nM 76,168,232\nR 164,189,40960\n"
"v_lshrrev_b32 v231, 1, v252\nv_lshrrev_b32 v230, 2, v252\nM 76,172,248\ns_lshl_b32 s62, s40, 9\ns_add_u32 s62, s62, 159232\nv_add_u32 v231, s62, v231\n"
"ds_write2_b32 v231, v0, v2 offset1:1\nM 92,168,236\nR 166,189,45056\ns_lshl_b32 s62, s40, 9\ns_add_u32 s62, s62, 159232\nv_add_u32 v230, s62, v230\n"
"M 92,172,252\ns_waitcnt vmcnt(10)\ns_barrier\nds_read2st64_b32 v[232:233], v230 offset1:1\nG 6\nv_mfma_f32_16x16x32_bf16 v[96:99], v[176:179], a[0:3], 0\n"
"R 168,190,32768\nM 96,180,16\ns_mov_b32 m0, s51\nv_mfma_f32_16x16x32_bf16 v[112:115], v[176:179], a[4:7], 0\nR 170,190,36864\nM 112,180,20\n"
"ds_write_addtid_b32 v64 offset:0\nv_mfma_f32_16x16x32_bf16 v[128:131], v[176:179], a[8:11], 0\nR 172,190,40960\nM 128,180,24\nG 4\nv_add_f32 v232, v232, v233\n"
"v_mov_b32 v233, v232\ns_nop 1\nv_permlane32_swap_b32 v232, v233\nv_mfma_f32_16x16x32_bf16 v[144:147], v[176:179], a[12:15], 0\nR 174,190,45056\nv_mov_b32 v230, v232\n"
"v_mov_b32 v231, v233\nM 144,180,28\nv_permlane16_swap_b32 v230, v232\nv_permlane16_swap_b32 v231, v233\nv_mul_f32 v230, 0xba800000, v230\nv_mfma_f32_16x16x32_bf16 v[100:103], v[160:163], a[0:3], 0\n"
"R 176,191,32768\nv_mul_f32 v214, v230, v230\nv_mul_f32 v231, 0x3a800000, v231\nM 100,164,16\nv_sub_f32 v231, v231, v214\nv_max_f32 v231, 0, v231\n"
"v_add_f32 v231, 0x3727c5ac, v231\nv_mfma_f32_16x16x32_bf16 v[116:119], v[160:163], a[4:7], 0\nR 178,191,36864\nv_and_b32 v208, s77, v230\nv_rsq_f32 v234, v231\nM 116,164,20\n"
"ds_write_addtid_b32 v65 offset:256\nv_mul_f32 v216, v231, v234\nv_sub_f32 v230, v230, v208\nv_cvt_pk_bf16_f32 v209, v230, v216\nv_mfma_f32_16x16x32_bf16 v[132:135], v[160:163], a[8:11], 0\nR 180,191,40960\n"
"ds_write_addtid_b32 v66 offset:512\nv_cvt_pk_bf16_f32 v208, v208, v208\nv_mul_f32 v214, 0xc0135761, v234\nM 132,164,24\ns_mov_b64 s[60:61], 0xffff\nv_cndmask_b32 v208, 0, v208, s[60:61]\n"
"ds_write_addtid_b32 v67 offset:768\nv_mul_f32 v215, v234, v234\nv_mul_f32 v215, v215, v214\nv_mfma_f32_16x16x32_bf16 v[148:151], v[160:163], a[12:15], 0\nR 182,191,45056\nds_write_addtid_b32 v68 offset:1024\n"
"v_mul_f32 v215, 0x3d372713, v215\nv_mul_f32 v232, 0xba800000, v232\nM 148,164,28\ns_mov_b64 s[60:61], 0xffff\nv_cndmask_b32 v209, 0, v209, s[60:61]\nds_write_addtid_b32 v69 offset:1280\n"
"G 9\nv_mul_f32 v217, v232, v232\nv_mfma_f32_16x16x32_bf16 v[104:107], v[168:171], a[0:3], 0\nR 160,188,49152\nds_write_addtid_b32 v70 offset:1536\nv_mul_f32 v233, 0x3a800000, v233\n"
"v_sub_f32 v233, v233, v217\nM 104,172,16\nds_write_addtid_b32 v71 offset:1792\nv_max_f32 v233, 0, v233\nv_add_f32 v233, 0x3727c5ac, v233\nv_rsq_f32 v235, v233\n"
"v_mfma_f32_16x16x32_bf16 v[120:123], v[168:171], a[4:7], 0\nR 162,188,53248\nds_write_addtid_b32 v72 offset:2048\nv_and_b32 v212, s77, v232\nv_mul_f32 v219, v233, v235\nM 120,172,20\n"
"ds_write_addtid_b32 v73 offset:2304\nv_sub_f32 v232, v232, v212\nv_cvt_pk_bf16_f32 v213, v232, v219\nv_cvt_pk_bf16_f32 v212, v212, v212\nv_mfma_f32_16x16x32_bf16 v[136:139], v[168:171], a[8:11], 0\nR 164,184,0\n"
"ds_write_addtid_b32 v74 offset:2560\nv_mul_f32 v217, 0xc0135761, v235\nv_mul_f32 v218, v235, v235\nM 136,172,24\ns_mov_b64 s[60:61], 0xffff\nv_cndmask_b32 v212, 0, v212, s[60:61]\n"
"ds_write_addtid_b32 v75 offset:2816\nv_mul_f32 v218, v218, v217\nv_mul_f32 v218, 0x3d372713, v218\nv_mfma_f32_16x16x32_bf16 v[152:155], v[168:171], a[12:15], 0\nR 166,184,4096\nds_write_addtid_b32 v76 offset:3072\n"
"s_mov_b64 s[60:61], 0xffff\nv_cndmask_b32 v213, 0, v213, s[60:61]\nM 152,172,28\nds_write_addtid_b32 v77 offset:3328\nds_write_addtid_b32 v78 offset:3584\nG 15\n"
"v_mfma_f32_16x16x32_bf16 v[108:111], v[176:179], a[0:3], 0\nR 168,189,49152\nds_write_addtid_b32 v79 offset:3840\nM 108,180,16\nds_write_addtid_b32 v80 offset:4096\nds_write_addtid_b32 v81 offset:4352\n"
"v_mfma_f32_16x16x32_bf16 v[124:127], v[176:179], a[4:7], 0\nR 170,189,53248\nds_write_addtid_b32 v82 offset:4608\nM 124,180,20\nds_write_addtid_b32 v83 offset:4864\nds_write_addtid_b32 v84 offset:5120\n"
"v_mfma_f32_16x16x32_bf16 v[140:143], v[176:179], a[8:11], 0\nR 172,185,0\nds_write_addtid_b32 v85 offset:5376\nM 140,180,24\nds_write_addtid_b32 v86 offset:5632\nds_write_addtid_b32 v87 offset:5888\n"
"v_mfma_f32_16x16x32_bf16 v[156:159], v[176:179], a[12:15], 0\nR 174,185,4096\nds_write_addtid_b32 v88 offset:6144\nM 156,180,28\nds_write_addtid_b32 v89 offset:6400\nds_write_addtid_b32 v90 offset:6656\n"
"ds_write_addtid_b32 v91 offset:6912\nds_write_addtid_b32 v92 offset:7168\nds_write_addtid_b32 v93 offset:7424\nds_write_addtid_b32 v94 offset:7680\nds_write_addtid_b32 v95 offset:7936\nG 15\n"
"M 96,160,32\nR 176,190,49152\ns_add_u32 m0, s50, 40960\ns_nop 0\nbuffer_load_dwordx4 v221, s[12:15], s63 offen offset:1024 lds\nM 96,164,48\n"
"s_add_u32 m0, s50, 49152\ns_add_u32 s63, s54, 2064512\nbuffer_load_dwordx4 v220, s[12:15], s63 offen lds\nM 112,160,36\nR 178,190,53248\ns_waitcnt vmcnt(3) lgkmcnt(2)\n"
"s_barrier\nM 112,164,52\nbuffer_load_dwordx4 v221, s[12:15], s63 offen offset:1024 lds\nds_read2st64_b32 v[64:65], v225 offset0:0 offset1:1\nM 128,160,40\nR 180,186,0\n"
"s_add_u32 s63, s54, 2293888\nds_read2st64_b32 v[66:67], v225 offset0:2 offset1:3\nM 128,164,56\ns_add_u32 m0, s50, 57344\nds_read2st64_b32 v[68:69], v225 offset0:4 offset1:5\nds_read2st64_b32 v[70:71], v225 offset0:6 offset1:7\n"
"M 144,160,44\nR 182,186,4096\nds_read2st64_b32 v[72:73], v225 offset0:8 offset1:9\nM 144,164,60\nds_read2st64_b32 v[74:75], v225 offset0:10 offset1:11\nds_read2st64_b32 v[76:77], v225 offset0:12 offset1:13\n"
"M 100,168,32\nR 160,191,49152\nds_read2st64_b32 v[78:79], v225 offset0:14 offset1:15\nM 100,172,48\nbuffer_load_dwordx4 v220, s[12:15], s63 offen lds\nG 10\n"
"v_add_f32 v32, v32, v64\nM 116,168,36\nR 162,191,53248\nv_add_f32 v33, v33, v65\nG 9\nv_add_f32 v34, v34, v66\n"
"M 116,172,52\nbuffer_load_dwordx4 v221, s[12:15], s63 offen offset:1024 lds\nv_add_f32 v35, v35, v67\nM 132,168,40\nR 164,187,0\nv_bfe_u32 v250, v252, 5, 1\n"
"G 9\nv_add_f32 v36, v36, v68\nv_mul_u32_u24 v250, 2560, v250\nM 132,172,56\nds_read2st64_b32 v[80:81], v225 offset0:16 offset1:17\nv_and_b32 v251, 16, v252\n"
"v_lshl_add_u32 v250, v251, 1, v250\ns_lshl_b32 s60, s90, 6\nv_add_u32 v250, s60, v250\nM 148,168,44\nR 166,187,4096\nv_add_f32 v37, v37, v69\n"
"G 10\nv_add_f32 v38, v38, v70\nM 148,172,60\nds_read2st64_b32 v[82:83], v225 offset0:18 offset1:19\nG 10\nv_add_f32 v39, v39, v71\n"
"M 104,176,32\nR 168,184,8192\nG 10\nv_add_f32 v40, v40, v72\nv_add_f32 v41, v41, v73\nM 104,180,48\n"
"s_add_u32 m0, s50, 65536\nds_read2st64_b32 v[84:85], v225 offset0:20 offset1:21\nG 10\nv_add_f32 v42, v42, v74\nM 120,176,36\nR 170,184,12288\n"
"v_add_f32 v43, v43, v75\nG 10\nv_add_f32 v44, v44, v76\nM 120,180,52\ns_add_u32 s63, s54, 2523264\nds_read2st64_b32 v[86:87], v225 offset0:22 offset1:23\n"
"v_add_f32 v45, v45, v77\nM 136,176,40\nR 172,184,16384\nG 10\nv_add_f32 v46, v46, v78\nv_add_f32 v47, v47, v79\n"
"M 136,180,56\nbuffer_load_dwordx4 v220, s[12:15], s63 offen lds\nG 7\nv_add_f32 v48, v48, v80\nM 152,176,44\nR 174,184,20480\n"
"v_add_f32 v49, v49, v81\nG 6\nv_add_f32 v50, v50, v82\nM 152,180,60\nds_read2st64_b32 v[88:89], v225 offset0:24 offset1:25\nv_add_f32 v51, v51, v83\n"
"M 108,160,32\nR 176,185,8192\nG 6\nv_add_f32 v52, v52, v84\nv_add_f32 v53, v53, v85\nM 108,164,48\n"
"buffer_load_dwordx4 v221, s[12:15], s63 offen offset:1024 lds\nG 4\nv_add_f32 v54, v54, v86\nM 124,160,36\nR 178,185,12288\ns_mov_b32 s60, 157184\n"
"v_bfe_u32 v246, v252, 4, 4\nv_lshl_add_u32 v246, v246, 3, s60\nv_add_f32 v55, v55, v87\nM 124,164,52\nds_read_b64 v[238:239], v246 offset:0\ns_add_u32 m0, s50, 73728\n"
"ds_read2st64_b32 v[90:91], v225 offset0:26 offset1:27\nM 140,160,40\nR 180,185,16384\nds_read_b64 v[240:241], v246 offset:128\nds_read2st64_b32 v[92:93], v225 offset0:28 offset1:29\nM 140,164,56\n"
"ds_read_b64 v[242:243], v246 offset:256\ns_add_u32 s63, s54, 2752640\nG 8\nv_add_f32 v56, v56, v88\nM 156,160,44\nR 182,185,20480\n"
"ds_read_b64 v[244:245], v246 offset:384\nv_add_f32 v57, v57, v89\nM 156,164,60\nds_read_b64 v[246:247], v246 offset:0\nM 96,168,64\nR 160,186,8192\n"
"ds_read2st64_b32 v[94:95], v225 offset0:30 offset1:31\nM 96,172,80\nbuffer_load_dwordx4 v220, s[12:15], s63 offen lds\nM 112,168,68\nR 162,186,12288\nG 10\n"
"v_add_f32 v58, v58, v90\nM 112,172,84\nv_add_f32 v59, v59, v91\nG 7\nv_add_f32 v60, v60, v92\nM 128,168,72\n"
"R 164,186,16384\nv_add_f32 v61, v61, v93\nM 128,172,88\nbuffer_load_dwordx4 v221, s[12:15], s63 offen offset:1024 lds\nM 144,168,76\nR 166,186,20480\n"
"s_add_u32 m0, s50, 81920\nM 144,172,92\nG 7\nG 3\nv_add_f32 v62, v62, v94\nv_add_f32 v63, v63, v95\n"
"M 100,176,64\nR 168,187,8192\nv_mfma_f32_16x16x32_bf16 v[32:35], v[238:241], v[208:211], v[32:35]\ns_add_u32 s63, s54, 2982016\nM 100,180,80\nv_mfma_f32_16x16x32_bf16 v[36:39], v[240:243], v[208:211], v[36:39]\n"
"buffer_load_dwordx4 v220, s[12:15], s63 offen lds\nM 116,176,68\nR 170,187,12288\nv_mfma_f32_16x16x32_bf16 v[40:43], v[242:245], v[208:211], v[40:43]\nM 116,180,84\nv_mfma_f32_16x16x32_bf16 v[44:47], v[244:247], v[208:211], v[44:47]\n"
"buffer_load_dwordx4 v221, s[12:15], s63 offen offset:1024 lds\nM 132,176,72\nR 172,187,16384\nv_mfma_f32_16x16x32_bf16 v[48:51], v[244:247], v[210:213], v[48:51]\ns_add_u32 s63, s54, 3211392\nM 132,180,88\n"
"v_mfma_f32_16x16x32_bf16 v[52:55], v[238:241], v[210:213], v[52:55]\ns_add_u32 m0, s50, 90112\nM 148,176,76\nR 174,187,20480\nv_mfma_f32_16x16x32_bf16 v[56:59], v[240:243], v[210:213], v[56:59]\nM 148,180,92\n"
"v_mfma_f32_16x16x32_bf16 v[60:63], v[242:245], v[210:213], v[60:63]\nG 4\nM 104,160,64\nR 176,184,24576\nM 104,164,80\nbuffer_load_dwordx4 v220, s[12:15], s63 offen lds\n"
"M 120,160,68\nR 178,184,28672\nM 120,164,84\nM 136,160,72\nR 180,184,32768\ns_waitcnt vmcnt(7)\n"
"s_barrier\nM 136,164,88\nM 152,160,76\nR 182,184,36864\nv_mul_f32 v230, v32, v32\nv_mul_f32 v231, v33, v33\n"
"M 152,164,92\ns_add_u32 s84, s73, 37748736\ns_add_u32 s85, s73, 37863424\nG 4\nv_mul_f32 v232, v34, v34\nM 108,168,64\n"
"R 160,185,24576\nv_mul_f32 v233, v35, v35\nv_fma_f32 v230, v230, v215, v214\nM 108,172,80\nbuffer_load_dwordx4 v221, s[12:15], s63 offen offset:1024 lds\nv_fma_f32 v231, v231, v215, v214\n"
"M 124,168,68\nR 162,185,28672\nv_fma_f32 v232, v232, v215, v214\nv_fma_f32 v233, v233, v215, v214\nM 124,172,84\ns_add_u32 m0, s50, 98304\n"
"v_mul_f32 v230, v32, v230\nv_mul_f32 v231, v33, v231\nv_mul_f32 v232, v34, v232\nM 140,168,72\nR 164,185,32768\nv_mul_f32 v233, v35, v233\n"
"v_exp_f32 v230, v230\nM 140,172,88\ns_add_u32 s63, s54, 3440768\nv_exp_f32 v231, v231\nv_exp_f32 v232, v232\nM 156,168,76\n"
"R 166,185,36864\nv_fma_f32 v230, v230, v216, v216\nv_exp_f32 v233, v233\nM 156,172,92\nG 4\nv_rcp_f32 v230, v230\n"
"v_fma_f32 v231, v231, v216, v216\nM 96,176,96\nR 168,186,24576\nv_fma_f32 v232, v232, v216, v216\nv_fma_f32 v233, v233, v216, v216\nM 96,180,112\n"
"buffer_load_dwordx4 v220, s[12:15], s63 offen lds\nv_rcp_f32 v231, v231\nM 112,176,100\nR 170,186,28672\nv_mul_f32 v230, v32, v230\nv_rcp_f32 v232, v232\n"
"M 112,180,116\nv_rcp_f32 v233, v233\nv_mul_f32 v231, v33, v231\nM 128,176,104\nR 172,186,32768\nv_mul_f32 v232, v34, v232\n"
"v_mul_f32 v233, v35, v233\nM 128,180,120\nv_cvt_pk_bf16_f32 v32, v230, v231\nv_cvt_pk_bf16_f32 v33, v232, v233\nv_mul_f32 v234, v36, v36\nM 144,176,108\n"
"R 174,186,36864\nv_mul_f32 v235, v37, v37\nv_mul_f32 v236, v38, v38\nM 144,180,124\nG 4\nv_mul_f32 v237, v39, v39\n"
"v_fma_f32 v234, v234, v215, v214\nv_fma_f32 v235, v235, v215, v214\nM 100,160,96\nR 176,187,24576\nv_fma_f32 v236, v236, v215, v214\nv_fma_f32 v237, v237, v215, v214\n"
"M 100,164,112\nbuffer_load_dwordx4 v221, s[12:15], s63 offen offset:1024 lds\nv_mul_f32 v234, v36, v234\nM 116,160,100\nR 178,187,28672\nv_mul_f32 v235, v37, v235\n"
"v_mul_f32 v236, v38, v236\nM 116,164,116\ns_add_u32 m0, s50, 106496\nv_exp_f32 v234, v234\nv_mul_f32 v237, v39, v237\nM 132,160,104\n"
"R 180,187,32768\nv_fma_f32 v234, v234, v216, v216\nv_exp_f32 v235, v235\nM 132,164,120\ns_add_u32 s63, s55, 0\nv_exp_f32 v236, v236\n"
"v_exp_f32 v237, v237\nM 148,160,108\nR 182,187,36864\nv_fma_f32 v235, v235, v216, v216\nv_fma_f32 v236, v236, v216, v216\nM 148,164,124\n"
"G 4\nv_rcp_f32 v234, v234\nv_fma_f32 v237, v237, v216, v216\nM 104,168,96\nR 160,184,40960\nv_mul_f32 v234, v36, v234\n"
"v_rcp_f32 v235, v235\nM 104,172,112\nbuffer_load_dwordx4 v222, s[12:15], s63 offen lds\nv_rcp_f32 v236, v236\nM 120,168,100\nR 162,184,45056\n"
"v_mul_f32 v235, v37, v235\nv_rcp_f32 v237, v237\nM 120,172,116\nv_mul_f32 v236, v38, v236\nv_mul_f32 v237, v39, v237\nv_cvt_pk_bf16_f32 v34, v234, v235\n"
"M 136,168,104\nR 164,184,49152\nv_cvt_pk_bf16_f32 v35, v236, v237\nv_mul_f32 v230, v40, v40\nM 136,172,120\nbuffer_store_dwordx4 v[32:35], v252, s[20:23], s84 offen offset:0 sc1\n"
"v_mul_f32 v231, v41, v41\nM 152,168,108\nR 166,184,53248\nv_mul_f32 v232, v42, v42\nv_mul_f32 v233, v43, v43\nM 152,172,124\n"
"G 4\nv_fma_f32 v230, v230, v215, v214\nv_fma_f32 v231, v231, v215, v214\nv_fma_f32 v232, v232, v215, v214\nM 108,176,96\nR 168,185,40960\n"
"v_fma_f32 v233, v233, v215, v214\nv_mul_f32 v230, v40, v230\nM 108,180,112\nbuffer_load_dwordx4 v223, s[12:15], s63 offen offset:1024 lds\nv_mul_f32 v231, v41, v231\nM 124,176,100\n"
"R 170,185,45056\nv_mul_f32 v232, v42, v232\nv_mul_f32 v233, v43, v233\nM 124,180,116\ns_add_u32 m0, s50, 114688\nv_exp_f32 v230, v230\n"
"v_exp_f32 v231, v231\nM 140,176,104\nR 172,185,49152\nv_fma_f32 v230, v230, v216, v216\nv_exp_f32 v232, v232\nM 140,180,120\n"
"s_add_u32 s63, s55, 458752\nv_exp_f32 v233, v233\nv_fma_f32 v231, v231, v216, v216\nM 156,176,108\nR 174,185,53248\nv_fma_f32 v232, v232, v216, v216\n"
"v_fma_f32 v233, v233, v216, v216\nM 156,180,124\nG 4\nv_rcp_f32 v230, v230\nv_rcp_f32 v231, v231\nM 96,160,128\n"
"R 176,186,40960\nv_mul_f32 v230, v40, v230\nv_rcp_f32 v232, v232\nM 96,164,144\nbuffer_load_dwordx4 v222, s[12:15], s63 offen lds\nv_rcp_f32 v233, v233\n"
"M 112,160,132\nR 178,186,45056\nv_mul_f32 v231, v41, v231\nv_mul_f32 v232, v42, v232\nM 112,164,148\nv_mul_f32 v233, v43, v233\n"
"v_cvt_pk_bf16_f32 v40, v230, v231\nv_cvt_pk_bf16_f32 v41, v232, v233\nM 128,160,136\nR 180,186,49152\nv_mul_f32 v234, v44, v44\nv_mul_f32 v235, v45, v45\n"
"M 128,164,152\nbuffer_load_dwordx4 v223, s[12:15], s63 offen offset:1024 lds\nv_mul_f32 v236, v46, v46\nM 144,160,140\nR 182,186,53248\ns_add_u32 s63, s55, 917504\n"
"v_mul_f32 v237, v47, v47\nM 144,164,156\nG 4\nv_fma_f32 v234, v234, v215, v214\nv_fma_f32 v235, v235, v215, v214\nv_fma_f32 v236, v236, v215, v214\n"
"M 100,168,128\nR 160,187,40960\nv_fma_f32 v237, v237, v215, v214\nv_mul_f32 v234, v44, v234\nM 100,172,144\ns_add_u32 m0, s50, 0\n"
"v_exp_f32 v234, v234\nv_mul_f32 v235, v45, v235\nM 116,168,132\nR 162,187,45056\nv_mul_f32 v236, v46, v236\nv_mul_f32 v237, v47, v237\n"
"M 116,172,148\nbuffer_load_dwordx4 v222, s[12:15], s63 offen lds\nv_exp_f32 v235, v235\nM 132,168,136\nR 164,187,49152\nv_fma_f32 v234, v234, v216, v216\n"
"v_exp_f32 v236, v236\nM 132,172,152\nv_exp_f32 v237, v237\nv_fma_f32 v235, v235, v216, v216\nM 148,168,140\nR 166,187,53248\n"
"v_fma_f32 v236, v236, v216, v216\nv_fma_f32 v237, v237, v216, v216\nM 148,172,156\nG 4\nv_rcp_f32 v234, v234\nv_rcp_f32 v235, v235\n"
"M 104,176,128\nR 168,184,57344\ns_waitcnt vmcnt(10)\ns_barrier\nv_rcp_f32 v236, v236\nM 104,180,144\n"
"buffer_load_dwordx4 v223, s[12:15], s63 offen offset:1024 lds\nv_rcp_f32 v237, v237\nM 120,176,132\nR 170,184,61440\nv_mul_f32 v234, v44, v234\nv_mul_f32 v235, v45, v235\n"
"M 120,180,148\ns_add_u32 m0, s50, 8192\nv_mul_f32 v236, v46, v236\nv_mul_f32 v237, v47, v237\nv_cvt_pk_bf16_f32 v42, v234, v235\nM 136,176,136\n"
"R 172,188,0\nv_cvt_pk_bf16_f32 v43, v236, v237\nv_mul_f32 v230, v48, v48\nM 136,180,152\ns_add_u32 s63, s55, 1376256\nv_mul_f32 v231, v49, v49\n"
"v_mul_f32 v232, v50, v50\nv_mul_f32 v233, v51, v51\nM 152,176,140\nR 174,188,4096\nv_fma_f32 v230, v230, v218, v217\nv_fma_f32 v231, v231, v218, v217\n"
"M 152,180,156\nG 4\nv_fma_f32 v232, v232, v218, v217\nv_fma_f32 v233, v233, v218, v217\nv_mul_f32 v230, v48, v230\nM 108,160,128\n"
"R 176,185,57344\nv_mul_f32 v231, v49, v231\nv_mul_f32 v232, v50, v232\nM 108,164,144\nbuffer_load_dwordx4 v222, s[12:15], s63 offen lds\nv_mul_f32 v233, v51, v233\n"
"M 124,160,132\nR 178,185,61440\nv_mul_f32 v234, v52, v52\nv_exp_f32 v230, v230\nM 124,164,148\nbuffer_store_dwordx4 v[40:43], v252, s[20:23], s84 offen offset:1024 sc1\n"
"v_exp_f32 v231, v231\nM 140,160,136\nR 180,189,0\nv_fma_f32 v230, v230, v219, v219\nv_exp_f32 v232, v232\nM 140,164,152\n"
"v_exp_f32 v233, v233\nv_fma_f32 v231, v231, v219, v219\nM 156,160,140\nR 182,189,4096\nv_fma_f32 v232, v232, v219, v219\nv_fma_f32 v233, v233, v219, v219\n"
"M 156,164,156\nG 4\nv_rcp_f32 v230, v230\nv_rcp_f32 v231, v231\nM 96,168,160\nR 160,186,57344\n"
"v_mul_f32 v230, v48, v230\nv_rcp_f32 v232, v232\nM 96,172,176\nbuffer_load_dwordx4 v223, s[12:15], s63 offen offset:1024 lds\nv_rcp_f32 v233, v233\nM 112,168,164\n"
"R 162,186,61440\nv_mul_f32 v231, v49, v231\nv_mul_f32 v232, v50, v232\nM 112,172,180\ns_add_u32 m0, s50, 16384\nv_mul_f32 v233, v51, v233\n"
"v_cvt_pk_bf16_f32 v48, v230, v231\nv_cvt_pk_bf16_f32 v49, v232, v233\nM 128,168,168\nR 164,190,0\nv_mul_f32 v235, v53, v53\nv_mul_f32 v236, v54, v54\n"
"M 128,172,184\ns_add_u32 s63, s55, 1835008\nv_mul_f32 v237, v55, v55\nv_fma_f32 v234, v234, v218, v217\nv_fma_f32 v235, v235, v218, v217\nM 144,168,172\n"
"R 166,190,4096\nv_fma_f32 v236, v236, v218, v217\nv_fma_f32 v237, v237, v218, v217\nM 144,172,188\nG 4\nv_mul_f32 v234, v52, v234\n"
"v_mul_f32 v235, v53, v235\nv_mul_f32 v236, v54, v236\nM 100,176,160\nR 168,187,57344\nv_mul_f32 v237, v55, v237\nv_exp_f32 v234, v234\n"
"M 100,180,176\nbuffer_load_dwordx4 v222, s[12:15], s63 offen lds\nv_exp_f32 v235, v235\nM 116,176,164\nR 170,187,61440\nv_fma_f32 v234, v234, v219, v219\n"
"v_exp_f32 v236, v236\nM 116,180,180\nv_exp_f32 v237, v237\nv_fma_f32 v235, v235, v219, v219\nM 132,176,168\nR 172,191,0\n"
"v_fma_f32 v236, v236, v219, v219\nv_fma_f32 v237, v237, v219, v219\nM 132,180,184\nv_rcp_f32 v234, v234\nv_rcp_f32 v235, v235\nM 148,176,172\n"
"R 174,191,4096\nv_mul_f32 v234, v52, v234\nv_rcp_f32 v236, v236\nM 148,180,188\nG 4\nv_rcp_f32 v237, v237\n"
"v_mul_f32 v235, v53, v235\nM 104,160,160\nR 176,188,8192\nv_mul_f32 v236, v54, v236\nv_mul_f32 v237, v55, v237\nM 104,164,176\n"
"buffer_load_dwordx4 v223, s[12:15], s63 offen offset:1024 lds\nv_cvt_pk_bf16_f32 v50, v234, v235\nM 120,160,164\nR 178,188,12288\ns_add_u32 s63, s55, 2293760\nv_cvt_pk_bf16_f32 v51, v236, v237\n"
"M 120,164,180\ns_add_u32 m0, s50, 24576\nv_mul_f32 v230, v56, v56\nv_mul_f32 v231, v57, v57\nv_mul_f32 v232, v58, v58\nM 136,160,168\n"
"R 180,188,16384\nv_mul_f32 v233, v59, v59\nv_fma_f32 v230, v230, v218, v217\nM 136,164,184\nbuffer_load_dwordx4 v222, s[12:15], s63 offen lds\nM 152,160,172\n"
"R 182,188,20480\nv_fma_f32 v231, v231, v218, v217\nM 152,164,188\nG 4\nv_fma_f32 v232, v232, v218, v217\nv_fma_f32 v233, v233, v218, v217\n"
"M 108,168,160\nR 160,189,8192\nv_mul_f32 v230, v56, v230\nM 108,172,176\nbuffer_store_dwordx4 v[48:51], v252, s[20:23], s85 offen offset:0 sc1\nM 124,168,164\n"
"R 162,189,12288\nv_mul_f32 v231, v57, v231\nM 124,172,180\nbuffer_load_dwordx4 v223, s[12:15], s63 offen offset:1024 lds\nM 140,168,168\nR 164,189,16384\n"
"s_add_u32 s63, s55, 2752512\nM 140,172,184\ns_add_u32 m0, s50, 32768\nv_exp_f32 v230, v230\nM 156,168,172\nR 166,189,20480\n"
"v_mul_f32 v232, v58, v232\nM 156,172,188\nG 4\nv_exp_f32 v231, v231\nM 96,176,192\nR 168,190,8192\n"
"v_mul_f32 v233, v59, v233\nM 96,180,208\nbuffer_load_dwordx4 v222, s[12:15], s63 offen lds\nM 112,176,196\nR 170,190,12288\ns_waitcnt vmcnt(13)\n"
"s_barrier\nM 112,180,212\nv_exp_f32 v232, v232\nM 128,176,200\nR 172,190,16384\nv_fma_f32 v230, v230, v219, v219\n"
"M 128,180,216\nv_exp_f32 v233, v233\nM 144,176,204\nR 174,190,20480\nv_fma_f32 v231, v231, v219, v219\nM 144,180,220\n"
"G 4\nv_rcp_f32 v230, v230\nM 100,160,192\nR 176,191,8192\nv_fma_f32 v232, v232, v219, v219\nM 100,164,208\n"
"buffer_load_dwordx4 v223, s[12:15], s63 offen offset:1024 lds\nM 116,160,196\nR 178,191,12288\ns_mov_b32 s60, 157696\nv_bfe_u32 v246, v252, 4, 4\nv_lshl_add_u32 v246, v246, 3, s60\n"
"M 116,164,212\nds_read_b64 v[238:239], v246 offset:0\ns_add_u32 m0, s50, 40960\nM 132,160,200\nR 180,191,16384\nds_read_b64 v[240:241], v246 offset:128\n"
"M 132,164,216\nds_read_b64 v[242:243], v246 offset:256\ns_add_u32 s63, s55, 3211264\nM 148,160,204\nR 182,191,20480\nds_read_b64 v[244:245], v246 offset:384\n"
"M 148,164,220\nds_read_b64 v[246:247], v246 offset:0\nG 9\nM 104,168,192\nR 160,188,24576\nv_fma_f32 v233, v233, v219, v219\n"
"M 104,172,208\nbuffer_load_dwordx4 v222, s[12:15], s63 offen lds\nM 120,168,196\nR 162,188,28672\nv_mul_f32 v230, v56, v230\nM 120,172,212\n"
"v_rcp_f32 v231, v231\nM 136,168,200\nR 164,188,32768\nv_mul_f32 v231, v57, v231\nM 136,172,216\nv_rcp_f32 v232, v232\n"
"M 152,168,204\nR 166,188,36864\nv_mul_f32 v232, v58, v232\nM 152,172,220\nG 6\nv_rcp_f32 v233, v233\n"
"M 108,176,192\nR 168,189,24576\nv_mfma_f32_16x16x32_bf16 v[96:99], v[238:241], v[208:211], v[96:99]\nv_mul_f32 v233, v59, v233\nM 108,180,208\nbuffer_load_dwordx4 v223, s[12:15], s63 offen offset:1024 lds\n"
"M 124,176,196\nR 170,189,28672\nv_mfma_f32_16x16x32_bf16 v[100:103], v[240:243], v[208:211], v[100:103]\nv_cvt_pk_bf16_f32 v56, v230, v231\nM 124,180,212\ns_add_u32 m0, s50, 49152\n"
"v_cvt_pk_bf16_f32 v57, v232, v233\nv_mul_f32 v234, v60, v60\nM 140,176,200\nR 172,189,32768\nG 8\nv_mfma_f32_16x16x32_bf16 v[104:107], v[242:245], v[208:211], v[104:107]\n"
"v_mul_f32 v235, v61, v61\nM 140,180,216\ns_add_u32 s63, s54, 256\nv_mul_f32 v236, v62, v62\nv_mul_f32 v237, v63, v63\nM 156,176,204\n"
"R 174,189,36864\nG 8\nv_mfma_f32_16x16x32_bf16 v[108:111], v[244:247], v[208:211], v[108:111]\nv_fma_f32 v234, v234, v218, v217\nM 156,180,220\nG 4\n"
"v_fma_f32 v235, v235, v218, v217\nv_fma_f32 v236, v236, v218, v217\nM 96,160,224\nR 176,190,24576\nv_mfma_f32_16x16x32_bf16 v[112:115], v[244:247], v[210:213], v[112:115]\nv_fma_f32 v237, v237, v218, v217\n"
"M 96,164,240\nbuffer_load_dwordx4 v220, s[12:15], s63 offen lds\nM 112,160,228\nR 178,190,28672\nv_mfma_f32_16x16x32_bf16 v[116:119], v[238:241], v[210:213], v[116:119]\nv_mul_f32 v234, v60, v234\n"
"M 112,164,244\nv_exp_f32 v234, v234\nM 128,160,232\nR 180,190,32768\nv_mfma_f32_16x16x32_bf16 v[120:123], v[240:243], v[210:213], v[120:123]\nv_mul_f32 v235, v61, v235\n"
"M 128,164,248\nbuffer_load_dwordx4 v221, s[12:15], s63 offen offset:1024 lds\nM 144,160,236\nR 182,190,36864\nv_mfma_f32_16x16x32_bf16 v[124:127], v[242:245], v[210:213], v[124:127]\ns_add_u32 s63, s54, 229632\n"
"M 144,164,252\nG 4\nv_exp_f32 v235, v235\nM 100,168,224\nR 160,191,24576\nv_mul_f32 v236, v62, v236\n"
"M 100,172,240\ns_add_u32 m0, s50, 57344\nv_exp_f32 v236, v236\nM 116,168,228\nR 162,191,28672\nv_mul_f32 v237, v63, v237\n"
"M 116,172,244\nbuffer_load_dwordx4 v220, s[12:15], s63 offen lds\nM 132,168,232\nR 164,191,32768\nv_fma_f32 v234, v234, v219, v219\nM 132,172,248\n"
"v_exp_f32 v237, v237\nM 148,168,236\nR 166,191,36864\nv_fma_f32 v235, v235, v219, v219\nM 148,172,252\nG 4\n"
"v_rcp_f32 v234, v234\nM 104,176,224\nR 168,188,40960\nv_fma_f32 v236, v236, v219, v219\nM 104,180,240\nbuffer_load_dwordx4 v221, s[12:15], s63 offen offset:1024 lds\n"
"M 120,176,228\nR 170,188,45056\nv_fma_f32 v237, v237, v219, v219\nM 120,180,244\ns_add_u32 m0, s50, 65536\nv_rcp_f32 v235, v235\n"
"M 136,176,232\nR 172,190,40960\nv_mul_f32 v234, v60, v234\nM 136,180,248\ns_add_u32 s63, s54, 459008\nv_rcp_f32 v236, v236\n"
"M 152,176,236\nR 174,190,45056\nv_mul_f32 v235, v61, v235\nM 152,180,252\nG 4\nv_rcp_f32 v237, v237\n"
"M 108,160,224\nR 176,189,40960\nv_mul_f32 v236, v62, v236\nM 108,164,240\nbuffer_load_dwordx4 v220, s[12:15], s63 offen lds\nM 124,160,228\n"
"R 178,189,45056\nv_mul_f32 v237, v63, v237\nM 124,164,244\nv_cvt_pk_bf16_f32 v58, v234, v235\nv_cvt_pk_bf16_f32 v59, v236, v237\nM 140,160,232\n"
"R 180,191,40960\nbuffer_store_dwordx4 v[56:59], v252, s[20:23], s85 offen offset:1024 sc1\nM 140,164,248\nbuffer_load_dwordx4 v221, s[12:15], s63 offen offset:1024 lds\nM 156,160,236\nR 182,191,45056\n"
"M 156,164,252\ns_waitcnt vmcnt(16)\ns_barrier\nG 4\nv_mfma_f32_16x16x32_bf16 v[0:3], v[168:171], a[0:3], 0\nR 160,188,49152\n"
"M 0,172,16\ns_mov_b32 m0, s51\nv_mfma_f32_16x16x32_bf16 v[16:19], v[168:171], a[4:7], 0\nR 162,188,53248\nM 16,172,20\nds_write_addtid_b32 v128 offset:0\n"
"v_mfma_f32_16x16x32_bf16 v[8:11], v[168:171], a[8:11], 0\nR 164,190,49152\nM 8,172,24\nds_write_addtid_b32 v129 offset:256\nv_mfma_f32_16x16x32_bf16 v[24:27], v[168:171], a[12:15], 0\nR 166,190,53248\n"
"M 24,172,28\nds_write_addtid_b32 v130 offset:512\nG 7\nv_mfma_f32_16x16x32_bf16 v[4:7], v[176:179], a[0:3], 0\nR 168,189,49152\nM 4,180,16\n"
"ds_write_addtid_b32 v131 offset:768\nv_mfma_f32_16x16x32_bf16 v[20:23], v[176:179], a[4:7], 0\nR 170,189,53248\nM 20,180,20\nds_write_addtid_b32 v132 offset:1024\nv_mfma_f32_16x16x32_bf16 v[12:15], v[176:179], a[8:11], 0\n"
"R 172,191,49152\nM 12,180,24\nds_write_addtid_b32 v133 offset:1280\nv_mfma_f32_16x16x32_bf16 v[28:31], v[176:179], a[12:15], 0\nR 174,191,53248\nM 28,180,28\n"
"ds_write_addtid_b32 v134 offset:1536\nds_write_addtid_b32 v135 offset:1792\nG 10\nM 0,160,32\nR 176,184,0\nds_write_addtid_b32 v136 offset:2048\n"
"M 0,164,48\nds_write_addtid_b32 v137 offset:2304\nds_write_addtid_b32 v138 offset:2560\nM 16,160,36\nR 178,184,4096\nds_write_addtid_b32 v139 offset:2816\n"
"M 16,164,52\nds_write_addtid_b32 v140 offset:3072\nds_write_addtid_b32 v141 offset:3328\nM 8,160,40\nR 180,186,0\nds_write_addtid_b32 v142 offset:3584\n"
"M 8,164,56\nds_write_addtid_b32 v143 offset:3840\nds_write_addtid_b32 v144 offset:4096\nM 24,160,44\nR 182,186,4096\nds_write_addtid_b32 v145 offset:4352\n"
"M 24,164,60\nds_write_addtid_b32 v146 offset:4608\nds_write_addtid_b32 v147 offset:4864\nG 15\nM 4,168,32\nR 160,185,0\n"
"ds_write_addtid_b32 v148 offset:5120\nM 4,172,48\nds_write_addtid_b32 v149 offset:5376\nds_write_addtid_b32 v150 offset:5632\nM 20,168,36\nR 162,185,4096\n"
"ds_write_addtid_b32 v151 offset:5888\nM 20,172,52\nds_write_addtid_b32 v152 offset:6144\nds_write_addtid_b32 v153 offset:6400\nM 12,168,40\nR 164,187,0\n"
"ds_write_addtid_b32 v154 offset:6656\nM 12,172,56\nds_write_addtid_b32 v155 offset:6912\nds_write_addtid_b32 v156 offset:7168\nM 28,168,44\nR 166,187,4096\n"
"ds_write_addtid_b32 v157 offset:7424\nM 28,172,60\nds_write_addtid_b32 v158 offset:7680\nds_write_addtid_b32 v159 offset:7936\nG 15\nM 0,176,64\n"
"R 168,184,8192\ns_add_u32 m0, s50, 73728\ns_add_u32 s63, s54, 688384\nM 0,180,80\nbuffer_load_dwordx4 v220, s[12:15], s63 offen lds\nM 16,176,68\n"
"R 170,184,12288\ns_waitcnt vmcnt(10) lgkmcnt(2)\ns_barrier\nM 16,180,84\nds_read2st64_b32 v[128:129], v225 offset0:0 offset1:1\nM 8,176,72\n"
"R 172,186,8192\nM 8,180,88\nds_read2st64_b32 v[130:131], v225 offset0:2 offset1:3\nds_read2st64_b32 v[132:133], v225 offset0:4 offset1:5\nM 24,176,76\nR 174,186,12288\n"
"ds_read2st64_b32 v[134:135], v225 offset0:6 offset1:7\nM 24,180,92\nds_read2st64_b32 v[136:137], v225 offset0:8 offset1:9\nds_read2st64_b32 v[138:139], v225 offset0:10 offset1:11\nM 4,160,64\nR 176,185,8192\n"
"ds_read2st64_b32 v[140:141], v225 offset0:12 offset1:13\nM 4,164,80\nbuffer_load_dwordx4 v221, s[12:15], s63 offen offset:1024 lds\nM 20,160,68\nR 178,185,12288\nG 10\n"
"v_add_f32 v96, v96, v128\nM 20,164,84\ns_add_u32 m0, s50, 81920\nds_read2st64_b32 v[142:143], v225 offset0:14 offset1:15\nv_add_f32 v97, v97, v129\nM 12,160,72\n"
"R 180,187,8192\nG 10\nv_add_f32 v98, v98, v130\nM 12,164,88\ns_add_u32 s63, s54, 917760\nds_read2st64_b32 v[144:145], v225 offset0:16 offset1:17\n"
"v_add_f32 v99, v99, v131\nM 28,160,76\nR 182,187,12288\nG 11\nv_add_f32 v100, v100, v132\nM 28,164,92\n"
"ds_read2st64_b32 v[146:147], v225 offset0:18 offset1:19\nG 11\nv_add_f32 v101, v101, v133\nM 0,168,96\nR 160,184,16384\nG 11\n"
"v_add_f32 v102, v102, v134\nM 0,172,112\nbuffer_load_dwordx4 v220, s[12:15], s63 offen lds\nM 16,168,100\nR 162,184,20480\nv_add_f32 v103, v103, v135\n"
"M 16,172,116\nds_read2st64_b32 v[148:149], v225 offset0:20 offset1:21\nG 12\nv_add_f32 v104, v104, v136\nM 8,168,104\nR 164,186,16384\n"
"v_add_f32 v105, v105, v137\nM 8,172,120\nbuffer_load_dwordx4 v221, s[12:15], s63 offen offset:1024 lds\nM 24,168,108\nR 166,186,20480\nG 13\n"
"v_add_f32 v106, v106, v138\nM 24,172,124\nds_read2st64_b32 v[150:151], v225 offset0:22 offset1:23\nG 7\nv_add_f32 v107, v107, v139\nM 4,176,96\n"
"R 168,185,16384\nv_add_f32 v108, v108, v140\nM 4,180,112\nds_read2st64_b32 v[152:153], v225 offset0:24 offset1:25\nv_add_f32 v109, v109, v141\nM 20,176,100\n"
"R 170,185,20480\nv_add_f32 v110, v110, v142\nM 20,180,116\nds_read2st64_b32 v[154:155], v225 offset0:26 offset1:27\nv_add_f32 v111, v111, v143\nM 12,176,104\n"
"R 172,187,16384\nv_add_f32 v112, v112, v144\nM 12,180,120\nds_read2st64_b32 v[156:157], v225 offset0:28 offset1:29\nv_add_f32 v113, v113, v145\nM 28,176,108\n"
"R 174,187,20480\nG 13\nv_add_f32 v114, v114, v146\nM 28,180,124\nds_read2st64_b32 v[158:159], v225 offset0:30 offset1:31\nG 9\n"
"v_add_f32 v115, v115, v147\nM 0,160,128\nR 176,184,24576\nv_add_f32 v116, v116, v148\nM 0,164,144\nv_add_f32 v117, v117, v149\n"
"G 9\nv_add_f32 v118, v118, v150\nM 16,160,132\nR 178,184,28672\nv_add_f32 v119, v119, v151\nM 16,164,148\n"
"G 8\nv_add_f32 v120, v120, v152\nv_add_f32 v121, v121, v153\nM 8,160,136\nR 180,186,24576\nG 7\n"
"v_add_f32 v122, v122, v154\nM 8,164,152\nv_add_f32 v123, v123, v155\nG 5\nv_add_f32 v124, v124, v156\nM 24,160,140\n"
"R 182,186,28672\nv_add_f32 v125, v125, v157\nM 24,164,156\nG 5\nG 4\nv_add_f32 v126, v126, v158\n"
"v_add_f32 v127, v127, v159\nM 4,168,128\nR 160,185,24576\nv_mul_f32 v230, v96, v96\nv_mul_f32 v231, v97, v97\nM 4,172,144\n"
"s_add_u32 s84, s73, 37750784\ns_add_u32 s85, s73, 37865472\nv_mul_f32 v232, v98, v98\nv_mul_f32 v233, v99, v99\nM 20,168,132\nR 162,185,28672\n"
"s_mov_b32 s60, 158720\nv_bfe_u32 v242, v252, 4, 4\nv_lshl_add_u32 v242, v242, 3, s60\nv_fma_f32 v230, v230, v215, v214\nM 20,172,148\nds_read_b64 v[238:239], v242 offset:0\n"
"v_fma_f32 v231, v231, v215, v214\nv_fma_f32 v232, v232, v215, v214\nM 12,168,136\nR 164,187,24576\nds_read_b64 v[240:241], v242 offset:128\nv_fma_f32 v233, v233, v215, v214\n"
"M 12,172,152\nds_read_b64 v[242:243], v242 offset:0\nv_mul_f32 v230, v96, v230\nv_mul_f32 v231, v97, v231\nM 28,168,140\nR 166,187,28672\n"
"v_mul_f32 v232, v98, v232\nv_mul_f32 v233, v99, v233\nM 28,172,156\nG 7\nv_exp_f32 v230, v230\nv_exp_f32 v231, v231\n"
"M 0,176,160\nR 168,184,32768\nv_fma_f32 v230, v230, v216, v216\nv_exp_f32 v232, v232\nM 0,180,176\nv_exp_f32 v233, v233\n"
"v_fma_f32 v231, v231, v216, v216\nM 16,176,164\nR 170,184,36864\nv_fma_f32 v232, v232, v216, v216\nv_fma_f32 v233, v233, v216, v216\nM 16,180,180\n"
"v_rcp_f32 v230, v230\nv_rcp_f32 v231, v231\nM 8,176,168\nR 172,186,32768\nv_mul_f32 v230, v96, v230\nv_rcp_f32 v232, v232\n"
"M 8,180,184\nv_rcp_f32 v233, v233\nv_mul_f32 v231, v97, v231\nM 24,176,172\nR 174,186,36864\nv_mul_f32 v232, v98, v232\n"
"v_mul_f32 v233, v99, v233\nM 24,180,188\nG 4\nv_cvt_pk_bf16_f32 v96, v230, v231\nv_cvt_pk_bf16_f32 v97, v232, v233\nv_mul_f32 v234, v100, v100\n"
"M 4,160,160\nR 176,185,32768\nv_mfma_f32_16x16x32_bf16 v[0:3], v[238:241], v[208:211], v[0:3]\ns_waitcnt vmcnt(7)\ns_barrier\nv_mul_f32 v235, v101, v101\n"
"M 4,164,176\ns_add_u32 m0, s50, 90112\nv_mul_f32 v236, v102, v102\nv_mul_f32 v237, v103, v103\nv_fma_f32 v234, v234, v215, v214\nM 20,160,164\n"
"R 178,185,36864\nv_mfma_f32_16x16x32_bf16 v[4:7], v[240:243], v[208:211], v[4:7]\nv_fma_f32 v235, v235, v215, v214\nv_fma_f32 v236, v236, v215, v214\nM 20,164,180\ns_add_u32 s63, s54, 1147136\n"
"v_fma_f32 v237, v237, v215, v214\nv_mul_f32 v234, v100, v234\nv_mul_f32 v235, v101, v235\nM 12,160,168\nR 180,187,32768\nv_mov_b32 v226, s46\n"
"v_mfma_f32_16x16x32_bf16 v[16:19], v[240:243], v[210:213], v[16:19]\nv_mul_f32 v236, v102, v236\nM 12,164,184\nv_mov_b32 v227, s47\nv_mul_f32 v237, v103, v237\nv_exp_f32 v234, v234\n"
"M 28,160,172\nR 182,187,36864\ns_add_u32 s61, s81, 845824\ns_lshl_b32 s62, s44, 5\ns_add_u32 s61, s61, s62\ns_waitcnt vmcnt(0)\n"
"s_mov_b64 exec, 1\nbuffer_atomic_swap_x2 v[226:227], off, s[20:23], s61 sc1\ns_mov_b64 exec, -1\nv_mfma_f32_16x16x32_bf16 v[20:23], v[238:241], v[210:213], v[20:23]\nv_exp_f32 v235, v235\nM 28,164,188\n"
"buffer_load_dwordx4 v220, s[12:15], s63 offen lds\nG 4\nv_exp_f32 v236, v236\nM 0,168,192\nR 160,184,40960\nbuffer_load_dwordx4 v221, s[12:15], s63 offen offset:1024 lds\n"
"v_exp_f32 v237, v237\nM 0,172,208\ns_add_u32 m0, s50, 98304\ns_add_u32 s63, s54, 1376512\nbuffer_load_dwordx4 v220, s[12:15], s63 offen lds\nv_fma_f32 v234, v234, v216, v216\n"
"M 16,168,196\nR 162,184,45056\nv_fma_f32 v235, v235, v216, v216\nv_fma_f32 v236, v236, v216, v216\nM 16,172,212\nbuffer_load_dwordx4 v221, s[12:15], s63 offen offset:1024 lds\n"
"v_fma_f32 v237, v237, v216, v216\nM 8,168,200\nR 164,186,40960\nv_mul_f32 v230, v104, v104\nv_rcp_f32 v234, v234\nM 8,172,216\n"
"v_rcp_f32 v235, v235\nv_rcp_f32 v236, v236\nM 24,168,204\nR 166,186,45056\nv_mul_f32 v234, v100, v234\nv_rcp_f32 v237, v237\n"
"M 24,172,220\nG 4\nv_mul_f32 v235, v101, v235\nv_mul_f32 v236, v102, v236\nv_mul_f32 v237, v103, v237\nM 4,176,192\n"
"R 168,185,40960\nv_cvt_pk_bf16_f32 v98, v234, v235\nv_cvt_pk_bf16_f32 v99, v236, v237\nM 4,180,208\nbuffer_store_dwordx4 v[96:99], v252, s[20:23], s84 offen offset:0 sc1\nv_mul_f32 v231, v105, v105\n"
"M 20,176,196\nR 170,185,45056\nv_mul_f32 v232, v106, v106\nv_mul_f32 v233, v107, v107\nM 20,180,212\ns_add_u32 m0, s50, 106496\n"
"v_fma_f32 v230, v230, v215, v214\nv_fma_f32 v231, v231, v215, v214\nv_fma_f32 v232, v232, v215, v214\nM 12,176,200\nR 172,187,40960\nLq1027:\n"
"s_lshr_b32 s60, s45, 3\ns_lshl2_add_u32 s60, s60, s40\ns_lshl_b32 s60, s60, 8\nv_lshl_add_u32 v224, v252, 3, s60\nv_add_u32 v224, Lq1027+4096-LqPC, v224\ns_mov_b64 exec, 3\n"
"global_load_dword v224, v224, s[98:99]\ns_mov_b64 exec, -1\nv_fma_f32 v233, v233, v215, v214\nM 12,180,216\ns_add_u32 s63, s54, 1605888\nv_mul_f32 v230, v104, v230\n"
"v_mul_f32 v231, v105, v231\nv_mul_f32 v232, v106, v232\nM 28,176,204\nR 174,187,45056\nv_mul_f32 v233, v107, v233\nv_exp_f32 v230, v230\n"
"M 28,180,220\nG 4\nv_exp_f32 v231, v231\nv_exp_f32 v232, v232\nM 0,160,224\nR 176,184,49152\n"
"v_fma_f32 v230, v230, v216, v216\nv_exp_f32 v233, v233\nM 0,164,240\nbuffer_load_dwordx4 v220, s[12:15], s63 offen lds\nv_fma_f32 v231, v231, v216, v216\nM 16,160,228\n"
"R 178,184,53248\nv_fma_f32 v232, v232, v216, v216\nv_fma_f32 v233, v233, v216, v216\nM 16,164,244\nv_rcp_f32 v230, v230\nv_rcp_f32 v231, v231\n"
"M 8,160,232\nR 180,184,57344\nv_mul_f32 v230, v104, v230\nv_rcp_f32 v232, v232\nM 8,164,248\nbuffer_load_dwordx4 v221, s[12:15], s63 offen offset:1024 lds\n"
"v_rcp_f32 v233, v233\nM 24,160,236\nR 182,184,61440\ns_add_u32 s63, s54, 1835264\nv_mul_f32 v231, v105, v231\nM 24,164,252\n"
"G 4\nv_mul_f32 v232, v106, v232\nv_mul_f32 v233, v107, v233\nv_cvt_pk_bf16_f32 v104, v230, v231\nM 4,168,224\nR 160,185,49152\n"
"v_cvt_pk_bf16_f32 v105, v232, v233\nv_mul_f32 v234, v108, v108\nM 4,172,240\ns_add_u32 m0, s50, 114688\nv_mul_f32 v235, v109, v109\nv_mul_f32 v236, v110, v110\n"
"v_mul_f32 v237, v111, v111\nM 20,168,228\nR 162,185,53248\nv_fma_f32 v234, v234, v215, v214\nv_fma_f32 v235, v235, v215, v214\nM 20,172,244\n"
"buffer_load_dwordx4 v220, s[12:15], s63 offen lds\nv_fma_f32 v236, v236, v215, v214\nM 12,168,232\nR 164,185,57344\nv_fma_f32 v237, v237, v215, v214\nv_mul_f32 v234, v108, v234\n"
"M 12,172,248\nv_exp_f32 v234, v234\nv_mul_f32 v235, v109, v235\nM 28,168,236\nR 166,185,61440\nv_mul_f32 v236, v110, v236\n"
"v_mul_f32 v237, v111, v237\nM 28,172,252\ns_barrier\nG 4\nv_exp_f32 v235, v235\nv_mfma_f32_16x16x32_bf16 v[32:35], v[176:179], a[0:3], 0\n"
"R 168,186,49152\nv_fma_f32 v234, v234, v216, v216\nv_exp_f32 v236, v236\nM 32,180,16\ns_mov_b32 m0, s51\nv_exp_f32 v237, v237\n"
"v_fma_f32 v235, v235, v216, v216\nv_mfma_f32_16x16x32_bf16 v[48:51], v[176:179], a[4:7], 0\nR 170,186,53248\nv_fma_f32 v236, v236, v216, v216\nv_fma_f32 v237, v237, v216, v216\nM 48,180,20\n"
"v_rcp_f32 v234, v234\nv_rcp_f32 v235, v235\nv_mfma_f32_16x16x32_bf16 v[64:67], v[176:179], a[8:11], 0\nR 172,186,57344\nv_mul_f32 v234, v108, v234\nv_rcp_f32 v236, v236\n"
"M 64,180,24\nv_rcp_f32 v237, v237\nv_mul_f32 v235, v109, v235\nv_mfma_f32_16x16x32_bf16 v[80:83], v[176:179], a[12:15], 0\nR 174,186,61440\nds_write_addtid_b32 v8 offset:0\n"
"v_mul_f32 v236, v110, v236\nv_mul_f32 v237, v111, v237\nM 80,180,28\nds_write_addtid_b32 v9 offset:256\nG 6\nv_cvt_pk_bf16_f32 v106, v234, v235\n"
"v_cvt_pk_bf16_f32 v107, v236, v237\nv_mul_f32 v230, v112, v112\nv_mfma_f32_16x16x32_bf16 v[36:39], v[160:163], a[0:3], 0\nR 176,187,49152\nds_write_addtid_b32 v10 offset:512\nv_mul_f32 v231, v113, v113\n"
"v_mul_f32 v232, v114, v114\nM 36,164,16\nbuffer_store_dwordx4 v[104:107], v252, s[20:23], s84 offen offset:1024 sc1\nds_write_addtid_b32 v11 offset:768\nv_mul_f32 v233, v115, v115\nv_mfma_f32_16x16x32_bf16 v[52:55], v[160:163], a[4:7], 0\n"
"R 178,187,53248\nds_write_addtid_b32 v12 offset:1024\nv_fma_f32 v230, v230, v218, v217\nv_fma_f32 v231, v231, v218, v217\nM 52,164,20\nds_write_addtid_b32 v13 offset:1280\n"
"v_fma_f32 v232, v232, v218, v217\nv_fma_f32 v233, v233, v218, v217\nv_mul_f32 v230, v112, v230\nv_mfma_f32_16x16x32_bf16 v[68:71], v[160:163], a[8:11], 0\nR 180,187,57344\nds_write_addtid_b32 v14 offset:1536\n"
"v_mul_f32 v231, v113, v231\nv_mul_f32 v232, v114, v232\nM 68,164,24\nds_write_addtid_b32 v15 offset:1792\nv_exp_f32 v230, v230\nv_mul_f32 v233, v115, v233\n"
"v_mfma_f32_16x16x32_bf16 v[84:87], v[160:163], a[12:15], 0\nR 182,187,61440\nds_write_addtid_b32 v24 offset:4096\nv_fma_f32 v230, v230, v219, v219\nv_exp_f32 v231, v231\nM 84,164,28\n"
"ds_write_addtid_b32 v25 offset:4352\nG 14\nv_exp_f32 v232, v232\nv_exp_f32 v233, v233\nv_mfma_f32_16x16x32_bf16 v[40:43], v[168:171], a[0:3], 0\nR 160,188,0\n"
"ds_write_addtid_b32 v26 offset:4608\nds_write_addtid_b32 v27 offset:4864\nds_write_addtid_b32 v28 offset:5120\nds_write_addtid_b32 v29 offset:5376\nds_write_addtid_b32 v30 offset:5632\nds_write_addtid_b32 v31 offset:5888\n"
"v_fma_f32 v231, v231, v219, v219\nv_fma_f32 v232, v232, v219, v219\nM 40,172,16\ns_add_u32 m0, s50, 114688\ns_nop 0\nbuffer_load_dwordx4 v221, s[12:15], s63 offen offset:1024 lds\n"
"v_fma_f32 v233, v233, v219, v219\nv_mfma_f32_16x16x32_bf16 v[56:59], v[168:171], a[4:7], 0\nR 162,188,4096\nv_mul_f32 v234, v116, v116\nv_rcp_f32 v230, v230\nM 56,172,20\n"
"s_waitcnt vmcnt(7) lgkmcnt(1)\ns_barrier\nv_mul_f32 v230, v112, v230\nv_rcp_f32 v231, v231\nv_mfma_f32_16x16x32_bf16 v[72:75], v[168:171], a[8:11], 0\nR 164,188,8192\n"
"v_mov_b32 v248, 0\nv_rcp_f32 v232, v232\nM 72,172,24\ns_add_u32 m0, s50, 0\nds_read2st64_b32 v[8:9], v225 offset0:0 offset1:1\nv_mov_b32 v249, 0\n"
"v_mul_f32 v231, v113, v231\nv_rcp_f32 v233, v233\nv_mfma_f32_16x16x32_bf16 v[88:91], v[168:171], a[12:15], 0\nR 166,188,12288\ns_add_u32 s61, s82, 845824\ns_mov_b64 exec, 0xf\n"
"buffer_atomic_or_x2 v[248:249], v250, s[20:23], s61 offen sc0 sc1\ns_mov_b64 exec, -1\nv_mul_f32 v232, v114, v232\nM 88,172,28\nds_read2st64_b32 v[10:11], v225 offset0:2 offset1:3\nv_mul_f32 v233, v115, v233\n"
"v_cvt_pk_bf16_f32 v112, v230, v231\nv_cvt_pk_bf16_f32 v113, v232, v233\nv_mfma_f32_16x16x32_bf16 v[44:47], v[176:179], a[0:3], 0\nR 168,189,0\nds_read2st64_b32 v[12:13], v225 offset0:4 offset1:5\nv_mul_f32 v235, v117, v117\n"
"v_mul_f32 v236, v118, v118\nM 44,180,16\ns_add_u32 s63, s54, 2064640\nds_read2st64_b32 v[14:15], v225 offset0:6 offset1:7\nv_mul_f32 v237, v119, v119\nv_fma_f32 v234, v234, v218, v217\n"
"v_fma_f32 v235, v235, v218, v217\nv_mfma_f32_16x16x32_bf16 v[60:63], v[176:179], a[4:7], 0\nR 170,189,4096\nds_read2st64_b32 v[24:25], v225 offset0:16 offset1:17\nv_fma_f32 v236, v236, v218, v217\nv_fma_f32 v237, v237, v218, v217\n"
"M 60,180,20\nbuffer_load_dwordx4 v220, s[12:15], s63 offen lds\nG 7\nv_add_f32 v0, v0, v8\nv_mfma_f32_16x16x32_bf16 v[76:79], v[176:179], a[8:11], 0\nR 172,189,8192\n"
"ds_read2st64_b32 v[26:27], v225 offset0:18 offset1:19\nv_add_f32 v1, v1, v9\nM 76,180,24\nds_read2st64_b32 v[28:29], v225 offset0:20 offset1:21\nG 8\nv_add_f32 v2, v2, v10\n"
"v_mfma_f32_16x16x32_bf16 v[92:95], v[176:179], a[12:15], 0\nR 174,189,12288\nv_add_f32 v3, v3, v11\nM 92,180,28\nds_read2st64_b32 v[30:31], v225 offset0:22 offset1:23\nG 8\n"
"v_add_f32 v4, v4, v12\nM 32,160,32\nR 176,190,0\nv_add_f32 v5, v5, v13\nM 32,164,48\nbuffer_load_dwordx4 v221, s[12:15], s63 offen offset:1024 lds\n"
"M 48,160,36\nR 178,190,4096\nG 9\nv_add_f32 v6, v6, v14\nM 48,164,52\ns_add_u32 m0, s50, 8192\n"
"v_add_f32 v7, v7, v15\nG 7\nv_add_f32 v16, v16, v24\nM 64,160,40\nR 180,190,8192\nv_add_f32 v17, v17, v25\n"
"M 64,164,56\ns_add_u32 s63, s54, 2294016\nG 6\nv_add_f32 v18, v18, v26\nv_add_f32 v19, v19, v27\nM 80,160,44\n"
"R 182,190,12288\nG 6\nv_add_f32 v20, v20, v28\nM 80,164,60\nG 5\nv_add_f32 v21, v21, v29\n"
"G 4\nv_add_f32 v22, v22, v30\nM 36,168,32\nR 160,191,0\nv_add_f32 v23, v23, v31\nM 36,172,48\n"
"buffer_load_dwordx4 v220, s[12:15], s63 offen lds\nM 52,168,36\nR 162,191,4096\nv_mul_f32 v234, v116, v234\nM 52,172,52\nv_exp_f32 v234, v234\n"
"M 68,168,40\nR 164,191,8192\nv_mul_f32 v235, v117, v235\nM 68,172,56\nbuffer_load_dwordx4 v221, s[12:15], s63 offen offset:1024 lds\nM 84,168,44\n"
"R 166,191,12288\ns_add_u32 s63, s54, 2523392\nM 84,172,60\nG 4\nv_exp_f32 v235, v235\nM 40,176,32\n"
"R 168,188,16384\nv_mul_f32 v236, v118, v236\nM 40,180,48\ns_add_u32 m0, s50, 16384\nv_exp_f32 v236, v236\nM 56,176,36\n"
"R 170,188,20480\nv_mul_f32 v237, v119, v237\nM 56,180,52\nbuffer_load_dwordx4 v220, s[12:15], s63 offen lds\nM 72,176,40\nR 172,188,24576\n"
"v_fma_f32 v234, v234, v219, v219\nM 72,180,56\ns_add_u32 s64, s68, 37748736\nv_exp_f32 v237, v237\nM 88,176,44\nR 174,188,28672\n"
"v_fma_f32 v235, v235, v219, v219\nM 88,180,60\nG 4\nv_rcp_f32 v234, v234\nM 44,160,32\nR 176,189,16384\n"
"s_waitcnt vmcnt(5)\nv_cmp_eq_u64 s[86:87], s[46:47], v[248:249]\nM 44,164,48\nbuffer_load_dwordx4 v221, s[12:15], s63 offen offset:1024 lds\nM 60,160,36\nR 178,189,20480\n"
"s_and_b32 s62, s86, 0xf\ns_cmp_eq_u32 s62, 0xf\ns_cbranch_scc0 Lq833\nLq834:\nM 60,164,52\ns_add_u32 s65, s68, 37863424\n"
"v_rcp_f32 v235, v235\nM 76,160,40\nR 180,189,24576\nv_fma_f32 v236, v236, v219, v219\nM 76,164,56\nbuffer_load_dwordx4 a[0:3], v252, s[20:23], s64 offen sc1\n"
"M 92,160,44\nR 182,189,28672\nv_fma_f32 v237, v237, v219, v219\nM 92,164,60\nG 4\nv_rcp_f32 v236, v236\n"
"M 32,168,64\nR 160,190,16384\nv_mul_f32 v234, v116, v234\nM 32,172,80\ns_add_u32 s66, s69, 37748736\nv_rcp_f32 v237, v237\n"
"M 48,168,68\nR 162,190,20480\nv_mul_f32 v235, v117, v235\nM 48,172,84\nbuffer_load_dwordx4 a[4:7], v252, s[20:23], s65 offen sc1\nM 64,168,72\n"
"R 164,190,24576\nv_mul_f32 v236, v118, v236\nM 64,172,88\nv_mul_f32 v237, v119, v237\nv_cvt_pk_bf16_f32 v114, v234, v235\nM 80,168,76\n"
"R 166,190,28672\nv_cvt_pk_bf16_f32 v115, v236, v237\nM 80,172,92\nG 4\nv_mul_f32 v230, v120, v120\nv_mul_f32 v231, v121, v121\n"
"M 36,176,64\nR 168,191,16384\nv_mul_f32 v232, v122, v122\nM 36,180,80\nbuffer_load_dwordx4 a[8:11], v252, s[20:23], s66 offen sc1\nM 52,176,68\n"
"R 170,191,20480\nv_mul_f32 v233, v123, v123\nM 52,180,84\ns_add_u32 s67, s69, 37863424\nv_fma_f32 v230, v230, v218, v217\nv_fma_f32 v231, v231, v218, v217\n"
"M 68,176,72\nR 172,191,24576\ns_add_u32 m0, s50, 24576\nM 68,180,88\ns_add_u32 s64, s68, 37755904\nv_fma_f32 v232, v232, v218, v217\n"
"v_fma_f32 v233, v233, v218, v217\nM 84,176,76\nR 174,191,28672\ns_add_u32 s63, s54, 2752768\nM 84,180,92\nG 4\n"
"v_mul_f32 v230, v120, v230\nv_mul_f32 v231, v121, v231\nM 40,160,64\nR 176,188,32768\ns_waitcnt vmcnt(7)\ns_barrier\n"
"M 40,164,80\nbuffer_load_dwordx4 a[12:15], v252, s[20:23], s67 offen sc1\nbuffer_load_dwordx4 v220, s[12:15], s63 offen lds\nM 56,160,68\nR 178,188,36864\nv_mul_f32 v232, v122, v232\n"
"M 56,164,84\ns_add_u32 s65, s68, 37870592\nv_exp_f32 v230, v230\nM 72,160,72\nR 180,188,40960\nv_mul_f32 v233, v123, v233\n"
"M 72,164,88\nbuffer_load_dwordx4 a[16:19], v252, s[20:23], s64 offen sc1\nbuffer_load_dwordx4 v221, s[12:15], s63 offen offset:1024 lds\nM 88,160,76\nR 182,188,45056\ns_add_u32 m0, s50, 32768\n"
"M 88,164,92\nG 4\nv_exp_f32 v231, v231\nM 44,168,64\nR 160,189,32768\ns_add_u32 s63, s54, 2982144\n"
"M 44,172,80\ns_add_u32 s66, s69, 37755904\nbuffer_load_dwordx4 v220, s[12:15], s63 offen lds\nM 60,168,68\nR 162,189,36864\nv_fma_f32 v230, v230, v219, v219\n"
"M 60,172,84\nbuffer_load_dwordx4 a[20:23], v252, s[20:23], s65 offen sc1\nM 76,168,72\nR 164,189,40960\nv_fma_f32 v231, v231, v219, v219\nM 76,172,88\n"
"buffer_load_dwordx4 v221, s[12:15], s63 offen offset:1024 lds\nM 92,168,76\nR 166,189,45056\nv_mul_f32 v234, v124, v124\nM 92,172,92\nG 4\n"
"v_exp_f32 v232, v232\nM 32,176,96\nR 168,190,32768\nv_fma_f32 v232, v232, v219, v219\nM 32,180,112\nbuffer_load_dwordx4 a[24:27], v252, s[20:23], s66 offen sc1\n"
"M 48,176,100\nR 170,190,36864\nv_mul_f32 v235, v125, v125\nM 48,180,116\ns_add_u32 s67, s69, 37870592\nv_exp_f32 v233, v233\n"
"M 64,176,104\nR 172,190,40960\nv_fma_f32 v233, v233, v219, v219\nM 64,180,120\nbuffer_store_dwordx4 v[112:115], v252, s[20:23], s85 offen offset:0 sc1\nM 80,176,108\n"
"R 174,190,45056\nv_mul_f32 v236, v126, v126\nM 80,180,124\nG 4\nv_rcp_f32 v230, v230\nM 36,160,96\n"
"R 176,191,32768\nv_mul_f32 v230, v120, v230\nM 36,164,112\nbuffer_load_dwordx4 a[28:31], v252, s[20:23], s67 offen sc1\nM 52,160,100\nR 178,191,36864\n"
"v_mul_f32 v237, v127, v127\nM 52,164,116\ns_add_u32 s64, s68, 37749760\nv_rcp_f32 v231, v231\nM 68,160,104\nR 180,191,40960\n"
"v_mul_f32 v231, v121, v231\nM 68,164,120\ns_add_u32 s65, s68, 37864448\nv_rcp_f32 v232, v232\nM 84,160,108\nR 182,191,45056\n"
"v_mul_f32 v232, v122, v232\nM 84,164,124\nG 4\nv_rcp_f32 v233, v233\nM 40,168,96\nR 160,188,49152\n"
"v_mul_f32 v233, v123, v233\nM 40,172,112\nbuffer_load_dwordx4 a[32:35], v252, s[20:23], s64 offen sc1\nM 56,168,100\nR 162,188,53248\nv_cvt_pk_bf16_f32 v120, v230, v231\n"
"M 56,172,116\ns_add_u32 s66, s69, 37749760\nv_cvt_pk_bf16_f32 v121, v232, v233\nv_fma_f32 v234, v234, v218, v217\nM 72,168,104\nR 164,184,0\n"
"v_fma_f32 v235, v235, v218, v217\nM 72,172,120\nbuffer_load_dwordx4 a[36:39], v252, s[20:23], s65 offen sc1\nM 88,168,108\nR 166,184,4096\nv_fma_f32 v236, v236, v218, v217\n"
"M 88,172,124\nG 4\nv_fma_f32 v237, v237, v218, v217\nv_mul_f32 v234, v124, v234\nM 44,176,96\nR 168,189,49152\n"
"s_add_u32 m0, s50, 40960\nM 44,180,112\ns_add_u32 s67, s69, 37864448\nv_exp_f32 v234, v234\nM 60,176,100\nR 170,189,53248\n"
"s_add_u32 s63, s54, 3211520\nM 60,180,116\nbuffer_load_dwordx4 a[40:43], v252, s[20:23], s66 offen sc1\nM 76,176,104\nR 172,185,0\nv_mul_f32 v235, v125, v235\n"
"M 76,180,120\nbuffer_load_dwordx4 v220, s[12:15], s63 offen lds\nM 92,176,108\nR 174,185,4096\nv_mul_f32 v236, v126, v236\nM 92,180,124\n"
"G 4\nv_exp_f32 v235, v235\nM 32,160,128\nR 176,190,49152\nv_mul_f32 v237, v127, v237\nM 32,164,144\n"
"buffer_load_dwordx4 a[44:47], v252, s[20:23], s67 offen sc1\nbuffer_load_dwordx4 v221, s[12:15], s63 offen offset:1024 lds\nM 48,160,132\nR 178,190,53248\ns_add_u32 m0, s50, 49152\nM 48,164,148\n"
"s_add_u32 s64, s68, 37756928\nv_exp_f32 v236, v236\nM 64,160,136\nR 180,186,0\ns_add_u32 s63, s54, 3440896\nM 64,164,152\n"
"s_add_u32 s65, s68, 37871616\nbuffer_load_dwordx4 v220, s[12:15], s63 offen lds\nM 80,160,140\nR 182,186,4096\nv_fma_f32 v234, v234, v219, v219\nM 80,164,156\n"
"G 4\nv_exp_f32 v237, v237\nM 36,168,128\nR 160,191,49152\nv_fma_f32 v235, v235, v219, v219\nM 36,172,144\n"
"buffer_load_dwordx4 a[48:51], v252, s[20:23], s64 offen sc1\nbuffer_load_dwordx4 v221, s[12:15], s63 offen offset:1024 lds\nM 52,168,132\nR 162,191,53248\nv_fma_f32 v236, v236, v219, v219\nM 52,172,148\n"
"s_add_u32 s66, s69, 37756928\nv_rcp_f32 v234, v234\nM 68,168,136\nR 164,187,0\ns_waitcnt vmcnt(12)\ns_barrier\n"
"M 68,172,152\nbuffer_load_dwordx4 a[52:55], v252, s[20:23], s65 offen sc1\ns_movk_i32 s63, 2048\nv_mad_u32_u24 v220, v228, s63, v229\nv_add_u32 v221, 916480, v220\ns_add_u32 m0, s50, 57344\n"
"s_add_u32 s63, s56, 0\nbuffer_load_dwordx4 v220, s[16:19], s63 offen lds\nM 84,168,140\nR 166,187,4096\nv_fma_f32 v237, v237, v219, v219\nM 84,172,156\n"
"G 4\nv_rcp_f32 v235, v235\nM 40,176,128\nR 168,184,8192\nv_mul_f32 v234, v124, v234\nM 40,180,144\n"
"s_add_u32 s67, s69, 37871616\nbuffer_load_dwordx4 v221, s[16:19], s63 offen offset:1024 lds\nM 56,176,132\nR 170,184,12288\ns_add_u32 m0, s50, 65536\nM 56,180,148\n"
"buffer_load_dwordx4 a[56:59], v252, s[20:23], s66 offen sc1\nM 72,176,136\nR 172,184,16384\ns_add_u32 s63, s56, 524288\nM 72,180,152\nbuffer_load_dwordx4 v220, s[16:19], s63 offen lds\n"
"M 88,176,140\nR 174,184,20480\nv_mul_f32 v235, v125, v235\nM 88,180,156\nG 4\nv_rcp_f32 v236, v236\n"
"M 44,160,128\nR 176,185,8192\nv_mul_f32 v236, v126, v236\nM 44,164,144\nbuffer_load_dwordx4 a[60:63], v252, s[20:23], s67 offen sc1\nbuffer_load_dwordx4 v221, s[16:19], s63 offen offset:1024 lds\n"
"M 60,160,132\nR 178,185,12288\nv_cvt_pk_bf16_f32 v122, v234, v235\nM 60,164,148\nv_rcp_f32 v237, v237\nM 76,160,136\n"
"R 180,185,16384\nv_mul_f32 v237, v127, v237\nM 76,164,152\ns_add_u32 m0, s50, 73728\nv_cvt_pk_bf16_f32 v123, v236, v237\nM 92,160,140\n"
"R 182,185,20480\nM 92,164,156\nbuffer_store_dwordx4 v[120:123], v252, s[20:23], s85 offen offset:1024 sc1\nG 4\nM 32,168,160\nR 160,186,8192\n"
"v_mul_f32 v230, v0, v0\nv_mul_f32 v231, v1, v1\nM 32,172,176\ns_add_u32 s63, s56, 65536\ns_add_u32 s84, s73, 37754880\ns_add_u32 s85, s73, 37869568\n"
"v_mul_f32 v232, v2, v2\nM 48,168,164\nR 162,186,12288\nv_mul_f32 v233, v3, v3\nv_fma_f32 v230, v230, v215, v214\nM 48,172,180\n"
"buffer_load_dwordx4 v220, s[16:19], s63 offen lds\nv_fma_f32 v231, v231, v215, v214\nM 64,168,168\nR 164,186,16384\nv_fma_f32 v232, v232, v215, v214\nv_fma_f32 v233, v233, v215, v214\n"
"M 64,172,184\nv_mul_f32 v230, v0, v230\nv_mul_f32 v231, v1, v231\nv_mul_f32 v232, v2, v232\nM 80,168,172\nR 166,186,20480\n"
"v_mul_f32 v233, v3, v233\nv_exp_f32 v230, v230\nM 80,172,188\nG 4\nv_exp_f32 v231, v231\nv_exp_f32 v232, v232\n"
"M 36,176,160\nR 168,187,8192\nv_fma_f32 v230, v230, v216, v216\nv_exp_f32 v233, v233\nM 36,180,176\nv_rcp_f32 v230, v230\n"
"v_fma_f32 v231, v231, v216, v216\nM 52,176,164\nR 170,187,12288\nv_fma_f32 v232, v232, v216, v216\nv_fma_f32 v233, v233, v216, v216\nM 52,180,180\n"
"v_rcp_f32 v231, v231\nv_rcp_f32 v232, v232\nM 68,176,168\nR 172,187,16384\nv_mul_f32 v230, v0, v230\nv_rcp_f32 v233, v233\n"
"M 68,180,184\nv_mul_f32 v231, v1, v231\nv_mul_f32 v232, v2, v232\nv_mul_f32 v233, v3, v233\nM 84,176,172\nR 174,187,20480\n"
"v_cvt_pk_bf16_f32 v0, v230, v231\nv_cvt_pk_bf16_f32 v1, v232, v233\nM 84,180,188\nG 4\nv_mul_f32 v234, v4, v4\nv_mul_f32 v235, v5, v5\n"
"v_mul_f32 v236, v6, v6\nM 40,160,160\nR 176,184,24576\nv_mul_f32 v237, v7, v7\nv_fma_f32 v234, v234, v215, v214\nM 40,164,176\n"
"v_fma_f32 v235, v235, v215, v214\nv_fma_f32 v236, v236, v215, v214\nv_fma_f32 v237, v237, v215, v214\nM 56,160,164\nR 178,184,28672\nv_mul_f32 v234, v4, v234\n"
"v_mul_f32 v235, v5, v235\nM 56,164,180\nv_exp_f32 v234, v234\nv_mul_f32 v236, v6, v236\nM 72,160,168\nR 180,184,32768\n"
"v_mul_f32 v237, v7, v237\nv_exp_f32 v235, v235\nM 72,164,184\nv_exp_f32 v236, v236\nv_exp_f32 v237, v237\nM 88,160,172\n"
"R 182,184,36864\nv_fma_f32 v234, v234, v216, v216\nv_fma_f32 v235, v235, v216, v216\nM 88,164,188\nG 4\nv_rcp_f32 v234, v234\n"
"v_fma_f32 v236, v236, v216, v216\nM 44,168,160\nR 160,185,24576\nv_fma_f32 v237, v237, v216, v216\nv_rcp_f32 v235, v235\nM 44,172,176\n"
"v_rcp_f32 v236, v236\nv_rcp_f32 v237, v237\nM 60,168,164\nR 162,185,28672\nv_mul_f32 v234, v4, v234\nv_mul_f32 v235, v5, v235\n"
"M 60,172,180\nv_mul_f32 v236, v6, v236\nv_mul_f32 v237, v7, v237\nv_cvt_pk_bf16_f32 v2, v234, v235\nM 76,168,168\nR 164,185,32768\n"
"v_cvt_pk_bf16_f32 v3, v236, v237\nv_mul_f32 v230, v16, v16\nM 76,172,184\nv_mul_f32 v231, v17, v17\nv_mul_f32 v232, v18, v18\nv_mul_f32 v233, v19, v19\n"
"M 92,168,172\nR 166,185,36864\nv_fma_f32 v230, v230, v218, v217\nv_fma_f32 v231, v231, v218, v217\nM 92,172,188\nG 4\n"
"v_fma_f32 v232, v232, v218, v217\nv_fma_f32 v233, v233, v218, v217\nv_mul_f32 v230, v16, v230\nM 32,176,192\nR 168,186,24576\nv_mul_f32 v231, v17, v231\n"
"v_mul_f32 v232, v18, v232\nM 32,180,208\nv_exp_f32 v230, v230\nv_mul_f32 v233, v19, v233\nM 48,176,196\nR 170,186,28672\n"
"v_fma_f32 v230, v230, v219, v219\nv_exp_f32 v231, v231\nM 48,180,212\nv_exp_f32 v232, v232\nv_exp_f32 v233, v233\nM 64,176,200\n"
"R 172,186,32768\nv_fma_f32 v231, v231, v219, v219\nv_fma_f32 v232, v232, v219, v219\nM 64,180,216\nv_rcp_f32 v230, v230\nv_fma_f32 v233, v233, v219, v219\n"
"M 80,176,204\nR 174,186,36864\nv_mul_f32 v230, v16, v230\nv_rcp_f32 v231, v231\nM 80,180,220\nG 4\n"
"v_rcp_f32 v232, v232\nv_rcp_f32 v233, v233\nM 36,160,192\nR 176,187,24576\ns_waitcnt vmcnt(2)\ns_barrier\n"
"v_mul_f32 v231, v17, v231\nM 36,164,208\nv_mul_f32 v232, v18, v232\nv_mul_f32 v233, v19, v233\nv_cvt_pk_bf16_f32 v16, v230, v231\nM 52,160,196\n"
"R 178,187,28672\ns_mov_b32 s60, 158208\nv_bfe_u32 v246, v252, 4, 4\nv_lshl_add_u32 v246, v246, 3, s60\nv_cvt_pk_bf16_f32 v17, v232, v233\nM 52,164,212\n"
"ds_read_b64 v[238:239], v246 offset:0\nv_mul_f32 v234, v20, v20\nv_mul_f32 v235, v21, v21\nM 68,160,200\nR 180,187,32768\nds_read_b64 v[240:241], v246 offset:128\n"
"v_mov_b32 v226, s46\nv_mul_f32 v236, v22, v22\nM 68,164,216\nds_read_b64 v[242:243], v246 offset:256\nv_mov_b32 v227, s47\nv_mul_f32 v237, v23, v23\n"
"M 84,160,204\nR 182,187,36864\ns_add_u32 s61, s81, 845832\ns_lshl_b32 s62, s44, 5\ns_add_u32 s61, s61, s62\ns_waitcnt vmcnt(0)\n"
"s_mov_b64 exec, 1\nbuffer_atomic_swap_x2 v[226:227], off, s[20:23], s61 sc1\ns_mov_b64 exec, -1\nds_read_b64 v[244:245], v246 offset:384\nv_fma_f32 v234, v234, v218, v217\nM 84,164,220\n"
"ds_read_b64 v[246:247], v246 offset:0\nG 9\nv_fma_f32 v235, v235, v218, v217\nM 40,168,192\nR 160,184,40960\nbuffer_load_dwordx4 v221, s[16:19], s63 offen offset:1024 lds\n"
"v_fma_f32 v236, v236, v218, v217\nM 40,172,208\ns_add_u32 m0, s50, 81920\ns_add_u32 s63, s56, 589824\nbuffer_load_dwordx4 v220, s[16:19], s63 offen lds\nv_fma_f32 v237, v237, v218, v217\n"
"M 56,168,196\nR 162,184,45056\nv_mul_f32 v234, v20, v234\nv_mul_f32 v235, v21, v235\nM 56,172,212\nbuffer_store_dwordx4 v[0:3], v252, s[20:23], s84 offen offset:0 sc1\n"
"v_mul_f32 v236, v22, v236\nM 72,168,200\nR 164,184,49152\nv_mul_f32 v237, v23, v237\nv_exp_f32 v234, v234\nM 72,172,216\n"
"buffer_load_dwordx4 v221, s[16:19], s63 offen offset:1024 lds\nv_exp_f32 v235, v235\nM 88,168,204\nR 166,184,53248\ns_add_u32 s63, s57, 0\nv_exp_f32 v236, v236\n"
"M 88,172,220\nG 6\nv_exp_f32 v237, v237\nv_fma_f32 v234, v234, v219, v219\nM 44,176,192\nR 168,185,40960\n"
"v_mfma_f32_16x16x32_bf16 v[32:35], v[238:241], v[208:211], v[32:35]\nv_fma_f32 v235, v235, v219, v219\nv_fma_f32 v236, v236, v219, v219\nM 44,180,208\ns_add_u32 m0, s50, 90112\nv_rcp_f32 v234, v234\n"
"M 60,176,196\nR 170,185,45056\nv_mfma_f32_16x16x32_bf16 v[36:39], v[240:243], v[208:211], v[36:39]\nv_fma_f32 v237, v237, v219, v219\nM 60,180,212\nbuffer_load_dwordx4 v220, s[16:19], s63 offen lds\n"
"M 76,176,200\nR 172,185,49152\nG 8\nv_mfma_f32_16x16x32_bf16 v[40:43], v[242:245], v[208:211], v[40:43]\nv_mul_f32 v234, v20, v234\nM 76,180,216\n"
"buffer_load_dwordx4 v221, s[16:19], s63 offen offset:1024 lds\nM 92,176,204\nR 174,185,53248\nG 8\nv_mfma_f32_16x16x32_bf16 v[44:47], v[244:247], v[208:211], v[44:47]\ns_add_u32 m0, s50, 98304\n"
"M 92,180,220\nG 4\nv_rcp_f32 v235, v235\nM 32,160,224\nR 176,186,40960\nv_mfma_f32_16x16x32_bf16 v[48:51], v[244:247], v[210:213], v[48:51]\n"
"s_add_u32 s63, s57, 524288\nM 32,164,240\nbuffer_load_dwordx4 v220, s[16:19], s63 offen lds\nM 48,160,228\nR 178,186,45056\nv_mfma_f32_16x16x32_bf16 v[52:55], v[238:241], v[210:213], v[52:55]\n"
"v_mul_f32 v235, v21, v235\nM 48,164,244\nbuffer_load_dwordx4 v221, s[16:19], s63 offen offset:1024 lds\nM 64,160,232\nR 180,186,49152\nv_mfma_f32_16x16x32_bf16 v[56:59], v[240:243], v[210:213], v[56:59]\n"
"s_add_u32 m0, s50, 106496\ns_add_u32 s63, s57, 65536\nM 64,164,248\nbuffer_load_dwordx4 v220, s[16:19], s63 offen lds\nM 80,160,236\nR 182,186,53248\n"
"v_mfma_f32_16x16x32_bf16 v[60:63], v[242:245], v[210:213], v[60:63]\nv_cvt_pk_bf16_f32 v18, v234, v235\nM 80,164,252\nG 4\nv_rcp_f32 v236, v236\nM 36,168,224\n"
"R 160,187,40960\nv_mul_f32 v236, v22, v236\nM 36,172,240\nbuffer_load_dwordx4 v221, s[16:19], s63 offen offset:1024 lds\nM 52,168,228\nR 162,187,45056\n"
"s_add_u32 s63, s57, 589824\nM 52,172,244\ns_add_u32 m0, s50, 114688\nv_rcp_f32 v237, v237\nM 68,168,232\nR 164,187,49152\n"
"v_mul_f32 v237, v23, v237\nM 68,172,248\nbuffer_load_dwordx4 v220, s[16:19], s63 offen lds\nM 84,168,236\nR 166,187,53248\nv_cvt_pk_bf16_f32 v19, v236, v237\n"
"M 84,172,252\nbuffer_store_dwordx4 v[16:19], v252, s[20:23], s85 offen offset:0 sc1\nG 4\nM 40,176,224\nR 168,184,57344\nM 40,180,240\n"
"buffer_load_dwordx4 v221, s[16:19], s63 offen offset:1024 lds\nM 56,176,228\nR 170,184,61440\nM 56,180,244\nM 72,176,232\nR 172,188,0\n"
"M 72,180,248\nM 88,176,236\nR 174,188,4096\nM 88,180,252\nG 4\nM 44,160,224\n"
"R 176,185,57344\nM 44,164,240\nM 60,160,228\nR 178,185,61440\nv_mov_b32 v248, 0\nM 60,164,244\n"
"v_mov_b32 v249, 0\nM 76,160,232\nR 180,189,0\ns_add_u32 s61, s82, 845832\ns_mov_b64 exec, 0xf\nbuffer_atomic_or_x2 v[248:249], v250, s[20:23], s61 offen sc0 sc1\n"
"s_mov_b64 exec, -1\nM 76,164,248\nM 92,160,236\nR 182,189,4096\nM 92,164,252\ns_waitcnt vmcnt(10)\n"
"s_barrier\nG 4\nv_mfma_f32_16x16x32_bf16 v[96:99], v[168:171], a[0:3], 0\nR 160,186,57344\nM 96,172,16\ns_mov_b32 m0, s51\n"
"v_mfma_f32_16x16x32_bf16 v[112:115], v[168:171], a[4:7], 0\nR 162,186,61440\nM 112,172,20\nds_write_addtid_b32 v64 offset:0\nv_mfma_f32_16x16x32_bf16 v[128:131], v[168:171], a[8:11], 0\nR 164,190,0\n"
"M 128,172,24\nds_write_addtid_b32 v65 offset:256\nv_mfma_f32_16x16x32_bf16 v[144:147], v[168:171], a[12:15], 0\nR 166,190,4096\nM 144,172,28\nds_write_addtid_b32 v66 offset:512\n"
"G 7\nv_mfma_f32_16x16x32_bf16 v[100:103], v[176:179], a[0:3], 0\nR 168,187,57344\nM 100,180,16\nds_write_addtid_b32 v67 offset:768\nv_mfma_f32_16x16x32_bf16 v[116:119], v[176:179], a[4:7], 0\n"
"R 170,187,61440\nM 116,180,20\nds_write_addtid_b32 v68 offset:1024\nv_mfma_f32_16x16x32_bf16 v[132:135], v[176:179], a[8:11], 0\nR 172,191,0\nds_write_addtid_b32 v69 offset:1280\n"
"M 132,180,24\nds_write_addtid_b32 v70 offset:1536\nds_write_addtid_b32 v71 offset:1792\nv_mfma_f32_16x16x32_bf16 v[148:151], v[176:179], a[12:15], 0\nR 174,191,4096\nds_write_addtid_b32 v72 offset:2048\n"
"M 148,180,28\nds_write_addtid_b32 v73 offset:2304\nds_write_addtid_b32 v74 offset:2560\nG 13\nv_mfma_f32_16x16x32_bf16 v[104:107], v[160:163], a[0:3], 0\nR 176,188,8192\n"
"ds_write_addtid_b32 v75 offset:2816\nM 104,164,16\nds_write_addtid_b32 v76 offset:3072\nds_write_addtid_b32 v77 offset:3328\nv_mfma_f32_16x16x32_bf16 v[120:123], v[160:163], a[4:7], 0\nR 178,188,12288\n"
"ds_write_addtid_b32 v78 offset:3584\nM 120,164,20\nds_write_addtid_b32 v79 offset:3840\nds_write_addtid_b32 v80 offset:4096\nv_mfma_f32_16x16x32_bf16 v[136:139], v[160:163], a[8:11], 0\nR 180,188,16384\n"
"ds_write_addtid_b32 v81 offset:4352\nM 136,164,24\nds_write_addtid_b32 v82 offset:4608\nds_write_addtid_b32 v83 offset:4864\nv_mfma_f32_16x16x32_bf16 v[152:155], v[160:163], a[12:15], 0\nR 182,188,20480\n"
"ds_write_addtid_b32 v84 offset:5120\nM 152,164,28\nds_write_addtid_b32 v85 offset:5376\nds_write_addtid_b32 v86 offset:5632\nG 15\nv_mfma_f32_16x16x32_bf16 v[108:111], v[168:171], a[0:3], 0\n"
"R 160,189,8192\nds_write_addtid_b32 v87 offset:5888\nM 108,172,16\nds_write_addtid_b32 v88 offset:6144\nds_write_addtid_b32 v89 offset:6400\nv_mfma_f32_16x16x32_bf16 v[124:127], v[168:171], a[4:7], 0\n"
"R 162,189,12288\nds_write_addtid_b32 v90 offset:6656\nM 124,172,20\ns_add_u32 s64, s68, 37750784\nds_write_addtid_b32 v91 offset:6912\nds_write_addtid_b32 v92 offset:7168\n"
"v_mfma_f32_16x16x32_bf16 v[140:143], v[168:171], a[8:11], 0\nR 164,189,16384\nv_mov_b32 v226, s46\nM 140,172,24\nds_write_addtid_b32 v93 offset:7424\nv_mov_b32 v227, s47\n"
"v_mfma_f32_16x16x32_bf16 v[156:159], v[168:171], a[12:15], 0\nR 166,189,20480\ns_add_u32 s61, s81, 845848\ns_lshl_b32 s62, s44, 5\ns_add_u32 s61, s61, s62\ns_waitcnt vmcnt(0)\n"
"s_mov_b64 exec, 1\nbuffer_atomic_swap_x2 v[226:227], off, s[20:23], s61 sc1\ns_mov_b64 exec, -1\nv_cmp_eq_u64 s[86:87], s[46:47], v[248:249]\nM 156,172,28\nds_write_addtid_b32 v94 offset:7680\n"
"ds_write_addtid_b32 v95 offset:7936\nG 15\nM 96,176,32\nR 168,190,8192\ns_and_b32 s62, s86, 0xf\ns_cmp_eq_u32 s62, 0xf\n"
"s_cbranch_scc0 Lq868\nLq869:\ns_add_u32 m0, s50, 0\ns_add_u32 s63, s56, 131072\nM 96,180,48\ns_add_u32 s65, s68, 37865472\n"
"buffer_load_dwordx4 v220, s[16:19], s63 offen lds\nM 112,176,36\nR 170,190,12288\nG 2\ns_barrier\nM 112,180,52\n"
"buffer_load_dwordx4 a[64:67], v252, s[20:23], s64 offen sc1\nbuffer_load_dwordx4 v221, s[16:19], s63 offen offset:1024 lds\nds_read2st64_b32 v[64:65], v225 offset0:0 offset1:1\nM 128,176,40\nR 172,190,16384\ns_add_u32 m0, s50, 8192\n"
"s_add_u32 s63, s56, 655360\nbuffer_load_dwordx4 v220, s[16:19], s63 offen lds\nds_read2st64_b32 v[66:67], v225 offset0:2 offset1:3\nM 128,180,56\ns_add_u32 s66, s69, 37750784\nds_read2st64_b32 v[68:69], v225 offset0:4 offset1:5\n"
"ds_read2st64_b32 v[70:71], v225 offset0:6 offset1:7\nM 144,176,44\nR 174,190,20480\nds_read2st64_b32 v[72:73], v225 offset0:8 offset1:9\nM 144,180,60\nds_read2st64_b32 v[74:75], v225 offset0:10 offset1:11\n"
"ds_read2st64_b32 v[76:77], v225 offset0:12 offset1:13\nM 100,160,32\nR 176,191,8192\nbuffer_load_dwordx4 v221, s[16:19], s63 offen offset:1024 lds\nds_read2st64_b32 v[78:79], v225 offset0:14 offset1:15\nM 100,164,48\n"
"buffer_load_dwordx4 a[68:71], v252, s[20:23], s65 offen sc1\nG 10\nv_add_f32 v32, v32, v64\nM 116,160,36\nR 178,191,12288\ns_add_u32 m0, s50, 16384\n"
"v_add_f32 v33, v33, v65\nM 116,164,52\ns_add_u32 s67, s69, 37865472\ns_add_u32 s63, s56, 196608\nG 9\nv_add_f32 v34, v34, v66\n"
"v_add_f32 v35, v35, v67\nM 132,160,40\nR 180,191,16384\nbuffer_load_dwordx4 v220, s[16:19], s63 offen lds\nG 9\nv_add_f32 v36, v36, v68\n"
"M 132,164,56\nbuffer_load_dwordx4 a[72:75], v252, s[20:23], s66 offen sc1\nv_add_f32 v37, v37, v69\nM 148,160,44\nR 182,191,20480\nG 9\n"
"v_add_f32 v38, v38, v70\nv_add_f32 v39, v39, v71\nM 148,164,60\nds_read2st64_b32 v[80:81], v225 offset0:16 offset1:17\nG 9\nG 8\n"
"v_add_f32 v40, v40, v72\nM 104,168,32\nR 160,188,24576\nbuffer_load_dwordx4 v221, s[16:19], s63 offen offset:1024 lds\nv_add_f32 v41, v41, v73\nM 104,172,48\n"
"buffer_load_dwordx4 a[76:79], v252, s[20:23], s67 offen sc1\nG 8\nv_add_f32 v42, v42, v74\nM 120,168,36\nR 162,188,28672\nv_add_f32 v43, v43, v75\n"
"G 8\nv_add_f32 v44, v44, v76\nM 120,172,52\ns_add_u32 s64, s68, 37757952\nds_read2st64_b32 v[82:83], v225 offset0:18 offset1:19\nv_add_f32 v45, v45, v77\n"
"M 136,168,40\nR 164,188,32768\nG 8\nv_add_f32 v46, v46, v78\nv_add_f32 v47, v47, v79\nM 136,172,56\n"
"s_add_u32 s65, s68, 37872640\nds_read2st64_b32 v[84:85], v225 offset0:20 offset1:21\nG 5\nv_add_f32 v48, v48, v80\nM 152,168,44\nR 166,188,36864\n"
"ds_read2st64_b32 v[86:87], v225 offset0:22 offset1:23\nv_add_f32 v49, v49, v81\nM 152,172,60\nds_read2st64_b32 v[88:89], v225 offset0:24 offset1:25\nds_read2st64_b32 v[90:91], v225 offset0:26 offset1:27\nM 108,176,32\n"
"R 168,189,24576\nds_read2st64_b32 v[92:93], v225 offset0:28 offset1:29\nM 108,180,48\nbuffer_load_dwordx4 a[80:83], v252, s[20:23], s64 offen sc1\nG 8\nv_add_f32 v50, v50, v82\n"
"M 124,176,36\nR 170,189,28672\nds_read2st64_b32 v[94:95], v225 offset0:30 offset1:31\nv_add_f32 v51, v51, v83\nM 124,180,52\nbuffer_load_dwordx4 a[84:87], v252, s[20:23], s65 offen sc1\n"
"M 140,176,40\nR 172,189,32768\nG 9\nv_add_f32 v52, v52, v84\nM 140,180,56\ns_add_u32 s66, s69, 37757952\n"
"v_add_f32 v53, v53, v85\nG 7\nv_add_f32 v54, v54, v86\nM 156,176,44\nR 174,189,36864\nv_add_f32 v55, v55, v87\n"
"M 156,180,60\nG 7\nv_add_f32 v56, v56, v88\nv_add_f32 v57, v57, v89\nv_mfma_f32_16x16x32_bf16 v[0:3], v[160:163], a[0:3], 0\nR 176,190,24576\n"
"s_lshr_b32 s60, s45, 3\ns_lshl2_add_u32 s60, s60, s40\ns_lshl_b32 s60, s60, 8\nv_lshl_add_u32 v224, v252, 3, s60\nv_add_u32 v224, LqF-32768-LqPC, v224\ns_mov_b64 exec, 3\n"
"global_load_dword v224, v224, s[98:99]\ns_mov_b64 exec, -1\nM 0,164,16\nbuffer_load_dwordx4 a[88:91], v252, s[20:23], s66 offen sc1\nv_mfma_f32_16x16x32_bf16 v[16:19], v[160:163], a[4:7], 0\nR 178,190,28672\n"
"G 8\nv_add_f32 v58, v58, v90\nM 16,164,20\ns_add_u32 s67, s69, 37872640\nv_add_f32 v59, v59, v91\nG 6\n"
"v_add_f32 v60, v60, v92\nv_mfma_f32_16x16x32_bf16 a[224:227], v[160:163], a[8:11], 0\nR 180,190,32768\nv_add_f32 v61, v61, v93\nv_mfma_f32_16x16x32_bf16 a[224:227], v[164:167], a[24:27], a[224:227]\nbuffer_load_dwordx4 a[92:95], v252, s[20:23], s67 offen sc1\n"
"v_mfma_f32_16x16x32_bf16 a[240:243], v[160:163], a[12:15], 0\nR 182,190,36864\nG 6\nv_add_f32 v62, v62, v94\nv_mfma_f32_16x16x32_bf16 a[240:243], v[164:167], a[28:31], a[240:243]\ns_add_u32 s63, s56, 720896\n"
"G 4\nv_add_f32 v63, v63, v95\nv_mfma_f32_16x16x32_bf16 v[4:7], v[168:171], a[0:3], 0\nR 160,191,24576\ns_add_u32 m0, s50, 24576\nv_mul_f32 v230, v32, v32\n"
"M 4,172,16\ns_add_u32 s64, s68, 37751808\ns_add_u32 s84, s73, 37752832\ns_add_u32 s85, s73, 37867520\nv_mul_f32 v231, v33, v33\nv_mfma_f32_16x16x32_bf16 v[20:23], v[168:171], a[4:7], 0\n"
"R 162,191,28672\nv_mov_b32 v248, 0\nv_mul_f32 v232, v34, v34\nM 20,172,20\nbuffer_load_dwordx4 a[96:99], v252, s[20:23], s64 offen sc1\nv_mov_b32 v249, 0\n"
"v_mul_f32 v233, v35, v35\nv_mfma_f32_16x16x32_bf16 a[228:231], v[168:171], a[8:11], 0\nR 164,191,32768\ns_add_u32 s61, s82, 845848\ns_mov_b64 exec, 0xf\nbuffer_atomic_or_x2 v[248:249], v250, s[20:23], s61 offen sc0 sc1\n"
"s_mov_b64 exec, -1\nv_fma_f32 v230, v230, v215, v214\nv_mfma_f32_16x16x32_bf16 a[228:231], v[172:175], a[24:27], a[228:231]\ns_add_u32 s65, s68, 37866496\nbuffer_load_dwordx4 v220, s[16:19], s63 offen lds\nv_fma_f32 v231, v231, v215, v214\n"
"v_mfma_f32_16x16x32_bf16 a[244:247], v[168:171], a[12:15], 0\nR 166,191,36864\nv_fma_f32 v232, v232, v215, v214\nv_fma_f32 v233, v233, v215, v214\nv_mfma_f32_16x16x32_bf16 a[244:247], v[172:175], a[28:31], a[244:247]\nG 4\n"
"v_mul_f32 v230, v32, v230\nv_mul_f32 v231, v33, v231\nv_mul_f32 v232, v34, v232\nv_mfma_f32_16x16x32_bf16 v[8:11], v[176:179], a[0:3], 0\nR 168,188,40960\nv_mul_f32 v233, v35, v233\n"
"v_exp_f32 v230, v230\nM 8,180,16\nbuffer_load_dwordx4 a[100:103], v252, s[20:23], s65 offen sc1\nbuffer_load_dwordx4 v221, s[16:19], s63 offen offset:1024 lds\nv_exp_f32 v231, v231\nv_mfma_f32_16x16x32_bf16 v[24:27], v[176:179], a[4:7], 0\n"
"R 170,188,45056\ns_add_u32 s67, s69, 37866496\nv_exp_f32 v232, v232\nM 24,180,20\ns_add_u32 s66, s69, 37751808\ns_add_u32 s63, s56, 393216\n"
"v_fma_f32 v230, v230, v216, v216\nv_exp_f32 v233, v233\nv_mfma_f32_16x16x32_bf16 a[232:235], v[176:179], a[8:11], 0\nR 172,188,49152\ns_add_u32 m0, s50, 32768\nv_fma_f32 v231, v231, v216, v216\n"
"v_mfma_f32_16x16x32_bf16 a[232:235], v[180:183], a[24:27], a[232:235]\nbuffer_load_dwordx4 a[104:107], v252, s[20:23], s66 offen sc1\nbuffer_load_dwordx4 v220, s[16:19], s63 offen lds\nv_fma_f32 v232, v232, v216, v216\nv_mfma_f32_16x16x32_bf16 a[248:251], v[176:179], a[12:15], 0\nR 174,188,53248\n"
"v_fma_f32 v233, v233, v216, v216\nv_rcp_f32 v230, v230\nv_mfma_f32_16x16x32_bf16 a[248:251], v[180:183], a[28:31], a[248:251]\nG 4\nv_rcp_f32 v231, v231\nv_rcp_f32 v232, v232\n"
"v_mfma_f32_16x16x32_bf16 v[12:15], v[160:163], a[0:3], 0\nR 176,189,40960\nv_mul_f32 v230, v32, v230\nv_rcp_f32 v233, v233\nM 12,164,16\nbuffer_load_dwordx4 a[108:111], v252, s[20:23], s67 offen sc1\n"
"buffer_load_dwordx4 v221, s[16:19], s63 offen offset:1024 lds\nv_mul_f32 v231, v33, v231\nv_mfma_f32_16x16x32_bf16 v[28:31], v[160:163], a[4:7], 0\nR 178,189,45056\ns_add_u32 m0, s50, 40960\nv_mul_f32 v232, v34, v232\n"
"M 28,164,20\ns_add_u32 s64, s68, 37758976\nv_mul_f32 v233, v35, v233\nv_cvt_pk_bf16_f32 v32, v230, v231\nv_cvt_pk_bf16_f32 v33, v232, v233\nv_mfma_f32_16x16x32_bf16 a[236:239], v[160:163], a[8:11], 0\n"
"R 180,189,49152\ns_add_u32 s63, s56, 458752\nv_mul_f32 v234, v36, v36\nv_mfma_f32_16x16x32_bf16 a[236:239], v[164:167], a[24:27], a[236:239]\ns_add_u32 s65, s68, 37873664\nbuffer_load_dwordx4 v220, s[16:19], s63 offen lds\n"
"v_mul_f32 v235, v37, v37\nv_mfma_f32_16x16x32_bf16 a[252:255], v[160:163], a[12:15], 0\nR 182,189,53248\nv_mul_f32 v236, v38, v38\nv_mul_f32 v237, v39, v39\nv_mfma_f32_16x16x32_bf16 a[252:255], v[164:167], a[28:31], a[252:255]\n"
"s_waitcnt vmcnt(16)\ns_barrier\nG 4\nv_fma_f32 v234, v234, v215, v214\nM 0,168,32\nR 160,190,40960\n"
"v_fma_f32 v235, v235, v215, v214\nv_fma_f32 v236, v236, v215, v214\nM 0,172,48\nbuffer_load_dwordx4 a[112:115], v252, s[20:23], s64 offen sc1\nbuffer_load_dwordx4 v221, s[16:19], s63 offen offset:1024 lds\nv_fma_f32 v237, v237, v215, v214\n"
"M 16,168,36\nR 162,190,45056\nv_mul_f32 v234, v36, v234\nv_mul_f32 v235, v37, v235\nM 16,172,52\nbuffer_load_dwordx4 a[116:119], v252, s[20:23], s65 offen sc1\n"
"v_mul_f32 v236, v38, v236\nv_mfma_f32_16x16x32_bf16 a[224:227], v[168:171], a[40:43], a[224:227]\nR 164,190,49152\nv_mul_f32 v237, v39, v237\nv_exp_f32 v234, v234\nv_mfma_f32_16x16x32_bf16 a[224:227], v[172:175], a[56:59], a[224:227]\n"
"s_add_u32 s66, s69, 37758976\nv_exp_f32 v235, v235\nv_exp_f32 v236, v236\nv_mfma_f32_16x16x32_bf16 a[240:243], v[168:171], a[44:47], a[240:243]\nR 166,190,53248\nv_fma_f32 v234, v234, v216, v216\n"
"v_exp_f32 v237, v237\nv_mfma_f32_16x16x32_bf16 a[240:243], v[172:175], a[60:63], a[240:243]\nG 4\nv_rcp_f32 v234, v234\nv_fma_f32 v235, v235, v216, v216\nM 4,176,32\n"
"R 168,191,40960\nv_fma_f32 v236, v236, v216, v216\nv_fma_f32 v237, v237, v216, v216\nM 4,180,48\nbuffer_load_dwordx4 a[120:123], v252, s[20:23], s66 offen sc1\nv_rcp_f32 v235, v235\n"
"M 20,176,36\nR 170,191,45056\nv_mul_f32 v234, v36, v234\nv_rcp_f32 v236, v236\nM 20,180,52\ns_add_u32 s67, s69, 37873664\n"
"v_rcp_f32 v237, v237\nv_mul_f32 v235, v37, v235\nv_mfma_f32_16x16x32_bf16 a[228:231], v[176:179], a[40:43], a[228:231]\nR 172,191,49152\nv_mul_f32 v236, v38, v236\nv_mul_f32 v237, v39, v237\n"
"v_mfma_f32_16x16x32_bf16 a[228:231], v[180:183], a[56:59], a[228:231]\nbuffer_load_dwordx4 a[124:127], v252, s[20:23], s67 offen sc1\nv_cvt_pk_bf16_f32 v34, v234, v235\nv_mfma_f32_16x16x32_bf16 a[244:247], v[176:179], a[44:47], a[244:247]\nR 174,191,53248\ns_waitcnt vmcnt(13)\n"
"v_cmp_eq_u64 s[86:87], s[46:47], v[248:249]\nv_cvt_pk_bf16_f32 v35, v236, v237\nv_mfma_f32_16x16x32_bf16 a[244:247], v[180:183], a[60:63], a[244:247]\ns_add_u32 s63, s57, 131072\nG 4\nv_mul_f32 v230, v40, v40\n"
"v_mul_f32 v231, v41, v41\nM 8,160,32\nR 176,184,0\ns_and_b32 s62, s86, 0xf\ns_cmp_eq_u32 s62, 0xf\ns_cbranch_scc0 Lq903\n"
"Lq904:\nv_mul_f32 v232, v42, v42\nM 8,164,48\ns_add_u32 s64, s68, 37754880\nv_mul_f32 v233, v43, v43\nv_fma_f32 v230, v230, v215, v214\n"
"v_fma_f32 v231, v231, v215, v214\nM 24,160,36\nR 178,184,4096\ns_add_u32 m0, s50, 49152\nv_fma_f32 v232, v232, v215, v214\nM 24,164,52\n"
"buffer_load_dwordx4 a[128:131], v252, s[20:23], s64 offen sc1\nv_fma_f32 v233, v233, v215, v214\nv_mfma_f32_16x16x32_bf16 a[232:235], v[160:163], a[40:43], a[232:235]\nR 180,184,8192\ns_add_u32 s66, s69, 37754880\nbuffer_load_dwordx4 v220, s[16:19], s63 offen lds\n"
"v_mul_f32 v230, v40, v230\nv_mfma_f32_16x16x32_bf16 a[232:235], v[164:167], a[56:59], a[232:235]\ns_add_u32 s65, s68, 37869568\nv_exp_f32 v230, v230\nv_mul_f32 v231, v41, v231\nv_mfma_f32_16x16x32_bf16 a[248:251], v[160:163], a[44:47], a[248:251]\n"
"R 182,184,12288\nv_mul_f32 v232, v42, v232\nv_mul_f32 v233, v43, v233\nv_mfma_f32_16x16x32_bf16 a[248:251], v[164:167], a[60:63], a[248:251]\nG 4\nv_exp_f32 v231, v231\n"
"v_exp_f32 v232, v232\nM 12,168,32\nR 160,185,0\nbuffer_load_dwordx4 v221, s[16:19], s63 offen offset:1024 lds\nv_exp_f32 v233, v233\nM 12,172,48\n"
"buffer_load_dwordx4 a[132:135], v252, s[20:23], s65 offen sc1\ns_add_u32 s63, s57, 655360\nv_fma_f32 v230, v230, v216, v216\nM 28,168,36\nR 162,185,4096\ns_add_u32 m0, s50, 57344\n"
"v_fma_f32 v231, v231, v216, v216\nM 28,172,52\nbuffer_load_dwordx4 a[136:139], v252, s[20:23], s66 offen sc1\nv_fma_f32 v232, v232, v216, v216\nv_mfma_f32_16x16x32_bf16 a[236:239], v[168:171], a[40:43], a[236:239]\nR 164,185,8192\n"
"s_add_u32 s64, s68, 37762048\nbuffer_load_dwordx4 v220, s[16:19], s63 offen lds\nv_fma_f32 v233, v233, v216, v216\nv_mfma_f32_16x16x32_bf16 a[236:239], v[172:175], a[56:59], a[236:239]\ns_add_u32 s67, s69, 37869568\nv_rcp_f32 v230, v230\n"
"v_rcp_f32 v231, v231\nv_mfma_f32_16x16x32_bf16 a[252:255], v[168:171], a[44:47], a[252:255]\nR 166,185,12288\nv_mul_f32 v230, v40, v230\nv_rcp_f32 v232, v232\nv_mfma_f32_16x16x32_bf16 a[252:255], v[172:175], a[60:63], a[252:255]\n"
"G 4\nv_rcp_f32 v233, v233\nv_mul_f32 v231, v41, v231\nM 96,176,64\nR 168,186,0\nbuffer_load_dwordx4 v221, s[16:19], s63 offen offset:1024 lds\n"
"v_mul_f32 v232, v42, v232\nM 96,180,80\nbuffer_load_dwordx4 a[140:143], v252, s[20:23], s67 offen sc1\ns_add_u32 s63, s57, 196608\nv_mul_f32 v233, v43, v233\nM 112,176,68\n"
"R 170,186,4096\ns_add_u32 m0, s50, 65536\nv_cvt_pk_bf16_f32 v40, v230, v231\nM 112,180,84\nbuffer_load_dwordx4 a[144:147], v252, s[20:23], s64 offen sc1\nv_cvt_pk_bf16_f32 v41, v232, v233\n"
"M 128,176,72\nR 172,186,8192\ns_add_u32 s66, s69, 37762048\nbuffer_load_dwordx4 v220, s[16:19], s63 offen lds\nv_mul_f32 v234, v44, v44\nM 128,180,88\n"
"s_add_u32 s65, s68, 37876736\nv_mul_f32 v235, v45, v45\nv_mul_f32 v236, v46, v46\nv_mul_f32 v237, v47, v47\nM 144,176,76\nR 174,186,12288\n"
"v_fma_f32 v234, v234, v215, v214\nv_fma_f32 v235, v235, v215, v214\nM 144,180,92\nG 4\nv_fma_f32 v236, v236, v215, v214\nv_fma_f32 v237, v237, v215, v214\n"
"v_mul_f32 v234, v44, v234\nM 100,160,64\nR 176,187,0\nbuffer_load_dwordx4 v221, s[16:19], s63 offen offset:1024 lds\nv_mul_f32 v235, v45, v235\nM 100,164,80\n"
"buffer_load_dwordx4 a[148:151], v252, s[20:23], s65 offen sc1\ns_add_u32 s63, s57, 720896\nv_mul_f32 v236, v46, v236\nM 116,160,68\nR 178,187,4096\ns_add_u32 m0, s50, 73728\n"
"v_mul_f32 v237, v47, v237\nM 116,164,84\nbuffer_load_dwordx4 a[152:155], v252, s[20:23], s66 offen sc1\nv_exp_f32 v234, v234\nM 132,160,72\nR 180,187,8192\n"
"buffer_load_dwordx4 v220, s[16:19], s63 offen lds\nv_exp_f32 v235, v235\nM 132,164,88\ns_add_u32 s67, s69, 37876736\nv_exp_f32 v236, v236\nv_exp_f32 v237, v237\n"
"M 148,160,76\nR 182,187,12288\nv_fma_f32 v234, v234, v216, v216\nv_fma_f32 v235, v235, v216, v216\nM 148,164,92\nG 4\n"
"v_rcp_f32 v234, v234\nv_fma_f32 v236, v236, v216, v216\nM 104,168,64\nR 160,184,16384\ns_waitcnt vmcnt(17)\ns_barrier\n"
"v_fma_f32 v237, v237, v216, v216\nM 104,172,80\nbuffer_load_dwordx4 a[156:159], v252, s[20:23], s67 offen sc1\nbuffer_load_dwordx4 v221, s[16:19], s63 offen offset:1024 lds\nv_rcp_f32 v235, v235\nM 120,168,68\n"
"R 162,184,20480\nv_mul_f32 v234, v44, v234\nv_rcp_f32 v236, v236\nM 120,172,84\nbuffer_store_dwordx4 v[32:35], v252, s[20:23], s84 offen offset:0 sc1\nv_rcp_f32 v237, v237\n"
"M 136,168,72\nR 164,184,24576\nv_mul_f32 v235, v45, v235\nv_mul_f32 v236, v46, v236\nM 136,172,88\ns_add_u32 m0, s50, 81920\n"
"v_mul_f32 v237, v47, v237\nv_cvt_pk_bf16_f32 v42, v234, v235\nv_cvt_pk_bf16_f32 v43, v236, v237\nM 152,168,76\nR 166,184,28672\nv_mul_f32 v230, v48, v48\n"
"v_mul_f32 v231, v49, v49\nM 152,172,92\nG 4\nv_mul_f32 v232, v50, v50\nv_mul_f32 v233, v51, v51\nv_fma_f32 v230, v230, v218, v217\n"
"M 108,176,64\nR 168,185,16384\nv_fma_f32 v231, v231, v218, v217\nv_fma_f32 v232, v232, v218, v217\nM 108,180,80\ns_add_u32 s63, s57, 393216\n"
"v_fma_f32 v233, v233, v218, v217\nv_mul_f32 v230, v48, v230\nv_mul_f32 v231, v49, v231\nM 124,176,68\nR 170,185,20480\nv_mul_f32 v232, v50, v232\n"
"v_mul_f32 v233, v51, v233\nM 124,180,84\nbuffer_load_dwordx4 v220, s[16:19], s63 offen lds\nv_exp_f32 v230, v230\nM 140,176,72\nR 172,185,24576\n"
"v_fma_f32 v230, v230, v219, v219\nv_exp_f32 v231, v231\nM 140,180,88\nbuffer_store_dwordx4 v[40:43], v252, s[20:23], s84 offen offset:1024 sc1\nv_exp_f32 v232, v232\nM 156,176,76\n"
"R 174,185,28672\nv_fma_f32 v231, v231, v219, v219\nv_exp_f32 v233, v233\nM 156,180,92\nG 4\nv_rcp_f32 v230, v230\n"
"v_fma_f32 v232, v232, v219, v219\nM 96,160,96\nR 176,186,16384\nv_fma_f32 v233, v233, v219, v219\nv_rcp_f32 v231, v231\nM 96,164,112\n"
"buffer_load_dwordx4 v221, s[16:19], s63 offen offset:1024 lds\nv_rcp_f32 v232, v232\nM 112,160,100\nR 178,186,20480\ns_add_u32 s63, s57, 458752\nv_rcp_f32 v233, v233\n"
"s_waitcnt vmcnt(22)\nM 112,164,116\ns_add_u32 m0, s50, 90112\nv_mul_f32 v230, v48, v230\nv_mul_f32 v231, v49, v231\nv_mul_f32 v232, v50, v232\n"
"M 128,160,104\nR 180,186,24576\nv_mul_f32 v233, v51, v233\nv_cvt_pk_bf16_f32 v48, v230, v231\ns_waitcnt vmcnt(21)\nM 128,164,120\n"
"buffer_load_dwordx4 v220, s[16:19], s63 offen lds\nv_cvt_pk_bf16_f32 v49, v232, v233\nM 144,160,108\nR 182,186,28672\nv_mul_f32 v234, v52, v52\nv_mul_f32 v235, v53, v53\n"
"s_waitcnt vmcnt(21)\nM 144,164,124\nG 4\nv_mul_f32 v236, v54, v54\nv_mul_f32 v237, v55, v55\nv_fma_f32 v234, v234, v218, v217\n"
"M 100,168,96\nR 160,187,16384\nv_fma_f32 v235, v235, v218, v217\nv_fma_f32 v236, v236, v218, v217\nM 100,172,112\nv_fma_f32 v237, v237, v218, v217\n"
"v_mul_f32 v234, v52, v234\nv_mul_f32 v235, v53, v235\nM 116,168,100\nR 162,187,20480\nv_mul_f32 v236, v54, v236\nv_mul_f32 v237, v55, v237\n"
"M 116,172,116\nbuffer_load_dwordx4 v221, s[16:19], s63 offen offset:1024 lds\nv_exp_f32 v234, v234\nM 132,168,104\nR 164,187,24576\ns_add_u32 s63, s58, 0\n"
"v_exp_f32 v235, v235\nM 132,172,120\ns_add_u32 m0, s50, 98304\nv_exp_f32 v236, v236\nv_exp_f32 v237, v237\nM 148,168,108\n"
"R 166,187,28672\nv_fma_f32 v234, v234, v219, v219\nv_fma_f32 v235, v235, v219, v219\nM 148,172,124\nG 4\nv_rcp_f32 v234, v234\n"
"v_fma_f32 v236, v236, v219, v219\nM 104,176,96\nR 168,184,32768\nv_fma_f32 v237, v237, v219, v219\nv_rcp_f32 v235, v235\nM 104,180,112\n"
"buffer_load_dwordx4 v220, s[16:19], s63 offen lds\nv_rcp_f32 v236, v236\nM 120,176,100\nR 170,184,36864\nv_mul_f32 v234, v52, v234\nv_rcp_f32 v237, v237\n"
"M 120,180,116\nv_mul_f32 v235, v53, v235\nv_mul_f32 v236, v54, v236\nv_mul_f32 v237, v55, v237\nM 136,176,104\nR 172,184,40960\n"
"v_cvt_pk_bf16_f32 v50, v234, v235\nM 136,180,120\nbuffer_load_dwordx4 v221, s[16:19], s63 offen offset:1024 lds\nM 152,176,108\nR 174,184,45056\ns_add_u32 s63, s58, 524288\n"
"M 152,180,124\nG 4\nv_cvt_pk_bf16_f32 v51, v236, v237\nv_mul_f32 v230, v56, v56\nM 108,160,96\nR 176,185,32768\n"
"v_mul_f32 v231, v57, v57\nM 108,164,112\ns_add_u32 m0, s50, 106496\nv_mul_f32 v232, v58, v58\nv_mul_f32 v233, v59, v59\nM 124,160,100\n"
"R 178,185,36864\nv_fma_f32 v230, v230, v218, v217\nM 124,164,116\nbuffer_load_dwordx4 v220, s[16:19], s63 offen lds\nM 140,160,104\nR 180,185,40960\n"
"v_fma_f32 v231, v231, v218, v217\nM 140,164,120\nbuffer_store_dwordx4 v[48:51], v252, s[20:23], s85 offen offset:0 sc1\nM 156,160,108\nR 182,185,45056\nv_fma_f32 v232, v232, v218, v217\n"
"M 156,164,124\ns_waitcnt vmcnt(10)\ns_barrier\nG 4\nM 96,168,128\nR 160,186,32768\n"
"v_fma_f32 v233, v233, v218, v217\nM 96,172,144\nbuffer_load_dwordx4 v221, s[16:19], s63 offen offset:1024 lds\nM 112,168,132\nR 162,186,36864\nv_mul_f32 v230, v56, v230\n"
"M 112,172,148\ns_add_u32 m0, s50, 114688\nv_exp_f32 v230, v230\nM 128,168,136\nR 164,186,40960\nv_mul_f32 v231, v57, v231\n"
"M 128,172,152\ns_add_u32 s63, s58, 65536\nv_exp_f32 v231, v231\nM 144,168,140\nR 166,186,45056\nv_mul_f32 v232, v58, v232\n"
"M 144,172,156\nG 4\nv_exp_f32 v232, v232\nM 100,176,128\nR 168,187,32768\nv_mul_f32 v233, v59, v233\n"
"M 100,180,144\nbuffer_load_dwordx4 v220, s[16:19], s63 offen lds\nM 116,176,132\nR 170,187,36864\nv_fma_f32 v230, v230, v219, v219\nM 116,180,148\n"
"v_exp_f32 v233, v233\nM 132,176,136\nR 172,187,40960\nv_fma_f32 v231, v231, v219, v219\nM 132,180,152\nv_rcp_f32 v230, v230\n"
"M 148,176,140\nR 174,187,45056\nv_fma_f32 v232, v232, v219, v219\nM 148,180,156\nG 4\nv_rcp_f32 v231, v231\n"
"M 104,160,128\nR 176,184,49152\nv_fma_f32 v233, v233, v219, v219\nM 104,164,144\nbuffer_load_dwordx4 v221, s[16:19], s63 offen offset:1024 lds\nM 120,160,132\n"
"R 178,184,53248\nv_mul_f32 v230, v56, v230\nM 120,164,148\ns_add_u32 m0, s50, 0\nv_rcp_f32 v232, v232\nM 136,160,136\n"
"R 180,184,57344\nv_mul_f32 v231, v57, v231\nM 136,164,152\ns_add_u32 s63, s58, 589824\nv_rcp_f32 v233, v233\nM 152,160,140\n"
"R 182,184,61440\nv_mul_f32 v232, v58, v232\nM 152,164,156\nG 4\nv_mul_f32 v233, v59, v233\nv_cvt_pk_bf16_f32 v56, v230, v231\n"
"M 108,168,128\nR 160,185,49152\nv_cvt_pk_bf16_f32 v57, v232, v233\nM 108,172,144\nbuffer_load_dwordx4 v220, s[16:19], s63 offen lds\nM 124,168,132\n"
"R 162,185,53248\nv_mul_f32 v234, v60, v60\nM 124,172,148\nv_mul_f32 v235, v61, v61\nv_mul_f32 v236, v62, v62\nM 140,168,136\n"
"R 164,185,57344\nv_mul_f32 v237, v63, v63\nM 140,172,152\nv_fma_f32 v234, v234, v218, v217\nv_fma_f32 v235, v235, v218, v217\nM 156,168,140\n"
"R 166,185,61440\nv_fma_f32 v236, v236, v218, v217\nM 156,172,156\nG 4\nv_fma_f32 v237, v237, v218, v217\nv_mul_f32 v234, v60, v234\n"
"M 0,176,64\nR 168,186,49152\nv_mul_f32 v235, v61, v235\nM 0,180,80\nbuffer_load_dwordx4 v221, s[16:19], s63 offen offset:1024 lds\nM 16,176,68\n"
"R 170,186,53248\nv_mul_f32 v236, v62, v236\nM 16,180,84\ns_add_u32 m0, s50, 8192\nv_exp_f32 v234, v234\nv_mfma_f32_16x16x32_bf16 a[224:227], v[176:179], a[72:75], a[224:227]\n"
"R 172,186,57344\nv_mul_f32 v237, v63, v237\nv_mfma_f32_16x16x32_bf16 a[224:227], v[180:183], a[88:91], a[224:227]\ns_add_u32 s63, s58, 131072\nv_exp_f32 v235, v235\nv_mfma_f32_16x16x32_bf16 a[240:243], v[176:179], a[76:79], a[240:243]\n"
"R 174,186,61440\nv_fma_f32 v234, v234, v219, v219\nv_mfma_f32_16x16x32_bf16 a[240:243], v[180:183], a[92:95], a[240:243]\nG 4\nv_exp_f32 v236, v236\nM 4,160,64\n"
"R 176,187,49152\nv_fma_f32 v235, v235, v219, v219\nM 4,164,80\nbuffer_load_dwordx4 v220, s[16:19], s63 offen lds\nM 20,160,68\nR 178,187,53248\n"
"v_fma_f32 v236, v236, v219, v219\nM 20,164,84\nv_exp_f32 v237, v237\nv_mfma_f32_16x16x32_bf16 a[228:231], v[160:163], a[72:75], a[228:231]\nR 180,187,57344\nv_fma_f32 v237, v237, v219, v219\n"
"v_mfma_f32_16x16x32_bf16 a[228:231], v[164:167], a[88:91], a[228:231]\nbuffer_load_dwordx4 v221, s[16:19], s63 offen offset:1024 lds\nv_mfma_f32_16x16x32_bf16 a[244:247], v[160:163], a[76:79], a[244:247]\nR 182,187,61440\ns_add_u32 s63, s58, 655360\nv_mfma_f32_16x16x32_bf16 a[244:247], v[164:167], a[92:95], a[244:247]\n"
"G 4\nv_rcp_f32 v234, v234\nM 8,168,64\nR 160,188,0\nv_mul_f32 v234, v60, v234\nM 8,172,80\n"
"s_add_u32 m0, s50, 16384\nv_rcp_f32 v235, v235\nM 24,168,68\nR 162,188,4096\nv_mul_f32 v235, v61, v235\nM 24,172,84\n"
"buffer_load_dwordx4 v220, s[16:19], s63 offen lds\nv_mfma_f32_16x16x32_bf16 a[232:235], v[168:171], a[72:75], a[232:235]\nR 164,188,8192\nv_cvt_pk_bf16_f32 v58, v234, v235\nv_mfma_f32_16x16x32_bf16 a[232:235], v[172:175], a[88:91], a[232:235]\nv_rcp_f32 v236, v236\n"
"v_mfma_f32_16x16x32_bf16 a[248:251], v[168:171], a[76:79], a[248:251]\nR 166,188,12288\nv_mul_f32 v236, v62, v236\nv_mfma_f32_16x16x32_bf16 a[248:251], v[172:175], a[92:95], a[248:251]\nG 4\nv_rcp_f32 v237, v237\n"
"M 12,176,64\nR 168,189,0\nv_mul_f32 v237, v63, v237\nM 12,180,80\nbuffer_load_dwordx4 v221, s[16:19], s63 offen offset:1024 lds\nM 28,176,68\n"
"R 170,189,4096\ns_add_u32 s63, s58, 196608\nM 28,180,84\ns_add_u32 m0, s50, 24576\nv_cvt_pk_bf16_f32 v59, v236, v237\nbuffer_store_dwordx4 v[56:59], v252, s[20:23], s85 offen offset:1024 sc1\n"
"v_mfma_f32_16x16x32_bf16 a[236:239], v[176:179], a[72:75], a[236:239]\nR 172,189,8192\nv_mfma_f32_16x16x32_bf16 a[236:239], v[180:183], a[88:91], a[236:239]\nbuffer_load_dwordx4 v220, s[16:19], s63 offen lds\nv_mfma_f32_16x16x32_bf16 a[252:255], v[176:179], a[76:79], a[252:255]\nR 174,189,12288\n"
"v_mfma_f32_16x16x32_bf16 a[252:255], v[180:183], a[92:95], a[252:255]\ns_waitcnt vmcnt(10)\ns_barrier\nG 4\nM 0,160,96\nR 176,190,0\n"
"M 0,164,112\nM 16,160,100\nR 178,190,4096\nM 16,164,116\nv_mfma_f32_16x16x32_bf16 a[224:227], v[160:163], a[104:107], a[224:227]\nR 180,190,8192\n"
"v_mfma_f32_16x16x32_bf16 a[224:227], v[164:167], a[120:123], a[224:227]\nv_mfma_f32_16x16x32_bf16 a[240:243], v[160:163], a[108:111], a[240:243]\nR 182,190,12288\nv_mfma_f32_16x16x32_bf16 a[240:243], v[164:167], a[124:127], a[240:243]\nG 4\nM 4,168,96\n"
"R 160,191,0\nM 4,172,112\nM 20,168,100\nR 162,191,4096\nM 20,172,116\nv_mfma_f32_16x16x32_bf16 a[228:231], v[168:171], a[104:107], a[228:231]\n"
"R 164,191,8192\nv_mfma_f32_16x16x32_bf16 a[228:231], v[172:175], a[120:123], a[228:231]\nv_mfma_f32_16x16x32_bf16 a[244:247], v[168:171], a[108:111], a[244:247]\nR 166,191,12288\nv_mfma_f32_16x16x32_bf16 a[244:247], v[172:175], a[124:127], a[244:247]\nG 4\n"
"M 8,176,96\nR 168,188,16384\nM 8,180,112\nM 24,176,100\nR 170,188,20480\nM 24,180,116\n"
"v_mfma_f32_16x16x32_bf16 a[232:235], v[176:179], a[104:107], a[232:235]\nR 172,188,24576\nv_mfma_f32_16x16x32_bf16 a[232:235], v[180:183], a[120:123], a[232:235]\nv_mfma_f32_16x16x32_bf16 a[248:251], v[176:179], a[108:111], a[248:251]\nR 174,188,28672\nv_mfma_f32_16x16x32_bf16 a[248:251], v[180:183], a[124:127], a[248:251]\n"
"G 4\nM 12,160,96\nR 176,189,16384\nM 12,164,112\nM 28,160,100\nR 178,189,20480\n"
"M 28,164,116\nv_mfma_f32_16x16x32_bf16 a[236:239], v[160:163], a[104:107], a[236:239]\nR 180,189,24576\nv_mfma_f32_16x16x32_bf16 a[236:239], v[164:167], a[120:123], a[236:239]\nv_mfma_f32_16x16x32_bf16 a[252:255], v[160:163], a[108:111], a[252:255]\nR 182,189,28672\n"
"v_mfma_f32_16x16x32_bf16 a[252:255], v[164:167], a[124:127], a[252:255]\nG 4\nM 0,168,128\nR 160,190,16384\nM 0,172,144\nM 16,168,132\n"
"R 162,190,20480\nM 16,172,148\nv_mfma_f32_16x16x32_bf16 a[224:227], v[168:171], a[136:139], a[224:227]\nR 164,190,24576\nv_mfma_f32_16x16x32_bf16 a[224:227], v[172:175], a[152:155], a[224:227]\nv_mfma_f32_16x16x32_bf16 a[240:243], v[168:171], a[140:143], a[240:243]\n"
"R 166,190,28672\nv_mfma_f32_16x16x32_bf16 a[240:243], v[172:175], a[156:159], a[240:243]\nG 4\nM 4,176,128\nR 168,191,16384\nM 4,180,144\n"
"M 20,176,132\nR 170,191,20480\nv_mov_b32 v226, s46\nM 20,180,148\nv_mov_b32 v227, s47\nv_mfma_f32_16x16x32_bf16 a[228:231], v[176:179], a[136:139], a[228:231]\n"
"R 172,191,24576\ns_add_u32 s61, s81, 845840\ns_lshl_b32 s62, s44, 5\ns_add_u32 s61, s61, s62\ns_waitcnt vmcnt(0)\ns_mov_b64 exec, 1\n"
"buffer_atomic_swap_x2 v[226:227], off, s[20:23], s61 sc1\ns_mov_b64 exec, -1\nv_mfma_f32_16x16x32_bf16 a[228:231], v[180:183], a[152:155], a[228:231]\nv_mfma_f32_16x16x32_bf16 a[244:247], v[176:179], a[140:143], a[244:247]\nR 174,191,28672\nv_mfma_f32_16x16x32_bf16 a[244:247], v[180:183], a[156:159], a[244:247]\n"
"G 4\nM 8,160,128\nR 176,188,32768\nM 8,164,144\nbuffer_load_dwordx4 v221, s[16:19], s63 offen offset:1024 lds\nM 24,160,132\n"
"R 178,188,36864\ns_add_u32 s63, s58, 720896\nM 24,164,148\ns_add_u32 m0, s50, 32768\nv_mfma_f32_16x16x32_bf16 a[232:235], v[160:163], a[136:139], a[232:235]\nR 180,188,40960\n"
"v_mfma_f32_16x16x32_bf16 a[232:235], v[164:167], a[152:155], a[232:235]\nbuffer_load_dwordx4 v220, s[16:19], s63 offen lds\nv_mfma_f32_16x16x32_bf16 a[248:251], v[160:163], a[140:143], a[248:251]\nR 182,188,45056\nv_mfma_f32_16x16x32_bf16 a[248:251], v[164:167], a[156:159], a[248:251]\nG 4\n"
"M 12,168,128\nR 160,189,32768\nM 12,172,144\nM 28,168,132\nR 162,189,36864\nM 28,172,148\n"
"buffer_load_dwordx4 v221, s[16:19], s63 offen offset:1024 lds\nv_mfma_f32_16x16x32_bf16 a[236:239], v[168:171], a[136:139], a[236:239]\nR 164,189,40960\ns_add_u32 s63, s58, 393216\nv_mfma_f32_16x16x32_bf16 a[236:239], v[172:175], a[152:155], a[236:239]\ns_add_u32 m0, s50, 40960\n"
"v_mfma_f32_16x16x32_bf16 a[252:255], v[168:171], a[140:143], a[252:255]\nR 166,189,45056\nv_mfma_f32_16x16x32_bf16 a[252:255], v[172:175], a[156:159], a[252:255]\ns_barrier\nG 4\nv_mfma_f32_16x16x32_bf16 v[32:35], v[176:179], a[0:3], 0\n"
"R 168,190,32768\nM 32,180,16\nbuffer_load_dwordx4 v220, s[16:19], s63 offen lds\nv_mfma_f32_16x16x32_bf16 v[48:51], v[176:179], a[4:7], 0\nR 170,190,36864\nM 48,180,20\n"
"v_mfma_f32_16x16x32_bf16 v[64:67], v[176:179], a[8:11], 0\nR 172,190,40960\nM 64,180,24\nbuffer_load_dwordx4 v221, s[16:19], s63 offen offset:1024 lds\nv_mfma_f32_16x16x32_bf16 v[80:83], v[176:179], a[12:15], 0\nR 174,190,45056\n"
"s_add_u32 s63, s58, 458752\nM 80,180,28\nG 4\nv_mfma_f32_16x16x32_bf16 v[36:39], v[160:163], a[0:3], 0\nR 176,191,32768\nM 36,164,16\n"
"s_add_u32 m0, s50, 49152\nv_mfma_f32_16x16x32_bf16 v[52:55], v[160:163], a[4:7], 0\nR 178,191,36864\nM 52,164,20\nbuffer_load_dwordx4 v220, s[16:19], s63 offen lds\nv_mfma_f32_16x16x32_bf16 v[68:71], v[160:163], a[8:11], 0\n"
"R 180,191,40960\nM 68,164,24\nv_mfma_f32_16x16x32_bf16 v[84:87], v[160:163], a[12:15], 0\nR 182,191,45056\nv_mov_b32 v248, 0\nM 84,164,28\n"
"G 4\nv_mov_b32 v249, 0\nv_mfma_f32_16x16x32_bf16 v[40:43], v[168:171], a[0:3], 0\nR 160,188,49152\ns_add_u32 s61, s82, 845840\ns_mov_b64 exec, 0xf\n"
"buffer_atomic_or_x2 v[248:249], v250, s[20:23], s61 offen sc0 sc1\ns_mov_b64 exec, -1\nM 40,172,16\nv_mfma_f32_16x16x32_bf16 v[56:59], v[168:171], a[4:7], 0\nR 162,188,53248\nM 56,172,20\n"
"v_mfma_f32_16x16x32_bf16 v[72:75], v[168:171], a[8:11], 0\nR 164,184,0\nM 72,172,24\nbuffer_load_dwordx4 v221, s[16:19], s63 offen offset:1024 lds\nv_mfma_f32_16x16x32_bf16 v[88:91], v[168:171], a[12:15], 0\nR 166,184,4096\n"
"s_add_u32 m0, s50, 57344\nM 88,172,28\nG 4\nv_mfma_f32_16x16x32_bf16 v[44:47], v[176:179], a[0:3], 0\nR 168,189,49152\ns_add_u32 s63, s56, 262144\n"
"M 44,180,16\nbuffer_load_dwordx4 v220, s[16:19], s63 offen lds\nv_mfma_f32_16x16x32_bf16 v[60:63], v[176:179], a[4:7], 0\nR 170,189,53248\nM 60,180,20\nv_mfma_f32_16x16x32_bf16 v[76:79], v[176:179], a[8:11], 0\n"
"R 172,185,0\nM 76,180,24\nv_mfma_f32_16x16x32_bf16 v[92:95], v[176:179], a[12:15], 0\nR 174,185,4096\nM 92,180,28\nG 4\n"
"M 32,160,32\nR 176,190,49152\nM 32,164,48\nbuffer_load_dwordx4 v221, s[16:19], s63 offen offset:1024 lds\nM 48,160,36\nR 178,190,53248\n"
"s_add_u32 s63, s56, 786432\nM 48,164,52\ns_add_u32 m0, s50, 65536\nM 64,160,40\nR 180,186,0\nM 64,164,56\n"
"buffer_load_dwordx4 v220, s[16:19], s63 offen lds\nM 80,160,44\nR 182,186,4096\nM 80,164,60\nG 4\nM 36,168,32\n"
"R 160,191,49152\nM 36,172,48\nM 52,168,36\nR 162,191,53248\nM 52,172,52\nbuffer_load_dwordx4 v221, s[16:19], s63 offen offset:1024 lds\n"
"M 68,168,40\nR 164,187,0\nM 68,172,56\ns_add_u32 m0, s50, 73728\nM 84,168,44\nR 166,187,4096\n"
"s_add_u32 s63, s56, 327680\nM 84,172,60\nG 4\nM 40,176,32\nR 168,184,8192\nM 40,180,48\n"
"s_add_u32 s64, s68, 37752832\nM 56,176,36\nR 170,184,12288\ns_waitcnt vmcnt(5)\nv_cmp_eq_u64 s[86:87], s[46:47], v[248:249]\nM 56,180,52\n"
"buffer_load_dwordx4 v220, s[16:19], s63 offen lds\nM 72,176,40\nR 172,184,16384\ns_and_b32 s62, s86, 0xf\ns_cmp_eq_u32 s62, 0xf\ns_cbranch_scc0 Lq922\n"
"Lq923:\nM 72,180,56\ns_add_u32 s65, s68, 37867520\nM 88,176,44\nR 174,184,20480\ns_barrier\n"
"M 88,180,60\nG 4\nM 44,160,32\nR 176,185,8192\nM 44,164,48\nbuffer_load_dwordx4 a[160:163], v252, s[20:23], s64 offen sc1\n"
"M 60,160,36\nR 178,185,12288\nM 60,164,52\ns_add_u32 s66, s69, 37752832\nM 76,160,40\nR 180,185,16384\n"
"M 76,164,56\nbuffer_load_dwordx4 a[164:167], v252, s[20:23], s65 offen sc1\nM 92,160,44\nR 182,185,20480\nM 92,164,60\nG 4\n"
"M 32,168,64\nR 160,186,8192\nM 32,172,80\ns_add_u32 s67, s69, 37867520\nM 48,168,68\nR 162,186,12288\n"
"M 48,172,84\nbuffer_load_dwordx4 a[168:171], v252, s[20:23], s66 offen sc1\nM 64,168,72\nR 164,186,16384\nM 64,172,88\nbuffer_load_dwordx4 v221, s[16:19], s63 offen offset:1024 lds\n"
"M 80,168,76\nR 166,186,20480\ns_add_u32 m0, s50, 81920\nM 80,172,92\nG 4\nM 36,176,64\n"
"R 168,187,8192\ns_add_u32 s63, s56, 851968\nM 36,180,80\nbuffer_load_dwordx4 a[172:175], v252, s[20:23], s67 offen sc1\nM 52,176,68\nR 170,187,12288\n"
"M 52,180,84\ns_add_u32 s64, s68, 37760000\nM 68,176,72\nR 172,187,16384\nbuffer_load_dwordx4 v220, s[16:19], s63 offen lds\nM 68,180,88\n"
"s_add_u32 s65, s68, 37874688\nM 84,176,76\nR 174,187,20480\nM 84,180,92\nG 4\nM 40,160,64\n"
"R 176,184,24576\nbuffer_load_dwordx4 v221, s[16:19], s63 offen offset:1024 lds\nM 40,164,80\nbuffer_load_dwordx4 a[176:179], v252, s[20:23], s64 offen sc1\nM 56,160,68\nR 178,184,28672\n"
"s_add_u32 m0, s50, 90112\nM 56,164,84\ns_add_u32 s66, s69, 37760000\ns_add_u32 s63, s57, 262144\nM 72,160,72\nR 180,184,32768\n"
"buffer_load_dwordx4 v220, s[16:19], s63 offen lds\nM 72,164,88\nbuffer_load_dwordx4 a[180:183], v252, s[20:23], s65 offen sc1\nM 88,160,76\nR 182,184,36864\nM 88,164,92\n"
"G 4\nM 44,168,64\nR 160,185,24576\nbuffer_load_dwordx4 v221, s[16:19], s63 offen offset:1024 lds\nM 44,172,80\ns_add_u32 s67, s69, 37874688\n"
"s_add_u32 s63, s57, 786432\nM 60,168,68\nR 162,185,28672\ns_add_u32 m0, s50, 98304\nM 60,172,84\nbuffer_load_dwordx4 a[184:187], v252, s[20:23], s66 offen sc1\n"
"M 76,168,72\nR 164,185,32768\nbuffer_load_dwordx4 v220, s[16:19], s63 offen lds\nM 76,172,88\nbuffer_load_dwordx4 v221, s[16:19], s63 offen offset:1024 lds\nM 92,168,76\n"
"R 166,185,36864\ns_add_u32 m0, s50, 106496\nM 92,172,92\nG 4\nM 32,176,96\nR 168,186,24576\n"
"s_add_u32 s63, s57, 327680\nM 32,180,112\nbuffer_load_dwordx4 a[188:191], v252, s[20:23], s67 offen sc1\nM 48,176,100\nR 170,186,28672\nM 48,180,116\n"
"s_add_u32 s64, s68, 37753856\nM 64,176,104\nR 172,186,32768\nbuffer_load_dwordx4 v220, s[16:19], s63 offen lds\nM 64,180,120\nbuffer_load_dwordx4 v221, s[16:19], s63 offen offset:1024 lds\n"
"M 80,176,108\nR 174,186,36864\ns_add_u32 m0, s50, 114688\nM 80,180,124\nG 4\nM 36,160,96\n"
"R 176,187,24576\ns_waitcnt vmcnt(18)\ns_barrier\nM 36,164,112\nbuffer_load_dwordx4 a[192:195], v252, s[20:23], s64 offen sc1\nM 52,160,100\n"
"R 178,187,28672\ns_add_u32 s63, s57, 851968\nM 52,164,116\ns_add_u32 s65, s68, 37868544\nM 68,160,104\nR 180,187,32768\n"
"buffer_load_dwordx4 v220, s[16:19], s63 offen lds\nM 68,164,120\ns_add_u32 s66, s69, 37753856\nM 84,160,108\nR 182,187,36864\nM 84,164,124\n"
"G 4\nM 40,168,96\nR 160,184,40960\nbuffer_load_dwordx4 v221, s[16:19], s63 offen offset:1024 lds\nM 40,172,112\nbuffer_load_dwordx4 a[196:199], v252, s[20:23], s65 offen sc1\n"
"M 56,168,100\nR 162,184,45056\nM 56,172,116\ns_add_u32 s67, s69, 37868544\nM 72,168,104\nR 164,184,49152\n"
"M 72,172,120\nbuffer_load_dwordx4 a[200:203], v252, s[20:23], s66 offen sc1\nM 88,168,108\nR 166,184,53248\nM 88,172,124\nG 4\n"
"M 44,176,96\nR 168,185,40960\nM 44,180,112\ns_add_u32 s64, s68, 37761024\nM 60,176,100\nR 170,185,45056\n"
"M 60,180,116\nbuffer_load_dwordx4 a[204:207], v252, s[20:23], s67 offen sc1\nM 76,176,104\nR 172,185,49152\nM 76,180,120\nM 92,176,108\n"
"R 174,185,53248\nM 92,180,124\nG 4\nM 32,160,128\nR 176,186,40960\nM 32,164,144\n"
"buffer_load_dwordx4 a[208:211], v252, s[20:23], s64 offen sc1\nM 48,160,132\nR 178,186,45056\nM 48,164,148\ns_add_u32 s65, s68, 37875712\nM 64,160,136\n"
"R 180,186,49152\nM 64,164,152\ns_add_u32 s66, s69, 37761024\nM 80,160,140\nR 182,186,53248\nM 80,164,156\n"
"G 4\nM 36,168,128\nR 160,187,40960\nM 36,172,144\nbuffer_load_dwordx4 a[212:215], v252, s[20:23], s65 offen sc1\nM 52,168,132\n"
"R 162,187,45056\nM 52,172,148\ns_add_u32 s67, s69, 37875712\nM 68,168,136\nR 164,187,49152\nM 68,172,152\n"
"buffer_load_dwordx4 a[216:219], v252, s[20:23], s66 offen sc1\nM 84,168,140\nR 166,187,53248\nM 84,172,156\nG 4\nM 40,176,128\n"
"R 168,184,57344\nM 40,180,144\nM 56,176,132\nR 170,184,61440\nM 56,180,148\nbuffer_load_dwordx4 a[220:223], v252, s[20:23], s67 offen sc1\n"
"M 72,176,136\nR 172,188,0\ns_add_u32 s63, s58, 262144\nM 72,180,152\ns_add_u32 m0, s50, 0\nM 88,176,140\n"
"R 174,188,4096\nM 88,180,156\nG 4\nM 44,160,128\nR 176,185,57344\nM 44,164,144\n"
"buffer_load_dwordx4 v220, s[16:19], s63 offen lds\nM 60,160,132\nR 178,185,61440\nM 60,164,148\nM 76,160,136\nR 180,189,0\n"
"M 76,164,152\nbuffer_load_dwordx4 v221, s[16:19], s63 offen offset:1024 lds\nM 92,160,140\nR 182,189,4096\nM 92,164,156\ns_waitcnt vmcnt(9)\n"
"s_barrier\nG 4\nM 96,168,160\nR 160,186,57344\nM 96,172,176\ns_add_u32 m0, s50, 8192\n"
"M 112,168,164\nR 162,186,61440\nM 112,172,180\ns_add_u32 s63, s58, 786432\nM 128,168,168\nR 164,190,0\n"
"v_lshrrev_b32 v230, 8, v252\nM 128,172,184\nbuffer_load_dwordx4 v220, s[16:19], s63 offen lds\nM 144,168,172\nR 166,190,4096\nv_lshlrev_b32 v230, 3, v230\n"
"M 144,172,188\nG 4\nM 100,176,160\nR 168,187,57344\nM 100,180,176\nbuffer_load_dwordx2 v[232:233], v230, s[36:39], s53 offen offset:0\n"
"M 116,176,164\nR 170,187,61440\nM 116,180,180\nbuffer_load_dwordx2 v[234:235], v230, s[36:39], s53 offen offset:32\nM 132,176,168\nR 172,191,0\n"
"M 132,180,184\nbuffer_load_dwordx4 v221, s[16:19], s63 offen offset:1024 lds\nM 148,176,172\nR 174,191,4096\nM 148,180,188\nG 4\n"
"M 104,160,160\nR 176,188,8192\nM 104,164,176\ns_add_u32 m0, s50, 16384\nM 120,160,164\nR 178,188,12288\n"
"M 120,164,180\ns_add_u32 s63, s58, 327680\nM 136,160,168\nR 180,188,16384\nbuffer_load_dwordx2 v[236:237], v230, s[36:39], s53 offen offset:64\nM 136,164,184\n"
"buffer_load_dwordx4 v220, s[16:19], s63 offen lds\nM 152,160,172\nR 182,188,20480\nM 152,164,188\nbuffer_load_dwordx2 v[238:239], v230, s[36:39], s53 offen offset:96\nG 4\n"
"M 108,168,160\nR 160,189,8192\nv_bfe_u32 v242, v252, 4, 4\nM 108,172,176\nv_lshrrev_b32 v243, 8, v252\nv_lshlrev_b32 v243, 2, v243\n"
"M 124,168,164\nR 162,189,12288\nv_sub_u32 v242, v242, v243\nM 124,172,180\nv_lshlrev_b32 v243, 4, v242\ns_mov_b64 s[60:61], 0x3f80\n"
"v_lshlrev_b64 v[208:209], v243, s[60:61]\nM 140,168,168\nR 164,189,16384\nM 140,172,184\nbuffer_load_dwordx4 v221, s[16:19], s63 offen offset:1024 lds\nM 156,168,172\n"
"R 166,189,20480\nM 156,172,188\nG 4\nM 96,176,192\nR 168,190,8192\ns_waitcnt vmcnt(13)\n"
"M 96,180,208\ns_add_u32 m0, s50, 24576\nM 112,176,196\nR 170,190,12288\ns_waitcnt vmcnt(12)\nM 112,180,212\n"
"s_add_u32 s63, s58, 851968\nM 128,176,200\nR 172,190,16384\ns_waitcnt vmcnt(11)\nM 128,180,216\nbuffer_load_dwordx4 v220, s[16:19], s63 offen lds\n"
"M 144,176,204\nR 174,190,20480\ns_waitcnt vmcnt(11)\nM 144,180,220\nG 4\nM 100,160,192\n"
"R 176,191,8192\nM 100,164,208\nv_cmp_gt_u32 s[60:61], 4, v242\ns_nop 1\nv_cndmask_b32 v212, 0, v208, s[60:61]\nv_cndmask_b32 v208, 0, v208, s[60:61]\n"
"v_cndmask_b32 v213, 0, v209, s[60:61]\nv_cndmask_b32 v209, 0, v209, s[60:61]\nM 116,160,196\nR 178,191,12288\nM 116,164,212\nM 132,160,200\n"
"R 180,191,16384\nM 132,164,216\nbuffer_load_dwordx4 v221, s[16:19], s63 offen offset:1024 lds\nM 148,160,204\nR 182,191,20480\nM 148,164,220\n"
"G 4\nM 104,168,192\nR 160,188,24576\nM 104,172,208\ns_add_u32 m0, s50, 32768\nM 120,168,196\n"
"R 162,188,28672\nM 120,172,212\ns_add_u32 s63, s59, 0\nM 136,168,200\nR 164,188,32768\nM 136,172,216\n"
"buffer_load_dwordx4 v220, s[16:19], s63 offen lds\nM 152,168,204\nR 166,188,36864\nM 152,172,220\nG 4\nM 108,176,192\n"
"R 168,189,24576\nM 108,180,208\nM 124,176,196\nR 170,189,28672\nM 124,180,212\nbuffer_load_dwordx4 v221, s[16:19], s63 offen offset:1024 lds\n"
"M 140,176,200\nR 172,189,32768\nM 140,180,216\nM 156,176,204\nR 174,189,36864\nM 156,180,220\n"
"G 4\nM 0,160,160\nR 176,190,24576\nM 0,164,176\ns_mov_b32 m0, s51\nM 16,160,164\n"
"R 178,190,28672\nds_write_addtid_b32 v128 offset:0\nM 16,164,180\nds_write_addtid_b32 v129 offset:256\nds_write_addtid_b32 v130 offset:512\nv_mfma_f32_16x16x32_bf16 a[224:227], v[160:163], a[168:171], a[224:227]\n"
"R 180,190,32768\nds_write_addtid_b32 v131 offset:768\nv_mfma_f32_16x16x32_bf16 a[224:227], v[164:167], a[184:187], a[224:227]\nds_write_addtid_b32 v132 offset:1024\nds_write_addtid_b32 v133 offset:1280\nv_mfma_f32_16x16x32_bf16 a[240:243], v[160:163], a[172:175], a[240:243]\n"
"R 182,190,36864\nds_write_addtid_b32 v134 offset:1536\nv_mfma_f32_16x16x32_bf16 a[240:243], v[164:167], a[188:191], a[240:243]\nds_write_addtid_b32 v135 offset:1792\nds_write_addtid_b32 v136 offset:2048\nG 13\n"
"M 4,168,160\nR 160,191,24576\nds_write_addtid_b32 v137 offset:2304\nM 4,172,176\nds_write_addtid_b32 v138 offset:2560\nds_write_addtid_b32 v139 offset:2816\n"
"M 20,168,164\nR 162,191,28672\nds_write_addtid_b32 v140 offset:3072\nM 20,172,180\nds_write_addtid_b32 v141 offset:3328\nds_write_addtid_b32 v142 offset:3584\n"
"v_mfma_f32_16x16x32_bf16 a[228:231], v[168:171], a[168:171], a[228:231]\nR 164,191,32768\nds_write_addtid_b32 v143 offset:3840\nv_mfma_f32_16x16x32_bf16 a[228:231], v[172:175], a[184:187], a[228:231]\nds_write_addtid_b32 v144 offset:4096\nds_write_addtid_b32 v145 offset:4352\n"
"v_mfma_f32_16x16x32_bf16 a[244:247], v[168:171], a[172:175], a[244:247]\nR 166,191,36864\nds_write_addtid_b32 v146 offset:4608\nv_mfma_f32_16x16x32_bf16 a[244:247], v[172:175], a[188:191], a[244:247]\nds_write_addtid_b32 v147 offset:4864\nds_write_addtid_b32 v148 offset:5120\n"
"G 15\nM 8,176,160\nR 168,188,40960\nds_write_addtid_b32 v149 offset:5376\nM 8,180,176\nds_write_addtid_b32 v150 offset:5632\n"
"ds_write_addtid_b32 v151 offset:5888\nM 24,176,164\nR 170,188,45056\nds_write_addtid_b32 v152 offset:6144\nM 24,180,180\nds_write_addtid_b32 v153 offset:6400\n"
"ds_write_addtid_b32 v154 offset:6656\nv_mfma_f32_16x16x32_bf16 a[232:235], v[176:179], a[168:171], a[232:235]\nR 172,188,49152\nds_write_addtid_b32 v155 offset:6912\nv_mfma_f32_16x16x32_bf16 a[232:235], v[180:183], a[184:187], a[232:235]\nds_write_addtid_b32 v156 offset:7168\n"
"ds_write_addtid_b32 v157 offset:7424\nds_write_addtid_b32 v158 offset:7680\nds_write_addtid_b32 v159 offset:7936\nv_mfma_f32_16x16x32_bf16 a[248:251], v[176:179], a[172:175], a[248:251]\nR 174,188,53248\ns_add_u32 m0, s50, 40960\n"
"v_mfma_f32_16x16x32_bf16 a[248:251], v[180:183], a[188:191], a[248:251]\ns_add_u32 s63, s59, 524288\nG 15\nM 12,160,160\nR 176,189,40960\ns_waitcnt vmcnt(8) lgkmcnt(2)\n"
"s_barrier\nM 12,164,176\nbuffer_load_dwordx4 v220, s[16:19], s63 offen lds\nds_read2st64_b32 v[128:129], v225 offset0:0 offset1:1\nM 28,160,164\nR 178,189,45056\n"
"ds_read2st64_b32 v[130:131], v225 offset0:2 offset1:3\nM 28,164,180\nds_read2st64_b32 v[132:133], v225 offset0:4 offset1:5\nds_read2st64_b32 v[134:135], v225 offset0:6 offset1:7\nv_mfma_f32_16x16x32_bf16 a[236:239], v[160:163], a[168:171], a[236:239]\nR 180,189,49152\n"
"ds_read2st64_b32 v[136:137], v225 offset0:8 offset1:9\nv_mfma_f32_16x16x32_bf16 a[236:239], v[164:167], a[184:187], a[236:239]\nbuffer_load_dwordx4 v221, s[16:19], s63 offen offset:1024 lds\nds_read2st64_b32 v[138:139], v225 offset0:10 offset1:11\nv_mfma_f32_16x16x32_bf16 a[252:255], v[160:163], a[172:175], a[252:255]\nR 182,189,53248\n"
"s_add_u32 s63, s59, 65536\nds_read2st64_b32 v[140:141], v225 offset0:12 offset1:13\nv_mfma_f32_16x16x32_bf16 a[252:255], v[164:167], a[188:191], a[252:255]\nds_read2st64_b32 v[142:143], v225 offset0:14 offset1:15\nG 12\nG 10\n"
"v_add_f32 v96, v96, v128\nM 0,168,192\nR 160,190,40960\nv_add_f32 v97, v97, v129\nG 9\nv_add_f32 v98, v98, v130\n"
"M 0,172,208\ns_add_u32 m0, s50, 49152\nds_read2st64_b32 v[144:145], v225 offset0:16 offset1:17\nv_add_f32 v99, v99, v131\nM 16,168,196\nR 162,190,45056\n"
"G 10\nv_add_f32 v100, v100, v132\nv_add_f32 v101, v101, v133\nM 16,172,212\nbuffer_load_dwordx4 v220, s[16:19], s63 offen lds\nG 9\n"
"v_add_f32 v102, v102, v134\nv_mfma_f32_16x16x32_bf16 a[224:227], v[168:171], a[200:203], a[224:227]\nR 164,190,49152\nv_add_f32 v103, v103, v135\nG 8\nv_add_f32 v104, v104, v136\n"
"v_mfma_f32_16x16x32_bf16 a[224:227], v[172:175], a[216:219], a[224:227]\nds_read2st64_b32 v[146:147], v225 offset0:18 offset1:19\nv_add_f32 v105, v105, v137\nG 8\nv_add_f32 v106, v106, v138\nv_mfma_f32_16x16x32_bf16 a[240:243], v[168:171], a[204:207], a[240:243]\n"
"R 166,190,53248\nv_add_f32 v107, v107, v139\nG 7\nv_add_f32 v108, v108, v140\nv_mfma_f32_16x16x32_bf16 a[240:243], v[172:175], a[220:223], a[240:243]\nds_read2st64_b32 v[148:149], v225 offset0:20 offset1:21\n"
"v_add_f32 v109, v109, v141\nM 4,176,192\nR 168,191,40960\nG 8\nv_add_f32 v110, v110, v142\nv_add_f32 v111, v111, v143\n"
"M 4,180,208\nbuffer_load_dwordx4 v221, s[16:19], s63 offen offset:1024 lds\nG 6\nv_add_f32 v112, v112, v144\nM 20,176,196\nR 170,191,45056\n"
"s_add_u32 s63, s59, 589824\nv_add_f32 v113, v113, v145\nM 20,180,212\ns_add_u32 m0, s50, 57344\nds_read2st64_b32 v[150:151], v225 offset0:22 offset1:23\nG 5\n"
"v_add_f32 v114, v114, v146\nv_mfma_f32_16x16x32_bf16 a[228:231], v[176:179], a[200:203], a[228:231]\nR 172,191,49152\nds_read2st64_b32 v[152:153], v225 offset0:24 offset1:25\nv_add_f32 v115, v115, v147\nv_mfma_f32_16x16x32_bf16 a[228:231], v[180:183], a[216:219], a[228:231]\n"
"buffer_load_dwordx4 v220, s[16:19], s63 offen lds\nG 5\nv_add_f32 v116, v116, v148\nv_mfma_f32_16x16x32_bf16 a[244:247], v[176:179], a[204:207], a[244:247]\nR 174,191,53248\nds_read2st64_b32 v[154:155], v225 offset0:26 offset1:27\n"
"v_add_f32 v117, v117, v149\nv_mfma_f32_16x16x32_bf16 a[244:247], v[180:183], a[220:223], a[244:247]\nds_read2st64_b32 v[156:157], v225 offset0:28 offset1:29\nds_read2st64_b32 v[158:159], v225 offset0:30 offset1:31\nM 8,160,192\nR 176,184,0\n"
"M 8,164,208\nG 7\nv_add_f32 v118, v118, v150\nv_add_f32 v119, v119, v151\nM 24,160,196\nR 178,184,4096\n"
"G 6\nv_add_f32 v120, v120, v152\nv_add_f32 v121, v121, v153\nM 24,164,212\nbuffer_load_dwordx4 v221, s[16:19], s63 offen offset:1024 lds\nv_mfma_f32_16x16x32_bf16 a[232:235], v[160:163], a[200:203], a[232:235]\n"
"R 180,184,8192\ns_add_u32 s63, s59, 131072\nG 5\nv_add_f32 v122, v122, v154\nv_mfma_f32_16x16x32_bf16 a[232:235], v[164:167], a[216:219], a[232:235]\ns_add_u32 m0, s50, 65536\n"
"v_add_f32 v123, v123, v155\nG 4\nv_add_f32 v124, v124, v156\nv_add_f32 v125, v125, v157\nG 3\nv_add_f32 v126, v126, v158\n"
"v_add_f32 v127, v127, v159\nv_mfma_f32_16x16x32_bf16 a[248:251], v[160:163], a[204:207], a[248:251]\nR 182,184,12288\nv_cvt_pk_bf16_f32 v96, v96, v97\nv_cvt_pk_bf16_f32 v97, v98, v99\nv_mfma_f32_16x16x32_bf16 a[248:251], v[164:167], a[220:223], a[248:251]\n"
"v_cvt_pk_bf16_f32 v98, v100, v101\nv_cvt_pk_bf16_f32 v99, v102, v103\nv_cvt_pk_bf16_f32 v104, v104, v105\nM 12,168,192\nR 160,185,0\nv_cvt_pk_bf16_f32 v105, v106, v107\n"
"v_cvt_pk_bf16_f32 v106, v108, v109\nM 12,172,208\nbuffer_load_dwordx4 v220, s[16:19], s63 offen lds\nv_cvt_pk_bf16_f32 v107, v110, v111\nM 28,168,196\nR 162,185,4096\n"
"v_cvt_pk_bf16_f32 v112, v112, v113\nv_cvt_pk_bf16_f32 v113, v114, v115\nM 28,172,212\nv_cvt_pk_bf16_f32 v114, v116, v117\nv_cvt_pk_bf16_f32 v115, v118, v119\nv_cvt_pk_bf16_f32 v120, v120, v121\n"
"v_mfma_f32_16x16x32_bf16 a[236:239], v[168:171], a[200:203], a[236:239]\nR 164,185,8192\nv_cvt_pk_bf16_f32 v121, v122, v123\nv_cvt_pk_bf16_f32 v122, v124, v125\nv_mfma_f32_16x16x32_bf16 a[236:239], v[172:175], a[216:219], a[236:239]\nbuffer_load_dwordx4 v221, s[16:19], s63 offen offset:1024 lds\n"
"v_cvt_pk_bf16_f32 v123, v126, v127\nv_mfma_f32_16x16x32_bf16 a[252:255], v[168:171], a[204:207], a[252:255]\nR 166,185,12288\ns_add_u32 s63, s59, 655360\ns_bfe_u32 s100, s56, 0x20007\ns_mul_i32 s100, s100, 49152\n"
"s_add_u32 s100, s100, s72\ns_add_u32 s100, s100, 65011712\nv_mfma_f32_16x16x32_bf16 a[252:255], v[172:175], a[220:223], a[252:255]\ns_waitcnt vmcnt(10)\ns_barrier\nbuffer_store_dwordx4 v[96:99], v252, s[20:23], s100 offen offset:0 sc1\n"
"G 4\nM 32,176,160\nR 168,186,0\nbuffer_store_dwordx4 v[104:107], v252, s[20:23], s100 offen offset:1024 sc1\nM 32,180,176\ns_add_u32 m0, s50, 73728\n"
"buffer_store_dwordx4 v[112:115], v252, s[20:23], s100 offen offset:2048 sc1\nv_xor_b32 v240, 0x2000, v225\nM 48,176,164\nR 170,186,4096\nds_write2st64_b32 v240, a224, a225 offset0:0 offset1:1\nM 48,180,180\n"
"buffer_load_dwordx4 v220, s[16:19], s63 offen lds\nds_write2st64_b32 v240, a226, a227 offset0:2 offset1:3\nM 64,176,168\nR 172,186,8192\nds_write2st64_b32 v240, a228, a229 offset0:4 offset1:5\nM 64,180,184\n"
"buffer_store_dwordx4 v[120:123], v252, s[20:23], s100 offen offset:3072 sc1\nds_write2st64_b32 v240, a230, a231 offset0:6 offset1:7\nM 80,176,172\nR 174,186,12288\nds_write2st64_b32 v240, a232, a233 offset0:8 offset1:9\nM 80,180,188\n"
"ds_write2st64_b32 v240, a234, a235 offset0:10 offset1:11\nG 10\nM 36,160,160\nR 176,187,0\nds_write2st64_b32 v240, a236, a237 offset0:12 offset1:13\nM 36,164,176\n"
"buffer_load_dwordx4 v221, s[16:19], s63 offen offset:1024 lds\nds_write2st64_b32 v240, a238, a239 offset0:14 offset1:15\nM 52,160,164\nR 178,187,4096\ns_add_u32 s63, s59, 196608\nds_write2st64_b32 v240, a240, a241 offset0:16 offset1:17\n"
"M 52,164,180\ns_add_u32 m0, s50, 81920\nds_write2st64_b32 v240, a242, a243 offset0:18 offset1:19\nM 68,160,168\nR 180,187,8192\nv_mfma_f32_16x16x32_bf16 v[96:99], v[208:211], v[192:195], 0\n"
"ds_write2st64_b32 v240, a244, a245 offset0:20 offset1:21\nM 68,164,184\nv_mfma_f32_16x16x32_bf16 v[100:103], v[210:213], v[192:195], 0\nbuffer_load_dwordx4 v220, s[16:19], s63 offen lds\nds_write2st64_b32 v240, a246, a247 offset0:22 offset1:23\nM 84,160,172\n"
"R 182,187,12288\nv_mfma_f32_16x16x32_bf16 v[104:107], v[208:211], v[196:199], 0\nds_write2st64_b32 v240, a248, a249 offset0:24 offset1:25\nM 84,164,188\nv_mfma_f32_16x16x32_bf16 v[108:111], v[210:213], v[196:199], 0\nds_write2st64_b32 v240, a250, a251 offset0:26 offset1:27\n"
"G 14\nM 40,168,160\nR 160,184,16384\nv_mfma_f32_16x16x32_bf16 v[112:115], v[208:211], v[200:203], 0\nds_write2st64_b32 v240, a252, a253 offset0:28 offset1:29\nM 40,172,176\n"
"v_mfma_f32_16x16x32_bf16 v[116:119], v[210:213], v[200:203], 0\nbuffer_load_dwordx4 v221, s[16:19], s63 offen offset:1024 lds\nM 56,168,164\nR 162,184,20480\nv_mfma_f32_16x16x32_bf16 v[120:123], v[208:211], v[204:207], 0\ns_add_u32 m0, s50, 90112\n"
"s_add_u32 s63, s59, 720896\nM 56,172,180\nv_mfma_f32_16x16x32_bf16 v[124:127], v[210:213], v[204:207], 0\nbuffer_load_dwordx4 v220, s[16:19], s63 offen lds\nM 72,168,168\nR 164,184,24576\n"
"v_mfma_f32_16x16x32_bf16 v[96:99], v[208:211], v[232:235], v[96:99]\nds_write2st64_b32 v240, a254, a255 offset0:30 offset1:31\nM 72,172,184\nv_mfma_f32_16x16x32_bf16 v[100:103], v[210:213], v[232:235], v[100:103]\nM 88,168,172\nR 166,184,28672\n"
"v_mfma_f32_16x16x32_bf16 v[104:107], v[208:211], v[236:239], v[104:107]\nM 88,172,188\nv_mfma_f32_16x16x32_bf16 v[108:111], v[210:213], v[236:239], v[108:111]\nG 8\nM 44,176,160\nR 168,185,16384\n"
"v_mfma_f32_16x16x32_bf16 v[112:115], v[208:211], v[232:235], v[112:115]\ns_waitcnt vmcnt(15) lgkmcnt(2)\ns_barrier\nM 44,180,176\nv_mfma_f32_16x16x32_bf16 v[116:119], v[210:213], v[232:235], v[116:119]\nbuffer_load_dwordx4 v221, s[16:19], s63 offen offset:1024 lds\n"
"ds_read2st64_b32 v[128:129], v225 offset0:0 offset1:1\nM 60,176,164\nR 170,185,20480\nv_mfma_f32_16x16x32_bf16 v[120:123], v[208:211], v[236:239], v[120:123]\ns_add_u32 s63, s59, 393216\nds_read2st64_b32 v[130:131], v225 offset0:2 offset1:3\n"
"M 60,180,180\nv_mfma_f32_16x16x32_bf16 v[124:127], v[210:213], v[236:239], v[124:127]\ns_add_u32 m0, s50, 98304\nds_read2st64_b32 v[132:133], v225 offset0:4 offset1:5\nds_read2st64_b32 v[134:135], v225 offset0:6 offset1:7\nM 76,176,168\n"
"R 172,185,24576\nds_read2st64_b32 v[136:137], v225 offset0:8 offset1:9\nM 76,180,184\nbuffer_load_dwordx4 v220, s[16:19], s63 offen lds\nds_read2st64_b32 v[138:139], v225 offset0:10 offset1:11\nM 92,176,172\n"
"R 174,185,28672\nds_read2st64_b32 v[140:141], v225 offset0:12 offset1:13\nM 92,180,188\nds_read2st64_b32 v[142:143], v225 offset0:14 offset1:15\nG 12\nG 10\n"
"v_add_f32 v0, v0, v128\nM 32,160,192\nR 176,186,16384\nv_add_f32 v1, v1, v129\nG 9\nv_add_f32 v2, v2, v130\n"
"M 32,164,208\nds_read2st64_b32 v[144:145], v225 offset0:16 offset1:17\nv_add_f32 v3, v3, v131\nG 9\nv_add_f32 v4, v4, v132\nM 48,160,196\n"
"R 178,186,20480\nv_add_f32 v5, v5, v133\nG 9\nv_add_f32 v6, v6, v134\nM 48,164,212\nbuffer_load_dwordx4 v221, s[16:19], s63 offen offset:1024 lds\n"
"v_add_f32 v7, v7, v135\nM 64,160,200\nR 180,186,24576\ns_add_u32 s63, s59, 458752\nG 8\nv_add_f32 v8, v8, v136\n"
"M 64,164,216\ns_add_u32 m0, s50, 106496\nds_read2st64_b32 v[146:147], v225 offset0:18 offset1:19\nv_add_f32 v9, v9, v137\nM 80,160,204\nR 182,186,28672\n"
"G 9\nv_add_f32 v10, v10, v138\nv_add_f32 v11, v11, v139\nM 80,164,220\nds_read2st64_b32 v[148:149], v225 offset0:20 offset1:21\nG 9\n"
"G 8\nv_add_f32 v12, v12, v140\nM 36,168,192\nR 160,187,16384\nv_add_f32 v13, v13, v141\nG 8\n"
"v_add_f32 v14, v14, v142\nM 36,172,208\nbuffer_load_dwordx4 v220, s[16:19], s63 offen lds\nv_add_f32 v15, v15, v143\nM 52,168,196\nR 162,187,20480\n"
"G 7\nv_add_f32 v16, v16, v144\nv_add_f32 v17, v17, v145\nM 52,172,212\nds_read2st64_b32 v[150:151], v225 offset0:22 offset1:23\nG 5\n"
"v_add_f32 v18, v18, v146\nv_add_f32 v19, v19, v147\nM 68,168,200\nR 164,187,24576\nds_read2st64_b32 v[152:153], v225 offset0:24 offset1:25\nM 68,172,216\n"
"buffer_load_dwordx4 v221, s[16:19], s63 offen offset:1024 lds\nG 5\nv_add_f32 v20, v20, v148\nM 84,168,204\nR 166,187,28672\ns_add_u32 s63, s59, 262144\n"
"v_add_f32 v21, v21, v149\nM 84,172,220\nds_read2st64_b32 v[154:155], v225 offset0:26 offset1:27\nds_read2st64_b32 v[156:157], v225 offset0:28 offset1:29\nM 40,176,192\nR 168,184,32768\n"
"ds_read2st64_b32 v[158:159], v225 offset0:30 offset1:31\nM 40,180,208\ns_add_u32 m0, s50, 114688\nG 7\nv_add_f32 v22, v22, v150\nv_add_f32 v23, v23, v151\n"
"M 56,176,196\nR 170,184,36864\nG 6\nv_add_f32 v24, v24, v152\nv_add_f32 v25, v25, v153\nM 56,180,212\n"
"buffer_load_dwordx4 v220, s[16:19], s63 offen lds\nM 72,176,200\nR 172,184,40960\nM 72,180,216\nG 5\nv_add_f32 v26, v26, v154\n"
"v_add_f32 v27, v27, v155\nG 4\nv_add_f32 v28, v28, v156\nv_add_f32 v29, v29, v157\nG 2\nv_add_f32 v30, v30, v158\n"
"v_add_f32 v31, v31, v159\nM 88,176,204\nR 174,184,45056\nv_cvt_pk_bf16_f32 v0, v0, v1\nv_cvt_pk_bf16_f32 v1, v2, v3\nM 88,180,220\n"
"v_cvt_pk_bf16_f32 v2, v4, v5\nv_cvt_pk_bf16_f32 v3, v6, v7\nv_cvt_pk_bf16_f32 v8, v8, v9\nM 44,160,192\nR 176,185,32768\nv_cvt_pk_bf16_f32 v9, v10, v11\n"
"v_cvt_pk_bf16_f32 v10, v12, v13\nM 44,164,208\nbuffer_load_dwordx4 v221, s[16:19], s63 offen offset:1024 lds\nv_cvt_pk_bf16_f32 v11, v14, v15\nM 60,160,196\nR 178,185,36864\n"
"s_add_u32 s63, s59, 786432\nv_cvt_pk_bf16_f32 v16, v16, v17\nM 60,164,212\ns_add_u32 m0, s50, 0\nv_cvt_pk_bf16_f32 v17, v18, v19\nv_cvt_pk_bf16_f32 v18, v20, v21\n"
"v_cvt_pk_bf16_f32 v19, v22, v23\nM 76,160,200\nR 180,185,40960\nv_cvt_pk_bf16_f32 v24, v24, v25\nv_cvt_pk_bf16_f32 v25, v26, v27\nM 76,164,216\n"
"buffer_load_dwordx4 v220, s[16:19], s63 offen lds\nv_cvt_pk_bf16_f32 v26, v28, v29\nM 92,160,204\nR 182,185,45056\ns_bfe_u32 s100, s57, 0x20007\ns_mul_i32 s100, s100, 49152\n"
"s_add_u32 s100, s100, s72\ns_add_u32 s100, s100, 65028096\nv_cvt_pk_bf16_f32 v27, v30, v31\nM 92,164,220\ns_waitcnt vmcnt(19)\ns_barrier\n"
"buffer_store_dwordx4 v[0:3], v252, s[20:23], s100 offen offset:0 sc1\nG 4\nM 96,168,0\nR 160,186,32768\nbuffer_store_dwordx4 v[8:11], v252, s[20:23], s100 offen offset:1024 sc1\nM 96,172,16\n"
"s_mov_b32 m0, s51\nbuffer_store_dwordx4 v[16:19], v252, s[20:23], s100 offen offset:2048 sc1\nM 112,168,4\nR 162,186,36864\nds_write_addtid_b32 v64 offset:0\nM 112,172,20\n"
"ds_write_addtid_b32 v65 offset:256\nds_write_addtid_b32 v66 offset:512\nv_mfma_f32_16x16x32_bf16 v[128:131], v[168:171], a[8:11], 0\nR 164,186,40960\nds_write_addtid_b32 v67 offset:768\nM 128,172,24\n"
"ds_write_addtid_b32 v68 offset:1024\nds_write_addtid_b32 v69 offset:1280\nv_mfma_f32_16x16x32_bf16 v[144:147], v[168:171], a[12:15], 0\nR 166,186,45056\nds_write_addtid_b32 v70 offset:1536\nM 144,172,28\n"
"ds_write_addtid_b32 v71 offset:1792\nds_write_addtid_b32 v72 offset:2048\nG 13\nM 100,176,0\nR 168,187,32768\nds_write_addtid_b32 v73 offset:2304\n"
"M 100,180,16\nds_write_addtid_b32 v74 offset:2560\nbuffer_store_dwordx4 v[24:27], v252, s[20:23], s100 offen offset:3072 sc1\nds_write_addtid_b32 v75 offset:2816\nM 116,176,4\nR 170,187,36864\n"
"ds_write_addtid_b32 v76 offset:3072\nM 116,180,20\nds_write_addtid_b32 v77 offset:3328\nds_write_addtid_b32 v78 offset:3584\nv_mfma_f32_16x16x32_bf16 v[132:135], v[176:179], a[8:11], 0\nR 172,187,40960\n"
"ds_write_addtid_b32 v79 offset:3840\nM 132,180,24\nds_write_addtid_b32 v80 offset:4096\nds_write_addtid_b32 v81 offset:4352\nv_mfma_f32_16x16x32_bf16 v[148:151], v[176:179], a[12:15], 0\nR 174,187,45056\n"
"ds_write_addtid_b32 v82 offset:4608\nM 148,180,28\nds_write_addtid_b32 v83 offset:4864\nds_write_addtid_b32 v84 offset:5120\nG 15\nM 104,160,0\n"
"R 176,184,49152\nds_write_addtid_b32 v85 offset:5376\nM 104,164,16\nds_write_addtid_b32 v86 offset:5632\nds_write_addtid_b32 v87 offset:5888\nM 120,160,4\n"
"R 178,184,53248\nds_write_addtid_b32 v88 offset:6144\nM 120,164,20\nds_write_addtid_b32 v89 offset:6400\nds_write_addtid_b32 v90 offset:6656\nv_mfma_f32_16x16x32_bf16 v[136:139], v[160:163], a[8:11], 0\n"
"R 180,184,57344\nds_write_addtid_b32 v91 offset:6912\nM 136,164,24\nds_write_addtid_b32 v92 offset:7168\nds_write_addtid_b32 v93 offset:7424\nds_write_addtid_b32 v94 offset:7680\n"
"ds_write_addtid_b32 v95 offset:7936\nv_mfma_f32_16x16x32_bf16 v[152:155], v[160:163], a[12:15], 0\nR 182,184,61440\nM 152,164,28\nG 15\nM 108,168,0\n"
"R 160,185,49152\ns_waitcnt vmcnt(15) lgkmcnt(2)\ns_barrier\nM 108,172,16\nds_read2st64_b32 v[64:65], v225 offset0:0 offset1:1\nds_read2st64_b32 v[66:67], v225 offset0:2 offset1:3\n"
"M 124,168,4\nR 162,185,53248\nds_read2st64_b32 v[68:69], v225 offset0:4 offset1:5\nM 124,172,20\nds_read2st64_b32 v[70:71], v225 offset0:6 offset1:7\nds_read2st64_b32 v[72:73], v225 offset0:8 offset1:9\n"
"v_mfma_f32_16x16x32_bf16 v[140:143], v[168:171], a[8:11], 0\nR 164,185,57344\nds_read2st64_b32 v[74:75], v225 offset0:10 offset1:11\nM 140,172,24\nds_read2st64_b32 v[76:77], v225 offset0:12 offset1:13\nds_read2st64_b32 v[78:79], v225 offset0:14 offset1:15\n"
"v_mfma_f32_16x16x32_bf16 v[156:159], v[168:171], a[12:15], 0\nR 166,185,61440\nds_read2st64_b32 v[80:81], v225 offset0:16 offset1:17\nM 156,172,28\nds_read2st64_b32 v[82:83], v225 offset0:18 offset1:19\nG 14\n"
"G 12\nv_add_f32 v32, v32, v64\nM 96,176,32\nR 168,186,49152\nv_add_f32 v33, v33, v65\nG 12\n"
"v_add_f32 v34, v34, v66\nM 96,180,48\nds_read2st64_b32 v[84:85], v225 offset0:20 offset1:21\nv_add_f32 v35, v35, v67\nG 11\nv_add_f32 v36, v36, v68\n"
"M 112,176,36\nR 170,186,53248\nv_add_f32 v37, v37, v69\nG 11\nv_add_f32 v38, v38, v70\nM 112,180,52\n"
"ds_read2st64_b32 v[86:87], v225 offset0:22 offset1:23\nv_add_f32 v39, v39, v71\nG 11\nv_add_f32 v40, v40, v72\nM 128,176,40\nR 172,186,57344\n"
"v_add_f32 v41, v41, v73\nG 10\nv_add_f32 v42, v42, v74\nM 128,180,56\nds_read2st64_b32 v[88:89], v225 offset0:24 offset1:25\nv_add_f32 v43, v43, v75\n"
"G 10\nv_add_f32 v44, v44, v76\nM 144,176,44\nR 174,186,61440\nv_add_f32 v45, v45, v77\nG 10\n"
"v_add_f32 v46, v46, v78\nM 144,180,60\nds_read2st64_b32 v[90:91], v225 offset0:26 offset1:27\nG 10\nv_add_f32 v47, v47, v79\nM 100,160,32\n"
"R 176,187,49152\nG 10\nv_add_f32 v48, v48, v80\nv_add_f32 v49, v49, v81\nM 100,164,48\nds_read2st64_b32 v[92:93], v225 offset0:28 offset1:29\n"
"G 10\nv_add_f32 v50, v50, v82\nv_add_f32 v51, v51, v83\nM 116,160,36\nR 178,187,53248\nG 9\n"
"v_add_f32 v52, v52, v84\nM 116,164,52\nds_read2st64_b32 v[94:95], v225 offset0:30 offset1:31\nv_add_f32 v53, v53, v85\nM 132,160,40\nR 180,187,57344\n"
"G 9\nv_add_f32 v54, v54, v86\nM 132,164,56\nv_add_f32 v55, v55, v87\nG 7\nv_add_f32 v56, v56, v88\n"
"M 148,160,44\nR 182,187,61440\nv_add_f32 v57, v57, v89\nM 148,164,60\nG 7\nG 6\n"
"v_add_f32 v58, v58, v90\nv_add_f32 v59, v59, v91\nM 104,168,32\nR 160,188,0\nG 5\nv_add_f32 v60, v60, v92\n"
"M 104,172,48\nv_add_f32 v61, v61, v93\nG 3\nv_add_f32 v62, v62, v94\nM 120,168,36\nR 162,188,4096\n"
"v_mov_b32 v226, s46\nM 120,172,52\nv_mov_b32 v227, s47\nv_add_f32 v63, v63, v95\nM 136,168,40\nR 164,188,8192\n"
"s_bfe_u32 s62, s56, 0x20007\ns_lshl_b32 s62, s62, 5\ns_add_u32 s61, s74, 1009664\ns_add_u32 s61, s61, s62\ns_waitcnt vmcnt(0)\ns_mov_b64 exec, 1\n"
"buffer_atomic_swap_x2 v[226:227], off, s[20:23], s61 sc1\ns_mov_b64 exec, -1\nM 136,172,56\ns_bfe_u32 s62, s57, 0x20007\ns_lshl_b32 s62, s62, 5\ns_add_u32 s61, s74, 1009672\n"
"s_add_u32 s61, s61, s62\ns_mov_b64 exec, 1\nbuffer_atomic_swap_x2 v[226:227], off, s[20:23], s61 sc1\ns_mov_b64 exec, -1\nM 152,168,44\nR 166,188,12288\n"
"v_cvt_pk_bf16_f32 v32, v32, v33\nv_cvt_pk_bf16_f32 v33, v34, v35\nM 152,172,60\nG 4\nv_cvt_pk_bf16_f32 v34, v36, v37\nv_cvt_pk_bf16_f32 v35, v38, v39\n"
"v_cvt_pk_bf16_f32 v40, v40, v41\nM 108,176,32\nR 168,189,0\nv_cvt_pk_bf16_f32 v41, v42, v43\nM 108,180,48\ns_add_u32 m0, s50, 0\n"
"v_cvt_pk_bf16_f32 v42, v44, v45\nv_cvt_pk_bf16_f32 v43, v46, v47\nM 124,176,36\nR 170,189,4096\nv_cvt_pk_bf16_f32 v48, v48, v49\nM 124,180,52\n"
"v_cvt_pk_bf16_f32 v49, v50, v51\nv_cvt_pk_bf16_f32 v50, v52, v53\nM 140,176,40\nR 172,189,8192\nv_cvt_pk_bf16_f32 v51, v54, v55\nM 140,180,56\n"
"buffer_load_dwordx4 v221, s[16:19], s63 offen offset:1024 lds\nM 156,176,44\nR 174,189,12288\nv_cvt_pk_bf16_f32 v56, v56, v57\nM 156,180,60\ns_barrier\n"
"G 4\nM 96,160,64\nR 176,190,0\nv_cvt_pk_bf16_f32 v57, v58, v59\nM 96,164,80\ns_add_u32 m0, s50, 8192\n"
"v_cvt_pk_bf16_f32 v58, v60, v61\nv_cvt_pk_bf16_f32 v59, v62, v63\nM 112,160,68\nR 178,190,4096\ns_bfe_u32 s100, s58, 0x20007\ns_mul_i32 s100, s100, 49152\n"
"s_add_u32 s100, s100, s72\ns_add_u32 s100, s100, 65044480\nM 112,164,84\ns_add_u32 s63, s59, 327680\nbuffer_store_dwordx4 v[32:35], v252, s[20:23], s100 offen offset:0 sc1\nM 128,160,72\n"
"R 180,190,8192\nbuffer_store_dwordx4 v[40:43], v252, s[20:23], s100 offen offset:1024 sc1\nM 128,164,88\nbuffer_load_dwordx4 v220, s[16:19], s63 offen lds\nM 144,160,76\nR 182,190,12288\n"
"M 144,164,92\nbuffer_store_dwordx4 v[48:51], v252, s[20:23], s100 offen offset:2048 sc1\nG 4\nM 100,168,64\nR 160,191,0\nv_bfe_u32 v250, v252, 4, 2\n"
"M 100,172,80\nbuffer_store_dwordx4 v[56:59], v252, s[20:23], s100 offen offset:3072 sc1\nM 116,168,68\nR 162,191,4096\nv_lshlrev_b32 v250, 3, v250\nM 116,172,84\n"
"s_lshl_b32 s60, s43, 5\nv_add_u32 v250, s60, v250\nM 132,168,72\nR 164,191,8192\nM 132,172,88\nbuffer_load_dwordx4 v221, s[16:19], s63 offen offset:1024 lds\n"
"M 148,168,76\nR 166,191,12288\nM 148,172,92\nG 4\nM 104,176,64\nR 168,188,16384\n"
"M 104,180,80\ns_add_u32 m0, s50, 16384\nM 120,176,68\nR 170,188,20480\nM 120,180,84\ns_add_u32 s63, s59, 851968\n"
"M 136,176,72\nR 172,188,24576\nM 136,180,88\nbuffer_load_dwordx4 v220, s[16:19], s63 offen lds\nM 152,176,76\nR 174,188,28672\n"
"M 152,180,92\nG 4\nM 108,160,64\nR 176,189,16384\nM 108,164,80\nM 124,160,68\n"
"R 178,189,20480\nM 124,164,84\nM 140,160,72\nR 180,189,24576\nM 140,164,88\nbuffer_load_dwordx4 v221, s[16:19], s63 offen offset:1024 lds\n"
"M 156,160,76\nR 182,189,28672\nM 156,164,92\nG 4\nM 96,168,96\nR 160,190,16384\n"
"M 96,172,112\nM 112,168,100\nR 162,190,20480\nM 112,172,116\nM 128,168,104\nR 164,190,24576\n"
"M 128,172,120\nM 144,168,108\nR 166,190,28672\nM 144,172,124\nG 4\nM 100,176,96\n"
"R 168,191,16384\nM 100,180,112\nM 116,176,100\nR 170,191,20480\nM 116,180,116\nM 132,176,104\n"
"R 172,191,24576\nv_mov_b32 v248, 0\nM 132,180,120\nv_mov_b32 v249, 0\nM 148,176,108\nR 174,191,28672\n"
"s_add_u32 s61, s74, 1009664\ns_mov_b64 exec, 3\nbuffer_atomic_or_x2 v[248:249], v250, s[20:23], s61 offen sc0 sc1\ns_mov_b64 exec, -1\nM 148,180,124\nG 4\n"
"M 104,160,96\nR 176,188,32768\ns_waitcnt vmcnt(9)\ns_barrier\nM 104,164,112\nM 120,160,100\n"
"R 178,188,36864\nM 120,164,116\nM 136,160,104\nR 180,188,40960\nM 136,164,120\nM 152,160,108\n"
"R 182,188,45056\nM 152,164,124\nG 4\nM 108,168,96\nR 160,189,32768\nM 108,172,112\n"
"M 124,168,100\nR 162,189,36864\nM 124,172,116\nM 140,168,104\nR 164,189,40960\nM 140,172,120\n"
"M 156,168,108\nR 166,189,45056\nM 156,172,124\nG 4\nM 96,176,128\nR 168,190,32768\n"
"M 96,180,144\nM 112,176,132\nR 170,190,36864\nM 112,180,148\nM 128,176,136\nR 172,190,40960\n"
"M 128,180,152\nM 144,176,140\nR 174,190,45056\nM 144,180,156\nG 4\nM 100,160,128\n"
"R 176,191,32768\nM 100,164,144\nM 116,160,132\nR 178,191,36864\nM 116,164,148\nM 132,160,136\n"
"R 180,191,40960\nM 132,164,152\ns_mul_i32 s64, s43, 49152\nM 148,160,140\nR 182,191,45056\nM 148,164,156\n"
"G 4\nM 104,168,128\nR 160,188,49152\ns_waitcnt vmcnt(0)\nv_cmp_eq_u64 s[86:87], s[46:47], v[248:249]\nM 104,172,144\n"
"s_add_u32 s64, s64, s72\nM 120,168,132\nR 162,188,53248\ns_and_b32 s62, s86, 3\ns_cmp_eq_u32 s62, 3\ns_cbranch_scc0 Lq964\n"
"Lq965:\nM 120,172,148\ns_add_u32 s64, s64, 65011712\nM 136,168,136\nR 164,184,0\nM 136,172,152\n"
"buffer_load_dwordx4 v[0:3], v252, s[20:23], s64 offen offset:0 sc1\nM 152,168,140\nR 166,184,4096\nM 152,172,156\nG 4\nM 108,176,128\n"
"R 168,189,49152\ns_barrier\nM 108,180,144\nbuffer_load_dwordx4 v[4:7], v252, s[20:23], s64 offen offset:1024 sc1\nM 124,176,132\nR 170,189,53248\n"
"s_mul_i32 s65, s43, 49152\nM 124,180,148\nbuffer_load_dwordx4 v[8:11], v252, s[20:23], s64 offen offset:2048 sc1\nM 140,176,136\nR 172,185,0\ns_add_u32 s65, s65, s72\n"
"M 140,180,152\nbuffer_load_dwordx4 v[12:15], v252, s[20:23], s64 offen offset:3072 sc1\nM 156,176,140\nR 174,185,4096\ns_add_u32 s65, s65, 65028096\nM 156,180,156\n"
"G 4\nM 128,160,168\nR 176,190,49152\nbuffer_load_dwordx4 v[16:19], v252, s[20:23], s65 offen offset:0 sc1\nM 128,164,184\nR 178,190,53248\n"
"M 144,160,172\nR 180,186,0\nM 144,164,188\nR 182,186,4096\nG 4\nM 132,168,168\n"
"R 160,191,49152\nbuffer_load_dwordx4 v[20:23], v252, s[20:23], s65 offen offset:1024 sc1\nM 132,172,184\nR 162,191,53248\nM 148,168,172\nR 164,187,0\n"
"buffer_load_dwordx4 v[24:27], v252, s[20:23], s65 offen offset:2048 sc1\nM 148,172,188\nR 166,187,4096\nG 4\nM 136,176,168\nR 168,184,8192\n"
"s_mul_i32 s66, s43, 49152\nM 136,180,184\nR 170,184,12288\nbuffer_load_dwordx4 v[28:31], v252, s[20:23], s65 offen offset:3072 sc1\nM 152,176,172\nR 172,184,16384\n"
"s_add_u32 s66, s66, s72\nM 152,180,188\nR 174,184,20480\nG 4\nM 140,160,168\nR 176,185,8192\n"
"s_add_u32 s66, s66, 65044480\nM 140,164,184\nR 178,185,12288\nM 156,160,172\nR 180,185,16384\nM 156,164,188\n"
"R 182,185,20480\nG 4\nM 128,168,200\nR 160,186,8192\nbuffer_load_dwordx4 v[64:67], v252, s[20:23], s66 offen offset:0 sc1\nM 128,172,216\n"
"R 162,186,12288\nM 144,168,204\nR 164,186,16384\nbuffer_load_dwordx4 v[68:71], v252, s[20:23], s66 offen offset:1024 sc1\nM 144,172,220\nR 166,186,20480\n"
"G 4\nM 132,176,200\nR 168,187,8192\nM 132,180,216\nR 170,187,12288\nbuffer_load_dwordx4 v[72:75], v252, s[20:23], s66 offen offset:2048 sc1\n"
"M 148,176,204\nR 172,187,16384\nM 148,180,220\nR 174,187,20480\nG 4\nM 136,160,200\n"
"R 176,188,49152\nbuffer_load_dwordx4 v[76:79], v252, s[20:23], s66 offen offset:3072 sc1\nM 136,164,216\nR 178,188,53248\nM 152,160,204\nR 180,184,0\n"
"M 152,164,220\nR 182,184,4096\nG 4\nM 140,168,200\nR 160,189,49152\nM 140,172,216\n"
"R 162,189,53248\nM 156,168,204\nR 164,185,0\nM 156,172,220\nR 166,185,4096\nG 4\n"
"M 96,176,160\nR 168,190,49152\nM 96,180,176\nR 170,190,53248\ns_mov_b32 m0, s51\nM 112,176,164\n"
"R 172,186,0\nds_write_addtid_b32 v128 offset:0\nM 112,180,180\nR 174,186,4096\nds_write_addtid_b32 v129 offset:256\nG 6\n"
"M 100,160,160\nR 176,191,49152\nds_write_addtid_b32 v130 offset:512\nM 100,164,176\nR 178,191,53248\nds_write_addtid_b32 v131 offset:768\n"
"M 116,160,164\nR 180,187,0\nds_write_addtid_b32 v132 offset:1024\nM 116,164,180\nR 182,187,4096\nds_write_addtid_b32 v133 offset:1280\n"
"G 9\nM 104,168,160\nR 160,184,8192\nds_write_addtid_b32 v134 offset:1536\nM 104,172,176\nR 162,184,12288\n"
"ds_write_addtid_b32 v135 offset:1792\nM 120,168,164\nR 164,184,16384\nds_write_addtid_b32 v136 offset:2048\nM 120,172,180\nR 166,184,20480\n"
"ds_write_addtid_b32 v137 offset:2304\nG 9\nM 108,176,160\nR 168,185,8192\nds_write_addtid_b32 v138 offset:2560\nM 108,180,176\n"
"R 170,185,12288\nds_write_addtid_b32 v139 offset:2816\nM 124,176,164\nR 172,185,16384\nds_write_addtid_b32 v140 offset:3072\nds_write_addtid_b32 v141 offset:3328\n"
"ds_write_addtid_b32 v142 offset:3584\nds_write_addtid_b32 v143 offset:3840\nds_write_addtid_b32 v144 offset:4096\nds_write_addtid_b32 v145 offset:4352\nds_write_addtid_b32 v146 offset:4608\nds_write_addtid_b32 v147 offset:4864\n"
"ds_write_addtid_b32 v148 offset:5120\nds_write_addtid_b32 v149 offset:5376\nds_write_addtid_b32 v150 offset:5632\nds_write_addtid_b32 v151 offset:5888\nds_write_addtid_b32 v152 offset:6144\nds_write_addtid_b32 v153 offset:6400\n"
"ds_write_addtid_b32 v154 offset:6656\nds_write_addtid_b32 v155 offset:6912\nds_write_addtid_b32 v156 offset:7168\nds_write_addtid_b32 v157 offset:7424\nds_write_addtid_b32 v158 offset:7680\nds_write_addtid_b32 v159 offset:7936\n"
"M 124,180,180\nR 174,185,20480\nG 15\nM 96,160,192\nR 176,186,8192\nM 96,164,208\n"
"R 178,186,12288\nG 3\ns_barrier\nM 112,160,196\nR 180,186,16384\nM 112,164,212\n"
"R 182,186,20480\nG 4\nM 100,168,192\nR 160,187,8192\nM 100,172,208\nR 162,187,12288\n"
"M 116,168,196\nR 164,187,16384\nM 116,172,212\nR 166,187,20480\nG 4\nM 104,176,192\n"
"ds_read2st64_b32 v[128:129], v225 offset0:0 offset1:1\nds_read2st64_b32 v[130:131], v225 offset0:2 offset1:3\nM 104,180,208\nds_read2st64_b32 v[132:133], v225 offset0:4 offset1:5\nds_read2st64_b32 v[134:135], v225 offset0:6 offset1:7\nM 120,176,196\n"
"ds_read2st64_b32 v[136:137], v225 offset0:8 offset1:9\nds_read2st64_b32 v[138:139], v225 offset0:10 offset1:11\nM 120,180,212\nds_read2st64_b32 v[140:141], v225 offset0:12 offset1:13\nds_read2st64_b32 v[142:143], v225 offset0:14 offset1:15\nG 8\n"
"M 108,160,192\nds_read2st64_b32 v[144:145], v225 offset0:16 offset1:17\nds_read2st64_b32 v[146:147], v225 offset0:18 offset1:19\nM 108,164,208\nds_read2st64_b32 v[148:149], v225 offset0:20 offset1:21\nds_read2st64_b32 v[150:151], v225 offset0:22 offset1:23\n"
"M 124,160,196\nds_read2st64_b32 v[152:153], v225 offset0:24 offset1:25\nds_read2st64_b32 v[154:155], v225 offset0:26 offset1:27\nM 124,164,212\nds_read2st64_b32 v[156:157], v225 offset0:28 offset1:29\nds_read2st64_b32 v[158:159], v225 offset0:30 offset1:31\n"
"s_waitcnt vmcnt(11)\nv_mfma_f32_16x16x32_bf16 v[96:99], v[208:211], v[0:3], v[96:99]\nv_mfma_f32_16x16x32_bf16 v[100:103], v[210:213], v[0:3], v[100:103]\ns_waitcnt vmcnt(10)\nv_mfma_f32_16x16x32_bf16 v[104:107], v[208:211], v[4:7], v[104:107]\nv_mfma_f32_16x16x32_bf16 v[108:111], v[210:213], v[4:7], v[108:111]\n"
"s_waitcnt vmcnt(9)\nv_mfma_f32_16x16x32_bf16 v[112:115], v[208:211], v[8:11], v[112:115]\nv_mfma_f32_16x16x32_bf16 v[116:119], v[210:213], v[8:11], v[116:119]\ns_waitcnt vmcnt(8)\nv_mfma_f32_16x16x32_bf16 v[120:123], v[208:211], v[12:15], v[120:123]\nv_mfma_f32_16x16x32_bf16 v[124:127], v[210:213], v[12:15], v[124:127]\n"
"s_waitcnt vmcnt(7)\nv_mfma_f32_16x16x32_bf16 v[96:99], v[208:211], v[16:19], v[96:99]\nv_mfma_f32_16x16x32_bf16 v[100:103], v[210:213], v[16:19], v[100:103]\ns_waitcnt vmcnt(6)\nv_mfma_f32_16x16x32_bf16 v[104:107], v[208:211], v[20:23], v[104:107]\nv_mfma_f32_16x16x32_bf16 v[108:111], v[210:213], v[20:23], v[108:111]\n"
"s_waitcnt vmcnt(5)\nv_mfma_f32_16x16x32_bf16 v[112:115], v[208:211], v[24:27], v[112:115]\nv_mfma_f32_16x16x32_bf16 v[116:119], v[210:213], v[24:27], v[116:119]\ns_waitcnt vmcnt(4)\nv_mfma_f32_16x16x32_bf16 v[120:123], v[208:211], v[28:31], v[120:123]\nv_mfma_f32_16x16x32_bf16 v[124:127], v[210:213], v[28:31], v[124:127]\n"
"s_waitcnt vmcnt(1)\nv_max3_u32 v248, v67, v71, v75\ns_waitcnt vmcnt(0)\nv_max_u32 v248, v248, v79\nv_cmp_eq_u32 vcc, -1, v248\ns_nop 1\n"
"s_cbranch_vccnz Lq987\nLq988:\nv_mfma_f32_16x16x32_bf16 v[96:99], v[208:211], v[64:67], v[96:99]\nv_mfma_f32_16x16x32_bf16 v[100:103], v[210:213], v[64:67], v[100:103]\nv_mfma_f32_16x16x32_bf16 v[104:107], v[208:211], v[68:71], v[104:107]\nv_mfma_f32_16x16x32_bf16 v[108:111], v[210:213], v[68:71], v[108:111]\n"
"v_mfma_f32_16x16x32_bf16 v[112:115], v[208:211], v[72:75], v[112:115]\nv_mfma_f32_16x16x32_bf16 v[116:119], v[210:213], v[72:75], v[116:119]\nv_mfma_f32_16x16x32_bf16 v[120:123], v[208:211], v[76:79], v[120:123]\nv_mfma_f32_16x16x32_bf16 v[124:127], v[210:213], v[76:79], v[124:127]\nG 15\nv_pk_add_f32 v[96:97], v[96:97], v[128:129]\n"
"G 14\nv_pk_add_f32 v[98:99], v[98:99], v[130:131]\nG 13\nv_pk_add_f32 v[100:101], v[100:101], v[132:133]\nG 12\nv_pk_add_f32 v[102:103], v[102:103], v[134:135]\n"
"v_cvt_pk_bf16_f32 v192, v96, v97\nv_cvt_pk_bf16_f32 v193, v98, v99\nv_cvt_pk_bf16_f32 v194, v100, v101\nv_cvt_pk_bf16_f32 v195, v102, v103\nG 11\nv_pk_add_f32 v[104:105], v[104:105], v[136:137]\n"
"G 10\nv_pk_add_f32 v[106:107], v[106:107], v[138:139]\nG 9\nv_pk_add_f32 v[108:109], v[108:109], v[140:141]\nG 8\nv_pk_add_f32 v[110:111], v[110:111], v[142:143]\n"
"v_cvt_pk_bf16_f32 v196, v104, v105\nv_cvt_pk_bf16_f32 v197, v106, v107\nv_cvt_pk_bf16_f32 v198, v108, v109\nv_cvt_pk_bf16_f32 v199, v110, v111\nG 7\nv_pk_add_f32 v[112:113], v[112:113], v[144:145]\n"
"G 6\nv_pk_add_f32 v[114:115], v[114:115], v[146:147]\nG 5\nv_pk_add_f32 v[116:117], v[116:117], v[148:149]\nG 4\nv_pk_add_f32 v[118:119], v[118:119], v[150:151]\n"
"v_cvt_pk_bf16_f32 v200, v112, v113\nv_cvt_pk_bf16_f32 v201, v114, v115\nv_cvt_pk_bf16_f32 v202, v116, v117\nv_cvt_pk_bf16_f32 v203, v118, v119\nG 3\nv_pk_add_f32 v[120:121], v[120:121], v[152:153]\n"
"G 2\nv_pk_add_f32 v[122:123], v[122:123], v[154:155]\nG 1\nv_pk_add_f32 v[124:125], v[124:125], v[156:157]\nG 0\nv_pk_add_f32 v[126:127], v[126:127], v[158:159]\n"
"v_cvt_pk_bf16_f32 v204, v120, v121\nv_cvt_pk_bf16_f32 v205, v122, v123\nv_cvt_pk_bf16_f32 v206, v124, v125\nv_cvt_pk_bf16_f32 v207, v126, v127\nv_and_b32 v230, 0xf0, v252\nv_lshlrev_b32 v230, 7, v230\n"
"v_bfe_u32 v231, v252, 8, 1\nv_lshl_add_u32 v230, v231, 5, v230\nv_lshrrev_b32 v231, 9, v252\nv_lshl_add_u32 v230, v231, 4, v230\ns_add_u32 s61, s49, s53\ns_add_u32 s62, s61, 32768\n"
"v_permlane16_swap_b32 v192, v194\nv_permlane16_swap_b32 v193, v195\nbuffer_store_dwordx4 v[192:195], v230, s[28:31], s61 offen offset:0\nv_permlane16_swap_b32 v196, v198\nv_permlane16_swap_b32 v197, v199\nbuffer_store_dwordx4 v[196:199], v230, s[28:31], s61 offen offset:64\n"
"v_permlane16_swap_b32 v200, v202\nv_permlane16_swap_b32 v201, v203\nbuffer_store_dwordx4 v[200:203], v230, s[28:31], s62 offen offset:0\nv_permlane16_swap_b32 v204, v206\nv_permlane16_swap_b32 v205, v207\nbuffer_store_dwordx4 v[204:207], v230, s[28:31], s62 offen offset:64\n"
"s_add_u32 s60, s83, 1\nv_mov_b32 v234, s60\ns_or_b32 s60, s45, s40\ns_cmp_eq_u32 s60, 0\ns_cselect_b64 exec, 1, 0\nbuffer_store_dword v234, off, s[20:23], 0 sc1\n"
"s_mov_b64 exec, -1\ns_endpgm\nLqF:\nLq588:\nv_mov_b32 v248, 0\nv_mov_b32 v249, 0\n"
"s_add_u32 s61, s80, 88358912\ns_mov_b64 exec, 0xffff\nbuffer_atomic_or_x2 v[248:249], v250, s[20:23], s61 offen sc0 sc1\ns_mov_b64 exec, -1\ns_waitcnt vmcnt(0)\nv_cmp_eq_u64 s[86:87], s[46:47], v[248:249]\n"
"s_nop 1\ns_and_b32 s62, s86, 0xffff\ns_cmp_eq_u32 s62, 0xffff\ns_cbranch_scc1 Lq589\ns_sleep 1\ns_sub_u32 s48, s48, 1\n"
"s_cmp_gt_i32 s48, 0\ns_cbranch_scc1 Lq588\ns_branch Lq589\nLq650:\ns_lshr_b32 s84, s49, 3\ns_add_u32 s84, s84, 82837504\n"
"buffer_load_dwordx4 v[128:131], v252, s[20:23], s84 offen offset:0 sc1\nbuffer_load_dwordx4 v[132:135], v252, s[20:23], s84 offen offset:1024 sc1\nbuffer_load_dwordx4 v[136:139], v252, s[20:23], s84 offen offset:2048 sc1\nbuffer_load_dwordx4 v[140:143], v252, s[20:23], s84 offen offset:3072 sc1\ns_lshr_b32 s85, s49, 3\ns_add_u32 s85, s85, 82841600\n"
"buffer_load_dwordx4 v[144:147], v252, s[20:23], s85 offen offset:0 sc1\nbuffer_load_dwordx4 v[148:151], v252, s[20:23], s85 offen offset:1024 sc1\nbuffer_load_dwordx4 v[152:155], v252, s[20:23], s85 offen offset:2048 sc1\nbuffer_load_dwordx4 v[156:159], v252, s[20:23], s85 offen offset:3072 sc1\ns_waitcnt vmcnt(6)\nv_min3_u32 v230, v129, v131, v133\n"
"s_waitcnt vmcnt(5)\nv_min3_u32 v230, v230, v135, v137\ns_waitcnt vmcnt(4)\nv_min3_u32 v230, v230, v139, v141\ns_waitcnt vmcnt(3)\nv_min3_u32 v230, v230, v143, v145\n"
"s_waitcnt vmcnt(2)\nv_min3_u32 v230, v230, v147, v149\ns_waitcnt vmcnt(1)\nv_min3_u32 v230, v230, v151, v153\ns_waitcnt vmcnt(0)\nv_min3_u32 v230, v230, v155, v157\n"
"v_min3_u32 v230, v230, v159, v159\nv_max3_u32 v231, v129, v131, v133\nv_max3_u32 v231, v231, v135, v137\nv_max3_u32 v231, v231, v139, v141\nv_max3_u32 v231, v231, v143, v145\nv_max3_u32 v231, v231, v147, v149\n"
"v_max3_u32 v231, v231, v151, v153\nv_max3_u32 v231, v231, v155, v157\nv_max3_u32 v231, v231, v159, v159\nv_cmp_ne_u32 s[86:87], s46, v230\nv_cmp_ne_u32 s[88:89], s46, v231\ns_nop 0\n"
"s_or_b64 s[86:87], s[86:87], s[88:89]\ns_cbranch_scc0 Lq651\ns_sub_u32 s48, s48, 1\ns_cmp_gt_i32 s48, 0\ns_cbranch_scc1 Lq650\ns_branch Lq651\n"
"Lq674:\ns_mov_b64 exec, s[86:87]\nbuffer_load_dwordx4 v[230:233], v252, s[20:23], s65 offen offset:0 sc1\nbuffer_load_dwordx4 v[234:237], v252, s[20:23], s65 offen offset:1024 sc1\nbuffer_load_dwordx2 v[64:65], v227, s[20:23], s65 offen offset:2048 sc1\nbuffer_load_dwordx2 v[70:71], v227, s[20:23], s65 offen offset:2560 sc1\n"
"buffer_load_dwordx2 v[76:77], v227, s[20:23], s65 offen offset:3072 sc1\nbuffer_load_dwordx2 v[82:83], v227, s[20:23], s65 offen offset:3584 sc1\ns_mov_b64 exec, -1\ns_waitcnt vmcnt(0)\nv_max3_u32 v248, v233, v237, v65\nv_max3_u32 v248, v248, v71, v77\n"
"v_max_u32 v248, v248, v83\nv_cmp_eq_u32 vcc, -1, v248\ns_nop 1\ns_cbranch_vccz Lq675\ns_sub_u32 s48, s48, 1\ns_cmp_gt_i32 s48, 0\n"
"s_cbranch_scc1 Lq674\ns_branch Lq675\nLq695:\nv_mov_b32 v248, 0\nv_mov_b32 v249, 0\ns_add_u32 s61, s80, 88498176\n"
"s_mov_b64 exec, 0xffff\nbuffer_atomic_or_x2 v[248:249], v250, s[20:23], s61 offen sc0 sc1\ns_mov_b64 exec, -1\ns_waitcnt vmcnt(0)\nv_cmp_eq_u64 s[86:87], s[46:47], v[248:249]\ns_nop 1\n"
"s_and_b32 s62, s86, 0xffff\ns_cmp_eq_u32 s62, 0xffff\ns_cbranch_scc1 Lq696\ns_sleep 1\ns_sub_u32 s48, s48, 1\ns_cmp_gt_i32 s48, 0\n"
"s_cbranch_scc1 Lq695\ns_branch Lq696\nLq754:\nv_mov_b32 v248, 0\nv_mov_b32 v249, 0\ns_add_u32 s61, s80, 88637440\n"
"s_mov_b64 exec, 0xffff\nbuffer_atomic_or_x2 v[248:249], v250, s[20:23], s61 offen sc0 sc1\ns_mov_b64 exec, -1\ns_waitcnt vmcnt(0)\nv_cmp_eq_u64 s[86:87], s[46:47], v[248:249]\ns_nop 1\n"
"s_and_b32 s62, s86, 0xffff\ns_cmp_eq_u32 s62, 0xffff\ns_cbranch_scc1 Lq755\ns_sleep 1\ns_sub_u32 s48, s48, 1\ns_cmp_gt_i32 s48, 0\n"
"s_cbranch_scc1 Lq754\ns_branch Lq755\nLq811:\ns_lshr_b32 s84, s49, 3\ns_add_u32 s84, s84, 83361792\nbuffer_load_dwordx4 v[0:3], v252, s[20:23], s84 offen offset:0 sc1\n"
"buffer_load_dwordx4 v[4:7], v252, s[20:23], s84 offen offset:1024 sc1\nbuffer_load_dwordx4 v[8:11], v252, s[20:23], s84 offen offset:2048 sc1\nbuffer_load_dwordx4 v[12:15], v252, s[20:23], s84 offen offset:3072 sc1\ns_lshr_b32 s85, s49, 3\ns_add_u32 s85, s85, 83365888\nbuffer_load_dwordx4 v[16:19], v252, s[20:23], s85 offen offset:0 sc1\n"
"buffer_load_dwordx4 v[20:23], v252, s[20:23], s85 offen offset:1024 sc1\nbuffer_load_dwordx4 v[24:27], v252, s[20:23], s85 offen offset:2048 sc1\nbuffer_load_dwordx4 v[28:31], v252, s[20:23], s85 offen offset:3072 sc1\ns_waitcnt vmcnt(6)\nv_min3_u32 v230, v1, v3, v5\ns_waitcnt vmcnt(5)\n"
"v_min3_u32 v230, v230, v7, v9\ns_waitcnt vmcnt(4)\nv_min3_u32 v230, v230, v11, v13\ns_waitcnt vmcnt(3)\nv_min3_u32 v230, v230, v15, v17\ns_waitcnt vmcnt(2)\n"
"v_min3_u32 v230, v230, v19, v21\ns_waitcnt vmcnt(1)\nv_min3_u32 v230, v230, v23, v25\ns_waitcnt vmcnt(0)\nv_min3_u32 v230, v230, v27, v29\nv_min3_u32 v230, v230, v31, v31\n"
"v_max3_u32 v231, v1, v3, v5\nv_max3_u32 v231, v231, v7, v9\nv_max3_u32 v231, v231, v11, v13\nv_max3_u32 v231, v231, v15, v17\nv_max3_u32 v231, v231, v19, v21\nv_max3_u32 v231, v231, v23, v25\n"
"v_max3_u32 v231, v231, v27, v29\nv_max3_u32 v231, v231, v31, v31\nv_cmp_ne_u32 s[86:87], s46, v230\nv_cmp_ne_u32 s[88:89], s46, v231\ns_nop 0\ns_or_b64 s[86:87], s[86:87], s[88:89]\n"
"s_cbranch_scc0 Lq812\ns_sub_u32 s48, s48, 1\ns_cmp_gt_i32 s48, 0\ns_cbranch_scc1 Lq811\ns_branch Lq812\nLq833:\n"
"v_mov_b32 v248, 0\nv_mov_b32 v249, 0\ns_add_u32 s61, s82, 845824\ns_mov_b64 exec, 0xf\nbuffer_atomic_or_x2 v[248:249], v250, s[20:23], s61 offen sc0 sc1\ns_mov_b64 exec, -1\n"
"s_waitcnt vmcnt(0)\nv_cmp_eq_u64 s[86:87], s[46:47], v[248:249]\ns_nop 1\ns_and_b32 s62, s86, 0xf\ns_cmp_eq_u32 s62, 0xf\ns_cbranch_scc1 Lq834\n"
"s_sleep 1\ns_sub_u32 s48, s48, 1\ns_cmp_gt_i32 s48, 0\ns_cbranch_scc1 Lq833\ns_branch Lq834\nLq868:\n"
"v_mov_b32 v248, 0\nv_mov_b32 v249, 0\ns_add_u32 s61, s82, 845832\ns_mov_b64 exec, 0xf\nbuffer_atomic_or_x2 v[248:249], v250, s[20:23], s61 offen sc0 sc1\ns_mov_b64 exec, -1\n"
"s_waitcnt vmcnt(0)\nv_cmp_eq_u64 s[86:87], s[46:47], v[248:249]\ns_nop 1\ns_and_b32 s62, s86, 0xf\ns_cmp_eq_u32 s62, 0xf\ns_cbranch_scc1 Lq869\n"
"s_sleep 1\ns_sub_u32 s48, s48, 1\ns_cmp_gt_i32 s48, 0\ns_cbranch_scc1 Lq868\ns_branch Lq869\nLq903:\n"
"v_mov_b32 v248, 0\nv_mov_b32 v249, 0\ns_add_u32 s61, s82, 845848\ns_mov_b64 exec, 0xf\nbuffer_atomic_or_x2 v[248:249], v250, s[20:23], s61 offen sc0 sc1\ns_mov_b64 exec, -1\n"
"s_waitcnt vmcnt(0)\nv_cmp_eq_u64 s[86:87], s[46:47], v[248:249]\ns_nop 1\ns_and_b32 s62, s86, 0xf\ns_cmp_eq_u32 s62, 0xf\ns_cbranch_scc1 Lq904\n"
"s_sleep 1\ns_sub_u32 s48, s48, 1\ns_cmp_gt_i32 s48, 0\ns_cbranch_scc1 Lq903\ns_branch Lq904\nLq922:\n"
"v_mov_b32 v248, 0\nv_mov_b32 v249, 0\ns_add_u32 s61, s82, 845840\ns_mov_b64 exec, 0xf\nbuffer_atomic_or_x2 v[248:249], v250, s[20:23], s61 offen sc0 sc1\ns_mov_b64 exec, -1\n"
"s_waitcnt vmcnt(0)\nv_cmp_eq_u64 s[86:87], s[46:47], v[248:249]\ns_nop 1\ns_and_b32 s62, s86, 0xf\ns_cmp_eq_u32 s62, 0xf\ns_cbranch_scc1 Lq923\n"
"s_sleep 1\ns_sub_u32 s48, s48, 1\ns_cmp_gt_i32 s48, 0\ns_cbranch_scc1 Lq922\ns_branch Lq923\nLq964:\n"
"v_mov_b32 v248, 0\nv_mov_b32 v249, 0\ns_add_u32 s61, s74, 1009664\ns_mov_b64 exec, 3\nbuffer_atomic_or_x2 v[248:249], v250, s[20:23], s61 offen sc0 sc1\ns_mov_b64 exec, -1\n"
"s_waitcnt vmcnt(0)\nv_cmp_eq_u64 s[86:87], s[46:47], v[248:249]\ns_nop 1\ns_and_b32 s62, s86, 3\ns_cmp_eq_u32 s62, 3\ns_cbranch_scc1 Lq965\n"
"s_sleep 1\ns_sub_u32 s48, s48, 1\ns_cmp_gt_i32 s48, 0\ns_cbranch_scc1 Lq964\ns_branch Lq965\nLq987:\n"
"s_mul_i32 s66, s43, 49152\ns_add_u32 s66, s66, s72\ns_add_u32 s66, s66, 65044480\nbuffer_load_dwordx4 v[64:67], v252, s[20:23], s66 offen offset:0 sc1\nbuffer_load_dwordx4 v[68:71], v252, s[20:23], s66 offen offset:1024 sc1\nbuffer_load_dwordx4 v[72:75], v252, s[20:23], s66 offen offset:2048 sc1\n"
"buffer_load_dwordx4 v[76:79], v252, s[20:23], s66 offen offset:3072 sc1\ns_waitcnt vmcnt(0)\nv_max3_u32 v248, v67, v71, v75\nv_max_u32 v248, v248, v79\nv_cmp_eq_u32 vcc, -1, v248\ns_nop 1\n"
"s_cbranch_vccz Lq988\ns_sub_u32 s48, s48, 1\ns_cmp_gt_i32 s48, 0\ns_cbranch_scc1 Lq987\ns_branch Lq988\nLqE:\n"
  : "+{v0}"(tid), "+{s[4:5]}"(wqkv), "+{s[8:9]}"(wo), "+{s[12:13]}"(w1), "+{s[16:17]}"(w2), "+{s[20:21]}"(ws), "+{s[24:25]}"(x), "+{s[28:29]}"(out), "+{s[32:33]}"(b1), "+{s[36:37]}"(b2), "+{s45}"(bid) :
  : "a0", "a1", "a2", "a3", "a4", "a5", "a6", "a7", "a8", "a9", "a10", "a11", "a12", "a13", "a14", "a15", "a16", "a17", "a18", "a19", "a20", "a21", "a22", "a23", "a24", "a25", "a26", "a27", "a28", "a29", "a30", "a31", "a32", "a33", "a34", "a35", "a36", "a37", "a38", "a39", "a40", "a41", "a42", "a43", "a44", "a45", "a46", "a47", "a48", "a49", "a50", "a51", "a52", "a53", "a54", "a55", "a56", "a57", "a58", "a59", "a60", "a61", "a62", "a63", "a64", "a65", "a66", "a67", "a68", "a69", "a70", "a71", "a72", "a73", "a74", "a75", "a76", "a77", "a78", "a79", "a80", "a81", "a82", "a83", "a84", "a85", "a86", "a87", "a88", "a89", "a90", "a91", "a92", "a93", "a94", "a95", "a96", "a97", "a98", "a99", "a100", "a101", "a102", "a103", "a104", "a105", "a106", "a107", "a108", "a109", "a110", "a111", "a112", "a113", "a114", "a115", "a116", "a117", "a118", "a119", "a120", "a121", "a122", "a123", "a124", "a125", "a126", "a127", "a128", "a129", "a130", "a131", "a132", "a133", "a134", "a135", "a136", "a137", "a138", "a139", "a140", "a141", "a142", "a143", "a144", "a145", "a146", "a147", "a148", "a149", "a150", "a151", "a152", "a153", "a154", "a155", "a156", "a157", "a158", "a159", "a160", "a161", "a162", "a163", "a164", "a165", "a166", "a167", "a168", "a169", "a170", "a171", "a172", "a173", "a174", "a175", "a176", "a177", "a178", "a179", "a180", "a181", "a182", "a183", "a184", "a185", "a186", "a187", "a188", "a189", "a190", "a191", "a192", "a193", "a194", "a195", "a196", "a197", "a198", "a199", "a200", "a201", "a202", "a203", "a204", "a205", "a206", "a207", "a208", "a209", "a210", "a211", "a212", "a213", "a214", "a215", "a216", "a217", "a218", "a219", "a220", "a221", "a222", "a223", "a224", "a225", "a226", "a227", "a228", "a229", "a230", "a231", "a232", "a233", "a234", "a235", "a236", "a237", "a238", "a239", "a240", "a241", "a242", "a243", "a244", "a245", "a246", "a247", "a248", "a249", "a250", "a251", "a252", "a253", "a254", "a255", "v1", "v2", "v3", "v4", "v5", "v6", "v7", "v8", "v9", "v10", "v11", "v12", "v13", "v14", "v15", "v16", "v17", "v18", "v19", "v20", "v21", "v22", "v23", "v24", "v25", "v26", "v27", "v28", "v29", "v30", "v31", "v32", "v33", "v34", "v35", "v36", "v37", "v38", "v39", "v40", "v41", "v42", "v43", "v44", "v45", "v46", "v47", "v48", "v49", "v50", "v51", "v52", "v53", "v54", "v55", "v56", "v57", "v58", "v59", "v60", "v61", "v62", "v63", "v64", "v65", "v66", "v67", "v68", "v69", "v70", "v71", "v72", "v73", "v74", "v75", "v76", "v77", "v78", "v79", "v80", "v81", "v82", "v83", "v84", "v85", "v86", "v87", "v88", "v89", "v90", "v91", "v92", "v93", "v94", "v95", "v96", "v97", "v98", "v99", "v100", "v101", "v102", "v103", "v104", "v105", "v106", "v107", "v108", "v109", "v110", "v111", "v112", "v113", "v114", "v115", "v116", "v117", "v118", "v119", "v120", "v121", "v122", "v123", "v124", "v125", "v126", "v127", "v128", "v129", "v130", "v131", "v132", "v133", "v134", "v135", "v136", "v137", "v138", "v139", "v140", "v141", "v142", "v143", "v144", "v145", "v146", "v147", "v148", "v149", "v150", "v151", "v152", "v153", "v154", "v155", "v156", "v157", "v158", "v159", "v160", "v161", "v162", "v163", "v164", "v165", "v166", "v167", "v168", "v169", "v170", "v171", "v172", "v173", "v174", "v175", "v176", "v177", "v178", "v179", "v180", "v181", "v182", "v183", "v184", "v185", "v186", "v187", "v188", "v189", "v190", "v191", "v192", "v193", "v194", "v195", "v196", "v197", "v198", "v199", "v200", "v201", "v202", "v203", "v204", "v205", "v206", "v207", "v208", "v209", "v210", "v211", "v212", "v213", "v214", "v215", "v216", "v217", "v218", "v219", "v220", "v221", "v222", "v223", "v224", "v225", "v226", "v227", "v228", "v229", "v230", "v231", "v232", "v233", "v234", "v235", "v236", "v237", "v238", "v239", "v240", "v241", "v242", "v243", "v244", "v245", "v246", "v247", "v248", "v249", "v250", "v251", "v252", "v253", "v254", "v255", "s6", "s7", "s10", "s11", "s14", "s15", "s18", "s19", "s22", "s23", "s26", "s27", "s30", "s31", "s34", "s35", "s38", "s39", "s40", "s41", "s42", "s43", "s44", "s46", "s47", "s48", "s49", "s50", "s51", "s52", "s53", "s54", "s55", "s56", "s57", "s58", "s59", "s60", "s61", "s62", "s63", "s64", "s65", "s66", "s67", "s68", "s69", "s70", "s71", "s72", "s73", "s74", "s75", "s76", "s77", "s78", "s79", "s80", "s81", "s82", "s83", "s84", "s85", "s86", "s87", "s88", "s89", "s90", "s91", "s92", "s93", "s94", "s95", "s96", "s97", "s98", "s99", "s100", "s101", "vcc", "m0", "scc", "memory");
#endif
}
extern "C" void kernel_launch(void* const* d_in, const int* in_sizes, int n_in, void* d_out, int out_size, void* d_ws, size_t ws_size, hipStream_t stream) {
  (void)hipFuncSetAttribute((const void*)layers, hipFuncAttributeMaxDynamicSharedMemorySize, 160 * 1024);
  hipLaunchKernelGGL(layers, dim3(256), dim3(256), 160 * 1024, stream, d_in[0], d_in[1], d_in[2], d_in[4], d_in[5], d_in[6], d_in[7], d_out, d_ws);
}
```

```cpp
#include <hip/hip_runtime.h>
__global__ __launch_bounds__(256, 1) void layers(const void* x, const void* wqkv, const void* wo, const void* w1, const void* b1, const void* w2, const void* b2, void* out, void* ws) {
#if defined(__HIP_DEVICE_COMPILE__)
  int tid = threadIdx.x, bid = blockIdx.x;
  asm volatile(
".macro M d,w,a\nv_mfma_f32_16x16x32_bf16 v[\\d:\\d+3], v[\\w:\\w+3], a[\\a:\\a+3], v[\\d:\\d+3]\n.endm\n.macro N d,a,w\nv_mfma_f32_16x16x32_bf16 v[\\d:\\d+3], a[\\a:\\a+3], v[\\w:\\w+3], v[\\d:\\d+3]\n.endm\n"
".macro R d,b,o\nds_read_b64_tr_b16 v[\\d:\\d+1], v\\b offset:\\o\n.endm\n.macro G n\ns_waitcnt lgkmcnt(\\n)\n.endm\n"
"v_lshrrev_b32 v1, 6, v0\nv_and_b32 v0, 63, v0\nv_readfirstlane_b32 s40, v1\nv_lshlrev_b32 v252, 4, v0\ns_mov_b32 s6, 6291456\ns_mov_b32 s7, 0x20000\n"
"s_mov_b32 s10, 2097152\ns_mov_b32 s11, 0x20000\ns_mov_b32 s14, 7340032\ns_mov_b32 s15, 0x20000\ns_mov_b32 s18, 7340032\ns_mov_b32 s19, 0x20000\n"
"s_mov_b32 s22, 89128960\ns_mov_b32 s23, 0x20000\ns_mov_b32 s26, 4194304\ns_mov_b32 s27, 0x20000\ns_mov_b32 s30, 4194304\ns_mov_b32 s31, 0x20000\n"
"s_mov_b32 s34, 7168\ns_mov_b32 s35, 0x20000\ns_mov_b32 s38, 2048\ns_mov_b32 s39, 0x20000\ns_bfe_u32 s41, s45, 0x30003\ns_and_b32 s60, s45, 1\n"
"s_lshl3_add_u32 s41, s60, s41\ns_bfe_u32 s43, s45, 0x20001\ns_lshr_b32 s44, s45, 6\ns_lshl2_add_u32 s42, s43, s44\ns_lshl2_add_u32 s75, s44, s43\ns_and_b32 s90, s40, 1\n"
"s_lshl2_add_u32 s60, s41, s40\ns_lshl_b32 s49, s60, 16\ns_lshl_b32 s50, s40, 11\ns_add_u32 s50, s50, 32768\ns_lshl_b32 s52, s42, 7\ns_lshl_b32 s51, s40, 13\n"
"s_lshl_b32 s60, s90, 10\ns_add_u32 s70, s49, s60\ns_xor_b32 s71, s70, 0x10000\ns_mul_i32 s101, s41, 196608\ns_add_u32 s101, s101, s52\ns_lshl_b32 s53, s75, 7\n"
"s_getpc_b64 s[98:99]\nLqPC:\nLqq0:\nv_lshrrev_b32 v96, 7, v252\nv_lshlrev_b32 v96, 11, v96\nv_lshrrev_b32 v97, 8, v252\n"
"v_lshlrev_b32 v97, 4, v97\nv_and_b32 v98, 0x70, v252\nv_xor_b32 v98, v98, v97\nv_add_u32 v226, v96, v98\nv_xor_b32 v227, 64, v226\ns_add_u32 m0, s51, 0\n"
"s_add_u32 s60, s70, 0\nbuffer_load_dwordx4 v226, s[24:27], s60 offen lds\ns_add_u32 m0, s51, 1024\ns_add_u32 s60, s70, 16384\nbuffer_load_dwordx4 v227, s[24:27], s60 offen lds\ns_add_u32 m0, s51, 2048\n"
"s_add_u32 s60, s70, 32768\nbuffer_load_dwordx4 v226, s[24:27], s60 offen lds\ns_add_u32 m0, s51, 3072\ns_add_u32 s60, s70, 49152\nbuffer_load_dwordx4 v227, s[24:27], s60 offen lds\ns_add_u32 m0, s51, 4096\n"
"s_add_u32 s60, s71, 0\nbuffer_load_dwordx4 v226, s[24:27], s60 offen lds\ns_add_u32 m0, s51, 5120\ns_add_u32 s60, s71, 16384\nbuffer_load_dwordx4 v227, s[24:27], s60 offen lds\ns_add_u32 m0, s51, 6144\n"
"s_add_u32 s60, s71, 32768\nbuffer_load_dwordx4 v226, s[24:27], s60 offen lds\ns_add_u32 m0, s51, 7168\ns_add_u32 s60, s71, 49152\nbuffer_load_dwordx4 v227, s[24:27], s60 offen lds\nbuffer_load_dwordx2 v[230:231], off, s[20:23], 0 sc1\n"
"s_lshr_b32 s60, s45, 3\ns_lshl2_add_u32 s60, s60, s40\ns_lshl_b32 s60, s60, 8\nv_lshl_add_u32 v224, v252, 3, s60\nv_add_u32 v224, LqPC-LqPC, v224\ns_mov_b64 exec, 3\n"
"global_load_dword v224, v224, s[98:99]\ns_mov_b64 exec, -1\nv_bfe_u32 v96, v252, 5, 2\nv_bfe_u32 v97, v252, 8, 2\nv_xor_b32 v96, v96, v97\ns_and_b32 s60, s40, 1\n"
"s_lshl_b32 s60, s60, 4\ns_and_b32 s61, s40, 2\ns_lshl1_add_u32 s60, s61, s60\nv_bfe_u32 v97, v252, 7, 2\nv_lshrrev_b32 v98, 9, v252\nv_lshl_add_u32 v97, v98, 3, v97\n"
"v_add_u32 v97, s60, v97\nv_mul_u32_u24 v97, 6144, v97\nv_lshl_add_u32 v97, v96, 5, v97\nv_and_b32 v96, 16, v252\nv_add_u32 v220, v96, v97\nv_add_u32 v221, 3144704, v220\n"
"s_add_u32 m0, s50, 0\ns_add_u32 s63, s52, 2048\nbuffer_load_dwordx4 v220, s[4:7], s63 offen lds\nbuffer_load_dwordx4 v221, s[4:7], s63 offen offset:1024 lds\ns_add_u32 m0, s50, 8192\ns_add_u32 s63, s52, 198656\n"
"buffer_load_dwordx4 v220, s[4:7], s63 offen lds\nbuffer_load_dwordx4 v221, s[4:7], s63 offen offset:1024 lds\ns_lshl2_add_u32 s60, s41, s40\ns_mul_i32 s79, s60, 2176\ns_and_b32 s61, s60, -2\ns_mul_i32 s80, s61, 2176\n"
"s_lshl2_add_u32 s61, s61, s43\ns_mul_i32 s82, s61, 640\ns_mul_i32 s61, s43, 28672\ns_mul_i32 s73, s60, 229376\ns_add_u32 s73, s73, s61\ns_mul_i32 s62, s90, 14336\n"
"s_add_u32 s68, s73, s62\ns_add_u32 s61, s61, s62\ns_xor_b32 s62, s60, 1\ns_mul_i32 s69, s62, 229376\ns_add_u32 s69, s69, s61\ns_mul_i32 s61, s44, 7168\n"
"s_add_u32 s73, s73, s61\ns_lshl2_add_u32 s61, s60, s43\ns_mul_i32 s81, s61, 640\ns_lshl2_add_u32 s61, s60, s44\ns_mul_i32 s74, s61, 640\ns_lshl2_add_u32 s61, s41, s44\n"
"s_mul_i32 s61, s61, 48\ns_add_u32 s61, s61, s40\ns_lshl_b32 s72, s61, 12\ns_mul_i32 s55, s42, 448\ns_add_u32 s54, s55, 64\ns_and_b32 s54, s54, 0xffffff80\n"
"s_bitcmp0_b32 s42, 0\ns_cselect_b32 s61, 384, 0\ns_add_u32 s55, s55, s61\ns_lshl1_add_u32 s61, s41, s90\ns_lshl_b32 s61, s61, 9\ns_lshl_b32 s91, s42, 14\n"
"s_add_u32 s91, s91, s61\ns_add_u32 s91, s91, 83886080\ns_mul_i32 s61, s43, 1835008\ns_lshl_b32 s62, s44, 9\ns_add_u32 s61, s61, s62\ns_add_u32 s62, s43, 1\n"
"s_and_b32 s62, s62, 3\ns_lshl_b32 s62, s62, 7\ns_add_u32 s56, s61, s62\ns_add_u32 s62, s43, 2\ns_and_b32 s62, s62, 3\ns_lshl_b32 s62, s62, 7\n"
"s_add_u32 s57, s61, s62\ns_add_u32 s62, s43, 3\ns_and_b32 s62, s62, 3\ns_lshl_b32 s62, s62, 7\ns_add_u32 s58, s61, s62\ns_add_u32 s62, s43, 4\n"
"s_and_b32 s62, s62, 3\ns_lshl_b32 s62, s62, 7\ns_add_u32 s59, s61, s62\ns_mov_b32 s76, 0x3f803f80\ns_mov_b32 s77, 0xffff0000\ns_mov_b32 s48, 131072\n"
"s_cmp_eq_u32 s60, 0\ns_cselect_b32 s78, 0xf149f2ca, 0\nv_bfe_u32 v96, v252, 6, 3\nv_lshlrev_b32 v96, 7, v96\nv_lshrrev_b32 v97, 9, v252\nv_lshl_add_u32 v96, v97, 11, v96\n"
"v_bfe_u32 v97, v252, 4, 2\nv_lshl_add_u32 v96, v97, 3, v96\ns_lshl_b32 s60, s90, 10\ns_add_u32 s60, s60, 32768\nv_add_u32 v96, s60, v96\nv_bfe_u32 v97, v252, 7, 2\n"
"v_xor_b32 v98, 0, v97\nv_lshl_add_u32 v184, v98, 5, v96\nv_add_u32 v188, 0x10000, v184\nv_xor_b32 v98, 1, v97\nv_lshl_add_u32 v185, v98, 5, v96\nv_add_u32 v189, 0x10000, v185\n"
"v_xor_b32 v98, 2, v97\nv_lshl_add_u32 v186, v98, 5, v96\nv_add_u32 v190, 0x10000, v186\nv_xor_b32 v98, 3, v97\nv_lshl_add_u32 v187, v98, 5, v96\nv_add_u32 v191, 0x10000, v187\n"
"v_lshrrev_b32 v96, 2, v252\ns_xor_b32 s60, s51, 0x2000\nv_add_u32 v225, s60, v96\nv_mov_b32 v210, 0\nv_mov_b32 v211, 0\nv_bfe_u32 v96, v252, 5, 2\n"
"v_bfe_u32 v97, v252, 8, 2\nv_xor_b32 v96, v96, v97\nv_lshrrev_b32 v97, 7, v252\ns_lshl_b32 s60, s40, 3\nv_add_u32 v97, s60, v97\nv_mov_b32 v228, v97\n"
"v_and_b32 v97, 16, v252\nv_lshl_add_u32 v229, v96, 5, v97\nv_bfe_u32 v96, v252, 5, 2\nv_bfe_u32 v97, v252, 8, 2\nv_xor_b32 v96, v96, v97\nv_lshrrev_b32 v97, 7, v252\n"
"s_lshl_b32 s60, s40, 3\nv_add_u32 v97, s60, v97\nv_lshrrev_b32 v98, 1, v96\nv_lshl_add_u32 v97, v98, 5, v97\nv_and_b32 v96, 1, v96\nv_mul_u32_u24 v97, 7168, v97\n"
"v_lshl_add_u32 v97, v96, 5, v97\nv_and_b32 v96, 16, v252\nv_add_u32 v222, v96, v97\nv_add_u32 v223, 3668992, v222\nv_bfe_u32 v96, v252, 4, 4\nv_lshrrev_b32 v97, 8, v252\n"
"v_lshrrev_b32 v98, 1, v96\nv_xor_b32 v97, v97, v98\nv_lshlrev_b32 v96, 7, v96\nv_lshl_add_u32 v96, v97, 4, v96\nv_add_u32 v248, s51, v96\nv_xor_b32 v249, 64, v248\n"
"v_add_u32 v250, 122880, v248\nv_add_u32 v251, 122880, v249\ns_add_u32 m0, s51, 122880\ns_add_u32 s60, s70, 128\nbuffer_load_dwordx4 v226, s[24:27], s60 offen lds\ns_add_u32 m0, s51, 123904\n"
"s_add_u32 s60, s70, 16512\nbuffer_load_dwordx4 v227, s[24:27], s60 offen lds\ns_add_u32 m0, s51, 124928\ns_add_u32 s60, s70, 32896\nbuffer_load_dwordx4 v226, s[24:27], s60 offen lds\ns_add_u32 m0, s51, 125952\n"
"s_add_u32 s60, s70, 49280\nbuffer_load_dwordx4 v227, s[24:27], s60 offen lds\ns_add_u32 m0, s51, 126976\ns_add_u32 s60, s71, 128\nbuffer_load_dwordx4 v226, s[24:27], s60 offen lds\ns_add_u32 m0, s51, 128000\n"
"s_add_u32 s60, s71, 16512\nbuffer_load_dwordx4 v227, s[24:27], s60 offen lds\ns_add_u32 m0, s51, 129024\ns_add_u32 s60, s71, 32896\nbuffer_load_dwordx4 v226, s[24:27], s60 offen lds\ns_add_u32 m0, s51, 130048\n"
"s_add_u32 s60, s71, 49280\nbuffer_load_dwordx4 v227, s[24:27], s60 offen lds\ns_add_u32 m0, s50, 16384\ns_add_u32 s63, s52, 4096\nbuffer_load_dwordx4 v220, s[4:7], s63 offen lds\nbuffer_load_dwordx4 v221, s[4:7], s63 offen offset:1024 lds\n"
"s_add_u32 m0, s50, 24576\ns_add_u32 s63, s52, 200704\nbuffer_load_dwordx4 v220, s[4:7], s63 offen lds\nbuffer_load_dwordx4 v221, s[4:7], s63 offen offset:1024 lds\ns_add_u32 m0, s50, 32768\ns_add_u32 s63, s101, 2048\n"
"buffer_load_dwordx4 v220, s[4:7], s63 offen lds\nbuffer_load_dwordx4 v221, s[4:7], s63 offen offset:1024 lds\ns_add_u32 m0, s50, 40960\ns_add_u32 s63, s101, 4096\nbuffer_load_dwordx4 v220, s[4:7], s63 offen lds\nbuffer_load_dwordx4 v221, s[4:7], s63 offen offset:1024 lds\n"
"v_bfe_u32 v100, v252, 4, 4\nv_lshlrev_b32 v100, 11, v100\nv_lshrrev_b32 v101, 8, v252\nv_lshl_add_u32 v100, v101, 3, v100\ns_add_u32 s60, s49, s53\ns_add_u32 s61, s60, 32768\n"
"buffer_load_dwordx2 v[192:193], v100, s[24:27], s60 offen offset:0\nbuffer_load_dwordx2 v[194:195], v100, s[24:27], s60 offen offset:32\nbuffer_load_dwordx2 v[196:197], v100, s[24:27], s60 offen offset:64\nbuffer_load_dwordx2 v[198:199], v100, s[24:27], s60 offen offset:96\nbuffer_load_dwordx2 v[200:201], v100, s[24:27], s61 offen offset:0\nbuffer_load_dwordx2 v[202:203], v100, s[24:27], s61 offen offset:32\n"
"buffer_load_dwordx2 v[204:205], v100, s[24:27], s61 offen offset:64\nbuffer_load_dwordx2 v[206:207], v100, s[24:27], s61 offen offset:96\nv_mov_b32 v106, 0xf149f149\nv_mov_b32 v104, 0xffff\nv_mov_b32 v105, 0xffff0000\ns_mov_b32 s60, 0x50000\n"
"s_mov_b32 s61, 0x5550055\nv_cndmask_b32 v96, 0, v104, s[60:61]\ns_mov_b32 s60, 0xa0000\ns_mov_b32 s61, 0xaaa00aa\nv_cndmask_b32 v96, v96, v105, s[60:61]\nv_bfi_b32 v100, v96, 0, v106\n"
"s_mov_b32 s60, 0x150001\ns_mov_b32 s61, 0x15550155\nv_cndmask_b32 v97, 0, v104, s[60:61]\ns_mov_b32 s60, 0x2a0002\ns_mov_b32 s61, 0x2aaa02aa\nv_cndmask_b32 v97, v97, v105, s[60:61]\n"
"v_bfi_b32 v101, v97, 0, v106\ns_mov_b32 s60, 0x55505555\ns_mov_b32 s61, 0x50005500\nv_cndmask_b32 v98, 0, v104, s[60:61]\ns_mov_b32 s60, 0xaaa0aaaa\ns_mov_b32 s61, 0xa000aa00\n"
"v_cndmask_b32 v98, v98, v105, s[60:61]\nv_bfi_b32 v102, v98, 0, v106\ns_mov_b32 s60, 0x55405554\ns_mov_b32 s61, 0x40005400\nv_cndmask_b32 v99, 0, v104, s[60:61]\ns_mov_b32 s60, 0xaa80aaa8\n"
"s_mov_b32 s61, 0x8000a800\nv_cndmask_b32 v99, v99, v105, s[60:61]\nv_bfi_b32 v103, v99, 0, v106\nv_add_u32 v104, 161792, v252\nds_write_b128 v104, v[96:99] offset:0\nds_write_b128 v104, v[100:103] offset:1024\n"
"s_waitcnt vmcnt(29)\nv_readfirstlane_b32 s83, v230\nv_readfirstlane_b32 s60, v231\ns_mul_i32 s46, s83, 0x9e3779b1\ns_add_u32 s46, s46, 0x7f4a7c15\ns_xor_b32 s47, s83, s60\n"
"s_mul_i32 s47, s47, 0x85ebca6b\ns_add_u32 s47, s47, 0xc2b2ae35\nv_mov_b32 v144, -1\nv_mov_b32 v145, -1\nv_mov_b32 v146, -1\nv_mov_b32 v147, -1\n"
"ds_read_b128 a[0:3], v248 offset:0\nds_read_b128 a[4:7], v248 offset:2048\nds_read_b128 a[8:11], v248 offset:4096\nds_read_b128 a[12:15], v248 offset:6144\nds_read_b128 a[16:19], v249 offset:0\nds_read_b128 a[20:23], v249 offset:2048\n"
"ds_read_b128 a[24:27], v249 offset:4096\nds_read_b128 a[28:31], v249 offset:6144\ns_waitcnt vmcnt(12) lgkmcnt(8)\ns_barrier\nR 160,184,0\nR 162,184,4096\n"
"R 164,184,8192\nR 166,184,12288\nR 168,185,0\nR 170,185,4096\nR 172,185,8192\nR 174,185,12288\n"
"R 176,186,0\nR 178,186,4096\nR 180,186,8192\nR 182,186,12288\nG 10\nv_mfma_f32_16x16x32_bf16 v[0:3], v[160:163], a[0:3], 0\n"
"s_add_u32 m0, s50, 49152\ns_add_u32 s63, s52, 395264\nbuffer_load_dwordx4 v220, s[4:7], s63 offen lds\nG 8\nM 0,164,16\nbuffer_load_dwordx4 v221, s[4:7], s63 offen offset:1024 lds\n"
"v_mfma_f32_16x16x32_bf16 v[16:19], v[160:163], a[4:7], 0\ns_add_u32 m0, s51, 0\ns_add_u32 s60, s70, 256\nbuffer_load_dwordx4 v226, s[24:27], s60 offen lds\nM 16,164,20\ns_lshl4_add_u32 s62, s41, s42\n"
"s_lshl_b32 s62, s62, 12\ns_add_u32 s62, s62, 77594624\ns_lshl_b32 s60, s40, 10\ns_add_u32 s62, s62, s60\nbuffer_store_dwordx4 v[144:147], v252, s[20:23], s62 offen sc1\ns_add_u32 m0, s50, 57344\n"
"s_add_u32 s63, s52, 591872\nv_mfma_f32_16x16x32_bf16 v[32:35], v[160:163], a[8:11], 0\nbuffer_load_dwordx4 v220, s[4:7], s63 offen lds\nM 32,164,24\nbuffer_load_dwordx4 v221, s[4:7], s63 offen offset:1024 lds\nv_mfma_f32_16x16x32_bf16 v[48:51], v[160:163], a[12:15], 0\n"
"s_add_u32 m0, s51, 1024\ns_add_u32 s60, s70, 16640\nbuffer_load_dwordx4 v227, s[24:27], s60 offen lds\nM 48,164,28\ns_add_u32 m0, s50, 65536\ns_add_u32 s63, s52, 397312\n"
"buffer_load_dwordx4 v220, s[4:7], s63 offen lds\nG 4\nv_mfma_f32_16x16x32_bf16 v[4:7], v[168:171], a[0:3], 0\nR 160,187,0\nM 4,172,16\nbuffer_load_dwordx4 v221, s[4:7], s63 offen offset:1024 lds\n"
"v_mfma_f32_16x16x32_bf16 v[20:23], v[168:171], a[4:7], 0\nR 162,187,4096\nM 20,172,20\ns_add_u32 m0, s51, 2048\ns_add_u32 s60, s70, 33024\nbuffer_load_dwordx4 v226, s[24:27], s60 offen lds\n"
"v_mfma_f32_16x16x32_bf16 v[36:39], v[168:171], a[8:11], 0\nR 164,187,8192\ns_add_u32 m0, s50, 73728\nM 36,172,24\ns_lshl4_add_u32 s62, s41, s42\ns_lshl_b32 s62, s62, 12\n"
"s_add_u32 s62, s62, 78643200\ns_lshl_b32 s60, s40, 10\ns_add_u32 s62, s62, s60\nbuffer_store_dwordx4 v[144:147], v252, s[20:23], s62 offen sc1\ns_add_u32 s63, s52, 593920\nv_mfma_f32_16x16x32_bf16 v[52:55], v[168:171], a[12:15], 0\n"
"R 166,187,12288\nbuffer_load_dwordx4 v220, s[4:7], s63 offen lds\nM 52,172,28\nG 4\nv_mfma_f32_16x16x32_bf16 v[8:11], v[176:179], a[0:3], 0\nR 168,184,16384\n"
"M 8,180,16\ns_add_u32 m0, s51, 3072\ns_add_u32 s60, s70, 49408\nbuffer_load_dwordx4 v227, s[24:27], s60 offen lds\nv_mfma_f32_16x16x32_bf16 v[24:27], v[176:179], a[4:7], 0\nR 170,184,20480\n"
"s_add_u32 m0, s50, 73728\ns_nop 0\nbuffer_load_dwordx4 v221, s[4:7], s63 offen offset:1024 lds\nM 24,180,20\ns_add_u32 m0, s51, 4096\ns_add_u32 s60, s71, 256\n"
"buffer_load_dwordx4 v226, s[24:27], s60 offen lds\nv_mfma_f32_16x16x32_bf16 v[40:43], v[176:179], a[8:11], 0\nR 172,184,24576\nM 40,180,24\nv_mfma_f32_16x16x32_bf16 v[56:59], v[176:179], a[12:15], 0\nR 174,184,28672\n"
"M 56,180,28\nG 4\nv_mfma_f32_16x16x32_bf16 v[12:15], v[160:163], a[0:3], 0\nR 176,185,16384\nM 12,164,16\ns_add_u32 m0, s51, 5120\n"
"s_add_u32 s60, s71, 16640\nbuffer_load_dwordx4 v227, s[24:27], s60 offen lds\nv_mfma_f32_16x16x32_bf16 v[28:31], v[160:163], a[4:7], 0\nR 178,185,20480\nM 28,164,20\ns_add_u32 m0, s51, 6144\n"
"s_add_u32 s60, s71, 33024\nbuffer_load_dwordx4 v226, s[24:27], s60 offen lds\nv_mfma_f32_16x16x32_bf16 v[44:47], v[160:163], a[8:11], 0\nR 180,185,24576\ns_add_u32 m0, s50, 81920\ns_add_u32 s63, s101, 0\n"
"M 44,164,24\nbuffer_load_dwordx4 v220, s[4:7], s63 offen lds\nv_mfma_f32_16x16x32_bf16 v[60:63], v[160:163], a[12:15], 0\nR 182,185,28672\nv_mov_b32 v230, 0\nv_mov_b32 v231, 0\n"
"M 60,164,28\ns_waitcnt vmcnt(12)\ns_barrier\nG 4\nv_mov_b32 v232, 0\nv_mfma_f32_16x16x32_bf16 v[64:67], a[0:3], v[168:171], 0\n"
"R 160,186,16384\nv_mov_b32 v233, 0\nv_dot2c_f32_bf16 v230, s76, v192\nN 64,16,172\ns_add_u32 m0, s51, 7168\ns_add_u32 s60, s71, 49408\n"
"buffer_load_dwordx4 v227, s[24:27], s60 offen lds\nv_dot2c_f32_bf16 v231, v192, v192\nv_mfma_f32_16x16x32_bf16 v[68:71], a[4:7], v[168:171], 0\nR 162,186,20480\ns_add_u32 m0, s50, 81920\nv_dot2c_f32_bf16 v232, s76, v200\n"
"N 68,20,172\nbuffer_load_dwordx4 v221, s[4:7], s63 offen offset:1024 lds\nv_dot2c_f32_bf16 v233, v200, v200\nv_mfma_f32_16x16x32_bf16 v[96:99], a[8:11], v[168:171], 0\nR 164,186,24576\ns_add_u32 m0, s50, 0\n"
"s_add_u32 s63, s52, 788480\nv_dot2c_f32_bf16 v230, s76, v193\nN 96,24,172\nbuffer_load_dwordx4 v220, s[4:7], s63 offen lds\nv_dot2c_f32_bf16 v231, v193, v193\nv_mfma_f32_16x16x32_bf16 v[100:103], a[12:15], v[168:171], 0\n"
"R 166,186,28672\nv_lshrrev_b32 v234, 1, v252\nv_dot2c_f32_bf16 v232, s76, v201\nN 100,28,172\nG 4\nv_dot2c_f32_bf16 v233, v201, v201\n"
"v_mfma_f32_16x16x32_bf16 v[72:75], a[0:3], v[176:179], 0\nR 168,187,16384\nv_dot2c_f32_bf16 v230, s76, v194\nN 72,16,180\nbuffer_load_dwordx4 v221, s[4:7], s63 offen offset:1024 lds\nv_mov_b32 v242, s76\n"
"v_dot2c_f32_bf16 v231, v194, v194\nv_mfma_f32_16x16x32_bf16 v[76:79], a[4:7], v[176:179], 0\nR 170,187,20480\ns_add_u32 s63, s52, 985088\nv_mov_b32 v243, s76\nv_dot2c_f32_bf16 v232, s76, v202\n"
"N 76,20,180\ns_add_u32 m0, s50, 8192\nv_mov_b32 v244, s76\nv_mov_b32 v245, s76\ns_and_b32 s60, s40, 2\ns_lshl_b32 s60, s60, 5\n"
"v_xor_b32 v238, s60, v184\nv_dot2c_f32_bf16 v233, v202, v202\nv_mfma_f32_16x16x32_bf16 v[104:107], a[8:11], v[176:179], 0\nR 172,187,24576\nv_xor_b32 v239, 32, v238\nv_dot2c_f32_bf16 v230, s76, v195\n"
"N 104,24,180\nbuffer_load_dwordx4 v220, s[4:7], s63 offen lds\nv_dot2c_f32_bf16 v231, v195, v195\nv_mfma_f32_16x16x32_bf16 v[108:111], a[12:15], v[176:179], 0\nR 174,187,28672\nv_dot2c_f32_bf16 v232, s76, v203\n"
"N 108,28,180\nG 4\nv_dot2c_f32_bf16 v233, v203, v203\nv_mfma_f32_16x16x32_bf16 v[80:83], a[0:3], v[160:163], 0\nR 176,184,49152\nds_read_b128 a[32:35], v250 offset:0\n"
"R 128,238,32768\nR 130,238,36864\nv_dot2c_f32_bf16 v230, s76, v196\nN 80,16,164\nds_read_b128 a[36:39], v250 offset:2048\nR 132,239,32768\n"
"R 134,239,36864\nv_dot2c_f32_bf16 v231, v196, v196\nv_mfma_f32_16x16x32_bf16 v[84:87], a[4:7], v[160:163], 0\nR 178,184,53248\nds_read_b128 a[40:43], v250 offset:4096\nv_dot2c_f32_bf16 v232, s76, v204\n"
"N 84,20,164\nds_read_b128 a[44:47], v250 offset:6144\nv_dot2c_f32_bf16 v233, v204, v204\nv_mfma_f32_16x16x32_bf16 v[112:115], a[8:11], v[160:163], 0\nR 180,184,57344\nbuffer_load_dwordx4 v221, s[4:7], s63 offen offset:1024 lds\n"
"v_dot2c_f32_bf16 v230, s76, v197\nN 112,24,164\nds_read_b128 a[48:51], v251 offset:0\nv_dot2c_f32_bf16 v231, v197, v197\nv_mfma_f32_16x16x32_bf16 v[116:119], a[12:15], v[160:163], 0\nR 182,184,61440\n"
"ds_read_b128 a[52:55], v251 offset:2048\nG 10\nv_mfma_f32_16x16x32_bf16 v[136:139], v[242:245], v[128:131], 0\nv_dot2c_f32_bf16 v232, s76, v205\nN 116,28,164\nds_read_b128 a[56:59], v251 offset:4096\n"
"v_dot2c_f32_bf16 v233, v205, v205\nv_mfma_f32_16x16x32_bf16 v[88:91], a[0:3], v[168:171], 0\nR 160,185,49152\nG 9\nv_mfma_f32_16x16x32_bf16 v[140:143], v[242:245], v[132:135], 0\nv_dot2c_f32_bf16 v230, s76, v198\n"
"N 88,16,172\nds_read_b128 a[60:63], v251 offset:6144\nv_dot2c_f32_bf16 v231, v198, v198\nv_mfma_f32_16x16x32_bf16 v[92:95], a[4:7], v[168:171], 0\nR 162,185,53248\nv_dot2c_f32_bf16 v232, s76, v206\n"
"N 92,20,172\nv_dot2c_f32_bf16 v233, v206, v206\nv_mfma_f32_16x16x32_bf16 v[120:123], a[8:11], v[168:171], 0\nR 164,185,57344\nv_dot2c_f32_bf16 v230, s76, v199\nN 120,24,172\n"
"G 2\ns_add_u32 m0, s51, 122880\ns_add_u32 s60, s70, 384\nbuffer_load_dwordx4 v226, s[24:27], s60 offen lds\nv_dot2c_f32_bf16 v231, v199, v199\nv_mfma_f32_16x16x32_bf16 v[124:127], a[12:15], v[168:171], 0\n"
"R 166,185,61440\ns_nop 0\nv_permlane32_swap_b32 v230, v231\nv_lshrrev_b32 v128, 1, v252\nv_dot2c_f32_bf16 v232, s76, v207\nN 124,28,172\n"
"s_waitcnt vmcnt(11)\ns_barrier\nv_mov_b32 v131, s46\nv_dot2c_f32_bf16 v233, v207, v207\nM 0,176,32\nR 168,186,49152\n"
"s_nop 0\nv_permlane32_swap_b32 v232, v233\ns_mov_b64 s[60:61], 0xffff\nv_cndmask_b32 v130, v140, v136, s[60:61]\nv_add_f32 v230, v230, v231\nM 0,180,48\n"
"s_add_u32 m0, s51, 123904\ns_add_u32 s60, s70, 16768\nbuffer_load_dwordx4 v227, s[24:27], s60 offen lds\ns_lshr_b32 s61, s40, 1\ns_lshl_b32 s61, s61, 8\ns_add_u32 s61, s61, s91\n"
"s_add_u32 s61, s61, 262144\ns_bfm_b64 exec, 32, 0\nbuffer_store_dwordx2 v[130:131], v128, s[20:23], s61 offen sc1\ns_mov_b64 exec, -1\nM 16,176,36\nR 170,186,53248\n"
"s_add_u32 m0, s50, 16384\ns_add_u32 s63, s52, 790528\nM 16,180,52\nbuffer_load_dwordx4 v220, s[4:7], s63 offen lds\nM 32,176,40\nR 172,186,57344\n"
"v_add_f32 v232, v232, v233\nM 32,180,56\ns_add_u32 m0, s51, 124928\ns_add_u32 s60, s70, 33152\nbuffer_load_dwordx4 v226, s[24:27], s60 offen lds\nM 48,176,44\n"
"R 174,186,61440\nv_mov_b32 v231, s46\nM 48,180,60\nG 4\nv_permlane16_swap_b32 v230, v232\nv_add_f32 v230, v230, v232\n"
"M 4,160,32\nR 176,187,49152\nR 128,238,40960\nR 130,238,45056\nM 4,164,48\ns_add_u32 m0, s51, 125952\n"
"s_add_u32 s60, s70, 49536\nbuffer_load_dwordx4 v227, s[24:27], s60 offen lds\nR 132,239,40960\nR 134,239,45056\nM 20,160,36\nR 178,187,53248\n"
"s_add_u32 m0, s50, 16384\ns_nop 0\nbuffer_load_dwordx4 v221, s[4:7], s63 offen offset:1024 lds\nM 20,164,52\ns_add_u32 m0, s50, 24576\ns_add_u32 s63, s52, 987136\n"
"buffer_load_dwordx4 v220, s[4:7], s63 offen lds\nM 36,160,40\nR 180,187,57344\ns_lshr_b32 s61, s49, 3\ns_lshl2_add_u32 s61, s53, s61\ns_add_u32 s61, s61, 81788928\n"
"buffer_store_dwordx2 v[230:231], v234, s[20:23], s61 offen sc1\nM 36,164,56\ns_add_u32 m0, s51, 126976\ns_add_u32 s60, s71, 384\nbuffer_load_dwordx4 v226, s[24:27], s60 offen lds\nM 52,160,44\n"
"R 182,187,61440\nG 5\nv_mfma_f32_16x16x32_bf16 v[136:139], v[242:245], v[128:131], 0\ns_add_u32 m0, s50, 24576\nM 52,164,60\nM 8,168,32\n"
"R 160,188,0\nG 4\nv_mfma_f32_16x16x32_bf16 v[140:143], v[242:245], v[132:135], 0\nbuffer_load_dwordx4 v221, s[4:7], s63 offen offset:1024 lds\nM 8,172,48\ns_add_u32 m0, s51, 128000\n"
"s_add_u32 s60, s71, 16768\nbuffer_load_dwordx4 v227, s[24:27], s60 offen lds\nM 24,168,36\nR 162,188,4096\nM 24,172,52\nM 40,168,40\n"
"R 164,188,8192\nM 40,172,56\ns_add_u32 m0, s51, 129024\ns_add_u32 s60, s71, 33152\nbuffer_load_dwordx4 v226, s[24:27], s60 offen lds\nM 56,168,44\n"
"R 166,188,12288\nv_lshrrev_b32 v128, 1, v252\nv_mov_b32 v131, s46\nM 56,172,60\ns_mov_b64 s[60:61], 0xffff\nv_cndmask_b32 v130, v140, v136, s[60:61]\n"
"s_lshr_b32 s61, s40, 1\ns_lshl_b32 s61, s61, 8\ns_add_u32 s61, s61, s91\ns_add_u32 s61, s61, 524288\ns_bfm_b64 exec, 32, 0\nbuffer_store_dwordx2 v[130:131], v128, s[20:23], s61 offen sc1\n"
"s_mov_b64 exec, -1\nG 4\nM 12,176,32\nR 168,189,0\nM 12,180,48\ns_add_u32 m0, s51, 130048\n"
"s_add_u32 s60, s71, 49536\nbuffer_load_dwordx4 v227, s[24:27], s60 offen lds\nM 28,176,36\nR 170,189,4096\nM 28,180,52\nM 44,176,40\n"
"R 172,189,8192\nM 44,180,56\ns_add_u32 m0, s50, 32768\nM 60,176,44\nR 174,189,12288\nM 60,180,60\n"
"s_waitcnt vmcnt(15)\ns_barrier\nG 4\nN 64,32,160\nR 176,190,0\nN 64,48,164\n"
"s_add_u32 s63, s52, 1181696\nN 68,36,160\nR 178,190,4096\nN 68,52,164\nbuffer_load_dwordx4 v220, s[4:7], s63 offen lds\nN 96,40,160\n"
"R 180,190,8192\nds_read_b128 a[64:67], v248 offset:0\nN 96,56,164\nds_read_b128 a[68:71], v248 offset:2048\nN 100,44,160\nR 182,190,12288\n"
"ds_read_b128 a[72:75], v248 offset:4096\nN 100,60,164\nds_read_b128 a[76:79], v248 offset:6144\nG 8\nN 72,32,168\nR 160,191,0\n"
"N 72,48,172\nds_read_b128 a[80:83], v249 offset:0\nN 76,36,168\nR 162,191,4096\nds_read_b128 a[84:87], v249 offset:2048\nN 76,52,172\n"
"ds_read_b128 a[88:91], v249 offset:4096\nbuffer_load_dwordx4 v221, s[4:7], s63 offen offset:1024 lds\nN 104,40,168\nR 164,191,8192\ns_add_u32 m0, s50, 40960\nN 104,56,172\n"
"ds_read_b128 a[92:95], v249 offset:6144\ns_add_u32 s63, s52, 1378304\nN 108,44,168\nR 166,191,12288\nbuffer_load_dwordx4 v220, s[4:7], s63 offen lds\nN 108,60,172\n"
"G 10\nN 80,32,176\nR 168,184,0\nN 80,48,180\nG 2\ns_add_u32 m0, s51, 0\n"
"s_add_u32 s60, s70, 512\nbuffer_load_dwordx4 v226, s[24:27], s60 offen lds\nN 84,36,176\nR 170,184,4096\ns_add_u32 m0, s50, 40960\ns_nop 0\n"
"buffer_load_dwordx4 v221, s[4:7], s63 offen offset:1024 lds\nN 84,52,180\nN 112,40,176\nR 172,184,8192\nN 112,56,180\ns_add_u32 m0, s51, 1024\n"
"s_add_u32 s60, s70, 16896\nbuffer_load_dwordx4 v227, s[24:27], s60 offen lds\nN 116,44,176\nR 174,184,12288\nN 116,60,180\nG 4\n"
"N 88,32,160\nR 176,185,0\nN 88,48,164\ns_add_u32 m0, s51, 2048\ns_add_u32 s60, s70, 33280\nbuffer_load_dwordx4 v226, s[24:27], s60 offen lds\n"
"N 92,36,160\nR 178,185,4096\nN 92,52,164\nN 120,40,160\nR 180,185,8192\nN 120,56,164\n"
"s_add_u32 m0, s51, 3072\ns_add_u32 s60, s70, 49664\nbuffer_load_dwordx4 v227, s[24:27], s60 offen lds\nN 124,44,160\nR 182,185,12288\nN 124,60,164\n"
"s_waitcnt vmcnt(12)\ns_barrier\nG 4\nM 0,168,64\nR 160,186,0\nv_add_u32 v238, 0x10000, v238\n"
"v_add_u32 v239, 0x10000, v239\nM 0,172,80\ns_add_u32 m0, s51, 4096\ns_add_u32 s60, s71, 512\nbuffer_load_dwordx4 v226, s[24:27], s60 offen lds\nM 16,168,68\n"
"R 162,186,4096\nR 128,238,16384\nR 130,238,20480\nM 16,172,84\nR 132,239,16384\nR 134,239,20480\n"
"M 32,168,72\nR 164,186,8192\nM 32,172,88\ns_add_u32 m0, s51, 5120\ns_add_u32 s60, s71, 16896\nbuffer_load_dwordx4 v227, s[24:27], s60 offen lds\n"
"M 48,168,76\nR 166,186,12288\ns_add_u32 m0, s50, 49152\ns_add_u32 s63, s52, 1183744\nbuffer_load_dwordx4 v220, s[4:7], s63 offen lds\nM 48,172,92\n"
"G 8\nM 4,176,64\nR 168,187,0\nG 5\nv_mfma_f32_16x16x32_bf16 v[136:139], v[242:245], v[128:131], 0\nM 4,180,80\n"
"s_add_u32 m0, s51, 6144\ns_add_u32 s60, s71, 33280\nbuffer_load_dwordx4 v226, s[24:27], s60 offen lds\nM 20,176,68\nR 170,187,4096\nG 4\n"
"v_mfma_f32_16x16x32_bf16 v[140:143], v[242:245], v[132:135], 0\ns_add_u32 m0, s50, 49152\ns_nop 0\nbuffer_load_dwordx4 v221, s[4:7], s63 offen offset:1024 lds\nM 20,180,84\ns_add_u32 m0, s50, 57344\n"
"s_add_u32 s63, s52, 1380352\nbuffer_load_dwordx4 v220, s[4:7], s63 offen lds\nM 36,176,72\nR 172,187,8192\nM 36,180,88\ns_add_u32 m0, s51, 7168\n"
"s_add_u32 s60, s71, 49664\nbuffer_load_dwordx4 v227, s[24:27], s60 offen lds\nM 52,176,76\nR 174,187,12288\ns_add_u32 m0, s50, 57344\nM 52,180,92\n"
"G 4\nM 8,160,64\nR 176,184,16384\nbuffer_load_dwordx4 v221, s[4:7], s63 offen offset:1024 lds\nv_lshrrev_b32 v128, 1, v252\nM 8,164,80\n"
"s_mov_b64 s[60:61], 0xffff\nv_cndmask_b32 v130, v140, v136, s[60:61]\nv_mov_b32 v131, s46\nM 24,160,68\nR 178,184,20480\nM 24,164,84\n"
"s_lshr_b32 s61, s40, 1\ns_lshl_b32 s61, s61, 8\ns_add_u32 s61, s61, s91\ns_add_u32 s61, s61, 0\ns_bfm_b64 exec, 32, 0\nbuffer_store_dwordx2 v[130:131], v128, s[20:23], s61 offen sc1\n"
"s_mov_b64 exec, -1\nM 40,160,72\nR 180,184,24576\nM 40,164,88\nM 56,160,76\nR 182,184,28672\n"
"M 56,164,92\nG 4\nM 12,168,64\nR 160,185,16384\nM 12,172,80\ns_add_u32 m0, s50, 65536\n"
"M 28,168,68\nR 162,185,20480\nM 28,172,84\ns_add_u32 s63, s52, 1574912\nM 44,168,72\nR 164,185,24576\n"
"M 44,172,88\nbuffer_load_dwordx4 v220, s[4:7], s63 offen lds\nM 60,168,76\nR 166,185,28672\nM 60,172,92\ns_waitcnt vmcnt(13)\n"
"s_barrier\nG 4\nN 64,64,176\nR 168,186,16384\nN 64,80,180\nv_lshrrev_b32 v230, 7, v252\n"
"N 68,68,176\nR 170,186,20480\nN 68,84,180\nbuffer_load_dwordx4 v221, s[4:7], s63 offen offset:1024 lds\nv_lshlrev_b32 v230, 9, v230\nN 96,72,176\n"
"R 172,186,24576\nN 96,88,180\nds_read_b128 a[96:99], v250 offset:0\nv_and_b32 v231, 0x70, v252\nN 100,76,176\nR 174,186,28672\n"
"ds_read_b128 a[100:103], v250 offset:2048\nN 100,92,180\nds_read_b128 a[104:107], v250 offset:4096\nG 7\nN 72,64,160\nR 176,187,16384\n"
"s_and_b32 s60, s40, 3\ns_lshl_b32 s60, s60, 7\nv_add3_u32 v230, v230, v231, s60\nN 72,80,164\nds_read_b128 a[108:111], v250 offset:6144\ns_add_u32 m0, s50, 73728\n"
"N 76,68,160\nR 178,187,20480\nds_read_b128 a[112:115], v251 offset:0\nN 76,84,164\nds_read_b128 a[116:119], v251 offset:2048\ns_lshl_b32 s61, s42, 14\n"
"s_add_u32 s61, s61, 84148224\nbuffer_load_dwordx4 v[128:131], v230, s[20:23], s61 offen sc1\ns_add_u32 s63, s52, 1771520\nN 104,72,160\nR 180,187,24576\nds_read_b128 a[120:123], v251 offset:4096\n"
"N 104,88,164\nds_read_b128 a[124:127], v251 offset:6144\ns_lshl_b32 s61, s42, 14\ns_add_u32 s61, s61, 84152320\nbuffer_load_dwordx4 v[132:135], v230, s[20:23], s61 offen sc1\nN 108,76,160\n"
"R 182,187,28672\nbuffer_load_dwordx4 v220, s[4:7], s63 offen lds\nN 108,92,164\ns_lshl_b32 s61, s42, 14\ns_add_u32 s61, s61, 84156416\nbuffer_load_dwordx4 v[136:139], v230, s[20:23], s61 offen sc1\n"
"G 11\nN 80,64,168\nR 160,184,32768\nbuffer_load_dwordx4 v221, s[4:7], s63 offen offset:1024 lds\nN 80,80,172\ns_lshl_b32 s61, s42, 14\n"
"s_add_u32 s61, s61, 84160512\nbuffer_load_dwordx4 v[140:143], v230, s[20:23], s61 offen sc1\nN 84,68,168\nR 162,184,36864\nN 84,84,172\nN 112,72,168\n"
"R 164,184,40960\ns_waitcnt vmcnt(10)\nds_read_b128 a[128:131], v248 offset:0\nN 112,88,172\nds_read_b128 a[132:135], v248 offset:2048\nN 116,76,168\n"
"R 166,184,45056\nds_read_b128 a[136:139], v248 offset:4096\nN 116,92,172\nds_read_b128 a[140:143], v248 offset:6144\nG 8\nN 88,64,176\n"
"R 168,185,32768\nN 88,80,180\nds_read_b128 a[144:147], v249 offset:0\nN 92,68,176\nR 170,185,36864\nds_read_b128 a[148:151], v249 offset:2048\n"
"N 92,84,180\nds_read_b128 a[152:155], v249 offset:4096\nN 120,72,176\nR 172,185,40960\nN 120,88,180\nds_read_b128 a[156:159], v249 offset:6144\n"
"N 124,76,176\nR 174,185,45056\nN 124,92,180\ns_waitcnt vmcnt(9)\ns_barrier\nG 10\n"
"M 0,160,96\nR 176,186,32768\nM 0,164,112\nG 2\ns_add_u32 m0, s51, 0\ns_add_u32 s60, s70, 640\n"
"buffer_load_dwordx4 v226, s[24:27], s60 offen lds\nM 16,160,100\nR 178,186,36864\nM 16,164,116\ns_add_u32 m0, s51, 1024\ns_add_u32 s60, s70, 17024\n"
"buffer_load_dwordx4 v227, s[24:27], s60 offen lds\nM 32,160,104\nR 180,186,40960\ns_add_u32 m0, s50, 81920\ns_add_u32 s63, s52, 1576960\nbuffer_load_dwordx4 v220, s[4:7], s63 offen lds\n"
"v_lshrrev_b32 v234, 7, v252\nM 32,164,120\ns_add_u32 m0, s51, 2048\ns_add_u32 s60, s70, 33408\nbuffer_load_dwordx4 v226, s[24:27], s60 offen lds\nM 48,160,108\n"
"R 182,186,45056\nv_lshlrev_b32 v234, 9, v234\nM 48,164,124\ns_add_u32 m0, s51, 3072\ns_add_u32 s60, s70, 49792\nbuffer_load_dwordx4 v227, s[24:27], s60 offen lds\n"
"G 4\nM 4,168,96\nR 160,187,32768\ns_add_u32 m0, s50, 81920\ns_nop 0\nbuffer_load_dwordx4 v221, s[4:7], s63 offen offset:1024 lds\n"
"M 4,172,112\ns_add_u32 m0, s51, 4096\ns_add_u32 s60, s71, 640\nbuffer_load_dwordx4 v226, s[24:27], s60 offen lds\nM 20,168,100\nR 162,187,36864\n"
"v_and_b32 v235, 0x70, v252\nM 20,172,116\ns_add_u32 m0, s51, 5120\ns_add_u32 s60, s71, 17024\nbuffer_load_dwordx4 v227, s[24:27], s60 offen lds\nM 36,168,104\n"
"R 164,187,40960\ns_and_b32 s60, s40, 3\ns_lshl_b32 s60, s60, 7\nv_add3_u32 v234, v234, v235, s60\nM 36,172,120\ns_add_u32 m0, s51, 6144\n"
"s_add_u32 s60, s71, 33408\nbuffer_load_dwordx4 v226, s[24:27], s60 offen lds\nM 52,168,108\nR 166,187,45056\ns_lshl_b32 s61, s42, 14\ns_add_u32 s61, s61, 84410368\n"
"buffer_load_dwordx4 v[144:147], v234, s[20:23], s61 offen sc1\nM 52,172,124\ns_add_u32 m0, s51, 7168\ns_add_u32 s60, s71, 49792\nbuffer_load_dwordx4 v227, s[24:27], s60 offen lds\nG 4\n"
"M 8,176,96\nR 168,184,49152\ns_add_u32 m0, s50, 90112\ns_add_u32 s63, s52, 1773568\nbuffer_load_dwordx4 v220, s[4:7], s63 offen lds\nM 8,180,112\n"
"s_lshl_b32 s61, s42, 14\ns_add_u32 s61, s61, 84414464\nbuffer_load_dwordx4 v[148:151], v234, s[20:23], s61 offen sc1\nM 24,176,100\nR 170,184,53248\nbuffer_load_dwordx4 v221, s[4:7], s63 offen offset:1024 lds\n"
"M 24,180,116\ns_lshl_b32 s61, s42, 14\ns_add_u32 s61, s61, 84418560\nbuffer_load_dwordx4 v[152:155], v234, s[20:23], s61 offen sc1\nM 40,176,104\nR 172,184,57344\n"
"M 40,180,120\ns_lshl_b32 s61, s42, 14\ns_add_u32 s61, s61, 84422656\nbuffer_load_dwordx4 v[156:159], v234, s[20:23], s61 offen sc1\nM 56,176,108\nR 174,184,61440\n"
"M 56,180,124\nG 4\nM 12,160,96\nR 176,185,49152\nM 12,164,112\nM 28,160,100\n"
"R 178,185,53248\nM 28,164,116\ns_add_u32 m0, s50, 98304\nM 44,160,104\nR 180,185,57344\nM 44,164,120\n"
"s_add_u32 s63, s52, 1968128\nM 60,160,108\nR 182,185,61440\nM 60,164,124\ns_waitcnt vmcnt(17)\ns_barrier\n"
"G 4\nN 64,96,168\nR 160,186,49152\nv_min3_u32 v232, v129, v131, v133\nv_min3_u32 v232, v232, v135, v137\nN 64,112,172\n"
"buffer_load_dwordx4 v220, s[4:7], s63 offen lds\ns_waitcnt vmcnt(17)\nv_min3_u32 v232, v232, v139, v141\nN 68,100,168\nR 162,186,53248\nv_min3_u32 v232, v232, v143, v143\n"
"v_max3_u32 v233, v129, v131, v133\nN 68,116,172\nv_max3_u32 v233, v233, v135, v137\nv_max3_u32 v233, v233, v139, v141\nv_max3_u32 v233, v233, v143, v143\nN 96,104,168\n"
"R 164,186,57344\nv_cmp_ne_u32 s[86:87], s46, v232\nv_cmp_ne_u32 s[88:89], s46, v233\nN 96,120,172\nbuffer_load_dwordx4 v221, s[4:7], s63 offen offset:1024 lds\ns_or_b64 s[86:87], s[86:87], s[88:89]\n"
"s_cbranch_scc1 Lq116\nLq117:\nN 100,108,168\nR 166,186,61440\ns_add_u32 m0, s50, 106496\nv_add_f32 v128, v128, v136\n"
"N 100,124,172\nG 4\nv_add_f32 v130, v130, v138\nv_add_f32 v132, v132, v140\nv_add_f32 v134, v134, v142\nN 72,96,176\n"
"R 168,187,49152\ns_add_u32 s63, s52, 2164736\nv_add_f32 v128, v128, v132\nN 72,112,180\nbuffer_load_dwordx4 v220, s[4:7], s63 offen lds\nv_add_f32 v130, v130, v134\n"
"N 76,100,176\nR 170,187,53248\nv_add_f32_dpp v128, v128, v128 row_ror:8 row_mask:0xf bank_mask:0xf\nv_add_f32_dpp v130, v130, v130 row_ror:8 row_mask:0xf bank_mask:0xf\nN 76,116,180\nv_mov_b32 v132, v128\n"
"v_mov_b32 v133, v130\ns_nop 0\nv_permlane32_swap_b32 v132, v128\nN 104,104,176\nR 172,187,57344\nv_permlane32_swap_b32 v133, v130\n"
"v_add_f32 v128, v128, v132\nN 104,120,180\nbuffer_load_dwordx4 v221, s[4:7], s63 offen offset:1024 lds\nv_add_f32 v130, v130, v133\nN 108,108,176\nR 174,187,61440\n"
"s_add_u32 s63, s52, 1970176\nv_mov_b32 v132, v128\nN 108,124,180\nG 4\nv_mov_b32 v133, v130\nv_permlane16_swap_b32 v132, v128\n"
"s_nop 0\nv_permlane16_swap_b32 v133, v130\nN 80,96,160\nR 176,188,0\nv_add_f32 v128, v128, v132\nv_add_f32 v130, v130, v133\n"
"N 80,112,164\ns_add_u32 m0, s50, 114688\nv_and_b32 v136, s77, v128\nv_sub_f32 v137, v128, v136\nv_cvt_pk_bf16_f32 v132, v136, v137\nN 84,100,160\n"
"R 178,188,4096\nv_cvt_pk_bf16_f32 v133, v136, 0\nv_and_b32 v136, s77, v130\nN 84,116,164\nbuffer_load_dwordx4 v220, s[4:7], s63 offen lds\nN 112,104,160\n"
"R 180,188,8192\nv_sub_f32 v137, v130, v136\nN 112,120,164\nv_cvt_pk_bf16_f32 v134, v136, v137\nv_cvt_pk_bf16_f32 v135, v136, 0\nN 116,108,160\n"
"R 182,188,12288\nv_and_b32 v230, 0x1ff, v230\nN 116,124,164\nv_add_u32 v230, 156160, v230\nds_write_b128 v230, v[132:135]\nG 5\n"
"N 88,96,168\nR 160,189,0\nN 88,112,172\nbuffer_load_dwordx4 v221, s[4:7], s63 offen offset:1024 lds\nN 92,100,168\nR 162,189,4096\n"
"s_add_u32 s63, s52, 2166784\nN 92,116,172\ns_add_u32 m0, s50, 0\nN 120,104,168\nR 164,189,8192\nN 120,120,172\n"
"buffer_load_dwordx4 v220, s[4:7], s63 offen lds\nN 124,108,168\nR 166,189,12288\nN 124,124,172\ns_waitcnt vmcnt(12)\nds_read_b128 a[160:163], v248 offset:0\n"
"s_waitcnt vmcnt(9) lgkmcnt(5)\ns_barrier\nM 0,176,128\nR 168,190,0\nds_read_b128 a[164:167], v248 offset:2048\nM 0,180,144\n"
"ds_read_b128 a[168:171], v248 offset:4096\nM 16,176,132\nR 170,190,4096\nv_min3_u32 v236, v145, v147, v149\ns_waitcnt vmcnt(8)\nv_min3_u32 v236, v236, v151, v153\n"
"M 16,180,148\nds_read_b128 a[172:175], v248 offset:6144\ns_waitcnt vmcnt(7)\nv_min3_u32 v236, v236, v155, v157\nv_min3_u32 v236, v236, v159, v159\nM 32,176,136\n"
"R 172,190,8192\nds_read_b128 a[176:179], v249 offset:0\nv_max3_u32 v237, v145, v147, v149\nM 32,180,152\nds_read_b128 a[180:183], v249 offset:2048\nv_lshrrev_b32 v230, 7, v252\n"
"v_max3_u32 v237, v237, v151, v153\nM 48,176,140\nR 174,190,12288\nds_read_b128 a[184:187], v249 offset:4096\nv_max3_u32 v237, v237, v155, v157\nM 48,180,156\n"
"ds_read_b128 a[188:191], v249 offset:6144\nG 12\nv_max3_u32 v237, v237, v159, v159\nM 4,160,128\nR 176,191,0\nv_lshlrev_b32 v230, 9, v230\n"
"v_cmp_ne_u32 s[86:87], s46, v236\nM 4,164,144\nbuffer_load_dwordx4 v221, s[4:7], s63 offen offset:1024 lds\nv_and_b32 v231, 0x70, v252\nv_cmp_ne_u32 s[88:89], s46, v237\nM 20,160,132\n"
"R 178,191,4096\ns_or_b64 s[86:87], s[86:87], s[88:89]\ns_cbranch_scc1 Lq126\nLq127:\nM 20,164,148\nG 2\n"
"s_add_u32 m0, s51, 0\ns_add_u32 s60, s70, 768\nbuffer_load_dwordx4 v226, s[24:27], s60 offen lds\nv_add_f32 v144, v144, v152\nM 36,160,136\nR 180,191,8192\n"
"s_and_b32 s60, s40, 3\ns_lshl_b32 s60, s60, 7\nv_add3_u32 v230, v230, v231, s60\nv_add_f32 v146, v146, v154\nM 36,164,152\ns_add_u32 m0, s51, 1024\n"
"s_add_u32 s60, s70, 17152\nbuffer_load_dwordx4 v227, s[24:27], s60 offen lds\nv_add_f32 v148, v148, v156\nM 52,160,140\nR 182,191,12288\ns_lshl_b32 s61, s42, 14\n"
"s_add_u32 s61, s61, 83886080\nbuffer_load_dwordx4 v[128:131], v230, s[20:23], s61 offen sc1\nv_add_f32 v150, v150, v158\nM 52,164,156\ns_add_u32 m0, s51, 2048\ns_add_u32 s60, s70, 33536\n"
"buffer_load_dwordx4 v226, s[24:27], s60 offen lds\nv_add_f32 v144, v144, v148\nM 8,168,128\nR 160,188,16384\ns_lshl_b32 s61, s42, 14\ns_add_u32 s61, s61, 83890176\n"
"buffer_load_dwordx4 v[132:135], v230, s[20:23], s61 offen sc1\nv_add_f32 v146, v146, v150\nM 8,172,144\ns_add_u32 m0, s51, 3072\ns_add_u32 s60, s70, 49920\nbuffer_load_dwordx4 v227, s[24:27], s60 offen lds\n"
"v_add_f32_dpp v144, v144, v144 row_ror:8 row_mask:0xf bank_mask:0xf\nM 24,168,132\nR 162,188,20480\nv_add_f32_dpp v146, v146, v146 row_ror:8 row_mask:0xf bank_mask:0xf\nv_mov_b32 v148, v144\nM 24,172,148\n"
"s_add_u32 m0, s51, 4096\ns_add_u32 s60, s71, 768\nbuffer_load_dwordx4 v226, s[24:27], s60 offen lds\nv_mov_b32 v149, v146\nM 40,168,136\nR 164,188,24576\n"
"s_lshl_b32 s61, s42, 14\ns_add_u32 s61, s61, 83894272\nbuffer_load_dwordx4 v[136:139], v230, s[20:23], s61 offen sc1\nv_permlane32_swap_b32 v148, v144\nM 40,172,152\ns_add_u32 m0, s51, 5120\n"
"s_add_u32 s60, s71, 17152\nbuffer_load_dwordx4 v227, s[24:27], s60 offen lds\nv_permlane32_swap_b32 v149, v146\nM 56,168,140\nR 166,188,28672\ns_lshl_b32 s61, s42, 14\n"
"s_add_u32 s61, s61, 83898368\nbuffer_load_dwordx4 v[140:143], v230, s[20:23], s61 offen sc1\nv_add_f32 v144, v144, v148\nM 56,172,156\ns_add_u32 m0, s51, 6144\ns_add_u32 s60, s71, 33536\n"
"buffer_load_dwordx4 v226, s[24:27], s60 offen lds\nG 4\nv_add_f32 v146, v146, v149\nM 12,176,128\nR 168,189,16384\nv_mov_b32 v148, v144\n"
"v_mov_b32 v149, v146\nM 12,180,144\ns_add_u32 m0, s51, 7168\ns_add_u32 s60, s71, 49920\nbuffer_load_dwordx4 v227, s[24:27], s60 offen lds\nv_permlane16_swap_b32 v148, v144\n"
"M 28,176,132\nR 170,189,20480\nv_permlane16_swap_b32 v149, v146\nv_add_f32 v144, v144, v148\nM 28,180,148\ns_add_u32 m0, s50, 8192\n"
"v_add_f32 v146, v146, v149\nv_and_b32 v152, s77, v144\nv_sub_f32 v153, v144, v152\nM 44,176,136\nR 172,189,24576\nv_cvt_pk_bf16_f32 v148, v152, v153\n"
"v_cvt_pk_bf16_f32 v149, v152, 0\nM 44,180,152\ns_add_u32 s63, s52, 2361344\nv_and_b32 v152, s77, v146\nv_sub_f32 v153, v146, v152\nv_cvt_pk_bf16_f32 v150, v152, v153\n"
"M 60,176,140\nR 174,189,28672\nv_cvt_pk_bf16_f32 v151, v152, 0\nv_and_b32 v234, 0x1ff, v234\nM 60,180,156\ns_waitcnt vmcnt(16)\n"
"s_barrier\nG 4\nv_add_u32 v234, 156672, v234\nN 64,128,160\nR 176,190,16384\nds_write_b128 v234, v[148:151]\n"
"N 64,144,164\nbuffer_load_dwordx4 v220, s[4:7], s63 offen lds\nN 68,132,160\nR 178,190,20480\nN 68,148,164\nN 96,136,160\n"
"R 180,190,24576\nN 96,152,164\nbuffer_load_dwordx4 v221, s[4:7], s63 offen offset:1024 lds\nN 100,140,160\nR 182,190,28672\ns_add_u32 m0, s50, 16384\n"
"N 100,156,164\nG 5\nN 72,128,168\nR 160,191,16384\ns_add_u32 s63, s52, 2557952\nN 72,144,172\n"
"buffer_load_dwordx4 v220, s[4:7], s63 offen lds\nN 76,132,168\nR 162,191,20480\nN 76,148,172\nN 104,136,168\nR 164,191,24576\n"
"Lq199:\ns_lshr_b32 s60, s45, 3\ns_lshl2_add_u32 s60, s60, s40\ns_lshl_b32 s60, s60, 8\nv_lshl_add_u32 v224, v252, 3, s60\nv_add_u32 v224, Lq199+4096-LqPC, v224\n"
"s_mov_b64 exec, 3\nglobal_load_dword v224, v224, s[98:99]\ns_mov_b64 exec, -1\nN 104,152,172\nbuffer_load_dwordx4 v221, s[4:7], s63 offen offset:1024 lds\nN 108,140,168\n"
"R 166,191,28672\nN 108,156,172\nG 4\nN 80,128,176\nR 168,188,32768\nN 80,144,180\n"
"N 84,132,176\nR 170,188,36864\nN 84,148,180\ns_add_u32 m0, s50, 24576\nN 112,136,176\nR 172,188,40960\n"
"N 112,152,180\ns_add_u32 s63, s52, 2754560\nN 116,140,176\nR 174,188,45056\nN 116,156,180\nG 4\n"
"N 88,128,160\nR 176,189,32768\nN 88,144,164\nbuffer_load_dwordx4 v220, s[4:7], s63 offen lds\nN 92,132,160\nR 178,189,36864\n"
"N 92,148,164\nN 120,136,160\nR 180,189,40960\ns_waitcnt vmcnt(13)\nv_min3_u32 v232, v129, v131, v133\ns_waitcnt vmcnt(10)\n"
"v_min3_u32 v232, v232, v135, v137\nN 120,152,164\nbuffer_load_dwordx4 v221, s[4:7], s63 offen offset:1024 lds\ns_waitcnt vmcnt(9)\nv_min3_u32 v232, v232, v139, v141\nN 124,140,160\n"
"R 182,189,45056\ns_add_u32 m0, s50, 32768\nv_min3_u32 v232, v232, v143, v143\nN 124,156,164\ns_barrier\nG 4\n"
"v_max3_u32 v233, v129, v131, v133\nM 0,168,160\nR 160,190,32768\ns_add_u32 s63, s52, 2951168\nv_max3_u32 v233, v233, v135, v137\nM 0,172,176\n"
"buffer_load_dwordx4 v220, s[4:7], s63 offen lds\nv_max3_u32 v233, v233, v139, v141\nM 16,168,164\nR 162,190,36864\nv_max3_u32 v233, v233, v143, v143\nv_cmp_ne_u32 s[86:87], s46, v232\n"
"M 16,172,180\nv_cmp_ne_u32 s[88:89], s46, v233\nM 32,168,168\nR 164,190,40960\ns_or_b64 s[86:87], s[86:87], s[88:89]\ns_cbranch_scc1 Lq136\n"
"Lq137:\nM 32,172,184\nv_add_f32 v128, v128, v136\nv_add_f32 v130, v130, v138\nv_add_f32 v132, v132, v140\nM 48,168,172\n"
"R 166,190,45056\nv_add_f32 v134, v134, v142\nv_add_f32 v128, v128, v132\nM 48,172,188\nG 4\nv_add_f32 v130, v130, v134\n"
"v_add_f32_dpp v128, v128, v128 row_ror:8 row_mask:0xf bank_mask:0xf\ns_nop 0\nv_add_f32_dpp v130, v130, v130 row_ror:8 row_mask:0xf bank_mask:0xf\nM 4,176,160\nR 168,191,32768\nv_mov_b32 v132, v128\n"
"v_mov_b32 v133, v130\nM 4,180,176\nbuffer_load_dwordx4 v221, s[4:7], s63 offen offset:1024 lds\nv_permlane32_swap_b32 v132, v128\nM 20,176,164\nR 170,191,36864\n"
"s_waitcnt vmcnt(9)\nds_read_b128 a[192:195], v248 offset:0\nv_permlane32_swap_b32 v133, v130\nM 20,180,180\nds_read_b128 a[196:199], v248 offset:2048\nv_add_f32 v128, v128, v132\n"
"v_add_f32 v130, v130, v133\nM 36,176,168\nR 172,191,40960\nds_read_b128 a[200:203], v248 offset:4096\nv_mov_b32 v132, v128\nM 36,180,184\n"
"ds_read_b128 a[204:207], v248 offset:6144\nv_mov_b32 v133, v130\nv_permlane16_swap_b32 v132, v128\nM 52,176,172\nR 174,191,45056\nv_permlane16_swap_b32 v133, v130\n"
"v_add_f32 v128, v128, v132\nM 52,180,188\nds_read_b128 a[208:211], v249 offset:0\nG 9\nv_add_f32 v130, v130, v133\nM 8,160,160\n"
"R 176,188,49152\nds_read_b128 a[212:215], v249 offset:2048\nv_and_b32 v136, s77, v128\nM 8,164,176\nds_read_b128 a[216:219], v249 offset:4096\nv_sub_f32 v137, v128, v136\n"
"v_cvt_pk_bf16_f32 v132, v136, v137\nM 24,160,164\nR 178,188,53248\nv_cvt_pk_bf16_f32 v133, v136, 0\nv_and_b32 v136, s77, v130\nM 24,164,180\n"
"ds_read_b128 a[220:223], v249 offset:6144\nv_sub_f32 v137, v130, v136\nv_cvt_pk_bf16_f32 v134, v136, v137\nv_cvt_pk_bf16_f32 v135, v136, 0\nv_and_b32 v230, 0x1ff, v230\nv_add_u32 v230, 155648, v230\n"
"ds_write_b128 v230, v[132:135]\nM 40,160,168\nR 180,184,0\nM 40,164,184\ns_lshr_b32 s84, s49, 3\nM 56,160,172\n"
"R 182,184,4096\ns_add_u32 s84, s84, 81788928\nM 56,164,188\nG 3\ns_add_u32 m0, s51, 0\ns_add_u32 s60, s70, 896\n"
"buffer_load_dwordx4 v226, s[24:27], s60 offen lds\nM 12,168,160\nR 160,189,49152\nbuffer_load_dwordx4 v[128:131], v252, s[20:23], s84 offen offset:0 sc1\nM 12,172,176\ns_add_u32 m0, s51, 1024\n"
"s_add_u32 s60, s70, 17280\nbuffer_load_dwordx4 v227, s[24:27], s60 offen lds\nM 28,168,164\nR 162,189,53248\nM 28,172,180\ns_add_u32 m0, s51, 2048\n"
"s_add_u32 s60, s70, 33664\nbuffer_load_dwordx4 v226, s[24:27], s60 offen lds\nM 44,168,168\nR 164,185,0\nbuffer_load_dwordx4 v[132:135], v252, s[20:23], s84 offen offset:1024 sc1\nM 44,172,184\n"
"s_add_u32 m0, s51, 3072\ns_add_u32 s60, s70, 50048\nbuffer_load_dwordx4 v227, s[24:27], s60 offen lds\nM 60,168,172\nR 166,185,4096\nbuffer_load_dwordx4 v[136:139], v252, s[20:23], s84 offen offset:2048 sc1\n"
"M 60,172,188\ns_add_u32 m0, s51, 4096\ns_add_u32 s60, s71, 896\nbuffer_load_dwordx4 v226, s[24:27], s60 offen lds\ns_waitcnt vmcnt(12) lgkmcnt(6)\ns_barrier\n"
"G 4\nN 64,160,176\nR 168,190,49152\ns_lshr_b32 s85, s49, 3\nN 64,176,180\ns_add_u32 m0, s51, 5120\n"
"s_add_u32 s60, s71, 17280\nbuffer_load_dwordx4 v227, s[24:27], s60 offen lds\ns_add_u32 s85, s85, 81793024\nN 68,164,176\nR 170,190,53248\nbuffer_load_dwordx4 v[140:143], v252, s[20:23], s84 offen offset:3072 sc1\n"
"N 68,180,180\ns_add_u32 m0, s51, 6144\ns_add_u32 s60, s71, 33664\nbuffer_load_dwordx4 v226, s[24:27], s60 offen lds\nN 96,168,176\nR 172,186,0\n"
"buffer_load_dwordx4 v[144:147], v252, s[20:23], s85 offen offset:0 sc1\nN 96,184,180\ns_add_u32 m0, s51, 7168\ns_add_u32 s60, s71, 50048\nbuffer_load_dwordx4 v227, s[24:27], s60 offen lds\nN 100,172,176\n"
"R 174,186,4096\ns_add_u32 m0, s50, 40960\ns_add_u32 s63, s52, 2363392\nbuffer_load_dwordx4 v220, s[4:7], s63 offen lds\nN 100,188,180\nG 4\n"
"N 72,160,160\nR 176,191,49152\nbuffer_load_dwordx4 v221, s[4:7], s63 offen offset:1024 lds\nN 72,176,164\nbuffer_load_dwordx4 v[148:151], v252, s[20:23], s85 offen offset:1024 sc1\ns_add_u32 s63, s52, 2560000\n"
"N 76,164,160\nR 178,191,53248\ns_add_u32 m0, s50, 49152\nN 76,180,164\nbuffer_load_dwordx4 v[152:155], v252, s[20:23], s85 offen offset:2048 sc1\nN 104,168,160\n"
"R 180,187,0\nbuffer_load_dwordx4 v220, s[4:7], s63 offen lds\nN 104,184,164\nbuffer_load_dwordx4 v[156:159], v252, s[20:23], s85 offen offset:3072 sc1\nN 108,172,160\nR 182,187,4096\n"
"N 108,188,164\nG 4\nN 80,160,168\nR 160,184,8192\nbuffer_load_dwordx4 v221, s[4:7], s63 offen offset:1024 lds\nN 80,176,172\n"
"N 84,164,168\nR 162,184,12288\nN 84,180,172\nN 112,168,168\nR 164,184,16384\nN 112,184,172\n"
"N 116,172,168\nR 166,184,20480\nN 116,188,172\nG 4\nN 88,160,176\nR 168,185,8192\n"
"N 88,176,180\ns_add_u32 m0, s50, 57344\nN 92,164,176\nR 170,185,12288\nN 92,180,180\ns_add_u32 s63, s52, 2756608\n"
"N 120,168,176\nR 172,185,16384\nN 120,184,180\nbuffer_load_dwordx4 v220, s[4:7], s63 offen lds\nN 124,172,176\nR 174,185,20480\n"
"N 124,188,180\ns_waitcnt vmcnt(21)\ns_barrier\nG 4\nM 0,160,192\nR 176,186,8192\n"
"M 0,164,208\nbuffer_load_dwordx4 v221, s[4:7], s63 offen offset:1024 lds\nM 16,160,196\nR 178,186,12288\nM 16,164,212\ns_add_u32 m0, s50, 65536\n"
"M 32,160,200\nR 180,186,16384\nM 32,164,216\ns_add_u32 s63, s52, 2953216\nM 48,160,204\nR 182,186,20480\n"
"s_lshl2_add_u32 s60, s45, s40\nv_lshlrev_b32 v224, 3, v252\ns_mul_i32 s60, s60, 2048\ns_bfm_b64 exec, 16, 0\nbuffer_load_dword v224, v224, s[8:11], s60 offen\ns_mov_b64 exec, -1\n"
"M 48,164,220\nG 4\nM 4,168,192\nR 160,187,8192\nM 4,172,208\nbuffer_load_dwordx4 v220, s[4:7], s63 offen lds\n"
"M 20,168,196\nR 162,187,12288\nM 20,172,212\nM 36,168,200\nR 164,187,16384\nM 36,172,216\n"
"buffer_load_dwordx4 v221, s[4:7], s63 offen offset:1024 lds\nM 52,168,204\nR 166,187,20480\nM 52,172,220\nG 4\nM 8,176,192\n"
"R 168,184,24576\nM 8,180,208\nM 24,176,196\nR 170,184,28672\ns_waitcnt vmcnt(12)\nds_read_b128 a[224:227], v248 offset:0\n"
"M 24,180,212\nds_read_b128 a[228:231], v248 offset:2048\nM 40,176,200\nR 172,184,32768\nds_read_b128 a[232:235], v248 offset:4096\nM 40,180,216\n"
"ds_read_b128 a[236:239], v248 offset:6144\nM 56,176,204\nR 174,184,36864\nM 56,180,220\nds_read_b128 a[240:243], v249 offset:0\nG 9\n"
"M 12,160,192\nR 176,185,24576\nds_read_b128 a[244:247], v249 offset:2048\nv_min3_u32 v230, v129, v131, v133\nM 12,164,208\nds_read_b128 a[248:251], v249 offset:4096\n"
"s_add_u32 m0, s50, 73728\nv_min3_u32 v230, v230, v135, v137\nM 28,160,196\nR 178,185,28672\ns_add_u32 s63, s52, 0\nv_min3_u32 v230, v230, v139, v141\n"
"M 28,164,212\nds_read_b128 a[252:255], v249 offset:6144\nv_min3_u32 v230, v230, v143, v145\ns_waitcnt vmcnt(9)\nv_min3_u32 v230, v230, v147, v149\nM 44,160,200\n"
"R 180,185,32768\ns_waitcnt vmcnt(8)\nv_min3_u32 v230, v230, v151, v153\ns_waitcnt vmcnt(6)\nv_min3_u32 v230, v230, v155, v157\nM 44,164,216\n"
"buffer_load_dwordx4 v220, s[4:7], s63 offen lds\nv_min3_u32 v230, v230, v159, v159\nM 60,160,204\nR 182,185,36864\nv_max3_u32 v231, v129, v131, v133\nv_max3_u32 v231, v231, v135, v137\n"
"M 60,164,220\ns_waitcnt vmcnt(6)\ns_barrier\nG 8\nv_max3_u32 v231, v231, v139, v141\nM 0,168,224\n"
"R 160,186,24576\nv_max3_u32 v231, v231, v143, v145\nv_max3_u32 v231, v231, v147, v149\nG 8\nM 0,172,240\nbuffer_load_dwordx4 v221, s[4:7], s63 offen offset:1024 lds\n"
"v_max3_u32 v231, v231, v151, v153\nM 16,168,228\nR 162,186,28672\ns_add_u32 s63, s52, 196608\nv_max3_u32 v231, v231, v155, v157\nG 7\n"
"M 16,172,244\ns_add_u32 m0, s50, 81920\nv_max3_u32 v231, v231, v159, v159\nv_cmp_ne_u32 s[86:87], s46, v230\nv_cmp_ne_u32 s[88:89], s46, v231\nM 32,168,232\n"
"R 164,186,32768\ns_or_b64 s[86:87], s[86:87], s[88:89]\ns_cbranch_scc1 Lq151\nLq152:\nG 7\nM 32,172,248\n"
"buffer_load_dwordx4 v220, s[4:7], s63 offen lds\nv_add_f32 v128, v128, v144\nM 48,168,236\nR 166,186,36864\nv_add_f32 v130, v130, v146\nv_add_f32 v132, v132, v148\n"
"G 6\nM 48,172,252\nG 4\nv_add_f32 v134, v134, v150\nv_add_f32 v136, v136, v152\nv_add_f32 v138, v138, v154\n"
"M 4,176,224\nR 168,187,24576\nv_add_f32 v140, v140, v156\nv_add_f32 v142, v142, v158\nM 4,180,240\nbuffer_load_dwordx4 v221, s[4:7], s63 offen offset:1024 lds\n"
"v_add_f32 v128, v128, v136\nM 20,176,228\nR 170,187,28672\ns_add_u32 s63, s52, 393216\nv_add_f32 v130, v130, v138\nM 20,180,244\n"
"s_add_u32 m0, s50, 90112\nv_add_f32 v132, v132, v140\nv_add_f32 v134, v134, v142\nv_add_f32 v128, v128, v132\nM 36,176,232\nR 172,187,32768\n"
"v_add_f32 v130, v130, v134\nv_lshrrev_b32 v231, 1, v252\nM 36,180,248\nbuffer_load_dwordx4 v220, s[4:7], s63 offen lds\nv_lshrrev_b32 v230, 2, v252\nM 52,176,236\n"
"R 174,187,36864\ns_lshl_b32 s60, s90, 14\ns_add_u32 s70, s49, s60\ns_xor_b32 s71, s70, 0x10000\ns_lshl_b32 s62, s40, 9\ns_add_u32 s62, s62, 159232\n"
"v_add_u32 v231, s62, v231\nM 52,180,252\nds_write2_b32 v231, v128, v130 offset1:1\nG 5\nM 8,160,224\nR 176,184,40960\n"
"s_lshl_b32 s62, s40, 9\ns_add_u32 s62, s62, 159232\nv_add_u32 v230, s62, v230\nM 8,164,240\nbuffer_load_dwordx4 v221, s[4:7], s63 offen offset:1024 lds\nds_read2st64_b32 v[232:233], v230 offset1:1\n"
"M 24,160,228\nR 178,184,45056\nM 24,164,244\ns_add_u32 m0, s50, 98304\nM 40,160,232\nR 180,184,49152\n"
"M 40,164,248\ns_add_u32 s63, s52, 589824\nM 56,160,236\nR 182,184,53248\nM 56,164,252\nG 6\n"
"G 3\nv_add_f32 v232, v232, v233\nv_mov_b32 v233, v232\ns_nop 1\nv_permlane32_swap_b32 v232, v233\nM 12,168,224\n"
"R 160,185,40960\nv_mov_b32 v230, v232\nv_mov_b32 v231, v233\nM 12,172,240\nbuffer_load_dwordx4 v220, s[4:7], s63 offen lds\nv_permlane16_swap_b32 v230, v232\n"
"M 28,168,228\nR 162,185,45056\nv_permlane16_swap_b32 v231, v233\nv_mul_f32 v230, 0xba800000, v230\nM 28,172,244\nv_mul_f32 v214, v230, v230\n"
"v_mul_f32 v231, 0x3a800000, v231\nv_sub_f32 v231, v231, v214\nM 44,168,232\nR 164,185,49152\nv_max_f32 v231, 0, v231\nv_add_f32 v231, 0x3727c5ac, v231\n"
"M 44,172,248\nbuffer_load_dwordx4 v221, s[4:7], s63 offen offset:1024 lds\nv_rsq_f32 v234, v231\nM 60,168,236\nR 166,185,53248\ns_mov_b32 s60, 156160\n"
"v_bfe_u32 v246, v252, 4, 4\nv_lshl_add_u32 v246, v246, 3, s60\nv_and_b32 v208, s77, v230\nM 60,172,252\nds_read_b64 v[238:239], v246 offset:0\ns_waitcnt vmcnt(8) lgkmcnt(0)\n"
"s_barrier\nv_mul_f32 v216, v231, v234\nN 64,192,176\nR 168,186,40960\nds_read_b64 v[240:241], v246 offset:128\nv_sub_f32 v230, v230, v208\n"
"N 64,208,180\nds_read_b64 v[242:243], v246 offset:256\ns_mov_b32 m0, s51\nv_cvt_pk_bf16_f32 v209, v230, v216\nN 68,196,176\nR 170,186,45056\n"
"s_lshl2_add_u32 s60, s45, s40\nv_lshlrev_b32 v224, 3, v252\ns_mul_i32 s60, s60, 7168\ns_bfm_b64 exec, 14, 0\nbuffer_load_dword v224, v224, s[12:15], s60 offen\ns_mov_b64 exec, -1\n"
"ds_read_b64 v[244:245], v246 offset:384\nds_write_addtid_b32 v32 offset:0\nv_cvt_pk_bf16_f32 v208, v208, v208\nN 68,212,180\nds_read_b64 v[246:247], v246 offset:0\nds_write_addtid_b32 v33 offset:256\n"
"v_mov_b32 v214, v234\nv_mul_f32 v215, 0x3e8293ee, v234\nN 96,200,176\nR 172,186,49152\nds_write_addtid_b32 v34 offset:512\nv_mul_f32 v232, 0xba800000, v232\n"
"v_mul_f32 v217, v232, v232\nN 96,216,180\nds_write_addtid_b32 v35 offset:768\nds_write_addtid_b32 v36 offset:1024\nv_mul_f32 v233, 0x3a800000, v233\nv_sub_f32 v233, v233, v217\n"
"N 100,204,176\nR 174,186,53248\nds_write_addtid_b32 v37 offset:1280\nv_max_f32 v233, 0, v233\nv_add_f32 v233, 0x3727c5ac, v233\nN 100,220,180\n"
"ds_write_addtid_b32 v38 offset:1536\nv_rsq_f32 v235, v233\nv_and_b32 v212, s77, v232\nv_mul_f32 v219, v233, v235\nv_sub_f32 v232, v232, v212\nv_cvt_pk_bf16_f32 v213, v232, v219\n"
"v_cvt_pk_bf16_f32 v212, v212, v212\nv_mov_b32 v217, v235\nv_mul_f32 v218, 0x3e8293ee, v235\ns_mov_b64 s[60:61], 0xffff\nv_cndmask_b32 v208, 0, v208, s[60:61]\ns_mov_b64 s[60:61], 0xffff\n"
"v_cndmask_b32 v209, 0, v209, s[60:61]\ns_mov_b64 s[60:61], 0xffff\nv_cndmask_b32 v212, 0, v212, s[60:61]\ns_mov_b64 s[60:61], 0xffff\nv_cndmask_b32 v213, 0, v213, s[60:61]\nv_bfe_u32 v230, v252, 2, 6\n"
"s_lshl_b32 s60, s40, 7\ns_add_u32 s60, s60, 161280\nv_add_u32 v230, s60, v230\nds_write_b32 v230, v214 offset:0\nds_write_b32 v230, v217 offset:64\nds_write_addtid_b32 v39 offset:1792\n"
"N 72,192,160\nR 176,187,40960\nds_write_addtid_b32 v40 offset:2048\nN 72,208,164\nds_write_addtid_b32 v41 offset:2304\nds_write_addtid_b32 v42 offset:2560\n"
"N 76,196,160\nR 178,187,45056\nds_write_addtid_b32 v43 offset:2816\nN 76,212,164\nds_write_addtid_b32 v44 offset:3072\nds_write_addtid_b32 v45 offset:3328\n"
"N 104,200,160\nR 180,187,49152\nG 15\nv_mfma_f32_16x16x32_bf16 v[0:3], v[238:241], v[208:211], v[0:3]\nds_write_addtid_b32 v46 offset:3584\nN 104,216,164\n"
"v_mfma_f32_16x16x32_bf16 v[4:7], v[240:243], v[208:211], v[4:7]\nds_write_addtid_b32 v47 offset:3840\nds_write_addtid_b32 v48 offset:4096\nN 108,204,160\nR 182,187,53248\nv_mfma_f32_16x16x32_bf16 v[8:11], v[242:245], v[208:211], v[8:11]\n"
"ds_write_addtid_b32 v49 offset:4352\nN 108,220,164\nv_mfma_f32_16x16x32_bf16 v[12:15], v[244:247], v[208:211], v[12:15]\nds_write_addtid_b32 v50 offset:4608\nds_write_addtid_b32 v51 offset:4864\nG 15\n"
"N 80,192,168\nR 160,184,57344\nv_mfma_f32_16x16x32_bf16 v[16:19], v[244:247], v[210:213], v[16:19]\nds_write_addtid_b32 v52 offset:5120\nN 80,208,172\nv_mfma_f32_16x16x32_bf16 v[20:23], v[238:241], v[210:213], v[20:23]\n"
"ds_write_addtid_b32 v53 offset:5376\nds_write_addtid_b32 v54 offset:5632\nN 84,196,168\nR 162,184,61440\nv_mfma_f32_16x16x32_bf16 v[24:27], v[240:243], v[210:213], v[24:27]\nds_write_addtid_b32 v55 offset:5888\n"
"N 84,212,172\nv_mfma_f32_16x16x32_bf16 v[28:31], v[242:245], v[210:213], v[28:31]\nds_write_addtid_b32 v56 offset:6144\nds_write_addtid_b32 v57 offset:6400\nN 112,200,168\nR 164,188,0\n"
"ds_write_addtid_b32 v58 offset:6656\nN 112,216,172\nds_write_addtid_b32 v59 offset:6912\nds_write_addtid_b32 v60 offset:7168\nN 116,204,168\nR 166,188,4096\n"
"ds_write_addtid_b32 v61 offset:7424\nN 116,220,172\nds_write_addtid_b32 v62 offset:7680\nds_write_addtid_b32 v63 offset:7936\nG 15\nN 88,192,176\n"
"R 168,185,57344\ns_add_u32 m0, s50, 106496\ns_add_u32 s63, s52, 786432\nN 88,208,180\nbuffer_load_dwordx4 v220, s[4:7], s63 offen lds\nv_mov_b32 v248, -1\n"
"v_mov_b32 v249, -1\nv_mov_b32 v250, -1\nv_mov_b32 v251, -1\nN 92,196,176\nR 170,185,61440\ns_waitcnt vmcnt(6) lgkmcnt(2)\n"
"s_barrier\nN 92,212,180\nds_read2st64_b32 v[32:33], v225 offset0:0 offset1:1\nds_read2st64_b32 v[34:35], v225 offset0:2 offset1:3\nN 120,200,176\nR 172,189,0\n"
"ds_read2st64_b32 v[36:37], v225 offset0:4 offset1:5\nN 120,216,180\nbuffer_load_dwordx4 v221, s[4:7], s63 offen offset:1024 lds\nds_read2st64_b32 v[38:39], v225 offset0:6 offset1:7\nN 124,204,176\nR 174,189,4096\n"
"ds_read2st64_b32 v[40:41], v225 offset0:8 offset1:9\nN 124,220,180\nds_read2st64_b32 v[42:43], v225 offset0:10 offset1:11\nds_read2st64_b32 v[44:45], v225 offset0:12 offset1:13\nN 64,224,160\nR 176,186,57344\n"
"s_mov_b32 s60, 156672\nv_bfe_u32 v246, v252, 4, 4\nv_lshl_add_u32 v246, v246, 3, s60\nds_read2st64_b32 v[46:47], v225 offset0:14 offset1:15\nN 64,240,164\nds_read_b64 v[238:239], v246 offset:0\n"
"G 11\nv_add_f32 v0, v0, v32\nv_add_f32 v1, v1, v33\nN 68,228,160\nR 178,186,61440\nds_read_b64 v[240:241], v246 offset:128\n"
"G 12\nv_add_f32 v2, v2, v34\nN 68,244,164\nds_read_b64 v[242:243], v246 offset:256\ns_add_u32 m0, s50, 114688\nv_add_f32 v3, v3, v35\n"
"N 96,232,160\nR 180,190,0\nds_read_b64 v[244:245], v246 offset:384\nG 13\nv_add_f32 v4, v4, v36\nN 96,248,164\n"
"ds_read_b64 v[246:247], v246 offset:0\ns_add_u32 s63, s52, 983040\nv_add_f32 v5, v5, v37\nN 100,236,160\nR 182,190,4096\nG 14\n"
"v_add_f32 v6, v6, v38\nv_add_f32 v7, v7, v39\nN 100,252,164\nds_read2st64_b32 v[48:49], v225 offset0:16 offset1:17\nG 14\nG 13\n"
"v_add_f32 v8, v8, v40\nN 72,224,168\nR 160,187,57344\nv_add_f32 v9, v9, v41\nG 13\nv_add_f32 v10, v10, v42\n"
"N 72,240,172\nbuffer_load_dwordx4 v220, s[4:7], s63 offen lds\nv_add_f32 v11, v11, v43\nN 76,228,168\nR 162,187,61440\nG 13\n"
"v_add_f32 v12, v12, v44\nv_add_f32 v13, v13, v45\nN 76,244,172\nds_read2st64_b32 v[50:51], v225 offset0:18 offset1:19\nG 12\nv_add_f32 v14, v14, v46\n"
"v_add_f32 v15, v15, v47\nN 104,232,168\nR 164,191,0\nds_read2st64_b32 v[52:53], v225 offset0:20 offset1:21\nN 104,248,172\nG 11\n"
"v_mfma_f32_16x16x32_bf16 v[64:67], v[208:211], v[238:241], v[64:67]\nbuffer_load_dwordx4 v221, s[4:7], s63 offen offset:1024 lds\nG 5\nv_add_f32 v16, v16, v48\nN 108,236,168\nR 166,191,4096\n"
"s_add_u32 m0, s50, 0\nv_add_f32 v17, v17, v49\nN 108,252,172\nv_mfma_f32_16x16x32_bf16 v[72:75], v[208:211], v[240:243], v[72:75]\nds_read2st64_b32 v[54:55], v225 offset0:22 offset1:23\nds_read2st64_b32 v[56:57], v225 offset0:24 offset1:25\n"
"N 80,224,176\nR 168,188,8192\ns_add_u32 s63, s52, 1179648\nds_read2st64_b32 v[58:59], v225 offset0:26 offset1:27\nN 80,240,180\nv_mfma_f32_16x16x32_bf16 v[80:83], v[208:211], v[242:245], v[80:83]\n"
"buffer_load_dwordx4 v220, s[4:7], s63 offen lds\nG 7\nv_add_f32 v18, v18, v50\nN 84,228,176\nR 170,188,12288\nv_add_f32 v19, v19, v51\n"
"G 6\nv_add_f32 v20, v20, v52\nN 84,244,180\nv_mfma_f32_16x16x32_bf16 v[88:91], v[208:211], v[244:247], v[88:91]\nbuffer_load_dwordx4 v221, s[4:7], s63 offen offset:1024 lds\nv_add_f32 v21, v21, v53\n"
"N 112,232,176\nR 172,188,16384\ns_add_u32 s63, s52, 1376256\nds_read2st64_b32 v[60:61], v225 offset0:28 offset1:29\nN 112,248,180\nv_mfma_f32_16x16x32_bf16 v[68:71], v[210:213], v[244:247], v[68:71]\n"
"s_add_u32 m0, s50, 8192\nds_read2st64_b32 v[62:63], v225 offset0:30 offset1:31\nG 7\nv_add_f32 v22, v22, v54\nN 116,236,176\nR 174,188,20480\n"
"v_add_f32 v23, v23, v55\nG 7\nv_add_f32 v24, v24, v56\nN 116,252,180\nv_mfma_f32_16x16x32_bf16 v[76:79], v[210:213], v[238:241], v[76:79]\nv_add_f32 v25, v25, v57\n"
"G 5\nv_add_f32 v26, v26, v58\nv_add_f32 v27, v27, v59\nN 88,224,160\nR 176,189,8192\nN 88,240,164\n"
"v_mfma_f32_16x16x32_bf16 v[84:87], v[210:213], v[240:243], v[84:87]\nbuffer_load_dwordx4 v220, s[4:7], s63 offen lds\nG 3\nv_add_f32 v28, v28, v60\nv_add_f32 v29, v29, v61\nG 2\n"
"v_add_f32 v30, v30, v62\nv_add_f32 v31, v31, v63\nN 92,228,160\nR 178,189,12288\nv_mul_f32 v16, v16, v217\nv_mul_f32 v17, v17, v217\n"
"N 92,244,164\nv_mfma_f32_16x16x32_bf16 v[92:95], v[210:213], v[242:245], v[92:95]\nbuffer_load_dwordx4 v221, s[4:7], s63 offen offset:1024 lds\nv_mul_f32 v18, v18, v217\nN 120,232,160\nR 180,189,16384\n"
"v_mul_f32 v19, v19, v217\nv_mul_f32 v20, v20, v217\nN 120,248,164\nv_mul_f32 v21, v21, v217\nv_mul_f32 v22, v22, v217\nv_mul_f32 v23, v23, v217\n"
"N 124,236,160\nR 182,189,20480\nv_mul_f32 v24, v24, v217\nv_mul_f32 v25, v25, v217\nN 124,252,164\ns_waitcnt vmcnt(9)\n"
"s_barrier\nG 4\nv_mul_f32 v26, v26, v217\nv_mfma_f32_16x16x32_bf16 v[128:131], v[168:171], a[0:3], 0\nR 160,190,8192\nv_mul_f32 v27, v27, v217\n"
"v_mul_f32 v28, v28, v217\nM 128,172,16\ns_mov_b32 m0, s51\nv_mul_f32 v29, v29, v217\nv_mul_f32 v30, v30, v217\nv_mul_f32 v31, v31, v217\n"
"v_mfma_f32_16x16x32_bf16 v[144:147], v[168:171], a[4:7], 0\nR 162,190,12288\ns_lshl2_add_u32 s60, s45, s40\ns_waitcnt vmcnt(8)\nv_lshlrev_b32 v224, 3, v252\ns_mul_i32 s60, s60, 7168\n"
"s_bfm_b64 exec, 14, 14\nbuffer_load_dword v224, v224, s[12:15], s60 offen\ns_mov_b64 exec, -1\nv_cvt_pk_bf16_f32 v16, v16, v17\nM 144,172,20\nv_cvt_pk_bf16_f32 v17, v18, v19\n"
"v_cvt_pk_bf16_f32 v18, v20, v21\nv_cvt_pk_bf16_f32 v19, v22, v23\nv_mfma_f32_16x16x32_bf16 v[32:35], v[168:171], a[8:11], 0\nR 164,190,16384\nv_cvt_pk_bf16_f32 v20, v24, v25\nv_cvt_pk_bf16_f32 v21, v26, v27\n"
"M 32,172,24\nv_cvt_pk_bf16_f32 v22, v28, v29\nv_cvt_pk_bf16_f32 v23, v30, v31\nv_mul_f32 v0, v0, v214\nv_mfma_f32_16x16x32_bf16 v[48:51], v[168:171], a[12:15], 0\nR 166,190,20480\n"
"v_mul_f32 v1, v1, v214\nv_mul_f32 v2, v2, v214\nM 48,172,28\nG 4\nv_mul_f32 v3, v3, v214\nv_mul_f32 v4, v4, v214\n"
"v_mul_f32 v5, v5, v214\nv_mfma_f32_16x16x32_bf16 v[132:135], v[176:179], a[0:3], 0\nR 168,191,8192\nv_mul_f32 v6, v6, v214\nv_mul_f32 v7, v7, v214\nM 132,180,16\n"
"ds_write_addtid_b32 v96 offset:0\nv_mul_f32 v8, v8, v214\nv_mul_f32 v9, v9, v214\nv_mul_f32 v10, v10, v214\nv_mfma_f32_16x16x32_bf16 v[148:151], v[176:179], a[4:7], 0\nR 170,191,12288\n"
"ds_write_addtid_b32 v97 offset:256\nv_mul_f32 v11, v11, v214\nv_mul_f32 v12, v12, v214\nM 148,180,20\nds_write_addtid_b32 v98 offset:512\nv_mul_f32 v13, v13, v214\n"
"v_mul_f32 v14, v14, v214\nv_mul_f32 v15, v15, v214\nv_mfma_f32_16x16x32_bf16 v[36:39], v[176:179], a[8:11], 0\nR 172,191,16384\nds_write_addtid_b32 v99 offset:768\nv_cvt_pk_bf16_f32 v0, v0, v1\n"
"v_cvt_pk_bf16_f32 v1, v2, v3\nM 36,180,24\ns_bfe_u32 s100, s58, 0x20007\ns_mul_i32 s100, s100, 49152\ns_add_u32 s100, s100, s72\ns_add_u32 s100, s100, 52461568\n"
"buffer_store_dwordx4 v[248:251], v252, s[20:23], s100 offen offset:0 sc1\nds_write_addtid_b32 v100 offset:1024\nv_cvt_pk_bf16_f32 v2, v4, v5\nv_mfma_f32_16x16x32_bf16 v[52:55], v[176:179], a[12:15], 0\nR 174,191,20480\nds_write_addtid_b32 v101 offset:1280\n"
"v_cvt_pk_bf16_f32 v3, v6, v7\nv_cvt_pk_bf16_f32 v4, v8, v9\nM 52,180,28\nds_write_addtid_b32 v102 offset:1536\nG 11\nv_cvt_pk_bf16_f32 v5, v10, v11\n"
"v_cvt_pk_bf16_f32 v6, v12, v13\nv_cvt_pk_bf16_f32 v7, v14, v15\nv_mfma_f32_16x16x32_bf16 v[136:139], v[160:163], a[0:3], 0\nR 176,188,24576\nds_write_addtid_b32 v103 offset:1792\nM 136,164,16\n"
"ds_write_addtid_b32 v104 offset:2048\nds_write_addtid_b32 v105 offset:2304\nv_mfma_f32_16x16x32_bf16 v[152:155], v[160:163], a[4:7], 0\nR 178,188,28672\nds_write_addtid_b32 v106 offset:2560\nM 152,164,20\n"
"ds_write_addtid_b32 v107 offset:2816\nds_write_addtid_b32 v108 offset:3072\nv_mfma_f32_16x16x32_bf16 v[40:43], v[160:163], a[8:11], 0\nR 180,188,32768\nds_write_addtid_b32 v109 offset:3328\nM 40,164,24\n"
"ds_write_addtid_b32 v110 offset:3584\nds_write_addtid_b32 v111 offset:3840\nv_mfma_f32_16x16x32_bf16 v[56:59], v[160:163], a[12:15], 0\nR 182,188,36864\nds_write_addtid_b32 v112 offset:4096\nM 56,164,28\n"
"ds_write_addtid_b32 v113 offset:4352\nds_write_addtid_b32 v114 offset:4608\nG 15\nv_mfma_f32_16x16x32_bf16 v[140:143], v[168:171], a[0:3], 0\nR 160,189,24576\nds_write_addtid_b32 v115 offset:4864\n"
"M 140,172,16\nds_write_addtid_b32 v116 offset:5120\nds_write_addtid_b32 v117 offset:5376\nv_mfma_f32_16x16x32_bf16 v[156:159], v[168:171], a[4:7], 0\nR 162,189,28672\nds_write_addtid_b32 v118 offset:5632\n"
"M 156,172,20\nds_write_addtid_b32 v119 offset:5888\nds_write_addtid_b32 v120 offset:6144\nv_mfma_f32_16x16x32_bf16 v[44:47], v[168:171], a[8:11], 0\nR 164,189,32768\nds_write_addtid_b32 v121 offset:6400\n"
"M 44,172,24\nds_write_addtid_b32 v122 offset:6656\nds_write_addtid_b32 v123 offset:6912\nv_mfma_f32_16x16x32_bf16 v[60:63], v[168:171], a[12:15], 0\nR 166,189,36864\nds_write_addtid_b32 v124 offset:7168\n"
"M 60,172,28\nds_write_addtid_b32 v125 offset:7424\nds_write_addtid_b32 v126 offset:7680\nds_write_addtid_b32 v127 offset:7936\nG 15\nM 128,176,32\n"
"R 168,190,24576\ns_add_u32 m0, s50, 16384\ns_add_u32 s63, s52, 1572864\nbuffer_load_dwordx4 v220, s[4:7], s63 offen lds\nM 128,180,48\nbuffer_load_dwordx4 v221, s[4:7], s63 offen offset:1024 lds\n"
"M 144,176,36\nR 170,190,28672\ns_waitcnt vmcnt(8) lgkmcnt(2)\ns_barrier\ns_add_u32 m0, s50, 24576\ns_add_u32 s63, s52, 1769472\n"
"M 144,180,52\nbuffer_load_dwordx4 v220, s[4:7], s63 offen lds\nds_read2st64_b32 v[96:97], v225 offset0:0 offset1:1\nv_lshrrev_b32 v226, 8, v252\nM 32,176,40\nR 172,190,32768\n"
"buffer_load_dwordx4 v221, s[4:7], s63 offen offset:1024 lds\nds_read2st64_b32 v[98:99], v225 offset0:2 offset1:3\ns_lshl_b32 s60, s40, 7\ns_add_u32 s60, s60, 161280\nv_lshl_add_u32 v226, v226, 4, s60\nM 32,180,56\n"
"ds_read2st64_b32 v[100:101], v225 offset0:4 offset1:5\nds_read2st64_b32 v[102:103], v225 offset0:6 offset1:7\nds_read_b128 v[230:233], v226 offset:0\nM 48,176,44\nR 174,190,36864\nds_read2st64_b32 v[104:105], v225 offset0:8 offset1:9\n"
"ds_read_b128 v[234:237], v226 offset:64\nM 48,180,60\nds_read2st64_b32 v[106:107], v225 offset0:10 offset1:11\nds_read2st64_b32 v[108:109], v225 offset0:12 offset1:13\nM 132,160,32\nR 176,191,24576\n"
"ds_read2st64_b32 v[110:111], v225 offset0:14 offset1:15\nM 132,164,48\nds_read2st64_b32 v[112:113], v225 offset0:16 offset1:17\nG 13\nv_add_f32 v64, v64, v96\nv_add_f32 v65, v65, v97\n"
"M 148,160,36\nR 178,191,28672\nG 12\nv_add_f32 v66, v66, v98\nv_add_f32 v67, v67, v99\nM 148,164,52\n"
"s_add_u32 m0, s50, 32768\nds_read2st64_b32 v[114:115], v225 offset0:18 offset1:19\nG 12\nv_add_f32 v68, v68, v100\nM 36,160,40\nR 180,191,32768\n"
"v_add_f32 v69, v69, v101\nG 12\nv_add_f32 v70, v70, v102\nM 36,164,56\ns_add_u32 s63, s52, 1966080\nds_read2st64_b32 v[116:117], v225 offset0:20 offset1:21\n"
"v_add_f32 v71, v71, v103\nM 52,160,44\nR 182,191,36864\nG 11\nv_add_f32 v72, v72, v104\nv_add_f32 v73, v73, v105\n"
"M 52,164,60\nds_read2st64_b32 v[118:119], v225 offset0:22 offset1:23\nG 10\nv_add_f32 v74, v74, v106\nM 136,168,32\nR 160,188,40960\n"
"v_add_f32 v75, v75, v107\nG 10\nv_add_f32 v76, v76, v108\nM 136,172,48\nbuffer_load_dwordx4 v220, s[4:7], s63 offen lds\nv_add_f32 v77, v77, v109\n"
"M 152,168,36\nR 162,188,45056\nG 9\nv_add_f32 v78, v78, v110\nv_add_f32 v79, v79, v111\nM 152,172,52\n"
"ds_read2st64_b32 v[120:121], v225 offset0:24 offset1:25\nG 9\nv_add_f32 v80, v80, v112\nv_add_f32 v81, v81, v113\nM 40,168,40\nR 164,188,49152\n"
"G 8\nv_add_f32 v82, v82, v114\nv_add_f32 v83, v83, v115\nM 40,172,56\nbuffer_load_dwordx4 v221, s[4:7], s63 offen offset:1024 lds\nM 56,168,44\n"
"R 166,188,53248\ns_add_u32 m0, s50, 40960\nM 56,172,60\nds_read2st64_b32 v[122:123], v225 offset0:26 offset1:27\nG 7\nv_add_f32 v84, v84, v116\n"
"M 140,176,32\nR 168,189,40960\ns_add_u32 s63, s52, 2162688\nM 140,180,48\nbuffer_load_dwordx4 v220, s[4:7], s63 offen lds\nM 156,176,36\n"
"R 170,189,45056\nv_add_f32 v85, v85, v117\nM 156,180,52\nds_read2st64_b32 v[124:125], v225 offset0:28 offset1:29\nG 9\nv_add_f32 v86, v86, v118\n"
"M 44,176,40\nR 172,189,49152\nv_add_f32 v87, v87, v119\nM 44,180,56\nbuffer_load_dwordx4 v221, s[4:7], s63 offen offset:1024 lds\nM 60,176,44\n"
"R 174,189,53248\ns_add_u32 m0, s50, 49152\nM 60,180,60\nds_read2st64_b32 v[126:127], v225 offset0:30 offset1:31\nG 7\nv_add_f32 v88, v88, v120\n"
"M 128,160,64\nR 176,190,40960\ns_add_u32 s63, s52, 2359296\nM 128,164,80\nbuffer_load_dwordx4 v220, s[4:7], s63 offen lds\nM 144,160,68\n"
"R 178,190,45056\nv_add_f32 v89, v89, v121\nM 144,164,84\nbuffer_store_dwordx4 v[248:251], v252, s[20:23], s100 offen offset:1024 sc1\nM 32,160,72\nR 180,190,49152\n"
"G 9\nv_add_f32 v90, v90, v122\nM 32,164,88\nv_add_f32 v91, v91, v123\nG 6\nv_add_f32 v92, v92, v124\n"
"M 48,160,76\nR 182,190,53248\nv_add_f32 v93, v93, v125\nM 48,164,92\nG 5\nG 4\n"
"v_add_f32 v94, v94, v126\nv_add_f32 v95, v95, v127\nM 132,168,64\nR 160,191,40960\nv_mul_f32 v64, v64, v230\nv_mul_f32 v65, v65, v231\n"
"M 132,172,80\ns_lshl4_add_u32 s64, s41, s42\nv_mul_f32 v66, v66, v232\nv_mul_f32 v67, v67, v233\nv_mul_f32 v68, v68, v234\nv_add_u32 v108, 161792, v252\n"
"M 148,168,68\nR 162,191,45056\nds_read_b128 v[112:115], v108\nv_mul_f32 v69, v69, v235\nv_mul_f32 v70, v70, v236\nM 148,172,84\n"
"buffer_load_dwordx4 v221, s[4:7], s63 offen offset:1024 lds\nds_read_b128 v[104:107], v108 offset:1024\nv_mul_f32 v71, v71, v237\nM 36,168,72\nR 164,191,49152\nv_cvt_pk_bf16_f32 v67, v66, v67\n"
"v_cvt_pk_bf16_f32 v66, v64, v65\nM 36,172,88\ns_add_u32 m0, s50, 57344\ns_cmp_eq_u32 s40, 3\ns_cselect_b64 s[66:67], -1, 0\nv_cvt_pk_bf16_f32 v68, v68, v69\n"
"M 52,168,76\nR 166,191,53248\ns_waitcnt vmcnt(7)\ns_barrier\nv_cvt_pk_bf16_f32 v69, v70, v71\nM 52,172,92\n"
"v_mul_f32 v72, v72, v230\nv_mul_f32 v73, v73, v231\nG 6\nv_mul_f32 v74, v74, v232\nM 136,176,64\nR 168,184,0\n"
"s_lshl2_add_u32 s60, s45, s40\nv_lshlrev_b32 v224, 3, v252\ns_mul_i32 s60, s60, 7168\ns_bfm_b64 exec, 14, 28\nbuffer_load_dword v224, v224, s[12:15], s60 offen\ns_mov_b64 exec, -1\n"
"v_mul_f32 v75, v75, v233\nv_mov_b32 v124, s76\nM 136,180,80\ns_add_u32 s63, s52, 2555904\nv_mul_f32 v76, v76, v234\nv_mul_f32 v77, v77, v235\n"
"v_mul_f32 v78, v78, v236\nG 3\nv_lshlrev_b32 v100, 16, v104\nM 152,176,68\nR 170,184,4096\nv_mul_f32 v79, v79, v237\n"
"v_cvt_pk_bf16_f32 v72, v72, v73\nv_and_b32 v101, s77, v104\nM 152,180,84\nbuffer_load_dwordx4 v220, s[4:7], s63 offen lds\nv_cvt_pk_bf16_f32 v73, v74, v75\nv_lshlrev_b32 v102, 16, v105\n"
"M 40,176,72\nR 172,184,8192\nv_cvt_pk_bf16_f32 v74, v76, v77\nv_cvt_pk_bf16_f32 v75, v78, v79\nv_and_b32 v103, s77, v105\nM 40,180,88\n"
"s_lshl_b32 s64, s64, 12\nv_mul_f32 v80, v80, v230\nv_mul_f32 v81, v81, v231\nv_mul_f32 v82, v82, v232\nv_lshlrev_b32 v96, 16, v106\nM 56,176,76\n"
"R 174,184,12288\nv_mul_f32 v83, v83, v233\nv_mul_f32 v84, v84, v234\nv_and_b32 v97, s77, v106\nM 56,180,92\nG 4\n"
"v_mul_f32 v85, v85, v235\nv_mul_f32 v86, v86, v236\nv_mul_f32 v87, v87, v237\nv_lshlrev_b32 v98, 16, v107\nM 140,160,64\nR 176,185,0\n"
"v_cvt_pk_bf16_f32 v78, v80, v81\nv_cvt_pk_bf16_f32 v79, v82, v83\nv_and_b32 v99, s77, v107\nM 140,164,80\ns_add_u32 s64, s64, 77594624\nv_cvt_pk_bf16_f32 v80, v84, v85\n"
"v_cvt_pk_bf16_f32 v81, v86, v87\nv_mul_f32 v88, v88, v230\nv_add_f32 v104, s78, v100\nM 156,160,68\nR 178,185,4096\nv_mul_f32 v89, v89, v231\n"
"v_mul_f32 v90, v90, v232\nv_add_f32 v105, s78, v101\nM 156,164,84\nbuffer_load_dwordx4 v221, s[4:7], s63 offen offset:1024 lds\nv_mul_f32 v91, v91, v233\nv_add_f32 v106, s78, v102\n"
"M 44,160,72\nR 180,185,8192\ns_add_u32 s63, s52, 2752512\nv_mul_f32 v92, v92, v234\nv_add_f32 v107, s78, v103\nM 44,164,88\n"
"s_add_u32 m0, s50, 65536\nv_mul_f32 v93, v93, v235\nv_mul_f32 v94, v94, v236\nv_mul_f32 v95, v95, v237\nv_mov_b32 v125, s76\nM 60,160,76\n"
"R 182,185,12288\nv_cvt_pk_bf16_f32 v84, v88, v89\nv_cvt_pk_bf16_f32 v85, v90, v91\nv_mov_b32 v126, s76\nM 60,164,92\nG 4\n"
"v_cvt_pk_bf16_f32 v86, v92, v93\nv_cvt_pk_bf16_f32 v87, v94, v95\nv_lshrrev_b32 v232, 1, v252\nM 128,168,96\nR 160,186,0\ns_mov_b64 exec, s[66:67]\n"
"buffer_store_dwordx4 v[16:19], v252, s[20:23], s64 offen offset:0 sc1\nbuffer_store_dwordx4 v[20:23], v252, s[20:23], s64 offen offset:1024 sc1\ns_mov_b64 exec, -1\nv_mov_b32 v127, s76\nM 128,172,112\nbuffer_load_dwordx4 v220, s[4:7], s63 offen lds\n"
"s_mov_b64 exec, s[66:67]\nbuffer_store_dwordx2 v[68:69], v232, s[20:23], s64 offen offset:2048 sc1\nbuffer_store_dwordx2 v[74:75], v232, s[20:23], s64 offen offset:2560 sc1\ns_mov_b64 exec, -1\nM 144,168,100\nR 162,186,4096\n"
"s_mov_b64 exec, s[66:67]\nbuffer_store_dwordx2 v[80:81], v232, s[20:23], s64 offen offset:3072 sc1\nbuffer_store_dwordx2 v[86:87], v232, s[20:23], s64 offen offset:3584 sc1\ns_mov_b64 exec, -1\nM 144,172,116\ns_lshl_b32 s60, s40, 12\n"
"s_add_u32 s60, s60, 118784\nv_add_u32 v230, s60, v252\nv_add_u32 v231, s60, v232\nM 32,168,104\nR 164,186,8192\ns_not_b64 exec, s[66:67]\n"
"ds_write_b128 v230, v[16:19] offset:0\nds_write_b128 v230, v[20:23] offset:1024\ns_mov_b64 exec, -1\nM 32,172,120\nM 48,168,108\nR 166,186,12288\n"
"M 48,172,124\nG 6\nM 132,176,96\nR 168,187,0\nM 132,180,112\nbuffer_load_dwordx4 v221, s[4:7], s63 offen offset:1024 lds\n"
"M 148,176,100\nR 170,187,4096\nM 148,180,116\ns_add_u32 m0, s50, 73728\nM 36,176,104\nR 172,187,8192\n"
"Lq201:\ns_lshr_b32 s60, s45, 3\ns_lshl2_add_u32 s60, s60, s40\ns_lshl_b32 s60, s60, 8\ns_waitcnt vmcnt(10)\nv_lshl_add_u32 v224, v252, 3, s60\n"
"v_add_u32 v224, Lq201+4096-LqPC, v224\ns_mov_b64 exec, 3\nglobal_load_dword v224, v224, s[98:99]\ns_mov_b64 exec, -1\nM 36,180,120\ns_add_u32 s63, s52, 2949120\n"
"M 52,176,108\nR 174,187,12288\nM 52,180,124\nG 4\nM 136,160,96\nR 176,184,16384\n"
"s_not_b64 exec, s[66:67]\nds_write_b64 v231, v[68:69] offset:2048\nds_write_b64 v231, v[74:75] offset:2560\ns_mov_b64 exec, -1\nM 136,164,112\nbuffer_load_dwordx4 v220, s[4:7], s63 offen lds\n"
"M 152,160,100\nR 178,184,20480\nM 152,164,116\nM 40,160,104\nR 180,184,24576\nM 40,164,120\n"
"M 56,160,108\nR 182,184,28672\nM 56,164,124\ns_not_b64 exec, s[66:67]\nds_write_b64 v231, v[80:81] offset:3072\nds_write_b64 v231, v[86:87] offset:3584\n"
"s_mov_b64 exec, -1\nG 8\nM 140,168,96\nR 160,185,16384\nM 140,172,112\nbuffer_store_dwordx4 v[248:251], v252, s[20:23], s100 offen offset:2048 sc1\n"
"M 156,168,100\nR 162,185,20480\nM 156,172,116\nM 44,168,104\nR 164,185,24576\nM 44,172,120\n"
"buffer_load_dwordx4 v221, s[4:7], s63 offen offset:1024 lds\nM 60,168,108\nR 166,185,28672\nM 60,172,124\nG 6\nM 128,176,128\n"
"R 168,186,16384\nM 128,180,144\ns_movk_i32 s63, 2048\nv_mad_u32_u24 v220, v228, s63, v229\nv_add_u32 v221, 1047552, v220\ns_add_u32 m0, s50, 98304\n"
"s_add_u32 s63, s53, 0\nbuffer_load_dwordx4 v220, s[8:11], s63 offen lds\nM 144,176,132\nR 170,186,20480\nM 144,180,148\nM 32,176,136\n"
"R 172,186,24576\nG 7\ns_barrier\nM 32,180,152\ns_lshl4_add_u32 s65, s41, s42\nv_mov_b32 v230, 0\n"
"v_mov_b32 v231, 0\nM 48,176,140\nR 174,186,28672\nv_mov_b32 v232, 0\nM 48,180,156\nG 4\n"
"v_mov_b32 v233, 0\nv_mov_b32 v234, 0\nM 132,160,128\nR 176,187,16384\nv_mov_b32 v235, 0\nM 132,164,144\n"
"buffer_load_dwordx4 v221, s[8:11], s63 offen offset:1024 lds\nM 148,160,132\nR 178,187,20480\nv_mov_b32 v236, 0\nM 148,164,148\ns_add_u32 m0, s50, 106496\n"
"v_mov_b32 v237, 0\nv_mov_b32 v64, 0\nM 36,160,136\nR 180,187,24576\nv_mov_b32 v65, 0\nM 36,164,152\n"
"s_add_u32 s63, s53, 65536\nv_mov_b32 v70, 0\nv_mov_b32 v71, 0\nM 52,160,140\nR 182,187,28672\nv_mov_b32 v76, 0\n"
"M 52,164,156\nG 4\nv_mov_b32 v77, 0\nv_mov_b32 v82, 0\nM 136,168,128\nR 160,184,32768\n"
"v_mov_b32 v83, 0\nM 136,172,144\nbuffer_load_dwordx4 v220, s[8:11], s63 offen lds\nM 152,168,132\nR 162,184,36864\nv_lshrrev_b32 v227, 1, v252\n"
"M 152,172,148\ns_lshl_b32 s65, s65, 12\ns_add_u32 s65, s65, 77529088\nM 40,168,136\nR 164,184,40960\ns_lshl2_add_u32 s60, s45, s40\n"
"s_waitcnt vmcnt(6)\nv_lshlrev_b32 v224, 3, v252\ns_mul_i32 s60, s60, 7168\ns_bfm_b64 exec, 14, 42\nbuffer_load_dword v224, v224, s[12:15], s60 offen\ns_mov_b64 exec, -1\n"
"M 40,172,152\nM 56,168,140\nR 166,184,45056\nM 56,172,156\nG 4\nM 140,176,128\n"
"R 168,185,32768\nM 140,180,144\nbuffer_load_dwordx4 v221, s[8:11], s63 offen offset:1024 lds\nM 156,176,132\nR 170,185,36864\ns_add_u32 s63, s53, 131072\n"
"M 156,180,148\ns_add_u32 m0, s50, 114688\nM 44,176,136\nR 172,185,40960\nM 44,180,152\nbuffer_load_dwordx4 v220, s[8:11], s63 offen lds\n"
"M 60,176,140\nR 174,185,45056\nM 60,180,156\nG 4\nM 128,160,160\nR 176,186,32768\n"
"M 128,164,176\nbuffer_load_dwordx4 v221, s[8:11], s63 offen offset:1024 lds\nM 144,160,164\nR 178,186,36864\ns_add_u32 s63, s53, 196608\nM 144,164,180\n"
"s_add_u32 m0, s50, 0\nM 32,160,168\nR 180,186,40960\ns_mov_b32 s60, 155648\nv_bfe_u32 v246, v252, 4, 4\nv_lshl_add_u32 v246, v246, 3, s60\n"
"M 32,164,184\nbuffer_load_dwordx4 v220, s[8:11], s63 offen lds\nM 48,160,172\nR 182,186,45056\nds_read_b64 v[238:239], v246 offset:0\nM 48,164,188\n"
"s_lshl_b32 s60, s40, 12\ns_add_u32 s60, s60, 114688\nv_add_u32 v226, s60, v252\nv_add_u32 v111, s60, v227\nG 5\nM 132,168,160\n"
"R 160,187,32768\nds_read_b64 v[240:241], v246 offset:128\nM 132,172,176\ns_cmp_eq_u32 s40, 0\ns_cselect_b64 s[88:89], 0, -1\ns_cselect_b32 s60, s41, 0\n"
"s_cmp_lg_u32 s60, 0\ns_cselect_b64 s[86:87], -1, 0\nM 148,168,164\nR 162,187,36864\nds_read_b64 v[242:243], v246 offset:256\nM 148,172,180\n"
"buffer_load_dwordx4 v221, s[8:11], s63 offen offset:1024 lds\nM 36,168,168\nR 164,187,40960\nds_read_b64 v[244:245], v246 offset:384\nM 36,172,184\ns_mov_b64 exec, s[88:89]\n"
"ds_read_b128 v[230:233], v226 offset:0\nds_read_b128 v[234:237], v226 offset:1024\ns_mov_b64 exec, -1\nM 52,168,172\nR 166,187,45056\nds_read_b64 v[246:247], v246 offset:0\n"
"M 52,172,188\nG 11\nM 136,176,160\nR 168,184,49152\nM 136,180,176\nbuffer_store_dwordx4 v[248:251], v252, s[20:23], s100 offen offset:3072 sc1\n"
"M 152,176,164\nR 170,184,53248\ns_waitcnt vmcnt(10) lgkmcnt(2)\ns_barrier\nM 152,180,180\ns_mov_b64 exec, s[88:89]\n"
"ds_read_b64 v[64:65], v111 offset:2048\nds_read_b64 v[70:71], v111 offset:2560\ns_mov_b64 exec, -1\nM 40,176,168\nR 172,184,57344\nv_mfma_f32_16x16x32_bf16 v[128:131], v[238:241], v[208:211], v[128:131]\n"
"s_add_u32 m0, s50, 8192\nM 40,180,184\ns_mov_b64 exec, s[88:89]\nds_read_b64 v[76:77], v111 offset:3072\nds_read_b64 v[82:83], v111 offset:3584\ns_mov_b64 exec, -1\n"
"M 56,176,172\nR 174,184,61440\nv_mfma_f32_16x16x32_bf16 v[132:135], v[240:243], v[208:211], v[132:135]\ns_add_u32 s63, s53, 262144\nM 56,180,188\nM 140,160,160\n"
"R 176,185,49152\nv_mfma_f32_16x16x32_bf16 v[136:139], v[242:245], v[208:211], v[136:139]\nM 140,164,176\nbuffer_load_dwordx4 v220, s[8:11], s63 offen lds\nM 156,160,164\nR 178,185,53248\n"
"v_mfma_f32_16x16x32_bf16 v[140:143], v[244:247], v[208:211], v[140:143]\nM 156,164,180\nM 44,160,168\nR 180,185,57344\nv_mfma_f32_16x16x32_bf16 v[144:147], v[244:247], v[210:213], v[144:147]\nM 44,164,184\n"
"s_mov_b64 exec, s[86:87]\nbuffer_load_dwordx4 v[230:233], v252, s[20:23], s65 offen offset:0 sc1\nbuffer_load_dwordx4 v[234:237], v252, s[20:23], s65 offen offset:1024 sc1\ns_mov_b64 exec, -1\nM 60,160,172\nR 182,185,61440\n"
"v_mfma_f32_16x16x32_bf16 v[148:151], v[238:241], v[210:213], v[148:151]\nM 60,164,188\nG 4\nM 32,168,200\nR 160,186,49152\nv_mfma_f32_16x16x32_bf16 v[152:155], v[240:243], v[210:213], v[152:155]\n"
"M 32,172,216\nR 162,186,53248\nM 48,168,204\nR 164,186,57344\nv_mfma_f32_16x16x32_bf16 v[156:159], v[242:245], v[210:213], v[156:159]\ns_mov_b64 exec, s[86:87]\n"
"buffer_load_dwordx2 v[64:65], v227, s[20:23], s65 offen offset:2048 sc1\nbuffer_load_dwordx2 v[70:71], v227, s[20:23], s65 offen offset:2560 sc1\ns_mov_b64 exec, -1\nM 48,172,220\nR 166,186,61440\nG 4\n"
"M 36,176,200\nR 168,187,49152\nM 36,180,216\nR 170,187,53248\nM 52,176,204\nR 172,187,57344\n"
"M 52,180,220\nR 174,187,61440\nG 4\nM 40,160,200\nR 176,188,0\ns_mov_b64 exec, s[86:87]\n"
"buffer_load_dwordx2 v[76:77], v227, s[20:23], s65 offen offset:3072 sc1\nbuffer_load_dwordx2 v[82:83], v227, s[20:23], s65 offen offset:3584 sc1\ns_mov_b64 exec, -1\nM 40,164,216\nR 178,188,4096\nM 56,160,204\n"
"R 180,188,8192\nM 56,164,220\nR 182,188,12288\nG 4\nM 44,168,200\nR 160,189,0\n"
"M 44,172,216\nR 162,189,4096\nM 60,168,204\nR 164,189,8192\nM 60,172,220\nR 166,189,12288\n"
"G 4\nM 32,176,232\nR 168,190,0\nM 32,180,248\nR 170,190,4096\nM 48,176,236\n"
"R 172,190,8192\nM 48,180,252\nR 174,190,12288\nG 4\nM 36,160,232\nR 176,191,0\n"
"M 36,164,248\nR 178,191,4096\nM 52,160,236\nR 180,191,8192\nM 52,164,252\nR 182,191,12288\n"
"G 4\nM 40,168,232\nR 160,184,49152\nM 40,172,248\nR 162,184,53248\nM 56,168,236\n"
"R 164,184,57344\nM 56,172,252\nR 166,184,61440\nG 4\nM 44,176,232\nR 168,185,49152\n"
"M 44,180,248\nR 170,185,53248\nM 60,176,236\nR 172,185,57344\nM 60,180,252\nR 174,185,61440\n"
"G 4\nM 128,160,192\nR 176,186,49152\nM 128,164,208\nR 178,186,53248\ns_mov_b32 m0, s51\n"
"M 144,160,196\nR 180,186,57344\nds_write_addtid_b32 v32 offset:0\nM 144,164,212\nR 182,186,61440\nds_write_addtid_b32 v33 offset:256\n"
"G 6\nM 132,168,192\nR 160,187,49152\nds_write_addtid_b32 v34 offset:512\nM 132,172,208\nR 162,187,53248\n"
"ds_write_addtid_b32 v35 offset:768\nM 148,168,196\nR 164,187,57344\nds_write_addtid_b32 v36 offset:1024\nM 148,172,212\nR 166,187,61440\n"
"ds_write_addtid_b32 v37 offset:1280\nG 9\nM 136,176,192\nR 168,188,0\nds_write_addtid_b32 v38 offset:1536\nM 136,180,208\n"
"R 170,188,4096\nds_write_addtid_b32 v39 offset:1792\nM 152,176,196\nR 172,188,8192\nds_write_addtid_b32 v40 offset:2048\nM 152,180,212\n"
"R 174,188,12288\nds_write_addtid_b32 v41 offset:2304\nG 9\nM 140,160,192\nR 176,189,0\nds_write_addtid_b32 v42 offset:2560\n"
"M 140,164,208\nR 178,189,4096\nds_write_addtid_b32 v43 offset:2816\nM 156,160,196\nR 180,189,8192\nds_write_addtid_b32 v44 offset:3072\n"
"ds_write_addtid_b32 v45 offset:3328\nds_write_addtid_b32 v46 offset:3584\nds_write_addtid_b32 v47 offset:3840\nds_write_addtid_b32 v48 offset:4096\nds_write_addtid_b32 v49 offset:4352\nds_write_addtid_b32 v50 offset:4608\n"
"ds_write_addtid_b32 v51 offset:4864\nds_write_addtid_b32 v52 offset:5120\nds_write_addtid_b32 v53 offset:5376\nds_write_addtid_b32 v54 offset:5632\nds_write_addtid_b32 v55 offset:5888\nds_write_addtid_b32 v56 offset:6144\n"
"ds_write_addtid_b32 v57 offset:6400\nds_write_addtid_b32 v58 offset:6656\nds_write_addtid_b32 v59 offset:6912\nds_write_addtid_b32 v60 offset:7168\nds_write_addtid_b32 v61 offset:7424\nds_write_addtid_b32 v62 offset:7680\n"
"ds_write_addtid_b32 v63 offset:7936\nM 156,164,212\nR 182,189,12288\ns_add_u32 m0, s50, 8192\ns_nop 0\nbuffer_load_dwordx4 v221, s[8:11], s63 offen offset:1024 lds\n"
"G 15\nM 128,168,224\nR 160,190,0\ns_add_u32 m0, s50, 16384\ns_add_u32 s63, s53, 327680\nbuffer_load_dwordx4 v220, s[8:11], s63 offen lds\n"
"M 128,172,240\nR 162,190,4096\nG 3\ns_barrier\nM 144,168,228\nR 164,190,8192\n"
"buffer_load_dwordx4 v221, s[8:11], s63 offen offset:1024 lds\nM 144,172,244\nR 166,190,12288\nG 4\nM 132,176,224\nR 168,191,0\n"
"M 132,180,240\nR 170,191,4096\nds_read2st64_b32 v[32:33], v225 offset0:0 offset1:1\nM 148,176,228\nR 172,191,8192\nds_read2st64_b32 v[34:35], v225 offset0:2 offset1:3\n"
"M 148,180,244\nR 174,191,12288\nG 6\nM 136,160,224\ns_add_u32 m0, s50, 24576\nds_read2st64_b32 v[36:37], v225 offset0:4 offset1:5\n"
"ds_read2st64_b32 v[38:39], v225 offset0:6 offset1:7\nM 136,164,240\ns_add_u32 s63, s53, 393216\nds_read2st64_b32 v[40:41], v225 offset0:8 offset1:9\nds_read2st64_b32 v[42:43], v225 offset0:10 offset1:11\nM 152,160,228\n"
"ds_read2st64_b32 v[44:45], v225 offset0:12 offset1:13\nds_read2st64_b32 v[46:47], v225 offset0:14 offset1:15\nM 152,164,244\nds_read2st64_b32 v[48:49], v225 offset0:16 offset1:17\nds_read2st64_b32 v[50:51], v225 offset0:18 offset1:19\nG 8\n"
"M 140,168,224\nbuffer_load_dwordx4 v220, s[8:11], s63 offen lds\nM 140,172,240\nds_read2st64_b32 v[52:53], v225 offset0:20 offset1:21\nds_read2st64_b32 v[54:55], v225 offset0:22 offset1:23\nM 156,168,228\n"
"ds_read2st64_b32 v[56:57], v225 offset0:24 offset1:25\nds_read2st64_b32 v[58:59], v225 offset0:26 offset1:27\nM 156,172,244\nds_read2st64_b32 v[60:61], v225 offset0:28 offset1:29\nds_read2st64_b32 v[62:63], v225 offset0:30 offset1:31\nG 0\n"
"s_barrier\ns_waitcnt vmcnt(7)\nv_max3_u32 v248, v233, v237, v65\ns_waitcnt vmcnt(5)\nv_max3_u32 v248, v248, v71, v77\ns_waitcnt vmcnt(4)\n"
"v_max_u32 v248, v248, v83\nv_cmp_eq_u32 vcc, -1, v248\ns_nop 1\ns_cbranch_vccnz Lq175\nLq176:\nv_pk_add_f32 v[144:145], v[144:145], v[48:49]\n"
"v_pk_add_f32 v[146:147], v[146:147], v[50:51]\nv_pk_add_f32 v[148:149], v[148:149], v[52:53]\nv_pk_add_f32 v[150:151], v[150:151], v[54:55]\nv_cvt_pk_bf16_f32 v144, v144, v145\nv_cvt_pk_bf16_f32 v145, v146, v147\nv_cvt_pk_bf16_f32 v146, v148, v149\n"
"v_cvt_pk_bf16_f32 v147, v150, v151\ns_nop 1\nv_mfma_f32_16x16x32_bf16 v[52:55], v[16:19], v[144:147], v[96:99]\nv_mfma_f32_16x16x32_bf16 v[48:51], v[0:3], v[144:147], v[100:103]\nv_pk_add_f32 v[152:153], v[152:153], v[56:57]\nv_pk_add_f32 v[154:155], v[154:155], v[58:59]\n"
"v_pk_add_f32 v[156:157], v[156:157], v[60:61]\nv_pk_add_f32 v[158:159], v[158:159], v[62:63]\nv_cvt_pk_bf16_f32 v152, v152, v153\nv_cvt_pk_bf16_f32 v153, v154, v155\nv_cvt_pk_bf16_f32 v154, v156, v157\nv_cvt_pk_bf16_f32 v155, v158, v159\n"
"s_nop 1\nv_mfma_f32_16x16x32_bf16 v[60:63], v[20:23], v[152:155], v[96:99]\nv_mfma_f32_16x16x32_bf16 v[56:59], v[4:7], v[152:155], v[100:103]\nv_pk_add_f32 v[128:129], v[128:129], v[32:33]\nv_pk_add_f32 v[130:131], v[130:131], v[34:35]\nv_pk_add_f32 v[132:133], v[132:133], v[36:37]\n"
"v_pk_add_f32 v[134:135], v[134:135], v[38:39]\nv_cvt_pk_bf16_f32 v128, v128, v129\nv_cvt_pk_bf16_f32 v129, v130, v131\nv_cvt_pk_bf16_f32 v130, v132, v133\nv_cvt_pk_bf16_f32 v131, v134, v135\ns_nop 1\n"
"v_mfma_f32_16x16x32_bf16 v[36:39], v[0:3], v[128:131], v[96:99]\nv_mfma_f32_16x16x32_bf16 v[32:35], v[230:233], v[128:131], v[104:107]\nv_pk_add_f32 v[136:137], v[136:137], v[40:41]\nv_pk_add_f32 v[138:139], v[138:139], v[42:43]\nv_pk_add_f32 v[140:141], v[140:141], v[44:45]\nv_pk_add_f32 v[142:143], v[142:143], v[46:47]\n"
"v_cvt_pk_bf16_f32 v136, v136, v137\nv_cvt_pk_bf16_f32 v137, v138, v139\nv_cvt_pk_bf16_f32 v138, v140, v141\nv_cvt_pk_bf16_f32 v139, v142, v143\ns_nop 1\nv_mfma_f32_16x16x32_bf16 v[44:47], v[4:7], v[136:139], v[96:99]\n"
"v_mfma_f32_16x16x32_bf16 v[40:43], v[234:237], v[136:139], v[104:107]\nv_max3_f32 v52, v52, v53, v48\nv_max3_f32 v60, v60, v61, v56\nv_max3_f32 v36, v36, v37, v32\ns_nop 4\nv_max3_f32 v44, v44, v45, v40\n"
"v_max_f32 v52, v52, v49\nv_max_f32 v60, v60, v57\nv_max_f32 v36, v36, v33\nv_max_f32 v44, v44, v41\nv_max3_f32 v54, v54, v55, v50\nv_max3_f32 v62, v62, v63, v58\n"
"v_max3_f32 v38, v38, v39, v34\nv_max3_f32 v46, v46, v47, v42\nv_max_f32 v54, v54, v51\nv_max_f32 v62, v62, v59\nv_max_f32 v38, v38, v35\nv_max_f32 v46, v46, v43\n"
"v_max_f32 v118, v52, v54\nv_max_f32 v119, v60, v62\nv_max_f32 v116, v36, v38\nv_max_f32 v117, v44, v46\nv_mov_b32 v120, v116\nv_mov_b32 v121, v117\n"
"v_mov_b32 v122, v118\nv_mov_b32 v123, v119\nv_permlane32_swap_b32 v120, v116\nv_permlane32_swap_b32 v121, v117\nv_permlane32_swap_b32 v122, v118\nv_permlane32_swap_b32 v123, v119\n"
"v_max_f32 v116, v116, v120\nv_max_f32 v117, v117, v121\nv_max_f32 v118, v118, v122\nv_max_f32 v119, v119, v123\nv_mov_b32 v120, v116\nv_mov_b32 v121, v117\n"
"v_mov_b32 v122, v118\nv_mov_b32 v123, v119\nv_permlane16_swap_b32 v120, v116\nv_permlane16_swap_b32 v121, v117\nv_permlane16_swap_b32 v122, v118\nv_permlane16_swap_b32 v123, v119\n"
"v_max_f32 v116, v116, v120\nv_max_f32 v117, v117, v121\nv_max_f32 v118, v118, v122\nv_max_f32 v119, v119, v123\nv_mul_f32 v118, v118, v218\nv_mul_f32 v119, v119, v218\n"
"v_mul_f32 v116, v116, v215\nv_mul_f32 v117, v117, v215\nv_fma_f32 v52, v52, v218, -v118\nv_fma_f32 v54, v54, v218, -v118\nv_fma_f32 v60, v60, v218, -v119\nv_fma_f32 v62, v62, v218, -v119\n"
"v_fma_f32 v36, v36, v215, -v116\nv_fma_f32 v38, v38, v215, -v116\nv_fma_f32 v44, v44, v215, -v117\nv_fma_f32 v46, v46, v215, -v117\nv_exp_f32 v52, v52\nv_exp_f32 v54, v54\n"
"v_exp_f32 v60, v60\nv_exp_f32 v62, v62\nv_exp_f32 v36, v36\nv_exp_f32 v38, v38\nv_exp_f32 v44, v44\nv_exp_f32 v46, v46\n"
"v_cvt_pk_bf16_f32 v52, v52, v52\nv_cvt_pk_bf16_f32 v54, v54, v54\nv_and_b32 v48, v52, v112\nv_and_b32 v49, v54, v113\nv_and_b32 v50, v52, v114\nv_and_b32 v51, v54, v115\n"
"s_nop 1\nv_mfma_f32_16x16x32_bf16 v[24:27], v[124:127], v[48:51], 0\nv_mfma_f32_16x16x32_bf16 v[144:147], v[66:69], v[48:51], 0\nv_mfma_f32_16x16x32_bf16 v[148:151], v[72:75], v[48:51], 0\nv_cvt_pk_bf16_f32 v60, v60, v60\nv_cvt_pk_bf16_f32 v62, v62, v62\n"
"v_and_b32 v56, v60, v112\nv_and_b32 v57, v62, v113\nv_and_b32 v58, v60, v114\nv_and_b32 v59, v62, v115\ns_nop 1\nv_mfma_f32_16x16x32_bf16 v[28:31], v[124:127], v[56:59], 0\n"
"v_mfma_f32_16x16x32_bf16 v[152:155], v[78:81], v[56:59], 0\nv_mfma_f32_16x16x32_bf16 v[156:159], v[84:87], v[56:59], 0\nv_cvt_pk_bf16_f32 v36, v36, v36\nv_cvt_pk_bf16_f32 v38, v38, v38\nv_and_b32 v32, v36, v112\nv_and_b32 v33, v38, v113\n"
"v_and_b32 v34, v36, v114\nv_and_b32 v35, v38, v115\ns_nop 1\nv_mfma_f32_16x16x32_bf16 v[8:11], v[124:127], v[32:35], 0\nv_mfma_f32_16x16x32_bf16 v[128:131], v[64:67], v[32:35], 0\nv_mfma_f32_16x16x32_bf16 v[132:135], v[70:73], v[32:35], 0\n"
"v_cvt_pk_bf16_f32 v44, v44, v44\nv_cvt_pk_bf16_f32 v46, v46, v46\nv_and_b32 v40, v44, v112\nv_and_b32 v41, v46, v113\nv_and_b32 v42, v44, v114\nv_and_b32 v43, v46, v115\n"
"s_nop 1\nv_mfma_f32_16x16x32_bf16 v[12:15], v[124:127], v[40:43], 0\nv_mfma_f32_16x16x32_bf16 v[136:139], v[76:79], v[40:43], 0\nv_mfma_f32_16x16x32_bf16 v[140:143], v[82:85], v[40:43], 0\ns_lshl_b32 s84, s42, 11\ns_add_u32 s84, s84, s49\n"
"s_add_u32 s84, s84, 2097152\ns_add_u32 s85, s84, 32768\nv_mov_b32 v238, s46\nv_mov_b32 v239, s47\nv_rcp_f32 v24, v24\nv_rcp_f32 v28, v28\n"
"v_rcp_f32 v8, v8\nv_rcp_f32 v12, v12\nv_pk_mul_f32 v[144:145], v[144:145], v[24:25] op_sel_hi:[1,0]\nv_pk_mul_f32 v[146:147], v[146:147], v[24:25] op_sel_hi:[1,0]\nv_pk_mul_f32 v[148:149], v[148:149], v[24:25] op_sel_hi:[1,0]\nv_pk_mul_f32 v[150:151], v[150:151], v[24:25] op_sel_hi:[1,0]\n"
"v_cvt_pk_bf16_f32 v144, v144, v145\nv_cvt_pk_bf16_f32 v145, v146, v147\nv_cvt_pk_bf16_f32 v146, v148, v149\nv_cvt_pk_bf16_f32 v147, v150, v151\nbuffer_store_dwordx4 v[144:147], v252, s[20:23], s85 offen offset:0 sc1\nv_pk_mul_f32 v[152:153], v[152:153], v[28:29] op_sel_hi:[1,0]\n"
"v_pk_mul_f32 v[154:155], v[154:155], v[28:29] op_sel_hi:[1,0]\nv_pk_mul_f32 v[156:157], v[156:157], v[28:29] op_sel_hi:[1,0]\nv_pk_mul_f32 v[158:159], v[158:159], v[28:29] op_sel_hi:[1,0]\nv_cvt_pk_bf16_f32 v152, v152, v153\nv_cvt_pk_bf16_f32 v153, v154, v155\nv_cvt_pk_bf16_f32 v154, v156, v157\n"
"v_cvt_pk_bf16_f32 v155, v158, v159\nbuffer_store_dwordx4 v[152:155], v252, s[20:23], s85 offen offset:1024 sc1\nv_pk_mul_f32 v[128:129], v[128:129], v[8:9] op_sel_hi:[1,0]\nv_pk_mul_f32 v[130:131], v[130:131], v[8:9] op_sel_hi:[1,0]\nv_pk_mul_f32 v[132:133], v[132:133], v[8:9] op_sel_hi:[1,0]\nv_pk_mul_f32 v[134:135], v[134:135], v[8:9] op_sel_hi:[1,0]\n"
"v_cvt_pk_bf16_f32 v128, v128, v129\nv_cvt_pk_bf16_f32 v129, v130, v131\nv_cvt_pk_bf16_f32 v130, v132, v133\nv_cvt_pk_bf16_f32 v131, v134, v135\nbuffer_store_dwordx4 v[128:131], v252, s[20:23], s84 offen offset:0 sc1\nv_pk_mul_f32 v[136:137], v[136:137], v[12:13] op_sel_hi:[1,0]\n"
"v_pk_mul_f32 v[138:139], v[138:139], v[12:13] op_sel_hi:[1,0]\nv_pk_mul_f32 v[140:141], v[140:141], v[12:13] op_sel_hi:[1,0]\nv_pk_mul_f32 v[142:143], v[142:143], v[12:13] op_sel_hi:[1,0]\nv_cvt_pk_bf16_f32 v136, v136, v137\nv_cvt_pk_bf16_f32 v137, v138, v139\nv_cvt_pk_bf16_f32 v138, v140, v141\n"
"v_cvt_pk_bf16_f32 v139, v142, v143\nbuffer_store_dwordx4 v[136:139], v252, s[20:23], s84 offen offset:1024 sc1\ns_add_u32 s61, s79, 88080384\ns_lshl_b32 s62, s42, 3\ns_add_u32 s61, s61, s62\ns_waitcnt vmcnt(0)\n"
"s_mov_b64 exec, 1\nbuffer_atomic_swap_x2 v[238:239], off, s[20:23], s61 sc1\ns_mov_b64 exec, -1\ns_mul_i32 s61, s41, 229376\ns_add_u32 s61, s61, s54\ns_lshr_b32 s62, s41, 1\n"
"s_mul_i32 s62, s62, 458752\ns_add_u32 s62, s62, s55\ns_movk_i32 s60, 7168\nv_mad_u32_u24 v230, v228, s60, v229\nv_add_u32 v231, 3668992, v230\ns_add_u32 m0, s50, -32768\n"
"s_add_u32 s60, s61, 0\nbuffer_load_dwordx4 v230, s[12:15], s60 offen offset:0 lds\nbuffer_load_dwordx4 v231, s[12:15], s60 offen offset:1024 lds\ns_add_u32 m0, s50, -24576\ns_add_u32 s60, s61, 128\nbuffer_load_dwordx4 v230, s[12:15], s60 offen offset:0 lds\n"
"buffer_load_dwordx4 v231, s[12:15], s60 offen offset:1024 lds\ns_add_u32 m0, s50, -16384\ns_add_u32 s60, s61, 256\nbuffer_load_dwordx4 v230, s[12:15], s60 offen offset:0 lds\nbuffer_load_dwordx4 v231, s[12:15], s60 offen offset:1024 lds\ns_add_u32 m0, s50, -8192\n"
"s_add_u32 s60, s62, 0\nbuffer_load_dwordx4 v222, s[12:15], s60 offen offset:0 lds\nbuffer_load_dwordx4 v223, s[12:15], s60 offen offset:1024 lds\nv_bfe_u32 v250, v252, 7, 1\nv_mul_u32_u24 v250, 2176, v250\nv_bfe_u32 v251, v252, 4, 3\n"
"v_lshl_add_u32 v250, v251, 3, v250\ns_lshl_b32 s60, s90, 6\nv_add_u32 v250, s60, v250\ns_add_u32 m0, s50, 24576\ns_nop 0\nbuffer_load_dwordx4 v221, s[8:11], s63 offen offset:1024 lds\n"
"s_add_u32 m0, s50, 32768\ns_add_u32 s63, s53, 458752\nbuffer_load_dwordx4 v220, s[8:11], s63 offen lds\nbuffer_load_dwordx4 v221, s[8:11], s63 offen offset:1024 lds\ns_add_u32 m0, s50, 40960\ns_add_u32 s63, s53, 524288\n"
"buffer_load_dwordx4 v220, s[8:11], s63 offen lds\nbuffer_load_dwordx4 v221, s[8:11], s63 offen offset:1024 lds\ns_add_u32 m0, s50, 49152\ns_add_u32 s63, s53, 589824\nbuffer_load_dwordx4 v220, s[8:11], s63 offen lds\nbuffer_load_dwordx4 v221, s[8:11], s63 offen offset:1024 lds\n"
"s_add_u32 m0, s50, 57344\ns_add_u32 s63, s53, 655360\nbuffer_load_dwordx4 v220, s[8:11], s63 offen lds\nbuffer_load_dwordx4 v221, s[8:11], s63 offen offset:1024 lds\ns_add_u32 m0, s50, 65536\ns_add_u32 s63, s53, 720896\n"
"buffer_load_dwordx4 v220, s[8:11], s63 offen lds\nbuffer_load_dwordx4 v221, s[8:11], s63 offen offset:1024 lds\ns_add_u32 m0, s50, 73728\ns_add_u32 s63, s53, 786432\nbuffer_load_dwordx4 v220, s[8:11], s63 offen lds\nv_mov_b32 v248, 0\n"
"v_mov_b32 v249, 0\ns_add_u32 s61, s80, 88080384\ns_mov_b64 exec, 0xffff\nbuffer_atomic_or_x2 v[248:249], v250, s[20:23], s61 offen sc0 sc1\ns_mov_b64 exec, -1\nv_bfe_u32 v234, v252, 4, 4\n"
"v_lshrrev_b32 v235, 8, v252\nv_lshlrev_b32 v235, 2, v235\nv_sub_u32 v234, v234, v235\nv_lshlrev_b32 v235, 4, v234\ns_mov_b64 s[60:61], 0x3f80\nv_lshlrev_b64 v[208:209], v235, s[60:61]\n"
"v_cmp_gt_u32 s[60:61], 4, v234\ns_nop 1\nv_cndmask_b32 v212, 0, v208, s[60:61]\nv_cndmask_b32 v208, 0, v208, s[60:61]\nv_cndmask_b32 v213, 0, v209, s[60:61]\nv_cndmask_b32 v209, 0, v209, s[60:61]\n"
"s_nop 1\nv_mfma_f32_16x16x32_bf16 v[0:3], v[208:211], v[192:195], 0\nv_mfma_f32_16x16x32_bf16 v[4:7], v[210:213], v[192:195], 0\nv_mfma_f32_16x16x32_bf16 v[8:11], v[208:211], v[196:199], 0\nv_mfma_f32_16x16x32_bf16 v[12:15], v[210:213], v[196:199], 0\nv_mfma_f32_16x16x32_bf16 v[16:19], v[208:211], v[200:203], 0\n"
"v_mfma_f32_16x16x32_bf16 v[20:23], v[210:213], v[200:203], 0\nv_mfma_f32_16x16x32_bf16 v[24:27], v[208:211], v[204:207], 0\nv_mfma_f32_16x16x32_bf16 v[28:31], v[210:213], v[204:207], 0\ns_waitcnt vmcnt(0)\nv_cmp_eq_u64 s[86:87], s[46:47], v[248:249]\ns_nop 0\n"
"s_and_b32 s62, s86, 0xffff\ns_cmp_eq_u32 s62, 0xffff\ns_cbranch_scc0 Lq204\nLq205:\ns_add_u32 s64, s70, 2097152\nbuffer_load_dwordx4 a[0:3], v252, s[20:23], s64 offen offset:0 sc1\n"
"s_add_u32 s65, s70, 2129920\nbuffer_load_dwordx4 a[4:7], v252, s[20:23], s65 offen offset:0 sc1\ns_add_u32 s66, s71, 2097152\nbuffer_load_dwordx4 a[8:11], v252, s[20:23], s66 offen offset:0 sc1\ns_add_u32 s67, s71, 2129920\nbuffer_load_dwordx4 a[12:15], v252, s[20:23], s67 offen offset:0 sc1\n"
"buffer_load_dwordx4 a[16:19], v252, s[20:23], s64 offen offset:1024 sc1\nbuffer_load_dwordx4 a[20:23], v252, s[20:23], s65 offen offset:1024 sc1\nbuffer_load_dwordx4 a[24:27], v252, s[20:23], s66 offen offset:1024 sc1\nbuffer_load_dwordx4 a[28:31], v252, s[20:23], s67 offen offset:1024 sc1\ns_barrier\nR 160,188,32768\n"
"R 162,188,36864\nR 164,188,40960\nR 166,188,45056\nR 168,189,32768\nR 170,189,36864\nR 172,189,40960\n"
"R 174,189,45056\nR 176,190,32768\nR 178,190,36864\nR 180,190,40960\nR 182,190,45056\nbuffer_load_dwordx4 a[32:35], v252, s[20:23], s64 offen offset:2048 sc1\n"
"buffer_load_dwordx4 a[36:39], v252, s[20:23], s65 offen offset:2048 sc1\nbuffer_load_dwordx4 a[40:43], v252, s[20:23], s66 offen offset:2048 sc1\nbuffer_load_dwordx4 a[44:47], v252, s[20:23], s67 offen offset:2048 sc1\nbuffer_load_dwordx4 a[48:51], v252, s[20:23], s64 offen offset:3072 sc1\nbuffer_load_dwordx4 a[52:55], v252, s[20:23], s65 offen offset:3072 sc1\nbuffer_load_dwordx4 a[56:59], v252, s[20:23], s66 offen offset:3072 sc1\n"
"buffer_load_dwordx4 a[60:63], v252, s[20:23], s67 offen offset:3072 sc1\ns_add_u32 s64, s70, 2101248\nbuffer_load_dwordx4 a[64:67], v252, s[20:23], s64 offen offset:0 sc1\ns_add_u32 s65, s70, 2134016\nbuffer_load_dwordx4 a[68:71], v252, s[20:23], s65 offen offset:0 sc1\ns_add_u32 s66, s71, 2101248\n"
"buffer_load_dwordx4 a[72:75], v252, s[20:23], s66 offen offset:0 sc1\ns_add_u32 s67, s71, 2134016\nbuffer_load_dwordx4 a[76:79], v252, s[20:23], s67 offen offset:0 sc1\ns_waitcnt vmcnt(19) lgkmcnt(10)\nM 0,160,0\nbuffer_load_dwordx4 a[80:83], v252, s[20:23], s64 offen offset:1024 sc1\n"
"s_waitcnt vmcnt(16) lgkmcnt(8)\nM 0,164,16\nM 16,160,4\ns_waitcnt vmcnt(15)\nM 16,164,20\nv_mfma_f32_16x16x32_bf16 v[32:35], v[160:163], a[8:11], 0\n"
"buffer_load_dwordx4 a[84:87], v252, s[20:23], s65 offen offset:1024 sc1\nv_mov_b32 v242, s76\ns_waitcnt vmcnt(15)\nM 32,164,24\ns_and_b32 s60, s40, 2\ns_lshl_b32 s60, s60, 5\n"
"v_xor_b32 v238, s60, v184\nv_mfma_f32_16x16x32_bf16 v[48:51], v[160:163], a[12:15], 0\nv_xor_b32 v239, 32, v238\nv_add_u32 v238, -32768, v238\ns_waitcnt vmcnt(14)\nM 48,164,28\n"
"G 4\nv_add_u32 v239, -32768, v239\nv_mov_b32 v243, s76\nM 4,168,0\nR 160,191,32768\nv_mov_b32 v244, s76\n"
"M 4,172,16\nbuffer_load_dwordx4 a[88:91], v252, s[20:23], s66 offen offset:1024 sc1\nM 20,168,4\nR 162,191,36864\nv_mov_b32 v245, s76\nM 20,172,20\n"
"s_and_b32 s60, s41, 1\ns_lshl_b32 s60, s60, 1\ns_lshr_b32 s61, s40, 1\ns_add_u32 s60, s60, s61\ns_lshl_b32 s60, s60, 5\nv_xor_b32 v240, s60, v184\n"
"v_mfma_f32_16x16x32_bf16 v[36:39], v[168:171], a[8:11], 0\nR 164,191,40960\nv_add_u32 v240, -32768, v240\nM 36,172,24\nbuffer_load_dwordx4 a[92:95], v252, s[20:23], s67 offen offset:1024 sc1\nv_mfma_f32_16x16x32_bf16 v[52:55], v[168:171], a[12:15], 0\n"
"R 166,191,45056\nM 52,172,28\nG 4\nM 8,176,0\nR 168,188,49152\nR 64,238,0\n"
"R 66,238,4096\nM 8,180,16\nR 68,239,0\nR 70,239,4096\nM 24,176,4\nR 170,188,53248\n"
"M 24,180,20\nbuffer_load_dwordx4 a[96:99], v252, s[20:23], s64 offen offset:2048 sc1\nv_mfma_f32_16x16x32_bf16 v[40:43], v[176:179], a[8:11], 0\nR 172,184,0\nM 40,180,24\nbuffer_load_dwordx4 a[100:103], v252, s[20:23], s65 offen offset:2048 sc1\n"
"v_mfma_f32_16x16x32_bf16 v[56:59], v[176:179], a[12:15], 0\nR 174,184,4096\nG 5\nv_mfma_f32_16x16x32_bf16 v[72:75], v[242:245], v[64:67], 0\nM 56,180,28\nM 12,160,0\n"
"R 176,189,49152\nG 4\nv_mfma_f32_16x16x32_bf16 v[76:79], v[242:245], v[68:71], 0\nM 12,164,16\nbuffer_load_dwordx4 a[104:107], v252, s[20:23], s66 offen offset:2048 sc1\nM 28,160,4\n"
"R 178,189,53248\nM 28,164,20\nv_mfma_f32_16x16x32_bf16 v[44:47], v[160:163], a[8:11], 0\nR 180,185,0\nM 44,164,24\nbuffer_load_dwordx4 a[108:111], v252, s[20:23], s67 offen offset:2048 sc1\n"
"v_mfma_f32_16x16x32_bf16 v[60:63], v[160:163], a[12:15], 0\nR 182,185,4096\nv_lshrrev_b32 v64, 1, v252\nv_mov_b32 v67, s46\nM 60,164,28\ns_mov_b64 s[60:61], 0xffff\n"
"v_cndmask_b32 v66, v76, v72, s[60:61]\nG 4\ns_waitcnt vmcnt(19)\nM 0,168,32\nR 160,190,49152\ns_lshr_b32 s61, s40, 1\n"
"s_lshl_b32 s61, s61, 8\ns_add_u32 s61, s61, s91\ns_add_u32 s61, s61, 786432\ns_bfm_b64 exec, 32, 0\nbuffer_store_dwordx2 v[66:67], v64, s[20:23], s61 offen sc1\ns_mov_b64 exec, -1\n"
"s_waitcnt vmcnt(16)\nM 0,172,48\nbuffer_load_dwordx4 a[112:115], v252, s[20:23], s64 offen offset:3072 sc1\nM 16,168,36\nR 162,190,53248\ns_waitcnt vmcnt(16)\n"
"M 16,172,52\nM 32,168,40\nR 164,186,0\ns_waitcnt vmcnt(15)\nM 32,172,56\nbuffer_load_dwordx4 a[116:119], v252, s[20:23], s65 offen offset:3072 sc1\n"
"M 48,168,44\nR 166,186,4096\ns_waitcnt vmcnt(15)\nM 48,172,60\nG 4\nM 4,176,32\n"
"R 168,191,49152\nM 4,180,48\nbuffer_load_dwordx4 a[120:123], v252, s[20:23], s66 offen offset:3072 sc1\nM 20,176,36\nR 170,191,53248\nR 64,238,8192\n"
"R 66,238,12288\nM 20,180,52\nR 68,239,8192\nR 70,239,12288\nM 36,176,40\nR 172,187,0\n"
"M 36,180,56\nbuffer_load_dwordx4 a[124:127], v252, s[20:23], s67 offen offset:3072 sc1\nM 52,176,44\nR 174,187,4096\nM 52,180,60\nG 8\n"
"M 8,160,32\nR 176,184,8192\nG 5\nv_mfma_f32_16x16x32_bf16 v[72:75], v[242:245], v[64:67], 0\nM 8,164,48\ns_add_u32 s64, s70, 2105344\n"
"buffer_load_dwordx4 a[128:131], v252, s[20:23], s64 offen offset:0 sc1\nM 24,160,36\nR 178,184,12288\nG 4\nv_mfma_f32_16x16x32_bf16 v[76:79], v[242:245], v[68:71], 0\nM 24,164,52\n"
"M 40,160,40\nR 180,184,16384\nM 40,164,56\ns_add_u32 s65, s70, 2138112\nbuffer_load_dwordx4 a[132:135], v252, s[20:23], s65 offen offset:0 sc1\nM 56,160,44\n"
"R 182,184,20480\nM 56,164,60\nG 4\nM 12,168,32\nR 160,185,8192\nv_lshrrev_b32 v64, 1, v252\n"
"v_mov_b32 v67, s46\nM 12,172,48\ns_add_u32 s66, s71, 2105344\nbuffer_load_dwordx4 a[136:139], v252, s[20:23], s66 offen offset:0 sc1\ns_mov_b64 s[60:61], 0xffff\nv_cndmask_b32 v66, v76, v72, s[60:61]\n"
"M 28,168,36\nR 162,185,12288\ns_lshr_b32 s61, s40, 1\ns_lshl_b32 s61, s61, 8\ns_add_u32 s61, s61, s91\ns_add_u32 s61, s61, 1048576\n"
"s_bfm_b64 exec, 32, 0\nbuffer_store_dwordx2 v[66:67], v64, s[20:23], s61 offen sc1\ns_mov_b64 exec, -1\nM 28,172,52\nM 44,168,40\nR 164,185,16384\n"
"M 44,172,56\ns_add_u32 s67, s71, 2138112\nbuffer_load_dwordx4 a[140:143], v252, s[20:23], s67 offen offset:0 sc1\nM 60,168,44\nR 166,185,20480\nM 60,172,60\n"
"s_barrier\nG 4\ns_waitcnt vmcnt(21)\nM 0,176,64\nR 168,186,8192\ns_waitcnt vmcnt(17)\n"
"M 0,180,80\nbuffer_load_dwordx4 a[144:147], v252, s[20:23], s64 offen offset:1024 sc1\nM 16,176,68\nR 170,186,12288\ns_waitcnt vmcnt(17)\nM 16,180,84\n"
"M 32,176,72\nR 172,186,16384\nR 64,238,16384\nR 66,238,20480\ns_waitcnt vmcnt(16)\nM 32,180,88\n"
"R 68,239,16384\nR 70,239,20480\nM 48,176,76\nR 174,186,20480\ns_waitcnt vmcnt(15)\nM 48,180,92\n"
"buffer_load_dwordx4 a[148:151], v252, s[20:23], s65 offen offset:1024 sc1\nG 8\nM 4,160,64\nR 176,187,8192\nM 4,164,80\nbuffer_load_dwordx4 a[152:155], v252, s[20:23], s66 offen offset:1024 sc1\n"
"M 20,160,68\nR 178,187,12288\nG 5\nv_mfma_f32_16x16x32_bf16 v[72:75], v[242:245], v[64:67], 0\nM 20,164,84\nM 36,160,72\n"
"R 180,187,16384\nG 4\nv_mfma_f32_16x16x32_bf16 v[76:79], v[242:245], v[68:71], 0\nM 36,164,88\nbuffer_load_dwordx4 a[156:159], v252, s[20:23], s67 offen offset:1024 sc1\nM 52,160,76\n"
"R 182,187,20480\nM 52,164,92\nG 4\nM 8,168,64\nR 160,184,24576\nM 8,172,80\n"
"buffer_load_dwordx4 a[160:163], v252, s[20:23], s64 offen offset:2048 sc1\nM 24,168,68\nR 162,184,28672\nv_lshrrev_b32 v64, 1, v252\nv_mov_b32 v67, s46\nM 24,172,84\n"
"s_mov_b64 s[60:61], 0xffff\nv_cndmask_b32 v66, v76, v72, s[60:61]\nM 40,168,72\nR 164,184,32768\ns_lshr_b32 s61, s40, 1\ns_lshl_b32 s61, s61, 8\n"
"s_add_u32 s61, s61, s91\ns_add_u32 s61, s61, 1310720\ns_bfm_b64 exec, 32, 0\nbuffer_store_dwordx2 v[66:67], v64, s[20:23], s61 offen sc1\ns_mov_b64 exec, -1\nM 40,172,88\n"
"buffer_load_dwordx4 a[164:167], v252, s[20:23], s65 offen offset:2048 sc1\nM 56,168,76\nR 166,184,36864\nM 56,172,92\nG 4\nM 12,176,64\n"
"R 168,185,24576\nM 12,180,80\nbuffer_load_dwordx4 a[168:171], v252, s[20:23], s66 offen offset:2048 sc1\nM 28,176,68\nR 170,185,28672\nM 28,180,84\n"
"M 44,176,72\nR 172,185,32768\nM 44,180,88\nbuffer_load_dwordx4 a[172:175], v252, s[20:23], s67 offen offset:2048 sc1\nM 60,176,76\nR 174,185,36864\n"
"R 64,240,24576\nR 66,240,28672\nM 60,180,92\nG 6\ns_waitcnt vmcnt(22)\nM 0,160,96\n"
"R 176,186,24576\ns_waitcnt vmcnt(17)\nM 0,164,112\nbuffer_load_dwordx4 a[176:179], v252, s[20:23], s64 offen offset:3072 sc1\nM 16,160,100\nR 178,186,28672\n"
"s_waitcnt vmcnt(17)\nM 16,164,116\nbuffer_load_dwordx4 v221, s[8:11], s63 offen offset:1024 lds\nM 32,160,104\nR 180,186,32768\nG 3\n"
"v_mfma_f32_16x16x32_bf16 v[72:75], v[242:245], v[64:67], 0\ns_add_u32 m0, s50, 81920\ns_waitcnt vmcnt(17)\nM 32,164,120\nbuffer_load_dwordx4 a[180:183], v252, s[20:23], s65 offen offset:3072 sc1\nM 48,160,108\n"
"R 182,186,36864\ns_add_u32 s63, s53, 851968\ns_waitcnt vmcnt(17)\nM 48,164,124\nbuffer_load_dwordx4 v220, s[8:11], s63 offen lds\nM 4,168,96\n"
"R 160,187,24576\nM 4,172,112\nbuffer_load_dwordx4 a[184:187], v252, s[20:23], s66 offen offset:3072 sc1\nM 20,168,100\nR 162,187,28672\nM 20,172,116\n"
"buffer_load_dwordx4 v221, s[8:11], s63 offen offset:1024 lds\nM 36,168,104\nR 164,187,32768\ns_add_u32 m0, s50, 90112\nv_lshrrev_b32 v64, 1, v252\nM 36,172,120\n"
"buffer_load_dwordx4 a[188:191], v252, s[20:23], s67 offen offset:3072 sc1\nv_mov_b32 v67, s46\nM 52,168,108\nR 166,187,36864\ns_add_u32 s63, s53, 917504\nv_mov_b32 v66, v72\n"
"M 52,172,124\nbuffer_load_dwordx4 v220, s[8:11], s63 offen lds\ns_lshr_b32 s61, s40, 1\ns_lshl_b32 s61, s61, 7\ns_add_u32 s61, s61, s91\ns_add_u32 s61, s61, 1572864\n"
"s_bfm_b64 exec, 16, 0\nbuffer_store_dwordx2 v[66:67], v64, s[20:23], s61 offen sc1\ns_mov_b64 exec, -1\nG 4\nM 8,176,96\nR 168,184,40960\n"
"M 8,180,112\ns_add_u32 s64, s70, 2109440\nbuffer_load_dwordx4 a[192:195], v252, s[20:23], s64 offen offset:0 sc1\nM 24,176,100\nR 170,184,45056\nM 24,180,116\n"
"buffer_load_dwordx4 v221, s[8:11], s63 offen offset:1024 lds\nM 40,176,104\nR 172,184,49152\ns_add_u32 m0, s50, 98304\nM 40,180,120\ns_add_u32 s65, s70, 2142208\n"
"buffer_load_dwordx4 a[196:199], v252, s[20:23], s65 offen offset:0 sc1\nM 56,176,108\nR 174,184,53248\ns_add_u32 s63, s53, 983040\nM 56,180,124\nbuffer_load_dwordx4 v220, s[8:11], s63 offen lds\n"
"G 4\nM 12,160,96\nR 176,185,40960\nM 12,164,112\ns_add_u32 s66, s71, 2109440\nbuffer_load_dwordx4 a[200:203], v252, s[20:23], s66 offen offset:0 sc1\n"
"M 28,160,100\nR 178,185,45056\nM 28,164,116\nbuffer_load_dwordx4 v221, s[8:11], s63 offen offset:1024 lds\nM 44,160,104\nR 180,185,49152\n"
"M 44,164,120\ns_add_u32 s67, s71, 2142208\nbuffer_load_dwordx4 a[204:207], v252, s[20:23], s67 offen offset:0 sc1\nM 60,160,108\nR 182,185,53248\nM 60,164,124\n"
"s_barrier\nG 4\ns_waitcnt vmcnt(29)\nM 0,168,128\nR 160,186,40960\ns_waitcnt vmcnt(24)\n"
"M 0,172,144\nbuffer_load_dwordx4 a[208:211], v252, s[20:23], s64 offen offset:1024 sc1\nM 16,168,132\nR 162,186,45056\ns_waitcnt vmcnt(24)\nM 16,172,148\n"
"M 32,168,136\nR 164,186,49152\ns_waitcnt vmcnt(23)\nM 32,172,152\nbuffer_load_dwordx4 a[212:215], v252, s[20:23], s65 offen offset:1024 sc1\nM 48,168,140\n"
"R 166,186,53248\ns_waitcnt vmcnt(23)\nM 48,172,156\nG 4\nM 4,176,128\nR 168,187,40960\n"
"M 4,180,144\nbuffer_load_dwordx4 a[216:219], v252, s[20:23], s66 offen offset:1024 sc1\nM 20,176,132\nR 170,187,45056\nM 20,180,148\nM 36,176,136\n"
"R 172,187,49152\nM 36,180,152\nbuffer_load_dwordx4 a[220:223], v252, s[20:23], s67 offen offset:1024 sc1\nM 52,176,140\nR 174,187,53248\nM 52,180,156\n"
"G 4\nM 8,160,128\nR 176,184,57344\nM 8,164,144\nbuffer_load_dwordx4 a[224:227], v252, s[20:23], s64 offen offset:2048 sc1\nM 24,160,132\n"
"R 178,184,61440\nM 24,164,148\nM 40,160,136\nR 180,188,0\nM 40,164,152\nbuffer_load_dwordx4 a[228:231], v252, s[20:23], s65 offen offset:2048 sc1\n"
"M 56,160,140\nR 182,188,4096\nM 56,164,156\nG 4\nM 12,168,128\nR 160,185,57344\n"
"M 12,172,144\nbuffer_load_dwordx4 a[232:235], v252, s[20:23], s66 offen offset:2048 sc1\nM 28,168,132\nR 162,185,61440\nM 28,172,148\nM 44,168,136\n"
"R 164,189,0\nM 44,172,152\nbuffer_load_dwordx4 a[236:239], v252, s[20:23], s67 offen offset:2048 sc1\nM 60,168,140\nR 166,189,4096\nM 60,172,156\n"
"G 4\ns_waitcnt vmcnt(28)\nM 0,176,160\nR 168,186,57344\ns_waitcnt vmcnt(23)\nM 0,180,176\n"
"buffer_load_dwordx4 a[240:243], v252, s[20:23], s64 offen offset:3072 sc1\nM 16,176,164\nR 170,186,61440\ns_waitcnt vmcnt(22)\nM 16,180,180\nM 32,176,168\n"
"R 172,190,0\ns_waitcnt vmcnt(20)\nM 32,180,184\nbuffer_load_dwordx4 a[244:247], v252, s[20:23], s65 offen offset:3072 sc1\nM 48,176,172\nR 174,190,4096\n"
"s_waitcnt vmcnt(19)\nM 48,180,188\nG 4\nM 4,160,160\nR 176,187,57344\nM 4,164,176\n"
"buffer_load_dwordx4 a[248:251], v252, s[20:23], s66 offen offset:3072 sc1\nM 20,160,164\nR 178,187,61440\ns_waitcnt vmcnt(12)\ns_barrier\nM 20,164,180\n"
"M 36,160,168\nR 180,191,0\nM 36,164,184\nbuffer_load_dwordx4 a[252:255], v252, s[20:23], s67 offen offset:3072 sc1\nM 52,160,172\nR 182,191,4096\n"
"M 52,164,188\nG 4\nM 8,168,160\nR 160,188,8192\nM 8,172,176\ns_movk_i32 s63, 7168\n"
"v_mad_u32_u24 v220, v228, s63, v229\nv_add_u32 v221, 3668992, v220\ns_add_u32 m0, s50, 106496\ns_add_u32 s63, s54, 0\nbuffer_load_dwordx4 v220, s[12:15], s63 offen lds\nM 24,168,164\n"
"R 162,188,12288\nM 24,172,180\nM 40,168,168\nR 164,188,16384\nM 40,172,184\nbuffer_load_dwordx4 v221, s[12:15], s63 offen offset:1024 lds\n"
"M 56,168,172\nR 166,188,20480\ns_add_u32 m0, s50, 114688\nM 56,172,188\nG 4\nM 12,176,160\n"
"R 168,189,8192\ns_add_u32 s63, s54, 229376\nM 12,180,176\nbuffer_load_dwordx4 v220, s[12:15], s63 offen lds\nM 28,176,164\nR 170,189,12288\n"
"M 28,180,180\nM 44,176,168\nR 172,189,16384\nM 44,180,184\nbuffer_load_dwordx4 v221, s[12:15], s63 offen offset:1024 lds\nM 60,176,172\n"
"R 174,189,20480\ns_add_u32 m0, s50, 0\nM 60,180,188\nG 4\nM 32,160,200\nR 176,190,8192\n"
"s_add_u32 s63, s54, 128\ns_waitcnt vmcnt(13)\nM 32,164,216\nR 178,190,12288\nM 48,160,204\nR 180,190,16384\n"
"s_waitcnt vmcnt(12)\nM 48,164,220\nR 182,190,20480\nG 4\nM 36,168,200\nR 160,191,8192\n"
"buffer_load_dwordx4 v220, s[12:15], s63 offen lds\nM 36,172,216\nR 162,191,12288\nM 52,168,204\nR 164,191,16384\nM 52,172,220\n"
"R 166,191,20480\nG 4\nM 40,176,200\nR 168,188,24576\nbuffer_load_dwordx4 v221, s[12:15], s63 offen offset:1024 lds\nM 40,180,216\n"
"R 170,188,28672\ns_add_u32 m0, s50, 8192\nM 56,176,204\nR 172,188,32768\ns_add_u32 s63, s54, 229504\nM 56,180,220\n"
"R 174,188,36864\nG 4\nM 44,160,200\nR 176,189,24576\nbuffer_load_dwordx4 v220, s[12:15], s63 offen lds\nM 44,164,216\n"
"R 178,189,28672\nM 60,160,204\nR 180,189,32768\nM 60,164,220\nR 182,189,36864\nG 4\n"
"s_waitcnt vmcnt(12)\nM 32,168,232\nR 160,190,24576\nbuffer_load_dwordx4 v221, s[12:15], s63 offen offset:1024 lds\ns_waitcnt vmcnt(9)\nM 32,172,248\n"
"R 162,190,28672\nM 48,168,236\nR 164,190,32768\ns_waitcnt vmcnt(8)\nM 48,172,252\nR 166,190,36864\n"
"G 4\nM 36,176,232\nR 168,191,24576\nM 36,180,248\nR 170,191,28672\nM 52,176,236\n"
"R 172,191,32768\nM 52,180,252\nR 174,191,36864\nG 4\nM 40,160,232\nR 176,188,8192\n"
"M 40,164,248\nR 178,188,12288\nM 56,160,236\nR 180,188,16384\nM 56,164,252\nR 182,188,20480\n"
"G 4\nM 44,168,232\nR 160,189,8192\nM 44,172,248\nR 162,189,12288\nM 60,168,236\n"
"R 164,189,16384\nM 60,172,252\nR 166,189,20480\nG 4\nM 0,176,192\nR 168,190,8192\n"
"M 0,180,208\nR 170,190,12288\ns_mov_b32 m0, s51\nM 16,176,196\nR 172,190,16384\nds_write_addtid_b32 v32 offset:0\n"
"M 16,180,212\nR 174,190,20480\nds_write_addtid_b32 v33 offset:256\nG 6\nM 4,160,192\nR 176,191,8192\n"
"ds_write_addtid_b32 v34 offset:512\nM 4,164,208\nR 178,191,12288\nds_write_addtid_b32 v35 offset:768\nM 20,160,196\nR 180,191,16384\n"
"ds_write_addtid_b32 v36 offset:1024\nM 20,164,212\nR 182,191,20480\nds_write_addtid_b32 v37 offset:1280\nG 9\nM 8,168,192\n"
"R 160,188,24576\nds_write_addtid_b32 v38 offset:1536\nM 8,172,208\nR 162,188,28672\nds_write_addtid_b32 v39 offset:1792\nM 24,168,196\n"
"R 164,188,32768\nds_write_addtid_b32 v40 offset:2048\nM 24,172,212\nR 166,188,36864\nds_write_addtid_b32 v41 offset:2304\nG 9\n"
"M 12,176,192\nR 168,189,24576\nds_write_addtid_b32 v42 offset:2560\nM 12,180,208\nR 170,189,28672\nds_write_addtid_b32 v43 offset:2816\n"
"M 28,176,196\nR 172,189,32768\nds_write_addtid_b32 v44 offset:3072\nds_write_addtid_b32 v45 offset:3328\nds_write_addtid_b32 v46 offset:3584\nds_write_addtid_b32 v47 offset:3840\n"
"ds_write_addtid_b32 v48 offset:4096\nds_write_addtid_b32 v49 offset:4352\nds_write_addtid_b32 v50 offset:4608\nds_write_addtid_b32 v51 offset:4864\nds_write_addtid_b32 v52 offset:5120\nds_write_addtid_b32 v53 offset:5376\n"
"ds_write_addtid_b32 v54 offset:5632\nds_write_addtid_b32 v55 offset:5888\nds_write_addtid_b32 v56 offset:6144\nds_write_addtid_b32 v57 offset:6400\nds_write_addtid_b32 v58 offset:6656\nds_write_addtid_b32 v59 offset:6912\n"
"ds_write_addtid_b32 v60 offset:7168\nds_write_addtid_b32 v61 offset:7424\nds_write_addtid_b32 v62 offset:7680\nds_write_addtid_b32 v63 offset:7936\nM 28,180,212\nR 174,189,36864\n"
"s_add_u32 m0, s50, 16384\ns_add_u32 s63, s54, 458752\nbuffer_load_dwordx4 v220, s[12:15], s63 offen lds\nG 15\nM 0,160,224\nR 176,190,24576\n"
"buffer_load_dwordx4 v221, s[12:15], s63 offen offset:1024 lds\nM 0,164,240\nR 178,190,28672\nG 3\ns_barrier\ns_add_u32 s63, s54, 688128\n"
"M 16,160,228\nR 180,190,32768\ns_add_u32 m0, s50, 24576\nM 16,164,244\nR 182,190,36864\nG 4\n"
"M 4,168,224\nR 160,191,24576\nbuffer_load_dwordx4 v220, s[12:15], s63 offen lds\nM 4,172,240\nR 162,191,28672\nds_read2st64_b32 v[32:33], v225 offset0:0 offset1:1\n"
"M 20,168,228\nR 164,191,32768\nds_read2st64_b32 v[34:35], v225 offset0:2 offset1:3\nM 20,172,244\nR 166,191,36864\nG 6\n"
"M 8,176,224\nbuffer_load_dwordx4 v221, s[12:15], s63 offen offset:1024 lds\nM 8,180,240\nds_read2st64_b32 v[36:37], v225 offset0:4 offset1:5\nds_read2st64_b32 v[38:39], v225 offset0:6 offset1:7\nM 24,176,228\n"
"ds_read2st64_b32 v[40:41], v225 offset0:8 offset1:9\nds_read2st64_b32 v[42:43], v225 offset0:10 offset1:11\nM 24,180,244\nds_read2st64_b32 v[44:45], v225 offset0:12 offset1:13\nds_read2st64_b32 v[46:47], v225 offset0:14 offset1:15\nG 6\n"
"M 12,160,224\nds_read2st64_b32 v[48:49], v225 offset0:16 offset1:17\nds_read2st64_b32 v[50:51], v225 offset0:18 offset1:19\nM 12,164,240\nds_read2st64_b32 v[52:53], v225 offset0:20 offset1:21\nds_read2st64_b32 v[54:55], v225 offset0:22 offset1:23\n"
"M 28,160,228\nds_read2st64_b32 v[56:57], v225 offset0:24 offset1:25\nds_read2st64_b32 v[58:59], v225 offset0:26 offset1:27\nM 28,164,244\nds_read2st64_b32 v[60:61], v225 offset0:28 offset1:29\nds_read2st64_b32 v[62:63], v225 offset0:30 offset1:31\n"
"G 0\ns_barrier\ns_lshl_b32 s84, s75, 11\ns_add_u32 s84, s84, s49\ns_add_u32 s84, s84, 10485760\ns_add_u32 s85, s84, 32768\n"
"v_mov_b32 v238, s46\nv_mov_b32 v239, s47\nv_pk_add_f32 v[0:1], v[0:1], v[32:33]\nv_pk_add_f32 v[2:3], v[2:3], v[34:35]\nv_pk_add_f32 v[4:5], v[4:5], v[36:37]\nv_pk_add_f32 v[6:7], v[6:7], v[38:39]\n"
"v_cvt_pk_bf16_f32 v192, v0, v1\nv_cvt_pk_bf16_f32 v193, v2, v3\nv_cvt_pk_bf16_f32 v194, v4, v5\nv_cvt_pk_bf16_f32 v195, v6, v7\nbuffer_store_dwordx4 v[192:195], v252, s[20:23], s84 offen offset:0 sc1\nv_pk_add_f32 v[8:9], v[8:9], v[40:41]\n"
"v_pk_add_f32 v[10:11], v[10:11], v[42:43]\nv_pk_add_f32 v[12:13], v[12:13], v[44:45]\nv_pk_add_f32 v[14:15], v[14:15], v[46:47]\nv_cvt_pk_bf16_f32 v196, v8, v9\nv_cvt_pk_bf16_f32 v197, v10, v11\nv_cvt_pk_bf16_f32 v198, v12, v13\n"
"v_cvt_pk_bf16_f32 v199, v14, v15\nbuffer_store_dwordx4 v[196:199], v252, s[20:23], s84 offen offset:1024 sc1\nv_pk_add_f32 v[16:17], v[16:17], v[48:49]\nv_pk_add_f32 v[18:19], v[18:19], v[50:51]\nv_pk_add_f32 v[20:21], v[20:21], v[52:53]\nv_pk_add_f32 v[22:23], v[22:23], v[54:55]\n"
"v_cvt_pk_bf16_f32 v200, v16, v17\nv_cvt_pk_bf16_f32 v201, v18, v19\nv_cvt_pk_bf16_f32 v202, v20, v21\nv_cvt_pk_bf16_f32 v203, v22, v23\nbuffer_store_dwordx4 v[200:203], v252, s[20:23], s85 offen offset:0 sc1\nv_pk_add_f32 v[24:25], v[24:25], v[56:57]\n"
"v_pk_add_f32 v[26:27], v[26:27], v[58:59]\nv_pk_add_f32 v[28:29], v[28:29], v[60:61]\nv_pk_add_f32 v[30:31], v[30:31], v[62:63]\nv_cvt_pk_bf16_f32 v204, v24, v25\nv_cvt_pk_bf16_f32 v205, v26, v27\nv_cvt_pk_bf16_f32 v206, v28, v29\n"
"v_cvt_pk_bf16_f32 v207, v30, v31\nbuffer_store_dwordx4 v[204:207], v252, s[20:23], s85 offen offset:1024 sc1\ns_add_u32 s61, s79, 88219648\ns_lshl_b32 s62, s75, 3\ns_add_u32 s61, s61, s62\ns_waitcnt vmcnt(0)\n"
"s_mov_b64 exec, 1\nbuffer_atomic_swap_x2 v[238:239], off, s[20:23], s61 sc1\ns_mov_b64 exec, -1\nLqw0:\nv_bfe_u32 v250, v252, 7, 1\nv_mul_u32_u24 v250, 2176, v250\n"
"v_bfe_u32 v251, v252, 4, 3\nv_lshl_add_u32 v250, v251, 3, v250\ns_lshl_b32 s60, s90, 6\nv_add_u32 v250, s60, v250\ns_add_u32 m0, s50, 32768\ns_add_u32 s63, s54, 458880\n"
"buffer_load_dwordx4 v220, s[12:15], s63 offen lds\nbuffer_load_dwordx4 v221, s[12:15], s63 offen offset:1024 lds\ns_add_u32 m0, s50, 40960\ns_add_u32 s63, s54, 688256\nbuffer_load_dwordx4 v220, s[12:15], s63 offen lds\nbuffer_load_dwordx4 v221, s[12:15], s63 offen offset:1024 lds\n"
"s_add_u32 m0, s50, 49152\ns_add_u32 s63, s54, 917504\nbuffer_load_dwordx4 v220, s[12:15], s63 offen lds\nbuffer_load_dwordx4 v221, s[12:15], s63 offen offset:1024 lds\ns_add_u32 m0, s50, 57344\ns_add_u32 s63, s54, 1146880\n"
"buffer_load_dwordx4 v220, s[12:15], s63 offen lds\nbuffer_load_dwordx4 v221, s[12:15], s63 offen offset:1024 lds\ns_add_u32 m0, s50, 65536\ns_add_u32 s63, s54, 917632\nbuffer_load_dwordx4 v220, s[12:15], s63 offen lds\nbuffer_load_dwordx4 v221, s[12:15], s63 offen offset:1024 lds\n"
"s_add_u32 m0, s50, 73728\ns_add_u32 s63, s54, 1147008\nbuffer_load_dwordx4 v220, s[12:15], s63 offen lds\nbuffer_load_dwordx4 v221, s[12:15], s63 offen offset:1024 lds\nv_mov_b32 v248, 0\nv_mov_b32 v249, 0\n"
"s_add_u32 s61, s80, 88219648\ns_mov_b64 exec, 0xffff\nbuffer_atomic_or_x2 v[248:249], v250, s[20:23], s61 offen sc0 sc1\ns_mov_b64 exec, -1\nv_mov_b32 v230, 0\nv_mov_b32 v231, 0\n"
"v_mov_b32 v232, 0\nv_mov_b32 v233, 0\nv_dot2c_f32_bf16 v230, s76, v192\nv_dot2c_f32_bf16 v231, v192, v192\nv_dot2c_f32_bf16 v232, s76, v200\nv_dot2c_f32_bf16 v233, v200, v200\n"
"v_dot2c_f32_bf16 v230, s76, v193\nv_dot2c_f32_bf16 v231, v193, v193\nv_dot2c_f32_bf16 v232, s76, v201\nv_dot2c_f32_bf16 v233, v201, v201\nv_dot2c_f32_bf16 v230, s76, v194\nv_dot2c_f32_bf16 v231, v194, v194\n"
"v_dot2c_f32_bf16 v232, s76, v202\nv_dot2c_f32_bf16 v233, v202, v202\nv_dot2c_f32_bf16 v230, s76, v195\nv_dot2c_f32_bf16 v231, v195, v195\nv_dot2c_f32_bf16 v232, s76, v203\nv_dot2c_f32_bf16 v233, v203, v203\n"
"v_dot2c_f32_bf16 v230, s76, v196\nv_dot2c_f32_bf16 v231, v196, v196\nv_dot2c_f32_bf16 v232, s76, v204\nv_dot2c_f32_bf16 v233, v204, v204\nv_dot2c_f32_bf16 v230, s76, v197\nv_dot2c_f32_bf16 v231, v197, v197\n"
"v_dot2c_f32_bf16 v232, s76, v205\nv_dot2c_f32_bf16 v233, v205, v205\nv_dot2c_f32_bf16 v230, s76, v198\nv_dot2c_f32_bf16 v231, v198, v198\nv_dot2c_f32_bf16 v232, s76, v206\nv_dot2c_f32_bf16 v233, v206, v206\n"
"v_dot2c_f32_bf16 v230, s76, v199\nv_dot2c_f32_bf16 v231, v199, v199\nv_dot2c_f32_bf16 v232, s76, v207\nv_dot2c_f32_bf16 v233, v207, v207\nv_lshrrev_b32 v234, 1, v252\nv_permlane32_swap_b32 v230, v231\n"
"s_nop 0\nv_permlane32_swap_b32 v232, v233\nv_add_f32 v230, v230, v231\nv_add_f32 v232, v232, v233\nv_mov_b32 v231, s46\ns_nop 0\n"
"v_permlane16_swap_b32 v230, v232\nv_add_f32 v230, v230, v232\ns_lshr_b32 s61, s49, 3\ns_lshl2_add_u32 s61, s53, s61\ns_add_u32 s61, s61, 82313216\nbuffer_store_dwordx2 v[230:231], v234, s[20:23], s61 offen sc1\n"
"s_waitcnt vmcnt(1)\nv_cmp_eq_u64 s[86:87], s[46:47], v[248:249]\ns_nop 0\ns_and_b32 s62, s86, 0xffff\ns_cmp_eq_u32 s62, 0xffff\ns_cbranch_scc0 Lq263\n"
"Lq264:\ns_add_u32 s64, s70, 10485760\nbuffer_load_dwordx4 a[0:3], v252, s[20:23], s64 offen offset:0 sc1\ns_add_u32 s65, s70, 10518528\nbuffer_load_dwordx4 a[4:7], v252, s[20:23], s65 offen offset:0 sc1\ns_add_u32 s66, s71, 10485760\n"
"buffer_load_dwordx4 a[8:11], v252, s[20:23], s66 offen offset:0 sc1\ns_add_u32 s67, s71, 10518528\nbuffer_load_dwordx4 a[12:15], v252, s[20:23], s67 offen offset:0 sc1\nbuffer_load_dwordx4 a[16:19], v252, s[20:23], s64 offen offset:1024 sc1\nbuffer_load_dwordx4 a[20:23], v252, s[20:23], s65 offen offset:1024 sc1\nbuffer_load_dwordx4 a[24:27], v252, s[20:23], s66 offen offset:1024 sc1\n"
"buffer_load_dwordx4 a[28:31], v252, s[20:23], s67 offen offset:1024 sc1\ns_barrier\nR 160,188,40960\nR 162,188,45056\nR 164,188,49152\nR 166,188,53248\n"
"R 168,189,40960\nR 170,189,45056\nR 172,189,49152\nR 174,189,53248\nR 176,190,40960\nR 178,190,45056\n"
"R 180,190,49152\nR 182,190,53248\nbuffer_load_dwordx4 a[32:35], v252, s[20:23], s64 offen offset:2048 sc1\nbuffer_load_dwordx4 a[36:39], v252, s[20:23], s65 offen offset:2048 sc1\nbuffer_load_dwordx4 a[40:43], v252, s[20:23], s66 offen offset:2048 sc1\nbuffer_load_dwordx4 a[44:47], v252, s[20:23], s67 offen offset:2048 sc1\n"
"buffer_load_dwordx4 a[48:51], v252, s[20:23], s64 offen offset:3072 sc1\nbuffer_load_dwordx4 a[52:55], v252, s[20:23], s65 offen offset:3072 sc1\nbuffer_load_dwordx4 a[56:59], v252, s[20:23], s66 offen offset:3072 sc1\nbuffer_load_dwordx4 a[60:63], v252, s[20:23], s67 offen offset:3072 sc1\ns_waitcnt vmcnt(15) lgkmcnt(10)\nv_mfma_f32_16x16x32_bf16 v[32:35], v[160:163], a[0:3], 0\n"
"s_add_u32 s64, s70, 10489856\nbuffer_load_dwordx4 a[64:67], v252, s[20:23], s64 offen offset:0 sc1\ns_waitcnt vmcnt(12) lgkmcnt(8)\nM 32,164,16\nv_mfma_f32_16x16x32_bf16 v[48:51], v[160:163], a[4:7], 0\ns_waitcnt vmcnt(11)\n"
"M 48,164,20\nv_mfma_f32_16x16x32_bf16 v[64:67], v[160:163], a[8:11], 0\nv_lshrrev_b32 v238, 7, v252\ns_waitcnt vmcnt(10)\nM 64,164,24\ns_add_u32 s65, s70, 10522624\n"
"buffer_load_dwordx4 a[68:71], v252, s[20:23], s65 offen offset:0 sc1\nv_lshlrev_b32 v238, 9, v238\nv_mfma_f32_16x16x32_bf16 v[80:83], v[160:163], a[12:15], 0\nv_and_b32 v239, 0x70, v252\ns_waitcnt vmcnt(10)\nM 80,164,28\n"
"s_and_b32 s60, s40, 3\ns_lshl_b32 s60, s60, 7\nv_add3_u32 v238, v238, v239, s60\nG 4\nv_mfma_f32_16x16x32_bf16 v[36:39], v[168:171], a[0:3], 0\nR 160,191,40960\n"
"M 36,172,16\ns_lshl_b32 s61, s42, 14\ns_add_u32 s61, s61, 84672512\nbuffer_load_dwordx4 v[0:3], v238, s[20:23], s61 offen sc1\nv_mfma_f32_16x16x32_bf16 v[52:55], v[168:171], a[4:7], 0\nR 162,191,45056\n"
"s_lshl_b32 s61, s42, 14\ns_add_u32 s61, s61, 84676608\nbuffer_load_dwordx4 v[4:7], v238, s[20:23], s61 offen sc1\nM 52,172,20\ns_add_u32 s66, s71, 10489856\nbuffer_load_dwordx4 a[72:75], v252, s[20:23], s66 offen offset:0 sc1\n"
"s_lshl_b32 s61, s42, 14\ns_add_u32 s61, s61, 84680704\nbuffer_load_dwordx4 v[8:11], v238, s[20:23], s61 offen sc1\nv_mfma_f32_16x16x32_bf16 v[68:71], v[168:171], a[8:11], 0\nR 164,191,49152\ns_lshl_b32 s61, s42, 14\n"
"s_add_u32 s61, s61, 84684800\nbuffer_load_dwordx4 v[12:15], v238, s[20:23], s61 offen sc1\nM 68,172,24\nv_bfe_u32 v239, v238, 2, 7\nv_mfma_f32_16x16x32_bf16 v[84:87], v[168:171], a[12:15], 0\nR 166,191,53248\n"
"M 84,172,28\nbuffer_load_dword v239, v239, s[32:35], s54 offen offset:0\nG 4\nv_mfma_f32_16x16x32_bf16 v[40:43], v[176:179], a[0:3], 0\nR 168,184,0\nM 40,180,16\n"
"s_add_u32 s67, s71, 10522624\nbuffer_load_dwordx4 a[76:79], v252, s[20:23], s67 offen offset:0 sc1\nv_mfma_f32_16x16x32_bf16 v[56:59], v[176:179], a[4:7], 0\nR 170,184,4096\nM 56,180,20\nv_mfma_f32_16x16x32_bf16 v[72:75], v[176:179], a[8:11], 0\n"
"R 172,184,8192\nM 72,180,24\nv_mfma_f32_16x16x32_bf16 v[88:91], v[176:179], a[12:15], 0\nR 174,184,12288\nM 88,180,28\nG 4\n"
"v_mfma_f32_16x16x32_bf16 v[44:47], v[160:163], a[0:3], 0\nR 176,185,0\nM 44,164,16\nbuffer_load_dwordx4 a[80:83], v252, s[20:23], s64 offen offset:1024 sc1\nv_mfma_f32_16x16x32_bf16 v[60:63], v[160:163], a[4:7], 0\nR 178,185,4096\n"
"M 60,164,20\nv_mfma_f32_16x16x32_bf16 v[76:79], v[160:163], a[8:11], 0\nR 180,185,8192\nM 76,164,24\nbuffer_load_dwordx4 a[84:87], v252, s[20:23], s65 offen offset:1024 sc1\nv_mfma_f32_16x16x32_bf16 v[92:95], v[160:163], a[12:15], 0\n"
"R 182,185,12288\nM 92,164,28\nG 4\nv_mfma_f32_16x16x32_bf16 v[96:99], v[168:171], a[0:3], 0\nR 160,186,0\nM 96,172,16\n"
"v_mfma_f32_16x16x32_bf16 v[112:115], v[168:171], a[4:7], 0\nR 162,186,4096\nM 112,172,20\nbuffer_load_dwordx4 a[88:91], v252, s[20:23], s66 offen offset:1024 sc1\nv_mfma_f32_16x16x32_bf16 v[128:131], v[168:171], a[8:11], 0\nR 164,186,8192\n"
"M 128,172,24\nv_mfma_f32_16x16x32_bf16 v[144:147], v[168:171], a[12:15], 0\nR 166,186,12288\nM 144,172,28\ns_barrier\nG 4\n"
"v_mfma_f32_16x16x32_bf16 v[100:103], v[176:179], a[0:3], 0\nR 168,187,0\nM 100,180,16\nbuffer_load_dwordx4 a[92:95], v252, s[20:23], s67 offen offset:1024 sc1\nv_mfma_f32_16x16x32_bf16 v[116:119], v[176:179], a[4:7], 0\nR 170,187,4096\n"
"M 116,180,20\nv_mfma_f32_16x16x32_bf16 v[132:135], v[176:179], a[8:11], 0\nR 172,187,8192\nM 132,180,24\nv_mfma_f32_16x16x32_bf16 v[148:151], v[176:179], a[12:15], 0\nR 174,187,12288\n"
"M 148,180,28\nG 4\nv_mfma_f32_16x16x32_bf16 v[104:107], v[160:163], a[0:3], 0\nR 176,184,16384\nv_lshrrev_b32 v242, 7, v252\nM 104,164,16\n"
"buffer_load_dwordx4 a[96:99], v252, s[20:23], s64 offen offset:2048 sc1\nv_lshlrev_b32 v242, 9, v242\nv_mfma_f32_16x16x32_bf16 v[120:123], v[160:163], a[4:7], 0\nR 178,184,20480\nv_and_b32 v243, 0x70, v252\ns_waitcnt vmcnt(10)\n"
"v_min3_u32 v240, v1, v3, v5\nM 120,164,20\nbuffer_load_dwordx4 a[100:103], v252, s[20:23], s65 offen offset:2048 sc1\ns_and_b32 s60, s40, 3\ns_lshl_b32 s60, s60, 7\nv_add3_u32 v242, v242, v243, s60\n"
"s_waitcnt vmcnt(9)\nv_min3_u32 v240, v240, v7, v9\nv_mfma_f32_16x16x32_bf16 v[136:139], v[160:163], a[8:11], 0\nR 180,184,24576\ns_waitcnt vmcnt(8)\nv_min3_u32 v240, v240, v11, v13\n"
"v_min3_u32 v240, v240, v15, v15\nM 136,164,24\ns_lshl_b32 s61, s42, 14\ns_add_u32 s61, s61, 84934656\nbuffer_load_dwordx4 v[16:19], v242, s[20:23], s61 offen sc1\nv_max3_u32 v241, v1, v3, v5\n"
"v_mfma_f32_16x16x32_bf16 v[152:155], v[160:163], a[12:15], 0\nR 182,184,28672\ns_lshl_b32 s61, s42, 14\ns_add_u32 s61, s61, 84938752\nbuffer_load_dwordx4 v[20:23], v242, s[20:23], s61 offen sc1\nv_max3_u32 v241, v241, v7, v9\n"
"M 152,164,28\ns_lshl_b32 s61, s42, 14\ns_add_u32 s61, s61, 84942848\nbuffer_load_dwordx4 v[24:27], v242, s[20:23], s61 offen sc1\nG 4\nv_max3_u32 v241, v241, v11, v13\n"
"v_mfma_f32_16x16x32_bf16 v[108:111], v[168:171], a[0:3], 0\nR 160,185,16384\ns_lshl_b32 s61, s42, 14\ns_add_u32 s61, s61, 84946944\nbuffer_load_dwordx4 v[28:31], v242, s[20:23], s61 offen sc1\nv_max3_u32 v241, v241, v15, v15\n"
"M 108,172,16\nbuffer_load_dwordx4 a[104:107], v252, s[20:23], s66 offen offset:2048 sc1\nv_bfe_u32 v243, v242, 2, 7\nv_cmp_ne_u32 s[86:87], s46, v240\nv_mfma_f32_16x16x32_bf16 v[124:127], v[168:171], a[4:7], 0\nR 162,185,20480\n"
"v_cmp_ne_u32 s[88:89], s46, v241\nM 124,172,20\nbuffer_load_dword v243, v243, s[32:35], s54 offen offset:128\ns_or_b64 s[86:87], s[86:87], s[88:89]\ns_cbranch_scc1 Lq326\nLq327:\n"
"v_mfma_f32_16x16x32_bf16 v[140:143], v[168:171], a[8:11], 0\nR 164,185,24576\nv_add_f32 v0, v0, v8\nv_add_f32 v2, v2, v10\nM 140,172,24\nv_add_f32 v4, v4, v12\n"
"v_add_f32 v6, v6, v14\nv_add_f32 v0, v0, v4\nv_mfma_f32_16x16x32_bf16 v[156:159], v[168:171], a[12:15], 0\nR 166,185,28672\nv_add_f32 v2, v2, v6\nv_add_f32_dpp v0, v0, v0 row_ror:8 row_mask:0xf bank_mask:0xf\n"
"M 156,172,28\nG 4\nv_add_f32_dpp v2, v2, v2 row_ror:8 row_mask:0xf bank_mask:0xf\nv_mov_b32 v4, v0\nv_mov_b32 v5, v2\nM 32,176,32\n"
"R 168,186,16384\nv_permlane32_swap_b32 v4, v0\nv_permlane32_swap_b32 v5, v2\nM 32,180,48\nbuffer_load_dwordx4 a[108:111], v252, s[20:23], s67 offen offset:2048 sc1\nv_add_f32 v0, v0, v4\n"
"M 48,176,36\nR 170,186,20480\nv_add_f32 v2, v2, v5\nM 48,180,52\nv_mov_b32 v4, v0\nv_mov_b32 v5, v2\n"
"M 64,176,40\nR 172,186,24576\nv_permlane16_swap_b32 v4, v0\nM 64,180,56\nbuffer_load_dwordx4 a[112:115], v252, s[20:23], s64 offen offset:3072 sc1\nM 80,176,44\n"
"R 174,186,28672\nv_permlane16_swap_b32 v5, v2\nM 80,180,60\ns_barrier\nG 4\nM 36,160,32\n"
"R 176,187,16384\nv_add_f32 v0, v0, v4\nM 36,164,48\nv_add_f32 v2, v2, v5\nv_and_b32 v8, s77, v0\nM 52,160,36\n"
"R 178,187,20480\nv_sub_f32 v9, v0, v8\nM 52,164,52\nbuffer_load_dwordx4 a[116:119], v252, s[20:23], s65 offen offset:3072 sc1\nM 68,160,40\nR 180,187,24576\n"
"s_waitcnt vmcnt(16)\nv_lshlrev_b32 v10, 16, v239\nM 68,164,56\nv_cvt_pk_bf16_f32 v4, v8, v9\nv_cvt_pk_bf16_f32 v5, v8, v10\nM 84,160,44\n"
"R 182,187,28672\nv_and_b32 v8, s77, v2\nM 84,164,60\nG 4\nv_sub_f32 v9, v2, v8\nv_and_b32 v10, s77, v239\n"
"M 40,168,32\nR 160,184,32768\nv_cvt_pk_bf16_f32 v6, v8, v9\nM 40,172,48\nbuffer_load_dwordx4 a[120:123], v252, s[20:23], s66 offen offset:3072 sc1\nM 56,168,36\n"
"R 162,184,36864\nv_cvt_pk_bf16_f32 v7, v8, v10\nM 56,172,52\nv_and_b32 v238, 0x1ff, v238\nv_add_u32 v238, 157184, v238\nM 72,168,40\n"
"R 164,184,40960\nds_write_b128 v238, v[4:7]\nM 72,172,56\nM 88,168,44\nR 166,184,45056\nM 88,172,60\n"
"G 5\nM 44,176,32\nR 168,185,32768\nM 44,180,48\nbuffer_load_dwordx4 a[124:127], v252, s[20:23], s67 offen offset:3072 sc1\nM 60,176,36\n"
"R 170,185,36864\nM 60,180,52\nM 76,176,40\nR 172,185,40960\nv_lshrrev_b32 v238, 7, v252\nM 76,180,56\n"
"s_add_u32 s64, s70, 10493952\nbuffer_load_dwordx4 a[128:131], v252, s[20:23], s64 offen offset:0 sc1\nv_lshlrev_b32 v238, 9, v238\nM 92,176,44\nR 174,185,45056\nv_and_b32 v239, 0x70, v252\n"
"s_waitcnt vmcnt(10)\nv_min3_u32 v244, v17, v19, v21\nM 92,180,60\ns_and_b32 s60, s40, 3\ns_lshl_b32 s60, s60, 7\nv_add3_u32 v238, v238, v239, s60\n"
"G 4\ns_waitcnt vmcnt(9)\nv_min3_u32 v244, v244, v23, v25\nM 96,160,32\nR 176,186,32768\ns_lshl_b32 s61, s42, 14\n"
"s_add_u32 s61, s61, 85196800\nbuffer_load_dwordx4 v[0:3], v238, s[20:23], s61 offen sc1\ns_waitcnt vmcnt(9)\nv_min3_u32 v244, v244, v27, v29\nM 96,164,48\ns_add_u32 s65, s70, 10526720\n"
"buffer_load_dwordx4 a[132:135], v252, s[20:23], s65 offen offset:0 sc1\nv_min3_u32 v244, v244, v31, v31\nM 112,160,36\nR 178,186,36864\ns_lshl_b32 s61, s42, 14\ns_add_u32 s61, s61, 85200896\n"
"buffer_load_dwordx4 v[4:7], v238, s[20:23], s61 offen sc1\nv_max3_u32 v245, v17, v19, v21\nM 112,164,52\ns_lshl_b32 s61, s42, 14\ns_add_u32 s61, s61, 85204992\nbuffer_load_dwordx4 v[8:11], v238, s[20:23], s61 offen sc1\n"
"v_max3_u32 v245, v245, v23, v25\nM 128,160,40\nR 180,186,40960\ns_lshl_b32 s61, s42, 14\ns_add_u32 s61, s61, 85209088\nbuffer_load_dwordx4 v[12:15], v238, s[20:23], s61 offen sc1\n"
"v_max3_u32 v245, v245, v27, v29\nM 128,164,56\ns_add_u32 m0, s50, 81920\nv_bfe_u32 v239, v238, 2, 7\nv_max3_u32 v245, v245, v31, v31\nv_cmp_ne_u32 s[86:87], s46, v244\n"
"M 144,160,44\nR 182,186,45056\ns_add_u32 s63, s54, 1376256\nv_cmp_ne_u32 s[88:89], s46, v245\nM 144,164,60\nbuffer_load_dword v239, v239, s[32:35], s54 offen offset:256\n"
"s_barrier\ns_or_b64 s[86:87], s[86:87], s[88:89]\ns_cbranch_scc1 Lq338\nLq339:\nG 4\nM 100,168,32\n"
"R 160,187,32768\nv_add_f32 v16, v16, v24\nv_add_f32 v18, v18, v26\nM 100,172,48\ns_add_u32 s66, s71, 10493952\nbuffer_load_dwordx4 a[136:139], v252, s[20:23], s66 offen offset:0 sc1\n"
"v_add_f32 v20, v20, v28\nM 116,168,36\nR 162,187,36864\nv_add_f32 v22, v22, v30\nv_add_f32 v16, v16, v20\nM 116,172,52\n"
"buffer_load_dwordx4 v220, s[12:15], s63 offen lds\nv_add_f32 v18, v18, v22\nM 132,168,40\nR 164,187,40960\nv_add_f32_dpp v16, v16, v16 row_ror:8 row_mask:0xf bank_mask:0xf\nv_add_f32_dpp v18, v18, v18 row_ror:8 row_mask:0xf bank_mask:0xf\n"
"M 132,172,56\ns_add_u32 s67, s71, 10526720\nbuffer_load_dwordx4 a[140:143], v252, s[20:23], s67 offen offset:0 sc1\nv_mov_b32 v20, v16\nM 148,168,44\nR 166,187,45056\n"
"v_mov_b32 v21, v18\nv_permlane32_swap_b32 v20, v16\nM 148,172,60\nG 4\nv_permlane32_swap_b32 v21, v18\nv_add_f32 v16, v16, v20\n"
"v_add_f32 v18, v18, v21\nM 104,176,32\nR 168,184,49152\nbuffer_load_dwordx4 v221, s[12:15], s63 offen offset:1024 lds\nv_mov_b32 v20, v16\nM 104,180,48\n"
"s_add_u32 m0, s50, 90112\ns_add_u32 s63, s54, 1605632\nbuffer_load_dwordx4 v220, s[12:15], s63 offen lds\nv_mov_b32 v21, v18\nM 120,176,36\nR 170,184,53248\n"
"v_permlane16_swap_b32 v20, v16\nv_permlane16_swap_b32 v21, v18\nM 120,180,52\nbuffer_load_dwordx4 a[144:147], v252, s[20:23], s64 offen offset:1024 sc1\nv_add_f32 v16, v16, v20\nM 136,176,40\n"
"R 172,184,57344\nv_add_f32 v18, v18, v21\nv_and_b32 v24, s77, v16\nM 136,180,56\nbuffer_load_dwordx4 v221, s[12:15], s63 offen offset:1024 lds\nv_sub_f32 v25, v16, v24\n"
"M 152,176,44\nR 174,184,61440\ns_waitcnt vmcnt(19)\nv_lshlrev_b32 v26, 16, v243\nM 152,180,60\nG 4\n"
"v_cvt_pk_bf16_f32 v20, v24, v25\nv_cvt_pk_bf16_f32 v21, v24, v26\nM 108,160,32\nR 176,185,49152\nv_and_b32 v24, s77, v18\nM 108,164,48\n"
"buffer_load_dwordx4 a[148:151], v252, s[20:23], s65 offen offset:1024 sc1\nM 124,160,36\nR 178,185,53248\nv_sub_f32 v25, v18, v24\nM 124,164,52\nv_and_b32 v26, s77, v243\n"
"v_cvt_pk_bf16_f32 v22, v24, v25\nM 140,160,40\nR 180,185,57344\nv_cvt_pk_bf16_f32 v23, v24, v26\nM 140,164,56\nv_and_b32 v242, 0x1ff, v242\n"
"v_add_u32 v242, 157696, v242\nM 156,160,44\nR 182,185,61440\nds_write_b128 v242, v[20:23]\nM 156,164,60\nG 5\n"
"M 32,168,64\nR 160,186,49152\nM 32,172,80\nbuffer_load_dwordx4 a[152:155], v252, s[20:23], s66 offen offset:1024 sc1\nM 48,168,68\nR 162,186,53248\n"
"M 48,172,84\ns_add_u32 m0, s50, 98304\nM 64,168,72\nR 164,186,57344\ns_add_u32 s63, s54, 1376384\nM 64,172,88\n"
"buffer_load_dwordx4 a[156:159], v252, s[20:23], s67 offen offset:1024 sc1\nM 80,168,76\nR 166,186,61440\nM 80,172,92\nG 4\ns_barrier\n"
"M 36,176,64\nR 168,187,49152\nv_lshrrev_b32 v242, 7, v252\nM 36,180,80\nbuffer_load_dwordx4 v220, s[12:15], s63 offen lds\nv_lshlrev_b32 v242, 9, v242\n"
"M 52,176,68\nR 170,187,53248\nv_and_b32 v243, 0x70, v252\ns_waitcnt vmcnt(14)\nv_min3_u32 v240, v1, v3, v5\nM 52,180,84\n"
"buffer_load_dwordx4 a[160:163], v252, s[20:23], s64 offen offset:2048 sc1\ns_and_b32 s60, s40, 1\ns_lshl_b32 s60, s60, 7\nv_add3_u32 v242, v242, v243, s60\ns_waitcnt vmcnt(14)\nv_min3_u32 v240, v240, v7, v9\n"
"M 68,176,72\nR 172,187,57344\ns_waitcnt vmcnt(13)\nv_min3_u32 v240, v240, v11, v13\nv_min3_u32 v240, v240, v15, v15\nM 68,180,88\n"
"s_lshl_b32 s61, s42, 14\ns_add_u32 s61, s61, 85458944\nbuffer_load_dwordx4 v[16:19], v242, s[20:23], s61 offen sc1\nv_max3_u32 v241, v1, v3, v5\nM 84,176,76\nR 174,187,61440\n"
"s_lshl_b32 s61, s42, 14\ns_add_u32 s61, s61, 85463040\nbuffer_load_dwordx4 v[20:23], v242, s[20:23], s61 offen sc1\nv_max3_u32 v241, v241, v7, v9\nM 84,180,92\ns_lshl_b32 s61, s42, 14\n"
"s_add_u32 s61, s61, 85467136\nbuffer_load_dwordx4 v[24:27], v242, s[20:23], s61 offen sc1\nG 4\nv_max3_u32 v241, v241, v11, v13\nM 40,160,64\nR 176,188,0\n"
"s_lshl_b32 s61, s42, 14\ns_add_u32 s61, s61, 85471232\nbuffer_load_dwordx4 v[28:31], v242, s[20:23], s61 offen sc1\nv_max3_u32 v241, v241, v15, v15\nM 40,164,80\nbuffer_load_dwordx4 a[164:167], v252, s[20:23], s65 offen offset:2048 sc1\n"
"buffer_load_dwordx4 v221, s[12:15], s63 offen offset:1024 lds\nv_bfe_u32 v243, v242, 2, 7\nv_cmp_ne_u32 s[86:87], s46, v240\nM 56,160,68\nR 178,188,4096\ns_add_u32 m0, s50, 106496\n"
"s_add_u32 s63, s54, 1605760\nv_cmp_ne_u32 s[88:89], s46, v241\nM 56,164,84\nbuffer_load_dword v243, v243, s[32:35], s55 offen offset:0\ns_or_b64 s[86:87], s[86:87], s[88:89]\ns_cbranch_scc1 Lq350\n"
"Lq351:\nM 72,160,72\nR 180,188,8192\nbuffer_load_dwordx4 v220, s[12:15], s63 offen lds\nv_add_f32 v0, v0, v8\nM 72,164,88\n"
"buffer_load_dwordx4 a[168:171], v252, s[20:23], s66 offen offset:2048 sc1\nv_add_f32 v2, v2, v10\nM 88,160,76\nR 182,188,12288\nv_add_f32 v4, v4, v12\nv_add_f32 v6, v6, v14\n"
"M 88,164,92\nG 4\nv_add_f32 v0, v0, v4\nv_add_f32 v2, v2, v6\ns_nop 0\nv_add_f32_dpp v0, v0, v0 row_ror:8 row_mask:0xf bank_mask:0xf\n"
"M 44,168,64\nR 160,189,0\nbuffer_load_dwordx4 v221, s[12:15], s63 offen offset:1024 lds\nv_add_f32_dpp v2, v2, v2 row_ror:8 row_mask:0xf bank_mask:0xf\nM 44,172,80\nv_mov_b32 v4, v0\n"
"v_mov_b32 v5, v2\ns_nop 0\nv_permlane32_swap_b32 v4, v0\nM 60,168,68\nR 162,189,4096\nv_permlane32_swap_b32 v5, v2\n"
"v_add_f32 v0, v0, v4\nM 60,172,84\nbuffer_load_dwordx4 a[172:175], v252, s[20:23], s67 offen offset:2048 sc1\nv_add_f32 v2, v2, v5\nM 76,168,72\nR 164,189,8192\n"
"v_mov_b32 v4, v0\nv_mov_b32 v5, v2\nM 76,172,88\nv_permlane16_swap_b32 v4, v0\nv_permlane16_swap_b32 v5, v2\nv_add_f32 v0, v0, v4\n"
"M 92,168,76\nR 166,189,12288\nv_add_f32 v2, v2, v5\nv_and_b32 v8, s77, v0\nM 92,172,92\nG 4\n"
"v_sub_f32 v9, v0, v8\ns_waitcnt vmcnt(23)\nv_lshlrev_b32 v10, 16, v239\nv_cvt_pk_bf16_f32 v4, v8, v9\nM 96,176,64\nR 168,190,0\n"
"v_cvt_pk_bf16_f32 v5, v8, v10\nv_and_b32 v8, s77, v2\nM 96,180,80\nbuffer_load_dwordx4 a[176:179], v252, s[20:23], s64 offen offset:3072 sc1\nv_sub_f32 v9, v2, v8\nM 112,176,68\n"
"R 170,190,4096\nv_and_b32 v10, s77, v239\nM 112,180,84\nv_cvt_pk_bf16_f32 v6, v8, v9\nv_cvt_pk_bf16_f32 v7, v8, v10\nM 128,176,72\n"
"R 172,190,8192\nv_and_b32 v238, 0x1ff, v238\nM 128,180,88\ns_add_u32 m0, s50, 114688\nv_add_u32 v238, 158208, v238\nds_write_b128 v238, v[4:7]\n"
"M 144,176,76\nR 174,190,12288\ns_add_u32 s63, s54, 1835008\nM 144,180,92\ns_waitcnt vmcnt(17) lgkmcnt(1)\ns_barrier\n"
"M 100,160,64\nR 176,191,0\nM 100,164,80\nbuffer_load_dwordx4 a[180:183], v252, s[20:23], s65 offen offset:3072 sc1\nM 116,160,68\nR 178,191,4096\n"
"M 116,164,84\nbuffer_load_dwordx4 v220, s[12:15], s63 offen lds\nM 132,160,72\nR 180,191,8192\nM 132,164,88\nbuffer_load_dwordx4 a[184:187], v252, s[20:23], s66 offen offset:3072 sc1\n"
"M 148,160,76\nR 182,191,12288\nM 148,164,92\nG 4\nM 104,168,64\nR 160,188,16384\n"
"buffer_load_dwordx4 v221, s[12:15], s63 offen offset:1024 lds\nM 104,172,80\ns_add_u32 m0, s50, 0\ns_add_u32 s63, s54, 2064384\nbuffer_load_dwordx4 v220, s[12:15], s63 offen lds\nM 120,168,68\n"
"R 162,188,20480\nM 120,172,84\nbuffer_load_dwordx4 a[188:191], v252, s[20:23], s67 offen offset:3072 sc1\nM 136,168,72\nR 164,188,24576\nM 136,172,88\n"
"buffer_load_dwordx4 v221, s[12:15], s63 offen offset:1024 lds\nM 152,168,76\nR 166,188,28672\ns_waitcnt vmcnt(17)\nv_min3_u32 v244, v17, v19, v21\ns_waitcnt vmcnt(16)\n"
"v_min3_u32 v244, v244, v23, v25\nM 152,172,92\nG 4\ns_waitcnt vmcnt(15)\nv_min3_u32 v244, v244, v27, v29\nv_min3_u32 v244, v244, v31, v31\n"
"v_max3_u32 v245, v17, v19, v21\nM 108,176,64\nR 168,189,16384\nv_max3_u32 v245, v245, v23, v25\nv_max3_u32 v245, v245, v27, v29\nM 108,180,80\n"
"s_add_u32 s64, s70, 10498048\nbuffer_load_dwordx4 a[192:195], v252, s[20:23], s64 offen offset:0 sc1\nv_max3_u32 v245, v245, v31, v31\nM 124,176,68\nR 170,189,20480\nv_cmp_ne_u32 s[86:87], s46, v244\n"
"v_cmp_ne_u32 s[88:89], s46, v245\nM 124,180,84\ns_or_b64 s[86:87], s[86:87], s[88:89]\ns_cbranch_scc1 Lq362\nLq363:\nv_add_f32 v16, v16, v24\n"
"v_add_f32 v18, v18, v26\nM 140,176,72\nR 172,189,24576\nv_add_f32 v20, v20, v28\nv_add_f32 v22, v22, v30\nM 140,180,88\n"
"s_add_u32 s65, s70, 10530816\nbuffer_load_dwordx4 a[196:199], v252, s[20:23], s65 offen offset:0 sc1\nv_add_f32 v16, v16, v20\nM 156,176,76\nR 174,189,28672\nv_add_f32 v18, v18, v22\n"
"v_add_f32_dpp v16, v16, v16 row_ror:8 row_mask:0xf bank_mask:0xf\nM 156,180,92\nG 4\nv_add_f32_dpp v18, v18, v18 row_ror:8 row_mask:0xf bank_mask:0xf\nv_mov_b32 v20, v16\nv_mov_b32 v21, v18\n"
"M 32,160,96\nR 176,190,16384\nv_permlane32_swap_b32 v20, v16\nv_permlane32_swap_b32 v21, v18\nM 32,164,112\nv_add_f32 v16, v16, v20\n"
"v_add_f32 v18, v18, v21\nv_mov_b32 v20, v16\nM 48,160,100\nR 178,190,20480\nv_mov_b32 v21, v18\nv_permlane16_swap_b32 v20, v16\n"
"M 48,164,116\ns_add_u32 s66, s71, 10498048\nbuffer_load_dwordx4 a[200:203], v252, s[20:23], s66 offen offset:0 sc1\nv_permlane16_swap_b32 v21, v18\nM 64,160,104\nR 180,190,24576\n"
"v_add_f32 v16, v16, v20\nv_add_f32 v18, v18, v21\nM 64,164,120\ns_add_u32 m0, s50, 8192\nv_and_b32 v24, s77, v16\nv_sub_f32 v25, v16, v24\n"
"s_waitcnt vmcnt(15)\nv_lshlrev_b32 v26, 16, v243\nM 80,160,108\nR 182,190,28672\ns_add_u32 s63, s54, 2293760\nv_cvt_pk_bf16_f32 v20, v24, v25\n"
"M 80,164,124\ns_waitcnt vmcnt(12)\ns_barrier\nG 4\nM 36,168,96\nR 160,191,16384\n"
"v_cvt_pk_bf16_f32 v21, v24, v26\nM 36,172,112\ns_add_u32 s67, s71, 10530816\nbuffer_load_dwordx4 a[204:207], v252, s[20:23], s67 offen offset:0 sc1\nM 52,168,100\nR 162,191,20480\n"
"v_and_b32 v24, s77, v18\nM 52,172,116\nbuffer_load_dwordx4 v220, s[12:15], s63 offen lds\nM 68,168,104\nR 164,191,24576\nv_sub_f32 v25, v18, v24\n"
"M 68,172,120\nbuffer_load_dwordx4 v221, s[12:15], s63 offen offset:1024 lds\nM 84,168,108\nR 166,191,28672\ns_add_u32 m0, s50, 16384\nM 84,172,124\n"
"G 4\nv_and_b32 v26, s77, v243\nv_cvt_pk_bf16_f32 v22, v24, v25\nM 40,176,96\nR 168,188,32768\ns_add_u32 s63, s54, 2523136\n"
"M 40,180,112\nbuffer_load_dwordx4 a[208:211], v252, s[20:23], s64 offen offset:1024 sc1\nM 56,176,100\nR 170,188,36864\nv_cvt_pk_bf16_f32 v23, v24, v26\nM 56,180,116\n"
"buffer_load_dwordx4 v220, s[12:15], s63 offen lds\nM 72,176,104\nR 172,188,40960\nv_and_b32 v242, 0x1ff, v242\nM 72,180,120\nbuffer_load_dwordx4 a[212:215], v252, s[20:23], s65 offen offset:1024 sc1\n"
"M 88,176,108\nR 174,188,45056\nv_add_u32 v242, 158720, v242\nM 88,180,124\nds_write_b128 v242, v[20:23]\nG 5\n"
"M 44,160,96\nR 176,189,32768\nbuffer_load_dwordx4 v221, s[12:15], s63 offen offset:1024 lds\nM 44,164,112\nM 60,160,100\nR 178,189,36864\n"
"M 60,164,116\nbuffer_load_dwordx4 a[216:219], v252, s[20:23], s66 offen offset:1024 sc1\nM 76,160,104\nR 180,189,40960\nLq597:\ns_lshr_b32 s60, s45, 3\n"
"s_lshl2_add_u32 s60, s60, s40\ns_lshl_b32 s60, s60, 8\nv_lshl_add_u32 v224, v252, 3, s60\nv_add_u32 v224, Lq597+4096-LqPC, v224\ns_mov_b64 exec, 3\nglobal_load_dword v224, v224, s[98:99]\n"
"s_mov_b64 exec, -1\nM 76,164,120\nM 92,160,108\nR 182,189,45056\nM 92,164,124\nG 5\n"
"M 96,168,96\nR 160,190,32768\nM 96,172,112\nbuffer_load_dwordx4 a[220:223], v252, s[20:23], s67 offen offset:1024 sc1\nM 112,168,100\nR 162,190,36864\n"
"M 112,172,116\nM 128,168,104\nR 164,190,40960\nM 128,172,120\nM 144,168,108\nR 166,190,45056\n"
"s_add_u32 m0, s50, 24576\nM 144,172,124\ns_waitcnt vmcnt(13) lgkmcnt(8)\ns_barrier\nG 4\nM 100,176,96\n"
"R 168,191,32768\ns_add_u32 s63, s54, 2752512\nM 100,180,112\nbuffer_load_dwordx4 a[224:227], v252, s[20:23], s64 offen offset:2048 sc1\nM 116,176,100\nR 170,191,36864\n"
"M 116,180,116\nbuffer_load_dwordx4 a[228:231], v252, s[20:23], s65 offen offset:2048 sc1\nM 132,176,104\nR 172,191,40960\nbuffer_load_dwordx4 v220, s[12:15], s63 offen lds\nM 132,180,120\n"
"buffer_load_dwordx4 v221, s[12:15], s63 offen offset:1024 lds\nM 148,176,108\nR 174,191,45056\ns_add_u32 m0, s50, 32768\nM 148,180,124\nG 4\n"
"M 104,160,96\nR 176,188,49152\ns_add_u32 s63, s54, 2981888\nM 104,164,112\nbuffer_load_dwordx4 a[232:235], v252, s[20:23], s66 offen offset:2048 sc1\nM 120,160,100\n"
"R 178,188,53248\nM 120,164,116\nbuffer_load_dwordx4 v220, s[12:15], s63 offen lds\nM 136,160,104\nR 180,184,0\nM 136,164,120\n"
"buffer_load_dwordx4 v221, s[12:15], s63 offen offset:1024 lds\nM 152,160,108\nR 182,184,4096\nM 152,164,124\nG 4\nM 108,168,96\n"
"R 160,189,49152\nM 108,172,112\nbuffer_load_dwordx4 a[236:239], v252, s[20:23], s67 offen offset:2048 sc1\nM 124,168,100\nR 162,189,53248\nM 124,172,116\n"
"M 140,168,104\nR 164,185,0\nM 140,172,120\nbuffer_load_dwordx4 a[240:243], v252, s[20:23], s64 offen offset:3072 sc1\nM 156,168,108\nR 166,185,4096\n"
"M 156,172,124\nG 4\nM 32,176,128\nR 168,190,49152\nM 32,180,144\nM 48,176,132\n"
"R 170,190,53248\nM 48,180,148\nbuffer_load_dwordx4 a[244:247], v252, s[20:23], s65 offen offset:3072 sc1\nM 64,176,136\nR 172,186,0\nM 64,180,152\n"
"M 80,176,140\nR 174,186,4096\ns_add_u32 m0, s50, 40960\nM 80,180,156\ns_waitcnt vmcnt(13)\ns_barrier\n"
"G 4\nM 36,160,128\nR 176,191,49152\ns_add_u32 s63, s54, 3211264\nM 36,164,144\nbuffer_load_dwordx4 a[248:251], v252, s[20:23], s66 offen offset:3072 sc1\n"
"M 52,160,132\nR 178,191,53248\nM 52,164,148\ns_lshr_b32 s84, s49, 3\nM 68,160,136\nR 180,187,0\n"
"buffer_load_dwordx4 v220, s[12:15], s63 offen lds\nM 68,164,152\ns_add_u32 s84, s84, 82313216\nM 84,160,140\nR 182,187,4096\nM 84,164,156\n"
"buffer_load_dwordx4 v[0:3], v252, s[20:23], s84 offen offset:0 sc1\nG 4\nM 40,168,128\nR 160,184,8192\nbuffer_load_dwordx4 v221, s[12:15], s63 offen offset:1024 lds\nM 40,172,144\n"
"buffer_load_dwordx4 a[252:255], v252, s[20:23], s67 offen offset:3072 sc1\ns_add_u32 s63, s54, 3440640\nM 56,168,132\nR 162,184,12288\ns_add_u32 m0, s50, 49152\nM 56,172,148\n"
"buffer_load_dwordx4 v[4:7], v252, s[20:23], s84 offen offset:1024 sc1\nM 72,168,136\nR 164,184,16384\ns_lshr_b32 s85, s49, 3\nbuffer_load_dwordx4 v220, s[12:15], s63 offen lds\nM 72,172,152\n"
"buffer_load_dwordx4 v[8:11], v252, s[20:23], s84 offen offset:2048 sc1\nM 88,168,140\nR 166,184,20480\ns_add_u32 s85, s85, 82317312\nM 88,172,156\nG 4\n"
"M 44,176,128\nR 168,185,8192\nbuffer_load_dwordx4 v221, s[12:15], s63 offen offset:1024 lds\nM 44,180,144\nbuffer_load_dwordx4 v[12:15], v252, s[20:23], s84 offen offset:3072 sc1\nM 60,176,132\n"
"R 170,185,12288\nM 60,180,148\nbuffer_load_dwordx4 v[16:19], v252, s[20:23], s85 offen offset:0 sc1\nM 76,176,136\nR 172,185,16384\nM 76,180,152\n"
"buffer_load_dwordx4 v[20:23], v252, s[20:23], s85 offen offset:1024 sc1\nM 92,176,140\nR 174,185,20480\nM 92,180,156\nG 4\nM 32,160,160\n"
"R 176,186,8192\nM 32,164,176\nbuffer_load_dwordx4 v[24:27], v252, s[20:23], s85 offen offset:2048 sc1\nM 48,160,164\nR 178,186,12288\nM 48,164,180\n"
"buffer_load_dwordx4 v[28:31], v252, s[20:23], s85 offen offset:3072 sc1\nM 64,160,168\nR 180,186,16384\ns_add_u32 s63, s54, 1835136\nM 64,164,184\ns_add_u32 m0, s50, 57344\n"
"M 80,160,172\nR 182,186,20480\nM 80,164,188\ns_waitcnt vmcnt(17)\ns_barrier\nG 4\n"
"M 36,168,160\nR 160,187,8192\nM 36,172,176\nbuffer_load_dwordx4 v220, s[12:15], s63 offen lds\nM 52,168,164\nR 162,187,12288\n"
"M 52,172,180\nM 68,168,168\nR 164,187,16384\nv_bfe_u32 v250, v252, 5, 1\nv_mul_u32_u24 v250, 2560, v250\nM 68,172,184\n"
"buffer_load_dwordx4 v221, s[12:15], s63 offen offset:1024 lds\nv_and_b32 v251, 16, v252\nM 84,168,172\nR 166,187,20480\ns_add_u32 m0, s50, 65536\nv_lshl_add_u32 v250, v251, 1, v250\n"
"M 84,172,188\ns_lshl_b32 s60, s90, 6\nv_add_u32 v250, s60, v250\nG 4\nM 40,176,160\nR 168,184,24576\n"
"s_add_u32 s63, s54, 2064512\nM 40,180,176\nbuffer_load_dwordx4 v220, s[12:15], s63 offen lds\nM 56,176,164\nR 170,184,28672\nM 56,180,180\n"
"M 72,176,168\nR 172,184,32768\nM 72,180,184\nbuffer_load_dwordx4 v221, s[12:15], s63 offen offset:1024 lds\nM 88,176,172\nR 174,184,36864\n"
"M 88,180,188\nG 4\nM 44,160,160\nR 176,185,24576\nM 44,164,176\nM 60,160,164\n"
"R 178,185,28672\nM 60,164,180\ns_add_u32 m0, s50, 73728\nM 76,160,168\nR 180,185,32768\nM 76,164,184\n"
"s_add_u32 s63, s54, 2293888\nM 92,160,172\nR 182,185,36864\nM 92,164,188\nG 4\nM 32,168,192\n"
"R 160,186,24576\nM 32,172,208\nbuffer_load_dwordx4 v220, s[12:15], s63 offen lds\nM 48,168,196\nR 162,186,28672\nM 48,172,212\n"
"M 64,168,200\nR 164,186,32768\nM 64,172,216\nbuffer_load_dwordx4 v221, s[12:15], s63 offen offset:1024 lds\nM 80,168,204\nR 166,186,36864\n"
"s_add_u32 m0, s50, 81920\nM 80,172,220\ns_waitcnt vmcnt(11)\ns_barrier\nG 4\nM 36,176,192\n"
"R 168,187,24576\ns_add_u32 s63, s54, 2523264\nM 36,180,208\nbuffer_load_dwordx4 v220, s[12:15], s63 offen lds\nM 52,176,196\nR 170,187,28672\n"
"v_min3_u32 v230, v1, v3, v5\nv_min3_u32 v230, v230, v7, v9\nM 52,180,212\ns_waitcnt vmcnt(11)\nv_min3_u32 v230, v230, v11, v13\ns_waitcnt vmcnt(10)\n"
"v_min3_u32 v230, v230, v15, v17\ns_waitcnt vmcnt(9)\nv_min3_u32 v230, v230, v19, v21\nM 68,176,200\nR 172,187,32768\ns_waitcnt vmcnt(8)\n"
"v_min3_u32 v230, v230, v23, v25\ns_waitcnt vmcnt(7)\nv_min3_u32 v230, v230, v27, v29\nM 68,180,216\nbuffer_load_dwordx4 v221, s[12:15], s63 offen offset:1024 lds\nv_min3_u32 v230, v230, v31, v31\n"
"M 84,176,204\nR 174,187,36864\ns_add_u32 s63, s54, 2752640\nv_max3_u32 v231, v1, v3, v5\nM 84,180,220\nG 4\n"
"v_max3_u32 v231, v231, v7, v9\nv_max3_u32 v231, v231, v11, v13\nv_max3_u32 v231, v231, v15, v17\nM 40,160,192\nR 176,184,40960\nv_max3_u32 v231, v231, v19, v21\n"
"v_max3_u32 v231, v231, v23, v25\nM 40,164,208\ns_add_u32 m0, s50, 90112\nv_max3_u32 v231, v231, v27, v29\nv_max3_u32 v231, v231, v31, v31\nv_cmp_ne_u32 s[86:87], s46, v230\n"
"M 56,160,196\nR 178,184,45056\nv_cmp_ne_u32 s[88:89], s46, v231\nM 56,164,212\nbuffer_load_dwordx4 v220, s[12:15], s63 offen lds\ns_or_b64 s[86:87], s[86:87], s[88:89]\n"
"s_cbranch_scc1 Lq324\nLq325:\nM 72,160,200\nR 180,184,49152\nv_add_f32 v0, v0, v16\nv_add_f32 v2, v2, v18\n"
"M 72,164,216\nv_add_f32 v4, v4, v20\nv_add_f32 v6, v6, v22\nv_add_f32 v8, v8, v24\nM 88,160,204\nR 182,184,53248\n"
"v_add_f32 v10, v10, v26\nv_add_f32 v12, v12, v28\nM 88,164,220\nG 4\nv_add_f32 v14, v14, v30\nv_add_f32 v0, v0, v8\n"
"v_add_f32 v2, v2, v10\nM 44,168,192\nR 160,185,40960\nv_add_f32 v4, v4, v12\nv_add_f32 v6, v6, v14\nM 44,172,208\n"
"buffer_load_dwordx4 v221, s[12:15], s63 offen offset:1024 lds\nv_add_f32 v0, v0, v4\nM 60,168,196\nR 162,185,45056\ns_add_u32 s63, s54, 2982016\nv_add_f32 v2, v2, v6\n"
"M 60,172,212\ns_add_u32 m0, s50, 98304\nv_lshrrev_b32 v231, 1, v252\nv_lshrrev_b32 v230, 2, v252\ns_lshl_b32 s62, s40, 9\ns_add_u32 s62, s62, 159232\n"
"v_add_u32 v231, s62, v231\nM 76,168,200\nR 164,185,49152\nds_write2_b32 v231, v0, v2 offset1:1\nM 76,172,216\nbuffer_load_dwordx4 v220, s[12:15], s63 offen lds\n"
"s_lshl_b32 s62, s40, 9\ns_add_u32 s62, s62, 159232\nv_add_u32 v230, s62, v230\nM 92,168,204\nR 166,185,53248\nds_read2st64_b32 v[232:233], v230 offset1:1\n"
"M 92,172,220\nG 6\nM 32,176,224\nR 168,186,40960\nM 32,180,240\nbuffer_load_dwordx4 v221, s[12:15], s63 offen offset:1024 lds\n"
"M 48,176,228\nR 170,186,45056\ns_add_u32 s63, s54, 3211392\nM 48,180,244\ns_add_u32 m0, s50, 106496\nM 64,176,232\n"
"R 172,186,49152\nG 3\nv_add_f32 v232, v232, v233\nv_mov_b32 v233, v232\nM 64,180,248\nbuffer_load_dwordx4 v220, s[12:15], s63 offen lds\n"
"v_permlane32_swap_b32 v232, v233\nM 80,176,236\nR 174,186,53248\nv_mov_b32 v230, v232\nv_mov_b32 v231, v233\nM 80,180,252\n"
"s_waitcnt vmcnt(9)\ns_barrier\nv_permlane16_swap_b32 v230, v232\nM 36,160,224\nR 176,187,40960\nv_permlane16_swap_b32 v231, v233\n"
"v_mul_f32 v230, 0xba800000, v230\nM 36,164,240\nbuffer_load_dwordx4 v221, s[12:15], s63 offen offset:1024 lds\nv_mul_f32 v214, v230, v230\nM 52,160,228\nR 178,187,45056\n"
"v_mul_f32 v231, 0x3a800000, v231\nv_sub_f32 v231, v231, v214\nM 52,164,244\ns_add_u32 m0, s50, 114688\nv_max_f32 v231, 0, v231\nv_add_f32 v231, 0x3727c5ac, v231\n"
"v_rsq_f32 v234, v231\nM 68,160,232\nR 180,187,49152\nv_and_b32 v208, s77, v230\nv_mul_f32 v216, v231, v234\nM 68,164,248\n"
"s_add_u32 s63, s54, 3440768\nv_sub_f32 v230, v230, v208\nv_cvt_pk_bf16_f32 v209, v230, v216\nv_cvt_pk_bf16_f32 v208, v208, v208\nM 84,160,236\nR 182,187,53248\n"
"v_mul_f32 v214, 0xc0135761, v234\nv_mul_f32 v215, v234, v234\nM 84,164,252\ns_mov_b64 s[60:61], 0xffff\nv_cndmask_b32 v208, 0, v208, s[60:61]\nG 4\n"
"v_mul_f32 v215, v215, v214\nM 40,168,224\nR 160,184,57344\nv_mul_f32 v215, 0x3d372713, v215\nv_mul_f32 v232, 0xba800000, v232\nM 40,172,240\n"
"buffer_load_dwordx4 v220, s[12:15], s63 offen lds\nv_mul_f32 v217, v232, v232\nM 56,168,228\nR 162,184,61440\nv_mul_f32 v233, 0x3a800000, v233\nv_sub_f32 v233, v233, v217\n"
"M 56,172,244\ns_mov_b64 s[60:61], 0xffff\nv_cndmask_b32 v209, 0, v209, s[60:61]\nv_max_f32 v233, 0, v233\nv_add_f32 v233, 0x3727c5ac, v233\nM 72,168,232\n"
"R 164,188,0\nv_and_b32 v212, s77, v232\nv_rsq_f32 v235, v233\nM 72,172,248\nbuffer_load_dwordx4 v221, s[12:15], s63 offen offset:1024 lds\nM 88,168,236\n"
"R 166,188,4096\ns_add_u32 m0, s50, 0\nM 88,172,252\nG 4\nv_mul_f32 v219, v233, v235\nv_sub_f32 v232, v232, v212\n"
"M 44,176,224\nR 168,185,57344\ns_add_u32 s63, s55, 0\nM 44,180,240\nbuffer_load_dwordx4 v222, s[12:15], s63 offen lds\nM 60,176,228\n"
"R 170,185,61440\nv_cvt_pk_bf16_f32 v213, v232, v219\nM 60,180,244\nv_cvt_pk_bf16_f32 v212, v212, v212\ns_mov_b64 s[60:61], 0xffff\nv_cndmask_b32 v212, 0, v212, s[60:61]\n"
"M 76,176,232\nR 172,189,0\nv_mul_f32 v217, 0xc0135761, v235\nM 76,180,248\nbuffer_load_dwordx4 v223, s[12:15], s63 offen offset:1024 lds\nM 92,176,236\n"
"R 174,189,4096\nv_mul_f32 v218, v235, v235\nM 92,180,252\ns_waitcnt vmcnt(10)\ns_barrier\nG 4\n"
"M 96,160,128\nR 176,186,57344\nv_mul_f32 v218, v218, v217\nM 96,164,144\ns_mov_b32 m0, s51\ns_mov_b64 s[60:61], 0xffff\n"
"v_cndmask_b32 v213, 0, v213, s[60:61]\nM 112,160,132\nR 178,186,61440\ns_lshl2_add_u32 s60, s45, s40\nv_lshlrev_b32 v224, 3, v252\ns_mul_i32 s60, s60, 7168\n"
"s_bfm_b64 exec, 14, 0\nbuffer_load_dword v224, v224, s[16:19], s60 offen\ns_mov_b64 exec, -1\nM 112,164,148\nds_write_addtid_b32 v64 offset:0\nv_mul_f32 v218, 0x3d372713, v218\n"
"M 128,160,136\nR 180,190,0\nM 128,164,152\nds_write_addtid_b32 v65 offset:256\nM 144,160,140\nR 182,190,4096\n"
"M 144,164,156\nds_write_addtid_b32 v66 offset:512\nG 7\nM 100,168,128\nR 160,187,57344\nM 100,172,144\n"
"ds_write_addtid_b32 v67 offset:768\nM 116,168,132\nR 162,187,61440\nM 116,172,148\nds_write_addtid_b32 v68 offset:1024\nM 132,168,136\n"
"R 164,191,0\nM 132,172,152\nds_write_addtid_b32 v69 offset:1280\nM 148,168,140\nR 166,191,4096\nM 148,172,156\n"
"ds_write_addtid_b32 v70 offset:1536\nds_write_addtid_b32 v71 offset:1792\nG 10\nM 104,176,128\nR 168,188,8192\nds_write_addtid_b32 v72 offset:2048\n"
"M 104,180,144\nds_write_addtid_b32 v73 offset:2304\nds_write_addtid_b32 v74 offset:2560\nM 120,176,132\nR 170,188,12288\nds_write_addtid_b32 v75 offset:2816\n"
"M 120,180,148\nds_write_addtid_b32 v76 offset:3072\nds_write_addtid_b32 v77 offset:3328\nM 136,176,136\nR 172,188,16384\nds_write_addtid_b32 v78 offset:3584\n"
"M 136,180,152\nds_write_addtid_b32 v79 offset:3840\nds_write_addtid_b32 v80 offset:4096\nM 152,176,140\nR 174,188,20480\nds_write_addtid_b32 v81 offset:4352\n"
"M 152,180,156\nds_write_addtid_b32 v82 offset:4608\nds_write_addtid_b32 v83 offset:4864\nG 15\nM 108,160,128\nR 176,189,8192\n"
"ds_write_addtid_b32 v84 offset:5120\nM 108,164,144\nds_write_addtid_b32 v85 offset:5376\nds_write_addtid_b32 v86 offset:5632\nM 124,160,132\nR 178,189,12288\n"
"ds_write_addtid_b32 v87 offset:5888\nM 124,164,148\nds_write_addtid_b32 v88 offset:6144\nds_write_addtid_b32 v89 offset:6400\nM 140,160,136\nR 180,189,16384\n"
"ds_write_addtid_b32 v90 offset:6656\nM 140,164,152\nds_write_addtid_b32 v91 offset:6912\nds_write_addtid_b32 v92 offset:7168\nM 156,160,140\nR 182,189,20480\n"
"ds_write_addtid_b32 v93 offset:7424\nM 156,164,156\nds_write_addtid_b32 v94 offset:7680\nds_write_addtid_b32 v95 offset:7936\nG 15\nM 96,168,160\n"
"R 160,190,8192\ns_add_u32 m0, s50, 8192\ns_add_u32 s63, s55, 458752\nM 96,172,176\nbuffer_load_dwordx4 v222, s[12:15], s63 offen lds\nM 112,168,164\n"
"R 162,190,12288\ns_waitcnt vmcnt(8) lgkmcnt(2)\ns_barrier\nM 112,172,180\nds_read2st64_b32 v[64:65], v225 offset0:0 offset1:1\nds_read2st64_b32 v[66:67], v225 offset0:2 offset1:3\n"
"M 128,168,168\nR 164,190,16384\nds_read2st64_b32 v[68:69], v225 offset0:4 offset1:5\nM 128,172,184\nbuffer_load_dwordx4 v223, s[12:15], s63 offen offset:1024 lds\nds_read2st64_b32 v[70:71], v225 offset0:6 offset1:7\n"
"M 144,168,172\nR 166,190,20480\ns_add_u32 m0, s50, 16384\nds_read2st64_b32 v[72:73], v225 offset0:8 offset1:9\nM 144,172,188\nds_read2st64_b32 v[74:75], v225 offset0:10 offset1:11\n"
"ds_read2st64_b32 v[76:77], v225 offset0:12 offset1:13\nM 100,176,160\nR 168,191,8192\ns_add_u32 s63, s55, 917504\nds_read2st64_b32 v[78:79], v225 offset0:14 offset1:15\nM 100,180,176\n"
"buffer_load_dwordx4 v222, s[12:15], s63 offen lds\nG 10\nv_add_f32 v32, v32, v64\nM 116,176,164\nR 170,191,12288\nv_add_f32 v33, v33, v65\n"
"G 10\nv_add_f32 v34, v34, v66\nM 116,180,180\nds_read2st64_b32 v[80:81], v225 offset0:16 offset1:17\nv_add_f32 v35, v35, v67\nG 9\n"
"v_add_f32 v36, v36, v68\nM 132,176,168\nR 172,191,16384\nv_add_f32 v37, v37, v69\nG 9\nv_add_f32 v38, v38, v70\n"
"M 132,180,184\nbuffer_load_dwordx4 v223, s[12:15], s63 offen offset:1024 lds\nv_add_f32 v39, v39, v71\nM 148,176,172\nR 174,191,20480\ns_add_u32 m0, s50, 24576\n"
"G 8\nv_add_f32 v40, v40, v72\nM 148,180,188\nds_read2st64_b32 v[82:83], v225 offset0:18 offset1:19\nv_add_f32 v41, v41, v73\nM 104,160,160\n"
"R 176,188,24576\ns_add_u32 s63, s55, 1376256\nG 9\nv_add_f32 v42, v42, v74\nM 104,164,176\nbuffer_load_dwordx4 v222, s[12:15], s63 offen lds\n"
"v_add_f32 v43, v43, v75\nM 120,160,164\nR 178,188,28672\nG 9\nv_add_f32 v44, v44, v76\nv_add_f32 v45, v45, v77\n"
"M 120,164,180\nds_read2st64_b32 v[84:85], v225 offset0:20 offset1:21\nG 8\nv_add_f32 v46, v46, v78\nv_add_f32 v47, v47, v79\nM 136,160,168\n"
"R 180,188,32768\nG 7\nv_add_f32 v48, v48, v80\nv_add_f32 v49, v49, v81\nM 136,164,184\nds_read2st64_b32 v[86:87], v225 offset0:22 offset1:23\n"
"G 5\nv_add_f32 v50, v50, v82\nv_add_f32 v51, v51, v83\nM 152,160,172\nR 182,188,36864\nds_read2st64_b32 v[88:89], v225 offset0:24 offset1:25\n"
"M 152,164,188\nds_read2st64_b32 v[90:91], v225 offset0:26 offset1:27\nds_read2st64_b32 v[92:93], v225 offset0:28 offset1:29\nM 108,168,160\nR 160,189,24576\nds_read2st64_b32 v[94:95], v225 offset0:30 offset1:31\n"
"M 108,172,176\nbuffer_load_dwordx4 v223, s[12:15], s63 offen offset:1024 lds\nG 8\nv_add_f32 v52, v52, v84\nM 124,168,164\nR 162,189,28672\n"
"s_mov_b32 s60, 157184\nv_bfe_u32 v246, v252, 4, 4\nv_lshl_add_u32 v246, v246, 3, s60\nM 124,172,180\nds_read_b64 v[238:239], v246 offset:0\nv_add_f32 v53, v53, v85\n"
"M 140,168,168\nR 164,189,32768\nds_read_b64 v[240:241], v246 offset:128\nM 140,172,184\nds_read_b64 v[242:243], v246 offset:256\nG 11\n"
"v_add_f32 v54, v54, v86\nM 156,168,172\nR 166,189,36864\nds_read_b64 v[244:245], v246 offset:384\nM 156,172,188\nds_read_b64 v[246:247], v246 offset:0\n"
"G 13\nM 96,176,192\nR 168,190,24576\nv_add_f32 v55, v55, v87\nM 96,180,208\ns_add_u32 m0, s50, 32768\n"
"G 13\nv_add_f32 v56, v56, v88\nv_add_f32 v57, v57, v89\nM 112,176,196\nR 170,190,28672\ns_lshl2_add_u32 s60, s45, s40\n"
"s_waitcnt vmcnt(6)\nv_lshlrev_b32 v224, 3, v252\ns_mul_i32 s60, s60, 7168\ns_bfm_b64 exec, 14, 14\nbuffer_load_dword v224, v224, s[16:19], s60 offen\ns_mov_b64 exec, -1\n"
"G 2\ns_barrier\nM 112,180,212\ns_add_u32 s63, s55, 1835008\nv_add_f32 v58, v58, v90\nv_add_f32 v59, v59, v91\n"
"M 128,176,200\nR 172,190,32768\nv_add_f32 v60, v60, v92\nM 128,180,216\nbuffer_load_dwordx4 v222, s[12:15], s63 offen lds\nM 144,176,204\n"
"R 174,190,36864\nv_add_f32 v61, v61, v93\nM 144,180,220\nv_add_f32 v62, v62, v94\nv_add_f32 v63, v63, v95\nM 100,160,192\n"
"R 176,191,24576\nv_mfma_f32_16x16x32_bf16 v[32:35], v[238:241], v[208:211], v[32:35]\nM 100,164,208\nv_mfma_f32_16x16x32_bf16 v[36:39], v[240:243], v[208:211], v[36:39]\nbuffer_load_dwordx4 v223, s[12:15], s63 offen offset:1024 lds\nM 116,160,196\n"
"R 178,191,28672\nv_mfma_f32_16x16x32_bf16 v[40:43], v[242:245], v[208:211], v[40:43]\ns_add_u32 s63, s55, 2293760\nM 116,164,212\nv_mfma_f32_16x16x32_bf16 v[44:47], v[244:247], v[208:211], v[44:47]\ns_add_u32 m0, s50, 40960\n"
"M 132,160,200\nR 180,191,32768\nv_mfma_f32_16x16x32_bf16 v[48:51], v[244:247], v[210:213], v[48:51]\nM 132,164,216\nv_mfma_f32_16x16x32_bf16 v[52:55], v[238:241], v[210:213], v[52:55]\nbuffer_load_dwordx4 v222, s[12:15], s63 offen lds\n"
"M 148,160,204\nR 182,191,36864\nv_mfma_f32_16x16x32_bf16 v[56:59], v[240:243], v[210:213], v[56:59]\nM 148,164,220\nv_mfma_f32_16x16x32_bf16 v[60:63], v[242:245], v[210:213], v[60:63]\nG 4\n"
"M 104,168,192\nR 160,188,40960\nM 104,172,208\nbuffer_load_dwordx4 v223, s[12:15], s63 offen offset:1024 lds\nM 120,168,196\nR 162,188,45056\n"
"M 120,172,212\ns_add_u32 m0, s50, 49152\nM 136,168,200\nR 164,188,49152\nM 136,172,216\ns_add_u32 s63, s55, 2752512\n"
"M 152,168,204\nR 166,188,53248\nv_mul_f32 v230, v32, v32\nv_mul_f32 v231, v33, v33\nM 152,172,220\ns_add_u32 s84, s73, 23068672\n"
"s_add_u32 s85, s73, 23183360\nG 4\nv_mul_f32 v232, v34, v34\nM 108,176,192\nR 168,189,40960\nv_mul_f32 v233, v35, v35\n"
"v_fma_f32 v230, v230, v215, v214\nM 108,180,208\nbuffer_load_dwordx4 v222, s[12:15], s63 offen lds\nv_fma_f32 v231, v231, v215, v214\nM 124,176,196\nR 170,189,45056\n"
"v_fma_f32 v232, v232, v215, v214\nv_fma_f32 v233, v233, v215, v214\nM 124,180,212\nv_mul_f32 v230, v32, v230\nv_mul_f32 v231, v33, v231\nv_mul_f32 v232, v34, v232\n"
"M 140,176,200\nR 172,189,49152\nv_mul_f32 v233, v35, v233\nv_exp_f32 v230, v230\nM 140,180,216\nv_exp_f32 v231, v231\n"
"v_exp_f32 v232, v232\nM 156,176,204\nR 174,189,53248\ns_mov_b32 s60, 157696\nv_bfe_u32 v246, v252, 4, 4\nv_lshl_add_u32 v246, v246, 3, s60\n"
"v_exp_f32 v233, v233\nM 156,180,220\nds_read_b64 v[238:239], v246 offset:0\nG 5\nv_fma_f32 v230, v230, v216, v216\nM 96,160,224\n"
"R 176,190,40960\nds_read_b64 v[240:241], v246 offset:128\nv_fma_f32 v231, v231, v216, v216\nM 96,164,240\nds_read_b64 v[242:243], v246 offset:256\nv_fma_f32 v232, v232, v216, v216\n"
"v_fma_f32 v233, v233, v216, v216\nM 112,160,228\nR 178,190,45056\nds_read_b64 v[244:245], v246 offset:384\nv_rcp_f32 v230, v230\nM 112,164,244\n"
"ds_read_b64 v[246:247], v246 offset:0\nv_mul_f32 v230, v32, v230\nv_rcp_f32 v231, v231\nM 128,160,232\nR 180,190,49152\nv_mul_f32 v231, v33, v231\n"
"v_rcp_f32 v232, v232\nM 128,164,248\nbuffer_load_dwordx4 v223, s[12:15], s63 offen offset:1024 lds\nv_rcp_f32 v233, v233\nM 144,160,236\nR 182,190,53248\n"
"s_add_u32 m0, s50, 57344\nv_mul_f32 v232, v34, v232\nM 144,164,252\nG 9\nv_mul_f32 v233, v35, v233\nv_cvt_pk_bf16_f32 v32, v230, v231\n"
"v_cvt_pk_bf16_f32 v33, v232, v233\nM 100,168,224\nR 160,191,40960\ns_add_u32 s63, s55, 3211264\nv_mul_f32 v234, v36, v36\nM 100,172,240\n"
"buffer_load_dwordx4 v222, s[12:15], s63 offen lds\nv_mul_f32 v235, v37, v37\nM 116,168,228\nR 162,191,45056\nv_mul_f32 v236, v38, v38\nv_mul_f32 v237, v39, v39\n"
"M 116,172,244\nv_fma_f32 v234, v234, v215, v214\nv_fma_f32 v235, v235, v215, v214\nv_fma_f32 v236, v236, v215, v214\nM 132,168,232\nR 164,191,49152\n"
"G 9\nv_mfma_f32_16x16x32_bf16 v[96:99], v[238:241], v[208:211], v[96:99]\nv_fma_f32 v237, v237, v215, v214\nv_mul_f32 v234, v36, v234\nM 132,172,248\nbuffer_load_dwordx4 v223, s[12:15], s63 offen offset:1024 lds\n"
"v_mul_f32 v235, v37, v235\nM 148,168,236\nR 166,191,53248\nG 9\nv_mfma_f32_16x16x32_bf16 v[100:103], v[240:243], v[208:211], v[100:103]\ns_add_u32 m0, s50, 65536\n"
"v_mul_f32 v236, v38, v236\nM 148,172,252\nG 4\nv_exp_f32 v234, v234\nv_mul_f32 v237, v39, v237\nM 104,176,224\n"
"R 168,184,0\nv_mfma_f32_16x16x32_bf16 v[104:107], v[242:245], v[208:211], v[104:107]\ns_add_u32 s63, s54, 256\nv_exp_f32 v235, v235\nM 104,180,240\nbuffer_load_dwordx4 v220, s[12:15], s63 offen lds\n"
"v_exp_f32 v236, v236\nM 120,176,228\nR 170,184,4096\nv_mfma_f32_16x16x32_bf16 v[108:111], v[244:247], v[208:211], v[108:111]\nv_fma_f32 v234, v234, v216, v216\nv_exp_f32 v237, v237\n"
"M 120,180,244\nv_rcp_f32 v234, v234\nv_fma_f32 v235, v235, v216, v216\nM 136,176,232\nR 172,186,0\nv_mfma_f32_16x16x32_bf16 v[112:115], v[244:247], v[210:213], v[112:115]\n"
"v_fma_f32 v236, v236, v216, v216\nv_fma_f32 v237, v237, v216, v216\nM 136,180,248\nbuffer_load_dwordx4 v221, s[12:15], s63 offen offset:1024 lds\nv_rcp_f32 v235, v235\nM 152,176,236\n"
"R 174,186,4096\nv_mfma_f32_16x16x32_bf16 v[116:119], v[238:241], v[210:213], v[116:119]\ns_add_u32 m0, s50, 73728\nv_rcp_f32 v236, v236\nM 152,180,252\nG 4\n"
"v_rcp_f32 v237, v237\nv_mul_f32 v234, v36, v234\nM 108,160,224\nR 176,185,0\nv_mfma_f32_16x16x32_bf16 v[120:123], v[240:243], v[210:213], v[120:123]\ns_add_u32 s63, s54, 229632\n"
"v_mul_f32 v235, v37, v235\nM 108,164,240\nbuffer_load_dwordx4 v220, s[12:15], s63 offen lds\nv_mul_f32 v236, v38, v236\nM 124,160,228\nR 178,185,4096\n"
"v_mfma_f32_16x16x32_bf16 v[124:127], v[242:245], v[210:213], v[124:127]\nv_mul_f32 v237, v39, v237\nv_cvt_pk_bf16_f32 v34, v234, v235\nM 124,164,244\nbuffer_load_dwordx4 v221, s[12:15], s63 offen offset:1024 lds\nv_cvt_pk_bf16_f32 v35, v236, v237\n"
"M 140,160,232\nR 180,187,0\nv_mul_f32 v230, v40, v40\nv_mul_f32 v231, v41, v41\nM 140,164,248\nbuffer_store_dwordx4 v[32:35], v252, s[20:23], s84 offen offset:0 sc1\n"
"v_mul_f32 v232, v42, v42\nM 156,160,236\nR 182,187,4096\nv_mul_f32 v233, v43, v43\nv_fma_f32 v230, v230, v215, v214\nM 156,164,252\n"
"s_waitcnt vmcnt(14)\ns_barrier\nG 4\nv_fma_f32 v231, v231, v215, v214\nv_mfma_f32_16x16x32_bf16 v[0:3], v[168:171], a[0:3], 0\nR 160,184,8192\n"
"v_fma_f32 v232, v232, v215, v214\nv_fma_f32 v233, v233, v215, v214\nM 0,172,16\ns_mov_b32 m0, s51\nv_mul_f32 v230, v40, v230\nv_mul_f32 v231, v41, v231\n"
"v_mul_f32 v232, v42, v232\nv_mfma_f32_16x16x32_bf16 v[16:19], v[168:171], a[4:7], 0\nR 162,184,12288\ns_lshl2_add_u32 s60, s45, s40\ns_waitcnt vmcnt(13)\nv_lshlrev_b32 v224, 3, v252\n"
"s_mul_i32 s60, s60, 7168\ns_bfm_b64 exec, 14, 28\nbuffer_load_dword v224, v224, s[16:19], s60 offen\ns_mov_b64 exec, -1\nv_mul_f32 v233, v43, v233\nM 16,172,20\n"
"v_exp_f32 v230, v230\nv_exp_f32 v231, v231\nv_mfma_f32_16x16x32_bf16 v[8:11], v[168:171], a[8:11], 0\nR 164,186,8192\nv_fma_f32 v230, v230, v216, v216\nv_exp_f32 v232, v232\n"
"M 8,172,24\nv_exp_f32 v233, v233\nv_fma_f32 v231, v231, v216, v216\nv_mfma_f32_16x16x32_bf16 v[24:27], v[168:171], a[12:15], 0\nR 166,186,12288\nv_fma_f32 v232, v232, v216, v216\n"
"v_fma_f32 v233, v233, v216, v216\nM 24,172,28\nG 4\nv_rcp_f32 v230, v230\nv_rcp_f32 v231, v231\nv_mfma_f32_16x16x32_bf16 v[4:7], v[176:179], a[0:3], 0\n"
"R 168,185,8192\nv_mul_f32 v230, v40, v230\nv_rcp_f32 v232, v232\nM 4,180,16\nv_rcp_f32 v233, v233\nv_mul_f32 v231, v41, v231\n"
"v_mfma_f32_16x16x32_bf16 v[20:23], v[176:179], a[4:7], 0\nR 170,185,12288\nv_mul_f32 v232, v42, v232\nv_mul_f32 v233, v43, v233\nM 20,180,20\nds_write_addtid_b32 v128 offset:0\n"
"v_cvt_pk_bf16_f32 v40, v230, v231\nv_cvt_pk_bf16_f32 v41, v232, v233\nv_mul_f32 v234, v44, v44\nv_mfma_f32_16x16x32_bf16 v[12:15], v[176:179], a[8:11], 0\nR 172,187,8192\nds_write_addtid_b32 v129 offset:256\n"
"v_mul_f32 v235, v45, v45\nv_mul_f32 v236, v46, v46\nM 12,180,24\nds_write_addtid_b32 v130 offset:512\nv_mul_f32 v237, v47, v47\nv_fma_f32 v234, v234, v215, v214\n"
"v_fma_f32 v235, v235, v215, v214\nv_mfma_f32_16x16x32_bf16 v[28:31], v[176:179], a[12:15], 0\nR 174,187,12288\nds_write_addtid_b32 v131 offset:768\nv_fma_f32 v236, v236, v215, v214\nv_fma_f32 v237, v237, v215, v214\n"
"M 28,180,28\nds_write_addtid_b32 v132 offset:1024\nG 9\nv_mul_f32 v234, v44, v234\nv_mul_f32 v235, v45, v235\nv_mul_f32 v236, v46, v236\n"
"M 0,160,32\nR 176,184,16384\nds_write_addtid_b32 v133 offset:1280\nv_mul_f32 v237, v47, v237\nv_exp_f32 v234, v234\nM 0,164,48\n"
"ds_write_addtid_b32 v134 offset:1536\nv_exp_f32 v235, v235\nv_exp_f32 v236, v236\nM 16,160,36\nR 178,184,20480\nds_write_addtid_b32 v135 offset:1792\n"
"v_fma_f32 v234, v234, v216, v216\nv_exp_f32 v237, v237\nM 16,164,52\nds_write_addtid_b32 v136 offset:2048\nv_rcp_f32 v234, v234\nv_fma_f32 v235, v235, v216, v216\n"
"M 8,160,40\nR 180,186,16384\nds_write_addtid_b32 v137 offset:2304\nv_fma_f32 v236, v236, v216, v216\nv_fma_f32 v237, v237, v216, v216\nM 8,164,56\n"
"ds_write_addtid_b32 v138 offset:2560\nv_rcp_f32 v235, v235\nv_rcp_f32 v236, v236\nM 24,160,44\nR 182,186,20480\nds_write_addtid_b32 v139 offset:2816\n"
"v_mul_f32 v234, v44, v234\nv_rcp_f32 v237, v237\nM 24,164,60\nds_write_addtid_b32 v140 offset:3072\nG 14\nv_mul_f32 v235, v45, v235\n"
"v_mul_f32 v236, v46, v236\nv_mul_f32 v237, v47, v237\nM 4,168,32\nR 160,185,16384\nds_write_addtid_b32 v141 offset:3328\nv_cvt_pk_bf16_f32 v42, v234, v235\n"
"v_cvt_pk_bf16_f32 v43, v236, v237\nM 4,172,48\nbuffer_store_dwordx4 v[40:43], v252, s[20:23], s84 offen offset:1024 sc1\nds_write_addtid_b32 v142 offset:3584\nv_mul_f32 v230, v48, v48\nM 20,168,36\n"
"R 162,185,20480\nds_write_addtid_b32 v143 offset:3840\nv_mul_f32 v231, v49, v49\nv_mul_f32 v232, v50, v50\nM 20,172,52\nds_write_addtid_b32 v144 offset:4096\n"
"v_mul_f32 v233, v51, v51\nv_fma_f32 v230, v230, v218, v217\nv_fma_f32 v231, v231, v218, v217\nM 12,168,40\nR 164,187,16384\nds_write_addtid_b32 v145 offset:4352\n"
"v_fma_f32 v232, v232, v218, v217\nv_fma_f32 v233, v233, v218, v217\nM 12,172,56\nds_write_addtid_b32 v146 offset:4608\nv_mul_f32 v230, v48, v230\nv_mul_f32 v231, v49, v231\n"
"v_mul_f32 v232, v50, v232\nM 28,168,44\nR 166,187,20480\nds_write_addtid_b32 v147 offset:4864\nv_mul_f32 v233, v51, v233\nv_exp_f32 v230, v230\n"
"M 28,172,60\nds_write_addtid_b32 v148 offset:5120\nds_write_addtid_b32 v149 offset:5376\nds_write_addtid_b32 v150 offset:5632\nds_write_addtid_b32 v151 offset:5888\nds_write_addtid_b32 v152 offset:6144\n"
"ds_write_addtid_b32 v153 offset:6400\nds_write_addtid_b32 v154 offset:6656\nds_write_addtid_b32 v155 offset:6912\nds_write_addtid_b32 v156 offset:7168\nds_write_addtid_b32 v157 offset:7424\nds_write_addtid_b32 v158 offset:7680\n"
"ds_write_addtid_b32 v159 offset:7936\nG 15\nv_exp_f32 v231, v231\nv_exp_f32 v232, v232\nM 0,176,64\nR 168,184,24576\n"
"s_add_u32 m0, s50, 81920\ns_add_u32 s63, s54, 459008\nv_exp_f32 v233, v233\nM 0,180,80\nbuffer_load_dwordx4 v220, s[12:15], s63 offen lds\nv_fma_f32 v230, v230, v219, v219\n"
"M 16,176,68\nR 170,184,28672\ns_waitcnt vmcnt(10) lgkmcnt(2)\ns_barrier\nv_fma_f32 v231, v231, v219, v219\nM 16,180,84\n"
"ds_read2st64_b32 v[128:129], v225 offset0:0 offset1:1\nv_rcp_f32 v230, v230\nv_fma_f32 v232, v232, v219, v219\nM 8,176,72\nR 172,186,24576\nds_read2st64_b32 v[130:131], v225 offset0:2 offset1:3\n"
"v_fma_f32 v233, v233, v219, v219\nv_rcp_f32 v231, v231\nM 8,180,88\nbuffer_load_dwordx4 v221, s[12:15], s63 offen offset:1024 lds\nds_read2st64_b32 v[132:133], v225 offset0:4 offset1:5\nv_rcp_f32 v232, v232\n"
"M 24,176,76\nR 174,186,28672\ns_add_u32 m0, s50, 90112\nds_read2st64_b32 v[134:135], v225 offset0:6 offset1:7\nv_rcp_f32 v233, v233\nM 24,180,92\n"
"ds_read2st64_b32 v[136:137], v225 offset0:8 offset1:9\nv_mul_f32 v230, v48, v230\nv_mul_f32 v231, v49, v231\nv_mul_f32 v232, v50, v232\nM 4,160,64\nR 176,185,24576\n"
"s_add_u32 s63, s54, 688384\nds_read2st64_b32 v[138:139], v225 offset0:10 offset1:11\nv_mul_f32 v233, v51, v233\nM 4,164,80\nbuffer_load_dwordx4 v220, s[12:15], s63 offen lds\nG 8\n"
"v_add_f32 v96, v96, v128\nv_cvt_pk_bf16_f32 v48, v230, v231\nM 20,160,68\nR 178,185,28672\nv_cvt_pk_bf16_f32 v49, v232, v233\nv_add_f32 v97, v97, v129\n"
"v_mul_f32 v234, v52, v52\nM 20,164,84\nds_read2st64_b32 v[140:141], v225 offset0:12 offset1:13\nv_mul_f32 v235, v53, v53\nv_mul_f32 v236, v54, v54\nv_mul_f32 v237, v55, v55\n"
"M 12,160,72\nR 180,187,24576\nv_fma_f32 v234, v234, v218, v217\nG 9\nv_add_f32 v98, v98, v130\nv_fma_f32 v235, v235, v218, v217\n"
"M 12,164,88\nbuffer_load_dwordx4 v221, s[12:15], s63 offen offset:1024 lds\nv_add_f32 v99, v99, v131\nv_fma_f32 v236, v236, v218, v217\nM 28,160,76\nR 182,187,28672\n"
"s_add_u32 m0, s50, 98304\nG 9\nv_add_f32 v100, v100, v132\nv_fma_f32 v237, v237, v218, v217\nM 28,164,92\nG 8\n"
"v_mul_f32 v234, v52, v234\nv_mul_f32 v235, v53, v235\nv_add_f32 v101, v101, v133\nv_mul_f32 v236, v54, v236\nM 0,168,96\nR 160,184,32768\n"
"s_add_u32 s63, s54, 917760\nG 8\nv_add_f32 v102, v102, v134\nv_mul_f32 v237, v55, v237\nM 0,172,112\nbuffer_load_dwordx4 v220, s[12:15], s63 offen lds\n"
"v_add_f32 v103, v103, v135\nv_exp_f32 v234, v234\nM 16,168,100\nR 162,184,36864\nv_fma_f32 v234, v234, v219, v219\nG 8\n"
"v_add_f32 v104, v104, v136\nv_exp_f32 v235, v235\nM 16,172,116\nds_read2st64_b32 v[142:143], v225 offset0:14 offset1:15\nv_exp_f32 v236, v236\nv_exp_f32 v237, v237\n"
"M 8,168,104\nR 164,186,32768\nv_fma_f32 v235, v235, v219, v219\nv_add_f32 v105, v105, v137\nv_fma_f32 v236, v236, v219, v219\nM 8,172,120\n"
"ds_read2st64_b32 v[144:145], v225 offset0:16 offset1:17\nv_rcp_f32 v234, v234\nv_fma_f32 v237, v237, v219, v219\nM 24,168,108\nR 166,186,36864\nv_mul_f32 v234, v52, v234\n"
"G 10\nv_add_f32 v106, v106, v138\nv_rcp_f32 v235, v235\nM 24,172,124\nG 6\nv_rcp_f32 v236, v236\n"
"v_add_f32 v107, v107, v139\nv_rcp_f32 v237, v237\nM 4,176,96\nR 168,185,32768\nv_mul_f32 v235, v53, v235\nv_add_f32 v108, v108, v140\n"
"v_mul_f32 v236, v54, v236\nM 4,180,112\nbuffer_load_dwordx4 v221, s[12:15], s63 offen offset:1024 lds\nv_add_f32 v109, v109, v141\nv_mul_f32 v237, v55, v237\nM 20,176,100\n"
"R 170,185,36864\ns_add_u32 s63, s54, 1147136\nG 5\nv_add_f32 v110, v110, v142\nv_cvt_pk_bf16_f32 v50, v234, v235\nM 20,180,116\n"
"s_add_u32 m0, s50, 106496\nv_cvt_pk_bf16_f32 v51, v236, v237\nv_mul_f32 v230, v56, v56\nv_add_f32 v111, v111, v143\nv_mul_f32 v231, v57, v57\nM 12,176,104\n"
"R 172,187,32768\nv_mul_f32 v232, v58, v58\nG 4\nv_add_f32 v112, v112, v144\nv_mul_f32 v233, v59, v59\nM 12,180,120\n"
"buffer_load_dwordx4 v220, s[12:15], s63 offen lds\nv_add_f32 v113, v113, v145\nv_fma_f32 v230, v230, v218, v217\nM 28,176,108\nR 174,187,36864\nds_read2st64_b32 v[146:147], v225 offset0:18 offset1:19\n"
"v_fma_f32 v231, v231, v218, v217\nv_fma_f32 v232, v232, v218, v217\nM 28,180,124\nds_read2st64_b32 v[148:149], v225 offset0:20 offset1:21\nG 6\nv_fma_f32 v233, v233, v218, v217\n"
"v_mul_f32 v230, v56, v230\nv_mul_f32 v231, v57, v231\nM 0,160,128\nR 176,184,40960\nds_read2st64_b32 v[150:151], v225 offset0:22 offset1:23\nv_mul_f32 v232, v58, v232\n"
"v_mul_f32 v233, v59, v233\nM 0,164,144\nbuffer_load_dwordx4 v221, s[12:15], s63 offen offset:1024 lds\nds_read2st64_b32 v[152:153], v225 offset0:24 offset1:25\nv_exp_f32 v230, v230\nM 16,160,132\n"
"R 178,184,45056\nds_read2st64_b32 v[154:155], v225 offset0:26 offset1:27\nv_fma_f32 v230, v230, v219, v219\nv_exp_f32 v231, v231\nM 16,164,148\ns_add_u32 m0, s50, 114688\n"
"ds_read2st64_b32 v[156:157], v225 offset0:28 offset1:29\nv_exp_f32 v232, v232\nv_exp_f32 v233, v233\nM 8,160,136\nR 180,186,40960\nLq599:\n"
"s_lshr_b32 s60, s45, 3\ns_lshl2_add_u32 s60, s60, s40\ns_lshl_b32 s60, s60, 8\ns_waitcnt vmcnt(9)\nv_lshl_add_u32 v224, v252, 3, s60\nv_add_u32 v224, Lq599+4096-LqPC, v224\n"
"s_mov_b64 exec, 3\nglobal_load_dword v224, v224, s[98:99]\ns_mov_b64 exec, -1\nG 8\nv_add_f32 v114, v114, v146\nv_fma_f32 v231, v231, v219, v219\n"
"M 8,164,152\ns_add_u32 s63, s54, 1376512\nv_rcp_f32 v230, v230\nv_add_f32 v115, v115, v147\nv_fma_f32 v232, v232, v219, v219\nM 24,160,140\n"
"R 182,186,45056\nv_fma_f32 v233, v233, v219, v219\nG 8\nv_add_f32 v116, v116, v148\nv_rcp_f32 v231, v231\nM 24,164,156\n"
"ds_read2st64_b32 v[158:159], v225 offset0:30 offset1:31\nv_rcp_f32 v232, v232\nv_add_f32 v117, v117, v149\nG 7\nv_add_f32 v118, v118, v150\nv_add_f32 v119, v119, v151\n"
"G 6\nv_add_f32 v120, v120, v152\nv_add_f32 v121, v121, v153\nG 4\nv_add_f32 v122, v122, v154\nv_add_f32 v123, v123, v155\n"
"G 3\nv_add_f32 v124, v124, v156\nv_add_f32 v125, v125, v157\nG 0\nv_add_f32 v126, v126, v158\nv_add_f32 v127, v127, v159\n"
"v_rcp_f32 v233, v233\nM 4,168,128\nR 160,185,40960\nv_mul_f32 v230, v56, v230\nv_mul_f32 v231, v57, v231\nM 4,172,144\n"
"buffer_load_dwordx4 v220, s[12:15], s63 offen lds\nv_mul_f32 v232, v58, v232\nM 20,168,132\nR 162,185,45056\nv_mul_f32 v233, v59, v233\nv_cvt_pk_bf16_f32 v56, v230, v231\n"
"M 20,172,148\nbuffer_store_dwordx4 v[48:51], v252, s[20:23], s85 offen offset:0 sc1\nv_cvt_pk_bf16_f32 v57, v232, v233\nM 12,168,136\nR 164,187,40960\nv_mul_f32 v234, v60, v60\n"
"v_mul_f32 v235, v61, v61\nM 12,172,152\nv_mul_f32 v236, v62, v62\nv_mul_f32 v237, v63, v63\nv_fma_f32 v234, v234, v218, v217\nM 28,168,140\n"
"R 166,187,45056\nv_fma_f32 v235, v235, v218, v217\nv_fma_f32 v236, v236, v218, v217\nM 28,172,156\nv_fma_f32 v237, v237, v218, v217\nv_mul_f32 v234, v60, v234\n"
"v_mul_f32 v235, v61, v235\nM 0,176,160\nR 168,184,49152\nv_mul_f32 v236, v62, v236\nv_mul_f32 v237, v63, v237\nM 0,180,176\n"
"buffer_load_dwordx4 v221, s[12:15], s63 offen offset:1024 lds\nv_exp_f32 v234, v234\nM 16,176,164\nR 170,184,53248\ns_add_u32 s63, s54, 1605888\nv_exp_f32 v235, v235\n"
"M 16,180,180\ns_add_u32 m0, s50, 0\nv_exp_f32 v236, v236\nv_exp_f32 v237, v237\nM 8,176,168\nR 172,186,49152\n"
"v_fma_f32 v234, v234, v219, v219\nv_fma_f32 v235, v235, v219, v219\nM 8,180,184\nbuffer_load_dwordx4 v220, s[12:15], s63 offen lds\nv_fma_f32 v236, v236, v219, v219\nM 24,176,172\n"
"R 174,186,53248\nv_fma_f32 v237, v237, v219, v219\nv_rcp_f32 v234, v234\nM 24,180,188\nv_rcp_f32 v235, v235\nv_rcp_f32 v236, v236\n"
"v_rcp_f32 v237, v237\nv_mul_f32 v234, v60, v234\nv_mul_f32 v235, v61, v235\nv_mul_f32 v236, v62, v236\nv_mul_f32 v237, v63, v237\nv_cvt_pk_bf16_f32 v58, v234, v235\n"
"v_cvt_pk_bf16_f32 v59, v236, v237\nbuffer_store_dwordx4 v[56:59], v252, s[20:23], s85 offen offset:1024 sc1\nG 4\nM 4,160,160\nR 176,185,49152\ns_barrier\n"
"v_mul_f32 v230, v96, v96\nM 4,164,176\ns_add_u32 s84, s73, 23070720\ns_add_u32 s85, s73, 23185408\nv_mul_f32 v231, v97, v97\nv_mul_f32 v232, v98, v98\n"
"M 20,160,164\nR 178,185,53248\nv_mul_f32 v233, v99, v99\nv_fma_f32 v230, v230, v215, v214\nM 20,164,180\nbuffer_load_dwordx4 v221, s[12:15], s63 offen offset:1024 lds\n"
"v_fma_f32 v231, v231, v215, v214\nM 12,160,168\nR 180,187,49152\nv_fma_f32 v232, v232, v215, v214\nv_fma_f32 v233, v233, v215, v214\nM 12,164,184\n"
"v_mul_f32 v230, v96, v230\nv_mul_f32 v231, v97, v231\nv_mul_f32 v232, v98, v232\nM 28,160,172\nR 182,187,53248\ns_mov_b32 s60, 158720\n"
"v_bfe_u32 v242, v252, 4, 4\nv_lshl_add_u32 v242, v242, 3, s60\nv_mul_f32 v233, v99, v233\nM 28,164,188\nds_read_b64 v[238:239], v242 offset:0\nG 5\n"
"v_exp_f32 v230, v230\nM 0,168,192\nR 160,184,57344\nds_read_b64 v[240:241], v242 offset:128\nv_exp_f32 v231, v231\nM 0,172,208\n"
"ds_read_b64 v[242:243], v242 offset:0\nv_fma_f32 v230, v230, v216, v216\nv_exp_f32 v232, v232\nM 16,168,196\nR 162,184,61440\nv_fma_f32 v231, v231, v216, v216\n"
"v_exp_f32 v233, v233\nM 16,172,212\nv_rcp_f32 v230, v230\nv_fma_f32 v232, v232, v216, v216\nM 8,168,200\nR 164,186,57344\n"
"v_fma_f32 v233, v233, v216, v216\nv_rcp_f32 v231, v231\nM 8,172,216\nv_rcp_f32 v232, v232\nv_rcp_f32 v233, v233\nM 24,168,204\n"
"R 166,186,61440\nv_mul_f32 v230, v96, v230\nv_mul_f32 v231, v97, v231\nM 24,172,220\nG 7\nv_mul_f32 v232, v98, v232\n"
"v_mul_f32 v233, v99, v233\nv_cvt_pk_bf16_f32 v96, v230, v231\nM 4,176,192\nR 168,185,57344\nv_cvt_pk_bf16_f32 v97, v232, v233\nv_mul_f32 v234, v100, v100\n"
"M 4,180,208\nv_mul_f32 v235, v101, v101\nv_mul_f32 v236, v102, v102\nv_mul_f32 v237, v103, v103\nM 20,176,196\nR 170,185,61440\n"
"v_fma_f32 v234, v234, v215, v214\nv_fma_f32 v235, v235, v215, v214\nM 20,180,212\nv_fma_f32 v236, v236, v215, v214\nv_fma_f32 v237, v237, v215, v214\nv_mul_f32 v234, v100, v234\n"
"M 12,176,200\nR 172,187,57344\nG 7\nv_mfma_f32_16x16x32_bf16 v[0:3], v[238:241], v[208:211], v[0:3]\nv_mul_f32 v235, v101, v235\nv_mul_f32 v236, v102, v236\n"
"M 12,180,216\nv_exp_f32 v234, v234\nv_mul_f32 v237, v103, v237\nM 28,176,204\nR 174,187,61440\nG 7\n"
"v_mfma_f32_16x16x32_bf16 v[4:7], v[240:243], v[208:211], v[4:7]\nv_fma_f32 v234, v234, v216, v216\nv_exp_f32 v235, v235\nM 28,180,220\nG 4\nv_exp_f32 v236, v236\n"
"v_exp_f32 v237, v237\nM 0,160,224\nR 176,188,0\nv_mfma_f32_16x16x32_bf16 v[16:19], v[240:243], v[210:213], v[16:19]\nv_fma_f32 v235, v235, v216, v216\nv_fma_f32 v236, v236, v216, v216\n"
"M 0,164,240\nv_rcp_f32 v234, v234\nv_fma_f32 v237, v237, v216, v216\nM 16,160,228\nR 178,188,4096\nv_mfma_f32_16x16x32_bf16 v[20:23], v[238:241], v[210:213], v[20:23]\n"
"v_mul_f32 v234, v100, v234\nv_rcp_f32 v235, v235\nM 16,164,244\nv_rcp_f32 v236, v236\nv_rcp_f32 v237, v237\nM 8,160,232\n"
"R 180,188,8192\nv_mul_f32 v235, v101, v235\nv_mul_f32 v236, v102, v236\nM 8,164,248\nv_mul_f32 v237, v103, v237\nv_cvt_pk_bf16_f32 v98, v234, v235\n"
"v_cvt_pk_bf16_f32 v99, v236, v237\nM 24,160,236\nR 182,188,12288\nv_mul_f32 v230, v104, v104\nv_mul_f32 v231, v105, v105\nM 24,164,252\n"
"G 4\nv_mul_f32 v232, v106, v106\nv_mul_f32 v233, v107, v107\nv_fma_f32 v230, v230, v215, v214\nM 4,168,224\nR 160,189,0\n"
"v_fma_f32 v231, v231, v215, v214\nv_fma_f32 v232, v232, v215, v214\nM 4,172,240\nv_fma_f32 v233, v233, v215, v214\nv_mul_f32 v230, v104, v230\nv_mul_f32 v231, v105, v231\n"
"M 20,168,228\nR 162,189,4096\nv_mul_f32 v232, v106, v232\nv_mul_f32 v233, v107, v233\nM 20,172,244\nv_exp_f32 v230, v230\n"
"v_exp_f32 v231, v231\nM 12,168,232\nR 164,189,8192\nv_fma_f32 v230, v230, v216, v216\nv_exp_f32 v232, v232\nM 12,172,248\n"
"v_exp_f32 v233, v233\nv_fma_f32 v231, v231, v216, v216\nM 28,168,236\nR 166,189,12288\nv_fma_f32 v232, v232, v216, v216\nv_fma_f32 v233, v233, v216, v216\n"
"M 28,172,252\ns_waitcnt vmcnt(13)\ns_barrier\nG 4\nv_rcp_f32 v230, v230\nv_mfma_f32_16x16x32_bf16 v[32:35], v[176:179], a[0:3], 0\n"
"R 168,190,0\nv_mul_f32 v230, v104, v230\nv_rcp_f32 v231, v231\nM 32,180,16\ns_mov_b32 m0, s51\nv_rcp_f32 v232, v232\n"
"v_rcp_f32 v233, v233\nv_mfma_f32_16x16x32_bf16 v[48:51], v[176:179], a[4:7], 0\nR 170,190,4096\nv_mul_f32 v231, v105, v231\nv_mul_f32 v232, v106, v232\nM 48,180,20\n"
"v_mul_f32 v233, v107, v233\nv_cvt_pk_bf16_f32 v104, v230, v231\nv_cvt_pk_bf16_f32 v105, v232, v233\nv_mfma_f32_16x16x32_bf16 v[64:67], v[176:179], a[8:11], 0\nR 172,190,8192\nv_mov_b32 v226, s46\n"
"v_mul_f32 v234, v108, v108\nM 64,180,24\nds_write_addtid_b32 v8 offset:0\nv_mov_b32 v227, s47\nv_mul_f32 v235, v109, v109\nv_mul_f32 v236, v110, v110\n"
"v_mfma_f32_16x16x32_bf16 v[80:83], v[176:179], a[12:15], 0\nR 174,190,12288\ns_add_u32 s61, s81, 385024\ns_lshl_b32 s62, s44, 5\ns_add_u32 s61, s61, s62\ns_waitcnt vmcnt(0)\n"
"s_mov_b64 exec, 1\nbuffer_atomic_swap_x2 v[226:227], off, s[20:23], s61 sc1\ns_mov_b64 exec, -1\nv_mul_f32 v237, v111, v111\nM 80,180,28\nds_write_addtid_b32 v9 offset:256\n"
"G 6\nv_fma_f32 v234, v234, v215, v214\nv_fma_f32 v235, v235, v215, v214\nv_fma_f32 v236, v236, v215, v214\nv_mfma_f32_16x16x32_bf16 v[36:39], v[160:163], a[0:3], 0\nR 176,191,0\n"
"ds_write_addtid_b32 v10 offset:512\nv_fma_f32 v237, v237, v215, v214\nv_mul_f32 v234, v108, v234\nM 36,164,16\nbuffer_store_dwordx4 v[96:99], v252, s[20:23], s84 offen offset:0 sc1\nds_write_addtid_b32 v11 offset:768\n"
"v_mul_f32 v235, v109, v235\nv_mfma_f32_16x16x32_bf16 v[52:55], v[160:163], a[4:7], 0\nR 178,191,4096\nds_write_addtid_b32 v12 offset:1024\nv_mul_f32 v236, v110, v236\nv_mul_f32 v237, v111, v237\n"
"M 52,164,20\nds_write_addtid_b32 v13 offset:1280\nv_exp_f32 v234, v234\nv_exp_f32 v235, v235\nv_mfma_f32_16x16x32_bf16 v[68:71], v[160:163], a[8:11], 0\nR 180,191,8192\n"
"ds_write_addtid_b32 v14 offset:1536\nv_fma_f32 v234, v234, v216, v216\nv_exp_f32 v236, v236\nM 68,164,24\nds_write_addtid_b32 v15 offset:1792\nv_exp_f32 v237, v237\n"
"v_fma_f32 v235, v235, v216, v216\nv_mfma_f32_16x16x32_bf16 v[84:87], v[160:163], a[12:15], 0\nR 182,191,12288\nds_write_addtid_b32 v24 offset:4096\nv_fma_f32 v236, v236, v216, v216\nv_fma_f32 v237, v237, v216, v216\n"
"M 84,164,28\nds_write_addtid_b32 v25 offset:4352\nG 13\nv_rcp_f32 v234, v234\nv_rcp_f32 v235, v235\nv_mfma_f32_16x16x32_bf16 v[40:43], v[168:171], a[0:3], 0\n"
"R 160,188,16384\nds_write_addtid_b32 v26 offset:4608\nds_write_addtid_b32 v27 offset:4864\nds_write_addtid_b32 v28 offset:5120\nds_write_addtid_b32 v29 offset:5376\nds_write_addtid_b32 v30 offset:5632\n"
"ds_write_addtid_b32 v31 offset:5888\nv_mul_f32 v234, v108, v234\nv_rcp_f32 v236, v236\nM 40,172,16\ns_add_u32 m0, s50, 8192\ns_add_u32 s63, s54, 1835264\n"
"buffer_load_dwordx4 v220, s[12:15], s63 offen lds\nv_rcp_f32 v237, v237\nv_mfma_f32_16x16x32_bf16 v[56:59], v[168:171], a[4:7], 0\nR 162,188,20480\nv_mul_f32 v235, v109, v235\nv_mul_f32 v236, v110, v236\n"
"M 56,172,20\nG 1\ns_barrier\nv_mul_f32 v237, v111, v237\nv_cvt_pk_bf16_f32 v106, v234, v235\nv_mfma_f32_16x16x32_bf16 v[72:75], v[168:171], a[8:11], 0\n"
"R 164,188,24576\ns_lshl2_add_u32 s60, s45, s40\nv_lshlrev_b32 v224, 3, v252\ns_mul_i32 s60, s60, 7168\ns_bfm_b64 exec, 14, 42\nbuffer_load_dword v224, v224, s[16:19], s60 offen\n"
"s_mov_b64 exec, -1\nds_read2st64_b32 v[8:9], v225 offset0:0 offset1:1\nv_mov_b32 v248, 0\nv_cvt_pk_bf16_f32 v107, v236, v237\nM 72,172,24\nds_read2st64_b32 v[10:11], v225 offset0:2 offset1:3\n"
"v_mov_b32 v249, 0\nv_mul_f32 v230, v112, v112\nv_mul_f32 v231, v113, v113\nv_mfma_f32_16x16x32_bf16 v[88:91], v[168:171], a[12:15], 0\nR 166,188,28672\ns_add_u32 s61, s82, 385024\n"
"s_mov_b64 exec, 0xf\nbuffer_atomic_or_x2 v[248:249], v250, s[20:23], s61 offen sc0 sc1\ns_mov_b64 exec, -1\nv_mul_f32 v232, v114, v114\nM 88,172,28\nds_read2st64_b32 v[12:13], v225 offset0:4 offset1:5\n"
"v_mul_f32 v233, v115, v115\nv_fma_f32 v230, v230, v218, v217\nv_fma_f32 v231, v231, v218, v217\nv_mfma_f32_16x16x32_bf16 v[44:47], v[176:179], a[0:3], 0\nR 168,189,16384\nds_read2st64_b32 v[14:15], v225 offset0:6 offset1:7\n"
"v_fma_f32 v232, v232, v218, v217\nv_fma_f32 v233, v233, v218, v217\nM 44,180,16\nbuffer_load_dwordx4 v221, s[12:15], s63 offen offset:1024 lds\nds_read2st64_b32 v[24:25], v225 offset0:16 offset1:17\nv_mul_f32 v230, v112, v230\n"
"v_mfma_f32_16x16x32_bf16 v[60:63], v[176:179], a[4:7], 0\nR 170,189,20480\ns_add_u32 m0, s50, 16384\ns_add_u32 s63, s54, 2064640\nv_mul_f32 v231, v113, v231\nM 60,180,20\n"
"buffer_load_dwordx4 v220, s[12:15], s63 offen lds\nv_mul_f32 v232, v114, v232\nv_mfma_f32_16x16x32_bf16 v[76:79], v[176:179], a[8:11], 0\nR 172,189,24576\nv_mul_f32 v233, v115, v233\nG 8\n"
"v_add_f32 v0, v0, v8\nv_exp_f32 v230, v230\nM 76,180,24\nbuffer_store_dwordx4 v[104:107], v252, s[20:23], s84 offen offset:1024 sc1\nv_add_f32 v1, v1, v9\nv_exp_f32 v231, v231\n"
"v_mfma_f32_16x16x32_bf16 v[92:95], v[176:179], a[12:15], 0\nR 174,189,28672\nv_fma_f32 v230, v230, v219, v219\nG 8\nv_add_f32 v2, v2, v10\nv_exp_f32 v232, v232\n"
"M 92,180,28\nG 7\nv_exp_f32 v233, v233\nv_add_f32 v3, v3, v11\nv_fma_f32 v231, v231, v219, v219\nM 32,160,32\n"
"R 176,190,16384\nv_fma_f32 v232, v232, v219, v219\nG 7\nv_add_f32 v4, v4, v12\nv_fma_f32 v233, v233, v219, v219\nM 32,164,48\n"
"buffer_load_dwordx4 v221, s[12:15], s63 offen offset:1024 lds\nv_add_f32 v5, v5, v13\nv_rcp_f32 v230, v230\nM 48,160,36\nR 178,190,20480\nv_mul_f32 v230, v112, v230\n"
"G 6\nv_add_f32 v6, v6, v14\nv_rcp_f32 v231, v231\nM 48,164,52\nds_read2st64_b32 v[26:27], v225 offset0:18 offset1:19\nv_rcp_f32 v232, v232\n"
"v_rcp_f32 v233, v233\nM 64,160,40\nR 180,190,24576\nv_mul_f32 v231, v113, v231\nv_add_f32 v7, v7, v15\nv_mul_f32 v232, v114, v232\n"
"M 64,164,56\ns_add_u32 m0, s50, 24576\nds_read2st64_b32 v[28:29], v225 offset0:20 offset1:21\nG 8\nv_add_f32 v16, v16, v24\nv_mul_f32 v233, v115, v233\n"
"M 80,160,44\nR 182,190,28672\nds_read2st64_b32 v[30:31], v225 offset0:22 offset1:23\nv_add_f32 v17, v17, v25\nM 80,164,60\nG 7\n"
"v_cvt_pk_bf16_f32 v112, v230, v231\nv_cvt_pk_bf16_f32 v113, v232, v233\nM 36,168,32\nR 160,191,16384\nv_mul_f32 v234, v116, v116\nM 36,172,48\n"
"s_add_u32 s63, s54, 2294016\nG 5\nv_add_f32 v18, v18, v26\nv_add_f32 v19, v19, v27\nG 3\nv_add_f32 v20, v20, v28\n"
"v_add_f32 v21, v21, v29\nG 1\nv_add_f32 v22, v22, v30\nv_add_f32 v23, v23, v31\nM 52,168,36\nR 162,191,20480\n"
"v_mul_f32 v235, v117, v117\nM 52,172,52\nbuffer_load_dwordx4 v220, s[12:15], s63 offen lds\nM 68,168,40\nR 164,191,24576\nv_mul_f32 v236, v118, v118\n"
"M 68,172,56\nv_mul_f32 v237, v119, v119\nv_fma_f32 v234, v234, v218, v217\nM 84,168,44\nR 166,191,28672\nv_fma_f32 v235, v235, v218, v217\n"
"M 84,172,60\nv_fma_f32 v236, v236, v218, v217\nv_fma_f32 v237, v237, v218, v217\nM 40,176,32\nR 168,188,32768\nv_mul_f32 v234, v116, v234\n"
"M 40,180,48\nbuffer_load_dwordx4 v221, s[12:15], s63 offen offset:1024 lds\nM 56,176,36\nR 170,188,36864\nv_mul_f32 v235, v117, v235\nM 56,180,52\n"
"s_add_u32 m0, s50, 32768\nv_exp_f32 v234, v234\nM 72,176,40\nR 172,188,40960\ns_add_u32 s63, s54, 2523392\nM 72,180,56\n"
"s_add_u32 s64, s68, 23068672\nv_exp_f32 v235, v235\nM 88,176,44\nR 174,188,45056\nv_mul_f32 v236, v118, v236\nM 88,180,60\n"
"G 4\nv_exp_f32 v236, v236\nM 44,160,32\nR 176,189,32768\ns_waitcnt vmcnt(6)\nv_cmp_eq_u64 s[86:87], s[46:47], v[248:249]\n"
"M 44,164,48\nbuffer_load_dwordx4 v220, s[12:15], s63 offen lds\nM 60,160,36\nR 178,189,36864\ns_and_b32 s62, s86, 0xf\ns_cmp_eq_u32 s62, 0xf\n"
"s_cbranch_scc0 Lq398\nLq399:\nM 60,164,52\ns_add_u32 s65, s68, 23183360\nv_mul_f32 v237, v119, v237\nv_fma_f32 v234, v234, v219, v219\n"
"M 76,160,40\nR 180,189,40960\nv_fma_f32 v235, v235, v219, v219\nM 76,164,56\nbuffer_load_dwordx4 a[0:3], v252, s[20:23], s64 offen sc1\nM 92,160,44\n"
"R 182,189,45056\nv_fma_f32 v236, v236, v219, v219\nM 92,164,60\nG 4\nv_exp_f32 v237, v237\nM 32,168,64\n"
"R 160,190,32768\nv_fma_f32 v237, v237, v219, v219\nM 32,172,80\ns_add_u32 s66, s69, 23068672\nv_rcp_f32 v234, v234\nM 48,168,68\n"
"R 162,190,36864\nv_mul_f32 v234, v116, v234\nM 48,172,84\nbuffer_load_dwordx4 a[4:7], v252, s[20:23], s65 offen sc1\nM 64,168,72\nR 164,190,40960\n"
"v_mul_f32 v230, v120, v120\nM 64,172,88\nv_rcp_f32 v235, v235\nM 80,168,76\nR 166,190,45056\nv_mul_f32 v235, v117, v235\n"
"M 80,172,92\nG 4\nv_rcp_f32 v236, v236\nM 36,176,64\nR 168,191,32768\nv_mul_f32 v236, v118, v236\n"
"M 36,180,80\nbuffer_load_dwordx4 a[8:11], v252, s[20:23], s66 offen sc1\nM 52,176,68\nR 170,191,36864\nv_cvt_pk_bf16_f32 v114, v234, v235\nM 52,180,84\n"
"s_add_u32 s67, s69, 23183360\nv_rcp_f32 v237, v237\nM 68,176,72\nR 172,191,40960\nv_mul_f32 v237, v119, v237\nM 68,180,88\n"
"s_add_u32 s64, s68, 23075840\nbuffer_load_dwordx4 v221, s[12:15], s63 offen offset:1024 lds\nM 84,176,76\nR 174,191,45056\ns_add_u32 m0, s50, 40960\nM 84,180,92\n"
"s_add_u32 s63, s54, 2752768\nG 4\nv_cvt_pk_bf16_f32 v115, v236, v237\nM 40,160,64\nR 176,188,49152\ns_waitcnt vmcnt(7)\n"
"s_barrier\nM 40,164,80\nbuffer_load_dwordx4 a[12:15], v252, s[20:23], s67 offen sc1\nbuffer_load_dwordx4 v220, s[12:15], s63 offen lds\nM 56,160,68\nR 178,188,53248\n"
"v_mul_f32 v231, v121, v121\nM 56,164,84\ns_add_u32 s65, s68, 23190528\nv_mul_f32 v232, v122, v122\nv_mul_f32 v233, v123, v123\nM 72,160,72\n"
"R 180,184,0\nv_fma_f32 v230, v230, v218, v217\nM 72,164,88\nbuffer_load_dwordx4 a[16:19], v252, s[20:23], s64 offen sc1\nbuffer_load_dwordx4 v221, s[12:15], s63 offen offset:1024 lds\nM 88,160,76\n"
"R 182,184,4096\ns_add_u32 m0, s50, 49152\nM 88,164,92\nG 4\nv_fma_f32 v231, v231, v218, v217\nv_fma_f32 v232, v232, v218, v217\n"
"M 44,168,64\nR 160,189,49152\ns_add_u32 s63, s54, 2982144\nM 44,172,80\ns_add_u32 s66, s69, 23075840\nbuffer_load_dwordx4 v220, s[12:15], s63 offen lds\n"
"M 60,168,68\nR 162,189,53248\nv_fma_f32 v233, v233, v218, v217\nM 60,172,84\nbuffer_load_dwordx4 a[20:23], v252, s[20:23], s65 offen sc1\nM 76,168,72\n"
"R 164,185,0\nv_mul_f32 v230, v120, v230\nM 76,172,88\nbuffer_load_dwordx4 v221, s[12:15], s63 offen offset:1024 lds\nM 92,168,76\nR 166,185,4096\n"
"v_mul_f32 v231, v121, v231\nM 92,172,92\nG 4\nv_exp_f32 v230, v230\nM 32,176,96\nR 168,190,49152\n"
"v_mul_f32 v232, v122, v232\nM 32,180,112\nbuffer_load_dwordx4 a[24:27], v252, s[20:23], s66 offen sc1\nM 48,176,100\nR 170,190,53248\nv_mul_f32 v233, v123, v233\n"
"M 48,180,116\ns_add_u32 s67, s69, 23190528\nv_exp_f32 v231, v231\nM 64,176,104\nR 172,186,0\nv_fma_f32 v230, v230, v219, v219\n"
"M 64,180,120\nbuffer_store_dwordx4 v[112:115], v252, s[20:23], s85 offen offset:0 sc1\nM 80,176,108\nR 174,186,4096\nv_fma_f32 v231, v231, v219, v219\nM 80,180,124\n"
"G 4\nv_exp_f32 v232, v232\nM 36,160,96\nR 176,191,49152\nv_fma_f32 v232, v232, v219, v219\nM 36,164,112\n"
"buffer_load_dwordx4 a[28:31], v252, s[20:23], s67 offen sc1\nM 52,160,100\nR 178,191,53248\nv_mul_f32 v234, v124, v124\nM 52,164,116\ns_add_u32 s64, s68, 23069696\n"
"v_exp_f32 v233, v233\nM 68,160,104\nR 180,187,0\nv_fma_f32 v233, v233, v219, v219\nM 68,164,120\ns_add_u32 s65, s68, 23184384\n"
"v_rcp_f32 v230, v230\nM 84,160,108\nR 182,187,4096\nv_mul_f32 v230, v120, v230\nM 84,164,124\nG 4\n"
"v_rcp_f32 v231, v231\nM 40,168,96\nR 160,184,8192\nv_mul_f32 v231, v121, v231\nM 40,172,112\nbuffer_load_dwordx4 a[32:35], v252, s[20:23], s64 offen sc1\n"
"M 56,168,100\nR 162,184,12288\nv_cvt_pk_bf16_f32 v120, v230, v231\nM 56,172,116\ns_add_u32 s66, s69, 23069696\nv_rcp_f32 v232, v232\n"
"M 72,168,104\nR 164,184,16384\nv_mul_f32 v232, v122, v232\nM 72,172,120\nbuffer_load_dwordx4 a[36:39], v252, s[20:23], s65 offen sc1\nM 88,168,108\n"
"R 166,184,20480\nv_mul_f32 v235, v125, v125\nM 88,172,124\nG 4\nv_rcp_f32 v233, v233\nM 44,176,96\n"
"R 168,185,8192\ns_add_u32 m0, s50, 57344\nM 44,180,112\ns_add_u32 s67, s69, 23184384\nv_mul_f32 v233, v123, v233\nv_cvt_pk_bf16_f32 v121, v232, v233\n"
"M 60,176,100\nR 170,185,12288\ns_add_u32 s63, s54, 3211520\nM 60,180,116\nbuffer_load_dwordx4 a[40:43], v252, s[20:23], s66 offen sc1\nM 76,176,104\n"
"R 172,185,16384\nv_mul_f32 v236, v126, v126\nM 76,180,120\nbuffer_load_dwordx4 v220, s[12:15], s63 offen lds\nM 92,176,108\nR 174,185,20480\n"
"v_mul_f32 v237, v127, v127\nM 92,180,124\nG 4\nv_fma_f32 v234, v234, v218, v217\nv_fma_f32 v235, v235, v218, v217\nM 32,160,128\n"
"R 176,186,8192\nv_fma_f32 v236, v236, v218, v217\nM 32,164,144\nbuffer_load_dwordx4 a[44:47], v252, s[20:23], s67 offen sc1\nbuffer_load_dwordx4 v221, s[12:15], s63 offen offset:1024 lds\nM 48,160,132\n"
"R 178,186,12288\ns_add_u32 m0, s50, 65536\nM 48,164,148\ns_add_u32 s64, s68, 23076864\nv_fma_f32 v237, v237, v218, v217\nv_mul_f32 v234, v124, v234\n"
"M 64,160,136\nR 180,186,16384\ns_add_u32 s63, s54, 3440896\nM 64,164,152\ns_add_u32 s65, s68, 23191552\nbuffer_load_dwordx4 v220, s[12:15], s63 offen lds\n"
"M 80,160,140\nR 182,186,20480\nv_mul_f32 v235, v125, v235\nM 80,164,156\nG 4\nv_exp_f32 v234, v234\n"
"M 36,168,128\nR 160,187,8192\nv_mul_f32 v236, v126, v236\nM 36,172,144\nbuffer_load_dwordx4 a[48:51], v252, s[20:23], s64 offen sc1\nbuffer_load_dwordx4 v221, s[12:15], s63 offen offset:1024 lds\n"
"M 52,168,132\nR 162,187,12288\nv_mul_f32 v237, v127, v237\nM 52,172,148\ns_add_u32 s66, s69, 23076864\nv_exp_f32 v235, v235\n"
"M 68,168,136\nR 164,187,16384\ns_waitcnt vmcnt(12)\ns_barrier\nM 68,172,152\nbuffer_load_dwordx4 a[52:55], v252, s[20:23], s65 offen sc1\n"
"s_movk_i32 s63, 2048\nv_mad_u32_u24 v220, v228, s63, v229\nv_add_u32 v221, 916480, v220\ns_add_u32 m0, s50, 73728\ns_add_u32 s63, s56, 0\nbuffer_load_dwordx4 v220, s[16:19], s63 offen lds\n"
"M 84,168,140\nR 166,187,20480\nv_fma_f32 v234, v234, v219, v219\nM 84,172,156\nG 4\nv_exp_f32 v236, v236\n"
"M 40,176,128\nR 168,184,24576\nv_fma_f32 v235, v235, v219, v219\nM 40,180,144\ns_add_u32 s67, s69, 23191552\nbuffer_load_dwordx4 v221, s[16:19], s63 offen offset:1024 lds\n"
"M 56,176,132\nR 170,184,28672\ns_add_u32 m0, s50, 81920\nM 56,180,148\nbuffer_load_dwordx4 a[56:59], v252, s[20:23], s66 offen sc1\nM 72,176,136\n"
"R 172,184,32768\ns_add_u32 s63, s56, 524288\nM 72,180,152\nbuffer_load_dwordx4 v220, s[16:19], s63 offen lds\nM 88,176,140\nR 174,184,36864\n"
"v_fma_f32 v236, v236, v219, v219\nM 88,180,156\nG 4\nv_exp_f32 v237, v237\nM 44,160,128\nR 176,185,24576\n"
"v_fma_f32 v237, v237, v219, v219\nM 44,164,144\nbuffer_load_dwordx4 a[60:63], v252, s[20:23], s67 offen sc1\nbuffer_load_dwordx4 v221, s[16:19], s63 offen offset:1024 lds\nM 60,160,132\nR 178,185,28672\n"
"M 60,164,148\nv_rcp_f32 v234, v234\nM 76,160,136\nR 180,185,32768\nv_mul_f32 v234, v124, v234\nM 76,164,152\n"
"v_rcp_f32 v235, v235\nM 92,160,140\nR 182,185,36864\nv_mul_f32 v235, v125, v235\nM 92,164,156\nG 4\n"
"v_rcp_f32 v236, v236\nM 32,168,160\nR 160,186,24576\nv_mul_f32 v236, v126, v236\nM 32,172,176\ns_add_u32 m0, s50, 90112\n"
"v_rcp_f32 v237, v237\nM 48,168,164\nR 162,186,28672\nv_mul_f32 v237, v127, v237\nM 48,172,180\ns_add_u32 s63, s56, 65536\n"
"v_cvt_pk_bf16_f32 v122, v234, v235\nv_cvt_pk_bf16_f32 v123, v236, v237\nM 64,168,168\nR 164,186,32768\nM 64,172,184\nbuffer_load_dwordx4 v220, s[16:19], s63 offen lds\n"
"M 80,168,172\nR 166,186,36864\nM 80,172,188\nbuffer_store_dwordx4 v[120:123], v252, s[20:23], s85 offen offset:1024 sc1\nG 4\nM 36,176,160\n"
"R 168,187,24576\nM 36,180,176\nbuffer_load_dwordx4 v221, s[16:19], s63 offen offset:1024 lds\nM 52,176,164\nR 170,187,28672\nv_mul_f32 v230, v0, v0\n"
"v_mul_f32 v231, v1, v1\nM 52,180,180\ns_add_u32 s84, s73, 23074816\ns_add_u32 s85, s73, 23189504\nv_mul_f32 v232, v2, v2\nv_mul_f32 v233, v3, v3\n"
"M 68,176,168\nR 172,187,32768\nv_fma_f32 v230, v230, v215, v214\nv_fma_f32 v231, v231, v215, v214\nM 68,180,184\nv_fma_f32 v232, v232, v215, v214\n"
"v_fma_f32 v233, v233, v215, v214\nv_mul_f32 v230, v0, v230\nM 84,176,172\nR 174,187,36864\nv_mul_f32 v231, v1, v231\nv_mul_f32 v232, v2, v232\n"
"M 84,180,188\nG 4\nv_exp_f32 v230, v230\nv_mul_f32 v233, v3, v233\nM 40,160,160\nR 176,184,40960\n"
"v_fma_f32 v230, v230, v216, v216\nv_exp_f32 v231, v231\nM 40,164,176\nv_exp_f32 v232, v232\nv_exp_f32 v233, v233\nM 56,160,164\n"
"R 178,184,45056\nv_fma_f32 v231, v231, v216, v216\nv_fma_f32 v232, v232, v216, v216\nM 56,164,180\nv_rcp_f32 v230, v230\nv_fma_f32 v233, v233, v216, v216\n"
"M 72,160,168\nR 180,184,49152\nv_mul_f32 v230, v0, v230\nv_rcp_f32 v231, v231\nM 72,164,184\nv_rcp_f32 v232, v232\n"
"v_rcp_f32 v233, v233\nM 88,160,172\nR 182,184,53248\nv_mul_f32 v231, v1, v231\nv_mul_f32 v232, v2, v232\nM 88,164,188\n"
"G 4\nv_mul_f32 v233, v3, v233\nv_cvt_pk_bf16_f32 v0, v230, v231\nv_cvt_pk_bf16_f32 v1, v232, v233\nM 44,168,160\nR 160,185,40960\n"
"v_mul_f32 v234, v4, v4\nv_mul_f32 v235, v5, v5\nM 44,172,176\nv_mul_f32 v236, v6, v6\nv_mul_f32 v237, v7, v7\nv_fma_f32 v234, v234, v215, v214\n"
"M 60,168,164\nR 162,185,45056\nv_fma_f32 v235, v235, v215, v214\nv_fma_f32 v236, v236, v215, v214\nM 60,172,180\nv_fma_f32 v237, v237, v215, v214\n"
"v_mul_f32 v234, v4, v234\nv_mul_f32 v235, v5, v235\nM 76,168,168\nR 164,185,49152\nv_mul_f32 v236, v6, v236\nv_mul_f32 v237, v7, v237\n"
"M 76,172,184\nv_exp_f32 v234, v234\nv_exp_f32 v235, v235\nM 92,168,172\nR 166,185,53248\nv_fma_f32 v234, v234, v216, v216\n"
"v_exp_f32 v236, v236\nM 92,172,188\nG 4\nv_exp_f32 v237, v237\nv_fma_f32 v235, v235, v216, v216\nM 32,176,192\n"
"R 168,186,40960\nv_fma_f32 v236, v236, v216, v216\nv_fma_f32 v237, v237, v216, v216\nM 32,180,208\nv_rcp_f32 v234, v234\nv_rcp_f32 v235, v235\n"
"M 48,176,196\nR 170,186,45056\nv_mul_f32 v234, v4, v234\nv_rcp_f32 v236, v236\nM 48,180,212\nv_rcp_f32 v237, v237\n"
"v_mul_f32 v235, v5, v235\nM 64,176,200\nR 172,186,49152\nv_mul_f32 v236, v6, v236\nv_mul_f32 v237, v7, v237\nM 64,180,216\n"
"v_cvt_pk_bf16_f32 v2, v234, v235\nv_cvt_pk_bf16_f32 v3, v236, v237\nv_mul_f32 v230, v16, v16\nM 80,176,204\nR 174,186,53248\nv_mul_f32 v231, v17, v17\n"
"v_mul_f32 v232, v18, v18\nM 80,180,220\nG 4\nv_mul_f32 v233, v19, v19\nv_fma_f32 v230, v230, v218, v217\nv_fma_f32 v231, v231, v218, v217\n"
"M 36,160,192\nR 176,187,40960\ns_waitcnt vmcnt(3)\ns_barrier\nv_fma_f32 v232, v232, v218, v217\nM 36,164,208\n"
"v_fma_f32 v233, v233, v218, v217\nv_mul_f32 v230, v16, v230\nv_mul_f32 v231, v17, v231\nM 52,160,196\nR 178,187,45056\nv_mul_f32 v232, v18, v232\n"
"v_mul_f32 v233, v19, v233\nM 52,164,212\nv_exp_f32 v230, v230\nv_exp_f32 v231, v231\nM 68,160,200\nR 180,187,49152\n"
"v_fma_f32 v230, v230, v219, v219\nv_exp_f32 v232, v232\nM 68,164,216\nv_exp_f32 v233, v233\nv_fma_f32 v231, v231, v219, v219\nM 84,160,204\n"
"R 182,187,53248\nv_fma_f32 v232, v232, v219, v219\nv_fma_f32 v233, v233, v219, v219\nM 84,164,220\nG 4\nv_rcp_f32 v230, v230\n"
"v_rcp_f32 v231, v231\nM 40,168,192\nR 160,184,57344\nv_mul_f32 v230, v16, v230\nv_rcp_f32 v232, v232\nM 40,172,208\n"
"v_rcp_f32 v233, v233\nv_mul_f32 v231, v17, v231\nM 56,168,196\nR 162,184,61440\nv_mul_f32 v232, v18, v232\nv_mul_f32 v233, v19, v233\n"
"M 56,172,212\ns_add_u32 m0, s50, 98304\nv_cvt_pk_bf16_f32 v16, v230, v231\nv_cvt_pk_bf16_f32 v17, v232, v233\nv_mul_f32 v234, v20, v20\nM 72,168,200\n"
"R 164,188,0\nv_mul_f32 v235, v21, v21\nv_mul_f32 v236, v22, v22\nM 72,172,216\ns_add_u32 s63, s56, 589824\nv_mul_f32 v237, v23, v23\n"
"v_fma_f32 v234, v234, v218, v217\nv_fma_f32 v235, v235, v218, v217\nM 88,168,204\nR 166,188,4096\nv_mov_b32 v226, s46\nv_fma_f32 v236, v236, v218, v217\n"
"M 88,172,220\nG 4\nv_mov_b32 v227, s47\nv_fma_f32 v237, v237, v218, v217\nv_mul_f32 v234, v20, v234\nM 44,176,192\n"
"R 168,185,57344\ns_add_u32 s61, s81, 385032\ns_lshl_b32 s62, s44, 5\ns_add_u32 s61, s61, s62\ns_waitcnt vmcnt(0)\ns_mov_b64 exec, 1\n"
"buffer_atomic_swap_x2 v[226:227], off, s[20:23], s61 sc1\ns_mov_b64 exec, -1\nv_mul_f32 v235, v21, v235\nM 44,180,208\nbuffer_load_dwordx4 v220, s[16:19], s63 offen lds\nv_mul_f32 v236, v22, v236\n"
"M 60,176,196\nR 170,185,61440\nbuffer_load_dwordx4 v221, s[16:19], s63 offen offset:1024 lds\nv_mul_f32 v237, v23, v237\nM 60,180,212\ns_add_u32 m0, s50, 106496\n"
"v_exp_f32 v234, v234\nv_exp_f32 v235, v235\nM 76,176,200\nR 172,189,0\ns_add_u32 s63, s57, 0\nv_exp_f32 v236, v236\n"
"M 76,180,216\nbuffer_load_dwordx4 v220, s[16:19], s63 offen lds\nv_exp_f32 v237, v237\nM 92,176,204\nR 174,189,4096\ns_mov_b32 s60, 158208\n"
"v_bfe_u32 v246, v252, 4, 4\nv_lshl_add_u32 v246, v246, 3, s60\nv_fma_f32 v234, v234, v219, v219\nM 92,180,220\nds_read_b64 v[238:239], v246 offset:0\nG 5\n"
"v_fma_f32 v235, v235, v219, v219\nM 32,160,224\nR 176,186,57344\nds_read_b64 v[240:241], v246 offset:128\nv_fma_f32 v236, v236, v219, v219\nM 32,164,240\n"
"ds_read_b64 v[242:243], v246 offset:256\nv_fma_f32 v237, v237, v219, v219\nv_rcp_f32 v234, v234\nM 48,160,228\nR 178,186,61440\nds_read_b64 v[244:245], v246 offset:384\n"
"v_rcp_f32 v235, v235\nM 48,164,244\nds_read_b64 v[246:247], v246 offset:0\nbuffer_load_dwordx4 v221, s[16:19], s63 offen offset:1024 lds\nM 64,160,232\nR 180,190,0\n"
"s_add_u32 m0, s50, 114688\ns_add_u32 s63, s57, 524288\nM 64,164,248\nbuffer_load_dwordx4 v220, s[16:19], s63 offen lds\nM 80,160,236\nR 182,190,4096\n"
"v_mul_f32 v234, v20, v234\nM 80,164,252\nG 9\nv_rcp_f32 v236, v236\nM 36,168,224\nR 160,187,57344\n"
"v_mul_f32 v235, v21, v235\nM 36,172,240\nbuffer_load_dwordx4 v221, s[16:19], s63 offen offset:1024 lds\nM 52,168,228\nR 162,187,61440\ns_add_u32 s63, s57, 65536\n"
"M 52,172,244\ns_add_u32 m0, s50, 0\nv_rcp_f32 v237, v237\nM 68,168,232\nR 164,191,0\nG 9\n"
"v_mfma_f32_16x16x32_bf16 v[32:35], v[238:241], v[208:211], v[32:35]\nv_mul_f32 v236, v22, v236\nM 68,172,248\nbuffer_load_dwordx4 v220, s[16:19], s63 offen lds\nM 84,168,236\nR 166,191,4096\n"
"G 9\nv_mfma_f32_16x16x32_bf16 v[36:39], v[240:243], v[208:211], v[36:39]\nv_mul_f32 v237, v23, v237\nM 84,172,252\nG 4\nv_cvt_pk_bf16_f32 v18, v234, v235\n"
"v_cvt_pk_bf16_f32 v19, v236, v237\nM 40,176,224\nR 168,188,8192\nv_mfma_f32_16x16x32_bf16 v[40:43], v[242:245], v[208:211], v[40:43]\nbuffer_load_dwordx4 v221, s[16:19], s63 offen offset:1024 lds\nM 40,180,240\n"
"s_add_u32 m0, s50, 8192\ns_add_u32 s63, s57, 589824\nbuffer_load_dwordx4 v220, s[16:19], s63 offen lds\nM 56,176,228\nR 170,188,12288\nv_mfma_f32_16x16x32_bf16 v[44:47], v[244:247], v[208:211], v[44:47]\n"
"buffer_store_dwordx4 v[0:3], v252, s[20:23], s84 offen offset:0 sc1\nM 56,180,244\nbuffer_store_dwordx4 v[16:19], v252, s[20:23], s85 offen offset:0 sc1\nM 72,176,232\nR 172,188,16384\nv_mfma_f32_16x16x32_bf16 v[48:51], v[244:247], v[210:213], v[48:51]\n"
"M 72,180,248\nbuffer_load_dwordx4 v221, s[16:19], s63 offen offset:1024 lds\nM 88,176,236\nR 174,188,20480\nv_mfma_f32_16x16x32_bf16 v[52:55], v[238:241], v[210:213], v[52:55]\nM 88,180,252\n"
"G 4\nM 44,160,224\nR 176,189,8192\nv_mfma_f32_16x16x32_bf16 v[56:59], v[240:243], v[210:213], v[56:59]\nM 44,164,240\nM 60,160,228\n"
"R 178,189,12288\nv_mfma_f32_16x16x32_bf16 v[60:63], v[242:245], v[210:213], v[60:63]\nv_mov_b32 v248, 0\nM 60,164,244\nv_mov_b32 v249, 0\nM 76,160,232\n"
"R 180,189,16384\ns_add_u32 s61, s82, 385032\ns_mov_b64 exec, 0xf\nbuffer_atomic_or_x2 v[248:249], v250, s[20:23], s61 offen sc0 sc1\ns_mov_b64 exec, -1\nM 76,164,248\n"
"M 92,160,236\nR 182,189,20480\nM 92,164,252\ns_waitcnt vmcnt(11)\ns_barrier\nG 4\n"
"v_mfma_f32_16x16x32_bf16 v[96:99], v[168:171], a[0:3], 0\nR 160,190,8192\nM 96,172,16\ns_mov_b32 m0, s51\nv_mfma_f32_16x16x32_bf16 v[112:115], v[168:171], a[4:7], 0\nR 162,190,12288\n"
"M 112,172,20\nds_write_addtid_b32 v64 offset:0\nv_mfma_f32_16x16x32_bf16 v[128:131], v[168:171], a[8:11], 0\nR 164,190,16384\nM 128,172,24\nds_write_addtid_b32 v65 offset:256\n"
"v_mfma_f32_16x16x32_bf16 v[144:147], v[168:171], a[12:15], 0\nR 166,190,20480\nM 144,172,28\nds_write_addtid_b32 v66 offset:512\nG 7\nv_mfma_f32_16x16x32_bf16 v[100:103], v[176:179], a[0:3], 0\n"
"R 168,191,8192\nM 100,180,16\nds_write_addtid_b32 v67 offset:768\nv_mfma_f32_16x16x32_bf16 v[116:119], v[176:179], a[4:7], 0\nR 170,191,12288\nM 116,180,20\n"
"ds_write_addtid_b32 v68 offset:1024\nv_mfma_f32_16x16x32_bf16 v[132:135], v[176:179], a[8:11], 0\nR 172,191,16384\nM 132,180,24\nds_write_addtid_b32 v69 offset:1280\nv_mfma_f32_16x16x32_bf16 v[148:151], v[176:179], a[12:15], 0\n"
"R 174,191,20480\nds_write_addtid_b32 v70 offset:1536\nM 148,180,28\nds_write_addtid_b32 v71 offset:1792\nds_write_addtid_b32 v72 offset:2048\nG 11\n"
"v_mfma_f32_16x16x32_bf16 v[104:107], v[160:163], a[0:3], 0\nR 176,188,24576\nds_write_addtid_b32 v73 offset:2304\nM 104,164,16\nds_write_addtid_b32 v74 offset:2560\nds_write_addtid_b32 v75 offset:2816\n"
"v_mfma_f32_16x16x32_bf16 v[120:123], v[160:163], a[4:7], 0\nR 178,188,28672\nds_write_addtid_b32 v76 offset:3072\nM 120,164,20\nds_write_addtid_b32 v77 offset:3328\nds_write_addtid_b32 v78 offset:3584\n"
"v_mfma_f32_16x16x32_bf16 v[136:139], v[160:163], a[8:11], 0\nR 180,188,32768\nds_write_addtid_b32 v79 offset:3840\nM 136,164,24\nds_write_addtid_b32 v80 offset:4096\nds_write_addtid_b32 v81 offset:4352\n"
"v_mfma_f32_16x16x32_bf16 v[152:155], v[160:163], a[12:15], 0\nR 182,188,36864\nds_write_addtid_b32 v82 offset:4608\nM 152,164,28\nds_write_addtid_b32 v83 offset:4864\nds_write_addtid_b32 v84 offset:5120\n"
"G 15\nv_mfma_f32_16x16x32_bf16 v[108:111], v[168:171], a[0:3], 0\nR 160,189,24576\nds_write_addtid_b32 v85 offset:5376\nM 108,172,16\nds_write_addtid_b32 v86 offset:5632\n"
"ds_write_addtid_b32 v87 offset:5888\nv_mfma_f32_16x16x32_bf16 v[124:127], v[168:171], a[4:7], 0\nR 162,189,28672\nds_write_addtid_b32 v88 offset:6144\nM 124,172,20\nds_write_addtid_b32 v89 offset:6400\n"
"ds_write_addtid_b32 v90 offset:6656\nv_mfma_f32_16x16x32_bf16 v[140:143], v[168:171], a[8:11], 0\nR 164,189,32768\nds_write_addtid_b32 v91 offset:6912\nM 140,172,24\ns_add_u32 s64, s68, 23070720\n"
"ds_write_addtid_b32 v92 offset:7168\nds_write_addtid_b32 v93 offset:7424\nv_mfma_f32_16x16x32_bf16 v[156:159], v[168:171], a[12:15], 0\nR 166,189,36864\ns_waitcnt vmcnt(0)\nv_cmp_eq_u64 s[86:87], s[46:47], v[248:249]\n"
"M 156,172,28\nds_write_addtid_b32 v94 offset:7680\nds_write_addtid_b32 v95 offset:7936\nG 15\nM 96,176,32\nR 168,190,24576\n"
"s_and_b32 s62, s86, 0xf\ns_cmp_eq_u32 s62, 0xf\ns_cbranch_scc0 Lq433\nLq434:\nM 96,180,48\ns_add_u32 s65, s68, 23185408\n"
"s_add_u32 m0, s50, 16384\nv_mov_b32 v226, s46\nM 112,176,36\nR 170,190,28672\nG 2\ns_barrier\n"
"v_mov_b32 v227, s47\nM 112,180,52\ns_add_u32 s61, s81, 385048\ns_lshl_b32 s62, s44, 5\ns_add_u32 s61, s61, s62\ns_waitcnt vmcnt(0)\n"
"s_mov_b64 exec, 1\nbuffer_atomic_swap_x2 v[226:227], off, s[20:23], s61 sc1\ns_mov_b64 exec, -1\ns_add_u32 s63, s56, 131072\nds_read2st64_b32 v[64:65], v225 offset0:0 offset1:1\nM 128,176,40\n"
"R 172,190,32768\nbuffer_load_dwordx4 v220, s[16:19], s63 offen lds\nds_read2st64_b32 v[66:67], v225 offset0:2 offset1:3\nM 128,180,56\nbuffer_load_dwordx4 a[64:67], v252, s[20:23], s64 offen sc1\nbuffer_load_dwordx4 v221, s[16:19], s63 offen offset:1024 lds\n"
"ds_read2st64_b32 v[68:69], v225 offset0:4 offset1:5\nM 144,176,44\nR 174,190,36864\ns_add_u32 m0, s50, 24576\ns_add_u32 s63, s56, 655360\nbuffer_load_dwordx4 v220, s[16:19], s63 offen lds\n"
"ds_read2st64_b32 v[70:71], v225 offset0:6 offset1:7\nM 144,180,60\nds_read2st64_b32 v[72:73], v225 offset0:8 offset1:9\nds_read2st64_b32 v[74:75], v225 offset0:10 offset1:11\nM 100,160,32\nR 176,191,24576\n"
"buffer_load_dwordx4 v221, s[16:19], s63 offen offset:1024 lds\nds_read2st64_b32 v[76:77], v225 offset0:12 offset1:13\nM 100,164,48\nbuffer_load_dwordx4 a[68:71], v252, s[20:23], s65 offen sc1\nG 9\nv_add_f32 v32, v32, v64\n"
"M 116,160,36\nR 178,191,28672\ns_add_u32 s67, s69, 23185408\nv_add_f32 v33, v33, v65\nM 116,164,52\ns_add_u32 s66, s69, 23070720\n"
"s_add_u32 m0, s50, 32768\ns_add_u32 s63, s56, 196608\nG 8\nv_add_f32 v34, v34, v66\nM 132,160,40\nR 180,191,32768\n"
"buffer_load_dwordx4 v220, s[16:19], s63 offen lds\nv_add_f32 v35, v35, v67\nM 132,164,56\nbuffer_load_dwordx4 a[72:75], v252, s[20:23], s66 offen sc1\nG 8\nv_add_f32 v36, v36, v68\n"
"M 148,160,44\nR 182,191,36864\nv_add_f32 v37, v37, v69\nG 7\nv_add_f32 v38, v38, v70\nM 148,164,60\n"
"ds_read2st64_b32 v[78:79], v225 offset0:14 offset1:15\nv_add_f32 v39, v39, v71\nM 104,168,32\nR 160,188,40960\nbuffer_load_dwordx4 v221, s[16:19], s63 offen offset:1024 lds\nG 8\n"
"v_add_f32 v40, v40, v72\nM 104,172,48\nbuffer_load_dwordx4 a[76:79], v252, s[20:23], s67 offen sc1\nv_add_f32 v41, v41, v73\nM 120,168,36\nR 162,188,45056\n"
"G 8\nv_add_f32 v42, v42, v74\nv_add_f32 v43, v43, v75\nM 120,172,52\ns_add_u32 s64, s68, 23077888\nds_read2st64_b32 v[80:81], v225 offset0:16 offset1:17\n"
"G 7\nv_add_f32 v44, v44, v76\nM 136,168,40\nR 164,188,49152\nds_read2st64_b32 v[82:83], v225 offset0:18 offset1:19\nv_add_f32 v45, v45, v77\n"
"M 136,172,56\ns_add_u32 s65, s68, 23192576\nds_read2st64_b32 v[84:85], v225 offset0:20 offset1:21\nG 6\nv_add_f32 v46, v46, v78\nM 152,168,44\n"
"R 166,188,53248\nds_read2st64_b32 v[86:87], v225 offset0:22 offset1:23\nv_add_f32 v47, v47, v79\nM 152,172,60\nds_read2st64_b32 v[88:89], v225 offset0:24 offset1:25\nds_read2st64_b32 v[90:91], v225 offset0:26 offset1:27\n"
"M 108,176,32\nR 168,189,40960\nds_read2st64_b32 v[92:93], v225 offset0:28 offset1:29\nM 108,180,48\nbuffer_load_dwordx4 a[80:83], v252, s[20:23], s64 offen sc1\nG 9\n"
"v_add_f32 v48, v48, v80\nM 124,176,36\nR 170,189,45056\nv_add_f32 v49, v49, v81\nG 8\nv_add_f32 v50, v50, v82\n"
"M 124,180,52\nbuffer_load_dwordx4 a[84:87], v252, s[20:23], s65 offen sc1\nv_add_f32 v51, v51, v83\nM 140,176,40\nR 172,189,49152\nG 8\n"
"v_add_f32 v52, v52, v84\nv_add_f32 v53, v53, v85\nM 140,180,56\ns_add_u32 s66, s69, 23077888\nds_read2st64_b32 v[94:95], v225 offset0:30 offset1:31\nG 7\n"
"v_add_f32 v54, v54, v86\nM 156,176,44\nR 174,189,53248\nv_add_f32 v55, v55, v87\nM 156,180,60\nG 7\n"
"v_add_f32 v56, v56, v88\nv_add_f32 v57, v57, v89\nv_mfma_f32_16x16x32_bf16 v[0:3], v[160:163], a[0:3], 0\nR 176,190,40960\nLq601:\ns_lshr_b32 s60, s45, 3\n"
"s_lshl2_add_u32 s60, s60, s40\ns_lshl_b32 s60, s60, 8\nv_lshl_add_u32 v224, v252, 3, s60\nv_add_u32 v224, Lq601+4096-LqPC, v224\ns_mov_b64 exec, 3\nglobal_load_dword v224, v224, s[98:99]\n"
"s_mov_b64 exec, -1\nM 0,164,16\nbuffer_load_dwordx4 a[88:91], v252, s[20:23], s66 offen sc1\nv_mfma_f32_16x16x32_bf16 v[16:19], v[160:163], a[4:7], 0\nR 178,190,45056\nG 8\n"
"v_add_f32 v58, v58, v90\nM 16,164,20\ns_add_u32 s67, s69, 23192576\nv_add_f32 v59, v59, v91\nG 6\nv_add_f32 v60, v60, v92\n"
"v_mfma_f32_16x16x32_bf16 a[224:227], v[160:163], a[8:11], 0\nR 180,190,49152\nv_add_f32 v61, v61, v93\nv_mfma_f32_16x16x32_bf16 a[224:227], v[164:167], a[24:27], a[224:227]\nbuffer_load_dwordx4 a[92:95], v252, s[20:23], s67 offen sc1\nv_mfma_f32_16x16x32_bf16 a[240:243], v[160:163], a[12:15], 0\n"
"R 182,190,53248\nG 5\nv_add_f32 v62, v62, v94\nv_mfma_f32_16x16x32_bf16 a[240:243], v[164:167], a[28:31], a[240:243]\ns_add_u32 s63, s56, 720896\nG 4\n"
"v_add_f32 v63, v63, v95\nv_mfma_f32_16x16x32_bf16 v[4:7], v[168:171], a[0:3], 0\nR 160,191,40960\ns_add_u32 m0, s50, 40960\nv_mul_f32 v230, v32, v32\nM 4,172,16\n"
"s_add_u32 s64, s68, 23071744\ns_add_u32 s84, s73, 23072768\ns_add_u32 s85, s73, 23187456\nv_mul_f32 v231, v33, v33\nv_mfma_f32_16x16x32_bf16 v[20:23], v[168:171], a[4:7], 0\nR 162,191,45056\n"
"v_mov_b32 v248, 0\nv_mul_f32 v232, v34, v34\nM 20,172,20\nbuffer_load_dwordx4 a[96:99], v252, s[20:23], s64 offen sc1\nv_mov_b32 v249, 0\nv_mul_f32 v233, v35, v35\n"
"v_mfma_f32_16x16x32_bf16 a[228:231], v[168:171], a[8:11], 0\nR 164,191,49152\ns_add_u32 s61, s82, 385048\ns_mov_b64 exec, 0xf\nbuffer_atomic_or_x2 v[248:249], v250, s[20:23], s61 offen sc0 sc1\ns_mov_b64 exec, -1\n"
"v_fma_f32 v230, v230, v215, v214\nv_mfma_f32_16x16x32_bf16 a[228:231], v[172:175], a[24:27], a[228:231]\ns_add_u32 s65, s68, 23186432\nbuffer_load_dwordx4 v220, s[16:19], s63 offen lds\nv_fma_f32 v231, v231, v215, v214\nv_mfma_f32_16x16x32_bf16 a[244:247], v[168:171], a[12:15], 0\n"
"R 166,191,53248\nv_fma_f32 v232, v232, v215, v214\nv_fma_f32 v233, v233, v215, v214\nv_mfma_f32_16x16x32_bf16 a[244:247], v[172:175], a[28:31], a[244:247]\nG 4\nv_mul_f32 v230, v32, v230\n"
"v_mul_f32 v231, v33, v231\nv_mul_f32 v232, v34, v232\nv_mfma_f32_16x16x32_bf16 v[8:11], v[176:179], a[0:3], 0\nR 168,184,0\nv_mul_f32 v233, v35, v233\nv_exp_f32 v230, v230\n"
"M 8,180,16\nbuffer_load_dwordx4 a[100:103], v252, s[20:23], s65 offen sc1\nbuffer_load_dwordx4 v221, s[16:19], s63 offen offset:1024 lds\nv_exp_f32 v231, v231\nv_mfma_f32_16x16x32_bf16 v[24:27], v[176:179], a[4:7], 0\nR 170,184,4096\n"
"s_add_u32 s67, s69, 23186432\nv_exp_f32 v232, v232\nM 24,180,20\ns_add_u32 s66, s69, 23071744\ns_add_u32 s63, s56, 393216\nv_fma_f32 v230, v230, v216, v216\n"
"v_exp_f32 v233, v233\nv_mfma_f32_16x16x32_bf16 a[232:235], v[176:179], a[8:11], 0\nR 172,184,8192\ns_add_u32 m0, s50, 49152\nv_fma_f32 v231, v231, v216, v216\nv_mfma_f32_16x16x32_bf16 a[232:235], v[180:183], a[24:27], a[232:235]\n"
"buffer_load_dwordx4 a[104:107], v252, s[20:23], s66 offen sc1\nbuffer_load_dwordx4 v220, s[16:19], s63 offen lds\nv_fma_f32 v232, v232, v216, v216\nv_mfma_f32_16x16x32_bf16 a[248:251], v[176:179], a[12:15], 0\nR 174,184,12288\nv_fma_f32 v233, v233, v216, v216\n"
"v_rcp_f32 v230, v230\nv_mfma_f32_16x16x32_bf16 a[248:251], v[180:183], a[28:31], a[248:251]\nG 4\nv_rcp_f32 v231, v231\nv_rcp_f32 v232, v232\nv_mfma_f32_16x16x32_bf16 v[12:15], v[160:163], a[0:3], 0\n"
"R 176,185,0\nv_mul_f32 v230, v32, v230\nv_rcp_f32 v233, v233\nM 12,164,16\nbuffer_load_dwordx4 a[108:111], v252, s[20:23], s67 offen sc1\nbuffer_load_dwordx4 v221, s[16:19], s63 offen offset:1024 lds\n"
"v_mul_f32 v231, v33, v231\nv_mfma_f32_16x16x32_bf16 v[28:31], v[160:163], a[4:7], 0\nR 178,185,4096\ns_add_u32 m0, s50, 57344\nv_mul_f32 v232, v34, v232\nM 28,164,20\n"
"s_add_u32 s64, s68, 23078912\nv_mul_f32 v233, v35, v233\nv_cvt_pk_bf16_f32 v32, v230, v231\nv_cvt_pk_bf16_f32 v33, v232, v233\nv_mfma_f32_16x16x32_bf16 a[236:239], v[160:163], a[8:11], 0\nR 180,185,8192\n"
"s_add_u32 s63, s56, 458752\nv_mul_f32 v234, v36, v36\nv_mfma_f32_16x16x32_bf16 a[236:239], v[164:167], a[24:27], a[236:239]\ns_add_u32 s65, s68, 23193600\nbuffer_load_dwordx4 v220, s[16:19], s63 offen lds\nv_mul_f32 v235, v37, v37\n"
"v_mfma_f32_16x16x32_bf16 a[252:255], v[160:163], a[12:15], 0\nR 182,185,12288\nv_mul_f32 v236, v38, v38\nv_mul_f32 v237, v39, v39\nv_mfma_f32_16x16x32_bf16 a[252:255], v[164:167], a[28:31], a[252:255]\ns_waitcnt vmcnt(16)\n"
"s_barrier\nG 4\nv_fma_f32 v234, v234, v215, v214\nM 0,168,32\nR 160,186,0\nv_fma_f32 v235, v235, v215, v214\n"
"v_fma_f32 v236, v236, v215, v214\nM 0,172,48\nbuffer_load_dwordx4 a[112:115], v252, s[20:23], s64 offen sc1\nbuffer_load_dwordx4 v221, s[16:19], s63 offen offset:1024 lds\nv_fma_f32 v237, v237, v215, v214\nM 16,168,36\n"
"R 162,186,4096\nv_mul_f32 v234, v36, v234\nv_mul_f32 v235, v37, v235\nM 16,172,52\nbuffer_load_dwordx4 a[116:119], v252, s[20:23], s65 offen sc1\nv_mul_f32 v236, v38, v236\n"
"v_mfma_f32_16x16x32_bf16 a[224:227], v[168:171], a[40:43], a[224:227]\nR 164,186,8192\nv_mul_f32 v237, v39, v237\nv_exp_f32 v234, v234\nv_mfma_f32_16x16x32_bf16 a[224:227], v[172:175], a[56:59], a[224:227]\ns_add_u32 s66, s69, 23078912\n"
"v_exp_f32 v235, v235\nv_exp_f32 v236, v236\nv_mfma_f32_16x16x32_bf16 a[240:243], v[168:171], a[44:47], a[240:243]\nR 166,186,12288\nv_fma_f32 v234, v234, v216, v216\nv_exp_f32 v237, v237\n"
"v_mfma_f32_16x16x32_bf16 a[240:243], v[172:175], a[60:63], a[240:243]\nG 4\nv_rcp_f32 v234, v234\nv_fma_f32 v235, v235, v216, v216\nM 4,176,32\nR 168,187,0\n"
"v_fma_f32 v236, v236, v216, v216\nv_fma_f32 v237, v237, v216, v216\nM 4,180,48\nbuffer_load_dwordx4 a[120:123], v252, s[20:23], s66 offen sc1\nv_rcp_f32 v235, v235\nM 20,176,36\n"
"R 170,187,4096\nv_mul_f32 v234, v36, v234\nv_rcp_f32 v236, v236\nM 20,180,52\ns_add_u32 s67, s69, 23193600\nv_rcp_f32 v237, v237\n"
"v_mul_f32 v235, v37, v235\nv_mfma_f32_16x16x32_bf16 a[228:231], v[176:179], a[40:43], a[228:231]\nR 172,187,8192\nv_mul_f32 v236, v38, v236\nv_mul_f32 v237, v39, v237\nv_mfma_f32_16x16x32_bf16 a[228:231], v[180:183], a[56:59], a[228:231]\n"
"buffer_load_dwordx4 a[124:127], v252, s[20:23], s67 offen sc1\nv_cvt_pk_bf16_f32 v34, v234, v235\nv_mfma_f32_16x16x32_bf16 a[244:247], v[176:179], a[44:47], a[244:247]\nR 174,187,12288\ns_waitcnt vmcnt(13)\nv_cmp_eq_u64 s[86:87], s[46:47], v[248:249]\n"
"v_cvt_pk_bf16_f32 v35, v236, v237\nv_mfma_f32_16x16x32_bf16 a[244:247], v[180:183], a[60:63], a[244:247]\ns_add_u32 s63, s57, 131072\nG 4\nv_mul_f32 v230, v40, v40\nv_mul_f32 v231, v41, v41\n"
"M 8,160,32\nR 176,184,16384\ns_and_b32 s62, s86, 0xf\ns_cmp_eq_u32 s62, 0xf\ns_cbranch_scc0 Lq468\nLq469:\n"
"v_mul_f32 v232, v42, v42\nM 8,164,48\ns_add_u32 s64, s68, 23074816\nv_mul_f32 v233, v43, v43\nv_fma_f32 v230, v230, v215, v214\nv_fma_f32 v231, v231, v215, v214\n"
"M 24,160,36\nR 178,184,20480\ns_add_u32 m0, s50, 65536\nv_fma_f32 v232, v232, v215, v214\nM 24,164,52\nbuffer_load_dwordx4 a[128:131], v252, s[20:23], s64 offen sc1\n"
"v_fma_f32 v233, v233, v215, v214\nv_mfma_f32_16x16x32_bf16 a[232:235], v[160:163], a[40:43], a[232:235]\nR 180,184,24576\ns_add_u32 s66, s69, 23074816\nbuffer_load_dwordx4 v220, s[16:19], s63 offen lds\nv_mul_f32 v230, v40, v230\n"
"v_mfma_f32_16x16x32_bf16 a[232:235], v[164:167], a[56:59], a[232:235]\ns_add_u32 s65, s68, 23189504\nv_exp_f32 v230, v230\nv_mul_f32 v231, v41, v231\nv_mfma_f32_16x16x32_bf16 a[248:251], v[160:163], a[44:47], a[248:251]\nR 182,184,28672\n"
"v_mul_f32 v232, v42, v232\nv_mul_f32 v233, v43, v233\nv_mfma_f32_16x16x32_bf16 a[248:251], v[164:167], a[60:63], a[248:251]\nG 4\nv_exp_f32 v231, v231\nv_exp_f32 v232, v232\n"
"M 12,168,32\nR 160,185,16384\nbuffer_load_dwordx4 v221, s[16:19], s63 offen offset:1024 lds\nv_exp_f32 v233, v233\nM 12,172,48\nbuffer_load_dwordx4 a[132:135], v252, s[20:23], s65 offen sc1\n"
"s_add_u32 s63, s57, 655360\nv_fma_f32 v230, v230, v216, v216\nM 28,168,36\nR 162,185,20480\ns_add_u32 m0, s50, 73728\nv_fma_f32 v231, v231, v216, v216\n"
"M 28,172,52\nbuffer_load_dwordx4 a[136:139], v252, s[20:23], s66 offen sc1\nv_fma_f32 v232, v232, v216, v216\nv_mfma_f32_16x16x32_bf16 a[236:239], v[168:171], a[40:43], a[236:239]\nR 164,185,24576\ns_add_u32 s64, s68, 23081984\n"
"buffer_load_dwordx4 v220, s[16:19], s63 offen lds\nv_fma_f32 v233, v233, v216, v216\nv_mfma_f32_16x16x32_bf16 a[236:239], v[172:175], a[56:59], a[236:239]\ns_add_u32 s67, s69, 23189504\nv_rcp_f32 v230, v230\nv_rcp_f32 v231, v231\n"
"v_mfma_f32_16x16x32_bf16 a[252:255], v[168:171], a[44:47], a[252:255]\nR 166,185,28672\nv_mul_f32 v230, v40, v230\nv_rcp_f32 v232, v232\nv_mfma_f32_16x16x32_bf16 a[252:255], v[172:175], a[60:63], a[252:255]\nG 4\n"
"v_rcp_f32 v233, v233\nv_mul_f32 v231, v41, v231\nM 96,176,64\nR 168,186,16384\nbuffer_load_dwordx4 v221, s[16:19], s63 offen offset:1024 lds\nv_mul_f32 v232, v42, v232\n"
"M 96,180,80\nbuffer_load_dwordx4 a[140:143], v252, s[20:23], s67 offen sc1\ns_add_u32 s63, s57, 196608\nv_mul_f32 v233, v43, v233\nM 112,176,68\nR 170,186,20480\n"
"s_add_u32 m0, s50, 81920\nv_cvt_pk_bf16_f32 v40, v230, v231\nM 112,180,84\nbuffer_load_dwordx4 a[144:147], v252, s[20:23], s64 offen sc1\nv_cvt_pk_bf16_f32 v41, v232, v233\nM 128,176,72\n"
"R 172,186,24576\ns_add_u32 s66, s69, 23081984\nbuffer_load_dwordx4 v220, s[16:19], s63 offen lds\nv_mul_f32 v234, v44, v44\nM 128,180,88\ns_add_u32 s65, s68, 23196672\n"
"v_mul_f32 v235, v45, v45\nv_mul_f32 v236, v46, v46\nv_mul_f32 v237, v47, v47\nM 144,176,76\nR 174,186,28672\nv_fma_f32 v234, v234, v215, v214\n"
"v_fma_f32 v235, v235, v215, v214\nM 144,180,92\nG 4\nv_fma_f32 v236, v236, v215, v214\nv_fma_f32 v237, v237, v215, v214\nv_mul_f32 v234, v44, v234\n"
"M 100,160,64\nR 176,187,16384\nbuffer_load_dwordx4 v221, s[16:19], s63 offen offset:1024 lds\nv_mul_f32 v235, v45, v235\nM 100,164,80\nbuffer_load_dwordx4 a[148:151], v252, s[20:23], s65 offen sc1\n"
"s_add_u32 s63, s57, 720896\nv_mul_f32 v236, v46, v236\nM 116,160,68\nR 178,187,20480\ns_add_u32 m0, s50, 90112\nv_mul_f32 v237, v47, v237\n"
"M 116,164,84\nbuffer_load_dwordx4 a[152:155], v252, s[20:23], s66 offen sc1\nv_exp_f32 v234, v234\nM 132,160,72\nR 180,187,24576\nbuffer_load_dwordx4 v220, s[16:19], s63 offen lds\n"
"v_exp_f32 v235, v235\nM 132,164,88\ns_add_u32 s67, s69, 23196672\nv_exp_f32 v236, v236\nv_exp_f32 v237, v237\nM 148,160,76\n"
"R 182,187,28672\nv_fma_f32 v234, v234, v216, v216\nv_fma_f32 v235, v235, v216, v216\nM 148,164,92\nG 4\nv_rcp_f32 v234, v234\n"
"v_fma_f32 v236, v236, v216, v216\nM 104,168,64\nR 160,184,32768\ns_waitcnt vmcnt(17)\ns_barrier\nv_fma_f32 v237, v237, v216, v216\n"
"M 104,172,80\nbuffer_load_dwordx4 a[156:159], v252, s[20:23], s67 offen sc1\nbuffer_load_dwordx4 v221, s[16:19], s63 offen offset:1024 lds\nv_rcp_f32 v235, v235\nM 120,168,68\nR 162,184,36864\n"
"v_mul_f32 v234, v44, v234\nv_rcp_f32 v236, v236\nM 120,172,84\nbuffer_store_dwordx4 v[32:35], v252, s[20:23], s84 offen offset:0 sc1\nv_rcp_f32 v237, v237\nM 136,168,72\n"
"R 164,184,40960\nv_mul_f32 v235, v45, v235\nv_mul_f32 v236, v46, v236\nM 136,172,88\nv_mul_f32 v237, v47, v237\nv_cvt_pk_bf16_f32 v42, v234, v235\n"
"v_cvt_pk_bf16_f32 v43, v236, v237\nM 152,168,76\nR 166,184,45056\nv_mul_f32 v230, v48, v48\nv_mul_f32 v231, v49, v49\nM 152,172,92\n"
"G 4\nv_mul_f32 v232, v50, v50\nv_mul_f32 v233, v51, v51\nv_fma_f32 v230, v230, v218, v217\nM 108,176,64\nR 168,185,32768\n"
"v_fma_f32 v231, v231, v218, v217\nv_fma_f32 v232, v232, v218, v217\nM 108,180,80\ns_add_u32 m0, s50, 98304\nv_fma_f32 v233, v233, v218, v217\nv_mul_f32 v230, v48, v230\n"
"v_mul_f32 v231, v49, v231\nM 124,176,68\nR 170,185,36864\nv_mul_f32 v232, v50, v232\nv_mul_f32 v233, v51, v233\nM 124,180,84\n"
"s_add_u32 s63, s57, 393216\nv_exp_f32 v230, v230\nv_exp_f32 v231, v231\nM 140,176,72\nR 172,185,40960\nv_fma_f32 v230, v230, v219, v219\n"
"v_exp_f32 v232, v232\nM 140,180,88\nbuffer_load_dwordx4 v220, s[16:19], s63 offen lds\nv_exp_f32 v233, v233\nM 156,176,76\nR 174,185,45056\n"
"v_fma_f32 v231, v231, v219, v219\nv_fma_f32 v232, v232, v219, v219\nM 156,180,92\nG 4\nv_rcp_f32 v230, v230\nv_fma_f32 v233, v233, v219, v219\n"
"M 96,160,96\nR 176,186,32768\nv_mul_f32 v230, v48, v230\nv_rcp_f32 v231, v231\nM 96,164,112\nbuffer_load_dwordx4 v221, s[16:19], s63 offen offset:1024 lds\n"
"v_rcp_f32 v232, v232\nM 112,160,100\nR 178,186,36864\ns_add_u32 s63, s57, 458752\nv_rcp_f32 v233, v233\ns_waitcnt vmcnt(21)\n"
"M 112,164,116\ns_add_u32 m0, s50, 106496\nv_mul_f32 v231, v49, v231\nv_mul_f32 v232, v50, v232\nv_mul_f32 v233, v51, v233\nM 128,160,104\n"
"R 180,186,40960\nv_cvt_pk_bf16_f32 v48, v230, v231\nv_cvt_pk_bf16_f32 v49, v232, v233\ns_waitcnt vmcnt(20)\nM 128,164,120\nbuffer_load_dwordx4 v220, s[16:19], s63 offen lds\n"
"v_mul_f32 v234, v52, v52\nM 144,160,108\nR 182,186,45056\nv_mul_f32 v235, v53, v53\nv_mul_f32 v236, v54, v54\ns_waitcnt vmcnt(20)\n"
"M 144,164,124\nG 4\nv_mul_f32 v237, v55, v55\nv_fma_f32 v234, v234, v218, v217\nv_fma_f32 v235, v235, v218, v217\nM 100,168,96\n"
"R 160,187,32768\nv_fma_f32 v236, v236, v218, v217\nv_fma_f32 v237, v237, v218, v217\nM 100,172,112\nbuffer_store_dwordx4 v[40:43], v252, s[20:23], s84 offen offset:1024 sc1\nv_mul_f32 v234, v52, v234\n"
"M 116,168,100\nR 162,187,36864\nv_mul_f32 v235, v53, v235\nv_mul_f32 v236, v54, v236\nM 116,172,116\nbuffer_load_dwordx4 v221, s[16:19], s63 offen offset:1024 lds\n"
"v_mul_f32 v237, v55, v237\nM 132,168,104\nR 164,187,40960\ns_add_u32 s63, s58, 0\nv_exp_f32 v234, v234\nM 132,172,120\n"
"s_add_u32 m0, s50, 114688\nv_exp_f32 v235, v235\nv_exp_f32 v236, v236\nM 148,168,108\nR 166,187,45056\nv_fma_f32 v234, v234, v219, v219\n"
"v_exp_f32 v237, v237\nM 148,172,124\nG 4\nv_rcp_f32 v234, v234\nv_fma_f32 v235, v235, v219, v219\nM 104,176,96\n"
"R 168,184,49152\nv_fma_f32 v236, v236, v219, v219\nv_fma_f32 v237, v237, v219, v219\nM 104,180,112\nbuffer_load_dwordx4 v220, s[16:19], s63 offen lds\nv_rcp_f32 v235, v235\n"
"M 120,176,100\nR 170,184,53248\nv_mul_f32 v234, v52, v234\nv_rcp_f32 v236, v236\nM 120,180,116\nv_rcp_f32 v237, v237\n"
"v_mul_f32 v235, v53, v235\nM 136,176,104\nR 172,184,57344\nv_mul_f32 v236, v54, v236\nv_mul_f32 v237, v55, v237\nM 136,180,120\n"
"buffer_load_dwordx4 v221, s[16:19], s63 offen offset:1024 lds\nv_cvt_pk_bf16_f32 v50, v234, v235\nM 152,176,108\nR 174,184,61440\ns_add_u32 m0, s50, 0\nv_cvt_pk_bf16_f32 v51, v236, v237\n"
"M 152,180,124\nG 4\nv_mul_f32 v230, v56, v56\nv_mul_f32 v231, v57, v57\nv_mul_f32 v232, v58, v58\nM 108,160,96\n"
"R 176,185,49152\ns_add_u32 s63, s58, 524288\nv_mul_f32 v233, v59, v59\nM 108,164,112\nbuffer_load_dwordx4 v220, s[16:19], s63 offen lds\nv_fma_f32 v230, v230, v218, v217\n"
"M 124,160,100\nR 178,185,53248\nv_fma_f32 v231, v231, v218, v217\nM 124,164,116\nbuffer_store_dwordx4 v[48:51], v252, s[20:23], s85 offen offset:0 sc1\nM 140,160,104\n"
"R 180,185,57344\nv_fma_f32 v232, v232, v218, v217\nM 140,164,120\nbuffer_load_dwordx4 v221, s[16:19], s63 offen offset:1024 lds\nM 156,160,108\nR 182,185,61440\n"
"s_add_u32 s63, s58, 65536\nM 156,164,124\ns_waitcnt vmcnt(11)\ns_barrier\nG 4\nM 96,168,128\n"
"R 160,186,49152\nv_fma_f32 v233, v233, v218, v217\nM 96,172,144\ns_add_u32 m0, s50, 8192\nv_mul_f32 v230, v56, v230\nv_mul_f32 v231, v57, v231\n"
"M 112,168,132\nR 162,186,53248\nv_mul_f32 v232, v58, v232\nM 112,172,148\nbuffer_load_dwordx4 v220, s[16:19], s63 offen lds\nM 128,168,136\n"
"R 164,186,57344\nv_mul_f32 v233, v59, v233\nM 128,172,152\nv_exp_f32 v230, v230\nM 144,168,140\nR 166,186,61440\n"
"v_fma_f32 v230, v230, v219, v219\nM 144,172,156\nG 4\nv_exp_f32 v231, v231\nM 100,176,128\nR 168,187,49152\n"
"v_fma_f32 v231, v231, v219, v219\nM 100,180,144\nbuffer_load_dwordx4 v221, s[16:19], s63 offen offset:1024 lds\nM 116,176,132\nR 170,187,53248\nv_mul_f32 v234, v60, v60\n"
"M 116,180,148\ns_add_u32 m0, s50, 16384\nv_exp_f32 v232, v232\nM 132,176,136\nR 172,187,57344\nv_fma_f32 v232, v232, v219, v219\n"
"M 132,180,152\ns_add_u32 s63, s58, 589824\nv_exp_f32 v233, v233\nM 148,176,140\nR 174,187,61440\nv_fma_f32 v233, v233, v219, v219\n"
"M 148,180,156\nG 4\nv_rcp_f32 v230, v230\nM 104,160,128\nR 176,188,0\nv_mul_f32 v230, v56, v230\n"
"M 104,164,144\nbuffer_load_dwordx4 v220, s[16:19], s63 offen lds\nM 120,160,132\nR 178,188,4096\nv_mul_f32 v235, v61, v61\nM 120,164,148\n"
"v_rcp_f32 v231, v231\nM 136,160,136\nR 180,188,8192\nv_mul_f32 v231, v57, v231\nM 136,164,152\nv_rcp_f32 v232, v232\n"
"M 152,160,140\nR 182,188,12288\nv_mul_f32 v232, v58, v232\nM 152,164,156\nG 4\nv_rcp_f32 v233, v233\n"
"M 108,168,128\nR 160,189,0\nv_mul_f32 v233, v59, v233\nM 108,172,144\nbuffer_load_dwordx4 v221, s[16:19], s63 offen offset:1024 lds\nM 124,168,132\n"
"R 162,189,4096\nv_cvt_pk_bf16_f32 v56, v230, v231\nM 124,172,148\ns_add_u32 m0, s50, 24576\nv_cvt_pk_bf16_f32 v57, v232, v233\nv_mul_f32 v236, v62, v62\n"
"M 140,168,136\nR 164,189,8192\nv_mul_f32 v237, v63, v63\nM 140,172,152\ns_add_u32 s63, s58, 131072\nv_fma_f32 v234, v234, v218, v217\n"
"v_fma_f32 v235, v235, v218, v217\nM 156,168,140\nR 166,189,12288\nv_fma_f32 v236, v236, v218, v217\nM 156,172,156\nG 4\n"
"v_fma_f32 v237, v237, v218, v217\nv_mul_f32 v234, v60, v234\nM 0,176,64\nR 168,190,0\nv_mul_f32 v235, v61, v235\nM 0,180,80\n"
"buffer_load_dwordx4 v220, s[16:19], s63 offen lds\nM 16,176,68\nR 170,190,4096\nv_mul_f32 v236, v62, v236\nM 16,180,84\nv_exp_f32 v234, v234\n"
"v_mfma_f32_16x16x32_bf16 a[224:227], v[176:179], a[72:75], a[224:227]\nR 172,190,8192\nv_mul_f32 v237, v63, v237\nv_mfma_f32_16x16x32_bf16 a[224:227], v[180:183], a[88:91], a[224:227]\nv_exp_f32 v235, v235\nv_mfma_f32_16x16x32_bf16 a[240:243], v[176:179], a[76:79], a[240:243]\n"
"R 174,190,12288\nv_fma_f32 v234, v234, v219, v219\nv_mfma_f32_16x16x32_bf16 a[240:243], v[180:183], a[92:95], a[240:243]\nG 4\nv_exp_f32 v236, v236\nM 4,160,64\n"
"R 176,191,0\nv_fma_f32 v235, v235, v219, v219\nM 4,164,80\nbuffer_load_dwordx4 v221, s[16:19], s63 offen offset:1024 lds\nM 20,160,68\nR 178,191,4096\n"
"v_fma_f32 v236, v236, v219, v219\nM 20,164,84\ns_add_u32 m0, s50, 32768\nv_exp_f32 v237, v237\nv_mfma_f32_16x16x32_bf16 a[228:231], v[160:163], a[72:75], a[228:231]\nR 180,191,8192\n"
"v_fma_f32 v237, v237, v219, v219\nv_mfma_f32_16x16x32_bf16 a[228:231], v[164:167], a[88:91], a[228:231]\ns_add_u32 s63, s58, 655360\nv_rcp_f32 v234, v234\nv_mfma_f32_16x16x32_bf16 a[244:247], v[160:163], a[76:79], a[244:247]\nR 182,191,12288\n"
"v_mul_f32 v234, v60, v234\nv_mfma_f32_16x16x32_bf16 a[244:247], v[164:167], a[92:95], a[244:247]\nG 4\nv_rcp_f32 v235, v235\nM 8,168,64\nR 160,188,16384\n"
"v_mul_f32 v235, v61, v235\nM 8,172,80\nbuffer_load_dwordx4 v220, s[16:19], s63 offen lds\nM 24,168,68\nR 162,188,20480\nv_cvt_pk_bf16_f32 v58, v234, v235\n"
"M 24,172,84\nv_rcp_f32 v236, v236\nv_mfma_f32_16x16x32_bf16 a[232:235], v[168:171], a[72:75], a[232:235]\nR 164,188,24576\nv_mul_f32 v236, v62, v236\nv_mfma_f32_16x16x32_bf16 a[232:235], v[172:175], a[88:91], a[232:235]\n"
"buffer_load_dwordx4 v221, s[16:19], s63 offen offset:1024 lds\nv_mfma_f32_16x16x32_bf16 a[248:251], v[168:171], a[76:79], a[248:251]\nR 166,188,28672\ns_add_u32 m0, s50, 40960\nv_mfma_f32_16x16x32_bf16 a[248:251], v[172:175], a[92:95], a[248:251]\nG 4\n"
"v_rcp_f32 v237, v237\nM 12,176,64\nR 168,189,16384\ns_add_u32 s63, s58, 196608\nM 12,180,80\nbuffer_load_dwordx4 v220, s[16:19], s63 offen lds\n"
"M 28,176,68\nR 170,189,20480\nv_mul_f32 v237, v63, v237\nM 28,180,84\nv_cvt_pk_bf16_f32 v59, v236, v237\nbuffer_store_dwordx4 v[56:59], v252, s[20:23], s85 offen offset:1024 sc1\n"
"v_mfma_f32_16x16x32_bf16 a[236:239], v[176:179], a[72:75], a[236:239]\nR 172,189,24576\nv_mfma_f32_16x16x32_bf16 a[236:239], v[180:183], a[88:91], a[236:239]\nbuffer_load_dwordx4 v221, s[16:19], s63 offen offset:1024 lds\nv_mfma_f32_16x16x32_bf16 a[252:255], v[176:179], a[76:79], a[252:255]\nR 174,189,28672\n"
"v_mfma_f32_16x16x32_bf16 a[252:255], v[180:183], a[92:95], a[252:255]\ns_waitcnt vmcnt(11)\ns_barrier\nG 4\nM 0,160,96\nR 176,190,16384\n"
"M 0,164,112\nM 16,160,100\nR 178,190,20480\nM 16,164,116\nv_mfma_f32_16x16x32_bf16 a[224:227], v[160:163], a[104:107], a[224:227]\nR 180,190,24576\n"
"v_mfma_f32_16x16x32_bf16 a[224:227], v[164:167], a[120:123], a[224:227]\nv_mfma_f32_16x16x32_bf16 a[240:243], v[160:163], a[108:111], a[240:243]\nR 182,190,28672\nv_mfma_f32_16x16x32_bf16 a[240:243], v[164:167], a[124:127], a[240:243]\nG 4\nM 4,168,96\n"
"R 160,191,16384\nM 4,172,112\nM 20,168,100\nR 162,191,20480\nM 20,172,116\nv_mfma_f32_16x16x32_bf16 a[228:231], v[168:171], a[104:107], a[228:231]\n"
"R 164,191,24576\nv_mfma_f32_16x16x32_bf16 a[228:231], v[172:175], a[120:123], a[228:231]\nv_mfma_f32_16x16x32_bf16 a[244:247], v[168:171], a[108:111], a[244:247]\nR 166,191,28672\nv_mfma_f32_16x16x32_bf16 a[244:247], v[172:175], a[124:127], a[244:247]\nG 4\n"
"M 8,176,96\nR 168,188,32768\nM 8,180,112\nM 24,176,100\nR 170,188,36864\nM 24,180,116\n"
"v_mfma_f32_16x16x32_bf16 a[232:235], v[176:179], a[104:107], a[232:235]\nR 172,188,40960\nv_mfma_f32_16x16x32_bf16 a[232:235], v[180:183], a[120:123], a[232:235]\nv_mfma_f32_16x16x32_bf16 a[248:251], v[176:179], a[108:111], a[248:251]\nR 174,188,45056\nv_mfma_f32_16x16x32_bf16 a[248:251], v[180:183], a[124:127], a[248:251]\n"
"G 4\nM 12,160,96\nR 176,189,32768\nM 12,164,112\nM 28,160,100\nR 178,189,36864\n"
"M 28,164,116\nv_mfma_f32_16x16x32_bf16 a[236:239], v[160:163], a[104:107], a[236:239]\nR 180,189,40960\nv_mfma_f32_16x16x32_bf16 a[236:239], v[164:167], a[120:123], a[236:239]\nv_mfma_f32_16x16x32_bf16 a[252:255], v[160:163], a[108:111], a[252:255]\nR 182,189,45056\n"
"v_mfma_f32_16x16x32_bf16 a[252:255], v[164:167], a[124:127], a[252:255]\nG 4\nM 0,168,128\nR 160,190,32768\nM 0,172,144\nM 16,168,132\n"
"R 162,190,36864\nM 16,172,148\nv_mfma_f32_16x16x32_bf16 a[224:227], v[168:171], a[136:139], a[224:227]\nR 164,190,40960\nv_mfma_f32_16x16x32_bf16 a[224:227], v[172:175], a[152:155], a[224:227]\nv_mfma_f32_16x16x32_bf16 a[240:243], v[168:171], a[140:143], a[240:243]\n"
"R 166,190,45056\nv_mfma_f32_16x16x32_bf16 a[240:243], v[172:175], a[156:159], a[240:243]\nG 4\nM 4,176,128\nR 168,191,32768\nM 4,180,144\n"
"M 20,176,132\nR 170,191,36864\nv_mov_b32 v226, s46\nM 20,180,148\ns_add_u32 m0, s50, 49152\nv_mov_b32 v227, s47\n"
"v_mfma_f32_16x16x32_bf16 a[228:231], v[176:179], a[136:139], a[228:231]\nR 172,191,40960\ns_add_u32 s61, s81, 385040\ns_lshl_b32 s62, s44, 5\ns_add_u32 s61, s61, s62\ns_waitcnt vmcnt(0)\n"
"s_mov_b64 exec, 1\nbuffer_atomic_swap_x2 v[226:227], off, s[20:23], s61 sc1\ns_mov_b64 exec, -1\nv_mfma_f32_16x16x32_bf16 a[228:231], v[180:183], a[152:155], a[228:231]\ns_add_u32 s63, s58, 720896\nv_mfma_f32_16x16x32_bf16 a[244:247], v[176:179], a[140:143], a[244:247]\n"
"R 174,191,45056\nv_mfma_f32_16x16x32_bf16 a[244:247], v[180:183], a[156:159], a[244:247]\nG 4\nM 8,160,128\nR 176,188,49152\nM 8,164,144\n"
"buffer_load_dwordx4 v220, s[16:19], s63 offen lds\nM 24,160,132\nR 178,188,53248\nM 24,164,148\nv_mfma_f32_16x16x32_bf16 a[232:235], v[160:163], a[136:139], a[232:235]\nR 180,184,0\n"
"v_mfma_f32_16x16x32_bf16 a[232:235], v[164:167], a[152:155], a[232:235]\nbuffer_load_dwordx4 v221, s[16:19], s63 offen offset:1024 lds\nv_mfma_f32_16x16x32_bf16 a[248:251], v[160:163], a[140:143], a[248:251]\nR 182,184,4096\ns_add_u32 m0, s50, 57344\nv_mfma_f32_16x16x32_bf16 a[248:251], v[164:167], a[156:159], a[248:251]\n"
"G 4\nM 12,168,128\nR 160,189,49152\ns_add_u32 s63, s58, 393216\nM 12,172,144\nbuffer_load_dwordx4 v220, s[16:19], s63 offen lds\n"
"M 28,168,132\nR 162,189,53248\nM 28,172,148\nv_mov_b32 v238, -1\nv_mov_b32 v239, -1\nv_mov_b32 v240, -1\n"
"v_mov_b32 v241, -1\nv_mfma_f32_16x16x32_bf16 a[236:239], v[168:171], a[136:139], a[236:239]\nR 164,185,0\nv_mfma_f32_16x16x32_bf16 a[236:239], v[172:175], a[152:155], a[236:239]\nbuffer_load_dwordx4 v221, s[16:19], s63 offen offset:1024 lds\nv_mfma_f32_16x16x32_bf16 a[252:255], v[168:171], a[140:143], a[252:255]\n"
"R 166,185,4096\ns_add_u32 s63, s58, 458752\nv_mfma_f32_16x16x32_bf16 a[252:255], v[172:175], a[156:159], a[252:255]\ns_barrier\nG 4\nv_mfma_f32_16x16x32_bf16 v[32:35], v[176:179], a[0:3], 0\n"
"R 168,190,49152\nM 32,180,16\ns_add_u32 m0, s50, 65536\nv_mfma_f32_16x16x32_bf16 v[48:51], v[176:179], a[4:7], 0\nR 170,190,53248\nM 48,180,20\n"
"buffer_load_dwordx4 v220, s[16:19], s63 offen lds\nv_mfma_f32_16x16x32_bf16 v[64:67], v[176:179], a[8:11], 0\nR 172,186,0\nM 64,180,24\nv_mfma_f32_16x16x32_bf16 v[80:83], v[176:179], a[12:15], 0\nR 174,186,4096\n"
"M 80,180,28\nG 4\nv_mfma_f32_16x16x32_bf16 v[36:39], v[160:163], a[0:3], 0\nR 176,191,49152\nM 36,164,16\nbuffer_load_dwordx4 v221, s[16:19], s63 offen offset:1024 lds\n"
"v_mfma_f32_16x16x32_bf16 v[52:55], v[160:163], a[4:7], 0\nR 178,191,53248\nM 52,164,20\nv_mfma_f32_16x16x32_bf16 v[68:71], v[160:163], a[8:11], 0\nR 180,187,0\nM 68,164,24\n"
"s_add_u32 m0, s50, 73728\nv_mfma_f32_16x16x32_bf16 v[84:87], v[160:163], a[12:15], 0\nR 182,187,4096\nv_mov_b32 v248, 0\nM 84,164,28\nG 4\n"
"v_mov_b32 v249, 0\nv_mfma_f32_16x16x32_bf16 v[40:43], v[168:171], a[0:3], 0\nR 160,184,8192\ns_add_u32 s61, s82, 385040\ns_mov_b64 exec, 0xf\nbuffer_atomic_or_x2 v[248:249], v250, s[20:23], s61 offen sc0 sc1\n"
"s_mov_b64 exec, -1\nM 40,172,16\ns_add_u32 s63, s56, 262144\nv_mfma_f32_16x16x32_bf16 v[56:59], v[168:171], a[4:7], 0\nR 162,184,12288\nM 56,172,20\n"
"buffer_load_dwordx4 v220, s[16:19], s63 offen lds\nv_mfma_f32_16x16x32_bf16 v[72:75], v[168:171], a[8:11], 0\nR 164,184,16384\nM 72,172,24\nv_mfma_f32_16x16x32_bf16 v[88:91], v[168:171], a[12:15], 0\nR 166,184,20480\n"
"M 88,172,28\nG 4\nv_mfma_f32_16x16x32_bf16 v[44:47], v[176:179], a[0:3], 0\nR 168,185,8192\nM 44,180,16\nbuffer_load_dwordx4 v221, s[16:19], s63 offen offset:1024 lds\n"
"v_mfma_f32_16x16x32_bf16 v[60:63], v[176:179], a[4:7], 0\nR 170,185,12288\ns_add_u32 s63, s56, 786432\nM 60,180,20\ns_add_u32 m0, s50, 81920\nv_mfma_f32_16x16x32_bf16 v[76:79], v[176:179], a[8:11], 0\n"
"R 172,185,16384\nM 76,180,24\nbuffer_load_dwordx4 v220, s[16:19], s63 offen lds\nv_mfma_f32_16x16x32_bf16 v[92:95], v[176:179], a[12:15], 0\nR 174,185,20480\nM 92,180,28\n"
"G 4\nM 32,160,32\nR 176,186,8192\nM 32,164,48\nbuffer_load_dwordx4 v221, s[16:19], s63 offen offset:1024 lds\nM 48,160,36\n"
"R 178,186,12288\ns_add_u32 s63, s56, 327680\nM 48,164,52\ns_add_u32 m0, s50, 90112\nM 64,160,40\nR 180,186,16384\n"
"M 64,164,56\nbuffer_load_dwordx4 v220, s[16:19], s63 offen lds\nM 80,160,44\nR 182,186,20480\nM 80,164,60\nG 4\n"
"M 36,168,32\nR 160,187,8192\nM 36,172,48\nbuffer_load_dwordx4 v221, s[16:19], s63 offen offset:1024 lds\nM 52,168,36\nR 162,187,12288\n"
"M 52,172,52\nM 68,168,40\nR 164,187,16384\nM 68,172,56\ns_add_u32 m0, s50, 98304\nM 84,168,44\n"
"R 166,187,20480\ns_add_u32 s63, s56, 851968\nM 84,172,60\nG 4\nM 40,176,32\nR 168,184,24576\n"
"M 40,180,48\ns_add_u32 s64, s68, 23072768\nM 56,176,36\nR 170,184,28672\ns_waitcnt vmcnt(6)\nv_cmp_eq_u64 s[86:87], s[46:47], v[248:249]\n"
"M 56,180,52\nbuffer_load_dwordx4 v220, s[16:19], s63 offen lds\nM 72,176,40\nR 172,184,32768\ns_and_b32 s62, s86, 0xf\ns_cmp_eq_u32 s62, 0xf\n"
"s_cbranch_scc0 Lq487\nLq488:\nM 72,180,56\ns_add_u32 s65, s68, 23187456\nM 88,176,44\nR 174,184,36864\n"
"s_barrier\nM 88,180,60\nG 4\nM 44,160,32\nR 176,185,24576\nM 44,164,48\n"
"buffer_load_dwordx4 a[160:163], v252, s[20:23], s64 offen sc1\nM 60,160,36\nR 178,185,28672\nM 60,164,52\ns_add_u32 s66, s69, 23072768\nM 76,160,40\n"
"R 180,185,32768\nM 76,164,56\nbuffer_load_dwordx4 a[164:167], v252, s[20:23], s65 offen sc1\nM 92,160,44\nR 182,185,36864\nM 92,164,60\n"
"G 4\nM 32,168,64\nR 160,186,24576\nM 32,172,80\ns_add_u32 s67, s69, 23187456\nM 48,168,68\n"
"R 162,186,28672\nM 48,172,84\nbuffer_load_dwordx4 a[168:171], v252, s[20:23], s66 offen sc1\nM 64,168,72\nR 164,186,32768\nM 64,172,88\n"
"M 80,168,76\nR 166,186,36864\nM 80,172,92\nG 4\nM 36,176,64\nR 168,187,24576\n"
"M 36,180,80\nbuffer_load_dwordx4 a[172:175], v252, s[20:23], s67 offen sc1\nM 52,176,68\nR 170,187,28672\nM 52,180,84\ns_add_u32 s64, s68, 23079936\n"
"M 68,176,72\nR 172,187,32768\nM 68,180,88\ns_add_u32 s65, s68, 23194624\nM 84,176,76\nR 174,187,36864\n"
"M 84,180,92\nG 4\nM 40,160,64\nR 176,184,40960\nbuffer_load_dwordx4 v221, s[16:19], s63 offen offset:1024 lds\nM 40,164,80\n"
"buffer_load_dwordx4 a[176:179], v252, s[20:23], s64 offen sc1\nM 56,160,68\nR 178,184,45056\ns_add_u32 m0, s50, 106496\nM 56,164,84\ns_add_u32 s66, s69, 23079936\n"
"s_add_u32 s63, s57, 262144\nM 72,160,72\nR 180,184,49152\nbuffer_load_dwordx4 v220, s[16:19], s63 offen lds\nLq603:\ns_lshr_b32 s60, s45, 3\n"
"s_lshl2_add_u32 s60, s60, s40\ns_lshl_b32 s60, s60, 8\nv_lshl_add_u32 v224, v252, 3, s60\nv_add_u32 v224, Lq603+4096-LqPC, v224\ns_mov_b64 exec, 3\nglobal_load_dword v224, v224, s[98:99]\n"
"s_mov_b64 exec, -1\nM 72,164,88\nbuffer_load_dwordx4 a[180:183], v252, s[20:23], s65 offen sc1\nM 88,160,76\nR 182,184,53248\nM 88,164,92\n"
"G 4\nM 44,168,64\nR 160,185,40960\nbuffer_load_dwordx4 v221, s[16:19], s63 offen offset:1024 lds\nM 44,172,80\ns_add_u32 s67, s69, 23194624\n"
"s_add_u32 s63, s57, 786432\nM 60,168,68\nR 162,185,45056\ns_add_u32 m0, s50, 114688\nM 60,172,84\nbuffer_load_dwordx4 a[184:187], v252, s[20:23], s66 offen sc1\n"
"M 76,168,72\nR 164,185,49152\nbuffer_load_dwordx4 v220, s[16:19], s63 offen lds\nM 76,172,88\nbuffer_load_dwordx4 v221, s[16:19], s63 offen offset:1024 lds\nM 92,168,76\n"
"R 166,185,53248\ns_add_u32 m0, s50, 0\nM 92,172,92\nG 4\nM 32,176,96\nR 168,186,40960\n"
"s_add_u32 s63, s57, 327680\nM 32,180,112\nbuffer_load_dwordx4 a[188:191], v252, s[20:23], s67 offen sc1\nM 48,176,100\nR 170,186,45056\nM 48,180,116\n"
"s_add_u32 s64, s68, 23073792\nM 64,176,104\nR 172,186,49152\nbuffer_load_dwordx4 v220, s[16:19], s63 offen lds\nM 64,180,120\nbuffer_load_dwordx4 v221, s[16:19], s63 offen offset:1024 lds\n"
"M 80,176,108\nR 174,186,53248\ns_add_u32 m0, s50, 8192\nM 80,180,124\nG 4\nM 36,160,96\n"
"R 176,187,40960\ns_waitcnt vmcnt(19)\ns_barrier\nM 36,164,112\nbuffer_load_dwordx4 a[192:195], v252, s[20:23], s64 offen sc1\nM 52,160,100\n"
"R 178,187,45056\ns_add_u32 s63, s57, 851968\nM 52,164,116\ns_add_u32 s65, s68, 23188480\nM 68,160,104\nR 180,187,49152\n"
"buffer_load_dwordx4 v220, s[16:19], s63 offen lds\nM 68,164,120\ns_add_u32 s66, s69, 23073792\nM 84,160,108\nR 182,187,53248\nM 84,164,124\n"
"G 4\nM 40,168,96\nR 160,184,57344\nbuffer_load_dwordx4 v221, s[16:19], s63 offen offset:1024 lds\nM 40,172,112\nbuffer_load_dwordx4 a[196:199], v252, s[20:23], s65 offen sc1\n"
"M 56,168,100\nR 162,184,61440\nM 56,172,116\ns_add_u32 s67, s69, 23188480\nM 72,168,104\nR 164,188,0\n"
"M 72,172,120\nbuffer_load_dwordx4 a[200:203], v252, s[20:23], s66 offen sc1\nM 88,168,108\nR 166,188,4096\nM 88,172,124\nG 4\n"
"M 44,176,96\nR 168,185,57344\nM 44,180,112\ns_add_u32 s64, s68, 23080960\nM 60,176,100\nR 170,185,61440\n"
"M 60,180,116\nbuffer_load_dwordx4 a[204:207], v252, s[20:23], s67 offen sc1\nM 76,176,104\nR 172,189,0\nM 76,180,120\nM 92,176,108\n"
"R 174,189,4096\nM 92,180,124\nG 4\nM 32,160,128\nR 176,186,57344\nM 32,164,144\n"
"buffer_load_dwordx4 a[208:211], v252, s[20:23], s64 offen sc1\nM 48,160,132\nR 178,186,61440\nM 48,164,148\ns_add_u32 s65, s68, 23195648\nM 64,160,136\n"
"R 180,190,0\nM 64,164,152\ns_add_u32 s66, s69, 23080960\nM 80,160,140\nR 182,190,4096\nM 80,164,156\n"
"G 4\nM 36,168,128\nR 160,187,57344\nM 36,172,144\nbuffer_load_dwordx4 a[212:215], v252, s[20:23], s65 offen sc1\nM 52,168,132\n"
"R 162,187,61440\nM 52,172,148\ns_add_u32 s67, s69, 23195648\nM 68,168,136\nR 164,191,0\nM 68,172,152\n"
"buffer_load_dwordx4 a[216:219], v252, s[20:23], s66 offen sc1\nM 84,168,140\nR 166,191,4096\nM 84,172,156\nG 4\nM 40,176,128\n"
"R 168,188,8192\nM 40,180,144\nM 56,176,132\nR 170,188,12288\nM 56,180,148\nbuffer_load_dwordx4 a[220:223], v252, s[20:23], s67 offen sc1\n"
"M 72,176,136\nR 172,188,16384\ns_add_u32 s63, s58, 262144\nM 72,180,152\ns_add_u32 m0, s50, 16384\nM 88,176,140\n"
"R 174,188,20480\nM 88,180,156\nG 4\nM 44,160,128\nR 176,189,8192\nM 44,164,144\n"
"buffer_load_dwordx4 v220, s[16:19], s63 offen lds\nM 60,160,132\nR 178,189,12288\nM 60,164,148\nM 76,160,136\nR 180,189,16384\n"
"M 76,164,152\nbuffer_load_dwordx4 v221, s[16:19], s63 offen offset:1024 lds\nM 92,160,140\nR 182,189,20480\nM 92,164,156\ns_waitcnt vmcnt(9)\n"
"s_barrier\nG 4\nM 96,168,160\nR 160,190,8192\nM 96,172,176\ns_add_u32 m0, s50, 24576\n"
"M 112,168,164\nR 162,190,12288\nM 112,172,180\ns_add_u32 s63, s58, 786432\nM 128,168,168\nR 164,190,16384\n"
"v_lshrrev_b32 v230, 8, v252\nM 128,172,184\nbuffer_load_dwordx4 v220, s[16:19], s63 offen lds\nM 144,168,172\nR 166,190,20480\nv_lshlrev_b32 v230, 3, v230\n"
"M 144,172,188\nG 4\nM 100,176,160\nR 168,191,8192\nM 100,180,176\nbuffer_load_dwordx2 v[232:233], v230, s[36:39], s53 offen offset:0\n"
"M 116,176,164\nR 170,191,12288\nM 116,180,180\nbuffer_load_dwordx2 v[234:235], v230, s[36:39], s53 offen offset:32\nM 132,176,168\nR 172,191,16384\n"
"M 132,180,184\nbuffer_load_dwordx4 v221, s[16:19], s63 offen offset:1024 lds\nM 148,176,172\nR 174,191,20480\nM 148,180,188\nG 4\n"
"M 104,160,160\nR 176,188,24576\nM 104,164,176\ns_add_u32 m0, s50, 32768\nM 120,160,164\nR 178,188,28672\n"
"M 120,164,180\ns_add_u32 s63, s58, 327680\nM 136,160,168\nR 180,188,32768\nbuffer_load_dwordx2 v[236:237], v230, s[36:39], s53 offen offset:64\nM 136,164,184\n"
"buffer_load_dwordx4 v220, s[16:19], s63 offen lds\nM 152,160,172\nR 182,188,36864\nM 152,164,188\nbuffer_load_dwordx2 v[238:239], v230, s[36:39], s53 offen offset:96\nG 4\n"
"M 108,168,160\nR 160,189,24576\nv_bfe_u32 v242, v252, 4, 4\nM 108,172,176\nv_lshrrev_b32 v243, 8, v252\nv_lshlrev_b32 v243, 2, v243\n"
"M 124,168,164\nR 162,189,28672\nv_sub_u32 v242, v242, v243\nM 124,172,180\nv_lshlrev_b32 v243, 4, v242\ns_mov_b64 s[60:61], 0x3f80\n"
"v_lshlrev_b64 v[208:209], v243, s[60:61]\nM 140,168,168\nR 164,189,32768\nM 140,172,184\nbuffer_load_dwordx4 v221, s[16:19], s63 offen offset:1024 lds\nM 156,168,172\n"
"R 166,189,36864\nM 156,172,188\nG 4\nM 96,176,192\nR 168,190,24576\ns_waitcnt vmcnt(13)\n"
"M 96,180,208\ns_add_u32 m0, s50, 40960\nM 112,176,196\nR 170,190,28672\ns_waitcnt vmcnt(12)\nM 112,180,212\n"
"s_add_u32 s63, s58, 851968\nM 128,176,200\nR 172,190,32768\ns_waitcnt vmcnt(11)\nM 128,180,216\nbuffer_load_dwordx4 v220, s[16:19], s63 offen lds\n"
"M 144,176,204\nR 174,190,36864\ns_waitcnt vmcnt(11)\nM 144,180,220\nG 4\nM 100,160,192\n"
"R 176,191,24576\nM 100,164,208\nv_cmp_gt_u32 s[60:61], 4, v242\ns_nop 1\nv_cndmask_b32 v212, 0, v208, s[60:61]\nv_cndmask_b32 v208, 0, v208, s[60:61]\n"
"v_cndmask_b32 v213, 0, v209, s[60:61]\nv_cndmask_b32 v209, 0, v209, s[60:61]\nM 116,160,196\nR 178,191,28672\nM 116,164,212\nM 132,160,200\n"
"R 180,191,32768\nM 132,164,216\nbuffer_load_dwordx4 v221, s[16:19], s63 offen offset:1024 lds\nM 148,160,204\nR 182,191,36864\nM 148,164,220\n"
"G 4\nM 104,168,192\nR 160,188,40960\nM 104,172,208\ns_add_u32 m0, s50, 49152\nM 120,168,196\n"
"R 162,188,45056\nM 120,172,212\ns_add_u32 s63, s59, 0\nM 136,168,200\nR 164,188,49152\nM 136,172,216\n"
"buffer_load_dwordx4 v220, s[16:19], s63 offen lds\nM 152,168,204\nR 166,188,53248\nM 152,172,220\nG 4\nM 108,176,192\n"
"R 168,189,40960\nM 108,180,208\nM 124,176,196\nR 170,189,45056\nM 124,180,212\nbuffer_load_dwordx4 v221, s[16:19], s63 offen offset:1024 lds\n"
"M 140,176,200\nR 172,189,49152\nM 140,180,216\nM 156,176,204\nR 174,189,53248\nM 156,180,220\n"
"G 4\nM 0,160,160\nR 176,190,40960\nM 0,164,176\ns_mov_b32 m0, s51\nM 16,160,164\n"
"R 178,190,45056\nds_write_addtid_b32 v128 offset:0\nM 16,164,180\nds_write_addtid_b32 v129 offset:256\nds_write_addtid_b32 v130 offset:512\nv_mfma_f32_16x16x32_bf16 a[224:227], v[160:163], a[168:171], a[224:227]\n"
"R 180,190,49152\nds_write_addtid_b32 v131 offset:768\nv_mfma_f32_16x16x32_bf16 a[224:227], v[164:167], a[184:187], a[224:227]\nds_write_addtid_b32 v132 offset:1024\nds_write_addtid_b32 v133 offset:1280\nv_mfma_f32_16x16x32_bf16 a[240:243], v[160:163], a[172:175], a[240:243]\n"
"R 182,190,53248\nds_write_addtid_b32 v134 offset:1536\nv_mfma_f32_16x16x32_bf16 a[240:243], v[164:167], a[188:191], a[240:243]\nds_write_addtid_b32 v135 offset:1792\nds_write_addtid_b32 v136 offset:2048\nG 13\n"
"M 4,168,160\nR 160,191,40960\nds_write_addtid_b32 v137 offset:2304\nM 4,172,176\nds_write_addtid_b32 v138 offset:2560\nds_write_addtid_b32 v139 offset:2816\n"
"M 20,168,164\nR 162,191,45056\nds_write_addtid_b32 v140 offset:3072\nM 20,172,180\nds_write_addtid_b32 v141 offset:3328\nds_write_addtid_b32 v142 offset:3584\n"
"v_mfma_f32_16x16x32_bf16 a[228:231], v[168:171], a[168:171], a[228:231]\nR 164,191,49152\nds_write_addtid_b32 v143 offset:3840\nv_mfma_f32_16x16x32_bf16 a[228:231], v[172:175], a[184:187], a[228:231]\nds_write_addtid_b32 v144 offset:4096\nds_write_addtid_b32 v145 offset:4352\n"
"v_mfma_f32_16x16x32_bf16 a[244:247], v[168:171], a[172:175], a[244:247]\nR 166,191,53248\nds_write_addtid_b32 v146 offset:4608\nv_mfma_f32_16x16x32_bf16 a[244:247], v[172:175], a[188:191], a[244:247]\nds_write_addtid_b32 v147 offset:4864\nds_write_addtid_b32 v148 offset:5120\n"
"G 15\nM 8,176,160\nR 168,184,0\nds_write_addtid_b32 v149 offset:5376\nM 8,180,176\nds_write_addtid_b32 v150 offset:5632\n"
"ds_write_addtid_b32 v151 offset:5888\nM 24,176,164\nR 170,184,4096\nds_write_addtid_b32 v152 offset:6144\nM 24,180,180\nds_write_addtid_b32 v153 offset:6400\n"
"ds_write_addtid_b32 v154 offset:6656\nv_mfma_f32_16x16x32_bf16 a[232:235], v[176:179], a[168:171], a[232:235]\nR 172,184,8192\nds_write_addtid_b32 v155 offset:6912\nv_mfma_f32_16x16x32_bf16 a[232:235], v[180:183], a[184:187], a[232:235]\nds_write_addtid_b32 v156 offset:7168\n"
"ds_write_addtid_b32 v157 offset:7424\nds_write_addtid_b32 v158 offset:7680\nds_write_addtid_b32 v159 offset:7936\nv_mfma_f32_16x16x32_bf16 a[248:251], v[176:179], a[172:175], a[248:251]\nR 174,184,12288\ns_add_u32 m0, s50, 57344\n"
"v_mfma_f32_16x16x32_bf16 a[248:251], v[180:183], a[188:191], a[248:251]\ns_add_u32 s63, s59, 524288\nG 15\nM 12,160,160\nR 176,185,0\ns_waitcnt vmcnt(8) lgkmcnt(2)\n"
"s_barrier\nM 12,164,176\nbuffer_load_dwordx4 v220, s[16:19], s63 offen lds\nds_read2st64_b32 v[128:129], v225 offset0:0 offset1:1\nM 28,160,164\nR 178,185,4096\n"
"ds_read2st64_b32 v[130:131], v225 offset0:2 offset1:3\nM 28,164,180\nds_read2st64_b32 v[132:133], v225 offset0:4 offset1:5\nds_read2st64_b32 v[134:135], v225 offset0:6 offset1:7\nv_mfma_f32_16x16x32_bf16 a[236:239], v[160:163], a[168:171], a[236:239]\nR 180,185,8192\n"
"ds_read2st64_b32 v[136:137], v225 offset0:8 offset1:9\nv_mfma_f32_16x16x32_bf16 a[236:239], v[164:167], a[184:187], a[236:239]\nbuffer_load_dwordx4 v221, s[16:19], s63 offen offset:1024 lds\nds_read2st64_b32 v[138:139], v225 offset0:10 offset1:11\nv_mfma_f32_16x16x32_bf16 a[252:255], v[160:163], a[172:175], a[252:255]\nR 182,185,12288\n"
"s_add_u32 m0, s50, 65536\nds_read2st64_b32 v[140:141], v225 offset0:12 offset1:13\nv_mfma_f32_16x16x32_bf16 a[252:255], v[164:167], a[188:191], a[252:255]\nds_read2st64_b32 v[142:143], v225 offset0:14 offset1:15\nG 12\nG 10\n"
"v_add_f32 v96, v96, v128\nM 0,168,192\nR 160,186,0\ns_add_u32 s63, s59, 65536\nv_add_f32 v97, v97, v129\nM 0,172,208\n"
"buffer_load_dwordx4 v220, s[16:19], s63 offen lds\nG 9\nv_add_f32 v98, v98, v130\nM 16,168,196\nR 162,186,4096\nv_add_f32 v99, v99, v131\n"
"G 9\nv_add_f32 v100, v100, v132\nM 16,172,212\nds_read2st64_b32 v[144:145], v225 offset0:16 offset1:17\nv_add_f32 v101, v101, v133\nG 9\n"
"v_add_f32 v102, v102, v134\nv_mfma_f32_16x16x32_bf16 a[224:227], v[168:171], a[200:203], a[224:227]\nR 164,186,8192\nv_add_f32 v103, v103, v135\nG 8\nv_add_f32 v104, v104, v136\n"
"v_mfma_f32_16x16x32_bf16 a[224:227], v[172:175], a[216:219], a[224:227]\nbuffer_load_dwordx4 v221, s[16:19], s63 offen offset:1024 lds\nv_add_f32 v105, v105, v137\nv_mfma_f32_16x16x32_bf16 a[240:243], v[168:171], a[204:207], a[240:243]\nR 166,186,12288\ns_add_u32 m0, s50, 73728\n"
"G 8\nv_add_f32 v106, v106, v138\nv_mfma_f32_16x16x32_bf16 a[240:243], v[172:175], a[220:223], a[240:243]\nds_read2st64_b32 v[146:147], v225 offset0:18 offset1:19\nG 8\nv_add_f32 v107, v107, v139\n"
"M 4,176,192\nR 168,187,0\ns_add_u32 s63, s59, 589824\nG 8\nv_add_f32 v108, v108, v140\nM 4,180,208\n"
"buffer_load_dwordx4 v220, s[16:19], s63 offen lds\nv_add_f32 v109, v109, v141\nM 20,176,196\nR 170,187,4096\nG 8\nv_add_f32 v110, v110, v142\n"
"v_add_f32 v111, v111, v143\nM 20,180,212\nds_read2st64_b32 v[148:149], v225 offset0:20 offset1:21\nG 6\nv_add_f32 v112, v112, v144\nv_add_f32 v113, v113, v145\n"
"v_mfma_f32_16x16x32_bf16 a[228:231], v[176:179], a[200:203], a[228:231]\nR 172,187,8192\nds_read2st64_b32 v[150:151], v225 offset0:22 offset1:23\nv_mfma_f32_16x16x32_bf16 a[228:231], v[180:183], a[216:219], a[228:231]\nds_read2st64_b32 v[152:153], v225 offset0:24 offset1:25\nG 6\n"
"v_add_f32 v114, v114, v146\nv_add_f32 v115, v115, v147\nv_mfma_f32_16x16x32_bf16 a[244:247], v[176:179], a[204:207], a[244:247]\nR 174,187,12288\nds_read2st64_b32 v[154:155], v225 offset0:26 offset1:27\nv_mfma_f32_16x16x32_bf16 a[244:247], v[180:183], a[220:223], a[244:247]\n"
"ds_read2st64_b32 v[156:157], v225 offset0:28 offset1:29\nds_read2st64_b32 v[158:159], v225 offset0:30 offset1:31\nM 8,160,192\nR 176,184,16384\nM 8,164,208\nbuffer_load_dwordx4 v221, s[16:19], s63 offen offset:1024 lds\n"
"G 8\nv_add_f32 v116, v116, v148\nM 24,160,196\nR 178,184,20480\ns_add_u32 s63, s59, 131072\nv_add_f32 v117, v117, v149\n"
"M 24,164,212\ns_add_u32 m0, s50, 81920\nG 7\nv_add_f32 v118, v118, v150\nv_add_f32 v119, v119, v151\nG 6\n"
"v_add_f32 v120, v120, v152\nv_mfma_f32_16x16x32_bf16 a[232:235], v[160:163], a[200:203], a[232:235]\nR 180,184,24576\nv_add_f32 v121, v121, v153\nG 5\nv_add_f32 v122, v122, v154\n"
"v_mfma_f32_16x16x32_bf16 a[232:235], v[164:167], a[216:219], a[232:235]\nbuffer_load_dwordx4 v220, s[16:19], s63 offen lds\nv_add_f32 v123, v123, v155\nG 4\nv_add_f32 v124, v124, v156\nv_add_f32 v125, v125, v157\n"
"G 3\nv_add_f32 v126, v126, v158\nv_add_f32 v127, v127, v159\nv_mfma_f32_16x16x32_bf16 a[248:251], v[160:163], a[204:207], a[248:251]\nR 182,184,28672\nv_cvt_pk_bf16_f32 v96, v96, v97\n"
"v_cvt_pk_bf16_f32 v97, v98, v99\nv_mfma_f32_16x16x32_bf16 a[248:251], v[164:167], a[220:223], a[248:251]\nv_cvt_pk_bf16_f32 v98, v100, v101\nv_cvt_pk_bf16_f32 v99, v102, v103\nv_cvt_pk_bf16_f32 v104, v104, v105\nM 12,168,192\n"
"R 160,185,16384\nv_cvt_pk_bf16_f32 v105, v106, v107\nv_cvt_pk_bf16_f32 v106, v108, v109\nM 12,172,208\nbuffer_load_dwordx4 v221, s[16:19], s63 offen offset:1024 lds\nv_cvt_pk_bf16_f32 v107, v110, v111\n"
"M 28,168,196\nR 162,185,20480\ns_add_u32 s63, s59, 655360\nv_cvt_pk_bf16_f32 v112, v112, v113\nM 28,172,212\ns_add_u32 m0, s50, 90112\n"
"v_cvt_pk_bf16_f32 v113, v114, v115\nv_cvt_pk_bf16_f32 v114, v116, v117\nv_cvt_pk_bf16_f32 v115, v118, v119\nv_mfma_f32_16x16x32_bf16 a[236:239], v[168:171], a[200:203], a[236:239]\nR 164,185,24576\nv_cvt_pk_bf16_f32 v120, v120, v121\n"
"v_cvt_pk_bf16_f32 v121, v122, v123\nv_mfma_f32_16x16x32_bf16 a[236:239], v[172:175], a[216:219], a[236:239]\nbuffer_load_dwordx4 v220, s[16:19], s63 offen lds\nv_cvt_pk_bf16_f32 v122, v124, v125\nv_mfma_f32_16x16x32_bf16 a[252:255], v[168:171], a[204:207], a[252:255]\nR 166,185,28672\n"
"s_bfe_u32 s100, s56, 0x20007\ns_mul_i32 s100, s100, 49152\ns_add_u32 s100, s100, s72\ns_add_u32 s100, s100, 52428800\nv_cvt_pk_bf16_f32 v123, v126, v127\nv_mfma_f32_16x16x32_bf16 a[252:255], v[172:175], a[220:223], a[252:255]\n"
"s_waitcnt vmcnt(11)\ns_barrier\nbuffer_store_dwordx4 v[96:99], v252, s[20:23], s100 offen offset:0 sc1\nG 4\nM 32,176,160\nR 168,186,16384\n"
"buffer_store_dwordx4 v[104:107], v252, s[20:23], s100 offen offset:1024 sc1\nM 32,180,176\nbuffer_load_dwordx4 v221, s[16:19], s63 offen offset:1024 lds\nbuffer_store_dwordx4 v[112:115], v252, s[20:23], s100 offen offset:2048 sc1\nv_xor_b32 v240, 0x2000, v225\nM 48,176,164\n"
"R 170,186,20480\nds_write2st64_b32 v240, a224, a225 offset0:0 offset1:1\nM 48,180,180\ns_add_u32 m0, s50, 98304\nds_write2st64_b32 v240, a226, a227 offset0:2 offset1:3\nM 64,176,168\n"
"R 172,186,24576\nds_write2st64_b32 v240, a228, a229 offset0:4 offset1:5\nM 64,180,184\ns_add_u32 s63, s59, 196608\nds_write2st64_b32 v240, a230, a231 offset0:6 offset1:7\nM 80,176,172\n"
"R 174,186,28672\nds_write2st64_b32 v240, a232, a233 offset0:8 offset1:9\nM 80,180,188\nbuffer_store_dwordx4 v[120:123], v252, s[20:23], s100 offen offset:3072 sc1\nds_write2st64_b32 v240, a234, a235 offset0:10 offset1:11\nG 10\n"
"M 36,160,160\nR 176,187,16384\nds_write2st64_b32 v240, a236, a237 offset0:12 offset1:13\nM 36,164,176\nbuffer_load_dwordx4 v220, s[16:19], s63 offen lds\nds_write2st64_b32 v240, a238, a239 offset0:14 offset1:15\n"
"M 52,160,164\nR 178,187,20480\nds_write2st64_b32 v240, a240, a241 offset0:16 offset1:17\nM 52,164,180\nds_write2st64_b32 v240, a242, a243 offset0:18 offset1:19\nM 68,160,168\n"
"R 180,187,24576\nv_mfma_f32_16x16x32_bf16 v[96:99], v[208:211], v[192:195], 0\nds_write2st64_b32 v240, a244, a245 offset0:20 offset1:21\nM 68,164,184\nv_mfma_f32_16x16x32_bf16 v[100:103], v[210:213], v[192:195], 0\nbuffer_load_dwordx4 v221, s[16:19], s63 offen offset:1024 lds\n"
"ds_write2st64_b32 v240, a246, a247 offset0:22 offset1:23\nM 84,160,172\nR 182,187,28672\nv_mfma_f32_16x16x32_bf16 v[104:107], v[208:211], v[196:199], 0\ns_add_u32 m0, s50, 106496\nds_write2st64_b32 v240, a248, a249 offset0:24 offset1:25\n"
"M 84,164,188\nv_mfma_f32_16x16x32_bf16 v[108:111], v[210:213], v[196:199], 0\nds_write2st64_b32 v240, a250, a251 offset0:26 offset1:27\nG 14\nM 40,168,160\nR 160,184,32768\n"
"v_mfma_f32_16x16x32_bf16 v[112:115], v[208:211], v[200:203], 0\ns_add_u32 s63, s59, 720896\nds_write2st64_b32 v240, a252, a253 offset0:28 offset1:29\nM 40,172,176\nv_mfma_f32_16x16x32_bf16 v[116:119], v[210:213], v[200:203], 0\nbuffer_load_dwordx4 v220, s[16:19], s63 offen lds\n"
"ds_write2st64_b32 v240, a254, a255 offset0:30 offset1:31\nM 56,168,164\nR 162,184,36864\nv_mfma_f32_16x16x32_bf16 v[120:123], v[208:211], v[204:207], 0\nM 56,172,180\nv_mfma_f32_16x16x32_bf16 v[124:127], v[210:213], v[204:207], 0\n"
"buffer_load_dwordx4 v221, s[16:19], s63 offen offset:1024 lds\nM 72,168,168\nR 164,184,40960\nv_mfma_f32_16x16x32_bf16 v[96:99], v[208:211], v[232:235], v[96:99]\ns_add_u32 m0, s50, 114688\ns_add_u32 s63, s59, 393216\n"
"M 72,172,184\nv_mfma_f32_16x16x32_bf16 v[100:103], v[210:213], v[232:235], v[100:103]\nbuffer_load_dwordx4 v220, s[16:19], s63 offen lds\nM 88,168,172\nR 166,184,45056\nv_mfma_f32_16x16x32_bf16 v[104:107], v[208:211], v[236:239], v[104:107]\n"
"M 88,172,188\nv_mfma_f32_16x16x32_bf16 v[108:111], v[210:213], v[236:239], v[108:111]\nG 8\nM 44,176,160\nR 168,185,32768\nv_mfma_f32_16x16x32_bf16 v[112:115], v[208:211], v[232:235], v[112:115]\n"
"s_waitcnt vmcnt(17) lgkmcnt(4)\ns_barrier\nM 44,180,176\nv_mfma_f32_16x16x32_bf16 v[116:119], v[210:213], v[232:235], v[116:119]\nbuffer_load_dwordx4 v221, s[16:19], s63 offen offset:1024 lds\nds_read2st64_b32 v[128:129], v225 offset0:0 offset1:1\n"
"M 60,176,164\nR 170,185,36864\nv_mfma_f32_16x16x32_bf16 v[120:123], v[208:211], v[236:239], v[120:123]\ns_add_u32 s63, s59, 458752\nds_read2st64_b32 v[130:131], v225 offset0:2 offset1:3\nM 60,180,180\n"
"v_mfma_f32_16x16x32_bf16 v[124:127], v[210:213], v[236:239], v[124:127]\ns_add_u32 m0, s50, 0\nds_read2st64_b32 v[132:133], v225 offset0:4 offset1:5\nds_read2st64_b32 v[134:135], v225 offset0:6 offset1:7\nM 76,176,168\nR 172,185,40960\n"
"ds_read2st64_b32 v[136:137], v225 offset0:8 offset1:9\nM 76,180,184\nbuffer_load_dwordx4 v220, s[16:19], s63 offen lds\nds_read2st64_b32 v[138:139], v225 offset0:10 offset1:11\nM 92,176,172\nR 174,185,45056\n"
"ds_read2st64_b32 v[140:141], v225 offset0:12 offset1:13\nM 92,180,188\nds_read2st64_b32 v[142:143], v225 offset0:14 offset1:15\nG 12\nG 10\nv_add_f32 v0, v0, v128\n"
"M 32,160,192\nR 176,186,32768\nv_add_f32 v1, v1, v129\nG 9\nv_add_f32 v2, v2, v130\nM 32,164,208\n"
"ds_read2st64_b32 v[144:145], v225 offset0:16 offset1:17\nv_add_f32 v3, v3, v131\nG 9\nv_add_f32 v4, v4, v132\nM 48,160,196\nR 178,186,36864\n"
"v_add_f32 v5, v5, v133\nG 9\nv_add_f32 v6, v6, v134\nM 48,164,212\nbuffer_load_dwordx4 v221, s[16:19], s63 offen offset:1024 lds\nv_add_f32 v7, v7, v135\n"
"M 64,160,200\nR 180,186,40960\ns_add_u32 s63, s59, 262144\nG 8\nv_add_f32 v8, v8, v136\nM 64,164,216\n"
"s_add_u32 m0, s50, 8192\nds_read2st64_b32 v[146:147], v225 offset0:18 offset1:19\nv_add_f32 v9, v9, v137\nM 80,160,204\nR 182,186,45056\nG 9\n"
"v_add_f32 v10, v10, v138\nv_add_f32 v11, v11, v139\nM 80,164,220\nds_read2st64_b32 v[148:149], v225 offset0:20 offset1:21\nG 9\nG 8\n"
"v_add_f32 v12, v12, v140\nM 36,168,192\nR 160,187,32768\nv_add_f32 v13, v13, v141\nG 8\nv_add_f32 v14, v14, v142\n"
"M 36,172,208\nbuffer_load_dwordx4 v220, s[16:19], s63 offen lds\nv_add_f32 v15, v15, v143\nM 52,168,196\nR 162,187,36864\nG 7\n"
"v_add_f32 v16, v16, v144\nv_add_f32 v17, v17, v145\nM 52,172,212\nds_read2st64_b32 v[150:151], v225 offset0:22 offset1:23\nG 5\nv_add_f32 v18, v18, v146\n"
"v_add_f32 v19, v19, v147\nM 68,168,200\nR 164,187,40960\nds_read2st64_b32 v[152:153], v225 offset0:24 offset1:25\nM 68,172,216\nds_read2st64_b32 v[154:155], v225 offset0:26 offset1:27\n"
"G 6\nv_add_f32 v20, v20, v148\nv_add_f32 v21, v21, v149\nM 84,168,204\nR 166,187,45056\nds_read2st64_b32 v[156:157], v225 offset0:28 offset1:29\n"
"M 84,172,220\nds_read2st64_b32 v[158:159], v225 offset0:30 offset1:31\nM 40,176,192\nR 168,184,49152\nM 40,180,208\nbuffer_load_dwordx4 v221, s[16:19], s63 offen offset:1024 lds\n"
"G 7\nv_add_f32 v22, v22, v150\nM 56,176,196\nR 170,184,53248\nv_add_f32 v23, v23, v151\nG 6\n"
"v_add_f32 v24, v24, v152\nM 56,180,212\ns_add_u32 m0, s50, 16384\nv_add_f32 v25, v25, v153\nG 5\nv_add_f32 v26, v26, v154\n"
"v_add_f32 v27, v27, v155\nM 72,176,200\nR 172,184,57344\nG 4\nv_add_f32 v28, v28, v156\nv_add_f32 v29, v29, v157\n"
"M 72,180,216\ns_add_u32 s63, s59, 786432\nG 3\nv_add_f32 v30, v30, v158\nv_add_f32 v31, v31, v159\nM 88,176,204\n"
"R 174,184,61440\nv_cvt_pk_bf16_f32 v0, v0, v1\nv_cvt_pk_bf16_f32 v1, v2, v3\nM 88,180,220\nv_cvt_pk_bf16_f32 v2, v4, v5\nv_cvt_pk_bf16_f32 v3, v6, v7\n"
"v_cvt_pk_bf16_f32 v8, v8, v9\nM 44,160,192\nR 176,185,49152\nv_cvt_pk_bf16_f32 v9, v10, v11\nv_cvt_pk_bf16_f32 v10, v12, v13\nM 44,164,208\n"
"buffer_load_dwordx4 v220, s[16:19], s63 offen lds\nv_cvt_pk_bf16_f32 v11, v14, v15\nM 60,160,196\nR 178,185,53248\nv_cvt_pk_bf16_f32 v16, v16, v17\nv_cvt_pk_bf16_f32 v17, v18, v19\n"
"M 60,164,212\nv_cvt_pk_bf16_f32 v18, v20, v21\nv_cvt_pk_bf16_f32 v19, v22, v23\nv_cvt_pk_bf16_f32 v24, v24, v25\nM 76,160,200\nR 180,185,57344\n"
"v_cvt_pk_bf16_f32 v25, v26, v27\nv_cvt_pk_bf16_f32 v26, v28, v29\nM 76,164,216\nbuffer_load_dwordx4 v221, s[16:19], s63 offen offset:1024 lds\nv_cvt_pk_bf16_f32 v27, v30, v31\nM 92,160,204\n"
"R 182,185,61440\ns_bfe_u32 s100, s57, 0x20007\ns_mul_i32 s100, s100, 49152\ns_add_u32 s100, s100, s72\ns_add_u32 s100, s100, 52445184\nM 92,164,220\n"
"s_waitcnt vmcnt(20)\ns_barrier\nbuffer_store_dwordx4 v[0:3], v252, s[20:23], s100 offen offset:0 sc1\nG 4\nM 96,168,0\nR 160,186,49152\n"
"buffer_store_dwordx4 v[8:11], v252, s[20:23], s100 offen offset:1024 sc1\nM 96,172,16\ns_mov_b32 m0, s51\nbuffer_store_dwordx4 v[16:19], v252, s[20:23], s100 offen offset:2048 sc1\nM 112,168,4\nR 162,186,53248\n"
"ds_write_addtid_b32 v64 offset:0\nM 112,172,20\nds_write_addtid_b32 v65 offset:256\nds_write_addtid_b32 v66 offset:512\nv_mfma_f32_16x16x32_bf16 v[128:131], v[168:171], a[8:11], 0\nR 164,186,57344\n"
"ds_write_addtid_b32 v67 offset:768\nM 128,172,24\nds_write_addtid_b32 v68 offset:1024\nds_write_addtid_b32 v69 offset:1280\nv_mfma_f32_16x16x32_bf16 v[144:147], v[168:171], a[12:15], 0\nR 166,186,61440\n"
"ds_write_addtid_b32 v70 offset:1536\nM 144,172,28\nds_write_addtid_b32 v71 offset:1792\nds_write_addtid_b32 v72 offset:2048\nG 13\nM 100,176,0\n"
"R 168,187,49152\nds_write_addtid_b32 v73 offset:2304\nM 100,180,16\nds_write_addtid_b32 v74 offset:2560\nbuffer_store_dwordx4 v[24:27], v252, s[20:23], s100 offen offset:3072 sc1\nds_write_addtid_b32 v75 offset:2816\n"
"M 116,176,4\nR 170,187,53248\nds_write_addtid_b32 v76 offset:3072\nM 116,180,20\nds_write_addtid_b32 v77 offset:3328\nds_write_addtid_b32 v78 offset:3584\n"
"v_mfma_f32_16x16x32_bf16 v[132:135], v[176:179], a[8:11], 0\nR 172,187,57344\nds_write_addtid_b32 v79 offset:3840\nM 132,180,24\nds_write_addtid_b32 v80 offset:4096\nds_write_addtid_b32 v81 offset:4352\n"
"v_mfma_f32_16x16x32_bf16 v[148:151], v[176:179], a[12:15], 0\nR 174,187,61440\nds_write_addtid_b32 v82 offset:4608\nM 148,180,28\nds_write_addtid_b32 v83 offset:4864\nds_write_addtid_b32 v84 offset:5120\n"
"G 15\nM 104,160,0\nR 176,188,0\nds_write_addtid_b32 v85 offset:5376\nM 104,164,16\nds_write_addtid_b32 v86 offset:5632\n"
"ds_write_addtid_b32 v87 offset:5888\nM 120,160,4\nR 178,188,4096\nds_write_addtid_b32 v88 offset:6144\nM 120,164,20\nds_write_addtid_b32 v89 offset:6400\n"
"ds_write_addtid_b32 v90 offset:6656\nv_mfma_f32_16x16x32_bf16 v[136:139], v[160:163], a[8:11], 0\nR 180,188,8192\nds_write_addtid_b32 v91 offset:6912\nM 136,164,24\nds_write_addtid_b32 v92 offset:7168\n"
"ds_write_addtid_b32 v93 offset:7424\nds_write_addtid_b32 v94 offset:7680\nds_write_addtid_b32 v95 offset:7936\nv_mfma_f32_16x16x32_bf16 v[152:155], v[160:163], a[12:15], 0\nR 182,188,12288\nM 152,164,28\n"
"G 15\nM 108,168,0\nR 160,189,0\ns_waitcnt vmcnt(18) lgkmcnt(2)\ns_barrier\nM 108,172,16\n"
"ds_read2st64_b32 v[64:65], v225 offset0:0 offset1:1\nds_read2st64_b32 v[66:67], v225 offset0:2 offset1:3\nM 124,168,4\nR 162,189,4096\nds_read2st64_b32 v[68:69], v225 offset0:4 offset1:5\nM 124,172,20\n"
"ds_read2st64_b32 v[70:71], v225 offset0:6 offset1:7\nds_read2st64_b32 v[72:73], v225 offset0:8 offset1:9\nv_mfma_f32_16x16x32_bf16 v[140:143], v[168:171], a[8:11], 0\nR 164,189,8192\nds_read2st64_b32 v[74:75], v225 offset0:10 offset1:11\nM 140,172,24\n"
"ds_read2st64_b32 v[76:77], v225 offset0:12 offset1:13\nds_read2st64_b32 v[78:79], v225 offset0:14 offset1:15\nv_mfma_f32_16x16x32_bf16 v[156:159], v[168:171], a[12:15], 0\nR 166,189,12288\nds_read2st64_b32 v[80:81], v225 offset0:16 offset1:17\nM 156,172,28\n"
"ds_read2st64_b32 v[82:83], v225 offset0:18 offset1:19\nG 14\nG 12\nv_add_f32 v32, v32, v64\nM 96,176,32\nR 168,190,0\n"
"v_add_f32 v33, v33, v65\nG 12\nv_add_f32 v34, v34, v66\nM 96,180,48\nds_read2st64_b32 v[84:85], v225 offset0:20 offset1:21\nv_add_f32 v35, v35, v67\n"
"G 11\nv_add_f32 v36, v36, v68\nM 112,176,36\nR 170,190,4096\nv_add_f32 v37, v37, v69\nG 11\n"
"v_add_f32 v38, v38, v70\nM 112,180,52\nds_read2st64_b32 v[86:87], v225 offset0:22 offset1:23\nv_add_f32 v39, v39, v71\nG 11\nv_add_f32 v40, v40, v72\n"
"M 128,176,40\nR 172,190,8192\nv_add_f32 v41, v41, v73\nG 10\nv_add_f32 v42, v42, v74\nM 128,180,56\n"
"ds_read2st64_b32 v[88:89], v225 offset0:24 offset1:25\nv_add_f32 v43, v43, v75\nG 10\nv_add_f32 v44, v44, v76\nM 144,176,44\nR 174,190,12288\n"
"v_add_f32 v45, v45, v77\nG 10\nv_add_f32 v46, v46, v78\nM 144,180,60\nds_read2st64_b32 v[90:91], v225 offset0:26 offset1:27\nG 10\n"
"v_add_f32 v47, v47, v79\nM 100,160,32\nR 176,191,0\nG 10\nv_add_f32 v48, v48, v80\nv_add_f32 v49, v49, v81\n"
"M 100,164,48\nds_read2st64_b32 v[92:93], v225 offset0:28 offset1:29\nG 10\nv_add_f32 v50, v50, v82\nv_add_f32 v51, v51, v83\nM 116,160,36\n"
"R 178,191,4096\nG 9\nv_add_f32 v52, v52, v84\nM 116,164,52\nds_read2st64_b32 v[94:95], v225 offset0:30 offset1:31\nv_add_f32 v53, v53, v85\n"
"M 132,160,40\nR 180,191,8192\nG 9\nv_add_f32 v54, v54, v86\nM 132,164,56\nv_add_f32 v55, v55, v87\n"
"G 7\nv_add_f32 v56, v56, v88\nM 148,160,44\nR 182,191,12288\nv_add_f32 v57, v57, v89\nM 148,164,60\n"
"G 7\nG 6\nv_add_f32 v58, v58, v90\nv_add_f32 v59, v59, v91\nM 104,168,32\nR 160,188,16384\n"
"G 5\nv_add_f32 v60, v60, v92\nM 104,172,48\nv_add_f32 v61, v61, v93\nG 3\nv_add_f32 v62, v62, v94\n"
"M 120,168,36\nR 162,188,20480\nv_mov_b32 v226, s46\nM 120,172,52\nv_mov_b32 v227, s47\nv_add_f32 v63, v63, v95\n"
"M 136,168,40\nR 164,188,24576\ns_bfe_u32 s62, s56, 0x20007\ns_lshl_b32 s62, s62, 5\ns_add_u32 s61, s74, 548864\ns_add_u32 s61, s61, s62\n"
"s_waitcnt vmcnt(0)\ns_mov_b64 exec, 1\nbuffer_atomic_swap_x2 v[226:227], off, s[20:23], s61 sc1\ns_mov_b64 exec, -1\nM 136,172,56\ns_bfe_u32 s62, s57, 0x20007\n"
"s_lshl_b32 s62, s62, 5\ns_add_u32 s61, s74, 548872\ns_add_u32 s61, s61, s62\ns_mov_b64 exec, 1\nbuffer_atomic_swap_x2 v[226:227], off, s[20:23], s61 sc1\ns_mov_b64 exec, -1\n"
"M 152,168,44\nR 166,188,28672\ns_add_u32 s63, s59, 327680\nv_cvt_pk_bf16_f32 v32, v32, v33\nM 152,172,60\nG 4\n"
"v_cvt_pk_bf16_f32 v33, v34, v35\nv_cvt_pk_bf16_f32 v34, v36, v37\nv_cvt_pk_bf16_f32 v35, v38, v39\nM 108,176,32\nR 168,189,16384\nv_cvt_pk_bf16_f32 v40, v40, v41\n"
"v_cvt_pk_bf16_f32 v41, v42, v43\nM 108,180,48\ns_add_u32 m0, s50, 24576\nv_cvt_pk_bf16_f32 v42, v44, v45\nv_cvt_pk_bf16_f32 v43, v46, v47\nv_cvt_pk_bf16_f32 v48, v48, v49\n"
"M 124,176,36\nR 170,189,20480\nv_cvt_pk_bf16_f32 v49, v50, v51\nv_cvt_pk_bf16_f32 v50, v52, v53\nM 124,180,52\nbuffer_load_dwordx4 v220, s[16:19], s63 offen lds\n"
"v_cvt_pk_bf16_f32 v51, v54, v55\nM 140,176,40\nR 172,189,24576\nv_cvt_pk_bf16_f32 v56, v56, v57\nv_cvt_pk_bf16_f32 v57, v58, v59\nM 140,180,56\n"
"v_cvt_pk_bf16_f32 v58, v60, v61\nv_cvt_pk_bf16_f32 v59, v62, v63\nM 156,176,44\nR 174,189,28672\ns_bfe_u32 s100, s58, 0x20007\ns_mul_i32 s100, s100, 49152\n"
"s_add_u32 s100, s100, s72\ns_add_u32 s100, s100, 52461568\nM 156,180,60\ns_barrier\nG 4\nM 96,160,64\n"
"R 176,190,16384\nbuffer_store_dwordx4 v[32:35], v252, s[20:23], s100 offen offset:0 sc1\nM 96,164,80\nbuffer_load_dwordx4 v221, s[16:19], s63 offen offset:1024 lds\nM 112,160,68\nR 178,190,20480\n"
"s_add_u32 s63, s59, 851968\nM 112,164,84\ns_add_u32 m0, s50, 32768\nbuffer_store_dwordx4 v[40:43], v252, s[20:23], s100 offen offset:1024 sc1\nM 128,160,72\nR 180,190,24576\n"
"M 128,164,88\nbuffer_load_dwordx4 v220, s[16:19], s63 offen lds\nM 144,160,76\nR 182,190,28672\nbuffer_store_dwordx4 v[48:51], v252, s[20:23], s100 offen offset:2048 sc1\nM 144,164,92\n"
"buffer_store_dwordx4 v[56:59], v252, s[20:23], s100 offen offset:3072 sc1\nG 4\nM 100,168,64\nR 160,191,16384\nLq605:\ns_lshr_b32 s60, s45, 3\n"
"s_lshl2_add_u32 s60, s60, s40\ns_lshl_b32 s60, s60, 8\nv_lshl_add_u32 v224, v252, 3, s60\nv_add_u32 v224, Lq605+4096-LqPC, v224\ns_mov_b64 exec, 3\nglobal_load_dword v224, v224, s[98:99]\n"
"s_mov_b64 exec, -1\nM 100,172,80\nv_bfe_u32 v250, v252, 4, 2\nv_lshlrev_b32 v250, 3, v250\nM 116,168,68\nR 162,191,20480\n"
"M 116,172,84\ns_lshl_b32 s60, s43, 5\nv_add_u32 v250, s60, v250\nM 132,168,72\nR 164,191,24576\nM 132,172,88\n"
"buffer_load_dwordx4 v221, s[16:19], s63 offen offset:1024 lds\nM 148,168,76\nR 166,191,28672\nM 148,172,92\nG 4\nM 104,176,64\n"
"R 168,188,32768\nM 104,180,80\ns_movk_i32 s63, 6144\nv_mad_u32_u24 v220, v228, s63, v229\nv_add_u32 v221, 3144704, v220\ns_add_u32 m0, s50, 40960\n"
"s_add_u32 s63, s52, 2048\nbuffer_load_dwordx4 v220, s[4:7], s63 offen lds\nM 120,176,68\nR 170,188,36864\nM 120,180,84\nM 136,176,72\n"
"R 172,188,40960\nM 136,180,88\nM 152,176,76\nR 174,188,45056\nM 152,180,92\nG 4\n"
"M 108,160,64\nR 176,189,32768\nM 108,164,80\nbuffer_load_dwordx4 v221, s[4:7], s63 offen offset:1024 lds\nM 124,160,68\nR 178,189,36864\n"
"s_add_u32 s63, s52, 198656\nM 124,164,84\ns_add_u32 m0, s50, 49152\nM 140,160,72\nR 180,189,40960\nM 140,164,88\n"
"buffer_load_dwordx4 v220, s[4:7], s63 offen lds\nM 156,160,76\nR 182,189,45056\nM 156,164,92\nG 4\nM 96,168,96\n"
"R 160,190,32768\nM 96,172,112\nbuffer_load_dwordx4 v221, s[4:7], s63 offen offset:1024 lds\nM 112,168,100\nR 162,190,36864\ns_add_u32 s63, s52, 4096\n"
"M 112,172,116\ns_add_u32 m0, s50, 57344\nM 128,168,104\nR 164,190,40960\nM 128,172,120\nbuffer_load_dwordx4 v220, s[4:7], s63 offen lds\n"
"M 144,168,108\nR 166,190,45056\nM 144,172,124\nG 4\nM 100,176,96\nR 168,191,32768\n"
"M 100,180,112\nbuffer_load_dwordx4 v221, s[4:7], s63 offen offset:1024 lds\nM 116,176,100\nR 170,191,36864\nM 116,180,116\nM 132,176,104\n"
"R 172,191,40960\nv_mov_b32 v248, 0\nM 132,180,120\ns_add_u32 m0, s50, 65536\nv_mov_b32 v249, 0\nM 148,176,108\n"
"R 174,191,45056\ns_add_u32 s61, s74, 548864\ns_mov_b64 exec, 3\nbuffer_atomic_or_x2 v[248:249], v250, s[20:23], s61 offen sc0 sc1\ns_mov_b64 exec, -1\nM 148,180,124\n"
"G 4\nM 104,160,96\nR 176,188,49152\ns_barrier\nM 104,164,112\ns_add_u32 s63, s52, 200704\n"
"M 120,160,100\nR 178,188,53248\nM 120,164,116\nbuffer_load_dwordx4 v220, s[4:7], s63 offen lds\nM 136,160,104\nR 180,184,0\n"
"M 136,164,120\nM 152,160,108\nR 182,184,4096\nM 152,164,124\nG 4\nM 108,168,96\n"
"R 160,189,49152\nM 108,172,112\nbuffer_load_dwordx4 v221, s[4:7], s63 offen offset:1024 lds\nM 124,168,100\nR 162,189,53248\nM 124,172,116\n"
"s_add_u32 m0, s50, 73728\nM 140,168,104\nR 164,185,0\nM 140,172,120\ns_add_u32 s63, s52, 395264\nM 156,168,108\n"
"R 166,185,4096\nM 156,172,124\nG 4\nM 96,176,128\nR 168,190,49152\nM 96,180,144\n"
"buffer_load_dwordx4 v220, s[4:7], s63 offen lds\nM 112,176,132\nR 170,190,53248\nM 112,180,148\nM 128,176,136\nR 172,186,0\n"
"s_lshr_b32 s60, s45, 3\ns_lshl2_add_u32 s60, s60, s40\ns_lshl_b32 s60, s60, 8\ns_waitcnt vmcnt(11)\nv_lshl_add_u32 v224, v252, 3, s60\nv_add_u32 v224, LqM-LqPC, v224\n"
"s_mov_b64 exec, 3\nglobal_load_dword v224, v224, s[98:99]\ns_mov_b64 exec, -1\nM 128,180,152\nM 144,176,140\nR 174,186,4096\n"
"M 144,180,156\nG 4\nM 100,160,128\nR 176,191,49152\nM 100,164,144\nbuffer_load_dwordx4 v221, s[4:7], s63 offen offset:1024 lds\n"
"M 116,160,132\nR 178,191,53248\nM 116,164,148\nM 132,160,136\nR 180,187,0\nM 132,164,152\n"
"s_mul_i32 s64, s43, 49152\nM 148,160,140\nR 182,187,4096\nM 148,164,156\nG 4\nM 104,168,128\n"
"R 160,184,8192\ns_waitcnt vmcnt(5)\nv_cmp_eq_u64 s[86:87], s[46:47], v[248:249]\nM 104,172,144\ns_add_u32 s64, s64, s72\nM 120,168,132\n"
"R 162,184,12288\ns_and_b32 s62, s86, 3\ns_cmp_eq_u32 s62, 3\ns_cbranch_scc0 Lq530\nLq531:\nM 120,172,148\n"
"s_add_u32 s64, s64, 52428800\nM 136,168,136\nR 164,184,16384\nM 136,172,152\nbuffer_load_dwordx4 v[0:3], v252, s[20:23], s64 offen offset:0 sc1\nM 152,168,140\n"
"R 166,184,20480\nM 152,172,156\nG 4\nM 108,176,128\nR 168,185,8192\ns_barrier\n"
"M 108,180,144\nbuffer_load_dwordx4 v[4:7], v252, s[20:23], s64 offen offset:1024 sc1\nM 124,176,132\nR 170,185,12288\ns_mul_i32 s65, s43, 49152\nM 124,180,148\n"
"buffer_load_dwordx4 v[8:11], v252, s[20:23], s64 offen offset:2048 sc1\nM 140,176,136\nR 172,185,16384\ns_add_u32 s65, s65, s72\nM 140,180,152\nbuffer_load_dwordx4 v[12:15], v252, s[20:23], s64 offen offset:3072 sc1\n"
"M 156,176,140\nR 174,185,20480\ns_add_u32 s65, s65, 52445184\nM 156,180,156\nG 4\nM 128,160,168\n"
"R 176,186,8192\nbuffer_load_dwordx4 v[16:19], v252, s[20:23], s65 offen offset:0 sc1\nM 128,164,184\nR 178,186,12288\nM 144,160,172\nR 180,186,16384\n"
"M 144,164,188\nR 182,186,20480\nG 4\nM 132,168,168\nR 160,187,8192\nbuffer_load_dwordx4 v[20:23], v252, s[20:23], s65 offen offset:1024 sc1\n"
"M 132,172,184\nR 162,187,12288\nM 148,168,172\nR 164,187,16384\nbuffer_load_dwordx4 v[24:27], v252, s[20:23], s65 offen offset:2048 sc1\nM 148,172,188\n"
"R 166,187,20480\nG 4\nM 136,176,168\nR 168,184,24576\ns_mul_i32 s66, s43, 49152\nM 136,180,184\n"
"R 170,184,28672\nbuffer_load_dwordx4 v[28:31], v252, s[20:23], s65 offen offset:3072 sc1\nM 152,176,172\nR 172,184,32768\ns_add_u32 s66, s66, s72\nM 152,180,188\n"
"R 174,184,36864\nG 4\nM 140,160,168\nR 176,185,24576\ns_add_u32 s66, s66, 52461568\nM 140,164,184\n"
"R 178,185,28672\nM 156,160,172\nR 180,185,32768\nM 156,164,188\nR 182,185,36864\nG 4\n"
"M 128,168,200\nR 160,186,24576\nbuffer_load_dwordx4 v[64:67], v252, s[20:23], s66 offen offset:0 sc1\nM 128,172,216\nR 162,186,28672\nM 144,168,204\n"
"R 164,186,32768\nbuffer_load_dwordx4 v[68:71], v252, s[20:23], s66 offen offset:1024 sc1\nM 144,172,220\nR 166,186,36864\nG 4\nM 132,176,200\n"
"R 168,187,24576\nM 132,180,216\nR 170,187,28672\nbuffer_load_dwordx4 v[72:75], v252, s[20:23], s66 offen offset:2048 sc1\nM 148,176,204\nR 172,187,32768\n"
"M 148,180,220\nR 174,187,36864\nG 4\nM 136,160,200\nR 176,184,8192\nbuffer_load_dwordx4 v[76:79], v252, s[20:23], s66 offen offset:3072 sc1\n"
"M 136,164,216\nR 178,184,12288\nM 152,160,204\nR 180,184,16384\nM 152,164,220\nR 182,184,20480\n"
"G 4\nM 140,168,200\nR 160,185,8192\nM 140,172,216\nR 162,185,12288\nM 156,168,204\n"
"R 164,185,16384\nM 156,172,220\nR 166,185,20480\nG 4\nM 96,176,160\nR 168,186,8192\n"
"M 96,180,176\nR 170,186,12288\ns_mov_b32 m0, s51\nM 112,176,164\nR 172,186,16384\nds_write_addtid_b32 v128 offset:0\n"
"M 112,180,180\nR 174,186,20480\nds_write_addtid_b32 v129 offset:256\nG 6\nM 100,160,160\nR 176,187,8192\n"
"ds_write_addtid_b32 v130 offset:512\nM 100,164,176\nR 178,187,12288\nds_write_addtid_b32 v131 offset:768\nM 116,160,164\nR 180,187,16384\n"
"ds_write_addtid_b32 v132 offset:1024\nM 116,164,180\nR 182,187,20480\nds_write_addtid_b32 v133 offset:1280\nG 9\nM 104,168,160\n"
"R 160,184,24576\nds_write_addtid_b32 v134 offset:1536\nM 104,172,176\nR 162,184,28672\nds_write_addtid_b32 v135 offset:1792\nM 120,168,164\n"
"R 164,184,32768\nds_write_addtid_b32 v136 offset:2048\nM 120,172,180\nR 166,184,36864\nds_write_addtid_b32 v137 offset:2304\nG 9\n"
"M 108,176,160\nR 168,185,24576\nds_write_addtid_b32 v138 offset:2560\nM 108,180,176\nR 170,185,28672\nds_write_addtid_b32 v139 offset:2816\n"
"M 124,176,164\nR 172,185,32768\nds_write_addtid_b32 v140 offset:3072\nds_write_addtid_b32 v141 offset:3328\nds_write_addtid_b32 v142 offset:3584\nds_write_addtid_b32 v143 offset:3840\n"
"ds_write_addtid_b32 v144 offset:4096\nds_write_addtid_b32 v145 offset:4352\nds_write_addtid_b32 v146 offset:4608\nds_write_addtid_b32 v147 offset:4864\nds_write_addtid_b32 v148 offset:5120\nds_write_addtid_b32 v149 offset:5376\n"
"ds_write_addtid_b32 v150 offset:5632\nds_write_addtid_b32 v151 offset:5888\nds_write_addtid_b32 v152 offset:6144\nds_write_addtid_b32 v153 offset:6400\nds_write_addtid_b32 v154 offset:6656\nds_write_addtid_b32 v155 offset:6912\n"
"ds_write_addtid_b32 v156 offset:7168\nds_write_addtid_b32 v157 offset:7424\nds_write_addtid_b32 v158 offset:7680\nds_write_addtid_b32 v159 offset:7936\nM 124,180,180\nR 174,185,36864\n"
"G 15\nM 96,160,192\nR 176,186,24576\nM 96,164,208\nR 178,186,28672\nG 3\n"
"s_barrier\nM 112,160,196\nR 180,186,32768\ns_add_u32 m0, s50, 81920\nds_read2st64_b32 v[128:129], v225 offset0:0 offset1:1\nM 112,164,212\n"
"R 182,186,36864\nds_read2st64_b32 v[130:131], v225 offset0:2 offset1:3\nG 6\nM 100,168,192\nR 160,187,24576\ns_add_u32 s63, s52, 591872\n"
"M 100,172,208\nR 162,187,28672\nds_read2st64_b32 v[132:133], v225 offset0:4 offset1:5\nM 116,168,196\nR 164,187,32768\nds_read2st64_b32 v[134:135], v225 offset0:6 offset1:7\n"
"M 116,172,212\nR 166,187,36864\nG 7\nM 104,176,192\nbuffer_load_dwordx4 v220, s[4:7], s63 offen lds\nM 104,180,208\n"
"ds_read2st64_b32 v[136:137], v225 offset0:8 offset1:9\nds_read2st64_b32 v[138:139], v225 offset0:10 offset1:11\nM 120,176,196\nds_read2st64_b32 v[140:141], v225 offset0:12 offset1:13\nds_read2st64_b32 v[142:143], v225 offset0:14 offset1:15\nM 120,180,212\n"
"ds_read2st64_b32 v[144:145], v225 offset0:16 offset1:17\nds_read2st64_b32 v[146:147], v225 offset0:18 offset1:19\nG 6\nM 108,160,192\nbuffer_load_dwordx4 v221, s[4:7], s63 offen offset:1024 lds\nM 108,164,208\n"
"ds_read2st64_b32 v[148:149], v225 offset0:20 offset1:21\nds_read2st64_b32 v[150:151], v225 offset0:22 offset1:23\nM 124,160,196\nds_read2st64_b32 v[152:153], v225 offset0:24 offset1:25\nds_read2st64_b32 v[154:155], v225 offset0:26 offset1:27\nM 124,164,212\n"
"ds_read2st64_b32 v[156:157], v225 offset0:28 offset1:29\nds_read2st64_b32 v[158:159], v225 offset0:30 offset1:31\nG 0\ns_barrier\ns_lshl_b32 s84, s75, 11\ns_add_u32 s84, s84, s49\n"
"s_add_u32 s84, s84, 18874368\ns_add_u32 s85, s84, 32768\nv_mov_b32 v238, s46\nv_mov_b32 v239, s47\ns_waitcnt vmcnt(13)\nv_mfma_f32_16x16x32_bf16 v[96:99], v[208:211], v[0:3], v[96:99]\n"
"v_mfma_f32_16x16x32_bf16 v[100:103], v[210:213], v[0:3], v[100:103]\ns_waitcnt vmcnt(12)\nv_mfma_f32_16x16x32_bf16 v[104:107], v[208:211], v[4:7], v[104:107]\nv_mfma_f32_16x16x32_bf16 v[108:111], v[210:213], v[4:7], v[108:111]\ns_waitcnt vmcnt(11)\nv_mfma_f32_16x16x32_bf16 v[112:115], v[208:211], v[8:11], v[112:115]\n"
"v_mfma_f32_16x16x32_bf16 v[116:119], v[210:213], v[8:11], v[116:119]\ns_waitcnt vmcnt(10)\nv_mfma_f32_16x16x32_bf16 v[120:123], v[208:211], v[12:15], v[120:123]\nv_mfma_f32_16x16x32_bf16 v[124:127], v[210:213], v[12:15], v[124:127]\ns_waitcnt vmcnt(9)\nv_mfma_f32_16x16x32_bf16 v[96:99], v[208:211], v[16:19], v[96:99]\n"
"v_mfma_f32_16x16x32_bf16 v[100:103], v[210:213], v[16:19], v[100:103]\ns_waitcnt vmcnt(8)\nv_mfma_f32_16x16x32_bf16 v[104:107], v[208:211], v[20:23], v[104:107]\nv_mfma_f32_16x16x32_bf16 v[108:111], v[210:213], v[20:23], v[108:111]\ns_waitcnt vmcnt(7)\nv_mfma_f32_16x16x32_bf16 v[112:115], v[208:211], v[24:27], v[112:115]\n"
"v_mfma_f32_16x16x32_bf16 v[116:119], v[210:213], v[24:27], v[116:119]\ns_waitcnt vmcnt(6)\nv_mfma_f32_16x16x32_bf16 v[120:123], v[208:211], v[28:31], v[120:123]\nv_mfma_f32_16x16x32_bf16 v[124:127], v[210:213], v[28:31], v[124:127]\ns_waitcnt vmcnt(3)\nv_max3_u32 v248, v67, v71, v75\n"
"s_waitcnt vmcnt(2)\nv_max_u32 v248, v248, v79\nv_cmp_eq_u32 vcc, -1, v248\ns_nop 1\ns_cbranch_vccnz Lq553\nLq554:\n"
"v_mfma_f32_16x16x32_bf16 v[96:99], v[208:211], v[64:67], v[96:99]\nv_mfma_f32_16x16x32_bf16 v[100:103], v[210:213], v[64:67], v[100:103]\nv_mfma_f32_16x16x32_bf16 v[104:107], v[208:211], v[68:71], v[104:107]\nv_mfma_f32_16x16x32_bf16 v[108:111], v[210:213], v[68:71], v[108:111]\nv_mfma_f32_16x16x32_bf16 v[112:115], v[208:211], v[72:75], v[112:115]\nv_mfma_f32_16x16x32_bf16 v[116:119], v[210:213], v[72:75], v[116:119]\n"
"v_mfma_f32_16x16x32_bf16 v[120:123], v[208:211], v[76:79], v[120:123]\nv_mfma_f32_16x16x32_bf16 v[124:127], v[210:213], v[76:79], v[124:127]\ns_nop 0\nv_pk_add_f32 v[96:97], v[96:97], v[128:129]\nv_pk_add_f32 v[98:99], v[98:99], v[130:131]\nv_pk_add_f32 v[100:101], v[100:101], v[132:133]\n"
"v_pk_add_f32 v[102:103], v[102:103], v[134:135]\nv_cvt_pk_bf16_f32 v192, v96, v97\nv_cvt_pk_bf16_f32 v193, v98, v99\nv_cvt_pk_bf16_f32 v194, v100, v101\nv_cvt_pk_bf16_f32 v195, v102, v103\nbuffer_store_dwordx4 v[192:195], v252, s[20:23], s84 offen offset:0 sc1\n"
"v_pk_add_f32 v[104:105], v[104:105], v[136:137]\nv_pk_add_f32 v[106:107], v[106:107], v[138:139]\nv_pk_add_f32 v[108:109], v[108:109], v[140:141]\nv_pk_add_f32 v[110:111], v[110:111], v[142:143]\nv_cvt_pk_bf16_f32 v196, v104, v105\nv_cvt_pk_bf16_f32 v197, v106, v107\n"
"v_cvt_pk_bf16_f32 v198, v108, v109\nv_cvt_pk_bf16_f32 v199, v110, v111\nbuffer_store_dwordx4 v[196:199], v252, s[20:23], s84 offen offset:1024 sc1\nv_pk_add_f32 v[112:113], v[112:113], v[144:145]\nv_pk_add_f32 v[114:115], v[114:115], v[146:147]\nv_pk_add_f32 v[116:117], v[116:117], v[148:149]\n"
"v_pk_add_f32 v[118:119], v[118:119], v[150:151]\nv_cvt_pk_bf16_f32 v200, v112, v113\nv_cvt_pk_bf16_f32 v201, v114, v115\nv_cvt_pk_bf16_f32 v202, v116, v117\nv_cvt_pk_bf16_f32 v203, v118, v119\nbuffer_store_dwordx4 v[200:203], v252, s[20:23], s85 offen offset:0 sc1\n"
"v_pk_add_f32 v[120:121], v[120:121], v[152:153]\nv_pk_add_f32 v[122:123], v[122:123], v[154:155]\nv_pk_add_f32 v[124:125], v[124:125], v[156:157]\nv_pk_add_f32 v[126:127], v[126:127], v[158:159]\nv_cvt_pk_bf16_f32 v204, v120, v121\nv_cvt_pk_bf16_f32 v205, v122, v123\n"
"v_cvt_pk_bf16_f32 v206, v124, v125\nv_cvt_pk_bf16_f32 v207, v126, v127\nbuffer_store_dwordx4 v[204:207], v252, s[20:23], s85 offen offset:1024 sc1\ns_add_u32 s61, s79, 88358912\ns_lshl_b32 s62, s75, 3\ns_add_u32 s61, s61, s62\n"
"s_waitcnt vmcnt(0)\ns_mov_b64 exec, 1\nbuffer_atomic_swap_x2 v[238:239], off, s[20:23], s61 sc1\ns_mov_b64 exec, -1\ns_branch LqM\nLq116:\n"
"s_lshl_b32 s61, s42, 14\ns_add_u32 s61, s61, 84148224\nbuffer_load_dwordx4 v[128:131], v230, s[20:23], s61 offen sc1\ns_lshl_b32 s61, s42, 14\ns_add_u32 s61, s61, 84152320\nbuffer_load_dwordx4 v[132:135], v230, s[20:23], s61 offen sc1\n"
"s_lshl_b32 s61, s42, 14\ns_add_u32 s61, s61, 84156416\nbuffer_load_dwordx4 v[136:139], v230, s[20:23], s61 offen sc1\ns_lshl_b32 s61, s42, 14\ns_add_u32 s61, s61, 84160512\nbuffer_load_dwordx4 v[140:143], v230, s[20:23], s61 offen sc1\n"
"s_waitcnt vmcnt(2)\nv_min3_u32 v232, v129, v131, v133\ns_waitcnt vmcnt(1)\nv_min3_u32 v232, v232, v135, v137\ns_waitcnt vmcnt(0)\nv_min3_u32 v232, v232, v139, v141\n"
"v_min3_u32 v232, v232, v143, v143\nv_max3_u32 v233, v129, v131, v133\nv_max3_u32 v233, v233, v135, v137\nv_max3_u32 v233, v233, v139, v141\nv_max3_u32 v233, v233, v143, v143\nv_cmp_ne_u32 s[86:87], s46, v232\n"
"v_cmp_ne_u32 s[88:89], s46, v233\ns_nop 0\ns_or_b64 s[86:87], s[86:87], s[88:89]\ns_cbranch_scc0 Lq117\ns_sub_u32 s48, s48, 1\ns_cmp_gt_i32 s48, 0\n"
"s_cbranch_scc1 Lq116\ns_branch Lq117\nLq126:\ns_lshl_b32 s61, s42, 14\ns_add_u32 s61, s61, 84410368\nbuffer_load_dwordx4 v[144:147], v234, s[20:23], s61 offen sc1\n"
"s_lshl_b32 s61, s42, 14\ns_add_u32 s61, s61, 84414464\nbuffer_load_dwordx4 v[148:151], v234, s[20:23], s61 offen sc1\ns_lshl_b32 s61, s42, 14\ns_add_u32 s61, s61, 84418560\nbuffer_load_dwordx4 v[152:155], v234, s[20:23], s61 offen sc1\n"
"s_lshl_b32 s61, s42, 14\ns_add_u32 s61, s61, 84422656\nbuffer_load_dwordx4 v[156:159], v234, s[20:23], s61 offen sc1\ns_waitcnt vmcnt(2)\nv_min3_u32 v236, v145, v147, v149\ns_waitcnt vmcnt(1)\n"
"v_min3_u32 v236, v236, v151, v153\ns_waitcnt vmcnt(0)\nv_min3_u32 v236, v236, v155, v157\nv_min3_u32 v236, v236, v159, v159\nv_max3_u32 v237, v145, v147, v149\nv_max3_u32 v237, v237, v151, v153\n"
"v_max3_u32 v237, v237, v155, v157\nv_max3_u32 v237, v237, v159, v159\nv_cmp_ne_u32 s[86:87], s46, v236\nv_cmp_ne_u32 s[88:89], s46, v237\ns_nop 0\ns_or_b64 s[86:87], s[86:87], s[88:89]\n"
"s_cbranch_scc0 Lq127\ns_sub_u32 s48, s48, 1\ns_cmp_gt_i32 s48, 0\ns_cbranch_scc1 Lq126\ns_branch Lq127\nLq136:\n"
"s_lshl_b32 s61, s42, 14\ns_add_u32 s61, s61, 83886080\nbuffer_load_dwordx4 v[128:131], v230, s[20:23], s61 offen sc1\ns_lshl_b32 s61, s42, 14\ns_add_u32 s61, s61, 83890176\nbuffer_load_dwordx4 v[132:135], v230, s[20:23], s61 offen sc1\n"
"s_lshl_b32 s61, s42, 14\ns_add_u32 s61, s61, 83894272\nbuffer_load_dwordx4 v[136:139], v230, s[20:23], s61 offen sc1\ns_lshl_b32 s61, s42, 14\ns_add_u32 s61, s61, 83898368\nbuffer_load_dwordx4 v[140:143], v230, s[20:23], s61 offen sc1\n"
"s_waitcnt vmcnt(2)\nv_min3_u32 v232, v129, v131, v133\ns_waitcnt vmcnt(1)\nv_min3_u32 v232, v232, v135, v137\ns_waitcnt vmcnt(0)\nv_min3_u32 v232, v232, v139, v141\n"
"v_min3_u32 v232, v232, v143, v143\nv_max3_u32 v233, v129, v131, v133\nv_max3_u32 v233, v233, v135, v137\nv_max3_u32 v233, v233, v139, v141\nv_max3_u32 v233, v233, v143, v143\nv_cmp_ne_u32 s[86:87], s46, v232\n"
"v_cmp_ne_u32 s[88:89], s46, v233\ns_nop 0\ns_or_b64 s[86:87], s[86:87], s[88:89]\ns_cbranch_scc0 Lq137\ns_sub_u32 s48, s48, 1\ns_cmp_gt_i32 s48, 0\n"
"s_cbranch_scc1 Lq136\ns_branch Lq137\nLq151:\ns_lshr_b32 s84, s49, 3\ns_add_u32 s84, s84, 81788928\nbuffer_load_dwordx4 v[128:131], v252, s[20:23], s84 offen offset:0 sc1\n"
"buffer_load_dwordx4 v[132:135], v252, s[20:23], s84 offen offset:1024 sc1\nbuffer_load_dwordx4 v[136:139], v252, s[20:23], s84 offen offset:2048 sc1\nbuffer_load_dwordx4 v[140:143], v252, s[20:23], s84 offen offset:3072 sc1\ns_lshr_b32 s85, s49, 3\ns_add_u32 s85, s85, 81793024\nbuffer_load_dwordx4 v[144:147], v252, s[20:23], s85 offen offset:0 sc1\n"
"buffer_load_dwordx4 v[148:151], v252, s[20:23], s85 offen offset:1024 sc1\nbuffer_load_dwordx4 v[152:155], v252, s[20:23], s85 offen offset:2048 sc1\nbuffer_load_dwordx4 v[156:159], v252, s[20:23], s85 offen offset:3072 sc1\ns_waitcnt vmcnt(6)\nv_min3_u32 v230, v129, v131, v133\ns_waitcnt vmcnt(5)\n"
"v_min3_u32 v230, v230, v135, v137\ns_waitcnt vmcnt(4)\nv_min3_u32 v230, v230, v139, v141\ns_waitcnt vmcnt(3)\nv_min3_u32 v230, v230, v143, v145\ns_waitcnt vmcnt(2)\n"
"v_min3_u32 v230, v230, v147, v149\ns_waitcnt vmcnt(1)\nv_min3_u32 v230, v230, v151, v153\ns_waitcnt vmcnt(0)\nv_min3_u32 v230, v230, v155, v157\nv_min3_u32 v230, v230, v159, v159\n"
"v_max3_u32 v231, v129, v131, v133\nv_max3_u32 v231, v231, v135, v137\nv_max3_u32 v231, v231, v139, v141\nv_max3_u32 v231, v231, v143, v145\nv_max3_u32 v231, v231, v147, v149\nv_max3_u32 v231, v231, v151, v153\n"
"v_max3_u32 v231, v231, v155, v157\nv_max3_u32 v231, v231, v159, v159\nv_cmp_ne_u32 s[86:87], s46, v230\nv_cmp_ne_u32 s[88:89], s46, v231\ns_nop 0\ns_or_b64 s[86:87], s[86:87], s[88:89]\n"
"s_cbranch_scc0 Lq152\ns_sub_u32 s48, s48, 1\ns_cmp_gt_i32 s48, 0\ns_cbranch_scc1 Lq151\ns_branch Lq152\nLq175:\n"
"s_mov_b64 exec, s[86:87]\nbuffer_load_dwordx4 v[230:233], v252, s[20:23], s65 offen offset:0 sc1\nbuffer_load_dwordx4 v[234:237], v252, s[20:23], s65 offen offset:1024 sc1\nbuffer_load_dwordx2 v[64:65], v227, s[20:23], s65 offen offset:2048 sc1\nbuffer_load_dwordx2 v[70:71], v227, s[20:23], s65 offen offset:2560 sc1\nbuffer_load_dwordx2 v[76:77], v227, s[20:23], s65 offen offset:3072 sc1\n"
"buffer_load_dwordx2 v[82:83], v227, s[20:23], s65 offen offset:3584 sc1\ns_mov_b64 exec, -1\ns_waitcnt vmcnt(0)\nv_max3_u32 v248, v233, v237, v65\nv_max3_u32 v248, v248, v71, v77\nv_max_u32 v248, v248, v83\n"
"v_cmp_eq_u32 vcc, -1, v248\ns_nop 1\ns_cbranch_vccz Lq176\ns_sub_u32 s48, s48, 1\ns_cmp_gt_i32 s48, 0\ns_cbranch_scc1 Lq175\n"
"s_branch Lq176\nLq204:\nv_mov_b32 v248, 0\nv_mov_b32 v249, 0\ns_add_u32 s61, s80, 88080384\ns_mov_b64 exec, 0xffff\n"
"buffer_atomic_or_x2 v[248:249], v250, s[20:23], s61 offen sc0 sc1\ns_mov_b64 exec, -1\ns_waitcnt vmcnt(0)\nv_cmp_eq_u64 s[86:87], s[46:47], v[248:249]\ns_nop 1\ns_and_b32 s62, s86, 0xffff\n"
"s_cmp_eq_u32 s62, 0xffff\ns_cbranch_scc1 Lq205\ns_sleep 1\ns_sub_u32 s48, s48, 1\ns_cmp_gt_i32 s48, 0\ns_cbranch_scc1 Lq204\n"
"s_branch Lq205\nLq263:\nv_mov_b32 v248, 0\nv_mov_b32 v249, 0\ns_add_u32 s61, s80, 88219648\ns_mov_b64 exec, 0xffff\n"
"buffer_atomic_or_x2 v[248:249], v250, s[20:23], s61 offen sc0 sc1\ns_mov_b64 exec, -1\ns_waitcnt vmcnt(0)\nv_cmp_eq_u64 s[86:87], s[46:47], v[248:249]\ns_nop 1\ns_and_b32 s62, s86, 0xffff\n"
"s_cmp_eq_u32 s62, 0xffff\ns_cbranch_scc1 Lq264\ns_sleep 1\ns_sub_u32 s48, s48, 1\ns_cmp_gt_i32 s48, 0\ns_cbranch_scc1 Lq263\n"
"s_branch Lq264\nLq324:\ns_lshr_b32 s84, s49, 3\ns_add_u32 s84, s84, 82313216\nbuffer_load_dwordx4 v[0:3], v252, s[20:23], s84 offen offset:0 sc1\nbuffer_load_dwordx4 v[4:7], v252, s[20:23], s84 offen offset:1024 sc1\n"
"buffer_load_dwordx4 v[8:11], v252, s[20:23], s84 offen offset:2048 sc1\nbuffer_load_dwordx4 v[12:15], v252, s[20:23], s84 offen offset:3072 sc1\ns_lshr_b32 s85, s49, 3\ns_add_u32 s85, s85, 82317312\nbuffer_load_dwordx4 v[16:19], v252, s[20:23], s85 offen offset:0 sc1\nbuffer_load_dwordx4 v[20:23], v252, s[20:23], s85 offen offset:1024 sc1\n"
"buffer_load_dwordx4 v[24:27], v252, s[20:23], s85 offen offset:2048 sc1\nbuffer_load_dwordx4 v[28:31], v252, s[20:23], s85 offen offset:3072 sc1\ns_waitcnt vmcnt(6)\nv_min3_u32 v230, v1, v3, v5\ns_waitcnt vmcnt(5)\nv_min3_u32 v230, v230, v7, v9\n"
"s_waitcnt vmcnt(4)\nv_min3_u32 v230, v230, v11, v13\ns_waitcnt vmcnt(3)\nv_min3_u32 v230, v230, v15, v17\ns_waitcnt vmcnt(2)\nv_min3_u32 v230, v230, v19, v21\n"
"s_waitcnt vmcnt(1)\nv_min3_u32 v230, v230, v23, v25\ns_waitcnt vmcnt(0)\nv_min3_u32 v230, v230, v27, v29\nv_min3_u32 v230, v230, v31, v31\nv_max3_u32 v231, v1, v3, v5\n"
"v_max3_u32 v231, v231, v7, v9\nv_max3_u32 v231, v231, v11, v13\nv_max3_u32 v231, v231, v15, v17\nv_max3_u32 v231, v231, v19, v21\nv_max3_u32 v231, v231, v23, v25\nv_max3_u32 v231, v231, v27, v29\n"
"v_max3_u32 v231, v231, v31, v31\nv_cmp_ne_u32 s[86:87], s46, v230\nv_cmp_ne_u32 s[88:89], s46, v231\ns_nop 0\ns_or_b64 s[86:87], s[86:87], s[88:89]\ns_cbranch_scc0 Lq325\n"
"s_sub_u32 s48, s48, 1\ns_cmp_gt_i32 s48, 0\ns_cbranch_scc1 Lq324\ns_branch Lq325\nLq326:\ns_lshl_b32 s61, s42, 14\n"
"s_add_u32 s61, s61, 84672512\nbuffer_load_dwordx4 v[0:3], v238, s[20:23], s61 offen sc1\ns_lshl_b32 s61, s42, 14\ns_add_u32 s61, s61, 84676608\nbuffer_load_dwordx4 v[4:7], v238, s[20:23], s61 offen sc1\ns_lshl_b32 s61, s42, 14\n"
"s_add_u32 s61, s61, 84680704\nbuffer_load_dwordx4 v[8:11], v238, s[20:23], s61 offen sc1\ns_lshl_b32 s61, s42, 14\ns_add_u32 s61, s61, 84684800\nbuffer_load_dwordx4 v[12:15], v238, s[20:23], s61 offen sc1\ns_waitcnt vmcnt(2)\n"
"v_min3_u32 v240, v1, v3, v5\ns_waitcnt vmcnt(1)\nv_min3_u32 v240, v240, v7, v9\ns_waitcnt vmcnt(0)\nv_min3_u32 v240, v240, v11, v13\nv_min3_u32 v240, v240, v15, v15\n"
"v_max3_u32 v241, v1, v3, v5\nv_max3_u32 v241, v241, v7, v9\nv_max3_u32 v241, v241, v11, v13\nv_max3_u32 v241, v241, v15, v15\nv_cmp_ne_u32 s[86:87], s46, v240\nv_cmp_ne_u32 s[88:89], s46, v241\n"
"s_nop 0\ns_or_b64 s[86:87], s[86:87], s[88:89]\ns_cbranch_scc0 Lq327\ns_sub_u32 s48, s48, 1\ns_cmp_gt_i32 s48, 0\ns_cbranch_scc1 Lq326\n"
"s_branch Lq327\nLq338:\ns_lshl_b32 s61, s42, 14\ns_add_u32 s61, s61, 84934656\nbuffer_load_dwordx4 v[16:19], v242, s[20:23], s61 offen sc1\ns_lshl_b32 s61, s42, 14\n"
"s_add_u32 s61, s61, 84938752\nbuffer_load_dwordx4 v[20:23], v242, s[20:23], s61 offen sc1\ns_lshl_b32 s61, s42, 14\ns_add_u32 s61, s61, 84942848\nbuffer_load_dwordx4 v[24:27], v242, s[20:23], s61 offen sc1\ns_lshl_b32 s61, s42, 14\n"
"s_add_u32 s61, s61, 84946944\nbuffer_load_dwordx4 v[28:31], v242, s[20:23], s61 offen sc1\ns_waitcnt vmcnt(2)\nv_min3_u32 v244, v17, v19, v21\ns_waitcnt vmcnt(1)\nv_min3_u32 v244, v244, v23, v25\n"
"s_waitcnt vmcnt(0)\nv_min3_u32 v244, v244, v27, v29\nv_min3_u32 v244, v244, v31, v31\nv_max3_u32 v245, v17, v19, v21\nv_max3_u32 v245, v245, v23, v25\nv_max3_u32 v245, v245, v27, v29\n"
"v_max3_u32 v245, v245, v31, v31\nv_cmp_ne_u32 s[86:87], s46, v244\nv_cmp_ne_u32 s[88:89], s46, v245\ns_nop 0\ns_or_b64 s[86:87], s[86:87], s[88:89]\ns_cbranch_scc0 Lq339\n"
"s_sub_u32 s48, s48, 1\ns_cmp_gt_i32 s48, 0\ns_cbranch_scc1 Lq338\ns_branch Lq339\nLq350:\ns_lshl_b32 s61, s42, 14\n"
"s_add_u32 s61, s61, 85196800\nbuffer_load_dwordx4 v[0:3], v238, s[20:23], s61 offen sc1\ns_lshl_b32 s61, s42, 14\ns_add_u32 s61, s61, 85200896\nbuffer_load_dwordx4 v[4:7], v238, s[20:23], s61 offen sc1\ns_lshl_b32 s61, s42, 14\n"
"s_add_u32 s61, s61, 85204992\nbuffer_load_dwordx4 v[8:11], v238, s[20:23], s61 offen sc1\ns_lshl_b32 s61, s42, 14\ns_add_u32 s61, s61, 85209088\nbuffer_load_dwordx4 v[12:15], v238, s[20:23], s61 offen sc1\ns_waitcnt vmcnt(2)\n"
"v_min3_u32 v240, v1, v3, v5\ns_waitcnt vmcnt(1)\nv_min3_u32 v240, v240, v7, v9\ns_waitcnt vmcnt(0)\nv_min3_u32 v240, v240, v11, v13\nv_min3_u32 v240, v240, v15, v15\n"
"v_max3_u32 v241, v1, v3, v5\nv_max3_u32 v241, v241, v7, v9\nv_max3_u32 v241, v241, v11, v13\nv_max3_u32 v241, v241, v15, v15\nv_cmp_ne_u32 s[86:87], s46, v240\nv_cmp_ne_u32 s[88:89], s46, v241\n"
"s_nop 0\ns_or_b64 s[86:87], s[86:87], s[88:89]\ns_cbranch_scc0 Lq351\ns_sub_u32 s48, s48, 1\ns_cmp_gt_i32 s48, 0\ns_cbranch_scc1 Lq350\n"
"s_branch Lq351\nLq362:\ns_lshl_b32 s61, s42, 14\ns_add_u32 s61, s61, 85458944\nbuffer_load_dwordx4 v[16:19], v242, s[20:23], s61 offen sc1\ns_lshl_b32 s61, s42, 14\n"
"s_add_u32 s61, s61, 85463040\nbuffer_load_dwordx4 v[20:23], v242, s[20:23], s61 offen sc1\ns_lshl_b32 s61, s42, 14\ns_add_u32 s61, s61, 85467136\nbuffer_load_dwordx4 v[24:27], v242, s[20:23], s61 offen sc1\ns_lshl_b32 s61, s42, 14\n"
"s_add_u32 s61, s61, 85471232\nbuffer_load_dwordx4 v[28:31], v242, s[20:23], s61 offen sc1\ns_waitcnt vmcnt(2)\nv_min3_u32 v244, v17, v19, v21\ns_waitcnt vmcnt(1)\nv_min3_u32 v244, v244, v23, v25\n"
"s_waitcnt vmcnt(0)\nv_min3_u32 v244, v244, v27, v29\nv_min3_u32 v244, v244, v31, v31\nv_max3_u32 v245, v17, v19, v21\nv_max3_u32 v245, v245, v23, v25\nv_max3_u32 v245, v245, v27, v29\n"
"v_max3_u32 v245, v245, v31, v31\nv_cmp_ne_u32 s[86:87], s46, v244\nv_cmp_ne_u32 s[88:89], s46, v245\ns_nop 0\ns_or_b64 s[86:87], s[86:87], s[88:89]\ns_cbranch_scc0 Lq363\n"
"s_sub_u32 s48, s48, 1\ns_cmp_gt_i32 s48, 0\ns_cbranch_scc1 Lq362\ns_branch Lq363\nLq398:\nv_mov_b32 v248, 0\n"
"v_mov_b32 v249, 0\ns_add_u32 s61, s82, 385024\ns_mov_b64 exec, 0xf\nbuffer_atomic_or_x2 v[248:249], v250, s[20:23], s61 offen sc0 sc1\ns_mov_b64 exec, -1\ns_waitcnt vmcnt(0)\n"
"v_cmp_eq_u64 s[86:87], s[46:47], v[248:249]\ns_nop 1\ns_and_b32 s62, s86, 0xf\ns_cmp_eq_u32 s62, 0xf\ns_cbranch_scc1 Lq399\ns_sleep 1\n"
"s_sub_u32 s48, s48, 1\ns_cmp_gt_i32 s48, 0\ns_cbranch_scc1 Lq398\ns_branch Lq399\nLq433:\nv_mov_b32 v248, 0\n"
"v_mov_b32 v249, 0\ns_add_u32 s61, s82, 385032\ns_mov_b64 exec, 0xf\nbuffer_atomic_or_x2 v[248:249], v250, s[20:23], s61 offen sc0 sc1\ns_mov_b64 exec, -1\ns_waitcnt vmcnt(0)\n"
"v_cmp_eq_u64 s[86:87], s[46:47], v[248:249]\ns_nop 1\ns_and_b32 s62, s86, 0xf\ns_cmp_eq_u32 s62, 0xf\ns_cbranch_scc1 Lq434\ns_sleep 1\n"
"s_sub_u32 s48, s48, 1\ns_cmp_gt_i32 s48, 0\ns_cbranch_scc1 Lq433\ns_branch Lq434\nLq468:\nv_mov_b32 v248, 0\n"
"v_mov_b32 v249, 0\ns_add_u32 s61, s82, 385048\ns_mov_b64 exec, 0xf\nbuffer_atomic_or_x2 v[248:249], v250, s[20:23], s61 offen sc0 sc1\ns_mov_b64 exec, -1\ns_waitcnt vmcnt(0)\n"
"v_cmp_eq_u64 s[86:87], s[46:47], v[248:249]\ns_nop 1\ns_and_b32 s62, s86, 0xf\ns_cmp_eq_u32 s62, 0xf\ns_cbranch_scc1 Lq469\ns_sleep 1\n"
"s_sub_u32 s48, s48, 1\ns_cmp_gt_i32 s48, 0\ns_cbranch_scc1 Lq468\ns_branch Lq469\nLq487:\nv_mov_b32 v248, 0\n"
"v_mov_b32 v249, 0\ns_add_u32 s61, s82, 385040\ns_mov_b64 exec, 0xf\nbuffer_atomic_or_x2 v[248:249], v250, s[20:23], s61 offen sc0 sc1\ns_mov_b64 exec, -1\ns_waitcnt vmcnt(0)\n"
"v_cmp_eq_u64 s[86:87], s[46:47], v[248:249]\ns_nop 1\ns_and_b32 s62, s86, 0xf\ns_cmp_eq_u32 s62, 0xf\ns_cbranch_scc1 Lq488\ns_sleep 1\n"
"s_sub_u32 s48, s48, 1\ns_cmp_gt_i32 s48, 0\ns_cbranch_scc1 Lq487\ns_branch Lq488\nLq530:\nv_mov_b32 v248, 0\n"
"v_mov_b32 v249, 0\ns_add_u32 s61, s74, 548864\ns_mov_b64 exec, 3\nbuffer_atomic_or_x2 v[248:249], v250, s[20:23], s61 offen sc0 sc1\ns_mov_b64 exec, -1\ns_waitcnt vmcnt(0)\n"
"v_cmp_eq_u64 s[86:87], s[46:47], v[248:249]\ns_nop 1\ns_and_b32 s62, s86, 3\ns_cmp_eq_u32 s62, 3\ns_cbranch_scc1 Lq531\ns_sleep 1\n"
"s_sub_u32 s48, s48, 1\ns_cmp_gt_i32 s48, 0\ns_cbranch_scc1 Lq530\ns_branch Lq531\nLq553:\ns_mul_i32 s66, s43, 49152\n"
"s_add_u32 s66, s66, s72\ns_add_u32 s66, s66, 52461568\nbuffer_load_dwordx4 v[64:67], v252, s[20:23], s66 offen offset:0 sc1\nbuffer_load_dwordx4 v[68:71], v252, s[20:23], s66 offen offset:1024 sc1\nbuffer_load_dwordx4 v[72:75], v252, s[20:23], s66 offen offset:2048 sc1\nbuffer_load_dwordx4 v[76:79], v252, s[20:23], s66 offen offset:3072 sc1\n"
"s_waitcnt vmcnt(0)\nv_max3_u32 v248, v67, v71, v75\nv_max_u32 v248, v248, v79\nv_cmp_eq_u32 vcc, -1, v248\ns_nop 1\ns_cbranch_vccz Lq554\n"
"s_sub_u32 s48, s48, 1\ns_cmp_gt_i32 s48, 0\ns_cbranch_scc1 Lq553\ns_branch Lq554\n.p2align 6\nLqM:\n"
"Lqq1:\nv_bfe_u32 v250, v252, 7, 1\nv_mul_u32_u24 v250, 2176, v250\nv_bfe_u32 v251, v252, 4, 3\nv_lshl_add_u32 v250, v251, 3, v250\ns_lshl_b32 s60, s90, 6\n"
"v_add_u32 v250, s60, v250\ns_add_u32 m0, s50, 90112\ns_add_u32 s63, s52, 397312\nbuffer_load_dwordx4 v220, s[4:7], s63 offen lds\nbuffer_load_dwordx4 v221, s[4:7], s63 offen offset:1024 lds\ns_add_u32 m0, s50, 98304\n"
"s_add_u32 s63, s52, 593920\nbuffer_load_dwordx4 v220, s[4:7], s63 offen lds\nbuffer_load_dwordx4 v221, s[4:7], s63 offen offset:1024 lds\ns_add_u32 m0, s50, 106496\ns_add_u32 s63, s52, 788480\nbuffer_load_dwordx4 v220, s[4:7], s63 offen lds\n"
"buffer_load_dwordx4 v221, s[4:7], s63 offen offset:1024 lds\ns_add_u32 m0, s50, 114688\ns_add_u32 s63, s52, 985088\nbuffer_load_dwordx4 v220, s[4:7], s63 offen lds\nbuffer_load_dwordx4 v221, s[4:7], s63 offen offset:1024 lds\ns_add_u32 m0, s50, 0\n"
"s_add_u32 s63, s52, 790528\nbuffer_load_dwordx4 v220, s[4:7], s63 offen lds\nbuffer_load_dwordx4 v221, s[4:7], s63 offen offset:1024 lds\ns_add_u32 m0, s50, 8192\ns_add_u32 s63, s52, 987136\nbuffer_load_dwordx4 v220, s[4:7], s63 offen lds\n"
"buffer_load_dwordx4 v221, s[4:7], s63 offen offset:1024 lds\nv_mov_b32 v248, 0\nv_mov_b32 v249, 0\ns_add_u32 s61, s80, 88358912\ns_mov_b64 exec, 0xffff\nbuffer_atomic_or_x2 v[248:249], v250, s[20:23], s61 offen sc0 sc1\n"
"s_mov_b64 exec, -1\nv_mov_b32 v230, 0\nv_mov_b32 v231, 0\nv_mov_b32 v232, 0\nv_mov_b32 v233, 0\nv_dot2c_f32_bf16 v230, s76, v192\n"
"v_dot2c_f32_bf16 v231, v192, v192\nv_dot2c_f32_bf16 v232, s76, v200\nv_dot2c_f32_bf16 v233, v200, v200\nv_dot2c_f32_bf16 v230, s76, v193\nv_dot2c_f32_bf16 v231, v193, v193\nv_dot2c_f32_bf16 v232, s76, v201\n"
"v_dot2c_f32_bf16 v233, v201, v201\nv_dot2c_f32_bf16 v230, s76, v194\nv_dot2c_f32_bf16 v231, v194, v194\nv_dot2c_f32_bf16 v232, s76, v202\nv_dot2c_f32_bf16 v233, v202, v202\nv_dot2c_f32_bf16 v230, s76, v195\n"
"v_dot2c_f32_bf16 v231, v195, v195\nv_dot2c_f32_bf16 v232, s76, v203\nv_dot2c_f32_bf16 v233, v203, v203\nv_dot2c_f32_bf16 v230, s76, v196\nv_dot2c_f32_bf16 v231, v196, v196\nv_dot2c_f32_bf16 v232, s76, v204\n"
"v_dot2c_f32_bf16 v233, v204, v204\nv_dot2c_f32_bf16 v230, s76, v197\nv_dot2c_f32_bf16 v231, v197, v197\nv_dot2c_f32_bf16 v232, s76, v205\nv_dot2c_f32_bf16 v233, v205, v205\nv_dot2c_f32_bf16 v230, s76, v198\n"
"v_dot2c_f32_bf16 v231, v198, v198\nv_dot2c_f32_bf16 v232, s76, v206\nv_dot2c_f32_bf16 v233, v206, v206\nv_dot2c_f32_bf16 v230, s76, v199\nv_dot2c_f32_bf16 v231, v199, v199\nv_dot2c_f32_bf16 v232, s76, v207\n"
"v_dot2c_f32_bf16 v233, v207, v207\nv_lshrrev_b32 v234, 1, v252\nv_permlane32_swap_b32 v230, v231\ns_nop 0\nv_permlane32_swap_b32 v232, v233\nv_add_f32 v230, v230, v231\n"
"v_add_f32 v232, v232, v233\nv_mov_b32 v231, s46\ns_nop 0\nv_permlane16_swap_b32 v230, v232\nv_add_f32 v230, v230, v232\ns_lshr_b32 s61, s49, 3\n"
"s_lshl2_add_u32 s61, s53, s61\ns_add_u32 s61, s61, 82837504\nbuffer_store_dwordx2 v[230:231], v234, s[20:23], s61 offen sc1\ns_waitcnt vmcnt(1)\nv_cmp_eq_u64 s[86:87], s[46:47], v[248:249]\ns_nop 0\n"
"s_and_b32 s62, s86, 0xffff\ns_cmp_eq_u32 s62, 0xffff\ns_cbranch_scc0 Lq610\nLq611:\ns_add_u32 s64, s70, 18874368\nbuffer_load_dwordx4 a[0:3], v252, s[20:23], s64 offen offset:0 sc1\n"
"s_add_u32 s65, s70, 18907136\nbuffer_load_dwordx4 a[4:7], v252, s[20:23], s65 offen offset:0 sc1\ns_add_u32 s66, s71, 18874368\nbuffer_load_dwordx4 a[8:11], v252, s[20:23], s66 offen offset:0 sc1\ns_add_u32 s67, s71, 18907136\nbuffer_load_dwordx4 a[12:15], v252, s[20:23], s67 offen offset:0 sc1\n"
"buffer_load_dwordx4 a[16:19], v252, s[20:23], s64 offen offset:1024 sc1\nbuffer_load_dwordx4 a[20:23], v252, s[20:23], s65 offen offset:1024 sc1\nbuffer_load_dwordx4 a[24:27], v252, s[20:23], s66 offen offset:1024 sc1\nbuffer_load_dwordx4 a[28:31], v252, s[20:23], s67 offen offset:1024 sc1\ns_barrier\nR 160,184,40960\n"
"R 162,184,45056\nR 164,184,49152\nR 166,184,53248\nR 168,185,40960\nR 170,185,45056\nR 172,185,49152\n"
"R 174,185,53248\nR 176,186,40960\nR 178,186,45056\nR 180,186,49152\nR 182,186,53248\nbuffer_load_dwordx4 a[32:35], v252, s[20:23], s64 offen offset:2048 sc1\n"
"buffer_load_dwordx4 a[36:39], v252, s[20:23], s65 offen offset:2048 sc1\nbuffer_load_dwordx4 a[40:43], v252, s[20:23], s66 offen offset:2048 sc1\nbuffer_load_dwordx4 a[44:47], v252, s[20:23], s67 offen offset:2048 sc1\nbuffer_load_dwordx4 a[48:51], v252, s[20:23], s64 offen offset:3072 sc1\nbuffer_load_dwordx4 a[52:55], v252, s[20:23], s65 offen offset:3072 sc1\nbuffer_load_dwordx4 a[56:59], v252, s[20:23], s66 offen offset:3072 sc1\n"
"buffer_load_dwordx4 a[60:63], v252, s[20:23], s67 offen offset:3072 sc1\ns_waitcnt vmcnt(15) lgkmcnt(10)\nv_mfma_f32_16x16x32_bf16 v[0:3], v[160:163], a[0:3], 0\ns_add_u32 s64, s70, 18878464\nbuffer_load_dwordx4 a[64:67], v252, s[20:23], s64 offen offset:0 sc1\ns_waitcnt vmcnt(12) lgkmcnt(8)\n"
"M 0,164,16\nv_mfma_f32_16x16x32_bf16 v[16:19], v[160:163], a[4:7], 0\ns_waitcnt vmcnt(11)\nM 16,164,20\nv_mfma_f32_16x16x32_bf16 v[32:35], v[160:163], a[8:11], 0\ns_waitcnt vmcnt(10)\n"
"M 32,164,24\nv_mfma_f32_16x16x32_bf16 v[48:51], v[160:163], a[12:15], 0\ns_add_u32 s65, s70, 18911232\nbuffer_load_dwordx4 a[68:71], v252, s[20:23], s65 offen offset:0 sc1\ns_waitcnt vmcnt(10)\nM 48,164,28\n"
"G 4\nv_mfma_f32_16x16x32_bf16 v[4:7], v[168:171], a[0:3], 0\nR 160,187,40960\nM 4,172,16\nv_mfma_f32_16x16x32_bf16 v[20:23], v[168:171], a[4:7], 0\nR 162,187,45056\n"
"M 20,172,20\nv_mfma_f32_16x16x32_bf16 v[36:39], v[168:171], a[8:11], 0\nR 164,187,49152\nM 36,172,24\ns_add_u32 s66, s71, 18878464\nbuffer_load_dwordx4 a[72:75], v252, s[20:23], s66 offen offset:0 sc1\n"
"v_mfma_f32_16x16x32_bf16 v[52:55], v[168:171], a[12:15], 0\nR 166,187,53248\nM 52,172,28\nG 4\nv_mfma_f32_16x16x32_bf16 v[8:11], v[176:179], a[0:3], 0\nR 168,184,57344\n"
"M 8,180,16\nv_mfma_f32_16x16x32_bf16 v[24:27], v[176:179], a[4:7], 0\nR 170,184,61440\nM 24,180,20\ns_add_u32 m0, s50, 16384\nv_mfma_f32_16x16x32_bf16 v[40:43], v[176:179], a[8:11], 0\n"
"R 172,188,0\ns_add_u32 s63, s52, 1181696\nM 40,180,24\ns_add_u32 s67, s71, 18911232\nbuffer_load_dwordx4 a[76:79], v252, s[20:23], s67 offen offset:0 sc1\nv_mfma_f32_16x16x32_bf16 v[56:59], v[176:179], a[12:15], 0\n"
"R 174,188,4096\nM 56,180,28\nG 4\nv_mfma_f32_16x16x32_bf16 v[12:15], v[160:163], a[0:3], 0\nR 176,185,57344\nM 12,164,16\n"
"buffer_load_dwordx4 v220, s[4:7], s63 offen lds\nv_mfma_f32_16x16x32_bf16 v[28:31], v[160:163], a[4:7], 0\nR 178,185,61440\nM 28,164,20\nbuffer_load_dwordx4 a[80:83], v252, s[20:23], s64 offen offset:1024 sc1\nv_mfma_f32_16x16x32_bf16 v[44:47], v[160:163], a[8:11], 0\n"
"R 180,189,0\nM 44,164,24\nv_mfma_f32_16x16x32_bf16 v[60:63], v[160:163], a[12:15], 0\nR 182,189,4096\nM 60,164,28\nG 4\n"
"v_mfma_f32_16x16x32_bf16 v[64:67], a[0:3], v[168:171], 0\nR 160,186,57344\nN 64,16,172\nbuffer_load_dwordx4 a[84:87], v252, s[20:23], s65 offen offset:1024 sc1\nv_mfma_f32_16x16x32_bf16 v[68:71], a[4:7], v[168:171], 0\nR 162,186,61440\n"
"N 68,20,172\nv_mfma_f32_16x16x32_bf16 v[96:99], a[8:11], v[168:171], 0\nR 164,190,0\nN 96,24,172\nv_mfma_f32_16x16x32_bf16 v[100:103], a[12:15], v[168:171], 0\nR 166,190,4096\n"
"N 100,28,172\ns_barrier\nG 4\nv_mfma_f32_16x16x32_bf16 v[72:75], a[0:3], v[176:179], 0\nR 168,187,57344\nN 72,16,180\n"
"buffer_load_dwordx4 a[88:91], v252, s[20:23], s66 offen offset:1024 sc1\nv_mfma_f32_16x16x32_bf16 v[76:79], a[4:7], v[176:179], 0\nR 170,187,61440\nN 76,20,180\nv_mfma_f32_16x16x32_bf16 v[104:107], a[8:11], v[176:179], 0\nR 172,191,0\n"
"N 104,24,180\nbuffer_load_dwordx4 v221, s[4:7], s63 offen offset:1024 lds\nv_mfma_f32_16x16x32_bf16 v[108:111], a[12:15], v[176:179], 0\nR 174,191,4096\nN 108,28,180\nG 4\n"
"v_mfma_f32_16x16x32_bf16 v[80:83], a[0:3], v[160:163], 0\nR 176,188,8192\ns_add_u32 s63, s52, 1378304\nN 80,16,164\nbuffer_load_dwordx4 a[92:95], v252, s[20:23], s67 offen offset:1024 sc1\nv_mfma_f32_16x16x32_bf16 v[84:87], a[4:7], v[160:163], 0\n"
"R 178,188,12288\nN 84,20,164\ns_add_u32 m0, s50, 24576\nv_mfma_f32_16x16x32_bf16 v[112:115], a[8:11], v[160:163], 0\nR 180,188,16384\nN 112,24,164\n"
"buffer_load_dwordx4 v220, s[4:7], s63 offen lds\nv_mfma_f32_16x16x32_bf16 v[116:119], a[12:15], v[160:163], 0\nR 182,188,20480\nN 116,28,164\nG 4\nv_mfma_f32_16x16x32_bf16 v[88:91], a[0:3], v[168:171], 0\n"
"R 160,189,8192\nN 88,16,172\nbuffer_load_dwordx4 a[96:99], v252, s[20:23], s64 offen offset:2048 sc1\nv_mfma_f32_16x16x32_bf16 v[92:95], a[4:7], v[168:171], 0\nR 162,189,12288\nN 92,20,172\n"
"v_mfma_f32_16x16x32_bf16 v[120:123], a[8:11], v[168:171], 0\nR 164,189,16384\nN 120,24,172\nbuffer_load_dwordx4 a[100:103], v252, s[20:23], s65 offen offset:2048 sc1\nv_mfma_f32_16x16x32_bf16 v[124:127], a[12:15], v[168:171], 0\nR 166,189,20480\n"
"N 124,28,172\nG 4\ns_waitcnt vmcnt(20)\nM 0,176,32\nR 168,190,8192\ns_waitcnt vmcnt(16)\n"
"M 0,180,48\nM 16,176,36\nR 170,190,12288\ns_waitcnt vmcnt(15)\nM 16,180,52\nbuffer_load_dwordx4 a[104:107], v252, s[20:23], s66 offen offset:2048 sc1\n"
"M 32,176,40\nR 172,190,16384\ns_waitcnt vmcnt(15)\nM 32,180,56\nM 48,176,44\nR 174,190,20480\n"
"s_waitcnt vmcnt(14)\nM 48,180,60\ns_barrier\nG 4\nM 4,160,32\nR 176,191,8192\n"
"M 4,164,48\nbuffer_load_dwordx4 v221, s[4:7], s63 offen offset:1024 lds\nM 20,160,36\nR 178,191,12288\nM 20,164,52\nbuffer_load_dwordx4 a[108:111], v252, s[20:23], s67 offen offset:2048 sc1\n"
"M 36,160,40\nR 180,191,16384\nM 36,164,56\nM 52,160,44\nR 182,191,20480\nM 52,164,60\n"
"G 4\nM 8,168,32\nR 160,188,24576\ns_add_u32 s63, s52, 1183744\nM 8,172,48\nbuffer_load_dwordx4 a[112:115], v252, s[20:23], s64 offen offset:3072 sc1\n"
"M 24,168,36\nR 162,188,28672\nM 24,172,52\ns_add_u32 m0, s50, 32768\nM 40,168,40\nR 164,188,32768\n"
"M 40,172,56\nbuffer_load_dwordx4 v220, s[4:7], s63 offen lds\nM 56,168,44\nR 166,188,36864\nM 56,172,60\nG 4\n"
"M 12,176,32\nR 168,189,24576\nM 12,180,48\nbuffer_load_dwordx4 a[116:119], v252, s[20:23], s65 offen offset:3072 sc1\nM 28,176,36\nR 170,189,28672\n"
"M 28,180,52\nM 44,176,40\nR 172,189,32768\nM 44,180,56\nM 60,176,44\nR 174,189,36864\n"
"M 60,180,60\nG 4\nN 64,32,160\nR 176,190,24576\nN 64,48,164\nbuffer_load_dwordx4 a[120:123], v252, s[20:23], s66 offen offset:3072 sc1\n"
"N 68,36,160\nR 178,190,28672\nN 68,52,164\nN 96,40,160\nR 180,190,32768\nN 96,56,164\n"
"buffer_load_dwordx4 a[124:127], v252, s[20:23], s67 offen offset:3072 sc1\nN 100,44,160\nR 182,190,36864\nN 100,60,164\ns_barrier\nG 4\n"
"N 72,32,168\nR 160,191,24576\nN 72,48,172\nbuffer_load_dwordx4 v221, s[4:7], s63 offen offset:1024 lds\nN 76,36,168\nR 162,191,28672\n"
"N 76,52,172\ns_add_u32 m0, s50, 40960\nN 104,40,168\nR 164,191,32768\ns_add_u32 s63, s52, 1380352\nN 104,56,172\n"
"s_add_u32 s64, s70, 18882560\nbuffer_load_dwordx4 a[128:131], v252, s[20:23], s64 offen offset:0 sc1\nN 108,44,168\nR 166,191,36864\nN 108,60,172\nG 4\n"
"N 80,32,176\nR 168,188,40960\nN 80,48,180\nbuffer_load_dwordx4 v220, s[4:7], s63 offen lds\nN 84,36,176\nR 170,188,45056\n"
"N 84,52,180\ns_add_u32 s65, s70, 18915328\nbuffer_load_dwordx4 a[132:135], v252, s[20:23], s65 offen offset:0 sc1\nN 112,40,176\nR 172,188,49152\nN 112,56,180\n"
"N 116,44,176\nR 174,188,53248\nN 116,60,180\nG 4\nN 88,32,160\nR 176,189,40960\n"
"N 88,48,164\ns_add_u32 s66, s71, 18882560\nbuffer_load_dwordx4 a[136:139], v252, s[20:23], s66 offen offset:0 sc1\nN 92,36,160\nR 178,189,45056\nN 92,52,164\n"
"N 120,40,160\nR 180,189,49152\nN 120,56,164\nN 124,44,160\nR 182,189,53248\nN 124,60,164\n"
"G 4\ns_waitcnt vmcnt(25)\nM 0,168,64\nR 160,190,40960\ns_waitcnt vmcnt(20)\nM 0,172,80\n"
"s_add_u32 s67, s71, 18915328\nbuffer_load_dwordx4 a[140:143], v252, s[20:23], s67 offen offset:0 sc1\nM 16,168,68\nR 162,190,45056\ns_waitcnt vmcnt(20)\nM 16,172,84\n"
"M 32,168,72\nR 164,190,49152\ns_waitcnt vmcnt(19)\nM 32,172,88\nbuffer_load_dwordx4 v221, s[4:7], s63 offen offset:1024 lds\nM 48,168,76\n"
"R 166,190,53248\ns_waitcnt vmcnt(18)\nM 48,172,92\ns_barrier\nG 4\nM 4,176,64\n"
"R 168,191,40960\nM 4,180,80\nbuffer_load_dwordx4 a[144:147], v252, s[20:23], s64 offen offset:1024 sc1\nM 20,176,68\nR 170,191,45056\nM 20,180,84\n"
"s_add_u32 m0, s50, 49152\nM 36,176,72\nR 172,191,49152\ns_add_u32 s63, s52, 1574912\nM 36,180,88\nbuffer_load_dwordx4 a[148:151], v252, s[20:23], s65 offen offset:1024 sc1\n"
"M 52,176,76\nR 174,191,53248\nM 52,180,92\nG 4\nM 8,160,64\nR 176,184,0\n"
"M 8,164,80\nbuffer_load_dwordx4 v220, s[4:7], s63 offen lds\nM 24,160,68\nR 178,184,4096\nM 24,164,84\nM 40,160,72\n"
"R 180,184,8192\nM 40,164,88\nbuffer_load_dwordx4 a[152:155], v252, s[20:23], s66 offen offset:1024 sc1\nM 56,160,76\nR 182,184,12288\nM 56,164,92\n"
"G 4\nM 12,168,64\nR 160,185,0\nM 12,172,80\nM 28,168,68\nR 162,185,4096\n"
"M 28,172,84\nbuffer_load_dwordx4 a[156:159], v252, s[20:23], s67 offen offset:1024 sc1\nM 44,168,72\nR 164,185,8192\nM 44,172,88\nM 60,168,76\n"
"R 166,185,12288\nM 60,172,92\nG 4\nN 64,64,176\nR 168,186,0\nN 64,80,180\n"
"buffer_load_dwordx4 v221, s[4:7], s63 offen offset:1024 lds\nN 68,68,176\nR 170,186,4096\nN 68,84,180\nbuffer_load_dwordx4 a[160:163], v252, s[20:23], s64 offen offset:2048 sc1\nN 96,72,176\n"
"R 172,186,8192\nN 96,88,180\nN 100,76,176\nR 174,186,12288\nN 100,92,180\ns_waitcnt vmcnt(20)\n"
"s_barrier\nG 4\nN 72,64,160\nR 176,187,0\ns_add_u32 s63, s52, 1771520\nN 72,80,164\n"
"buffer_load_dwordx4 a[164:167], v252, s[20:23], s65 offen offset:2048 sc1\nN 76,68,160\nR 178,187,4096\nN 76,84,164\ns_add_u32 m0, s50, 57344\nN 104,72,160\n"
"R 180,187,8192\nN 104,88,164\nbuffer_load_dwordx4 v220, s[4:7], s63 offen lds\nN 108,76,160\nR 182,187,12288\nN 108,92,164\n"
"G 4\nN 80,64,168\nR 160,184,16384\nN 80,80,172\nbuffer_load_dwordx4 a[168:171], v252, s[20:23], s66 offen offset:2048 sc1\nN 84,68,168\n"
"R 162,184,20480\nN 84,84,172\nN 112,72,168\nR 164,184,24576\nN 112,88,172\nbuffer_load_dwordx4 v221, s[4:7], s63 offen offset:1024 lds\n"
"N 116,76,168\nR 166,184,28672\nN 116,92,172\nG 4\nN 88,64,176\nR 168,185,16384\n"
"N 88,80,180\nbuffer_load_dwordx4 a[172:175], v252, s[20:23], s67 offen offset:2048 sc1\nN 92,68,176\nR 170,185,20480\nN 92,84,180\nN 120,72,176\n"
"R 172,185,24576\nN 120,88,180\nbuffer_load_dwordx4 a[176:179], v252, s[20:23], s64 offen offset:3072 sc1\nN 124,76,176\nR 174,185,28672\nN 124,92,180\n"
"G 4\nM 0,160,96\nR 176,186,16384\ns_waitcnt vmcnt(24)\nM 0,164,112\nM 16,160,100\n"
"R 178,186,20480\ns_waitcnt vmcnt(22)\nM 16,164,116\nbuffer_load_dwordx4 a[180:183], v252, s[20:23], s65 offen offset:3072 sc1\nM 32,160,104\nR 180,186,24576\n"
"s_add_u32 s63, s52, 1576960\ns_waitcnt vmcnt(22)\nM 32,164,120\ns_add_u32 m0, s50, 65536\nM 48,160,108\nR 182,186,28672\n"
"s_waitcnt vmcnt(21)\nM 48,164,124\ns_waitcnt vmcnt(14)\ns_barrier\nG 4\nM 4,168,96\n"
"R 160,187,16384\nM 4,172,112\nbuffer_load_dwordx4 v220, s[4:7], s63 offen lds\nM 20,168,100\nR 162,187,20480\nM 20,172,116\n"
"buffer_load_dwordx4 a[184:187], v252, s[20:23], s66 offen offset:3072 sc1\nM 36,168,104\nR 164,187,24576\nM 36,172,120\nbuffer_load_dwordx4 v221, s[4:7], s63 offen offset:1024 lds\nM 52,168,108\n"
"R 166,187,28672\ns_add_u32 m0, s50, 73728\nM 52,172,124\nG 4\nM 8,176,96\nR 168,184,32768\n"
"s_add_u32 s63, s52, 1773568\nM 8,180,112\nbuffer_load_dwordx4 a[188:191], v252, s[20:23], s67 offen offset:3072 sc1\nM 24,176,100\nR 170,184,36864\nM 24,180,116\n"
"buffer_load_dwordx4 v220, s[4:7], s63 offen lds\nM 40,176,104\nR 172,184,40960\nM 40,180,120\nbuffer_load_dwordx4 v221, s[4:7], s63 offen offset:1024 lds\nM 56,176,108\n"
"R 174,184,45056\nM 56,180,124\nG 4\nM 12,160,96\nR 176,185,32768\nM 12,164,112\n"
"s_add_u32 s64, s70, 18886656\nbuffer_load_dwordx4 a[192:195], v252, s[20:23], s64 offen offset:0 sc1\nM 28,160,100\nR 178,185,36864\nM 28,164,116\nM 44,160,104\n"
"R 180,185,40960\nM 44,164,120\nM 60,160,108\nR 182,185,45056\nM 60,164,124\nG 4\n"
"N 64,96,168\nR 160,186,32768\nN 64,112,172\ns_add_u32 s65, s70, 18919424\nbuffer_load_dwordx4 a[196:199], v252, s[20:23], s65 offen offset:0 sc1\nN 68,100,168\n"
"R 162,186,36864\nN 68,116,172\ns_add_u32 m0, s50, 81920\nN 96,104,168\nR 164,186,40960\ns_add_u32 s63, s52, 1968128\n"
"N 96,120,172\ns_add_u32 s66, s71, 18886656\nbuffer_load_dwordx4 a[200:203], v252, s[20:23], s66 offen offset:0 sc1\nN 100,108,168\nR 166,186,45056\nN 100,124,172\n"
"s_waitcnt vmcnt(12)\ns_barrier\nG 4\nN 72,96,176\nR 168,187,32768\nN 72,112,180\n"
"buffer_load_dwordx4 v220, s[4:7], s63 offen lds\nN 76,100,176\nR 170,187,36864\nN 76,116,180\nN 104,104,176\nR 172,187,40960\n"
"N 104,120,180\ns_add_u32 s67, s71, 18919424\nbuffer_load_dwordx4 a[204:207], v252, s[20:23], s67 offen offset:0 sc1\nN 108,108,176\nR 174,187,45056\nN 108,124,180\n"
"G 4\nN 80,96,160\nR 176,184,49152\nbuffer_load_dwordx4 v221, s[4:7], s63 offen offset:1024 lds\nN 80,112,164\ns_add_u32 m0, s50, 90112\n"
"s_add_u32 s63, s52, 2164736\nbuffer_load_dwordx4 v220, s[4:7], s63 offen lds\nN 84,100,160\nR 178,184,53248\nN 84,116,164\nbuffer_load_dwordx4 a[208:211], v252, s[20:23], s64 offen offset:1024 sc1\n"
"N 112,104,160\nR 180,184,57344\nN 112,120,164\nbuffer_load_dwordx4 v221, s[4:7], s63 offen offset:1024 lds\nN 116,108,160\nR 182,184,61440\n"
"N 116,124,164\nG 4\nN 88,96,168\nR 160,185,49152\nN 88,112,172\nbuffer_load_dwordx4 a[212:215], v252, s[20:23], s65 offen offset:1024 sc1\n"
"N 92,100,168\nR 162,185,53248\nN 92,116,172\nN 120,104,168\nR 164,185,57344\nN 120,120,172\n"
"N 124,108,168\nR 166,185,61440\nN 124,124,172\nG 4\nM 0,176,128\nR 168,186,49152\n"
"M 0,180,144\nbuffer_load_dwordx4 a[216:219], v252, s[20:23], s66 offen offset:1024 sc1\nM 16,176,132\nR 170,186,53248\nM 16,180,148\nM 32,176,136\n"
"R 172,186,57344\nM 32,180,152\ns_add_u32 m0, s50, 98304\nM 48,176,140\nR 174,186,61440\ns_add_u32 s63, s52, 1970176\n"
"M 48,180,156\ns_waitcnt vmcnt(11)\ns_barrier\nG 4\nM 4,160,128\nR 176,187,49152\n"
"M 4,164,144\nbuffer_load_dwordx4 a[220:223], v252, s[20:23], s67 offen offset:1024 sc1\nM 20,160,132\nR 178,187,53248\nM 20,164,148\nbuffer_load_dwordx4 v220, s[4:7], s63 offen lds\n"
"M 36,160,136\nR 180,187,57344\nM 36,164,152\nbuffer_load_dwordx4 v221, s[4:7], s63 offen offset:1024 lds\nM 52,160,140\nR 182,187,61440\n"
"s_add_u32 m0, s50, 106496\nM 52,164,156\nG 4\nM 8,168,128\nR 160,188,0\ns_add_u32 s63, s52, 2166784\n"
"M 8,172,144\nbuffer_load_dwordx4 a[224:227], v252, s[20:23], s64 offen offset:2048 sc1\nM 24,168,132\nR 162,188,4096\nM 24,172,148\nbuffer_load_dwordx4 v220, s[4:7], s63 offen lds\n"
"M 40,168,136\nR 164,188,8192\nM 40,172,152\nbuffer_load_dwordx4 a[228:231], v252, s[20:23], s65 offen offset:2048 sc1\nM 56,168,140\nR 166,188,12288\n"
"M 56,172,156\nG 4\nM 12,176,128\nR 168,189,0\nbuffer_load_dwordx4 v221, s[4:7], s63 offen offset:1024 lds\nM 12,180,144\n"
"M 28,176,132\nR 170,189,4096\nM 28,180,148\nbuffer_load_dwordx4 a[232:235], v252, s[20:23], s66 offen offset:2048 sc1\nM 44,176,136\nR 172,189,8192\n"
"M 44,180,152\nM 60,176,140\nR 174,189,12288\nM 60,180,156\nG 4\nN 64,128,160\n"
"R 176,190,0\nN 64,144,164\nN 68,132,160\nR 178,190,4096\nN 68,148,164\nbuffer_load_dwordx4 a[236:239], v252, s[20:23], s67 offen offset:2048 sc1\n"
"N 96,136,160\nR 180,190,8192\nN 96,152,164\ns_add_u32 m0, s50, 114688\nN 100,140,160\nR 182,190,12288\n"
"s_add_u32 s63, s52, 2361344\nN 100,156,164\ns_waitcnt vmcnt(11)\ns_barrier\nG 4\nN 72,128,168\n"
"R 160,191,0\nN 72,144,172\nbuffer_load_dwordx4 a[240:243], v252, s[20:23], s64 offen offset:3072 sc1\nN 76,132,168\nR 162,191,4096\nN 76,148,172\n"
"buffer_load_dwordx4 v220, s[4:7], s63 offen lds\nN 104,136,168\nR 164,191,8192\nLq720:\ns_lshr_b32 s60, s45, 3\ns_lshl2_add_u32 s60, s60, s40\n"
"s_lshl_b32 s60, s60, 8\nv_lshl_add_u32 v224, v252, 3, s60\nv_add_u32 v224, Lq720+4096-LqPC, v224\ns_mov_b64 exec, 3\nglobal_load_dword v224, v224, s[98:99]\ns_mov_b64 exec, -1\n"
"N 104,152,172\nbuffer_load_dwordx4 v221, s[4:7], s63 offen offset:1024 lds\nN 108,140,168\nR 166,191,12288\ns_add_u32 m0, s50, 0\nN 108,156,172\n"
"G 4\nN 80,128,176\nR 168,188,16384\ns_add_u32 s63, s52, 2557952\nN 80,144,180\nbuffer_load_dwordx4 a[244:247], v252, s[20:23], s65 offen offset:3072 sc1\n"
"N 84,132,176\nR 170,188,20480\nN 84,148,180\nbuffer_load_dwordx4 v220, s[4:7], s63 offen lds\nN 112,136,176\nR 172,188,24576\n"
"N 112,152,180\nbuffer_load_dwordx4 v221, s[4:7], s63 offen offset:1024 lds\nN 116,140,176\nR 174,188,28672\nN 116,156,180\nG 4\n"
"N 88,128,160\nR 176,189,16384\nN 88,144,164\nbuffer_load_dwordx4 a[248:251], v252, s[20:23], s66 offen offset:3072 sc1\nN 92,132,160\nR 178,189,20480\n"
"N 92,148,164\nN 120,136,160\nR 180,189,24576\nN 120,152,164\nbuffer_load_dwordx4 a[252:255], v252, s[20:23], s67 offen offset:3072 sc1\nN 124,140,160\n"
"R 182,189,28672\nN 124,156,164\nG 4\nM 0,168,160\nR 160,190,16384\nM 0,172,176\n"
"M 16,168,164\nR 162,190,20480\nM 16,172,180\ns_lshr_b32 s84, s49, 3\nM 32,168,168\nR 164,190,24576\n"
"s_add_u32 m0, s50, 8192\nM 32,172,184\ns_add_u32 s84, s84, 82837504\nM 48,168,172\nR 166,190,28672\ns_add_u32 s63, s52, 2754560\n"
"M 48,172,188\ns_waitcnt vmcnt(11)\ns_barrier\nG 4\nM 4,176,160\nR 168,191,16384\n"
"M 4,180,176\nbuffer_load_dwordx4 v220, s[4:7], s63 offen lds\nM 20,176,164\nR 170,191,20480\nM 20,180,180\nbuffer_load_dwordx4 v[128:131], v252, s[20:23], s84 offen offset:0 sc1\n"
"M 36,176,168\nR 172,191,24576\nM 36,180,184\nbuffer_load_dwordx4 v[132:135], v252, s[20:23], s84 offen offset:1024 sc1\nM 52,176,172\nR 174,191,28672\n"
"M 52,180,188\nG 4\nM 8,160,160\nR 176,188,32768\nbuffer_load_dwordx4 v221, s[4:7], s63 offen offset:1024 lds\nM 8,164,176\n"
"buffer_load_dwordx4 v[136:139], v252, s[20:23], s84 offen offset:2048 sc1\ns_add_u32 s63, s52, 2951168\nM 24,160,164\nR 178,188,36864\ns_add_u32 m0, s50, 16384\nM 24,164,180\n"
"buffer_load_dwordx4 v[140:143], v252, s[20:23], s84 offen offset:3072 sc1\nM 40,160,168\nR 180,188,40960\nbuffer_load_dwordx4 v220, s[4:7], s63 offen lds\nM 40,164,184\ns_lshr_b32 s85, s49, 3\n"
"M 56,160,172\nR 182,188,45056\ns_add_u32 s85, s85, 82841600\nM 56,164,188\nbuffer_load_dwordx4 v[144:147], v252, s[20:23], s85 offen offset:0 sc1\nG 4\n"
"M 12,168,160\nR 160,189,32768\nbuffer_load_dwordx4 v221, s[4:7], s63 offen offset:1024 lds\nM 12,172,176\nbuffer_load_dwordx4 v[148:151], v252, s[20:23], s85 offen offset:1024 sc1\nM 28,168,164\n"
"R 162,189,36864\nM 28,172,180\nM 44,168,168\nR 164,189,40960\nM 44,172,184\nbuffer_load_dwordx4 v[152:155], v252, s[20:23], s85 offen offset:2048 sc1\n"
"M 60,168,172\nR 166,189,45056\nM 60,172,188\nG 4\nN 64,160,176\nR 168,190,32768\n"
"N 64,176,180\nbuffer_load_dwordx4 v[156:159], v252, s[20:23], s85 offen offset:3072 sc1\nN 68,164,176\nR 170,190,36864\nN 68,180,180\ns_add_u32 m0, s50, 24576\n"
"N 96,168,176\nR 172,190,40960\nN 96,184,180\ns_add_u32 s63, s52, 2363392\nN 100,172,176\nR 174,190,45056\n"
"N 100,188,180\ns_waitcnt vmcnt(14)\ns_barrier\nG 4\nN 72,160,160\nR 176,191,32768\n"
"N 72,176,164\nbuffer_load_dwordx4 v220, s[4:7], s63 offen lds\nN 76,164,160\nR 178,191,36864\nN 76,180,164\nN 104,168,160\n"
"R 180,191,40960\nN 104,184,164\nbuffer_load_dwordx4 v221, s[4:7], s63 offen offset:1024 lds\nN 108,172,160\nR 182,191,45056\ns_add_u32 m0, s50, 32768\n"
"N 108,188,164\nG 4\nN 80,160,168\nR 160,188,49152\ns_add_u32 s63, s52, 2560000\nN 80,176,172\n"
"buffer_load_dwordx4 v220, s[4:7], s63 offen lds\nN 84,164,168\nR 162,188,53248\nN 84,180,172\nN 112,168,168\nR 164,184,0\n"
"N 112,184,172\nbuffer_load_dwordx4 v221, s[4:7], s63 offen offset:1024 lds\nN 116,172,168\nR 166,184,4096\nN 116,188,172\nG 4\n"
"N 88,160,176\nR 168,189,49152\nN 88,176,180\nN 92,164,176\nR 170,189,53248\nN 92,180,180\n"
"s_add_u32 m0, s50, 40960\nN 120,168,176\nR 172,185,0\nN 120,184,180\ns_add_u32 s63, s52, 2756608\nN 124,172,176\n"
"R 174,185,4096\nN 124,188,180\nG 4\nM 0,160,192\nR 176,190,49152\nM 0,164,208\n"
"buffer_load_dwordx4 v220, s[4:7], s63 offen lds\nM 16,160,196\nR 178,190,53248\nM 16,164,212\nM 32,160,200\nR 180,186,0\n"
"M 32,164,216\nbuffer_load_dwordx4 v221, s[4:7], s63 offen offset:1024 lds\nM 48,160,204\nR 182,186,4096\ns_add_u32 m0, s50, 49152\nM 48,164,220\n"
"s_waitcnt vmcnt(9)\ns_barrier\nG 4\nM 4,168,192\nR 160,191,49152\ns_add_u32 s63, s52, 2953216\n"
"M 4,172,208\nbuffer_load_dwordx4 v220, s[4:7], s63 offen lds\nM 20,168,196\nR 162,191,53248\nv_min3_u32 v230, v129, v131, v133\nv_min3_u32 v230, v230, v135, v137\n"
"M 20,172,212\nv_min3_u32 v230, v230, v139, v141\nv_min3_u32 v230, v230, v143, v145\ns_waitcnt vmcnt(9)\nv_min3_u32 v230, v230, v147, v149\nM 36,168,200\n"
"R 164,187,0\ns_waitcnt vmcnt(8)\nv_min3_u32 v230, v230, v151, v153\ns_waitcnt vmcnt(7)\nv_min3_u32 v230, v230, v155, v157\nM 36,172,216\n"
"buffer_load_dwordx4 v221, s[4:7], s63 offen offset:1024 lds\nv_min3_u32 v230, v230, v159, v159\nM 52,168,204\nR 166,187,4096\ns_add_u32 m0, s50, 57344\nv_max3_u32 v231, v129, v131, v133\n"
"M 52,172,220\nG 4\nv_max3_u32 v231, v231, v135, v137\nv_max3_u32 v231, v231, v139, v141\nv_max3_u32 v231, v231, v143, v145\nM 8,176,192\n"
"R 168,184,8192\ns_add_u32 s63, s52, 0\nv_max3_u32 v231, v231, v147, v149\nM 8,180,208\nbuffer_load_dwordx4 v220, s[4:7], s63 offen lds\nv_max3_u32 v231, v231, v151, v153\n"
"M 24,176,196\nR 170,184,12288\nv_max3_u32 v231, v231, v155, v157\nv_max3_u32 v231, v231, v159, v159\nM 24,180,212\nv_cmp_ne_u32 s[86:87], s46, v230\n"
"v_cmp_ne_u32 s[88:89], s46, v231\nM 40,176,200\nR 172,184,16384\ns_or_b64 s[86:87], s[86:87], s[88:89]\ns_cbranch_scc1 Lq672\nLq673:\n"
"M 40,180,216\nv_add_f32 v128, v128, v144\nv_add_f32 v130, v130, v146\nv_add_f32 v132, v132, v148\nM 56,176,204\nR 174,184,20480\n"
"v_add_f32 v134, v134, v150\nv_add_f32 v136, v136, v152\nM 56,180,220\nG 4\nv_add_f32 v138, v138, v154\nv_add_f32 v140, v140, v156\n"
"v_add_f32 v142, v142, v158\nM 12,160,192\nR 176,185,8192\nv_add_f32 v128, v128, v136\nv_add_f32 v130, v130, v138\nM 12,164,208\n"
"buffer_load_dwordx4 v221, s[4:7], s63 offen offset:1024 lds\nv_add_f32 v132, v132, v140\nM 28,160,196\nR 178,185,12288\ns_add_u32 s63, s52, 196608\nv_add_f32 v134, v134, v142\n"
"M 28,164,212\ns_add_u32 m0, s50, 65536\nv_add_f32 v128, v128, v132\nv_add_f32 v130, v130, v134\nv_lshrrev_b32 v231, 1, v252\nM 44,160,200\n"
"R 180,185,16384\nv_lshrrev_b32 v230, 2, v252\ns_lshl_b32 s62, s40, 9\ns_add_u32 s62, s62, 159232\nv_add_u32 v231, s62, v231\nM 44,164,216\n"
"buffer_load_dwordx4 v220, s[4:7], s63 offen lds\nds_write2_b32 v231, v128, v130 offset1:1\nM 60,160,204\nR 182,185,20480\ns_lshl_b32 s62, s40, 9\ns_add_u32 s62, s62, 159232\n"
"v_add_u32 v230, s62, v230\nM 60,164,220\nds_read2st64_b32 v[232:233], v230 offset1:1\nG 6\nM 0,168,224\nR 160,186,8192\n"
"M 0,172,240\nbuffer_load_dwordx4 v221, s[4:7], s63 offen offset:1024 lds\nM 16,168,228\nR 162,186,12288\ns_add_u32 s63, s52, 393216\nM 16,172,244\n"
"s_add_u32 m0, s50, 73728\nM 32,168,232\nR 164,186,16384\nM 32,172,248\nbuffer_load_dwordx4 v220, s[4:7], s63 offen lds\nG 3\n"
"v_add_f32 v232, v232, v233\nM 48,168,236\nR 166,186,20480\nv_mov_b32 v233, v232\ns_nop 1\nv_permlane32_swap_b32 v232, v233\n"
"M 48,172,252\ns_waitcnt vmcnt(9)\ns_barrier\nv_mov_b32 v230, v232\nM 4,176,224\nR 168,187,8192\n"
"v_mov_b32 v231, v233\nv_permlane16_swap_b32 v230, v232\nM 4,180,240\nbuffer_load_dwordx4 v221, s[4:7], s63 offen offset:1024 lds\nv_permlane16_swap_b32 v231, v233\nM 20,176,228\n"
"R 170,187,12288\nv_mul_f32 v230, 0xba800000, v230\nv_mul_f32 v214, v230, v230\nM 20,180,244\ns_add_u32 m0, s50, 81920\nv_mul_f32 v231, 0x3a800000, v231\n"
"v_sub_f32 v231, v231, v214\nv_max_f32 v231, 0, v231\nM 36,176,232\nR 172,187,16384\nv_add_f32 v231, 0x3727c5ac, v231\nv_rsq_f32 v234, v231\n"
"M 36,180,248\ns_add_u32 s63, s52, 589824\nv_and_b32 v208, s77, v230\nv_mul_f32 v216, v231, v234\nv_sub_f32 v230, v230, v208\nM 52,176,236\n"
"R 174,187,20480\nv_cvt_pk_bf16_f32 v209, v230, v216\nv_cvt_pk_bf16_f32 v208, v208, v208\nM 52,180,252\ns_mov_b64 s[60:61], 0xffff\nv_cndmask_b32 v208, 0, v208, s[60:61]\n"
"G 4\nv_mov_b32 v214, v234\nM 8,160,224\nR 176,184,24576\nv_mul_f32 v215, 0x3e8293ee, v234\nv_mul_f32 v232, 0xba800000, v232\n"
"M 8,164,240\nbuffer_load_dwordx4 v220, s[4:7], s63 offen lds\nv_mul_f32 v217, v232, v232\nM 24,160,228\nR 178,184,28672\nv_mul_f32 v233, 0x3a800000, v233\n"
"v_sub_f32 v233, v233, v217\nM 24,164,244\ns_mov_b64 s[60:61], 0xffff\nv_cndmask_b32 v209, 0, v209, s[60:61]\nv_max_f32 v233, 0, v233\nv_add_f32 v233, 0x3727c5ac, v233\n"
"M 40,160,232\nR 180,184,32768\nv_and_b32 v212, s77, v232\nv_rsq_f32 v235, v233\nM 40,164,248\nbuffer_load_dwordx4 v221, s[4:7], s63 offen offset:1024 lds\n"
"v_mul_f32 v219, v233, v235\nM 56,160,236\nR 182,184,36864\ns_add_u32 m0, s50, 90112\nv_sub_f32 v232, v232, v212\nM 56,164,252\n"
"G 4\nv_cvt_pk_bf16_f32 v213, v232, v219\nv_cvt_pk_bf16_f32 v212, v212, v212\nv_mov_b32 v217, v235\nM 12,168,224\nR 160,185,24576\n"
"s_add_u32 s63, s52, 786432\nv_mul_f32 v218, 0x3e8293ee, v235\nM 12,172,240\nbuffer_load_dwordx4 v220, s[4:7], s63 offen lds\nv_bfe_u32 v230, v252, 2, 6\nM 28,168,228\n"
"R 162,185,28672\ns_mov_b64 s[60:61], 0xffff\nv_cndmask_b32 v212, 0, v212, s[60:61]\nM 28,172,244\ns_mov_b64 s[60:61], 0xffff\nv_cndmask_b32 v213, 0, v213, s[60:61]\n"
"s_lshl_b32 s60, s40, 7\ns_add_u32 s60, s60, 161280\nv_add_u32 v230, s60, v230\nM 44,168,232\nR 164,185,32768\nds_write_b32 v230, v214 offset:0\n"
"M 44,172,248\nbuffer_load_dwordx4 v221, s[4:7], s63 offen offset:1024 lds\nds_write_b32 v230, v217 offset:64\nM 60,168,236\nR 166,185,36864\ns_mov_b32 s60, 156160\n"
"v_bfe_u32 v246, v252, 4, 4\nv_lshl_add_u32 v246, v246, 3, s60\nM 60,172,252\nds_read_b64 v[238:239], v246 offset:0\ns_waitcnt vmcnt(10) lgkmcnt(0)\ns_barrier\n"
"N 64,192,176\nR 168,186,24576\nds_read_b64 v[240:241], v246 offset:128\nN 64,208,180\nds_read_b64 v[242:243], v246 offset:256\ns_mov_b32 m0, s51\n"
"N 68,196,176\nR 170,186,28672\nds_read_b64 v[244:245], v246 offset:384\nds_write_addtid_b32 v32 offset:0\nN 68,212,180\nds_read_b64 v[246:247], v246 offset:0\n"
"ds_write_addtid_b32 v33 offset:256\nN 96,200,176\nR 172,186,32768\nds_write_addtid_b32 v34 offset:512\nN 96,216,180\nds_write_addtid_b32 v35 offset:768\n"
"ds_write_addtid_b32 v36 offset:1024\nN 100,204,176\nR 174,186,36864\nds_write_addtid_b32 v37 offset:1280\nN 100,220,180\nds_write_addtid_b32 v38 offset:1536\n"
"ds_write_addtid_b32 v39 offset:1792\nN 72,192,160\nR 176,187,24576\nds_write_addtid_b32 v40 offset:2048\nN 72,208,164\nds_write_addtid_b32 v41 offset:2304\n"
"ds_write_addtid_b32 v42 offset:2560\nN 76,196,160\nR 178,187,28672\nds_write_addtid_b32 v43 offset:2816\nN 76,212,164\nds_write_addtid_b32 v44 offset:3072\n"
"ds_write_addtid_b32 v45 offset:3328\nN 104,200,160\nR 180,187,32768\nG 15\nv_mfma_f32_16x16x32_bf16 v[0:3], v[238:241], v[208:211], v[0:3]\nds_write_addtid_b32 v46 offset:3584\n"
"N 104,216,164\nv_mfma_f32_16x16x32_bf16 v[4:7], v[240:243], v[208:211], v[4:7]\nds_write_addtid_b32 v47 offset:3840\nds_write_addtid_b32 v48 offset:4096\nN 108,204,160\nR 182,187,36864\n"
"v_mfma_f32_16x16x32_bf16 v[8:11], v[242:245], v[208:211], v[8:11]\nds_write_addtid_b32 v49 offset:4352\nN 108,220,164\nv_mfma_f32_16x16x32_bf16 v[12:15], v[244:247], v[208:211], v[12:15]\nds_write_addtid_b32 v50 offset:4608\nds_write_addtid_b32 v51 offset:4864\n"
"G 15\nN 80,192,168\nR 160,184,40960\nv_mfma_f32_16x16x32_bf16 v[16:19], v[244:247], v[210:213], v[16:19]\nds_write_addtid_b32 v52 offset:5120\nN 80,208,172\n"
"v_mfma_f32_16x16x32_bf16 v[20:23], v[238:241], v[210:213], v[20:23]\nds_write_addtid_b32 v53 offset:5376\nds_write_addtid_b32 v54 offset:5632\nN 84,196,168\nR 162,184,45056\nv_mfma_f32_16x16x32_bf16 v[24:27], v[240:243], v[210:213], v[24:27]\n"
"ds_write_addtid_b32 v55 offset:5888\nN 84,212,172\nv_mfma_f32_16x16x32_bf16 v[28:31], v[242:245], v[210:213], v[28:31]\nds_write_addtid_b32 v56 offset:6144\nds_write_addtid_b32 v57 offset:6400\nN 112,200,168\n"
"R 164,184,49152\nds_write_addtid_b32 v58 offset:6656\nN 112,216,172\nds_write_addtid_b32 v59 offset:6912\nds_write_addtid_b32 v60 offset:7168\nN 116,204,168\n"
"R 166,184,53248\nds_write_addtid_b32 v61 offset:7424\nN 116,220,172\nds_write_addtid_b32 v62 offset:7680\nds_write_addtid_b32 v63 offset:7936\nG 15\n"
"N 88,192,176\nR 168,185,40960\ns_add_u32 m0, s50, 98304\ns_add_u32 s63, s52, 983040\nN 88,208,180\nbuffer_load_dwordx4 v220, s[4:7], s63 offen lds\n"
"v_mov_b32 v248, -1\nv_mov_b32 v249, -1\nv_mov_b32 v250, -1\nv_mov_b32 v251, -1\nN 92,196,176\nR 170,185,45056\n"
"s_waitcnt vmcnt(7) lgkmcnt(2)\ns_barrier\nN 92,212,180\nds_read2st64_b32 v[32:33], v225 offset0:0 offset1:1\nds_read2st64_b32 v[34:35], v225 offset0:2 offset1:3\nN 120,200,176\n"
"R 172,185,49152\nds_read2st64_b32 v[36:37], v225 offset0:4 offset1:5\nN 120,216,180\nbuffer_load_dwordx4 v221, s[4:7], s63 offen offset:1024 lds\nds_read2st64_b32 v[38:39], v225 offset0:6 offset1:7\nN 124,204,176\n"
"R 174,185,53248\nds_read2st64_b32 v[40:41], v225 offset0:8 offset1:9\nN 124,220,180\nds_read2st64_b32 v[42:43], v225 offset0:10 offset1:11\nds_read2st64_b32 v[44:45], v225 offset0:12 offset1:13\nN 64,224,160\n"
"R 176,186,40960\ns_mov_b32 s60, 156672\nv_bfe_u32 v246, v252, 4, 4\nv_lshl_add_u32 v246, v246, 3, s60\nds_read2st64_b32 v[46:47], v225 offset0:14 offset1:15\nN 64,240,164\n"
"ds_read_b64 v[238:239], v246 offset:0\nG 11\nv_add_f32 v0, v0, v32\nv_add_f32 v1, v1, v33\nN 68,228,160\nR 178,186,45056\n"
"ds_read_b64 v[240:241], v246 offset:128\nG 12\nv_add_f32 v2, v2, v34\nN 68,244,164\nds_read_b64 v[242:243], v246 offset:256\ns_add_u32 m0, s50, 106496\n"
"v_add_f32 v3, v3, v35\nN 96,232,160\nR 180,186,49152\nds_read_b64 v[244:245], v246 offset:384\nG 13\nv_add_f32 v4, v4, v36\n"
"N 96,248,164\nds_read_b64 v[246:247], v246 offset:0\ns_add_u32 s63, s52, 1179648\nv_add_f32 v5, v5, v37\nN 100,236,160\nR 182,186,53248\n"
"G 14\nv_add_f32 v6, v6, v38\nv_add_f32 v7, v7, v39\nN 100,252,164\nds_read2st64_b32 v[48:49], v225 offset0:16 offset1:17\nG 14\n"
"G 13\nv_add_f32 v8, v8, v40\nN 72,224,168\nR 160,187,40960\nv_add_f32 v9, v9, v41\nG 13\n"
"v_add_f32 v10, v10, v42\nN 72,240,172\nbuffer_load_dwordx4 v220, s[4:7], s63 offen lds\nv_add_f32 v11, v11, v43\nN 76,228,168\nR 162,187,45056\n"
"G 13\nv_add_f32 v12, v12, v44\nv_add_f32 v13, v13, v45\nN 76,244,172\nds_read2st64_b32 v[50:51], v225 offset0:18 offset1:19\nG 12\n"
"v_add_f32 v14, v14, v46\nv_add_f32 v15, v15, v47\nN 104,232,168\nR 164,187,49152\nds_read2st64_b32 v[52:53], v225 offset0:20 offset1:21\nN 104,248,172\n"
"G 11\nv_mfma_f32_16x16x32_bf16 v[64:67], v[208:211], v[238:241], v[64:67]\nbuffer_load_dwordx4 v221, s[4:7], s63 offen offset:1024 lds\nG 5\nv_add_f32 v16, v16, v48\nN 108,236,168\n"
"R 166,187,53248\ns_add_u32 m0, s50, 114688\nv_add_f32 v17, v17, v49\nN 108,252,172\nv_mfma_f32_16x16x32_bf16 v[72:75], v[208:211], v[240:243], v[72:75]\nds_read2st64_b32 v[54:55], v225 offset0:22 offset1:23\n"
"ds_read2st64_b32 v[56:57], v225 offset0:24 offset1:25\nN 80,224,176\nR 168,184,57344\ns_add_u32 s63, s52, 1376256\nds_read2st64_b32 v[58:59], v225 offset0:26 offset1:27\nN 80,240,180\n"
"v_mfma_f32_16x16x32_bf16 v[80:83], v[208:211], v[242:245], v[80:83]\nbuffer_load_dwordx4 v220, s[4:7], s63 offen lds\nG 7\nv_add_f32 v18, v18, v50\nN 84,228,176\nR 170,184,61440\n"
"v_add_f32 v19, v19, v51\nG 6\nv_add_f32 v20, v20, v52\nN 84,244,180\nv_mfma_f32_16x16x32_bf16 v[88:91], v[208:211], v[244:247], v[88:91]\nbuffer_load_dwordx4 v221, s[4:7], s63 offen offset:1024 lds\n"
"v_add_f32 v21, v21, v53\nN 112,232,176\nR 172,188,0\ns_add_u32 s63, s52, 1572864\nds_read2st64_b32 v[60:61], v225 offset0:28 offset1:29\nN 112,248,180\n"
"v_mfma_f32_16x16x32_bf16 v[68:71], v[210:213], v[244:247], v[68:71]\ns_add_u32 m0, s50, 0\nds_read2st64_b32 v[62:63], v225 offset0:30 offset1:31\nG 7\nv_add_f32 v22, v22, v54\nN 116,236,176\n"
"R 174,188,4096\nv_add_f32 v23, v23, v55\nG 7\nv_add_f32 v24, v24, v56\nN 116,252,180\nv_mfma_f32_16x16x32_bf16 v[76:79], v[210:213], v[238:241], v[76:79]\n"
"v_add_f32 v25, v25, v57\nG 5\nv_add_f32 v26, v26, v58\nv_add_f32 v27, v27, v59\nN 88,224,160\nR 176,185,57344\n"
"N 88,240,164\nv_mfma_f32_16x16x32_bf16 v[84:87], v[210:213], v[240:243], v[84:87]\nbuffer_load_dwordx4 v220, s[4:7], s63 offen lds\nG 3\nv_add_f32 v28, v28, v60\nv_add_f32 v29, v29, v61\n"
"G 2\nv_add_f32 v30, v30, v62\nv_add_f32 v31, v31, v63\nN 92,228,160\nR 178,185,61440\nv_mul_f32 v16, v16, v217\n"
"v_mul_f32 v17, v17, v217\nN 92,244,164\nv_mfma_f32_16x16x32_bf16 v[92:95], v[210:213], v[242:245], v[92:95]\nbuffer_load_dwordx4 v221, s[4:7], s63 offen offset:1024 lds\nv_mul_f32 v18, v18, v217\nN 120,232,160\n"
"R 180,189,0\nv_mul_f32 v19, v19, v217\nv_mul_f32 v20, v20, v217\nN 120,248,164\nv_mul_f32 v21, v21, v217\nv_mul_f32 v22, v22, v217\n"
"v_mul_f32 v23, v23, v217\nN 124,236,160\nR 182,189,4096\nv_mul_f32 v24, v24, v217\nv_mul_f32 v25, v25, v217\nN 124,252,164\n"
"s_waitcnt vmcnt(10)\ns_barrier\nG 4\nv_mul_f32 v26, v26, v217\nv_mfma_f32_16x16x32_bf16 v[128:131], v[168:171], a[0:3], 0\nR 160,186,57344\n"
"v_mul_f32 v27, v27, v217\nv_mul_f32 v28, v28, v217\nM 128,172,16\ns_mov_b32 m0, s51\nv_mul_f32 v29, v29, v217\nv_mul_f32 v30, v30, v217\n"
"v_mul_f32 v31, v31, v217\nv_mfma_f32_16x16x32_bf16 v[144:147], v[168:171], a[4:7], 0\nR 162,186,61440\nv_cvt_pk_bf16_f32 v16, v16, v17\nv_cvt_pk_bf16_f32 v17, v18, v19\nM 144,172,20\n"
"v_cvt_pk_bf16_f32 v18, v20, v21\nv_cvt_pk_bf16_f32 v19, v22, v23\nv_cvt_pk_bf16_f32 v20, v24, v25\nv_mfma_f32_16x16x32_bf16 v[32:35], v[168:171], a[8:11], 0\nR 164,190,0\nv_cvt_pk_bf16_f32 v21, v26, v27\n"
"v_cvt_pk_bf16_f32 v22, v28, v29\nM 32,172,24\nv_cvt_pk_bf16_f32 v23, v30, v31\nv_mul_f32 v0, v0, v214\nv_mul_f32 v1, v1, v214\nv_mfma_f32_16x16x32_bf16 v[48:51], v[168:171], a[12:15], 0\n"
"R 166,190,4096\nv_mul_f32 v2, v2, v214\nv_mul_f32 v3, v3, v214\nM 48,172,28\nG 4\nv_mul_f32 v4, v4, v214\n"
"v_mul_f32 v5, v5, v214\nv_mul_f32 v6, v6, v214\nv_mfma_f32_16x16x32_bf16 v[132:135], v[176:179], a[0:3], 0\nR 168,187,57344\nv_mul_f32 v7, v7, v214\nv_mul_f32 v8, v8, v214\n"
"M 132,180,16\nds_write_addtid_b32 v96 offset:0\nv_mul_f32 v9, v9, v214\nv_mul_f32 v10, v10, v214\nv_mul_f32 v11, v11, v214\nv_mfma_f32_16x16x32_bf16 v[148:151], v[176:179], a[4:7], 0\n"
"R 170,187,61440\nds_write_addtid_b32 v97 offset:256\nv_mul_f32 v12, v12, v214\nv_mul_f32 v13, v13, v214\nM 148,180,20\nds_write_addtid_b32 v98 offset:512\n"
"v_mul_f32 v14, v14, v214\nv_mul_f32 v15, v15, v214\nv_cvt_pk_bf16_f32 v0, v0, v1\nv_mfma_f32_16x16x32_bf16 v[36:39], v[176:179], a[8:11], 0\nR 172,191,0\nds_write_addtid_b32 v99 offset:768\n"
"v_cvt_pk_bf16_f32 v1, v2, v3\nv_cvt_pk_bf16_f32 v2, v4, v5\nM 36,180,24\ns_bfe_u32 s100, s58, 0x20007\ns_mul_i32 s100, s100, 49152\ns_add_u32 s100, s100, s72\n"
"s_add_u32 s100, s100, 65044480\nbuffer_store_dwordx4 v[248:251], v252, s[20:23], s100 offen offset:0 sc1\nds_write_addtid_b32 v100 offset:1024\nv_cvt_pk_bf16_f32 v3, v6, v7\nv_mfma_f32_16x16x32_bf16 v[52:55], v[176:179], a[12:15], 0\nR 174,191,4096\n"
"ds_write_addtid_b32 v101 offset:1280\nv_cvt_pk_bf16_f32 v4, v8, v9\nv_cvt_pk_bf16_f32 v5, v10, v11\nM 52,180,28\nds_write_addtid_b32 v102 offset:1536\nG 11\n"
"v_cvt_pk_bf16_f32 v6, v12, v13\nv_cvt_pk_bf16_f32 v7, v14, v15\nv_mfma_f32_16x16x32_bf16 v[136:139], v[160:163], a[0:3], 0\nR 176,188,8192\nds_write_addtid_b32 v103 offset:1792\nM 136,164,16\n"
"ds_write_addtid_b32 v104 offset:2048\nds_write_addtid_b32 v105 offset:2304\nv_mfma_f32_16x16x32_bf16 v[152:155], v[160:163], a[4:7], 0\nR 178,188,12288\nds_write_addtid_b32 v106 offset:2560\nM 152,164,20\n"
"ds_write_addtid_b32 v107 offset:2816\nds_write_addtid_b32 v108 offset:3072\nv_mfma_f32_16x16x32_bf16 v[40:43], v[160:163], a[8:11], 0\nR 180,188,16384\nds_write_addtid_b32 v109 offset:3328\nM 40,164,24\n"
"ds_write_addtid_b32 v110 offset:3584\nds_write_addtid_b32 v111 offset:3840\nv_mfma_f32_16x16x32_bf16 v[56:59], v[160:163], a[12:15], 0\nR 182,188,20480\nds_write_addtid_b32 v112 offset:4096\nM 56,164,28\n"
"ds_write_addtid_b32 v113 offset:4352\nds_write_addtid_b32 v114 offset:4608\nG 15\nv_mfma_f32_16x16x32_bf16 v[140:143], v[168:171], a[0:3], 0\nR 160,189,8192\nds_write_addtid_b32 v115 offset:4864\n"
"M 140,172,16\nds_write_addtid_b32 v116 offset:5120\nds_write_addtid_b32 v117 offset:5376\nv_mfma_f32_16x16x32_bf16 v[156:159], v[168:171], a[4:7], 0\nR 162,189,12288\nds_write_addtid_b32 v118 offset:5632\n"
"M 156,172,20\nds_write_addtid_b32 v119 offset:5888\nds_write_addtid_b32 v120 offset:6144\nv_mfma_f32_16x16x32_bf16 v[44:47], v[168:171], a[8:11], 0\nR 164,189,16384\nds_write_addtid_b32 v121 offset:6400\n"
"M 44,172,24\nds_write_addtid_b32 v122 offset:6656\nds_write_addtid_b32 v123 offset:6912\nv_mfma_f32_16x16x32_bf16 v[60:63], v[168:171], a[12:15], 0\nR 166,189,20480\nds_write_addtid_b32 v124 offset:7168\n"
"M 60,172,28\nds_write_addtid_b32 v125 offset:7424\nds_write_addtid_b32 v126 offset:7680\nds_write_addtid_b32 v127 offset:7936\nG 15\nM 128,176,32\n"
"R 168,190,8192\ns_add_u32 m0, s50, 8192\ns_add_u32 s63, s52, 1769472\nbuffer_load_dwordx4 v220, s[4:7], s63 offen lds\nM 128,180,48\nbuffer_load_dwordx4 v221, s[4:7], s63 offen offset:1024 lds\n"
"M 144,176,36\nR 170,190,12288\ns_waitcnt vmcnt(9) lgkmcnt(2)\ns_barrier\nM 144,180,52\ns_add_u32 m0, s50, 16384\n"
"ds_read2st64_b32 v[96:97], v225 offset0:0 offset1:1\nds_read2st64_b32 v[98:99], v225 offset0:2 offset1:3\nv_lshrrev_b32 v226, 8, v252\nM 32,176,40\nR 172,190,16384\nds_read2st64_b32 v[100:101], v225 offset0:4 offset1:5\n"
"s_lshl_b32 s60, s40, 7\ns_add_u32 s60, s60, 161280\nv_lshl_add_u32 v226, v226, 4, s60\nM 32,180,56\ns_add_u32 s63, s52, 1966080\nds_read2st64_b32 v[102:103], v225 offset0:6 offset1:7\n"
"ds_read2st64_b32 v[104:105], v225 offset0:8 offset1:9\nds_read_b128 v[230:233], v226 offset:0\nM 48,176,44\nR 174,190,20480\nds_read2st64_b32 v[106:107], v225 offset0:10 offset1:11\nds_read_b128 v[234:237], v226 offset:64\n"
"M 48,180,60\nds_read2st64_b32 v[108:109], v225 offset0:12 offset1:13\nds_read2st64_b32 v[110:111], v225 offset0:14 offset1:15\nM 132,160,32\nR 176,191,8192\nds_read2st64_b32 v[112:113], v225 offset0:16 offset1:17\n"
"M 132,164,48\nbuffer_load_dwordx4 v220, s[4:7], s63 offen lds\nG 13\nv_add_f32 v64, v64, v96\nM 148,160,36\nR 178,191,12288\n"
"v_add_f32 v65, v65, v97\nG 13\nv_add_f32 v66, v66, v98\nM 148,164,52\nds_read2st64_b32 v[114:115], v225 offset0:18 offset1:19\nv_add_f32 v67, v67, v99\n"
"G 12\nv_add_f32 v68, v68, v100\nM 36,160,40\nR 180,191,16384\nv_add_f32 v69, v69, v101\nG 12\n"
"v_add_f32 v70, v70, v102\nM 36,164,56\nbuffer_load_dwordx4 v221, s[4:7], s63 offen offset:1024 lds\nv_add_f32 v71, v71, v103\nM 52,160,44\nR 182,191,20480\n"
"s_add_u32 m0, s50, 24576\nG 12\nv_add_f32 v72, v72, v104\nM 52,164,60\nds_read2st64_b32 v[116:117], v225 offset0:20 offset1:21\nG 11\n"
"v_add_f32 v73, v73, v105\nM 136,168,32\nR 160,188,24576\ns_add_u32 s63, s52, 2162688\nG 11\nv_add_f32 v74, v74, v106\n"
"M 136,172,48\nbuffer_load_dwordx4 v220, s[4:7], s63 offen lds\nv_add_f32 v75, v75, v107\nM 152,168,36\nR 162,188,28672\nG 10\n"
"v_add_f32 v76, v76, v108\nv_add_f32 v77, v77, v109\nM 152,172,52\nds_read2st64_b32 v[118:119], v225 offset0:22 offset1:23\nG 10\nv_add_f32 v78, v78, v110\n"
"v_add_f32 v79, v79, v111\nM 40,168,40\nR 164,188,32768\nG 9\nv_add_f32 v80, v80, v112\nv_add_f32 v81, v81, v113\n"
"M 40,172,56\nbuffer_load_dwordx4 v221, s[4:7], s63 offen offset:1024 lds\nM 56,168,44\nR 166,188,36864\ns_add_u32 m0, s50, 32768\nM 56,172,60\n"
"ds_read2st64_b32 v[120:121], v225 offset0:24 offset1:25\nG 7\nv_add_f32 v82, v82, v114\nM 140,176,32\nR 168,189,24576\ns_add_u32 s63, s52, 2359296\n"
"M 140,180,48\nbuffer_load_dwordx4 v220, s[4:7], s63 offen lds\nM 156,176,36\nR 170,189,28672\nv_add_f32 v83, v83, v115\nM 156,180,52\n"
"ds_read2st64_b32 v[122:123], v225 offset0:26 offset1:27\nG 9\nv_add_f32 v84, v84, v116\nM 44,176,40\nR 172,189,32768\nv_add_f32 v85, v85, v117\n"
"M 44,180,56\nds_read2st64_b32 v[124:125], v225 offset0:28 offset1:29\nG 8\nv_add_f32 v86, v86, v118\nM 60,176,44\nR 174,189,36864\n"
"v_add_f32 v87, v87, v119\nM 60,180,60\nds_read2st64_b32 v[126:127], v225 offset0:30 offset1:31\nG 8\nG 7\nv_add_f32 v88, v88, v120\n"
"M 128,160,64\nR 176,190,24576\nv_add_f32 v89, v89, v121\nM 128,164,80\nbuffer_load_dwordx4 v221, s[4:7], s63 offen offset:1024 lds\nM 144,160,68\n"
"R 178,190,28672\nG 6\nv_add_f32 v90, v90, v122\nM 144,164,84\nbuffer_store_dwordx4 v[248:251], v252, s[20:23], s100 offen offset:1024 sc1\nM 32,160,72\n"
"R 180,190,32768\ns_add_u32 s63, s52, 2555904\nM 32,164,88\ns_add_u32 m0, s50, 40960\nv_add_f32 v91, v91, v123\nG 5\n"
"v_add_f32 v92, v92, v124\nM 48,160,76\nR 182,190,36864\nv_add_f32 v93, v93, v125\nM 48,164,92\nG 5\n"
"G 4\nv_add_f32 v94, v94, v126\nv_add_f32 v95, v95, v127\nM 132,168,64\nR 160,191,24576\nv_mul_f32 v64, v64, v230\n"
"v_mul_f32 v65, v65, v231\nM 132,172,80\nbuffer_load_dwordx4 v220, s[4:7], s63 offen lds\nv_mul_f32 v66, v66, v232\nv_add_u32 v108, 161792, v252\nM 148,168,68\n"
"R 162,191,28672\nds_read_b128 v[112:115], v108\nv_mul_f32 v67, v67, v233\nv_mul_f32 v68, v68, v234\nM 148,172,84\ns_cmp_eq_u32 s40, 3\n"
"s_cselect_b64 s[66:67], -1, 0\nds_read_b128 v[104:107], v108 offset:1024\nv_mul_f32 v69, v69, v235\nv_mul_f32 v70, v70, v236\nM 36,168,72\nR 164,191,32768\n"
"v_mul_f32 v71, v71, v237\nv_cvt_pk_bf16_f32 v67, v66, v67\nM 36,172,88\nbuffer_load_dwordx4 v221, s[4:7], s63 offen offset:1024 lds\nv_cvt_pk_bf16_f32 v66, v64, v65\nM 52,168,76\n"
"R 166,191,36864\ns_waitcnt vmcnt(9)\ns_barrier\nv_cvt_pk_bf16_f32 v68, v68, v69\nM 52,172,92\nv_cvt_pk_bf16_f32 v69, v70, v71\n"
"v_mul_f32 v72, v72, v230\nG 6\nv_mul_f32 v73, v73, v231\nM 136,176,64\nR 168,188,40960\nv_mul_f32 v74, v74, v232\n"
"v_mul_f32 v75, v75, v233\nv_mov_b32 v124, s76\nM 136,180,80\ns_add_u32 m0, s50, 49152\nv_mul_f32 v76, v76, v234\nv_mul_f32 v77, v77, v235\n"
"v_mul_f32 v78, v78, v236\nG 3\nv_lshlrev_b32 v100, 16, v104\nM 152,176,68\nR 170,188,45056\nv_mul_f32 v79, v79, v237\n"
"v_cvt_pk_bf16_f32 v72, v72, v73\nv_and_b32 v101, s77, v104\nM 152,180,84\ns_add_u32 s63, s52, 2752512\nv_cvt_pk_bf16_f32 v73, v74, v75\nv_cvt_pk_bf16_f32 v74, v76, v77\n"
"v_cvt_pk_bf16_f32 v75, v78, v79\nv_lshlrev_b32 v102, 16, v105\nM 40,176,72\nR 172,188,49152\nv_mul_f32 v80, v80, v230\nv_mul_f32 v81, v81, v231\n"
"v_and_b32 v103, s77, v105\nM 40,180,88\nbuffer_load_dwordx4 v220, s[4:7], s63 offen lds\nv_mul_f32 v82, v82, v232\nv_lshlrev_b32 v96, 16, v106\nM 56,176,76\n"
"R 174,188,53248\nv_mul_f32 v83, v83, v233\nv_mul_f32 v84, v84, v234\nv_and_b32 v97, s77, v106\nM 56,180,92\nG 4\n"
"v_mul_f32 v85, v85, v235\nv_mul_f32 v86, v86, v236\nv_mul_f32 v87, v87, v237\nv_lshlrev_b32 v98, 16, v107\nM 140,160,64\nR 176,189,40960\n"
"v_cvt_pk_bf16_f32 v78, v80, v81\nv_cvt_pk_bf16_f32 v79, v82, v83\nv_and_b32 v99, s77, v107\nM 140,164,80\ns_lshl4_add_u32 s64, s41, s42\nv_cvt_pk_bf16_f32 v80, v84, v85\n"
"v_cvt_pk_bf16_f32 v81, v86, v87\nv_mul_f32 v88, v88, v230\nv_add_f32 v104, s78, v100\nM 156,160,68\nR 178,189,45056\nv_mul_f32 v89, v89, v231\n"
"v_mul_f32 v90, v90, v232\nv_add_f32 v105, s78, v101\nM 156,164,84\ns_lshl_b32 s64, s64, 12\nv_mul_f32 v91, v91, v233\nv_mul_f32 v92, v92, v234\n"
"v_mul_f32 v93, v93, v235\nv_add_f32 v106, s78, v102\nM 44,160,72\nR 180,189,49152\nv_mul_f32 v94, v94, v236\nv_mul_f32 v95, v95, v237\n"
"M 44,164,88\nbuffer_load_dwordx4 v221, s[4:7], s63 offen offset:1024 lds\nv_cvt_pk_bf16_f32 v84, v88, v89\nM 60,160,76\nR 182,189,53248\nv_cvt_pk_bf16_f32 v85, v90, v91\n"
"v_cvt_pk_bf16_f32 v86, v92, v93\nM 60,164,92\nv_cvt_pk_bf16_f32 v87, v94, v95\nv_lshrrev_b32 v232, 1, v252\ns_lshl_b32 s60, s40, 12\ns_add_u32 s60, s60, 102400\n"
"v_add_u32 v230, s60, v252\nv_add_u32 v231, s60, v232\nG 4\nM 128,168,96\nR 160,190,40960\ns_add_u32 s64, s64, 78643200\n"
"s_mov_b64 exec, s[66:67]\nbuffer_store_dwordx4 v[16:19], v252, s[20:23], s64 offen offset:0 sc1\nbuffer_store_dwordx4 v[20:23], v252, s[20:23], s64 offen offset:1024 sc1\ns_mov_b64 exec, -1\nM 128,172,112\ns_add_u32 m0, s50, 57344\n"
"s_mov_b64 exec, s[66:67]\nbuffer_store_dwordx2 v[68:69], v232, s[20:23], s64 offen offset:2048 sc1\nbuffer_store_dwordx2 v[74:75], v232, s[20:23], s64 offen offset:2560 sc1\ns_mov_b64 exec, -1\nv_add_f32 v107, s78, v103\nv_mov_b32 v125, s76\n"
"M 144,168,100\nR 162,190,45056\ns_mov_b64 exec, s[66:67]\nbuffer_store_dwordx2 v[80:81], v232, s[20:23], s64 offen offset:3072 sc1\nbuffer_store_dwordx2 v[86:87], v232, s[20:23], s64 offen offset:3584 sc1\ns_mov_b64 exec, -1\n"
"v_mov_b32 v126, s76\nM 144,172,116\ns_add_u32 s63, s52, 2949120\ns_not_b64 exec, s[66:67]\nds_write_b128 v230, v[16:19] offset:0\nds_write_b128 v230, v[20:23] offset:1024\n"
"s_mov_b64 exec, -1\nv_mov_b32 v127, s76\nM 32,168,104\nR 164,190,49152\nM 32,172,120\nbuffer_load_dwordx4 v220, s[4:7], s63 offen lds\n"
"M 48,168,108\nR 166,190,53248\nM 48,172,124\nG 6\nM 132,176,96\nR 168,191,40960\n"
"M 132,180,112\nM 148,176,100\nR 170,191,45056\nM 148,180,116\nM 36,176,104\nR 172,191,49152\n"
"Lq722:\ns_lshr_b32 s60, s45, 3\ns_lshl2_add_u32 s60, s60, s40\ns_lshl_b32 s60, s60, 8\nv_lshl_add_u32 v224, v252, 3, s60\nv_add_u32 v224, Lq722+4096-LqPC, v224\n"
"s_mov_b64 exec, 3\nglobal_load_dword v224, v224, s[98:99]\ns_mov_b64 exec, -1\nM 36,180,120\nM 52,176,108\nR 174,191,53248\n"
"M 52,180,124\nG 4\nM 136,160,96\nR 176,184,0\ns_not_b64 exec, s[66:67]\nds_write_b64 v231, v[68:69] offset:2048\n"
"ds_write_b64 v231, v[74:75] offset:2560\ns_mov_b64 exec, -1\nM 136,164,112\nbuffer_load_dwordx4 v221, s[4:7], s63 offen offset:1024 lds\nM 152,160,100\nR 178,184,4096\n"
"M 152,164,116\nM 40,160,104\nR 180,184,8192\nM 40,164,120\nM 56,160,108\nR 182,184,12288\n"
"M 56,164,124\ns_not_b64 exec, s[66:67]\nds_write_b64 v231, v[80:81] offset:3072\nds_write_b64 v231, v[86:87] offset:3584\ns_mov_b64 exec, -1\nG 8\n"
"M 140,168,96\nR 160,185,0\nM 140,172,112\nbuffer_store_dwordx4 v[248:251], v252, s[20:23], s100 offen offset:2048 sc1\nM 156,168,100\nR 162,185,4096\n"
"M 156,172,116\nM 44,168,104\nR 164,185,8192\nM 44,172,120\ns_movk_i32 s63, 2048\nv_mad_u32_u24 v220, v228, s63, v229\n"
"v_add_u32 v221, 1047552, v220\ns_add_u32 m0, s50, 81920\ns_add_u32 s63, s53, 0\nbuffer_load_dwordx4 v220, s[8:11], s63 offen lds\nM 60,168,108\nR 166,185,12288\n"
"M 60,172,124\nG 6\nM 128,176,128\nR 168,186,0\nM 128,180,144\nbuffer_load_dwordx4 v221, s[8:11], s63 offen offset:1024 lds\n"
"M 144,176,132\nR 170,186,4096\nM 144,180,148\ns_add_u32 m0, s50, 90112\nM 32,176,136\nR 172,186,8192\n"
"s_waitcnt vmcnt(17) lgkmcnt(7)\ns_barrier\nM 32,180,152\ns_add_u32 s63, s53, 65536\nv_mov_b32 v230, 0\nv_mov_b32 v231, 0\n"
"M 48,176,140\nR 174,186,12288\nv_mov_b32 v232, 0\nM 48,180,156\nG 4\nv_mov_b32 v233, 0\n"
"v_mov_b32 v234, 0\nM 132,160,128\nR 176,187,0\nv_mov_b32 v235, 0\nM 132,164,144\nbuffer_load_dwordx4 v220, s[8:11], s63 offen lds\n"
"M 148,160,132\nR 178,187,4096\nv_mov_b32 v236, 0\nM 148,164,148\ns_lshl4_add_u32 s65, s41, s42\nv_mov_b32 v237, 0\n"
"v_mov_b32 v64, 0\nM 36,160,136\nR 180,187,8192\nv_mov_b32 v65, 0\nM 36,164,152\ns_lshl_b32 s65, s65, 12\n"
"v_mov_b32 v70, 0\nv_mov_b32 v71, 0\nM 52,160,140\nR 182,187,12288\nv_mov_b32 v76, 0\nM 52,164,156\n"
"G 4\nv_mov_b32 v77, 0\nv_mov_b32 v82, 0\nM 136,168,128\nR 160,184,16384\nv_mov_b32 v83, 0\n"
"M 136,172,144\nbuffer_load_dwordx4 v221, s[8:11], s63 offen offset:1024 lds\nM 152,168,132\nR 162,184,20480\nv_lshrrev_b32 v227, 1, v252\nM 152,172,148\n"
"s_add_u32 m0, s50, 98304\ns_add_u32 s65, s65, 78577664\nM 40,168,136\nR 164,184,24576\nM 40,172,152\ns_add_u32 s63, s53, 131072\n"
"M 56,168,140\nR 166,184,28672\nM 56,172,156\nG 4\nM 140,176,128\nR 168,185,16384\n"
"M 140,180,144\nbuffer_load_dwordx4 v220, s[8:11], s63 offen lds\nM 156,176,132\nR 170,185,20480\nM 156,180,148\nM 44,176,136\n"
"R 172,185,24576\nM 44,180,152\nM 60,176,140\nR 174,185,28672\nM 60,180,156\nG 4\n"
"M 128,160,160\nR 176,186,16384\nM 128,164,176\nbuffer_load_dwordx4 v221, s[8:11], s63 offen offset:1024 lds\nM 144,160,164\nR 178,186,20480\n"
"s_add_u32 s63, s53, 196608\nM 144,164,180\ns_add_u32 m0, s50, 106496\ns_lshl_b32 s60, s40, 12\ns_add_u32 s60, s60, 98304\nv_add_u32 v226, s60, v252\n"
"v_add_u32 v111, s60, v227\nM 32,160,168\nR 180,186,24576\ns_mov_b32 s60, 155648\nv_bfe_u32 v246, v252, 4, 4\nv_lshl_add_u32 v246, v246, 3, s60\n"
"M 32,164,184\nbuffer_load_dwordx4 v220, s[8:11], s63 offen lds\nM 48,160,172\nR 182,186,28672\nds_read_b64 v[238:239], v246 offset:0\nM 48,164,188\n"
"s_cmp_eq_u32 s40, 0\ns_cselect_b64 s[88:89], 0, -1\ns_cselect_b32 s60, s41, 0\ns_cmp_lg_u32 s60, 0\ns_cselect_b64 s[86:87], -1, 0\nG 5\n"
"M 132,168,160\nR 160,187,16384\nds_read_b64 v[240:241], v246 offset:128\nM 132,172,176\nbuffer_load_dwordx4 v221, s[8:11], s63 offen offset:1024 lds\nM 148,168,164\n"
"R 162,187,20480\nds_read_b64 v[242:243], v246 offset:256\nM 148,172,180\nM 36,168,168\nR 164,187,24576\nds_read_b64 v[244:245], v246 offset:384\n"
"M 36,172,184\ns_mov_b64 exec, s[88:89]\nds_read_b128 v[230:233], v226 offset:0\nds_read_b128 v[234:237], v226 offset:1024\ns_mov_b64 exec, -1\nM 52,168,172\n"
"R 166,187,28672\nds_read_b64 v[246:247], v246 offset:0\nM 52,172,188\nG 11\nM 136,176,160\nR 168,184,32768\n"
"M 136,180,176\nbuffer_store_dwordx4 v[248:251], v252, s[20:23], s100 offen offset:3072 sc1\nM 152,176,164\nR 170,184,36864\ns_waitcnt vmcnt(10) lgkmcnt(2)\ns_barrier\n"
"M 152,180,180\ns_mov_b64 exec, s[88:89]\nds_read_b64 v[64:65], v111 offset:2048\nds_read_b64 v[70:71], v111 offset:2560\ns_mov_b64 exec, -1\nM 40,176,168\n"
"R 172,184,40960\nv_mfma_f32_16x16x32_bf16 v[128:131], v[238:241], v[208:211], v[128:131]\ns_add_u32 m0, s50, 114688\nM 40,180,184\ns_mov_b64 exec, s[88:89]\nds_read_b64 v[76:77], v111 offset:3072\n"
"ds_read_b64 v[82:83], v111 offset:3584\ns_mov_b64 exec, -1\nM 56,176,172\nR 174,184,45056\nv_mfma_f32_16x16x32_bf16 v[132:135], v[240:243], v[208:211], v[132:135]\ns_add_u32 s63, s53, 262144\n"
"M 56,180,188\nM 140,160,160\nR 176,185,32768\nv_mfma_f32_16x16x32_bf16 v[136:139], v[242:245], v[208:211], v[136:139]\nM 140,164,176\nbuffer_load_dwordx4 v220, s[8:11], s63 offen lds\n"
"M 156,160,164\nR 178,185,36864\nv_mfma_f32_16x16x32_bf16 v[140:143], v[244:247], v[208:211], v[140:143]\nM 156,164,180\nM 44,160,168\nR 180,185,40960\n"
"v_mfma_f32_16x16x32_bf16 v[144:147], v[244:247], v[210:213], v[144:147]\nM 44,164,184\ns_mov_b64 exec, s[86:87]\nbuffer_load_dwordx4 v[230:233], v252, s[20:23], s65 offen offset:0 sc1\nbuffer_load_dwordx4 v[234:237], v252, s[20:23], s65 offen offset:1024 sc1\ns_mov_b64 exec, -1\n"
"M 60,160,172\nR 182,185,45056\nv_mfma_f32_16x16x32_bf16 v[148:151], v[238:241], v[210:213], v[148:151]\nM 60,164,188\nG 4\nM 32,168,200\n"
"R 160,186,32768\nv_mfma_f32_16x16x32_bf16 v[152:155], v[240:243], v[210:213], v[152:155]\nM 32,172,216\nR 162,186,36864\nM 48,168,204\nR 164,186,40960\n"
"v_mfma_f32_16x16x32_bf16 v[156:159], v[242:245], v[210:213], v[156:159]\ns_mov_b64 exec, s[86:87]\nbuffer_load_dwordx2 v[64:65], v227, s[20:23], s65 offen offset:2048 sc1\nbuffer_load_dwordx2 v[70:71], v227, s[20:23], s65 offen offset:2560 sc1\ns_mov_b64 exec, -1\nM 48,172,220\n"
"R 166,186,45056\nG 4\nM 36,176,200\nR 168,187,32768\nM 36,180,216\nR 170,187,36864\n"
"M 52,176,204\nR 172,187,40960\nM 52,180,220\nR 174,187,45056\nG 4\nM 40,160,200\n"
"R 176,184,49152\ns_mov_b64 exec, s[86:87]\nbuffer_load_dwordx2 v[76:77], v227, s[20:23], s65 offen offset:3072 sc1\nbuffer_load_dwordx2 v[82:83], v227, s[20:23], s65 offen offset:3584 sc1\ns_mov_b64 exec, -1\nM 40,164,216\n"
"R 178,184,53248\nM 56,160,204\nR 180,184,57344\nM 56,164,220\nR 182,184,61440\nG 4\n"
"M 44,168,200\nR 160,185,49152\nM 44,172,216\nR 162,185,53248\nM 60,168,204\nR 164,185,57344\n"
"M 60,172,220\nR 166,185,61440\nG 4\nM 32,176,232\nR 168,186,49152\nM 32,180,248\n"
"R 170,186,53248\nM 48,176,236\nR 172,186,57344\nM 48,180,252\nR 174,186,61440\nG 4\n"
"M 36,160,232\nR 176,187,49152\nM 36,164,248\nR 178,187,53248\nM 52,160,236\nR 180,187,57344\n"
"M 52,164,252\nR 182,187,61440\nG 4\nM 40,168,232\nR 160,184,32768\nM 40,172,248\n"
"R 162,184,36864\nM 56,168,236\nR 164,184,40960\nM 56,172,252\nR 166,184,45056\nG 4\n"
"M 44,176,232\nR 168,185,32768\nM 44,180,248\nR 170,185,36864\nM 60,176,236\nR 172,185,40960\n"
"M 60,180,252\nR 174,185,45056\nG 4\nM 128,160,192\nR 176,186,32768\nM 128,164,208\n"
"R 178,186,36864\ns_mov_b32 m0, s51\nM 144,160,196\nR 180,186,40960\nds_write_addtid_b32 v32 offset:0\nM 144,164,212\n"
"R 182,186,45056\nds_write_addtid_b32 v33 offset:256\nG 6\nM 132,168,192\nR 160,187,32768\nds_write_addtid_b32 v34 offset:512\n"
"M 132,172,208\nR 162,187,36864\nds_write_addtid_b32 v35 offset:768\nM 148,168,196\nR 164,187,40960\nds_write_addtid_b32 v36 offset:1024\n"
"M 148,172,212\nR 166,187,45056\nds_write_addtid_b32 v37 offset:1280\nG 9\nM 136,176,192\nR 168,184,49152\n"
"ds_write_addtid_b32 v38 offset:1536\nM 136,180,208\nR 170,184,53248\nds_write_addtid_b32 v39 offset:1792\nM 152,176,196\nR 172,184,57344\n"
"ds_write_addtid_b32 v40 offset:2048\nM 152,180,212\nR 174,184,61440\nds_write_addtid_b32 v41 offset:2304\nG 9\nM 140,160,192\n"
"R 176,185,49152\nds_write_addtid_b32 v42 offset:2560\nM 140,164,208\nR 178,185,53248\nds_write_addtid_b32 v43 offset:2816\nM 156,160,196\n"
"R 180,185,57344\nds_write_addtid_b32 v44 offset:3072\nds_write_addtid_b32 v45 offset:3328\nds_write_addtid_b32 v46 offset:3584\nds_write_addtid_b32 v47 offset:3840\nds_write_addtid_b32 v48 offset:4096\n"
"ds_write_addtid_b32 v49 offset:4352\nds_write_addtid_b32 v50 offset:4608\nds_write_addtid_b32 v51 offset:4864\nds_write_addtid_b32 v52 offset:5120\nds_write_addtid_b32 v53 offset:5376\nds_write_addtid_b32 v54 offset:5632\n"
"ds_write_addtid_b32 v55 offset:5888\nds_write_addtid_b32 v56 offset:6144\nds_write_addtid_b32 v57 offset:6400\nds_write_addtid_b32 v58 offset:6656\nds_write_addtid_b32 v59 offset:6912\nds_write_addtid_b32 v60 offset:7168\n"
"ds_write_addtid_b32 v61 offset:7424\nds_write_addtid_b32 v62 offset:7680\nds_write_addtid_b32 v63 offset:7936\nM 156,164,212\nR 182,185,61440\ns_add_u32 m0, s50, 114688\n"
"s_nop 0\nbuffer_load_dwordx4 v221, s[8:11], s63 offen offset:1024 lds\nG 15\nM 128,168,224\nR 160,186,49152\ns_add_u32 m0, s50, 0\n"
"s_add_u32 s63, s53, 327680\nbuffer_load_dwordx4 v220, s[8:11], s63 offen lds\nM 128,172,240\nR 162,186,53248\nG 3\ns_barrier\n"
"M 144,168,228\nR 164,186,57344\nbuffer_load_dwordx4 v221, s[8:11], s63 offen offset:1024 lds\nM 144,172,244\nR 166,186,61440\nG 4\n"
"M 132,176,224\nR 168,187,49152\nM 132,180,240\nR 170,187,53248\nds_read2st64_b32 v[32:33], v225 offset0:0 offset1:1\nM 148,176,228\n"
"R 172,187,57344\nds_read2st64_b32 v[34:35], v225 offset0:2 offset1:3\nM 148,180,244\nR 174,187,61440\nG 6\nM 136,160,224\n"
"s_add_u32 m0, s50, 8192\nds_read2st64_b32 v[36:37], v225 offset0:4 offset1:5\nds_read2st64_b32 v[38:39], v225 offset0:6 offset1:7\nM 136,164,240\ns_add_u32 s63, s53, 393216\nds_read2st64_b32 v[40:41], v225 offset0:8 offset1:9\n"
"ds_read2st64_b32 v[42:43], v225 offset0:10 offset1:11\nM 152,160,228\nds_read2st64_b32 v[44:45], v225 offset0:12 offset1:13\nds_read2st64_b32 v[46:47], v225 offset0:14 offset1:15\nM 152,164,244\nds_read2st64_b32 v[48:49], v225 offset0:16 offset1:17\n"
"ds_read2st64_b32 v[50:51], v225 offset0:18 offset1:19\nG 8\nM 140,168,224\nbuffer_load_dwordx4 v220, s[8:11], s63 offen lds\nM 140,172,240\nds_read2st64_b32 v[52:53], v225 offset0:20 offset1:21\n"
"ds_read2st64_b32 v[54:55], v225 offset0:22 offset1:23\nM 156,168,228\nds_read2st64_b32 v[56:57], v225 offset0:24 offset1:25\nds_read2st64_b32 v[58:59], v225 offset0:26 offset1:27\nM 156,172,244\nds_read2st64_b32 v[60:61], v225 offset0:28 offset1:29\n"
"ds_read2st64_b32 v[62:63], v225 offset0:30 offset1:31\nG 0\ns_barrier\ns_waitcnt vmcnt(7)\nv_max3_u32 v248, v233, v237, v65\ns_waitcnt vmcnt(5)\n"
"v_max3_u32 v248, v248, v71, v77\ns_waitcnt vmcnt(4)\nv_max_u32 v248, v248, v83\nv_cmp_eq_u32 vcc, -1, v248\ns_nop 1\ns_cbranch_vccnz Lq696\n"
"Lq697:\nv_pk_add_f32 v[144:145], v[144:145], v[48:49]\nv_pk_add_f32 v[146:147], v[146:147], v[50:51]\nv_pk_add_f32 v[148:149], v[148:149], v[52:53]\nv_pk_add_f32 v[150:151], v[150:151], v[54:55]\nv_cvt_pk_bf16_f32 v144, v144, v145\n"
"v_cvt_pk_bf16_f32 v145, v146, v147\nv_cvt_pk_bf16_f32 v146, v148, v149\nv_cvt_pk_bf16_f32 v147, v150, v151\ns_nop 1\nv_mfma_f32_16x16x32_bf16 v[52:55], v[16:19], v[144:147], v[96:99]\nv_mfma_f32_16x16x32_bf16 v[48:51], v[0:3], v[144:147], v[100:103]\n"
"v_pk_add_f32 v[152:153], v[152:153], v[56:57]\nv_pk_add_f32 v[154:155], v[154:155], v[58:59]\nv_pk_add_f32 v[156:157], v[156:157], v[60:61]\nv_pk_add_f32 v[158:159], v[158:159], v[62:63]\nv_cvt_pk_bf16_f32 v152, v152, v153\nv_cvt_pk_bf16_f32 v153, v154, v155\n"
"v_cvt_pk_bf16_f32 v154, v156, v157\nv_cvt_pk_bf16_f32 v155, v158, v159\ns_nop 1\nv_mfma_f32_16x16x32_bf16 v[60:63], v[20:23], v[152:155], v[96:99]\nv_mfma_f32_16x16x32_bf16 v[56:59], v[4:7], v[152:155], v[100:103]\nv_pk_add_f32 v[128:129], v[128:129], v[32:33]\n"
"v_pk_add_f32 v[130:131], v[130:131], v[34:35]\nv_pk_add_f32 v[132:133], v[132:133], v[36:37]\nv_pk_add_f32 v[134:135], v[134:135], v[38:39]\nv_cvt_pk_bf16_f32 v128, v128, v129\nv_cvt_pk_bf16_f32 v129, v130, v131\nv_cvt_pk_bf16_f32 v130, v132, v133\n"
"v_cvt_pk_bf16_f32 v131, v134, v135\ns_nop 1\nv_mfma_f32_16x16x32_bf16 v[36:39], v[0:3], v[128:131], v[96:99]\nv_mfma_f32_16x16x32_bf16 v[32:35], v[230:233], v[128:131], v[104:107]\nv_pk_add_f32 v[136:137], v[136:137], v[40:41]\nv_pk_add_f32 v[138:139], v[138:139], v[42:43]\n"
"v_pk_add_f32 v[140:141], v[140:141], v[44:45]\nv_pk_add_f32 v[142:143], v[142:143], v[46:47]\nv_cvt_pk_bf16_f32 v136, v136, v137\nv_cvt_pk_bf16_f32 v137, v138, v139\nv_cvt_pk_bf16_f32 v138, v140, v141\nv_cvt_pk_bf16_f32 v139, v142, v143\n"
"s_nop 1\nv_mfma_f32_16x16x32_bf16 v[44:47], v[4:7], v[136:139], v[96:99]\nv_mfma_f32_16x16x32_bf16 v[40:43], v[234:237], v[136:139], v[104:107]\nv_max3_f32 v52, v52, v53, v48\nv_max3_f32 v60, v60, v61, v56\nv_max3_f32 v36, v36, v37, v32\n"
"s_nop 4\nv_max3_f32 v44, v44, v45, v40\nv_max_f32 v52, v52, v49\nv_max_f32 v60, v60, v57\nv_max_f32 v36, v36, v33\nv_max_f32 v44, v44, v41\n"
"v_max3_f32 v54, v54, v55, v50\nv_max3_f32 v62, v62, v63, v58\nv_max3_f32 v38, v38, v39, v34\nv_max3_f32 v46, v46, v47, v42\nv_max_f32 v54, v54, v51\nv_max_f32 v62, v62, v59\n"
"v_max_f32 v38, v38, v35\nv_max_f32 v46, v46, v43\nv_max_f32 v118, v52, v54\nv_max_f32 v119, v60, v62\nv_max_f32 v116, v36, v38\nv_max_f32 v117, v44, v46\n"
"v_mov_b32 v120, v116\nv_mov_b32 v121, v117\nv_mov_b32 v122, v118\nv_mov_b32 v123, v119\nv_permlane32_swap_b32 v120, v116\nv_permlane32_swap_b32 v121, v117\n"
"v_permlane32_swap_b32 v122, v118\nv_permlane32_swap_b32 v123, v119\nv_max_f32 v116, v116, v120\nv_max_f32 v117, v117, v121\nv_max_f32 v118, v118, v122\nv_max_f32 v119, v119, v123\n"
"v_mov_b32 v120, v116\nv_mov_b32 v121, v117\nv_mov_b32 v122, v118\nv_mov_b32 v123, v119\nv_permlane16_swap_b32 v120, v116\nv_permlane16_swap_b32 v121, v117\n"
"v_permlane16_swap_b32 v122, v118\nv_permlane16_swap_b32 v123, v119\nv_max_f32 v116, v116, v120\nv_max_f32 v117, v117, v121\nv_max_f32 v118, v118, v122\nv_max_f32 v119, v119, v123\n"
"v_mul_f32 v118, v118, v218\nv_mul_f32 v119, v119, v218\nv_mul_f32 v116, v116, v215\nv_mul_f32 v117, v117, v215\nv_fma_f32 v52, v52, v218, -v118\nv_fma_f32 v54, v54, v218, -v118\n"
"v_fma_f32 v60, v60, v218, -v119\nv_fma_f32 v62, v62, v218, -v119\nv_fma_f32 v36, v36, v215, -v116\nv_fma_f32 v38, v38, v215, -v116\nv_fma_f32 v44, v44, v215, -v117\nv_fma_f32 v46, v46, v215, -v117\n"
"v_exp_f32 v52, v52\nv_exp_f32 v54, v54\nv_exp_f32 v60, v60\nv_exp_f32 v62, v62\nv_exp_f32 v36, v36\nv_exp_f32 v38, v38\n"
"v_exp_f32 v44, v44\nv_exp_f32 v46, v46\nv_cvt_pk_bf16_f32 v52, v52, v52\nv_cvt_pk_bf16_f32 v54, v54, v54\nv_and_b32 v48, v52, v112\nv_and_b32 v49, v54, v113\n"
"v_and_b32 v50, v52, v114\nv_and_b32 v51, v54, v115\ns_nop 1\nv_mfma_f32_16x16x32_bf16 v[24:27], v[124:127], v[48:51], 0\nv_mfma_f32_16x16x32_bf16 v[144:147], v[66:69], v[48:51], 0\nv_mfma_f32_16x16x32_bf16 v[148:151], v[72:75], v[48:51], 0\n"
"v_cvt_pk_bf16_f32 v60, v60, v60\nv_cvt_pk_bf16_f32 v62, v62, v62\nv_and_b32 v56, v60, v112\nv_and_b32 v57, v62, v113\nv_and_b32 v58, v60, v114\nv_and_b32 v59, v62, v115\n"
"s_nop 1\nv_mfma_f32_16x16x32_bf16 v[28:31], v[124:127], v[56:59], 0\nv_mfma_f32_16x16x32_bf16 v[152:155], v[78:81], v[56:59], 0\nv_mfma_f32_16x16x32_bf16 v[156:159], v[84:87], v[56:59], 0\nv_cvt_pk_bf16_f32 v36, v36, v36\nv_cvt_pk_bf16_f32 v38, v38, v38\n"
"v_and_b32 v32, v36, v112\nv_and_b32 v33, v38, v113\nv_and_b32 v34, v36, v114\nv_and_b32 v35, v38, v115\ns_nop 1\nv_mfma_f32_16x16x32_bf16 v[8:11], v[124:127], v[32:35], 0\n"
"v_mfma_f32_16x16x32_bf16 v[128:131], v[64:67], v[32:35], 0\nv_mfma_f32_16x16x32_bf16 v[132:135], v[70:73], v[32:35], 0\nv_cvt_pk_bf16_f32 v44, v44, v44\nv_cvt_pk_bf16_f32 v46, v46, v46\nv_and_b32 v40, v44, v112\nv_and_b32 v41, v46, v113\n"
"v_and_b32 v42, v44, v114\nv_and_b32 v43, v46, v115\ns_nop 1\nv_mfma_f32_16x16x32_bf16 v[12:15], v[124:127], v[40:43], 0\nv_mfma_f32_16x16x32_bf16 v[136:139], v[76:79], v[40:43], 0\nv_mfma_f32_16x16x32_bf16 v[140:143], v[82:85], v[40:43], 0\n"
"s_lshl_b32 s84, s42, 11\ns_add_u32 s84, s84, s49\ns_add_u32 s84, s84, 6291456\ns_add_u32 s85, s84, 32768\nv_mov_b32 v238, s46\nv_mov_b32 v239, s47\n"
"v_rcp_f32 v24, v24\nv_rcp_f32 v28, v28\nv_rcp_f32 v8, v8\nv_rcp_f32 v12, v12\nv_pk_mul_f32 v[144:145], v[144:145], v[24:25] op_sel_hi:[1,0]\nv_pk_mul_f32 v[146:147], v[146:147], v[24:25] op_sel_hi:[1,0]\n"
"v_pk_mul_f32 v[148:149], v[148:149], v[24:25] op_sel_hi:[1,0]\nv_pk_mul_f32 v[150:151], v[150:151], v[24:25] op_sel_hi:[1,0]\nv_cvt_pk_bf16_f32 v144, v144, v145\nv_cvt_pk_bf16_f32 v145, v146, v147\nv_cvt_pk_bf16_f32 v146, v148, v149\nv_cvt_pk_bf16_f32 v147, v150, v151\n"
"buffer_store_dwordx4 v[144:147], v252, s[20:23], s85 offen offset:0 sc1\nv_pk_mul_f32 v[152:153], v[152:153], v[28:29] op_sel_hi:[1,0]\nv_pk_mul_f32 v[154:155], v[154:155], v[28:29] op_sel_hi:[1,0]\nv_pk_mul_f32 v[156:157], v[156:157], v[28:29] op_sel_hi:[1,0]\nv_pk_mul_f32 v[158:159], v[158:159], v[28:29] op_sel_hi:[1,0]\nv_cvt_pk_bf16_f32 v152, v152, v153\n"
"v_cvt_pk_bf16_f32 v153, v154, v155\nv_cvt_pk_bf16_f32 v154, v156, v157\nv_cvt_pk_bf16_f32 v155, v158, v159\nbuffer_store_dwordx4 v[152:155], v252, s[20:23], s85 offen offset:1024 sc1\nv_pk_mul_f32 v[128:129], v[128:129], v[8:9] op_sel_hi:[1,0]\nv_pk_mul_f32 v[130:131], v[130:131], v[8:9] op_sel_hi:[1,0]\n"
"v_pk_mul_f32 v[132:133], v[132:133], v[8:9] op_sel_hi:[1,0]\nv_pk_mul_f32 v[134:135], v[134:135], v[8:9] op_sel_hi:[1,0]\nv_cvt_pk_bf16_f32 v128, v128, v129\nv_cvt_pk_bf16_f32 v129, v130, v131\nv_cvt_pk_bf16_f32 v130, v132, v133\nv_cvt_pk_bf16_f32 v131, v134, v135\n"
"buffer_store_dwordx4 v[128:131], v252, s[20:23], s84 offen offset:0 sc1\nv_pk_mul_f32 v[136:137], v[136:137], v[12:13] op_sel_hi:[1,0]\nv_pk_mul_f32 v[138:139], v[138:139], v[12:13] op_sel_hi:[1,0]\nv_pk_mul_f32 v[140:141], v[140:141], v[12:13] op_sel_hi:[1,0]\nv_pk_mul_f32 v[142:143], v[142:143], v[12:13] op_sel_hi:[1,0]\nv_cvt_pk_bf16_f32 v136, v136, v137\n"
"v_cvt_pk_bf16_f32 v137, v138, v139\nv_cvt_pk_bf16_f32 v138, v140, v141\nv_cvt_pk_bf16_f32 v139, v142, v143\nbuffer_store_dwordx4 v[136:139], v252, s[20:23], s84 offen offset:1024 sc1\ns_add_u32 s61, s79, 88498176\ns_lshl_b32 s62, s42, 3\n"
"s_add_u32 s61, s61, s62\ns_waitcnt vmcnt(0)\ns_mov_b64 exec, 1\nbuffer_atomic_swap_x2 v[238:239], off, s[20:23], s61 sc1\ns_mov_b64 exec, -1\nv_bfe_u32 v250, v252, 7, 1\n"
"v_mul_u32_u24 v250, 2176, v250\nv_bfe_u32 v251, v252, 4, 3\nv_lshl_add_u32 v250, v251, 3, v250\ns_lshl_b32 s60, s90, 6\nv_add_u32 v250, s60, v250\nbuffer_load_dwordx4 v221, s[8:11], s63 offen offset:1024 lds\n"
"s_add_u32 m0, s50, 16384\ns_add_u32 s63, s53, 458752\nbuffer_load_dwordx4 v220, s[8:11], s63 offen lds\nbuffer_load_dwordx4 v221, s[8:11], s63 offen offset:1024 lds\ns_add_u32 m0, s50, 24576\ns_add_u32 s63, s53, 524288\n"
"buffer_load_dwordx4 v220, s[8:11], s63 offen lds\nbuffer_load_dwordx4 v221, s[8:11], s63 offen offset:1024 lds\ns_add_u32 m0, s50, 32768\ns_add_u32 s63, s53, 589824\nbuffer_load_dwordx4 v220, s[8:11], s63 offen lds\nbuffer_load_dwordx4 v221, s[8:11], s63 offen offset:1024 lds\n"
"s_add_u32 m0, s50, 40960\ns_add_u32 s63, s53, 655360\nbuffer_load_dwordx4 v220, s[8:11], s63 offen lds\nbuffer_load_dwordx4 v221, s[8:11], s63 offen offset:1024 lds\ns_add_u32 m0, s50, 49152\ns_add_u32 s63, s53, 720896\n"
"buffer_load_dwordx4 v220, s[8:11], s63 offen lds\nbuffer_load_dwordx4 v221, s[8:11], s63 offen offset:1024 lds\ns_add_u32 m0, s50, 57344\ns_add_u32 s63, s53, 786432\nbuffer_load_dwordx4 v220, s[8:11], s63 offen lds\nv_mov_b32 v248, 0\n"
"v_mov_b32 v249, 0\ns_add_u32 s61, s80, 88498176\ns_mov_b64 exec, 0xffff\nbuffer_atomic_or_x2 v[248:249], v250, s[20:23], s61 offen sc0 sc1\ns_mov_b64 exec, -1\nv_bfe_u32 v234, v252, 4, 4\n"
"v_lshrrev_b32 v235, 8, v252\nv_lshlrev_b32 v235, 2, v235\nv_sub_u32 v234, v234, v235\nv_lshlrev_b32 v235, 4, v234\ns_mov_b64 s[60:61], 0x3f80\nv_lshlrev_b64 v[208:209], v235, s[60:61]\n"
"v_cmp_gt_u32 s[60:61], 4, v234\ns_nop 1\nv_cndmask_b32 v212, 0, v208, s[60:61]\nv_cndmask_b32 v208, 0, v208, s[60:61]\nv_cndmask_b32 v213, 0, v209, s[60:61]\nv_cndmask_b32 v209, 0, v209, s[60:61]\n"
"s_nop 1\nv_mfma_f32_16x16x32_bf16 v[0:3], v[208:211], v[192:195], 0\nv_mfma_f32_16x16x32_bf16 v[4:7], v[210:213], v[192:195], 0\nv_mfma_f32_16x16x32_bf16 v[8:11], v[208:211], v[196:199], 0\nv_mfma_f32_16x16x32_bf16 v[12:15], v[210:213], v[196:199], 0\nv_mfma_f32_16x16x32_bf16 v[16:19], v[208:211], v[200:203], 0\n"
"v_mfma_f32_16x16x32_bf16 v[20:23], v[210:213], v[200:203], 0\nv_mfma_f32_16x16x32_bf16 v[24:27], v[208:211], v[204:207], 0\nv_mfma_f32_16x16x32_bf16 v[28:31], v[210:213], v[204:207], 0\ns_waitcnt vmcnt(0)\nv_cmp_eq_u64 s[86:87], s[46:47], v[248:249]\ns_nop 0\n"
"s_and_b32 s62, s86, 0xffff\ns_cmp_eq_u32 s62, 0xffff\ns_cbranch_scc0 Lq725\nLq726:\ns_add_u32 s64, s70, 6291456\nbuffer_load_dwordx4 a[0:3], v252, s[20:23], s64 offen offset:0 sc1\n"
"s_add_u32 s65, s70, 6324224\nbuffer_load_dwordx4 a[4:7], v252, s[20:23], s65 offen offset:0 sc1\ns_add_u32 s66, s71, 6291456\nbuffer_load_dwordx4 a[8:11], v252, s[20:23], s66 offen offset:0 sc1\ns_add_u32 s67, s71, 6324224\nbuffer_load_dwordx4 a[12:15], v252, s[20:23], s67 offen offset:0 sc1\n"
"buffer_load_dwordx4 a[16:19], v252, s[20:23], s64 offen offset:1024 sc1\nbuffer_load_dwordx4 a[20:23], v252, s[20:23], s65 offen offset:1024 sc1\nbuffer_load_dwordx4 a[24:27], v252, s[20:23], s66 offen offset:1024 sc1\nbuffer_load_dwordx4 a[28:31], v252, s[20:23], s67 offen offset:1024 sc1\ns_barrier\nR 160,188,16384\n"
"R 162,188,20480\nR 164,188,24576\nR 166,188,28672\nR 168,189,16384\nR 170,189,20480\nR 172,189,24576\n"
"R 174,189,28672\nR 176,190,16384\nR 178,190,20480\nR 180,190,24576\nR 182,190,28672\nbuffer_load_dwordx4 a[32:35], v252, s[20:23], s64 offen offset:2048 sc1\n"
"buffer_load_dwordx4 a[36:39], v252, s[20:23], s65 offen offset:2048 sc1\nbuffer_load_dwordx4 a[40:43], v252, s[20:23], s66 offen offset:2048 sc1\nbuffer_load_dwordx4 a[44:47], v252, s[20:23], s67 offen offset:2048 sc1\nbuffer_load_dwordx4 a[48:51], v252, s[20:23], s64 offen offset:3072 sc1\nbuffer_load_dwordx4 a[52:55], v252, s[20:23], s65 offen offset:3072 sc1\nbuffer_load_dwordx4 a[56:59], v252, s[20:23], s66 offen offset:3072 sc1\n"
"buffer_load_dwordx4 a[60:63], v252, s[20:23], s67 offen offset:3072 sc1\ns_add_u32 s64, s70, 6295552\nbuffer_load_dwordx4 a[64:67], v252, s[20:23], s64 offen offset:0 sc1\ns_add_u32 s65, s70, 6328320\nbuffer_load_dwordx4 a[68:71], v252, s[20:23], s65 offen offset:0 sc1\ns_add_u32 s66, s71, 6295552\n"
"buffer_load_dwordx4 a[72:75], v252, s[20:23], s66 offen offset:0 sc1\ns_add_u32 s67, s71, 6328320\nbuffer_load_dwordx4 a[76:79], v252, s[20:23], s67 offen offset:0 sc1\ns_waitcnt vmcnt(19) lgkmcnt(10)\nM 0,160,0\nbuffer_load_dwordx4 a[80:83], v252, s[20:23], s64 offen offset:1024 sc1\n"
"s_waitcnt vmcnt(16) lgkmcnt(8)\nM 0,164,16\nM 16,160,4\ns_waitcnt vmcnt(15)\nM 16,164,20\nv_mfma_f32_16x16x32_bf16 v[32:35], v[160:163], a[8:11], 0\n"
"buffer_load_dwordx4 a[84:87], v252, s[20:23], s65 offen offset:1024 sc1\ns_waitcnt vmcnt(15)\nM 32,164,24\nv_mfma_f32_16x16x32_bf16 v[48:51], v[160:163], a[12:15], 0\ns_waitcnt vmcnt(14)\nM 48,164,28\n"
"G 4\nM 4,168,0\nR 160,191,16384\nM 4,172,16\nbuffer_load_dwordx4 a[88:91], v252, s[20:23], s66 offen offset:1024 sc1\nM 20,168,4\n"
"R 162,191,20480\nM 20,172,20\nv_mfma_f32_16x16x32_bf16 v[36:39], v[168:171], a[8:11], 0\nR 164,191,24576\nM 36,172,24\nbuffer_load_dwordx4 a[92:95], v252, s[20:23], s67 offen offset:1024 sc1\n"
"v_mfma_f32_16x16x32_bf16 v[52:55], v[168:171], a[12:15], 0\nR 166,191,28672\nM 52,172,28\nG 4\nM 8,176,0\nR 168,188,32768\n"
"M 8,180,16\nbuffer_load_dwordx4 a[96:99], v252, s[20:23], s64 offen offset:2048 sc1\nM 24,176,4\nR 170,188,36864\nM 24,180,20\nv_mfma_f32_16x16x32_bf16 v[40:43], v[176:179], a[8:11], 0\n"
"R 172,188,40960\nM 40,180,24\nbuffer_load_dwordx4 a[100:103], v252, s[20:23], s65 offen offset:2048 sc1\nv_mfma_f32_16x16x32_bf16 v[56:59], v[176:179], a[12:15], 0\nR 174,188,45056\nM 56,180,28\n"
"G 4\nM 12,160,0\nR 176,189,32768\nM 12,164,16\nbuffer_load_dwordx4 a[104:107], v252, s[20:23], s66 offen offset:2048 sc1\nM 28,160,4\n"
"R 178,189,36864\nM 28,164,20\nv_mfma_f32_16x16x32_bf16 v[44:47], v[160:163], a[8:11], 0\nR 180,189,40960\nM 44,164,24\nbuffer_load_dwordx4 a[108:111], v252, s[20:23], s67 offen offset:2048 sc1\n"
"v_mfma_f32_16x16x32_bf16 v[60:63], v[160:163], a[12:15], 0\nR 182,189,45056\nM 60,164,28\nG 4\ns_waitcnt vmcnt(19)\nM 0,168,32\n"
"R 160,190,32768\ns_waitcnt vmcnt(15)\nM 0,172,48\nbuffer_load_dwordx4 a[112:115], v252, s[20:23], s64 offen offset:3072 sc1\nM 16,168,36\nR 162,190,36864\n"
"s_waitcnt vmcnt(15)\nM 16,172,52\nM 32,168,40\nR 164,190,40960\ns_waitcnt vmcnt(14)\nM 32,172,56\n"
"buffer_load_dwordx4 a[116:119], v252, s[20:23], s65 offen offset:3072 sc1\nM 48,168,44\nR 166,190,45056\ns_waitcnt vmcnt(14)\nM 48,172,60\nG 4\n"
"M 4,176,32\nR 168,191,32768\nM 4,180,48\nbuffer_load_dwordx4 a[120:123], v252, s[20:23], s66 offen offset:3072 sc1\nM 20,176,36\nR 170,191,36864\n"
"M 20,180,52\nM 36,176,40\nR 172,191,40960\nM 36,180,56\nbuffer_load_dwordx4 a[124:127], v252, s[20:23], s67 offen offset:3072 sc1\nM 52,176,44\n"
"R 174,191,45056\nM 52,180,60\nG 4\nM 8,160,32\nR 176,188,49152\nM 8,164,48\n"
"s_add_u32 s64, s70, 6299648\nbuffer_load_dwordx4 a[128:131], v252, s[20:23], s64 offen offset:0 sc1\nM 24,160,36\nR 178,188,53248\nM 24,164,52\nM 40,160,40\n"
"R 180,184,0\nM 40,164,56\ns_add_u32 s65, s70, 6332416\nbuffer_load_dwordx4 a[132:135], v252, s[20:23], s65 offen offset:0 sc1\nM 56,160,44\nR 182,184,4096\n"
"M 56,164,60\nG 4\nM 12,168,32\nR 160,189,49152\nM 12,172,48\ns_add_u32 s66, s71, 6299648\n"
"buffer_load_dwordx4 a[136:139], v252, s[20:23], s66 offen offset:0 sc1\nM 28,168,36\nR 162,189,53248\nM 28,172,52\nM 44,168,40\nR 164,185,0\n"
"M 44,172,56\ns_add_u32 s67, s71, 6332416\nbuffer_load_dwordx4 a[140:143], v252, s[20:23], s67 offen offset:0 sc1\nM 60,168,44\nR 166,185,4096\nM 60,172,60\n"
"s_barrier\nG 4\ns_waitcnt vmcnt(19)\nM 0,176,64\nR 168,190,49152\ns_waitcnt vmcnt(15)\n"
"M 0,180,80\nbuffer_load_dwordx4 a[144:147], v252, s[20:23], s64 offen offset:1024 sc1\nM 16,176,68\nR 170,190,53248\ns_waitcnt vmcnt(15)\nM 16,180,84\n"
"M 32,176,72\nR 172,186,0\ns_waitcnt vmcnt(14)\nM 32,180,88\nbuffer_load_dwordx4 a[148:151], v252, s[20:23], s65 offen offset:1024 sc1\nM 48,176,76\n"
"R 174,186,4096\ns_waitcnt vmcnt(14)\nM 48,180,92\nG 4\nM 4,160,64\nR 176,191,49152\n"
"M 4,164,80\nbuffer_load_dwordx4 a[152:155], v252, s[20:23], s66 offen offset:1024 sc1\nM 20,160,68\nR 178,191,53248\nM 20,164,84\nM 36,160,72\n"
"R 180,187,0\nM 36,164,88\nbuffer_load_dwordx4 a[156:159], v252, s[20:23], s67 offen offset:1024 sc1\nM 52,160,76\nR 182,187,4096\nM 52,164,92\n"
"G 4\nM 8,168,64\nR 160,184,8192\nM 8,172,80\nbuffer_load_dwordx4 a[160:163], v252, s[20:23], s64 offen offset:2048 sc1\nM 24,168,68\n"
"R 162,184,12288\nM 24,172,84\nM 40,168,72\nR 164,184,16384\nM 40,172,88\nbuffer_load_dwordx4 a[164:167], v252, s[20:23], s65 offen offset:2048 sc1\n"
"M 56,168,76\nR 166,184,20480\nM 56,172,92\nG 4\nM 12,176,64\nR 168,185,8192\n"
"M 12,180,80\nbuffer_load_dwordx4 a[168:171], v252, s[20:23], s66 offen offset:2048 sc1\nM 28,176,68\nR 170,185,12288\nM 28,180,84\nM 44,176,72\n"
"R 172,185,16384\nM 44,180,88\nbuffer_load_dwordx4 a[172:175], v252, s[20:23], s67 offen offset:2048 sc1\nM 60,176,76\nR 174,185,20480\nM 60,180,92\n"
"G 4\ns_waitcnt vmcnt(19)\nM 0,160,96\nR 176,186,8192\ns_waitcnt vmcnt(15)\nM 0,164,112\n"
"buffer_load_dwordx4 a[176:179], v252, s[20:23], s64 offen offset:3072 sc1\nM 16,160,100\nR 178,186,12288\ns_waitcnt vmcnt(15)\nM 16,164,116\nbuffer_load_dwordx4 v221, s[8:11], s63 offen offset:1024 lds\n"
"M 32,160,104\nR 180,186,16384\ns_add_u32 m0, s50, 65536\ns_waitcnt vmcnt(15)\nM 32,164,120\nbuffer_load_dwordx4 a[180:183], v252, s[20:23], s65 offen offset:3072 sc1\n"
"M 48,160,108\nR 182,186,20480\ns_add_u32 s63, s53, 851968\ns_waitcnt vmcnt(15)\nM 48,164,124\nbuffer_load_dwordx4 v220, s[8:11], s63 offen lds\n"
"G 4\nM 4,168,96\nR 160,187,8192\nM 4,172,112\nbuffer_load_dwordx4 a[184:187], v252, s[20:23], s66 offen offset:3072 sc1\nM 20,168,100\n"
"R 162,187,12288\nM 20,172,116\nbuffer_load_dwordx4 v221, s[8:11], s63 offen offset:1024 lds\nM 36,168,104\nR 164,187,16384\ns_add_u32 m0, s50, 73728\n"
"M 36,172,120\nbuffer_load_dwordx4 a[188:191], v252, s[20:23], s67 offen offset:3072 sc1\nM 52,168,108\nR 166,187,20480\ns_add_u32 s63, s53, 917504\nM 52,172,124\n"
"buffer_load_dwordx4 v220, s[8:11], s63 offen lds\nG 4\nM 8,176,96\nR 168,184,24576\nM 8,180,112\ns_add_u32 s64, s70, 6303744\n"
"buffer_load_dwordx4 a[192:195], v252, s[20:23], s64 offen offset:0 sc1\nM 24,176,100\nR 170,184,28672\nM 24,180,116\nbuffer_load_dwordx4 v221, s[8:11], s63 offen offset:1024 lds\nM 40,176,104\n"
"R 172,184,32768\ns_add_u32 m0, s50, 81920\nM 40,180,120\ns_add_u32 s65, s70, 6336512\nbuffer_load_dwordx4 a[196:199], v252, s[20:23], s65 offen offset:0 sc1\nM 56,176,108\n"
"R 174,184,36864\ns_add_u32 s63, s53, 983040\nM 56,180,124\nbuffer_load_dwordx4 v220, s[8:11], s63 offen lds\nG 4\nM 12,160,96\n"
"R 176,185,24576\nM 12,164,112\ns_add_u32 s66, s71, 6303744\nbuffer_load_dwordx4 a[200:203], v252, s[20:23], s66 offen offset:0 sc1\nM 28,160,100\nR 178,185,28672\n"
"M 28,164,116\nbuffer_load_dwordx4 v221, s[8:11], s63 offen offset:1024 lds\nM 44,160,104\nR 180,185,32768\nM 44,164,120\ns_add_u32 s67, s71, 6336512\n"
"buffer_load_dwordx4 a[204:207], v252, s[20:23], s67 offen offset:0 sc1\nM 60,160,108\nR 182,185,36864\nM 60,164,124\ns_barrier\nG 4\n"
"s_waitcnt vmcnt(26)\nM 0,168,128\nR 160,186,24576\ns_waitcnt vmcnt(22)\nM 0,172,144\nbuffer_load_dwordx4 a[208:211], v252, s[20:23], s64 offen offset:1024 sc1\n"
"M 16,168,132\nR 162,186,28672\ns_waitcnt vmcnt(22)\nM 16,172,148\nM 32,168,136\nR 164,186,32768\n"
"s_waitcnt vmcnt(21)\nM 32,172,152\nbuffer_load_dwordx4 a[212:215], v252, s[20:23], s65 offen offset:1024 sc1\nM 48,168,140\nR 166,186,36864\ns_waitcnt vmcnt(21)\n"
"M 48,172,156\nG 4\nM 4,176,128\nR 168,187,24576\nM 4,180,144\nbuffer_load_dwordx4 a[216:219], v252, s[20:23], s66 offen offset:1024 sc1\n"
"M 20,176,132\nR 170,187,28672\nM 20,180,148\nM 36,176,136\nR 172,187,32768\nM 36,180,152\n"
"buffer_load_dwordx4 a[220:223], v252, s[20:23], s67 offen offset:1024 sc1\nM 52,176,140\nR 174,187,36864\nM 52,180,156\nG 4\nM 8,160,128\n"
"R 176,184,40960\nM 8,164,144\nbuffer_load_dwordx4 a[224:227], v252, s[20:23], s64 offen offset:2048 sc1\nM 24,160,132\nR 178,184,45056\nM 24,164,148\n"
"M 40,160,136\nR 180,184,49152\nM 40,164,152\nbuffer_load_dwordx4 a[228:231], v252, s[20:23], s65 offen offset:2048 sc1\nM 56,160,140\nR 182,184,53248\n"
"M 56,164,156\nG 4\nM 12,168,128\nR 160,185,40960\nM 12,172,144\nbuffer_load_dwordx4 a[232:235], v252, s[20:23], s66 offen offset:2048 sc1\n"
"M 28,168,132\nR 162,185,45056\nM 28,172,148\nM 44,168,136\nR 164,185,49152\nM 44,172,152\n"
"buffer_load_dwordx4 a[236:239], v252, s[20:23], s67 offen offset:2048 sc1\nM 60,168,140\nR 166,185,53248\nM 60,172,156\nG 4\ns_waitcnt vmcnt(26)\n"
"M 0,176,160\nR 168,186,40960\ns_waitcnt vmcnt(22)\nM 0,180,176\nbuffer_load_dwordx4 a[240:243], v252, s[20:23], s64 offen offset:3072 sc1\nM 16,176,164\n"
"R 170,186,45056\ns_waitcnt vmcnt(21)\nM 16,180,180\nM 32,176,168\nR 172,186,49152\ns_waitcnt vmcnt(19)\n"
"M 32,180,184\nbuffer_load_dwordx4 a[244:247], v252, s[20:23], s65 offen offset:3072 sc1\nM 48,176,172\nR 174,186,53248\ns_waitcnt vmcnt(18)\nM 48,180,188\n"
"G 4\nM 4,160,160\nR 176,187,40960\nM 4,164,176\nbuffer_load_dwordx4 a[248:251], v252, s[20:23], s66 offen offset:3072 sc1\nM 20,160,164\n"
"R 178,187,45056\ns_waitcnt vmcnt(12)\ns_barrier\nM 20,164,180\nM 36,160,168\nR 180,187,49152\n"
"M 36,164,184\nbuffer_load_dwordx4 a[252:255], v252, s[20:23], s67 offen offset:3072 sc1\nM 52,160,172\nR 182,187,53248\nM 52,164,188\nG 4\n"
"M 8,168,160\nR 160,184,57344\nM 8,172,176\ns_movk_i32 s63, 7168\nv_mad_u32_u24 v220, v228, s63, v229\nv_add_u32 v221, 3668992, v220\n"
"s_add_u32 m0, s50, 90112\ns_add_u32 s63, s54, 0\nbuffer_load_dwordx4 v220, s[12:15], s63 offen lds\nM 24,168,164\nR 162,184,61440\nM 24,172,180\n"
"M 40,168,168\nR 164,188,0\nM 40,172,184\nbuffer_load_dwordx4 v221, s[12:15], s63 offen offset:1024 lds\nM 56,168,172\nR 166,188,4096\n"
"s_add_u32 m0, s50, 98304\nM 56,172,188\nG 4\nM 12,176,160\nR 168,185,57344\ns_add_u32 s63, s54, 229376\n"
"M 12,180,176\nbuffer_load_dwordx4 v220, s[12:15], s63 offen lds\nM 28,176,164\nR 170,185,61440\nM 28,180,180\nM 44,176,168\n"
"R 172,189,0\nM 44,180,184\nbuffer_load_dwordx4 v221, s[12:15], s63 offen offset:1024 lds\nM 60,176,172\nR 174,189,4096\ns_add_u32 m0, s50, 106496\n"
"M 60,180,188\nG 4\nM 32,160,200\nR 176,186,57344\ns_add_u32 s63, s54, 128\ns_waitcnt vmcnt(13)\n"
"M 32,164,216\nR 178,186,61440\nM 48,160,204\nR 180,190,0\ns_waitcnt vmcnt(12)\nM 48,164,220\n"
"R 182,190,4096\nG 4\nM 36,168,200\nR 160,187,57344\nbuffer_load_dwordx4 v220, s[12:15], s63 offen lds\nM 36,172,216\n"
"R 162,187,61440\nM 52,168,204\nR 164,191,0\nM 52,172,220\nR 166,191,4096\nG 4\n"
"M 40,176,200\nR 168,188,8192\nbuffer_load_dwordx4 v221, s[12:15], s63 offen offset:1024 lds\nM 40,180,216\nR 170,188,12288\ns_add_u32 m0, s50, 114688\n"
"M 56,176,204\nR 172,188,16384\ns_add_u32 s63, s54, 229504\nM 56,180,220\nR 174,188,20480\nG 4\n"
"M 44,160,200\nR 176,189,8192\nbuffer_load_dwordx4 v220, s[12:15], s63 offen lds\nM 44,164,216\nR 178,189,12288\nM 60,160,204\n"
"R 180,189,16384\nM 60,164,220\nR 182,189,20480\nG 4\ns_waitcnt vmcnt(12)\nM 32,168,232\n"
"R 160,190,8192\nbuffer_load_dwordx4 v221, s[12:15], s63 offen offset:1024 lds\ns_waitcnt vmcnt(9)\nM 32,172,248\nR 162,190,12288\nM 48,168,236\n"
"R 164,190,16384\ns_waitcnt vmcnt(8)\nM 48,172,252\nR 166,190,20480\nG 4\nM 36,176,232\n"
"R 168,191,8192\nM 36,180,248\nR 170,191,12288\nM 52,176,236\nR 172,191,16384\nM 52,180,252\n"
"R 174,191,20480\nG 4\nM 40,160,232\nR 176,184,57344\nM 40,164,248\nR 178,184,61440\n"
"M 56,160,236\nR 180,188,0\nM 56,164,252\nR 182,188,4096\nG 4\nM 44,168,232\n"
"R 160,185,57344\nM 44,172,248\nR 162,185,61440\nM 60,168,236\nR 164,189,0\nM 60,172,252\n"
"R 166,189,4096\nG 4\nM 0,176,192\nR 168,186,57344\nM 0,180,208\nR 170,186,61440\n"
"s_mov_b32 m0, s51\nM 16,176,196\nR 172,190,0\nds_write_addtid_b32 v32 offset:0\nM 16,180,212\nR 174,190,4096\n"
"ds_write_addtid_b32 v33 offset:256\nG 6\nM 4,160,192\nR 176,187,57344\nds_write_addtid_b32 v34 offset:512\nM 4,164,208\n"
"R 178,187,61440\nds_write_addtid_b32 v35 offset:768\nM 20,160,196\nR 180,191,0\nds_write_addtid_b32 v36 offset:1024\nM 20,164,212\n"
"R 182,191,4096\nds_write_addtid_b32 v37 offset:1280\nG 9\nM 8,168,192\nR 160,188,8192\nds_write_addtid_b32 v38 offset:1536\n"
"M 8,172,208\nR 162,188,12288\nds_write_addtid_b32 v39 offset:1792\nM 24,168,196\nR 164,188,16384\nds_write_addtid_b32 v40 offset:2048\n"
"M 24,172,212\nR 166,188,20480\nds_write_addtid_b32 v41 offset:2304\nG 9\nM 12,176,192\nR 168,189,8192\n"
"ds_write_addtid_b32 v42 offset:2560\nM 12,180,208\nR 170,189,12288\nds_write_addtid_b32 v43 offset:2816\nM 28,176,196\nR 172,189,16384\n"
"ds_write_addtid_b32 v44 offset:3072\nds_write_addtid_b32 v45 offset:3328\nds_write_addtid_b32 v46 offset:3584\nds_write_addtid_b32 v47 offset:3840\nds_write_addtid_b32 v48 offset:4096\nds_write_addtid_b32 v49 offset:4352\n"
"ds_write_addtid_b32 v50 offset:4608\nds_write_addtid_b32 v51 offset:4864\nds_write_addtid_b32 v52 offset:5120\nds_write_addtid_b32 v53 offset:5376\nds_write_addtid_b32 v54 offset:5632\nds_write_addtid_b32 v55 offset:5888\n"
"ds_write_addtid_b32 v56 offset:6144\nds_write_addtid_b32 v57 offset:6400\nds_write_addtid_b32 v58 offset:6656\nds_write_addtid_b32 v59 offset:6912\nds_write_addtid_b32 v60 offset:7168\nds_write_addtid_b32 v61 offset:7424\n"
"ds_write_addtid_b32 v62 offset:7680\nds_write_addtid_b32 v63 offset:7936\nM 28,180,212\nR 174,189,20480\ns_add_u32 m0, s50, 0\ns_add_u32 s63, s54, 458752\n"
"buffer_load_dwordx4 v220, s[12:15], s63 offen lds\nG 15\nM 0,160,224\nR 176,190,8192\nbuffer_load_dwordx4 v221, s[12:15], s63 offen offset:1024 lds\nM 0,164,240\n"
"R 178,190,12288\nG 3\ns_barrier\ns_add_u32 s63, s54, 688128\nM 16,160,228\nR 180,190,16384\n"
"s_add_u32 m0, s50, 8192\nM 16,164,244\nR 182,190,20480\nG 4\nM 4,168,224\nR 160,191,8192\n"
"buffer_load_dwordx4 v220, s[12:15], s63 offen lds\nM 4,172,240\nR 162,191,12288\nds_read2st64_b32 v[32:33], v225 offset0:0 offset1:1\nM 20,168,228\nR 164,191,16384\n"
"ds_read2st64_b32 v[34:35], v225 offset0:2 offset1:3\nM 20,172,244\nR 166,191,20480\nG 6\nM 8,176,224\nbuffer_load_dwordx4 v221, s[12:15], s63 offen offset:1024 lds\n"
"M 8,180,240\nds_read2st64_b32 v[36:37], v225 offset0:4 offset1:5\nds_read2st64_b32 v[38:39], v225 offset0:6 offset1:7\nM 24,176,228\nds_read2st64_b32 v[40:41], v225 offset0:8 offset1:9\nds_read2st64_b32 v[42:43], v225 offset0:10 offset1:11\n"
"M 24,180,244\nds_read2st64_b32 v[44:45], v225 offset0:12 offset1:13\nds_read2st64_b32 v[46:47], v225 offset0:14 offset1:15\nG 6\nM 12,160,224\nds_read2st64_b32 v[48:49], v225 offset0:16 offset1:17\n"
"ds_read2st64_b32 v[50:51], v225 offset0:18 offset1:19\nM 12,164,240\nds_read2st64_b32 v[52:53], v225 offset0:20 offset1:21\nds_read2st64_b32 v[54:55], v225 offset0:22 offset1:23\nM 28,160,228\nds_read2st64_b32 v[56:57], v225 offset0:24 offset1:25\n"
"ds_read2st64_b32 v[58:59], v225 offset0:26 offset1:27\nM 28,164,244\nds_read2st64_b32 v[60:61], v225 offset0:28 offset1:29\nds_read2st64_b32 v[62:63], v225 offset0:30 offset1:31\nG 0\ns_barrier\n"
"s_lshl_b32 s84, s75, 11\ns_add_u32 s84, s84, s49\ns_add_u32 s84, s84, 14680064\ns_add_u32 s85, s84, 32768\nv_mov_b32 v238, s46\nv_mov_b32 v239, s47\n"
"v_pk_add_f32 v[0:1], v[0:1], v[32:33]\nv_pk_add_f32 v[2:3], v[2:3], v[34:35]\nv_pk_add_f32 v[4:5], v[4:5], v[36:37]\nv_pk_add_f32 v[6:7], v[6:7], v[38:39]\nv_cvt_pk_bf16_f32 v192, v0, v1\nv_cvt_pk_bf16_f32 v193, v2, v3\n"
"v_cvt_pk_bf16_f32 v194, v4, v5\nv_cvt_pk_bf16_f32 v195, v6, v7\nbuffer_store_dwordx4 v[192:195], v252, s[20:23], s84 offen offset:0 sc1\nv_pk_add_f32 v[8:9], v[8:9], v[40:41]\nv_pk_add_f32 v[10:11], v[10:11], v[42:43]\nv_pk_add_f32 v[12:13], v[12:13], v[44:45]\n"
"v_pk_add_f32 v[14:15], v[14:15], v[46:47]\nv_cvt_pk_bf16_f32 v196, v8, v9\nv_cvt_pk_bf16_f32 v197, v10, v11\nv_cvt_pk_bf16_f32 v198, v12, v13\nv_cvt_pk_bf16_f32 v199, v14, v15\nbuffer_store_dwordx4 v[196:199], v252, s[20:23], s84 offen offset:1024 sc1\n"
"v_pk_add_f32 v[16:17], v[16:17], v[48:49]\nv_pk_add_f32 v[18:19], v[18:19], v[50:51]\nv_pk_add_f32 v[20:21], v[20:21], v[52:53]\nv_pk_add_f32 v[22:23], v[22:23], v[54:55]\nv_cvt_pk_bf16_f32 v200, v16, v17\nv_cvt_pk_bf16_f32 v201, v18, v19\n"
"v_cvt_pk_bf16_f32 v202, v20, v21\nv_cvt_pk_bf16_f32 v203, v22, v23\nbuffer_store_dwordx4 v[200:203], v252, s[20:23], s85 offen offset:0 sc1\nv_pk_add_f32 v[24:25], v[24:25], v[56:57]\nv_pk_add_f32 v[26:27], v[26:27], v[58:59]\nv_pk_add_f32 v[28:29], v[28:29], v[60:61]\n"
"v_pk_add_f32 v[30:31], v[30:31], v[62:63]\nv_cvt_pk_bf16_f32 v204, v24, v25\nv_cvt_pk_bf16_f32 v205, v26, v27\nv_cvt_pk_bf16_f32 v206, v28, v29\nv_cvt_pk_bf16_f32 v207, v30, v31\nbuffer_store_dwordx4 v[204:207], v252, s[20:23], s85 offen offset:1024 sc1\n"
"s_add_u32 s61, s79, 88637440\ns_lshl_b32 s62, s75, 3\ns_add_u32 s61, s61, s62\ns_waitcnt vmcnt(0)\ns_mov_b64 exec, 1\nbuffer_atomic_swap_x2 v[238:239], off, s[20:23], s61 sc1\n"
"s_mov_b64 exec, -1\nLqw1:\nv_bfe_u32 v250, v252, 7, 1\nv_mul_u32_u24 v250, 2176, v250\nv_bfe_u32 v251, v252, 4, 3\nv_lshl_add_u32 v250, v251, 3, v250\n"
"s_lshl_b32 s60, s90, 6\nv_add_u32 v250, s60, v250\ns_add_u32 m0, s50, 16384\ns_add_u32 s63, s54, 458880\nbuffer_load_dwordx4 v220, s[12:15], s63 offen lds\nbuffer_load_dwordx4 v221, s[12:15], s63 offen offset:1024 lds\n"
"s_add_u32 m0, s50, 24576\ns_add_u32 s63, s54, 688256\nbuffer_load_dwordx4 v220, s[12:15], s63 offen lds\nbuffer_load_dwordx4 v221, s[12:15], s63 offen offset:1024 lds\ns_add_u32 m0, s50, 32768\ns_add_u32 s63, s54, 917504\n"
"buffer_load_dwordx4 v220, s[12:15], s63 offen lds\nbuffer_load_dwordx4 v221, s[12:15], s63 offen offset:1024 lds\ns_add_u32 m0, s50, 40960\ns_add_u32 s63, s54, 1146880\nbuffer_load_dwordx4 v220, s[12:15], s63 offen lds\nbuffer_load_dwordx4 v221, s[12:15], s63 offen offset:1024 lds\n"
"s_add_u32 m0, s50, 49152\ns_add_u32 s63, s54, 917632\nbuffer_load_dwordx4 v220, s[12:15], s63 offen lds\nbuffer_load_dwordx4 v221, s[12:15], s63 offen offset:1024 lds\ns_add_u32 m0, s50, 57344\ns_add_u32 s63, s54, 1147008\n"
"buffer_load_dwordx4 v220, s[12:15], s63 offen lds\nbuffer_load_dwordx4 v221, s[12:15], s63 offen offset:1024 lds\nv_mov_b32 v248, 0\nv_mov_b32 v249, 0\ns_add_u32 s61, s80, 88637440\ns_mov_b64 exec, 0xffff\n"
"buffer_atomic_or_x2 v[248:249], v250, s[20:23], s61 offen sc0 sc1\ns_mov_b64 exec, -1\nv_mov_b32 v230, 0\nv_mov_b32 v231, 0\nv_mov_b32 v232, 0\nv_mov_b32 v233, 0\n"
"v_dot2c_f32_bf16 v230, s76, v192\nv_dot2c_f32_bf16 v231, v192, v192\nv_dot2c_f32_bf16 v232, s76, v200\nv_dot2c_f32_bf16 v233, v200, v200\nv_dot2c_f32_bf16 v230, s76, v193\nv_dot2c_f32_bf16 v231, v193, v193\n"
"v_dot2c_f32_bf16 v232, s76, v201\nv_dot2c_f32_bf16 v233, v201, v201\nv_dot2c_f32_bf16 v230, s76, v194\nv_dot2c_f32_bf16 v231, v194, v194\nv_dot2c_f32_bf16 v232, s76, v202\nv_dot2c_f32_bf16 v233, v202, v202\n"
"v_dot2c_f32_bf16 v230, s76, v195\nv_dot2c_f32_bf16 v231, v195, v195\nv_dot2c_f32_bf16 v232, s76, v203\nv_dot2c_f32_bf16 v233, v203, v203\nv_dot2c_f32_bf16 v230, s76, v196\nv_dot2c_f32_bf16 v231, v196, v196\n"
"v_dot2c_f32_bf16 v232, s76, v204\nv_dot2c_f32_bf16 v233, v204, v204\nv_dot2c_f32_bf16 v230, s76, v197\nv_dot2c_f32_bf16 v231, v197, v197\nv_dot2c_f32_bf16 v232, s76, v205\nv_dot2c_f32_bf16 v233, v205, v205\n"
"v_dot2c_f32_bf16 v230, s76, v198\nv_dot2c_f32_bf16 v231, v198, v198\nv_dot2c_f32_bf16 v232, s76, v206\nv_dot2c_f32_bf16 v233, v206, v206\nv_dot2c_f32_bf16 v230, s76, v199\nv_dot2c_f32_bf16 v231, v199, v199\n"
"v_dot2c_f32_bf16 v232, s76, v207\nv_dot2c_f32_bf16 v233, v207, v207\nv_lshrrev_b32 v234, 1, v252\nv_permlane32_swap_b32 v230, v231\ns_nop 0\nv_permlane32_swap_b32 v232, v233\n"
"v_add_f32 v230, v230, v231\nv_add_f32 v232, v232, v233\nv_mov_b32 v231, s46\ns_nop 0\nv_permlane16_swap_b32 v230, v232\nv_add_f32 v230, v230, v232\n"
"s_lshr_b32 s61, s49, 3\ns_lshl2_add_u32 s61, s53, s61\ns_add_u32 s61, s61, 83361792\nbuffer_store_dwordx2 v[230:231], v234, s[20:23], s61 offen sc1\ns_waitcnt vmcnt(1)\nv_cmp_eq_u64 s[86:87], s[46:47], v[248:249]\n"
"s_nop 0\ns_and_b32 s62, s86, 0xffff\ns_cmp_eq_u32 s62, 0xffff\ns_cbranch_scc0 Lq784\nLq785:\ns_add_u32 s64, s70, 14680064\n"
"buffer_load_dwordx4 a[0:3], v252, s[20:23], s64 offen offset:0 sc1\ns_add_u32 s65, s70, 14712832\nbuffer_load_dwordx4 a[4:7], v252, s[20:23], s65 offen offset:0 sc1\ns_add_u32 s66, s71, 14680064\nbuffer_load_dwordx4 a[8:11], v252, s[20:23], s66 offen offset:0 sc1\ns_add_u32 s67, s71, 14712832\n"
"buffer_load_dwordx4 a[12:15], v252, s[20:23], s67 offen offset:0 sc1\nbuffer_load_dwordx4 a[16:19], v252, s[20:23], s64 offen offset:1024 sc1\nbuffer_load_dwordx4 a[20:23], v252, s[20:23], s65 offen offset:1024 sc1\nbuffer_load_dwordx4 a[24:27], v252, s[20:23], s66 offen offset:1024 sc1\nbuffer_load_dwordx4 a[28:31], v252, s[20:23], s67 offen offset:1024 sc1\ns_barrier\n"
"R 160,188,24576\nR 162,188,28672\nR 164,188,32768\nR 166,188,36864\nR 168,189,24576\nR 170,189,28672\n"
"R 172,189,32768\nR 174,189,36864\nR 176,190,24576\nR 178,190,28672\nR 180,190,32768\nR 182,190,36864\n"
"buffer_load_dwordx4 a[32:35], v252, s[20:23], s64 offen offset:2048 sc1\nbuffer_load_dwordx4 a[36:39], v252, s[20:23], s65 offen offset:2048 sc1\nbuffer_load_dwordx4 a[40:43], v252, s[20:23], s66 offen offset:2048 sc1\nbuffer_load_dwordx4 a[44:47], v252, s[20:23], s67 offen offset:2048 sc1\nbuffer_load_dwordx4 a[48:51], v252, s[20:23], s64 offen offset:3072 sc1\nbuffer_load_dwordx4 a[52:55], v252, s[20:23], s65 offen offset:3072 sc1\n"
"buffer_load_dwordx4 a[56:59], v252, s[20:23], s66 offen offset:3072 sc1\nbuffer_load_dwordx4 a[60:63], v252, s[20:23], s67 offen offset:3072 sc1\ns_waitcnt vmcnt(15) lgkmcnt(10)\nv_mfma_f32_16x16x32_bf16 v[32:35], v[160:163], a[0:3], 0\ns_add_u32 s64, s70, 14684160\nbuffer_load_dwordx4 a[64:67], v252, s[20:23], s64 offen offset:0 sc1\n"
"s_waitcnt vmcnt(12) lgkmcnt(8)\nM 32,164,16\nv_mfma_f32_16x16x32_bf16 v[48:51], v[160:163], a[4:7], 0\ns_waitcnt vmcnt(11)\nM 48,164,20\nv_mfma_f32_16x16x32_bf16 v[64:67], v[160:163], a[8:11], 0\n"
"s_waitcnt vmcnt(10)\nM 64,164,24\ns_add_u32 s65, s70, 14716928\nbuffer_load_dwordx4 a[68:71], v252, s[20:23], s65 offen offset:0 sc1\nv_mfma_f32_16x16x32_bf16 v[80:83], v[160:163], a[12:15], 0\ns_waitcnt vmcnt(10)\n"
"M 80,164,28\nG 4\nv_mfma_f32_16x16x32_bf16 v[36:39], v[168:171], a[0:3], 0\nR 160,191,24576\nM 36,172,16\nv_mfma_f32_16x16x32_bf16 v[52:55], v[168:171], a[4:7], 0\n"
"R 162,191,28672\nM 52,172,20\ns_add_u32 s66, s71, 14684160\nbuffer_load_dwordx4 a[72:75], v252, s[20:23], s66 offen offset:0 sc1\nv_mfma_f32_16x16x32_bf16 v[68:71], v[168:171], a[8:11], 0\nR 164,191,32768\n"
"M 68,172,24\nv_mfma_f32_16x16x32_bf16 v[84:87], v[168:171], a[12:15], 0\nR 166,191,36864\nM 84,172,28\nG 4\nv_mfma_f32_16x16x32_bf16 v[40:43], v[176:179], a[0:3], 0\n"
"R 168,188,40960\nM 40,180,16\ns_add_u32 s67, s71, 14716928\nbuffer_load_dwordx4 a[76:79], v252, s[20:23], s67 offen offset:0 sc1\nv_mfma_f32_16x16x32_bf16 v[56:59], v[176:179], a[4:7], 0\nR 170,188,45056\n"
"M 56,180,20\nv_mfma_f32_16x16x32_bf16 v[72:75], v[176:179], a[8:11], 0\nR 172,188,49152\nM 72,180,24\nv_mfma_f32_16x16x32_bf16 v[88:91], v[176:179], a[12:15], 0\nR 174,188,53248\n"
"M 88,180,28\nG 4\nv_mfma_f32_16x16x32_bf16 v[44:47], v[160:163], a[0:3], 0\nR 176,189,40960\nM 44,164,16\nbuffer_load_dwordx4 a[80:83], v252, s[20:23], s64 offen offset:1024 sc1\n"
"v_mfma_f32_16x16x32_bf16 v[60:63], v[160:163], a[4:7], 0\nR 178,189,45056\nM 60,164,20\nv_mfma_f32_16x16x32_bf16 v[76:79], v[160:163], a[8:11], 0\nR 180,189,49152\nM 76,164,24\n"
"buffer_load_dwordx4 a[84:87], v252, s[20:23], s65 offen offset:1024 sc1\nv_mfma_f32_16x16x32_bf16 v[92:95], v[160:163], a[12:15], 0\nR 182,189,53248\nM 92,164,28\nG 4\nv_mfma_f32_16x16x32_bf16 v[96:99], v[168:171], a[0:3], 0\n"
"R 160,190,40960\nM 96,172,16\nv_mfma_f32_16x16x32_bf16 v[112:115], v[168:171], a[4:7], 0\nR 162,190,45056\nM 112,172,20\nbuffer_load_dwordx4 a[88:91], v252, s[20:23], s66 offen offset:1024 sc1\n"
"v_mfma_f32_16x16x32_bf16 v[128:131], v[168:171], a[8:11], 0\nR 164,190,49152\nM 128,172,24\nv_mfma_f32_16x16x32_bf16 v[144:147], v[168:171], a[12:15], 0\nR 166,190,53248\nM 144,172,28\n"
"s_barrier\nG 4\nv_mfma_f32_16x16x32_bf16 v[100:103], v[176:179], a[0:3], 0\nR 168,191,40960\ns_add_u32 s63, s54, 1376256\nM 100,180,16\n"
"buffer_load_dwordx4 a[92:95], v252, s[20:23], s67 offen offset:1024 sc1\nv_mfma_f32_16x16x32_bf16 v[116:119], v[176:179], a[4:7], 0\nR 170,191,45056\nM 116,180,20\ns_add_u32 m0, s50, 65536\nv_mfma_f32_16x16x32_bf16 v[132:135], v[176:179], a[8:11], 0\n"
"R 172,191,49152\nM 132,180,24\nbuffer_load_dwordx4 v220, s[12:15], s63 offen lds\nv_mfma_f32_16x16x32_bf16 v[148:151], v[176:179], a[12:15], 0\nR 174,191,53248\nM 148,180,28\n"
"G 4\nv_mfma_f32_16x16x32_bf16 v[104:107], v[160:163], a[0:3], 0\nR 176,184,0\nM 104,164,16\nbuffer_load_dwordx4 a[96:99], v252, s[20:23], s64 offen offset:2048 sc1\nv_mfma_f32_16x16x32_bf16 v[120:123], v[160:163], a[4:7], 0\n"
"R 178,184,4096\nM 120,164,20\nbuffer_load_dwordx4 a[100:103], v252, s[20:23], s65 offen offset:2048 sc1\nv_mfma_f32_16x16x32_bf16 v[136:139], v[160:163], a[8:11], 0\nR 180,184,8192\nM 136,164,24\n"
"v_mfma_f32_16x16x32_bf16 v[152:155], v[160:163], a[12:15], 0\nR 182,184,12288\nM 152,164,28\nG 4\nv_mfma_f32_16x16x32_bf16 v[108:111], v[168:171], a[0:3], 0\nR 160,185,0\n"
"M 108,172,16\nbuffer_load_dwordx4 a[104:107], v252, s[20:23], s66 offen offset:2048 sc1\nv_mfma_f32_16x16x32_bf16 v[124:127], v[168:171], a[4:7], 0\nR 162,185,4096\nM 124,172,20\nv_mfma_f32_16x16x32_bf16 v[140:143], v[168:171], a[8:11], 0\n"
"R 164,185,8192\nM 140,172,24\nv_mfma_f32_16x16x32_bf16 v[156:159], v[168:171], a[12:15], 0\nR 166,185,12288\nM 156,172,28\nG 4\n"
"s_waitcnt vmcnt(19)\nM 32,176,32\nR 168,186,0\ns_waitcnt vmcnt(15)\nM 32,180,48\nbuffer_load_dwordx4 a[108:111], v252, s[20:23], s67 offen offset:2048 sc1\n"
"M 48,176,36\nR 170,186,4096\ns_waitcnt vmcnt(15)\nM 48,180,52\nM 64,176,40\nR 172,186,8192\n"
"s_waitcnt vmcnt(14)\nM 64,180,56\nbuffer_load_dwordx4 a[112:115], v252, s[20:23], s64 offen offset:3072 sc1\nM 80,176,44\nR 174,186,12288\ns_waitcnt vmcnt(14)\n"
"M 80,180,60\ns_barrier\nG 4\nM 36,160,32\nR 176,187,0\nM 36,164,48\n"
"buffer_load_dwordx4 v221, s[12:15], s63 offen offset:1024 lds\nM 52,160,36\nR 178,187,4096\nM 52,164,52\nbuffer_load_dwordx4 a[116:119], v252, s[20:23], s65 offen offset:3072 sc1\nM 68,160,40\n"
"R 180,187,8192\nM 68,164,56\nM 84,160,44\nR 182,187,12288\nM 84,164,60\nG 4\n"
"M 40,168,32\nR 160,184,16384\ns_add_u32 s63, s54, 1605632\nM 40,172,48\nbuffer_load_dwordx4 a[120:123], v252, s[20:23], s66 offen offset:3072 sc1\nM 56,168,36\n"
"R 162,184,20480\nM 56,172,52\ns_add_u32 m0, s50, 73728\nM 72,168,40\nR 164,184,24576\nM 72,172,56\n"
"buffer_load_dwordx4 v220, s[12:15], s63 offen lds\nM 88,168,44\nR 166,184,28672\nM 88,172,60\nG 4\nM 44,176,32\n"
"R 168,185,16384\nM 44,180,48\nbuffer_load_dwordx4 a[124:127], v252, s[20:23], s67 offen offset:3072 sc1\nM 60,176,36\nR 170,185,20480\nM 60,180,52\n"
"M 76,176,40\nR 172,185,24576\nM 76,180,56\ns_add_u32 s64, s70, 14688256\nbuffer_load_dwordx4 a[128:131], v252, s[20:23], s64 offen offset:0 sc1\nM 92,176,44\n"
"R 174,185,28672\nM 92,180,60\nG 4\nM 96,160,32\nR 176,186,16384\nM 96,164,48\n"
"s_add_u32 s65, s70, 14721024\nbuffer_load_dwordx4 a[132:135], v252, s[20:23], s65 offen offset:0 sc1\nM 112,160,36\nR 178,186,20480\nM 112,164,52\nM 128,160,40\n"
"R 180,186,24576\nM 128,164,56\nM 144,160,44\nR 182,186,28672\nM 144,164,60\ns_barrier\n"
"G 4\nM 100,168,32\nR 160,187,16384\nM 100,172,48\ns_add_u32 s66, s71, 14688256\nbuffer_load_dwordx4 a[136:139], v252, s[20:23], s66 offen offset:0 sc1\n"
"M 116,168,36\nR 162,187,20480\nM 116,172,52\nM 132,168,40\nR 164,187,24576\nM 132,172,56\n"
"s_add_u32 s67, s71, 14721024\nbuffer_load_dwordx4 a[140:143], v252, s[20:23], s67 offen offset:0 sc1\nM 148,168,44\nR 166,187,28672\nM 148,172,60\nG 4\n"
"M 104,176,32\nR 168,184,32768\nM 104,180,48\nbuffer_load_dwordx4 v221, s[12:15], s63 offen offset:1024 lds\nM 120,176,36\nR 170,184,36864\n"
"M 120,180,52\nbuffer_load_dwordx4 a[144:147], v252, s[20:23], s64 offen offset:1024 sc1\nM 136,176,40\nR 172,184,40960\nM 136,180,56\nM 152,176,44\n"
"R 174,184,45056\nM 152,180,60\nG 4\nM 108,160,32\nR 176,185,32768\ns_add_u32 s63, s54, 1376384\n"
"M 108,164,48\nbuffer_load_dwordx4 a[148:151], v252, s[20:23], s65 offen offset:1024 sc1\nM 124,160,36\nR 178,185,36864\nM 124,164,52\ns_add_u32 m0, s50, 81920\n"
"M 140,160,40\nR 180,185,40960\nM 140,164,56\nbuffer_load_dwordx4 v220, s[12:15], s63 offen lds\nM 156,160,44\nR 182,185,45056\n"
"M 156,164,60\nG 4\ns_waitcnt vmcnt(26)\nM 32,168,64\nR 160,186,32768\ns_waitcnt vmcnt(22)\n"
"M 32,172,80\nbuffer_load_dwordx4 a[152:155], v252, s[20:23], s66 offen offset:1024 sc1\nM 48,168,68\nR 162,186,36864\ns_waitcnt vmcnt(22)\nM 48,172,84\n"
"M 64,168,72\nR 164,186,40960\ns_waitcnt vmcnt(21)\nM 64,172,88\nbuffer_load_dwordx4 a[156:159], v252, s[20:23], s67 offen offset:1024 sc1\nM 80,168,76\n"
"R 166,186,45056\ns_waitcnt vmcnt(21)\nM 80,172,92\ns_barrier\nG 4\nM 36,176,64\n"
"R 168,187,32768\nM 36,180,80\nM 52,176,68\nR 170,187,36864\nM 52,180,84\nbuffer_load_dwordx4 a[160:163], v252, s[20:23], s64 offen offset:2048 sc1\n"
"M 68,176,72\nR 172,187,40960\nM 68,180,88\nbuffer_load_dwordx4 v221, s[12:15], s63 offen offset:1024 lds\nM 84,176,76\nR 174,187,45056\n"
"s_add_u32 m0, s50, 90112\nM 84,180,92\nG 4\nM 40,160,64\nR 176,184,49152\ns_add_u32 s63, s54, 1605760\n"
"M 40,164,80\nbuffer_load_dwordx4 a[164:167], v252, s[20:23], s65 offen offset:2048 sc1\nM 56,160,68\nR 178,184,53248\nM 56,164,84\nbuffer_load_dwordx4 v220, s[12:15], s63 offen lds\n"
"M 72,160,72\nR 180,184,57344\nM 72,164,88\nbuffer_load_dwordx4 a[168:171], v252, s[20:23], s66 offen offset:2048 sc1\nM 88,160,76\nR 182,184,61440\n"
"M 88,164,92\nG 4\nM 44,168,64\nR 160,185,49152\nbuffer_load_dwordx4 v221, s[12:15], s63 offen offset:1024 lds\nM 44,172,80\n"
"M 60,168,68\nR 162,185,53248\nM 60,172,84\nbuffer_load_dwordx4 a[172:175], v252, s[20:23], s67 offen offset:2048 sc1\nM 76,168,72\nR 164,185,57344\n"
"M 76,172,88\nM 92,168,76\nR 166,185,61440\nM 92,172,92\nG 4\nM 96,176,64\n"
"R 168,186,49152\nM 96,180,80\nbuffer_load_dwordx4 a[176:179], v252, s[20:23], s64 offen offset:3072 sc1\nM 112,176,68\nR 170,186,53248\nM 112,180,84\n"
"M 128,176,72\nR 172,186,57344\nM 128,180,88\ns_add_u32 m0, s50, 98304\nM 144,176,76\nR 174,186,61440\n"
"s_add_u32 s63, s54, 1835008\nM 144,180,92\ns_waitcnt vmcnt(13)\ns_barrier\nG 4\nM 100,160,64\n"
"R 176,187,49152\nM 100,164,80\nbuffer_load_dwordx4 a[180:183], v252, s[20:23], s65 offen offset:3072 sc1\nM 116,160,68\nR 178,187,53248\nM 116,164,84\n"
"buffer_load_dwordx4 v220, s[12:15], s63 offen lds\nM 132,160,72\nR 180,187,57344\nM 132,164,88\nbuffer_load_dwordx4 a[184:187], v252, s[20:23], s66 offen offset:3072 sc1\nM 148,160,76\n"
"R 182,187,61440\nM 148,164,92\nG 4\nM 104,168,64\nR 160,188,0\nbuffer_load_dwordx4 v221, s[12:15], s63 offen offset:1024 lds\n"
"M 104,172,80\ns_add_u32 m0, s50, 106496\ns_add_u32 s63, s54, 2064384\nbuffer_load_dwordx4 v220, s[12:15], s63 offen lds\nM 120,168,68\nR 162,188,4096\n"
"M 120,172,84\nbuffer_load_dwordx4 a[188:191], v252, s[20:23], s67 offen offset:3072 sc1\nM 136,168,72\nR 164,188,8192\nM 136,172,88\nbuffer_load_dwordx4 v221, s[12:15], s63 offen offset:1024 lds\n"
"M 152,168,76\nR 166,188,12288\nM 152,172,92\nG 4\nM 108,176,64\nR 168,189,0\n"
"M 108,180,80\ns_add_u32 s64, s70, 14692352\nbuffer_load_dwordx4 a[192:195], v252, s[20:23], s64 offen offset:0 sc1\nM 124,176,68\nR 170,189,4096\nM 124,180,84\n"
"M 140,176,72\nR 172,189,8192\nM 140,180,88\ns_add_u32 s65, s70, 14725120\nbuffer_load_dwordx4 a[196:199], v252, s[20:23], s65 offen offset:0 sc1\nM 156,176,76\n"
"R 174,189,12288\nM 156,180,92\nG 4\nM 32,160,96\nR 176,190,0\nM 32,164,112\n"
"M 48,160,100\nR 178,190,4096\nM 48,164,116\ns_add_u32 s66, s71, 14692352\nbuffer_load_dwordx4 a[200:203], v252, s[20:23], s66 offen offset:0 sc1\nM 64,160,104\n"
"R 180,190,8192\nM 64,164,120\ns_add_u32 m0, s50, 114688\nM 80,160,108\nR 182,190,12288\ns_add_u32 s63, s54, 2293760\n"
"M 80,164,124\ns_waitcnt vmcnt(12)\ns_barrier\nG 4\nM 36,168,96\nR 160,191,0\n"
"M 36,172,112\ns_add_u32 s67, s71, 14725120\nbuffer_load_dwordx4 a[204:207], v252, s[20:23], s67 offen offset:0 sc1\nM 52,168,100\nR 162,191,4096\nM 52,172,116\n"
"buffer_load_dwordx4 v220, s[12:15], s63 offen lds\nM 68,168,104\nR 164,191,8192\nM 68,172,120\nbuffer_load_dwordx4 v221, s[12:15], s63 offen offset:1024 lds\nM 84,168,108\n"
"R 166,191,12288\ns_add_u32 m0, s50, 0\nM 84,172,124\nG 4\nM 40,176,96\nR 168,188,16384\n"
"s_add_u32 s63, s54, 2523136\nM 40,180,112\nbuffer_load_dwordx4 a[208:211], v252, s[20:23], s64 offen offset:1024 sc1\nM 56,176,100\nR 170,188,20480\nM 56,180,116\n"
"buffer_load_dwordx4 v220, s[12:15], s63 offen lds\nM 72,176,104\nR 172,188,24576\nM 72,180,120\nbuffer_load_dwordx4 a[212:215], v252, s[20:23], s65 offen offset:1024 sc1\nM 88,176,108\n"
"R 174,188,28672\nM 88,180,124\nG 4\nM 44,160,96\nR 176,189,16384\nbuffer_load_dwordx4 v221, s[12:15], s63 offen offset:1024 lds\n"
"M 44,164,112\nM 60,160,100\nR 178,189,20480\nM 60,164,116\nbuffer_load_dwordx4 a[216:219], v252, s[20:23], s66 offen offset:1024 sc1\nM 76,160,104\n"
"R 180,189,24576\nLq1065:\ns_lshr_b32 s60, s45, 3\ns_lshl2_add_u32 s60, s60, s40\ns_lshl_b32 s60, s60, 8\nv_lshl_add_u32 v224, v252, 3, s60\n"
"v_add_u32 v224, Lq1065+4096-LqPC, v224\ns_mov_b64 exec, 3\nglobal_load_dword v224, v224, s[98:99]\ns_mov_b64 exec, -1\nM 76,164,120\nM 92,160,108\n"
"R 182,189,28672\nM 92,164,124\nG 4\nM 96,168,96\nR 160,190,16384\nM 96,172,112\n"
"buffer_load_dwordx4 a[220:223], v252, s[20:23], s67 offen offset:1024 sc1\nM 112,168,100\nR 162,190,20480\nM 112,172,116\nM 128,168,104\nR 164,190,24576\n"
"M 128,172,120\nM 144,168,108\nR 166,190,28672\ns_add_u32 m0, s50, 8192\nM 144,172,124\ns_waitcnt vmcnt(13)\n"
"s_barrier\nG 4\nM 100,176,96\nR 168,191,16384\ns_add_u32 s63, s54, 2752512\nM 100,180,112\n"
"buffer_load_dwordx4 a[224:227], v252, s[20:23], s64 offen offset:2048 sc1\nM 116,176,100\nR 170,191,20480\nM 116,180,116\nbuffer_load_dwordx4 a[228:231], v252, s[20:23], s65 offen offset:2048 sc1\nM 132,176,104\n"
"R 172,191,24576\nbuffer_load_dwordx4 v220, s[12:15], s63 offen lds\nM 132,180,120\nbuffer_load_dwordx4 v221, s[12:15], s63 offen offset:1024 lds\nM 148,176,108\nR 174,191,28672\n"
"s_add_u32 m0, s50, 16384\nM 148,180,124\nG 4\nM 104,160,96\nR 176,188,32768\ns_add_u32 s63, s54, 2981888\n"
"M 104,164,112\nbuffer_load_dwordx4 a[232:235], v252, s[20:23], s66 offen offset:2048 sc1\nM 120,160,100\nR 178,188,36864\nM 120,164,116\nbuffer_load_dwordx4 v220, s[12:15], s63 offen lds\n"
"M 136,160,104\nR 180,188,40960\nM 136,164,120\nbuffer_load_dwordx4 v221, s[12:15], s63 offen offset:1024 lds\nM 152,160,108\nR 182,188,45056\n"
"M 152,164,124\nG 4\nM 108,168,96\nR 160,189,32768\nM 108,172,112\nbuffer_load_dwordx4 a[236:239], v252, s[20:23], s67 offen offset:2048 sc1\n"
"M 124,168,100\nR 162,189,36864\nM 124,172,116\nM 140,168,104\nR 164,189,40960\nM 140,172,120\n"
"buffer_load_dwordx4 a[240:243], v252, s[20:23], s64 offen offset:3072 sc1\nM 156,168,108\nR 166,189,45056\nM 156,172,124\nG 4\nM 32,176,128\n"
"R 168,190,32768\nM 32,180,144\nM 48,176,132\nR 170,190,36864\nM 48,180,148\nbuffer_load_dwordx4 a[244:247], v252, s[20:23], s65 offen offset:3072 sc1\n"
"M 64,176,136\nR 172,190,40960\nM 64,180,152\nM 80,176,140\nR 174,190,45056\ns_add_u32 m0, s50, 24576\n"
"M 80,180,156\ns_waitcnt vmcnt(13)\ns_barrier\nG 4\nM 36,160,128\nR 176,191,32768\n"
"s_add_u32 s63, s54, 3211264\nM 36,164,144\nbuffer_load_dwordx4 a[248:251], v252, s[20:23], s66 offen offset:3072 sc1\nM 52,160,132\nR 178,191,36864\nM 52,164,148\n"
"s_lshr_b32 s84, s49, 3\nM 68,160,136\nR 180,191,40960\nbuffer_load_dwordx4 v220, s[12:15], s63 offen lds\nM 68,164,152\ns_add_u32 s84, s84, 83361792\n"
"M 84,160,140\nR 182,191,45056\nM 84,164,156\nbuffer_load_dwordx4 v[0:3], v252, s[20:23], s84 offen offset:0 sc1\nG 4\nM 40,168,128\n"
"R 160,188,49152\nbuffer_load_dwordx4 v221, s[12:15], s63 offen offset:1024 lds\nM 40,172,144\nbuffer_load_dwordx4 a[252:255], v252, s[20:23], s67 offen offset:3072 sc1\ns_add_u32 s63, s54, 3440640\nM 56,168,132\n"
"R 162,188,53248\ns_add_u32 m0, s50, 32768\nM 56,172,148\nbuffer_load_dwordx4 v[4:7], v252, s[20:23], s84 offen offset:1024 sc1\nM 72,168,136\nR 164,184,0\n"
"s_lshr_b32 s85, s49, 3\nbuffer_load_dwordx4 v220, s[12:15], s63 offen lds\nM 72,172,152\nbuffer_load_dwordx4 v[8:11], v252, s[20:23], s84 offen offset:2048 sc1\nM 88,168,140\nR 166,184,4096\n"
"s_add_u32 s85, s85, 83365888\nM 88,172,156\nG 4\nM 44,176,128\nR 168,189,49152\nbuffer_load_dwordx4 v221, s[12:15], s63 offen offset:1024 lds\n"
"M 44,180,144\nbuffer_load_dwordx4 v[12:15], v252, s[20:23], s84 offen offset:3072 sc1\nM 60,176,132\nR 170,189,53248\nM 60,180,148\nbuffer_load_dwordx4 v[16:19], v252, s[20:23], s85 offen offset:0 sc1\n"
"M 76,176,136\nR 172,185,0\nM 76,180,152\nbuffer_load_dwordx4 v[20:23], v252, s[20:23], s85 offen offset:1024 sc1\nM 92,176,140\nR 174,185,4096\n"
"M 92,180,156\nG 4\nM 32,160,160\nR 176,190,49152\nM 32,164,176\nbuffer_load_dwordx4 v[24:27], v252, s[20:23], s85 offen offset:2048 sc1\n"
"M 48,160,164\nR 178,190,53248\nM 48,164,180\nbuffer_load_dwordx4 v[28:31], v252, s[20:23], s85 offen offset:3072 sc1\nM 64,160,168\nR 180,186,0\n"
"s_add_u32 s63, s54, 1835136\nM 64,164,184\ns_add_u32 m0, s50, 40960\nM 80,160,172\nR 182,186,4096\nM 80,164,188\n"
"s_waitcnt vmcnt(17)\ns_barrier\nG 4\nM 36,168,160\nR 160,191,49152\nM 36,172,176\n"
"buffer_load_dwordx4 v220, s[12:15], s63 offen lds\nM 52,168,164\nR 162,191,53248\nM 52,172,180\nM 68,168,168\nR 164,187,0\n"
"v_bfe_u32 v250, v252, 5, 1\nv_mul_u32_u24 v250, 2560, v250\nM 68,172,184\nbuffer_load_dwordx4 v221, s[12:15], s63 offen offset:1024 lds\nv_and_b32 v251, 16, v252\nM 84,168,172\n"
"R 166,187,4096\ns_add_u32 m0, s50, 49152\nv_lshl_add_u32 v250, v251, 1, v250\nM 84,172,188\ns_lshl_b32 s60, s90, 6\nv_add_u32 v250, s60, v250\n"
"G 4\nM 40,176,160\nR 168,184,8192\ns_add_u32 s63, s54, 2064512\nM 40,180,176\nbuffer_load_dwordx4 v220, s[12:15], s63 offen lds\n"
"M 56,176,164\nR 170,184,12288\nM 56,180,180\nM 72,176,168\nR 172,184,16384\nM 72,180,184\n"
"buffer_load_dwordx4 v221, s[12:15], s63 offen offset:1024 lds\nM 88,176,172\nR 174,184,20480\nM 88,180,188\nG 4\nM 44,160,160\n"
"R 176,185,8192\nM 44,164,176\nM 60,160,164\nR 178,185,12288\nM 60,164,180\ns_add_u32 m0, s50, 57344\n"
"M 76,160,168\nR 180,185,16384\nM 76,164,184\ns_add_u32 s63, s54, 2293888\nM 92,160,172\nR 182,185,20480\n"
"M 92,164,188\nG 4\nM 32,168,192\nR 160,186,8192\nM 32,172,208\nbuffer_load_dwordx4 v220, s[12:15], s63 offen lds\n"
"M 48,168,196\nR 162,186,12288\nM 48,172,212\nM 64,168,200\nR 164,186,16384\nM 64,172,216\n"
"buffer_load_dwordx4 v221, s[12:15], s63 offen offset:1024 lds\nM 80,168,204\nR 166,186,20480\ns_add_u32 m0, s50, 65536\nM 80,172,220\ns_waitcnt vmcnt(11)\n"
"s_barrier\nG 4\nM 36,176,192\nR 168,187,8192\ns_add_u32 s63, s54, 2523264\nM 36,180,208\n"
"buffer_load_dwordx4 v220, s[12:15], s63 offen lds\nM 52,176,196\nR 170,187,12288\nv_min3_u32 v230, v1, v3, v5\nv_min3_u32 v230, v230, v7, v9\nM 52,180,212\n"
"s_waitcnt vmcnt(11)\nv_min3_u32 v230, v230, v11, v13\ns_waitcnt vmcnt(10)\nv_min3_u32 v230, v230, v15, v17\ns_waitcnt vmcnt(9)\nv_min3_u32 v230, v230, v19, v21\n"
"M 68,176,200\nR 172,187,16384\ns_waitcnt vmcnt(8)\nv_min3_u32 v230, v230, v23, v25\ns_waitcnt vmcnt(7)\nv_min3_u32 v230, v230, v27, v29\n"
"M 68,180,216\nbuffer_load_dwordx4 v221, s[12:15], s63 offen offset:1024 lds\nv_min3_u32 v230, v230, v31, v31\nM 84,176,204\nR 174,187,20480\ns_add_u32 s63, s54, 2752640\n"
"v_max3_u32 v231, v1, v3, v5\nM 84,180,220\nG 4\nv_max3_u32 v231, v231, v7, v9\nv_max3_u32 v231, v231, v11, v13\nv_max3_u32 v231, v231, v15, v17\n"
"M 40,160,192\nR 176,184,24576\nv_max3_u32 v231, v231, v19, v21\nv_max3_u32 v231, v231, v23, v25\nM 40,164,208\ns_add_u32 m0, s50, 73728\n"
"v_max3_u32 v231, v231, v27, v29\nv_max3_u32 v231, v231, v31, v31\nv_cmp_ne_u32 s[86:87], s46, v230\nM 56,160,196\nR 178,184,28672\nv_cmp_ne_u32 s[88:89], s46, v231\n"
"M 56,164,212\nbuffer_load_dwordx4 v220, s[12:15], s63 offen lds\ns_or_b64 s[86:87], s[86:87], s[88:89]\ns_cbranch_scc1 Lq841\nLq842:\nM 72,160,200\n"
"R 180,184,32768\nv_add_f32 v0, v0, v16\nv_add_f32 v2, v2, v18\nM 72,164,216\nv_add_f32 v4, v4, v20\nv_add_f32 v6, v6, v22\n"
"v_add_f32 v8, v8, v24\nM 88,160,204\nR 182,184,36864\nv_add_f32 v10, v10, v26\nv_add_f32 v12, v12, v28\nM 88,164,220\n"
"G 4\nv_add_f32 v14, v14, v30\nv_add_f32 v0, v0, v8\nv_add_f32 v2, v2, v10\nM 44,168,192\nR 160,185,24576\n"
"v_add_f32 v4, v4, v12\nv_add_f32 v6, v6, v14\nM 44,172,208\nbuffer_load_dwordx4 v221, s[12:15], s63 offen offset:1024 lds\nv_add_f32 v0, v0, v4\nM 60,168,196\n"
"R 162,185,28672\ns_add_u32 s63, s54, 2982016\nv_add_f32 v2, v2, v6\nM 60,172,212\ns_add_u32 m0, s50, 81920\nv_lshrrev_b32 v231, 1, v252\n"
"v_lshrrev_b32 v230, 2, v252\ns_lshl_b32 s62, s40, 9\ns_add_u32 s62, s62, 159232\nv_add_u32 v231, s62, v231\nM 76,168,200\nR 164,185,32768\n"
"ds_write2_b32 v231, v0, v2 offset1:1\nM 76,172,216\nbuffer_load_dwordx4 v220, s[12:15], s63 offen lds\ns_lshl_b32 s62, s40, 9\ns_add_u32 s62, s62, 159232\nv_add_u32 v230, s62, v230\n"
"M 92,168,204\nR 166,185,36864\nds_read2st64_b32 v[232:233], v230 offset1:1\nM 92,172,220\nG 6\nM 32,176,224\n"
"R 168,186,24576\nM 32,180,240\nbuffer_load_dwordx4 v221, s[12:15], s63 offen offset:1024 lds\nM 48,176,228\nR 170,186,28672\ns_add_u32 s63, s54, 3211392\n"
"M 48,180,244\ns_add_u32 m0, s50, 90112\nM 64,176,232\nR 172,186,32768\nG 3\nv_add_f32 v232, v232, v233\n"
"v_mov_b32 v233, v232\nM 64,180,248\nbuffer_load_dwordx4 v220, s[12:15], s63 offen lds\nv_permlane32_swap_b32 v232, v233\nM 80,176,236\nR 174,186,36864\n"
"v_mov_b32 v230, v232\nv_mov_b32 v231, v233\nM 80,180,252\ns_waitcnt vmcnt(9)\ns_barrier\nv_permlane16_swap_b32 v230, v232\n"
"M 36,160,224\nR 176,187,24576\nv_permlane16_swap_b32 v231, v233\nv_mul_f32 v230, 0xba800000, v230\nM 36,164,240\nbuffer_load_dwordx4 v221, s[12:15], s63 offen offset:1024 lds\n"
"v_mul_f32 v214, v230, v230\nM 52,160,228\nR 178,187,28672\ns_add_u32 s63, s54, 3440768\nv_mul_f32 v231, 0x3a800000, v231\nM 52,164,244\n"
"s_add_u32 m0, s50, 98304\nv_sub_f32 v231, v231, v214\nv_max_f32 v231, 0, v231\nv_add_f32 v231, 0x3727c5ac, v231\nM 68,160,232\nR 180,187,32768\n"
"v_and_b32 v208, s77, v230\nv_rsq_f32 v234, v231\nM 68,164,248\nbuffer_load_dwordx4 v220, s[12:15], s63 offen lds\nv_mul_f32 v216, v231, v234\nM 84,160,236\n"
"R 182,187,36864\nv_sub_f32 v230, v230, v208\nv_cvt_pk_bf16_f32 v209, v230, v216\nM 84,164,252\nG 4\nv_cvt_pk_bf16_f32 v208, v208, v208\n"
"v_mul_f32 v214, 0xc0135761, v234\nv_mul_f32 v215, v234, v234\nM 40,168,224\nR 160,184,40960\nv_mul_f32 v215, v215, v214\nv_mul_f32 v215, 0x3d372713, v215\n"
"M 40,172,240\ns_mov_b64 s[60:61], 0xffff\nv_cndmask_b32 v208, 0, v208, s[60:61]\nv_mul_f32 v232, 0xba800000, v232\nv_mul_f32 v217, v232, v232\nM 56,168,228\n"
"R 162,184,45056\nv_mul_f32 v233, 0x3a800000, v233\nv_sub_f32 v233, v233, v217\nM 56,172,244\nbuffer_load_dwordx4 v221, s[12:15], s63 offen offset:1024 lds\nM 72,168,232\n"
"R 164,184,49152\ns_add_u32 s63, s55, 0\nM 72,172,248\ns_add_u32 m0, s50, 106496\ns_mov_b64 s[60:61], 0xffff\nv_cndmask_b32 v209, 0, v209, s[60:61]\n"
"v_max_f32 v233, 0, v233\nM 88,168,236\nR 166,184,53248\nv_add_f32 v233, 0x3727c5ac, v233\nM 88,172,252\nG 4\n"
"v_rsq_f32 v235, v233\nM 44,176,224\nR 168,185,40960\nv_and_b32 v212, s77, v232\nM 44,180,240\nbuffer_load_dwordx4 v222, s[12:15], s63 offen lds\n"
"M 60,176,228\nR 170,185,45056\nv_mul_f32 v219, v233, v235\nM 60,180,244\nv_sub_f32 v232, v232, v212\nv_cvt_pk_bf16_f32 v213, v232, v219\n"
"M 76,176,232\nR 172,185,49152\nv_cvt_pk_bf16_f32 v212, v212, v212\nM 76,180,248\nbuffer_load_dwordx4 v223, s[12:15], s63 offen offset:1024 lds\nM 92,176,236\n"
"R 174,185,53248\nv_mul_f32 v217, 0xc0135761, v235\nM 92,180,252\ns_waitcnt vmcnt(10)\ns_barrier\nG 4\n"
"M 96,160,128\nR 176,186,40960\nv_mul_f32 v218, v235, v235\nM 96,164,144\ns_mov_b32 m0, s51\ns_mov_b64 s[60:61], 0xffff\n"
"v_cndmask_b32 v212, 0, v212, s[60:61]\nv_mul_f32 v218, v218, v217\nM 112,160,132\nR 178,186,45056\nv_mul_f32 v218, 0x3d372713, v218\nM 112,164,148\n"
"s_mov_b64 s[60:61], 0xffff\nv_cndmask_b32 v213, 0, v213, s[60:61]\nM 128,160,136\nR 180,186,49152\nM 128,164,152\nds_write_addtid_b32 v64 offset:0\n"
"M 144,160,140\nR 182,186,53248\nM 144,164,156\nds_write_addtid_b32 v65 offset:256\nG 6\nM 100,168,128\n"
"R 160,187,40960\nM 100,172,144\nds_write_addtid_b32 v66 offset:512\nM 116,168,132\nR 162,187,45056\nM 116,172,148\n"
"ds_write_addtid_b32 v67 offset:768\nM 132,168,136\nR 164,187,49152\nM 132,172,152\nds_write_addtid_b32 v68 offset:1024\nM 148,168,140\n"
"R 166,187,53248\nds_write_addtid_b32 v69 offset:1280\nM 148,172,156\nds_write_addtid_b32 v70 offset:1536\nds_write_addtid_b32 v71 offset:1792\nG 11\n"
"M 104,176,128\nR 168,184,57344\nds_write_addtid_b32 v72 offset:2048\nM 104,180,144\nds_write_addtid_b32 v73 offset:2304\nds_write_addtid_b32 v74 offset:2560\n"
"M 120,176,132\nR 170,184,61440\nds_write_addtid_b32 v75 offset:2816\nM 120,180,148\nds_write_addtid_b32 v76 offset:3072\nds_write_addtid_b32 v77 offset:3328\n"
"M 136,176,136\nR 172,188,0\nds_write_addtid_b32 v78 offset:3584\nM 136,180,152\nds_write_addtid_b32 v79 offset:3840\nds_write_addtid_b32 v80 offset:4096\n"
"M 152,176,140\nR 174,188,4096\nds_write_addtid_b32 v81 offset:4352\nM 152,180,156\nds_write_addtid_b32 v82 offset:4608\nds_write_addtid_b32 v83 offset:4864\n"
"G 15\nM 108,160,128\nR 176,185,57344\nds_write_addtid_b32 v84 offset:5120\nM 108,164,144\nds_write_addtid_b32 v85 offset:5376\n"
"ds_write_addtid_b32 v86 offset:5632\nM 124,160,132\nR 178,185,61440\nds_write_addtid_b32 v87 offset:5888\nM 124,164,148\nds_write_addtid_b32 v88 offset:6144\n"
"ds_write_addtid_b32 v89 offset:6400\nM 140,160,136\nR 180,189,0\nds_write_addtid_b32 v90 offset:6656\nM 140,164,152\nds_write_addtid_b32 v91 offset:6912\n"
"ds_write_addtid_b32 v92 offset:7168\nM 156,160,140\nR 182,189,4096\nds_write_addtid_b32 v93 offset:7424\nM 156,164,156\nds_write_addtid_b32 v94 offset:7680\n"
"ds_write_addtid_b32 v95 offset:7936\nG 15\nM 96,168,160\nR 160,186,57344\ns_add_u32 m0, s50, 114688\ns_add_u32 s63, s55, 458752\n"
"M 96,172,176\nbuffer_load_dwordx4 v222, s[12:15], s63 offen lds\nM 112,168,164\nR 162,186,61440\ns_waitcnt vmcnt(7) lgkmcnt(2)\ns_barrier\n"
"M 112,172,180\nds_read2st64_b32 v[64:65], v225 offset0:0 offset1:1\nds_read2st64_b32 v[66:67], v225 offset0:2 offset1:3\nM 128,168,168\nR 164,190,0\nds_read2st64_b32 v[68:69], v225 offset0:4 offset1:5\n"
"M 128,172,184\nbuffer_load_dwordx4 v223, s[12:15], s63 offen offset:1024 lds\nds_read2st64_b32 v[70:71], v225 offset0:6 offset1:7\nM 144,168,172\nR 166,190,4096\ns_add_u32 m0, s50, 0\n"
"ds_read2st64_b32 v[72:73], v225 offset0:8 offset1:9\nM 144,172,188\nds_read2st64_b32 v[74:75], v225 offset0:10 offset1:11\nds_read2st64_b32 v[76:77], v225 offset0:12 offset1:13\nM 100,176,160\nR 168,187,57344\n"
"s_add_u32 s63, s55, 917504\nds_read2st64_b32 v[78:79], v225 offset0:14 offset1:15\nM 100,180,176\nbuffer_load_dwordx4 v222, s[12:15], s63 offen lds\nG 10\nv_add_f32 v32, v32, v64\n"
"M 116,176,164\nR 170,187,61440\nv_add_f32 v33, v33, v65\nG 10\nv_add_f32 v34, v34, v66\nM 116,180,180\n"
"ds_read2st64_b32 v[80:81], v225 offset0:16 offset1:17\nv_add_f32 v35, v35, v67\nG 9\nv_add_f32 v36, v36, v68\nM 132,176,168\nR 172,191,0\n"
"v_add_f32 v37, v37, v69\nG 9\nv_add_f32 v38, v38, v70\nM 132,180,184\nbuffer_load_dwordx4 v223, s[12:15], s63 offen offset:1024 lds\nv_add_f32 v39, v39, v71\n"
"M 148,176,172\nR 174,191,4096\ns_add_u32 m0, s50, 8192\nG 8\nv_add_f32 v40, v40, v72\nM 148,180,188\n"
"ds_read2st64_b32 v[82:83], v225 offset0:18 offset1:19\nv_add_f32 v41, v41, v73\nM 104,160,160\nR 176,188,8192\ns_add_u32 s63, s55, 1376256\nG 9\n"
"v_add_f32 v42, v42, v74\nM 104,164,176\nbuffer_load_dwordx4 v222, s[12:15], s63 offen lds\nv_add_f32 v43, v43, v75\nM 120,160,164\nR 178,188,12288\n"
"G 9\nv_add_f32 v44, v44, v76\nv_add_f32 v45, v45, v77\nM 120,164,180\nds_read2st64_b32 v[84:85], v225 offset0:20 offset1:21\nG 8\n"
"v_add_f32 v46, v46, v78\nv_add_f32 v47, v47, v79\nM 136,160,168\nR 180,188,16384\nG 7\nv_add_f32 v48, v48, v80\n"
"v_add_f32 v49, v49, v81\nM 136,164,184\nbuffer_load_dwordx4 v223, s[12:15], s63 offen offset:1024 lds\nG 4\nv_add_f32 v50, v50, v82\nM 152,160,172\n"
"R 182,188,20480\nds_read2st64_b32 v[86:87], v225 offset0:22 offset1:23\nv_add_f32 v51, v51, v83\nM 152,164,188\nds_read2st64_b32 v[88:89], v225 offset0:24 offset1:25\nds_read2st64_b32 v[90:91], v225 offset0:26 offset1:27\n"
"M 108,168,160\nR 160,189,8192\nds_read2st64_b32 v[92:93], v225 offset0:28 offset1:29\nM 108,172,176\nds_read2st64_b32 v[94:95], v225 offset0:30 offset1:31\nG 8\n"
"v_add_f32 v52, v52, v84\nv_add_f32 v53, v53, v85\nM 124,168,164\nR 162,189,12288\ns_mov_b32 s60, 157184\nv_bfe_u32 v246, v252, 4, 4\n"
"v_lshl_add_u32 v246, v246, 3, s60\nM 124,172,180\nds_read_b64 v[238:239], v246 offset:0\ns_add_u32 m0, s50, 16384\nM 140,168,168\nR 164,189,16384\n"
"ds_read_b64 v[240:241], v246 offset:128\nG 9\nv_add_f32 v54, v54, v86\nM 140,172,184\nds_read_b64 v[242:243], v246 offset:256\ns_add_u32 s63, s55, 1835008\n"
"M 156,168,172\nR 166,189,20480\nds_read_b64 v[244:245], v246 offset:384\nM 156,172,188\nds_read_b64 v[246:247], v246 offset:0\nM 96,176,192\n"
"R 168,190,8192\nv_add_f32 v55, v55, v87\nM 96,180,208\nbuffer_load_dwordx4 v222, s[12:15], s63 offen lds\nM 112,176,196\nR 170,190,12288\n"
"s_waitcnt vmcnt(7) lgkmcnt(2)\ns_barrier\nM 112,180,212\nv_add_f32 v56, v56, v88\nv_add_f32 v57, v57, v89\nM 128,176,200\n"
"R 172,190,16384\nv_add_f32 v58, v58, v90\nM 128,180,216\nv_add_f32 v59, v59, v91\nv_add_f32 v60, v60, v92\nM 144,176,204\n"
"R 174,190,20480\nv_add_f32 v61, v61, v93\nM 144,180,220\nv_add_f32 v62, v62, v94\nv_add_f32 v63, v63, v95\nM 100,160,192\n"
"R 176,191,8192\nv_mfma_f32_16x16x32_bf16 v[32:35], v[238:241], v[208:211], v[32:35]\nM 100,164,208\nv_mfma_f32_16x16x32_bf16 v[36:39], v[240:243], v[208:211], v[36:39]\nbuffer_load_dwordx4 v223, s[12:15], s63 offen offset:1024 lds\nM 116,160,196\n"
"R 178,191,12288\nv_mfma_f32_16x16x32_bf16 v[40:43], v[242:245], v[208:211], v[40:43]\ns_add_u32 s63, s55, 2293760\nM 116,164,212\nv_mfma_f32_16x16x32_bf16 v[44:47], v[244:247], v[208:211], v[44:47]\ns_add_u32 m0, s50, 24576\n"
"M 132,160,200\nR 180,191,16384\nv_mfma_f32_16x16x32_bf16 v[48:51], v[244:247], v[210:213], v[48:51]\nM 132,164,216\nv_mfma_f32_16x16x32_bf16 v[52:55], v[238:241], v[210:213], v[52:55]\nbuffer_load_dwordx4 v222, s[12:15], s63 offen lds\n"
"M 148,160,204\nR 182,191,20480\nv_mfma_f32_16x16x32_bf16 v[56:59], v[240:243], v[210:213], v[56:59]\nM 148,164,220\nv_mfma_f32_16x16x32_bf16 v[60:63], v[242:245], v[210:213], v[60:63]\nG 4\n"
"M 104,168,192\nR 160,188,24576\nM 104,172,208\nbuffer_load_dwordx4 v223, s[12:15], s63 offen offset:1024 lds\nM 120,168,196\nR 162,188,28672\n"
"M 120,172,212\ns_add_u32 m0, s50, 32768\nM 136,168,200\nR 164,188,32768\nM 136,172,216\ns_add_u32 s63, s55, 2752512\n"
"M 152,168,204\nR 166,188,36864\nv_mul_f32 v230, v32, v32\nv_mul_f32 v231, v33, v33\nM 152,172,220\ns_add_u32 s84, s73, 37748736\n"
"s_add_u32 s85, s73, 37863424\nG 4\nv_mul_f32 v232, v34, v34\nM 108,176,192\nR 168,189,24576\nv_mul_f32 v233, v35, v35\n"
"v_fma_f32 v230, v230, v215, v214\nM 108,180,208\nbuffer_load_dwordx4 v222, s[12:15], s63 offen lds\nv_fma_f32 v231, v231, v215, v214\nM 124,176,196\nR 170,189,28672\n"
"v_fma_f32 v232, v232, v215, v214\nv_fma_f32 v233, v233, v215, v214\nM 124,180,212\nv_mul_f32 v230, v32, v230\nv_mul_f32 v231, v33, v231\nv_mul_f32 v232, v34, v232\n"
"M 140,176,200\nR 172,189,32768\nv_mul_f32 v233, v35, v233\nv_exp_f32 v230, v230\nM 140,180,216\nv_exp_f32 v231, v231\n"
"v_exp_f32 v232, v232\nM 156,176,204\nR 174,189,36864\ns_mov_b32 s60, 157696\nv_bfe_u32 v246, v252, 4, 4\nv_lshl_add_u32 v246, v246, 3, s60\n"
"v_exp_f32 v233, v233\nM 156,180,220\nds_read_b64 v[238:239], v246 offset:0\nG 5\nv_fma_f32 v230, v230, v216, v216\nM 96,160,224\n"
"R 176,190,24576\nds_read_b64 v[240:241], v246 offset:128\nv_fma_f32 v231, v231, v216, v216\nM 96,164,240\nds_read_b64 v[242:243], v246 offset:256\nv_fma_f32 v232, v232, v216, v216\n"
"v_fma_f32 v233, v233, v216, v216\nM 112,160,228\nR 178,190,28672\nds_read_b64 v[244:245], v246 offset:384\nv_rcp_f32 v230, v230\nM 112,164,244\n"
"ds_read_b64 v[246:247], v246 offset:0\nv_mul_f32 v230, v32, v230\nv_rcp_f32 v231, v231\nM 128,160,232\nR 180,190,32768\nv_mul_f32 v231, v33, v231\n"
"v_rcp_f32 v232, v232\nM 128,164,248\nbuffer_load_dwordx4 v223, s[12:15], s63 offen offset:1024 lds\nv_rcp_f32 v233, v233\nM 144,160,236\nR 182,190,36864\n"
"s_add_u32 m0, s50, 40960\nv_mul_f32 v232, v34, v232\nM 144,164,252\nG 9\nv_mul_f32 v233, v35, v233\nv_cvt_pk_bf16_f32 v32, v230, v231\n"
"v_cvt_pk_bf16_f32 v33, v232, v233\nM 100,168,224\nR 160,191,24576\ns_add_u32 s63, s55, 3211264\nv_mul_f32 v234, v36, v36\nM 100,172,240\n"
"buffer_load_dwordx4 v222, s[12:15], s63 offen lds\nv_mul_f32 v235, v37, v37\nM 116,168,228\nR 162,191,28672\nv_mul_f32 v236, v38, v38\nv_mul_f32 v237, v39, v39\n"
"M 116,172,244\nv_fma_f32 v234, v234, v215, v214\nv_fma_f32 v235, v235, v215, v214\nv_fma_f32 v236, v236, v215, v214\nM 132,168,232\nR 164,191,32768\n"
"G 9\nv_mfma_f32_16x16x32_bf16 v[96:99], v[238:241], v[208:211], v[96:99]\nv_fma_f32 v237, v237, v215, v214\nv_mul_f32 v234, v36, v234\nM 132,172,248\nbuffer_load_dwordx4 v223, s[12:15], s63 offen offset:1024 lds\n"
"v_mul_f32 v235, v37, v235\nM 148,168,236\nR 166,191,36864\nG 9\nv_mfma_f32_16x16x32_bf16 v[100:103], v[240:243], v[208:211], v[100:103]\ns_add_u32 m0, s50, 49152\n"
"v_mul_f32 v236, v38, v236\nM 148,172,252\nG 4\nv_exp_f32 v234, v234\nv_mul_f32 v237, v39, v237\nM 104,176,224\n"
"R 168,188,40960\nv_mfma_f32_16x16x32_bf16 v[104:107], v[242:245], v[208:211], v[104:107]\ns_add_u32 s63, s54, 256\nv_exp_f32 v235, v235\nM 104,180,240\nbuffer_load_dwordx4 v220, s[12:15], s63 offen lds\n"
"v_exp_f32 v236, v236\nM 120,176,228\nR 170,188,45056\nv_mfma_f32_16x16x32_bf16 v[108:111], v[244:247], v[208:211], v[108:111]\nv_fma_f32 v234, v234, v216, v216\nv_exp_f32 v237, v237\n"
"M 120,180,244\nv_rcp_f32 v234, v234\nv_fma_f32 v235, v235, v216, v216\nM 136,176,232\nR 172,190,40960\nv_mfma_f32_16x16x32_bf16 v[112:115], v[244:247], v[210:213], v[112:115]\n"
"v_fma_f32 v236, v236, v216, v216\nv_fma_f32 v237, v237, v216, v216\nM 136,180,248\nbuffer_load_dwordx4 v221, s[12:15], s63 offen offset:1024 lds\nv_rcp_f32 v235, v235\nM 152,176,236\n"
"R 174,190,45056\nv_mfma_f32_16x16x32_bf16 v[116:119], v[238:241], v[210:213], v[116:119]\ns_add_u32 m0, s50, 57344\nv_rcp_f32 v236, v236\nM 152,180,252\nG 4\n"
"v_rcp_f32 v237, v237\nv_mul_f32 v234, v36, v234\nM 108,160,224\nR 176,189,40960\nv_mfma_f32_16x16x32_bf16 v[120:123], v[240:243], v[210:213], v[120:123]\ns_add_u32 s63, s54, 229632\n"
"v_mul_f32 v235, v37, v235\nM 108,164,240\nbuffer_load_dwordx4 v220, s[12:15], s63 offen lds\nv_mul_f32 v236, v38, v236\nM 124,160,228\nR 178,189,45056\n"
"v_mfma_f32_16x16x32_bf16 v[124:127], v[242:245], v[210:213], v[124:127]\nv_mul_f32 v237, v39, v237\nv_cvt_pk_bf16_f32 v34, v234, v235\nM 124,164,244\nbuffer_load_dwordx4 v221, s[12:15], s63 offen offset:1024 lds\nv_cvt_pk_bf16_f32 v35, v236, v237\n"
"M 140,160,232\nR 180,191,40960\nv_mul_f32 v230, v40, v40\nv_mul_f32 v231, v41, v41\nM 140,164,248\nbuffer_store_dwordx4 v[32:35], v252, s[20:23], s84 offen offset:0 sc1\n"
"v_mul_f32 v232, v42, v42\nM 156,160,236\nR 182,191,45056\nv_mul_f32 v233, v43, v43\nv_fma_f32 v230, v230, v215, v214\nM 156,164,252\n"
"s_waitcnt vmcnt(13)\ns_barrier\nG 4\nv_fma_f32 v231, v231, v215, v214\nv_mfma_f32_16x16x32_bf16 v[0:3], v[168:171], a[0:3], 0\nR 160,188,49152\n"
"v_fma_f32 v232, v232, v215, v214\nv_fma_f32 v233, v233, v215, v214\nM 0,172,16\ns_mov_b32 m0, s51\nv_mul_f32 v230, v40, v230\nv_mul_f32 v231, v41, v231\n"
"v_mul_f32 v232, v42, v232\nv_mfma_f32_16x16x32_bf16 v[16:19], v[168:171], a[4:7], 0\nR 162,188,53248\nv_mul_f32 v233, v43, v233\nv_exp_f32 v230, v230\nM 16,172,20\n"
"v_exp_f32 v231, v231\nv_exp_f32 v232, v232\nv_mfma_f32_16x16x32_bf16 v[8:11], v[168:171], a[8:11], 0\nR 164,190,49152\nv_fma_f32 v230, v230, v216, v216\nv_exp_f32 v233, v233\n"
"M 8,172,24\nv_rcp_f32 v230, v230\nv_fma_f32 v231, v231, v216, v216\nv_mfma_f32_16x16x32_bf16 v[24:27], v[168:171], a[12:15], 0\nR 166,190,53248\nv_fma_f32 v232, v232, v216, v216\n"
"v_fma_f32 v233, v233, v216, v216\nM 24,172,28\nG 4\nv_rcp_f32 v231, v231\nv_rcp_f32 v232, v232\nv_mfma_f32_16x16x32_bf16 v[4:7], v[176:179], a[0:3], 0\n"
"R 168,189,49152\nv_mul_f32 v230, v40, v230\nv_rcp_f32 v233, v233\nM 4,180,16\nv_mul_f32 v231, v41, v231\nv_mul_f32 v232, v42, v232\n"
"v_mul_f32 v233, v43, v233\nv_mfma_f32_16x16x32_bf16 v[20:23], v[176:179], a[4:7], 0\nR 170,189,53248\nv_cvt_pk_bf16_f32 v40, v230, v231\nv_cvt_pk_bf16_f32 v41, v232, v233\nM 20,180,20\n"
"ds_write_addtid_b32 v128 offset:0\nv_mul_f32 v234, v44, v44\nv_mul_f32 v235, v45, v45\nv_mul_f32 v236, v46, v46\nv_mfma_f32_16x16x32_bf16 v[12:15], v[176:179], a[8:11], 0\nR 172,191,49152\n"
"ds_write_addtid_b32 v129 offset:256\nv_mul_f32 v237, v47, v47\nv_fma_f32 v234, v234, v215, v214\nM 12,180,24\nds_write_addtid_b32 v130 offset:512\nv_fma_f32 v235, v235, v215, v214\n"
"v_fma_f32 v236, v236, v215, v214\nv_fma_f32 v237, v237, v215, v214\nv_mfma_f32_16x16x32_bf16 v[28:31], v[176:179], a[12:15], 0\nR 174,191,53248\nds_write_addtid_b32 v131 offset:768\nv_mul_f32 v234, v44, v234\n"
"v_mul_f32 v235, v45, v235\nM 28,180,28\nds_write_addtid_b32 v132 offset:1024\nG 9\nv_exp_f32 v234, v234\nv_mul_f32 v236, v46, v236\n"
"M 0,160,32\nR 176,184,0\nds_write_addtid_b32 v133 offset:1280\nv_mul_f32 v237, v47, v237\nv_exp_f32 v235, v235\nM 0,164,48\n"
"ds_write_addtid_b32 v134 offset:1536\nv_exp_f32 v236, v236\nv_exp_f32 v237, v237\nM 16,160,36\nR 178,184,4096\nds_write_addtid_b32 v135 offset:1792\n"
"v_fma_f32 v234, v234, v216, v216\nv_fma_f32 v235, v235, v216, v216\nM 16,164,52\nds_write_addtid_b32 v136 offset:2048\nv_rcp_f32 v234, v234\nv_fma_f32 v236, v236, v216, v216\n"
"M 8,160,40\nR 180,186,0\nds_write_addtid_b32 v137 offset:2304\nv_fma_f32 v237, v237, v216, v216\nv_rcp_f32 v235, v235\nM 8,164,56\n"
"ds_write_addtid_b32 v138 offset:2560\nv_rcp_f32 v236, v236\nv_rcp_f32 v237, v237\nM 24,160,44\nR 182,186,4096\nds_write_addtid_b32 v139 offset:2816\n"
"v_mul_f32 v234, v44, v234\nv_mul_f32 v235, v45, v235\nM 24,164,60\nds_write_addtid_b32 v140 offset:3072\nG 14\nv_mul_f32 v236, v46, v236\n"
"v_mul_f32 v237, v47, v237\nv_cvt_pk_bf16_f32 v42, v234, v235\nM 4,168,32\nR 160,185,0\nds_write_addtid_b32 v141 offset:3328\nv_cvt_pk_bf16_f32 v43, v236, v237\n"
"v_mul_f32 v230, v48, v48\nM 4,172,48\nbuffer_store_dwordx4 v[40:43], v252, s[20:23], s84 offen offset:1024 sc1\nds_write_addtid_b32 v142 offset:3584\nv_mul_f32 v231, v49, v49\nM 20,168,36\n"
"R 162,185,4096\nds_write_addtid_b32 v143 offset:3840\nv_mul_f32 v232, v50, v50\nv_mul_f32 v233, v51, v51\nM 20,172,52\nds_write_addtid_b32 v144 offset:4096\n"
"v_fma_f32 v230, v230, v218, v217\nv_fma_f32 v231, v231, v218, v217\nv_fma_f32 v232, v232, v218, v217\nM 12,168,40\nR 164,187,0\nds_write_addtid_b32 v145 offset:4352\n"
"v_fma_f32 v233, v233, v218, v217\nv_mul_f32 v230, v48, v230\nM 12,172,56\nds_write_addtid_b32 v146 offset:4608\nv_exp_f32 v230, v230\nv_mul_f32 v231, v49, v231\n"
"M 28,168,44\nR 166,187,4096\nds_write_addtid_b32 v147 offset:4864\nv_mul_f32 v232, v50, v232\nv_mul_f32 v233, v51, v233\nM 28,172,60\n"
"ds_write_addtid_b32 v148 offset:5120\nds_write_addtid_b32 v149 offset:5376\nds_write_addtid_b32 v150 offset:5632\nds_write_addtid_b32 v151 offset:5888\nds_write_addtid_b32 v152 offset:6144\nds_write_addtid_b32 v153 offset:6400\n"
"ds_write_addtid_b32 v154 offset:6656\nds_write_addtid_b32 v155 offset:6912\nds_write_addtid_b32 v156 offset:7168\nds_write_addtid_b32 v157 offset:7424\nds_write_addtid_b32 v158 offset:7680\nds_write_addtid_b32 v159 offset:7936\n"
"G 15\nv_exp_f32 v231, v231\nv_exp_f32 v232, v232\nM 0,176,64\nR 168,184,8192\ns_add_u32 m0, s50, 65536\n"
"s_add_u32 s63, s54, 459008\nv_exp_f32 v233, v233\nM 0,180,80\nbuffer_load_dwordx4 v220, s[12:15], s63 offen lds\nv_fma_f32 v230, v230, v219, v219\nM 16,176,68\n"
"R 170,184,12288\ns_waitcnt vmcnt(9) lgkmcnt(2)\ns_barrier\nv_fma_f32 v231, v231, v219, v219\nM 16,180,84\nds_read2st64_b32 v[128:129], v225 offset0:0 offset1:1\n"
"v_rcp_f32 v230, v230\nv_fma_f32 v232, v232, v219, v219\nM 8,176,72\nR 172,186,8192\nds_read2st64_b32 v[130:131], v225 offset0:2 offset1:3\nv_fma_f32 v233, v233, v219, v219\n"
"v_rcp_f32 v231, v231\nM 8,180,88\nbuffer_load_dwordx4 v221, s[12:15], s63 offen offset:1024 lds\nds_read2st64_b32 v[132:133], v225 offset0:4 offset1:5\nv_rcp_f32 v232, v232\nM 24,176,76\n"
"R 174,186,12288\ns_add_u32 m0, s50, 73728\nds_read2st64_b32 v[134:135], v225 offset0:6 offset1:7\nv_rcp_f32 v233, v233\nM 24,180,92\nds_read2st64_b32 v[136:137], v225 offset0:8 offset1:9\n"
"v_mul_f32 v230, v48, v230\nv_mul_f32 v231, v49, v231\nv_mul_f32 v232, v50, v232\nM 4,160,64\nR 176,185,8192\ns_add_u32 s63, s54, 688384\n"
"ds_read2st64_b32 v[138:139], v225 offset0:10 offset1:11\nv_mul_f32 v233, v51, v233\nM 4,164,80\nbuffer_load_dwordx4 v220, s[12:15], s63 offen lds\nG 8\nv_add_f32 v96, v96, v128\n"
"v_cvt_pk_bf16_f32 v48, v230, v231\nM 20,160,68\nR 178,185,12288\nv_cvt_pk_bf16_f32 v49, v232, v233\nv_add_f32 v97, v97, v129\nv_mul_f32 v234, v52, v52\n"
"M 20,164,84\nds_read2st64_b32 v[140:141], v225 offset0:12 offset1:13\nv_mul_f32 v235, v53, v53\nv_mul_f32 v236, v54, v54\nv_mul_f32 v237, v55, v55\nM 12,160,72\n"
"R 180,187,8192\nv_fma_f32 v234, v234, v218, v217\nG 9\nv_add_f32 v98, v98, v130\nv_fma_f32 v235, v235, v218, v217\nM 12,164,88\n"
"buffer_load_dwordx4 v221, s[12:15], s63 offen offset:1024 lds\nv_add_f32 v99, v99, v131\nv_fma_f32 v236, v236, v218, v217\nM 28,160,76\nR 182,187,12288\ns_add_u32 m0, s50, 81920\n"
"G 9\nv_add_f32 v100, v100, v132\nv_fma_f32 v237, v237, v218, v217\nM 28,164,92\nG 8\nv_mul_f32 v234, v52, v234\n"
"v_mul_f32 v235, v53, v235\nv_add_f32 v101, v101, v133\nv_mul_f32 v236, v54, v236\nM 0,168,96\nR 160,184,16384\ns_add_u32 s63, s54, 917760\n"
"G 8\nv_add_f32 v102, v102, v134\nv_mul_f32 v237, v55, v237\nM 0,172,112\nbuffer_load_dwordx4 v220, s[12:15], s63 offen lds\nv_add_f32 v103, v103, v135\n"
"v_exp_f32 v234, v234\nM 16,168,100\nR 162,184,20480\nv_fma_f32 v234, v234, v219, v219\nG 8\nv_add_f32 v104, v104, v136\n"
"v_exp_f32 v235, v235\nM 16,172,116\nds_read2st64_b32 v[142:143], v225 offset0:14 offset1:15\nv_exp_f32 v236, v236\nv_exp_f32 v237, v237\nM 8,168,104\n"
"R 164,186,16384\nv_fma_f32 v235, v235, v219, v219\nv_add_f32 v105, v105, v137\nv_fma_f32 v236, v236, v219, v219\nM 8,172,120\nbuffer_load_dwordx4 v221, s[12:15], s63 offen offset:1024 lds\n"
"G 8\nv_add_f32 v106, v106, v138\nv_fma_f32 v237, v237, v219, v219\nM 24,168,108\nR 166,186,20480\ns_add_u32 s63, s54, 1147136\n"
"v_add_f32 v107, v107, v139\nv_rcp_f32 v234, v234\nM 24,172,124\nG 5\nv_rcp_f32 v235, v235\nv_add_f32 v108, v108, v140\n"
"v_rcp_f32 v236, v236\nM 4,176,96\nR 168,185,16384\nv_mul_f32 v234, v52, v234\nv_add_f32 v109, v109, v141\nv_rcp_f32 v237, v237\n"
"M 4,180,112\ns_add_u32 m0, s50, 90112\nv_mul_f32 v235, v53, v235\nv_mul_f32 v236, v54, v236\nG 3\nv_add_f32 v110, v110, v142\n"
"v_mul_f32 v237, v55, v237\nM 20,176,100\nR 170,185,20480\nv_cvt_pk_bf16_f32 v50, v234, v235\nv_add_f32 v111, v111, v143\nv_cvt_pk_bf16_f32 v51, v236, v237\n"
"M 20,180,116\nbuffer_load_dwordx4 v220, s[12:15], s63 offen lds\nds_read2st64_b32 v[144:145], v225 offset0:16 offset1:17\nv_mul_f32 v230, v56, v56\nM 12,176,104\nR 172,187,16384\n"
"ds_read2st64_b32 v[146:147], v225 offset0:18 offset1:19\nv_mul_f32 v231, v57, v57\nv_mul_f32 v232, v58, v58\nM 12,180,120\nbuffer_store_dwordx4 v[48:51], v252, s[20:23], s85 offen offset:0 sc1\nds_read2st64_b32 v[148:149], v225 offset0:20 offset1:21\n"
"v_mul_f32 v233, v59, v59\nM 28,176,108\nR 174,187,20480\nds_read2st64_b32 v[150:151], v225 offset0:22 offset1:23\nv_fma_f32 v230, v230, v218, v217\nv_fma_f32 v231, v231, v218, v217\n"
"M 28,180,124\nds_read2st64_b32 v[152:153], v225 offset0:24 offset1:25\nG 9\nv_fma_f32 v232, v232, v218, v217\nv_fma_f32 v233, v233, v218, v217\nv_mul_f32 v230, v56, v230\n"
"M 0,160,128\nR 176,184,24576\nds_read2st64_b32 v[154:155], v225 offset0:26 offset1:27\nv_mul_f32 v231, v57, v231\nv_mul_f32 v232, v58, v232\nM 0,164,144\n"
"buffer_load_dwordx4 v221, s[12:15], s63 offen offset:1024 lds\nG 8\nv_add_f32 v112, v112, v144\nv_mul_f32 v233, v59, v233\nM 16,160,132\nR 178,184,28672\n"
"v_mul_f32 v234, v60, v60\nv_add_f32 v113, v113, v145\nv_exp_f32 v230, v230\nM 16,164,148\ns_add_u32 m0, s50, 98304\nv_exp_f32 v231, v231\n"
"G 7\nv_add_f32 v114, v114, v146\nv_exp_f32 v232, v232\nM 8,160,136\nR 180,186,24576\nLq1067:\n"
"s_lshr_b32 s60, s45, 3\ns_lshl2_add_u32 s60, s60, s40\ns_lshl_b32 s60, s60, 8\nv_lshl_add_u32 v224, v252, 3, s60\nv_add_u32 v224, Lq1067+4096-LqPC, v224\ns_mov_b64 exec, 3\n"
"global_load_dword v224, v224, s[98:99]\ns_mov_b64 exec, -1\nv_add_f32 v115, v115, v147\nv_exp_f32 v233, v233\nM 8,164,152\ns_add_u32 s63, s54, 1376512\n"
"v_fma_f32 v230, v230, v219, v219\nv_fma_f32 v231, v231, v219, v219\nG 7\nv_add_f32 v116, v116, v148\nv_fma_f32 v232, v232, v219, v219\nM 24,160,140\n"
"R 182,186,28672\nv_fma_f32 v233, v233, v219, v219\nv_add_f32 v117, v117, v149\nv_rcp_f32 v230, v230\nM 24,164,156\nds_read2st64_b32 v[156:157], v225 offset0:28 offset1:29\n"
"ds_read2st64_b32 v[158:159], v225 offset0:30 offset1:31\nG 9\nv_rcp_f32 v231, v231\nG 8\nv_add_f32 v118, v118, v150\nv_add_f32 v119, v119, v151\n"
"G 7\nv_add_f32 v120, v120, v152\nv_add_f32 v121, v121, v153\nG 5\nv_add_f32 v122, v122, v154\nv_add_f32 v123, v123, v155\n"
"G 1\nv_add_f32 v124, v124, v156\nv_add_f32 v125, v125, v157\nG 0\nv_add_f32 v126, v126, v158\nv_add_f32 v127, v127, v159\n"
"v_rcp_f32 v232, v232\nM 4,168,128\nR 160,185,24576\nv_mul_f32 v230, v56, v230\nv_rcp_f32 v233, v233\nM 4,172,144\n"
"buffer_load_dwordx4 v220, s[12:15], s63 offen lds\nv_mul_f32 v231, v57, v231\nM 20,168,132\nR 162,185,28672\nv_mul_f32 v232, v58, v232\nv_mul_f32 v233, v59, v233\n"
"M 20,172,148\nv_cvt_pk_bf16_f32 v56, v230, v231\nv_cvt_pk_bf16_f32 v57, v232, v233\nv_mul_f32 v235, v61, v61\nM 12,168,136\nR 164,187,24576\n"
"v_mul_f32 v236, v62, v62\nv_mul_f32 v237, v63, v63\nM 12,172,152\nbuffer_load_dwordx4 v221, s[12:15], s63 offen offset:1024 lds\nv_fma_f32 v234, v234, v218, v217\nM 28,168,140\n"
"R 166,187,28672\ns_add_u32 m0, s50, 106496\nv_fma_f32 v235, v235, v218, v217\nM 28,172,156\nv_fma_f32 v236, v236, v218, v217\nv_fma_f32 v237, v237, v218, v217\n"
"v_mul_f32 v234, v60, v234\nM 0,176,160\nR 168,184,32768\ns_add_u32 s63, s54, 1605888\nv_mul_f32 v235, v61, v235\nM 0,180,176\n"
"buffer_load_dwordx4 v220, s[12:15], s63 offen lds\nv_mul_f32 v236, v62, v236\nM 16,176,164\nR 170,184,36864\nv_mul_f32 v237, v63, v237\nv_exp_f32 v234, v234\n"
"M 16,180,180\nv_exp_f32 v235, v235\nv_exp_f32 v236, v236\nM 8,176,168\nR 172,186,32768\nv_fma_f32 v234, v234, v219, v219\n"
"v_exp_f32 v237, v237\nM 8,180,184\nbuffer_load_dwordx4 v221, s[12:15], s63 offen offset:1024 lds\nv_fma_f32 v235, v235, v219, v219\nM 24,176,172\nR 174,186,36864\n"
"s_add_u32 s63, s54, 1835264\nv_fma_f32 v236, v236, v219, v219\nM 24,180,188\nv_rcp_f32 v234, v234\nv_fma_f32 v237, v237, v219, v219\nv_rcp_f32 v235, v235\n"
"v_rcp_f32 v236, v236\nv_rcp_f32 v237, v237\nv_mul_f32 v234, v60, v234\nv_mul_f32 v235, v61, v235\nv_mul_f32 v236, v62, v236\nv_mul_f32 v237, v63, v237\n"
"v_cvt_pk_bf16_f32 v58, v234, v235\nv_cvt_pk_bf16_f32 v59, v236, v237\nbuffer_store_dwordx4 v[56:59], v252, s[20:23], s85 offen offset:1024 sc1\nG 4\nM 4,160,160\nR 176,185,32768\n"
"s_waitcnt vmcnt(17)\ns_barrier\nv_mul_f32 v230, v96, v96\nM 4,164,176\ns_add_u32 m0, s50, 114688\ns_add_u32 s84, s73, 37750784\n"
"s_add_u32 s85, s73, 37865472\nv_mul_f32 v231, v97, v97\nM 20,160,164\nR 178,185,36864\nv_mul_f32 v232, v98, v98\nv_mul_f32 v233, v99, v99\n"
"M 20,164,180\nbuffer_load_dwordx4 v220, s[12:15], s63 offen lds\nv_fma_f32 v230, v230, v215, v214\nM 12,160,168\nR 180,187,32768\nv_fma_f32 v231, v231, v215, v214\n"
"v_fma_f32 v232, v232, v215, v214\nM 12,164,184\nv_fma_f32 v233, v233, v215, v214\nv_mul_f32 v230, v96, v230\nv_mul_f32 v231, v97, v231\nM 28,160,172\n"
"R 182,187,36864\ns_mov_b32 s60, 158720\nv_bfe_u32 v242, v252, 4, 4\nv_lshl_add_u32 v242, v242, 3, s60\nv_mul_f32 v232, v98, v232\nM 28,164,188\n"
"ds_read_b64 v[238:239], v242 offset:0\nG 5\nv_mul_f32 v233, v99, v233\nM 0,168,192\nR 160,184,40960\nds_read_b64 v[240:241], v242 offset:128\n"
"v_exp_f32 v230, v230\nM 0,172,208\nds_read_b64 v[242:243], v242 offset:0\nv_fma_f32 v230, v230, v216, v216\nv_exp_f32 v231, v231\nM 16,168,196\n"
"R 162,184,45056\nv_fma_f32 v231, v231, v216, v216\nv_exp_f32 v232, v232\nM 16,172,212\nv_exp_f32 v233, v233\nv_fma_f32 v232, v232, v216, v216\n"
"M 8,168,200\nR 164,186,40960\nv_fma_f32 v233, v233, v216, v216\nv_rcp_f32 v230, v230\nM 8,172,216\nv_rcp_f32 v231, v231\n"
"v_rcp_f32 v232, v232\nM 24,168,204\nR 166,186,45056\nv_mul_f32 v230, v96, v230\nv_rcp_f32 v233, v233\nM 24,172,220\n"
"G 7\nv_mul_f32 v231, v97, v231\nv_mul_f32 v232, v98, v232\nv_mul_f32 v233, v99, v233\nM 4,176,192\nR 168,185,40960\n"
"v_cvt_pk_bf16_f32 v96, v230, v231\nv_cvt_pk_bf16_f32 v97, v232, v233\nM 4,180,208\nv_mul_f32 v234, v100, v100\nv_mul_f32 v235, v101, v101\nv_mul_f32 v236, v102, v102\n"
"M 20,176,196\nR 170,185,45056\nv_mul_f32 v237, v103, v103\nv_fma_f32 v234, v234, v215, v214\nM 20,180,212\nv_fma_f32 v235, v235, v215, v214\n"
"v_fma_f32 v236, v236, v215, v214\nv_fma_f32 v237, v237, v215, v214\nM 12,176,200\nR 172,187,40960\nG 7\nv_mfma_f32_16x16x32_bf16 v[0:3], v[238:241], v[208:211], v[0:3]\n"
"v_mul_f32 v234, v100, v234\nv_mul_f32 v235, v101, v235\nM 12,180,216\nv_exp_f32 v234, v234\nv_mul_f32 v236, v102, v236\nM 28,176,204\n"
"R 174,187,45056\nG 7\nv_mfma_f32_16x16x32_bf16 v[4:7], v[240:243], v[208:211], v[4:7]\nv_mul_f32 v237, v103, v237\nv_exp_f32 v235, v235\nM 28,180,220\n"
"G 4\nv_exp_f32 v236, v236\nv_exp_f32 v237, v237\nM 0,160,224\nR 176,184,49152\nv_mfma_f32_16x16x32_bf16 v[16:19], v[240:243], v[210:213], v[16:19]\n"
"v_fma_f32 v234, v234, v216, v216\nv_fma_f32 v235, v235, v216, v216\nM 0,164,240\nv_rcp_f32 v234, v234\nv_fma_f32 v236, v236, v216, v216\nM 16,160,228\n"
"R 178,184,53248\nv_mfma_f32_16x16x32_bf16 v[20:23], v[238:241], v[210:213], v[20:23]\nv_fma_f32 v237, v237, v216, v216\nv_rcp_f32 v235, v235\nM 16,164,244\nv_rcp_f32 v236, v236\n"
"v_rcp_f32 v237, v237\nM 8,160,232\nR 180,184,57344\nv_mul_f32 v234, v100, v234\nv_mul_f32 v235, v101, v235\nM 8,164,248\n"
"v_mul_f32 v236, v102, v236\nv_mul_f32 v237, v103, v237\nv_cvt_pk_bf16_f32 v98, v234, v235\nM 24,160,236\nR 182,184,61440\nv_cvt_pk_bf16_f32 v99, v236, v237\n"
"v_mul_f32 v230, v104, v104\nM 24,164,252\nG 4\nv_mul_f32 v231, v105, v105\nv_mul_f32 v232, v106, v106\nv_mul_f32 v233, v107, v107\n"
"M 4,168,224\nR 160,185,49152\nv_fma_f32 v230, v230, v215, v214\nv_fma_f32 v231, v231, v215, v214\nM 4,172,240\nv_fma_f32 v232, v232, v215, v214\n"
"v_fma_f32 v233, v233, v215, v214\nv_mul_f32 v230, v104, v230\nM 20,168,228\nR 162,185,53248\nv_mul_f32 v231, v105, v231\nv_mul_f32 v232, v106, v232\n"
"M 20,172,244\nv_exp_f32 v230, v230\nv_mul_f32 v233, v107, v233\nM 12,168,232\nR 164,185,57344\nv_fma_f32 v230, v230, v216, v216\n"
"v_exp_f32 v231, v231\nM 12,172,248\nv_exp_f32 v232, v232\nv_exp_f32 v233, v233\nM 28,168,236\nR 166,185,61440\n"
"v_fma_f32 v231, v231, v216, v216\nv_fma_f32 v232, v232, v216, v216\nM 28,172,252\ns_waitcnt vmcnt(14)\ns_barrier\nG 4\n"
"v_fma_f32 v233, v233, v216, v216\nv_mfma_f32_16x16x32_bf16 v[32:35], v[176:179], a[0:3], 0\nR 168,186,49152\nv_mul_f32 v234, v108, v108\nv_rcp_f32 v230, v230\nM 32,180,16\n"
"s_mov_b32 m0, s51\nv_rcp_f32 v231, v231\nv_rcp_f32 v232, v232\nv_mfma_f32_16x16x32_bf16 v[48:51], v[176:179], a[4:7], 0\nR 170,186,53248\nv_mul_f32 v230, v104, v230\n"
"v_rcp_f32 v233, v233\nM 48,180,20\nv_mul_f32 v231, v105, v231\nv_mul_f32 v232, v106, v232\nv_mul_f32 v233, v107, v233\nv_mfma_f32_16x16x32_bf16 v[64:67], v[176:179], a[8:11], 0\n"
"R 172,186,57344\nv_mov_b32 v226, s46\nv_cvt_pk_bf16_f32 v104, v230, v231\nM 64,180,24\nds_write_addtid_b32 v8 offset:0\nv_mov_b32 v227, s47\n"
"v_cvt_pk_bf16_f32 v105, v232, v233\nv_mul_f32 v235, v109, v109\nv_mfma_f32_16x16x32_bf16 v[80:83], v[176:179], a[12:15], 0\nR 174,186,61440\ns_add_u32 s61, s81, 845824\ns_lshl_b32 s62, s44, 5\n"
"s_add_u32 s61, s61, s62\ns_waitcnt vmcnt(0)\ns_mov_b64 exec, 1\nbuffer_atomic_swap_x2 v[226:227], off, s[20:23], s61 sc1\ns_mov_b64 exec, -1\nv_mul_f32 v236, v110, v110\n"
"M 80,180,28\nds_write_addtid_b32 v9 offset:256\nG 6\nv_mul_f32 v237, v111, v111\nv_fma_f32 v234, v234, v215, v214\nv_fma_f32 v235, v235, v215, v214\n"
"v_mfma_f32_16x16x32_bf16 v[36:39], v[160:163], a[0:3], 0\nR 176,187,49152\nds_write_addtid_b32 v10 offset:512\nv_fma_f32 v236, v236, v215, v214\nv_fma_f32 v237, v237, v215, v214\nM 36,164,16\n"
"buffer_store_dwordx4 v[96:99], v252, s[20:23], s84 offen offset:0 sc1\nds_write_addtid_b32 v11 offset:768\nv_mul_f32 v234, v108, v234\nv_mfma_f32_16x16x32_bf16 v[52:55], v[160:163], a[4:7], 0\nR 178,187,53248\nds_write_addtid_b32 v12 offset:1024\n"
"v_mul_f32 v235, v109, v235\nv_mul_f32 v236, v110, v236\nM 52,164,20\nds_write_addtid_b32 v13 offset:1280\nv_exp_f32 v234, v234\nv_mul_f32 v237, v111, v237\n"
"v_mfma_f32_16x16x32_bf16 v[68:71], v[160:163], a[8:11], 0\nR 180,187,57344\nds_write_addtid_b32 v14 offset:1536\nv_fma_f32 v234, v234, v216, v216\nv_exp_f32 v235, v235\nM 68,164,24\n"
"ds_write_addtid_b32 v15 offset:1792\nv_exp_f32 v236, v236\nv_exp_f32 v237, v237\nv_mfma_f32_16x16x32_bf16 v[84:87], v[160:163], a[12:15], 0\nR 182,187,61440\nds_write_addtid_b32 v24 offset:4096\n"
"v_fma_f32 v235, v235, v216, v216\nv_fma_f32 v236, v236, v216, v216\nM 84,164,28\nds_write_addtid_b32 v25 offset:4352\nG 13\nv_rcp_f32 v234, v234\n"
"v_fma_f32 v237, v237, v216, v216\nv_mfma_f32_16x16x32_bf16 v[40:43], v[168:171], a[0:3], 0\nR 160,188,0\nds_write_addtid_b32 v26 offset:4608\nds_write_addtid_b32 v27 offset:4864\nds_write_addtid_b32 v28 offset:5120\n"
"ds_write_addtid_b32 v29 offset:5376\nds_write_addtid_b32 v30 offset:5632\nds_write_addtid_b32 v31 offset:5888\nv_mul_f32 v234, v108, v234\nv_rcp_f32 v235, v235\nM 40,172,16\n"
"s_add_u32 m0, s50, 114688\ns_nop 0\nbuffer_load_dwordx4 v221, s[12:15], s63 offen offset:1024 lds\nv_rcp_f32 v236, v236\nv_mfma_f32_16x16x32_bf16 v[56:59], v[168:171], a[4:7], 0\nR 162,188,4096\n"
"v_mul_f32 v235, v109, v235\nv_rcp_f32 v237, v237\nM 56,172,20\nG 1\ns_barrier\ns_add_u32 s63, s54, 2064640\n"
"v_mul_f32 v236, v110, v236\nv_mfma_f32_16x16x32_bf16 v[72:75], v[168:171], a[8:11], 0\nR 164,188,8192\nv_mov_b32 v248, 0\nv_mul_f32 v237, v111, v237\nM 72,172,24\n"
"s_add_u32 m0, s50, 0\nds_read2st64_b32 v[8:9], v225 offset0:0 offset1:1\nv_mov_b32 v249, 0\nv_cvt_pk_bf16_f32 v106, v234, v235\nv_cvt_pk_bf16_f32 v107, v236, v237\nv_mfma_f32_16x16x32_bf16 v[88:91], v[168:171], a[12:15], 0\n"
"R 166,188,12288\ns_add_u32 s61, s82, 845824\ns_mov_b64 exec, 0xf\nbuffer_atomic_or_x2 v[248:249], v250, s[20:23], s61 offen sc0 sc1\ns_mov_b64 exec, -1\nv_mul_f32 v230, v112, v112\n"
"M 88,172,28\nds_read2st64_b32 v[10:11], v225 offset0:2 offset1:3\nv_mul_f32 v231, v113, v113\nv_mul_f32 v232, v114, v114\nv_mul_f32 v233, v115, v115\nv_mfma_f32_16x16x32_bf16 v[44:47], v[176:179], a[0:3], 0\n"
"R 168,189,0\nds_read2st64_b32 v[12:13], v225 offset0:4 offset1:5\nv_fma_f32 v230, v230, v218, v217\nv_fma_f32 v231, v231, v218, v217\nM 44,180,16\nbuffer_load_dwordx4 v220, s[12:15], s63 offen lds\n"
"ds_read2st64_b32 v[14:15], v225 offset0:6 offset1:7\nv_fma_f32 v232, v232, v218, v217\nv_mfma_f32_16x16x32_bf16 v[60:63], v[176:179], a[4:7], 0\nR 170,189,4096\nds_read2st64_b32 v[24:25], v225 offset0:16 offset1:17\nv_fma_f32 v233, v233, v218, v217\n"
"v_mul_f32 v230, v112, v230\nM 60,180,20\nbuffer_store_dwordx4 v[104:107], v252, s[20:23], s84 offen offset:1024 sc1\nG 7\nv_add_f32 v0, v0, v8\nv_mul_f32 v231, v113, v231\n"
"v_mfma_f32_16x16x32_bf16 v[76:79], v[176:179], a[8:11], 0\nR 172,189,8192\nv_mul_f32 v232, v114, v232\nv_add_f32 v1, v1, v9\nv_mul_f32 v233, v115, v233\nM 76,180,24\n"
"ds_read2st64_b32 v[26:27], v225 offset0:18 offset1:19\nv_exp_f32 v230, v230\nv_exp_f32 v231, v231\nv_mfma_f32_16x16x32_bf16 v[92:95], v[176:179], a[12:15], 0\nR 174,189,12288\nv_fma_f32 v230, v230, v219, v219\n"
"G 8\nv_add_f32 v2, v2, v10\nv_exp_f32 v232, v232\nM 92,180,28\nv_exp_f32 v233, v233\nv_add_f32 v3, v3, v11\n"
"v_fma_f32 v231, v231, v219, v219\nM 32,160,32\nR 176,190,0\nv_fma_f32 v232, v232, v219, v219\nG 7\nv_add_f32 v4, v4, v12\n"
"v_fma_f32 v233, v233, v219, v219\nM 32,164,48\nbuffer_load_dwordx4 v221, s[12:15], s63 offen offset:1024 lds\nv_add_f32 v5, v5, v13\nv_rcp_f32 v230, v230\nM 48,160,36\n"
"R 178,190,4096\nv_mul_f32 v230, v112, v230\nG 7\nv_add_f32 v6, v6, v14\nv_rcp_f32 v231, v231\nM 48,164,52\n"
"s_add_u32 m0, s50, 8192\nv_rcp_f32 v232, v232\nv_add_f32 v7, v7, v15\nv_rcp_f32 v233, v233\nM 64,160,40\nR 180,190,8192\n"
"v_mul_f32 v231, v113, v231\nG 6\nv_add_f32 v16, v16, v24\nv_mul_f32 v232, v114, v232\nM 64,164,56\ns_add_u32 s63, s54, 2294016\n"
"ds_read2st64_b32 v[28:29], v225 offset0:20 offset1:21\nv_add_f32 v17, v17, v25\nv_mul_f32 v233, v115, v233\nM 80,160,44\nR 182,190,12288\nG 6\n"
"v_add_f32 v18, v18, v26\nv_add_f32 v19, v19, v27\nM 80,164,60\nds_read2st64_b32 v[30:31], v225 offset0:22 offset1:23\nG 6\nv_cvt_pk_bf16_f32 v112, v230, v231\n"
"M 36,168,32\nR 160,191,0\nv_cvt_pk_bf16_f32 v113, v232, v233\nM 36,172,48\nbuffer_load_dwordx4 v220, s[12:15], s63 offen lds\nG 3\n"
"v_add_f32 v20, v20, v28\nv_add_f32 v21, v21, v29\nG 1\nv_add_f32 v22, v22, v30\nv_add_f32 v23, v23, v31\nM 52,168,36\n"
"R 162,191,4096\nv_mul_f32 v234, v116, v116\nM 52,172,52\nv_mul_f32 v235, v117, v117\nv_mul_f32 v236, v118, v118\nM 68,168,40\n"
"R 164,191,8192\nv_mul_f32 v237, v119, v119\nM 68,172,56\nbuffer_load_dwordx4 v221, s[12:15], s63 offen offset:1024 lds\nM 84,168,44\nR 166,191,12288\n"
"s_add_u32 m0, s50, 16384\nM 84,172,60\nv_fma_f32 v234, v234, v218, v217\nv_fma_f32 v235, v235, v218, v217\nM 40,176,32\nR 168,188,16384\n"
"s_add_u32 s63, s54, 2523392\nM 40,180,48\nbuffer_load_dwordx4 v220, s[12:15], s63 offen lds\nM 56,176,36\nR 170,188,20480\nv_fma_f32 v236, v236, v218, v217\n"
"M 56,180,52\nv_fma_f32 v237, v237, v218, v217\nv_mul_f32 v234, v116, v234\nM 72,176,40\nR 172,188,24576\nv_mul_f32 v235, v117, v235\n"
"M 72,180,56\ns_add_u32 s64, s68, 37748736\nv_exp_f32 v234, v234\nM 88,176,44\nR 174,188,28672\nv_mul_f32 v236, v118, v236\n"
"M 88,180,60\nG 4\nv_exp_f32 v235, v235\nM 44,160,32\nR 176,189,16384\ns_waitcnt vmcnt(6)\n"
"v_cmp_eq_u64 s[86:87], s[46:47], v[248:249]\nM 44,164,48\nbuffer_load_dwordx4 v221, s[12:15], s63 offen offset:1024 lds\nM 60,160,36\nR 178,189,20480\ns_and_b32 s62, s86, 0xf\n"
"s_cmp_eq_u32 s62, 0xf\ns_cbranch_scc0 Lq867\nLq868:\nM 60,164,52\ns_add_u32 s65, s68, 37863424\nv_exp_f32 v236, v236\n"
"M 76,160,40\nR 180,189,24576\nv_mul_f32 v237, v119, v237\nM 76,164,56\nbuffer_load_dwordx4 a[0:3], v252, s[20:23], s64 offen sc1\nM 92,160,44\n"
"R 182,189,28672\nv_fma_f32 v234, v234, v219, v219\nM 92,164,60\nG 4\nv_exp_f32 v237, v237\nM 32,168,64\n"
"R 160,190,16384\nv_fma_f32 v235, v235, v219, v219\nM 32,172,80\ns_add_u32 s66, s69, 37748736\nv_rcp_f32 v234, v234\nM 48,168,68\n"
"R 162,190,20480\nv_fma_f32 v236, v236, v219, v219\nM 48,172,84\nbuffer_load_dwordx4 a[4:7], v252, s[20:23], s65 offen sc1\nM 64,168,72\nR 164,190,24576\n"
"v_fma_f32 v237, v237, v219, v219\nM 64,172,88\nv_rcp_f32 v235, v235\nM 80,168,76\nR 166,190,28672\nv_mul_f32 v234, v116, v234\n"
"M 80,172,92\nG 4\nv_rcp_f32 v236, v236\nM 36,176,64\nR 168,191,16384\nv_mul_f32 v235, v117, v235\n"
"M 36,180,80\nbuffer_load_dwordx4 a[8:11], v252, s[20:23], s66 offen sc1\nM 52,176,68\nR 170,191,20480\nv_mul_f32 v236, v118, v236\nM 52,180,84\n"
"s_add_u32 s67, s69, 37863424\nv_rcp_f32 v237, v237\nM 68,176,72\nR 172,191,24576\ns_add_u32 m0, s50, 24576\nM 68,180,88\n"
"s_add_u32 s64, s68, 37755904\nv_mul_f32 v237, v119, v237\nv_cvt_pk_bf16_f32 v114, v234, v235\nM 84,176,76\nR 174,191,28672\ns_add_u32 s63, s54, 2752768\n"
"M 84,180,92\nG 4\nv_cvt_pk_bf16_f32 v115, v236, v237\nv_mul_f32 v230, v120, v120\nM 40,160,64\nR 176,188,32768\n"
"s_waitcnt vmcnt(7)\ns_barrier\nM 40,164,80\nbuffer_load_dwordx4 a[12:15], v252, s[20:23], s67 offen sc1\nbuffer_load_dwordx4 v220, s[12:15], s63 offen lds\nM 56,160,68\n"
"R 178,188,36864\nv_mul_f32 v231, v121, v121\nM 56,164,84\ns_add_u32 s65, s68, 37870592\nv_mul_f32 v232, v122, v122\nv_mul_f32 v233, v123, v123\n"
"M 72,160,72\nR 180,188,40960\nv_fma_f32 v230, v230, v218, v217\nM 72,164,88\nbuffer_load_dwordx4 a[16:19], v252, s[20:23], s64 offen sc1\nbuffer_load_dwordx4 v221, s[12:15], s63 offen offset:1024 lds\n"
"M 88,160,76\nR 182,188,45056\ns_add_u32 m0, s50, 32768\nM 88,164,92\nG 4\nv_fma_f32 v231, v231, v218, v217\n"
"v_fma_f32 v232, v232, v218, v217\nM 44,168,64\nR 160,189,32768\ns_add_u32 s63, s54, 2982144\nM 44,172,80\ns_add_u32 s66, s69, 37755904\n"
"buffer_load_dwordx4 v220, s[12:15], s63 offen lds\nM 60,168,68\nR 162,189,36864\nv_fma_f32 v233, v233, v218, v217\nM 60,172,84\nbuffer_load_dwordx4 a[20:23], v252, s[20:23], s65 offen sc1\n"
"M 76,168,72\nR 164,189,40960\nv_mul_f32 v230, v120, v230\nM 76,172,88\nbuffer_load_dwordx4 v221, s[12:15], s63 offen offset:1024 lds\nM 92,168,76\n"
"R 166,189,45056\nv_mul_f32 v231, v121, v231\nM 92,172,92\nG 4\nv_exp_f32 v230, v230\nM 32,176,96\n"
"R 168,190,32768\nv_mul_f32 v232, v122, v232\nM 32,180,112\nbuffer_load_dwordx4 a[24:27], v252, s[20:23], s66 offen sc1\nM 48,176,100\nR 170,190,36864\n"
"v_mul_f32 v233, v123, v233\nM 48,180,116\ns_add_u32 s67, s69, 37870592\nv_exp_f32 v231, v231\nM 64,176,104\nR 172,190,40960\n"
"v_fma_f32 v230, v230, v219, v219\nM 64,180,120\nbuffer_store_dwordx4 v[112:115], v252, s[20:23], s85 offen offset:0 sc1\nM 80,176,108\nR 174,190,45056\nv_fma_f32 v231, v231, v219, v219\n"
"M 80,180,124\nG 4\nv_exp_f32 v232, v232\nM 36,160,96\nR 176,191,32768\nv_fma_f32 v232, v232, v219, v219\n"
"M 36,164,112\nbuffer_load_dwordx4 a[28:31], v252, s[20:23], s67 offen sc1\nM 52,160,100\nR 178,191,36864\nv_mul_f32 v234, v124, v124\nM 52,164,116\n"
"s_add_u32 s64, s68, 37749760\nv_exp_f32 v233, v233\nM 68,160,104\nR 180,191,40960\nv_fma_f32 v233, v233, v219, v219\nM 68,164,120\n"
"s_add_u32 s65, s68, 37864448\nv_rcp_f32 v230, v230\nM 84,160,108\nR 182,191,45056\nv_mul_f32 v230, v120, v230\nM 84,164,124\n"
"G 4\nv_rcp_f32 v231, v231\nM 40,168,96\nR 160,188,49152\nv_mul_f32 v231, v121, v231\nM 40,172,112\n"
"buffer_load_dwordx4 a[32:35], v252, s[20:23], s64 offen sc1\nM 56,168,100\nR 162,188,53248\nv_cvt_pk_bf16_f32 v120, v230, v231\nM 56,172,116\ns_add_u32 s66, s69, 37749760\n"
"v_rcp_f32 v232, v232\nM 72,168,104\nR 164,184,0\nv_mul_f32 v232, v122, v232\nM 72,172,120\nbuffer_load_dwordx4 a[36:39], v252, s[20:23], s65 offen sc1\n"
"M 88,168,108\nR 166,184,4096\nv_mul_f32 v235, v125, v125\nM 88,172,124\nG 4\nv_rcp_f32 v233, v233\n"
"M 44,176,96\nR 168,189,49152\ns_add_u32 m0, s50, 40960\nM 44,180,112\ns_add_u32 s67, s69, 37864448\nv_mul_f32 v233, v123, v233\n"
"v_cvt_pk_bf16_f32 v121, v232, v233\nM 60,176,100\nR 170,189,53248\ns_add_u32 s63, s54, 3211520\nM 60,180,116\nbuffer_load_dwordx4 a[40:43], v252, s[20:23], s66 offen sc1\n"
"M 76,176,104\nR 172,185,0\nv_mul_f32 v236, v126, v126\nM 76,180,120\nbuffer_load_dwordx4 v220, s[12:15], s63 offen lds\nM 92,176,108\n"
"R 174,185,4096\nv_mul_f32 v237, v127, v127\nM 92,180,124\nG 4\nv_fma_f32 v234, v234, v218, v217\nv_fma_f32 v235, v235, v218, v217\n"
"M 32,160,128\nR 176,190,49152\nv_fma_f32 v236, v236, v218, v217\nM 32,164,144\nbuffer_load_dwordx4 a[44:47], v252, s[20:23], s67 offen sc1\nbuffer_load_dwordx4 v221, s[12:15], s63 offen offset:1024 lds\n"
"M 48,160,132\nR 178,190,53248\ns_add_u32 m0, s50, 49152\nM 48,164,148\ns_add_u32 s64, s68, 37756928\nv_fma_f32 v237, v237, v218, v217\n"
"v_mul_f32 v234, v124, v234\nM 64,160,136\nR 180,186,0\ns_add_u32 s63, s54, 3440896\nM 64,164,152\ns_add_u32 s65, s68, 37871616\n"
"buffer_load_dwordx4 v220, s[12:15], s63 offen lds\nM 80,160,140\nR 182,186,4096\nv_mul_f32 v235, v125, v235\nM 80,164,156\nG 4\n"
"v_exp_f32 v234, v234\nM 36,168,128\nR 160,191,49152\nv_mul_f32 v236, v126, v236\nM 36,172,144\nbuffer_load_dwordx4 a[48:51], v252, s[20:23], s64 offen sc1\n"
"buffer_load_dwordx4 v221, s[12:15], s63 offen offset:1024 lds\nM 52,168,132\nR 162,191,53248\nv_mul_f32 v237, v127, v237\nM 52,172,148\ns_add_u32 s66, s69, 37756928\n"
"v_exp_f32 v235, v235\nM 68,168,136\nR 164,187,0\ns_waitcnt vmcnt(12)\ns_barrier\nM 68,172,152\n"
"buffer_load_dwordx4 a[52:55], v252, s[20:23], s65 offen sc1\ns_movk_i32 s63, 2048\nv_mad_u32_u24 v220, v228, s63, v229\nv_add_u32 v221, 916480, v220\ns_add_u32 m0, s50, 57344\ns_add_u32 s63, s56, 0\n"
"buffer_load_dwordx4 v220, s[16:19], s63 offen lds\nM 84,168,140\nR 166,187,4096\nv_fma_f32 v234, v234, v219, v219\nM 84,172,156\nG 4\n"
"v_exp_f32 v236, v236\nM 40,176,128\nR 168,184,8192\nv_fma_f32 v235, v235, v219, v219\nM 40,180,144\ns_add_u32 s67, s69, 37871616\n"
"buffer_load_dwordx4 v221, s[16:19], s63 offen offset:1024 lds\nM 56,176,132\nR 170,184,12288\ns_add_u32 m0, s50, 65536\nM 56,180,148\nbuffer_load_dwordx4 a[56:59], v252, s[20:23], s66 offen sc1\n"
"M 72,176,136\nR 172,184,16384\ns_add_u32 s63, s56, 524288\nM 72,180,152\nbuffer_load_dwordx4 v220, s[16:19], s63 offen lds\nM 88,176,140\n"
"R 174,184,20480\nv_fma_f32 v236, v236, v219, v219\nM 88,180,156\nG 4\nv_exp_f32 v237, v237\nM 44,160,128\n"
"R 176,185,8192\nv_fma_f32 v237, v237, v219, v219\nM 44,164,144\nbuffer_load_dwordx4 a[60:63], v252, s[20:23], s67 offen sc1\nbuffer_load_dwordx4 v221, s[16:19], s63 offen offset:1024 lds\nM 60,160,132\n"
"R 178,185,12288\nM 60,164,148\nv_rcp_f32 v234, v234\nM 76,160,136\nR 180,185,16384\nv_mul_f32 v234, v124, v234\n"
"M 76,164,152\nv_rcp_f32 v235, v235\nM 92,160,140\nR 182,185,20480\nv_mul_f32 v235, v125, v235\nM 92,164,156\n"
"G 4\nv_rcp_f32 v236, v236\nM 32,168,160\nR 160,186,8192\nv_mul_f32 v236, v126, v236\nM 32,172,176\n"
"s_add_u32 m0, s50, 73728\nv_rcp_f32 v237, v237\nM 48,168,164\nR 162,186,12288\nv_mul_f32 v237, v127, v237\nM 48,172,180\n"
"s_add_u32 s63, s56, 65536\nv_cvt_pk_bf16_f32 v122, v234, v235\nv_cvt_pk_bf16_f32 v123, v236, v237\nM 64,168,168\nR 164,186,16384\nM 64,172,184\n"
"buffer_load_dwordx4 v220, s[16:19], s63 offen lds\nM 80,168,172\nR 166,186,20480\nM 80,172,188\nbuffer_store_dwordx4 v[120:123], v252, s[20:23], s85 offen offset:1024 sc1\nG 4\n"
"M 36,176,160\nR 168,187,8192\nM 36,180,176\nbuffer_load_dwordx4 v221, s[16:19], s63 offen offset:1024 lds\nM 52,176,164\nR 170,187,12288\n"
"s_add_u32 s63, s56, 589824\nv_mul_f32 v230, v0, v0\nM 52,180,180\ns_add_u32 m0, s50, 81920\ns_add_u32 s84, s73, 37754880\ns_add_u32 s85, s73, 37869568\n"
"v_mul_f32 v231, v1, v1\nM 68,176,168\nR 172,187,16384\nv_mul_f32 v232, v2, v2\nv_mul_f32 v233, v3, v3\nM 68,180,184\n"
"buffer_load_dwordx4 v220, s[16:19], s63 offen lds\nv_fma_f32 v230, v230, v215, v214\nM 84,176,172\nR 174,187,20480\nv_fma_f32 v231, v231, v215, v214\nv_fma_f32 v232, v232, v215, v214\n"
"M 84,180,188\nG 4\nv_fma_f32 v233, v233, v215, v214\nv_mul_f32 v230, v0, v230\nv_mul_f32 v231, v1, v231\nM 40,160,160\n"
"R 176,184,24576\nv_mul_f32 v232, v2, v232\nv_mul_f32 v233, v3, v233\nM 40,164,176\nv_exp_f32 v230, v230\nv_exp_f32 v231, v231\n"
"M 56,160,164\nR 178,184,28672\nv_fma_f32 v230, v230, v216, v216\nv_exp_f32 v232, v232\nM 56,164,180\nv_exp_f32 v233, v233\n"
"v_fma_f32 v231, v231, v216, v216\nM 72,160,168\nR 180,184,32768\nv_fma_f32 v232, v232, v216, v216\nv_fma_f32 v233, v233, v216, v216\nM 72,164,184\n"
"v_rcp_f32 v230, v230\nv_rcp_f32 v231, v231\nM 88,160,172\nR 182,184,36864\nv_mul_f32 v230, v0, v230\nv_rcp_f32 v232, v232\n"
"M 88,164,188\nG 4\nv_rcp_f32 v233, v233\nv_mul_f32 v231, v1, v231\nM 44,168,160\nR 160,185,24576\n"
"v_mul_f32 v232, v2, v232\nv_mul_f32 v233, v3, v233\nM 44,172,176\nv_cvt_pk_bf16_f32 v0, v230, v231\nv_cvt_pk_bf16_f32 v1, v232, v233\nv_mul_f32 v234, v4, v4\n"
"M 60,168,164\nR 162,185,28672\nv_mul_f32 v235, v5, v5\nv_mul_f32 v236, v6, v6\nM 60,172,180\nv_mul_f32 v237, v7, v7\n"
"v_fma_f32 v234, v234, v215, v214\nv_fma_f32 v235, v235, v215, v214\nM 76,168,168\nR 164,185,32768\nv_fma_f32 v236, v236, v215, v214\nv_fma_f32 v237, v237, v215, v214\n"
"M 76,172,184\nv_mul_f32 v234, v4, v234\nv_mul_f32 v235, v5, v235\nv_mul_f32 v236, v6, v236\nM 92,168,172\nR 166,185,36864\n"
"v_mul_f32 v237, v7, v237\nv_exp_f32 v234, v234\nM 92,172,188\nG 4\nv_exp_f32 v235, v235\nv_exp_f32 v236, v236\n"
"M 32,176,192\nR 168,186,24576\nv_fma_f32 v234, v234, v216, v216\nv_exp_f32 v237, v237\nM 32,180,208\nv_rcp_f32 v234, v234\n"
"v_fma_f32 v235, v235, v216, v216\nM 48,176,196\nR 170,186,28672\nv_fma_f32 v236, v236, v216, v216\nv_fma_f32 v237, v237, v216, v216\nM 48,180,212\n"
"v_rcp_f32 v235, v235\nv_rcp_f32 v236, v236\nM 64,176,200\nR 172,186,32768\nv_mul_f32 v234, v4, v234\nv_rcp_f32 v237, v237\n"
"M 64,180,216\nv_mul_f32 v235, v5, v235\nv_mul_f32 v236, v6, v236\nv_mul_f32 v237, v7, v237\nM 80,176,204\nR 174,186,36864\n"
"v_cvt_pk_bf16_f32 v2, v234, v235\nv_cvt_pk_bf16_f32 v3, v236, v237\nM 80,180,220\nG 4\nv_mul_f32 v230, v16, v16\nv_mul_f32 v231, v17, v17\n"
"v_mul_f32 v232, v18, v18\nM 36,160,192\nR 176,187,24576\ns_waitcnt vmcnt(4)\ns_barrier\nv_mul_f32 v233, v19, v19\n"
"M 36,164,208\nv_fma_f32 v230, v230, v218, v217\nv_fma_f32 v231, v231, v218, v217\nv_fma_f32 v232, v232, v218, v217\nM 52,160,196\nR 178,187,28672\n"
"v_fma_f32 v233, v233, v218, v217\nv_mul_f32 v230, v16, v230\nM 52,164,212\nv_exp_f32 v230, v230\nv_mul_f32 v231, v17, v231\nM 68,160,200\n"
"R 180,187,32768\nv_mul_f32 v232, v18, v232\nv_mul_f32 v233, v19, v233\nM 68,164,216\nv_exp_f32 v231, v231\nv_exp_f32 v232, v232\n"
"M 84,160,204\nR 182,187,36864\nv_fma_f32 v230, v230, v219, v219\nv_exp_f32 v233, v233\nM 84,164,220\nG 4\n"
"v_rcp_f32 v230, v230\nv_fma_f32 v231, v231, v219, v219\nM 40,168,192\nR 160,184,40960\nv_fma_f32 v232, v232, v219, v219\nv_fma_f32 v233, v233, v219, v219\n"
"M 40,172,208\nv_rcp_f32 v231, v231\nv_rcp_f32 v232, v232\nM 56,168,196\nR 162,184,45056\nv_mul_f32 v230, v16, v230\n"
"v_rcp_f32 v233, v233\nM 56,172,212\nv_mul_f32 v231, v17, v231\nv_mul_f32 v232, v18, v232\nv_mul_f32 v233, v19, v233\nM 72,168,200\n"
"R 164,184,49152\nv_cvt_pk_bf16_f32 v16, v230, v231\nv_cvt_pk_bf16_f32 v17, v232, v233\nM 72,172,216\nv_mul_f32 v234, v20, v20\nv_mul_f32 v235, v21, v21\n"
"v_mul_f32 v236, v22, v22\nM 88,168,204\nR 166,184,53248\nv_mov_b32 v226, s46\nv_mul_f32 v237, v23, v23\nM 88,172,220\n"
"G 4\nv_mov_b32 v227, s47\nv_fma_f32 v234, v234, v218, v217\nv_fma_f32 v235, v235, v218, v217\nM 44,176,192\nR 168,185,40960\n"
"s_add_u32 s61, s81, 845832\ns_lshl_b32 s62, s44, 5\ns_add_u32 s61, s61, s62\ns_waitcnt vmcnt(0)\ns_mov_b64 exec, 1\nbuffer_atomic_swap_x2 v[226:227], off, s[20:23], s61 sc1\n"
"s_mov_b64 exec, -1\nv_fma_f32 v236, v236, v218, v217\nM 44,180,208\nbuffer_load_dwordx4 v221, s[16:19], s63 offen offset:1024 lds\nv_fma_f32 v237, v237, v218, v217\nM 60,176,196\n"
"R 170,185,45056\ns_add_u32 s63, s57, 0\nv_mul_f32 v234, v20, v234\nM 60,180,212\ns_add_u32 m0, s50, 90112\nv_exp_f32 v234, v234\n"
"v_mul_f32 v235, v21, v235\nM 76,176,200\nR 172,185,49152\nv_mul_f32 v236, v22, v236\nv_mul_f32 v237, v23, v237\nM 76,180,216\n"
"buffer_load_dwordx4 v220, s[16:19], s63 offen lds\nv_exp_f32 v235, v235\nM 92,176,204\nR 174,185,53248\ns_mov_b32 s60, 158208\nv_bfe_u32 v246, v252, 4, 4\n"
"v_lshl_add_u32 v246, v246, 3, s60\nv_exp_f32 v236, v236\nM 92,180,220\nds_read_b64 v[238:239], v246 offset:0\nG 5\nv_exp_f32 v237, v237\n"
"M 32,160,224\nR 176,186,40960\nds_read_b64 v[240:241], v246 offset:128\nv_fma_f32 v234, v234, v219, v219\nM 32,164,240\nds_read_b64 v[242:243], v246 offset:256\n"
"v_fma_f32 v235, v235, v219, v219\nv_fma_f32 v236, v236, v219, v219\nM 48,160,228\nR 178,186,45056\nds_read_b64 v[244:245], v246 offset:384\nv_fma_f32 v237, v237, v219, v219\n"
"M 48,164,244\nds_read_b64 v[246:247], v246 offset:0\nbuffer_load_dwordx4 v221, s[16:19], s63 offen offset:1024 lds\nv_rcp_f32 v234, v234\nM 64,160,232\nR 180,186,49152\n"
"s_add_u32 m0, s50, 98304\ns_add_u32 s63, s57, 524288\nv_rcp_f32 v235, v235\nM 64,164,248\nbuffer_load_dwordx4 v220, s[16:19], s63 offen lds\nM 80,160,236\n"
"R 182,186,53248\nv_mul_f32 v234, v20, v234\nM 80,164,252\nG 9\nv_rcp_f32 v236, v236\nM 36,168,224\n"
"R 160,187,40960\nv_mul_f32 v235, v21, v235\nM 36,172,240\nbuffer_load_dwordx4 v221, s[16:19], s63 offen offset:1024 lds\nM 52,168,228\nR 162,187,45056\n"
"s_add_u32 s63, s57, 65536\nM 52,172,244\ns_add_u32 m0, s50, 106496\nv_rcp_f32 v237, v237\nM 68,168,232\nR 164,187,49152\n"
"G 9\nv_mfma_f32_16x16x32_bf16 v[32:35], v[238:241], v[208:211], v[32:35]\nv_mul_f32 v236, v22, v236\nM 68,172,248\nbuffer_load_dwordx4 v220, s[16:19], s63 offen lds\nM 84,168,236\n"
"R 166,187,53248\nG 9\nv_mfma_f32_16x16x32_bf16 v[36:39], v[240:243], v[208:211], v[36:39]\nv_mul_f32 v237, v23, v237\nM 84,172,252\nG 4\n"
"v_cvt_pk_bf16_f32 v18, v234, v235\nv_cvt_pk_bf16_f32 v19, v236, v237\nM 40,176,224\nR 168,184,57344\nv_mfma_f32_16x16x32_bf16 v[40:43], v[242:245], v[208:211], v[40:43]\nbuffer_load_dwordx4 v221, s[16:19], s63 offen offset:1024 lds\n"
"M 40,180,240\ns_add_u32 m0, s50, 114688\ns_add_u32 s63, s57, 589824\nbuffer_load_dwordx4 v220, s[16:19], s63 offen lds\nM 56,176,228\nR 170,184,61440\n"
"v_mfma_f32_16x16x32_bf16 v[44:47], v[244:247], v[208:211], v[44:47]\nbuffer_store_dwordx4 v[0:3], v252, s[20:23], s84 offen offset:0 sc1\nM 56,180,244\nbuffer_store_dwordx4 v[16:19], v252, s[20:23], s85 offen offset:0 sc1\nM 72,176,232\nR 172,188,0\n"
"v_mfma_f32_16x16x32_bf16 v[48:51], v[244:247], v[210:213], v[48:51]\nM 72,180,248\nbuffer_load_dwordx4 v221, s[16:19], s63 offen offset:1024 lds\nM 88,176,236\nR 174,188,4096\nv_mfma_f32_16x16x32_bf16 v[52:55], v[238:241], v[210:213], v[52:55]\n"
"M 88,180,252\nG 4\nM 44,160,224\nR 176,185,57344\nv_mfma_f32_16x16x32_bf16 v[56:59], v[240:243], v[210:213], v[56:59]\nM 44,164,240\n"
"M 60,160,228\nR 178,185,61440\nv_mfma_f32_16x16x32_bf16 v[60:63], v[242:245], v[210:213], v[60:63]\nv_mov_b32 v248, 0\nM 60,164,244\nv_mov_b32 v249, 0\n"
"M 76,160,232\nR 180,189,0\ns_add_u32 s61, s82, 845832\ns_mov_b64 exec, 0xf\nbuffer_atomic_or_x2 v[248:249], v250, s[20:23], s61 offen sc0 sc1\ns_mov_b64 exec, -1\n"
"M 76,164,248\nM 92,160,236\nR 182,189,4096\nM 92,164,252\ns_waitcnt vmcnt(11)\ns_barrier\n"
"G 4\nv_mfma_f32_16x16x32_bf16 v[96:99], v[168:171], a[0:3], 0\nR 160,186,57344\nM 96,172,16\ns_mov_b32 m0, s51\nv_mfma_f32_16x16x32_bf16 v[112:115], v[168:171], a[4:7], 0\n"
"R 162,186,61440\nM 112,172,20\nds_write_addtid_b32 v64 offset:0\nv_mfma_f32_16x16x32_bf16 v[128:131], v[168:171], a[8:11], 0\nR 164,190,0\nM 128,172,24\n"
"ds_write_addtid_b32 v65 offset:256\nv_mfma_f32_16x16x32_bf16 v[144:147], v[168:171], a[12:15], 0\nR 166,190,4096\nM 144,172,28\nds_write_addtid_b32 v66 offset:512\nG 7\n"
"v_mfma_f32_16x16x32_bf16 v[100:103], v[176:179], a[0:3], 0\nR 168,187,57344\nM 100,180,16\nds_write_addtid_b32 v67 offset:768\nv_mfma_f32_16x16x32_bf16 v[116:119], v[176:179], a[4:7], 0\nR 170,187,61440\n"
"M 116,180,20\nds_write_addtid_b32 v68 offset:1024\nv_mfma_f32_16x16x32_bf16 v[132:135], v[176:179], a[8:11], 0\nR 172,191,0\nM 132,180,24\nds_write_addtid_b32 v69 offset:1280\n"
"v_mfma_f32_16x16x32_bf16 v[148:151], v[176:179], a[12:15], 0\nR 174,191,4096\nds_write_addtid_b32 v70 offset:1536\nM 148,180,28\nds_write_addtid_b32 v71 offset:1792\nds_write_addtid_b32 v72 offset:2048\n"
"G 11\nv_mfma_f32_16x16x32_bf16 v[104:107], v[160:163], a[0:3], 0\nR 176,188,8192\nds_write_addtid_b32 v73 offset:2304\nM 104,164,16\nds_write_addtid_b32 v74 offset:2560\n"
"ds_write_addtid_b32 v75 offset:2816\nv_mfma_f32_16x16x32_bf16 v[120:123], v[160:163], a[4:7], 0\nR 178,188,12288\nds_write_addtid_b32 v76 offset:3072\nM 120,164,20\nds_write_addtid_b32 v77 offset:3328\n"
"ds_write_addtid_b32 v78 offset:3584\nv_mfma_f32_16x16x32_bf16 v[136:139], v[160:163], a[8:11], 0\nR 180,188,16384\nds_write_addtid_b32 v79 offset:3840\nM 136,164,24\nds_write_addtid_b32 v80 offset:4096\n"
"ds_write_addtid_b32 v81 offset:4352\nv_mfma_f32_16x16x32_bf16 v[152:155], v[160:163], a[12:15], 0\nR 182,188,20480\nds_write_addtid_b32 v82 offset:4608\nM 152,164,28\nds_write_addtid_b32 v83 offset:4864\n"
"ds_write_addtid_b32 v84 offset:5120\nG 15\nv_mfma_f32_16x16x32_bf16 v[108:111], v[168:171], a[0:3], 0\nR 160,189,8192\nds_write_addtid_b32 v85 offset:5376\nM 108,172,16\n"
"ds_write_addtid_b32 v86 offset:5632\nds_write_addtid_b32 v87 offset:5888\nv_mfma_f32_16x16x32_bf16 v[124:127], v[168:171], a[4:7], 0\nR 162,189,12288\nds_write_addtid_b32 v88 offset:6144\nM 124,172,20\n"
"ds_write_addtid_b32 v89 offset:6400\nds_write_addtid_b32 v90 offset:6656\nv_mfma_f32_16x16x32_bf16 v[140:143], v[168:171], a[8:11], 0\nR 164,189,16384\nds_write_addtid_b32 v91 offset:6912\nM 140,172,24\n"
"s_add_u32 s64, s68, 37750784\nds_write_addtid_b32 v92 offset:7168\nds_write_addtid_b32 v93 offset:7424\nv_mfma_f32_16x16x32_bf16 v[156:159], v[168:171], a[12:15], 0\nR 166,189,20480\ns_waitcnt vmcnt(0)\n"
"v_cmp_eq_u64 s[86:87], s[46:47], v[248:249]\nM 156,172,28\nds_write_addtid_b32 v94 offset:7680\nds_write_addtid_b32 v95 offset:7936\nG 15\nM 96,176,32\n"
"R 168,190,8192\ns_and_b32 s62, s86, 0xf\ns_cmp_eq_u32 s62, 0xf\ns_cbranch_scc0 Lq902\nLq903:\nM 96,180,48\n"
"s_add_u32 s65, s68, 37865472\ns_add_u32 m0, s50, 0\nv_mov_b32 v226, s46\nM 112,176,36\nR 170,190,12288\nG 2\n"
"s_barrier\nv_mov_b32 v227, s47\nM 112,180,52\ns_add_u32 s61, s81, 845848\ns_lshl_b32 s62, s44, 5\ns_add_u32 s61, s61, s62\n"
"s_waitcnt vmcnt(0)\ns_mov_b64 exec, 1\nbuffer_atomic_swap_x2 v[226:227], off, s[20:23], s61 sc1\ns_mov_b64 exec, -1\ns_add_u32 s63, s56, 131072\nds_read2st64_b32 v[64:65], v225 offset0:0 offset1:1\n"
"M 128,176,40\nR 172,190,16384\nbuffer_load_dwordx4 v220, s[16:19], s63 offen lds\nds_read2st64_b32 v[66:67], v225 offset0:2 offset1:3\nM 128,180,56\nbuffer_load_dwordx4 a[64:67], v252, s[20:23], s64 offen sc1\n"
"buffer_load_dwordx4 v221, s[16:19], s63 offen offset:1024 lds\nds_read2st64_b32 v[68:69], v225 offset0:4 offset1:5\nM 144,176,44\nR 174,190,20480\ns_add_u32 m0, s50, 8192\ns_add_u32 s63, s56, 655360\n"
"buffer_load_dwordx4 v220, s[16:19], s63 offen lds\nds_read2st64_b32 v[70:71], v225 offset0:6 offset1:7\nM 144,180,60\nds_read2st64_b32 v[72:73], v225 offset0:8 offset1:9\nds_read2st64_b32 v[74:75], v225 offset0:10 offset1:11\nM 100,160,32\n"
"R 176,191,8192\nbuffer_load_dwordx4 v221, s[16:19], s63 offen offset:1024 lds\nds_read2st64_b32 v[76:77], v225 offset0:12 offset1:13\nM 100,164,48\nbuffer_load_dwordx4 a[68:71], v252, s[20:23], s65 offen sc1\nG 9\n"
"v_add_f32 v32, v32, v64\nM 116,160,36\nR 178,191,12288\ns_add_u32 s67, s69, 37865472\nv_add_f32 v33, v33, v65\nM 116,164,52\n"
"s_add_u32 s66, s69, 37750784\ns_add_u32 m0, s50, 16384\ns_add_u32 s63, s56, 196608\nG 8\nv_add_f32 v34, v34, v66\nM 132,160,40\n"
"R 180,191,16384\nbuffer_load_dwordx4 v220, s[16:19], s63 offen lds\nv_add_f32 v35, v35, v67\nM 132,164,56\nbuffer_load_dwordx4 a[72:75], v252, s[20:23], s66 offen sc1\nG 8\n"
"v_add_f32 v36, v36, v68\nM 148,160,44\nR 182,191,20480\nv_add_f32 v37, v37, v69\nG 7\nv_add_f32 v38, v38, v70\n"
"M 148,164,60\nds_read2st64_b32 v[78:79], v225 offset0:14 offset1:15\nv_add_f32 v39, v39, v71\nM 104,168,32\nR 160,188,24576\nbuffer_load_dwordx4 v221, s[16:19], s63 offen offset:1024 lds\n"
"G 8\nv_add_f32 v40, v40, v72\nM 104,172,48\nbuffer_load_dwordx4 a[76:79], v252, s[20:23], s67 offen sc1\nv_add_f32 v41, v41, v73\nM 120,168,36\n"
"R 162,188,28672\nG 8\nv_add_f32 v42, v42, v74\nv_add_f32 v43, v43, v75\nM 120,172,52\ns_add_u32 s64, s68, 37757952\n"
"ds_read2st64_b32 v[80:81], v225 offset0:16 offset1:17\nG 7\nv_add_f32 v44, v44, v76\nM 136,168,40\nR 164,188,32768\nds_read2st64_b32 v[82:83], v225 offset0:18 offset1:19\n"
"v_add_f32 v45, v45, v77\nM 136,172,56\ns_add_u32 s65, s68, 37872640\nds_read2st64_b32 v[84:85], v225 offset0:20 offset1:21\nG 6\nv_add_f32 v46, v46, v78\n"
"M 152,168,44\nR 166,188,36864\nds_read2st64_b32 v[86:87], v225 offset0:22 offset1:23\nv_add_f32 v47, v47, v79\nM 152,172,60\nds_read2st64_b32 v[88:89], v225 offset0:24 offset1:25\n"
"ds_read2st64_b32 v[90:91], v225 offset0:26 offset1:27\nM 108,176,32\nR 168,189,24576\nds_read2st64_b32 v[92:93], v225 offset0:28 offset1:29\nM 108,180,48\nbuffer_load_dwordx4 a[80:83], v252, s[20:23], s64 offen sc1\n"
"G 9\nv_add_f32 v48, v48, v80\nM 124,176,36\nR 170,189,28672\nv_add_f32 v49, v49, v81\nG 8\n"
"v_add_f32 v50, v50, v82\nM 124,180,52\nbuffer_load_dwordx4 a[84:87], v252, s[20:23], s65 offen sc1\nv_add_f32 v51, v51, v83\nM 140,176,40\nR 172,189,32768\n"
"G 8\nv_add_f32 v52, v52, v84\nv_add_f32 v53, v53, v85\nM 140,180,56\ns_add_u32 s66, s69, 37757952\nds_read2st64_b32 v[94:95], v225 offset0:30 offset1:31\n"
"G 7\nv_add_f32 v54, v54, v86\nM 156,176,44\nR 174,189,36864\nv_add_f32 v55, v55, v87\nM 156,180,60\n"
"G 7\nv_add_f32 v56, v56, v88\nv_add_f32 v57, v57, v89\nv_mfma_f32_16x16x32_bf16 v[0:3], v[160:163], a[0:3], 0\nR 176,190,24576\ns_lshr_b32 s60, s45, 3\n"
"s_lshl2_add_u32 s60, s60, s40\ns_lshl_b32 s60, s60, 8\nv_lshl_add_u32 v224, v252, 3, s60\nv_add_u32 v224, LqF-32768-LqPC, v224\ns_mov_b64 exec, 3\nglobal_load_dword v224, v224, s[98:99]\n"
"s_mov_b64 exec, -1\nM 0,164,16\nbuffer_load_dwordx4 a[88:91], v252, s[20:23], s66 offen sc1\nv_mfma_f32_16x16x32_bf16 v[16:19], v[160:163], a[4:7], 0\nR 178,190,28672\nG 8\n"
"v_add_f32 v58, v58, v90\nM 16,164,20\ns_add_u32 s67, s69, 37872640\nv_add_f32 v59, v59, v91\nG 6\nv_add_f32 v60, v60, v92\n"
"v_mfma_f32_16x16x32_bf16 a[224:227], v[160:163], a[8:11], 0\nR 180,190,32768\nv_add_f32 v61, v61, v93\nv_mfma_f32_16x16x32_bf16 a[224:227], v[164:167], a[24:27], a[224:227]\nbuffer_load_dwordx4 a[92:95], v252, s[20:23], s67 offen sc1\nv_mfma_f32_16x16x32_bf16 a[240:243], v[160:163], a[12:15], 0\n"
"R 182,190,36864\nG 5\nv_add_f32 v62, v62, v94\nv_mfma_f32_16x16x32_bf16 a[240:243], v[164:167], a[28:31], a[240:243]\ns_add_u32 s63, s56, 720896\nG 4\n"
"v_add_f32 v63, v63, v95\nv_mfma_f32_16x16x32_bf16 v[4:7], v[168:171], a[0:3], 0\nR 160,191,24576\ns_add_u32 m0, s50, 24576\nv_mul_f32 v230, v32, v32\nM 4,172,16\n"
"s_add_u32 s64, s68, 37751808\ns_add_u32 s84, s73, 37752832\ns_add_u32 s85, s73, 37867520\nv_mul_f32 v231, v33, v33\nv_mfma_f32_16x16x32_bf16 v[20:23], v[168:171], a[4:7], 0\nR 162,191,28672\n"
"v_mov_b32 v248, 0\nv_mul_f32 v232, v34, v34\nM 20,172,20\nbuffer_load_dwordx4 a[96:99], v252, s[20:23], s64 offen sc1\nv_mov_b32 v249, 0\nv_mul_f32 v233, v35, v35\n"
"v_mfma_f32_16x16x32_bf16 a[228:231], v[168:171], a[8:11], 0\nR 164,191,32768\ns_add_u32 s61, s82, 845848\ns_mov_b64 exec, 0xf\nbuffer_atomic_or_x2 v[248:249], v250, s[20:23], s61 offen sc0 sc1\ns_mov_b64 exec, -1\n"
"v_fma_f32 v230, v230, v215, v214\nv_mfma_f32_16x16x32_bf16 a[228:231], v[172:175], a[24:27], a[228:231]\ns_add_u32 s65, s68, 37866496\nbuffer_load_dwordx4 v220, s[16:19], s63 offen lds\nv_fma_f32 v231, v231, v215, v214\nv_mfma_f32_16x16x32_bf16 a[244:247], v[168:171], a[12:15], 0\n"
"R 166,191,36864\nv_fma_f32 v232, v232, v215, v214\nv_fma_f32 v233, v233, v215, v214\nv_mfma_f32_16x16x32_bf16 a[244:247], v[172:175], a[28:31], a[244:247]\nG 4\nv_mul_f32 v230, v32, v230\n"
"v_mul_f32 v231, v33, v231\nv_mul_f32 v232, v34, v232\nv_mfma_f32_16x16x32_bf16 v[8:11], v[176:179], a[0:3], 0\nR 168,188,40960\nv_mul_f32 v233, v35, v233\nv_exp_f32 v230, v230\n"
"M 8,180,16\nbuffer_load_dwordx4 a[100:103], v252, s[20:23], s65 offen sc1\nbuffer_load_dwordx4 v221, s[16:19], s63 offen offset:1024 lds\nv_exp_f32 v231, v231\nv_mfma_f32_16x16x32_bf16 v[24:27], v[176:179], a[4:7], 0\nR 170,188,45056\n"
"s_add_u32 s67, s69, 37866496\nv_exp_f32 v232, v232\nM 24,180,20\ns_add_u32 s66, s69, 37751808\ns_add_u32 s63, s56, 393216\nv_fma_f32 v230, v230, v216, v216\n"
"v_exp_f32 v233, v233\nv_mfma_f32_16x16x32_bf16 a[232:235], v[176:179], a[8:11], 0\nR 172,188,49152\ns_add_u32 m0, s50, 32768\nv_fma_f32 v231, v231, v216, v216\nv_mfma_f32_16x16x32_bf16 a[232:235], v[180:183], a[24:27], a[232:235]\n"
"buffer_load_dwordx4 a[104:107], v252, s[20:23], s66 offen sc1\nbuffer_load_dwordx4 v220, s[16:19], s63 offen lds\nv_fma_f32 v232, v232, v216, v216\nv_mfma_f32_16x16x32_bf16 a[248:251], v[176:179], a[12:15], 0\nR 174,188,53248\nv_fma_f32 v233, v233, v216, v216\n"
"v_rcp_f32 v230, v230\nv_mfma_f32_16x16x32_bf16 a[248:251], v[180:183], a[28:31], a[248:251]\nG 4\nv_rcp_f32 v231, v231\nv_rcp_f32 v232, v232\nv_mfma_f32_16x16x32_bf16 v[12:15], v[160:163], a[0:3], 0\n"
"R 176,189,40960\nv_mul_f32 v230, v32, v230\nv_rcp_f32 v233, v233\nM 12,164,16\nbuffer_load_dwordx4 a[108:111], v252, s[20:23], s67 offen sc1\nbuffer_load_dwordx4 v221, s[16:19], s63 offen offset:1024 lds\n"
"v_mul_f32 v231, v33, v231\nv_mfma_f32_16x16x32_bf16 v[28:31], v[160:163], a[4:7], 0\nR 178,189,45056\ns_add_u32 m0, s50, 40960\nv_mul_f32 v232, v34, v232\nM 28,164,20\n"
"s_add_u32 s64, s68, 37758976\nv_mul_f32 v233, v35, v233\nv_cvt_pk_bf16_f32 v32, v230, v231\nv_cvt_pk_bf16_f32 v33, v232, v233\nv_mfma_f32_16x16x32_bf16 a[236:239], v[160:163], a[8:11], 0\nR 180,189,49152\n"
"s_add_u32 s63, s56, 458752\nv_mul_f32 v234, v36, v36\nv_mfma_f32_16x16x32_bf16 a[236:239], v[164:167], a[24:27], a[236:239]\ns_add_u32 s65, s68, 37873664\nbuffer_load_dwordx4 v220, s[16:19], s63 offen lds\nv_mul_f32 v235, v37, v37\n"
"v_mfma_f32_16x16x32_bf16 a[252:255], v[160:163], a[12:15], 0\nR 182,189,53248\nv_mul_f32 v236, v38, v38\nv_mul_f32 v237, v39, v39\nv_mfma_f32_16x16x32_bf16 a[252:255], v[164:167], a[28:31], a[252:255]\ns_waitcnt vmcnt(16)\n"
"s_barrier\nG 4\nv_fma_f32 v234, v234, v215, v214\nM 0,168,32\nR 160,190,40960\nv_fma_f32 v235, v235, v215, v214\n"
"v_fma_f32 v236, v236, v215, v214\nM 0,172,48\nbuffer_load_dwordx4 a[112:115], v252, s[20:23], s64 offen sc1\nbuffer_load_dwordx4 v221, s[16:19], s63 offen offset:1024 lds\nv_fma_f32 v237, v237, v215, v214\nM 16,168,36\n"
"R 162,190,45056\nv_mul_f32 v234, v36, v234\nv_mul_f32 v235, v37, v235\nM 16,172,52\nbuffer_load_dwordx4 a[116:119], v252, s[20:23], s65 offen sc1\nv_mul_f32 v236, v38, v236\n"
"v_mfma_f32_16x16x32_bf16 a[224:227], v[168:171], a[40:43], a[224:227]\nR 164,190,49152\nv_mul_f32 v237, v39, v237\nv_exp_f32 v234, v234\nv_mfma_f32_16x16x32_bf16 a[224:227], v[172:175], a[56:59], a[224:227]\ns_add_u32 s66, s69, 37758976\n"
"v_exp_f32 v235, v235\nv_exp_f32 v236, v236\nv_mfma_f32_16x16x32_bf16 a[240:243], v[168:171], a[44:47], a[240:243]\nR 166,190,53248\nv_fma_f32 v234, v234, v216, v216\nv_exp_f32 v237, v237\n"
"v_mfma_f32_16x16x32_bf16 a[240:243], v[172:175], a[60:63], a[240:243]\nG 4\nv_rcp_f32 v234, v234\nv_fma_f32 v235, v235, v216, v216\nM 4,176,32\nR 168,191,40960\n"
"v_fma_f32 v236, v236, v216, v216\nv_fma_f32 v237, v237, v216, v216\nM 4,180,48\nbuffer_load_dwordx4 a[120:123], v252, s[20:23], s66 offen sc1\nv_rcp_f32 v235, v235\nM 20,176,36\n"
"R 170,191,45056\nv_mul_f32 v234, v36, v234\nv_rcp_f32 v236, v236\nM 20,180,52\ns_add_u32 s67, s69, 37873664\nv_rcp_f32 v237, v237\n"
"v_mul_f32 v235, v37, v235\nv_mfma_f32_16x16x32_bf16 a[228:231], v[176:179], a[40:43], a[228:231]\nR 172,191,49152\nv_mul_f32 v236, v38, v236\nv_mul_f32 v237, v39, v237\nv_mfma_f32_16x16x32_bf16 a[228:231], v[180:183], a[56:59], a[228:231]\n"
"buffer_load_dwordx4 a[124:127], v252, s[20:23], s67 offen sc1\nv_cvt_pk_bf16_f32 v34, v234, v235\nv_mfma_f32_16x16x32_bf16 a[244:247], v[176:179], a[44:47], a[244:247]\nR 174,191,53248\ns_waitcnt vmcnt(13)\nv_cmp_eq_u64 s[86:87], s[46:47], v[248:249]\n"
"v_cvt_pk_bf16_f32 v35, v236, v237\nv_mfma_f32_16x16x32_bf16 a[244:247], v[180:183], a[60:63], a[244:247]\ns_add_u32 s63, s57, 131072\nG 4\nv_mul_f32 v230, v40, v40\nv_mul_f32 v231, v41, v41\n"
"M 8,160,32\nR 176,184,0\ns_and_b32 s62, s86, 0xf\ns_cmp_eq_u32 s62, 0xf\ns_cbranch_scc0 Lq937\nLq938:\n"
"v_mul_f32 v232, v42, v42\nM 8,164,48\ns_add_u32 s64, s68, 37754880\nv_mul_f32 v233, v43, v43\nv_fma_f32 v230, v230, v215, v214\nv_fma_f32 v231, v231, v215, v214\n"
"M 24,160,36\nR 178,184,4096\ns_add_u32 m0, s50, 49152\nv_fma_f32 v232, v232, v215, v214\nM 24,164,52\nbuffer_load_dwordx4 a[128:131], v252, s[20:23], s64 offen sc1\n"
"v_fma_f32 v233, v233, v215, v214\nv_mfma_f32_16x16x32_bf16 a[232:235], v[160:163], a[40:43], a[232:235]\nR 180,184,8192\ns_add_u32 s66, s69, 37754880\nbuffer_load_dwordx4 v220, s[16:19], s63 offen lds\nv_mul_f32 v230, v40, v230\n"
"v_mfma_f32_16x16x32_bf16 a[232:235], v[164:167], a[56:59], a[232:235]\ns_add_u32 s65, s68, 37869568\nv_exp_f32 v230, v230\nv_mul_f32 v231, v41, v231\nv_mfma_f32_16x16x32_bf16 a[248:251], v[160:163], a[44:47], a[248:251]\nR 182,184,12288\n"
"v_mul_f32 v232, v42, v232\nv_mul_f32 v233, v43, v233\nv_mfma_f32_16x16x32_bf16 a[248:251], v[164:167], a[60:63], a[248:251]\nG 4\nv_exp_f32 v231, v231\nv_exp_f32 v232, v232\n"
"M 12,168,32\nR 160,185,0\nbuffer_load_dwordx4 v221, s[16:19], s63 offen offset:1024 lds\nv_exp_f32 v233, v233\nM 12,172,48\nbuffer_load_dwordx4 a[132:135], v252, s[20:23], s65 offen sc1\n"
"s_add_u32 s63, s57, 655360\nv_fma_f32 v230, v230, v216, v216\nM 28,168,36\nR 162,185,4096\ns_add_u32 m0, s50, 57344\nv_fma_f32 v231, v231, v216, v216\n"
"M 28,172,52\nbuffer_load_dwordx4 a[136:139], v252, s[20:23], s66 offen sc1\nv_fma_f32 v232, v232, v216, v216\nv_mfma_f32_16x16x32_bf16 a[236:239], v[168:171], a[40:43], a[236:239]\nR 164,185,8192\ns_add_u32 s64, s68, 37762048\n"
"buffer_load_dwordx4 v220, s[16:19], s63 offen lds\nv_fma_f32 v233, v233, v216, v216\nv_mfma_f32_16x16x32_bf16 a[236:239], v[172:175], a[56:59], a[236:239]\ns_add_u32 s67, s69, 37869568\nv_rcp_f32 v230, v230\nv_rcp_f32 v231, v231\n"
"v_mfma_f32_16x16x32_bf16 a[252:255], v[168:171], a[44:47], a[252:255]\nR 166,185,12288\nv_mul_f32 v230, v40, v230\nv_rcp_f32 v232, v232\nv_mfma_f32_16x16x32_bf16 a[252:255], v[172:175], a[60:63], a[252:255]\nG 4\n"
"v_rcp_f32 v233, v233\nv_mul_f32 v231, v41, v231\nM 96,176,64\nR 168,186,0\nbuffer_load_dwordx4 v221, s[16:19], s63 offen offset:1024 lds\nv_mul_f32 v232, v42, v232\n"
"M 96,180,80\nbuffer_load_dwordx4 a[140:143], v252, s[20:23], s67 offen sc1\ns_add_u32 s63, s57, 196608\nv_mul_f32 v233, v43, v233\nM 112,176,68\nR 170,186,4096\n"
"s_add_u32 m0, s50, 65536\nv_cvt_pk_bf16_f32 v40, v230, v231\nM 112,180,84\nbuffer_load_dwordx4 a[144:147], v252, s[20:23], s64 offen sc1\nv_cvt_pk_bf16_f32 v41, v232, v233\nM 128,176,72\n"
"R 172,186,8192\ns_add_u32 s66, s69, 37762048\nbuffer_load_dwordx4 v220, s[16:19], s63 offen lds\nv_mul_f32 v234, v44, v44\nM 128,180,88\ns_add_u32 s65, s68, 37876736\n"
"v_mul_f32 v235, v45, v45\nv_mul_f32 v236, v46, v46\nv_mul_f32 v237, v47, v47\nM 144,176,76\nR 174,186,12288\nv_fma_f32 v234, v234, v215, v214\n"
"v_fma_f32 v235, v235, v215, v214\nM 144,180,92\nG 4\nv_fma_f32 v236, v236, v215, v214\nv_fma_f32 v237, v237, v215, v214\nv_mul_f32 v234, v44, v234\n"
"M 100,160,64\nR 176,187,0\nbuffer_load_dwordx4 v221, s[16:19], s63 offen offset:1024 lds\nv_mul_f32 v235, v45, v235\nM 100,164,80\nbuffer_load_dwordx4 a[148:151], v252, s[20:23], s65 offen sc1\n"
"s_add_u32 s63, s57, 720896\nv_mul_f32 v236, v46, v236\nM 116,160,68\nR 178,187,4096\ns_add_u32 m0, s50, 73728\nv_mul_f32 v237, v47, v237\n"
"M 116,164,84\nbuffer_load_dwordx4 a[152:155], v252, s[20:23], s66 offen sc1\nv_exp_f32 v234, v234\nM 132,160,72\nR 180,187,8192\nbuffer_load_dwordx4 v220, s[16:19], s63 offen lds\n"
"v_exp_f32 v235, v235\nM 132,164,88\ns_add_u32 s67, s69, 37876736\nv_exp_f32 v236, v236\nv_exp_f32 v237, v237\nM 148,160,76\n"
"R 182,187,12288\nv_fma_f32 v234, v234, v216, v216\nv_fma_f32 v235, v235, v216, v216\nM 148,164,92\nG 4\nv_rcp_f32 v234, v234\n"
"v_fma_f32 v236, v236, v216, v216\nM 104,168,64\nR 160,184,16384\ns_waitcnt vmcnt(17)\ns_barrier\nv_fma_f32 v237, v237, v216, v216\n"
"M 104,172,80\nbuffer_load_dwordx4 a[156:159], v252, s[20:23], s67 offen sc1\nbuffer_load_dwordx4 v221, s[16:19], s63 offen offset:1024 lds\nv_rcp_f32 v235, v235\nM 120,168,68\nR 162,184,20480\n"
"v_mul_f32 v234, v44, v234\nv_rcp_f32 v236, v236\nM 120,172,84\nbuffer_store_dwordx4 v[32:35], v252, s[20:23], s84 offen offset:0 sc1\nv_rcp_f32 v237, v237\nM 136,168,72\n"
"R 164,184,24576\nv_mul_f32 v235, v45, v235\nv_mul_f32 v236, v46, v236\nM 136,172,88\nv_mul_f32 v237, v47, v237\nv_cvt_pk_bf16_f32 v42, v234, v235\n"
"v_cvt_pk_bf16_f32 v43, v236, v237\nM 152,168,76\nR 166,184,28672\nv_mul_f32 v230, v48, v48\nv_mul_f32 v231, v49, v49\nM 152,172,92\n"
"G 4\nv_mul_f32 v232, v50, v50\nv_mul_f32 v233, v51, v51\nv_fma_f32 v230, v230, v218, v217\nM 108,176,64\nR 168,185,16384\n"
"v_fma_f32 v231, v231, v218, v217\nv_fma_f32 v232, v232, v218, v217\nM 108,180,80\ns_add_u32 m0, s50, 81920\nv_fma_f32 v233, v233, v218, v217\nv_mul_f32 v230, v48, v230\n"
"v_mul_f32 v231, v49, v231\nM 124,176,68\nR 170,185,20480\nv_mul_f32 v232, v50, v232\nv_mul_f32 v233, v51, v233\nM 124,180,84\n"
"s_add_u32 s63, s57, 393216\nv_exp_f32 v230, v230\nv_exp_f32 v231, v231\nM 140,176,72\nR 172,185,24576\nv_fma_f32 v230, v230, v219, v219\n"
"v_exp_f32 v232, v232\nM 140,180,88\nbuffer_load_dwordx4 v220, s[16:19], s63 offen lds\nv_exp_f32 v233, v233\nM 156,176,76\nR 174,185,28672\n"
"v_fma_f32 v231, v231, v219, v219\nv_fma_f32 v232, v232, v219, v219\nM 156,180,92\nG 4\nv_rcp_f32 v230, v230\nv_fma_f32 v233, v233, v219, v219\n"
"M 96,160,96\nR 176,186,16384\nv_mul_f32 v230, v48, v230\nv_rcp_f32 v231, v231\nM 96,164,112\nbuffer_load_dwordx4 v221, s[16:19], s63 offen offset:1024 lds\n"
"v_rcp_f32 v232, v232\nM 112,160,100\nR 178,186,20480\ns_add_u32 s63, s57, 458752\nv_rcp_f32 v233, v233\ns_waitcnt vmcnt(21)\n"
"M 112,164,116\ns_add_u32 m0, s50, 90112\nv_mul_f32 v231, v49, v231\nv_mul_f32 v232, v50, v232\nv_mul_f32 v233, v51, v233\nM 128,160,104\n"
"R 180,186,24576\nv_cvt_pk_bf16_f32 v48, v230, v231\nv_cvt_pk_bf16_f32 v49, v232, v233\ns_waitcnt vmcnt(20)\nM 128,164,120\nbuffer_load_dwordx4 v220, s[16:19], s63 offen lds\n"
"v_mul_f32 v234, v52, v52\nM 144,160,108\nR 182,186,28672\nv_mul_f32 v235, v53, v53\nv_mul_f32 v236, v54, v54\ns_waitcnt vmcnt(20)\n"
"M 144,164,124\nG 4\nv_mul_f32 v237, v55, v55\nv_fma_f32 v234, v234, v218, v217\nv_fma_f32 v235, v235, v218, v217\nM 100,168,96\n"
"R 160,187,16384\nv_fma_f32 v236, v236, v218, v217\nv_fma_f32 v237, v237, v218, v217\nM 100,172,112\nbuffer_store_dwordx4 v[40:43], v252, s[20:23], s84 offen offset:1024 sc1\nv_mul_f32 v234, v52, v234\n"
"M 116,168,100\nR 162,187,20480\nv_mul_f32 v235, v53, v235\nv_mul_f32 v236, v54, v236\nM 116,172,116\nbuffer_load_dwordx4 v221, s[16:19], s63 offen offset:1024 lds\n"
"v_mul_f32 v237, v55, v237\nM 132,168,104\nR 164,187,24576\ns_add_u32 s63, s58, 0\nv_exp_f32 v234, v234\nM 132,172,120\n"
"s_add_u32 m0, s50, 98304\nv_exp_f32 v235, v235\nv_exp_f32 v236, v236\nM 148,168,108\nR 166,187,28672\nv_fma_f32 v234, v234, v219, v219\n"
"v_exp_f32 v237, v237\nM 148,172,124\nG 4\nv_rcp_f32 v234, v234\nv_fma_f32 v235, v235, v219, v219\nM 104,176,96\n"
"R 168,184,32768\nv_fma_f32 v236, v236, v219, v219\nv_fma_f32 v237, v237, v219, v219\nM 104,180,112\nbuffer_load_dwordx4 v220, s[16:19], s63 offen lds\nv_rcp_f32 v235, v235\n"
"M 120,176,100\nR 170,184,36864\nv_mul_f32 v234, v52, v234\nv_rcp_f32 v236, v236\nM 120,180,116\nv_rcp_f32 v237, v237\n"
"v_mul_f32 v235, v53, v235\nM 136,176,104\nR 172,184,40960\nv_mul_f32 v236, v54, v236\nv_mul_f32 v237, v55, v237\nM 136,180,120\n"
"buffer_load_dwordx4 v221, s[16:19], s63 offen offset:1024 lds\nv_cvt_pk_bf16_f32 v50, v234, v235\nM 152,176,108\nR 174,184,45056\ns_add_u32 m0, s50, 106496\nv_cvt_pk_bf16_f32 v51, v236, v237\n"
"M 152,180,124\nG 4\nv_mul_f32 v230, v56, v56\nv_mul_f32 v231, v57, v57\nv_mul_f32 v232, v58, v58\nM 108,160,96\n"
"R 176,185,32768\ns_add_u32 s63, s58, 524288\nv_mul_f32 v233, v59, v59\nM 108,164,112\nbuffer_load_dwordx4 v220, s[16:19], s63 offen lds\nv_fma_f32 v230, v230, v218, v217\n"
"M 124,160,100\nR 178,185,36864\nv_fma_f32 v231, v231, v218, v217\nM 124,164,116\nbuffer_store_dwordx4 v[48:51], v252, s[20:23], s85 offen offset:0 sc1\nM 140,160,104\n"
"R 180,185,40960\nv_fma_f32 v232, v232, v218, v217\nM 140,164,120\nbuffer_load_dwordx4 v221, s[16:19], s63 offen offset:1024 lds\nM 156,160,108\nR 182,185,45056\n"
"s_add_u32 s63, s58, 65536\nM 156,164,124\ns_waitcnt vmcnt(11)\ns_barrier\nG 4\nM 96,168,128\n"
"R 160,186,32768\nv_fma_f32 v233, v233, v218, v217\nM 96,172,144\ns_add_u32 m0, s50, 114688\nv_mul_f32 v230, v56, v230\nv_mul_f32 v231, v57, v231\n"
"M 112,168,132\nR 162,186,36864\nv_mul_f32 v232, v58, v232\nM 112,172,148\nbuffer_load_dwordx4 v220, s[16:19], s63 offen lds\nM 128,168,136\n"
"R 164,186,40960\nv_mul_f32 v233, v59, v233\nM 128,172,152\nv_exp_f32 v230, v230\nM 144,168,140\nR 166,186,45056\n"
"v_fma_f32 v230, v230, v219, v219\nM 144,172,156\nG 4\nv_exp_f32 v231, v231\nM 100,176,128\nR 168,187,32768\n"
"v_fma_f32 v231, v231, v219, v219\nM 100,180,144\nbuffer_load_dwordx4 v221, s[16:19], s63 offen offset:1024 lds\nM 116,176,132\nR 170,187,36864\nv_mul_f32 v234, v60, v60\n"
"M 116,180,148\ns_add_u32 m0, s50, 0\nv_exp_f32 v232, v232\nM 132,176,136\nR 172,187,40960\nv_fma_f32 v232, v232, v219, v219\n"
"M 132,180,152\ns_add_u32 s63, s58, 589824\nv_exp_f32 v233, v233\nM 148,176,140\nR 174,187,45056\nv_fma_f32 v233, v233, v219, v219\n"
"M 148,180,156\nG 4\nv_rcp_f32 v230, v230\nM 104,160,128\nR 176,184,49152\nv_mul_f32 v230, v56, v230\n"
"M 104,164,144\nbuffer_load_dwordx4 v220, s[16:19], s63 offen lds\nM 120,160,132\nR 178,184,53248\nv_mul_f32 v235, v61, v61\nM 120,164,148\n"
"v_rcp_f32 v231, v231\nM 136,160,136\nR 180,184,57344\nv_mul_f32 v231, v57, v231\nM 136,164,152\nv_rcp_f32 v232, v232\n"
"M 152,160,140\nR 182,184,61440\nv_mul_f32 v232, v58, v232\nM 152,164,156\nG 4\nv_rcp_f32 v233, v233\n"
"M 108,168,128\nR 160,185,49152\nv_mul_f32 v233, v59, v233\nM 108,172,144\nbuffer_load_dwordx4 v221, s[16:19], s63 offen offset:1024 lds\nM 124,168,132\n"
"R 162,185,53248\nv_cvt_pk_bf16_f32 v56, v230, v231\nM 124,172,148\ns_add_u32 m0, s50, 8192\nv_cvt_pk_bf16_f32 v57, v232, v233\nv_mul_f32 v236, v62, v62\n"
"M 140,168,136\nR 164,185,57344\nv_mul_f32 v237, v63, v63\nM 140,172,152\ns_add_u32 s63, s58, 131072\nv_fma_f32 v234, v234, v218, v217\n"
"v_fma_f32 v235, v235, v218, v217\nM 156,168,140\nR 166,185,61440\nv_fma_f32 v236, v236, v218, v217\nM 156,172,156\nG 4\n"
"v_fma_f32 v237, v237, v218, v217\nv_mul_f32 v234, v60, v234\nM 0,176,64\nR 168,186,49152\nv_mul_f32 v235, v61, v235\nM 0,180,80\n"
"buffer_load_dwordx4 v220, s[16:19], s63 offen lds\nM 16,176,68\nR 170,186,53248\nv_mul_f32 v236, v62, v236\nM 16,180,84\nv_exp_f32 v234, v234\n"
"v_mfma_f32_16x16x32_bf16 a[224:227], v[176:179], a[72:75], a[224:227]\nR 172,186,57344\nv_mul_f32 v237, v63, v237\nv_mfma_f32_16x16x32_bf16 a[224:227], v[180:183], a[88:91], a[224:227]\nv_exp_f32 v235, v235\nv_mfma_f32_16x16x32_bf16 a[240:243], v[176:179], a[76:79], a[240:243]\n"
"R 174,186,61440\nv_fma_f32 v234, v234, v219, v219\nv_mfma_f32_16x16x32_bf16 a[240:243], v[180:183], a[92:95], a[240:243]\nG 4\nv_exp_f32 v236, v236\nM 4,160,64\n"
"R 176,187,49152\nv_fma_f32 v235, v235, v219, v219\nM 4,164,80\nbuffer_load_dwordx4 v221, s[16:19], s63 offen offset:1024 lds\nM 20,160,68\nR 178,187,53248\n"
"v_fma_f32 v236, v236, v219, v219\nM 20,164,84\ns_add_u32 m0, s50, 16384\nv_exp_f32 v237, v237\nv_mfma_f32_16x16x32_bf16 a[228:231], v[160:163], a[72:75], a[228:231]\nR 180,187,57344\n"
"v_fma_f32 v237, v237, v219, v219\nv_mfma_f32_16x16x32_bf16 a[228:231], v[164:167], a[88:91], a[228:231]\ns_add_u32 s63, s58, 655360\nv_rcp_f32 v234, v234\nv_mfma_f32_16x16x32_bf16 a[244:247], v[160:163], a[76:79], a[244:247]\nR 182,187,61440\n"
"v_mul_f32 v234, v60, v234\nv_mfma_f32_16x16x32_bf16 a[244:247], v[164:167], a[92:95], a[244:247]\nG 4\nv_rcp_f32 v235, v235\nM 8,168,64\nR 160,188,0\n"
"v_mul_f32 v235, v61, v235\nM 8,172,80\nbuffer_load_dwordx4 v220, s[16:19], s63 offen lds\nM 24,168,68\nR 162,188,4096\nv_cvt_pk_bf16_f32 v58, v234, v235\n"
"M 24,172,84\nv_rcp_f32 v236, v236\nv_mfma_f32_16x16x32_bf16 a[232:235], v[168:171], a[72:75], a[232:235]\nR 164,188,8192\nv_mul_f32 v236, v62, v236\nv_mfma_f32_16x16x32_bf16 a[232:235], v[172:175], a[88:91], a[232:235]\n"
"buffer_load_dwordx4 v221, s[16:19], s63 offen offset:1024 lds\nv_mfma_f32_16x16x32_bf16 a[248:251], v[168:171], a[76:79], a[248:251]\nR 166,188,12288\ns_add_u32 m0, s50, 24576\nv_mfma_f32_16x16x32_bf16 a[248:251], v[172:175], a[92:95], a[248:251]\nG 4\n"
"v_rcp_f32 v237, v237\nM 12,176,64\nR 168,189,0\ns_add_u32 s63, s58, 196608\nM 12,180,80\nbuffer_load_dwordx4 v220, s[16:19], s63 offen lds\n"
"M 28,176,68\nR 170,189,4096\nv_mul_f32 v237, v63, v237\nM 28,180,84\nv_cvt_pk_bf16_f32 v59, v236, v237\nbuffer_store_dwordx4 v[56:59], v252, s[20:23], s85 offen offset:1024 sc1\n"
"v_mfma_f32_16x16x32_bf16 a[236:239], v[176:179], a[72:75], a[236:239]\nR 172,189,8192\nv_mfma_f32_16x16x32_bf16 a[236:239], v[180:183], a[88:91], a[236:239]\nbuffer_load_dwordx4 v221, s[16:19], s63 offen offset:1024 lds\nv_mfma_f32_16x16x32_bf16 a[252:255], v[176:179], a[76:79], a[252:255]\nR 174,189,12288\n"
"v_mfma_f32_16x16x32_bf16 a[252:255], v[180:183], a[92:95], a[252:255]\ns_waitcnt vmcnt(11)\ns_barrier\nG 4\nM 0,160,96\nR 176,190,0\n"
"M 0,164,112\nM 16,160,100\nR 178,190,4096\nM 16,164,116\nv_mfma_f32_16x16x32_bf16 a[224:227], v[160:163], a[104:107], a[224:227]\nR 180,190,8192\n"
"v_mfma_f32_16x16x32_bf16 a[224:227], v[164:167], a[120:123], a[224:227]\nv_mfma_f32_16x16x32_bf16 a[240:243], v[160:163], a[108:111], a[240:243]\nR 182,190,12288\nv_mfma_f32_16x16x32_bf16 a[240:243], v[164:167], a[124:127], a[240:243]\nG 4\nM 4,168,96\n"
"R 160,191,0\nM 4,172,112\nM 20,168,100\nR 162,191,4096\nM 20,172,116\nv_mfma_f32_16x16x32_bf16 a[228:231], v[168:171], a[104:107], a[228:231]\n"
"R 164,191,8192\nv_mfma_f32_16x16x32_bf16 a[228:231], v[172:175], a[120:123], a[228:231]\nv_mfma_f32_16x16x32_bf16 a[244:247], v[168:171], a[108:111], a[244:247]\nR 166,191,12288\nv_mfma_f32_16x16x32_bf16 a[244:247], v[172:175], a[124:127], a[244:247]\nG 4\n"
"M 8,176,96\nR 168,188,16384\nM 8,180,112\nM 24,176,100\nR 170,188,20480\nM 24,180,116\n"
"v_mfma_f32_16x16x32_bf16 a[232:235], v[176:179], a[104:107], a[232:235]\nR 172,188,24576\nv_mfma_f32_16x16x32_bf16 a[232:235], v[180:183], a[120:123], a[232:235]\nv_mfma_f32_16x16x32_bf16 a[248:251], v[176:179], a[108:111], a[248:251]\nR 174,188,28672\nv_mfma_f32_16x16x32_bf16 a[248:251], v[180:183], a[124:127], a[248:251]\n"
"G 4\nM 12,160,96\nR 176,189,16384\nM 12,164,112\nM 28,160,100\nR 178,189,20480\n"
"M 28,164,116\nv_mfma_f32_16x16x32_bf16 a[236:239], v[160:163], a[104:107], a[236:239]\nR 180,189,24576\nv_mfma_f32_16x16x32_bf16 a[236:239], v[164:167], a[120:123], a[236:239]\nv_mfma_f32_16x16x32_bf16 a[252:255], v[160:163], a[108:111], a[252:255]\nR 182,189,28672\n"
"v_mfma_f32_16x16x32_bf16 a[252:255], v[164:167], a[124:127], a[252:255]\nG 4\nM 0,168,128\nR 160,190,16384\nM 0,172,144\nM 16,168,132\n"
"R 162,190,20480\nM 16,172,148\nv_mfma_f32_16x16x32_bf16 a[224:227], v[168:171], a[136:139], a[224:227]\nR 164,190,24576\nv_mfma_f32_16x16x32_bf16 a[224:227], v[172:175], a[152:155], a[224:227]\nv_mfma_f32_16x16x32_bf16 a[240:243], v[168:171], a[140:143], a[240:243]\n"
"R 166,190,28672\nv_mfma_f32_16x16x32_bf16 a[240:243], v[172:175], a[156:159], a[240:243]\nG 4\nM 4,176,128\nR 168,191,16384\nM 4,180,144\n"
"M 20,176,132\nR 170,191,20480\nv_mov_b32 v226, s46\nM 20,180,148\ns_add_u32 m0, s50, 32768\nv_mov_b32 v227, s47\n"
"v_mfma_f32_16x16x32_bf16 a[228:231], v[176:179], a[136:139], a[228:231]\nR 172,191,24576\ns_add_u32 s61, s81, 845840\ns_lshl_b32 s62, s44, 5\ns_add_u32 s61, s61, s62\ns_waitcnt vmcnt(0)\n"
"s_mov_b64 exec, 1\nbuffer_atomic_swap_x2 v[226:227], off, s[20:23], s61 sc1\ns_mov_b64 exec, -1\nv_mfma_f32_16x16x32_bf16 a[228:231], v[180:183], a[152:155], a[228:231]\ns_add_u32 s63, s58, 720896\nv_mfma_f32_16x16x32_bf16 a[244:247], v[176:179], a[140:143], a[244:247]\n"
"R 174,191,28672\nv_mfma_f32_16x16x32_bf16 a[244:247], v[180:183], a[156:159], a[244:247]\nG 4\nM 8,160,128\nR 176,188,32768\nM 8,164,144\n"
"buffer_load_dwordx4 v220, s[16:19], s63 offen lds\nM 24,160,132\nR 178,188,36864\nM 24,164,148\nv_mfma_f32_16x16x32_bf16 a[232:235], v[160:163], a[136:139], a[232:235]\nR 180,188,40960\n"
"v_mfma_f32_16x16x32_bf16 a[232:235], v[164:167], a[152:155], a[232:235]\nbuffer_load_dwordx4 v221, s[16:19], s63 offen offset:1024 lds\nv_mfma_f32_16x16x32_bf16 a[248:251], v[160:163], a[140:143], a[248:251]\nR 182,188,45056\ns_add_u32 m0, s50, 40960\nv_mfma_f32_16x16x32_bf16 a[248:251], v[164:167], a[156:159], a[248:251]\n"
"G 4\nM 12,168,128\nR 160,189,32768\ns_add_u32 s63, s58, 393216\nM 12,172,144\nbuffer_load_dwordx4 v220, s[16:19], s63 offen lds\n"
"M 28,168,132\nR 162,189,36864\nM 28,172,148\nv_mfma_f32_16x16x32_bf16 a[236:239], v[168:171], a[136:139], a[236:239]\nR 164,189,40960\nv_mfma_f32_16x16x32_bf16 a[236:239], v[172:175], a[152:155], a[236:239]\n"
"buffer_load_dwordx4 v221, s[16:19], s63 offen offset:1024 lds\nv_mfma_f32_16x16x32_bf16 a[252:255], v[168:171], a[140:143], a[252:255]\nR 166,189,45056\ns_add_u32 s63, s58, 458752\nv_mfma_f32_16x16x32_bf16 a[252:255], v[172:175], a[156:159], a[252:255]\ns_barrier\n"
"G 4\nv_mfma_f32_16x16x32_bf16 v[32:35], v[176:179], a[0:3], 0\nR 168,190,32768\nM 32,180,16\ns_add_u32 m0, s50, 49152\nv_mfma_f32_16x16x32_bf16 v[48:51], v[176:179], a[4:7], 0\n"
"R 170,190,36864\nM 48,180,20\nbuffer_load_dwordx4 v220, s[16:19], s63 offen lds\nv_mfma_f32_16x16x32_bf16 v[64:67], v[176:179], a[8:11], 0\nR 172,190,40960\nM 64,180,24\n"
"v_mfma_f32_16x16x32_bf16 v[80:83], v[176:179], a[12:15], 0\nR 174,190,45056\nM 80,180,28\nG 4\nv_mfma_f32_16x16x32_bf16 v[36:39], v[160:163], a[0:3], 0\nR 176,191,32768\n"
"M 36,164,16\nbuffer_load_dwordx4 v221, s[16:19], s63 offen offset:1024 lds\nv_mfma_f32_16x16x32_bf16 v[52:55], v[160:163], a[4:7], 0\nR 178,191,36864\nM 52,164,20\nv_mfma_f32_16x16x32_bf16 v[68:71], v[160:163], a[8:11], 0\n"
"R 180,191,40960\nM 68,164,24\ns_add_u32 m0, s50, 57344\nv_mfma_f32_16x16x32_bf16 v[84:87], v[160:163], a[12:15], 0\nR 182,191,45056\nv_mov_b32 v248, 0\n"
"M 84,164,28\nG 4\nv_mov_b32 v249, 0\nv_mfma_f32_16x16x32_bf16 v[40:43], v[168:171], a[0:3], 0\nR 160,188,49152\ns_add_u32 s61, s82, 845840\n"
"s_mov_b64 exec, 0xf\nbuffer_atomic_or_x2 v[248:249], v250, s[20:23], s61 offen sc0 sc1\ns_mov_b64 exec, -1\nM 40,172,16\ns_add_u32 s63, s56, 262144\nv_mfma_f32_16x16x32_bf16 v[56:59], v[168:171], a[4:7], 0\n"
"R 162,188,53248\nM 56,172,20\nbuffer_load_dwordx4 v220, s[16:19], s63 offen lds\nv_mfma_f32_16x16x32_bf16 v[72:75], v[168:171], a[8:11], 0\nR 164,184,0\nM 72,172,24\n"
"v_mfma_f32_16x16x32_bf16 v[88:91], v[168:171], a[12:15], 0\nR 166,184,4096\nM 88,172,28\nG 4\nv_mfma_f32_16x16x32_bf16 v[44:47], v[176:179], a[0:3], 0\nR 168,189,49152\n"
"M 44,180,16\nbuffer_load_dwordx4 v221, s[16:19], s63 offen offset:1024 lds\nv_mfma_f32_16x16x32_bf16 v[60:63], v[176:179], a[4:7], 0\nR 170,189,53248\ns_add_u32 s63, s56, 786432\nM 60,180,20\n"
"s_add_u32 m0, s50, 65536\nv_mfma_f32_16x16x32_bf16 v[76:79], v[176:179], a[8:11], 0\nR 172,185,0\nM 76,180,24\nbuffer_load_dwordx4 v220, s[16:19], s63 offen lds\nv_mfma_f32_16x16x32_bf16 v[92:95], v[176:179], a[12:15], 0\n"
"R 174,185,4096\nM 92,180,28\nG 4\nM 32,160,32\nR 176,190,49152\nM 32,164,48\n"
"buffer_load_dwordx4 v221, s[16:19], s63 offen offset:1024 lds\nM 48,160,36\nR 178,190,53248\ns_add_u32 s63, s56, 327680\nM 48,164,52\ns_add_u32 m0, s50, 73728\n"
"M 64,160,40\nR 180,186,0\nM 64,164,56\nbuffer_load_dwordx4 v220, s[16:19], s63 offen lds\nM 80,160,44\nR 182,186,4096\n"
"M 80,164,60\nG 4\nM 36,168,32\nR 160,191,49152\nM 36,172,48\nbuffer_load_dwordx4 v221, s[16:19], s63 offen offset:1024 lds\n"
"M 52,168,36\nR 162,191,53248\ns_add_u32 s63, s56, 851968\nM 52,172,52\ns_add_u32 m0, s50, 81920\nM 68,168,40\n"
"R 164,187,0\nM 68,172,56\nbuffer_load_dwordx4 v220, s[16:19], s63 offen lds\nM 84,168,44\nR 166,187,4096\nM 84,172,60\n"
"G 4\nM 40,176,32\nR 168,184,8192\nM 40,180,48\ns_add_u32 s64, s68, 37752832\nM 56,176,36\n"
"R 170,184,12288\ns_waitcnt vmcnt(7)\nv_cmp_eq_u64 s[86:87], s[46:47], v[248:249]\nM 56,180,52\nbuffer_load_dwordx4 v221, s[16:19], s63 offen offset:1024 lds\nM 72,176,40\n"
"R 172,184,16384\ns_and_b32 s62, s86, 0xf\ns_cmp_eq_u32 s62, 0xf\ns_cbranch_scc0 Lq956\nLq957:\nM 72,180,56\n"
"s_add_u32 s65, s68, 37867520\nM 88,176,44\nR 174,184,20480\ns_barrier\nM 88,180,60\nG 4\n"
"M 44,160,32\nR 176,185,8192\nM 44,164,48\nbuffer_load_dwordx4 a[160:163], v252, s[20:23], s64 offen sc1\nM 60,160,36\nR 178,185,12288\n"
"M 60,164,52\ns_add_u32 s66, s69, 37752832\nM 76,160,40\nR 180,185,16384\nM 76,164,56\nbuffer_load_dwordx4 a[164:167], v252, s[20:23], s65 offen sc1\n"
"M 92,160,44\nR 182,185,20480\nM 92,164,60\nG 4\nM 32,168,64\nR 160,186,8192\n"
"M 32,172,80\ns_add_u32 s67, s69, 37867520\nM 48,168,68\nR 162,186,12288\nM 48,172,84\nbuffer_load_dwordx4 a[168:171], v252, s[20:23], s66 offen sc1\n"
"M 64,168,72\nR 164,186,16384\nM 64,172,88\nM 80,168,76\nR 166,186,20480\nM 80,172,92\n"
"G 4\nM 36,176,64\nR 168,187,8192\nM 36,180,80\nbuffer_load_dwordx4 a[172:175], v252, s[20:23], s67 offen sc1\nM 52,176,68\n"
"R 170,187,12288\nM 52,180,84\ns_add_u32 s64, s68, 37760000\nM 68,176,72\nR 172,187,16384\nM 68,180,88\n"
"s_add_u32 s65, s68, 37874688\nM 84,176,76\nR 174,187,20480\ns_add_u32 m0, s50, 90112\nM 84,180,92\nG 4\n"
"M 40,160,64\nR 176,184,24576\ns_add_u32 s63, s57, 262144\nM 40,164,80\nbuffer_load_dwordx4 a[176:179], v252, s[20:23], s64 offen sc1\nM 56,160,68\n"
"R 178,184,28672\nM 56,164,84\ns_add_u32 s66, s69, 37760000\nM 72,160,72\nR 180,184,32768\nbuffer_load_dwordx4 v220, s[16:19], s63 offen lds\n"
"M 72,164,88\nbuffer_load_dwordx4 a[180:183], v252, s[20:23], s65 offen sc1\nM 88,160,76\nR 182,184,36864\nM 88,164,92\nG 4\n"
"M 44,168,64\nR 160,185,24576\nbuffer_load_dwordx4 v221, s[16:19], s63 offen offset:1024 lds\nM 44,172,80\ns_add_u32 s67, s69, 37874688\ns_add_u32 s63, s57, 786432\n"
"M 60,168,68\nR 162,185,28672\ns_add_u32 m0, s50, 98304\nM 60,172,84\nbuffer_load_dwordx4 a[184:187], v252, s[20:23], s66 offen sc1\nM 76,168,72\n"
"R 164,185,32768\nbuffer_load_dwordx4 v220, s[16:19], s63 offen lds\nM 76,172,88\nbuffer_load_dwordx4 v221, s[16:19], s63 offen offset:1024 lds\nM 92,168,76\nR 166,185,36864\n"
"s_add_u32 m0, s50, 106496\nM 92,172,92\nG 4\nM 32,176,96\nR 168,186,24576\ns_add_u32 s63, s57, 327680\n"
"M 32,180,112\nbuffer_load_dwordx4 a[188:191], v252, s[20:23], s67 offen sc1\nM 48,176,100\nR 170,186,28672\nM 48,180,116\ns_add_u32 s64, s68, 37753856\n"
"M 64,176,104\nR 172,186,32768\nbuffer_load_dwordx4 v220, s[16:19], s63 offen lds\nM 64,180,120\nbuffer_load_dwordx4 v221, s[16:19], s63 offen offset:1024 lds\nM 80,176,108\n"
"R 174,186,36864\ns_add_u32 m0, s50, 114688\nM 80,180,124\nG 4\nM 36,160,96\nR 176,187,24576\n"
"s_waitcnt vmcnt(18)\ns_barrier\nM 36,164,112\nbuffer_load_dwordx4 a[192:195], v252, s[20:23], s64 offen sc1\nM 52,160,100\nR 178,187,28672\n"
"s_add_u32 s63, s57, 851968\nM 52,164,116\ns_add_u32 s65, s68, 37868544\nM 68,160,104\nR 180,187,32768\nbuffer_load_dwordx4 v220, s[16:19], s63 offen lds\n"
"M 68,164,120\ns_add_u32 s66, s69, 37753856\nM 84,160,108\nR 182,187,36864\nM 84,164,124\nG 4\n"
"M 40,168,96\nR 160,184,40960\nbuffer_load_dwordx4 v221, s[16:19], s63 offen offset:1024 lds\nM 40,172,112\nbuffer_load_dwordx4 a[196:199], v252, s[20:23], s65 offen sc1\nM 56,168,100\n"
"R 162,184,45056\nM 56,172,116\ns_add_u32 s67, s69, 37868544\nM 72,168,104\nR 164,184,49152\nM 72,172,120\n"
"buffer_load_dwordx4 a[200:203], v252, s[20:23], s66 offen sc1\nM 88,168,108\nR 166,184,53248\nM 88,172,124\nG 4\nM 44,176,96\n"
"R 168,185,40960\nM 44,180,112\ns_add_u32 s64, s68, 37761024\nM 60,176,100\nR 170,185,45056\nM 60,180,116\n"
"buffer_load_dwordx4 a[204:207], v252, s[20:23], s67 offen sc1\nM 76,176,104\nR 172,185,49152\nM 76,180,120\nM 92,176,108\nR 174,185,53248\n"
"M 92,180,124\nG 4\nM 32,160,128\nR 176,186,40960\nM 32,164,144\nbuffer_load_dwordx4 a[208:211], v252, s[20:23], s64 offen sc1\n"
"M 48,160,132\nR 178,186,45056\nM 48,164,148\ns_add_u32 s65, s68, 37875712\nM 64,160,136\nR 180,186,49152\n"
"M 64,164,152\ns_add_u32 s66, s69, 37761024\nM 80,160,140\nR 182,186,53248\nM 80,164,156\nG 4\n"
"M 36,168,128\nR 160,187,40960\nM 36,172,144\nbuffer_load_dwordx4 a[212:215], v252, s[20:23], s65 offen sc1\nM 52,168,132\nR 162,187,45056\n"
"M 52,172,148\ns_add_u32 s67, s69, 37875712\nM 68,168,136\nR 164,187,49152\nM 68,172,152\nbuffer_load_dwordx4 a[216:219], v252, s[20:23], s66 offen sc1\n"
"M 84,168,140\nR 166,187,53248\nM 84,172,156\nG 4\nM 40,176,128\nR 168,184,57344\n"
"M 40,180,144\nM 56,176,132\nR 170,184,61440\nM 56,180,148\nbuffer_load_dwordx4 a[220:223], v252, s[20:23], s67 offen sc1\nM 72,176,136\n"
"R 172,188,0\ns_add_u32 s63, s58, 262144\nM 72,180,152\ns_add_u32 m0, s50, 0\nM 88,176,140\nR 174,188,4096\n"
"M 88,180,156\nG 4\nM 44,160,128\nR 176,185,57344\nM 44,164,144\nbuffer_load_dwordx4 v220, s[16:19], s63 offen lds\n"
"M 60,160,132\nR 178,185,61440\nM 60,164,148\nM 76,160,136\nR 180,189,0\nM 76,164,152\n"
"buffer_load_dwordx4 v221, s[16:19], s63 offen offset:1024 lds\nM 92,160,140\nR 182,189,4096\nM 92,164,156\ns_waitcnt vmcnt(9)\ns_barrier\n"
"G 4\nM 96,168,160\nR 160,186,57344\nM 96,172,176\ns_add_u32 m0, s50, 8192\nM 112,168,164\n"
"R 162,186,61440\nM 112,172,180\ns_add_u32 s63, s58, 786432\nM 128,168,168\nR 164,190,0\nv_lshrrev_b32 v230, 8, v252\n"
"M 128,172,184\nbuffer_load_dwordx4 v220, s[16:19], s63 offen lds\nM 144,168,172\nR 166,190,4096\nv_lshlrev_b32 v230, 3, v230\nM 144,172,188\n"
"G 4\nM 100,176,160\nR 168,187,57344\nM 100,180,176\nbuffer_load_dwordx2 v[232:233], v230, s[36:39], s53 offen offset:0\nM 116,176,164\n"
"R 170,187,61440\nM 116,180,180\nbuffer_load_dwordx2 v[234:235], v230, s[36:39], s53 offen offset:32\nM 132,176,168\nR 172,191,0\nM 132,180,184\n"
"buffer_load_dwordx4 v221, s[16:19], s63 offen offset:1024 lds\nM 148,176,172\nR 174,191,4096\nM 148,180,188\nG 4\nM 104,160,160\n"
"R 176,188,8192\nM 104,164,176\ns_add_u32 m0, s50, 16384\nM 120,160,164\nR 178,188,12288\nM 120,164,180\n"
"s_add_u32 s63, s58, 327680\nM 136,160,168\nR 180,188,16384\nbuffer_load_dwordx2 v[236:237], v230, s[36:39], s53 offen offset:64\nM 136,164,184\nbuffer_load_dwordx4 v220, s[16:19], s63 offen lds\n"
"M 152,160,172\nR 182,188,20480\nM 152,164,188\nbuffer_load_dwordx2 v[238:239], v230, s[36:39], s53 offen offset:96\nG 4\nM 108,168,160\n"
"R 160,189,8192\nv_bfe_u32 v242, v252, 4, 4\nM 108,172,176\nv_lshrrev_b32 v243, 8, v252\nv_lshlrev_b32 v243, 2, v243\nM 124,168,164\n"
"R 162,189,12288\nv_sub_u32 v242, v242, v243\nM 124,172,180\nv_lshlrev_b32 v243, 4, v242\ns_mov_b64 s[60:61], 0x3f80\nv_lshlrev_b64 v[208:209], v243, s[60:61]\n"
"M 140,168,168\nR 164,189,16384\nM 140,172,184\nbuffer_load_dwordx4 v221, s[16:19], s63 offen offset:1024 lds\nM 156,168,172\nR 166,189,20480\n"
"M 156,172,188\nG 4\nM 96,176,192\nR 168,190,8192\ns_waitcnt vmcnt(13)\nM 96,180,208\n"
"s_add_u32 m0, s50, 24576\nM 112,176,196\nR 170,190,12288\ns_waitcnt vmcnt(12)\nM 112,180,212\ns_add_u32 s63, s58, 851968\n"
"M 128,176,200\nR 172,190,16384\ns_waitcnt vmcnt(11)\nM 128,180,216\nbuffer_load_dwordx4 v220, s[16:19], s63 offen lds\nM 144,176,204\n"
"R 174,190,20480\ns_waitcnt vmcnt(11)\nM 144,180,220\nG 4\nM 100,160,192\nR 176,191,8192\n"
"M 100,164,208\nv_cmp_gt_u32 s[60:61], 4, v242\ns_nop 1\nv_cndmask_b32 v212, 0, v208, s[60:61]\nv_cndmask_b32 v208, 0, v208, s[60:61]\nv_cndmask_b32 v213, 0, v209, s[60:61]\n"
"v_cndmask_b32 v209, 0, v209, s[60:61]\nM 116,160,196\nR 178,191,12288\nM 116,164,212\nM 132,160,200\nR 180,191,16384\n"
"M 132,164,216\nbuffer_load_dwordx4 v221, s[16:19], s63 offen offset:1024 lds\nM 148,160,204\nR 182,191,20480\nM 148,164,220\nG 4\n"
"M 104,168,192\nR 160,188,24576\nM 104,172,208\ns_add_u32 m0, s50, 32768\nM 120,168,196\nR 162,188,28672\n"
"M 120,172,212\ns_add_u32 s63, s59, 0\nM 136,168,200\nR 164,188,32768\nM 136,172,216\nbuffer_load_dwordx4 v220, s[16:19], s63 offen lds\n"
"M 152,168,204\nR 166,188,36864\nM 152,172,220\nG 4\nM 108,176,192\nR 168,189,24576\n"
"M 108,180,208\nM 124,176,196\nR 170,189,28672\nM 124,180,212\nbuffer_load_dwordx4 v221, s[16:19], s63 offen offset:1024 lds\nM 140,176,200\n"
"R 172,189,32768\nM 140,180,216\nM 156,176,204\nR 174,189,36864\nM 156,180,220\nG 4\n"
"M 0,160,160\nR 176,190,24576\nM 0,164,176\ns_mov_b32 m0, s51\nM 16,160,164\nR 178,190,28672\n"
"ds_write_addtid_b32 v128 offset:0\nM 16,164,180\nds_write_addtid_b32 v129 offset:256\nds_write_addtid_b32 v130 offset:512\nv_mfma_f32_16x16x32_bf16 a[224:227], v[160:163], a[168:171], a[224:227]\nR 180,190,32768\n"
"ds_write_addtid_b32 v131 offset:768\nv_mfma_f32_16x16x32_bf16 a[224:227], v[164:167], a[184:187], a[224:227]\nds_write_addtid_b32 v132 offset:1024\nds_write_addtid_b32 v133 offset:1280\nv_mfma_f32_16x16x32_bf16 a[240:243], v[160:163], a[172:175], a[240:243]\nR 182,190,36864\n"
"ds_write_addtid_b32 v134 offset:1536\nv_mfma_f32_16x16x32_bf16 a[240:243], v[164:167], a[188:191], a[240:243]\nds_write_addtid_b32 v135 offset:1792\nds_write_addtid_b32 v136 offset:2048\nG 13\nM 4,168,160\n"
"R 160,191,24576\nds_write_addtid_b32 v137 offset:2304\nM 4,172,176\nds_write_addtid_b32 v138 offset:2560\nds_write_addtid_b32 v139 offset:2816\nM 20,168,164\n"
"R 162,191,28672\nds_write_addtid_b32 v140 offset:3072\nM 20,172,180\nds_write_addtid_b32 v141 offset:3328\nds_write_addtid_b32 v142 offset:3584\nv_mfma_f32_16x16x32_bf16 a[228:231], v[168:171], a[168:171], a[228:231]\n"
"R 164,191,32768\nds_write_addtid_b32 v143 offset:3840\nv_mfma_f32_16x16x32_bf16 a[228:231], v[172:175], a[184:187], a[228:231]\nds_write_addtid_b32 v144 offset:4096\nds_write_addtid_b32 v145 offset:4352\nv_mfma_f32_16x16x32_bf16 a[244:247], v[168:171], a[172:175], a[244:247]\n"
"R 166,191,36864\nds_write_addtid_b32 v146 offset:4608\nv_mfma_f32_16x16x32_bf16 a[244:247], v[172:175], a[188:191], a[244:247]\nds_write_addtid_b32 v147 offset:4864\nds_write_addtid_b32 v148 offset:5120\nG 15\n"
"M 8,176,160\nR 168,188,40960\nds_write_addtid_b32 v149 offset:5376\nM 8,180,176\nds_write_addtid_b32 v150 offset:5632\nds_write_addtid_b32 v151 offset:5888\n"
"M 24,176,164\nR 170,188,45056\nds_write_addtid_b32 v152 offset:6144\nM 24,180,180\nds_write_addtid_b32 v153 offset:6400\nds_write_addtid_b32 v154 offset:6656\n"
"v_mfma_f32_16x16x32_bf16 a[232:235], v[176:179], a[168:171], a[232:235]\nR 172,188,49152\nds_write_addtid_b32 v155 offset:6912\nv_mfma_f32_16x16x32_bf16 a[232:235], v[180:183], a[184:187], a[232:235]\nds_write_addtid_b32 v156 offset:7168\nds_write_addtid_b32 v157 offset:7424\n"
"ds_write_addtid_b32 v158 offset:7680\nds_write_addtid_b32 v159 offset:7936\nv_mfma_f32_16x16x32_bf16 a[248:251], v[176:179], a[172:175], a[248:251]\nR 174,188,53248\ns_add_u32 m0, s50, 40960\nv_mfma_f32_16x16x32_bf16 a[248:251], v[180:183], a[188:191], a[248:251]\n"
"s_add_u32 s63, s59, 524288\nG 15\nM 12,160,160\nR 176,189,40960\ns_waitcnt vmcnt(8) lgkmcnt(2)\ns_barrier\n"
"M 12,164,176\nbuffer_load_dwordx4 v220, s[16:19], s63 offen lds\nds_read2st64_b32 v[128:129], v225 offset0:0 offset1:1\nM 28,160,164\nR 178,189,45056\nds_read2st64_b32 v[130:131], v225 offset0:2 offset1:3\n"
"M 28,164,180\nds_read2st64_b32 v[132:133], v225 offset0:4 offset1:5\nds_read2st64_b32 v[134:135], v225 offset0:6 offset1:7\nv_mfma_f32_16x16x32_bf16 a[236:239], v[160:163], a[168:171], a[236:239]\nR 180,189,49152\nds_read2st64_b32 v[136:137], v225 offset0:8 offset1:9\n"
"v_mfma_f32_16x16x32_bf16 a[236:239], v[164:167], a[184:187], a[236:239]\nbuffer_load_dwordx4 v221, s[16:19], s63 offen offset:1024 lds\nds_read2st64_b32 v[138:139], v225 offset0:10 offset1:11\nv_mfma_f32_16x16x32_bf16 a[252:255], v[160:163], a[172:175], a[252:255]\nR 182,189,53248\ns_add_u32 m0, s50, 49152\n"
"ds_read2st64_b32 v[140:141], v225 offset0:12 offset1:13\nv_mfma_f32_16x16x32_bf16 a[252:255], v[164:167], a[188:191], a[252:255]\nds_read2st64_b32 v[142:143], v225 offset0:14 offset1:15\nG 12\nG 10\nv_add_f32 v96, v96, v128\n"
"M 0,168,192\nR 160,190,40960\ns_add_u32 s63, s59, 65536\nv_add_f32 v97, v97, v129\nM 0,172,208\nbuffer_load_dwordx4 v220, s[16:19], s63 offen lds\n"
"G 9\nv_add_f32 v98, v98, v130\nM 16,168,196\nR 162,190,45056\nv_add_f32 v99, v99, v131\nG 9\n"
"v_add_f32 v100, v100, v132\nM 16,172,212\nds_read2st64_b32 v[144:145], v225 offset0:16 offset1:17\nv_add_f32 v101, v101, v133\nG 9\nv_add_f32 v102, v102, v134\n"
"v_mfma_f32_16x16x32_bf16 a[224:227], v[168:171], a[200:203], a[224:227]\nR 164,190,49152\nv_add_f32 v103, v103, v135\nG 8\nv_add_f32 v104, v104, v136\nv_mfma_f32_16x16x32_bf16 a[224:227], v[172:175], a[216:219], a[224:227]\n"
"buffer_load_dwordx4 v221, s[16:19], s63 offen offset:1024 lds\nv_add_f32 v105, v105, v137\nv_mfma_f32_16x16x32_bf16 a[240:243], v[168:171], a[204:207], a[240:243]\nR 166,190,53248\ns_add_u32 m0, s50, 57344\nG 8\n"
"v_add_f32 v106, v106, v138\nv_mfma_f32_16x16x32_bf16 a[240:243], v[172:175], a[220:223], a[240:243]\nds_read2st64_b32 v[146:147], v225 offset0:18 offset1:19\nG 8\nv_add_f32 v107, v107, v139\nM 4,176,192\n"
"R 168,191,40960\ns_add_u32 s63, s59, 589824\nG 8\nv_add_f32 v108, v108, v140\nM 4,180,208\nbuffer_load_dwordx4 v220, s[16:19], s63 offen lds\n"
"v_add_f32 v109, v109, v141\nM 20,176,196\nR 170,191,45056\nG 8\nv_add_f32 v110, v110, v142\nv_add_f32 v111, v111, v143\n"
"M 20,180,212\nds_read2st64_b32 v[148:149], v225 offset0:20 offset1:21\nG 6\nv_add_f32 v112, v112, v144\nv_add_f32 v113, v113, v145\nv_mfma_f32_16x16x32_bf16 a[228:231], v[176:179], a[200:203], a[228:231]\n"
"R 172,191,49152\nds_read2st64_b32 v[150:151], v225 offset0:22 offset1:23\nv_mfma_f32_16x16x32_bf16 a[228:231], v[180:183], a[216:219], a[228:231]\nbuffer_load_dwordx4 v221, s[16:19], s63 offen offset:1024 lds\nG 5\nv_add_f32 v114, v114, v146\n"
"v_mfma_f32_16x16x32_bf16 a[244:247], v[176:179], a[204:207], a[244:247]\nR 174,191,53248\ns_add_u32 s63, s59, 131072\nv_add_f32 v115, v115, v147\nv_mfma_f32_16x16x32_bf16 a[244:247], v[180:183], a[220:223], a[244:247]\nds_read2st64_b32 v[152:153], v225 offset0:24 offset1:25\n"
"ds_read2st64_b32 v[154:155], v225 offset0:26 offset1:27\nM 8,160,192\nR 176,184,0\nds_read2st64_b32 v[156:157], v225 offset0:28 offset1:29\nM 8,164,208\ns_add_u32 m0, s50, 65536\n"
"ds_read2st64_b32 v[158:159], v225 offset0:30 offset1:31\nG 8\nv_add_f32 v116, v116, v148\nM 24,160,196\nR 178,184,4096\nv_add_f32 v117, v117, v149\n"
"G 7\nv_add_f32 v118, v118, v150\nM 24,164,212\nbuffer_load_dwordx4 v220, s[16:19], s63 offen lds\nv_add_f32 v119, v119, v151\nv_mfma_f32_16x16x32_bf16 a[232:235], v[160:163], a[200:203], a[232:235]\n"
"R 180,184,8192\nv_mfma_f32_16x16x32_bf16 a[232:235], v[164:167], a[216:219], a[232:235]\nG 6\nv_add_f32 v120, v120, v152\nv_add_f32 v121, v121, v153\nG 5\n"
"v_add_f32 v122, v122, v154\nv_add_f32 v123, v123, v155\nG 3\nv_add_f32 v124, v124, v156\nv_add_f32 v125, v125, v157\nG 2\n"
"v_add_f32 v126, v126, v158\nv_add_f32 v127, v127, v159\nv_mfma_f32_16x16x32_bf16 a[248:251], v[160:163], a[204:207], a[248:251]\nR 182,184,12288\nv_cvt_pk_bf16_f32 v96, v96, v97\nv_cvt_pk_bf16_f32 v97, v98, v99\n"
"v_mfma_f32_16x16x32_bf16 a[248:251], v[164:167], a[220:223], a[248:251]\nv_cvt_pk_bf16_f32 v98, v100, v101\nv_cvt_pk_bf16_f32 v99, v102, v103\nv_cvt_pk_bf16_f32 v104, v104, v105\nM 12,168,192\nR 160,185,0\n"
"v_cvt_pk_bf16_f32 v105, v106, v107\nv_cvt_pk_bf16_f32 v106, v108, v109\nM 12,172,208\nbuffer_load_dwordx4 v221, s[16:19], s63 offen offset:1024 lds\nv_cvt_pk_bf16_f32 v107, v110, v111\nM 28,168,196\n"
"R 162,185,4096\ns_add_u32 s63, s59, 655360\nv_cvt_pk_bf16_f32 v112, v112, v113\nM 28,172,212\ns_add_u32 m0, s50, 73728\nv_cvt_pk_bf16_f32 v113, v114, v115\n"
"v_cvt_pk_bf16_f32 v114, v116, v117\nv_cvt_pk_bf16_f32 v115, v118, v119\nv_mfma_f32_16x16x32_bf16 a[236:239], v[168:171], a[200:203], a[236:239]\nR 164,185,8192\nv_cvt_pk_bf16_f32 v120, v120, v121\nv_cvt_pk_bf16_f32 v121, v122, v123\n"
"v_mfma_f32_16x16x32_bf16 a[236:239], v[172:175], a[216:219], a[236:239]\nbuffer_load_dwordx4 v220, s[16:19], s63 offen lds\nv_cvt_pk_bf16_f32 v122, v124, v125\nv_mfma_f32_16x16x32_bf16 a[252:255], v[168:171], a[204:207], a[252:255]\nR 166,185,12288\ns_bfe_u32 s100, s56, 0x20007\n"
"s_mul_i32 s100, s100, 49152\ns_add_u32 s100, s100, s72\ns_add_u32 s100, s100, 65011712\nv_cvt_pk_bf16_f32 v123, v126, v127\nv_mfma_f32_16x16x32_bf16 a[252:255], v[172:175], a[220:223], a[252:255]\ns_waitcnt vmcnt(11)\n"
"s_barrier\nbuffer_store_dwordx4 v[96:99], v252, s[20:23], s100 offen offset:0 sc1\nG 4\nM 32,176,160\nR 168,186,0\nbuffer_store_dwordx4 v[104:107], v252, s[20:23], s100 offen offset:1024 sc1\n"
"M 32,180,176\nbuffer_load_dwordx4 v221, s[16:19], s63 offen offset:1024 lds\nbuffer_store_dwordx4 v[112:115], v252, s[20:23], s100 offen offset:2048 sc1\nv_xor_b32 v240, 0x2000, v225\nM 48,176,164\nR 170,186,4096\n"
"s_add_u32 s63, s59, 196608\nds_write2st64_b32 v240, a224, a225 offset0:0 offset1:1\nM 48,180,180\ns_add_u32 m0, s50, 81920\nds_write2st64_b32 v240, a226, a227 offset0:2 offset1:3\nM 64,176,168\n"
"R 172,186,8192\nds_write2st64_b32 v240, a228, a229 offset0:4 offset1:5\nM 64,180,184\nbuffer_load_dwordx4 v220, s[16:19], s63 offen lds\nds_write2st64_b32 v240, a230, a231 offset0:6 offset1:7\nM 80,176,172\n"
"R 174,186,12288\nds_write2st64_b32 v240, a232, a233 offset0:8 offset1:9\nM 80,180,188\nbuffer_store_dwordx4 v[120:123], v252, s[20:23], s100 offen offset:3072 sc1\nds_write2st64_b32 v240, a234, a235 offset0:10 offset1:11\nG 10\n"
"M 36,160,160\nR 176,187,0\nds_write2st64_b32 v240, a236, a237 offset0:12 offset1:13\nM 36,164,176\nbuffer_load_dwordx4 v221, s[16:19], s63 offen offset:1024 lds\nds_write2st64_b32 v240, a238, a239 offset0:14 offset1:15\n"
"M 52,160,164\nR 178,187,4096\ns_add_u32 s63, s59, 720896\nds_write2st64_b32 v240, a240, a241 offset0:16 offset1:17\nM 52,164,180\ns_add_u32 m0, s50, 90112\n"
"ds_write2st64_b32 v240, a242, a243 offset0:18 offset1:19\nM 68,160,168\nR 180,187,8192\nv_mfma_f32_16x16x32_bf16 v[96:99], v[208:211], v[192:195], 0\nds_write2st64_b32 v240, a244, a245 offset0:20 offset1:21\nM 68,164,184\n"
"v_mfma_f32_16x16x32_bf16 v[100:103], v[210:213], v[192:195], 0\nbuffer_load_dwordx4 v220, s[16:19], s63 offen lds\nds_write2st64_b32 v240, a246, a247 offset0:22 offset1:23\nM 84,160,172\nR 182,187,12288\nv_mfma_f32_16x16x32_bf16 v[104:107], v[208:211], v[196:199], 0\n"
"ds_write2st64_b32 v240, a248, a249 offset0:24 offset1:25\nM 84,164,188\nv_mfma_f32_16x16x32_bf16 v[108:111], v[210:213], v[196:199], 0\nds_write2st64_b32 v240, a250, a251 offset0:26 offset1:27\nG 14\nM 40,168,160\n"
"R 160,184,16384\nv_mfma_f32_16x16x32_bf16 v[112:115], v[208:211], v[200:203], 0\nds_write2st64_b32 v240, a252, a253 offset0:28 offset1:29\nM 40,172,176\nv_mfma_f32_16x16x32_bf16 v[116:119], v[210:213], v[200:203], 0\nbuffer_load_dwordx4 v221, s[16:19], s63 offen offset:1024 lds\n"
"ds_write2st64_b32 v240, a254, a255 offset0:30 offset1:31\nM 56,168,164\nR 162,184,20480\nv_mfma_f32_16x16x32_bf16 v[120:123], v[208:211], v[204:207], 0\ns_add_u32 m0, s50, 98304\ns_add_u32 s63, s59, 393216\n"
"M 56,172,180\nv_mfma_f32_16x16x32_bf16 v[124:127], v[210:213], v[204:207], 0\nbuffer_load_dwordx4 v220, s[16:19], s63 offen lds\nM 72,168,168\nR 164,184,24576\nv_mfma_f32_16x16x32_bf16 v[96:99], v[208:211], v[232:235], v[96:99]\n"
"M 72,172,184\nv_mfma_f32_16x16x32_bf16 v[100:103], v[210:213], v[232:235], v[100:103]\nbuffer_load_dwordx4 v221, s[16:19], s63 offen offset:1024 lds\nM 88,168,172\nR 166,184,28672\nv_mfma_f32_16x16x32_bf16 v[104:107], v[208:211], v[236:239], v[104:107]\n"
"s_add_u32 m0, s50, 106496\nM 88,172,188\nv_mfma_f32_16x16x32_bf16 v[108:111], v[210:213], v[236:239], v[108:111]\ns_add_u32 s63, s59, 458752\nG 8\nM 44,176,160\n"
"R 168,185,16384\nv_mfma_f32_16x16x32_bf16 v[112:115], v[208:211], v[232:235], v[112:115]\ns_waitcnt vmcnt(18) lgkmcnt(4)\ns_barrier\nM 44,180,176\nv_mfma_f32_16x16x32_bf16 v[116:119], v[210:213], v[232:235], v[116:119]\n"
"buffer_load_dwordx4 v220, s[16:19], s63 offen lds\nds_read2st64_b32 v[128:129], v225 offset0:0 offset1:1\nM 60,176,164\nR 170,185,20480\nv_mfma_f32_16x16x32_bf16 v[120:123], v[208:211], v[236:239], v[120:123]\nds_read2st64_b32 v[130:131], v225 offset0:2 offset1:3\n"
"M 60,180,180\nv_mfma_f32_16x16x32_bf16 v[124:127], v[210:213], v[236:239], v[124:127]\nds_read2st64_b32 v[132:133], v225 offset0:4 offset1:5\nds_read2st64_b32 v[134:135], v225 offset0:6 offset1:7\nM 76,176,168\nR 172,185,24576\n"
"ds_read2st64_b32 v[136:137], v225 offset0:8 offset1:9\nM 76,180,184\nds_read2st64_b32 v[138:139], v225 offset0:10 offset1:11\nds_read2st64_b32 v[140:141], v225 offset0:12 offset1:13\nM 92,176,172\nR 174,185,28672\n"
"ds_read2st64_b32 v[142:143], v225 offset0:14 offset1:15\nM 92,180,188\nds_read2st64_b32 v[144:145], v225 offset0:16 offset1:17\nG 13\nG 11\nv_add_f32 v0, v0, v128\n"
"M 32,160,192\nR 176,186,16384\nv_add_f32 v1, v1, v129\nG 10\nv_add_f32 v2, v2, v130\nM 32,164,208\n"
"buffer_load_dwordx4 v221, s[16:19], s63 offen offset:1024 lds\nv_add_f32 v3, v3, v131\nM 48,160,196\nR 178,186,20480\nG 10\nv_add_f32 v4, v4, v132\n"
"v_add_f32 v5, v5, v133\nM 48,164,212\ns_add_u32 m0, s50, 114688\nds_read2st64_b32 v[146:147], v225 offset0:18 offset1:19\nG 10\nv_add_f32 v6, v6, v134\n"
"M 64,160,200\nR 180,186,24576\nv_add_f32 v7, v7, v135\nG 9\nv_add_f32 v8, v8, v136\nM 64,164,216\n"
"s_add_u32 s63, s59, 262144\nds_read2st64_b32 v[148:149], v225 offset0:20 offset1:21\nv_add_f32 v9, v9, v137\nM 80,160,204\nR 182,186,28672\nG 10\n"
"v_add_f32 v10, v10, v138\nv_add_f32 v11, v11, v139\nM 80,164,220\nds_read2st64_b32 v[150:151], v225 offset0:22 offset1:23\nG 9\nv_add_f32 v12, v12, v140\n"
"M 36,168,192\nR 160,187,16384\nv_add_f32 v13, v13, v141\nG 9\nv_add_f32 v14, v14, v142\nM 36,172,208\n"
"buffer_load_dwordx4 v220, s[16:19], s63 offen lds\nv_add_f32 v15, v15, v143\nM 52,168,196\nR 162,187,20480\nG 9\nv_add_f32 v16, v16, v144\n"
"v_add_f32 v17, v17, v145\nM 52,172,212\nds_read2st64_b32 v[152:153], v225 offset0:24 offset1:25\nG 7\nv_add_f32 v18, v18, v146\nv_add_f32 v19, v19, v147\n"
"M 68,168,200\nR 164,187,24576\nG 6\nv_add_f32 v20, v20, v148\nv_add_f32 v21, v21, v149\nM 68,172,216\n"
"ds_read2st64_b32 v[154:155], v225 offset0:26 offset1:27\nG 5\nv_add_f32 v22, v22, v150\nv_add_f32 v23, v23, v151\nM 84,168,204\nR 166,187,28672\n"
"ds_read2st64_b32 v[156:157], v225 offset0:28 offset1:29\nM 84,172,220\nds_read2st64_b32 v[158:159], v225 offset0:30 offset1:31\nM 40,176,192\nR 168,184,32768\nM 40,180,208\n"
"buffer_load_dwordx4 v221, s[16:19], s63 offen offset:1024 lds\nG 6\nv_add_f32 v24, v24, v152\nM 56,176,196\nR 170,184,36864\nv_add_f32 v25, v25, v153\n"
"M 56,180,212\ns_add_u32 m0, s50, 0\nG 5\nv_add_f32 v26, v26, v154\nv_add_f32 v27, v27, v155\nM 72,176,200\n"
"R 172,184,40960\nG 4\nv_add_f32 v28, v28, v156\nv_add_f32 v29, v29, v157\nM 72,180,216\ns_add_u32 s63, s59, 786432\n"
"G 3\nv_add_f32 v30, v30, v158\nv_add_f32 v31, v31, v159\nM 88,176,204\nR 174,184,45056\nv_cvt_pk_bf16_f32 v0, v0, v1\n"
"v_cvt_pk_bf16_f32 v1, v2, v3\nM 88,180,220\nv_cvt_pk_bf16_f32 v2, v4, v5\nv_cvt_pk_bf16_f32 v3, v6, v7\nv_cvt_pk_bf16_f32 v8, v8, v9\nM 44,160,192\n"
"R 176,185,32768\nv_cvt_pk_bf16_f32 v9, v10, v11\nv_cvt_pk_bf16_f32 v10, v12, v13\nM 44,164,208\nbuffer_load_dwordx4 v220, s[16:19], s63 offen lds\nv_cvt_pk_bf16_f32 v11, v14, v15\n"
"M 60,160,196\nR 178,185,36864\nv_cvt_pk_bf16_f32 v16, v16, v17\nv_cvt_pk_bf16_f32 v17, v18, v19\nM 60,164,212\nv_cvt_pk_bf16_f32 v18, v20, v21\n"
"v_cvt_pk_bf16_f32 v19, v22, v23\nv_cvt_pk_bf16_f32 v24, v24, v25\nM 76,160,200\nR 180,185,40960\nv_cvt_pk_bf16_f32 v25, v26, v27\nv_cvt_pk_bf16_f32 v26, v28, v29\n"
"M 76,164,216\nbuffer_load_dwordx4 v221, s[16:19], s63 offen offset:1024 lds\nv_cvt_pk_bf16_f32 v27, v30, v31\nM 92,160,204\nR 182,185,45056\ns_bfe_u32 s100, s57, 0x20007\n"
"s_mul_i32 s100, s100, 49152\ns_add_u32 s100, s100, s72\ns_add_u32 s100, s100, 65028096\nM 92,164,220\ns_waitcnt vmcnt(20)\ns_barrier\n"
"buffer_store_dwordx4 v[0:3], v252, s[20:23], s100 offen offset:0 sc1\nG 4\nM 96,168,0\nR 160,186,32768\nbuffer_store_dwordx4 v[8:11], v252, s[20:23], s100 offen offset:1024 sc1\nM 96,172,16\n"
"s_mov_b32 m0, s51\nbuffer_store_dwordx4 v[16:19], v252, s[20:23], s100 offen offset:2048 sc1\nM 112,168,4\nR 162,186,36864\nds_write_addtid_b32 v64 offset:0\nM 112,172,20\n"
"ds_write_addtid_b32 v65 offset:256\nds_write_addtid_b32 v66 offset:512\nv_mfma_f32_16x16x32_bf16 v[128:131], v[168:171], a[8:11], 0\nR 164,186,40960\nds_write_addtid_b32 v67 offset:768\nM 128,172,24\n"
"ds_write_addtid_b32 v68 offset:1024\nds_write_addtid_b32 v69 offset:1280\nv_mfma_f32_16x16x32_bf16 v[144:147], v[168:171], a[12:15], 0\nR 166,186,45056\nds_write_addtid_b32 v70 offset:1536\nM 144,172,28\n"
"ds_write_addtid_b32 v71 offset:1792\nds_write_addtid_b32 v72 offset:2048\nG 13\nM 100,176,0\nR 168,187,32768\nds_write_addtid_b32 v73 offset:2304\n"
"M 100,180,16\nds_write_addtid_b32 v74 offset:2560\nbuffer_store_dwordx4 v[24:27], v252, s[20:23], s100 offen offset:3072 sc1\nds_write_addtid_b32 v75 offset:2816\nM 116,176,4\nR 170,187,36864\n"
"ds_write_addtid_b32 v76 offset:3072\nM 116,180,20\nds_write_addtid_b32 v77 offset:3328\nds_write_addtid_b32 v78 offset:3584\nv_mfma_f32_16x16x32_bf16 v[132:135], v[176:179], a[8:11], 0\nR 172,187,40960\n"
"ds_write_addtid_b32 v79 offset:3840\nM 132,180,24\nds_write_addtid_b32 v80 offset:4096\nds_write_addtid_b32 v81 offset:4352\nv_mfma_f32_16x16x32_bf16 v[148:151], v[176:179], a[12:15], 0\nR 174,187,45056\n"
"ds_write_addtid_b32 v82 offset:4608\nM 148,180,28\nds_write_addtid_b32 v83 offset:4864\nds_write_addtid_b32 v84 offset:5120\nG 15\nM 104,160,0\n"
"R 176,184,49152\nds_write_addtid_b32 v85 offset:5376\nM 104,164,16\nds_write_addtid_b32 v86 offset:5632\nds_write_addtid_b32 v87 offset:5888\nM 120,160,4\n"
"R 178,184,53248\nds_write_addtid_b32 v88 offset:6144\nM 120,164,20\nds_write_addtid_b32 v89 offset:6400\nds_write_addtid_b32 v90 offset:6656\nv_mfma_f32_16x16x32_bf16 v[136:139], v[160:163], a[8:11], 0\n"
"R 180,184,57344\nds_write_addtid_b32 v91 offset:6912\nM 136,164,24\nds_write_addtid_b32 v92 offset:7168\nds_write_addtid_b32 v93 offset:7424\nds_write_addtid_b32 v94 offset:7680\n"
"ds_write_addtid_b32 v95 offset:7936\nv_mfma_f32_16x16x32_bf16 v[152:155], v[160:163], a[12:15], 0\nR 182,184,61440\nM 152,164,28\nG 15\nM 108,168,0\n"
"R 160,185,49152\ns_waitcnt vmcnt(18) lgkmcnt(2)\ns_barrier\nM 108,172,16\nds_read2st64_b32 v[64:65], v225 offset0:0 offset1:1\nds_read2st64_b32 v[66:67], v225 offset0:2 offset1:3\n"
"M 124,168,4\nR 162,185,53248\nds_read2st64_b32 v[68:69], v225 offset0:4 offset1:5\nM 124,172,20\nds_read2st64_b32 v[70:71], v225 offset0:6 offset1:7\nds_read2st64_b32 v[72:73], v225 offset0:8 offset1:9\n"
"v_mfma_f32_16x16x32_bf16 v[140:143], v[168:171], a[8:11], 0\nR 164,185,57344\nds_read2st64_b32 v[74:75], v225 offset0:10 offset1:11\nM 140,172,24\nds_read2st64_b32 v[76:77], v225 offset0:12 offset1:13\nds_read2st64_b32 v[78:79], v225 offset0:14 offset1:15\n"
"v_mfma_f32_16x16x32_bf16 v[156:159], v[168:171], a[12:15], 0\nR 166,185,61440\nds_read2st64_b32 v[80:81], v225 offset0:16 offset1:17\nM 156,172,28\nds_read2st64_b32 v[82:83], v225 offset0:18 offset1:19\nG 14\n"
"G 12\nv_add_f32 v32, v32, v64\nM 96,176,32\nR 168,186,49152\nv_add_f32 v33, v33, v65\nG 12\n"
"v_add_f32 v34, v34, v66\nM 96,180,48\nds_read2st64_b32 v[84:85], v225 offset0:20 offset1:21\nv_add_f32 v35, v35, v67\nG 11\nv_add_f32 v36, v36, v68\n"
"M 112,176,36\nR 170,186,53248\nv_add_f32 v37, v37, v69\nG 11\nv_add_f32 v38, v38, v70\nM 112,180,52\n"
"ds_read2st64_b32 v[86:87], v225 offset0:22 offset1:23\nv_add_f32 v39, v39, v71\nG 11\nv_add_f32 v40, v40, v72\nM 128,176,40\nR 172,186,57344\n"
"v_add_f32 v41, v41, v73\nG 10\nv_add_f32 v42, v42, v74\nM 128,180,56\nds_read2st64_b32 v[88:89], v225 offset0:24 offset1:25\nv_add_f32 v43, v43, v75\n"
"G 10\nv_add_f32 v44, v44, v76\nM 144,176,44\nR 174,186,61440\nv_add_f32 v45, v45, v77\nG 10\n"
"v_add_f32 v46, v46, v78\nM 144,180,60\nds_read2st64_b32 v[90:91], v225 offset0:26 offset1:27\nG 10\nv_add_f32 v47, v47, v79\nM 100,160,32\n"
"R 176,187,49152\nG 10\nv_add_f32 v48, v48, v80\nv_add_f32 v49, v49, v81\nM 100,164,48\nds_read2st64_b32 v[92:93], v225 offset0:28 offset1:29\n"
"G 10\nv_add_f32 v50, v50, v82\nv_add_f32 v51, v51, v83\nM 116,160,36\nR 178,187,53248\nG 9\n"
"v_add_f32 v52, v52, v84\nM 116,164,52\nds_read2st64_b32 v[94:95], v225 offset0:30 offset1:31\nv_add_f32 v53, v53, v85\nM 132,160,40\nR 180,187,57344\n"
"G 9\nv_add_f32 v54, v54, v86\nM 132,164,56\nv_add_f32 v55, v55, v87\nG 7\nv_add_f32 v56, v56, v88\n"
"M 148,160,44\nR 182,187,61440\nv_add_f32 v57, v57, v89\nM 148,164,60\nG 7\nG 6\n"
"v_add_f32 v58, v58, v90\nv_add_f32 v59, v59, v91\nM 104,168,32\nR 160,188,0\nG 5\nv_add_f32 v60, v60, v92\n"
"M 104,172,48\nv_add_f32 v61, v61, v93\nG 3\nv_add_f32 v62, v62, v94\nM 120,168,36\nR 162,188,4096\n"
"v_mov_b32 v226, s46\nM 120,172,52\nv_mov_b32 v227, s47\nv_add_f32 v63, v63, v95\nM 136,168,40\nR 164,188,8192\n"
"s_bfe_u32 s62, s56, 0x20007\ns_lshl_b32 s62, s62, 5\ns_add_u32 s61, s74, 1009664\ns_add_u32 s61, s61, s62\ns_waitcnt vmcnt(0)\ns_mov_b64 exec, 1\n"
"buffer_atomic_swap_x2 v[226:227], off, s[20:23], s61 sc1\ns_mov_b64 exec, -1\nM 136,172,56\ns_bfe_u32 s62, s57, 0x20007\ns_lshl_b32 s62, s62, 5\ns_add_u32 s61, s74, 1009672\n"
"s_add_u32 s61, s61, s62\ns_mov_b64 exec, 1\nbuffer_atomic_swap_x2 v[226:227], off, s[20:23], s61 sc1\ns_mov_b64 exec, -1\nM 152,168,44\nR 166,188,12288\n"
"s_add_u32 s63, s59, 327680\nv_cvt_pk_bf16_f32 v32, v32, v33\nM 152,172,60\nG 4\nv_cvt_pk_bf16_f32 v33, v34, v35\nv_cvt_pk_bf16_f32 v34, v36, v37\n"
"v_cvt_pk_bf16_f32 v35, v38, v39\nM 108,176,32\nR 168,189,0\nv_cvt_pk_bf16_f32 v40, v40, v41\nv_cvt_pk_bf16_f32 v41, v42, v43\nM 108,180,48\n"
"s_add_u32 m0, s50, 8192\nv_cvt_pk_bf16_f32 v42, v44, v45\nv_cvt_pk_bf16_f32 v43, v46, v47\nM 124,176,36\nR 170,189,4096\nv_cvt_pk_bf16_f32 v48, v48, v49\n"
"M 124,180,52\nbuffer_load_dwordx4 v220, s[16:19], s63 offen lds\nM 140,176,40\nR 172,189,8192\nv_cvt_pk_bf16_f32 v49, v50, v51\nM 140,180,56\n"
"v_cvt_pk_bf16_f32 v50, v52, v53\nv_cvt_pk_bf16_f32 v51, v54, v55\nM 156,176,44\nR 174,189,12288\nv_cvt_pk_bf16_f32 v56, v56, v57\nM 156,180,60\n"
"s_barrier\nG 4\nM 96,160,64\nR 176,190,0\nv_cvt_pk_bf16_f32 v57, v58, v59\nM 96,164,80\n"
"v_cvt_pk_bf16_f32 v58, v60, v61\nv_cvt_pk_bf16_f32 v59, v62, v63\nM 112,160,68\nR 178,190,4096\ns_bfe_u32 s100, s58, 0x20007\ns_mul_i32 s100, s100, 49152\n"
"s_add_u32 s100, s100, s72\ns_add_u32 s100, s100, 65044480\nM 112,164,84\nbuffer_load_dwordx4 v221, s[16:19], s63 offen offset:1024 lds\nM 128,160,72\nR 180,190,8192\n"
"buffer_store_dwordx4 v[32:35], v252, s[20:23], s100 offen offset:0 sc1\nM 128,164,88\ns_add_u32 m0, s50, 16384\nbuffer_store_dwordx4 v[40:43], v252, s[20:23], s100 offen offset:1024 sc1\nM 144,160,76\nR 182,190,12288\n"
"M 144,164,92\nbuffer_store_dwordx4 v[48:51], v252, s[20:23], s100 offen offset:2048 sc1\nG 4\nM 100,168,64\nR 160,191,0\nv_bfe_u32 v250, v252, 4, 2\n"
"M 100,172,80\ns_add_u32 s63, s59, 851968\nbuffer_store_dwordx4 v[56:59], v252, s[20:23], s100 offen offset:3072 sc1\nv_lshlrev_b32 v250, 3, v250\nM 116,168,68\nR 162,191,4096\n"
"M 116,172,84\nbuffer_load_dwordx4 v220, s[16:19], s63 offen lds\nM 132,168,72\nR 164,191,8192\nM 132,172,88\ns_lshl_b32 s60, s43, 5\n"
"v_add_u32 v250, s60, v250\nM 148,168,76\nR 166,191,12288\nM 148,172,92\nG 4\nM 104,176,64\n"
"R 168,188,16384\nM 104,180,80\nM 120,176,68\nR 170,188,20480\nM 120,180,84\nbuffer_load_dwordx4 v221, s[16:19], s63 offen offset:1024 lds\n"
"M 136,176,72\nR 172,188,24576\nM 136,180,88\nM 152,176,76\nR 174,188,28672\nM 152,180,92\n"
"G 4\nM 108,160,64\nR 176,189,16384\nM 108,164,80\nM 124,160,68\nR 178,189,20480\n"
"M 124,164,84\nM 140,160,72\nR 180,189,24576\nM 140,164,88\nM 156,160,76\nR 182,189,28672\n"
"M 156,164,92\nG 4\nM 96,168,96\nR 160,190,16384\nM 96,172,112\nM 112,168,100\n"
"R 162,190,20480\nM 112,172,116\nM 128,168,104\nR 164,190,24576\nM 128,172,120\nM 144,168,108\n"
"R 166,190,28672\nM 144,172,124\nG 4\nM 100,176,96\nR 168,191,16384\nM 100,180,112\n"
"M 116,176,100\nR 170,191,20480\nM 116,180,116\nM 132,176,104\nR 172,191,24576\nv_mov_b32 v248, 0\n"
"M 132,180,120\nv_mov_b32 v249, 0\nM 148,176,108\nR 174,191,28672\ns_add_u32 s61, s74, 1009664\ns_mov_b64 exec, 3\n"
"buffer_atomic_or_x2 v[248:249], v250, s[20:23], s61 offen sc0 sc1\ns_mov_b64 exec, -1\nM 148,180,124\nG 4\nM 104,160,96\nR 176,188,32768\n"
"s_barrier\nM 104,164,112\nM 120,160,100\nR 178,188,36864\nM 120,164,116\nM 136,160,104\n"
"R 180,188,40960\nM 136,164,120\nM 152,160,108\nR 182,188,45056\nM 152,164,124\nG 4\n"
"M 108,168,96\nR 160,189,32768\nM 108,172,112\nM 124,168,100\nR 162,189,36864\nM 124,172,116\n"
"M 140,168,104\nR 164,189,40960\nM 140,172,120\nM 156,168,108\nR 166,189,45056\nM 156,172,124\n"
"G 4\nM 96,176,128\nR 168,190,32768\nM 96,180,144\nM 112,176,132\nR 170,190,36864\n"
"M 112,180,148\nM 128,176,136\nR 172,190,40960\nM 128,180,152\nM 144,176,140\nR 174,190,45056\n"
"M 144,180,156\nG 4\nM 100,160,128\nR 176,191,32768\nM 100,164,144\nM 116,160,132\n"
"R 178,191,36864\nM 116,164,148\nM 132,160,136\nR 180,191,40960\nM 132,164,152\ns_mul_i32 s64, s43, 49152\n"
"M 148,160,140\nR 182,191,45056\nM 148,164,156\nG 4\nM 104,168,128\nR 160,188,49152\n"
"s_waitcnt vmcnt(0)\nv_cmp_eq_u64 s[86:87], s[46:47], v[248:249]\nM 104,172,144\ns_add_u32 s64, s64, s72\nM 120,168,132\nR 162,188,53248\n"
"s_and_b32 s62, s86, 3\ns_cmp_eq_u32 s62, 3\ns_cbranch_scc0 Lq998\nLq999:\nM 120,172,148\ns_add_u32 s64, s64, 65011712\n"
"M 136,168,136\nR 164,184,0\nM 136,172,152\nbuffer_load_dwordx4 v[0:3], v252, s[20:23], s64 offen offset:0 sc1\nM 152,168,140\nR 166,184,4096\n"
"M 152,172,156\nG 4\nM 108,176,128\nR 168,189,49152\ns_barrier\nM 108,180,144\n"
"buffer_load_dwordx4 v[4:7], v252, s[20:23], s64 offen offset:1024 sc1\nM 124,176,132\nR 170,189,53248\ns_mul_i32 s65, s43, 49152\nM 124,180,148\nbuffer_load_dwordx4 v[8:11], v252, s[20:23], s64 offen offset:2048 sc1\n"
"M 140,176,136\nR 172,185,0\ns_add_u32 s65, s65, s72\nM 140,180,152\nbuffer_load_dwordx4 v[12:15], v252, s[20:23], s64 offen offset:3072 sc1\nM 156,176,140\n"
"R 174,185,4096\ns_add_u32 s65, s65, 65028096\nM 156,180,156\nG 4\nM 128,160,168\nR 176,190,49152\n"
"buffer_load_dwordx4 v[16:19], v252, s[20:23], s65 offen offset:0 sc1\nM 128,164,184\nR 178,190,53248\nM 144,160,172\nR 180,186,0\nM 144,164,188\n"
"R 182,186,4096\nG 4\nM 132,168,168\nR 160,191,49152\nbuffer_load_dwordx4 v[20:23], v252, s[20:23], s65 offen offset:1024 sc1\nM 132,172,184\n"
"R 162,191,53248\nM 148,168,172\nR 164,187,0\nbuffer_load_dwordx4 v[24:27], v252, s[20:23], s65 offen offset:2048 sc1\nM 148,172,188\nR 166,187,4096\n"
"G 4\nM 136,176,168\nR 168,184,8192\ns_mul_i32 s66, s43, 49152\nM 136,180,184\nR 170,184,12288\n"
"buffer_load_dwordx4 v[28:31], v252, s[20:23], s65 offen offset:3072 sc1\nM 152,176,172\nR 172,184,16384\ns_add_u32 s66, s66, s72\nM 152,180,188\nR 174,184,20480\n"
"G 4\nM 140,160,168\nR 176,185,8192\ns_add_u32 s66, s66, 65044480\nM 140,164,184\nR 178,185,12288\n"
"M 156,160,172\nR 180,185,16384\nM 156,164,188\nR 182,185,20480\nG 4\nM 128,168,200\n"
"R 160,186,8192\nbuffer_load_dwordx4 v[64:67], v252, s[20:23], s66 offen offset:0 sc1\nM 128,172,216\nR 162,186,12288\nM 144,168,204\nR 164,186,16384\n"
"buffer_load_dwordx4 v[68:71], v252, s[20:23], s66 offen offset:1024 sc1\nM 144,172,220\nR 166,186,20480\nG 4\nM 132,176,200\nR 168,187,8192\n"
"M 132,180,216\nR 170,187,12288\nbuffer_load_dwordx4 v[72:75], v252, s[20:23], s66 offen offset:2048 sc1\nM 148,176,204\nR 172,187,16384\nM 148,180,220\n"
"R 174,187,20480\nG 4\nM 136,160,200\nR 176,188,49152\nbuffer_load_dwordx4 v[76:79], v252, s[20:23], s66 offen offset:3072 sc1\nM 136,164,216\n"
"R 178,188,53248\nM 152,160,204\nR 180,184,0\nM 152,164,220\nR 182,184,4096\nG 4\n"
"M 140,168,200\nR 160,189,49152\nM 140,172,216\nR 162,189,53248\nM 156,168,204\nR 164,185,0\n"
"M 156,172,220\nR 166,185,4096\nG 4\nM 96,176,160\nR 168,190,49152\nM 96,180,176\n"
"R 170,190,53248\ns_mov_b32 m0, s51\nM 112,176,164\nR 172,186,0\nds_write_addtid_b32 v128 offset:0\nM 112,180,180\n"
"R 174,186,4096\nds_write_addtid_b32 v129 offset:256\nG 6\nM 100,160,160\nR 176,191,49152\nds_write_addtid_b32 v130 offset:512\n"
"M 100,164,176\nR 178,191,53248\nds_write_addtid_b32 v131 offset:768\nM 116,160,164\nR 180,187,0\nds_write_addtid_b32 v132 offset:1024\n"
"M 116,164,180\nR 182,187,4096\nds_write_addtid_b32 v133 offset:1280\nG 9\nM 104,168,160\nR 160,184,8192\n"
"ds_write_addtid_b32 v134 offset:1536\nM 104,172,176\nR 162,184,12288\nds_write_addtid_b32 v135 offset:1792\nM 120,168,164\nR 164,184,16384\n"
"ds_write_addtid_b32 v136 offset:2048\nM 120,172,180\nR 166,184,20480\nds_write_addtid_b32 v137 offset:2304\nG 9\nM 108,176,160\n"
"R 168,185,8192\nds_write_addtid_b32 v138 offset:2560\nM 108,180,176\nR 170,185,12288\nds_write_addtid_b32 v139 offset:2816\nM 124,176,164\n"
"R 172,185,16384\nds_write_addtid_b32 v140 offset:3072\nds_write_addtid_b32 v141 offset:3328\nds_write_addtid_b32 v142 offset:3584\nds_write_addtid_b32 v143 offset:3840\nds_write_addtid_b32 v144 offset:4096\n"
"ds_write_addtid_b32 v145 offset:4352\nds_write_addtid_b32 v146 offset:4608\nds_write_addtid_b32 v147 offset:4864\nds_write_addtid_b32 v148 offset:5120\nds_write_addtid_b32 v149 offset:5376\nds_write_addtid_b32 v150 offset:5632\n"
"ds_write_addtid_b32 v151 offset:5888\nds_write_addtid_b32 v152 offset:6144\nds_write_addtid_b32 v153 offset:6400\nds_write_addtid_b32 v154 offset:6656\nds_write_addtid_b32 v155 offset:6912\nds_write_addtid_b32 v156 offset:7168\n"
"ds_write_addtid_b32 v157 offset:7424\nds_write_addtid_b32 v158 offset:7680\nds_write_addtid_b32 v159 offset:7936\nM 124,180,180\nR 174,185,20480\nG 15\n"
"M 96,160,192\nR 176,186,8192\nM 96,164,208\nR 178,186,12288\nG 3\ns_barrier\n"
"M 112,160,196\nR 180,186,16384\nM 112,164,212\nR 182,186,20480\nG 4\nM 100,168,192\n"
"R 160,187,8192\nM 100,172,208\nR 162,187,12288\nM 116,168,196\nR 164,187,16384\nM 116,172,212\n"
"R 166,187,20480\nG 4\nM 104,176,192\nds_read2st64_b32 v[128:129], v225 offset0:0 offset1:1\nds_read2st64_b32 v[130:131], v225 offset0:2 offset1:3\nM 104,180,208\n"
"ds_read2st64_b32 v[132:133], v225 offset0:4 offset1:5\nds_read2st64_b32 v[134:135], v225 offset0:6 offset1:7\nM 120,176,196\nds_read2st64_b32 v[136:137], v225 offset0:8 offset1:9\nds_read2st64_b32 v[138:139], v225 offset0:10 offset1:11\nM 120,180,212\n"
"ds_read2st64_b32 v[140:141], v225 offset0:12 offset1:13\nds_read2st64_b32 v[142:143], v225 offset0:14 offset1:15\nG 8\nM 108,160,192\nds_read2st64_b32 v[144:145], v225 offset0:16 offset1:17\nds_read2st64_b32 v[146:147], v225 offset0:18 offset1:19\n"
"M 108,164,208\nds_read2st64_b32 v[148:149], v225 offset0:20 offset1:21\nds_read2st64_b32 v[150:151], v225 offset0:22 offset1:23\nM 124,160,196\nds_read2st64_b32 v[152:153], v225 offset0:24 offset1:25\nds_read2st64_b32 v[154:155], v225 offset0:26 offset1:27\n"
"M 124,164,212\nds_read2st64_b32 v[156:157], v225 offset0:28 offset1:29\nds_read2st64_b32 v[158:159], v225 offset0:30 offset1:31\ns_waitcnt vmcnt(11)\nv_mfma_f32_16x16x32_bf16 v[96:99], v[208:211], v[0:3], v[96:99]\nv_mfma_f32_16x16x32_bf16 v[100:103], v[210:213], v[0:3], v[100:103]\n"
"s_waitcnt vmcnt(10)\nv_mfma_f32_16x16x32_bf16 v[104:107], v[208:211], v[4:7], v[104:107]\nv_mfma_f32_16x16x32_bf16 v[108:111], v[210:213], v[4:7], v[108:111]\ns_waitcnt vmcnt(9)\nv_mfma_f32_16x16x32_bf16 v[112:115], v[208:211], v[8:11], v[112:115]\nv_mfma_f32_16x16x32_bf16 v[116:119], v[210:213], v[8:11], v[116:119]\n"
"s_waitcnt vmcnt(8)\nv_mfma_f32_16x16x32_bf16 v[120:123], v[208:211], v[12:15], v[120:123]\nv_mfma_f32_16x16x32_bf16 v[124:127], v[210:213], v[12:15], v[124:127]\ns_waitcnt vmcnt(7)\nv_mfma_f32_16x16x32_bf16 v[96:99], v[208:211], v[16:19], v[96:99]\nv_mfma_f32_16x16x32_bf16 v[100:103], v[210:213], v[16:19], v[100:103]\n"
"s_waitcnt vmcnt(6)\nv_mfma_f32_16x16x32_bf16 v[104:107], v[208:211], v[20:23], v[104:107]\nv_mfma_f32_16x16x32_bf16 v[108:111], v[210:213], v[20:23], v[108:111]\ns_waitcnt vmcnt(5)\nv_mfma_f32_16x16x32_bf16 v[112:115], v[208:211], v[24:27], v[112:115]\nv_mfma_f32_16x16x32_bf16 v[116:119], v[210:213], v[24:27], v[116:119]\n"
"s_waitcnt vmcnt(4)\nv_mfma_f32_16x16x32_bf16 v[120:123], v[208:211], v[28:31], v[120:123]\nv_mfma_f32_16x16x32_bf16 v[124:127], v[210:213], v[28:31], v[124:127]\ns_waitcnt vmcnt(1)\nv_max3_u32 v248, v67, v71, v75\ns_waitcnt vmcnt(0)\n"
"v_max_u32 v248, v248, v79\nv_cmp_eq_u32 vcc, -1, v248\ns_nop 1\ns_cbranch_vccnz Lq1021\nLq1022:\nv_mfma_f32_16x16x32_bf16 v[96:99], v[208:211], v[64:67], v[96:99]\n"
"v_mfma_f32_16x16x32_bf16 v[100:103], v[210:213], v[64:67], v[100:103]\nv_mfma_f32_16x16x32_bf16 v[104:107], v[208:211], v[68:71], v[104:107]\nv_mfma_f32_16x16x32_bf16 v[108:111], v[210:213], v[68:71], v[108:111]\nv_mfma_f32_16x16x32_bf16 v[112:115], v[208:211], v[72:75], v[112:115]\nv_mfma_f32_16x16x32_bf16 v[116:119], v[210:213], v[72:75], v[116:119]\nv_mfma_f32_16x16x32_bf16 v[120:123], v[208:211], v[76:79], v[120:123]\n"
"v_mfma_f32_16x16x32_bf16 v[124:127], v[210:213], v[76:79], v[124:127]\nG 15\nv_pk_add_f32 v[96:97], v[96:97], v[128:129]\nG 14\nv_pk_add_f32 v[98:99], v[98:99], v[130:131]\nG 13\n"
"v_pk_add_f32 v[100:101], v[100:101], v[132:133]\nG 12\nv_pk_add_f32 v[102:103], v[102:103], v[134:135]\nv_cvt_pk_bf16_f32 v192, v96, v97\nv_cvt_pk_bf16_f32 v193, v98, v99\nv_cvt_pk_bf16_f32 v194, v100, v101\n"
"v_cvt_pk_bf16_f32 v195, v102, v103\nG 11\nv_pk_add_f32 v[104:105], v[104:105], v[136:137]\nG 10\nv_pk_add_f32 v[106:107], v[106:107], v[138:139]\nG 9\n"
"v_pk_add_f32 v[108:109], v[108:109], v[140:141]\nG 8\nv_pk_add_f32 v[110:111], v[110:111], v[142:143]\nv_cvt_pk_bf16_f32 v196, v104, v105\nv_cvt_pk_bf16_f32 v197, v106, v107\nv_cvt_pk_bf16_f32 v198, v108, v109\n"
"v_cvt_pk_bf16_f32 v199, v110, v111\nG 7\nv_pk_add_f32 v[112:113], v[112:113], v[144:145]\nG 6\nv_pk_add_f32 v[114:115], v[114:115], v[146:147]\nG 5\n"
"v_pk_add_f32 v[116:117], v[116:117], v[148:149]\nG 4\nv_pk_add_f32 v[118:119], v[118:119], v[150:151]\nv_cvt_pk_bf16_f32 v200, v112, v113\nv_cvt_pk_bf16_f32 v201, v114, v115\nv_cvt_pk_bf16_f32 v202, v116, v117\n"
"v_cvt_pk_bf16_f32 v203, v118, v119\nG 3\nv_pk_add_f32 v[120:121], v[120:121], v[152:153]\nG 2\nv_pk_add_f32 v[122:123], v[122:123], v[154:155]\nG 1\n"
"v_pk_add_f32 v[124:125], v[124:125], v[156:157]\nG 0\nv_pk_add_f32 v[126:127], v[126:127], v[158:159]\nv_cvt_pk_bf16_f32 v204, v120, v121\nv_cvt_pk_bf16_f32 v205, v122, v123\nv_cvt_pk_bf16_f32 v206, v124, v125\n"
"v_cvt_pk_bf16_f32 v207, v126, v127\nv_and_b32 v230, 0xf0, v252\nv_lshlrev_b32 v230, 7, v230\nv_bfe_u32 v231, v252, 8, 1\nv_lshl_add_u32 v230, v231, 5, v230\nv_lshrrev_b32 v231, 9, v252\n"
"v_lshl_add_u32 v230, v231, 4, v230\ns_add_u32 s61, s49, s53\ns_add_u32 s62, s61, 32768\nv_permlane16_swap_b32 v192, v194\nv_permlane16_swap_b32 v193, v195\nbuffer_store_dwordx4 v[192:195], v230, s[28:31], s61 offen offset:0\n"
"v_permlane16_swap_b32 v196, v198\nv_permlane16_swap_b32 v197, v199\nbuffer_store_dwordx4 v[196:199], v230, s[28:31], s61 offen offset:64\nv_permlane16_swap_b32 v200, v202\nv_permlane16_swap_b32 v201, v203\nbuffer_store_dwordx4 v[200:203], v230, s[28:31], s62 offen offset:0\n"
"v_permlane16_swap_b32 v204, v206\nv_permlane16_swap_b32 v205, v207\nbuffer_store_dwordx4 v[204:207], v230, s[28:31], s62 offen offset:64\ns_add_u32 s60, s83, 1\nv_mov_b32 v234, s60\ns_or_b32 s60, s45, s40\n"
"s_cmp_eq_u32 s60, 0\ns_cselect_b64 exec, 1, 0\nbuffer_store_dword v234, off, s[20:23], 0 sc1\ns_mov_b64 exec, -1\ns_endpgm\nLqF:\n"
"Lq610:\nv_mov_b32 v248, 0\nv_mov_b32 v249, 0\ns_add_u32 s61, s80, 88358912\ns_mov_b64 exec, 0xffff\nbuffer_atomic_or_x2 v[248:249], v250, s[20:23], s61 offen sc0 sc1\n"
"s_mov_b64 exec, -1\ns_waitcnt vmcnt(0)\nv_cmp_eq_u64 s[86:87], s[46:47], v[248:249]\ns_nop 1\ns_and_b32 s62, s86, 0xffff\ns_cmp_eq_u32 s62, 0xffff\n"
"s_cbranch_scc1 Lq611\ns_sleep 1\ns_sub_u32 s48, s48, 1\ns_cmp_gt_i32 s48, 0\ns_cbranch_scc1 Lq610\ns_branch Lq611\n"
"Lq672:\ns_lshr_b32 s84, s49, 3\ns_add_u32 s84, s84, 82837504\nbuffer_load_dwordx4 v[128:131], v252, s[20:23], s84 offen offset:0 sc1\nbuffer_load_dwordx4 v[132:135], v252, s[20:23], s84 offen offset:1024 sc1\nbuffer_load_dwordx4 v[136:139], v252, s[20:23], s84 offen offset:2048 sc1\n"
"buffer_load_dwordx4 v[140:143], v252, s[20:23], s84 offen offset:3072 sc1\ns_lshr_b32 s85, s49, 3\ns_add_u32 s85, s85, 82841600\nbuffer_load_dwordx4 v[144:147], v252, s[20:23], s85 offen offset:0 sc1\nbuffer_load_dwordx4 v[148:151], v252, s[20:23], s85 offen offset:1024 sc1\nbuffer_load_dwordx4 v[152:155], v252, s[20:23], s85 offen offset:2048 sc1\n"
"buffer_load_dwordx4 v[156:159], v252, s[20:23], s85 offen offset:3072 sc1\ns_waitcnt vmcnt(6)\nv_min3_u32 v230, v129, v131, v133\ns_waitcnt vmcnt(5)\nv_min3_u32 v230, v230, v135, v137\ns_waitcnt vmcnt(4)\n"
"v_min3_u32 v230, v230, v139, v141\ns_waitcnt vmcnt(3)\nv_min3_u32 v230, v230, v143, v145\ns_waitcnt vmcnt(2)\nv_min3_u32 v230, v230, v147, v149\ns_waitcnt vmcnt(1)\n"
"v_min3_u32 v230, v230, v151, v153\ns_waitcnt vmcnt(0)\nv_min3_u32 v230, v230, v155, v157\nv_min3_u32 v230, v230, v159, v159\nv_max3_u32 v231, v129, v131, v133\nv_max3_u32 v231, v231, v135, v137\n"
"v_max3_u32 v231, v231, v139, v141\nv_max3_u32 v231, v231, v143, v145\nv_max3_u32 v231, v231, v147, v149\nv_max3_u32 v231, v231, v151, v153\nv_max3_u32 v231, v231, v155, v157\nv_max3_u32 v231, v231, v159, v159\n"
"v_cmp_ne_u32 s[86:87], s46, v230\nv_cmp_ne_u32 s[88:89], s46, v231\ns_nop 0\ns_or_b64 s[86:87], s[86:87], s[88:89]\ns_cbranch_scc0 Lq673\ns_sub_u32 s48, s48, 1\n"
"s_cmp_gt_i32 s48, 0\ns_cbranch_scc1 Lq672\ns_branch Lq673\nLq696:\ns_mov_b64 exec, s[86:87]\nbuffer_load_dwordx4 v[230:233], v252, s[20:23], s65 offen offset:0 sc1\n"
"buffer_load_dwordx4 v[234:237], v252, s[20:23], s65 offen offset:1024 sc1\nbuffer_load_dwordx2 v[64:65], v227, s[20:23], s65 offen offset:2048 sc1\nbuffer_load_dwordx2 v[70:71], v227, s[20:23], s65 offen offset:2560 sc1\nbuffer_load_dwordx2 v[76:77], v227, s[20:23], s65 offen offset:3072 sc1\nbuffer_load_dwordx2 v[82:83], v227, s[20:23], s65 offen offset:3584 sc1\ns_mov_b64 exec, -1\n"
"s_waitcnt vmcnt(0)\nv_max3_u32 v248, v233, v237, v65\nv_max3_u32 v248, v248, v71, v77\nv_max_u32 v248, v248, v83\nv_cmp_eq_u32 vcc, -1, v248\ns_nop 1\n"
"s_cbranch_vccz Lq697\ns_sub_u32 s48, s48, 1\ns_cmp_gt_i32 s48, 0\ns_cbranch_scc1 Lq696\ns_branch Lq697\nLq725:\n"
"v_mov_b32 v248, 0\nv_mov_b32 v249, 0\ns_add_u32 s61, s80, 88498176\ns_mov_b64 exec, 0xffff\nbuffer_atomic_or_x2 v[248:249], v250, s[20:23], s61 offen sc0 sc1\ns_mov_b64 exec, -1\n"
"s_waitcnt vmcnt(0)\nv_cmp_eq_u64 s[86:87], s[46:47], v[248:249]\ns_nop 1\ns_and_b32 s62, s86, 0xffff\ns_cmp_eq_u32 s62, 0xffff\ns_cbranch_scc1 Lq726\n"
"s_sleep 1\ns_sub_u32 s48, s48, 1\ns_cmp_gt_i32 s48, 0\ns_cbranch_scc1 Lq725\ns_branch Lq726\nLq784:\n"
"v_mov_b32 v248, 0\nv_mov_b32 v249, 0\ns_add_u32 s61, s80, 88637440\ns_mov_b64 exec, 0xffff\nbuffer_atomic_or_x2 v[248:249], v250, s[20:23], s61 offen sc0 sc1\ns_mov_b64 exec, -1\n"
"s_waitcnt vmcnt(0)\nv_cmp_eq_u64 s[86:87], s[46:47], v[248:249]\ns_nop 1\ns_and_b32 s62, s86, 0xffff\ns_cmp_eq_u32 s62, 0xffff\ns_cbranch_scc1 Lq785\n"
"s_sleep 1\ns_sub_u32 s48, s48, 1\ns_cmp_gt_i32 s48, 0\ns_cbranch_scc1 Lq784\ns_branch Lq785\nLq841:\n"
"s_lshr_b32 s84, s49, 3\ns_add_u32 s84, s84, 83361792\nbuffer_load_dwordx4 v[0:3], v252, s[20:23], s84 offen offset:0 sc1\nbuffer_load_dwordx4 v[4:7], v252, s[20:23], s84 offen offset:1024 sc1\nbuffer_load_dwordx4 v[8:11], v252, s[20:23], s84 offen offset:2048 sc1\nbuffer_load_dwordx4 v[12:15], v252, s[20:23], s84 offen offset:3072 sc1\n"
"s_lshr_b32 s85, s49, 3\ns_add_u32 s85, s85, 83365888\nbuffer_load_dwordx4 v[16:19], v252, s[20:23], s85 offen offset:0 sc1\nbuffer_load_dwordx4 v[20:23], v252, s[20:23], s85 offen offset:1024 sc1\nbuffer_load_dwordx4 v[24:27], v252, s[20:23], s85 offen offset:2048 sc1\nbuffer_load_dwordx4 v[28:31], v252, s[20:23], s85 offen offset:3072 sc1\n"
"s_waitcnt vmcnt(6)\nv_min3_u32 v230, v1, v3, v5\ns_waitcnt vmcnt(5)\nv_min3_u32 v230, v230, v7, v9\ns_waitcnt vmcnt(4)\nv_min3_u32 v230, v230, v11, v13\n"
"s_waitcnt vmcnt(3)\nv_min3_u32 v230, v230, v15, v17\ns_waitcnt vmcnt(2)\nv_min3_u32 v230, v230, v19, v21\ns_waitcnt vmcnt(1)\nv_min3_u32 v230, v230, v23, v25\n"
"s_waitcnt vmcnt(0)\nv_min3_u32 v230, v230, v27, v29\nv_min3_u32 v230, v230, v31, v31\nv_max3_u32 v231, v1, v3, v5\nv_max3_u32 v231, v231, v7, v9\nv_max3_u32 v231, v231, v11, v13\n"
"v_max3_u32 v231, v231, v15, v17\nv_max3_u32 v231, v231, v19, v21\nv_max3_u32 v231, v231, v23, v25\nv_max3_u32 v231, v231, v27, v29\nv_max3_u32 v231, v231, v31, v31\nv_cmp_ne_u32 s[86:87], s46, v230\n"
"v_cmp_ne_u32 s[88:89], s46, v231\ns_nop 0\ns_or_b64 s[86:87], s[86:87], s[88:89]\ns_cbranch_scc0 Lq842\ns_sub_u32 s48, s48, 1\ns_cmp_gt_i32 s48, 0\n"
"s_cbranch_scc1 Lq841\ns_branch Lq842\nLq867:\nv_mov_b32 v248, 0\nv_mov_b32 v249, 0\ns_add_u32 s61, s82, 845824\n"
"s_mov_b64 exec, 0xf\nbuffer_atomic_or_x2 v[248:249], v250, s[20:23], s61 offen sc0 sc1\ns_mov_b64 exec, -1\ns_waitcnt vmcnt(0)\nv_cmp_eq_u64 s[86:87], s[46:47], v[248:249]\ns_nop 1\n"
"s_and_b32 s62, s86, 0xf\ns_cmp_eq_u32 s62, 0xf\ns_cbranch_scc1 Lq868\ns_sleep 1\ns_sub_u32 s48, s48, 1\ns_cmp_gt_i32 s48, 0\n"
"s_cbranch_scc1 Lq867\ns_branch Lq868\nLq902:\nv_mov_b32 v248, 0\nv_mov_b32 v249, 0\ns_add_u32 s61, s82, 845832\n"
"s_mov_b64 exec, 0xf\nbuffer_atomic_or_x2 v[248:249], v250, s[20:23], s61 offen sc0 sc1\ns_mov_b64 exec, -1\ns_waitcnt vmcnt(0)\nv_cmp_eq_u64 s[86:87], s[46:47], v[248:249]\ns_nop 1\n"
"s_and_b32 s62, s86, 0xf\ns_cmp_eq_u32 s62, 0xf\ns_cbranch_scc1 Lq903\ns_sleep 1\ns_sub_u32 s48, s48, 1\ns_cmp_gt_i32 s48, 0\n"
"s_cbranch_scc1 Lq902\ns_branch Lq903\nLq937:\nv_mov_b32 v248, 0\nv_mov_b32 v249, 0\ns_add_u32 s61, s82, 845848\n"
"s_mov_b64 exec, 0xf\nbuffer_atomic_or_x2 v[248:249], v250, s[20:23], s61 offen sc0 sc1\ns_mov_b64 exec, -1\ns_waitcnt vmcnt(0)\nv_cmp_eq_u64 s[86:87], s[46:47], v[248:249]\ns_nop 1\n"
"s_and_b32 s62, s86, 0xf\ns_cmp_eq_u32 s62, 0xf\ns_cbranch_scc1 Lq938\ns_sleep 1\ns_sub_u32 s48, s48, 1\ns_cmp_gt_i32 s48, 0\n"
"s_cbranch_scc1 Lq937\ns_branch Lq938\nLq956:\nv_mov_b32 v248, 0\nv_mov_b32 v249, 0\ns_add_u32 s61, s82, 845840\n"
"s_mov_b64 exec, 0xf\nbuffer_atomic_or_x2 v[248:249], v250, s[20:23], s61 offen sc0 sc1\ns_mov_b64 exec, -1\ns_waitcnt vmcnt(0)\nv_cmp_eq_u64 s[86:87], s[46:47], v[248:249]\ns_nop 1\n"
"s_and_b32 s62, s86, 0xf\ns_cmp_eq_u32 s62, 0xf\ns_cbranch_scc1 Lq957\ns_sleep 1\ns_sub_u32 s48, s48, 1\ns_cmp_gt_i32 s48, 0\n"
"s_cbranch_scc1 Lq956\ns_branch Lq957\nLq998:\nv_mov_b32 v248, 0\nv_mov_b32 v249, 0\ns_add_u32 s61, s74, 1009664\n"
"s_mov_b64 exec, 3\nbuffer_atomic_or_x2 v[248:249], v250, s[20:23], s61 offen sc0 sc1\ns_mov_b64 exec, -1\ns_waitcnt vmcnt(0)\nv_cmp_eq_u64 s[86:87], s[46:47], v[248:249]\ns_nop 1\n"
"s_and_b32 s62, s86, 3\ns_cmp_eq_u32 s62, 3\ns_cbranch_scc1 Lq999\ns_sleep 1\ns_sub_u32 s48, s48, 1\ns_cmp_gt_i32 s48, 0\n"
"s_cbranch_scc1 Lq998\ns_branch Lq999\nLq1021:\ns_mul_i32 s66, s43, 49152\ns_add_u32 s66, s66, s72\ns_add_u32 s66, s66, 65044480\n"
"buffer_load_dwordx4 v[64:67], v252, s[20:23], s66 offen offset:0 sc1\nbuffer_load_dwordx4 v[68:71], v252, s[20:23], s66 offen offset:1024 sc1\nbuffer_load_dwordx4 v[72:75], v252, s[20:23], s66 offen offset:2048 sc1\nbuffer_load_dwordx4 v[76:79], v252, s[20:23], s66 offen offset:3072 sc1\ns_waitcnt vmcnt(0)\nv_max3_u32 v248, v67, v71, v75\n"
"v_max_u32 v248, v248, v79\nv_cmp_eq_u32 vcc, -1, v248\ns_nop 1\ns_cbranch_vccz Lq1022\ns_sub_u32 s48, s48, 1\ns_cmp_gt_i32 s48, 0\n"
"s_cbranch_scc1 Lq1021\ns_branch Lq1022\nLqE:\n"
  : "+{v0}"(tid), "+{s[4:5]}"(wqkv), "+{s[8:9]}"(wo), "+{s[12:13]}"(w1), "+{s[16:17]}"(w2), "+{s[20:21]}"(ws), "+{s[24:25]}"(x), "+{s[28:29]}"(out), "+{s[32:33]}"(b1), "+{s[36:37]}"(b2), "+{s45}"(bid) :
  : "a0", "a1", "a2", "a3", "a4", "a5", "a6", "a7", "a8", "a9", "a10", "a11", "a12", "a13", "a14", "a15", "a16", "a17", "a18", "a19", "a20", "a21", "a22", "a23", "a24", "a25", "a26", "a27", "a28", "a29", "a30", "a31", "a32", "a33", "a34", "a35", "a36", "a37", "a38", "a39", "a40", "a41", "a42", "a43", "a44", "a45", "a46", "a47", "a48", "a49", "a50", "a51", "a52", "a53", "a54", "a55", "a56", "a57", "a58", "a59", "a60", "a61", "a62", "a63", "a64", "a65", "a66", "a67", "a68", "a69", "a70", "a71", "a72", "a73", "a74", "a75", "a76", "a77", "a78", "a79", "a80", "a81", "a82", "a83", "a84", "a85", "a86", "a87", "a88", "a89", "a90", "a91", "a92", "a93", "a94", "a95", "a96", "a97", "a98", "a99", "a100", "a101", "a102", "a103", "a104", "a105", "a106", "a107", "a108", "a109", "a110", "a111", "a112", "a113", "a114", "a115", "a116", "a117", "a118", "a119", "a120", "a121", "a122", "a123", "a124", "a125", "a126", "a127", "a128", "a129", "a130", "a131", "a132", "a133", "a134", "a135", "a136", "a137", "a138", "a139", "a140", "a141", "a142", "a143", "a144", "a145", "a146", "a147", "a148", "a149", "a150", "a151", "a152", "a153", "a154", "a155", "a156", "a157", "a158", "a159", "a160", "a161", "a162", "a163", "a164", "a165", "a166", "a167", "a168", "a169", "a170", "a171", "a172", "a173", "a174", "a175", "a176", "a177", "a178", "a179", "a180", "a181", "a182", "a183", "a184", "a185", "a186", "a187", "a188", "a189", "a190", "a191", "a192", "a193", "a194", "a195", "a196", "a197", "a198", "a199", "a200", "a201", "a202", "a203", "a204", "a205", "a206", "a207", "a208", "a209", "a210", "a211", "a212", "a213", "a214", "a215", "a216", "a217", "a218", "a219", "a220", "a221", "a222", "a223", "a224", "a225", "a226", "a227", "a228", "a229", "a230", "a231", "a232", "a233", "a234", "a235", "a236", "a237", "a238", "a239", "a240", "a241", "a242", "a243", "a244", "a245", "a246", "a247", "a248", "a249", "a250", "a251", "a252", "a253", "a254", "a255", "v1", "v2", "v3", "v4", "v5", "v6", "v7", "v8", "v9", "v10", "v11", "v12", "v13", "v14", "v15", "v16", "v17", "v18", "v19", "v20", "v21", "v22", "v23", "v24", "v25", "v26", "v27", "v28", "v29", "v30", "v31", "v32", "v33", "v34", "v35", "v36", "v37", "v38", "v39", "v40", "v41", "v42", "v43", "v44", "v45", "v46", "v47", "v48", "v49", "v50", "v51", "v52", "v53", "v54", "v55", "v56", "v57", "v58", "v59", "v60", "v61", "v62", "v63", "v64", "v65", "v66", "v67", "v68", "v69", "v70", "v71", "v72", "v73", "v74", "v75", "v76", "v77", "v78", "v79", "v80", "v81", "v82", "v83", "v84", "v85", "v86", "v87", "v88", "v89", "v90", "v91", "v92", "v93", "v94", "v95", "v96", "v97", "v98", "v99", "v100", "v101", "v102", "v103", "v104", "v105", "v106", "v107", "v108", "v109", "v110", "v111", "v112", "v113", "v114", "v115", "v116", "v117", "v118", "v119", "v120", "v121", "v122", "v123", "v124", "v125", "v126", "v127", "v128", "v129", "v130", "v131", "v132", "v133", "v134", "v135", "v136", "v137", "v138", "v139", "v140", "v141", "v142", "v143", "v144", "v145", "v146", "v147", "v148", "v149", "v150", "v151", "v152", "v153", "v154", "v155", "v156", "v157", "v158", "v159", "v160", "v161", "v162", "v163", "v164", "v165", "v166", "v167", "v168", "v169", "v170", "v171", "v172", "v173", "v174", "v175", "v176", "v177", "v178", "v179", "v180", "v181", "v182", "v183", "v184", "v185", "v186", "v187", "v188", "v189", "v190", "v191", "v192", "v193", "v194", "v195", "v196", "v197", "v198", "v199", "v200", "v201", "v202", "v203", "v204", "v205", "v206", "v207", "v208", "v209", "v210", "v211", "v212", "v213", "v214", "v215", "v216", "v217", "v218", "v219", "v220", "v221", "v222", "v223", "v224", "v225", "v226", "v227", "v228", "v229", "v230", "v231", "v232", "v233", "v234", "v235", "v236", "v237", "v238", "v239", "v240", "v241", "v242", "v243", "v244", "v245", "v246", "v247", "v248", "v249", "v250", "v251", "v252", "v253", "v254", "v255", "s6", "s7", "s10", "s11", "s14", "s15", "s18", "s19", "s22", "s23", "s26", "s27", "s30", "s31", "s34", "s35", "s38", "s39", "s40", "s41", "s42", "s43", "s44", "s46", "s47", "s48", "s49", "s50", "s51", "s52", "s53", "s54", "s55", "s56", "s57", "s58", "s59", "s60", "s61", "s62", "s63", "s64", "s65", "s66", "s67", "s68", "s69", "s70", "s71", "s72", "s73", "s74", "s75", "s76", "s77", "s78", "s79", "s80", "s81", "s82", "s83", "s84", "s85", "s86", "s87", "s88", "s89", "s90", "s91", "s92", "s93", "s94", "s95", "s96", "s97", "s98", "s99", "s100", "s101", "vcc", "m0", "scc", "memory");
#endif
}
extern "C" void kernel_launch(void* const* d_in, const int* in_sizes, int n_in, void* d_out, int out_size, void* d_ws, size_t ws_size, hipStream_t stream) {
  (void)hipFuncSetAttribute((const void*)layers, hipFuncAttributeMaxDynamicSharedMemorySize, 160 * 1024);
  hipLaunchKernelGGL(layers, dim3(256), dim3(256), 160 * 1024, stream, d_in[0], d_in[1], d_in[2], d_in[4], d_in[5], d_in[6], d_in[7], d_out, d_ws);
}
```

```cpp
#include <hip/hip_runtime.h>
__global__ __launch_bounds__(256, 1) void layers(const void* x, const void* wqkv, const void* wo, const void* w1, const void* b1, const void* w2, const void* b2, void* out, void* ws) {
#if defined(__HIP_DEVICE_COMPILE__)
  int tid = threadIdx.x, bid = blockIdx.x;
  unsigned long long ka = (unsigned long long)__builtin_amdgcn_kernarg_segment_ptr();
  asm volatile(
".macro M d,w,a\nv_mfma_f32_16x16x32_bf16 v[\\d:\\d+3], v[\\w:\\w+3], a[\\a:\\a+3], v[\\d:\\d+3]\n.endm\n.macro N d,a,w\nv_mfma_f32_16x16x32_bf16 v[\\d:\\d+3], a[\\a:\\a+3], v[\\w:\\w+3], v[\\d:\\d+3]\n.endm\n"
".macro R d,b,o\nds_read_b64_tr_b16 v[\\d:\\d+1], v\\b offset:\\o\n.endm\n.macro G n\ns_waitcnt lgkmcnt(\\n)\n.endm\n"
"s_load_dwordx2 s[24:25], s[92:93], 0\ns_load_dwordx2 s[4:5], s[92:93], 8\ns_load_dwordx2 s[8:9], s[92:93], 16\ns_load_dwordx2 s[12:13], s[92:93], 24\ns_load_dwordx2 s[32:33], s[92:93], 32\ns_load_dwordx2 s[16:17], s[92:93], 40\n"
"s_load_dwordx2 s[36:37], s[92:93], 48\ns_load_dwordx2 s[28:29], s[92:93], 56\ns_load_dwordx2 s[20:21], s[92:93], 64\nv_lshrrev_b32 v1, 6, v0\nv_and_b32 v0, 63, v0\nv_readfirstlane_b32 s40, v1\n"
"v_lshlrev_b32 v252, 4, v0\ns_getpc_b64 s[98:99]\nLqPC:\ns_lshr_b32 s60, s45, 3\ns_lshl2_add_u32 s60, s60, s40\ns_lshl_b32 s60, s60, 8\n"
"v_lshl_add_u32 v224, v252, 3, s60\nv_add_u32 v224, LqPC-LqPC, v224\ns_mov_b64 exec, 3\nglobal_load_dword v224, v224, s[98:99]\ns_mov_b64 exec, -1\ns_mov_b32 s6, 6291456\n"
"s_mov_b32 s7, 0x20000\ns_mov_b32 s10, 2097152\ns_mov_b32 s11, 0x20000\ns_mov_b32 s14, 7340032\ns_mov_b32 s15, 0x20000\ns_mov_b32 s18, 7340032\n"
"s_mov_b32 s19, 0x20000\ns_mov_b32 s22, 89128960\ns_mov_b32 s23, 0x20000\ns_mov_b32 s26, 4194304\ns_mov_b32 s27, 0x20000\ns_mov_b32 s30, 4194304\n"
"s_mov_b32 s31, 0x20000\ns_mov_b32 s34, 7168\ns_mov_b32 s35, 0x20000\ns_mov_b32 s38, 2048\ns_mov_b32 s39, 0x20000\ns_bfe_u32 s41, s45, 0x30003\n"
"s_and_b32 s60, s45, 1\ns_lshl3_add_u32 s41, s60, s41\ns_bfe_u32 s43, s45, 0x20001\ns_lshr_b32 s44, s45, 6\ns_lshl2_add_u32 s42, s43, s44\ns_lshl2_add_u32 s75, s44, s43\n"
"s_and_b32 s90, s40, 1\ns_lshl2_add_u32 s60, s41, s40\ns_lshl_b32 s49, s60, 16\ns_lshl_b32 s50, s40, 11\ns_add_u32 s50, s50, 32768\ns_lshl_b32 s52, s42, 7\n"
"s_lshl_b32 s51, s40, 13\ns_lshl_b32 s60, s90, 10\ns_add_u32 s70, s49, s60\ns_xor_b32 s71, s70, 0x10000\ns_mul_i32 s101, s41, 196608\ns_add_u32 s101, s101, s52\n"
"s_lshl_b32 s53, s75, 7\nLqq0:\nv_lshrrev_b32 v96, 7, v252\nv_lshlrev_b32 v96, 11, v96\nv_lshrrev_b32 v97, 8, v252\nv_lshlrev_b32 v97, 4, v97\n"
"v_and_b32 v98, 0x70, v252\nv_xor_b32 v98, v98, v97\nv_add_u32 v226, v96, v98\nv_xor_b32 v227, 64, v226\nG 0\ns_add_u32 m0, s51, 0\n"
"s_add_u32 s60, s70, 0\nbuffer_load_dwordx4 v226, s[24:27], s60 offen lds\ns_add_u32 m0, s51, 1024\ns_add_u32 s60, s70, 16384\nbuffer_load_dwordx4 v227, s[24:27], s60 offen lds\ns_add_u32 m0, s51, 2048\n"
"s_add_u32 s60, s70, 32768\nbuffer_load_dwordx4 v226, s[24:27], s60 offen lds\ns_add_u32 m0, s51, 3072\ns_add_u32 s60, s70, 49152\nbuffer_load_dwordx4 v227, s[24:27], s60 offen lds\ns_add_u32 m0, s51, 4096\n"
"s_add_u32 s60, s71, 0\nbuffer_load_dwordx4 v226, s[24:27], s60 offen lds\ns_add_u32 m0, s51, 5120\ns_add_u32 s60, s71, 16384\nbuffer_load_dwordx4 v227, s[24:27], s60 offen lds\ns_add_u32 m0, s51, 6144\n"
"s_add_u32 s60, s71, 32768\nbuffer_load_dwordx4 v226, s[24:27], s60 offen lds\ns_add_u32 m0, s51, 7168\ns_add_u32 s60, s71, 49152\nbuffer_load_dwordx4 v227, s[24:27], s60 offen lds\nbuffer_load_dwordx2 v[230:231], off, s[20:23], 0 sc1\n"
"v_bfe_u32 v96, v252, 5, 2\nv_bfe_u32 v97, v252, 8, 2\nv_xor_b32 v96, v96, v97\ns_and_b32 s60, s40, 1\ns_lshl_b32 s60, s60, 4\ns_and_b32 s61, s40, 2\n"
"s_lshl1_add_u32 s60, s61, s60\nv_bfe_u32 v97, v252, 7, 2\nv_lshrrev_b32 v98, 9, v252\nv_lshl_add_u32 v97, v98, 3, v97\nv_add_u32 v97, s60, v97\nv_mul_u32_u24 v97, 6144, v97\n"
"v_lshl_add_u32 v97, v96, 5, v97\nv_and_b32 v96, 16, v252\nv_add_u32 v220, v96, v97\nv_add_u32 v221, 3144704, v220\ns_add_u32 m0, s50, 0\ns_add_u32 s63, s52, 2048\n"
"buffer_load_dwordx4 v220, s[4:7], s63 offen lds\nbuffer_load_dwordx4 v221, s[4:7], s63 offen offset:1024 lds\ns_add_u32 m0, s50, 8192\ns_add_u32 s63, s52, 198656\nbuffer_load_dwordx4 v220, s[4:7], s63 offen lds\nbuffer_load_dwordx4 v221, s[4:7], s63 offen offset:1024 lds\n"
"s_lshl2_add_u32 s60, s41, s40\ns_mul_i32 s79, s60, 2176\ns_and_b32 s61, s60, -2\ns_mul_i32 s80, s61, 2176\ns_lshl2_add_u32 s61, s61, s43\ns_mul_i32 s82, s61, 640\n"
"s_mul_i32 s61, s43, 28672\ns_mul_i32 s73, s60, 229376\ns_add_u32 s73, s73, s61\ns_mul_i32 s62, s90, 14336\ns_add_u32 s68, s73, s62\ns_add_u32 s61, s61, s62\n"
"s_xor_b32 s62, s60, 1\ns_mul_i32 s69, s62, 229376\ns_add_u32 s69, s69, s61\ns_mul_i32 s61, s44, 7168\ns_add_u32 s73, s73, s61\ns_lshl2_add_u32 s61, s60, s43\n"
"s_mul_i32 s81, s61, 640\ns_lshl2_add_u32 s61, s60, s44\ns_mul_i32 s74, s61, 640\ns_lshl2_add_u32 s61, s41, s44\ns_mul_i32 s61, s61, 48\ns_add_u32 s61, s61, s40\n"
"s_lshl_b32 s72, s61, 12\ns_mul_i32 s55, s42, 448\ns_add_u32 s54, s55, 64\ns_and_b32 s54, s54, 0xffffff80\ns_bitcmp0_b32 s42, 0\ns_cselect_b32 s61, 384, 0\n"
"s_add_u32 s55, s55, s61\ns_lshl1_add_u32 s61, s41, s90\ns_lshl_b32 s61, s61, 9\ns_lshl_b32 s91, s42, 14\ns_add_u32 s91, s91, s61\ns_add_u32 s91, s91, 83886080\n"
"s_mul_i32 s61, s43, 1835008\ns_lshl_b32 s62, s44, 9\ns_add_u32 s61, s61, s62\ns_add_u32 s62, s43, 1\ns_and_b32 s62, s62, 3\ns_lshl_b32 s62, s62, 7\n"
"s_add_u32 s56, s61, s62\ns_add_u32 s62, s43, 2\ns_and_b32 s62, s62, 3\ns_lshl_b32 s62, s62, 7\ns_add_u32 s57, s61, s62\ns_add_u32 s62, s43, 3\n"
"s_and_b32 s62, s62, 3\ns_lshl_b32 s62, s62, 7\ns_add_u32 s58, s61, s62\ns_add_u32 s62, s43, 4\ns_and_b32 s62, s62, 3\ns_lshl_b32 s62, s62, 7\n"
"s_add_u32 s59, s61, s62\ns_mov_b32 s76, 0x3f803f80\ns_mov_b32 s77, 0xffff0000\ns_mov_b32 s48, 131072\ns_cmp_eq_u32 s60, 0\ns_cselect_b32 s78, 0xf149f2ca, 0\n"
"v_bfe_u32 v96, v252, 6, 3\nv_lshlrev_b32 v96, 7, v96\nv_lshrrev_b32 v97, 9, v252\nv_lshl_add_u32 v96, v97, 11, v96\nv_bfe_u32 v97, v252, 4, 2\nv_lshl_add_u32 v96, v97, 3, v96\n"
"s_lshl_b32 s60, s90, 10\ns_add_u32 s60, s60, 32768\nv_add_u32 v96, s60, v96\nv_bfe_u32 v97, v252, 7, 2\nv_xor_b32 v98, 0, v97\nv_lshl_add_u32 v184, v98, 5, v96\n"
"v_add_u32 v188, 0x10000, v184\nv_xor_b32 v98, 1, v97\nv_lshl_add_u32 v185, v98, 5, v96\nv_add_u32 v189, 0x10000, v185\nv_xor_b32 v98, 2, v97\nv_lshl_add_u32 v186, v98, 5, v96\n"
"v_add_u32 v190, 0x10000, v186\nv_xor_b32 v98, 3, v97\nv_lshl_add_u32 v187, v98, 5, v96\nv_add_u32 v191, 0x10000, v187\nv_lshrrev_b32 v96, 2, v252\ns_xor_b32 s60, s51, 0x2000\n"
"v_add_u32 v225, s60, v96\nv_mov_b32 v210, 0\nv_mov_b32 v211, 0\nv_bfe_u32 v96, v252, 5, 2\nv_bfe_u32 v97, v252, 8, 2\nv_xor_b32 v96, v96, v97\n"
"v_lshrrev_b32 v97, 7, v252\ns_lshl_b32 s60, s40, 3\nv_add_u32 v97, s60, v97\nv_mov_b32 v228, v97\nv_and_b32 v97, 16, v252\nv_lshl_add_u32 v229, v96, 5, v97\n"
"v_bfe_u32 v96, v252, 5, 2\nv_bfe_u32 v97, v252, 8, 2\nv_xor_b32 v96, v96, v97\nv_lshrrev_b32 v97, 7, v252\ns_lshl_b32 s60, s40, 3\nv_add_u32 v97, s60, v97\n"
"v_lshrrev_b32 v98, 1, v96\nv_lshl_add_u32 v97, v98, 5, v97\nv_and_b32 v96, 1, v96\nv_mul_u32_u24 v97, 7168, v97\nv_lshl_add_u32 v97, v96, 5, v97\nv_and_b32 v96, 16, v252\n"
"v_add_u32 v222, v96, v97\nv_add_u32 v223, 3668992, v222\nv_bfe_u32 v96, v252, 4, 4\nv_lshrrev_b32 v97, 8, v252\nv_lshrrev_b32 v98, 1, v96\nv_xor_b32 v97, v97, v98\n"
"v_lshlrev_b32 v96, 7, v96\nv_lshl_add_u32 v96, v97, 4, v96\nv_add_u32 v248, s51, v96\nv_xor_b32 v249, 64, v248\nv_add_u32 v250, 122880, v248\nv_add_u32 v251, 122880, v249\n"
"s_add_u32 m0, s51, 122880\ns_add_u32 s60, s70, 128\nbuffer_load_dwordx4 v226, s[24:27], s60 offen lds\ns_add_u32 m0, s51, 123904\ns_add_u32 s60, s70, 16512\nbuffer_load_dwordx4 v227, s[24:27], s60 offen lds\n"
"s_add_u32 m0, s51, 124928\ns_add_u32 s60, s70, 32896\nbuffer_load_dwordx4 v226, s[24:27], s60 offen lds\ns_add_u32 m0, s51, 125952\ns_add_u32 s60, s70, 49280\nbuffer_load_dwordx4 v227, s[24:27], s60 offen lds\n"
"s_add_u32 m0, s51, 126976\ns_add_u32 s60, s71, 128\nbuffer_load_dwordx4 v226, s[24:27], s60 offen lds\ns_add_u32 m0, s51, 128000\ns_add_u32 s60, s71, 16512\nbuffer_load_dwordx4 v227, s[24:27], s60 offen lds\n"
"s_add_u32 m0, s51, 129024\ns_add_u32 s60, s71, 32896\nbuffer_load_dwordx4 v226, s[24:27], s60 offen lds\ns_add_u32 m0, s51, 130048\ns_add_u32 s60, s71, 49280\nbuffer_load_dwordx4 v227, s[24:27], s60 offen lds\n"
"s_add_u32 m0, s50, 16384\ns_add_u32 s63, s52, 4096\nbuffer_load_dwordx4 v220, s[4:7], s63 offen lds\nbuffer_load_dwordx4 v221, s[4:7], s63 offen offset:1024 lds\ns_add_u32 m0, s50, 24576\ns_add_u32 s63, s52, 200704\n"
"buffer_load_dwordx4 v220, s[4:7], s63 offen lds\nbuffer_load_dwordx4 v221, s[4:7], s63 offen offset:1024 lds\ns_add_u32 m0, s50, 32768\ns_add_u32 s63, s101, 2048\nbuffer_load_dwordx4 v220, s[4:7], s63 offen lds\nbuffer_load_dwordx4 v221, s[4:7], s63 offen offset:1024 lds\n"
"s_add_u32 m0, s50, 40960\ns_add_u32 s63, s101, 4096\nbuffer_load_dwordx4 v220, s[4:7], s63 offen lds\nbuffer_load_dwordx4 v221, s[4:7], s63 offen offset:1024 lds\nv_bfe_u32 v100, v252, 4, 4\nv_lshlrev_b32 v100, 11, v100\n"
"v_lshrrev_b32 v101, 8, v252\nv_lshl_add_u32 v100, v101, 3, v100\ns_add_u32 s60, s49, s53\ns_add_u32 s61, s60, 32768\nbuffer_load_dwordx2 v[192:193], v100, s[24:27], s60 offen offset:0\nbuffer_load_dwordx2 v[194:195], v100, s[24:27], s60 offen offset:32\n"
"buffer_load_dwordx2 v[196:197], v100, s[24:27], s60 offen offset:64\nbuffer_load_dwordx2 v[198:199], v100, s[24:27], s60 offen offset:96\nbuffer_load_dwordx2 v[200:201], v100, s[24:27], s61 offen offset:0\nbuffer_load_dwordx2 v[202:203], v100, s[24:27], s61 offen offset:32\nbuffer_load_dwordx2 v[204:205], v100, s[24:27], s61 offen offset:64\nbuffer_load_dwordx2 v[206:207], v100, s[24:27], s61 offen offset:96\n"
"v_mov_b32 v106, 0xf149f149\nv_mov_b32 v104, 0xffff\nv_mov_b32 v105, 0xffff0000\ns_mov_b32 s60, 0x50000\ns_mov_b32 s61, 0x5550055\nv_cndmask_b32 v96, 0, v104, s[60:61]\n"
"s_mov_b32 s60, 0xa0000\ns_mov_b32 s61, 0xaaa00aa\nv_cndmask_b32 v96, v96, v105, s[60:61]\nv_bfi_b32 v100, v96, 0, v106\ns_mov_b32 s60, 0x150001\ns_mov_b32 s61, 0x15550155\n"
"v_cndmask_b32 v97, 0, v104, s[60:61]\ns_mov_b32 s60, 0x2a0002\ns_mov_b32 s61, 0x2aaa02aa\nv_cndmask_b32 v97, v97, v105, s[60:61]\nv_bfi_b32 v101, v97, 0, v106\ns_mov_b32 s60, 0x55505555\n"
"s_mov_b32 s61, 0x50005500\nv_cndmask_b32 v98, 0, v104, s[60:61]\ns_mov_b32 s60, 0xaaa0aaaa\ns_mov_b32 s61, 0xa000aa00\nv_cndmask_b32 v98, v98, v105, s[60:61]\nv_bfi_b32 v102, v98, 0, v106\n"
"s_mov_b32 s60, 0x55405554\ns_mov_b32 s61, 0x40005400\nv_cndmask_b32 v99, 0, v104, s[60:61]\ns_mov_b32 s60, 0xaa80aaa8\ns_mov_b32 s61, 0x8000a800\nv_cndmask_b32 v99, v99, v105, s[60:61]\n"
"v_bfi_b32 v103, v99, 0, v106\nv_add_u32 v104, 161792, v252\nds_write_b128 v104, v[96:99] offset:0\nds_write_b128 v104, v[100:103] offset:1024\ns_waitcnt vmcnt(28)\nv_readfirstlane_b32 s83, v230\n"
"v_readfirstlane_b32 s60, v231\ns_mul_i32 s46, s83, 0x9e3779b1\ns_add_u32 s46, s46, 0x7f4a7c15\ns_xor_b32 s47, s83, s60\ns_mul_i32 s47, s47, 0x85ebca6b\ns_add_u32 s47, s47, 0xc2b2ae35\n"
"v_mov_b32 v144, -1\nv_mov_b32 v145, -1\nv_mov_b32 v146, -1\nv_mov_b32 v147, -1\nds_read_b128 a[0:3], v248 offset:0\nds_read_b128 a[4:7], v248 offset:2048\n"
"ds_read_b128 a[8:11], v248 offset:4096\nds_read_b128 a[12:15], v248 offset:6144\nds_read_b128 a[16:19], v249 offset:0\nds_read_b128 a[20:23], v249 offset:2048\nds_read_b128 a[24:27], v249 offset:4096\nds_read_b128 a[28:31], v249 offset:6144\n"
"s_waitcnt vmcnt(12) lgkmcnt(8)\ns_barrier\nR 160,184,0\nR 162,184,4096\nR 164,184,8192\nR 166,184,12288\n"
"R 168,185,0\nR 170,185,4096\nR 172,185,8192\nR 174,185,12288\nR 176,186,0\nR 178,186,4096\n"
"R 180,186,8192\nR 182,186,12288\nG 10\nv_mfma_f32_16x16x32_bf16 v[0:3], v[160:163], a[0:3], 0\ns_add_u32 m0, s50, 49152\ns_add_u32 s63, s52, 395264\n"
"buffer_load_dwordx4 v220, s[4:7], s63 offen lds\nG 8\nM 0,164,16\nbuffer_load_dwordx4 v221, s[4:7], s63 offen offset:1024 lds\nv_mfma_f32_16x16x32_bf16 v[16:19], v[160:163], a[4:7], 0\ns_add_u32 m0, s51, 0\n"
"s_add_u32 s60, s70, 256\nbuffer_load_dwordx4 v226, s[24:27], s60 offen lds\nM 16,164,20\ns_lshl4_add_u32 s62, s41, s42\ns_lshl_b32 s62, s62, 12\ns_add_u32 s62, s62, 77594624\n"
"s_lshl_b32 s60, s40, 10\ns_add_u32 s62, s62, s60\nbuffer_store_dwordx4 v[144:147], v252, s[20:23], s62 offen sc1\ns_add_u32 m0, s50, 57344\ns_add_u32 s63, s52, 591872\nv_mfma_f32_16x16x32_bf16 v[32:35], v[160:163], a[8:11], 0\n"
"buffer_load_dwordx4 v220, s[4:7], s63 offen lds\nM 32,164,24\nbuffer_load_dwordx4 v221, s[4:7], s63 offen offset:1024 lds\nv_mfma_f32_16x16x32_bf16 v[48:51], v[160:163], a[12:15], 0\ns_add_u32 m0, s51, 1024\ns_add_u32 s60, s70, 16640\n"
"buffer_load_dwordx4 v227, s[24:27], s60 offen lds\nM 48,164,28\ns_add_u32 m0, s50, 65536\ns_add_u32 s63, s52, 397312\nbuffer_load_dwordx4 v220, s[4:7], s63 offen lds\nG 4\n"
"v_mfma_f32_16x16x32_bf16 v[52:55], v[168:171], a[12:15], 0\nR 160,187,0\nM 52,172,28\nbuffer_load_dwordx4 v221, s[4:7], s63 offen offset:1024 lds\nv_mfma_f32_16x16x32_bf16 v[36:39], v[168:171], a[8:11], 0\nR 162,187,4096\n"
"M 36,172,24\ns_add_u32 m0, s51, 2048\ns_add_u32 s60, s70, 33024\nbuffer_load_dwordx4 v226, s[24:27], s60 offen lds\nv_mfma_f32_16x16x32_bf16 v[20:23], v[168:171], a[4:7], 0\nR 164,187,8192\n"
"s_add_u32 m0, s50, 73728\nM 20,172,20\ns_lshl4_add_u32 s62, s41, s42\ns_lshl_b32 s62, s62, 12\ns_add_u32 s62, s62, 78643200\ns_lshl_b32 s60, s40, 10\n"
"s_add_u32 s62, s62, s60\nbuffer_store_dwordx4 v[144:147], v252, s[20:23], s62 offen sc1\ns_add_u32 s63, s52, 593920\nv_mfma_f32_16x16x32_bf16 v[4:7], v[168:171], a[0:3], 0\nR 166,187,12288\nbuffer_load_dwordx4 v220, s[4:7], s63 offen lds\n"
"M 4,172,16\nG 4\nv_mfma_f32_16x16x32_bf16 v[8:11], v[176:179], a[0:3], 0\nR 168,184,16384\nM 8,180,16\ns_add_u32 m0, s51, 3072\n"
"s_add_u32 s60, s70, 49408\nbuffer_load_dwordx4 v227, s[24:27], s60 offen lds\nv_mfma_f32_16x16x32_bf16 v[24:27], v[176:179], a[4:7], 0\nR 170,184,20480\ns_add_u32 m0, s50, 73728\ns_nop 0\n"
"buffer_load_dwordx4 v221, s[4:7], s63 offen offset:1024 lds\nM 24,180,20\ns_add_u32 m0, s51, 4096\ns_add_u32 s60, s71, 256\nbuffer_load_dwordx4 v226, s[24:27], s60 offen lds\nv_mfma_f32_16x16x32_bf16 v[40:43], v[176:179], a[8:11], 0\n"
"R 172,184,24576\nM 40,180,24\nv_mfma_f32_16x16x32_bf16 v[56:59], v[176:179], a[12:15], 0\nR 174,184,28672\nM 56,180,28\nG 4\n"
"v_mfma_f32_16x16x32_bf16 v[60:63], v[160:163], a[12:15], 0\nR 176,185,16384\nM 60,164,28\ns_add_u32 m0, s51, 5120\ns_add_u32 s60, s71, 16640\nbuffer_load_dwordx4 v227, s[24:27], s60 offen lds\n"
"v_mfma_f32_16x16x32_bf16 v[44:47], v[160:163], a[8:11], 0\nR 178,185,20480\nM 44,164,24\ns_add_u32 m0, s51, 6144\ns_add_u32 s60, s71, 33024\nbuffer_load_dwordx4 v226, s[24:27], s60 offen lds\n"
"v_mfma_f32_16x16x32_bf16 v[28:31], v[160:163], a[4:7], 0\nR 180,185,24576\ns_add_u32 m0, s50, 81920\ns_add_u32 s63, s101, 0\nM 28,164,20\nbuffer_load_dwordx4 v220, s[4:7], s63 offen lds\n"
"v_mfma_f32_16x16x32_bf16 v[12:15], v[160:163], a[0:3], 0\nR 182,185,28672\nM 12,164,16\ns_waitcnt vmcnt(12)\ns_barrier\nG 4\n"
"v_mfma_f32_16x16x32_bf16 v[64:67], a[0:3], v[168:171], 0\nR 160,186,16384\nN 64,16,172\ns_add_u32 m0, s51, 7168\ns_add_u32 s60, s71, 49408\nbuffer_load_dwordx4 v227, s[24:27], s60 offen lds\n"
"v_mfma_f32_16x16x32_bf16 v[68:71], a[4:7], v[168:171], 0\nR 162,186,20480\ns_add_u32 m0, s50, 81920\nN 68,20,172\nbuffer_load_dwordx4 v221, s[4:7], s63 offen offset:1024 lds\nv_mfma_f32_16x16x32_bf16 v[96:99], a[8:11], v[168:171], 0\n"
"R 164,186,24576\ns_add_u32 m0, s50, 0\ns_add_u32 s63, s52, 788480\nN 96,24,172\nbuffer_load_dwordx4 v220, s[4:7], s63 offen lds\nv_mfma_f32_16x16x32_bf16 v[100:103], a[12:15], v[168:171], 0\n"
"R 166,186,28672\nN 100,28,172\nG 4\nv_mfma_f32_16x16x32_bf16 v[108:111], a[12:15], v[176:179], 0\nR 168,187,16384\nN 108,28,180\n"
"buffer_load_dwordx4 v221, s[4:7], s63 offen offset:1024 lds\nv_mov_b32 v242, s76\nv_mfma_f32_16x16x32_bf16 v[104:107], a[8:11], v[176:179], 0\nR 170,187,20480\ns_add_u32 s63, s52, 985088\nv_mov_b32 v243, s76\n"
"N 104,24,180\ns_add_u32 m0, s50, 8192\nv_mov_b32 v244, s76\nv_mov_b32 v245, s76\ns_and_b32 s60, s40, 2\ns_lshl_b32 s60, s60, 5\n"
"v_xor_b32 v238, s60, v184\nv_mfma_f32_16x16x32_bf16 v[76:79], a[4:7], v[176:179], 0\nR 172,187,24576\nv_xor_b32 v239, 32, v238\nN 76,20,180\nbuffer_load_dwordx4 v220, s[4:7], s63 offen lds\n"
"v_mfma_f32_16x16x32_bf16 v[72:75], a[0:3], v[176:179], 0\nR 174,187,28672\nN 72,16,180\nG 4\nv_mfma_f32_16x16x32_bf16 v[80:83], a[0:3], v[160:163], 0\nR 176,184,49152\n"
"ds_read_b128 a[32:35], v250 offset:0\nR 128,238,32768\nR 130,238,36864\nN 80,16,164\nds_read_b128 a[36:39], v250 offset:2048\nR 132,239,32768\n"
"R 134,239,36864\nv_mfma_f32_16x16x32_bf16 v[84:87], a[4:7], v[160:163], 0\nR 178,184,53248\nds_read_b128 a[40:43], v250 offset:4096\nN 84,20,164\nds_read_b128 a[44:47], v250 offset:6144\n"
"v_mfma_f32_16x16x32_bf16 v[112:115], a[8:11], v[160:163], 0\nR 180,184,57344\nbuffer_load_dwordx4 v221, s[4:7], s63 offen offset:1024 lds\nN 112,24,164\nds_read_b128 a[48:51], v251 offset:0\nv_mfma_f32_16x16x32_bf16 v[116:119], a[12:15], v[160:163], 0\n"
"R 182,184,61440\nds_read_b128 a[52:55], v251 offset:2048\nG 10\nv_mfma_f32_16x16x32_bf16 v[136:139], v[242:245], v[128:131], 0\nN 116,28,164\nds_read_b128 a[56:59], v251 offset:4096\n"
"v_mfma_f32_16x16x32_bf16 v[124:127], a[12:15], v[168:171], 0\nR 160,185,49152\nG 9\nv_mfma_f32_16x16x32_bf16 v[140:143], v[242:245], v[132:135], 0\nN 124,28,172\nds_read_b128 a[60:63], v251 offset:6144\n"
"v_mfma_f32_16x16x32_bf16 v[120:123], a[8:11], v[168:171], 0\nR 162,185,53248\nN 120,24,172\nv_mfma_f32_16x16x32_bf16 v[92:95], a[4:7], v[168:171], 0\nR 164,185,57344\nN 92,20,172\n"
"G 2\ns_add_u32 m0, s51, 122880\ns_add_u32 s60, s70, 384\nbuffer_load_dwordx4 v226, s[24:27], s60 offen lds\nv_mfma_f32_16x16x32_bf16 v[88:91], a[0:3], v[168:171], 0\nR 166,185,61440\n"
"v_lshrrev_b32 v128, 1, v252\nv_mov_b32 v131, s46\nN 88,16,172\ns_waitcnt vmcnt(11)\ns_barrier\ns_mov_b64 s[60:61], 0xffff\n"
"v_cndmask_b32 v130, v140, v136, s[60:61]\nM 0,176,32\nR 168,186,49152\nM 0,180,48\ns_add_u32 m0, s51, 123904\ns_add_u32 s60, s70, 16768\n"
"buffer_load_dwordx4 v227, s[24:27], s60 offen lds\ns_lshr_b32 s61, s40, 1\ns_lshl_b32 s61, s61, 8\ns_add_u32 s61, s61, s91\ns_add_u32 s61, s61, 262144\ns_bfm_b64 exec, 32, 0\n"
"buffer_store_dwordx2 v[130:131], v128, s[20:23], s61 offen sc1\ns_mov_b64 exec, -1\nM 16,176,36\nR 170,186,53248\ns_add_u32 m0, s50, 16384\ns_add_u32 s63, s52, 790528\n"
"M 16,180,52\nbuffer_load_dwordx4 v220, s[4:7], s63 offen lds\nM 32,176,40\nR 172,186,57344\nM 32,180,56\ns_add_u32 m0, s51, 124928\n"
"s_add_u32 s60, s70, 33152\nbuffer_load_dwordx4 v226, s[24:27], s60 offen lds\nM 48,176,44\nR 174,186,61440\ns_add_u32 m0, s50, 16384\nv_mov_b32 v230, 0\n"
"M 48,180,60\nbuffer_load_dwordx4 v221, s[4:7], s63 offen offset:1024 lds\nG 4\nv_mov_b32 v231, 0\nM 52,160,44\nR 176,187,49152\n"
"R 128,238,40960\nR 130,238,45056\nv_mov_b32 v232, 0\nM 52,164,60\ns_add_u32 m0, s51, 125952\ns_add_u32 s60, s70, 49536\n"
"buffer_load_dwordx4 v227, s[24:27], s60 offen lds\nR 132,239,40960\nR 134,239,45056\nv_mov_b32 v233, 0\nM 36,160,40\nR 178,187,53248\n"
"s_add_u32 m0, s50, 24576\ns_add_u32 s63, s52, 987136\nv_dot2c_f32_bf16 v230, s76, v192\nM 36,164,56\nbuffer_load_dwordx4 v220, s[4:7], s63 offen lds\nv_dot2c_f32_bf16 v231, v192, v192\n"
"M 20,160,36\nR 180,187,57344\nv_lshrrev_b32 v234, 1, v252\nv_dot2c_f32_bf16 v232, s76, v200\nM 20,164,52\ns_add_u32 m0, s51, 126976\n"
"s_add_u32 s60, s71, 384\nbuffer_load_dwordx4 v226, s[24:27], s60 offen lds\nv_dot2c_f32_bf16 v233, v200, v200\nM 4,160,32\nR 182,187,61440\nG 5\n"
"v_mfma_f32_16x16x32_bf16 v[136:139], v[242:245], v[128:131], 0\ns_add_u32 m0, s50, 24576\nv_dot2c_f32_bf16 v230, s76, v193\nM 4,164,48\nv_dot2c_f32_bf16 v231, v193, v193\nM 8,168,32\n"
"R 160,188,0\nG 4\nv_mfma_f32_16x16x32_bf16 v[140:143], v[242:245], v[132:135], 0\nbuffer_load_dwordx4 v221, s[4:7], s63 offen offset:1024 lds\nv_dot2c_f32_bf16 v232, s76, v201\nM 8,172,48\n"
"s_add_u32 m0, s51, 128000\ns_add_u32 s60, s71, 16768\nbuffer_load_dwordx4 v227, s[24:27], s60 offen lds\nv_dot2c_f32_bf16 v233, v201, v201\nM 24,168,36\nR 162,188,4096\n"
"v_dot2c_f32_bf16 v230, s76, v194\nM 24,172,52\nv_dot2c_f32_bf16 v231, v194, v194\nM 40,168,40\nR 164,188,8192\nv_dot2c_f32_bf16 v232, s76, v202\n"
"M 40,172,56\ns_add_u32 m0, s51, 129024\ns_add_u32 s60, s71, 33152\nbuffer_load_dwordx4 v226, s[24:27], s60 offen lds\nv_dot2c_f32_bf16 v233, v202, v202\nM 56,168,44\n"
"R 166,188,12288\nv_lshrrev_b32 v128, 1, v252\nv_mov_b32 v131, s46\nv_dot2c_f32_bf16 v230, s76, v195\nM 56,172,60\ns_mov_b64 s[60:61], 0xffff\n"
"v_cndmask_b32 v130, v140, v136, s[60:61]\nG 4\nv_dot2c_f32_bf16 v231, v195, v195\nM 60,176,44\nR 168,189,0\nv_dot2c_f32_bf16 v232, s76, v203\n"
"M 60,180,60\ns_add_u32 m0, s51, 130048\ns_add_u32 s60, s71, 49536\nbuffer_load_dwordx4 v227, s[24:27], s60 offen lds\ns_lshr_b32 s61, s40, 1\ns_lshl_b32 s61, s61, 8\n"
"s_add_u32 s61, s61, s91\ns_add_u32 s61, s61, 524288\ns_bfm_b64 exec, 32, 0\nbuffer_store_dwordx2 v[130:131], v128, s[20:23], s61 offen sc1\ns_mov_b64 exec, -1\nv_dot2c_f32_bf16 v233, v203, v203\n"
"M 44,176,40\nR 170,189,4096\nv_dot2c_f32_bf16 v230, s76, v196\nM 44,180,56\nv_dot2c_f32_bf16 v231, v196, v196\nM 28,176,36\n"
"R 172,189,8192\nv_dot2c_f32_bf16 v232, s76, v204\nM 28,180,52\ns_add_u32 m0, s50, 32768\nv_dot2c_f32_bf16 v233, v204, v204\nM 12,176,32\n"
"R 174,189,12288\nv_dot2c_f32_bf16 v230, s76, v197\nM 12,180,48\ns_waitcnt vmcnt(14)\ns_barrier\nG 4\n"
"v_dot2c_f32_bf16 v231, v197, v197\nN 64,32,160\nR 176,190,0\nv_dot2c_f32_bf16 v232, s76, v205\nN 64,48,164\ns_add_u32 s63, s52, 1181696\n"
"v_dot2c_f32_bf16 v233, v205, v205\nN 68,36,160\nR 178,190,4096\nv_dot2c_f32_bf16 v230, s76, v198\nN 68,52,164\nbuffer_load_dwordx4 v220, s[4:7], s63 offen lds\n"
"v_dot2c_f32_bf16 v231, v198, v198\nN 96,40,160\nR 180,190,8192\nds_read_b128 a[64:67], v248 offset:0\nv_dot2c_f32_bf16 v232, s76, v206\nN 96,56,164\n"
"ds_read_b128 a[68:71], v248 offset:2048\nv_dot2c_f32_bf16 v233, v206, v206\nN 100,44,160\nR 182,190,12288\nds_read_b128 a[72:75], v248 offset:4096\nv_dot2c_f32_bf16 v230, s76, v199\n"
"N 100,60,164\nds_read_b128 a[76:79], v248 offset:6144\nG 8\nv_dot2c_f32_bf16 v231, v199, v199\nN 108,44,168\nR 160,191,0\n"
"s_nop 0\nv_permlane32_swap_b32 v230, v231\nv_dot2c_f32_bf16 v232, s76, v207\nN 108,60,172\nds_read_b128 a[80:83], v249 offset:0\nbuffer_load_dwordx4 v221, s[4:7], s63 offen offset:1024 lds\n"
"v_dot2c_f32_bf16 v233, v207, v207\nN 104,40,168\nR 162,191,4096\nds_read_b128 a[84:87], v249 offset:2048\nN 104,56,172\nds_read_b128 a[88:91], v249 offset:4096\n"
"s_add_u32 m0, s50, 40960\nN 76,36,168\nR 164,191,8192\ns_add_u32 s63, s52, 1378304\nN 76,52,172\nds_read_b128 a[92:95], v249 offset:6144\n"
"v_permlane32_swap_b32 v232, v233\nN 72,32,168\nR 166,191,12288\nbuffer_load_dwordx4 v220, s[4:7], s63 offen lds\nN 72,48,172\nG 10\n"
"v_add_f32 v230, v230, v231\nv_add_f32 v232, v232, v233\nN 80,32,176\nR 168,184,0\nv_mov_b32 v231, s46\nN 80,48,180\n"
"G 2\ns_add_u32 m0, s51, 0\ns_add_u32 s60, s70, 512\nbuffer_load_dwordx4 v226, s[24:27], s60 offen lds\nN 84,36,176\nR 170,184,4096\n"
"s_add_u32 m0, s50, 40960\ns_nop 0\nbuffer_load_dwordx4 v221, s[4:7], s63 offen offset:1024 lds\nN 84,52,180\nv_permlane16_swap_b32 v230, v232\nv_add_f32 v230, v230, v232\n"
"N 112,40,176\nR 172,184,8192\nN 112,56,180\ns_add_u32 m0, s51, 1024\ns_add_u32 s60, s70, 16896\nbuffer_load_dwordx4 v227, s[24:27], s60 offen lds\n"
"N 116,44,176\nR 174,184,12288\nN 116,60,180\nG 4\nN 124,44,160\nR 176,185,0\n"
"N 124,60,164\ns_add_u32 m0, s51, 2048\ns_add_u32 s60, s70, 33280\nbuffer_load_dwordx4 v226, s[24:27], s60 offen lds\nN 120,40,160\nR 178,185,4096\n"
"N 120,56,164\nN 92,36,160\nR 180,185,8192\nN 92,52,164\ns_add_u32 m0, s51, 3072\ns_add_u32 s60, s70, 49664\n"
"buffer_load_dwordx4 v227, s[24:27], s60 offen lds\ns_lshr_b32 s61, s49, 3\ns_lshl2_add_u32 s61, s53, s61\ns_add_u32 s61, s61, 81788928\nbuffer_store_dwordx2 v[230:231], v234, s[20:23], s61 offen sc1\nN 88,32,160\n"
"R 182,185,12288\nN 88,48,164\ns_waitcnt vmcnt(13)\ns_barrier\nG 4\nM 0,168,64\n"
"R 160,186,0\nv_add_u32 v238, 0x10000, v238\nv_add_u32 v239, 0x10000, v239\nM 0,172,80\ns_add_u32 m0, s51, 4096\ns_add_u32 s60, s71, 512\n"
"buffer_load_dwordx4 v226, s[24:27], s60 offen lds\nM 16,168,68\nR 162,186,4096\nR 128,238,16384\nR 130,238,20480\ns_add_u32 m0, s50, 49152\n"
"s_add_u32 s63, s52, 1183744\nM 16,172,84\nR 132,239,16384\nR 134,239,20480\nM 32,168,72\nR 164,186,8192\n"
"buffer_load_dwordx4 v220, s[4:7], s63 offen lds\nM 32,172,88\ns_add_u32 m0, s51, 5120\ns_add_u32 s60, s71, 16896\nbuffer_load_dwordx4 v227, s[24:27], s60 offen lds\nM 48,168,76\n"
"R 166,186,12288\ns_add_u32 m0, s50, 49152\nM 48,172,92\nG 8\nM 52,176,76\nR 168,187,0\n"
"G 5\nv_mfma_f32_16x16x32_bf16 v[136:139], v[242:245], v[128:131], 0\nbuffer_load_dwordx4 v221, s[4:7], s63 offen offset:1024 lds\nM 52,180,92\ns_add_u32 m0, s51, 6144\ns_add_u32 s60, s71, 33280\n"
"buffer_load_dwordx4 v226, s[24:27], s60 offen lds\nM 36,176,72\nR 170,187,4096\nG 4\nv_mfma_f32_16x16x32_bf16 v[140:143], v[242:245], v[132:135], 0\ns_add_u32 m0, s50, 57344\n"
"s_add_u32 s63, s52, 1380352\nM 36,180,88\nbuffer_load_dwordx4 v220, s[4:7], s63 offen lds\nM 20,176,68\nR 172,187,8192\nM 20,180,84\n"
"s_add_u32 m0, s51, 7168\ns_add_u32 s60, s71, 49664\nbuffer_load_dwordx4 v227, s[24:27], s60 offen lds\nM 4,176,64\nR 174,187,12288\ns_add_u32 m0, s50, 57344\n"
"M 4,180,80\nG 4\nM 8,160,64\nR 176,184,16384\nbuffer_load_dwordx4 v221, s[4:7], s63 offen offset:1024 lds\nv_lshrrev_b32 v128, 1, v252\n"
"M 8,164,80\ns_mov_b64 s[60:61], 0xffff\nv_cndmask_b32 v130, v140, v136, s[60:61]\nv_mov_b32 v131, s46\nM 24,160,68\nR 178,184,20480\n"
"M 24,164,84\ns_lshr_b32 s61, s40, 1\ns_lshl_b32 s61, s61, 8\ns_add_u32 s61, s61, s91\ns_add_u32 s61, s61, 0\ns_bfm_b64 exec, 32, 0\n"
"buffer_store_dwordx2 v[130:131], v128, s[20:23], s61 offen sc1\ns_mov_b64 exec, -1\nM 40,160,72\nR 180,184,24576\nM 40,164,88\nM 56,160,76\n"
"R 182,184,28672\nM 56,164,92\nG 4\nM 60,168,76\nR 160,185,16384\nM 60,172,92\n"
"s_add_u32 m0, s50, 65536\nM 44,168,72\nR 162,185,20480\nM 44,172,88\ns_add_u32 s63, s52, 1574912\nM 28,168,68\n"
"R 164,185,24576\nM 28,172,84\nbuffer_load_dwordx4 v220, s[4:7], s63 offen lds\nM 12,168,64\nR 166,185,28672\nM 12,172,80\n"
"s_waitcnt vmcnt(14)\ns_barrier\nG 4\nN 64,64,176\nR 168,186,16384\nN 64,80,180\n"
"v_lshrrev_b32 v230, 7, v252\nN 68,68,176\nR 170,186,20480\nN 68,84,180\nbuffer_load_dwordx4 v221, s[4:7], s63 offen offset:1024 lds\nv_lshlrev_b32 v230, 9, v230\n"
"N 96,72,176\nR 172,186,24576\ns_add_u32 m0, s50, 73728\nN 96,88,180\nds_read_b128 a[96:99], v250 offset:0\nv_and_b32 v231, 0x70, v252\n"
"N 100,76,176\nR 174,186,28672\nds_read_b128 a[100:103], v250 offset:2048\ns_add_u32 s63, s52, 1771520\nN 100,92,180\nds_read_b128 a[104:107], v250 offset:4096\n"
"G 7\nN 108,76,160\nR 176,187,16384\ns_and_b32 s60, s40, 3\ns_lshl_b32 s60, s60, 7\nv_add3_u32 v230, v230, v231, s60\n"
"N 108,92,164\nds_read_b128 a[108:111], v250 offset:6144\nbuffer_load_dwordx4 v220, s[4:7], s63 offen lds\nN 104,72,160\nR 178,187,20480\nds_read_b128 a[112:115], v251 offset:0\n"
"N 104,88,164\nds_read_b128 a[116:119], v251 offset:2048\ns_lshl_b32 s61, s42, 14\ns_add_u32 s61, s61, 84148224\nbuffer_load_dwordx4 v[128:131], v230, s[20:23], s61 offen sc1\nN 76,68,160\n"
"R 180,187,24576\nds_read_b128 a[120:123], v251 offset:4096\nN 76,84,164\nds_read_b128 a[124:127], v251 offset:6144\ns_lshl_b32 s61, s42, 14\ns_add_u32 s61, s61, 84152320\n"
"buffer_load_dwordx4 v[132:135], v230, s[20:23], s61 offen sc1\nN 72,64,160\nR 182,187,28672\nN 72,80,164\ns_lshl_b32 s61, s42, 14\ns_add_u32 s61, s61, 84156416\n"
"buffer_load_dwordx4 v[136:139], v230, s[20:23], s61 offen sc1\nG 11\nN 80,64,168\nR 160,184,32768\nbuffer_load_dwordx4 v221, s[4:7], s63 offen offset:1024 lds\nN 80,80,172\n"
"s_lshl_b32 s61, s42, 14\ns_add_u32 s61, s61, 84160512\nbuffer_load_dwordx4 v[140:143], v230, s[20:23], s61 offen sc1\nN 84,68,168\nR 162,184,36864\nN 84,84,172\n"
"N 112,72,168\nR 164,184,40960\ns_waitcnt vmcnt(10)\nds_read_b128 a[128:131], v248 offset:0\nN 112,88,172\nds_read_b128 a[132:135], v248 offset:2048\n"
"N 116,76,168\nR 166,184,45056\nds_read_b128 a[136:139], v248 offset:4096\nN 116,92,172\nds_read_b128 a[140:143], v248 offset:6144\nG 8\n"
"N 124,76,176\nR 168,185,32768\nN 124,92,180\nds_read_b128 a[144:147], v249 offset:0\nN 120,72,176\nR 170,185,36864\n"
"ds_read_b128 a[148:151], v249 offset:2048\nN 120,88,180\nds_read_b128 a[152:155], v249 offset:4096\nN 92,68,176\nR 172,185,40960\nN 92,84,180\n"
"ds_read_b128 a[156:159], v249 offset:6144\nN 88,64,176\nR 174,185,45056\nN 88,80,180\ns_waitcnt vmcnt(9)\ns_barrier\n"
"G 10\nM 0,160,96\nR 176,186,32768\nM 0,164,112\nG 2\ns_add_u32 m0, s51, 0\n"
"s_add_u32 s60, s70, 640\nbuffer_load_dwordx4 v226, s[24:27], s60 offen lds\nM 16,160,100\nR 178,186,36864\ns_add_u32 m0, s50, 81920\ns_add_u32 s63, s52, 1576960\n"
"buffer_load_dwordx4 v220, s[4:7], s63 offen lds\nM 16,164,116\ns_add_u32 m0, s51, 1024\ns_add_u32 s60, s70, 17024\nbuffer_load_dwordx4 v227, s[24:27], s60 offen lds\nM 32,160,104\n"
"R 180,186,40960\nv_lshrrev_b32 v234, 7, v252\nM 32,164,120\ns_add_u32 m0, s51, 2048\ns_add_u32 s60, s70, 33408\nbuffer_load_dwordx4 v226, s[24:27], s60 offen lds\n"
"M 48,160,108\nR 182,186,45056\ns_add_u32 m0, s50, 81920\ns_nop 0\nbuffer_load_dwordx4 v221, s[4:7], s63 offen offset:1024 lds\nv_lshlrev_b32 v234, 9, v234\n"
"M 48,164,124\ns_add_u32 m0, s51, 3072\ns_add_u32 s60, s70, 49792\nbuffer_load_dwordx4 v227, s[24:27], s60 offen lds\nG 4\nM 52,168,108\n"
"R 160,187,32768\nM 52,172,124\ns_add_u32 m0, s51, 4096\ns_add_u32 s60, s71, 640\nbuffer_load_dwordx4 v226, s[24:27], s60 offen lds\nM 36,168,104\n"
"R 162,187,36864\ns_add_u32 m0, s50, 90112\ns_add_u32 s63, s52, 1773568\nbuffer_load_dwordx4 v220, s[4:7], s63 offen lds\nv_and_b32 v235, 0x70, v252\nM 36,172,120\n"
"s_add_u32 m0, s51, 5120\ns_add_u32 s60, s71, 17024\nbuffer_load_dwordx4 v227, s[24:27], s60 offen lds\nM 20,168,100\nR 164,187,40960\ns_and_b32 s60, s40, 3\n"
"s_lshl_b32 s60, s60, 7\nv_add3_u32 v234, v234, v235, s60\nM 20,172,116\ns_add_u32 m0, s51, 6144\ns_add_u32 s60, s71, 33408\nbuffer_load_dwordx4 v226, s[24:27], s60 offen lds\n"
"M 4,168,96\nR 166,187,45056\ns_lshl_b32 s61, s42, 14\ns_add_u32 s61, s61, 84410368\nbuffer_load_dwordx4 v[144:147], v234, s[20:23], s61 offen sc1\nM 4,172,112\n"
"s_add_u32 m0, s51, 7168\ns_add_u32 s60, s71, 49792\nbuffer_load_dwordx4 v227, s[24:27], s60 offen lds\nG 4\nM 8,176,96\nR 168,184,49152\n"
"s_add_u32 m0, s50, 90112\ns_nop 0\nbuffer_load_dwordx4 v221, s[4:7], s63 offen offset:1024 lds\nM 8,180,112\ns_lshl_b32 s61, s42, 14\ns_add_u32 s61, s61, 84414464\n"
"buffer_load_dwordx4 v[148:151], v234, s[20:23], s61 offen sc1\nM 24,176,100\nR 170,184,53248\nM 24,180,116\ns_lshl_b32 s61, s42, 14\ns_add_u32 s61, s61, 84418560\n"
"buffer_load_dwordx4 v[152:155], v234, s[20:23], s61 offen sc1\nM 40,176,104\nR 172,184,57344\nM 40,180,120\ns_lshl_b32 s61, s42, 14\ns_add_u32 s61, s61, 84422656\n"
"buffer_load_dwordx4 v[156:159], v234, s[20:23], s61 offen sc1\nM 56,176,108\nR 174,184,61440\nM 56,180,124\nG 4\nM 60,160,108\n"
"R 176,185,49152\nM 60,164,124\nM 44,160,104\nR 178,185,53248\nM 44,164,120\ns_add_u32 m0, s50, 98304\n"
"M 28,160,100\nR 180,185,57344\nM 28,164,116\ns_add_u32 s63, s52, 1968128\nM 12,160,96\nR 182,185,61440\n"
"M 12,164,112\ns_waitcnt vmcnt(17)\ns_barrier\nG 4\nN 64,96,168\nR 160,186,49152\n"
"v_min3_u32 v232, v129, v131, v133\nv_min3_u32 v232, v232, v135, v137\nN 64,112,172\nbuffer_load_dwordx4 v220, s[4:7], s63 offen lds\ns_waitcnt vmcnt(17)\nv_min3_u32 v232, v232, v139, v141\n"
"N 68,100,168\nR 162,186,53248\nv_min3_u32 v232, v232, v143, v143\nv_max3_u32 v233, v129, v131, v133\nN 68,116,172\nv_max3_u32 v233, v233, v135, v137\n"
"v_max3_u32 v233, v233, v139, v141\nv_max3_u32 v233, v233, v143, v143\nN 96,104,168\nR 164,186,57344\nv_cmp_ne_u32 s[86:87], s46, v232\nv_cmp_ne_u32 s[88:89], s46, v233\n"
"N 96,120,172\nbuffer_load_dwordx4 v221, s[4:7], s63 offen offset:1024 lds\ns_or_b64 s[86:87], s[86:87], s[88:89]\ns_cbranch_scc1 Lq116\nLq117:\nN 100,108,168\n"
"R 166,186,61440\ns_add_u32 m0, s50, 106496\nv_add_f32 v128, v128, v136\nN 100,124,172\nG 4\nv_add_f32 v130, v130, v138\n"
"v_add_f32 v132, v132, v140\nv_add_f32 v134, v134, v142\nN 108,108,176\nR 168,187,49152\ns_add_u32 s63, s52, 2164736\nv_add_f32 v128, v128, v132\n"
"N 108,124,180\nbuffer_load_dwordx4 v220, s[4:7], s63 offen lds\nv_add_f32 v130, v130, v134\nN 104,104,176\nR 170,187,53248\nv_add_f32_dpp v128, v128, v128 row_ror:8 row_mask:0xf bank_mask:0xf\n"
"v_add_f32_dpp v130, v130, v130 row_ror:8 row_mask:0xf bank_mask:0xf\nN 104,120,180\nv_mov_b32 v132, v128\nv_mov_b32 v133, v130\ns_nop 0\nv_permlane32_swap_b32 v132, v128\n"
"N 76,100,176\nR 172,187,57344\nv_permlane32_swap_b32 v133, v130\nv_add_f32 v128, v128, v132\nN 76,116,180\nbuffer_load_dwordx4 v221, s[4:7], s63 offen offset:1024 lds\n"
"v_add_f32 v130, v130, v133\nN 72,96,176\nR 174,187,61440\ns_add_u32 s63, s52, 1970176\nv_mov_b32 v132, v128\nN 72,112,180\n"
"G 4\nv_mov_b32 v133, v130\nv_permlane16_swap_b32 v132, v128\ns_nop 0\nv_permlane16_swap_b32 v133, v130\nN 80,96,160\n"
"R 176,188,0\nv_add_f32 v128, v128, v132\nv_add_f32 v130, v130, v133\nN 80,112,164\ns_add_u32 m0, s50, 114688\nv_and_b32 v136, s77, v128\n"
"v_sub_f32 v137, v128, v136\nv_cvt_pk_bf16_f32 v132, v136, v137\nN 84,100,160\nR 178,188,4096\nv_cvt_pk_bf16_f32 v133, v136, 0\nv_and_b32 v136, s77, v130\n"
"N 84,116,164\nbuffer_load_dwordx4 v220, s[4:7], s63 offen lds\nN 112,104,160\nR 180,188,8192\nv_sub_f32 v137, v130, v136\nN 112,120,164\n"
"v_cvt_pk_bf16_f32 v134, v136, v137\nv_cvt_pk_bf16_f32 v135, v136, 0\nN 116,108,160\nR 182,188,12288\nv_and_b32 v230, 0x1ff, v230\nN 116,124,164\n"
"v_add_u32 v230, 156160, v230\nds_write_b128 v230, v[132:135]\nG 5\nN 124,108,168\nR 160,189,0\nN 124,124,172\n"
"buffer_load_dwordx4 v221, s[4:7], s63 offen offset:1024 lds\nN 120,104,168\nR 162,189,4096\ns_add_u32 s63, s52, 2166784\nN 120,120,172\ns_add_u32 m0, s50, 0\n"
"N 92,100,168\nR 164,189,8192\nN 92,116,172\nbuffer_load_dwordx4 v220, s[4:7], s63 offen lds\nN 88,96,168\nR 166,189,12288\n"
"N 88,112,172\ns_waitcnt vmcnt(11)\nds_read_b128 a[160:163], v248 offset:0\ns_waitcnt vmcnt(10) lgkmcnt(5)\ns_barrier\nM 0,176,128\n"
"R 168,190,0\nds_read_b128 a[164:167], v248 offset:2048\nM 0,180,144\nds_read_b128 a[168:171], v248 offset:4096\nM 16,176,132\nR 170,190,4096\n"
"s_waitcnt vmcnt(9)\nv_min3_u32 v236, v145, v147, v149\ns_waitcnt vmcnt(8)\nv_min3_u32 v236, v236, v151, v153\nM 16,180,148\nds_read_b128 a[172:175], v248 offset:6144\n"
"s_waitcnt vmcnt(7)\nv_min3_u32 v236, v236, v155, v157\nv_min3_u32 v236, v236, v159, v159\nM 32,176,136\nR 172,190,8192\nds_read_b128 a[176:179], v249 offset:0\n"
"v_max3_u32 v237, v145, v147, v149\nM 32,180,152\nds_read_b128 a[180:183], v249 offset:2048\nv_lshrrev_b32 v230, 7, v252\nv_max3_u32 v237, v237, v151, v153\nM 48,176,140\n"
"R 174,190,12288\nds_read_b128 a[184:187], v249 offset:4096\nv_max3_u32 v237, v237, v155, v157\nM 48,180,156\nds_read_b128 a[188:191], v249 offset:6144\nG 12\n"
"v_max3_u32 v237, v237, v159, v159\nM 52,160,140\nR 176,191,0\nv_lshlrev_b32 v230, 9, v230\nv_cmp_ne_u32 s[86:87], s46, v236\nM 52,164,156\n"
"buffer_load_dwordx4 v221, s[4:7], s63 offen offset:1024 lds\nv_and_b32 v231, 0x70, v252\nv_cmp_ne_u32 s[88:89], s46, v237\nM 36,160,136\nR 178,191,4096\ns_or_b64 s[86:87], s[86:87], s[88:89]\n"
"s_cbranch_scc1 Lq126\nLq127:\nM 36,164,152\nG 2\ns_add_u32 m0, s51, 0\ns_add_u32 s60, s70, 768\n"
"buffer_load_dwordx4 v226, s[24:27], s60 offen lds\nv_add_f32 v144, v144, v152\nM 20,160,132\nR 180,191,8192\ns_and_b32 s60, s40, 3\ns_lshl_b32 s60, s60, 7\n"
"v_add3_u32 v230, v230, v231, s60\nv_add_f32 v146, v146, v154\nM 20,164,148\ns_add_u32 m0, s51, 1024\ns_add_u32 s60, s70, 17152\nbuffer_load_dwordx4 v227, s[24:27], s60 offen lds\n"
"v_add_f32 v148, v148, v156\nM 4,160,128\nR 182,191,12288\ns_lshl_b32 s61, s42, 14\ns_add_u32 s61, s61, 83886080\nbuffer_load_dwordx4 v[128:131], v230, s[20:23], s61 offen sc1\n"
"v_add_f32 v150, v150, v158\nM 4,164,144\ns_add_u32 m0, s51, 2048\ns_add_u32 s60, s70, 33536\nbuffer_load_dwordx4 v226, s[24:27], s60 offen lds\nv_add_f32 v144, v144, v148\n"
"M 8,168,128\nR 160,188,16384\ns_lshl_b32 s61, s42, 14\ns_add_u32 s61, s61, 83890176\nbuffer_load_dwordx4 v[132:135], v230, s[20:23], s61 offen sc1\nv_add_f32 v146, v146, v150\n"
"M 8,172,144\ns_add_u32 m0, s51, 3072\ns_add_u32 s60, s70, 49920\nbuffer_load_dwordx4 v227, s[24:27], s60 offen lds\nv_add_f32_dpp v144, v144, v144 row_ror:8 row_mask:0xf bank_mask:0xf\nM 24,168,132\n"
"R 162,188,20480\nv_add_f32_dpp v146, v146, v146 row_ror:8 row_mask:0xf bank_mask:0xf\nv_mov_b32 v148, v144\nM 24,172,148\ns_add_u32 m0, s51, 4096\ns_add_u32 s60, s71, 768\n"
"buffer_load_dwordx4 v226, s[24:27], s60 offen lds\nv_mov_b32 v149, v146\nM 40,168,136\nR 164,188,24576\ns_lshl_b32 s61, s42, 14\ns_add_u32 s61, s61, 83894272\n"
"buffer_load_dwordx4 v[136:139], v230, s[20:23], s61 offen sc1\nv_permlane32_swap_b32 v148, v144\nM 40,172,152\ns_add_u32 m0, s51, 5120\ns_add_u32 s60, s71, 17152\nbuffer_load_dwordx4 v227, s[24:27], s60 offen lds\n"
"v_permlane32_swap_b32 v149, v146\nM 56,168,140\nR 166,188,28672\ns_lshl_b32 s61, s42, 14\ns_add_u32 s61, s61, 83898368\nbuffer_load_dwordx4 v[140:143], v230, s[20:23], s61 offen sc1\n"
"v_add_f32 v144, v144, v148\nM 56,172,156\ns_add_u32 m0, s51, 6144\ns_add_u32 s60, s71, 33536\nbuffer_load_dwordx4 v226, s[24:27], s60 offen lds\nG 4\n"
"v_add_f32 v146, v146, v149\nM 60,176,140\nR 168,189,16384\nv_mov_b32 v148, v144\nv_mov_b32 v149, v146\nM 60,180,156\n"
"s_add_u32 m0, s51, 7168\ns_add_u32 s60, s71, 49920\nbuffer_load_dwordx4 v227, s[24:27], s60 offen lds\nv_permlane16_swap_b32 v148, v144\nM 44,176,136\nR 170,189,20480\n"
"v_permlane16_swap_b32 v149, v146\nv_add_f32 v144, v144, v148\nM 44,180,152\ns_add_u32 m0, s50, 8192\nv_add_f32 v146, v146, v149\nv_and_b32 v152, s77, v144\n"
"v_sub_f32 v153, v144, v152\nM 28,176,132\nR 172,189,24576\nv_cvt_pk_bf16_f32 v148, v152, v153\nv_cvt_pk_bf16_f32 v149, v152, 0\nM 28,180,148\n"
"s_add_u32 s63, s52, 2361344\nv_and_b32 v152, s77, v146\nv_sub_f32 v153, v146, v152\nv_cvt_pk_bf16_f32 v150, v152, v153\nM 12,176,128\nR 174,189,28672\n"
"v_cvt_pk_bf16_f32 v151, v152, 0\nv_and_b32 v234, 0x1ff, v234\nM 12,180,144\ns_waitcnt vmcnt(16)\ns_barrier\nG 4\n"
"v_add_u32 v234, 156672, v234\nN 64,128,160\nR 176,190,16384\nds_write_b128 v234, v[148:151]\nN 64,144,164\nbuffer_load_dwordx4 v220, s[4:7], s63 offen lds\n"
"N 68,132,160\nR 178,190,20480\nN 68,148,164\nN 96,136,160\nR 180,190,24576\nN 96,152,164\n"
"buffer_load_dwordx4 v221, s[4:7], s63 offen offset:1024 lds\nN 100,140,160\nR 182,190,28672\ns_add_u32 m0, s50, 16384\nN 100,156,164\nG 5\n"
"N 108,140,168\nR 160,191,16384\ns_add_u32 s63, s52, 2557952\nN 108,156,172\nbuffer_load_dwordx4 v220, s[4:7], s63 offen lds\nN 104,136,168\n"
"R 162,191,20480\nN 104,152,172\nN 76,132,168\nR 164,191,24576\nLq192:\ns_lshr_b32 s60, s45, 3\n"
"s_lshl2_add_u32 s60, s60, s40\ns_lshl_b32 s60, s60, 8\nv_lshl_add_u32 v224, v252, 3, s60\nv_add_u32 v224, Lq192+4096-LqPC, v224\ns_mov_b64 exec, 3\nglobal_load_dword v224, v224, s[98:99]\n"
"s_mov_b64 exec, -1\nN 76,148,172\nbuffer_load_dwordx4 v221, s[4:7], s63 offen offset:1024 lds\nN 72,128,168\nR 166,191,28672\nN 72,144,172\n"
"G 4\nN 80,128,176\nR 168,188,32768\nN 80,144,180\nN 84,132,176\nR 170,188,36864\n"
"N 84,148,180\ns_add_u32 m0, s50, 24576\nN 112,136,176\nR 172,188,40960\nN 112,152,180\ns_add_u32 s63, s52, 2754560\n"
"N 116,140,176\nR 174,188,45056\nN 116,156,180\nG 4\nN 124,140,160\nR 176,189,32768\n"
"N 124,156,164\nbuffer_load_dwordx4 v220, s[4:7], s63 offen lds\nN 120,136,160\nR 178,189,36864\nN 120,152,164\nN 92,132,160\n"
"R 180,189,40960\ns_waitcnt vmcnt(13)\nv_min3_u32 v232, v129, v131, v133\ns_waitcnt vmcnt(10)\nv_min3_u32 v232, v232, v135, v137\nN 92,148,164\n"
"buffer_load_dwordx4 v221, s[4:7], s63 offen offset:1024 lds\ns_waitcnt vmcnt(9)\nv_min3_u32 v232, v232, v139, v141\nN 88,128,160\nR 182,189,45056\ns_add_u32 m0, s50, 32768\n"
"v_min3_u32 v232, v232, v143, v143\nN 88,144,164\ns_barrier\nG 4\nv_max3_u32 v233, v129, v131, v133\nM 0,168,160\n"
"R 160,190,32768\ns_add_u32 s63, s52, 2951168\nv_max3_u32 v233, v233, v135, v137\nM 0,172,176\nbuffer_load_dwordx4 v220, s[4:7], s63 offen lds\nv_max3_u32 v233, v233, v139, v141\n"
"M 16,168,164\nR 162,190,36864\nv_max3_u32 v233, v233, v143, v143\nv_cmp_ne_u32 s[86:87], s46, v232\nM 16,172,180\nv_cmp_ne_u32 s[88:89], s46, v233\n"
"M 32,168,168\nR 164,190,40960\ns_or_b64 s[86:87], s[86:87], s[88:89]\ns_cbranch_scc1 Lq136\nLq137:\nM 32,172,184\n"
"v_add_f32 v128, v128, v136\nv_add_f32 v130, v130, v138\nv_add_f32 v132, v132, v140\nM 48,168,172\nR 166,190,45056\nv_add_f32 v134, v134, v142\n"
"v_add_f32 v128, v128, v132\nM 48,172,188\nG 4\nv_add_f32 v130, v130, v134\nv_add_f32_dpp v128, v128, v128 row_ror:8 row_mask:0xf bank_mask:0xf\ns_nop 0\n"
"v_add_f32_dpp v130, v130, v130 row_ror:8 row_mask:0xf bank_mask:0xf\nM 52,176,172\nR 168,191,32768\nv_mov_b32 v132, v128\nv_mov_b32 v133, v130\nM 52,180,188\n"
"buffer_load_dwordx4 v221, s[4:7], s63 offen offset:1024 lds\nv_permlane32_swap_b32 v132, v128\nM 36,176,168\nR 170,191,36864\ns_waitcnt vmcnt(9)\nds_read_b128 a[192:195], v248 offset:0\n"
"v_permlane32_swap_b32 v133, v130\nM 36,180,184\nds_read_b128 a[196:199], v248 offset:2048\nv_add_f32 v128, v128, v132\nv_add_f32 v130, v130, v133\nM 20,176,164\n"
"R 172,191,40960\nds_read_b128 a[200:203], v248 offset:4096\nv_mov_b32 v132, v128\nM 20,180,180\nds_read_b128 a[204:207], v248 offset:6144\nv_mov_b32 v133, v130\n"
"v_permlane16_swap_b32 v132, v128\nM 4,176,160\nR 174,191,45056\nv_permlane16_swap_b32 v133, v130\nv_add_f32 v128, v128, v132\nM 4,180,176\n"
"ds_read_b128 a[208:211], v249 offset:0\nG 9\nv_add_f32 v130, v130, v133\nM 8,160,160\nR 176,188,49152\nds_read_b128 a[212:215], v249 offset:2048\n"
"v_and_b32 v136, s77, v128\nM 8,164,176\nds_read_b128 a[216:219], v249 offset:4096\nv_sub_f32 v137, v128, v136\nv_cvt_pk_bf16_f32 v132, v136, v137\nM 24,160,164\n"
"R 178,188,53248\nv_cvt_pk_bf16_f32 v133, v136, 0\nv_and_b32 v136, s77, v130\nM 24,164,180\nds_read_b128 a[220:223], v249 offset:6144\nv_sub_f32 v137, v130, v136\n"
"v_cvt_pk_bf16_f32 v134, v136, v137\nv_cvt_pk_bf16_f32 v135, v136, 0\nv_and_b32 v230, 0x1ff, v230\nv_add_u32 v230, 155648, v230\nds_write_b128 v230, v[132:135]\nM 40,160,168\n"
"R 180,184,0\nM 40,164,184\ns_lshr_b32 s84, s49, 3\nM 56,160,172\nR 182,184,4096\ns_add_u32 s84, s84, 81788928\n"
"M 56,164,188\nG 3\ns_add_u32 m0, s51, 0\ns_add_u32 s60, s70, 896\nbuffer_load_dwordx4 v226, s[24:27], s60 offen lds\nM 60,168,172\n"
"R 160,189,49152\nbuffer_load_dwordx4 v[128:131], v252, s[20:23], s84 offen offset:0 sc1\nM 60,172,188\ns_add_u32 m0, s51, 1024\ns_add_u32 s60, s70, 17280\nbuffer_load_dwordx4 v227, s[24:27], s60 offen lds\n"
"M 44,168,168\nR 162,189,53248\nM 44,172,184\ns_add_u32 m0, s51, 2048\ns_add_u32 s60, s70, 33664\nbuffer_load_dwordx4 v226, s[24:27], s60 offen lds\n"
"M 28,168,164\nR 164,185,0\nbuffer_load_dwordx4 v[132:135], v252, s[20:23], s84 offen offset:1024 sc1\nM 28,172,180\ns_add_u32 m0, s51, 3072\ns_add_u32 s60, s70, 50048\n"
"buffer_load_dwordx4 v227, s[24:27], s60 offen lds\nM 12,168,160\nR 166,185,4096\nbuffer_load_dwordx4 v[136:139], v252, s[20:23], s84 offen offset:2048 sc1\nM 12,172,176\ns_add_u32 m0, s51, 4096\n"
"s_add_u32 s60, s71, 896\nbuffer_load_dwordx4 v226, s[24:27], s60 offen lds\ns_waitcnt vmcnt(12) lgkmcnt(6)\ns_barrier\nG 4\nN 64,160,176\n"
"R 168,190,49152\ns_lshr_b32 s85, s49, 3\ns_add_u32 m0, s50, 40960\ns_add_u32 s63, s52, 2363392\nbuffer_load_dwordx4 v220, s[4:7], s63 offen lds\nN 64,176,180\n"
"s_add_u32 m0, s51, 5120\ns_add_u32 s60, s71, 17280\nbuffer_load_dwordx4 v227, s[24:27], s60 offen lds\ns_add_u32 s85, s85, 81793024\nN 68,164,176\nR 170,190,53248\n"
"buffer_load_dwordx4 v[140:143], v252, s[20:23], s84 offen offset:3072 sc1\nN 68,180,180\ns_add_u32 m0, s51, 6144\ns_add_u32 s60, s71, 33664\nbuffer_load_dwordx4 v226, s[24:27], s60 offen lds\nN 96,168,176\n"
"R 172,186,0\nbuffer_load_dwordx4 v[144:147], v252, s[20:23], s85 offen offset:0 sc1\nN 96,184,180\ns_add_u32 m0, s51, 7168\ns_add_u32 s60, s71, 50048\nbuffer_load_dwordx4 v227, s[24:27], s60 offen lds\n"
"N 100,172,176\nR 174,186,4096\ns_add_u32 m0, s50, 40960\nN 100,188,180\nG 4\nN 108,172,160\n"
"R 176,191,49152\nbuffer_load_dwordx4 v221, s[4:7], s63 offen offset:1024 lds\nN 108,188,164\nbuffer_load_dwordx4 v[148:151], v252, s[20:23], s85 offen offset:1024 sc1\ns_add_u32 s63, s52, 2560000\nN 104,168,160\n"
"R 178,191,53248\ns_add_u32 m0, s50, 49152\nN 104,184,164\nbuffer_load_dwordx4 v[152:155], v252, s[20:23], s85 offen offset:2048 sc1\nN 76,164,160\nR 180,187,0\n"
"buffer_load_dwordx4 v220, s[4:7], s63 offen lds\nN 76,180,164\nbuffer_load_dwordx4 v[156:159], v252, s[20:23], s85 offen offset:3072 sc1\nN 72,160,160\nR 182,187,4096\nN 72,176,164\n"
"G 4\nN 80,160,168\nR 160,184,8192\nbuffer_load_dwordx4 v221, s[4:7], s63 offen offset:1024 lds\nN 80,176,172\nN 84,164,168\n"
"R 162,184,12288\nN 84,180,172\nN 112,168,168\nR 164,184,16384\nN 112,184,172\nN 116,172,168\n"
"R 166,184,20480\nN 116,188,172\nG 4\nN 124,172,176\nR 168,185,8192\nN 124,188,180\n"
"s_add_u32 m0, s50, 57344\nN 120,168,176\nR 170,185,12288\nN 120,184,180\ns_add_u32 s63, s52, 2756608\nN 92,164,176\n"
"R 172,185,16384\nN 92,180,180\nbuffer_load_dwordx4 v220, s[4:7], s63 offen lds\nN 88,160,176\nR 174,185,20480\nN 88,176,180\n"
"s_waitcnt vmcnt(21)\ns_barrier\nG 4\nM 0,160,192\nR 176,186,8192\nM 0,164,208\n"
"buffer_load_dwordx4 v221, s[4:7], s63 offen offset:1024 lds\nM 16,160,196\nR 178,186,12288\nM 16,164,212\ns_add_u32 m0, s50, 65536\nM 32,160,200\n"
"R 180,186,16384\nM 32,164,216\ns_add_u32 s63, s52, 2953216\nM 48,160,204\nR 182,186,20480\ns_lshl2_add_u32 s60, s45, s40\n"
"v_lshlrev_b32 v224, 3, v252\ns_mul_i32 s60, s60, 2048\ns_bfm_b64 exec, 16, 0\nbuffer_load_dword v224, v224, s[8:11], s60 offen\ns_mov_b64 exec, -1\nM 48,164,220\n"
"G 4\nM 52,168,204\nR 160,187,8192\nM 52,172,220\nbuffer_load_dwordx4 v220, s[4:7], s63 offen lds\nM 36,168,200\n"
"R 162,187,12288\nM 36,172,216\nM 20,168,196\nR 164,187,16384\nM 20,172,212\nbuffer_load_dwordx4 v221, s[4:7], s63 offen offset:1024 lds\n"
"M 4,168,192\nR 166,187,20480\nM 4,172,208\nG 4\nM 8,176,192\nR 168,184,24576\n"
"M 8,180,208\nM 24,176,196\nR 170,184,28672\ns_waitcnt vmcnt(11)\nds_read_b128 a[224:227], v248 offset:0\nM 24,180,212\n"
"ds_read_b128 a[228:231], v248 offset:2048\nM 40,176,200\nR 172,184,32768\nds_read_b128 a[232:235], v248 offset:4096\nM 40,180,216\nds_read_b128 a[236:239], v248 offset:6144\n"
"M 56,176,204\nR 174,184,36864\nM 56,180,220\nds_read_b128 a[240:243], v249 offset:0\nG 9\nM 60,160,204\n"
"R 176,185,24576\nds_read_b128 a[244:247], v249 offset:2048\nv_min3_u32 v230, v129, v131, v133\nM 60,164,220\nds_read_b128 a[248:251], v249 offset:4096\ns_add_u32 m0, s50, 73728\n"
"v_min3_u32 v230, v230, v135, v137\nM 44,160,200\nR 178,185,28672\ns_add_u32 s63, s52, 0\nv_min3_u32 v230, v230, v139, v141\nM 44,164,216\n"
"ds_read_b128 a[252:255], v249 offset:6144\nv_min3_u32 v230, v230, v143, v145\ns_waitcnt vmcnt(9)\nv_min3_u32 v230, v230, v147, v149\nM 28,160,196\nR 180,185,32768\n"
"s_waitcnt vmcnt(8)\nv_min3_u32 v230, v230, v151, v153\ns_waitcnt vmcnt(6)\nv_min3_u32 v230, v230, v155, v157\nM 28,164,212\nbuffer_load_dwordx4 v220, s[4:7], s63 offen lds\n"
"v_min3_u32 v230, v230, v159, v159\nM 12,160,192\nR 182,185,36864\nv_max3_u32 v231, v129, v131, v133\nv_max3_u32 v231, v231, v135, v137\nM 12,164,208\n"
"s_waitcnt vmcnt(6)\ns_barrier\nG 8\nv_max3_u32 v231, v231, v139, v141\nM 0,168,224\nR 160,186,24576\n"
"v_max3_u32 v231, v231, v143, v145\nv_max3_u32 v231, v231, v147, v149\nG 8\nM 0,172,240\nbuffer_load_dwordx4 v221, s[4:7], s63 offen offset:1024 lds\nv_max3_u32 v231, v231, v151, v153\n"
"M 16,168,228\nR 162,186,28672\ns_add_u32 s63, s52, 196608\nv_max3_u32 v231, v231, v155, v157\nG 7\nM 16,172,244\n"
"s_add_u32 m0, s50, 81920\nv_max3_u32 v231, v231, v159, v159\nv_cmp_ne_u32 s[86:87], s46, v230\nv_cmp_ne_u32 s[88:89], s46, v231\nM 32,168,232\nR 164,186,32768\n"
"s_or_b64 s[86:87], s[86:87], s[88:89]\ns_cbranch_scc1 Lq146\nLq147:\nG 7\nM 32,172,248\nbuffer_load_dwordx4 v220, s[4:7], s63 offen lds\n"
"v_add_f32 v128, v128, v144\nM 48,168,236\nR 166,186,36864\ns_lshl_b32 s60, s90, 14\ns_add_u32 s70, s49, s60\ns_xor_b32 s71, s70, 0x10000\n"
"v_add_f32 v130, v130, v146\nG 6\nM 48,172,252\nG 4\nv_add_f32 v132, v132, v148\nv_add_f32 v134, v134, v150\n"
"v_add_f32 v136, v136, v152\nM 52,176,236\nR 168,187,24576\nv_add_f32 v138, v138, v154\nv_add_f32 v140, v140, v156\nM 52,180,252\n"
"buffer_load_dwordx4 v221, s[4:7], s63 offen offset:1024 lds\nv_add_f32 v142, v142, v158\nM 36,176,232\nR 170,187,28672\ns_add_u32 s63, s52, 393216\nv_add_f32 v128, v128, v136\n"
"M 36,180,248\ns_add_u32 m0, s50, 90112\nv_add_f32 v130, v130, v138\nv_add_f32 v132, v132, v140\nv_add_f32 v134, v134, v142\nM 20,176,228\n"
"R 172,187,32768\nv_add_f32 v128, v128, v132\nv_add_f32 v130, v130, v134\nM 20,180,244\nbuffer_load_dwordx4 v220, s[4:7], s63 offen lds\nv_lshrrev_b32 v231, 1, v252\n"
"M 4,176,224\nR 174,187,36864\nv_lshrrev_b32 v230, 2, v252\ns_lshl_b32 s62, s40, 9\ns_add_u32 s62, s62, 159232\nv_add_u32 v231, s62, v231\n"
"M 4,180,240\nds_write2_b32 v231, v128, v130 offset1:1\nG 5\nM 8,160,224\nR 176,184,40960\ns_lshl_b32 s62, s40, 9\n"
"s_add_u32 s62, s62, 159232\nv_add_u32 v230, s62, v230\nM 8,164,240\nbuffer_load_dwordx4 v221, s[4:7], s63 offen offset:1024 lds\nds_read2st64_b32 v[232:233], v230 offset1:1\nM 24,160,228\n"
"R 178,184,45056\nM 24,164,244\ns_add_u32 m0, s50, 98304\nM 40,160,232\nR 180,184,49152\nM 40,164,248\n"
"s_add_u32 s63, s52, 589824\nM 56,160,236\nR 182,184,53248\nM 56,164,252\nG 6\nG 3\n"
"v_add_f32 v232, v232, v233\nv_mov_b32 v233, v232\ns_nop 1\nv_permlane32_swap_b32 v232, v233\nM 60,168,236\nR 160,185,40960\n"
"v_mov_b32 v230, v232\nv_mov_b32 v231, v233\nM 60,172,252\nbuffer_load_dwordx4 v220, s[4:7], s63 offen lds\nv_permlane16_swap_b32 v230, v232\nM 44,168,232\n"
"R 162,185,45056\nv_permlane16_swap_b32 v231, v233\nv_mul_f32 v230, 0xba800000, v230\nM 44,172,248\nv_mul_f32 v214, v230, v230\nv_mul_f32 v231, 0x3a800000, v231\n"
"v_sub_f32 v231, v231, v214\nM 28,168,228\nR 164,185,49152\nv_max_f32 v231, 0, v231\nv_add_f32 v231, 0x3727c5ac, v231\nM 28,172,244\n"
"buffer_load_dwordx4 v221, s[4:7], s63 offen offset:1024 lds\nv_rsq_f32 v234, v231\nM 12,168,224\nR 166,185,53248\ns_mov_b32 s60, 156160\nv_bfe_u32 v246, v252, 4, 4\n"
"v_lshl_add_u32 v246, v246, 3, s60\nv_and_b32 v208, s77, v230\nM 12,172,240\nds_read_b64 v[238:239], v246 offset:0\ns_waitcnt vmcnt(8) lgkmcnt(0)\ns_barrier\n"
"v_mul_f32 v216, v231, v234\nN 64,192,176\nR 168,186,40960\nds_read_b64 v[240:241], v246 offset:128\nv_sub_f32 v230, v230, v208\nN 64,208,180\n"
"s_mov_b32 m0, s51\nds_read_b64 v[242:243], v246 offset:256\nv_cvt_pk_bf16_f32 v209, v230, v216\nN 68,196,176\nR 170,186,45056\ns_lshl2_add_u32 s60, s45, s40\n"
"v_lshlrev_b32 v224, 3, v252\ns_mul_i32 s60, s60, 7168\ns_bfm_b64 exec, 14, 0\nbuffer_load_dword v224, v224, s[12:15], s60 offen\ns_mov_b64 exec, -1\nds_read_b64 v[244:245], v246 offset:384\n"
"ds_write_addtid_b32 v32 offset:0\nv_cvt_pk_bf16_f32 v208, v208, v208\nN 68,212,180\nds_read_b64 v[246:247], v246 offset:0\nds_write_addtid_b32 v33 offset:256\nv_mov_b32 v214, v234\n"
"v_mul_f32 v215, 0x3e8293ee, v234\nN 96,200,176\nR 172,186,49152\nds_write_addtid_b32 v34 offset:512\nv_mul_f32 v232, 0xba800000, v232\nv_mul_f32 v217, v232, v232\n"
"N 96,216,180\nds_write_addtid_b32 v35 offset:768\nds_write_addtid_b32 v36 offset:1024\nv_mul_f32 v233, 0x3a800000, v233\nv_sub_f32 v233, v233, v217\nN 100,204,176\n"
"R 174,186,53248\nds_write_addtid_b32 v37 offset:1280\nv_max_f32 v233, 0, v233\nv_add_f32 v233, 0x3727c5ac, v233\nN 100,220,180\nds_write_addtid_b32 v38 offset:1536\n"
"v_rsq_f32 v235, v233\nv_and_b32 v212, s77, v232\nv_mul_f32 v219, v233, v235\nv_sub_f32 v232, v232, v212\nv_cvt_pk_bf16_f32 v213, v232, v219\nv_cvt_pk_bf16_f32 v212, v212, v212\n"
"v_mov_b32 v217, v235\nv_mul_f32 v218, 0x3e8293ee, v235\ns_mov_b64 s[60:61], 0xffff\nv_cndmask_b32 v208, 0, v208, s[60:61]\ns_mov_b64 s[60:61], 0xffff\nv_cndmask_b32 v209, 0, v209, s[60:61]\n"
"s_mov_b64 s[60:61], 0xffff\nv_cndmask_b32 v212, 0, v212, s[60:61]\ns_mov_b64 s[60:61], 0xffff\nv_cndmask_b32 v213, 0, v213, s[60:61]\nv_bfe_u32 v230, v252, 2, 6\ns_lshl_b32 s60, s40, 7\n"
"s_add_u32 s60, s60, 161280\nv_add_u32 v230, s60, v230\nds_write_b32 v230, v214 offset:0\nds_write_b32 v230, v217 offset:64\nds_write_addtid_b32 v39 offset:1792\nN 108,204,160\n"
"R 176,187,40960\nds_write_addtid_b32 v40 offset:2048\nN 108,220,164\nds_write_addtid_b32 v41 offset:2304\nds_write_addtid_b32 v42 offset:2560\nN 104,200,160\n"
"R 178,187,45056\nds_write_addtid_b32 v43 offset:2816\nN 104,216,164\nds_write_addtid_b32 v44 offset:3072\nds_write_addtid_b32 v45 offset:3328\nN 76,196,160\n"
"R 180,187,49152\nG 15\nv_mfma_f32_16x16x32_bf16 v[0:3], v[238:241], v[208:211], v[0:3]\nds_write_addtid_b32 v46 offset:3584\nN 76,212,164\nv_mfma_f32_16x16x32_bf16 v[4:7], v[240:243], v[208:211], v[4:7]\n"
"ds_write_addtid_b32 v47 offset:3840\nds_write_addtid_b32 v48 offset:4096\nN 72,192,160\nR 182,187,53248\nv_mfma_f32_16x16x32_bf16 v[8:11], v[242:245], v[208:211], v[8:11]\nds_write_addtid_b32 v49 offset:4352\n"
"N 72,208,164\nv_mfma_f32_16x16x32_bf16 v[12:15], v[244:247], v[208:211], v[12:15]\nds_write_addtid_b32 v50 offset:4608\nds_write_addtid_b32 v51 offset:4864\nG 15\nN 80,192,168\n"
"R 160,184,57344\nv_mfma_f32_16x16x32_bf16 v[16:19], v[244:247], v[210:213], v[16:19]\nds_write_addtid_b32 v52 offset:5120\nN 80,208,172\nv_mfma_f32_16x16x32_bf16 v[20:23], v[238:241], v[210:213], v[20:23]\nds_write_addtid_b32 v53 offset:5376\n"
"ds_write_addtid_b32 v54 offset:5632\nN 84,196,168\nR 162,184,61440\nv_mfma_f32_16x16x32_bf16 v[24:27], v[240:243], v[210:213], v[24:27]\nds_write_addtid_b32 v55 offset:5888\nN 84,212,172\n"
"v_mfma_f32_16x16x32_bf16 v[28:31], v[242:245], v[210:213], v[28:31]\nds_write_addtid_b32 v56 offset:6144\nds_write_addtid_b32 v57 offset:6400\nN 112,200,168\nR 164,188,0\nds_write_addtid_b32 v58 offset:6656\n"
"N 112,216,172\nds_write_addtid_b32 v59 offset:6912\nds_write_addtid_b32 v60 offset:7168\nN 116,204,168\nR 166,188,4096\nds_write_addtid_b32 v61 offset:7424\n"
"N 116,220,172\nds_write_addtid_b32 v62 offset:7680\nds_write_addtid_b32 v63 offset:7936\nG 15\nN 124,204,176\nR 168,185,57344\n"
"s_add_u32 m0, s50, 106496\ns_add_u32 s63, s52, 786432\nN 124,220,180\nbuffer_load_dwordx4 v220, s[4:7], s63 offen lds\nN 120,200,176\nR 170,185,61440\n"
"s_waitcnt vmcnt(6) lgkmcnt(2)\ns_barrier\nN 120,216,180\nds_read2st64_b32 v[32:33], v225 offset0:0 offset1:1\nds_read2st64_b32 v[34:35], v225 offset0:2 offset1:3\nN 92,196,176\n"
"R 172,189,0\nds_read2st64_b32 v[36:37], v225 offset0:4 offset1:5\nN 92,212,180\nbuffer_load_dwordx4 v221, s[4:7], s63 offen offset:1024 lds\nds_read2st64_b32 v[38:39], v225 offset0:6 offset1:7\nN 88,192,176\n"
"R 174,189,4096\nds_read2st64_b32 v[40:41], v225 offset0:8 offset1:9\nN 88,208,180\nds_read2st64_b32 v[42:43], v225 offset0:10 offset1:11\nds_read2st64_b32 v[44:45], v225 offset0:12 offset1:13\nN 64,224,160\n"
"R 176,186,57344\ns_mov_b32 s60, 156672\nv_bfe_u32 v246, v252, 4, 4\nv_lshl_add_u32 v246, v246, 3, s60\nds_read2st64_b32 v[46:47], v225 offset0:14 offset1:15\nN 64,240,164\n"
"ds_read_b64 v[238:239], v246 offset:0\ns_add_u32 m0, s50, 114688\nG 11\nv_add_f32 v0, v0, v32\nN 68,228,160\nR 178,186,61440\n"
"ds_read_b64 v[240:241], v246 offset:128\nv_add_f32 v1, v1, v33\nN 68,244,164\nds_read_b64 v[242:243], v246 offset:256\ns_add_u32 s63, s52, 983040\nG 13\n"
"v_add_f32 v2, v2, v34\nN 96,232,160\nR 180,190,0\nds_read_b64 v[244:245], v246 offset:384\nv_add_f32 v3, v3, v35\nN 96,248,164\n"
"ds_read_b64 v[246:247], v246 offset:0\nG 14\nv_add_f32 v4, v4, v36\nv_add_f32 v5, v5, v37\nN 100,236,160\nR 182,190,4096\n"
"buffer_load_dwordx4 v220, s[4:7], s63 offen lds\nG 14\nv_add_f32 v6, v6, v38\nN 100,252,164\nds_read2st64_b32 v[48:49], v225 offset0:16 offset1:17\nG 14\n"
"v_add_f32 v7, v7, v39\nN 108,236,168\nR 160,187,57344\nG 14\nv_add_f32 v8, v8, v40\nv_add_f32 v9, v9, v41\n"
"N 108,252,172\nbuffer_load_dwordx4 v221, s[4:7], s63 offen offset:1024 lds\nG 13\nv_add_f32 v10, v10, v42\nN 104,232,168\nR 162,187,61440\n"
"s_add_u32 m0, s50, 0\ns_add_u32 s63, s52, 1179648\nv_add_f32 v11, v11, v43\nN 104,248,172\nbuffer_load_dwordx4 v220, s[4:7], s63 offen lds\nG 13\n"
"v_add_f32 v12, v12, v44\nN 76,228,168\nR 164,191,0\nv_add_f32 v13, v13, v45\nG 12\nv_add_f32 v14, v14, v46\n"
"N 76,244,172\nG 9\nv_mfma_f32_16x16x32_bf16 v[64:67], v[208:211], v[238:241], v[64:67]\nds_read2st64_b32 v[50:51], v225 offset0:18 offset1:19\nv_add_f32 v15, v15, v47\nG 4\n"
"v_add_f32 v16, v16, v48\nN 72,224,168\nR 166,191,4096\nds_read2st64_b32 v[52:53], v225 offset0:20 offset1:21\nv_add_f32 v17, v17, v49\nN 72,240,172\n"
"v_mfma_f32_16x16x32_bf16 v[72:75], v[208:211], v[240:243], v[72:75]\nds_read2st64_b32 v[54:55], v225 offset0:22 offset1:23\nds_read2st64_b32 v[56:57], v225 offset0:24 offset1:25\nN 80,224,176\nR 168,188,8192\nbuffer_load_dwordx4 v221, s[4:7], s63 offen offset:1024 lds\n"
"ds_read2st64_b32 v[58:59], v225 offset0:26 offset1:27\nN 80,240,180\nv_mfma_f32_16x16x32_bf16 v[80:83], v[208:211], v[242:245], v[80:83]\ns_add_u32 m0, s50, 8192\ns_add_u32 s63, s52, 1376256\nbuffer_load_dwordx4 v220, s[4:7], s63 offen lds\n"
"ds_read2st64_b32 v[60:61], v225 offset0:28 offset1:29\nN 84,228,176\nR 170,188,12288\nds_read2st64_b32 v[62:63], v225 offset0:30 offset1:31\nN 84,244,180\nv_mfma_f32_16x16x32_bf16 v[88:91], v[208:211], v[244:247], v[88:91]\n"
"G 9\nv_add_f32 v18, v18, v50\nv_add_f32 v19, v19, v51\nN 112,232,176\nR 172,188,16384\nG 8\n"
"v_add_f32 v20, v20, v52\nv_add_f32 v21, v21, v53\nN 112,248,180\nv_mfma_f32_16x16x32_bf16 v[68:71], v[210:213], v[244:247], v[68:71]\nbuffer_load_dwordx4 v221, s[4:7], s63 offen offset:1024 lds\nG 7\n"
"v_add_f32 v22, v22, v54\nN 116,236,176\nR 174,188,20480\ns_add_u32 m0, s50, 32768\nv_add_f32 v23, v23, v55\nN 116,252,180\n"
"v_mfma_f32_16x16x32_bf16 v[76:79], v[210:213], v[238:241], v[76:79]\nG 7\nv_add_f32 v24, v24, v56\nv_add_f32 v25, v25, v57\nG 5\nv_add_f32 v26, v26, v58\n"
"N 124,236,160\nR 176,189,8192\ns_add_u32 s63, s52, 1572864\nv_add_f32 v27, v27, v59\nN 124,252,164\nv_mfma_f32_16x16x32_bf16 v[84:87], v[210:213], v[240:243], v[84:87]\n"
"buffer_load_dwordx4 v220, s[4:7], s63 offen lds\nG 5\nv_add_f32 v28, v28, v60\nv_add_f32 v29, v29, v61\nG 3\nv_add_f32 v30, v30, v62\n"
"v_add_f32 v31, v31, v63\nN 120,232,160\nR 178,189,12288\ns_lshl4_add_u32 s64, s41, s42\nv_lshrrev_b32 v227, 1, v252\nN 120,248,164\n"
"v_mfma_f32_16x16x32_bf16 v[92:95], v[210:213], v[242:245], v[92:95]\nbuffer_load_dwordx4 v221, s[4:7], s63 offen offset:1024 lds\nv_mul_f32 v16, v16, v217\nN 92,228,160\nR 180,189,16384\ns_add_u32 m0, s50, 40960\n"
"s_add_u32 s63, s52, 1769472\nv_mul_f32 v17, v17, v217\nN 92,244,164\nbuffer_load_dwordx4 v220, s[4:7], s63 offen lds\nv_mul_f32 v18, v18, v217\nN 88,224,160\n"
"R 182,189,20480\ns_lshl_b32 s64, s64, 12\nv_mul_f32 v19, v19, v217\nN 88,240,164\ns_waitcnt vmcnt(12)\ns_barrier\n"
"G 4\nv_mul_f32 v20, v20, v217\nv_mfma_f32_16x16x32_bf16 v[128:131], v[168:171], a[0:3], 0\nR 160,190,8192\ns_add_u32 s64, s64, 77594624\nv_mul_f32 v21, v21, v217\n"
"M 128,172,16\ns_mov_b32 m0, s51\ns_cmp_eq_u32 s40, 3\ns_cselect_b64 s[66:67], -1, 0\nv_mul_f32 v22, v22, v217\nv_mfma_f32_16x16x32_bf16 v[144:147], v[168:171], a[4:7], 0\n"
"R 162,190,12288\ns_lshl2_add_u32 s60, s45, s40\ns_waitcnt vmcnt(11)\nv_lshlrev_b32 v224, 3, v252\ns_mul_i32 s60, s60, 7168\ns_bfm_b64 exec, 14, 14\n"
"buffer_load_dword v224, v224, s[12:15], s60 offen\ns_mov_b64 exec, -1\nv_mul_f32 v23, v23, v217\nM 144,172,20\nv_mul_f32 v24, v24, v217\nv_mul_f32 v25, v25, v217\n"
"v_mul_f32 v26, v26, v217\nv_mfma_f32_16x16x32_bf16 v[32:35], v[168:171], a[8:11], 0\nR 164,190,16384\nv_mul_f32 v27, v27, v217\nv_mul_f32 v28, v28, v217\nM 32,172,24\n"
"v_mul_f32 v29, v29, v217\nv_mul_f32 v30, v30, v217\nv_mul_f32 v31, v31, v217\nv_mfma_f32_16x16x32_bf16 v[48:51], v[168:171], a[12:15], 0\nR 166,190,20480\nds_write_addtid_b32 v96 offset:0\n"
"v_cvt_pk_bf16_f32 v16, v16, v17\nv_cvt_pk_bf16_f32 v17, v18, v19\nM 48,172,28\nds_write_addtid_b32 v97 offset:256\nG 6\nv_cvt_pk_bf16_f32 v18, v20, v21\n"
"v_cvt_pk_bf16_f32 v19, v22, v23\nv_cvt_pk_bf16_f32 v20, v24, v25\nv_mfma_f32_16x16x32_bf16 v[52:55], v[176:179], a[12:15], 0\nR 168,191,8192\nds_write_addtid_b32 v98 offset:512\ns_lshr_b32 s100, s41, 1\n"
"s_mul_i32 s100, s100, 458752\ns_add_u32 s100, s100, s55\nv_cvt_pk_bf16_f32 v21, v26, v27\nv_cvt_pk_bf16_f32 v22, v28, v29\nM 52,180,28\nds_write_addtid_b32 v99 offset:768\n"
"s_mul_i32 s101, s41, 229376\ns_add_u32 s101, s101, s54\nv_cvt_pk_bf16_f32 v23, v30, v31\nv_mul_f32 v0, v0, v214\nv_mul_f32 v1, v1, v214\nv_mfma_f32_16x16x32_bf16 v[36:39], v[176:179], a[8:11], 0\n"
"R 170,191,12288\nds_write_addtid_b32 v100 offset:1024\nv_mul_f32 v2, v2, v214\nv_mul_f32 v3, v3, v214\nM 36,180,24\nds_write_addtid_b32 v101 offset:1280\n"
"v_mul_f32 v4, v4, v214\nv_mul_f32 v5, v5, v214\nv_mul_f32 v6, v6, v214\nv_mfma_f32_16x16x32_bf16 v[148:151], v[176:179], a[4:7], 0\nR 172,191,16384\nds_write_addtid_b32 v102 offset:1536\n"
"v_mul_f32 v7, v7, v214\nv_mul_f32 v8, v8, v214\nM 148,180,20\nds_write_addtid_b32 v103 offset:1792\nv_mul_f32 v9, v9, v214\nv_mul_f32 v10, v10, v214\n"
"v_mul_f32 v11, v11, v214\nv_mfma_f32_16x16x32_bf16 v[132:135], v[176:179], a[0:3], 0\nR 174,191,20480\nds_write_addtid_b32 v104 offset:2048\nv_mul_f32 v12, v12, v214\nv_mul_f32 v13, v13, v214\n"
"M 132,180,16\nds_write_addtid_b32 v105 offset:2304\nG 14\nv_mul_f32 v14, v14, v214\nv_mul_f32 v15, v15, v214\nv_cvt_pk_bf16_f32 v0, v0, v1\n"
"v_mfma_f32_16x16x32_bf16 v[136:139], v[160:163], a[0:3], 0\nR 176,188,24576\nds_write_addtid_b32 v106 offset:2560\nv_cvt_pk_bf16_f32 v1, v2, v3\nv_cvt_pk_bf16_f32 v2, v4, v5\nM 136,164,16\n"
"ds_write_addtid_b32 v107 offset:2816\nv_cvt_pk_bf16_f32 v3, v6, v7\nv_cvt_pk_bf16_f32 v4, v8, v9\nv_cvt_pk_bf16_f32 v5, v10, v11\nv_mfma_f32_16x16x32_bf16 v[152:155], v[160:163], a[4:7], 0\nR 178,188,28672\n"
"ds_write_addtid_b32 v108 offset:3072\nv_cvt_pk_bf16_f32 v6, v12, v13\nv_cvt_pk_bf16_f32 v7, v14, v15\nM 152,164,20\nds_write_addtid_b32 v109 offset:3328\nds_write_addtid_b32 v110 offset:3584\n"
"s_mov_b64 exec, s[66:67]\nbuffer_store_dwordx4 v[16:19], v252, s[20:23], s64 offen offset:0 sc1\nbuffer_store_dwordx4 v[20:23], v252, s[20:23], s64 offen offset:1024 sc1\ns_mov_b64 exec, -1\nv_mfma_f32_16x16x32_bf16 v[40:43], v[160:163], a[8:11], 0\nR 180,188,32768\n"
"ds_write_addtid_b32 v111 offset:3840\nM 40,164,24\nds_write_addtid_b32 v112 offset:4096\nds_write_addtid_b32 v113 offset:4352\nv_mfma_f32_16x16x32_bf16 v[56:59], v[160:163], a[12:15], 0\nR 182,188,36864\n"
"ds_write_addtid_b32 v114 offset:4608\nM 56,164,28\nds_write_addtid_b32 v115 offset:4864\nds_write_addtid_b32 v116 offset:5120\nG 15\nv_mfma_f32_16x16x32_bf16 v[60:63], v[168:171], a[12:15], 0\n"
"R 160,189,24576\nds_write_addtid_b32 v117 offset:5376\nM 60,172,28\nds_write_addtid_b32 v118 offset:5632\nds_write_addtid_b32 v119 offset:5888\nv_mfma_f32_16x16x32_bf16 v[44:47], v[168:171], a[8:11], 0\n"
"R 162,189,28672\nds_write_addtid_b32 v120 offset:6144\nM 44,172,24\nds_write_addtid_b32 v121 offset:6400\nds_write_addtid_b32 v122 offset:6656\nds_write_addtid_b32 v123 offset:6912\n"
"ds_write_addtid_b32 v124 offset:7168\nds_write_addtid_b32 v125 offset:7424\nds_write_addtid_b32 v126 offset:7680\nds_write_addtid_b32 v127 offset:7936\nv_mfma_f32_16x16x32_bf16 v[156:159], v[168:171], a[4:7], 0\nR 164,189,32768\n"
"s_add_u32 m0, s50, 40960\nM 156,172,20\nbuffer_load_dwordx4 v221, s[4:7], s63 offen offset:1024 lds\nv_mfma_f32_16x16x32_bf16 v[140:143], v[168:171], a[0:3], 0\nR 166,189,36864\ns_waitcnt vmcnt(11) lgkmcnt(2)\n"
"s_barrier\nM 140,172,16\nds_read2st64_b32 v[96:97], v225 offset0:0 offset1:1\nds_read2st64_b32 v[98:99], v225 offset0:2 offset1:3\nv_lshrrev_b32 v226, 8, v252\nM 128,176,32\n"
"R 168,190,24576\ns_add_u32 s63, s52, 1966080\nds_read2st64_b32 v[100:101], v225 offset0:4 offset1:5\ns_lshl_b32 s60, s40, 7\ns_add_u32 s60, s60, 161280\nv_lshl_add_u32 v226, v226, 4, s60\n"
"M 128,180,48\ns_add_u32 m0, s50, 49152\nds_read2st64_b32 v[102:103], v225 offset0:6 offset1:7\nds_read2st64_b32 v[104:105], v225 offset0:8 offset1:9\nds_read_b128 v[230:233], v226 offset:0\nM 144,176,36\n"
"R 170,190,28672\nds_read2st64_b32 v[106:107], v225 offset0:10 offset1:11\nds_read_b128 v[234:237], v226 offset:64\nM 144,180,52\nbuffer_load_dwordx4 v220, s[4:7], s63 offen lds\nds_read2st64_b32 v[108:109], v225 offset0:12 offset1:13\n"
"M 32,176,40\nR 172,190,32768\nds_read2st64_b32 v[110:111], v225 offset0:14 offset1:15\nM 32,180,56\nds_read2st64_b32 v[112:113], v225 offset0:16 offset1:17\nG 13\n"
"v_add_f32 v64, v64, v96\nv_add_f32 v65, v65, v97\nM 48,176,44\nR 174,190,36864\nG 13\nv_add_f32 v66, v66, v98\n"
"v_add_f32 v67, v67, v99\nM 48,180,60\nds_read2st64_b32 v[114:115], v225 offset0:18 offset1:19\nG 12\nv_add_f32 v68, v68, v100\nM 52,160,44\n"
"R 176,191,24576\nv_add_f32 v69, v69, v101\nG 12\nv_add_f32 v70, v70, v102\nM 52,164,60\nbuffer_load_dwordx4 v221, s[4:7], s63 offen offset:1024 lds\n"
"v_add_f32 v71, v71, v103\nM 36,160,40\nR 178,191,28672\nG 12\nv_add_f32 v72, v72, v104\nv_add_f32 v73, v73, v105\n"
"M 36,164,56\ns_add_u32 m0, s50, 57344\nds_read2st64_b32 v[116:117], v225 offset0:20 offset1:21\nG 10\nv_add_f32 v74, v74, v106\nM 148,160,36\n"
"R 180,191,32768\nv_add_f32 v75, v75, v107\nG 9\nv_add_f32 v76, v76, v108\nM 148,164,52\ns_add_u32 s63, s52, 2162688\n"
"ds_read2st64_b32 v[118:119], v225 offset0:22 offset1:23\nv_add_f32 v77, v77, v109\nM 132,160,32\nR 182,191,36864\nG 9\nv_add_f32 v78, v78, v110\n"
"v_add_f32 v79, v79, v111\nM 132,164,48\nds_read2st64_b32 v[120:121], v225 offset0:24 offset1:25\nG 8\nv_add_f32 v80, v80, v112\nM 136,168,32\n"
"R 160,188,40960\nv_add_f32 v81, v81, v113\nG 8\nv_add_f32 v82, v82, v114\nM 136,172,48\nbuffer_load_dwordx4 v220, s[4:7], s63 offen lds\n"
"v_add_f32 v83, v83, v115\nM 152,168,36\nR 162,188,45056\nG 6\nv_add_f32 v84, v84, v116\nv_add_f32 v85, v85, v117\n"
"M 152,172,52\nds_read2st64_b32 v[122:123], v225 offset0:26 offset1:27\nG 5\nv_add_f32 v86, v86, v118\nv_add_f32 v87, v87, v119\nM 40,168,40\n"
"R 164,188,49152\nds_read2st64_b32 v[124:125], v225 offset0:28 offset1:29\nM 40,172,56\nbuffer_load_dwordx4 v221, s[4:7], s63 offen offset:1024 lds\nG 5\nv_add_f32 v88, v88, v120\n"
"M 56,168,44\nR 166,188,53248\ns_add_u32 m0, s50, 65536\nM 56,172,60\nds_read2st64_b32 v[126:127], v225 offset0:30 offset1:31\nv_add_f32 v89, v89, v121\n"
"M 60,176,44\nR 168,189,40960\ns_add_u32 s63, s52, 2359296\nM 60,180,60\nbuffer_load_dwordx4 v220, s[4:7], s63 offen lds\nM 44,176,40\n"
"R 170,189,45056\nG 6\nv_add_f32 v90, v90, v122\nM 44,180,56\nv_add_f32 v91, v91, v123\nG 4\n"
"v_add_f32 v92, v92, v124\nM 156,176,36\nR 172,189,49152\nv_add_f32 v93, v93, v125\nM 156,180,52\nbuffer_load_dwordx4 v221, s[4:7], s63 offen offset:1024 lds\n"
"M 140,176,32\nR 174,189,53248\ns_add_u32 m0, s50, 73728\nM 140,180,48\nG 5\nG 4\n"
"v_add_f32 v94, v94, v126\nv_add_f32 v95, v95, v127\nM 128,160,64\nR 176,190,40960\ns_add_u32 s63, s52, 2555904\nv_mul_f32 v64, v64, v230\n"
"M 128,164,80\nbuffer_load_dwordx4 v220, s[4:7], s63 offen lds\nv_mul_f32 v65, v65, v231\nM 144,160,68\nR 178,190,45056\nv_mul_f32 v66, v66, v232\n"
"v_mul_f32 v67, v67, v233\nM 144,164,84\nv_mul_f32 v68, v68, v234\nv_mul_f32 v69, v69, v235\nv_mul_f32 v70, v70, v236\nM 32,160,72\n"
"R 180,190,49152\nv_mul_f32 v71, v71, v237\nv_cvt_pk_bf16_f32 v67, v66, v67\nM 32,164,88\nbuffer_load_dwordx4 v221, s[4:7], s63 offen offset:1024 lds\nv_cvt_pk_bf16_f32 v66, v64, v65\n"
"M 48,160,76\nR 182,190,53248\ns_add_u32 s63, s52, 2752512\nv_cvt_pk_bf16_f32 v68, v68, v69\nM 48,164,92\nG 4\n"
"v_cvt_pk_bf16_f32 v69, v70, v71\nv_mul_f32 v72, v72, v230\nv_mul_f32 v73, v73, v231\nM 52,168,76\nR 160,191,40960\nv_mul_f32 v74, v74, v232\n"
"v_mul_f32 v75, v75, v233\nM 52,172,92\ns_add_u32 m0, s50, 81920\nv_mul_f32 v76, v76, v234\nv_mul_f32 v77, v77, v235\nv_mul_f32 v78, v78, v236\n"
"M 36,168,72\nR 162,191,45056\nv_mul_f32 v79, v79, v237\nv_cvt_pk_bf16_f32 v72, v72, v73\nM 36,172,88\nbuffer_load_dwordx4 v220, s[4:7], s63 offen lds\n"
"v_cvt_pk_bf16_f32 v73, v74, v75\nM 148,168,68\nR 164,191,49152\nv_cvt_pk_bf16_f32 v74, v76, v77\nv_cvt_pk_bf16_f32 v75, v78, v79\nM 148,172,84\n"
"v_mul_f32 v80, v80, v230\nv_mul_f32 v81, v81, v231\nv_mul_f32 v82, v82, v232\nM 132,168,64\nR 166,191,53248\ns_waitcnt vmcnt(9)\n"
"s_barrier\nv_mul_f32 v83, v83, v233\nM 132,172,80\nG 4\nv_mul_f32 v84, v84, v234\nv_mul_f32 v85, v85, v235\n"
"v_mul_f32 v86, v86, v236\nM 136,176,64\nR 168,184,0\ns_lshl2_add_u32 s60, s45, s40\nv_lshlrev_b32 v224, 3, v252\ns_mul_i32 s60, s60, 7168\n"
"s_bfm_b64 exec, 14, 28\nbuffer_load_dword v224, v224, s[12:15], s60 offen\ns_mov_b64 exec, -1\nv_mul_f32 v87, v87, v237\nM 136,180,80\nv_cvt_pk_bf16_f32 v78, v80, v81\n"
"v_cvt_pk_bf16_f32 v79, v82, v83\nv_cvt_pk_bf16_f32 v80, v84, v85\nM 152,176,68\nR 170,184,4096\nv_cvt_pk_bf16_f32 v81, v86, v87\nv_mul_f32 v88, v88, v230\n"
"M 152,180,84\nbuffer_load_dwordx4 v221, s[4:7], s63 offen offset:1024 lds\nv_mul_f32 v89, v89, v231\nM 40,176,72\nR 172,184,8192\ns_mov_b32 s60, 155648\n"
"v_bfe_u32 v246, v252, 4, 4\nv_lshl_add_u32 v246, v246, 3, s60\nv_mul_f32 v90, v90, v232\nM 40,180,88\ns_add_u32 m0, s50, 90112\nv_mul_f32 v91, v91, v233\n"
"v_mul_f32 v92, v92, v234\nv_mul_f32 v93, v93, v235\nM 56,176,76\nR 174,184,12288\nds_read_b64 v[238:239], v246 offset:0\nv_mul_f32 v94, v94, v236\n"
"M 56,180,92\nG 5\nv_mul_f32 v95, v95, v237\nv_cvt_pk_bf16_f32 v84, v88, v89\nv_cvt_pk_bf16_f32 v85, v90, v91\nM 60,160,76\n"
"R 176,185,0\nds_read_b64 v[240:241], v246 offset:128\nv_cvt_pk_bf16_f32 v86, v92, v93\nM 60,164,92\ns_add_u32 s63, s52, 2949120\nv_cvt_pk_bf16_f32 v87, v94, v95\n"
"v_add_u32 v108, 161792, v252\ns_lshl_b32 s60, s40, 12\ns_add_u32 s60, s60, 53248\nv_add_u32 v230, s60, v252\nv_add_u32 v231, s60, v227\nM 44,160,72\n"
"R 178,185,4096\nds_read_b64 v[242:243], v246 offset:256\nM 44,164,88\nbuffer_load_dwordx4 v220, s[4:7], s63 offen lds\nM 156,160,68\nR 180,185,8192\n"
"ds_read_b64 v[244:245], v246 offset:384\nM 156,164,84\nds_read_b128 v[112:115], v108\nM 140,160,64\nR 182,185,12288\nds_read_b64 v[246:247], v246 offset:0\n"
"M 140,164,80\nds_read_b128 v[104:107], v108 offset:1024\ns_mov_b64 exec, s[66:67]\nbuffer_store_dwordx2 v[68:69], v227, s[20:23], s64 offen offset:2048 sc1\nbuffer_store_dwordx2 v[74:75], v227, s[20:23], s64 offen offset:2560 sc1\ns_mov_b64 exec, -1\n"
"G 11\nM 128,168,96\nR 160,186,0\nM 128,172,112\nM 144,168,100\nR 162,186,4096\n"
"M 144,172,116\nbuffer_load_dwordx4 v221, s[4:7], s63 offen offset:1024 lds\nM 32,168,104\nR 164,186,8192\nG 11\nv_mfma_f32_16x16x32_bf16 v[128:131], v[238:241], v[208:211], v[128:131]\n"
"v_mov_b32 v124, s76\nM 32,172,120\nG 3\nv_lshlrev_b32 v100, 16, v104\nv_and_b32 v101, s77, v104\nM 48,168,108\n"
"R 166,186,12288\nv_mfma_f32_16x16x32_bf16 v[132:135], v[240:243], v[208:211], v[132:135]\nv_lshlrev_b32 v102, 16, v105\nM 48,172,124\nv_and_b32 v103, s77, v105\nv_lshlrev_b32 v96, 16, v106\n"
"M 52,176,108\nR 168,187,0\nv_mfma_f32_16x16x32_bf16 v[144:147], v[244:247], v[210:213], v[144:147]\nv_and_b32 v97, s77, v106\nM 52,180,124\ns_movk_i32 s63, 7168\n"
"v_mad_u32_u24 v220, v228, s63, v229\nv_add_u32 v221, 3668992, v220\ns_add_u32 m0, s50, 98304\ns_add_u32 s63, s101, 0\nbuffer_load_dwordx4 v220, s[12:15], s63 offen lds\nM 36,176,104\n"
"R 170,187,4096\nv_mfma_f32_16x16x32_bf16 v[148:151], v[238:241], v[210:213], v[148:151]\nv_lshlrev_b32 v98, 16, v107\nM 36,180,120\nv_and_b32 v99, s77, v107\nv_add_f32 v104, s78, v100\n"
"M 148,176,100\nR 172,187,8192\nv_add_f32 v105, s78, v101\nM 148,180,116\ns_mov_b64 exec, s[66:67]\nbuffer_store_dwordx2 v[80:81], v227, s[20:23], s64 offen offset:3072 sc1\n"
"buffer_store_dwordx2 v[86:87], v227, s[20:23], s64 offen offset:3584 sc1\ns_mov_b64 exec, -1\nv_add_f32 v106, s78, v102\nv_add_f32 v107, s78, v103\nM 132,176,96\nR 174,187,12288\n"
"s_not_b64 exec, s[66:67]\nds_write_b128 v230, v[16:19] offset:0\nds_write_b128 v230, v[20:23] offset:1024\ns_mov_b64 exec, -1\nv_mov_b32 v125, s76\nM 132,180,112\n"
"G 6\nv_mov_b32 v126, s76\nv_mov_b32 v127, s76\nM 136,160,96\nR 176,184,32768\nM 136,164,112\n"
"buffer_load_dwordx4 v221, s[12:15], s63 offen offset:1024 lds\nM 152,160,100\nR 178,184,36864\ns_not_b64 exec, s[66:67]\nds_write_b64 v231, v[68:69] offset:2048\nds_write_b64 v231, v[74:75] offset:2560\n"
"s_mov_b64 exec, -1\nM 152,164,116\nM 40,160,104\nR 180,184,40960\nM 40,164,120\nM 56,160,108\n"
"R 182,184,45056\nM 56,164,124\nG 8\nM 60,168,108\nR 160,185,32768\ns_not_b64 exec, s[66:67]\n"
"ds_write_b64 v231, v[80:81] offset:3072\nds_write_b64 v231, v[86:87] offset:3584\ns_mov_b64 exec, -1\nM 60,172,124\nM 44,168,104\nR 162,185,36864\n"
"M 44,172,120\ns_add_u32 m0, s50, 106496\nM 156,168,100\nR 164,185,40960\nM 156,172,116\ns_add_u32 s63, s101, 128\n"
"M 140,168,96\nR 166,185,45056\nM 140,172,112\nG 6\nM 128,176,128\nR 168,186,32768\n"
"s_waitcnt vmcnt(13) lgkmcnt(4)\ns_barrier\nM 128,180,144\nbuffer_load_dwordx4 v220, s[12:15], s63 offen lds\nM 144,176,132\nR 170,186,36864\n"
"M 144,180,148\ns_cmp_eq_u32 s78, 0\ns_cbranch_scc1 Lq163\nv_mov_b32 v230, 0\nv_mov_b32 v231, 0\nv_mov_b32 v232, 0\n"
"v_mov_b32 v233, 0\nv_mov_b32 v64, 0\nv_mov_b32 v65, 0\nv_mov_b32 v70, 0\nv_mov_b32 v71, 0\nv_mov_b32 v234, 0\n"
"v_mov_b32 v235, 0\nv_mov_b32 v236, 0\nv_mov_b32 v237, 0\nv_mov_b32 v76, 0\nv_mov_b32 v77, 0\nv_mov_b32 v82, 0\n"
"v_mov_b32 v83, 0\nLq163:\ns_lshl4_add_u32 s65, s41, s42\nM 32,176,136\nR 172,186,40960\ns_lshl_b32 s65, s65, 12\n"
"M 32,180,152\ns_add_u32 s65, s65, 77529088\nM 48,176,140\nR 174,186,45056\nM 48,180,156\nG 4\n"
"M 52,160,140\nR 176,187,32768\nM 52,164,156\nbuffer_load_dwordx4 v221, s[12:15], s63 offen offset:1024 lds\nM 36,160,136\nR 178,187,36864\n"
"M 36,164,152\ns_add_u32 m0, s50, 114688\nM 148,160,132\nR 180,187,40960\nM 148,164,148\ns_add_u32 s63, s101, 256\n"
"M 132,160,128\nR 182,187,45056\nM 132,164,144\nG 4\nM 136,168,128\nR 160,184,49152\n"
"M 136,172,144\nbuffer_load_dwordx4 v220, s[12:15], s63 offen lds\nM 152,168,132\nR 162,184,53248\nM 152,172,148\nM 40,168,136\n"
"R 164,184,57344\ns_lshl2_add_u32 s60, s45, s40\ns_waitcnt vmcnt(12)\nv_lshlrev_b32 v224, 3, v252\ns_mul_i32 s60, s60, 7168\ns_bfm_b64 exec, 14, 42\n"
"buffer_load_dword v224, v224, s[12:15], s60 offen\ns_mov_b64 exec, -1\nM 40,172,152\nM 56,168,140\nR 166,184,61440\nv_mfma_f32_16x16x32_bf16 v[136:139], v[242:245], v[208:211], v[136:139]\n"
"M 56,172,156\nG 4\nM 60,176,140\nR 168,185,49152\nv_mfma_f32_16x16x32_bf16 v[140:143], v[244:247], v[208:211], v[140:143]\nM 60,180,156\n"
"buffer_load_dwordx4 v221, s[12:15], s63 offen offset:1024 lds\nM 44,176,136\nR 170,185,53248\nv_mfma_f32_16x16x32_bf16 v[152:155], v[240:243], v[210:213], v[152:155]\ns_add_u32 s63, s100, 0\nM 44,180,152\n"
"s_add_u32 m0, s50, 0\nM 156,176,132\nR 172,185,57344\nv_mfma_f32_16x16x32_bf16 v[156:159], v[242:245], v[210:213], v[156:159]\nM 156,180,148\nbuffer_load_dwordx4 v222, s[12:15], s63 offen lds\n"
"M 140,176,128\nR 174,185,61440\nM 140,180,144\nG 4\nM 128,160,160\nR 176,186,49152\n"
"M 128,164,176\nM 144,160,164\nR 178,186,53248\nM 144,164,180\nbuffer_load_dwordx4 v223, s[12:15], s63 offen offset:1024 lds\nM 32,160,168\n"
"R 180,186,57344\nM 32,164,184\nM 48,160,172\nR 182,186,61440\nM 48,164,188\nG 4\n"
"M 52,168,172\nR 160,187,49152\nM 52,172,188\nM 36,168,168\nR 162,187,53248\nM 36,172,184\n"
"s_movk_i32 s63, 2048\nv_mad_u32_u24 v220, v228, s63, v229\nv_add_u32 v221, 1047552, v220\ns_add_u32 m0, s50, 8192\ns_add_u32 s63, s53, 0\nbuffer_load_dwordx4 v220, s[8:11], s63 offen lds\n"
"M 148,168,164\nR 164,187,57344\ns_lshl_b32 s60, s40, 12\ns_add_u32 s60, s60, 49152\nv_add_u32 v226, s60, v252\nv_add_u32 v111, s60, v227\n"
"M 148,172,180\nM 132,168,160\nR 166,187,61440\ns_cmp_eq_u32 s40, 0\ns_cselect_b64 s[88:89], 0, -1\ns_cselect_b32 s60, s41, 0\n"
"s_cmp_lg_u32 s60, 0\ns_cselect_b64 s[66:67], -1, 0\nM 132,172,176\nG 4\nM 136,176,160\nR 168,188,0\n"
"M 136,180,176\ns_mov_b64 exec, s[88:89]\nds_read_b128 v[230:233], v226 offset:0\nds_read_b64 v[64:65], v111 offset:2048\ns_mov_b64 exec, -1\nM 152,176,164\n"
"R 170,188,4096\ns_waitcnt vmcnt(12) lgkmcnt(1)\ns_barrier\nM 152,180,180\nbuffer_load_dwordx4 v221, s[8:11], s63 offen offset:1024 lds\nM 40,176,168\n"
"R 172,188,8192\nM 40,180,184\ns_mov_b64 exec, s[88:89]\nds_read_b64 v[70:71], v111 offset:2560\nds_read_b128 v[234:237], v226 offset:1024\ns_mov_b64 exec, -1\n"
"M 56,176,172\nR 174,188,12288\nM 56,180,188\nM 60,160,172\nR 176,189,0\nM 60,164,188\n"
"s_mov_b64 exec, s[88:89]\nds_read_b64 v[76:77], v111 offset:3072\nds_read_b64 v[82:83], v111 offset:3584\ns_mov_b64 exec, -1\nM 44,160,168\nR 178,189,4096\n"
"M 44,164,184\nM 156,160,164\nR 180,189,8192\nM 156,164,180\nM 140,160,160\nR 182,189,12288\n"
"M 140,164,176\nG 6\nM 32,168,200\nR 160,190,0\ns_mov_b64 exec, s[66:67]\nbuffer_load_dwordx4 v[230:233], v252, s[20:23], s65 offen offset:0 sc1\n"
"buffer_load_dwordx2 v[64:65], v227, s[20:23], s65 offen offset:2048 sc1\ns_mov_b64 exec, -1\nM 32,172,216\nR 162,190,4096\nM 48,168,204\nR 164,190,8192\n"
"M 48,172,220\nR 166,190,12288\nG 4\nM 52,176,204\nR 168,191,0\nM 52,180,220\n"
"R 170,191,4096\ns_mov_b64 exec, s[66:67]\nbuffer_load_dwordx2 v[70:71], v227, s[20:23], s65 offen offset:2560 sc1\nbuffer_load_dwordx4 v[234:237], v252, s[20:23], s65 offen offset:1024 sc1\ns_mov_b64 exec, -1\nM 36,176,200\n"
"R 172,191,8192\nM 36,180,216\nR 174,191,12288\nG 4\nM 40,160,200\nR 176,188,16384\n"
"M 40,164,216\nR 178,188,20480\nM 56,160,204\nR 180,188,24576\ns_mov_b64 exec, s[66:67]\nbuffer_load_dwordx2 v[76:77], v227, s[20:23], s65 offen offset:3072 sc1\n"
"buffer_load_dwordx2 v[82:83], v227, s[20:23], s65 offen offset:3584 sc1\ns_mov_b64 exec, -1\nM 56,164,220\nR 182,188,28672\nG 4\nM 60,168,204\n"
"R 160,189,16384\nM 60,172,220\nR 162,189,20480\nM 44,168,200\nR 164,189,24576\nM 44,172,216\n"
"R 166,189,28672\nG 4\nM 32,176,232\nR 168,190,16384\nLq194:\ns_lshr_b32 s60, s45, 3\n"
"s_lshl2_add_u32 s60, s60, s40\ns_lshl_b32 s60, s60, 8\ns_waitcnt vmcnt(11)\nv_lshl_add_u32 v224, v252, 3, s60\nv_add_u32 v224, Lq194+4096-LqPC, v224\ns_mov_b64 exec, 3\n"
"global_load_dword v224, v224, s[98:99]\ns_mov_b64 exec, -1\nM 32,180,248\nR 170,190,20480\nM 48,176,236\nR 172,190,24576\n"
"M 48,180,252\nR 174,190,28672\nG 4\nM 52,160,236\nR 176,191,16384\nM 52,164,252\n"
"R 178,191,20480\nM 36,160,232\nR 180,191,24576\nM 36,164,248\nR 182,191,28672\nG 4\n"
"M 40,168,232\nR 160,188,0\nM 40,172,248\nR 162,188,4096\nM 56,168,236\nR 164,188,8192\n"
"M 56,172,252\nR 166,188,12288\nG 4\nM 60,176,236\nR 168,189,0\nM 60,180,252\n"
"R 170,189,4096\nM 44,176,232\nR 172,189,8192\nM 44,180,248\nR 174,189,12288\nG 4\n"
"M 128,160,192\nR 176,190,0\nM 128,164,208\nR 178,190,4096\ns_mov_b32 m0, s51\nM 144,160,196\n"
"R 180,190,8192\nds_write_addtid_b32 v32 offset:0\nM 144,164,212\nR 182,190,12288\nds_write_addtid_b32 v33 offset:256\nG 6\n"
"M 148,168,196\nR 160,191,0\nds_write_addtid_b32 v34 offset:512\nM 148,172,212\nR 162,191,4096\nds_write_addtid_b32 v35 offset:768\n"
"M 132,168,192\nR 164,191,8192\nds_write_addtid_b32 v36 offset:1024\nM 132,172,208\nR 166,191,12288\nds_write_addtid_b32 v37 offset:1280\n"
"G 9\nM 136,176,192\nR 168,188,16384\nds_write_addtid_b32 v38 offset:1536\nM 136,180,208\nR 170,188,20480\n"
"ds_write_addtid_b32 v39 offset:1792\nM 152,176,196\nR 172,188,24576\nds_write_addtid_b32 v40 offset:2048\nM 152,180,212\nR 174,188,28672\n"
"ds_write_addtid_b32 v41 offset:2304\nG 9\nM 156,160,196\nR 176,189,16384\nds_write_addtid_b32 v42 offset:2560\nM 156,164,212\n"
"R 178,189,20480\nds_write_addtid_b32 v43 offset:2816\nM 140,160,192\nR 180,189,24576\nds_write_addtid_b32 v44 offset:3072\nds_write_addtid_b32 v45 offset:3328\n"
"ds_write_addtid_b32 v46 offset:3584\nds_write_addtid_b32 v47 offset:3840\nds_write_addtid_b32 v48 offset:4096\nds_write_addtid_b32 v49 offset:4352\nds_write_addtid_b32 v50 offset:4608\nds_write_addtid_b32 v51 offset:4864\n"
"ds_write_addtid_b32 v52 offset:5120\nds_write_addtid_b32 v53 offset:5376\nds_write_addtid_b32 v54 offset:5632\nds_write_addtid_b32 v55 offset:5888\nds_write_addtid_b32 v56 offset:6144\nds_write_addtid_b32 v57 offset:6400\n"
"ds_write_addtid_b32 v58 offset:6656\nds_write_addtid_b32 v59 offset:6912\nds_write_addtid_b32 v60 offset:7168\nds_write_addtid_b32 v61 offset:7424\nds_write_addtid_b32 v62 offset:7680\nds_write_addtid_b32 v63 offset:7936\n"
"M 140,164,208\nR 182,189,28672\nG 15\nM 128,168,224\nR 160,190,16384\ns_add_u32 m0, s50, 16384\n"
"M 128,172,240\nR 162,190,20480\nG 3\ns_barrier\ns_add_u32 s63, s53, 65536\nM 144,168,228\n"
"R 164,190,24576\nbuffer_load_dwordx4 v220, s[8:11], s63 offen lds\nds_read2st64_b32 v[32:33], v225 offset0:0 offset1:1\nM 144,172,244\nR 166,190,28672\nbuffer_load_dwordx4 v221, s[8:11], s63 offen offset:1024 lds\n"
"ds_read2st64_b32 v[34:35], v225 offset0:2 offset1:3\nG 6\nM 148,176,228\nR 168,191,16384\ns_add_u32 m0, s50, 24576\ns_add_u32 s63, s53, 131072\n"
"buffer_load_dwordx4 v220, s[8:11], s63 offen lds\nds_read2st64_b32 v[36:37], v225 offset0:4 offset1:5\nM 148,180,244\nR 170,191,20480\nbuffer_load_dwordx4 v221, s[8:11], s63 offen offset:1024 lds\nds_read2st64_b32 v[38:39], v225 offset0:6 offset1:7\n"
"M 132,176,224\nR 172,191,24576\ns_add_u32 m0, s50, 32768\ns_add_u32 s63, s53, 196608\nbuffer_load_dwordx4 v220, s[8:11], s63 offen lds\nds_read2st64_b32 v[40:41], v225 offset0:8 offset1:9\n"
"M 132,180,240\nR 174,191,28672\nbuffer_load_dwordx4 v221, s[8:11], s63 offen offset:1024 lds\nds_read2st64_b32 v[42:43], v225 offset0:10 offset1:11\nG 9\nM 136,160,224\n"
"s_add_u32 m0, s50, 40960\ns_add_u32 s63, s53, 262144\nbuffer_load_dwordx4 v220, s[8:11], s63 offen lds\nM 136,164,240\nbuffer_load_dwordx4 v221, s[8:11], s63 offen offset:1024 lds\nM 152,160,228\n"
"s_add_u32 m0, s50, 49152\ns_add_u32 s63, s53, 327680\nbuffer_load_dwordx4 v220, s[8:11], s63 offen lds\nM 152,164,244\nds_read2st64_b32 v[44:45], v225 offset0:12 offset1:13\nds_read2st64_b32 v[46:47], v225 offset0:14 offset1:15\n"
"G 3\nM 156,168,228\nds_read2st64_b32 v[48:49], v225 offset0:16 offset1:17\nds_read2st64_b32 v[50:51], v225 offset0:18 offset1:19\nM 156,172,244\nds_read2st64_b32 v[52:53], v225 offset0:20 offset1:21\n"
"ds_read2st64_b32 v[54:55], v225 offset0:22 offset1:23\nM 140,168,224\nds_read2st64_b32 v[56:57], v225 offset0:24 offset1:25\nds_read2st64_b32 v[58:59], v225 offset0:26 offset1:27\nM 140,172,240\nds_read2st64_b32 v[60:61], v225 offset0:28 offset1:29\n"
"ds_read2st64_b32 v[62:63], v225 offset0:30 offset1:31\nG 0\ns_barrier\ns_waitcnt vmcnt(13)\nv_max3_u32 v248, v233, v65, v71\nv_cmp_eq_u32 vcc, -1, v248\n"
"s_nop 0\ns_cbranch_vccnz Lq196\nLq197:\ns_waitcnt vmcnt(10)\nv_max3_u32 v248, v237, v77, v83\nv_cmp_eq_u32 vcc, -1, v248\n"
"s_nop 0\ns_cbranch_vccnz Lq198\nLq199:\ns_lshl2_add_u32 s60, s45, s40\ns_waitcnt vmcnt(9)\nv_lshlrev_b32 v224, 3, v252\n"
"s_mul_i32 s60, s60, 7168\ns_bfm_b64 exec, 28, 28\nbuffer_load_dword v224, v224, s[16:19], s60 offen\ns_mov_b64 exec, -1\ns_lshl_b32 s84, s42, 11\ns_add_u32 s84, s84, s49\n"
"s_add_u32 s84, s84, 2097152\ns_add_u32 s85, s84, 32768\nv_mov_b32 v238, s46\nv_mov_b32 v239, s47\nv_pk_add_f32 v[144:145], v[144:145], v[48:49]\nv_pk_add_f32 v[146:147], v[146:147], v[50:51]\n"
"v_pk_add_f32 v[148:149], v[148:149], v[52:53]\nv_pk_add_f32 v[150:151], v[150:151], v[54:55]\nv_cvt_pk_bf16_f32 v144, v144, v145\nv_cvt_pk_bf16_f32 v145, v146, v147\nv_cvt_pk_bf16_f32 v146, v148, v149\nv_cvt_pk_bf16_f32 v147, v150, v151\n"
"s_nop 1\nv_mfma_f32_16x16x32_bf16 v[52:55], v[16:19], v[144:147], v[96:99]\nv_mfma_f32_16x16x32_bf16 v[48:51], v[0:3], v[144:147], v[100:103]\nv_pk_add_f32 v[152:153], v[152:153], v[56:57]\nv_pk_add_f32 v[154:155], v[154:155], v[58:59]\nv_pk_add_f32 v[156:157], v[156:157], v[60:61]\n"
"v_pk_add_f32 v[158:159], v[158:159], v[62:63]\nv_cvt_pk_bf16_f32 v152, v152, v153\nv_cvt_pk_bf16_f32 v153, v154, v155\nv_cvt_pk_bf16_f32 v154, v156, v157\nv_cvt_pk_bf16_f32 v155, v158, v159\ns_nop 1\n"
"v_mfma_f32_16x16x32_bf16 v[60:63], v[20:23], v[152:155], v[96:99]\nv_mfma_f32_16x16x32_bf16 v[56:59], v[4:7], v[152:155], v[100:103]\nv_pk_add_f32 v[128:129], v[128:129], v[32:33]\nv_pk_add_f32 v[130:131], v[130:131], v[34:35]\nv_pk_add_f32 v[132:133], v[132:133], v[36:37]\nv_pk_add_f32 v[134:135], v[134:135], v[38:39]\n"
"v_cvt_pk_bf16_f32 v128, v128, v129\nv_cvt_pk_bf16_f32 v129, v130, v131\nv_cvt_pk_bf16_f32 v130, v132, v133\nv_cvt_pk_bf16_f32 v131, v134, v135\ns_nop 1\nv_mfma_f32_16x16x32_bf16 v[36:39], v[0:3], v[128:131], v[96:99]\n"
"v_mfma_f32_16x16x32_bf16 v[32:35], v[230:233], v[128:131], v[104:107]\nv_pk_add_f32 v[136:137], v[136:137], v[40:41]\nv_pk_add_f32 v[138:139], v[138:139], v[42:43]\nv_pk_add_f32 v[140:141], v[140:141], v[44:45]\nv_pk_add_f32 v[142:143], v[142:143], v[46:47]\nv_cvt_pk_bf16_f32 v136, v136, v137\n"
"v_cvt_pk_bf16_f32 v137, v138, v139\nv_cvt_pk_bf16_f32 v138, v140, v141\nv_cvt_pk_bf16_f32 v139, v142, v143\ns_nop 1\nv_mfma_f32_16x16x32_bf16 v[44:47], v[4:7], v[136:139], v[96:99]\nv_mfma_f32_16x16x32_bf16 v[40:43], v[234:237], v[136:139], v[104:107]\n"
"v_max3_f32 v52, v52, v53, v48\nv_max3_f32 v60, v60, v61, v56\nv_max3_f32 v36, v36, v37, v32\ns_nop 4\nv_max3_f32 v44, v44, v45, v40\nv_max_f32 v52, v52, v49\n"
"v_max_f32 v60, v60, v57\nv_max_f32 v36, v36, v33\nv_max_f32 v44, v44, v41\nv_max3_f32 v54, v54, v55, v50\nv_max3_f32 v62, v62, v63, v58\nv_max3_f32 v38, v38, v39, v34\n"
"v_max3_f32 v46, v46, v47, v42\nv_max_f32 v54, v54, v51\nv_max_f32 v62, v62, v59\nv_max_f32 v38, v38, v35\nv_max_f32 v46, v46, v43\nv_max_f32 v118, v52, v54\n"
"v_max_f32 v119, v60, v62\nv_max_f32 v116, v36, v38\nv_max_f32 v117, v44, v46\nv_mov_b32 v120, v118\nv_mov_b32 v121, v119\nv_mov_b32 v122, v116\n"
"v_mov_b32 v123, v117\nv_permlane32_swap_b32 v120, v118\nv_permlane32_swap_b32 v121, v119\nv_permlane32_swap_b32 v122, v116\nv_permlane32_swap_b32 v123, v117\nv_max_f32 v118, v118, v120\n"
"v_max_f32 v119, v119, v121\nv_max_f32 v116, v116, v122\nv_max_f32 v117, v117, v123\nv_mov_b32 v120, v118\nv_mov_b32 v121, v119\nv_mov_b32 v122, v116\n"
"v_mov_b32 v123, v117\nv_permlane16_swap_b32 v120, v118\nv_permlane16_swap_b32 v121, v119\nv_permlane16_swap_b32 v122, v116\nv_permlane16_swap_b32 v123, v117\nv_max_f32 v118, v118, v120\n"
"v_max_f32 v119, v119, v121\nv_max_f32 v116, v116, v122\nv_max_f32 v117, v117, v123\nv_mul_f32 v118, v118, v218\nv_mul_f32 v119, v119, v218\nv_mul_f32 v116, v116, v215\n"
"v_mul_f32 v117, v117, v215\nv_fma_f32 v52, v52, v218, -v118\nv_fma_f32 v54, v54, v218, -v118\nv_fma_f32 v60, v60, v218, -v119\nv_fma_f32 v62, v62, v218, -v119\nv_fma_f32 v36, v36, v215, -v116\n"
"v_fma_f32 v38, v38, v215, -v116\nv_fma_f32 v44, v44, v215, -v117\nv_fma_f32 v46, v46, v215, -v117\nv_exp_f32 v52, v52\nv_exp_f32 v54, v54\nv_exp_f32 v60, v60\n"
"v_exp_f32 v62, v62\nv_exp_f32 v36, v36\nv_exp_f32 v38, v38\nv_exp_f32 v44, v44\nv_exp_f32 v46, v46\nv_cvt_pk_bf16_f32 v52, v52, v52\n"
"v_cvt_pk_bf16_f32 v54, v54, v54\nv_and_b32 v48, v52, v112\nv_and_b32 v49, v54, v113\nv_and_b32 v50, v52, v114\nv_and_b32 v51, v54, v115\ns_nop 1\n"
"v_mfma_f32_16x16x32_bf16 v[24:27], v[124:127], v[48:51], 0\nv_mfma_f32_16x16x32_bf16 v[144:147], v[66:69], v[48:51], 0\nv_mfma_f32_16x16x32_bf16 v[148:151], v[72:75], v[48:51], 0\nv_cvt_pk_bf16_f32 v60, v60, v60\nv_cvt_pk_bf16_f32 v62, v62, v62\nv_and_b32 v56, v60, v112\n"
"v_and_b32 v57, v62, v113\nv_and_b32 v58, v60, v114\nv_and_b32 v59, v62, v115\ns_nop 1\nv_mfma_f32_16x16x32_bf16 v[28:31], v[124:127], v[56:59], 0\nv_mfma_f32_16x16x32_bf16 v[152:155], v[78:81], v[56:59], 0\n"
"v_mfma_f32_16x16x32_bf16 v[156:159], v[84:87], v[56:59], 0\nv_cvt_pk_bf16_f32 v36, v36, v36\nv_cvt_pk_bf16_f32 v38, v38, v38\nv_and_b32 v32, v36, v112\nv_and_b32 v33, v38, v113\nv_and_b32 v34, v36, v114\n"
"v_and_b32 v35, v38, v115\ns_nop 1\nv_mfma_f32_16x16x32_bf16 v[8:11], v[124:127], v[32:35], 0\nv_mfma_f32_16x16x32_bf16 v[128:131], v[64:67], v[32:35], 0\nv_mfma_f32_16x16x32_bf16 v[132:135], v[70:73], v[32:35], 0\nv_cvt_pk_bf16_f32 v44, v44, v44\n"
"v_cvt_pk_bf16_f32 v46, v46, v46\nv_and_b32 v40, v44, v112\nv_and_b32 v41, v46, v113\nv_and_b32 v42, v44, v114\nv_and_b32 v43, v46, v115\ns_nop 1\n"
"v_mfma_f32_16x16x32_bf16 v[12:15], v[124:127], v[40:43], 0\nv_mfma_f32_16x16x32_bf16 v[136:139], v[76:79], v[40:43], 0\nv_mfma_f32_16x16x32_bf16 v[140:143], v[82:85], v[40:43], 0\nv_rcp_f32 v24, v24\nv_rcp_f32 v28, v28\nv_rcp_f32 v8, v8\n"
"s_nop 2\nv_rcp_f32 v12, v12\nv_pk_mul_f32 v[144:145], v[144:145], v[24:25] op_sel_hi:[1,0]\nv_pk_mul_f32 v[146:147], v[146:147], v[24:25] op_sel_hi:[1,0]\nv_pk_mul_f32 v[148:149], v[148:149], v[24:25] op_sel_hi:[1,0]\nv_pk_mul_f32 v[150:151], v[150:151], v[24:25] op_sel_hi:[1,0]\n"
"v_cvt_pk_bf16_f32 v144, v144, v145\nv_cvt_pk_bf16_f32 v145, v146, v147\nv_cvt_pk_bf16_f32 v146, v148, v149\nv_cvt_pk_bf16_f32 v147, v150, v151\nbuffer_store_dwordx4 v[144:147], v252, s[20:23], s85 offen offset:0 sc1\nv_pk_mul_f32 v[152:153], v[152:153], v[28:29] op_sel_hi:[1,0]\n"
"v_pk_mul_f32 v[154:155], v[154:155], v[28:29] op_sel_hi:[1,0]\nv_pk_mul_f32 v[156:157], v[156:157], v[28:29] op_sel_hi:[1,0]\nv_pk_mul_f32 v[158:159], v[158:159], v[28:29] op_sel_hi:[1,0]\nv_cvt_pk_bf16_f32 v152, v152, v153\nv_cvt_pk_bf16_f32 v153, v154, v155\nv_cvt_pk_bf16_f32 v154, v156, v157\n"
"v_cvt_pk_bf16_f32 v155, v158, v159\nbuffer_store_dwordx4 v[152:155], v252, s[20:23], s85 offen offset:1024 sc1\nv_pk_mul_f32 v[128:129], v[128:129], v[8:9] op_sel_hi:[1,0]\nv_pk_mul_f32 v[130:131], v[130:131], v[8:9] op_sel_hi:[1,0]\nv_pk_mul_f32 v[132:133], v[132:133], v[8:9] op_sel_hi:[1,0]\nv_pk_mul_f32 v[134:135], v[134:135], v[8:9] op_sel_hi:[1,0]\n"
"v_cvt_pk_bf16_f32 v128, v128, v129\nv_cvt_pk_bf16_f32 v129, v130, v131\nv_cvt_pk_bf16_f32 v130, v132, v133\nv_cvt_pk_bf16_f32 v131, v134, v135\nbuffer_store_dwordx4 v[128:131], v252, s[20:23], s84 offen offset:0 sc1\nv_pk_mul_f32 v[136:137], v[136:137], v[12:13] op_sel_hi:[1,0]\n"
"v_pk_mul_f32 v[138:139], v[138:139], v[12:13] op_sel_hi:[1,0]\nv_pk_mul_f32 v[140:141], v[140:141], v[12:13] op_sel_hi:[1,0]\nv_pk_mul_f32 v[142:143], v[142:143], v[12:13] op_sel_hi:[1,0]\nv_cvt_pk_bf16_f32 v136, v136, v137\nv_cvt_pk_bf16_f32 v137, v138, v139\nv_cvt_pk_bf16_f32 v138, v140, v141\n"
"v_cvt_pk_bf16_f32 v139, v142, v143\nbuffer_store_dwordx4 v[136:139], v252, s[20:23], s84 offen offset:1024 sc1\ns_add_u32 s61, s79, 88080384\ns_lshl_b32 s62, s42, 3\ns_add_u32 s61, s61, s62\ns_waitcnt vmcnt(0)\n"
"s_mov_b64 exec, 1\nbuffer_atomic_swap_x2 v[238:239], off, s[20:23], s61 sc1\ns_mov_b64 exec, -1\nv_bfe_u32 v250, v252, 7, 1\nv_mul_u32_u24 v250, 2176, v250\nv_bfe_u32 v251, v252, 4, 3\n"
"v_lshl_add_u32 v250, v251, 3, v250\ns_lshl_b32 s60, s90, 6\nv_add_u32 v250, s60, v250\ns_sleep 5\nv_mov_b32 v248, 0\nv_mov_b32 v249, 0\n"
"s_add_u32 s61, s80, 88080384\ns_mov_b64 exec, 0xffff\nbuffer_atomic_or_x2 v[248:249], v250, s[20:23], s61 offen sc0 sc1\ns_mov_b64 exec, -1\nbuffer_load_dwordx4 v221, s[8:11], s63 offen offset:1024 lds\ns_add_u32 m0, s50, 57344\n"
"s_add_u32 s63, s53, 393216\nbuffer_load_dwordx4 v220, s[8:11], s63 offen lds\nbuffer_load_dwordx4 v221, s[8:11], s63 offen offset:1024 lds\ns_add_u32 m0, s50, 65536\ns_add_u32 s63, s53, 458752\nbuffer_load_dwordx4 v220, s[8:11], s63 offen lds\n"
"buffer_load_dwordx4 v221, s[8:11], s63 offen offset:1024 lds\ns_add_u32 m0, s50, 73728\ns_add_u32 s63, s53, 524288\nbuffer_load_dwordx4 v220, s[8:11], s63 offen lds\nbuffer_load_dwordx4 v221, s[8:11], s63 offen offset:1024 lds\ns_add_u32 m0, s50, 81920\n"
"s_add_u32 s63, s53, 589824\nbuffer_load_dwordx4 v220, s[8:11], s63 offen lds\nbuffer_load_dwordx4 v221, s[8:11], s63 offen offset:1024 lds\ns_add_u32 m0, s50, 90112\ns_add_u32 s63, s53, 655360\nbuffer_load_dwordx4 v220, s[8:11], s63 offen lds\n"
"buffer_load_dwordx4 v221, s[8:11], s63 offen offset:1024 lds\nv_bfe_u32 v234, v252, 4, 4\nv_lshrrev_b32 v235, 8, v252\nv_lshlrev_b32 v235, 2, v235\nv_sub_u32 v234, v234, v235\nv_lshlrev_b32 v235, 4, v234\n"
"s_mov_b64 s[60:61], 0x3f80\nv_lshlrev_b64 v[208:209], v235, s[60:61]\nv_cmp_gt_u32 s[60:61], 4, v234\ns_nop 1\nv_cndmask_b32 v212, 0, v208, s[60:61]\nv_cndmask_b32 v208, 0, v208, s[60:61]\n"
"v_cndmask_b32 v213, 0, v209, s[60:61]\nv_cndmask_b32 v209, 0, v209, s[60:61]\ns_nop 1\nv_mfma_f32_16x16x32_bf16 v[0:3], v[208:211], v[192:195], 0\nv_mfma_f32_16x16x32_bf16 v[4:7], v[210:213], v[192:195], 0\nv_mfma_f32_16x16x32_bf16 v[8:11], v[208:211], v[196:199], 0\n"
"v_mfma_f32_16x16x32_bf16 v[12:15], v[210:213], v[196:199], 0\nv_mfma_f32_16x16x32_bf16 v[16:19], v[208:211], v[200:203], 0\nv_mfma_f32_16x16x32_bf16 v[20:23], v[210:213], v[200:203], 0\nv_mfma_f32_16x16x32_bf16 v[24:27], v[208:211], v[204:207], 0\nv_mfma_f32_16x16x32_bf16 v[28:31], v[210:213], v[204:207], 0\ns_waitcnt vmcnt(11)\n"
"v_cmp_eq_u64 s[86:87], s[46:47], v[248:249]\ns_nop 0\ns_and_b32 s62, s86, 0xffff\ns_cmp_eq_u32 s62, 0xffff\ns_cbranch_scc0 Lq201\nLq202:\n"
"s_add_u32 s64, s70, 2097152\nbuffer_load_dwordx4 a[0:3], v252, s[20:23], s64 offen offset:0 sc1\ns_add_u32 s65, s70, 2129920\nbuffer_load_dwordx4 a[4:7], v252, s[20:23], s65 offen offset:0 sc1\ns_add_u32 s66, s71, 2097152\nbuffer_load_dwordx4 a[8:11], v252, s[20:23], s66 offen offset:0 sc1\n"
"s_add_u32 s67, s71, 2129920\nbuffer_load_dwordx4 a[12:15], v252, s[20:23], s67 offen offset:0 sc1\nbuffer_load_dwordx4 a[16:19], v252, s[20:23], s64 offen offset:1024 sc1\nbuffer_load_dwordx4 a[20:23], v252, s[20:23], s65 offen offset:1024 sc1\nbuffer_load_dwordx4 a[24:27], v252, s[20:23], s66 offen offset:1024 sc1\nbuffer_load_dwordx4 a[28:31], v252, s[20:23], s67 offen offset:1024 sc1\n"
"s_barrier\nR 160,184,8192\nR 162,184,12288\nR 164,184,16384\nR 166,184,20480\nR 168,185,8192\n"
"R 170,185,12288\nR 172,185,16384\nR 174,185,20480\nR 176,186,8192\nR 178,186,12288\nR 180,186,16384\n"
"R 182,186,20480\nbuffer_load_dwordx4 a[32:35], v252, s[20:23], s64 offen offset:2048 sc1\nbuffer_load_dwordx4 a[36:39], v252, s[20:23], s65 offen offset:2048 sc1\nbuffer_load_dwordx4 a[40:43], v252, s[20:23], s66 offen offset:2048 sc1\nbuffer_load_dwordx4 a[44:47], v252, s[20:23], s67 offen offset:2048 sc1\nbuffer_load_dwordx4 a[48:51], v252, s[20:23], s64 offen offset:3072 sc1\n"
"buffer_load_dwordx4 a[52:55], v252, s[20:23], s65 offen offset:3072 sc1\nbuffer_load_dwordx4 a[56:59], v252, s[20:23], s66 offen offset:3072 sc1\nbuffer_load_dwordx4 a[60:63], v252, s[20:23], s67 offen offset:3072 sc1\ns_add_u32 s64, s70, 2101248\nbuffer_load_dwordx4 a[64:67], v252, s[20:23], s64 offen offset:0 sc1\ns_add_u32 s65, s70, 2134016\n"
"buffer_load_dwordx4 a[68:71], v252, s[20:23], s65 offen offset:0 sc1\ns_add_u32 s66, s71, 2101248\nbuffer_load_dwordx4 a[72:75], v252, s[20:23], s66 offen offset:0 sc1\ns_add_u32 s67, s71, 2134016\nbuffer_load_dwordx4 a[76:79], v252, s[20:23], s67 offen offset:0 sc1\ns_waitcnt vmcnt(19) lgkmcnt(10)\n"
"M 0,160,0\nbuffer_load_dwordx4 a[80:83], v252, s[20:23], s64 offen offset:1024 sc1\ns_waitcnt vmcnt(16) lgkmcnt(8)\nM 0,164,16\nM 16,160,4\ns_and_b32 s60, s40, 2\n"
"s_lshl_b32 s60, s60, 5\nv_xor_b32 v238, s60, v188\ns_waitcnt vmcnt(15)\nM 16,164,20\nv_xor_b32 v239, 32, v238\nv_mov_b32 v242, s76\n"
"v_mfma_f32_16x16x32_bf16 v[32:35], v[160:163], a[8:11], 0\nbuffer_load_dwordx4 a[84:87], v252, s[20:23], s65 offen offset:1024 sc1\ns_waitcnt vmcnt(15)\nM 32,164,24\nv_mov_b32 v243, s76\nv_mov_b32 v244, s76\n"
"v_mfma_f32_16x16x32_bf16 v[48:51], v[160:163], a[12:15], 0\nv_mov_b32 v245, s76\ns_waitcnt vmcnt(14)\nM 48,164,28\nG 4\nv_mfma_f32_16x16x32_bf16 v[52:55], v[168:171], a[12:15], 0\n"
"R 160,187,8192\nM 52,172,28\nbuffer_load_dwordx4 a[88:91], v252, s[20:23], s66 offen offset:1024 sc1\nv_mfma_f32_16x16x32_bf16 v[36:39], v[168:171], a[8:11], 0\nR 162,187,12288\nM 36,172,24\n"
"M 20,168,4\nR 164,187,16384\nR 64,238,32768\nR 66,238,36864\nM 20,172,20\nR 68,239,32768\n"
"R 70,239,36864\nM 4,168,0\nR 166,187,20480\nM 4,172,16\nbuffer_load_dwordx4 a[92:95], v252, s[20:23], s67 offen offset:1024 sc1\nG 8\n"
"M 8,176,0\nR 168,184,24576\nM 8,180,16\nbuffer_load_dwordx4 a[96:99], v252, s[20:23], s64 offen offset:2048 sc1\nM 24,176,4\nR 170,184,28672\n"
"G 5\nv_mfma_f32_16x16x32_bf16 v[72:75], v[242:245], v[64:67], 0\nM 24,180,20\nv_mfma_f32_16x16x32_bf16 v[40:43], v[176:179], a[8:11], 0\nR 172,184,32768\nG 4\n"
"v_mfma_f32_16x16x32_bf16 v[76:79], v[242:245], v[68:71], 0\nM 40,180,24\nbuffer_load_dwordx4 a[100:103], v252, s[20:23], s65 offen offset:2048 sc1\nv_mfma_f32_16x16x32_bf16 v[56:59], v[176:179], a[12:15], 0\nR 174,184,36864\nM 56,180,28\n"
"G 4\nv_mfma_f32_16x16x32_bf16 v[60:63], v[160:163], a[12:15], 0\nR 176,185,24576\nv_lshrrev_b32 v64, 1, v252\nv_mov_b32 v67, s46\nM 60,164,28\n"
"buffer_load_dwordx4 a[104:107], v252, s[20:23], s66 offen offset:2048 sc1\ns_mov_b64 s[60:61], 0xffff\nv_cndmask_b32 v66, v76, v72, s[60:61]\nv_mfma_f32_16x16x32_bf16 v[44:47], v[160:163], a[8:11], 0\nR 178,185,28672\nM 44,164,24\n"
"s_lshr_b32 s61, s40, 1\ns_lshl_b32 s61, s61, 8\ns_add_u32 s61, s61, s91\ns_add_u32 s61, s61, 786432\ns_bfm_b64 exec, 32, 0\nbuffer_store_dwordx2 v[66:67], v64, s[20:23], s61 offen sc1\n"
"s_mov_b64 exec, -1\nM 28,160,4\nR 180,185,32768\nM 28,164,20\nbuffer_load_dwordx4 a[108:111], v252, s[20:23], s67 offen offset:2048 sc1\nM 12,160,0\n"
"R 182,185,36864\nM 12,164,16\nG 4\ns_waitcnt vmcnt(20)\nM 0,168,32\nR 160,186,24576\n"
"s_waitcnt vmcnt(16)\nM 0,172,48\nbuffer_load_dwordx4 a[112:115], v252, s[20:23], s64 offen offset:3072 sc1\nM 16,168,36\nR 162,186,28672\ns_waitcnt vmcnt(16)\n"
"M 16,172,52\ns_and_b32 s60, s40, 2\ns_lshl_b32 s60, s60, 5\nv_xor_b32 v238, s60, v188\nM 32,168,40\nR 164,186,32768\n"
"v_xor_b32 v239, 32, v238\ns_waitcnt vmcnt(15)\nM 32,172,56\nbuffer_load_dwordx4 a[116:119], v252, s[20:23], s65 offen offset:3072 sc1\ns_barrier\nM 48,168,44\n"
"R 166,186,36864\nR 64,238,40960\nR 66,238,45056\ns_waitcnt vmcnt(15)\nM 48,172,60\nR 68,239,40960\n"
"R 70,239,45056\nG 8\nM 52,176,44\nR 168,187,24576\nM 52,180,60\nbuffer_load_dwordx4 a[120:123], v252, s[20:23], s66 offen offset:3072 sc1\n"
"M 36,176,40\nR 170,187,28672\nM 36,180,56\nM 20,176,36\nR 172,187,32768\nG 5\n"
"v_mfma_f32_16x16x32_bf16 v[72:75], v[242:245], v[64:67], 0\nM 20,180,52\nbuffer_load_dwordx4 a[124:127], v252, s[20:23], s67 offen offset:3072 sc1\nM 4,176,32\nR 174,187,36864\nG 4\n"
"v_mfma_f32_16x16x32_bf16 v[76:79], v[242:245], v[68:71], 0\nM 4,180,48\nM 8,160,32\nR 176,184,40960\nM 8,164,48\ns_add_u32 s64, s70, 2105344\n"
"buffer_load_dwordx4 a[128:131], v252, s[20:23], s64 offen offset:0 sc1\nM 24,160,36\nR 178,184,45056\nv_lshrrev_b32 v64, 1, v252\nv_mov_b32 v67, s46\nM 24,164,52\n"
"s_mov_b64 s[60:61], 0xffff\nv_cndmask_b32 v66, v76, v72, s[60:61]\nM 40,160,40\nR 180,184,49152\nM 40,164,56\ns_add_u32 s65, s70, 2138112\n"
"buffer_load_dwordx4 a[132:135], v252, s[20:23], s65 offen offset:0 sc1\ns_lshr_b32 s61, s40, 1\ns_lshl_b32 s61, s61, 8\ns_add_u32 s61, s61, s91\ns_add_u32 s61, s61, 1048576\ns_bfm_b64 exec, 32, 0\n"
"buffer_store_dwordx2 v[66:67], v64, s[20:23], s61 offen sc1\ns_mov_b64 exec, -1\nM 56,160,44\nR 182,184,53248\nM 56,164,60\nG 4\n"
"M 60,168,44\nR 160,185,40960\nM 60,172,60\ns_add_u32 s66, s71, 2105344\nbuffer_load_dwordx4 a[136:139], v252, s[20:23], s66 offen offset:0 sc1\nM 44,168,40\n"
"R 162,185,45056\nM 44,172,56\ns_and_b32 s60, s40, 2\ns_lshl_b32 s60, s60, 5\nv_xor_b32 v238, s60, v188\nM 28,168,36\n"
"R 164,185,49152\nv_xor_b32 v239, 32, v238\nM 28,172,52\ns_add_u32 s67, s71, 2138112\nbuffer_load_dwordx4 a[140:143], v252, s[20:23], s67 offen offset:0 sc1\nM 12,168,32\n"
"R 166,185,53248\nM 12,172,48\ns_barrier\nG 4\ns_waitcnt vmcnt(21)\nM 0,176,64\n"
"R 168,186,40960\nR 64,238,49152\nR 66,238,53248\ns_waitcnt vmcnt(17)\nM 0,180,80\nR 68,239,49152\n"
"R 70,239,53248\nM 16,176,68\nR 170,186,45056\ns_waitcnt vmcnt(16)\nM 16,180,84\nbuffer_load_dwordx4 a[144:147], v252, s[20:23], s64 offen offset:1024 sc1\n"
"M 32,176,72\nR 172,186,49152\ns_waitcnt vmcnt(16)\nM 32,180,88\nbuffer_load_dwordx4 a[148:151], v252, s[20:23], s65 offen offset:1024 sc1\nM 48,176,76\n"
"R 174,186,53248\nG 5\nv_mfma_f32_16x16x32_bf16 v[72:75], v[242:245], v[64:67], 0\ns_add_u32 m0, s50, 98304\ns_waitcnt vmcnt(16)\nM 48,180,92\n"
"M 52,160,76\nR 176,187,40960\nG 4\nv_mfma_f32_16x16x32_bf16 v[76:79], v[242:245], v[68:71], 0\ns_add_u32 s63, s53, 720896\nM 52,164,92\n"
"buffer_load_dwordx4 a[152:155], v252, s[20:23], s66 offen offset:1024 sc1\nM 36,160,72\nR 178,187,45056\nM 36,164,88\nbuffer_load_dwordx4 v220, s[8:11], s63 offen lds\nM 20,160,68\n"
"R 180,187,49152\nv_lshrrev_b32 v64, 1, v252\nv_mov_b32 v67, s46\nM 20,164,84\nbuffer_load_dwordx4 a[156:159], v252, s[20:23], s67 offen offset:1024 sc1\ns_mov_b64 s[60:61], 0xffff\n"
"v_cndmask_b32 v66, v76, v72, s[60:61]\nM 4,160,64\nR 182,187,53248\nM 4,164,80\ns_lshr_b32 s61, s40, 1\ns_lshl_b32 s61, s61, 8\n"
"s_add_u32 s61, s61, s91\ns_add_u32 s61, s61, 1310720\ns_bfm_b64 exec, 32, 0\nbuffer_store_dwordx2 v[66:67], v64, s[20:23], s61 offen sc1\ns_mov_b64 exec, -1\nG 4\n"
"M 8,168,64\nR 160,184,57344\nM 8,172,80\nbuffer_load_dwordx4 a[160:163], v252, s[20:23], s64 offen offset:2048 sc1\nbuffer_load_dwordx4 v221, s[8:11], s63 offen offset:1024 lds\nM 24,168,68\n"
"R 162,184,61440\nM 24,172,84\nM 40,168,72\nR 164,188,0\nM 40,172,88\nbuffer_load_dwordx4 a[164:167], v252, s[20:23], s65 offen offset:2048 sc1\n"
"M 56,168,76\nR 166,188,4096\nM 56,172,92\ns_and_b32 s60, s41, 1\ns_lshl_b32 s60, s60, 1\ns_lshr_b32 s61, s40, 1\n"
"s_add_u32 s60, s60, s61\ns_lshl_b32 s60, s60, 5\nv_xor_b32 v238, s60, v184\nG 4\nM 60,176,76\nR 168,185,57344\n"
"M 60,180,92\nbuffer_load_dwordx4 a[168:171], v252, s[20:23], s66 offen offset:2048 sc1\nM 44,176,72\nR 170,185,61440\nR 64,238,0\nR 66,238,4096\n"
"M 44,180,88\ns_add_u32 m0, s50, 106496\nM 28,176,68\nR 172,189,0\ns_add_u32 s63, s53, 786432\nM 28,180,84\n"
"buffer_load_dwordx4 a[172:175], v252, s[20:23], s67 offen offset:2048 sc1\nM 12,176,64\nR 174,189,4096\nM 12,180,80\ns_barrier\nbuffer_load_dwordx4 v220, s[8:11], s63 offen lds\n"
"G 6\ns_waitcnt vmcnt(25)\nM 0,160,96\nR 176,186,57344\nG 3\nv_mfma_f32_16x16x32_bf16 v[72:75], v[242:245], v[64:67], 0\n"
"s_waitcnt vmcnt(20)\nM 0,164,112\nbuffer_load_dwordx4 a[176:179], v252, s[20:23], s64 offen offset:3072 sc1\nM 16,160,100\nR 178,186,61440\ns_waitcnt vmcnt(20)\n"
"M 16,164,116\nbuffer_load_dwordx4 v221, s[8:11], s63 offen offset:1024 lds\nM 32,160,104\nR 180,190,0\ns_add_u32 m0, s50, 114688\ns_waitcnt vmcnt(20)\n"
"M 32,164,120\nbuffer_load_dwordx4 a[180:183], v252, s[20:23], s65 offen offset:3072 sc1\nM 48,160,108\nR 182,190,4096\ns_add_u32 s63, s53, 851968\nv_lshrrev_b32 v64, 1, v252\n"
"s_waitcnt vmcnt(20)\nM 48,164,124\nbuffer_load_dwordx4 v220, s[8:11], s63 offen lds\nG 4\nv_mov_b32 v67, s46\nM 52,168,108\n"
"R 160,187,57344\nv_mov_b32 v66, v72\nM 52,172,124\nbuffer_load_dwordx4 a[184:187], v252, s[20:23], s66 offen offset:3072 sc1\ns_lshr_b32 s61, s40, 1\ns_lshl_b32 s61, s61, 7\n"
"s_add_u32 s61, s61, s91\ns_add_u32 s61, s61, 1572864\ns_bfm_b64 exec, 16, 0\nbuffer_store_dwordx2 v[66:67], v64, s[20:23], s61 offen sc1\ns_mov_b64 exec, -1\nM 36,168,104\n"
"R 162,187,61440\nM 36,172,120\nbuffer_load_dwordx4 v221, s[8:11], s63 offen offset:1024 lds\nM 20,168,100\nR 164,191,0\nM 20,172,116\n"
"buffer_load_dwordx4 a[188:191], v252, s[20:23], s67 offen offset:3072 sc1\nM 4,168,96\nR 166,191,4096\nM 4,172,112\nG 4\nM 8,176,96\n"
"R 168,188,8192\nM 8,180,112\ns_add_u32 s64, s70, 2109440\nbuffer_load_dwordx4 a[192:195], v252, s[20:23], s64 offen offset:0 sc1\nM 24,176,100\nR 170,188,12288\n"
"M 24,180,116\nM 40,176,104\nR 172,188,16384\nM 40,180,120\ns_add_u32 s65, s70, 2142208\nbuffer_load_dwordx4 a[196:199], v252, s[20:23], s65 offen offset:0 sc1\n"
"M 56,176,108\nR 174,188,20480\nM 56,180,124\nG 4\nM 60,160,108\nR 176,189,8192\n"
"M 60,164,124\ns_add_u32 s66, s71, 2109440\nbuffer_load_dwordx4 a[200:203], v252, s[20:23], s66 offen offset:0 sc1\nM 44,160,104\nR 178,189,12288\ns_add_u32 s63, s53, 917504\n"
"M 44,164,120\ns_add_u32 m0, s50, 0\nM 28,160,100\nR 180,189,16384\nM 28,164,116\ns_add_u32 s67, s71, 2142208\n"
"buffer_load_dwordx4 a[204:207], v252, s[20:23], s67 offen offset:0 sc1\nM 12,160,96\nR 182,189,20480\nM 12,164,112\ns_waitcnt vmcnt(16)\ns_barrier\n"
"buffer_load_dwordx4 v220, s[8:11], s63 offen lds\nG 4\nM 0,168,128\nR 160,190,8192\nbuffer_load_dwordx4 v221, s[8:11], s63 offen offset:1024 lds\nM 0,172,144\n"
"buffer_load_dwordx4 a[208:211], v252, s[20:23], s64 offen offset:1024 sc1\ns_add_u32 m0, s50, 8192\ns_add_u32 s63, s53, 983040\nbuffer_load_dwordx4 v220, s[8:11], s63 offen lds\nM 16,168,132\nR 162,190,12288\n"
"buffer_load_dwordx4 v221, s[8:11], s63 offen offset:1024 lds\nM 16,172,148\nM 32,168,136\nR 164,190,16384\nM 32,172,152\nbuffer_load_dwordx4 a[212:215], v252, s[20:23], s65 offen offset:1024 sc1\n"
"M 48,168,140\nR 166,190,20480\nM 48,172,156\nG 4\nM 52,176,140\nR 168,191,8192\n"
"M 52,180,156\nbuffer_load_dwordx4 a[216:219], v252, s[20:23], s66 offen offset:1024 sc1\nM 36,176,136\nR 170,191,12288\nM 36,180,152\nM 20,176,132\n"
"R 172,191,16384\nM 20,180,148\nbuffer_load_dwordx4 a[220:223], v252, s[20:23], s67 offen offset:1024 sc1\nM 4,176,128\nR 174,191,20480\nM 4,180,144\n"
"G 4\nM 8,160,128\nR 176,188,24576\nM 8,164,144\nbuffer_load_dwordx4 a[224:227], v252, s[20:23], s64 offen offset:2048 sc1\nM 24,160,132\n"
"R 178,188,28672\nM 24,164,148\nM 40,160,136\nR 180,188,32768\nM 40,164,152\nbuffer_load_dwordx4 a[228:231], v252, s[20:23], s65 offen offset:2048 sc1\n"
"M 56,160,140\nR 182,188,36864\nM 56,164,156\nG 4\nM 60,168,140\nR 160,189,24576\n"
"M 60,172,156\nbuffer_load_dwordx4 a[232:235], v252, s[20:23], s66 offen offset:2048 sc1\nM 44,168,136\nR 162,189,28672\nM 44,172,152\nM 28,168,132\n"
"R 164,189,32768\nM 28,172,148\nbuffer_load_dwordx4 a[236:239], v252, s[20:23], s67 offen offset:2048 sc1\nM 12,168,128\nR 166,189,36864\nM 12,172,144\n"
"G 4\nM 0,176,160\nR 168,190,24576\ns_waitcnt vmcnt(23)\nM 0,180,176\nbuffer_load_dwordx4 a[240:243], v252, s[20:23], s64 offen offset:3072 sc1\n"
"M 16,176,164\nR 170,190,28672\ns_waitcnt vmcnt(22)\nM 16,180,180\nM 32,176,168\nR 172,190,32768\n"
"s_waitcnt vmcnt(20)\nM 32,180,184\nbuffer_load_dwordx4 a[244:247], v252, s[20:23], s65 offen offset:3072 sc1\nM 48,176,172\nR 174,190,36864\ns_waitcnt vmcnt(18)\n"
"M 48,180,188\nG 4\nM 52,160,172\nR 176,191,24576\nM 52,164,188\nbuffer_load_dwordx4 a[248:251], v252, s[20:23], s66 offen offset:3072 sc1\n"
"M 36,160,168\nR 178,191,28672\ns_waitcnt vmcnt(10)\ns_barrier\nM 36,164,184\nM 20,160,164\n"
"R 180,191,32768\nM 20,164,180\nbuffer_load_dwordx4 a[252:255], v252, s[20:23], s67 offen offset:3072 sc1\nM 4,160,160\nR 182,191,36864\nM 4,164,176\n"
"G 4\nM 8,168,160\nR 160,188,40960\nM 8,172,176\ns_movk_i32 s63, 7168\nv_mad_u32_u24 v220, v228, s63, v229\n"
"v_add_u32 v221, 3668992, v220\ns_add_u32 m0, s50, 16384\ns_add_u32 s63, s54, 0\nbuffer_load_dwordx4 v220, s[12:15], s63 offen lds\nM 24,168,164\nR 162,188,45056\n"
"M 24,172,180\nM 40,168,168\nR 164,188,49152\nM 40,172,184\nbuffer_load_dwordx4 v221, s[12:15], s63 offen offset:1024 lds\nM 56,168,172\n"
"R 166,188,53248\ns_add_u32 m0, s50, 24576\nM 56,172,188\nG 4\nM 60,176,172\nR 168,189,40960\n"
"s_add_u32 s63, s54, 229376\nM 60,180,188\nbuffer_load_dwordx4 v220, s[12:15], s63 offen lds\nM 44,176,168\nR 170,189,45056\nM 44,180,184\n"
"M 28,176,164\nR 172,189,49152\nM 28,180,180\nbuffer_load_dwordx4 v221, s[12:15], s63 offen offset:1024 lds\nM 12,176,160\nR 174,189,53248\n"
"s_add_u32 m0, s50, 32768\nM 12,180,176\nG 4\nM 32,160,200\nR 176,190,40960\ns_add_u32 s63, s54, 128\n"
"s_waitcnt vmcnt(13)\nM 32,164,216\nR 178,190,45056\nM 48,160,204\nR 180,190,49152\ns_waitcnt vmcnt(12)\n"
"M 48,164,220\nR 182,190,53248\nG 4\nM 52,168,204\nR 160,191,40960\nbuffer_load_dwordx4 v220, s[12:15], s63 offen lds\n"
"M 52,172,220\nR 162,191,45056\nM 36,168,200\nR 164,191,49152\nM 36,172,216\nR 166,191,53248\n"
"G 4\nM 40,176,200\nR 168,184,0\nbuffer_load_dwordx4 v221, s[12:15], s63 offen offset:1024 lds\nM 40,180,216\nR 170,184,4096\n"
"s_add_u32 m0, s50, 40960\nM 56,176,204\nR 172,184,8192\ns_add_u32 s63, s54, 229504\nM 56,180,220\nR 174,184,12288\n"
"G 4\nM 60,160,204\nR 176,185,0\nbuffer_load_dwordx4 v220, s[12:15], s63 offen lds\nM 60,164,220\nR 178,185,4096\n"
"M 44,160,200\nR 180,185,8192\nM 44,164,216\nR 182,185,12288\nG 4\ns_waitcnt vmcnt(12)\n"
"M 32,168,232\nR 160,186,0\nbuffer_load_dwordx4 v221, s[12:15], s63 offen offset:1024 lds\ns_waitcnt vmcnt(9)\nM 32,172,248\nR 162,186,4096\n"
"M 48,168,236\nR 164,186,8192\ns_waitcnt vmcnt(8)\nM 48,172,252\nR 166,186,12288\nG 4\n"
"M 52,176,236\nR 168,187,0\nM 52,180,252\nR 170,187,4096\nM 36,176,232\nR 172,187,8192\n"
"M 36,180,248\nR 174,187,12288\nG 4\nM 40,160,232\nR 176,188,40960\nM 40,164,248\n"
"R 178,188,45056\nM 56,160,236\nR 180,188,49152\ns_add_u32 m0, s50, 49152\nM 56,164,252\nR 182,188,53248\n"
"G 4\nM 60,168,236\nR 160,189,40960\ns_add_u32 s63, s54, 458752\nM 60,172,252\nR 162,189,45056\n"
"M 44,168,232\nR 164,189,49152\nM 44,172,248\nR 166,189,53248\nG 4\nM 0,176,192\n"
"R 168,190,40960\nbuffer_load_dwordx4 v220, s[12:15], s63 offen lds\nM 0,180,208\nR 170,190,45056\ns_mov_b32 m0, s51\nM 16,176,196\n"
"R 172,190,49152\nds_write_addtid_b32 v32 offset:0\nM 16,180,212\nR 174,190,53248\nds_write_addtid_b32 v33 offset:256\nG 6\n"
"M 20,160,196\nR 176,191,40960\nds_write_addtid_b32 v34 offset:512\nM 20,164,212\nR 178,191,45056\nds_write_addtid_b32 v35 offset:768\n"
"M 4,160,192\nR 180,191,49152\nds_write_addtid_b32 v36 offset:1024\nM 4,164,208\nR 182,191,53248\nds_write_addtid_b32 v37 offset:1280\n"
"G 9\nM 8,168,192\nR 160,184,0\nds_write_addtid_b32 v38 offset:1536\nM 8,172,208\nR 162,184,4096\n"
"ds_write_addtid_b32 v39 offset:1792\nM 24,168,196\nR 164,184,8192\nds_write_addtid_b32 v40 offset:2048\nM 24,172,212\nR 166,184,12288\n"
"ds_write_addtid_b32 v41 offset:2304\nG 9\nM 28,176,196\nR 168,185,0\nds_write_addtid_b32 v42 offset:2560\nM 28,180,212\n"
"R 170,185,4096\nds_write_addtid_b32 v43 offset:2816\nM 12,176,192\nR 172,185,8192\nds_write_addtid_b32 v44 offset:3072\nds_write_addtid_b32 v45 offset:3328\n"
"ds_write_addtid_b32 v46 offset:3584\nds_write_addtid_b32 v47 offset:3840\nds_write_addtid_b32 v48 offset:4096\nds_write_addtid_b32 v49 offset:4352\nds_write_addtid_b32 v50 offset:4608\nds_write_addtid_b32 v51 offset:4864\n"
"ds_write_addtid_b32 v52 offset:5120\nds_write_addtid_b32 v53 offset:5376\nds_write_addtid_b32 v54 offset:5632\nds_write_addtid_b32 v55 offset:5888\nds_write_addtid_b32 v56 offset:6144\nds_write_addtid_b32 v57 offset:6400\n"
"ds_write_addtid_b32 v58 offset:6656\nds_write_addtid_b32 v59 offset:6912\nds_write_addtid_b32 v60 offset:7168\nds_write_addtid_b32 v61 offset:7424\nds_write_addtid_b32 v62 offset:7680\nds_write_addtid_b32 v63 offset:7936\n"
"M 12,180,208\nR 174,185,12288\ns_add_u32 m0, s50, 49152\nG 15\nM 0,160,224\nR 176,186,0\n"
"buffer_load_dwordx4 v221, s[12:15], s63 offen offset:1024 lds\nM 0,164,240\nR 178,186,4096\nG 3\ns_barrier\ns_add_u32 s63, s54, 688128\n"
"M 16,160,228\nR 180,186,8192\ns_add_u32 m0, s50, 57344\nds_read2st64_b32 v[32:33], v225 offset0:0 offset1:1\nM 16,164,244\nR 182,186,12288\n"
"ds_read2st64_b32 v[34:35], v225 offset0:2 offset1:3\nG 6\nM 20,168,228\nR 160,187,0\nbuffer_load_dwordx4 v220, s[12:15], s63 offen lds\nM 20,172,244\n"
"R 162,187,4096\nds_read2st64_b32 v[36:37], v225 offset0:4 offset1:5\nM 4,168,224\nR 164,187,8192\nds_read2st64_b32 v[38:39], v225 offset0:6 offset1:7\nM 4,172,240\n"
"R 166,187,12288\nG 7\nM 8,176,224\nbuffer_load_dwordx4 v221, s[12:15], s63 offen offset:1024 lds\nM 8,180,240\ns_add_u32 m0, s50, 65536\n"
"ds_read2st64_b32 v[40:41], v225 offset0:8 offset1:9\nds_read2st64_b32 v[42:43], v225 offset0:10 offset1:11\nM 24,176,228\ns_add_u32 s63, s54, 458880\nds_read2st64_b32 v[44:45], v225 offset0:12 offset1:13\nds_read2st64_b32 v[46:47], v225 offset0:14 offset1:15\n"
"M 24,180,244\nds_read2st64_b32 v[48:49], v225 offset0:16 offset1:17\nds_read2st64_b32 v[50:51], v225 offset0:18 offset1:19\nG 6\nM 28,160,228\nbuffer_load_dwordx4 v220, s[12:15], s63 offen lds\n"
"M 28,164,244\nds_read2st64_b32 v[52:53], v225 offset0:20 offset1:21\nds_read2st64_b32 v[54:55], v225 offset0:22 offset1:23\nM 12,160,224\nds_read2st64_b32 v[56:57], v225 offset0:24 offset1:25\nds_read2st64_b32 v[58:59], v225 offset0:26 offset1:27\n"
"M 12,164,240\nds_read2st64_b32 v[60:61], v225 offset0:28 offset1:29\nds_read2st64_b32 v[62:63], v225 offset0:30 offset1:31\nG 0\ns_barrier\ns_lshl_b32 s84, s75, 11\n"
"s_add_u32 s84, s84, s49\ns_add_u32 s84, s84, 10485760\ns_add_u32 s85, s84, 32768\nv_mov_b32 v238, s46\nv_mov_b32 v239, s47\nv_pk_add_f32 v[0:1], v[0:1], v[32:33]\n"
"v_pk_add_f32 v[2:3], v[2:3], v[34:35]\nv_pk_add_f32 v[4:5], v[4:5], v[36:37]\nv_pk_add_f32 v[6:7], v[6:7], v[38:39]\nv_cvt_pk_bf16_f32 v192, v0, v1\nv_cvt_pk_bf16_f32 v193, v2, v3\nv_cvt_pk_bf16_f32 v194, v4, v5\n"
"v_cvt_pk_bf16_f32 v195, v6, v7\nbuffer_store_dwordx4 v[192:195], v252, s[20:23], s84 offen offset:0 sc1\nv_pk_add_f32 v[8:9], v[8:9], v[40:41]\nv_pk_add_f32 v[10:11], v[10:11], v[42:43]\nv_pk_add_f32 v[12:13], v[12:13], v[44:45]\nv_pk_add_f32 v[14:15], v[14:15], v[46:47]\n"
"v_cvt_pk_bf16_f32 v196, v8, v9\nv_cvt_pk_bf16_f32 v197, v10, v11\nv_cvt_pk_bf16_f32 v198, v12, v13\nv_cvt_pk_bf16_f32 v199, v14, v15\nbuffer_store_dwordx4 v[196:199], v252, s[20:23], s84 offen offset:1024 sc1\nv_pk_add_f32 v[16:17], v[16:17], v[48:49]\n"
"v_pk_add_f32 v[18:19], v[18:19], v[50:51]\nv_pk_add_f32 v[20:21], v[20:21], v[52:53]\nv_pk_add_f32 v[22:23], v[22:23], v[54:55]\nv_cvt_pk_bf16_f32 v200, v16, v17\nv_cvt_pk_bf16_f32 v201, v18, v19\nv_cvt_pk_bf16_f32 v202, v20, v21\n"
"v_cvt_pk_bf16_f32 v203, v22, v23\nbuffer_store_dwordx4 v[200:203], v252, s[20:23], s85 offen offset:0 sc1\nv_pk_add_f32 v[24:25], v[24:25], v[56:57]\nv_pk_add_f32 v[26:27], v[26:27], v[58:59]\nv_pk_add_f32 v[28:29], v[28:29], v[60:61]\nv_pk_add_f32 v[30:31], v[30:31], v[62:63]\n"
"v_cvt_pk_bf16_f32 v204, v24, v25\nv_cvt_pk_bf16_f32 v205, v26, v27\nv_cvt_pk_bf16_f32 v206, v28, v29\nv_cvt_pk_bf16_f32 v207, v30, v31\nbuffer_store_dwordx4 v[204:207], v252, s[20:23], s85 offen offset:1024 sc1\ns_add_u32 s61, s79, 88219648\n"
"s_lshl_b32 s62, s75, 3\ns_add_u32 s61, s61, s62\ns_waitcnt vmcnt(0)\ns_mov_b64 exec, 1\nbuffer_atomic_swap_x2 v[238:239], off, s[20:23], s61 sc1\ns_mov_b64 exec, -1\n"
"Lqw0:\nv_bfe_u32 v250, v252, 7, 1\nv_mul_u32_u24 v250, 2176, v250\nv_bfe_u32 v251, v252, 4, 3\nv_lshl_add_u32 v250, v251, 3, v250\ns_lshl_b32 s60, s90, 6\n"
"v_add_u32 v250, s60, v250\ns_sleep 5\nv_mov_b32 v248, 0\nv_mov_b32 v249, 0\ns_add_u32 s61, s80, 88219648\ns_mov_b64 exec, 0xffff\n"
"buffer_atomic_or_x2 v[248:249], v250, s[20:23], s61 offen sc0 sc1\ns_mov_b64 exec, -1\nbuffer_load_dwordx4 v221, s[12:15], s63 offen offset:1024 lds\ns_add_u32 m0, s50, 73728\ns_add_u32 s63, s54, 688256\nbuffer_load_dwordx4 v220, s[12:15], s63 offen lds\n"
"buffer_load_dwordx4 v221, s[12:15], s63 offen offset:1024 lds\ns_add_u32 m0, s50, 81920\ns_add_u32 s63, s54, 917504\nbuffer_load_dwordx4 v220, s[12:15], s63 offen lds\nbuffer_load_dwordx4 v221, s[12:15], s63 offen offset:1024 lds\ns_add_u32 m0, s50, 90112\n"
"s_add_u32 s63, s54, 1146880\nbuffer_load_dwordx4 v220, s[12:15], s63 offen lds\nbuffer_load_dwordx4 v221, s[12:15], s63 offen offset:1024 lds\ns_add_u32 m0, s50, 98304\ns_add_u32 s63, s54, 917632\nbuffer_load_dwordx4 v220, s[12:15], s63 offen lds\n"
"buffer_load_dwordx4 v221, s[12:15], s63 offen offset:1024 lds\ns_add_u32 m0, s50, 106496\ns_add_u32 s63, s54, 1147008\nbuffer_load_dwordx4 v220, s[12:15], s63 offen lds\nbuffer_load_dwordx4 v221, s[12:15], s63 offen offset:1024 lds\ns_add_u32 m0, s50, 114688\n"
"s_add_u32 s63, s54, 1376256\nbuffer_load_dwordx4 v220, s[12:15], s63 offen lds\nv_mov_b32 v230, 0\nv_mov_b32 v231, 0\nv_mov_b32 v232, 0\nv_mov_b32 v233, 0\n"
"v_dot2c_f32_bf16 v230, s76, v192\nv_dot2c_f32_bf16 v231, v192, v192\nv_dot2c_f32_bf16 v232, s76, v200\nv_dot2c_f32_bf16 v233, v200, v200\nv_dot2c_f32_bf16 v230, s76, v193\nv_dot2c_f32_bf16 v231, v193, v193\n"
"v_dot2c_f32_bf16 v232, s76, v201\nv_dot2c_f32_bf16 v233, v201, v201\nv_dot2c_f32_bf16 v230, s76, v194\nv_dot2c_f32_bf16 v231, v194, v194\nv_dot2c_f32_bf16 v232, s76, v202\nv_dot2c_f32_bf16 v233, v202, v202\n"
"v_dot2c_f32_bf16 v230, s76, v195\nv_dot2c_f32_bf16 v231, v195, v195\nv_dot2c_f32_bf16 v232, s76, v203\nv_dot2c_f32_bf16 v233, v203, v203\nv_dot2c_f32_bf16 v230, s76, v196\nv_dot2c_f32_bf16 v231, v196, v196\n"
"v_dot2c_f32_bf16 v232, s76, v204\nv_dot2c_f32_bf16 v233, v204, v204\nv_dot2c_f32_bf16 v230, s76, v197\nv_dot2c_f32_bf16 v231, v197, v197\nv_dot2c_f32_bf16 v232, s76, v205\nv_dot2c_f32_bf16 v233, v205, v205\n"
"v_dot2c_f32_bf16 v230, s76, v198\nv_dot2c_f32_bf16 v231, v198, v198\nv_dot2c_f32_bf16 v232, s76, v206\nv_dot2c_f32_bf16 v233, v206, v206\nv_dot2c_f32_bf16 v230, s76, v199\nv_dot2c_f32_bf16 v231, v199, v199\n"
"v_dot2c_f32_bf16 v232, s76, v207\nv_dot2c_f32_bf16 v233, v207, v207\nv_lshrrev_b32 v234, 1, v252\nv_permlane32_swap_b32 v230, v231\ns_nop 0\nv_permlane32_swap_b32 v232, v233\n"
"v_add_f32 v230, v230, v231\nv_add_f32 v232, v232, v233\nv_mov_b32 v231, s46\ns_nop 0\nv_permlane16_swap_b32 v230, v232\nv_add_f32 v230, v230, v232\n"
"s_lshr_b32 s61, s49, 3\ns_lshl2_add_u32 s61, s53, s61\ns_add_u32 s61, s61, 82313216\nbuffer_store_dwordx2 v[230:231], v234, s[20:23], s61 offen sc1\ns_waitcnt vmcnt(13)\nv_cmp_eq_u64 s[86:87], s[46:47], v[248:249]\n"
"s_nop 0\ns_and_b32 s62, s86, 0xffff\ns_cmp_eq_u32 s62, 0xffff\ns_cbranch_scc0 Lq255\nLq256:\ns_add_u32 s64, s70, 10485760\n"
"buffer_load_dwordx4 a[0:3], v252, s[20:23], s64 offen offset:0 sc1\ns_add_u32 s65, s70, 10518528\nbuffer_load_dwordx4 a[4:7], v252, s[20:23], s65 offen offset:0 sc1\ns_add_u32 s66, s71, 10485760\nbuffer_load_dwordx4 a[8:11], v252, s[20:23], s66 offen offset:0 sc1\ns_add_u32 s67, s71, 10518528\n"
"buffer_load_dwordx4 a[12:15], v252, s[20:23], s67 offen offset:0 sc1\nbuffer_load_dwordx4 a[16:19], v252, s[20:23], s64 offen offset:1024 sc1\nbuffer_load_dwordx4 a[20:23], v252, s[20:23], s65 offen offset:1024 sc1\nbuffer_load_dwordx4 a[24:27], v252, s[20:23], s66 offen offset:1024 sc1\nbuffer_load_dwordx4 a[28:31], v252, s[20:23], s67 offen offset:1024 sc1\ns_barrier\n"
"R 160,184,16384\nR 162,184,20480\nR 164,184,24576\nR 166,184,28672\nR 168,185,16384\nR 170,185,20480\n"
"R 172,185,24576\nR 174,185,28672\nR 176,186,16384\nR 178,186,20480\nR 180,186,24576\nR 182,186,28672\n"
"buffer_load_dwordx4 a[32:35], v252, s[20:23], s64 offen offset:2048 sc1\nbuffer_load_dwordx4 a[36:39], v252, s[20:23], s65 offen offset:2048 sc1\nbuffer_load_dwordx4 a[40:43], v252, s[20:23], s66 offen offset:2048 sc1\nbuffer_load_dwordx4 a[44:47], v252, s[20:23], s67 offen offset:2048 sc1\nbuffer_load_dwordx4 a[48:51], v252, s[20:23], s64 offen offset:3072 sc1\nbuffer_load_dwordx4 a[52:55], v252, s[20:23], s65 offen offset:3072 sc1\n"
"buffer_load_dwordx4 a[56:59], v252, s[20:23], s66 offen offset:3072 sc1\nbuffer_load_dwordx4 a[60:63], v252, s[20:23], s67 offen offset:3072 sc1\ns_waitcnt vmcnt(15) lgkmcnt(10)\nv_mfma_f32_16x16x32_bf16 v[32:35], v[160:163], a[0:3], 0\ns_add_u32 s64, s70, 10489856\nbuffer_load_dwordx4 a[64:67], v252, s[20:23], s64 offen offset:0 sc1\n"
"s_waitcnt vmcnt(12) lgkmcnt(8)\nM 32,164,16\nv_mfma_f32_16x16x32_bf16 v[48:51], v[160:163], a[4:7], 0\ns_waitcnt vmcnt(11)\nM 48,164,20\nv_mfma_f32_16x16x32_bf16 v[64:67], v[160:163], a[8:11], 0\n"
"v_lshrrev_b32 v238, 7, v252\ns_waitcnt vmcnt(10)\nM 64,164,24\ns_add_u32 s65, s70, 10522624\nbuffer_load_dwordx4 a[68:71], v252, s[20:23], s65 offen offset:0 sc1\nv_lshlrev_b32 v238, 9, v238\n"
"v_mfma_f32_16x16x32_bf16 v[80:83], v[160:163], a[12:15], 0\nv_and_b32 v239, 0x70, v252\ns_waitcnt vmcnt(10)\nM 80,164,28\ns_and_b32 s60, s40, 3\ns_lshl_b32 s60, s60, 7\n"
"v_add3_u32 v238, v238, v239, s60\nG 4\nv_mfma_f32_16x16x32_bf16 v[84:87], v[168:171], a[12:15], 0\nR 160,187,16384\nM 84,172,28\ns_lshl_b32 s61, s42, 14\n"
"s_add_u32 s61, s61, 84672512\nbuffer_load_dwordx4 v[0:3], v238, s[20:23], s61 offen sc1\nv_mfma_f32_16x16x32_bf16 v[68:71], v[168:171], a[8:11], 0\nR 162,187,20480\ns_lshl_b32 s61, s42, 14\ns_add_u32 s61, s61, 84676608\n"
"buffer_load_dwordx4 v[4:7], v238, s[20:23], s61 offen sc1\nM 68,172,24\ns_add_u32 s66, s71, 10489856\nbuffer_load_dwordx4 a[72:75], v252, s[20:23], s66 offen offset:0 sc1\ns_lshl_b32 s61, s42, 14\ns_add_u32 s61, s61, 84680704\n"
"buffer_load_dwordx4 v[8:11], v238, s[20:23], s61 offen sc1\nv_mfma_f32_16x16x32_bf16 v[52:55], v[168:171], a[4:7], 0\nR 164,187,24576\ns_lshl_b32 s61, s42, 14\ns_add_u32 s61, s61, 84684800\nbuffer_load_dwordx4 v[12:15], v238, s[20:23], s61 offen sc1\n"
"M 52,172,20\nv_bfe_u32 v239, v238, 2, 7\nv_mfma_f32_16x16x32_bf16 v[36:39], v[168:171], a[0:3], 0\nR 166,187,28672\nM 36,172,16\nbuffer_load_dword v239, v239, s[32:35], s54 offen offset:0\n"
"G 4\nv_mfma_f32_16x16x32_bf16 v[40:43], v[176:179], a[0:3], 0\nR 168,184,32768\nM 40,180,16\ns_add_u32 s67, s71, 10522624\nbuffer_load_dwordx4 a[76:79], v252, s[20:23], s67 offen offset:0 sc1\n"
"v_mfma_f32_16x16x32_bf16 v[56:59], v[176:179], a[4:7], 0\nR 170,184,36864\nM 56,180,20\nv_mfma_f32_16x16x32_bf16 v[72:75], v[176:179], a[8:11], 0\nR 172,184,40960\nM 72,180,24\n"
"v_mfma_f32_16x16x32_bf16 v[88:91], v[176:179], a[12:15], 0\nR 174,184,45056\nM 88,180,28\nG 4\nv_mfma_f32_16x16x32_bf16 v[92:95], v[160:163], a[12:15], 0\nR 176,185,32768\n"
"M 92,164,28\nbuffer_load_dwordx4 a[80:83], v252, s[20:23], s64 offen offset:1024 sc1\nv_mfma_f32_16x16x32_bf16 v[76:79], v[160:163], a[8:11], 0\nR 178,185,36864\nM 76,164,24\nv_mfma_f32_16x16x32_bf16 v[60:63], v[160:163], a[4:7], 0\n"
"R 180,185,40960\nM 60,164,20\nbuffer_load_dwordx4 a[84:87], v252, s[20:23], s65 offen offset:1024 sc1\nv_mfma_f32_16x16x32_bf16 v[44:47], v[160:163], a[0:3], 0\nR 182,185,45056\nM 44,164,16\n"
"G 4\nv_mfma_f32_16x16x32_bf16 v[96:99], v[168:171], a[0:3], 0\nR 160,186,32768\nM 96,172,16\nv_mfma_f32_16x16x32_bf16 v[112:115], v[168:171], a[4:7], 0\nR 162,186,36864\n"
"M 112,172,20\nbuffer_load_dwordx4 a[88:91], v252, s[20:23], s66 offen offset:1024 sc1\nv_mfma_f32_16x16x32_bf16 v[128:131], v[168:171], a[8:11], 0\nR 164,186,40960\nM 128,172,24\nv_mfma_f32_16x16x32_bf16 v[144:147], v[168:171], a[12:15], 0\n"
"R 166,186,45056\nM 144,172,28\ns_barrier\nG 4\nv_mfma_f32_16x16x32_bf16 v[148:151], v[176:179], a[12:15], 0\nR 168,187,32768\n"
"M 148,180,28\nbuffer_load_dwordx4 a[92:95], v252, s[20:23], s67 offen offset:1024 sc1\nv_mfma_f32_16x16x32_bf16 v[132:135], v[176:179], a[8:11], 0\nR 170,187,36864\nM 132,180,24\nv_mfma_f32_16x16x32_bf16 v[116:119], v[176:179], a[4:7], 0\n"
"R 172,187,40960\nM 116,180,20\nv_mfma_f32_16x16x32_bf16 v[100:103], v[176:179], a[0:3], 0\nR 174,187,45056\nM 100,180,16\nG 4\n"
"v_mfma_f32_16x16x32_bf16 v[104:107], v[160:163], a[0:3], 0\nR 176,184,49152\nv_lshrrev_b32 v242, 7, v252\nM 104,164,16\nbuffer_load_dwordx4 a[96:99], v252, s[20:23], s64 offen offset:2048 sc1\nv_lshlrev_b32 v242, 9, v242\n"
"v_mfma_f32_16x16x32_bf16 v[120:123], v[160:163], a[4:7], 0\nR 178,184,53248\nv_and_b32 v243, 0x70, v252\ns_waitcnt vmcnt(10)\nv_min3_u32 v240, v1, v3, v5\nM 120,164,20\n"
"buffer_load_dwordx4 a[100:103], v252, s[20:23], s65 offen offset:2048 sc1\ns_and_b32 s60, s40, 3\ns_lshl_b32 s60, s60, 7\nv_add3_u32 v242, v242, v243, s60\ns_waitcnt vmcnt(9)\nv_min3_u32 v240, v240, v7, v9\n"
"v_mfma_f32_16x16x32_bf16 v[136:139], v[160:163], a[8:11], 0\nR 180,184,57344\ns_waitcnt vmcnt(8)\nv_min3_u32 v240, v240, v11, v13\nv_min3_u32 v240, v240, v15, v15\nM 136,164,24\n"
"s_lshl_b32 s61, s42, 14\ns_add_u32 s61, s61, 84934656\nbuffer_load_dwordx4 v[16:19], v242, s[20:23], s61 offen sc1\nv_max3_u32 v241, v1, v3, v5\nv_mfma_f32_16x16x32_bf16 v[152:155], v[160:163], a[12:15], 0\nR 182,184,61440\n"
"s_lshl_b32 s61, s42, 14\ns_add_u32 s61, s61, 84938752\nbuffer_load_dwordx4 v[20:23], v242, s[20:23], s61 offen sc1\nv_max3_u32 v241, v241, v7, v9\nM 152,164,28\ns_lshl_b32 s61, s42, 14\n"
"s_add_u32 s61, s61, 84942848\nbuffer_load_dwordx4 v[24:27], v242, s[20:23], s61 offen sc1\nG 4\nv_max3_u32 v241, v241, v11, v13\nv_mfma_f32_16x16x32_bf16 v[156:159], v[168:171], a[12:15], 0\nR 160,185,49152\n"
"s_lshl_b32 s61, s42, 14\ns_add_u32 s61, s61, 84946944\nbuffer_load_dwordx4 v[28:31], v242, s[20:23], s61 offen sc1\nv_max3_u32 v241, v241, v15, v15\nM 156,172,28\nbuffer_load_dwordx4 a[104:107], v252, s[20:23], s66 offen offset:2048 sc1\n"
"v_bfe_u32 v243, v242, 2, 7\nv_cmp_ne_u32 s[86:87], s46, v240\nv_mfma_f32_16x16x32_bf16 v[140:143], v[168:171], a[8:11], 0\nR 162,185,53248\nv_cmp_ne_u32 s[88:89], s46, v241\nM 140,172,24\n"
"buffer_load_dword v243, v243, s[32:35], s54 offen offset:128\ns_or_b64 s[86:87], s[86:87], s[88:89]\ns_cbranch_scc1 Lq309\nLq310:\nv_mfma_f32_16x16x32_bf16 v[124:127], v[168:171], a[4:7], 0\nR 164,185,57344\n"
"v_add_f32 v0, v0, v8\nv_add_f32 v2, v2, v10\nM 124,172,20\nv_add_f32 v4, v4, v12\nv_add_f32 v6, v6, v14\nv_add_f32 v0, v0, v4\n"
"v_mfma_f32_16x16x32_bf16 v[108:111], v[168:171], a[0:3], 0\nR 166,185,61440\nv_add_f32 v2, v2, v6\nv_add_f32_dpp v0, v0, v0 row_ror:8 row_mask:0xf bank_mask:0xf\nM 108,172,16\nG 4\n"
"v_add_f32_dpp v2, v2, v2 row_ror:8 row_mask:0xf bank_mask:0xf\nv_mov_b32 v4, v0\nv_mov_b32 v5, v2\nM 32,176,32\nR 168,186,49152\nv_permlane32_swap_b32 v4, v0\n"
"v_permlane32_swap_b32 v5, v2\nM 32,180,48\nbuffer_load_dwordx4 a[108:111], v252, s[20:23], s67 offen offset:2048 sc1\nv_add_f32 v0, v0, v4\nM 48,176,36\nR 170,186,53248\n"
"v_add_f32 v2, v2, v5\nM 48,180,52\nv_mov_b32 v4, v0\nv_mov_b32 v5, v2\nM 64,176,40\nR 172,186,57344\n"
"v_permlane16_swap_b32 v4, v0\nM 64,180,56\nbuffer_load_dwordx4 a[112:115], v252, s[20:23], s64 offen offset:3072 sc1\nM 80,176,44\nR 174,186,61440\nv_permlane16_swap_b32 v5, v2\n"
"M 80,180,60\ns_barrier\nG 4\nM 84,160,44\nR 176,187,49152\nv_add_f32 v0, v0, v4\n"
"M 84,164,60\nv_add_f32 v2, v2, v5\nv_and_b32 v8, s77, v0\nM 68,160,40\nR 178,187,53248\nv_sub_f32 v9, v0, v8\n"
"M 68,164,56\nbuffer_load_dwordx4 a[116:119], v252, s[20:23], s65 offen offset:3072 sc1\nM 52,160,36\nR 180,187,57344\ns_waitcnt vmcnt(16)\nv_lshlrev_b32 v10, 16, v239\n"
"M 52,164,52\nv_cvt_pk_bf16_f32 v4, v8, v9\nv_cvt_pk_bf16_f32 v5, v8, v10\nM 36,160,32\nR 182,187,61440\nv_and_b32 v8, s77, v2\n"
"M 36,164,48\nG 4\nv_sub_f32 v9, v2, v8\nv_and_b32 v10, s77, v239\nM 40,168,32\nR 160,188,0\n"
"v_cvt_pk_bf16_f32 v6, v8, v9\nM 40,172,48\nbuffer_load_dwordx4 a[120:123], v252, s[20:23], s66 offen offset:3072 sc1\nM 56,168,36\nR 162,188,4096\nv_cvt_pk_bf16_f32 v7, v8, v10\n"
"M 56,172,52\nv_and_b32 v238, 0x1ff, v238\nv_add_u32 v238, 157184, v238\nM 72,168,40\nR 164,188,8192\nds_write_b128 v238, v[4:7]\n"
"M 72,172,56\nM 88,168,44\nR 166,188,12288\nM 88,172,60\nG 5\nM 92,176,44\n"
"R 168,189,0\nM 92,180,60\nbuffer_load_dwordx4 a[124:127], v252, s[20:23], s67 offen offset:3072 sc1\nM 76,176,40\nR 170,189,4096\nM 76,180,56\n"
"M 60,176,36\nR 172,189,8192\nv_lshrrev_b32 v238, 7, v252\nM 60,180,52\ns_add_u32 s64, s70, 10493952\nbuffer_load_dwordx4 a[128:131], v252, s[20:23], s64 offen offset:0 sc1\n"
"v_lshlrev_b32 v238, 9, v238\nM 44,176,32\nR 174,189,12288\nv_and_b32 v239, 0x70, v252\ns_waitcnt vmcnt(10)\nv_min3_u32 v244, v17, v19, v21\n"
"M 44,180,48\ns_and_b32 s60, s40, 3\ns_lshl_b32 s60, s60, 7\nv_add3_u32 v238, v238, v239, s60\nG 4\ns_waitcnt vmcnt(9)\n"
"v_min3_u32 v244, v244, v23, v25\nM 96,160,32\nR 176,190,0\ns_lshl_b32 s61, s42, 14\ns_add_u32 s61, s61, 85196800\nbuffer_load_dwordx4 v[0:3], v238, s[20:23], s61 offen sc1\n"
"s_waitcnt vmcnt(9)\nv_min3_u32 v244, v244, v27, v29\nM 96,164,48\ns_add_u32 s65, s70, 10526720\nbuffer_load_dwordx4 a[132:135], v252, s[20:23], s65 offen offset:0 sc1\nv_min3_u32 v244, v244, v31, v31\n"
"M 112,160,36\nR 178,190,4096\ns_lshl_b32 s61, s42, 14\ns_add_u32 s61, s61, 85200896\nbuffer_load_dwordx4 v[4:7], v238, s[20:23], s61 offen sc1\nv_max3_u32 v245, v17, v19, v21\n"
"M 112,164,52\ns_lshl_b32 s61, s42, 14\ns_add_u32 s61, s61, 85204992\nbuffer_load_dwordx4 v[8:11], v238, s[20:23], s61 offen sc1\nv_max3_u32 v245, v245, v23, v25\nM 128,160,40\n"
"R 180,190,8192\ns_lshl_b32 s61, s42, 14\ns_add_u32 s61, s61, 85209088\nbuffer_load_dwordx4 v[12:15], v238, s[20:23], s61 offen sc1\nv_max3_u32 v245, v245, v27, v29\nM 128,164,56\n"
"v_bfe_u32 v239, v238, 2, 7\nv_max3_u32 v245, v245, v31, v31\nv_cmp_ne_u32 s[86:87], s46, v244\nM 144,160,44\nR 182,190,12288\nv_cmp_ne_u32 s[88:89], s46, v245\n"
"M 144,164,60\nbuffer_load_dword v239, v239, s[32:35], s54 offen offset:256\ns_barrier\ns_or_b64 s[86:87], s[86:87], s[88:89]\ns_cbranch_scc1 Lq321\nLq322:\n"
"G 4\nM 148,168,44\nR 160,191,0\nv_add_f32 v16, v16, v24\nv_add_f32 v18, v18, v26\nM 148,172,60\n"
"s_add_u32 s66, s71, 10493952\nbuffer_load_dwordx4 a[136:139], v252, s[20:23], s66 offen offset:0 sc1\nv_add_f32 v20, v20, v28\nM 132,168,40\nR 162,191,4096\nv_add_f32 v22, v22, v30\n"
"v_add_f32 v16, v16, v20\nM 132,172,56\nv_add_f32 v18, v18, v22\nv_add_f32_dpp v16, v16, v16 row_ror:8 row_mask:0xf bank_mask:0xf\ns_nop 0\nv_add_f32_dpp v18, v18, v18 row_ror:8 row_mask:0xf bank_mask:0xf\n"
"M 116,168,36\nR 164,191,8192\nv_mov_b32 v20, v16\nv_mov_b32 v21, v18\nM 116,172,52\ns_add_u32 s67, s71, 10526720\n"
"buffer_load_dwordx4 a[140:143], v252, s[20:23], s67 offen offset:0 sc1\nv_permlane32_swap_b32 v20, v16\nM 100,168,32\nR 166,191,12288\nv_permlane32_swap_b32 v21, v18\nv_add_f32 v16, v16, v20\n"
"M 100,172,48\nG 4\nv_add_f32 v18, v18, v21\nv_mov_b32 v20, v16\nv_mov_b32 v21, v18\nM 104,176,32\n"
"R 168,188,16384\nbuffer_load_dwordx4 v221, s[12:15], s63 offen offset:1024 lds\nv_permlane16_swap_b32 v20, v16\nM 104,180,48\ns_add_u32 m0, s50, 0\ns_add_u32 s63, s54, 1605632\n"
"buffer_load_dwordx4 v220, s[12:15], s63 offen lds\nv_permlane16_swap_b32 v21, v18\nM 120,176,36\nR 170,188,20480\nv_add_f32 v16, v16, v20\nv_add_f32 v18, v18, v21\n"
"M 120,180,52\nbuffer_load_dwordx4 a[144:147], v252, s[20:23], s64 offen offset:1024 sc1\nv_and_b32 v24, s77, v16\nM 136,176,40\nR 172,188,24576\nv_sub_f32 v25, v16, v24\n"
"M 136,180,56\nbuffer_load_dwordx4 v221, s[12:15], s63 offen offset:1024 lds\nM 152,176,44\nR 174,188,28672\ns_waitcnt vmcnt(18)\nv_lshlrev_b32 v26, 16, v243\n"
"M 152,180,60\nG 4\nv_cvt_pk_bf16_f32 v20, v24, v25\nv_cvt_pk_bf16_f32 v21, v24, v26\nM 156,160,44\nR 176,189,16384\n"
"v_and_b32 v24, s77, v18\nM 156,164,60\nbuffer_load_dwordx4 a[148:151], v252, s[20:23], s65 offen offset:1024 sc1\nM 140,160,40\nR 178,189,20480\nv_sub_f32 v25, v18, v24\n"
"M 140,164,56\nv_and_b32 v26, s77, v243\nv_cvt_pk_bf16_f32 v22, v24, v25\nM 124,160,36\nR 180,189,24576\nv_cvt_pk_bf16_f32 v23, v24, v26\n"
"M 124,164,52\nv_and_b32 v242, 0x1ff, v242\nv_add_u32 v242, 157696, v242\nM 108,160,32\nR 182,189,28672\nds_write_b128 v242, v[20:23]\n"
"M 108,164,48\nG 5\nM 32,168,64\nR 160,190,16384\nM 32,172,80\nbuffer_load_dwordx4 a[152:155], v252, s[20:23], s66 offen offset:1024 sc1\n"
"M 48,168,68\nR 162,190,20480\ns_add_u32 s63, s54, 1376384\nM 48,172,84\ns_add_u32 m0, s50, 8192\nM 64,168,72\n"
"R 164,190,24576\nM 64,172,88\nbuffer_load_dwordx4 a[156:159], v252, s[20:23], s67 offen offset:1024 sc1\nM 80,168,76\nR 166,190,28672\nM 80,172,92\n"
"G 4\ns_barrier\nM 84,176,76\nR 168,191,16384\nbuffer_load_dwordx4 v220, s[12:15], s63 offen lds\nv_lshrrev_b32 v242, 7, v252\n"
"M 84,180,92\nbuffer_load_dwordx4 v221, s[12:15], s63 offen offset:1024 lds\nv_lshlrev_b32 v242, 9, v242\nM 68,176,72\nR 170,191,20480\nv_and_b32 v243, 0x70, v252\n"
"s_waitcnt vmcnt(14)\nv_min3_u32 v240, v1, v3, v5\nM 68,180,88\nbuffer_load_dwordx4 a[160:163], v252, s[20:23], s64 offen offset:2048 sc1\ns_and_b32 s60, s40, 1\ns_lshl_b32 s60, s60, 7\n"
"v_add3_u32 v242, v242, v243, s60\ns_waitcnt vmcnt(14)\nv_min3_u32 v240, v240, v7, v9\nM 52,176,68\nR 172,191,24576\ns_waitcnt vmcnt(13)\n"
"v_min3_u32 v240, v240, v11, v13\nv_min3_u32 v240, v240, v15, v15\nM 52,180,84\ns_lshl_b32 s61, s42, 14\ns_add_u32 s61, s61, 85458944\nbuffer_load_dwordx4 v[16:19], v242, s[20:23], s61 offen sc1\n"
"v_max3_u32 v241, v1, v3, v5\nM 36,176,64\nR 174,191,28672\ns_lshl_b32 s61, s42, 14\ns_add_u32 s61, s61, 85463040\nbuffer_load_dwordx4 v[20:23], v242, s[20:23], s61 offen sc1\n"
"s_add_u32 s63, s54, 1605760\nv_max3_u32 v241, v241, v7, v9\nM 36,180,80\ns_lshl_b32 s61, s42, 14\ns_add_u32 s61, s61, 85467136\nbuffer_load_dwordx4 v[24:27], v242, s[20:23], s61 offen sc1\n"
"G 4\nv_max3_u32 v241, v241, v11, v13\nM 40,160,64\nR 176,188,32768\ns_lshl_b32 s61, s42, 14\ns_add_u32 s61, s61, 85471232\n"
"buffer_load_dwordx4 v[28:31], v242, s[20:23], s61 offen sc1\nv_max3_u32 v241, v241, v15, v15\nM 40,164,80\nbuffer_load_dwordx4 a[164:167], v252, s[20:23], s65 offen offset:2048 sc1\nv_bfe_u32 v243, v242, 2, 7\nv_cmp_ne_u32 s[86:87], s46, v240\n"
"M 56,160,68\nR 178,188,36864\ns_add_u32 m0, s50, 16384\nv_cmp_ne_u32 s[88:89], s46, v241\nM 56,164,84\nbuffer_load_dword v243, v243, s[32:35], s55 offen offset:0\n"
"s_or_b64 s[86:87], s[86:87], s[88:89]\ns_cbranch_scc1 Lq333\nLq334:\nM 72,160,72\nR 180,188,40960\nbuffer_load_dwordx4 v220, s[12:15], s63 offen lds\n"
"v_add_f32 v0, v0, v8\nM 72,164,88\nbuffer_load_dwordx4 a[168:171], v252, s[20:23], s66 offen offset:2048 sc1\nv_add_f32 v2, v2, v10\nM 88,160,76\nR 182,188,45056\n"
"v_add_f32 v4, v4, v12\nv_add_f32 v6, v6, v14\nM 88,164,92\nG 4\nv_add_f32 v0, v0, v4\nv_add_f32 v2, v2, v6\n"
"s_nop 0\nv_add_f32_dpp v0, v0, v0 row_ror:8 row_mask:0xf bank_mask:0xf\nM 92,168,76\nR 160,189,32768\nbuffer_load_dwordx4 v221, s[12:15], s63 offen offset:1024 lds\nv_add_f32_dpp v2, v2, v2 row_ror:8 row_mask:0xf bank_mask:0xf\n"
"M 92,172,92\nv_mov_b32 v4, v0\nv_mov_b32 v5, v2\ns_nop 0\nv_permlane32_swap_b32 v4, v0\nM 76,168,72\n"
"R 162,189,36864\nv_permlane32_swap_b32 v5, v2\nv_add_f32 v0, v0, v4\nM 76,172,88\nbuffer_load_dwordx4 a[172:175], v252, s[20:23], s67 offen offset:2048 sc1\nv_add_f32 v2, v2, v5\n"
"M 60,168,68\nR 164,189,40960\nv_mov_b32 v4, v0\nv_mov_b32 v5, v2\nM 60,172,84\nv_permlane16_swap_b32 v4, v0\n"
"v_permlane16_swap_b32 v5, v2\nv_add_f32 v0, v0, v4\nM 44,168,64\nR 166,189,45056\nv_add_f32 v2, v2, v5\nv_and_b32 v8, s77, v0\n"
"M 44,172,80\nG 4\nv_sub_f32 v9, v0, v8\ns_waitcnt vmcnt(22)\nv_lshlrev_b32 v10, 16, v239\nv_cvt_pk_bf16_f32 v4, v8, v9\n"
"M 96,176,64\nR 168,190,32768\nv_cvt_pk_bf16_f32 v5, v8, v10\nv_and_b32 v8, s77, v2\nM 96,180,80\nbuffer_load_dwordx4 a[176:179], v252, s[20:23], s64 offen offset:3072 sc1\n"
"v_sub_f32 v9, v2, v8\nM 112,176,68\nR 170,190,36864\nv_and_b32 v10, s77, v239\nM 112,180,84\nv_cvt_pk_bf16_f32 v6, v8, v9\n"
"v_cvt_pk_bf16_f32 v7, v8, v10\nM 128,176,72\nR 172,190,40960\nv_and_b32 v238, 0x1ff, v238\nM 128,180,88\ns_add_u32 m0, s50, 24576\n"
"v_add_u32 v238, 158208, v238\nds_write_b128 v238, v[4:7]\nM 144,176,76\nR 174,190,45056\ns_add_u32 s63, s54, 1835008\nM 144,180,92\n"
"s_waitcnt vmcnt(17) lgkmcnt(1)\ns_barrier\nM 148,160,76\nR 176,191,32768\nM 148,164,92\nbuffer_load_dwordx4 a[180:183], v252, s[20:23], s65 offen offset:3072 sc1\n"
"M 132,160,72\nR 178,191,36864\nM 132,164,88\nbuffer_load_dwordx4 v220, s[12:15], s63 offen lds\nM 116,160,68\nR 180,191,40960\n"
"M 116,164,84\nbuffer_load_dwordx4 a[184:187], v252, s[20:23], s66 offen offset:3072 sc1\nM 100,160,64\nR 182,191,45056\nM 100,164,80\nG 4\n"
"M 104,168,64\nR 160,188,49152\nbuffer_load_dwordx4 v221, s[12:15], s63 offen offset:1024 lds\nM 104,172,80\ns_add_u32 m0, s50, 32768\ns_add_u32 s63, s54, 2064384\n"
"buffer_load_dwordx4 v220, s[12:15], s63 offen lds\nM 120,168,68\nR 162,188,53248\nM 120,172,84\nbuffer_load_dwordx4 a[188:191], v252, s[20:23], s67 offen offset:3072 sc1\nM 136,168,72\n"
"R 164,184,0\nM 136,172,88\nbuffer_load_dwordx4 v221, s[12:15], s63 offen offset:1024 lds\nM 152,168,76\nR 166,184,4096\ns_waitcnt vmcnt(16)\n"
"v_min3_u32 v244, v17, v19, v21\ns_waitcnt vmcnt(15)\nv_min3_u32 v244, v244, v23, v25\nM 152,172,92\nG 4\ns_waitcnt vmcnt(14)\n"
"v_min3_u32 v244, v244, v27, v29\nv_min3_u32 v244, v244, v31, v31\nv_max3_u32 v245, v17, v19, v21\nM 156,176,76\nR 168,189,49152\nv_max3_u32 v245, v245, v23, v25\n"
"v_max3_u32 v245, v245, v27, v29\nM 156,180,92\ns_add_u32 s64, s70, 10498048\nbuffer_load_dwordx4 a[192:195], v252, s[20:23], s64 offen offset:0 sc1\nv_max3_u32 v245, v245, v31, v31\nM 140,176,72\n"
"R 170,189,53248\nv_cmp_ne_u32 s[86:87], s46, v244\nv_cmp_ne_u32 s[88:89], s46, v245\nM 140,180,88\ns_or_b64 s[86:87], s[86:87], s[88:89]\ns_cbranch_scc1 Lq345\n"
"Lq346:\nv_add_f32 v16, v16, v24\nv_add_f32 v18, v18, v26\nM 124,176,68\nR 172,185,0\nv_add_f32 v20, v20, v28\n"
"v_add_f32 v22, v22, v30\nM 124,180,84\ns_add_u32 s65, s70, 10530816\nbuffer_load_dwordx4 a[196:199], v252, s[20:23], s65 offen offset:0 sc1\nv_add_f32 v16, v16, v20\nM 108,176,64\n"
"R 174,185,4096\nv_add_f32 v18, v18, v22\nv_add_f32_dpp v16, v16, v16 row_ror:8 row_mask:0xf bank_mask:0xf\nM 108,180,80\nG 4\nv_add_f32_dpp v18, v18, v18 row_ror:8 row_mask:0xf bank_mask:0xf\n"
"v_mov_b32 v20, v16\nv_mov_b32 v21, v18\nM 32,160,96\nR 176,190,49152\nv_permlane32_swap_b32 v20, v16\nv_permlane32_swap_b32 v21, v18\n"
"M 32,164,112\nv_add_f32 v16, v16, v20\nv_add_f32 v18, v18, v21\nv_mov_b32 v20, v16\nM 48,160,100\nR 178,190,53248\n"
"v_mov_b32 v21, v18\nv_permlane16_swap_b32 v20, v16\nM 48,164,116\ns_add_u32 s66, s71, 10498048\nbuffer_load_dwordx4 a[200:203], v252, s[20:23], s66 offen offset:0 sc1\nv_permlane16_swap_b32 v21, v18\n"
"M 64,160,104\nR 180,186,0\nv_add_f32 v16, v16, v20\nv_add_f32 v18, v18, v21\nM 64,164,120\ns_add_u32 m0, s50, 40960\n"
"v_and_b32 v24, s77, v16\nv_sub_f32 v25, v16, v24\ns_waitcnt vmcnt(15)\nv_lshlrev_b32 v26, 16, v243\nM 80,160,108\nR 182,186,4096\n"
"s_add_u32 s63, s54, 2293760\nv_cvt_pk_bf16_f32 v20, v24, v25\nM 80,164,124\ns_waitcnt vmcnt(12)\ns_barrier\nG 4\n"
"M 84,168,108\nR 160,191,49152\nv_cvt_pk_bf16_f32 v21, v24, v26\nM 84,172,124\ns_add_u32 s67, s71, 10530816\nbuffer_load_dwordx4 a[204:207], v252, s[20:23], s67 offen offset:0 sc1\n"
"M 68,168,104\nR 162,191,53248\nv_and_b32 v24, s77, v18\nM 68,172,120\nbuffer_load_dwordx4 v220, s[12:15], s63 offen lds\nM 52,168,100\n"
"R 164,187,0\nv_sub_f32 v25, v18, v24\nM 52,172,116\nbuffer_load_dwordx4 v221, s[12:15], s63 offen offset:1024 lds\nM 36,168,96\nR 166,187,4096\n"
"s_add_u32 m0, s50, 49152\nM 36,172,112\nG 4\nv_and_b32 v26, s77, v243\nv_cvt_pk_bf16_f32 v22, v24, v25\nM 40,176,96\n"
"R 168,184,8192\ns_add_u32 s63, s54, 2523136\nM 40,180,112\nbuffer_load_dwordx4 a[208:211], v252, s[20:23], s64 offen offset:1024 sc1\nM 56,176,100\nR 170,184,12288\n"
"v_cvt_pk_bf16_f32 v23, v24, v26\nM 56,180,116\nbuffer_load_dwordx4 v220, s[12:15], s63 offen lds\nM 72,176,104\nR 172,184,16384\nv_and_b32 v242, 0x1ff, v242\n"
"M 72,180,120\nbuffer_load_dwordx4 a[212:215], v252, s[20:23], s65 offen offset:1024 sc1\nM 88,176,108\nR 174,184,20480\nv_add_u32 v242, 158720, v242\nM 88,180,124\n"
"ds_write_b128 v242, v[20:23]\nG 5\nM 92,160,108\nR 176,185,8192\nbuffer_load_dwordx4 v221, s[12:15], s63 offen offset:1024 lds\nM 92,164,124\n"
"M 76,160,104\nR 178,185,12288\nM 76,164,120\nbuffer_load_dwordx4 a[216:219], v252, s[20:23], s66 offen offset:1024 sc1\nM 60,160,100\nR 180,185,16384\n"
"Lq580:\ns_lshr_b32 s60, s45, 3\ns_lshl2_add_u32 s60, s60, s40\ns_lshl_b32 s60, s60, 8\nv_lshl_add_u32 v224, v252, 3, s60\nv_add_u32 v224, Lq580+4096-LqPC, v224\n"
"s_mov_b64 exec, 3\nglobal_load_dword v224, v224, s[98:99]\ns_mov_b64 exec, -1\nM 60,164,116\nM 44,160,96\nR 182,185,20480\n"
"M 44,164,112\nG 5\nM 96,168,96\nR 160,186,8192\nM 96,172,112\nbuffer_load_dwordx4 a[220:223], v252, s[20:23], s67 offen offset:1024 sc1\n"
"M 112,168,100\nR 162,186,12288\nM 112,172,116\nM 128,168,104\nR 164,186,16384\nM 128,172,120\n"
"M 144,168,108\nR 166,186,20480\ns_add_u32 m0, s50, 57344\nM 144,172,124\ns_waitcnt vmcnt(13) lgkmcnt(8)\ns_barrier\n"
"G 4\nM 148,176,108\nR 168,187,8192\ns_add_u32 s63, s54, 2752512\nM 148,180,124\nbuffer_load_dwordx4 a[224:227], v252, s[20:23], s64 offen offset:2048 sc1\n"
"M 132,176,104\nR 170,187,12288\nM 132,180,120\nbuffer_load_dwordx4 a[228:231], v252, s[20:23], s65 offen offset:2048 sc1\nM 116,176,100\nR 172,187,16384\n"
"buffer_load_dwordx4 v220, s[12:15], s63 offen lds\nM 116,180,116\nbuffer_load_dwordx4 v221, s[12:15], s63 offen offset:1024 lds\nM 100,176,96\nR 174,187,20480\ns_add_u32 m0, s50, 65536\n"
"M 100,180,112\nG 4\nM 104,160,96\nR 176,184,24576\ns_add_u32 s63, s54, 2981888\nM 104,164,112\n"
"buffer_load_dwordx4 a[232:235], v252, s[20:23], s66 offen offset:2048 sc1\nM 120,160,100\nR 178,184,28672\nM 120,164,116\nbuffer_load_dwordx4 v220, s[12:15], s63 offen lds\nM 136,160,104\n"
"R 180,184,32768\nM 136,164,120\nbuffer_load_dwordx4 v221, s[12:15], s63 offen offset:1024 lds\nM 152,160,108\nR 182,184,36864\nM 152,164,124\n"
"G 4\nM 156,168,108\nR 160,185,24576\nM 156,172,124\nbuffer_load_dwordx4 a[236:239], v252, s[20:23], s67 offen offset:2048 sc1\nM 140,168,104\n"
"R 162,185,28672\nM 140,172,120\nM 124,168,100\nR 164,185,32768\nM 124,172,116\nbuffer_load_dwordx4 a[240:243], v252, s[20:23], s64 offen offset:3072 sc1\n"
"M 108,168,96\nR 166,185,36864\nM 108,172,112\nG 4\nM 32,176,128\nR 168,186,24576\n"
"M 32,180,144\nM 48,176,132\nR 170,186,28672\nM 48,180,148\nbuffer_load_dwordx4 a[244:247], v252, s[20:23], s65 offen offset:3072 sc1\nM 64,176,136\n"
"R 172,186,32768\nM 64,180,152\nM 80,176,140\nR 174,186,36864\ns_add_u32 m0, s50, 73728\nM 80,180,156\n"
"s_waitcnt vmcnt(13)\ns_barrier\nG 4\nM 84,160,140\nR 176,187,24576\ns_add_u32 s63, s54, 3211264\n"
"M 84,164,156\nbuffer_load_dwordx4 a[248:251], v252, s[20:23], s66 offen offset:3072 sc1\nM 68,160,136\nR 178,187,28672\nM 68,164,152\ns_lshr_b32 s84, s49, 3\n"
"M 52,160,132\nR 180,187,32768\nbuffer_load_dwordx4 v220, s[12:15], s63 offen lds\nM 52,164,148\ns_add_u32 s84, s84, 82313216\nM 36,160,128\n"
"R 182,187,36864\nM 36,164,144\nbuffer_load_dwordx4 v[0:3], v252, s[20:23], s84 offen offset:0 sc1\nG 4\nM 40,168,128\nR 160,184,40960\n"
"buffer_load_dwordx4 v221, s[12:15], s63 offen offset:1024 lds\nM 40,172,144\nbuffer_load_dwordx4 a[252:255], v252, s[20:23], s67 offen offset:3072 sc1\ns_add_u32 s63, s54, 3440640\nM 56,168,132\nR 162,184,45056\n"
"s_add_u32 m0, s50, 81920\nM 56,172,148\nbuffer_load_dwordx4 v[4:7], v252, s[20:23], s84 offen offset:1024 sc1\nM 72,168,136\nR 164,184,49152\ns_lshr_b32 s85, s49, 3\n"
"buffer_load_dwordx4 v220, s[12:15], s63 offen lds\nM 72,172,152\nbuffer_load_dwordx4 v[8:11], v252, s[20:23], s84 offen offset:2048 sc1\nM 88,168,140\nR 166,184,53248\ns_add_u32 s85, s85, 82317312\n"
"M 88,172,156\nG 4\nM 92,176,140\nR 168,185,40960\nbuffer_load_dwordx4 v221, s[12:15], s63 offen offset:1024 lds\nM 92,180,156\n"
"buffer_load_dwordx4 v[12:15], v252, s[20:23], s84 offen offset:3072 sc1\nM 76,176,136\nR 170,185,45056\nM 76,180,152\nbuffer_load_dwordx4 v[16:19], v252, s[20:23], s85 offen offset:0 sc1\nM 60,176,132\n"
"R 172,185,49152\nM 60,180,148\nbuffer_load_dwordx4 v[20:23], v252, s[20:23], s85 offen offset:1024 sc1\nM 44,176,128\nR 174,185,53248\nM 44,180,144\n"
"G 4\nM 32,160,160\nR 176,186,40960\nM 32,164,176\nbuffer_load_dwordx4 v[24:27], v252, s[20:23], s85 offen offset:2048 sc1\nM 48,160,164\n"
"R 178,186,45056\nM 48,164,180\nbuffer_load_dwordx4 v[28:31], v252, s[20:23], s85 offen offset:3072 sc1\nM 64,160,168\nR 180,186,49152\ns_add_u32 s63, s54, 1835136\n"
"M 64,164,184\ns_add_u32 m0, s50, 90112\nM 80,160,172\nR 182,186,53248\nM 80,164,188\ns_waitcnt vmcnt(17)\n"
"s_barrier\nG 4\nM 84,168,172\nR 160,187,40960\nM 84,172,188\nbuffer_load_dwordx4 v220, s[12:15], s63 offen lds\n"
"M 68,168,168\nR 162,187,45056\nM 68,172,184\nM 52,168,164\nR 164,187,49152\nv_bfe_u32 v250, v252, 5, 1\n"
"v_mul_u32_u24 v250, 2560, v250\nM 52,172,180\nbuffer_load_dwordx4 v221, s[12:15], s63 offen offset:1024 lds\nv_and_b32 v251, 16, v252\nM 36,168,160\nR 166,187,53248\n"
"s_add_u32 m0, s50, 98304\nv_lshl_add_u32 v250, v251, 1, v250\nM 36,172,176\ns_lshl_b32 s60, s90, 6\nv_add_u32 v250, s60, v250\nG 4\n"
"M 40,176,160\nR 168,184,57344\ns_add_u32 s63, s54, 2064512\nM 40,180,176\nbuffer_load_dwordx4 v220, s[12:15], s63 offen lds\nM 56,176,164\n"
"R 170,184,61440\nM 56,180,180\nM 72,176,168\nR 172,188,0\nM 72,180,184\nbuffer_load_dwordx4 v221, s[12:15], s63 offen offset:1024 lds\n"
"M 88,176,172\nR 174,188,4096\nM 88,180,188\nG 4\nM 92,160,172\nR 176,185,57344\n"
"M 92,164,188\nM 76,160,168\nR 178,185,61440\nM 76,164,184\ns_add_u32 m0, s50, 106496\nM 60,160,164\n"
"R 180,189,0\nM 60,164,180\ns_add_u32 s63, s54, 2293888\nM 44,160,160\nR 182,189,4096\nM 44,164,176\n"
"G 4\nM 32,168,192\nR 160,186,57344\nM 32,172,208\nbuffer_load_dwordx4 v220, s[12:15], s63 offen lds\nM 48,168,196\n"
"R 162,186,61440\nM 48,172,212\nM 64,168,200\nR 164,190,0\nM 64,172,216\nbuffer_load_dwordx4 v221, s[12:15], s63 offen offset:1024 lds\n"
"M 80,168,204\nR 166,190,4096\ns_add_u32 m0, s50, 114688\nM 80,172,220\ns_waitcnt vmcnt(11)\ns_barrier\n"
"G 4\nM 84,176,204\nR 168,187,57344\ns_add_u32 s63, s54, 2523264\nM 84,180,220\nbuffer_load_dwordx4 v220, s[12:15], s63 offen lds\n"
"M 68,176,200\nR 170,187,61440\nv_min3_u32 v230, v1, v3, v5\nv_min3_u32 v230, v230, v7, v9\nM 68,180,216\ns_waitcnt vmcnt(11)\n"
"v_min3_u32 v230, v230, v11, v13\ns_waitcnt vmcnt(10)\nv_min3_u32 v230, v230, v15, v17\ns_waitcnt vmcnt(9)\nv_min3_u32 v230, v230, v19, v21\nM 52,176,196\n"
"R 172,191,0\ns_waitcnt vmcnt(8)\nv_min3_u32 v230, v230, v23, v25\ns_waitcnt vmcnt(7)\nv_min3_u32 v230, v230, v27, v29\nM 52,180,212\n"
"buffer_load_dwordx4 v221, s[12:15], s63 offen offset:1024 lds\nv_min3_u32 v230, v230, v31, v31\nM 36,176,192\nR 174,191,4096\ns_add_u32 s63, s54, 2752640\nv_max3_u32 v231, v1, v3, v5\n"
"M 36,180,208\nG 4\nv_max3_u32 v231, v231, v7, v9\nv_max3_u32 v231, v231, v11, v13\nv_max3_u32 v231, v231, v15, v17\nM 40,160,192\n"
"R 176,188,8192\nv_max3_u32 v231, v231, v19, v21\nv_max3_u32 v231, v231, v23, v25\nM 40,164,208\ns_add_u32 m0, s50, 0\nv_max3_u32 v231, v231, v27, v29\n"
"v_max3_u32 v231, v231, v31, v31\nv_cmp_ne_u32 s[86:87], s46, v230\nM 56,160,196\nR 178,188,12288\nv_cmp_ne_u32 s[88:89], s46, v231\nM 56,164,212\n"
"buffer_load_dwordx4 v220, s[12:15], s63 offen lds\ns_or_b64 s[86:87], s[86:87], s[88:89]\ns_cbranch_scc1 Lq307\nLq308:\nM 72,160,200\nR 180,188,16384\n"
"v_add_f32 v0, v0, v16\nv_add_f32 v2, v2, v18\nM 72,164,216\nv_add_f32 v4, v4, v20\nv_add_f32 v6, v6, v22\nv_add_f32 v8, v8, v24\n"
"M 88,160,204\nR 182,188,20480\nv_add_f32 v10, v10, v26\nv_add_f32 v12, v12, v28\nM 88,164,220\nG 4\n"
"v_add_f32 v14, v14, v30\nv_add_f32 v0, v0, v8\nv_add_f32 v2, v2, v10\nM 92,168,204\nR 160,189,8192\nv_add_f32 v4, v4, v12\n"
"v_add_f32 v6, v6, v14\nM 92,172,220\nbuffer_load_dwordx4 v221, s[12:15], s63 offen offset:1024 lds\nv_add_f32 v0, v0, v4\nM 76,168,200\nR 162,189,12288\n"
"s_add_u32 s63, s54, 2982016\nv_add_f32 v2, v2, v6\nM 76,172,216\ns_add_u32 m0, s50, 8192\nv_lshrrev_b32 v231, 1, v252\nv_lshrrev_b32 v230, 2, v252\n"
"s_lshl_b32 s62, s40, 9\ns_add_u32 s62, s62, 159232\nv_add_u32 v231, s62, v231\nM 60,168,196\nR 164,189,16384\nds_write2_b32 v231, v0, v2 offset1:1\n"
"M 60,172,212\nbuffer_load_dwordx4 v220, s[12:15], s63 offen lds\ns_lshl_b32 s62, s40, 9\ns_add_u32 s62, s62, 159232\nv_add_u32 v230, s62, v230\nM 44,168,192\n"
"R 166,189,20480\nds_read2st64_b32 v[232:233], v230 offset1:1\nM 44,172,208\nG 6\nM 32,176,224\nR 168,190,8192\n"
"M 32,180,240\nbuffer_load_dwordx4 v221, s[12:15], s63 offen offset:1024 lds\nM 48,176,228\nR 170,190,12288\ns_add_u32 s63, s54, 3211392\nM 48,180,244\n"
"s_add_u32 m0, s50, 16384\nM 64,176,232\nR 172,190,16384\nG 3\nv_add_f32 v232, v232, v233\nv_mov_b32 v233, v232\n"
"M 64,180,248\nbuffer_load_dwordx4 v220, s[12:15], s63 offen lds\nv_permlane32_swap_b32 v232, v233\nM 80,176,236\nR 174,190,20480\nv_mov_b32 v230, v232\n"
"v_mov_b32 v231, v233\nM 80,180,252\ns_waitcnt vmcnt(9)\ns_barrier\nv_permlane16_swap_b32 v230, v232\nM 84,160,236\n"
"R 176,191,8192\nv_permlane16_swap_b32 v231, v233\nv_mul_f32 v230, 0xba800000, v230\nM 84,164,252\nbuffer_load_dwordx4 v221, s[12:15], s63 offen offset:1024 lds\nv_mul_f32 v214, v230, v230\n"
"M 68,160,232\nR 178,191,12288\nv_mul_f32 v231, 0x3a800000, v231\nv_sub_f32 v231, v231, v214\nM 68,164,248\ns_add_u32 m0, s50, 24576\n"
"v_max_f32 v231, 0, v231\nv_add_f32 v231, 0x3727c5ac, v231\nv_rsq_f32 v234, v231\nM 52,160,228\nR 180,191,16384\nv_and_b32 v208, s77, v230\n"
"v_mul_f32 v216, v231, v234\nM 52,164,244\ns_add_u32 s63, s54, 3440768\nv_sub_f32 v230, v230, v208\nv_cvt_pk_bf16_f32 v209, v230, v216\nv_cvt_pk_bf16_f32 v208, v208, v208\n"
"M 36,160,224\nR 182,191,20480\nv_mul_f32 v214, 0xc0135761, v234\nv_mul_f32 v215, v234, v234\nM 36,164,240\ns_mov_b64 s[60:61], 0xffff\n"
"v_cndmask_b32 v208, 0, v208, s[60:61]\nG 4\nv_mul_f32 v215, v215, v214\nM 40,168,224\nR 160,188,24576\nv_mul_f32 v215, 0x3d372713, v215\n"
"v_mul_f32 v232, 0xba800000, v232\nM 40,172,240\nbuffer_load_dwordx4 v220, s[12:15], s63 offen lds\nv_mul_f32 v217, v232, v232\nM 56,168,228\nR 162,188,28672\n"
"v_mul_f32 v233, 0x3a800000, v233\nM 56,172,244\ns_mov_b64 s[60:61], 0xffff\nv_cndmask_b32 v209, 0, v209, s[60:61]\nv_sub_f32 v233, v233, v217\nM 72,168,232\n"
"R 164,188,32768\nv_max_f32 v233, 0, v233\nM 72,172,248\nbuffer_load_dwordx4 v221, s[12:15], s63 offen offset:1024 lds\nM 88,168,236\nR 166,188,36864\n"
"s_add_u32 m0, s50, 32768\nM 88,172,252\nG 4\nv_add_f32 v233, 0x3727c5ac, v233\nv_and_b32 v212, s77, v232\nM 92,176,236\n"
"R 168,189,24576\ns_add_u32 s63, s55, 0\nM 92,180,252\nbuffer_load_dwordx4 v222, s[12:15], s63 offen lds\nM 76,176,232\nR 170,189,28672\n"
"v_sub_f32 v232, v232, v212\nM 76,180,248\nv_rsq_f32 v235, v233\nM 60,176,228\nR 172,189,32768\nv_mul_f32 v219, v233, v235\n"
"M 60,180,244\nbuffer_load_dwordx4 v223, s[12:15], s63 offen offset:1024 lds\nM 44,176,224\nR 174,189,36864\nv_cvt_pk_bf16_f32 v213, v232, v219\nM 44,180,240\n"
"s_waitcnt vmcnt(10)\ns_barrier\nG 4\nM 96,160,128\nR 176,190,24576\nv_cvt_pk_bf16_f32 v212, v212, v212\n"
"M 96,164,144\ns_mov_b32 m0, s51\ns_mov_b64 s[60:61], 0xffff\nv_cndmask_b32 v212, 0, v212, s[60:61]\nv_mul_f32 v217, 0xc0135761, v235\nM 112,160,132\n"
"R 178,190,28672\ns_lshl2_add_u32 s60, s45, s40\nv_lshlrev_b32 v224, 3, v252\ns_mul_i32 s60, s60, 7168\ns_bfm_b64 exec, 14, 0\nbuffer_load_dword v224, v224, s[16:19], s60 offen\n"
"s_mov_b64 exec, -1\nM 112,164,148\ns_mov_b64 s[60:61], 0xffff\nv_cndmask_b32 v213, 0, v213, s[60:61]\nv_mul_f32 v218, v235, v235\nM 128,160,136\n"
"R 180,190,32768\nv_mul_f32 v218, v218, v217\nM 128,164,152\nds_write_addtid_b32 v64 offset:0\nv_mul_f32 v218, 0x3d372713, v218\nM 144,160,140\n"
"R 182,190,36864\nM 144,164,156\nds_write_addtid_b32 v65 offset:256\nG 6\nM 148,168,140\nR 160,191,24576\n"
"M 148,172,156\nds_write_addtid_b32 v66 offset:512\nM 132,168,136\nR 162,191,28672\nM 132,172,152\nds_write_addtid_b32 v67 offset:768\n"
"M 116,168,132\nR 164,191,32768\nM 116,172,148\nds_write_addtid_b32 v68 offset:1024\nM 100,168,128\nR 166,191,36864\n"
"ds_write_addtid_b32 v69 offset:1280\nM 100,172,144\nds_write_addtid_b32 v70 offset:1536\nds_write_addtid_b32 v71 offset:1792\nG 11\nM 104,176,128\n"
"R 168,188,40960\nds_write_addtid_b32 v72 offset:2048\nM 104,180,144\nds_write_addtid_b32 v73 offset:2304\nds_write_addtid_b32 v74 offset:2560\nM 120,176,132\n"
"R 170,188,45056\nds_write_addtid_b32 v75 offset:2816\nM 120,180,148\nds_write_addtid_b32 v76 offset:3072\nds_write_addtid_b32 v77 offset:3328\nM 136,176,136\n"
"R 172,188,49152\nds_write_addtid_b32 v78 offset:3584\nM 136,180,152\nds_write_addtid_b32 v79 offset:3840\nds_write_addtid_b32 v80 offset:4096\nM 152,176,140\n"
"R 174,188,53248\nds_write_addtid_b32 v81 offset:4352\nM 152,180,156\nds_write_addtid_b32 v82 offset:4608\nds_write_addtid_b32 v83 offset:4864\nG 15\n"
"M 156,160,140\nR 176,189,40960\nds_write_addtid_b32 v84 offset:5120\nM 156,164,156\nds_write_addtid_b32 v85 offset:5376\nds_write_addtid_b32 v86 offset:5632\n"
"M 140,160,136\nR 178,189,45056\nds_write_addtid_b32 v87 offset:5888\nM 140,164,152\nds_write_addtid_b32 v88 offset:6144\nds_write_addtid_b32 v89 offset:6400\n"
"M 124,160,132\nR 180,189,49152\nds_write_addtid_b32 v90 offset:6656\nM 124,164,148\nds_write_addtid_b32 v91 offset:6912\nds_write_addtid_b32 v92 offset:7168\n"
"M 108,160,128\nR 182,189,53248\nds_write_addtid_b32 v93 offset:7424\nM 108,164,144\nds_write_addtid_b32 v94 offset:7680\nds_write_addtid_b32 v95 offset:7936\n"
"G 15\nM 96,168,160\nR 160,190,40960\ns_add_u32 m0, s50, 40960\ns_add_u32 s63, s55, 458752\nM 96,172,176\n"
"buffer_load_dwordx4 v222, s[12:15], s63 offen lds\nM 112,168,164\nR 162,190,45056\ns_waitcnt vmcnt(8) lgkmcnt(2)\ns_barrier\nM 112,172,180\n"
"ds_read2st64_b32 v[64:65], v225 offset0:0 offset1:1\nds_read2st64_b32 v[66:67], v225 offset0:2 offset1:3\nM 128,168,168\nR 164,190,49152\nds_read2st64_b32 v[68:69], v225 offset0:4 offset1:5\nM 128,172,184\n"
"ds_read2st64_b32 v[70:71], v225 offset0:6 offset1:7\nds_read2st64_b32 v[72:73], v225 offset0:8 offset1:9\nM 144,168,172\nR 166,190,53248\nds_read2st64_b32 v[74:75], v225 offset0:10 offset1:11\nM 144,172,188\n"
"ds_read2st64_b32 v[76:77], v225 offset0:12 offset1:13\nds_read2st64_b32 v[78:79], v225 offset0:14 offset1:15\nM 148,176,172\nR 168,191,40960\nds_read2st64_b32 v[80:81], v225 offset0:16 offset1:17\nM 148,180,188\n"
"buffer_load_dwordx4 v223, s[12:15], s63 offen offset:1024 lds\nG 11\nv_add_f32 v32, v32, v64\nM 132,176,168\nR 170,191,45056\nv_add_f32 v33, v33, v65\n"
"G 11\nv_add_f32 v34, v34, v66\nM 132,180,184\ns_add_u32 m0, s50, 49152\nds_read2st64_b32 v[82:83], v225 offset0:18 offset1:19\nv_add_f32 v35, v35, v67\n"
"M 116,176,164\nR 172,191,49152\nG 11\nv_add_f32 v36, v36, v68\nv_add_f32 v37, v37, v69\nM 116,180,180\n"
"s_add_u32 s63, s55, 917504\nds_read2st64_b32 v[84:85], v225 offset0:20 offset1:21\nG 11\nv_add_f32 v38, v38, v70\nM 100,176,160\nR 174,191,53248\n"
"v_add_f32 v39, v39, v71\nG 11\nv_add_f32 v40, v40, v72\nM 100,180,176\nds_read2st64_b32 v[86:87], v225 offset0:22 offset1:23\nG 11\n"
"v_add_f32 v41, v41, v73\nM 104,160,160\nR 176,184,0\nG 11\nv_add_f32 v42, v42, v74\nv_add_f32 v43, v43, v75\n"
"M 104,164,176\nbuffer_load_dwordx4 v222, s[12:15], s63 offen lds\nG 10\nv_add_f32 v44, v44, v76\nM 120,160,164\nR 178,184,4096\n"
"v_add_f32 v45, v45, v77\nG 10\nv_add_f32 v46, v46, v78\nM 120,164,180\nds_read2st64_b32 v[88:89], v225 offset0:24 offset1:25\nv_add_f32 v47, v47, v79\n"
"G 9\nv_add_f32 v48, v48, v80\nM 136,160,168\nR 180,184,8192\nv_add_f32 v49, v49, v81\nM 136,164,184\n"
"ds_read2st64_b32 v[90:91], v225 offset0:26 offset1:27\nG 9\nv_add_f32 v50, v50, v82\nM 152,160,172\nR 182,184,12288\nv_add_f32 v51, v51, v83\n"
"M 152,164,188\nds_read2st64_b32 v[92:93], v225 offset0:28 offset1:29\nG 8\nv_add_f32 v52, v52, v84\nM 156,168,172\nR 160,185,0\n"
"v_add_f32 v53, v53, v85\nM 156,172,188\nbuffer_load_dwordx4 v223, s[12:15], s63 offen offset:1024 lds\nM 140,168,168\nR 162,185,4096\ns_mov_b32 s60, 157184\n"
"v_bfe_u32 v246, v252, 4, 4\nv_lshl_add_u32 v246, v246, 3, s60\nM 140,172,184\nds_read_b64 v[238:239], v246 offset:0\nG 10\nv_add_f32 v54, v54, v86\n"
"M 124,168,164\nR 164,185,8192\nds_read_b64 v[240:241], v246 offset:128\nM 124,172,180\nds_read_b64 v[242:243], v246 offset:256\nv_add_f32 v55, v55, v87\n"
"M 108,168,160\nR 166,185,12288\nds_read_b64 v[244:245], v246 offset:384\nM 108,172,176\nds_read_b64 v[246:247], v246 offset:0\nG 10\n"
"M 96,176,192\nR 168,186,0\nv_add_f32 v56, v56, v88\nM 96,180,208\ns_add_u32 m0, s50, 57344\nds_read2st64_b32 v[94:95], v225 offset0:30 offset1:31\n"
"v_add_f32 v57, v57, v89\nM 112,176,196\nR 170,186,4096\ns_lshl2_add_u32 s60, s45, s40\ns_waitcnt vmcnt(4)\nv_lshlrev_b32 v224, 3, v252\n"
"s_mul_i32 s60, s60, 7168\ns_bfm_b64 exec, 14, 14\nbuffer_load_dword v224, v224, s[16:19], s60 offen\ns_mov_b64 exec, -1\nG 1\ns_barrier\n"
"M 112,180,212\ns_add_u32 s63, s55, 1376256\nv_add_f32 v58, v58, v90\nv_add_f32 v59, v59, v91\nM 128,176,200\nR 172,186,8192\n"
"v_add_f32 v60, v60, v92\nM 128,180,216\nbuffer_load_dwordx4 v222, s[12:15], s63 offen lds\nM 144,176,204\nR 174,186,12288\nv_add_f32 v61, v61, v93\n"
"M 144,180,220\nv_add_f32 v62, v62, v94\nv_add_f32 v63, v63, v95\nM 148,160,204\nR 176,187,0\nv_mfma_f32_16x16x32_bf16 v[32:35], v[238:241], v[208:211], v[32:35]\n"
"M 148,164,220\nv_mfma_f32_16x16x32_bf16 v[36:39], v[240:243], v[208:211], v[36:39]\nM 132,160,200\nR 178,187,4096\nv_mfma_f32_16x16x32_bf16 v[40:43], v[242:245], v[208:211], v[40:43]\nM 132,164,216\n"
"v_mfma_f32_16x16x32_bf16 v[44:47], v[244:247], v[208:211], v[44:47]\nbuffer_load_dwordx4 v223, s[12:15], s63 offen offset:1024 lds\nM 116,160,196\nR 180,187,8192\nv_mfma_f32_16x16x32_bf16 v[48:51], v[244:247], v[210:213], v[48:51]\ns_add_u32 s63, s55, 1835008\n"
"M 116,164,212\nv_mfma_f32_16x16x32_bf16 v[52:55], v[238:241], v[210:213], v[52:55]\ns_add_u32 m0, s50, 65536\nM 100,160,192\nR 182,187,12288\nv_mfma_f32_16x16x32_bf16 v[56:59], v[240:243], v[210:213], v[56:59]\n"
"M 100,164,208\nv_mfma_f32_16x16x32_bf16 v[60:63], v[242:245], v[210:213], v[60:63]\nG 4\nM 104,168,192\nR 160,184,16384\nM 104,172,208\n"
"buffer_load_dwordx4 v222, s[12:15], s63 offen lds\nM 120,168,196\nR 162,184,20480\nM 120,172,212\nM 136,168,200\nR 164,184,24576\n"
"M 136,172,216\nM 152,168,204\nR 166,184,28672\nv_mul_f32 v230, v32, v32\nv_mul_f32 v231, v33, v33\nM 152,172,220\n"
"s_add_u32 s84, s73, 23068672\ns_add_u32 s85, s73, 23183360\nG 4\nv_mul_f32 v232, v34, v34\nM 156,176,204\nR 168,185,16384\n"
"v_mul_f32 v233, v35, v35\nv_fma_f32 v230, v230, v215, v214\nM 156,180,220\nbuffer_load_dwordx4 v223, s[12:15], s63 offen offset:1024 lds\nv_fma_f32 v231, v231, v215, v214\nM 140,176,200\n"
"R 170,185,20480\nv_fma_f32 v232, v232, v215, v214\nv_fma_f32 v233, v233, v215, v214\nM 140,180,216\nv_mul_f32 v230, v32, v230\nv_mul_f32 v231, v33, v231\n"
"v_mul_f32 v232, v34, v232\nM 124,176,196\nR 172,185,24576\nv_mul_f32 v233, v35, v233\nv_exp_f32 v230, v230\nM 124,180,212\n"
"v_exp_f32 v231, v231\nv_exp_f32 v232, v232\nM 108,176,192\nR 174,185,28672\ns_mov_b32 s60, 157696\nv_bfe_u32 v246, v252, 4, 4\n"
"v_lshl_add_u32 v246, v246, 3, s60\nv_exp_f32 v233, v233\nM 108,180,208\nds_read_b64 v[238:239], v246 offset:0\nG 5\nv_fma_f32 v230, v230, v216, v216\n"
"M 96,160,224\nR 176,186,16384\nds_read_b64 v[240:241], v246 offset:128\nv_fma_f32 v231, v231, v216, v216\nM 96,164,240\nds_read_b64 v[242:243], v246 offset:256\n"
"s_add_u32 m0, s50, 73728\nv_fma_f32 v232, v232, v216, v216\nM 112,160,228\nR 178,186,20480\nds_read_b64 v[244:245], v246 offset:384\nv_fma_f32 v233, v233, v216, v216\n"
"M 112,164,244\nds_read_b64 v[246:247], v246 offset:0\ns_add_u32 s63, s55, 2293760\nv_rcp_f32 v230, v230\nM 128,160,232\nR 180,186,24576\n"
"v_mul_f32 v230, v32, v230\nv_rcp_f32 v231, v231\nM 128,164,248\nbuffer_load_dwordx4 v222, s[12:15], s63 offen lds\nv_rcp_f32 v232, v232\nM 144,160,236\n"
"R 182,186,28672\nv_mul_f32 v231, v33, v231\nv_rcp_f32 v233, v233\nM 144,164,252\nG 9\nv_mul_f32 v232, v34, v232\n"
"v_mul_f32 v233, v35, v233\nv_cvt_pk_bf16_f32 v32, v230, v231\nM 148,168,236\nR 160,187,16384\nv_cvt_pk_bf16_f32 v33, v232, v233\nv_mul_f32 v234, v36, v36\n"
"M 148,172,252\nv_mul_f32 v235, v37, v37\nv_mul_f32 v236, v38, v38\nv_mul_f32 v237, v39, v39\nM 132,168,232\nR 162,187,20480\n"
"v_fma_f32 v234, v234, v215, v214\nv_fma_f32 v235, v235, v215, v214\nM 132,172,248\nbuffer_load_dwordx4 v223, s[12:15], s63 offen offset:1024 lds\nv_fma_f32 v236, v236, v215, v214\nM 116,168,228\n"
"R 164,187,24576\nG 9\nv_mfma_f32_16x16x32_bf16 v[96:99], v[238:241], v[208:211], v[96:99]\ns_add_u32 s63, s55, 2752512\nv_fma_f32 v237, v237, v215, v214\nM 116,172,244\n"
"s_add_u32 m0, s50, 81920\nv_mul_f32 v234, v36, v234\nv_mul_f32 v235, v37, v235\nv_mul_f32 v236, v38, v236\nM 100,168,224\nR 166,187,28672\n"
"G 9\nv_mfma_f32_16x16x32_bf16 v[100:103], v[240:243], v[208:211], v[100:103]\nv_mul_f32 v237, v39, v237\nv_exp_f32 v234, v234\nM 100,172,240\nG 4\n"
"v_exp_f32 v235, v235\nv_exp_f32 v236, v236\nM 104,176,224\nR 168,184,32768\nv_mfma_f32_16x16x32_bf16 v[104:107], v[242:245], v[208:211], v[104:107]\nv_fma_f32 v234, v234, v216, v216\n"
"v_exp_f32 v237, v237\nM 104,180,240\nbuffer_load_dwordx4 v222, s[12:15], s63 offen lds\nv_fma_f32 v235, v235, v216, v216\nM 120,176,228\nR 170,184,36864\n"
"v_mfma_f32_16x16x32_bf16 v[108:111], v[244:247], v[208:211], v[108:111]\nv_fma_f32 v236, v236, v216, v216\nv_fma_f32 v237, v237, v216, v216\nM 120,180,244\nbuffer_load_dwordx4 v223, s[12:15], s63 offen offset:1024 lds\nv_rcp_f32 v234, v234\n"
"M 136,176,232\nR 172,186,32768\nv_mfma_f32_16x16x32_bf16 v[112:115], v[244:247], v[210:213], v[112:115]\ns_add_u32 s63, s55, 3211264\nv_rcp_f32 v235, v235\nM 136,180,248\n"
"s_add_u32 m0, s50, 90112\nv_rcp_f32 v236, v236\nv_rcp_f32 v237, v237\nM 152,176,236\nR 174,186,36864\nv_mfma_f32_16x16x32_bf16 v[116:119], v[238:241], v[210:213], v[116:119]\n"
"v_mul_f32 v234, v36, v234\nv_mul_f32 v235, v37, v235\nM 152,180,252\nG 4\nv_mul_f32 v236, v38, v236\nv_mul_f32 v237, v39, v237\n"
"v_cvt_pk_bf16_f32 v34, v234, v235\nM 156,160,236\nR 176,185,32768\nv_mfma_f32_16x16x32_bf16 v[120:123], v[240:243], v[210:213], v[120:123]\nv_cvt_pk_bf16_f32 v35, v236, v237\nv_mul_f32 v230, v40, v40\n"
"M 156,164,252\nbuffer_load_dwordx4 v222, s[12:15], s63 offen lds\nv_mul_f32 v231, v41, v41\nM 140,160,232\nR 178,185,36864\nv_mfma_f32_16x16x32_bf16 v[124:127], v[242:245], v[210:213], v[124:127]\n"
"v_mul_f32 v232, v42, v42\nv_mul_f32 v233, v43, v43\nM 140,164,248\nbuffer_load_dwordx4 v223, s[12:15], s63 offen offset:1024 lds\nv_fma_f32 v230, v230, v215, v214\nM 124,160,228\n"
"R 180,187,32768\ns_add_u32 m0, s50, 98304\ns_add_u32 s63, s54, 256\nv_fma_f32 v231, v231, v215, v214\nM 124,164,244\nbuffer_load_dwordx4 v220, s[12:15], s63 offen lds\n"
"v_fma_f32 v232, v232, v215, v214\nM 108,160,224\nR 182,187,36864\nv_fma_f32 v233, v233, v215, v214\nv_mul_f32 v230, v40, v230\nM 108,164,240\n"
"s_waitcnt vmcnt(9)\ns_barrier\nG 4\nv_mul_f32 v231, v41, v231\nv_mfma_f32_16x16x32_bf16 v[0:3], v[168:171], a[0:3], 0\nR 160,184,40960\n"
"v_mul_f32 v232, v42, v232\nv_mul_f32 v233, v43, v233\nM 0,172,16\ns_mov_b32 m0, s51\nv_exp_f32 v230, v230\nv_exp_f32 v231, v231\n"
"v_mfma_f32_16x16x32_bf16 v[16:19], v[168:171], a[4:7], 0\nR 162,184,45056\nv_fma_f32 v230, v230, v216, v216\nv_exp_f32 v232, v232\nM 16,172,20\nbuffer_store_dwordx4 v[32:35], v252, s[20:23], s84 offen offset:0 sc1\n"
"v_exp_f32 v233, v233\nv_mfma_f32_16x16x32_bf16 v[8:11], v[168:171], a[8:11], 0\nR 164,186,40960\nv_fma_f32 v231, v231, v216, v216\nv_fma_f32 v232, v232, v216, v216\nM 8,172,24\n"
"v_rcp_f32 v230, v230\nv_fma_f32 v233, v233, v216, v216\nv_mfma_f32_16x16x32_bf16 v[24:27], v[168:171], a[12:15], 0\nR 166,186,45056\nv_mul_f32 v230, v40, v230\nv_rcp_f32 v231, v231\n"
"M 24,172,28\nG 4\nv_rcp_f32 v232, v232\nv_rcp_f32 v233, v233\nv_mfma_f32_16x16x32_bf16 v[28:31], v[176:179], a[12:15], 0\nR 168,185,40960\n"
"v_mul_f32 v231, v41, v231\nv_mul_f32 v232, v42, v232\nM 28,180,28\nv_mul_f32 v233, v43, v233\nv_cvt_pk_bf16_f32 v40, v230, v231\nv_cvt_pk_bf16_f32 v41, v232, v233\n"
"v_mfma_f32_16x16x32_bf16 v[12:15], v[176:179], a[8:11], 0\nR 170,185,45056\nv_mul_f32 v234, v44, v44\nv_mul_f32 v235, v45, v45\nM 12,180,24\nds_write_addtid_b32 v128 offset:0\n"
"v_mul_f32 v236, v46, v46\nv_mul_f32 v237, v47, v47\nv_fma_f32 v234, v234, v215, v214\nv_mfma_f32_16x16x32_bf16 v[20:23], v[176:179], a[4:7], 0\nR 172,187,40960\nds_write_addtid_b32 v129 offset:256\n"
"v_fma_f32 v235, v235, v215, v214\nv_fma_f32 v236, v236, v215, v214\nM 20,180,20\nds_write_addtid_b32 v130 offset:512\nv_fma_f32 v237, v237, v215, v214\nv_mul_f32 v234, v44, v234\n"
"v_mul_f32 v235, v45, v235\nv_mfma_f32_16x16x32_bf16 v[4:7], v[176:179], a[0:3], 0\nR 174,187,45056\nds_write_addtid_b32 v131 offset:768\nv_mul_f32 v236, v46, v236\nv_mul_f32 v237, v47, v237\n"
"M 4,180,16\nds_write_addtid_b32 v132 offset:1024\nG 9\nv_exp_f32 v234, v234\nv_exp_f32 v235, v235\nM 0,160,32\n"
"R 176,184,49152\nds_write_addtid_b32 v133 offset:1280\nv_fma_f32 v234, v234, v216, v216\nv_exp_f32 v236, v236\nM 0,164,48\nds_write_addtid_b32 v134 offset:1536\n"
"v_exp_f32 v237, v237\nv_fma_f32 v235, v235, v216, v216\nM 16,160,36\nR 178,184,53248\nds_write_addtid_b32 v135 offset:1792\nv_fma_f32 v236, v236, v216, v216\n"
"v_fma_f32 v237, v237, v216, v216\nM 16,164,52\nds_write_addtid_b32 v136 offset:2048\nv_rcp_f32 v234, v234\nv_rcp_f32 v235, v235\nM 8,160,40\n"
"R 180,186,49152\nds_write_addtid_b32 v137 offset:2304\nv_mul_f32 v234, v44, v234\nv_rcp_f32 v236, v236\nM 8,164,56\nds_write_addtid_b32 v138 offset:2560\n"
"v_rcp_f32 v237, v237\nv_mul_f32 v235, v45, v235\nM 24,160,44\nR 182,186,53248\nds_write_addtid_b32 v139 offset:2816\nv_mul_f32 v236, v46, v236\n"
"v_mul_f32 v237, v47, v237\nM 24,164,60\nds_write_addtid_b32 v140 offset:3072\nG 14\nv_cvt_pk_bf16_f32 v42, v234, v235\nv_cvt_pk_bf16_f32 v43, v236, v237\n"
"v_mul_f32 v230, v48, v48\nM 28,168,44\nR 160,185,49152\nds_write_addtid_b32 v141 offset:3328\nv_mul_f32 v231, v49, v49\nv_mul_f32 v232, v50, v50\n"
"M 28,172,60\nbuffer_store_dwordx4 v[40:43], v252, s[20:23], s84 offen offset:1024 sc1\nds_write_addtid_b32 v142 offset:3584\nv_mul_f32 v233, v51, v51\nM 12,168,40\nR 162,185,53248\n"
"ds_write_addtid_b32 v143 offset:3840\nv_fma_f32 v230, v230, v218, v217\nv_fma_f32 v231, v231, v218, v217\nM 12,172,56\nds_write_addtid_b32 v144 offset:4096\nv_fma_f32 v232, v232, v218, v217\n"
"v_fma_f32 v233, v233, v218, v217\nv_mul_f32 v230, v48, v230\nM 20,168,36\nR 164,187,49152\nds_write_addtid_b32 v145 offset:4352\nv_mul_f32 v231, v49, v231\n"
"v_mul_f32 v232, v50, v232\nM 20,172,52\nds_write_addtid_b32 v146 offset:4608\nv_exp_f32 v230, v230\nv_mul_f32 v233, v51, v233\nM 4,168,32\n"
"R 166,187,53248\nds_write_addtid_b32 v147 offset:4864\nv_fma_f32 v230, v230, v219, v219\nv_exp_f32 v231, v231\nM 4,172,48\nds_write_addtid_b32 v148 offset:5120\n"
"ds_write_addtid_b32 v149 offset:5376\nds_write_addtid_b32 v150 offset:5632\nds_write_addtid_b32 v151 offset:5888\nds_write_addtid_b32 v152 offset:6144\nds_write_addtid_b32 v153 offset:6400\nds_write_addtid_b32 v154 offset:6656\n"
"ds_write_addtid_b32 v155 offset:6912\nds_write_addtid_b32 v156 offset:7168\nds_write_addtid_b32 v157 offset:7424\nds_write_addtid_b32 v158 offset:7680\nds_write_addtid_b32 v159 offset:7936\nG 15\n"
"v_exp_f32 v232, v232\nv_exp_f32 v233, v233\nM 0,176,64\nR 168,184,57344\ns_add_u32 m0, s50, 98304\nv_fma_f32 v231, v231, v219, v219\n"
"M 0,180,80\nbuffer_load_dwordx4 v221, s[12:15], s63 offen offset:1024 lds\nv_fma_f32 v232, v232, v219, v219\nM 16,176,68\nR 170,184,61440\ns_waitcnt vmcnt(6) lgkmcnt(2)\n"
"s_barrier\ns_add_u32 m0, s50, 106496\ns_add_u32 s63, s54, 229632\nv_fma_f32 v233, v233, v219, v219\nM 16,180,84\nbuffer_load_dwordx4 v220, s[12:15], s63 offen lds\n"
"ds_read2st64_b32 v[128:129], v225 offset0:0 offset1:1\nv_rcp_f32 v230, v230\nM 8,176,72\nR 172,186,57344\nds_read2st64_b32 v[130:131], v225 offset0:2 offset1:3\nv_mul_f32 v230, v48, v230\n"
"v_rcp_f32 v231, v231\nM 8,180,88\nds_read2st64_b32 v[132:133], v225 offset0:4 offset1:5\nv_rcp_f32 v232, v232\nv_rcp_f32 v233, v233\nM 24,176,76\n"
"R 174,186,61440\nds_read2st64_b32 v[134:135], v225 offset0:6 offset1:7\nv_mul_f32 v231, v49, v231\nv_mul_f32 v232, v50, v232\nM 24,180,92\nds_read2st64_b32 v[136:137], v225 offset0:8 offset1:9\n"
"v_mul_f32 v233, v51, v233\nv_cvt_pk_bf16_f32 v48, v230, v231\nv_cvt_pk_bf16_f32 v49, v232, v233\nM 28,160,76\nR 176,185,57344\nds_read2st64_b32 v[138:139], v225 offset0:10 offset1:11\n"
"v_mul_f32 v234, v52, v52\nv_mul_f32 v235, v53, v53\nM 28,164,92\nbuffer_load_dwordx4 v221, s[12:15], s63 offen offset:1024 lds\nG 8\nv_add_f32 v96, v96, v128\n"
"v_mul_f32 v236, v54, v54\nM 12,160,72\nR 178,185,61440\ns_add_u32 s63, s54, 459008\nv_add_f32 v97, v97, v129\nv_mul_f32 v237, v55, v55\n"
"M 12,164,88\ns_add_u32 m0, s50, 114688\nv_fma_f32 v234, v234, v218, v217\nv_fma_f32 v235, v235, v218, v217\nG 7\nv_add_f32 v98, v98, v130\n"
"v_fma_f32 v236, v236, v218, v217\nM 20,160,68\nR 180,187,57344\nv_fma_f32 v237, v237, v218, v217\nv_add_f32 v99, v99, v131\nv_mul_f32 v234, v52, v234\n"
"M 20,164,84\nbuffer_load_dwordx4 v220, s[12:15], s63 offen lds\nG 7\nv_add_f32 v100, v100, v132\nv_mul_f32 v235, v53, v235\nM 4,160,64\n"
"R 182,187,61440\nv_mul_f32 v236, v54, v236\nv_add_f32 v101, v101, v133\nv_mul_f32 v237, v55, v237\nM 4,164,80\nG 7\n"
"v_exp_f32 v234, v234\nG 6\nv_add_f32 v102, v102, v134\nv_exp_f32 v235, v235\nM 0,168,96\nR 160,188,0\n"
"v_fma_f32 v234, v234, v219, v219\nv_add_f32 v103, v103, v135\nv_exp_f32 v236, v236\nM 0,172,112\nbuffer_load_dwordx4 v221, s[12:15], s63 offen offset:1024 lds\nG 6\n"
"v_add_f32 v104, v104, v136\nv_exp_f32 v237, v237\nM 16,168,100\nR 162,188,4096\nv_fma_f32 v235, v235, v219, v219\nv_add_f32 v105, v105, v137\n"
"v_fma_f32 v236, v236, v219, v219\nM 16,172,116\ns_add_u32 m0, s50, 0\nv_rcp_f32 v234, v234\nG 5\nv_add_f32 v106, v106, v138\n"
"v_fma_f32 v237, v237, v219, v219\nM 8,168,104\nR 164,190,0\nv_mul_f32 v234, v52, v234\nv_add_f32 v107, v107, v139\nv_rcp_f32 v235, v235\n"
"M 8,172,120\ns_add_u32 s63, s54, 688384\nds_read2st64_b32 v[140:141], v225 offset0:12 offset1:13\nv_rcp_f32 v236, v236\nv_rcp_f32 v237, v237\nM 24,168,108\n"
"R 166,190,4096\nds_read2st64_b32 v[142:143], v225 offset0:14 offset1:15\nv_mul_f32 v235, v53, v235\nv_mul_f32 v236, v54, v236\nM 24,172,124\nds_read2st64_b32 v[144:145], v225 offset0:16 offset1:17\n"
"G 7\nv_mul_f32 v237, v55, v237\nv_cvt_pk_bf16_f32 v50, v234, v235\nv_cvt_pk_bf16_f32 v51, v236, v237\nM 28,176,108\nR 168,189,0\n"
"ds_read2st64_b32 v[146:147], v225 offset0:18 offset1:19\nv_mul_f32 v230, v56, v56\nv_mul_f32 v231, v57, v57\nM 28,180,124\nbuffer_load_dwordx4 v220, s[12:15], s63 offen lds\nds_read2st64_b32 v[148:149], v225 offset0:20 offset1:21\n"
"v_mul_f32 v232, v58, v58\nM 12,176,104\nR 170,189,4096\nds_read2st64_b32 v[150:151], v225 offset0:22 offset1:23\nv_mul_f32 v233, v59, v59\nv_fma_f32 v230, v230, v218, v217\n"
"M 12,180,120\nds_read2st64_b32 v[152:153], v225 offset0:24 offset1:25\nv_fma_f32 v231, v231, v218, v217\nv_fma_f32 v232, v232, v218, v217\nv_fma_f32 v233, v233, v218, v217\nM 20,176,100\n"
"R 172,191,0\nv_mul_f32 v230, v56, v230\nG 10\nv_add_f32 v108, v108, v140\nv_mul_f32 v231, v57, v231\nM 20,180,116\n"
"buffer_load_dwordx4 v221, s[12:15], s63 offen offset:1024 lds\nv_add_f32 v109, v109, v141\nv_mul_f32 v232, v58, v232\nM 4,176,96\nR 174,191,4096\ns_add_u32 m0, s50, 8192\n"
"G 9\nv_add_f32 v110, v110, v142\nv_mul_f32 v233, v59, v233\nM 4,180,112\nv_exp_f32 v230, v230\nv_add_f32 v111, v111, v143\n"
"v_exp_f32 v231, v231\nM 0,160,128\nR 176,188,8192\ns_add_u32 s63, s54, 917760\nG 9\nv_add_f32 v112, v112, v144\n"
"v_exp_f32 v232, v232\nM 0,164,144\nbuffer_load_dwordx4 v220, s[12:15], s63 offen lds\nv_add_f32 v113, v113, v145\nv_exp_f32 v233, v233\nM 16,160,132\n"
"R 178,188,12288\nv_fma_f32 v230, v230, v219, v219\nG 8\nv_add_f32 v114, v114, v146\nv_fma_f32 v231, v231, v219, v219\nM 16,164,148\n"
"ds_read2st64_b32 v[154:155], v225 offset0:26 offset1:27\nv_rcp_f32 v230, v230\nv_fma_f32 v232, v232, v219, v219\nM 8,160,136\nR 180,190,8192\nLq582:\n"
"s_lshr_b32 s60, s45, 3\ns_lshl2_add_u32 s60, s60, s40\ns_lshl_b32 s60, s60, 8\nv_lshl_add_u32 v224, v252, 3, s60\nv_add_u32 v224, Lq582+4096-LqPC, v224\ns_mov_b64 exec, 3\n"
"global_load_dword v224, v224, s[98:99]\ns_mov_b64 exec, -1\nv_add_f32 v115, v115, v147\nv_fma_f32 v233, v233, v219, v219\nM 8,164,152\nds_read2st64_b32 v[156:157], v225 offset0:28 offset1:29\n"
"v_rcp_f32 v231, v231\nv_rcp_f32 v232, v232\nM 24,160,140\nR 182,190,12288\nv_mul_f32 v230, v56, v230\nG 11\n"
"v_add_f32 v116, v116, v148\nv_rcp_f32 v233, v233\nM 24,164,156\nds_read2st64_b32 v[158:159], v225 offset0:30 offset1:31\nG 7\nv_mul_f32 v231, v57, v231\n"
"v_mul_f32 v232, v58, v232\nv_add_f32 v117, v117, v149\nv_add_f32 v118, v118, v150\nv_add_f32 v119, v119, v151\nv_add_f32 v120, v120, v152\nv_add_f32 v121, v121, v153\n"
"G 4\nv_add_f32 v122, v122, v154\nv_add_f32 v123, v123, v155\nG 2\nv_add_f32 v124, v124, v156\nv_add_f32 v125, v125, v157\n"
"G 0\nv_add_f32 v126, v126, v158\nv_add_f32 v127, v127, v159\nv_mul_f32 v233, v59, v233\nM 28,168,140\nR 160,189,8192\n"
"v_cvt_pk_bf16_f32 v56, v230, v231\nv_cvt_pk_bf16_f32 v57, v232, v233\nM 28,172,156\nbuffer_load_dwordx4 v221, s[12:15], s63 offen offset:1024 lds\nv_mul_f32 v234, v60, v60\nM 12,168,136\n"
"R 162,189,12288\ns_add_u32 m0, s50, 16384\ns_add_u32 s63, s54, 1147136\nv_mul_f32 v235, v61, v61\nM 12,172,152\nbuffer_load_dwordx4 v220, s[12:15], s63 offen lds\n"
"v_mul_f32 v236, v62, v62\nM 20,168,132\nR 164,191,8192\nv_mul_f32 v237, v63, v63\nv_fma_f32 v234, v234, v218, v217\nM 20,172,148\n"
"v_fma_f32 v235, v235, v218, v217\nv_fma_f32 v236, v236, v218, v217\nv_fma_f32 v237, v237, v218, v217\nM 4,168,128\nR 166,191,12288\nv_mul_f32 v234, v60, v234\n"
"v_mul_f32 v235, v61, v235\nM 4,172,144\nv_exp_f32 v234, v234\nv_mul_f32 v236, v62, v236\nM 0,176,160\nR 168,188,16384\n"
"v_mul_f32 v237, v63, v237\nv_exp_f32 v235, v235\nM 0,180,176\nbuffer_load_dwordx4 v221, s[12:15], s63 offen offset:1024 lds\nv_exp_f32 v236, v236\nM 16,176,164\n"
"R 170,188,20480\ns_add_u32 m0, s50, 24576\ns_add_u32 s63, s54, 1376512\nv_exp_f32 v237, v237\nM 16,180,180\nbuffer_load_dwordx4 v220, s[12:15], s63 offen lds\n"
"v_fma_f32 v234, v234, v219, v219\nM 8,176,168\nR 172,190,16384\nv_fma_f32 v235, v235, v219, v219\nv_fma_f32 v236, v236, v219, v219\nM 8,180,184\n"
"buffer_store_dwordx4 v[48:51], v252, s[20:23], s85 offen offset:0 sc1\nv_fma_f32 v237, v237, v219, v219\nM 24,176,172\nR 174,190,20480\nv_rcp_f32 v234, v234\nM 24,180,188\n"
"v_rcp_f32 v235, v235\nv_rcp_f32 v236, v236\nv_rcp_f32 v237, v237\nv_mul_f32 v234, v60, v234\nv_mul_f32 v235, v61, v235\nv_mul_f32 v236, v62, v236\n"
"v_mul_f32 v237, v63, v237\nv_cvt_pk_bf16_f32 v58, v234, v235\nv_cvt_pk_bf16_f32 v59, v236, v237\nbuffer_store_dwordx4 v[56:59], v252, s[20:23], s85 offen offset:1024 sc1\nG 4\nM 28,160,172\n"
"R 176,189,16384\ns_waitcnt vmcnt(12)\ns_barrier\nv_mul_f32 v230, v96, v96\nM 28,164,188\nbuffer_load_dwordx4 v221, s[12:15], s63 offen offset:1024 lds\n"
"v_mul_f32 v231, v97, v97\nM 12,160,168\nR 178,189,20480\ns_add_u32 m0, s50, 32768\ns_add_u32 s63, s54, 1605888\nv_mul_f32 v232, v98, v98\n"
"M 12,164,184\nbuffer_load_dwordx4 v220, s[12:15], s63 offen lds\nv_mul_f32 v233, v99, v99\nM 20,160,164\nR 180,191,16384\nv_fma_f32 v230, v230, v215, v214\n"
"v_fma_f32 v231, v231, v215, v214\nM 20,164,180\ns_add_u32 s84, s73, 23070720\ns_add_u32 s85, s73, 23185408\nv_fma_f32 v232, v232, v215, v214\nv_fma_f32 v233, v233, v215, v214\n"
"M 4,160,160\nR 182,191,20480\ns_mov_b32 s60, 158720\nv_bfe_u32 v242, v252, 4, 4\nv_lshl_add_u32 v242, v242, 3, s60\nv_mul_f32 v230, v96, v230\n"
"M 4,164,176\nds_read_b64 v[238:239], v242 offset:0\nG 5\nv_mul_f32 v231, v97, v231\nM 0,168,192\nR 160,188,24576\n"
"ds_read_b64 v[240:241], v242 offset:128\nv_mul_f32 v232, v98, v232\nM 0,172,208\nds_read_b64 v[242:243], v242 offset:0\nv_mul_f32 v233, v99, v233\nv_exp_f32 v230, v230\n"
"M 16,168,196\nR 162,188,28672\nv_fma_f32 v230, v230, v216, v216\nv_exp_f32 v231, v231\nM 16,172,212\nv_exp_f32 v232, v232\n"
"v_exp_f32 v233, v233\nM 8,168,200\nR 164,190,24576\nv_fma_f32 v231, v231, v216, v216\nv_fma_f32 v232, v232, v216, v216\nM 8,172,216\n"
"v_rcp_f32 v230, v230\nv_fma_f32 v233, v233, v216, v216\nM 24,168,204\nR 166,190,28672\nv_mul_f32 v230, v96, v230\nv_rcp_f32 v231, v231\n"
"M 24,172,220\nG 7\nv_rcp_f32 v232, v232\nv_rcp_f32 v233, v233\nM 28,176,204\nR 168,189,24576\n"
"v_mul_f32 v231, v97, v231\nv_mul_f32 v232, v98, v232\nM 28,180,220\nv_mul_f32 v233, v99, v233\nv_cvt_pk_bf16_f32 v96, v230, v231\nv_cvt_pk_bf16_f32 v97, v232, v233\n"
"M 12,176,200\nR 170,189,28672\nv_mul_f32 v234, v100, v100\nv_mul_f32 v235, v101, v101\nM 12,180,216\nv_mul_f32 v236, v102, v102\n"
"v_mul_f32 v237, v103, v103\nv_fma_f32 v234, v234, v215, v214\nM 20,176,196\nR 172,191,24576\nG 7\nv_mfma_f32_16x16x32_bf16 v[0:3], v[238:241], v[208:211], v[0:3]\n"
"v_fma_f32 v235, v235, v215, v214\nv_fma_f32 v236, v236, v215, v214\nM 20,180,212\nv_fma_f32 v237, v237, v215, v214\nv_mul_f32 v234, v100, v234\nv_mul_f32 v235, v101, v235\n"
"M 4,176,192\nR 174,191,28672\nG 7\nv_mfma_f32_16x16x32_bf16 v[4:7], v[240:243], v[208:211], v[4:7]\nv_mul_f32 v236, v102, v236\nv_mul_f32 v237, v103, v237\n"
"M 4,180,208\nG 4\nv_exp_f32 v234, v234\nv_exp_f32 v235, v235\nM 0,160,224\nR 176,188,32768\n"
"v_mfma_f32_16x16x32_bf16 v[16:19], v[240:243], v[210:213], v[16:19]\nv_fma_f32 v234, v234, v216, v216\nv_exp_f32 v236, v236\nM 0,164,240\nv_exp_f32 v237, v237\nv_fma_f32 v235, v235, v216, v216\n"
"M 16,160,228\nR 178,188,36864\nv_mfma_f32_16x16x32_bf16 v[20:23], v[238:241], v[210:213], v[20:23]\nv_fma_f32 v236, v236, v216, v216\nv_fma_f32 v237, v237, v216, v216\nM 16,164,244\n"
"v_rcp_f32 v234, v234\nv_rcp_f32 v235, v235\nM 8,160,232\nR 180,188,40960\nv_mul_f32 v234, v100, v234\nv_rcp_f32 v236, v236\n"
"M 8,164,248\nv_rcp_f32 v237, v237\nv_mul_f32 v235, v101, v235\nM 24,160,236\nR 182,188,45056\nv_mul_f32 v236, v102, v236\n"
"v_mul_f32 v237, v103, v237\nM 24,164,252\nG 4\nv_cvt_pk_bf16_f32 v98, v234, v235\nv_cvt_pk_bf16_f32 v99, v236, v237\nv_mul_f32 v230, v104, v104\n"
"M 28,168,236\nR 160,189,32768\nv_mul_f32 v231, v105, v105\nv_mul_f32 v232, v106, v106\nM 28,172,252\nv_mul_f32 v233, v107, v107\n"
"v_fma_f32 v230, v230, v215, v214\nv_fma_f32 v231, v231, v215, v214\nM 12,168,232\nR 162,189,36864\nv_fma_f32 v232, v232, v215, v214\nv_fma_f32 v233, v233, v215, v214\n"
"M 12,172,248\nv_mul_f32 v230, v104, v230\nv_mul_f32 v231, v105, v231\nv_mul_f32 v232, v106, v232\nM 20,168,228\nR 164,189,40960\n"
"v_mul_f32 v233, v107, v233\nv_exp_f32 v230, v230\nM 20,172,244\nv_exp_f32 v231, v231\nv_exp_f32 v232, v232\nM 4,168,224\n"
"R 166,189,45056\nv_fma_f32 v230, v230, v216, v216\nv_exp_f32 v233, v233\nM 4,172,240\ns_waitcnt vmcnt(12)\ns_barrier\n"
"G 4\nv_fma_f32 v231, v231, v216, v216\nv_mfma_f32_16x16x32_bf16 v[32:35], v[176:179], a[0:3], 0\nR 168,190,32768\nv_fma_f32 v232, v232, v216, v216\nv_fma_f32 v233, v233, v216, v216\n"
"M 32,180,16\ns_mov_b32 m0, s51\nv_rcp_f32 v230, v230\nv_rcp_f32 v231, v231\nv_mfma_f32_16x16x32_bf16 v[48:51], v[176:179], a[4:7], 0\nR 170,190,36864\n"
"v_mul_f32 v230, v104, v230\nv_rcp_f32 v232, v232\nM 48,180,20\nv_rcp_f32 v233, v233\nv_mul_f32 v231, v105, v231\nv_mfma_f32_16x16x32_bf16 v[64:67], v[176:179], a[8:11], 0\n"
"R 172,190,40960\nv_mov_b32 v226, s46\nv_mul_f32 v232, v106, v232\nM 64,180,24\nds_write_addtid_b32 v8 offset:0\nv_mov_b32 v227, s47\n"
"v_mul_f32 v233, v107, v233\nv_cvt_pk_bf16_f32 v104, v230, v231\nv_mfma_f32_16x16x32_bf16 v[80:83], v[176:179], a[12:15], 0\nR 174,190,45056\ns_add_u32 s61, s81, 385024\ns_lshl_b32 s62, s44, 5\n"
"s_add_u32 s61, s61, s62\ns_waitcnt vmcnt(0)\ns_mov_b64 exec, 1\nbuffer_atomic_swap_x2 v[226:227], off, s[20:23], s61 sc1\ns_mov_b64 exec, -1\nv_cvt_pk_bf16_f32 v105, v232, v233\n"
"M 80,180,28\nds_write_addtid_b32 v9 offset:256\nG 6\nv_mul_f32 v234, v108, v108\nv_mul_f32 v235, v109, v109\nv_mul_f32 v236, v110, v110\n"
"v_mfma_f32_16x16x32_bf16 v[84:87], v[160:163], a[12:15], 0\nR 176,191,32768\nds_write_addtid_b32 v10 offset:512\nv_mul_f32 v237, v111, v111\nv_fma_f32 v234, v234, v215, v214\nM 84,164,28\n"
"ds_write_addtid_b32 v11 offset:768\nv_fma_f32 v235, v235, v215, v214\nv_fma_f32 v236, v236, v215, v214\nv_fma_f32 v237, v237, v215, v214\nv_mfma_f32_16x16x32_bf16 v[68:71], v[160:163], a[8:11], 0\nR 178,191,36864\n"
"ds_write_addtid_b32 v12 offset:1024\nv_mul_f32 v234, v108, v234\nv_mul_f32 v235, v109, v235\nM 68,164,24\nbuffer_store_dwordx4 v[96:99], v252, s[20:23], s84 offen offset:0 sc1\nds_write_addtid_b32 v13 offset:1280\n"
"v_mul_f32 v236, v110, v236\nv_mfma_f32_16x16x32_bf16 v[52:55], v[160:163], a[4:7], 0\nR 180,191,40960\nds_write_addtid_b32 v14 offset:1536\nv_mul_f32 v237, v111, v237\nv_exp_f32 v234, v234\n"
"M 52,164,20\nds_write_addtid_b32 v15 offset:1792\nv_exp_f32 v235, v235\nv_exp_f32 v236, v236\nv_mfma_f32_16x16x32_bf16 v[36:39], v[160:163], a[0:3], 0\nR 182,191,45056\n"
"ds_write_addtid_b32 v24 offset:4096\nv_fma_f32 v234, v234, v216, v216\nv_exp_f32 v237, v237\nM 36,164,16\nds_write_addtid_b32 v25 offset:4352\nG 13\n"
"v_rcp_f32 v234, v234\nv_fma_f32 v235, v235, v216, v216\nv_mfma_f32_16x16x32_bf16 v[40:43], v[168:171], a[0:3], 0\nR 160,188,49152\nds_write_addtid_b32 v26 offset:4608\nds_write_addtid_b32 v27 offset:4864\n"
"ds_write_addtid_b32 v28 offset:5120\nds_write_addtid_b32 v29 offset:5376\nds_write_addtid_b32 v30 offset:5632\nds_write_addtid_b32 v31 offset:5888\nv_fma_f32 v236, v236, v216, v216\nv_fma_f32 v237, v237, v216, v216\n"
"M 40,172,16\ns_add_u32 m0, s50, 32768\ns_nop 0\nbuffer_load_dwordx4 v221, s[12:15], s63 offen offset:1024 lds\nv_rcp_f32 v235, v235\nv_mfma_f32_16x16x32_bf16 v[56:59], v[168:171], a[4:7], 0\n"
"R 162,188,53248\ns_add_u32 m0, s50, 40960\nv_rcp_f32 v236, v236\nM 56,172,20\nG 1\ns_barrier\n"
"s_add_u32 s63, s54, 1835264\nv_rcp_f32 v237, v237\nv_mfma_f32_16x16x32_bf16 v[72:75], v[168:171], a[8:11], 0\nR 164,184,0\nds_read2st64_b32 v[8:9], v225 offset0:0 offset1:1\nv_mov_b32 v248, 0\n"
"v_mul_f32 v234, v108, v234\nM 72,172,24\nbuffer_load_dwordx4 v220, s[12:15], s63 offen lds\nds_read2st64_b32 v[10:11], v225 offset0:2 offset1:3\nv_mov_b32 v249, 0\nv_mul_f32 v235, v109, v235\n"
"v_mfma_f32_16x16x32_bf16 v[88:91], v[168:171], a[12:15], 0\nR 166,184,4096\ns_add_u32 s61, s82, 385024\ns_mov_b64 exec, 0xf\nbuffer_atomic_or_x2 v[248:249], v250, s[20:23], s61 offen sc0 sc1\ns_mov_b64 exec, -1\n"
"v_mul_f32 v236, v110, v236\nM 88,172,28\nds_read2st64_b32 v[12:13], v225 offset0:4 offset1:5\nv_mul_f32 v237, v111, v237\nv_cvt_pk_bf16_f32 v106, v234, v235\nv_cvt_pk_bf16_f32 v107, v236, v237\n"
"v_mfma_f32_16x16x32_bf16 v[92:95], v[176:179], a[12:15], 0\nR 168,189,49152\nds_read2st64_b32 v[14:15], v225 offset0:6 offset1:7\nv_mul_f32 v230, v112, v112\nv_mul_f32 v231, v113, v113\nM 92,180,28\n"
"buffer_load_dwordx4 v221, s[12:15], s63 offen offset:1024 lds\nds_read2st64_b32 v[24:25], v225 offset0:16 offset1:17\nv_mul_f32 v232, v114, v114\nv_mfma_f32_16x16x32_bf16 v[76:79], v[176:179], a[8:11], 0\nR 170,189,53248\ns_add_u32 m0, s50, 49152\n"
"s_add_u32 s63, s54, 2064640\nv_mul_f32 v233, v115, v115\nM 76,180,24\nbuffer_load_dwordx4 v220, s[12:15], s63 offen lds\nv_fma_f32 v230, v230, v218, v217\nv_mfma_f32_16x16x32_bf16 v[60:63], v[176:179], a[4:7], 0\n"
"R 172,185,0\nv_fma_f32 v231, v231, v218, v217\nG 8\nv_add_f32 v0, v0, v8\nv_fma_f32 v232, v232, v218, v217\nM 60,180,20\n"
"ds_read2st64_b32 v[26:27], v225 offset0:18 offset1:19\nv_fma_f32 v233, v233, v218, v217\nv_mul_f32 v230, v112, v230\nv_mul_f32 v231, v113, v231\nv_mfma_f32_16x16x32_bf16 v[44:47], v[176:179], a[0:3], 0\nR 174,185,4096\n"
"v_mul_f32 v232, v114, v232\nv_add_f32 v1, v1, v9\nv_mul_f32 v233, v115, v233\nM 44,180,16\nG 8\nv_exp_f32 v230, v230\n"
"v_add_f32 v2, v2, v10\nv_exp_f32 v231, v231\nM 32,160,32\nR 176,190,49152\nv_fma_f32 v230, v230, v219, v219\nv_add_f32 v3, v3, v11\n"
"v_exp_f32 v232, v232\nM 32,164,48\nbuffer_load_dwordx4 v221, s[12:15], s63 offen offset:1024 lds\nG 8\nv_add_f32 v4, v4, v12\nv_exp_f32 v233, v233\n"
"M 48,160,36\nR 178,190,53248\nv_fma_f32 v231, v231, v219, v219\nv_add_f32 v5, v5, v13\nv_fma_f32 v232, v232, v219, v219\nM 48,164,52\n"
"ds_read2st64_b32 v[28:29], v225 offset0:20 offset1:21\nv_rcp_f32 v230, v230\nv_fma_f32 v233, v233, v219, v219\nM 64,160,40\nR 180,186,0\nv_mul_f32 v230, v112, v230\n"
"G 9\nv_add_f32 v6, v6, v14\nv_rcp_f32 v231, v231\nM 64,164,56\ns_add_u32 m0, s50, 57344\nv_rcp_f32 v232, v232\n"
"v_add_f32 v7, v7, v15\nv_rcp_f32 v233, v233\nM 80,160,44\nR 182,186,4096\nv_mul_f32 v231, v113, v231\nG 9\n"
"v_add_f32 v16, v16, v24\nv_mul_f32 v232, v114, v232\nM 80,164,60\nG 5\nv_mul_f32 v233, v115, v233\nv_cvt_pk_bf16_f32 v112, v230, v231\n"
"v_add_f32 v17, v17, v25\nv_cvt_pk_bf16_f32 v113, v232, v233\nM 84,168,44\nR 160,191,49152\nv_mul_f32 v234, v116, v116\nv_add_f32 v18, v18, v26\n"
"v_mul_f32 v235, v117, v117\nM 84,172,60\ns_add_u32 s63, s54, 2294016\nds_read2st64_b32 v[30:31], v225 offset0:22 offset1:23\nv_mul_f32 v236, v118, v118\nv_add_f32 v19, v19, v27\n"
"G 4\nv_add_f32 v20, v20, v28\nv_add_f32 v21, v21, v29\nG 0\nv_add_f32 v22, v22, v30\nv_add_f32 v23, v23, v31\n"
"M 68,168,40\nR 162,191,53248\nv_mul_f32 v237, v119, v119\nM 68,172,56\nbuffer_load_dwordx4 v220, s[12:15], s63 offen lds\nM 52,168,36\n"
"R 164,187,0\nv_fma_f32 v234, v234, v218, v217\nM 52,172,52\nv_fma_f32 v235, v235, v218, v217\nv_fma_f32 v236, v236, v218, v217\nM 36,168,32\n"
"R 166,187,4096\nv_fma_f32 v237, v237, v218, v217\nM 36,172,48\nv_mul_f32 v234, v116, v234\nv_mul_f32 v235, v117, v235\nM 40,176,32\n"
"R 168,184,8192\nv_mul_f32 v236, v118, v236\nM 40,180,48\nbuffer_load_dwordx4 v221, s[12:15], s63 offen offset:1024 lds\nM 56,176,36\nR 170,184,12288\n"
"v_mul_f32 v237, v119, v237\nM 56,180,52\ns_add_u32 m0, s50, 65536\nv_exp_f32 v234, v234\nM 72,176,40\nR 172,184,16384\n"
"s_add_u32 s63, s54, 2523392\nM 72,180,56\ns_add_u32 s64, s68, 23068672\nv_exp_f32 v235, v235\nM 88,176,44\nR 174,184,20480\n"
"v_fma_f32 v234, v234, v219, v219\nM 88,180,60\nG 4\nv_exp_f32 v236, v236\nM 92,160,44\nR 176,185,8192\n"
"s_waitcnt vmcnt(5)\nv_cmp_eq_u64 s[86:87], s[46:47], v[248:249]\nM 92,164,60\nbuffer_load_dwordx4 v220, s[12:15], s63 offen lds\nM 76,160,40\nR 178,185,12288\n"
"s_and_b32 s62, s86, 0xf\ns_cmp_eq_u32 s62, 0xf\ns_cbranch_scc0 Lq381\nLq382:\nM 76,164,56\ns_add_u32 s65, s68, 23183360\n"
"v_exp_f32 v237, v237\nM 60,160,36\nR 180,185,16384\nv_fma_f32 v235, v235, v219, v219\nM 60,164,52\nbuffer_load_dwordx4 a[0:3], v252, s[20:23], s64 offen sc1\n"
"M 44,160,32\nR 182,185,20480\nv_fma_f32 v236, v236, v219, v219\nM 44,164,48\nG 4\nv_rcp_f32 v234, v234\n"
"M 32,168,64\nR 160,186,8192\nv_fma_f32 v237, v237, v219, v219\nM 32,172,80\ns_add_u32 s66, s69, 23068672\nv_rcp_f32 v235, v235\n"
"M 48,168,68\nR 162,186,12288\nv_mul_f32 v234, v116, v234\nM 48,172,84\nbuffer_load_dwordx4 a[4:7], v252, s[20:23], s65 offen sc1\nM 64,168,72\n"
"R 164,186,16384\nv_mul_f32 v235, v117, v235\nM 64,172,88\nv_rcp_f32 v236, v236\nM 80,168,76\nR 166,186,20480\n"
"v_mul_f32 v236, v118, v236\nM 80,172,92\nG 4\nv_rcp_f32 v237, v237\nM 84,176,76\nR 168,187,8192\n"
"v_mul_f32 v237, v119, v237\nM 84,180,92\nbuffer_load_dwordx4 a[8:11], v252, s[20:23], s66 offen sc1\nM 68,176,72\nR 170,187,12288\nv_cvt_pk_bf16_f32 v114, v234, v235\n"
"M 68,180,88\ns_add_u32 s67, s69, 23183360\nv_cvt_pk_bf16_f32 v115, v236, v237\nv_mul_f32 v230, v120, v120\nM 52,176,68\nR 172,187,16384\n"
"v_mul_f32 v231, v121, v121\nM 52,180,84\ns_add_u32 s64, s68, 23075840\nbuffer_load_dwordx4 v221, s[12:15], s63 offen offset:1024 lds\nM 36,176,64\nR 174,187,20480\n"
"s_add_u32 m0, s50, 73728\nM 36,180,80\ns_add_u32 s63, s54, 2752768\nG 4\nv_mul_f32 v232, v122, v122\nM 40,160,64\n"
"R 176,184,24576\ns_waitcnt vmcnt(7)\ns_barrier\nM 40,164,80\nbuffer_load_dwordx4 a[12:15], v252, s[20:23], s67 offen sc1\nbuffer_load_dwordx4 v220, s[12:15], s63 offen lds\n"
"M 56,160,68\nR 178,184,28672\nv_mul_f32 v233, v123, v123\nM 56,164,84\ns_add_u32 s65, s68, 23190528\nv_fma_f32 v230, v230, v218, v217\n"
"v_fma_f32 v231, v231, v218, v217\nM 72,160,72\nR 180,184,32768\nv_fma_f32 v232, v232, v218, v217\nM 72,164,88\nbuffer_load_dwordx4 a[16:19], v252, s[20:23], s64 offen sc1\n"
"buffer_load_dwordx4 v221, s[12:15], s63 offen offset:1024 lds\nM 88,160,76\nR 182,184,36864\ns_add_u32 m0, s50, 81920\nM 88,164,92\nG 4\n"
"v_fma_f32 v233, v233, v218, v217\nv_mul_f32 v230, v120, v230\nM 92,168,76\nR 160,185,24576\ns_add_u32 s63, s54, 2982144\nM 92,172,92\n"
"s_add_u32 s66, s69, 23075840\nbuffer_load_dwordx4 v220, s[12:15], s63 offen lds\nM 76,168,72\nR 162,185,28672\nv_mul_f32 v231, v121, v231\nM 76,172,88\n"
"buffer_load_dwordx4 a[20:23], v252, s[20:23], s65 offen sc1\nM 60,168,68\nR 164,185,32768\nv_mul_f32 v232, v122, v232\nM 60,172,84\nbuffer_load_dwordx4 v221, s[12:15], s63 offen offset:1024 lds\n"
"M 44,168,64\nR 166,185,36864\nv_mul_f32 v233, v123, v233\nM 44,172,80\nG 4\nv_exp_f32 v230, v230\n"
"M 32,176,96\nR 168,186,24576\nv_fma_f32 v230, v230, v219, v219\nM 32,180,112\nbuffer_load_dwordx4 a[24:27], v252, s[20:23], s66 offen sc1\nM 48,176,100\n"
"R 170,186,28672\nv_mul_f32 v234, v124, v124\nM 48,180,116\ns_add_u32 s67, s69, 23190528\nv_exp_f32 v231, v231\nM 64,176,104\n"
"R 172,186,32768\nv_fma_f32 v231, v231, v219, v219\nM 64,180,120\nbuffer_store_dwordx4 v[104:107], v252, s[20:23], s84 offen offset:1024 sc1\nM 80,176,108\nR 174,186,36864\n"
"v_mul_f32 v235, v125, v125\nM 80,180,124\nG 4\nv_exp_f32 v232, v232\nM 84,160,108\nR 176,187,24576\n"
"v_fma_f32 v232, v232, v219, v219\nM 84,164,124\nbuffer_load_dwordx4 a[28:31], v252, s[20:23], s67 offen sc1\nM 68,160,104\nR 178,187,28672\nv_mul_f32 v236, v126, v126\n"
"M 68,164,120\ns_add_u32 s64, s68, 23069696\nv_exp_f32 v233, v233\nM 52,160,100\nR 180,187,32768\nv_fma_f32 v233, v233, v219, v219\n"
"M 52,164,116\ns_add_u32 s65, s68, 23184384\nv_rcp_f32 v230, v230\nM 36,160,96\nR 182,187,36864\nv_mul_f32 v230, v120, v230\n"
"M 36,164,112\nG 4\nv_rcp_f32 v231, v231\nM 40,168,96\nR 160,184,40960\nv_mul_f32 v231, v121, v231\n"
"M 40,172,112\nbuffer_load_dwordx4 a[32:35], v252, s[20:23], s64 offen sc1\nM 56,168,100\nR 162,184,45056\nv_cvt_pk_bf16_f32 v120, v230, v231\nM 56,172,116\n"
"s_add_u32 s66, s69, 23069696\nv_rcp_f32 v232, v232\nM 72,168,104\nR 164,184,49152\nv_mul_f32 v232, v122, v232\nM 72,172,120\n"
"buffer_load_dwordx4 a[36:39], v252, s[20:23], s65 offen sc1\nM 88,168,108\nR 166,184,53248\nv_mul_f32 v237, v127, v127\nM 88,172,124\nG 4\n"
"v_rcp_f32 v233, v233\nM 92,176,108\nR 168,185,40960\ns_add_u32 m0, s50, 90112\nM 92,180,124\ns_add_u32 s67, s69, 23184384\n"
"v_mul_f32 v233, v123, v233\nv_cvt_pk_bf16_f32 v121, v232, v233\nM 76,176,104\nR 170,185,45056\ns_add_u32 s63, s54, 3211520\nM 76,180,120\n"
"buffer_load_dwordx4 a[40:43], v252, s[20:23], s66 offen sc1\nM 60,176,100\nR 172,185,49152\nv_fma_f32 v234, v234, v218, v217\nM 60,180,116\nbuffer_load_dwordx4 v220, s[12:15], s63 offen lds\n"
"M 44,176,96\nR 174,185,53248\nv_fma_f32 v235, v235, v218, v217\nM 44,180,112\nG 4\nv_fma_f32 v236, v236, v218, v217\n"
"v_fma_f32 v237, v237, v218, v217\nM 32,160,128\nR 176,186,40960\nv_mul_f32 v234, v124, v234\nM 32,164,144\nbuffer_load_dwordx4 a[44:47], v252, s[20:23], s67 offen sc1\n"
"buffer_load_dwordx4 v221, s[12:15], s63 offen offset:1024 lds\nM 48,160,132\nR 178,186,45056\ns_add_u32 m0, s50, 98304\nM 48,164,148\ns_add_u32 s64, s68, 23076864\n"
"v_exp_f32 v234, v234\nM 64,160,136\nR 180,186,49152\ns_add_u32 s63, s54, 3440896\nM 64,164,152\ns_add_u32 s65, s68, 23191552\n"
"buffer_load_dwordx4 v220, s[12:15], s63 offen lds\nM 80,160,140\nR 182,186,53248\nv_mul_f32 v235, v125, v235\nM 80,164,156\nG 4\n"
"v_exp_f32 v235, v235\nM 84,168,140\nR 160,187,40960\nv_mul_f32 v236, v126, v236\nM 84,172,156\nbuffer_load_dwordx4 a[48:51], v252, s[20:23], s64 offen sc1\n"
"buffer_load_dwordx4 v221, s[12:15], s63 offen offset:1024 lds\nM 68,168,136\nR 162,187,45056\nv_mul_f32 v237, v127, v237\nM 68,172,152\ns_add_u32 s66, s69, 23076864\n"
"v_exp_f32 v236, v236\nM 52,168,132\nR 164,187,49152\ns_waitcnt vmcnt(12)\ns_barrier\nM 52,172,148\n"
"buffer_load_dwordx4 a[52:55], v252, s[20:23], s65 offen sc1\ns_movk_i32 s63, 2048\nv_mad_u32_u24 v220, v228, s63, v229\nv_add_u32 v221, 916480, v220\ns_add_u32 m0, s50, 106496\ns_add_u32 s63, s56, 0\n"
"buffer_load_dwordx4 v220, s[16:19], s63 offen lds\nM 36,168,128\nR 166,187,53248\nv_fma_f32 v234, v234, v219, v219\nM 36,172,144\nG 4\n"
"v_exp_f32 v237, v237\nM 40,176,128\nR 168,184,57344\nv_fma_f32 v235, v235, v219, v219\nM 40,180,144\ns_add_u32 s67, s69, 23191552\n"
"buffer_load_dwordx4 v221, s[16:19], s63 offen offset:1024 lds\nM 56,176,132\nR 170,184,61440\ns_add_u32 m0, s50, 114688\nM 56,180,148\nbuffer_load_dwordx4 a[56:59], v252, s[20:23], s66 offen sc1\n"
"M 72,176,136\nR 172,188,0\ns_add_u32 s63, s56, 524288\nM 72,180,152\nbuffer_load_dwordx4 v220, s[16:19], s63 offen lds\nM 88,176,140\n"
"R 174,188,4096\nv_fma_f32 v236, v236, v219, v219\nM 88,180,156\nG 4\nv_rcp_f32 v234, v234\nM 92,160,140\n"
"R 176,185,57344\nv_fma_f32 v237, v237, v219, v219\nM 92,164,156\nbuffer_load_dwordx4 a[60:63], v252, s[20:23], s67 offen sc1\nbuffer_load_dwordx4 v221, s[16:19], s63 offen offset:1024 lds\nM 76,160,136\n"
"R 178,185,61440\nv_mul_f32 v234, v124, v234\nM 76,164,152\ns_add_u32 m0, s50, 0\nv_rcp_f32 v235, v235\nM 60,160,132\n"
"R 180,189,0\nv_mul_f32 v235, v125, v235\nM 60,164,148\ns_add_u32 s63, s56, 65536\nv_rcp_f32 v236, v236\nM 44,160,128\n"
"R 182,189,4096\nv_mul_f32 v236, v126, v236\nM 44,164,144\nG 4\nv_rcp_f32 v237, v237\nM 32,168,160\n"
"R 160,186,57344\nv_mul_f32 v237, v127, v237\nM 32,172,176\nbuffer_load_dwordx4 v220, s[16:19], s63 offen lds\nM 48,168,164\nR 162,186,61440\n"
"v_cvt_pk_bf16_f32 v122, v234, v235\nM 48,172,180\nbuffer_store_dwordx4 v[112:115], v252, s[20:23], s85 offen offset:0 sc1\nM 64,168,168\nR 164,190,0\nv_cvt_pk_bf16_f32 v123, v236, v237\n"
"M 64,172,184\nbuffer_load_dwordx4 v221, s[16:19], s63 offen offset:1024 lds\nM 80,168,172\nR 166,190,4096\ns_add_u32 m0, s50, 8192\nM 80,172,188\n"
"buffer_store_dwordx4 v[120:123], v252, s[20:23], s85 offen offset:1024 sc1\nG 4\nM 84,176,172\nR 168,187,57344\ns_add_u32 s63, s56, 589824\nM 84,180,188\n"
"buffer_load_dwordx4 v220, s[16:19], s63 offen lds\nM 68,176,168\nR 170,187,61440\nv_mul_f32 v230, v0, v0\nv_mul_f32 v231, v1, v1\nM 68,180,184\n"
"s_add_u32 s84, s73, 23074816\ns_add_u32 s85, s73, 23189504\nv_mul_f32 v232, v2, v2\nv_mul_f32 v233, v3, v3\nM 52,176,164\nR 172,191,0\n"
"v_fma_f32 v230, v230, v215, v214\nv_fma_f32 v231, v231, v215, v214\nM 52,180,180\nbuffer_load_dwordx4 v221, s[16:19], s63 offen offset:1024 lds\nv_fma_f32 v232, v232, v215, v214\nM 36,176,160\n"
"R 174,191,4096\nv_fma_f32 v233, v233, v215, v214\nv_mul_f32 v230, v0, v230\nM 36,180,176\nG 4\nv_exp_f32 v230, v230\n"
"v_mul_f32 v231, v1, v231\nM 40,160,160\nR 176,188,8192\nv_mul_f32 v232, v2, v232\nv_mul_f32 v233, v3, v233\nM 40,164,176\n"
"v_exp_f32 v231, v231\nv_exp_f32 v232, v232\nM 56,160,164\nR 178,188,12288\nv_fma_f32 v230, v230, v216, v216\nv_exp_f32 v233, v233\n"
"M 56,164,180\nv_rcp_f32 v230, v230\nv_fma_f32 v231, v231, v216, v216\nM 72,160,168\nR 180,188,16384\nv_fma_f32 v232, v232, v216, v216\n"
"v_fma_f32 v233, v233, v216, v216\nM 72,164,184\nv_rcp_f32 v231, v231\nv_rcp_f32 v232, v232\nM 88,160,172\nR 182,188,20480\n"
"v_mul_f32 v230, v0, v230\nv_rcp_f32 v233, v233\nM 88,164,188\nG 4\nv_mul_f32 v231, v1, v231\nv_mul_f32 v232, v2, v232\n"
"v_mul_f32 v233, v3, v233\nM 92,168,172\nR 160,189,8192\nv_cvt_pk_bf16_f32 v0, v230, v231\nv_cvt_pk_bf16_f32 v1, v232, v233\nM 92,172,188\n"
"v_mul_f32 v234, v4, v4\nv_mul_f32 v235, v5, v5\nv_mul_f32 v236, v6, v6\nM 76,168,168\nR 162,189,12288\nv_mul_f32 v237, v7, v7\n"
"v_fma_f32 v234, v234, v215, v214\nM 76,172,184\nv_fma_f32 v235, v235, v215, v214\nv_fma_f32 v236, v236, v215, v214\nv_fma_f32 v237, v237, v215, v214\nM 60,168,164\n"
"R 164,189,16384\nv_mul_f32 v234, v4, v234\nv_mul_f32 v235, v5, v235\nM 60,172,180\nv_exp_f32 v234, v234\nv_mul_f32 v236, v6, v236\n"
"M 44,168,160\nR 166,189,20480\nv_mul_f32 v237, v7, v237\nv_exp_f32 v235, v235\nM 44,172,176\nG 4\n"
"v_exp_f32 v236, v236\nv_exp_f32 v237, v237\nM 32,176,192\nR 168,190,8192\nv_fma_f32 v234, v234, v216, v216\nv_fma_f32 v235, v235, v216, v216\n"
"M 32,180,208\nv_rcp_f32 v234, v234\nv_fma_f32 v236, v236, v216, v216\nM 48,176,196\nR 170,190,12288\nv_fma_f32 v237, v237, v216, v216\n"
"v_rcp_f32 v235, v235\nM 48,180,212\nv_rcp_f32 v236, v236\nv_rcp_f32 v237, v237\nM 64,176,200\nR 172,190,16384\n"
"v_mul_f32 v234, v4, v234\nv_mul_f32 v235, v5, v235\nM 64,180,216\nv_mul_f32 v236, v6, v236\nv_mul_f32 v237, v7, v237\nv_cvt_pk_bf16_f32 v2, v234, v235\n"
"M 80,176,204\nR 174,190,20480\nv_cvt_pk_bf16_f32 v3, v236, v237\nv_mul_f32 v230, v16, v16\nM 80,180,220\nG 4\n"
"v_mul_f32 v231, v17, v17\nv_mul_f32 v232, v18, v18\nv_mul_f32 v233, v19, v19\nM 84,160,204\nR 176,191,8192\ns_waitcnt vmcnt(6)\n"
"s_barrier\nv_fma_f32 v230, v230, v218, v217\nM 84,164,220\nv_fma_f32 v231, v231, v218, v217\nv_fma_f32 v232, v232, v218, v217\nv_fma_f32 v233, v233, v218, v217\n"
"M 68,160,200\nR 178,191,12288\nv_mul_f32 v230, v16, v230\nv_mul_f32 v231, v17, v231\nM 68,164,216\nv_exp_f32 v230, v230\n"
"v_mul_f32 v232, v18, v232\nM 52,160,196\nR 180,191,16384\nv_mul_f32 v233, v19, v233\nv_exp_f32 v231, v231\nM 52,164,212\n"
"v_exp_f32 v232, v232\nv_exp_f32 v233, v233\nM 36,160,192\nR 182,191,20480\nv_fma_f32 v230, v230, v219, v219\nv_fma_f32 v231, v231, v219, v219\n"
"M 36,164,208\nG 4\nv_rcp_f32 v230, v230\nv_fma_f32 v232, v232, v219, v219\nM 40,168,192\nR 160,188,24576\n"
"v_fma_f32 v233, v233, v219, v219\nv_rcp_f32 v231, v231\nM 40,172,208\nv_rcp_f32 v232, v232\nv_rcp_f32 v233, v233\nM 56,168,196\n"
"R 162,188,28672\nv_mul_f32 v230, v16, v230\nv_mul_f32 v231, v17, v231\nM 56,172,212\ns_add_u32 m0, s50, 16384\nv_mul_f32 v232, v18, v232\n"
"v_mul_f32 v233, v19, v233\nv_cvt_pk_bf16_f32 v16, v230, v231\nM 72,168,200\nR 164,188,32768\nv_cvt_pk_bf16_f32 v17, v232, v233\nv_mul_f32 v234, v20, v20\n"
"M 72,172,216\ns_add_u32 s63, s57, 0\nv_mul_f32 v235, v21, v21\nv_mul_f32 v236, v22, v22\nv_mul_f32 v237, v23, v23\nM 88,168,204\n"
"R 166,188,36864\nv_mov_b32 v226, s46\nv_fma_f32 v234, v234, v218, v217\nM 88,172,220\nG 4\nv_mov_b32 v227, s47\n"
"v_fma_f32 v235, v235, v218, v217\nv_fma_f32 v236, v236, v218, v217\nM 92,176,204\nR 168,189,24576\ns_add_u32 s61, s81, 385032\ns_lshl_b32 s62, s44, 5\n"
"s_add_u32 s61, s61, s62\ns_waitcnt vmcnt(0)\ns_mov_b64 exec, 1\nbuffer_atomic_swap_x2 v[226:227], off, s[20:23], s61 sc1\ns_mov_b64 exec, -1\nv_fma_f32 v237, v237, v218, v217\n"
"M 92,180,220\nbuffer_load_dwordx4 v220, s[16:19], s63 offen lds\nv_mul_f32 v234, v20, v234\nM 76,176,200\nR 170,189,28672\nv_mul_f32 v235, v21, v235\n"
"v_mul_f32 v236, v22, v236\nM 76,180,216\nv_exp_f32 v234, v234\nv_mul_f32 v237, v23, v237\nM 60,176,196\nR 172,189,32768\n"
"v_fma_f32 v234, v234, v219, v219\nv_exp_f32 v235, v235\nM 60,180,212\nbuffer_load_dwordx4 v221, s[16:19], s63 offen offset:1024 lds\nv_exp_f32 v236, v236\nM 44,176,192\n"
"R 174,189,36864\ns_mov_b32 s60, 158208\nv_bfe_u32 v246, v252, 4, 4\nv_lshl_add_u32 v246, v246, 3, s60\nv_exp_f32 v237, v237\nM 44,180,208\n"
"ds_read_b64 v[238:239], v246 offset:0\nG 5\nv_fma_f32 v235, v235, v219, v219\nM 32,160,224\nR 176,190,24576\nds_read_b64 v[240:241], v246 offset:128\n"
"s_add_u32 s63, s57, 524288\nv_fma_f32 v236, v236, v219, v219\nM 32,164,240\nds_read_b64 v[242:243], v246 offset:256\ns_add_u32 m0, s50, 24576\nv_fma_f32 v237, v237, v219, v219\n"
"M 48,160,228\nR 178,190,28672\nds_read_b64 v[244:245], v246 offset:384\nv_rcp_f32 v234, v234\nM 48,164,244\nds_read_b64 v[246:247], v246 offset:0\n"
"buffer_load_dwordx4 v220, s[16:19], s63 offen lds\nv_rcp_f32 v235, v235\nM 64,160,232\nR 180,190,32768\nv_mul_f32 v234, v20, v234\nv_rcp_f32 v236, v236\n"
"M 64,164,248\nbuffer_load_dwordx4 v221, s[16:19], s63 offen offset:1024 lds\nv_rcp_f32 v237, v237\nM 80,160,236\nR 182,190,36864\ns_add_u32 m0, s50, 32768\n"
"M 80,164,252\nG 9\nv_mul_f32 v235, v21, v235\nv_mul_f32 v236, v22, v236\nM 84,168,236\nR 160,191,24576\n"
"s_add_u32 s63, s57, 65536\nM 84,172,252\nbuffer_load_dwordx4 v220, s[16:19], s63 offen lds\nM 68,168,232\nR 162,191,28672\nv_mul_f32 v237, v23, v237\n"
"M 68,172,248\nbuffer_store_dwordx4 v[0:3], v252, s[20:23], s84 offen offset:0 sc1\nM 52,168,228\nR 164,191,32768\nG 9\nv_mfma_f32_16x16x32_bf16 v[32:35], v[238:241], v[208:211], v[32:35]\n"
"v_cvt_pk_bf16_f32 v18, v234, v235\nM 52,172,244\nbuffer_load_dwordx4 v221, s[16:19], s63 offen offset:1024 lds\nM 36,168,224\nR 166,191,36864\nG 9\n"
"v_mfma_f32_16x16x32_bf16 v[36:39], v[240:243], v[208:211], v[36:39]\ns_add_u32 m0, s50, 40960\nM 36,172,240\nG 4\nv_cvt_pk_bf16_f32 v19, v236, v237\nM 40,176,224\n"
"R 168,188,40960\nv_mfma_f32_16x16x32_bf16 v[40:43], v[242:245], v[208:211], v[40:43]\ns_add_u32 s63, s57, 589824\nbuffer_store_dwordx4 v[16:19], v252, s[20:23], s85 offen offset:0 sc1\nM 40,180,240\nbuffer_load_dwordx4 v220, s[16:19], s63 offen lds\n"
"M 56,176,228\nR 170,188,45056\nv_mfma_f32_16x16x32_bf16 v[44:47], v[244:247], v[208:211], v[44:47]\nM 56,180,244\nbuffer_load_dwordx4 v221, s[16:19], s63 offen offset:1024 lds\nM 72,176,232\n"
"R 172,188,49152\nv_mfma_f32_16x16x32_bf16 v[48:51], v[244:247], v[210:213], v[48:51]\ns_add_u32 m0, s50, 49152\ns_add_u32 s63, s56, 131072\nM 72,180,248\nbuffer_load_dwordx4 v220, s[16:19], s63 offen lds\n"
"M 88,176,236\nR 174,188,53248\nv_mfma_f32_16x16x32_bf16 v[52:55], v[238:241], v[210:213], v[52:55]\nM 88,180,252\nG 4\nM 92,160,236\n"
"R 176,189,40960\nv_mfma_f32_16x16x32_bf16 v[56:59], v[240:243], v[210:213], v[56:59]\nM 92,164,252\nbuffer_load_dwordx4 v221, s[16:19], s63 offen offset:1024 lds\nM 76,160,232\nR 178,189,45056\n"
"v_mfma_f32_16x16x32_bf16 v[60:63], v[242:245], v[210:213], v[60:63]\nv_mov_b32 v248, 0\nM 76,164,248\nv_mov_b32 v249, 0\nM 60,160,228\nR 180,189,49152\n"
"s_add_u32 s61, s82, 385032\ns_mov_b64 exec, 0xf\nbuffer_atomic_or_x2 v[248:249], v250, s[20:23], s61 offen sc0 sc1\ns_mov_b64 exec, -1\nM 60,164,244\nM 44,160,224\n"
"R 182,189,53248\nM 44,164,240\ns_barrier\nG 4\nv_mfma_f32_16x16x32_bf16 v[96:99], v[168:171], a[0:3], 0\nR 160,190,40960\n"
"M 96,172,16\ns_mov_b32 m0, s51\nv_mfma_f32_16x16x32_bf16 v[112:115], v[168:171], a[4:7], 0\nR 162,190,45056\nM 112,172,20\nds_write_addtid_b32 v64 offset:0\n"
"v_mfma_f32_16x16x32_bf16 v[128:131], v[168:171], a[8:11], 0\nR 164,190,49152\nM 128,172,24\nds_write_addtid_b32 v65 offset:256\nv_mfma_f32_16x16x32_bf16 v[144:147], v[168:171], a[12:15], 0\nR 166,190,53248\n"
"M 144,172,28\nds_write_addtid_b32 v66 offset:512\nG 7\nv_mfma_f32_16x16x32_bf16 v[148:151], v[176:179], a[12:15], 0\nR 168,191,40960\nM 148,180,28\n"
"ds_write_addtid_b32 v67 offset:768\nv_mfma_f32_16x16x32_bf16 v[132:135], v[176:179], a[8:11], 0\nR 170,191,45056\nM 132,180,24\nds_write_addtid_b32 v68 offset:1024\nv_mfma_f32_16x16x32_bf16 v[116:119], v[176:179], a[4:7], 0\n"
"R 172,191,49152\nM 116,180,20\nds_write_addtid_b32 v69 offset:1280\nv_mfma_f32_16x16x32_bf16 v[100:103], v[176:179], a[0:3], 0\nR 174,191,53248\nds_write_addtid_b32 v70 offset:1536\n"
"M 100,180,16\nds_write_addtid_b32 v71 offset:1792\nds_write_addtid_b32 v72 offset:2048\nG 11\nv_mfma_f32_16x16x32_bf16 v[104:107], v[160:163], a[0:3], 0\nR 176,184,0\n"
"ds_write_addtid_b32 v73 offset:2304\nM 104,164,16\nds_write_addtid_b32 v74 offset:2560\nds_write_addtid_b32 v75 offset:2816\nv_mfma_f32_16x16x32_bf16 v[120:123], v[160:163], a[4:7], 0\nR 178,184,4096\n"
"ds_write_addtid_b32 v76 offset:3072\nM 120,164,20\nds_write_addtid_b32 v77 offset:3328\nds_write_addtid_b32 v78 offset:3584\nv_mfma_f32_16x16x32_bf16 v[136:139], v[160:163], a[8:11], 0\nR 180,184,8192\n"
"ds_write_addtid_b32 v79 offset:3840\nM 136,164,24\nds_write_addtid_b32 v80 offset:4096\nds_write_addtid_b32 v81 offset:4352\nv_mfma_f32_16x16x32_bf16 v[152:155], v[160:163], a[12:15], 0\nR 182,184,12288\n"
"ds_write_addtid_b32 v82 offset:4608\nM 152,164,28\nds_write_addtid_b32 v83 offset:4864\nds_write_addtid_b32 v84 offset:5120\nG 15\nv_mfma_f32_16x16x32_bf16 v[156:159], v[168:171], a[12:15], 0\n"
"R 160,185,0\nds_write_addtid_b32 v85 offset:5376\nM 156,172,28\nds_write_addtid_b32 v86 offset:5632\nds_write_addtid_b32 v87 offset:5888\nv_mfma_f32_16x16x32_bf16 v[140:143], v[168:171], a[8:11], 0\n"
"R 162,185,4096\nds_write_addtid_b32 v88 offset:6144\nM 140,172,24\nds_write_addtid_b32 v89 offset:6400\nds_write_addtid_b32 v90 offset:6656\nv_mfma_f32_16x16x32_bf16 v[124:127], v[168:171], a[4:7], 0\n"
"R 164,185,8192\nds_write_addtid_b32 v91 offset:6912\nM 124,172,20\ns_add_u32 s64, s68, 23070720\nds_write_addtid_b32 v92 offset:7168\nds_write_addtid_b32 v93 offset:7424\n"
"v_mfma_f32_16x16x32_bf16 v[108:111], v[168:171], a[0:3], 0\nR 166,185,12288\ns_waitcnt vmcnt(0)\nv_cmp_eq_u64 s[86:87], s[46:47], v[248:249]\nM 108,172,16\nds_write_addtid_b32 v94 offset:7680\n"
"ds_write_addtid_b32 v95 offset:7936\nG 15\nM 96,176,32\nR 168,186,0\ns_and_b32 s62, s86, 0xf\ns_cmp_eq_u32 s62, 0xf\n"
"s_cbranch_scc0 Lq416\nLq417:\nM 96,180,48\ns_add_u32 s65, s68, 23185408\ns_add_u32 m0, s50, 57344\nv_mov_b32 v226, s46\n"
"M 112,176,36\nR 170,186,4096\nG 2\ns_barrier\nv_mov_b32 v227, s47\nM 112,180,52\n"
"s_add_u32 s61, s81, 385048\ns_lshl_b32 s62, s44, 5\ns_add_u32 s61, s61, s62\ns_waitcnt vmcnt(0)\ns_mov_b64 exec, 1\nbuffer_atomic_swap_x2 v[226:227], off, s[20:23], s61 sc1\n"
"s_mov_b64 exec, -1\ns_add_u32 s63, s56, 655360\nds_read2st64_b32 v[64:65], v225 offset0:0 offset1:1\nM 128,176,40\nR 172,186,8192\nbuffer_load_dwordx4 v220, s[16:19], s63 offen lds\n"
"ds_read2st64_b32 v[66:67], v225 offset0:2 offset1:3\nM 128,180,56\nbuffer_load_dwordx4 a[64:67], v252, s[20:23], s64 offen sc1\nds_read2st64_b32 v[68:69], v225 offset0:4 offset1:5\nM 144,176,44\nR 174,186,12288\n"
"ds_read2st64_b32 v[70:71], v225 offset0:6 offset1:7\nM 144,180,60\nds_read2st64_b32 v[72:73], v225 offset0:8 offset1:9\nds_read2st64_b32 v[74:75], v225 offset0:10 offset1:11\nM 148,160,44\nR 176,187,0\n"
"buffer_load_dwordx4 v221, s[16:19], s63 offen offset:1024 lds\nds_read2st64_b32 v[76:77], v225 offset0:12 offset1:13\nM 148,164,60\nbuffer_load_dwordx4 a[68:71], v252, s[20:23], s65 offen sc1\nG 9\nv_add_f32 v32, v32, v64\n"
"M 132,160,40\nR 178,187,4096\ns_add_u32 s67, s69, 23185408\nv_add_f32 v33, v33, v65\nM 132,164,56\ns_add_u32 s66, s69, 23070720\n"
"s_add_u32 m0, s50, 65536\ns_add_u32 s63, s56, 196608\nG 8\nv_add_f32 v34, v34, v66\nM 116,160,36\nR 180,187,8192\n"
"buffer_load_dwordx4 v220, s[16:19], s63 offen lds\nv_add_f32 v35, v35, v67\nM 116,164,52\nbuffer_load_dwordx4 a[72:75], v252, s[20:23], s66 offen sc1\nG 8\nv_add_f32 v36, v36, v68\n"
"M 100,160,32\nR 182,187,12288\nv_add_f32 v37, v37, v69\nG 7\nv_add_f32 v38, v38, v70\nM 100,164,48\n"
"ds_read2st64_b32 v[78:79], v225 offset0:14 offset1:15\nv_add_f32 v39, v39, v71\nM 104,168,32\nR 160,184,16384\nbuffer_load_dwordx4 v221, s[16:19], s63 offen offset:1024 lds\nG 8\n"
"v_add_f32 v40, v40, v72\nM 104,172,48\nbuffer_load_dwordx4 a[76:79], v252, s[20:23], s67 offen sc1\nv_add_f32 v41, v41, v73\nM 120,168,36\nR 162,184,20480\n"
"G 8\nv_add_f32 v42, v42, v74\nv_add_f32 v43, v43, v75\nM 120,172,52\ns_add_u32 s64, s68, 23077888\nds_read2st64_b32 v[80:81], v225 offset0:16 offset1:17\n"
"G 7\nv_add_f32 v44, v44, v76\nM 136,168,40\nR 164,184,24576\nds_read2st64_b32 v[82:83], v225 offset0:18 offset1:19\nv_add_f32 v45, v45, v77\n"
"M 136,172,56\ns_add_u32 s65, s68, 23192576\nds_read2st64_b32 v[84:85], v225 offset0:20 offset1:21\nG 6\nv_add_f32 v46, v46, v78\nM 152,168,44\n"
"R 166,184,28672\nds_read2st64_b32 v[86:87], v225 offset0:22 offset1:23\nv_add_f32 v47, v47, v79\nM 152,172,60\nds_read2st64_b32 v[88:89], v225 offset0:24 offset1:25\nds_read2st64_b32 v[90:91], v225 offset0:26 offset1:27\n"
"M 156,176,44\nR 168,185,16384\nds_read2st64_b32 v[92:93], v225 offset0:28 offset1:29\nM 156,180,60\nbuffer_load_dwordx4 a[80:83], v252, s[20:23], s64 offen sc1\nG 9\n"
"v_add_f32 v48, v48, v80\nM 140,176,40\nR 170,185,20480\nv_add_f32 v49, v49, v81\nG 8\nv_add_f32 v50, v50, v82\n"
"M 140,180,56\nbuffer_load_dwordx4 a[84:87], v252, s[20:23], s65 offen sc1\nv_add_f32 v51, v51, v83\nM 124,176,36\nR 172,185,24576\nG 8\n"
"v_add_f32 v52, v52, v84\nv_add_f32 v53, v53, v85\nM 124,180,52\ns_add_u32 s66, s69, 23077888\nds_read2st64_b32 v[94:95], v225 offset0:30 offset1:31\nG 7\n"
"v_add_f32 v54, v54, v86\nM 108,176,32\nR 174,185,28672\nv_add_f32 v55, v55, v87\nM 108,180,48\nG 7\n"
"v_add_f32 v56, v56, v88\nv_add_f32 v57, v57, v89\nv_mfma_f32_16x16x32_bf16 v[0:3], v[160:163], a[0:3], 0\nR 176,186,16384\nLq584:\ns_lshr_b32 s60, s45, 3\n"
"s_lshl2_add_u32 s60, s60, s40\ns_lshl_b32 s60, s60, 8\nv_lshl_add_u32 v224, v252, 3, s60\nv_add_u32 v224, Lq584+4096-LqPC, v224\ns_mov_b64 exec, 3\nglobal_load_dword v224, v224, s[98:99]\n"
"s_mov_b64 exec, -1\nM 0,164,16\nbuffer_load_dwordx4 a[88:91], v252, s[20:23], s66 offen sc1\nv_mfma_f32_16x16x32_bf16 v[16:19], v[160:163], a[4:7], 0\nR 178,186,20480\nG 8\n"
"v_add_f32 v58, v58, v90\nM 16,164,20\ns_add_u32 s67, s69, 23192576\nv_add_f32 v59, v59, v91\nG 6\nv_add_f32 v60, v60, v92\n"
"v_mfma_f32_16x16x32_bf16 a[224:227], v[160:163], a[8:11], 0\nR 180,186,24576\nv_add_f32 v61, v61, v93\nv_mfma_f32_16x16x32_bf16 a[224:227], v[164:167], a[24:27], a[224:227]\nbuffer_load_dwordx4 a[92:95], v252, s[20:23], s67 offen sc1\nv_mfma_f32_16x16x32_bf16 a[240:243], v[160:163], a[12:15], 0\n"
"R 182,186,28672\nG 5\nv_add_f32 v62, v62, v94\nv_mfma_f32_16x16x32_bf16 a[240:243], v[164:167], a[28:31], a[240:243]\ns_add_u32 s63, s56, 720896\nG 4\n"
"v_add_f32 v63, v63, v95\nv_mfma_f32_16x16x32_bf16 a[244:247], v[168:171], a[12:15], 0\nR 160,187,16384\ns_add_u32 m0, s50, 73728\nv_mul_f32 v230, v32, v32\nv_mfma_f32_16x16x32_bf16 a[244:247], v[172:175], a[28:31], a[244:247]\n"
"s_add_u32 s64, s68, 23071744\ns_add_u32 s84, s73, 23072768\ns_add_u32 s85, s73, 23187456\nv_mul_f32 v231, v33, v33\nv_mfma_f32_16x16x32_bf16 a[228:231], v[168:171], a[8:11], 0\nR 162,187,20480\n"
"v_mov_b32 v248, 0\nv_mul_f32 v232, v34, v34\nv_mfma_f32_16x16x32_bf16 a[228:231], v[172:175], a[24:27], a[228:231]\nbuffer_load_dwordx4 a[96:99], v252, s[20:23], s64 offen sc1\nv_mov_b32 v249, 0\nv_mul_f32 v233, v35, v35\n"
"v_mfma_f32_16x16x32_bf16 v[20:23], v[168:171], a[4:7], 0\nR 164,187,24576\ns_add_u32 s61, s82, 385048\ns_mov_b64 exec, 0xf\nbuffer_atomic_or_x2 v[248:249], v250, s[20:23], s61 offen sc0 sc1\ns_mov_b64 exec, -1\n"
"v_fma_f32 v230, v230, v215, v214\nM 20,172,20\ns_add_u32 s65, s68, 23186432\nbuffer_load_dwordx4 v220, s[16:19], s63 offen lds\nv_fma_f32 v231, v231, v215, v214\nv_mfma_f32_16x16x32_bf16 v[4:7], v[168:171], a[0:3], 0\n"
"R 166,187,28672\nv_fma_f32 v232, v232, v215, v214\nv_fma_f32 v233, v233, v215, v214\nM 4,172,16\nG 4\nv_mul_f32 v230, v32, v230\n"
"v_mul_f32 v231, v33, v231\nv_mul_f32 v232, v34, v232\nv_mfma_f32_16x16x32_bf16 v[8:11], v[176:179], a[0:3], 0\nR 168,184,32768\nv_mul_f32 v233, v35, v233\nv_exp_f32 v230, v230\n"
"M 8,180,16\nbuffer_load_dwordx4 a[100:103], v252, s[20:23], s65 offen sc1\nbuffer_load_dwordx4 v221, s[16:19], s63 offen offset:1024 lds\nv_exp_f32 v231, v231\nv_mfma_f32_16x16x32_bf16 v[24:27], v[176:179], a[4:7], 0\nR 170,184,36864\n"
"s_add_u32 s67, s69, 23186432\nv_exp_f32 v232, v232\nM 24,180,20\ns_add_u32 s66, s69, 23071744\ns_add_u32 s63, s56, 393216\nv_fma_f32 v230, v230, v216, v216\n"
"v_exp_f32 v233, v233\nv_mfma_f32_16x16x32_bf16 a[232:235], v[176:179], a[8:11], 0\nR 172,184,40960\ns_add_u32 m0, s50, 81920\nv_fma_f32 v231, v231, v216, v216\nv_mfma_f32_16x16x32_bf16 a[232:235], v[180:183], a[24:27], a[232:235]\n"
"buffer_load_dwordx4 a[104:107], v252, s[20:23], s66 offen sc1\nbuffer_load_dwordx4 v220, s[16:19], s63 offen lds\nv_fma_f32 v232, v232, v216, v216\nv_mfma_f32_16x16x32_bf16 a[248:251], v[176:179], a[12:15], 0\nR 174,184,45056\nv_fma_f32 v233, v233, v216, v216\n"
"v_rcp_f32 v230, v230\nv_mfma_f32_16x16x32_bf16 a[248:251], v[180:183], a[28:31], a[248:251]\nG 4\nv_rcp_f32 v231, v231\nv_rcp_f32 v232, v232\nv_mfma_f32_16x16x32_bf16 a[252:255], v[160:163], a[12:15], 0\n"
"R 176,185,32768\nv_mul_f32 v230, v32, v230\nv_rcp_f32 v233, v233\nv_mfma_f32_16x16x32_bf16 a[252:255], v[164:167], a[28:31], a[252:255]\nbuffer_load_dwordx4 a[108:111], v252, s[20:23], s67 offen sc1\nbuffer_load_dwordx4 v221, s[16:19], s63 offen offset:1024 lds\n"
"v_mul_f32 v231, v33, v231\nv_mfma_f32_16x16x32_bf16 a[236:239], v[160:163], a[8:11], 0\nR 178,185,36864\ns_add_u32 m0, s50, 90112\nv_mul_f32 v232, v34, v232\nv_mfma_f32_16x16x32_bf16 a[236:239], v[164:167], a[24:27], a[236:239]\n"
"s_add_u32 s64, s68, 23078912\nv_mul_f32 v233, v35, v233\nv_cvt_pk_bf16_f32 v32, v230, v231\nv_cvt_pk_bf16_f32 v33, v232, v233\nv_mfma_f32_16x16x32_bf16 v[28:31], v[160:163], a[4:7], 0\nR 180,185,40960\n"
"s_add_u32 s63, s56, 458752\nv_mul_f32 v234, v36, v36\nM 28,164,20\ns_add_u32 s65, s68, 23193600\nbuffer_load_dwordx4 v220, s[16:19], s63 offen lds\nv_mul_f32 v235, v37, v37\n"
"v_mfma_f32_16x16x32_bf16 v[12:15], v[160:163], a[0:3], 0\nR 182,185,45056\nv_mul_f32 v236, v38, v38\nv_mul_f32 v237, v39, v39\nM 12,164,16\ns_waitcnt vmcnt(16)\n"
"s_barrier\nG 4\nv_fma_f32 v234, v234, v215, v214\nM 0,168,32\nR 160,186,32768\nv_fma_f32 v235, v235, v215, v214\n"
"v_fma_f32 v236, v236, v215, v214\nM 0,172,48\nbuffer_load_dwordx4 a[112:115], v252, s[20:23], s64 offen sc1\nbuffer_load_dwordx4 v221, s[16:19], s63 offen offset:1024 lds\nv_fma_f32 v237, v237, v215, v214\nM 16,168,36\n"
"R 162,186,36864\nv_mul_f32 v234, v36, v234\nv_mul_f32 v235, v37, v235\nM 16,172,52\nbuffer_load_dwordx4 a[116:119], v252, s[20:23], s65 offen sc1\nv_mul_f32 v236, v38, v236\n"
"v_mfma_f32_16x16x32_bf16 a[224:227], v[168:171], a[40:43], a[224:227]\nR 164,186,40960\nv_mul_f32 v237, v39, v237\nv_exp_f32 v234, v234\nv_mfma_f32_16x16x32_bf16 a[224:227], v[172:175], a[56:59], a[224:227]\ns_add_u32 s66, s69, 23078912\n"
"v_exp_f32 v235, v235\nv_exp_f32 v236, v236\nv_mfma_f32_16x16x32_bf16 a[240:243], v[168:171], a[44:47], a[240:243]\nR 166,186,45056\nv_fma_f32 v234, v234, v216, v216\nv_exp_f32 v237, v237\n"
"v_mfma_f32_16x16x32_bf16 a[240:243], v[172:175], a[60:63], a[240:243]\nG 4\nv_rcp_f32 v234, v234\nv_fma_f32 v235, v235, v216, v216\nv_mfma_f32_16x16x32_bf16 a[244:247], v[176:179], a[44:47], a[244:247]\nR 168,187,32768\n"
"v_fma_f32 v236, v236, v216, v216\nv_fma_f32 v237, v237, v216, v216\nv_mfma_f32_16x16x32_bf16 a[244:247], v[180:183], a[60:63], a[244:247]\nbuffer_load_dwordx4 a[120:123], v252, s[20:23], s66 offen sc1\nv_rcp_f32 v235, v235\nv_mfma_f32_16x16x32_bf16 a[228:231], v[176:179], a[40:43], a[228:231]\n"
"R 170,187,36864\nv_mul_f32 v234, v36, v234\nv_rcp_f32 v236, v236\nv_mfma_f32_16x16x32_bf16 a[228:231], v[180:183], a[56:59], a[228:231]\ns_add_u32 s67, s69, 23193600\nv_rcp_f32 v237, v237\n"
"v_mul_f32 v235, v37, v235\nM 20,176,36\nR 172,187,40960\nv_mul_f32 v236, v38, v236\nv_mul_f32 v237, v39, v237\nM 20,180,52\n"
"buffer_load_dwordx4 a[124:127], v252, s[20:23], s67 offen sc1\nv_cvt_pk_bf16_f32 v34, v234, v235\nM 4,176,32\nR 174,187,45056\ns_waitcnt vmcnt(13)\nv_cmp_eq_u64 s[86:87], s[46:47], v[248:249]\n"
"v_cvt_pk_bf16_f32 v35, v236, v237\nM 4,180,48\ns_add_u32 s63, s57, 131072\nG 4\nv_mul_f32 v230, v40, v40\nv_mul_f32 v231, v41, v41\n"
"M 8,160,32\nR 176,184,49152\ns_and_b32 s62, s86, 0xf\ns_cmp_eq_u32 s62, 0xf\ns_cbranch_scc0 Lq451\nLq452:\n"
"v_mul_f32 v232, v42, v42\nM 8,164,48\ns_add_u32 s64, s68, 23074816\nv_mul_f32 v233, v43, v43\nv_fma_f32 v230, v230, v215, v214\nv_fma_f32 v231, v231, v215, v214\n"
"M 24,160,36\nR 178,184,53248\ns_add_u32 m0, s50, 98304\nv_fma_f32 v232, v232, v215, v214\nM 24,164,52\nbuffer_load_dwordx4 a[128:131], v252, s[20:23], s64 offen sc1\n"
"buffer_load_dwordx4 v220, s[16:19], s63 offen lds\nv_fma_f32 v233, v233, v215, v214\nv_mfma_f32_16x16x32_bf16 a[232:235], v[160:163], a[40:43], a[232:235]\nR 180,184,57344\ns_add_u32 s66, s69, 23074816\nv_mul_f32 v230, v40, v230\n"
"v_mfma_f32_16x16x32_bf16 a[232:235], v[164:167], a[56:59], a[232:235]\ns_add_u32 s65, s68, 23189504\nv_exp_f32 v230, v230\nv_mul_f32 v231, v41, v231\nv_mfma_f32_16x16x32_bf16 a[248:251], v[160:163], a[44:47], a[248:251]\nR 182,184,61440\n"
"v_mul_f32 v232, v42, v232\nv_mul_f32 v233, v43, v233\nv_mfma_f32_16x16x32_bf16 a[248:251], v[164:167], a[60:63], a[248:251]\nbuffer_load_dwordx4 v221, s[16:19], s63 offen offset:1024 lds\nG 4\nv_exp_f32 v231, v231\n"
"v_mfma_f32_16x16x32_bf16 a[252:255], v[168:171], a[44:47], a[252:255]\nR 160,185,49152\ns_add_u32 m0, s50, 106496\nv_exp_f32 v232, v232\nv_mfma_f32_16x16x32_bf16 a[252:255], v[172:175], a[60:63], a[252:255]\nbuffer_load_dwordx4 a[132:135], v252, s[20:23], s65 offen sc1\n"
"v_exp_f32 v233, v233\nv_mfma_f32_16x16x32_bf16 a[236:239], v[168:171], a[40:43], a[236:239]\nR 162,185,53248\ns_add_u32 s63, s57, 655360\nv_fma_f32 v230, v230, v216, v216\nv_mfma_f32_16x16x32_bf16 a[236:239], v[172:175], a[56:59], a[236:239]\n"
"buffer_load_dwordx4 a[136:139], v252, s[20:23], s66 offen sc1\nbuffer_load_dwordx4 v220, s[16:19], s63 offen lds\nv_fma_f32 v231, v231, v216, v216\nM 28,168,36\nR 164,185,57344\ns_add_u32 s64, s68, 23081984\n"
"v_fma_f32 v232, v232, v216, v216\nM 28,172,52\ns_add_u32 s67, s69, 23189504\nv_rcp_f32 v230, v230\nv_fma_f32 v233, v233, v216, v216\nM 12,168,32\n"
"R 166,185,61440\nv_mul_f32 v230, v40, v230\nv_rcp_f32 v231, v231\nM 12,172,48\nbuffer_load_dwordx4 v221, s[16:19], s63 offen offset:1024 lds\nG 4\n"
"v_rcp_f32 v232, v232\nM 96,176,64\nR 168,186,49152\ns_add_u32 m0, s50, 114688\nv_rcp_f32 v233, v233\nM 96,180,80\n"
"buffer_load_dwordx4 a[140:143], v252, s[20:23], s67 offen sc1\nv_mul_f32 v231, v41, v231\nM 112,176,68\nR 170,186,53248\ns_add_u32 s63, s57, 196608\nv_mul_f32 v232, v42, v232\n"
"M 112,180,84\nbuffer_load_dwordx4 a[144:147], v252, s[20:23], s64 offen sc1\nbuffer_load_dwordx4 v220, s[16:19], s63 offen lds\nv_mul_f32 v233, v43, v233\nM 128,176,72\nR 172,186,57344\n"
"s_add_u32 s66, s69, 23081984\nv_cvt_pk_bf16_f32 v40, v230, v231\nM 128,180,88\ns_add_u32 s65, s68, 23196672\nv_cvt_pk_bf16_f32 v41, v232, v233\nv_mul_f32 v234, v44, v44\n"
"v_mul_f32 v235, v45, v45\nM 144,176,76\nR 174,186,61440\nv_mul_f32 v236, v46, v46\nv_mul_f32 v237, v47, v47\nM 144,180,92\n"
"buffer_load_dwordx4 v221, s[16:19], s63 offen offset:1024 lds\nG 4\nv_fma_f32 v234, v234, v215, v214\nM 148,160,76\nR 176,187,49152\ns_add_u32 m0, s50, 0\n"
"v_fma_f32 v235, v235, v215, v214\nM 148,164,92\nbuffer_load_dwordx4 a[148:151], v252, s[20:23], s65 offen sc1\nv_fma_f32 v236, v236, v215, v214\nM 132,160,72\nR 178,187,53248\n"
"s_add_u32 s63, s57, 720896\nv_fma_f32 v237, v237, v215, v214\nM 132,164,88\nbuffer_load_dwordx4 a[152:155], v252, s[20:23], s66 offen sc1\nbuffer_load_dwordx4 v220, s[16:19], s63 offen lds\nv_mul_f32 v234, v44, v234\n"
"M 116,160,68\nR 180,187,57344\nv_mul_f32 v235, v45, v235\nv_mul_f32 v236, v46, v236\nM 116,164,84\ns_add_u32 s67, s69, 23196672\n"
"v_exp_f32 v234, v234\nv_mul_f32 v237, v47, v237\nM 100,160,64\nR 182,187,61440\nv_fma_f32 v234, v234, v216, v216\nv_exp_f32 v235, v235\n"
"M 100,164,80\nbuffer_load_dwordx4 v221, s[16:19], s63 offen offset:1024 lds\nG 4\nv_exp_f32 v236, v236\nM 104,168,64\nR 160,188,0\n"
"s_waitcnt vmcnt(18)\ns_barrier\nv_exp_f32 v237, v237\nM 104,172,80\nbuffer_load_dwordx4 a[156:159], v252, s[20:23], s67 offen sc1\nv_fma_f32 v235, v235, v216, v216\n"
"M 120,168,68\nR 162,188,4096\nv_fma_f32 v236, v236, v216, v216\nv_fma_f32 v237, v237, v216, v216\nM 120,172,84\nv_rcp_f32 v234, v234\n"
"v_rcp_f32 v235, v235\nM 136,168,72\nR 164,188,8192\nv_mul_f32 v234, v44, v234\nv_rcp_f32 v236, v236\nM 136,172,88\n"
"buffer_store_dwordx4 v[32:35], v252, s[20:23], s84 offen offset:0 sc1\nv_rcp_f32 v237, v237\nM 152,168,76\nR 166,188,12288\nv_mul_f32 v235, v45, v235\nv_mul_f32 v236, v46, v236\n"
"M 152,172,92\nG 4\nv_mul_f32 v237, v47, v237\nv_cvt_pk_bf16_f32 v42, v234, v235\nv_cvt_pk_bf16_f32 v43, v236, v237\nM 156,176,76\n"
"R 168,189,0\nv_mul_f32 v230, v48, v48\nv_mul_f32 v231, v49, v49\nM 156,180,92\ns_add_u32 m0, s50, 8192\nv_mul_f32 v232, v50, v50\n"
"v_mul_f32 v233, v51, v51\nv_fma_f32 v230, v230, v218, v217\nM 140,176,72\nR 170,189,4096\nv_fma_f32 v231, v231, v218, v217\nv_fma_f32 v232, v232, v218, v217\n"
"M 140,180,88\ns_add_u32 s63, s57, 393216\nv_fma_f32 v233, v233, v218, v217\nv_mul_f32 v230, v48, v230\nv_mul_f32 v231, v49, v231\nM 124,176,68\n"
"R 172,189,8192\nv_mul_f32 v232, v50, v232\nv_mul_f32 v233, v51, v233\nM 124,180,84\nbuffer_load_dwordx4 v220, s[16:19], s63 offen lds\nv_exp_f32 v230, v230\n"
"M 108,176,64\nR 174,189,12288\nv_fma_f32 v230, v230, v219, v219\nv_exp_f32 v231, v231\nM 108,180,80\nG 4\n"
"v_exp_f32 v232, v232\nv_exp_f32 v233, v233\nM 96,160,96\nR 176,190,0\nv_fma_f32 v231, v231, v219, v219\nv_fma_f32 v232, v232, v219, v219\n"
"M 96,164,112\nbuffer_load_dwordx4 v221, s[16:19], s63 offen offset:1024 lds\nv_fma_f32 v233, v233, v219, v219\nM 112,160,100\nR 178,190,4096\ns_add_u32 s63, s57, 458752\n"
"v_rcp_f32 v230, v230\ns_waitcnt vmcnt(21)\nM 112,164,116\ns_add_u32 m0, s50, 16384\nv_rcp_f32 v231, v231\nv_rcp_f32 v232, v232\n"
"M 128,160,104\nR 180,190,8192\nv_mul_f32 v230, v48, v230\nv_rcp_f32 v233, v233\ns_waitcnt vmcnt(20)\nM 128,164,120\n"
"buffer_load_dwordx4 v220, s[16:19], s63 offen lds\nv_mul_f32 v231, v49, v231\nM 144,160,108\nR 182,190,12288\nv_mul_f32 v232, v50, v232\nv_mul_f32 v233, v51, v233\n"
"s_waitcnt vmcnt(20)\nM 144,164,124\nG 4\nv_cvt_pk_bf16_f32 v48, v230, v231\nv_cvt_pk_bf16_f32 v49, v232, v233\nv_mul_f32 v234, v52, v52\n"
"M 148,168,108\nR 160,191,0\nv_mul_f32 v235, v53, v53\nv_mul_f32 v236, v54, v54\nM 148,172,124\nv_mul_f32 v237, v55, v55\n"
"v_fma_f32 v234, v234, v218, v217\nv_fma_f32 v235, v235, v218, v217\nM 132,168,104\nR 162,191,4096\nv_fma_f32 v236, v236, v218, v217\nv_fma_f32 v237, v237, v218, v217\n"
"M 132,172,120\nbuffer_load_dwordx4 v221, s[16:19], s63 offen offset:1024 lds\nv_mul_f32 v234, v52, v234\nM 116,168,100\nR 164,191,8192\ns_add_u32 s63, s58, 0\n"
"v_mul_f32 v235, v53, v235\nM 116,172,116\ns_add_u32 m0, s50, 24576\nv_exp_f32 v234, v234\nv_mul_f32 v236, v54, v236\nM 100,168,96\n"
"R 166,191,12288\nv_mul_f32 v237, v55, v237\nv_exp_f32 v235, v235\nM 100,172,112\nG 4\nv_exp_f32 v236, v236\n"
"v_exp_f32 v237, v237\nM 104,176,96\nR 168,188,16384\nv_fma_f32 v234, v234, v219, v219\nv_fma_f32 v235, v235, v219, v219\nM 104,180,112\n"
"buffer_load_dwordx4 v220, s[16:19], s63 offen lds\nv_fma_f32 v236, v236, v219, v219\nM 120,176,100\nR 170,188,20480\nv_fma_f32 v237, v237, v219, v219\nv_rcp_f32 v234, v234\n"
"M 120,180,116\nv_rcp_f32 v235, v235\nv_rcp_f32 v236, v236\nM 136,176,104\nR 172,188,24576\nv_mul_f32 v234, v52, v234\n"
"v_rcp_f32 v237, v237\nM 136,180,120\nbuffer_load_dwordx4 v221, s[16:19], s63 offen offset:1024 lds\nv_mul_f32 v235, v53, v235\nM 152,176,108\nR 174,188,28672\n"
"s_add_u32 m0, s50, 32768\nv_mul_f32 v236, v54, v236\nM 152,180,124\nG 4\nv_mul_f32 v237, v55, v237\nv_cvt_pk_bf16_f32 v50, v234, v235\n"
"v_cvt_pk_bf16_f32 v51, v236, v237\nM 156,160,108\nR 176,189,16384\ns_add_u32 s63, s58, 524288\nv_mul_f32 v230, v56, v56\nM 156,164,124\n"
"buffer_load_dwordx4 v220, s[16:19], s63 offen lds\nv_mul_f32 v231, v57, v57\nM 140,160,104\nR 178,189,20480\nv_mul_f32 v232, v58, v58\nv_mul_f32 v233, v59, v59\n"
"M 140,164,120\nv_fma_f32 v230, v230, v218, v217\nv_fma_f32 v231, v231, v218, v217\nv_fma_f32 v232, v232, v218, v217\nM 124,160,100\nR 180,189,24576\n"
"v_fma_f32 v233, v233, v218, v217\nv_mul_f32 v230, v56, v230\nM 124,164,116\nbuffer_load_dwordx4 v221, s[16:19], s63 offen offset:1024 lds\nv_mul_f32 v231, v57, v231\nM 108,160,96\n"
"R 182,189,28672\ns_add_u32 s63, s58, 65536\nv_mul_f32 v232, v58, v232\nM 108,164,112\ns_waitcnt vmcnt(10)\ns_barrier\n"
"G 4\nM 96,168,128\nR 160,190,16384\nv_mul_f32 v233, v59, v233\nM 96,172,144\ns_add_u32 m0, s50, 40960\n"
"v_exp_f32 v230, v230\nM 112,168,132\nR 162,190,20480\nv_fma_f32 v230, v230, v219, v219\nM 112,172,148\nbuffer_load_dwordx4 v220, s[16:19], s63 offen lds\n"
"M 128,168,136\nR 164,190,24576\nv_mul_f32 v234, v60, v60\nM 128,172,152\nv_exp_f32 v231, v231\nM 144,168,140\n"
"R 166,190,28672\nv_fma_f32 v231, v231, v219, v219\ns_waitcnt vmcnt(10)\nM 144,172,156\nG 4\nv_exp_f32 v232, v232\n"
"M 148,176,140\nR 168,191,16384\nv_fma_f32 v232, v232, v219, v219\nM 148,180,156\nbuffer_load_dwordx4 v221, s[16:19], s63 offen offset:1024 lds\nM 132,176,136\n"
"R 170,191,20480\nv_mul_f32 v235, v61, v61\nM 132,180,152\ns_add_u32 m0, s50, 49152\nv_exp_f32 v233, v233\nM 116,176,132\n"
"R 172,191,24576\nv_fma_f32 v233, v233, v219, v219\nM 116,180,148\ns_add_u32 s63, s58, 589824\nv_rcp_f32 v230, v230\nM 100,176,128\n"
"R 174,191,28672\nv_mul_f32 v230, v56, v230\nM 100,180,144\nG 4\nv_rcp_f32 v231, v231\nM 104,160,128\n"
"R 176,188,32768\nv_mul_f32 v231, v57, v231\nM 104,164,144\nbuffer_load_dwordx4 v220, s[16:19], s63 offen lds\nM 120,160,132\nR 178,188,36864\n"
"v_cvt_pk_bf16_f32 v56, v230, v231\nM 120,164,148\nv_rcp_f32 v232, v232\nM 136,160,136\nR 180,188,40960\nv_mul_f32 v232, v58, v232\n"
"M 136,164,152\nbuffer_store_dwordx4 v[40:43], v252, s[20:23], s84 offen offset:1024 sc1\nM 152,160,140\nR 182,188,45056\nv_mul_f32 v236, v62, v62\nM 152,164,156\n"
"G 4\nv_rcp_f32 v233, v233\nM 156,168,140\nR 160,189,32768\nv_mul_f32 v233, v59, v233\nM 156,172,156\n"
"buffer_load_dwordx4 v221, s[16:19], s63 offen offset:1024 lds\nM 140,168,136\nR 162,189,36864\nv_cvt_pk_bf16_f32 v57, v232, v233\nM 140,172,152\ns_add_u32 m0, s50, 57344\n"
"v_mul_f32 v237, v63, v63\nv_fma_f32 v234, v234, v218, v217\nM 124,168,132\nR 164,189,40960\nv_fma_f32 v235, v235, v218, v217\nM 124,172,148\n"
"s_add_u32 s63, s58, 131072\nv_fma_f32 v236, v236, v218, v217\nv_fma_f32 v237, v237, v218, v217\nM 108,168,128\nR 166,189,45056\nv_mul_f32 v234, v60, v234\n"
"M 108,172,144\nG 4\nv_exp_f32 v234, v234\nM 0,176,64\nR 168,190,32768\nv_mul_f32 v235, v61, v235\n"
"M 0,180,80\nbuffer_load_dwordx4 v220, s[16:19], s63 offen lds\nM 16,176,68\nR 170,190,36864\nv_mul_f32 v236, v62, v236\nM 16,180,84\n"
"v_exp_f32 v235, v235\nv_mfma_f32_16x16x32_bf16 a[224:227], v[176:179], a[72:75], a[224:227]\nR 172,190,40960\nv_mul_f32 v237, v63, v237\nv_mfma_f32_16x16x32_bf16 a[224:227], v[180:183], a[88:91], a[224:227]\nbuffer_store_dwordx4 v[48:51], v252, s[20:23], s85 offen offset:0 sc1\n"
"v_mfma_f32_16x16x32_bf16 a[240:243], v[176:179], a[76:79], a[240:243]\nR 174,190,45056\nv_fma_f32 v234, v234, v219, v219\nv_mfma_f32_16x16x32_bf16 a[240:243], v[180:183], a[92:95], a[240:243]\nG 4\nv_exp_f32 v236, v236\n"
"v_mfma_f32_16x16x32_bf16 a[244:247], v[160:163], a[76:79], a[244:247]\nR 176,191,32768\nv_fma_f32 v235, v235, v219, v219\nv_mfma_f32_16x16x32_bf16 a[244:247], v[164:167], a[92:95], a[244:247]\nbuffer_load_dwordx4 v221, s[16:19], s63 offen offset:1024 lds\nv_mfma_f32_16x16x32_bf16 a[228:231], v[160:163], a[72:75], a[228:231]\n"
"R 178,191,36864\nv_fma_f32 v236, v236, v219, v219\nv_mfma_f32_16x16x32_bf16 a[228:231], v[164:167], a[88:91], a[228:231]\ns_add_u32 m0, s50, 65536\nv_exp_f32 v237, v237\nM 20,160,68\n"
"R 180,191,40960\nv_fma_f32 v237, v237, v219, v219\nM 20,164,84\ns_add_u32 s63, s58, 655360\nv_rcp_f32 v234, v234\nM 4,160,64\n"
"R 182,191,45056\nv_mul_f32 v234, v60, v234\nM 4,164,80\nG 4\nv_rcp_f32 v235, v235\nM 8,168,64\n"
"R 160,188,49152\nv_mul_f32 v235, v61, v235\nM 8,172,80\nbuffer_load_dwordx4 v220, s[16:19], s63 offen lds\nM 24,168,68\nR 162,188,53248\n"
"v_cvt_pk_bf16_f32 v58, v234, v235\nM 24,172,84\nv_rcp_f32 v236, v236\nv_mfma_f32_16x16x32_bf16 a[232:235], v[168:171], a[72:75], a[232:235]\nR 164,184,0\nv_mul_f32 v236, v62, v236\n"
"v_mfma_f32_16x16x32_bf16 a[232:235], v[172:175], a[88:91], a[232:235]\nbuffer_load_dwordx4 v221, s[16:19], s63 offen offset:1024 lds\nv_mfma_f32_16x16x32_bf16 a[248:251], v[168:171], a[76:79], a[248:251]\nR 166,184,4096\ns_add_u32 m0, s50, 73728\nv_mfma_f32_16x16x32_bf16 a[248:251], v[172:175], a[92:95], a[248:251]\n"
"G 4\nv_rcp_f32 v237, v237\nv_mfma_f32_16x16x32_bf16 a[252:255], v[176:179], a[76:79], a[252:255]\nR 168,189,49152\ns_add_u32 s63, s58, 196608\nv_mfma_f32_16x16x32_bf16 a[252:255], v[180:183], a[92:95], a[252:255]\n"
"buffer_load_dwordx4 v220, s[16:19], s63 offen lds\nv_mfma_f32_16x16x32_bf16 a[236:239], v[176:179], a[72:75], a[236:239]\nR 170,189,53248\nv_mul_f32 v237, v63, v237\nv_mfma_f32_16x16x32_bf16 a[236:239], v[180:183], a[88:91], a[236:239]\nv_cvt_pk_bf16_f32 v59, v236, v237\n"
"buffer_store_dwordx4 v[56:59], v252, s[20:23], s85 offen offset:1024 sc1\nM 28,176,68\nR 172,185,0\nM 28,180,84\nbuffer_load_dwordx4 v221, s[16:19], s63 offen offset:1024 lds\nM 12,176,64\n"
"R 174,185,4096\nM 12,180,80\ns_waitcnt vmcnt(13)\ns_barrier\nG 4\nM 0,160,96\n"
"R 176,190,49152\nM 0,164,112\nM 16,160,100\nR 178,190,53248\nM 16,164,116\nv_mfma_f32_16x16x32_bf16 a[224:227], v[160:163], a[104:107], a[224:227]\n"
"R 180,186,0\nv_mfma_f32_16x16x32_bf16 a[224:227], v[164:167], a[120:123], a[224:227]\nv_mfma_f32_16x16x32_bf16 a[240:243], v[160:163], a[108:111], a[240:243]\nR 182,186,4096\nv_mfma_f32_16x16x32_bf16 a[240:243], v[164:167], a[124:127], a[240:243]\nG 4\n"
"v_mfma_f32_16x16x32_bf16 a[244:247], v[168:171], a[108:111], a[244:247]\nR 160,191,49152\nv_mfma_f32_16x16x32_bf16 a[244:247], v[172:175], a[124:127], a[244:247]\nv_mfma_f32_16x16x32_bf16 a[228:231], v[168:171], a[104:107], a[228:231]\nR 162,191,53248\nv_mfma_f32_16x16x32_bf16 a[228:231], v[172:175], a[120:123], a[228:231]\n"
"M 20,168,100\nR 164,187,0\nM 20,172,116\nM 4,168,96\nR 166,187,4096\nM 4,172,112\n"
"G 4\nM 8,176,96\nR 168,184,8192\nM 8,180,112\nM 24,176,100\nR 170,184,12288\n"
"M 24,180,116\nv_mfma_f32_16x16x32_bf16 a[232:235], v[176:179], a[104:107], a[232:235]\nR 172,184,16384\nv_mfma_f32_16x16x32_bf16 a[232:235], v[180:183], a[120:123], a[232:235]\nv_mfma_f32_16x16x32_bf16 a[248:251], v[176:179], a[108:111], a[248:251]\nR 174,184,20480\n"
"v_mfma_f32_16x16x32_bf16 a[248:251], v[180:183], a[124:127], a[248:251]\nG 4\nv_mfma_f32_16x16x32_bf16 a[252:255], v[160:163], a[108:111], a[252:255]\nR 176,185,8192\nv_mfma_f32_16x16x32_bf16 a[252:255], v[164:167], a[124:127], a[252:255]\nv_mfma_f32_16x16x32_bf16 a[236:239], v[160:163], a[104:107], a[236:239]\n"
"R 178,185,12288\nv_mfma_f32_16x16x32_bf16 a[236:239], v[164:167], a[120:123], a[236:239]\nM 28,160,100\nR 180,185,16384\nM 28,164,116\nM 12,160,96\n"
"R 182,185,20480\nM 12,164,112\nG 4\nM 0,168,128\nR 160,186,8192\nM 0,172,144\n"
"M 16,168,132\nR 162,186,12288\nM 16,172,148\nv_mfma_f32_16x16x32_bf16 a[224:227], v[168:171], a[136:139], a[224:227]\nR 164,186,16384\nv_mfma_f32_16x16x32_bf16 a[224:227], v[172:175], a[152:155], a[224:227]\n"
"v_mfma_f32_16x16x32_bf16 a[240:243], v[168:171], a[140:143], a[240:243]\nR 166,186,20480\nv_mfma_f32_16x16x32_bf16 a[240:243], v[172:175], a[156:159], a[240:243]\nG 4\nv_mfma_f32_16x16x32_bf16 a[244:247], v[176:179], a[140:143], a[244:247]\nR 168,187,8192\n"
"v_mfma_f32_16x16x32_bf16 a[244:247], v[180:183], a[156:159], a[244:247]\nv_mfma_f32_16x16x32_bf16 a[228:231], v[176:179], a[136:139], a[228:231]\nR 170,187,12288\nv_mov_b32 v226, s46\nv_mfma_f32_16x16x32_bf16 a[228:231], v[180:183], a[152:155], a[228:231]\ns_add_u32 m0, s50, 81920\n"
"v_mov_b32 v227, s47\nM 20,176,132\nR 172,187,16384\ns_add_u32 s61, s81, 385040\ns_lshl_b32 s62, s44, 5\ns_add_u32 s61, s61, s62\n"
"s_waitcnt vmcnt(0)\ns_mov_b64 exec, 1\nbuffer_atomic_swap_x2 v[226:227], off, s[20:23], s61 sc1\ns_mov_b64 exec, -1\nM 20,180,148\ns_add_u32 s63, s58, 720896\n"
"M 4,176,128\nR 174,187,20480\nM 4,180,144\nG 4\nM 8,160,128\nR 176,184,24576\n"
"M 8,164,144\nbuffer_load_dwordx4 v220, s[16:19], s63 offen lds\nM 24,160,132\nR 178,184,28672\nM 24,164,148\nv_mfma_f32_16x16x32_bf16 a[232:235], v[160:163], a[136:139], a[232:235]\n"
"R 180,184,32768\nv_mfma_f32_16x16x32_bf16 a[232:235], v[164:167], a[152:155], a[232:235]\nbuffer_load_dwordx4 v221, s[16:19], s63 offen offset:1024 lds\nv_mfma_f32_16x16x32_bf16 a[248:251], v[160:163], a[140:143], a[248:251]\nR 182,184,36864\ns_add_u32 m0, s50, 90112\n"
"v_mfma_f32_16x16x32_bf16 a[248:251], v[164:167], a[156:159], a[248:251]\nG 4\nv_mfma_f32_16x16x32_bf16 a[252:255], v[168:171], a[140:143], a[252:255]\nR 160,185,24576\ns_add_u32 s63, s58, 393216\nv_mfma_f32_16x16x32_bf16 a[252:255], v[172:175], a[156:159], a[252:255]\n"
"buffer_load_dwordx4 v220, s[16:19], s63 offen lds\nv_mfma_f32_16x16x32_bf16 a[236:239], v[168:171], a[136:139], a[236:239]\nR 162,185,28672\nv_mfma_f32_16x16x32_bf16 a[236:239], v[172:175], a[152:155], a[236:239]\nM 28,168,132\nR 164,185,32768\n"
"M 28,172,148\nbuffer_load_dwordx4 v221, s[16:19], s63 offen offset:1024 lds\nM 12,168,128\nR 166,185,36864\ns_add_u32 s63, s58, 458752\nM 12,172,144\n"
"s_barrier\nG 4\nv_mfma_f32_16x16x32_bf16 v[32:35], v[176:179], a[0:3], 0\nR 168,186,24576\nM 32,180,16\ns_add_u32 m0, s50, 98304\n"
"v_mfma_f32_16x16x32_bf16 v[48:51], v[176:179], a[4:7], 0\nR 170,186,28672\nM 48,180,20\nbuffer_load_dwordx4 v220, s[16:19], s63 offen lds\nv_mfma_f32_16x16x32_bf16 v[64:67], v[176:179], a[8:11], 0\nR 172,186,32768\n"
"M 64,180,24\nv_mfma_f32_16x16x32_bf16 v[80:83], v[176:179], a[12:15], 0\nR 174,186,36864\nM 80,180,28\nG 4\nv_mfma_f32_16x16x32_bf16 v[84:87], v[160:163], a[12:15], 0\n"
"R 176,187,24576\nM 84,164,28\nbuffer_load_dwordx4 v221, s[16:19], s63 offen offset:1024 lds\nv_mfma_f32_16x16x32_bf16 v[68:71], v[160:163], a[8:11], 0\nR 178,187,28672\nM 68,164,24\n"
"v_mfma_f32_16x16x32_bf16 v[52:55], v[160:163], a[4:7], 0\nR 180,187,32768\nM 52,164,20\ns_add_u32 m0, s50, 106496\nv_mfma_f32_16x16x32_bf16 v[36:39], v[160:163], a[0:3], 0\nR 182,187,36864\n"
"v_mov_b32 v248, 0\nM 36,164,16\nG 4\nv_mov_b32 v249, 0\nv_mfma_f32_16x16x32_bf16 v[40:43], v[168:171], a[0:3], 0\nR 160,184,40960\n"
"s_add_u32 s61, s82, 385040\ns_mov_b64 exec, 0xf\nbuffer_atomic_or_x2 v[248:249], v250, s[20:23], s61 offen sc0 sc1\ns_mov_b64 exec, -1\nM 40,172,16\ns_add_u32 s63, s56, 262144\n"
"v_mfma_f32_16x16x32_bf16 v[56:59], v[168:171], a[4:7], 0\nR 162,184,45056\nM 56,172,20\nbuffer_load_dwordx4 v220, s[16:19], s63 offen lds\nv_mfma_f32_16x16x32_bf16 v[72:75], v[168:171], a[8:11], 0\nR 164,184,49152\n"
"M 72,172,24\nv_mfma_f32_16x16x32_bf16 v[88:91], v[168:171], a[12:15], 0\nR 166,184,53248\nM 88,172,28\nG 4\nv_mfma_f32_16x16x32_bf16 v[92:95], v[176:179], a[12:15], 0\n"
"R 168,185,40960\nM 92,180,28\nbuffer_load_dwordx4 v221, s[16:19], s63 offen offset:1024 lds\nv_mfma_f32_16x16x32_bf16 v[76:79], v[176:179], a[8:11], 0\nR 170,185,45056\ns_add_u32 s63, s56, 786432\n"
"M 76,180,24\ns_add_u32 m0, s50, 114688\nv_mfma_f32_16x16x32_bf16 v[60:63], v[176:179], a[4:7], 0\nR 172,185,49152\nM 60,180,20\nbuffer_load_dwordx4 v220, s[16:19], s63 offen lds\n"
"v_mfma_f32_16x16x32_bf16 v[44:47], v[176:179], a[0:3], 0\nR 174,185,53248\nM 44,180,16\nG 4\nM 32,160,32\nR 176,186,40960\n"
"M 32,164,48\nbuffer_load_dwordx4 v221, s[16:19], s63 offen offset:1024 lds\nM 48,160,36\nR 178,186,45056\ns_add_u32 s63, s56, 327680\nM 48,164,52\n"
"s_add_u32 m0, s50, 0\nM 64,160,40\nR 180,186,49152\nM 64,164,56\nbuffer_load_dwordx4 v220, s[16:19], s63 offen lds\nM 80,160,44\n"
"R 182,186,53248\nM 80,164,60\nG 4\nM 84,168,44\nR 160,187,40960\nM 84,172,60\n"
"buffer_load_dwordx4 v221, s[16:19], s63 offen offset:1024 lds\nM 68,168,40\nR 162,187,45056\nM 68,172,56\nM 52,168,36\nR 164,187,49152\n"
"M 52,172,52\ns_add_u32 m0, s50, 8192\nM 36,168,32\nR 166,187,53248\ns_add_u32 s63, s56, 851968\nM 36,172,48\n"
"G 4\nM 40,176,32\nR 168,184,57344\nM 40,180,48\ns_add_u32 s64, s68, 23072768\nM 56,176,36\n"
"R 170,184,61440\ns_waitcnt vmcnt(6)\nv_cmp_eq_u64 s[86:87], s[46:47], v[248:249]\nM 56,180,52\nbuffer_load_dwordx4 v220, s[16:19], s63 offen lds\nM 72,176,40\n"
"R 172,188,0\ns_and_b32 s62, s86, 0xf\ns_cmp_eq_u32 s62, 0xf\ns_cbranch_scc0 Lq470\nLq471:\nM 72,180,56\n"
"s_add_u32 s65, s68, 23187456\nM 88,176,44\nR 174,188,4096\ns_barrier\nM 88,180,60\nG 4\n"
"M 92,160,44\nR 176,185,57344\nM 92,164,60\nbuffer_load_dwordx4 a[160:163], v252, s[20:23], s64 offen sc1\nM 76,160,40\nR 178,185,61440\n"
"M 76,164,56\ns_add_u32 s66, s69, 23072768\nM 60,160,36\nR 180,189,0\nM 60,164,52\nbuffer_load_dwordx4 a[164:167], v252, s[20:23], s65 offen sc1\n"
"M 44,160,32\nR 182,189,4096\nM 44,164,48\nG 4\nM 32,168,64\nR 160,186,57344\n"
"M 32,172,80\ns_add_u32 s67, s69, 23187456\nM 48,168,68\nR 162,186,61440\nM 48,172,84\nbuffer_load_dwordx4 a[168:171], v252, s[20:23], s66 offen sc1\n"
"M 64,168,72\nR 164,190,0\nM 64,172,88\nM 80,168,76\nR 166,190,4096\nM 80,172,92\n"
"G 4\nM 84,176,76\nR 168,187,57344\nM 84,180,92\nbuffer_load_dwordx4 a[172:175], v252, s[20:23], s67 offen sc1\nM 68,176,72\n"
"R 170,187,61440\nM 68,180,88\ns_add_u32 s64, s68, 23079936\nM 52,176,68\nR 172,191,0\nM 52,180,84\n"
"s_add_u32 s65, s68, 23194624\nM 36,176,64\nR 174,191,4096\nM 36,180,80\nbuffer_load_dwordx4 v221, s[16:19], s63 offen offset:1024 lds\nG 4\n"
"M 40,160,64\nR 176,188,8192\ns_add_u32 m0, s50, 16384\nM 40,164,80\nbuffer_load_dwordx4 a[176:179], v252, s[20:23], s64 offen sc1\nM 56,160,68\n"
"R 178,188,12288\ns_add_u32 s63, s57, 262144\nM 56,164,84\ns_add_u32 s66, s69, 23079936\nbuffer_load_dwordx4 v220, s[16:19], s63 offen lds\nM 72,160,72\n"
"R 180,188,16384\nLq586:\ns_lshr_b32 s60, s45, 3\ns_lshl2_add_u32 s60, s60, s40\ns_lshl_b32 s60, s60, 8\nv_lshl_add_u32 v224, v252, 3, s60\n"
"v_add_u32 v224, Lq586+4096-LqPC, v224\ns_mov_b64 exec, 3\nglobal_load_dword v224, v224, s[98:99]\ns_mov_b64 exec, -1\nM 72,164,88\nbuffer_load_dwordx4 a[180:183], v252, s[20:23], s65 offen sc1\n"
"M 88,160,76\nR 182,188,20480\nM 88,164,92\nbuffer_load_dwordx4 v221, s[16:19], s63 offen offset:1024 lds\nG 4\nM 92,168,76\n"
"R 160,189,8192\ns_add_u32 m0, s50, 24576\nM 92,172,92\ns_add_u32 s67, s69, 23194624\nM 76,168,72\nR 162,189,12288\n"
"s_add_u32 s63, s57, 786432\nM 76,172,88\nbuffer_load_dwordx4 a[184:187], v252, s[20:23], s66 offen sc1\nbuffer_load_dwordx4 v220, s[16:19], s63 offen lds\nM 60,168,68\nR 164,189,16384\n"
"M 60,172,84\nbuffer_load_dwordx4 v221, s[16:19], s63 offen offset:1024 lds\nM 44,168,64\nR 166,189,20480\ns_add_u32 m0, s50, 32768\nM 44,172,80\n"
"G 4\nM 32,176,96\nR 168,190,8192\ns_add_u32 s63, s57, 327680\nM 32,180,112\nbuffer_load_dwordx4 a[188:191], v252, s[20:23], s67 offen sc1\n"
"M 48,176,100\nR 170,190,12288\nM 48,180,116\ns_add_u32 s64, s68, 23073792\nbuffer_load_dwordx4 v220, s[16:19], s63 offen lds\nM 64,176,104\n"
"R 172,190,16384\nM 64,180,120\nbuffer_load_dwordx4 v221, s[16:19], s63 offen offset:1024 lds\nM 80,176,108\nR 174,190,20480\ns_add_u32 m0, s50, 40960\n"
"M 80,180,124\nG 4\nM 84,160,108\nR 176,191,8192\ns_waitcnt vmcnt(19)\ns_barrier\n"
"M 84,164,124\nbuffer_load_dwordx4 a[192:195], v252, s[20:23], s64 offen sc1\nM 68,160,104\nR 178,191,12288\ns_add_u32 s63, s57, 851968\nM 68,164,120\n"
"s_add_u32 s65, s68, 23188480\nM 52,160,100\nR 180,191,16384\nbuffer_load_dwordx4 v220, s[16:19], s63 offen lds\nM 52,164,116\ns_add_u32 s66, s69, 23073792\n"
"M 36,160,96\nR 182,191,20480\nM 36,164,112\nG 4\nM 40,168,96\nR 160,188,24576\n"
"buffer_load_dwordx4 v221, s[16:19], s63 offen offset:1024 lds\nM 40,172,112\nbuffer_load_dwordx4 a[196:199], v252, s[20:23], s65 offen sc1\nM 56,168,100\nR 162,188,28672\nM 56,172,116\n"
"s_add_u32 s67, s69, 23188480\nM 72,168,104\nR 164,188,32768\nM 72,172,120\nbuffer_load_dwordx4 a[200:203], v252, s[20:23], s66 offen sc1\nM 88,168,108\n"
"R 166,188,36864\nM 88,172,124\nG 4\nM 92,176,108\nR 168,189,24576\nM 92,180,124\n"
"s_add_u32 s64, s68, 23080960\nM 76,176,104\nR 170,189,28672\nM 76,180,120\nbuffer_load_dwordx4 a[204:207], v252, s[20:23], s67 offen sc1\nM 60,176,100\n"
"R 172,189,32768\nM 60,180,116\nM 44,176,96\nR 174,189,36864\nM 44,180,112\nG 4\n"
"M 32,160,128\nR 176,190,24576\nM 32,164,144\nbuffer_load_dwordx4 a[208:211], v252, s[20:23], s64 offen sc1\nM 48,160,132\nR 178,190,28672\n"
"M 48,164,148\ns_add_u32 s65, s68, 23195648\nM 64,160,136\nR 180,190,32768\nM 64,164,152\ns_add_u32 s66, s69, 23080960\n"
"M 80,160,140\nR 182,190,36864\nM 80,164,156\nG 4\nM 84,168,140\nR 160,191,24576\n"
"M 84,172,156\nbuffer_load_dwordx4 a[212:215], v252, s[20:23], s65 offen sc1\nM 68,168,136\nR 162,191,28672\nM 68,172,152\ns_add_u32 s67, s69, 23195648\n"
"M 52,168,132\nR 164,191,32768\nM 52,172,148\nbuffer_load_dwordx4 a[216:219], v252, s[20:23], s66 offen sc1\nM 36,168,128\nR 166,191,36864\n"
"M 36,172,144\nG 4\nM 40,176,128\nR 168,188,40960\nM 40,180,144\nM 56,176,132\n"
"R 170,188,45056\nM 56,180,148\nbuffer_load_dwordx4 a[220:223], v252, s[20:23], s67 offen sc1\nM 72,176,136\nR 172,188,49152\ns_add_u32 s63, s58, 262144\n"
"M 72,180,152\ns_add_u32 m0, s50, 49152\nM 88,176,140\nR 174,188,53248\nM 88,180,156\nG 4\n"
"M 92,160,140\nR 176,189,40960\nM 92,164,156\nbuffer_load_dwordx4 v220, s[16:19], s63 offen lds\nM 76,160,136\nR 178,189,45056\n"
"M 76,164,152\nM 60,160,132\nR 180,189,49152\nM 60,164,148\nbuffer_load_dwordx4 v221, s[16:19], s63 offen offset:1024 lds\nM 44,160,128\n"
"R 182,189,53248\nM 44,164,144\ns_waitcnt vmcnt(9)\ns_barrier\nG 4\nM 96,168,160\n"
"R 160,190,40960\nM 96,172,176\ns_add_u32 m0, s50, 57344\nM 112,168,164\nR 162,190,45056\nM 112,172,180\n"
"s_add_u32 s63, s58, 786432\nM 128,168,168\nR 164,190,49152\nv_lshrrev_b32 v230, 8, v252\nM 128,172,184\nbuffer_load_dwordx4 v220, s[16:19], s63 offen lds\n"
"M 144,168,172\nR 166,190,53248\nv_lshlrev_b32 v230, 3, v230\nM 144,172,188\nG 4\nM 148,176,172\n"
"R 168,191,40960\nM 148,180,188\nbuffer_load_dwordx2 v[232:233], v230, s[36:39], s53 offen offset:0\nM 132,176,168\nR 170,191,45056\nM 132,180,184\n"
"buffer_load_dwordx2 v[234:235], v230, s[36:39], s53 offen offset:32\nM 116,176,164\nR 172,191,49152\nM 116,180,180\nbuffer_load_dwordx4 v221, s[16:19], s63 offen offset:1024 lds\nM 100,176,160\n"
"R 174,191,53248\nM 100,180,176\nG 4\nM 104,160,160\nR 176,184,0\nM 104,164,176\n"
"s_add_u32 m0, s50, 65536\nM 120,160,164\nR 178,184,4096\nM 120,164,180\ns_add_u32 s63, s58, 327680\nM 136,160,168\n"
"R 180,184,8192\nbuffer_load_dwordx2 v[236:237], v230, s[36:39], s53 offen offset:64\nM 136,164,184\nbuffer_load_dwordx4 v220, s[16:19], s63 offen lds\nM 152,160,172\nR 182,184,12288\n"
"M 152,164,188\nbuffer_load_dwordx2 v[238:239], v230, s[36:39], s53 offen offset:96\nG 4\nM 156,168,172\nR 160,185,0\nv_bfe_u32 v242, v252, 4, 4\n"
"M 156,172,188\nv_lshrrev_b32 v243, 8, v252\nv_lshlrev_b32 v243, 2, v243\nM 140,168,168\nR 162,185,4096\nv_sub_u32 v242, v242, v243\n"
"M 140,172,184\nv_lshlrev_b32 v243, 4, v242\ns_mov_b64 s[60:61], 0x3f80\nv_lshlrev_b64 v[208:209], v243, s[60:61]\nM 124,168,164\nR 164,185,8192\n"
"M 124,172,180\nbuffer_load_dwordx4 v221, s[16:19], s63 offen offset:1024 lds\nM 108,168,160\nR 166,185,12288\nM 108,172,176\nG 4\n"
"M 96,176,192\nR 168,186,0\ns_waitcnt vmcnt(13)\nM 96,180,208\ns_add_u32 m0, s50, 73728\nM 112,176,196\n"
"R 170,186,4096\ns_waitcnt vmcnt(12)\nM 112,180,212\ns_add_u32 s63, s58, 851968\nM 128,176,200\nR 172,186,8192\n"
"s_waitcnt vmcnt(11)\nM 128,180,216\nbuffer_load_dwordx4 v220, s[16:19], s63 offen lds\nM 144,176,204\nR 174,186,12288\ns_waitcnt vmcnt(11)\n"
"M 144,180,220\nG 4\nM 148,160,204\nR 176,187,0\nM 148,164,220\nv_cmp_gt_u32 s[60:61], 4, v242\n"
"s_nop 1\nv_cndmask_b32 v212, 0, v208, s[60:61]\nv_cndmask_b32 v208, 0, v208, s[60:61]\nv_cndmask_b32 v213, 0, v209, s[60:61]\nv_cndmask_b32 v209, 0, v209, s[60:61]\nM 132,160,200\n"
"R 178,187,4096\nM 132,164,216\nM 116,160,196\nR 180,187,8192\nM 116,164,212\nbuffer_load_dwordx4 v221, s[16:19], s63 offen offset:1024 lds\n"
"M 100,160,192\nR 182,187,12288\nM 100,164,208\nG 4\nM 104,168,192\nR 160,184,16384\n"
"M 104,172,208\ns_add_u32 m0, s50, 81920\nM 120,168,196\nR 162,184,20480\nM 120,172,212\ns_add_u32 s63, s59, 0\n"
"M 136,168,200\nR 164,184,24576\nM 136,172,216\nbuffer_load_dwordx4 v220, s[16:19], s63 offen lds\nM 152,168,204\nR 166,184,28672\n"
"M 152,172,220\nG 4\nM 156,176,204\nR 168,185,16384\nM 156,180,220\nM 140,176,200\n"
"R 170,185,20480\nM 140,180,216\nbuffer_load_dwordx4 v221, s[16:19], s63 offen offset:1024 lds\nM 124,176,196\nR 172,185,24576\nM 124,180,212\n"
"M 108,176,192\nR 174,185,28672\nM 108,180,208\nG 4\nM 0,160,160\nR 176,186,16384\n"
"M 0,164,176\ns_mov_b32 m0, s51\nM 16,160,164\nR 178,186,20480\nds_write_addtid_b32 v128 offset:0\nM 16,164,180\n"
"ds_write_addtid_b32 v129 offset:256\nds_write_addtid_b32 v130 offset:512\nv_mfma_f32_16x16x32_bf16 a[224:227], v[160:163], a[168:171], a[224:227]\nR 180,186,24576\nds_write_addtid_b32 v131 offset:768\nv_mfma_f32_16x16x32_bf16 a[224:227], v[164:167], a[184:187], a[224:227]\n"
"ds_write_addtid_b32 v132 offset:1024\nds_write_addtid_b32 v133 offset:1280\nv_mfma_f32_16x16x32_bf16 a[240:243], v[160:163], a[172:175], a[240:243]\nR 182,186,28672\nds_write_addtid_b32 v134 offset:1536\nv_mfma_f32_16x16x32_bf16 a[240:243], v[164:167], a[188:191], a[240:243]\n"
"ds_write_addtid_b32 v135 offset:1792\nds_write_addtid_b32 v136 offset:2048\nG 13\nv_mfma_f32_16x16x32_bf16 a[244:247], v[168:171], a[172:175], a[244:247]\nR 160,187,16384\nds_write_addtid_b32 v137 offset:2304\n"
"v_mfma_f32_16x16x32_bf16 a[244:247], v[172:175], a[188:191], a[244:247]\nds_write_addtid_b32 v138 offset:2560\nds_write_addtid_b32 v139 offset:2816\nv_mfma_f32_16x16x32_bf16 a[228:231], v[168:171], a[168:171], a[228:231]\nR 162,187,20480\nds_write_addtid_b32 v140 offset:3072\n"
"v_mfma_f32_16x16x32_bf16 a[228:231], v[172:175], a[184:187], a[228:231]\nds_write_addtid_b32 v141 offset:3328\nds_write_addtid_b32 v142 offset:3584\nM 20,168,164\nR 164,187,24576\nds_write_addtid_b32 v143 offset:3840\n"
"M 20,172,180\nds_write_addtid_b32 v144 offset:4096\nds_write_addtid_b32 v145 offset:4352\nM 4,168,160\nR 166,187,28672\nds_write_addtid_b32 v146 offset:4608\n"
"M 4,172,176\nds_write_addtid_b32 v147 offset:4864\nds_write_addtid_b32 v148 offset:5120\nG 15\nM 8,176,160\nR 168,184,32768\n"
"ds_write_addtid_b32 v149 offset:5376\nM 8,180,176\nds_write_addtid_b32 v150 offset:5632\nds_write_addtid_b32 v151 offset:5888\nM 24,176,164\nR 170,184,36864\n"
"ds_write_addtid_b32 v152 offset:6144\nM 24,180,180\nds_write_addtid_b32 v153 offset:6400\nds_write_addtid_b32 v154 offset:6656\nv_mfma_f32_16x16x32_bf16 a[232:235], v[176:179], a[168:171], a[232:235]\nR 172,184,40960\n"
"ds_write_addtid_b32 v155 offset:6912\nv_mfma_f32_16x16x32_bf16 a[232:235], v[180:183], a[184:187], a[232:235]\nds_write_addtid_b32 v156 offset:7168\nds_write_addtid_b32 v157 offset:7424\nds_write_addtid_b32 v158 offset:7680\nds_write_addtid_b32 v159 offset:7936\n"
"v_mfma_f32_16x16x32_bf16 a[248:251], v[176:179], a[172:175], a[248:251]\nR 174,184,45056\ns_add_u32 m0, s50, 90112\nv_mfma_f32_16x16x32_bf16 a[248:251], v[180:183], a[188:191], a[248:251]\ns_add_u32 s63, s59, 524288\nG 15\n"
"v_mfma_f32_16x16x32_bf16 a[252:255], v[160:163], a[172:175], a[252:255]\nR 176,185,32768\ns_waitcnt vmcnt(8) lgkmcnt(2)\ns_barrier\nv_mfma_f32_16x16x32_bf16 a[252:255], v[164:167], a[188:191], a[252:255]\nbuffer_load_dwordx4 v220, s[16:19], s63 offen lds\n"
"ds_read2st64_b32 v[128:129], v225 offset0:0 offset1:1\nv_mfma_f32_16x16x32_bf16 a[236:239], v[160:163], a[168:171], a[236:239]\nR 178,185,36864\nds_read2st64_b32 v[130:131], v225 offset0:2 offset1:3\nv_mfma_f32_16x16x32_bf16 a[236:239], v[164:167], a[184:187], a[236:239]\nds_read2st64_b32 v[132:133], v225 offset0:4 offset1:5\n"
"ds_read2st64_b32 v[134:135], v225 offset0:6 offset1:7\nM 28,160,164\nR 180,185,40960\nds_read2st64_b32 v[136:137], v225 offset0:8 offset1:9\nM 28,164,180\nbuffer_load_dwordx4 v221, s[16:19], s63 offen offset:1024 lds\n"
"ds_read2st64_b32 v[138:139], v225 offset0:10 offset1:11\nM 12,160,160\nR 182,185,45056\ns_add_u32 m0, s50, 98304\nds_read2st64_b32 v[140:141], v225 offset0:12 offset1:13\nM 12,164,176\n"
"ds_read2st64_b32 v[142:143], v225 offset0:14 offset1:15\nG 12\nG 10\nv_add_f32 v96, v96, v128\nM 0,168,192\nR 160,186,32768\n"
"s_add_u32 s63, s59, 65536\nv_add_f32 v97, v97, v129\nM 0,172,208\nbuffer_load_dwordx4 v220, s[16:19], s63 offen lds\nG 9\nv_add_f32 v98, v98, v130\n"
"M 16,168,196\nR 162,186,36864\nv_add_f32 v99, v99, v131\nG 9\nv_add_f32 v100, v100, v132\nM 16,172,212\n"
"ds_read2st64_b32 v[144:145], v225 offset0:16 offset1:17\nv_add_f32 v101, v101, v133\nG 9\nv_add_f32 v102, v102, v134\nv_mfma_f32_16x16x32_bf16 a[224:227], v[168:171], a[200:203], a[224:227]\nR 164,186,40960\n"
"v_add_f32 v103, v103, v135\nG 8\nv_add_f32 v104, v104, v136\nv_mfma_f32_16x16x32_bf16 a[224:227], v[172:175], a[216:219], a[224:227]\nbuffer_load_dwordx4 v221, s[16:19], s63 offen offset:1024 lds\nv_add_f32 v105, v105, v137\n"
"v_mfma_f32_16x16x32_bf16 a[240:243], v[168:171], a[204:207], a[240:243]\nR 166,186,45056\ns_add_u32 m0, s50, 106496\nG 8\nv_add_f32 v106, v106, v138\nv_mfma_f32_16x16x32_bf16 a[240:243], v[172:175], a[220:223], a[240:243]\n"
"ds_read2st64_b32 v[146:147], v225 offset0:18 offset1:19\nG 8\nv_add_f32 v107, v107, v139\nv_mfma_f32_16x16x32_bf16 a[244:247], v[176:179], a[204:207], a[244:247]\nR 168,187,32768\ns_add_u32 s63, s59, 589824\n"
"G 8\nv_add_f32 v108, v108, v140\nv_mfma_f32_16x16x32_bf16 a[244:247], v[180:183], a[220:223], a[244:247]\nbuffer_load_dwordx4 v220, s[16:19], s63 offen lds\nv_add_f32 v109, v109, v141\nv_mfma_f32_16x16x32_bf16 a[228:231], v[176:179], a[200:203], a[228:231]\n"
"R 170,187,36864\nG 8\nv_add_f32 v110, v110, v142\nv_add_f32 v111, v111, v143\nv_mfma_f32_16x16x32_bf16 a[228:231], v[180:183], a[216:219], a[228:231]\nds_read2st64_b32 v[148:149], v225 offset0:20 offset1:21\n"
"G 6\nv_add_f32 v112, v112, v144\nv_add_f32 v113, v113, v145\nM 20,176,196\nR 172,187,40960\nds_read2st64_b32 v[150:151], v225 offset0:22 offset1:23\n"
"M 20,180,212\nds_read2st64_b32 v[152:153], v225 offset0:24 offset1:25\nG 6\nv_add_f32 v114, v114, v146\nv_add_f32 v115, v115, v147\nM 4,176,192\n"
"R 174,187,45056\nds_read2st64_b32 v[154:155], v225 offset0:26 offset1:27\nM 4,180,208\nds_read2st64_b32 v[156:157], v225 offset0:28 offset1:29\nds_read2st64_b32 v[158:159], v225 offset0:30 offset1:31\nM 8,160,192\n"
"R 176,184,49152\nM 8,164,208\nbuffer_load_dwordx4 v221, s[16:19], s63 offen offset:1024 lds\nG 8\nv_add_f32 v116, v116, v148\nM 24,160,196\n"
"R 178,184,53248\ns_add_u32 s63, s59, 131072\nv_add_f32 v117, v117, v149\nM 24,164,212\ns_add_u32 m0, s50, 114688\nG 7\n"
"v_add_f32 v118, v118, v150\nv_add_f32 v119, v119, v151\nG 6\nv_add_f32 v120, v120, v152\nv_add_f32 v121, v121, v153\nG 4\n"
"v_add_f32 v122, v122, v154\nv_add_f32 v123, v123, v155\nG 3\nv_add_f32 v124, v124, v156\nv_add_f32 v125, v125, v157\nG 2\n"
"v_add_f32 v126, v126, v158\nv_add_f32 v127, v127, v159\nv_mfma_f32_16x16x32_bf16 a[232:235], v[160:163], a[200:203], a[232:235]\nR 180,184,57344\nv_cvt_pk_bf16_f32 v96, v96, v97\nv_cvt_pk_bf16_f32 v97, v98, v99\n"
"v_mfma_f32_16x16x32_bf16 a[232:235], v[164:167], a[216:219], a[232:235]\nbuffer_load_dwordx4 v220, s[16:19], s63 offen lds\nv_cvt_pk_bf16_f32 v98, v100, v101\nv_mfma_f32_16x16x32_bf16 a[248:251], v[160:163], a[204:207], a[248:251]\nR 182,184,61440\nv_cvt_pk_bf16_f32 v99, v102, v103\n"
"v_cvt_pk_bf16_f32 v104, v104, v105\nv_mfma_f32_16x16x32_bf16 a[248:251], v[164:167], a[220:223], a[248:251]\nv_cvt_pk_bf16_f32 v105, v106, v107\nv_cvt_pk_bf16_f32 v106, v108, v109\nv_cvt_pk_bf16_f32 v107, v110, v111\nv_mfma_f32_16x16x32_bf16 a[252:255], v[168:171], a[204:207], a[252:255]\n"
"R 160,185,49152\nv_cvt_pk_bf16_f32 v112, v112, v113\nv_cvt_pk_bf16_f32 v113, v114, v115\nv_mfma_f32_16x16x32_bf16 a[252:255], v[172:175], a[220:223], a[252:255]\nbuffer_load_dwordx4 v221, s[16:19], s63 offen offset:1024 lds\nv_cvt_pk_bf16_f32 v114, v116, v117\n"
"v_mfma_f32_16x16x32_bf16 a[236:239], v[168:171], a[200:203], a[236:239]\nR 162,185,53248\ns_add_u32 s63, s59, 655360\nv_cvt_pk_bf16_f32 v115, v118, v119\nv_mfma_f32_16x16x32_bf16 a[236:239], v[172:175], a[216:219], a[236:239]\ns_add_u32 m0, s50, 0\n"
"v_cvt_pk_bf16_f32 v120, v120, v121\nv_cvt_pk_bf16_f32 v121, v122, v123\nv_cvt_pk_bf16_f32 v122, v124, v125\nM 28,168,196\nR 164,185,57344\ns_bfe_u32 s100, s56, 0x20007\n"
"s_mul_i32 s100, s100, 49152\ns_add_u32 s100, s100, s72\ns_add_u32 s100, s100, 52428800\nv_cvt_pk_bf16_f32 v123, v126, v127\nM 28,172,212\nbuffer_load_dwordx4 v220, s[16:19], s63 offen lds\n"
"buffer_store_dwordx4 v[96:99], v252, s[20:23], s100 offen offset:0 sc1\nM 12,168,192\nR 166,185,61440\nbuffer_store_dwordx4 v[104:107], v252, s[20:23], s100 offen offset:1024 sc1\nM 12,172,208\ns_waitcnt vmcnt(13)\n"
"s_barrier\nbuffer_store_dwordx4 v[112:115], v252, s[20:23], s100 offen offset:2048 sc1\nG 4\nM 32,176,160\nR 168,186,49152\nbuffer_store_dwordx4 v[120:123], v252, s[20:23], s100 offen offset:3072 sc1\n"
"M 32,180,176\nbuffer_load_dwordx4 v221, s[16:19], s63 offen offset:1024 lds\nv_xor_b32 v240, 0x2000, v225\nM 48,176,164\nR 170,186,53248\nds_write2st64_b32 v240, a224, a225 offset0:0 offset1:1\n"
"M 48,180,180\ns_add_u32 m0, s50, 8192\nds_write2st64_b32 v240, a226, a227 offset0:2 offset1:3\nM 64,176,168\nR 172,186,57344\nds_write2st64_b32 v240, a228, a229 offset0:4 offset1:5\n"
"M 64,180,184\ns_add_u32 s63, s59, 196608\nds_write2st64_b32 v240, a230, a231 offset0:6 offset1:7\nM 80,176,172\nR 174,186,61440\nv_mfma_f32_16x16x32_bf16 v[96:99], v[208:211], v[192:195], 0\n"
"ds_write2st64_b32 v240, a232, a233 offset0:8 offset1:9\nM 80,180,188\nv_mfma_f32_16x16x32_bf16 v[100:103], v[210:213], v[192:195], 0\nds_write2st64_b32 v240, a234, a235 offset0:10 offset1:11\nG 10\nM 84,160,172\n"
"R 176,187,49152\nv_mfma_f32_16x16x32_bf16 v[104:107], v[208:211], v[196:199], 0\nds_write2st64_b32 v240, a236, a237 offset0:12 offset1:13\nM 84,164,188\nv_mfma_f32_16x16x32_bf16 v[108:111], v[210:213], v[196:199], 0\nbuffer_load_dwordx4 v220, s[16:19], s63 offen lds\n"
"ds_write2st64_b32 v240, a238, a239 offset0:14 offset1:15\nM 68,160,168\nR 178,187,53248\nv_mfma_f32_16x16x32_bf16 v[112:115], v[208:211], v[200:203], 0\nds_write2st64_b32 v240, a240, a241 offset0:16 offset1:17\nM 68,164,184\n"
"v_mfma_f32_16x16x32_bf16 v[116:119], v[210:213], v[200:203], 0\nbuffer_load_dwordx4 v221, s[16:19], s63 offen offset:1024 lds\nds_write2st64_b32 v240, a242, a243 offset0:18 offset1:19\nM 52,160,164\nR 180,187,57344\nv_mfma_f32_16x16x32_bf16 v[120:123], v[208:211], v[204:207], 0\n"
"s_add_u32 m0, s50, 16384\ns_add_u32 s63, s59, 720896\nds_write2st64_b32 v240, a244, a245 offset0:20 offset1:21\nM 52,164,180\nv_mfma_f32_16x16x32_bf16 v[124:127], v[210:213], v[204:207], 0\nbuffer_load_dwordx4 v220, s[16:19], s63 offen lds\n"
"ds_write2st64_b32 v240, a246, a247 offset0:22 offset1:23\nM 36,160,160\nR 182,187,61440\nv_mfma_f32_16x16x32_bf16 v[96:99], v[208:211], v[232:235], v[96:99]\nds_write2st64_b32 v240, a248, a249 offset0:24 offset1:25\nM 36,164,176\n"
"v_mfma_f32_16x16x32_bf16 v[100:103], v[210:213], v[232:235], v[100:103]\nds_write2st64_b32 v240, a250, a251 offset0:26 offset1:27\nG 14\nM 40,168,160\nR 160,188,0\nv_mfma_f32_16x16x32_bf16 v[104:107], v[208:211], v[236:239], v[104:107]\n"
"ds_write2st64_b32 v240, a252, a253 offset0:28 offset1:29\nM 40,172,176\nv_mfma_f32_16x16x32_bf16 v[108:111], v[210:213], v[236:239], v[108:111]\nbuffer_load_dwordx4 v221, s[16:19], s63 offen offset:1024 lds\nds_write2st64_b32 v240, a254, a255 offset0:30 offset1:31\nM 56,168,164\n"
"R 162,188,4096\nv_mfma_f32_16x16x32_bf16 v[112:115], v[208:211], v[232:235], v[112:115]\ns_add_u32 m0, s50, 24576\ns_add_u32 s63, s59, 393216\nM 56,172,180\nv_mfma_f32_16x16x32_bf16 v[116:119], v[210:213], v[232:235], v[116:119]\n"
"buffer_load_dwordx4 v220, s[16:19], s63 offen lds\nM 72,168,168\nR 164,188,8192\nv_mfma_f32_16x16x32_bf16 v[120:123], v[208:211], v[236:239], v[120:123]\nM 72,172,184\nv_mfma_f32_16x16x32_bf16 v[124:127], v[210:213], v[236:239], v[124:127]\n"
"buffer_load_dwordx4 v221, s[16:19], s63 offen offset:1024 lds\nM 88,168,172\nR 166,188,12288\ns_add_u32 s63, s59, 458752\nM 88,172,188\nG 8\n"
"M 92,176,172\nR 168,189,0\ns_waitcnt vmcnt(18) lgkmcnt(4)\ns_barrier\nM 92,180,188\ns_add_u32 m0, s50, 32768\n"
"ds_read2st64_b32 v[128:129], v225 offset0:0 offset1:1\nds_read2st64_b32 v[130:131], v225 offset0:2 offset1:3\nM 76,176,168\nR 170,189,4096\nds_read2st64_b32 v[132:133], v225 offset0:4 offset1:5\nM 76,180,184\n"
"buffer_load_dwordx4 v220, s[16:19], s63 offen lds\nds_read2st64_b32 v[134:135], v225 offset0:6 offset1:7\nM 60,176,164\nR 172,189,8192\nds_read2st64_b32 v[136:137], v225 offset0:8 offset1:9\nM 60,180,180\n"
"ds_read2st64_b32 v[138:139], v225 offset0:10 offset1:11\nds_read2st64_b32 v[140:141], v225 offset0:12 offset1:13\nM 44,176,160\nR 174,189,12288\nds_read2st64_b32 v[142:143], v225 offset0:14 offset1:15\nM 44,180,176\n"
"ds_read2st64_b32 v[144:145], v225 offset0:16 offset1:17\nG 13\nG 11\nv_add_f32 v0, v0, v128\nM 32,160,192\nR 176,190,0\n"
"v_add_f32 v1, v1, v129\nG 11\nv_add_f32 v2, v2, v130\nM 32,164,208\nbuffer_load_dwordx4 v221, s[16:19], s63 offen offset:1024 lds\nv_add_f32 v3, v3, v131\n"
"M 48,160,196\nR 178,190,4096\nG 10\nv_add_f32 v4, v4, v132\nv_add_f32 v5, v5, v133\nM 48,164,212\n"
"s_add_u32 m0, s50, 40960\nds_read2st64_b32 v[146:147], v225 offset0:18 offset1:19\nG 10\nv_add_f32 v6, v6, v134\nM 64,160,200\nR 180,190,8192\n"
"v_add_f32 v7, v7, v135\nG 9\nv_add_f32 v8, v8, v136\nM 64,164,216\ns_add_u32 s63, s59, 262144\nds_read2st64_b32 v[148:149], v225 offset0:20 offset1:21\n"
"v_add_f32 v9, v9, v137\nM 80,160,204\nR 182,190,12288\nG 10\nv_add_f32 v10, v10, v138\nv_add_f32 v11, v11, v139\n"
"M 80,164,220\nds_read2st64_b32 v[150:151], v225 offset0:22 offset1:23\nG 9\nv_add_f32 v12, v12, v140\nM 84,168,204\nR 160,191,0\n"
"v_add_f32 v13, v13, v141\nG 9\nv_add_f32 v14, v14, v142\nM 84,172,220\nbuffer_load_dwordx4 v220, s[16:19], s63 offen lds\nv_add_f32 v15, v15, v143\n"
"M 68,168,200\nR 162,191,4096\nG 9\nv_add_f32 v16, v16, v144\nv_add_f32 v17, v17, v145\nM 68,172,216\n"
"ds_read2st64_b32 v[152:153], v225 offset0:24 offset1:25\nG 7\nv_add_f32 v18, v18, v146\nv_add_f32 v19, v19, v147\nM 52,168,196\nR 164,191,8192\n"
"G 6\nv_add_f32 v20, v20, v148\nv_add_f32 v21, v21, v149\nM 52,172,212\nds_read2st64_b32 v[154:155], v225 offset0:26 offset1:27\nG 5\n"
"v_add_f32 v22, v22, v150\nv_add_f32 v23, v23, v151\nM 36,168,192\nR 166,191,12288\nds_read2st64_b32 v[156:157], v225 offset0:28 offset1:29\nM 36,172,208\n"
"ds_read2st64_b32 v[158:159], v225 offset0:30 offset1:31\nM 40,176,192\nR 168,188,16384\nM 40,180,208\nbuffer_load_dwordx4 v221, s[16:19], s63 offen offset:1024 lds\nG 6\n"
"v_add_f32 v24, v24, v152\nM 56,176,196\nR 170,188,20480\nv_add_f32 v25, v25, v153\nM 56,180,212\ns_add_u32 m0, s50, 49152\n"
"G 5\nv_add_f32 v26, v26, v154\nv_add_f32 v27, v27, v155\nG 3\nv_add_f32 v28, v28, v156\nv_add_f32 v29, v29, v157\n"
"G 2\nv_add_f32 v30, v30, v158\nv_add_f32 v31, v31, v159\nM 72,176,200\nR 172,188,24576\nv_cvt_pk_bf16_f32 v0, v0, v1\n"
"v_cvt_pk_bf16_f32 v1, v2, v3\nM 72,180,216\ns_add_u32 s63, s59, 786432\nv_cvt_pk_bf16_f32 v2, v4, v5\nv_cvt_pk_bf16_f32 v3, v6, v7\nv_cvt_pk_bf16_f32 v8, v8, v9\n"
"M 88,176,204\nR 174,188,28672\nv_cvt_pk_bf16_f32 v9, v10, v11\nv_cvt_pk_bf16_f32 v10, v12, v13\nM 88,180,220\nv_cvt_pk_bf16_f32 v11, v14, v15\n"
"v_cvt_pk_bf16_f32 v16, v16, v17\nv_cvt_pk_bf16_f32 v17, v18, v19\nM 92,160,204\nR 176,189,16384\nv_cvt_pk_bf16_f32 v18, v20, v21\nv_cvt_pk_bf16_f32 v19, v22, v23\n"
"M 92,164,220\nbuffer_load_dwordx4 v220, s[16:19], s63 offen lds\nv_cvt_pk_bf16_f32 v24, v24, v25\nM 76,160,200\nR 178,189,20480\nv_cvt_pk_bf16_f32 v25, v26, v27\n"
"v_cvt_pk_bf16_f32 v26, v28, v29\nM 76,164,216\ns_bfe_u32 s100, s57, 0x20007\ns_mul_i32 s100, s100, 49152\ns_add_u32 s100, s100, s72\ns_add_u32 s100, s100, 52445184\n"
"v_cvt_pk_bf16_f32 v27, v30, v31\nM 60,160,196\nR 180,189,24576\nbuffer_store_dwordx4 v[0:3], v252, s[20:23], s100 offen offset:0 sc1\nM 60,164,212\nbuffer_load_dwordx4 v221, s[16:19], s63 offen offset:1024 lds\n"
"buffer_store_dwordx4 v[8:11], v252, s[20:23], s100 offen offset:1024 sc1\nM 44,160,192\nR 182,189,28672\nbuffer_store_dwordx4 v[16:19], v252, s[20:23], s100 offen offset:2048 sc1\nM 44,164,208\ns_waitcnt vmcnt(23)\n"
"s_barrier\nG 4\nM 96,168,0\nR 160,190,16384\nbuffer_store_dwordx4 v[24:27], v252, s[20:23], s100 offen offset:3072 sc1\nM 96,172,16\n"
"s_mov_b32 m0, s51\nM 112,168,4\nR 162,190,20480\nds_write_addtid_b32 v64 offset:0\nM 112,172,20\nds_write_addtid_b32 v65 offset:256\n"
"ds_write_addtid_b32 v66 offset:512\nv_mfma_f32_16x16x32_bf16 v[128:131], v[168:171], a[8:11], 0\nR 164,190,24576\nds_write_addtid_b32 v67 offset:768\nM 128,172,24\nds_write_addtid_b32 v68 offset:1024\n"
"ds_write_addtid_b32 v69 offset:1280\nv_mfma_f32_16x16x32_bf16 v[144:147], v[168:171], a[12:15], 0\nR 166,190,28672\nds_write_addtid_b32 v70 offset:1536\nM 144,172,28\nds_write_addtid_b32 v71 offset:1792\n"
"ds_write_addtid_b32 v72 offset:2048\nG 13\nv_mfma_f32_16x16x32_bf16 v[148:151], v[176:179], a[12:15], 0\nR 168,191,16384\nds_write_addtid_b32 v73 offset:2304\nM 148,180,28\n"
"ds_write_addtid_b32 v74 offset:2560\nds_write_addtid_b32 v75 offset:2816\nv_mfma_f32_16x16x32_bf16 v[132:135], v[176:179], a[8:11], 0\nR 170,191,20480\nds_write_addtid_b32 v76 offset:3072\nM 132,180,24\n"
"ds_write_addtid_b32 v77 offset:3328\nds_write_addtid_b32 v78 offset:3584\nM 116,176,4\nR 172,191,24576\nds_write_addtid_b32 v79 offset:3840\nM 116,180,20\n"
"ds_write_addtid_b32 v80 offset:4096\nds_write_addtid_b32 v81 offset:4352\nM 100,176,0\nR 174,191,28672\nds_write_addtid_b32 v82 offset:4608\nM 100,180,16\n"
"ds_write_addtid_b32 v83 offset:4864\nds_write_addtid_b32 v84 offset:5120\nG 15\nM 104,160,0\nR 176,188,32768\nds_write_addtid_b32 v85 offset:5376\n"
"M 104,164,16\nds_write_addtid_b32 v86 offset:5632\nds_write_addtid_b32 v87 offset:5888\nM 120,160,4\nR 178,188,36864\nds_write_addtid_b32 v88 offset:6144\n"
"M 120,164,20\nds_write_addtid_b32 v89 offset:6400\nds_write_addtid_b32 v90 offset:6656\nv_mfma_f32_16x16x32_bf16 v[136:139], v[160:163], a[8:11], 0\nR 180,188,40960\nds_write_addtid_b32 v91 offset:6912\n"
"M 136,164,24\nds_write_addtid_b32 v92 offset:7168\nds_write_addtid_b32 v93 offset:7424\nds_write_addtid_b32 v94 offset:7680\nds_write_addtid_b32 v95 offset:7936\nv_mfma_f32_16x16x32_bf16 v[152:155], v[160:163], a[12:15], 0\n"
"R 182,188,45056\nM 152,164,28\nG 15\nv_mfma_f32_16x16x32_bf16 v[156:159], v[168:171], a[12:15], 0\nR 160,189,32768\ns_waitcnt vmcnt(16) lgkmcnt(2)\n"
"s_barrier\nM 156,172,28\nds_read2st64_b32 v[64:65], v225 offset0:0 offset1:1\nds_read2st64_b32 v[66:67], v225 offset0:2 offset1:3\nv_mfma_f32_16x16x32_bf16 v[140:143], v[168:171], a[8:11], 0\nR 162,189,36864\n"
"ds_read2st64_b32 v[68:69], v225 offset0:4 offset1:5\nM 140,172,24\nds_read2st64_b32 v[70:71], v225 offset0:6 offset1:7\nds_read2st64_b32 v[72:73], v225 offset0:8 offset1:9\nM 124,168,4\nR 164,189,40960\n"
"ds_read2st64_b32 v[74:75], v225 offset0:10 offset1:11\nM 124,172,20\nds_read2st64_b32 v[76:77], v225 offset0:12 offset1:13\nds_read2st64_b32 v[78:79], v225 offset0:14 offset1:15\nM 108,168,0\nR 166,189,45056\n"
"ds_read2st64_b32 v[80:81], v225 offset0:16 offset1:17\nM 108,172,16\nds_read2st64_b32 v[82:83], v225 offset0:18 offset1:19\nG 14\nG 12\nv_add_f32 v32, v32, v64\n"
"M 96,176,32\nR 168,190,32768\nv_add_f32 v33, v33, v65\nG 12\nv_add_f32 v34, v34, v66\nM 96,180,48\n"
"ds_read2st64_b32 v[84:85], v225 offset0:20 offset1:21\nv_add_f32 v35, v35, v67\nG 11\nv_add_f32 v36, v36, v68\nM 112,176,36\nR 170,190,36864\n"
"v_add_f32 v37, v37, v69\nG 11\nv_add_f32 v38, v38, v70\nM 112,180,52\nds_read2st64_b32 v[86:87], v225 offset0:22 offset1:23\nv_add_f32 v39, v39, v71\n"
"G 11\nv_add_f32 v40, v40, v72\nM 128,176,40\nR 172,190,40960\nv_add_f32 v41, v41, v73\nG 10\n"
"v_add_f32 v42, v42, v74\nM 128,180,56\nds_read2st64_b32 v[88:89], v225 offset0:24 offset1:25\nv_add_f32 v43, v43, v75\nG 10\nv_add_f32 v44, v44, v76\n"
"M 144,176,44\nR 174,190,45056\nv_add_f32 v45, v45, v77\nG 10\nv_add_f32 v46, v46, v78\nM 144,180,60\n"
"ds_read2st64_b32 v[90:91], v225 offset0:26 offset1:27\nG 10\nv_add_f32 v47, v47, v79\nM 148,160,44\nR 176,191,32768\nG 10\n"
"v_add_f32 v48, v48, v80\nv_add_f32 v49, v49, v81\nM 148,164,60\nds_read2st64_b32 v[92:93], v225 offset0:28 offset1:29\nG 10\nv_add_f32 v50, v50, v82\n"
"v_add_f32 v51, v51, v83\nM 132,160,40\nR 178,191,36864\nG 9\nv_add_f32 v52, v52, v84\nv_add_f32 v53, v53, v85\n"
"M 132,164,56\nds_read2st64_b32 v[94:95], v225 offset0:30 offset1:31\nG 8\nv_add_f32 v54, v54, v86\nv_add_f32 v55, v55, v87\nM 116,160,36\n"
"R 180,191,40960\nG 7\nv_add_f32 v56, v56, v88\nv_add_f32 v57, v57, v89\nM 116,164,52\nG 5\n"
"v_add_f32 v58, v58, v90\nv_add_f32 v59, v59, v91\nM 100,160,32\nR 182,191,45056\nv_mov_b32 v226, s46\nM 100,164,48\n"
"v_mov_b32 v227, s47\nG 4\nv_add_f32 v60, v60, v92\nM 104,168,32\nR 160,188,49152\ns_bfe_u32 s62, s56, 0x20007\n"
"s_lshl_b32 s62, s62, 5\ns_add_u32 s61, s74, 548864\ns_add_u32 s61, s61, s62\ns_waitcnt vmcnt(0)\ns_mov_b64 exec, 1\nbuffer_atomic_swap_x2 v[226:227], off, s[20:23], s61 sc1\n"
"s_mov_b64 exec, -1\nM 104,172,48\ns_bfe_u32 s62, s57, 0x20007\ns_lshl_b32 s62, s62, 5\ns_add_u32 s61, s74, 548872\ns_add_u32 s61, s61, s62\n"
"s_mov_b64 exec, 1\nbuffer_atomic_swap_x2 v[226:227], off, s[20:23], s61 sc1\ns_mov_b64 exec, -1\nM 120,168,36\nR 162,188,53248\nv_add_f32 v61, v61, v93\n"
"M 120,172,52\ns_add_u32 m0, s50, 57344\nG 4\nv_add_f32 v62, v62, v94\nv_add_f32 v63, v63, v95\nM 136,168,40\n"
"R 164,184,0\nv_cvt_pk_bf16_f32 v32, v32, v33\nv_cvt_pk_bf16_f32 v33, v34, v35\nM 136,172,56\ns_add_u32 s63, s59, 327680\nv_cvt_pk_bf16_f32 v34, v36, v37\n"
"v_cvt_pk_bf16_f32 v35, v38, v39\nv_cvt_pk_bf16_f32 v40, v40, v41\nM 152,168,44\nR 166,184,4096\nv_cvt_pk_bf16_f32 v41, v42, v43\nv_cvt_pk_bf16_f32 v42, v44, v45\n"
"M 152,172,60\nG 4\nv_cvt_pk_bf16_f32 v43, v46, v47\nv_cvt_pk_bf16_f32 v48, v48, v49\nv_cvt_pk_bf16_f32 v49, v50, v51\nM 156,176,44\n"
"R 168,189,49152\nv_cvt_pk_bf16_f32 v50, v52, v53\nv_cvt_pk_bf16_f32 v51, v54, v55\nM 156,180,60\nbuffer_load_dwordx4 v220, s[16:19], s63 offen lds\nv_cvt_pk_bf16_f32 v56, v56, v57\n"
"M 140,176,40\nR 170,189,53248\nv_cvt_pk_bf16_f32 v57, v58, v59\nv_cvt_pk_bf16_f32 v58, v60, v61\nM 140,180,56\nbuffer_load_dwordx4 v221, s[16:19], s63 offen offset:1024 lds\n"
"v_cvt_pk_bf16_f32 v59, v62, v63\nM 124,176,36\nR 172,185,0\ns_add_u32 s63, s59, 851968\ns_bfe_u32 s100, s58, 0x20007\ns_mul_i32 s100, s100, 49152\n"
"s_add_u32 s100, s100, s72\ns_add_u32 s100, s100, 52461568\nM 124,180,52\ns_add_u32 m0, s50, 65536\nbuffer_store_dwordx4 v[32:35], v252, s[20:23], s100 offen offset:0 sc1\nM 108,176,32\n"
"R 174,185,4096\nbuffer_store_dwordx4 v[40:43], v252, s[20:23], s100 offen offset:1024 sc1\nM 108,180,48\ns_barrier\nbuffer_store_dwordx4 v[48:51], v252, s[20:23], s100 offen offset:2048 sc1\nG 4\n"
"M 96,160,64\nR 176,190,49152\nbuffer_store_dwordx4 v[56:59], v252, s[20:23], s100 offen offset:3072 sc1\nM 96,164,80\nbuffer_load_dwordx4 v220, s[16:19], s63 offen lds\nM 112,160,68\n"
"R 178,190,53248\nM 112,164,84\nbuffer_load_dwordx4 v221, s[16:19], s63 offen offset:1024 lds\nM 128,160,72\nR 180,186,0\nM 128,164,88\n"
"M 144,160,76\nR 182,186,4096\nM 144,164,92\nG 4\nM 148,168,76\nR 160,191,49152\n"
"Lq588:\ns_lshr_b32 s60, s45, 3\ns_lshl2_add_u32 s60, s60, s40\ns_lshl_b32 s60, s60, 8\nv_lshl_add_u32 v224, v252, 3, s60\nv_add_u32 v224, Lq588+4096-LqPC, v224\n"
"s_mov_b64 exec, 3\nglobal_load_dword v224, v224, s[98:99]\ns_mov_b64 exec, -1\nM 148,172,92\nv_bfe_u32 v250, v252, 4, 2\nv_lshlrev_b32 v250, 3, v250\n"
"M 132,168,72\nR 162,191,53248\nM 132,172,88\ns_lshl_b32 s60, s43, 5\nv_add_u32 v250, s60, v250\nM 116,168,68\n"
"R 164,187,0\nM 116,172,84\nM 100,168,64\nR 166,187,4096\nM 100,172,80\nG 4\n"
"M 104,176,64\nR 168,184,8192\nM 104,180,80\nM 120,176,68\nR 170,184,12288\nM 120,180,84\n"
"M 136,176,72\nR 172,184,16384\nM 136,180,88\nM 152,176,76\nR 174,184,20480\nM 152,180,92\n"
"G 4\nM 156,160,76\nR 176,185,8192\nM 156,164,92\nM 140,160,72\nR 178,185,12288\n"
"M 140,164,88\nM 124,160,68\nR 180,185,16384\nM 124,164,84\nM 108,160,64\nR 182,185,20480\n"
"M 108,164,80\nG 4\nM 96,168,96\nR 160,186,8192\nM 96,172,112\nM 112,168,100\n"
"R 162,186,12288\nM 112,172,116\nM 128,168,104\nR 164,186,16384\nM 128,172,120\nM 144,168,108\n"
"R 166,186,20480\nM 144,172,124\nG 4\nM 148,176,108\nR 168,187,8192\nM 148,180,124\n"
"M 132,176,104\nR 170,187,12288\nM 132,180,120\nM 116,176,100\nR 172,187,16384\nM 116,180,116\n"
"M 100,176,96\nR 174,187,20480\nv_mov_b32 v226, s46\nM 100,180,112\nG 4\nv_mov_b32 v227, s47\n"
"M 104,160,96\nR 176,184,24576\ns_bfe_u32 s62, s58, 0x20007\ns_lshl_b32 s62, s62, 5\ns_add_u32 s61, s74, 548880\ns_add_u32 s61, s61, s62\n"
"s_waitcnt vmcnt(0)\ns_mov_b64 exec, 1\nbuffer_atomic_swap_x2 v[226:227], off, s[20:23], s61 sc1\ns_mov_b64 exec, -1\ns_barrier\nM 104,164,112\n"
"M 120,160,100\nR 178,184,28672\nM 120,164,116\ns_movk_i32 s63, 6144\nv_mad_u32_u24 v220, v228, s63, v229\nv_add_u32 v221, 3144704, v220\n"
"s_add_u32 m0, s50, 73728\ns_add_u32 s63, s52, 2048\nbuffer_load_dwordx4 v220, s[4:7], s63 offen lds\nM 136,160,104\nR 180,184,32768\nv_mov_b32 v248, 0\n"
"M 136,164,120\nv_mov_b32 v249, 0\nM 152,160,108\nR 182,184,36864\ns_add_u32 s61, s74, 548864\ns_mov_b64 exec, 3\n"
"buffer_atomic_or_x2 v[248:249], v250, s[20:23], s61 offen sc0 sc1\ns_mov_b64 exec, -1\nM 152,164,124\nG 4\nM 156,168,108\nR 160,185,24576\n"
"M 156,172,124\nM 140,168,104\nR 162,185,28672\nM 140,172,120\nM 124,168,100\nR 164,185,32768\n"
"M 124,172,116\nbuffer_load_dwordx4 v221, s[4:7], s63 offen offset:1024 lds\nM 108,168,96\nR 166,185,36864\ns_add_u32 m0, s50, 81920\nM 108,172,112\n"
"G 4\nM 96,176,128\nR 168,186,24576\ns_add_u32 s63, s52, 198656\nM 96,180,144\nbuffer_load_dwordx4 v220, s[4:7], s63 offen lds\n"
"M 112,176,132\nR 170,186,28672\nM 112,180,148\nM 128,176,136\nR 172,186,32768\ns_lshr_b32 s60, s45, 3\n"
"s_lshl2_add_u32 s60, s60, s40\ns_lshl_b32 s60, s60, 8\nv_lshl_add_u32 v224, v252, 3, s60\nv_add_u32 v224, LqM-LqPC, v224\ns_mov_b64 exec, 3\nglobal_load_dword v224, v224, s[98:99]\n"
"s_mov_b64 exec, -1\nM 128,180,152\nM 144,176,140\nR 174,186,36864\nM 144,180,156\nG 4\n"
"M 148,160,140\nR 176,187,24576\nM 148,164,156\nbuffer_load_dwordx4 v221, s[4:7], s63 offen offset:1024 lds\nM 132,160,136\nR 178,187,28672\n"
"s_add_u32 s63, s52, 4096\nM 132,164,152\ns_add_u32 m0, s50, 90112\nM 116,160,132\nR 180,187,32768\nM 116,164,148\n"
"buffer_load_dwordx4 v220, s[4:7], s63 offen lds\nM 100,160,128\nR 182,187,36864\nM 100,164,144\nG 4\nM 104,168,128\n"
"R 160,184,40960\nM 104,172,144\nM 120,168,132\nR 162,184,45056\nM 120,172,148\nbuffer_load_dwordx4 v221, s[4:7], s63 offen offset:1024 lds\n"
"M 136,168,136\nR 164,184,49152\nM 136,172,152\ns_mul_i32 s64, s43, 49152\nM 152,168,140\nR 166,184,53248\n"
"M 152,172,156\nG 4\nM 156,176,140\nR 168,185,40960\ns_barrier\ns_waitcnt vmcnt(6)\n"
"v_cmp_eq_u64 s[86:87], s[46:47], v[248:249]\nM 156,180,156\ns_add_u32 s64, s64, s72\nM 140,176,136\nR 170,185,45056\ns_and_b32 s62, s86, 3\n"
"s_cmp_eq_u32 s62, 3\ns_cbranch_scc0 Lq513\nLq514:\nv_mov_b32 v248, 0\nM 140,180,152\ns_add_u32 s64, s64, 52428800\n"
"v_mov_b32 v249, 0\nM 124,176,132\nR 172,185,49152\ns_add_u32 s61, s74, 548864\ns_mov_b64 exec, 4\nbuffer_atomic_or_x2 v[248:249], v250, s[20:23], s61 offen sc0 sc1\n"
"s_mov_b64 exec, -1\nM 124,180,148\nbuffer_load_dwordx4 v[0:3], v252, s[20:23], s64 offen offset:0 sc1\nM 108,176,128\nR 174,185,53248\nM 108,180,144\n"
"buffer_load_dwordx4 v[4:7], v252, s[20:23], s64 offen offset:1024 sc1\nG 4\nM 128,160,168\nR 176,186,40960\ns_mul_i32 s65, s43, 49152\nM 128,164,184\n"
"R 178,186,45056\ns_add_u32 s65, s65, s72\nM 144,160,172\nR 180,186,49152\nbuffer_load_dwordx4 v[8:11], v252, s[20:23], s64 offen offset:2048 sc1\nM 144,164,188\n"
"R 182,186,53248\nG 4\nM 148,168,172\nR 160,187,40960\ns_add_u32 s65, s65, 52445184\nM 148,172,188\n"
"R 162,187,45056\nbuffer_load_dwordx4 v[12:15], v252, s[20:23], s64 offen offset:3072 sc1\nM 132,168,168\nR 164,187,49152\nM 132,172,184\nR 166,187,53248\n"
"G 4\nM 136,176,168\nR 168,184,57344\nbuffer_load_dwordx4 v[16:19], v252, s[20:23], s65 offen offset:0 sc1\nM 136,180,184\nR 170,184,61440\n"
"M 152,176,172\nR 172,188,0\nM 152,180,188\nR 174,188,4096\nG 4\nM 156,160,172\n"
"R 176,185,57344\nbuffer_load_dwordx4 v[20:23], v252, s[20:23], s65 offen offset:1024 sc1\nM 156,164,188\nR 178,185,61440\nM 140,160,168\nR 180,189,0\n"
"buffer_load_dwordx4 v[24:27], v252, s[20:23], s65 offen offset:2048 sc1\nM 140,164,184\nR 182,189,4096\nG 4\nM 128,168,200\nR 160,186,57344\n"
"M 128,172,216\nR 162,186,61440\nbuffer_load_dwordx4 v[28:31], v252, s[20:23], s65 offen offset:3072 sc1\nM 144,168,204\nR 164,190,0\nM 144,172,220\n"
"R 166,190,4096\nG 4\nM 148,176,204\nR 168,187,57344\nM 148,180,220\nR 170,187,61440\n"
"s_add_u32 m0, s50, 98304\nM 132,176,200\nR 172,191,0\ns_add_u32 s63, s52, 200704\nM 132,180,216\nR 174,191,4096\n"
"G 4\nM 136,160,200\nR 176,184,40960\nM 136,164,216\nR 178,184,45056\nM 152,160,204\n"
"R 180,184,49152\ns_mul_i32 s66, s43, 49152\nM 152,164,220\nR 182,184,53248\nbuffer_load_dwordx4 v220, s[4:7], s63 offen lds\nG 4\n"
"M 156,168,204\nR 160,185,40960\ns_add_u32 s66, s66, s72\nM 156,172,220\nR 162,185,45056\ns_add_u32 s66, s66, 52461568\n"
"M 140,168,200\nR 164,185,49152\ns_waitcnt vmcnt(9)\nv_cmp_eq_u64 s[86:87], s[46:47], v[248:249]\nM 140,172,216\nR 166,185,53248\n"
"buffer_load_dwordx4 v221, s[4:7], s63 offen offset:1024 lds\nG 4\nM 96,176,160\nR 168,186,40960\ns_and_b32 s62, s86, 4\ns_cmp_eq_u32 s62, 4\n"
"s_cbranch_scc0 Lq529\nLq530:\nM 96,180,176\nR 170,186,45056\ns_mov_b32 m0, s51\nM 112,176,164\n"
"R 172,186,49152\nbuffer_load_dwordx4 v[64:67], v252, s[20:23], s66 offen offset:0 sc1\nds_write_addtid_b32 v128 offset:0\nM 112,180,180\nR 174,186,53248\nds_write_addtid_b32 v129 offset:256\n"
"G 6\nM 116,160,164\nR 176,187,40960\nbuffer_load_dwordx4 v[68:71], v252, s[20:23], s66 offen offset:1024 sc1\nds_write_addtid_b32 v130 offset:512\nM 116,164,180\n"
"R 178,187,45056\nds_write_addtid_b32 v131 offset:768\nM 100,160,160\nR 180,187,49152\nds_write_addtid_b32 v132 offset:1024\nM 100,164,176\n"
"R 182,187,53248\nds_write_addtid_b32 v133 offset:1280\nG 9\nM 104,168,160\nR 160,184,57344\nbuffer_load_dwordx4 v[72:75], v252, s[20:23], s66 offen offset:2048 sc1\n"
"ds_write_addtid_b32 v134 offset:1536\nM 104,172,176\nR 162,184,61440\nbuffer_load_dwordx4 v[76:79], v252, s[20:23], s66 offen offset:3072 sc1\nds_write_addtid_b32 v135 offset:1792\nM 120,168,164\n"
"R 164,188,0\nds_write_addtid_b32 v136 offset:2048\nM 120,172,180\nR 166,188,4096\nds_write_addtid_b32 v137 offset:2304\nG 9\n"
"M 124,176,164\nR 168,185,57344\nds_write_addtid_b32 v138 offset:2560\nM 124,180,180\nR 170,185,61440\nds_write_addtid_b32 v139 offset:2816\n"
"M 108,176,160\nR 172,189,0\nds_write_addtid_b32 v140 offset:3072\nds_write_addtid_b32 v141 offset:3328\nds_write_addtid_b32 v142 offset:3584\nds_write_addtid_b32 v143 offset:3840\n"
"ds_write_addtid_b32 v144 offset:4096\nds_write_addtid_b32 v145 offset:4352\nds_write_addtid_b32 v146 offset:4608\nds_write_addtid_b32 v147 offset:4864\nds_write_addtid_b32 v148 offset:5120\nds_write_addtid_b32 v149 offset:5376\n"
"ds_write_addtid_b32 v150 offset:5632\nds_write_addtid_b32 v151 offset:5888\nds_write_addtid_b32 v152 offset:6144\nds_write_addtid_b32 v153 offset:6400\nds_write_addtid_b32 v154 offset:6656\nds_write_addtid_b32 v155 offset:6912\n"
"ds_write_addtid_b32 v156 offset:7168\nds_write_addtid_b32 v157 offset:7424\nds_write_addtid_b32 v158 offset:7680\nds_write_addtid_b32 v159 offset:7936\nM 108,180,176\nR 174,189,4096\n"
"s_add_u32 m0, s50, 106496\ns_add_u32 s63, s52, 395264\nG 15\nM 96,160,192\nR 176,186,57344\nbuffer_load_dwordx4 v220, s[4:7], s63 offen lds\n"
"M 96,164,208\nR 178,186,61440\nG 3\ns_barrier\nM 112,160,196\nR 180,190,0\n"
"ds_read2st64_b32 v[128:129], v225 offset0:0 offset1:1\nM 112,164,212\nR 182,190,4096\nds_read2st64_b32 v[130:131], v225 offset0:2 offset1:3\nG 6\nM 116,168,196\n"
"R 160,187,57344\nds_read2st64_b32 v[132:133], v225 offset0:4 offset1:5\nM 116,172,212\nR 162,187,61440\nbuffer_load_dwordx4 v221, s[4:7], s63 offen offset:1024 lds\nds_read2st64_b32 v[134:135], v225 offset0:6 offset1:7\n"
"M 100,168,192\nR 164,191,0\ns_add_u32 m0, s50, 114688\ns_add_u32 s63, s52, 591872\nM 100,172,208\nR 166,191,4096\n"
"G 7\nM 104,176,192\nbuffer_load_dwordx4 v220, s[4:7], s63 offen lds\nM 104,180,208\nds_read2st64_b32 v[136:137], v225 offset0:8 offset1:9\nds_read2st64_b32 v[138:139], v225 offset0:10 offset1:11\n"
"M 120,176,196\nbuffer_load_dwordx4 v221, s[4:7], s63 offen offset:1024 lds\nM 120,180,212\nds_read2st64_b32 v[140:141], v225 offset0:12 offset1:13\nds_read2st64_b32 v[142:143], v225 offset0:14 offset1:15\nG 4\n"
"M 124,160,196\nds_read2st64_b32 v[144:145], v225 offset0:16 offset1:17\nds_read2st64_b32 v[146:147], v225 offset0:18 offset1:19\nM 124,164,212\nds_read2st64_b32 v[148:149], v225 offset0:20 offset1:21\nds_read2st64_b32 v[150:151], v225 offset0:22 offset1:23\n"
"M 108,160,192\nds_read2st64_b32 v[152:153], v225 offset0:24 offset1:25\nds_read2st64_b32 v[154:155], v225 offset0:26 offset1:27\nM 108,164,208\nds_read2st64_b32 v[156:157], v225 offset0:28 offset1:29\nds_read2st64_b32 v[158:159], v225 offset0:30 offset1:31\n"
"G 0\ns_barrier\ns_lshl_b32 s84, s75, 11\ns_add_u32 s84, s84, s49\ns_add_u32 s84, s84, 18874368\ns_add_u32 s85, s84, 32768\n"
"v_mov_b32 v238, s46\nv_mov_b32 v239, s47\ns_waitcnt vmcnt(17)\nv_mfma_f32_16x16x32_bf16 v[96:99], v[208:211], v[0:3], v[96:99]\nv_mfma_f32_16x16x32_bf16 v[100:103], v[210:213], v[0:3], v[100:103]\ns_waitcnt vmcnt(16)\n"
"v_mfma_f32_16x16x32_bf16 v[104:107], v[208:211], v[4:7], v[104:107]\nv_mfma_f32_16x16x32_bf16 v[108:111], v[210:213], v[4:7], v[108:111]\ns_waitcnt vmcnt(15)\nv_mfma_f32_16x16x32_bf16 v[112:115], v[208:211], v[8:11], v[112:115]\nv_mfma_f32_16x16x32_bf16 v[116:119], v[210:213], v[8:11], v[116:119]\ns_waitcnt vmcnt(14)\n"
"v_mfma_f32_16x16x32_bf16 v[120:123], v[208:211], v[12:15], v[120:123]\nv_mfma_f32_16x16x32_bf16 v[124:127], v[210:213], v[12:15], v[124:127]\ns_waitcnt vmcnt(13)\nv_mfma_f32_16x16x32_bf16 v[96:99], v[208:211], v[16:19], v[96:99]\nv_mfma_f32_16x16x32_bf16 v[100:103], v[210:213], v[16:19], v[100:103]\ns_waitcnt vmcnt(12)\n"
"v_mfma_f32_16x16x32_bf16 v[104:107], v[208:211], v[20:23], v[104:107]\nv_mfma_f32_16x16x32_bf16 v[108:111], v[210:213], v[20:23], v[108:111]\ns_waitcnt vmcnt(11)\nv_mfma_f32_16x16x32_bf16 v[112:115], v[208:211], v[24:27], v[112:115]\nv_mfma_f32_16x16x32_bf16 v[116:119], v[210:213], v[24:27], v[116:119]\ns_waitcnt vmcnt(10)\n"
"v_mfma_f32_16x16x32_bf16 v[120:123], v[208:211], v[28:31], v[120:123]\nv_mfma_f32_16x16x32_bf16 v[124:127], v[210:213], v[28:31], v[124:127]\ns_waitcnt vmcnt(7)\nv_mfma_f32_16x16x32_bf16 v[96:99], v[208:211], v[64:67], v[96:99]\nv_mfma_f32_16x16x32_bf16 v[100:103], v[210:213], v[64:67], v[100:103]\ns_waitcnt vmcnt(6)\n"
"v_mfma_f32_16x16x32_bf16 v[104:107], v[208:211], v[68:71], v[104:107]\nv_mfma_f32_16x16x32_bf16 v[108:111], v[210:213], v[68:71], v[108:111]\ns_waitcnt vmcnt(5)\nv_mfma_f32_16x16x32_bf16 v[112:115], v[208:211], v[72:75], v[112:115]\nv_mfma_f32_16x16x32_bf16 v[116:119], v[210:213], v[72:75], v[116:119]\ns_waitcnt vmcnt(4)\n"
"v_mfma_f32_16x16x32_bf16 v[120:123], v[208:211], v[76:79], v[120:123]\nv_mfma_f32_16x16x32_bf16 v[124:127], v[210:213], v[76:79], v[124:127]\nv_pk_add_f32 v[96:97], v[96:97], v[128:129]\nv_pk_add_f32 v[98:99], v[98:99], v[130:131]\nv_pk_add_f32 v[100:101], v[100:101], v[132:133]\nv_pk_add_f32 v[102:103], v[102:103], v[134:135]\n"
"v_cvt_pk_bf16_f32 v192, v96, v97\nv_cvt_pk_bf16_f32 v193, v98, v99\nv_cvt_pk_bf16_f32 v194, v100, v101\nv_cvt_pk_bf16_f32 v195, v102, v103\nbuffer_store_dwordx4 v[192:195], v252, s[20:23], s84 offen offset:0 sc1\nv_pk_add_f32 v[104:105], v[104:105], v[136:137]\n"
"v_pk_add_f32 v[106:107], v[106:107], v[138:139]\nv_pk_add_f32 v[108:109], v[108:109], v[140:141]\nv_pk_add_f32 v[110:111], v[110:111], v[142:143]\nv_cvt_pk_bf16_f32 v196, v104, v105\nv_cvt_pk_bf16_f32 v197, v106, v107\nv_cvt_pk_bf16_f32 v198, v108, v109\n"
"v_cvt_pk_bf16_f32 v199, v110, v111\nbuffer_store_dwordx4 v[196:199], v252, s[20:23], s84 offen offset:1024 sc1\nv_pk_add_f32 v[112:113], v[112:113], v[144:145]\nv_pk_add_f32 v[114:115], v[114:115], v[146:147]\nv_pk_add_f32 v[116:117], v[116:117], v[148:149]\nv_pk_add_f32 v[118:119], v[118:119], v[150:151]\n"
"v_cvt_pk_bf16_f32 v200, v112, v113\nv_cvt_pk_bf16_f32 v201, v114, v115\nv_cvt_pk_bf16_f32 v202, v116, v117\nv_cvt_pk_bf16_f32 v203, v118, v119\nbuffer_store_dwordx4 v[200:203], v252, s[20:23], s85 offen offset:0 sc1\nv_pk_add_f32 v[120:121], v[120:121], v[152:153]\n"
"v_pk_add_f32 v[122:123], v[122:123], v[154:155]\nv_pk_add_f32 v[124:125], v[124:125], v[156:157]\nv_pk_add_f32 v[126:127], v[126:127], v[158:159]\nv_cvt_pk_bf16_f32 v204, v120, v121\nv_cvt_pk_bf16_f32 v205, v122, v123\nv_cvt_pk_bf16_f32 v206, v124, v125\n"
"v_cvt_pk_bf16_f32 v207, v126, v127\nbuffer_store_dwordx4 v[204:207], v252, s[20:23], s85 offen offset:1024 sc1\ns_add_u32 s61, s79, 88358912\ns_lshl_b32 s62, s75, 3\ns_add_u32 s61, s61, s62\ns_waitcnt vmcnt(0)\n"
"s_mov_b64 exec, 1\nbuffer_atomic_swap_x2 v[238:239], off, s[20:23], s61 sc1\ns_mov_b64 exec, -1\ns_branch LqM\nLq116:\ns_lshl_b32 s61, s42, 14\n"
"s_add_u32 s61, s61, 84148224\nbuffer_load_dwordx4 v[128:131], v230, s[20:23], s61 offen sc1\ns_lshl_b32 s61, s42, 14\ns_add_u32 s61, s61, 84152320\nbuffer_load_dwordx4 v[132:135], v230, s[20:23], s61 offen sc1\ns_lshl_b32 s61, s42, 14\n"
"s_add_u32 s61, s61, 84156416\nbuffer_load_dwordx4 v[136:139], v230, s[20:23], s61 offen sc1\ns_lshl_b32 s61, s42, 14\ns_add_u32 s61, s61, 84160512\nbuffer_load_dwordx4 v[140:143], v230, s[20:23], s61 offen sc1\ns_waitcnt vmcnt(2)\n"
"v_min3_u32 v232, v129, v131, v133\ns_waitcnt vmcnt(1)\nv_min3_u32 v232, v232, v135, v137\ns_waitcnt vmcnt(0)\nv_min3_u32 v232, v232, v139, v141\nv_min3_u32 v232, v232, v143, v143\n"
"v_max3_u32 v233, v129, v131, v133\nv_max3_u32 v233, v233, v135, v137\nv_max3_u32 v233, v233, v139, v141\nv_max3_u32 v233, v233, v143, v143\nv_cmp_ne_u32 s[86:87], s46, v232\nv_cmp_ne_u32 s[88:89], s46, v233\n"
"s_nop 0\ns_or_b64 s[86:87], s[86:87], s[88:89]\ns_cbranch_scc0 Lq117\ns_sub_u32 s48, s48, 1\ns_cmp_gt_i32 s48, 0\ns_cbranch_scc1 Lq116\n"
"s_branch Lq117\nLq126:\ns_lshl_b32 s61, s42, 14\ns_add_u32 s61, s61, 84410368\nbuffer_load_dwordx4 v[144:147], v234, s[20:23], s61 offen sc1\ns_lshl_b32 s61, s42, 14\n"
"s_add_u32 s61, s61, 84414464\nbuffer_load_dwordx4 v[148:151], v234, s[20:23], s61 offen sc1\ns_lshl_b32 s61, s42, 14\ns_add_u32 s61, s61, 84418560\nbuffer_load_dwordx4 v[152:155], v234, s[20:23], s61 offen sc1\ns_lshl_b32 s61, s42, 14\n"
"s_add_u32 s61, s61, 84422656\nbuffer_load_dwordx4 v[156:159], v234, s[20:23], s61 offen sc1\ns_waitcnt vmcnt(2)\nv_min3_u32 v236, v145, v147, v149\ns_waitcnt vmcnt(1)\nv_min3_u32 v236, v236, v151, v153\n"
"s_waitcnt vmcnt(0)\nv_min3_u32 v236, v236, v155, v157\nv_min3_u32 v236, v236, v159, v159\nv_max3_u32 v237, v145, v147, v149\nv_max3_u32 v237, v237, v151, v153\nv_max3_u32 v237, v237, v155, v157\n"
"v_max3_u32 v237, v237, v159, v159\nv_cmp_ne_u32 s[86:87], s46, v236\nv_cmp_ne_u32 s[88:89], s46, v237\ns_nop 0\ns_or_b64 s[86:87], s[86:87], s[88:89]\ns_cbranch_scc0 Lq127\n"
"s_sub_u32 s48, s48, 1\ns_cmp_gt_i32 s48, 0\ns_cbranch_scc1 Lq126\ns_branch Lq127\nLq136:\ns_lshl_b32 s61, s42, 14\n"
"s_add_u32 s61, s61, 83886080\nbuffer_load_dwordx4 v[128:131], v230, s[20:23], s61 offen sc1\ns_lshl_b32 s61, s42, 14\ns_add_u32 s61, s61, 83890176\nbuffer_load_dwordx4 v[132:135], v230, s[20:23], s61 offen sc1\ns_lshl_b32 s61, s42, 14\n"
"s_add_u32 s61, s61, 83894272\nbuffer_load_dwordx4 v[136:139], v230, s[20:23], s61 offen sc1\ns_lshl_b32 s61, s42, 14\ns_add_u32 s61, s61, 83898368\nbuffer_load_dwordx4 v[140:143], v230, s[20:23], s61 offen sc1\ns_waitcnt vmcnt(2)\n"
"v_min3_u32 v232, v129, v131, v133\ns_waitcnt vmcnt(1)\nv_min3_u32 v232, v232, v135, v137\ns_waitcnt vmcnt(0)\nv_min3_u32 v232, v232, v139, v141\nv_min3_u32 v232, v232, v143, v143\n"
"v_max3_u32 v233, v129, v131, v133\nv_max3_u32 v233, v233, v135, v137\nv_max3_u32 v233, v233, v139, v141\nv_max3_u32 v233, v233, v143, v143\nv_cmp_ne_u32 s[86:87], s46, v232\nv_cmp_ne_u32 s[88:89], s46, v233\n"
"s_nop 0\ns_or_b64 s[86:87], s[86:87], s[88:89]\ns_cbranch_scc0 Lq137\ns_sub_u32 s48, s48, 1\ns_cmp_gt_i32 s48, 0\ns_cbranch_scc1 Lq136\n"
"s_branch Lq137\nLq146:\ns_lshr_b32 s84, s49, 3\ns_add_u32 s84, s84, 81788928\nbuffer_load_dwordx4 v[128:131], v252, s[20:23], s84 offen offset:0 sc1\nbuffer_load_dwordx4 v[132:135], v252, s[20:23], s84 offen offset:1024 sc1\n"
"buffer_load_dwordx4 v[136:139], v252, s[20:23], s84 offen offset:2048 sc1\nbuffer_load_dwordx4 v[140:143], v252, s[20:23], s84 offen offset:3072 sc1\ns_lshr_b32 s85, s49, 3\ns_add_u32 s85, s85, 81793024\nbuffer_load_dwordx4 v[144:147], v252, s[20:23], s85 offen offset:0 sc1\nbuffer_load_dwordx4 v[148:151], v252, s[20:23], s85 offen offset:1024 sc1\n"
"buffer_load_dwordx4 v[152:155], v252, s[20:23], s85 offen offset:2048 sc1\nbuffer_load_dwordx4 v[156:159], v252, s[20:23], s85 offen offset:3072 sc1\ns_waitcnt vmcnt(6)\nv_min3_u32 v230, v129, v131, v133\ns_waitcnt vmcnt(5)\nv_min3_u32 v230, v230, v135, v137\n"
"s_waitcnt vmcnt(4)\nv_min3_u32 v230, v230, v139, v141\ns_waitcnt vmcnt(3)\nv_min3_u32 v230, v230, v143, v145\ns_waitcnt vmcnt(2)\nv_min3_u32 v230, v230, v147, v149\n"
"s_waitcnt vmcnt(1)\nv_min3_u32 v230, v230, v151, v153\ns_waitcnt vmcnt(0)\nv_min3_u32 v230, v230, v155, v157\nv_min3_u32 v230, v230, v159, v159\nv_max3_u32 v231, v129, v131, v133\n"
"v_max3_u32 v231, v231, v135, v137\nv_max3_u32 v231, v231, v139, v141\nv_max3_u32 v231, v231, v143, v145\nv_max3_u32 v231, v231, v147, v149\nv_max3_u32 v231, v231, v151, v153\nv_max3_u32 v231, v231, v155, v157\n"
"v_max3_u32 v231, v231, v159, v159\nv_cmp_ne_u32 s[86:87], s46, v230\nv_cmp_ne_u32 s[88:89], s46, v231\ns_nop 0\ns_or_b64 s[86:87], s[86:87], s[88:89]\ns_cbranch_scc0 Lq147\n"
"s_sub_u32 s48, s48, 1\ns_cmp_gt_i32 s48, 0\ns_cbranch_scc1 Lq146\ns_branch Lq147\nLq196:\ns_mov_b64 exec, s[66:67]\n"
"buffer_load_dwordx4 v[230:233], v252, s[20:23], s65 offen offset:0 sc1\nbuffer_load_dwordx2 v[64:65], v227, s[20:23], s65 offen offset:2048 sc1\nbuffer_load_dwordx2 v[70:71], v227, s[20:23], s65 offen offset:2560 sc1\ns_mov_b64 exec, -1\ns_waitcnt vmcnt(0)\nv_max3_u32 v248, v233, v65, v71\n"
"v_cmp_eq_u32 vcc, -1, v248\ns_nop 1\ns_cbranch_vccz Lq197\ns_sub_u32 s48, s48, 1\ns_cmp_gt_i32 s48, 0\ns_cbranch_scc1 Lq196\n"
"s_branch Lq197\nLq198:\ns_mov_b64 exec, s[66:67]\nbuffer_load_dwordx4 v[234:237], v252, s[20:23], s65 offen offset:1024 sc1\nbuffer_load_dwordx2 v[76:77], v227, s[20:23], s65 offen offset:3072 sc1\nbuffer_load_dwordx2 v[82:83], v227, s[20:23], s65 offen offset:3584 sc1\n"
"s_mov_b64 exec, -1\ns_waitcnt vmcnt(0)\nv_max3_u32 v248, v237, v77, v83\nv_cmp_eq_u32 vcc, -1, v248\ns_nop 1\ns_cbranch_vccz Lq199\n"
"s_sub_u32 s48, s48, 1\ns_cmp_gt_i32 s48, 0\ns_cbranch_scc1 Lq198\ns_branch Lq199\nLq201:\nv_mov_b32 v248, 0\n"
"v_mov_b32 v249, 0\ns_add_u32 s61, s80, 88080384\ns_mov_b64 exec, 0xffff\nbuffer_atomic_or_x2 v[248:249], v250, s[20:23], s61 offen sc0 sc1\ns_mov_b64 exec, -1\ns_waitcnt vmcnt(0)\n"
"v_cmp_eq_u64 s[86:87], s[46:47], v[248:249]\ns_nop 1\ns_and_b32 s62, s86, 0xffff\ns_cmp_eq_u32 s62, 0xffff\ns_cbranch_scc1 Lq202\ns_sleep 1\n"
"s_sub_u32 s48, s48, 1\ns_cmp_gt_i32 s48, 0\ns_cbranch_scc1 Lq201\ns_branch Lq202\nLq255:\nv_mov_b32 v248, 0\n"
"v_mov_b32 v249, 0\ns_add_u32 s61, s80, 88219648\ns_mov_b64 exec, 0xffff\nbuffer_atomic_or_x2 v[248:249], v250, s[20:23], s61 offen sc0 sc1\ns_mov_b64 exec, -1\ns_waitcnt vmcnt(0)\n"
"v_cmp_eq_u64 s[86:87], s[46:47], v[248:249]\ns_nop 1\ns_and_b32 s62, s86, 0xffff\ns_cmp_eq_u32 s62, 0xffff\ns_cbranch_scc1 Lq256\ns_sleep 1\n"
"s_sub_u32 s48, s48, 1\ns_cmp_gt_i32 s48, 0\ns_cbranch_scc1 Lq255\ns_branch Lq256\nLq307:\ns_lshr_b32 s84, s49, 3\n"
"s_add_u32 s84, s84, 82313216\nbuffer_load_dwordx4 v[0:3], v252, s[20:23], s84 offen offset:0 sc1\nbuffer_load_dwordx4 v[4:7], v252, s[20:23], s84 offen offset:1024 sc1\nbuffer_load_dwordx4 v[8:11], v252, s[20:23], s84 offen offset:2048 sc1\nbuffer_load_dwordx4 v[12:15], v252, s[20:23], s84 offen offset:3072 sc1\ns_lshr_b32 s85, s49, 3\n"
"s_add_u32 s85, s85, 82317312\nbuffer_load_dwordx4 v[16:19], v252, s[20:23], s85 offen offset:0 sc1\nbuffer_load_dwordx4 v[20:23], v252, s[20:23], s85 offen offset:1024 sc1\nbuffer_load_dwordx4 v[24:27], v252, s[20:23], s85 offen offset:2048 sc1\nbuffer_load_dwordx4 v[28:31], v252, s[20:23], s85 offen offset:3072 sc1\ns_waitcnt vmcnt(6)\n"
"v_min3_u32 v230, v1, v3, v5\ns_waitcnt vmcnt(5)\nv_min3_u32 v230, v230, v7, v9\ns_waitcnt vmcnt(4)\nv_min3_u32 v230, v230, v11, v13\ns_waitcnt vmcnt(3)\n"
"v_min3_u32 v230, v230, v15, v17\ns_waitcnt vmcnt(2)\nv_min3_u32 v230, v230, v19, v21\ns_waitcnt vmcnt(1)\nv_min3_u32 v230, v230, v23, v25\ns_waitcnt vmcnt(0)\n"
"v_min3_u32 v230, v230, v27, v29\nv_min3_u32 v230, v230, v31, v31\nv_max3_u32 v231, v1, v3, v5\nv_max3_u32 v231, v231, v7, v9\nv_max3_u32 v231, v231, v11, v13\nv_max3_u32 v231, v231, v15, v17\n"
"v_max3_u32 v231, v231, v19, v21\nv_max3_u32 v231, v231, v23, v25\nv_max3_u32 v231, v231, v27, v29\nv_max3_u32 v231, v231, v31, v31\nv_cmp_ne_u32 s[86:87], s46, v230\nv_cmp_ne_u32 s[88:89], s46, v231\n"
"s_nop 0\ns_or_b64 s[86:87], s[86:87], s[88:89]\ns_cbranch_scc0 Lq308\ns_sub_u32 s48, s48, 1\ns_cmp_gt_i32 s48, 0\ns_cbranch_scc1 Lq307\n"
"s_branch Lq308\nLq309:\ns_lshl_b32 s61, s42, 14\ns_add_u32 s61, s61, 84672512\nbuffer_load_dwordx4 v[0:3], v238, s[20:23], s61 offen sc1\ns_lshl_b32 s61, s42, 14\n"
"s_add_u32 s61, s61, 84676608\nbuffer_load_dwordx4 v[4:7], v238, s[20:23], s61 offen sc1\ns_lshl_b32 s61, s42, 14\ns_add_u32 s61, s61, 84680704\nbuffer_load_dwordx4 v[8:11], v238, s[20:23], s61 offen sc1\ns_lshl_b32 s61, s42, 14\n"
"s_add_u32 s61, s61, 84684800\nbuffer_load_dwordx4 v[12:15], v238, s[20:23], s61 offen sc1\ns_waitcnt vmcnt(2)\nv_min3_u32 v240, v1, v3, v5\ns_waitcnt vmcnt(1)\nv_min3_u32 v240, v240, v7, v9\n"
"s_waitcnt vmcnt(0)\nv_min3_u32 v240, v240, v11, v13\nv_min3_u32 v240, v240, v15, v15\nv_max3_u32 v241, v1, v3, v5\nv_max3_u32 v241, v241, v7, v9\nv_max3_u32 v241, v241, v11, v13\n"
"v_max3_u32 v241, v241, v15, v15\nv_cmp_ne_u32 s[86:87], s46, v240\nv_cmp_ne_u32 s[88:89], s46, v241\ns_nop 0\ns_or_b64 s[86:87], s[86:87], s[88:89]\ns_cbranch_scc0 Lq310\n"
"s_sub_u32 s48, s48, 1\ns_cmp_gt_i32 s48, 0\ns_cbranch_scc1 Lq309\ns_branch Lq310\nLq321:\ns_lshl_b32 s61, s42, 14\n"
"s_add_u32 s61, s61, 84934656\nbuffer_load_dwordx4 v[16:19], v242, s[20:23], s61 offen sc1\ns_lshl_b32 s61, s42, 14\ns_add_u32 s61, s61, 84938752\nbuffer_load_dwordx4 v[20:23], v242, s[20:23], s61 offen sc1\ns_lshl_b32 s61, s42, 14\n"
"s_add_u32 s61, s61, 84942848\nbuffer_load_dwordx4 v[24:27], v242, s[20:23], s61 offen sc1\ns_lshl_b32 s61, s42, 14\ns_add_u32 s61, s61, 84946944\nbuffer_load_dwordx4 v[28:31], v242, s[20:23], s61 offen sc1\ns_waitcnt vmcnt(2)\n"
"v_min3_u32 v244, v17, v19, v21\ns_waitcnt vmcnt(1)\nv_min3_u32 v244, v244, v23, v25\ns_waitcnt vmcnt(0)\nv_min3_u32 v244, v244, v27, v29\nv_min3_u32 v244, v244, v31, v31\n"
"v_max3_u32 v245, v17, v19, v21\nv_max3_u32 v245, v245, v23, v25\nv_max3_u32 v245, v245, v27, v29\nv_max3_u32 v245, v245, v31, v31\nv_cmp_ne_u32 s[86:87], s46, v244\nv_cmp_ne_u32 s[88:89], s46, v245\n"
"s_nop 0\ns_or_b64 s[86:87], s[86:87], s[88:89]\ns_cbranch_scc0 Lq322\ns_sub_u32 s48, s48, 1\ns_cmp_gt_i32 s48, 0\ns_cbranch_scc1 Lq321\n"
"s_branch Lq322\nLq333:\ns_lshl_b32 s61, s42, 14\ns_add_u32 s61, s61, 85196800\nbuffer_load_dwordx4 v[0:3], v238, s[20:23], s61 offen sc1\ns_lshl_b32 s61, s42, 14\n"
"s_add_u32 s61, s61, 85200896\nbuffer_load_dwordx4 v[4:7], v238, s[20:23], s61 offen sc1\ns_lshl_b32 s61, s42, 14\ns_add_u32 s61, s61, 85204992\nbuffer_load_dwordx4 v[8:11], v238, s[20:23], s61 offen sc1\ns_lshl_b32 s61, s42, 14\n"
"s_add_u32 s61, s61, 85209088\nbuffer_load_dwordx4 v[12:15], v238, s[20:23], s61 offen sc1\ns_waitcnt vmcnt(2)\nv_min3_u32 v240, v1, v3, v5\ns_waitcnt vmcnt(1)\nv_min3_u32 v240, v240, v7, v9\n"
"s_waitcnt vmcnt(0)\nv_min3_u32 v240, v240, v11, v13\nv_min3_u32 v240, v240, v15, v15\nv_max3_u32 v241, v1, v3, v5\nv_max3_u32 v241, v241, v7, v9\nv_max3_u32 v241, v241, v11, v13\n"
"v_max3_u32 v241, v241, v15, v15\nv_cmp_ne_u32 s[86:87], s46, v240\nv_cmp_ne_u32 s[88:89], s46, v241\ns_nop 0\ns_or_b64 s[86:87], s[86:87], s[88:89]\ns_cbranch_scc0 Lq334\n"
"s_sub_u32 s48, s48, 1\ns_cmp_gt_i32 s48, 0\ns_cbranch_scc1 Lq333\ns_branch Lq334\nLq345:\ns_lshl_b32 s61, s42, 14\n"
"s_add_u32 s61, s61, 85458944\nbuffer_load_dwordx4 v[16:19], v242, s[20:23], s61 offen sc1\ns_lshl_b32 s61, s42, 14\ns_add_u32 s61, s61, 85463040\nbuffer_load_dwordx4 v[20:23], v242, s[20:23], s61 offen sc1\ns_lshl_b32 s61, s42, 14\n"
"s_add_u32 s61, s61, 85467136\nbuffer_load_dwordx4 v[24:27], v242, s[20:23], s61 offen sc1\ns_lshl_b32 s61, s42, 14\ns_add_u32 s61, s61, 85471232\nbuffer_load_dwordx4 v[28:31], v242, s[20:23], s61 offen sc1\ns_waitcnt vmcnt(2)\n"
"v_min3_u32 v244, v17, v19, v21\ns_waitcnt vmcnt(1)\nv_min3_u32 v244, v244, v23, v25\ns_waitcnt vmcnt(0)\nv_min3_u32 v244, v244, v27, v29\nv_min3_u32 v244, v244, v31, v31\n"
"v_max3_u32 v245, v17, v19, v21\nv_max3_u32 v245, v245, v23, v25\nv_max3_u32 v245, v245, v27, v29\nv_max3_u32 v245, v245, v31, v31\nv_cmp_ne_u32 s[86:87], s46, v244\nv_cmp_ne_u32 s[88:89], s46, v245\n"
"s_nop 0\ns_or_b64 s[86:87], s[86:87], s[88:89]\ns_cbranch_scc0 Lq346\ns_sub_u32 s48, s48, 1\ns_cmp_gt_i32 s48, 0\ns_cbranch_scc1 Lq345\n"
"s_branch Lq346\nLq381:\nv_mov_b32 v248, 0\nv_mov_b32 v249, 0\ns_add_u32 s61, s82, 385024\ns_mov_b64 exec, 0xf\n"
"buffer_atomic_or_x2 v[248:249], v250, s[20:23], s61 offen sc0 sc1\ns_mov_b64 exec, -1\ns_waitcnt vmcnt(0)\nv_cmp_eq_u64 s[86:87], s[46:47], v[248:249]\ns_nop 1\ns_and_b32 s62, s86, 0xf\n"
"s_cmp_eq_u32 s62, 0xf\ns_cbranch_scc1 Lq382\ns_sleep 1\ns_sub_u32 s48, s48, 1\ns_cmp_gt_i32 s48, 0\ns_cbranch_scc1 Lq381\n"
"s_branch Lq382\nLq416:\nv_mov_b32 v248, 0\nv_mov_b32 v249, 0\ns_add_u32 s61, s82, 385032\ns_mov_b64 exec, 0xf\n"
"buffer_atomic_or_x2 v[248:249], v250, s[20:23], s61 offen sc0 sc1\ns_mov_b64 exec, -1\ns_waitcnt vmcnt(0)\nv_cmp_eq_u64 s[86:87], s[46:47], v[248:249]\ns_nop 1\ns_and_b32 s62, s86, 0xf\n"
"s_cmp_eq_u32 s62, 0xf\ns_cbranch_scc1 Lq417\ns_sleep 1\ns_sub_u32 s48, s48, 1\ns_cmp_gt_i32 s48, 0\ns_cbranch_scc1 Lq416\n"
"s_branch Lq417\nLq451:\nv_mov_b32 v248, 0\nv_mov_b32 v249, 0\ns_add_u32 s61, s82, 385048\ns_mov_b64 exec, 0xf\n"
"buffer_atomic_or_x2 v[248:249], v250, s[20:23], s61 offen sc0 sc1\ns_mov_b64 exec, -1\ns_waitcnt vmcnt(0)\nv_cmp_eq_u64 s[86:87], s[46:47], v[248:249]\ns_nop 1\ns_and_b32 s62, s86, 0xf\n"
"s_cmp_eq_u32 s62, 0xf\ns_cbranch_scc1 Lq452\ns_sleep 1\ns_sub_u32 s48, s48, 1\ns_cmp_gt_i32 s48, 0\ns_cbranch_scc1 Lq451\n"
"s_branch Lq452\nLq470:\nv_mov_b32 v248, 0\nv_mov_b32 v249, 0\ns_add_u32 s61, s82, 385040\ns_mov_b64 exec, 0xf\n"
"buffer_atomic_or_x2 v[248:249], v250, s[20:23], s61 offen sc0 sc1\ns_mov_b64 exec, -1\ns_waitcnt vmcnt(0)\nv_cmp_eq_u64 s[86:87], s[46:47], v[248:249]\ns_nop 1\ns_and_b32 s62, s86, 0xf\n"
"s_cmp_eq_u32 s62, 0xf\ns_cbranch_scc1 Lq471\ns_sleep 1\ns_sub_u32 s48, s48, 1\ns_cmp_gt_i32 s48, 0\ns_cbranch_scc1 Lq470\n"
"s_branch Lq471\nLq513:\nv_mov_b32 v248, 0\nv_mov_b32 v249, 0\ns_add_u32 s61, s74, 548864\ns_mov_b64 exec, 3\n"
"buffer_atomic_or_x2 v[248:249], v250, s[20:23], s61 offen sc0 sc1\ns_mov_b64 exec, -1\ns_waitcnt vmcnt(0)\nv_cmp_eq_u64 s[86:87], s[46:47], v[248:249]\ns_nop 1\ns_and_b32 s62, s86, 3\n"
"s_cmp_eq_u32 s62, 3\ns_cbranch_scc1 Lq514\ns_sleep 1\ns_sub_u32 s48, s48, 1\ns_cmp_gt_i32 s48, 0\ns_cbranch_scc1 Lq513\n"
"s_branch Lq514\nLq529:\nv_mov_b32 v248, 0\nv_mov_b32 v249, 0\ns_add_u32 s61, s74, 548864\ns_mov_b64 exec, 4\n"
"buffer_atomic_or_x2 v[248:249], v250, s[20:23], s61 offen sc0 sc1\ns_mov_b64 exec, -1\ns_waitcnt vmcnt(0)\nv_cmp_eq_u64 s[86:87], s[46:47], v[248:249]\ns_nop 1\ns_and_b32 s62, s86, 4\n"
"s_cmp_eq_u32 s62, 4\ns_cbranch_scc1 Lq530\ns_sleep 1\ns_sub_u32 s48, s48, 1\ns_cmp_gt_i32 s48, 0\ns_cbranch_scc1 Lq529\n"
"s_branch Lq530\n.p2align 6\nLqM:\nLqq1:\nv_bfe_u32 v250, v252, 7, 1\nv_mul_u32_u24 v250, 2176, v250\n"
"v_bfe_u32 v251, v252, 4, 3\nv_lshl_add_u32 v250, v251, 3, v250\ns_lshl_b32 s60, s90, 6\nv_add_u32 v250, s60, v250\ns_sleep 5\nv_mov_b32 v248, 0\n"
"v_mov_b32 v249, 0\ns_add_u32 s61, s80, 88358912\ns_mov_b64 exec, 0xffff\nbuffer_atomic_or_x2 v[248:249], v250, s[20:23], s61 offen sc0 sc1\ns_mov_b64 exec, -1\ns_add_u32 m0, s50, 0\n"
"s_add_u32 s63, s52, 397312\nbuffer_load_dwordx4 v220, s[4:7], s63 offen lds\nbuffer_load_dwordx4 v221, s[4:7], s63 offen offset:1024 lds\ns_add_u32 m0, s50, 8192\ns_add_u32 s63, s52, 593920\nbuffer_load_dwordx4 v220, s[4:7], s63 offen lds\n"
"buffer_load_dwordx4 v221, s[4:7], s63 offen offset:1024 lds\ns_add_u32 m0, s50, 16384\ns_add_u32 s63, s52, 788480\nbuffer_load_dwordx4 v220, s[4:7], s63 offen lds\nbuffer_load_dwordx4 v221, s[4:7], s63 offen offset:1024 lds\ns_add_u32 m0, s50, 24576\n"
"s_add_u32 s63, s52, 985088\nbuffer_load_dwordx4 v220, s[4:7], s63 offen lds\nbuffer_load_dwordx4 v221, s[4:7], s63 offen offset:1024 lds\ns_add_u32 m0, s50, 32768\ns_add_u32 s63, s52, 790528\nbuffer_load_dwordx4 v220, s[4:7], s63 offen lds\n"
"buffer_load_dwordx4 v221, s[4:7], s63 offen offset:1024 lds\ns_add_u32 m0, s50, 40960\ns_add_u32 s63, s52, 987136\nbuffer_load_dwordx4 v220, s[4:7], s63 offen lds\nbuffer_load_dwordx4 v221, s[4:7], s63 offen offset:1024 lds\nv_mov_b32 v230, 0\n"
"v_mov_b32 v231, 0\nv_mov_b32 v232, 0\nv_mov_b32 v233, 0\nv_dot2c_f32_bf16 v230, s76, v192\nv_dot2c_f32_bf16 v231, v192, v192\nv_dot2c_f32_bf16 v232, s76, v200\n"
"v_dot2c_f32_bf16 v233, v200, v200\nv_dot2c_f32_bf16 v230, s76, v193\nv_dot2c_f32_bf16 v231, v193, v193\nv_dot2c_f32_bf16 v232, s76, v201\nv_dot2c_f32_bf16 v233, v201, v201\nv_dot2c_f32_bf16 v230, s76, v194\n"
"v_dot2c_f32_bf16 v231, v194, v194\nv_dot2c_f32_bf16 v232, s76, v202\nv_dot2c_f32_bf16 v233, v202, v202\nv_dot2c_f32_bf16 v230, s76, v195\nv_dot2c_f32_bf16 v231, v195, v195\nv_dot2c_f32_bf16 v232, s76, v203\n"
"v_dot2c_f32_bf16 v233, v203, v203\nv_dot2c_f32_bf16 v230, s76, v196\nv_dot2c_f32_bf16 v231, v196, v196\nv_dot2c_f32_bf16 v232, s76, v204\nv_dot2c_f32_bf16 v233, v204, v204\nv_dot2c_f32_bf16 v230, s76, v197\n"
"v_dot2c_f32_bf16 v231, v197, v197\nv_dot2c_f32_bf16 v232, s76, v205\nv_dot2c_f32_bf16 v233, v205, v205\nv_dot2c_f32_bf16 v230, s76, v198\nv_dot2c_f32_bf16 v231, v198, v198\nv_dot2c_f32_bf16 v232, s76, v206\n"
"v_dot2c_f32_bf16 v233, v206, v206\nv_dot2c_f32_bf16 v230, s76, v199\nv_dot2c_f32_bf16 v231, v199, v199\nv_dot2c_f32_bf16 v232, s76, v207\nv_dot2c_f32_bf16 v233, v207, v207\nv_lshrrev_b32 v234, 1, v252\n"
"v_permlane32_swap_b32 v230, v231\ns_nop 0\nv_permlane32_swap_b32 v232, v233\nv_add_f32 v230, v230, v231\nv_add_f32 v232, v232, v233\nv_mov_b32 v231, s46\n"
"s_nop 0\nv_permlane16_swap_b32 v230, v232\nv_add_f32 v230, v230, v232\ns_lshr_b32 s61, s49, 3\ns_lshl2_add_u32 s61, s53, s61\ns_add_u32 s61, s61, 82837504\n"
"buffer_store_dwordx2 v[230:231], v234, s[20:23], s61 offen sc1\ns_waitcnt vmcnt(13)\nv_cmp_eq_u64 s[86:87], s[46:47], v[248:249]\ns_nop 0\ns_and_b32 s62, s86, 0xffff\ns_cmp_eq_u32 s62, 0xffff\n"
"s_cbranch_scc0 Lq593\nLq594:\ns_add_u32 s64, s70, 18874368\nbuffer_load_dwordx4 a[0:3], v252, s[20:23], s64 offen offset:0 sc1\ns_add_u32 s65, s70, 18907136\nbuffer_load_dwordx4 a[4:7], v252, s[20:23], s65 offen offset:0 sc1\n"
"s_add_u32 s66, s71, 18874368\nbuffer_load_dwordx4 a[8:11], v252, s[20:23], s66 offen offset:0 sc1\ns_add_u32 s67, s71, 18907136\nbuffer_load_dwordx4 a[12:15], v252, s[20:23], s67 offen offset:0 sc1\nbuffer_load_dwordx4 a[16:19], v252, s[20:23], s64 offen offset:1024 sc1\nbuffer_load_dwordx4 a[20:23], v252, s[20:23], s65 offen offset:1024 sc1\n"
"buffer_load_dwordx4 a[24:27], v252, s[20:23], s66 offen offset:1024 sc1\nbuffer_load_dwordx4 a[28:31], v252, s[20:23], s67 offen offset:1024 sc1\ns_barrier\nR 160,188,8192\nR 162,188,12288\nR 164,188,16384\n"
"R 166,188,20480\nR 168,189,8192\nR 170,189,12288\nR 172,189,16384\nR 174,189,20480\nR 176,190,8192\n"
"R 178,190,12288\nR 180,190,16384\nR 182,190,20480\nbuffer_load_dwordx4 a[32:35], v252, s[20:23], s64 offen offset:2048 sc1\nbuffer_load_dwordx4 a[36:39], v252, s[20:23], s65 offen offset:2048 sc1\nbuffer_load_dwordx4 a[40:43], v252, s[20:23], s66 offen offset:2048 sc1\n"
"buffer_load_dwordx4 a[44:47], v252, s[20:23], s67 offen offset:2048 sc1\nbuffer_load_dwordx4 a[48:51], v252, s[20:23], s64 offen offset:3072 sc1\nbuffer_load_dwordx4 a[52:55], v252, s[20:23], s65 offen offset:3072 sc1\nbuffer_load_dwordx4 a[56:59], v252, s[20:23], s66 offen offset:3072 sc1\nbuffer_load_dwordx4 a[60:63], v252, s[20:23], s67 offen offset:3072 sc1\ns_waitcnt vmcnt(15) lgkmcnt(10)\n"
"v_mfma_f32_16x16x32_bf16 v[0:3], v[160:163], a[0:3], 0\ns_add_u32 s64, s70, 18878464\nbuffer_load_dwordx4 a[64:67], v252, s[20:23], s64 offen offset:0 sc1\ns_waitcnt vmcnt(12) lgkmcnt(8)\nM 0,164,16\nv_mfma_f32_16x16x32_bf16 v[16:19], v[160:163], a[4:7], 0\n"
"s_waitcnt vmcnt(11)\nM 16,164,20\nv_mfma_f32_16x16x32_bf16 v[32:35], v[160:163], a[8:11], 0\ns_waitcnt vmcnt(10)\nM 32,164,24\nv_mfma_f32_16x16x32_bf16 v[48:51], v[160:163], a[12:15], 0\n"
"s_add_u32 s65, s70, 18911232\nbuffer_load_dwordx4 a[68:71], v252, s[20:23], s65 offen offset:0 sc1\ns_waitcnt vmcnt(10)\nM 48,164,28\nG 4\nv_mfma_f32_16x16x32_bf16 v[52:55], v[168:171], a[12:15], 0\n"
"R 160,191,8192\nM 52,172,28\nv_mfma_f32_16x16x32_bf16 v[36:39], v[168:171], a[8:11], 0\nR 162,191,12288\nM 36,172,24\nv_mfma_f32_16x16x32_bf16 v[20:23], v[168:171], a[4:7], 0\n"
"R 164,191,16384\nM 20,172,20\ns_add_u32 s66, s71, 18878464\nbuffer_load_dwordx4 a[72:75], v252, s[20:23], s66 offen offset:0 sc1\nv_mfma_f32_16x16x32_bf16 v[4:7], v[168:171], a[0:3], 0\nR 166,191,20480\n"
"M 4,172,16\nG 4\nv_mfma_f32_16x16x32_bf16 v[8:11], v[176:179], a[0:3], 0\nR 168,188,24576\nM 8,180,16\nv_mfma_f32_16x16x32_bf16 v[24:27], v[176:179], a[4:7], 0\n"
"R 170,188,28672\nM 24,180,20\ns_add_u32 m0, s50, 49152\nv_mfma_f32_16x16x32_bf16 v[40:43], v[176:179], a[8:11], 0\nR 172,188,32768\ns_add_u32 s63, s52, 1181696\n"
"M 40,180,24\ns_add_u32 s67, s71, 18911232\nbuffer_load_dwordx4 a[76:79], v252, s[20:23], s67 offen offset:0 sc1\nv_mfma_f32_16x16x32_bf16 v[56:59], v[176:179], a[12:15], 0\nR 174,188,36864\nM 56,180,28\n"
"G 4\nv_mfma_f32_16x16x32_bf16 v[60:63], v[160:163], a[12:15], 0\nR 176,189,24576\nM 60,164,28\nbuffer_load_dwordx4 v220, s[4:7], s63 offen lds\nv_mfma_f32_16x16x32_bf16 v[44:47], v[160:163], a[8:11], 0\n"
"R 178,189,28672\nM 44,164,24\nbuffer_load_dwordx4 a[80:83], v252, s[20:23], s64 offen offset:1024 sc1\nv_mfma_f32_16x16x32_bf16 v[28:31], v[160:163], a[4:7], 0\nR 180,189,32768\nM 28,164,20\n"
"v_mfma_f32_16x16x32_bf16 v[12:15], v[160:163], a[0:3], 0\nR 182,189,36864\nM 12,164,16\nG 4\nv_mfma_f32_16x16x32_bf16 v[64:67], a[0:3], v[168:171], 0\nR 160,190,24576\n"
"N 64,16,172\nbuffer_load_dwordx4 a[84:87], v252, s[20:23], s65 offen offset:1024 sc1\nv_mfma_f32_16x16x32_bf16 v[68:71], a[4:7], v[168:171], 0\nR 162,190,28672\nN 68,20,172\nv_mfma_f32_16x16x32_bf16 v[96:99], a[8:11], v[168:171], 0\n"
"R 164,190,32768\nN 96,24,172\nv_mfma_f32_16x16x32_bf16 v[100:103], a[12:15], v[168:171], 0\nR 166,190,36864\nN 100,28,172\ns_barrier\n"
"G 4\nv_mfma_f32_16x16x32_bf16 v[108:111], a[12:15], v[176:179], 0\nR 168,191,24576\nN 108,28,180\nbuffer_load_dwordx4 a[88:91], v252, s[20:23], s66 offen offset:1024 sc1\nv_mfma_f32_16x16x32_bf16 v[104:107], a[8:11], v[176:179], 0\n"
"R 170,191,28672\nN 104,24,180\nv_mfma_f32_16x16x32_bf16 v[76:79], a[4:7], v[176:179], 0\nR 172,191,32768\nN 76,20,180\nbuffer_load_dwordx4 v221, s[4:7], s63 offen offset:1024 lds\n"
"v_mfma_f32_16x16x32_bf16 v[72:75], a[0:3], v[176:179], 0\nR 174,191,36864\nN 72,16,180\nG 4\nv_mfma_f32_16x16x32_bf16 v[80:83], a[0:3], v[160:163], 0\nR 176,188,40960\n"
"s_add_u32 s63, s52, 1378304\nN 80,16,164\nbuffer_load_dwordx4 a[92:95], v252, s[20:23], s67 offen offset:1024 sc1\nv_mfma_f32_16x16x32_bf16 v[84:87], a[4:7], v[160:163], 0\nR 178,188,45056\nN 84,20,164\n"
"s_add_u32 m0, s50, 57344\nv_mfma_f32_16x16x32_bf16 v[112:115], a[8:11], v[160:163], 0\nR 180,188,49152\nN 112,24,164\nbuffer_load_dwordx4 v220, s[4:7], s63 offen lds\nv_mfma_f32_16x16x32_bf16 v[116:119], a[12:15], v[160:163], 0\n"
"R 182,188,53248\nN 116,28,164\nG 4\nv_mfma_f32_16x16x32_bf16 v[124:127], a[12:15], v[168:171], 0\nR 160,189,40960\nN 124,28,172\n"
"buffer_load_dwordx4 a[96:99], v252, s[20:23], s64 offen offset:2048 sc1\nv_mfma_f32_16x16x32_bf16 v[120:123], a[8:11], v[168:171], 0\nR 162,189,45056\nN 120,24,172\nv_mfma_f32_16x16x32_bf16 v[92:95], a[4:7], v[168:171], 0\nR 164,189,49152\n"
"N 92,20,172\nbuffer_load_dwordx4 a[100:103], v252, s[20:23], s65 offen offset:2048 sc1\nv_mfma_f32_16x16x32_bf16 v[88:91], a[0:3], v[168:171], 0\nR 166,189,53248\nN 88,16,172\nG 4\n"
"s_waitcnt vmcnt(20)\nM 0,176,32\nR 168,190,40960\ns_waitcnt vmcnt(16)\nM 0,180,48\nM 16,176,36\n"
"R 170,190,45056\ns_waitcnt vmcnt(15)\nM 16,180,52\nbuffer_load_dwordx4 a[104:107], v252, s[20:23], s66 offen offset:2048 sc1\nM 32,176,40\nR 172,190,49152\n"
"s_waitcnt vmcnt(15)\nM 32,180,56\nM 48,176,44\nR 174,190,53248\ns_waitcnt vmcnt(14)\nM 48,180,60\n"
"s_barrier\nG 4\nM 52,160,44\nR 176,191,40960\nM 52,164,60\nbuffer_load_dwordx4 v221, s[4:7], s63 offen offset:1024 lds\n"
"M 36,160,40\nR 178,191,45056\nM 36,164,56\nbuffer_load_dwordx4 a[108:111], v252, s[20:23], s67 offen offset:2048 sc1\nM 20,160,36\nR 180,191,49152\n"
"M 20,164,52\nM 4,160,32\nR 182,191,53248\nM 4,164,48\nG 4\nM 8,168,32\n"
"R 160,184,0\ns_add_u32 s63, s52, 1183744\nM 8,172,48\nbuffer_load_dwordx4 a[112:115], v252, s[20:23], s64 offen offset:3072 sc1\nM 24,168,36\nR 162,184,4096\n"
"M 24,172,52\ns_add_u32 m0, s50, 65536\nM 40,168,40\nR 164,184,8192\nM 40,172,56\nbuffer_load_dwordx4 v220, s[4:7], s63 offen lds\n"
"M 56,168,44\nR 166,184,12288\nM 56,172,60\nG 4\nM 60,176,44\nR 168,185,0\n"
"M 60,180,60\nbuffer_load_dwordx4 a[116:119], v252, s[20:23], s65 offen offset:3072 sc1\nM 44,176,40\nR 170,185,4096\nM 44,180,56\nM 28,176,36\n"
"R 172,185,8192\nM 28,180,52\nM 12,176,32\nR 174,185,12288\nM 12,180,48\nG 4\n"
"N 64,32,160\nR 176,186,0\nN 64,48,164\nbuffer_load_dwordx4 a[120:123], v252, s[20:23], s66 offen offset:3072 sc1\nN 68,36,160\nR 178,186,4096\n"
"N 68,52,164\nN 96,40,160\nR 180,186,8192\nN 96,56,164\nbuffer_load_dwordx4 a[124:127], v252, s[20:23], s67 offen offset:3072 sc1\nN 100,44,160\n"
"R 182,186,12288\nN 100,60,164\ns_barrier\nG 4\nN 108,44,168\nR 160,187,0\n"
"N 108,60,172\nbuffer_load_dwordx4 v221, s[4:7], s63 offen offset:1024 lds\nN 104,40,168\nR 162,187,4096\nN 104,56,172\ns_add_u32 m0, s50, 73728\n"
"N 76,36,168\nR 164,187,8192\ns_add_u32 s63, s52, 1380352\nN 76,52,172\ns_add_u32 s64, s70, 18882560\nbuffer_load_dwordx4 a[128:131], v252, s[20:23], s64 offen offset:0 sc1\n"
"N 72,32,168\nR 166,187,12288\nN 72,48,172\nG 4\nN 80,32,176\nR 168,184,16384\n"
"N 80,48,180\nbuffer_load_dwordx4 v220, s[4:7], s63 offen lds\nN 84,36,176\nR 170,184,20480\nN 84,52,180\ns_add_u32 s65, s70, 18915328\n"
"buffer_load_dwordx4 a[132:135], v252, s[20:23], s65 offen offset:0 sc1\nN 112,40,176\nR 172,184,24576\nN 112,56,180\nN 116,44,176\nR 174,184,28672\n"
"N 116,60,180\nG 4\nN 124,44,160\nR 176,185,16384\nN 124,60,164\ns_add_u32 s66, s71, 18882560\n"
"buffer_load_dwordx4 a[136:139], v252, s[20:23], s66 offen offset:0 sc1\nN 120,40,160\nR 178,185,20480\nN 120,56,164\nN 92,36,160\nR 180,185,24576\n"
"N 92,52,164\nN 88,32,160\nR 182,185,28672\nN 88,48,164\nG 4\ns_waitcnt vmcnt(25)\n"
"M 0,168,64\nR 160,186,16384\ns_waitcnt vmcnt(20)\nM 0,172,80\ns_add_u32 s67, s71, 18915328\nbuffer_load_dwordx4 a[140:143], v252, s[20:23], s67 offen offset:0 sc1\n"
"M 16,168,68\nR 162,186,20480\ns_waitcnt vmcnt(20)\nM 16,172,84\nM 32,168,72\nR 164,186,24576\n"
"s_waitcnt vmcnt(19)\nM 32,172,88\nbuffer_load_dwordx4 v221, s[4:7], s63 offen offset:1024 lds\nM 48,168,76\nR 166,186,28672\ns_waitcnt vmcnt(18)\n"
"M 48,172,92\ns_barrier\nG 4\nM 52,176,76\nR 168,187,16384\nM 52,180,92\n"
"buffer_load_dwordx4 a[144:147], v252, s[20:23], s64 offen offset:1024 sc1\nM 36,176,72\nR 170,187,20480\nM 36,180,88\ns_add_u32 m0, s50, 81920\nM 20,176,68\n"
"R 172,187,24576\ns_add_u32 s63, s52, 1574912\nM 20,180,84\nbuffer_load_dwordx4 a[148:151], v252, s[20:23], s65 offen offset:1024 sc1\nM 4,176,64\nR 174,187,28672\n"
"M 4,180,80\nG 4\nM 8,160,64\nR 176,184,32768\nM 8,164,80\nbuffer_load_dwordx4 v220, s[4:7], s63 offen lds\n"
"M 24,160,68\nR 178,184,36864\nM 24,164,84\nM 40,160,72\nR 180,184,40960\nM 40,164,88\n"
"buffer_load_dwordx4 a[152:155], v252, s[20:23], s66 offen offset:1024 sc1\nM 56,160,76\nR 182,184,45056\nM 56,164,92\nG 4\nM 60,168,76\n"
"R 160,185,32768\nM 60,172,92\nM 44,168,72\nR 162,185,36864\nM 44,172,88\nbuffer_load_dwordx4 a[156:159], v252, s[20:23], s67 offen offset:1024 sc1\n"
"M 28,168,68\nR 164,185,40960\nM 28,172,84\nM 12,168,64\nR 166,185,45056\nM 12,172,80\n"
"G 4\nN 64,64,176\nR 168,186,32768\nN 64,80,180\nbuffer_load_dwordx4 v221, s[4:7], s63 offen offset:1024 lds\nN 68,68,176\n"
"R 170,186,36864\nN 68,84,180\nbuffer_load_dwordx4 a[160:163], v252, s[20:23], s64 offen offset:2048 sc1\nN 96,72,176\nR 172,186,40960\nN 96,88,180\n"
"N 100,76,176\nR 174,186,45056\nN 100,92,180\ns_waitcnt vmcnt(20)\ns_barrier\nG 4\n"
"N 108,76,160\nR 176,187,32768\ns_add_u32 s63, s52, 1771520\nN 108,92,164\nbuffer_load_dwordx4 a[164:167], v252, s[20:23], s65 offen offset:2048 sc1\nN 104,72,160\n"
"R 178,187,36864\nN 104,88,164\ns_add_u32 m0, s50, 90112\nN 76,68,160\nR 180,187,40960\nN 76,84,164\n"
"buffer_load_dwordx4 v220, s[4:7], s63 offen lds\nN 72,64,160\nR 182,187,45056\nN 72,80,164\nG 4\nN 80,64,168\n"
"R 160,184,49152\nN 80,80,172\nbuffer_load_dwordx4 a[168:171], v252, s[20:23], s66 offen offset:2048 sc1\nN 84,68,168\nR 162,184,53248\nN 84,84,172\n"
"N 112,72,168\nR 164,184,57344\nN 112,88,172\nbuffer_load_dwordx4 v221, s[4:7], s63 offen offset:1024 lds\nN 116,76,168\nR 166,184,61440\n"
"N 116,92,172\nG 4\nN 124,76,176\nR 168,185,49152\nN 124,92,180\nbuffer_load_dwordx4 a[172:175], v252, s[20:23], s67 offen offset:2048 sc1\n"
"N 120,72,176\nR 170,185,53248\nN 120,88,180\nN 92,68,176\nR 172,185,57344\nN 92,84,180\n"
"buffer_load_dwordx4 a[176:179], v252, s[20:23], s64 offen offset:3072 sc1\nN 88,64,176\nR 174,185,61440\nN 88,80,180\nG 4\nM 0,160,96\n"
"R 176,186,49152\ns_waitcnt vmcnt(24)\nM 0,164,112\nM 16,160,100\nR 178,186,53248\ns_waitcnt vmcnt(22)\n"
"M 16,164,116\nbuffer_load_dwordx4 a[180:183], v252, s[20:23], s65 offen offset:3072 sc1\nM 32,160,104\nR 180,186,57344\ns_add_u32 s63, s52, 1576960\ns_waitcnt vmcnt(22)\n"
"M 32,164,120\ns_add_u32 m0, s50, 98304\nM 48,160,108\nR 182,186,61440\ns_waitcnt vmcnt(21)\nM 48,164,124\n"
"s_waitcnt vmcnt(14)\ns_barrier\nG 4\nM 52,168,108\nR 160,187,49152\nM 52,172,124\n"
"buffer_load_dwordx4 v220, s[4:7], s63 offen lds\nM 36,168,104\nR 162,187,53248\nM 36,172,120\nbuffer_load_dwordx4 a[184:187], v252, s[20:23], s66 offen offset:3072 sc1\nM 20,168,100\n"
"R 164,187,57344\nM 20,172,116\nbuffer_load_dwordx4 v221, s[4:7], s63 offen offset:1024 lds\nM 4,168,96\nR 166,187,61440\ns_add_u32 m0, s50, 106496\n"
"M 4,172,112\nG 4\nM 8,176,96\nR 168,188,0\ns_add_u32 s63, s52, 1773568\nM 8,180,112\n"
"buffer_load_dwordx4 a[188:191], v252, s[20:23], s67 offen offset:3072 sc1\nM 24,176,100\nR 170,188,4096\nM 24,180,116\nbuffer_load_dwordx4 v220, s[4:7], s63 offen lds\nM 40,176,104\n"
"R 172,188,8192\nM 40,180,120\nbuffer_load_dwordx4 v221, s[4:7], s63 offen offset:1024 lds\nM 56,176,108\nR 174,188,12288\nM 56,180,124\n"
"G 4\nM 60,160,108\nR 176,189,0\nM 60,164,124\ns_add_u32 s64, s70, 18886656\nbuffer_load_dwordx4 a[192:195], v252, s[20:23], s64 offen offset:0 sc1\n"
"M 44,160,104\nR 178,189,4096\nM 44,164,120\nM 28,160,100\nR 180,189,8192\nM 28,164,116\n"
"M 12,160,96\nR 182,189,12288\nM 12,164,112\nG 4\nN 64,96,168\nR 160,190,0\n"
"N 64,112,172\ns_add_u32 s65, s70, 18919424\nbuffer_load_dwordx4 a[196:199], v252, s[20:23], s65 offen offset:0 sc1\nN 68,100,168\nR 162,190,4096\nN 68,116,172\n"
"s_add_u32 m0, s50, 114688\nN 96,104,168\nR 164,190,8192\ns_add_u32 s63, s52, 1968128\nN 96,120,172\ns_add_u32 s66, s71, 18886656\n"
"buffer_load_dwordx4 a[200:203], v252, s[20:23], s66 offen offset:0 sc1\nN 100,108,168\nR 166,190,12288\nN 100,124,172\ns_waitcnt vmcnt(12)\ns_barrier\n"
"G 4\nN 108,108,176\nR 168,191,0\nN 108,124,180\nbuffer_load_dwordx4 v220, s[4:7], s63 offen lds\nN 104,104,176\n"
"R 170,191,4096\nN 104,120,180\nN 76,100,176\nR 172,191,8192\nN 76,116,180\ns_add_u32 s67, s71, 18919424\n"
"buffer_load_dwordx4 a[204:207], v252, s[20:23], s67 offen offset:0 sc1\nN 72,96,176\nR 174,191,12288\nN 72,112,180\nG 4\nN 80,96,160\n"
"R 176,188,16384\nbuffer_load_dwordx4 v221, s[4:7], s63 offen offset:1024 lds\nN 80,112,164\ns_add_u32 m0, s50, 0\ns_add_u32 s63, s52, 2164736\nbuffer_load_dwordx4 v220, s[4:7], s63 offen lds\n"
"N 84,100,160\nR 178,188,20480\nN 84,116,164\nbuffer_load_dwordx4 a[208:211], v252, s[20:23], s64 offen offset:1024 sc1\nN 112,104,160\nR 180,188,24576\n"
"N 112,120,164\nbuffer_load_dwordx4 v221, s[4:7], s63 offen offset:1024 lds\nN 116,108,160\nR 182,188,28672\nN 116,124,164\nG 4\n"
"N 124,108,168\nR 160,189,16384\nN 124,124,172\nbuffer_load_dwordx4 a[212:215], v252, s[20:23], s65 offen offset:1024 sc1\nN 120,104,168\nR 162,189,20480\n"
"N 120,120,172\nN 92,100,168\nR 164,189,24576\nN 92,116,172\nN 88,96,168\nR 166,189,28672\n"
"N 88,112,172\nG 4\nM 0,176,128\nR 168,190,16384\nM 0,180,144\nbuffer_load_dwordx4 a[216:219], v252, s[20:23], s66 offen offset:1024 sc1\n"
"M 16,176,132\nR 170,190,20480\nM 16,180,148\nM 32,176,136\nR 172,190,24576\nM 32,180,152\n"
"s_add_u32 m0, s50, 8192\nM 48,176,140\nR 174,190,28672\ns_add_u32 s63, s52, 1970176\nM 48,180,156\ns_waitcnt vmcnt(11)\n"
"s_barrier\nG 4\nM 52,160,140\nR 176,191,16384\nM 52,164,156\nbuffer_load_dwordx4 a[220:223], v252, s[20:23], s67 offen offset:1024 sc1\n"
"M 36,160,136\nR 178,191,20480\nM 36,164,152\nbuffer_load_dwordx4 v220, s[4:7], s63 offen lds\nM 20,160,132\nR 180,191,24576\n"
"M 20,164,148\nbuffer_load_dwordx4 v221, s[4:7], s63 offen offset:1024 lds\nM 4,160,128\nR 182,191,28672\ns_add_u32 m0, s50, 16384\nM 4,164,144\n"
"G 4\nM 8,168,128\nR 160,188,32768\ns_add_u32 s63, s52, 2166784\nM 8,172,144\nbuffer_load_dwordx4 a[224:227], v252, s[20:23], s64 offen offset:2048 sc1\n"
"M 24,168,132\nR 162,188,36864\nM 24,172,148\nbuffer_load_dwordx4 v220, s[4:7], s63 offen lds\nM 40,168,136\nR 164,188,40960\n"
"M 40,172,152\nbuffer_load_dwordx4 a[228:231], v252, s[20:23], s65 offen offset:2048 sc1\nM 56,168,140\nR 166,188,45056\nM 56,172,156\nG 4\n"
"M 60,176,140\nR 168,189,32768\nbuffer_load_dwordx4 v221, s[4:7], s63 offen offset:1024 lds\nM 60,180,156\nM 44,176,136\nR 170,189,36864\n"
"M 44,180,152\nbuffer_load_dwordx4 a[232:235], v252, s[20:23], s66 offen offset:2048 sc1\nM 28,176,132\nR 172,189,40960\nM 28,180,148\nM 12,176,128\n"
"R 174,189,45056\nM 12,180,144\nG 4\nN 64,128,160\nR 176,190,32768\nN 64,144,164\n"
"N 68,132,160\nR 178,190,36864\nN 68,148,164\nbuffer_load_dwordx4 a[236:239], v252, s[20:23], s67 offen offset:2048 sc1\nN 96,136,160\nR 180,190,40960\n"
"N 96,152,164\ns_add_u32 m0, s50, 24576\nN 100,140,160\nR 182,190,45056\ns_add_u32 s63, s52, 2361344\nN 100,156,164\n"
"s_waitcnt vmcnt(11)\ns_barrier\nG 4\nN 108,140,168\nR 160,191,32768\nN 108,156,172\n"
"buffer_load_dwordx4 a[240:243], v252, s[20:23], s64 offen offset:3072 sc1\nN 104,136,168\nR 162,191,36864\nN 104,152,172\nbuffer_load_dwordx4 v220, s[4:7], s63 offen lds\nN 76,132,168\n"
"R 164,191,40960\nLq689:\ns_lshr_b32 s60, s45, 3\ns_lshl2_add_u32 s60, s60, s40\ns_lshl_b32 s60, s60, 8\nv_lshl_add_u32 v224, v252, 3, s60\n"
"v_add_u32 v224, Lq689+4096-LqPC, v224\ns_mov_b64 exec, 3\nglobal_load_dword v224, v224, s[98:99]\ns_mov_b64 exec, -1\nN 76,148,172\nbuffer_load_dwordx4 v221, s[4:7], s63 offen offset:1024 lds\n"
"N 72,128,168\nR 166,191,45056\ns_add_u32 m0, s50, 32768\nN 72,144,172\nG 4\nN 80,128,176\n"
"R 168,188,49152\ns_add_u32 s63, s52, 2557952\nN 80,144,180\nbuffer_load_dwordx4 a[244:247], v252, s[20:23], s65 offen offset:3072 sc1\nN 84,132,176\nR 170,188,53248\n"
"N 84,148,180\nbuffer_load_dwordx4 v220, s[4:7], s63 offen lds\nN 112,136,176\nR 172,184,0\nN 112,152,180\nbuffer_load_dwordx4 v221, s[4:7], s63 offen offset:1024 lds\n"
"N 116,140,176\nR 174,184,4096\nN 116,156,180\nG 4\nN 124,140,160\nR 176,189,49152\n"
"N 124,156,164\nbuffer_load_dwordx4 a[248:251], v252, s[20:23], s66 offen offset:3072 sc1\nN 120,136,160\nR 178,189,53248\nN 120,152,164\nN 92,132,160\n"
"R 180,185,0\nN 92,148,164\nbuffer_load_dwordx4 a[252:255], v252, s[20:23], s67 offen offset:3072 sc1\nN 88,128,160\nR 182,185,4096\nN 88,144,164\n"
"G 4\nM 0,168,160\nR 160,190,49152\nM 0,172,176\nM 16,168,164\nR 162,190,53248\n"
"M 16,172,180\ns_lshr_b32 s84, s49, 3\nM 32,168,168\nR 164,186,0\ns_add_u32 m0, s50, 40960\nM 32,172,184\n"
"s_add_u32 s84, s84, 82837504\nM 48,168,172\nR 166,186,4096\ns_add_u32 s63, s52, 2754560\nM 48,172,188\ns_waitcnt vmcnt(11)\n"
"s_barrier\nG 4\nM 52,176,172\nR 168,191,49152\nM 52,180,188\nbuffer_load_dwordx4 v220, s[4:7], s63 offen lds\n"
"M 36,176,168\nR 170,191,53248\nM 36,180,184\nbuffer_load_dwordx4 v[128:131], v252, s[20:23], s84 offen offset:0 sc1\nM 20,176,164\nR 172,187,0\n"
"M 20,180,180\nbuffer_load_dwordx4 v[132:135], v252, s[20:23], s84 offen offset:1024 sc1\nM 4,176,160\nR 174,187,4096\nM 4,180,176\nG 4\n"
"M 8,160,160\nR 176,184,8192\nbuffer_load_dwordx4 v221, s[4:7], s63 offen offset:1024 lds\nM 8,164,176\nbuffer_load_dwordx4 v[136:139], v252, s[20:23], s84 offen offset:2048 sc1\ns_add_u32 s63, s52, 2951168\n"
"M 24,160,164\nR 178,184,12288\ns_add_u32 m0, s50, 49152\nM 24,164,180\nbuffer_load_dwordx4 v[140:143], v252, s[20:23], s84 offen offset:3072 sc1\nM 40,160,168\n"
"R 180,184,16384\nbuffer_load_dwordx4 v220, s[4:7], s63 offen lds\nM 40,164,184\ns_lshr_b32 s85, s49, 3\nM 56,160,172\nR 182,184,20480\n"
"s_add_u32 s85, s85, 82841600\nM 56,164,188\nbuffer_load_dwordx4 v[144:147], v252, s[20:23], s85 offen offset:0 sc1\nG 4\nM 60,168,172\nR 160,185,8192\n"
"buffer_load_dwordx4 v221, s[4:7], s63 offen offset:1024 lds\nM 60,172,188\nbuffer_load_dwordx4 v[148:151], v252, s[20:23], s85 offen offset:1024 sc1\nM 44,168,168\nR 162,185,12288\nM 44,172,184\n"
"M 28,168,164\nR 164,185,16384\nM 28,172,180\nbuffer_load_dwordx4 v[152:155], v252, s[20:23], s85 offen offset:2048 sc1\nM 12,168,160\nR 166,185,20480\n"
"M 12,172,176\nG 4\nN 64,160,176\nR 168,186,8192\nN 64,176,180\nbuffer_load_dwordx4 v[156:159], v252, s[20:23], s85 offen offset:3072 sc1\n"
"N 68,164,176\nR 170,186,12288\nN 68,180,180\ns_add_u32 m0, s50, 57344\nN 96,168,176\nR 172,186,16384\n"
"N 96,184,180\ns_add_u32 s63, s52, 2363392\nN 100,172,176\nR 174,186,20480\nN 100,188,180\ns_waitcnt vmcnt(14)\n"
"s_barrier\nG 4\nN 108,172,160\nR 176,187,8192\nN 108,188,164\nbuffer_load_dwordx4 v220, s[4:7], s63 offen lds\n"
"N 104,168,160\nR 178,187,12288\nN 104,184,164\nN 76,164,160\nR 180,187,16384\nN 76,180,164\n"
"buffer_load_dwordx4 v221, s[4:7], s63 offen offset:1024 lds\nN 72,160,160\nR 182,187,20480\ns_add_u32 m0, s50, 65536\nN 72,176,164\nG 4\n"
"N 80,160,168\nR 160,184,24576\ns_add_u32 s63, s52, 2560000\nN 80,176,172\nbuffer_load_dwordx4 v220, s[4:7], s63 offen lds\nN 84,164,168\n"
"R 162,184,28672\nN 84,180,172\nN 112,168,168\nR 164,184,32768\nN 112,184,172\nbuffer_load_dwordx4 v221, s[4:7], s63 offen offset:1024 lds\n"
"N 116,172,168\nR 166,184,36864\nN 116,188,172\nG 4\nN 124,172,176\nR 168,185,24576\n"
"N 124,188,180\nN 120,168,176\nR 170,185,28672\nN 120,184,180\ns_add_u32 m0, s50, 73728\nN 92,164,176\n"
"R 172,185,32768\nN 92,180,180\ns_add_u32 s63, s52, 2756608\nN 88,160,176\nR 174,185,36864\nN 88,176,180\n"
"G 4\nM 0,160,192\nR 176,186,24576\nM 0,164,208\nbuffer_load_dwordx4 v220, s[4:7], s63 offen lds\nM 16,160,196\n"
"R 178,186,28672\nM 16,164,212\nM 32,160,200\nR 180,186,32768\nM 32,164,216\nbuffer_load_dwordx4 v221, s[4:7], s63 offen offset:1024 lds\n"
"M 48,160,204\nR 182,186,36864\ns_add_u32 m0, s50, 81920\nM 48,164,220\ns_waitcnt vmcnt(9)\ns_barrier\n"
"G 4\nM 52,168,204\nR 160,187,24576\ns_add_u32 s63, s52, 2953216\nM 52,172,220\nbuffer_load_dwordx4 v220, s[4:7], s63 offen lds\n"
"M 36,168,200\nR 162,187,28672\nv_min3_u32 v230, v129, v131, v133\nv_min3_u32 v230, v230, v135, v137\nM 36,172,216\nv_min3_u32 v230, v230, v139, v141\n"
"v_min3_u32 v230, v230, v143, v145\ns_waitcnt vmcnt(9)\nv_min3_u32 v230, v230, v147, v149\nM 20,168,196\nR 164,187,32768\ns_waitcnt vmcnt(8)\n"
"v_min3_u32 v230, v230, v151, v153\ns_waitcnt vmcnt(7)\nv_min3_u32 v230, v230, v155, v157\nM 20,172,212\nbuffer_load_dwordx4 v221, s[4:7], s63 offen offset:1024 lds\nv_min3_u32 v230, v230, v159, v159\n"
"M 4,168,192\nR 166,187,36864\ns_add_u32 m0, s50, 90112\nv_max3_u32 v231, v129, v131, v133\nM 4,172,208\nG 4\n"
"v_max3_u32 v231, v231, v135, v137\nv_max3_u32 v231, v231, v139, v141\nv_max3_u32 v231, v231, v143, v145\nM 8,176,192\nR 168,184,40960\ns_add_u32 s63, s52, 0\n"
"v_max3_u32 v231, v231, v147, v149\nM 8,180,208\nbuffer_load_dwordx4 v220, s[4:7], s63 offen lds\nv_max3_u32 v231, v231, v151, v153\nM 24,176,196\nR 170,184,45056\n"
"v_max3_u32 v231, v231, v155, v157\nv_max3_u32 v231, v231, v159, v159\nM 24,180,212\nv_cmp_ne_u32 s[86:87], s46, v230\nv_cmp_ne_u32 s[88:89], s46, v231\nM 40,176,200\n"
"R 172,184,49152\ns_or_b64 s[86:87], s[86:87], s[88:89]\ns_cbranch_scc1 Lq643\nLq644:\nM 40,180,216\nv_add_f32 v128, v128, v144\n"
"v_add_f32 v130, v130, v146\nv_add_f32 v132, v132, v148\nM 56,176,204\nR 174,184,53248\nv_add_f32 v134, v134, v150\nv_add_f32 v136, v136, v152\n"
"M 56,180,220\nG 4\nv_add_f32 v138, v138, v154\nv_add_f32 v140, v140, v156\nv_add_f32 v142, v142, v158\nM 60,160,204\n"
"R 176,185,40960\nv_add_f32 v128, v128, v136\nv_add_f32 v130, v130, v138\nM 60,164,220\nbuffer_load_dwordx4 v221, s[4:7], s63 offen offset:1024 lds\nv_add_f32 v132, v132, v140\n"
"M 44,160,200\nR 178,185,45056\ns_add_u32 s63, s52, 196608\nv_add_f32 v134, v134, v142\nM 44,164,216\ns_add_u32 m0, s50, 98304\n"
"v_add_f32 v128, v128, v132\nv_add_f32 v130, v130, v134\nv_lshrrev_b32 v231, 1, v252\nM 28,160,196\nR 180,185,49152\nv_lshrrev_b32 v230, 2, v252\n"
"s_lshl_b32 s62, s40, 9\ns_add_u32 s62, s62, 159232\nv_add_u32 v231, s62, v231\nM 28,164,212\nbuffer_load_dwordx4 v220, s[4:7], s63 offen lds\nds_write2_b32 v231, v128, v130 offset1:1\n"
"M 12,160,192\nR 182,185,53248\ns_lshl_b32 s62, s40, 9\ns_add_u32 s62, s62, 159232\nv_add_u32 v230, s62, v230\nM 12,164,208\n"
"ds_read2st64_b32 v[232:233], v230 offset1:1\nG 6\nM 0,168,224\nR 160,186,40960\nM 0,172,240\nbuffer_load_dwordx4 v221, s[4:7], s63 offen offset:1024 lds\n"
"M 16,168,228\nR 162,186,45056\ns_add_u32 s63, s52, 393216\nM 16,172,244\ns_add_u32 m0, s50, 106496\nM 32,168,232\n"
"R 164,186,49152\nM 32,172,248\nbuffer_load_dwordx4 v220, s[4:7], s63 offen lds\nG 3\nv_add_f32 v232, v232, v233\nM 48,168,236\n"
"R 166,186,53248\nv_mov_b32 v233, v232\ns_nop 1\nv_permlane32_swap_b32 v232, v233\nM 48,172,252\ns_waitcnt vmcnt(9)\n"
"s_barrier\nv_mov_b32 v230, v232\nM 52,176,236\nR 168,187,40960\nv_mov_b32 v231, v233\nv_permlane16_swap_b32 v230, v232\n"
"M 52,180,252\nbuffer_load_dwordx4 v221, s[4:7], s63 offen offset:1024 lds\nv_permlane16_swap_b32 v231, v233\nM 36,176,232\nR 170,187,45056\nv_mul_f32 v230, 0xba800000, v230\n"
"v_mul_f32 v214, v230, v230\nM 36,180,248\ns_add_u32 m0, s50, 114688\nv_mul_f32 v231, 0x3a800000, v231\nv_sub_f32 v231, v231, v214\nv_max_f32 v231, 0, v231\n"
"M 20,176,228\nR 172,187,49152\nv_add_f32 v231, 0x3727c5ac, v231\nv_rsq_f32 v234, v231\nM 20,180,244\ns_add_u32 s63, s52, 589824\n"
"v_and_b32 v208, s77, v230\nv_mul_f32 v216, v231, v234\nv_sub_f32 v230, v230, v208\nM 4,176,224\nR 174,187,53248\nv_cvt_pk_bf16_f32 v209, v230, v216\n"
"v_cvt_pk_bf16_f32 v208, v208, v208\nM 4,180,240\ns_mov_b64 s[60:61], 0xffff\nv_cndmask_b32 v208, 0, v208, s[60:61]\nG 4\nv_mov_b32 v214, v234\n"
"M 8,160,224\nR 176,184,57344\nv_mul_f32 v215, 0x3e8293ee, v234\nv_mul_f32 v232, 0xba800000, v232\nM 8,164,240\nbuffer_load_dwordx4 v220, s[4:7], s63 offen lds\n"
"v_mul_f32 v217, v232, v232\nM 24,160,228\nR 178,184,61440\nv_mul_f32 v233, 0x3a800000, v233\nv_sub_f32 v233, v233, v217\nM 24,164,244\n"
"s_mov_b64 s[60:61], 0xffff\nv_cndmask_b32 v209, 0, v209, s[60:61]\nv_max_f32 v233, 0, v233\nv_add_f32 v233, 0x3727c5ac, v233\nM 40,160,232\nR 180,188,0\n"
"v_and_b32 v212, s77, v232\nv_rsq_f32 v235, v233\nM 40,164,248\nbuffer_load_dwordx4 v221, s[4:7], s63 offen offset:1024 lds\nv_mul_f32 v219, v233, v235\nM 56,160,236\n"
"R 182,188,4096\ns_add_u32 m0, s50, 0\nv_sub_f32 v232, v232, v212\nM 56,164,252\nG 4\nv_cvt_pk_bf16_f32 v213, v232, v219\n"
"v_cvt_pk_bf16_f32 v212, v212, v212\nv_mov_b32 v217, v235\nM 60,168,236\nR 160,185,57344\ns_add_u32 s63, s52, 786432\nv_mul_f32 v218, 0x3e8293ee, v235\n"
"M 60,172,252\nbuffer_load_dwordx4 v220, s[4:7], s63 offen lds\nv_bfe_u32 v230, v252, 2, 6\nM 44,168,232\nR 162,185,61440\ns_mov_b64 s[60:61], 0xffff\n"
"v_cndmask_b32 v212, 0, v212, s[60:61]\nM 44,172,248\ns_mov_b64 s[60:61], 0xffff\nv_cndmask_b32 v213, 0, v213, s[60:61]\ns_lshl_b32 s60, s40, 7\ns_add_u32 s60, s60, 161280\n"
"v_add_u32 v230, s60, v230\nM 28,168,228\nR 164,189,0\nds_write_b32 v230, v214 offset:0\nM 28,172,244\nbuffer_load_dwordx4 v221, s[4:7], s63 offen offset:1024 lds\n"
"ds_write_b32 v230, v217 offset:64\nM 12,168,224\nR 166,189,4096\ns_mov_b32 s60, 156160\nv_bfe_u32 v246, v252, 4, 4\nv_lshl_add_u32 v246, v246, 3, s60\n"
"M 12,172,240\nds_read_b64 v[238:239], v246 offset:0\ns_waitcnt vmcnt(10) lgkmcnt(0)\ns_barrier\nN 64,192,176\nR 168,186,57344\n"
"ds_read_b64 v[240:241], v246 offset:128\nN 64,208,180\ns_mov_b32 m0, s51\nds_read_b64 v[242:243], v246 offset:256\nN 68,196,176\nR 170,186,61440\n"
"ds_read_b64 v[244:245], v246 offset:384\nds_write_addtid_b32 v32 offset:0\nN 68,212,180\nds_read_b64 v[246:247], v246 offset:0\nds_write_addtid_b32 v33 offset:256\nN 96,200,176\n"
"R 172,190,0\nds_write_addtid_b32 v34 offset:512\nN 96,216,180\nds_write_addtid_b32 v35 offset:768\nds_write_addtid_b32 v36 offset:1024\nN 100,204,176\n"
"R 174,190,4096\nds_write_addtid_b32 v37 offset:1280\nN 100,220,180\nds_write_addtid_b32 v38 offset:1536\nds_write_addtid_b32 v39 offset:1792\nN 108,204,160\n"
"R 176,187,57344\nds_write_addtid_b32 v40 offset:2048\nN 108,220,164\nds_write_addtid_b32 v41 offset:2304\nds_write_addtid_b32 v42 offset:2560\nN 104,200,160\n"
"R 178,187,61440\nds_write_addtid_b32 v43 offset:2816\nN 104,216,164\nds_write_addtid_b32 v44 offset:3072\nds_write_addtid_b32 v45 offset:3328\nN 76,196,160\n"
"R 180,191,0\nG 15\nv_mfma_f32_16x16x32_bf16 v[0:3], v[238:241], v[208:211], v[0:3]\nds_write_addtid_b32 v46 offset:3584\nN 76,212,164\nv_mfma_f32_16x16x32_bf16 v[4:7], v[240:243], v[208:211], v[4:7]\n"
"ds_write_addtid_b32 v47 offset:3840\nds_write_addtid_b32 v48 offset:4096\nN 72,192,160\nR 182,191,4096\nv_mfma_f32_16x16x32_bf16 v[8:11], v[242:245], v[208:211], v[8:11]\nds_write_addtid_b32 v49 offset:4352\n"
"N 72,208,164\nv_mfma_f32_16x16x32_bf16 v[12:15], v[244:247], v[208:211], v[12:15]\nds_write_addtid_b32 v50 offset:4608\nds_write_addtid_b32 v51 offset:4864\nG 15\nN 80,192,168\n"
"R 160,188,8192\nv_mfma_f32_16x16x32_bf16 v[16:19], v[244:247], v[210:213], v[16:19]\nds_write_addtid_b32 v52 offset:5120\nN 80,208,172\nv_mfma_f32_16x16x32_bf16 v[20:23], v[238:241], v[210:213], v[20:23]\nds_write_addtid_b32 v53 offset:5376\n"
"ds_write_addtid_b32 v54 offset:5632\nN 84,196,168\nR 162,188,12288\nv_mfma_f32_16x16x32_bf16 v[24:27], v[240:243], v[210:213], v[24:27]\nds_write_addtid_b32 v55 offset:5888\nN 84,212,172\n"
"v_mfma_f32_16x16x32_bf16 v[28:31], v[242:245], v[210:213], v[28:31]\nds_write_addtid_b32 v56 offset:6144\nds_write_addtid_b32 v57 offset:6400\nN 112,200,168\nR 164,188,16384\nds_write_addtid_b32 v58 offset:6656\n"
"N 112,216,172\nds_write_addtid_b32 v59 offset:6912\nds_write_addtid_b32 v60 offset:7168\nN 116,204,168\nR 166,188,20480\nds_write_addtid_b32 v61 offset:7424\n"
"N 116,220,172\nds_write_addtid_b32 v62 offset:7680\nds_write_addtid_b32 v63 offset:7936\nG 15\nN 124,204,176\nR 168,189,8192\n"
"s_add_u32 m0, s50, 8192\ns_add_u32 s63, s52, 983040\nN 124,220,180\nbuffer_load_dwordx4 v220, s[4:7], s63 offen lds\nN 120,200,176\nR 170,189,12288\n"
"s_waitcnt vmcnt(7) lgkmcnt(2)\ns_barrier\nN 120,216,180\nds_read2st64_b32 v[32:33], v225 offset0:0 offset1:1\nds_read2st64_b32 v[34:35], v225 offset0:2 offset1:3\nN 92,196,176\n"
"R 172,189,16384\nds_read2st64_b32 v[36:37], v225 offset0:4 offset1:5\nN 92,212,180\nbuffer_load_dwordx4 v221, s[4:7], s63 offen offset:1024 lds\nds_read2st64_b32 v[38:39], v225 offset0:6 offset1:7\nN 88,192,176\n"
"R 174,189,20480\nds_read2st64_b32 v[40:41], v225 offset0:8 offset1:9\nN 88,208,180\nds_read2st64_b32 v[42:43], v225 offset0:10 offset1:11\nds_read2st64_b32 v[44:45], v225 offset0:12 offset1:13\nN 64,224,160\n"
"R 176,190,8192\ns_mov_b32 s60, 156672\nv_bfe_u32 v246, v252, 4, 4\nv_lshl_add_u32 v246, v246, 3, s60\nds_read2st64_b32 v[46:47], v225 offset0:14 offset1:15\nN 64,240,164\n"
"ds_read_b64 v[238:239], v246 offset:0\nG 11\nv_add_f32 v0, v0, v32\nv_add_f32 v1, v1, v33\nN 68,228,160\nR 178,190,12288\n"
"ds_read_b64 v[240:241], v246 offset:128\nG 12\nv_add_f32 v2, v2, v34\nN 68,244,164\nds_read_b64 v[242:243], v246 offset:256\ns_add_u32 m0, s50, 16384\n"
"v_add_f32 v3, v3, v35\nN 96,232,160\nR 180,190,16384\nds_read_b64 v[244:245], v246 offset:384\nG 13\nv_add_f32 v4, v4, v36\n"
"N 96,248,164\nds_read_b64 v[246:247], v246 offset:0\ns_add_u32 s63, s52, 1179648\nv_add_f32 v5, v5, v37\nN 100,236,160\nR 182,190,20480\n"
"G 14\nv_add_f32 v6, v6, v38\nv_add_f32 v7, v7, v39\nN 100,252,164\nds_read2st64_b32 v[48:49], v225 offset0:16 offset1:17\nG 14\n"
"G 13\nv_add_f32 v8, v8, v40\nN 108,236,168\nR 160,191,8192\nv_add_f32 v9, v9, v41\nG 13\n"
"v_add_f32 v10, v10, v42\nN 108,252,172\nbuffer_load_dwordx4 v220, s[4:7], s63 offen lds\nv_add_f32 v11, v11, v43\nN 104,232,168\nR 162,191,12288\n"
"G 13\nv_add_f32 v12, v12, v44\nv_add_f32 v13, v13, v45\nN 104,248,172\nds_read2st64_b32 v[50:51], v225 offset0:18 offset1:19\nG 12\n"
"v_add_f32 v14, v14, v46\nv_add_f32 v15, v15, v47\nN 76,228,168\nR 164,191,16384\nds_read2st64_b32 v[52:53], v225 offset0:20 offset1:21\nN 76,244,172\n"
"G 11\nv_mfma_f32_16x16x32_bf16 v[64:67], v[208:211], v[238:241], v[64:67]\nbuffer_load_dwordx4 v221, s[4:7], s63 offen offset:1024 lds\nG 5\nv_add_f32 v16, v16, v48\nN 72,224,168\n"
"R 166,191,20480\ns_add_u32 m0, s50, 24576\nv_add_f32 v17, v17, v49\nN 72,240,172\nv_mfma_f32_16x16x32_bf16 v[72:75], v[208:211], v[240:243], v[72:75]\nds_read2st64_b32 v[54:55], v225 offset0:22 offset1:23\n"
"ds_read2st64_b32 v[56:57], v225 offset0:24 offset1:25\nN 80,224,176\nR 168,188,24576\ns_add_u32 s63, s52, 1376256\nds_read2st64_b32 v[58:59], v225 offset0:26 offset1:27\nN 80,240,180\n"
"v_mfma_f32_16x16x32_bf16 v[80:83], v[208:211], v[242:245], v[80:83]\nbuffer_load_dwordx4 v220, s[4:7], s63 offen lds\nG 7\nv_add_f32 v18, v18, v50\nN 84,228,176\nR 170,188,28672\n"
"v_add_f32 v19, v19, v51\nG 6\nv_add_f32 v20, v20, v52\nN 84,244,180\nv_mfma_f32_16x16x32_bf16 v[88:91], v[208:211], v[244:247], v[88:91]\nbuffer_load_dwordx4 v221, s[4:7], s63 offen offset:1024 lds\n"
"v_add_f32 v21, v21, v53\nN 112,232,176\nR 172,188,32768\ns_add_u32 s63, s52, 1572864\nds_read2st64_b32 v[60:61], v225 offset0:28 offset1:29\nN 112,248,180\n"
"v_mfma_f32_16x16x32_bf16 v[68:71], v[210:213], v[244:247], v[68:71]\ns_add_u32 m0, s50, 49152\nds_read2st64_b32 v[62:63], v225 offset0:30 offset1:31\nG 7\nv_add_f32 v22, v22, v54\nN 116,236,176\n"
"R 174,188,36864\nv_add_f32 v23, v23, v55\nG 7\nv_add_f32 v24, v24, v56\nN 116,252,180\nv_mfma_f32_16x16x32_bf16 v[76:79], v[210:213], v[238:241], v[76:79]\n"
"v_add_f32 v25, v25, v57\nG 5\nv_add_f32 v26, v26, v58\nv_add_f32 v27, v27, v59\nN 124,236,160\nR 176,189,24576\n"
"N 124,252,164\nv_mfma_f32_16x16x32_bf16 v[84:87], v[210:213], v[240:243], v[84:87]\nbuffer_load_dwordx4 v220, s[4:7], s63 offen lds\nG 3\nv_add_f32 v28, v28, v60\nv_add_f32 v29, v29, v61\n"
"G 2\nv_add_f32 v30, v30, v62\nv_add_f32 v31, v31, v63\nN 120,232,160\nR 178,189,28672\ns_lshl4_add_u32 s64, s41, s42\n"
"v_lshrrev_b32 v227, 1, v252\nN 120,248,164\nv_mfma_f32_16x16x32_bf16 v[92:95], v[210:213], v[242:245], v[92:95]\nbuffer_load_dwordx4 v221, s[4:7], s63 offen offset:1024 lds\nv_mul_f32 v16, v16, v217\nN 92,228,160\n"
"R 180,189,32768\ns_add_u32 m0, s50, 57344\ns_add_u32 s63, s52, 1769472\nv_mul_f32 v17, v17, v217\nN 92,244,164\nbuffer_load_dwordx4 v220, s[4:7], s63 offen lds\n"
"v_mul_f32 v18, v18, v217\nN 88,224,160\nR 182,189,36864\ns_lshl_b32 s64, s64, 12\nv_mul_f32 v19, v19, v217\nN 88,240,164\n"
"s_waitcnt vmcnt(11)\ns_barrier\nG 4\nv_mul_f32 v20, v20, v217\nv_mfma_f32_16x16x32_bf16 v[128:131], v[168:171], a[0:3], 0\nR 160,190,24576\n"
"s_add_u32 s64, s64, 78643200\nv_mul_f32 v21, v21, v217\nM 128,172,16\ns_mov_b32 m0, s51\ns_cmp_eq_u32 s40, 3\ns_cselect_b64 s[66:67], -1, 0\n"
"v_mul_f32 v22, v22, v217\nv_mfma_f32_16x16x32_bf16 v[144:147], v[168:171], a[4:7], 0\nR 162,190,28672\nv_mul_f32 v23, v23, v217\nv_mul_f32 v24, v24, v217\nM 144,172,20\n"
"v_mul_f32 v25, v25, v217\nv_mul_f32 v26, v26, v217\nv_mul_f32 v27, v27, v217\nv_mfma_f32_16x16x32_bf16 v[32:35], v[168:171], a[8:11], 0\nR 164,190,32768\nv_mul_f32 v28, v28, v217\n"
"v_mul_f32 v29, v29, v217\nM 32,172,24\nv_mul_f32 v30, v30, v217\nv_mul_f32 v31, v31, v217\nv_cvt_pk_bf16_f32 v16, v16, v17\nv_mfma_f32_16x16x32_bf16 v[48:51], v[168:171], a[12:15], 0\n"
"R 166,190,36864\nv_cvt_pk_bf16_f32 v17, v18, v19\nv_cvt_pk_bf16_f32 v18, v20, v21\nM 48,172,28\nds_write_addtid_b32 v96 offset:0\nG 5\n"
"v_cvt_pk_bf16_f32 v19, v22, v23\nv_cvt_pk_bf16_f32 v20, v24, v25\nv_cvt_pk_bf16_f32 v21, v26, v27\nv_mfma_f32_16x16x32_bf16 v[52:55], v[176:179], a[12:15], 0\nR 168,191,24576\nds_write_addtid_b32 v97 offset:256\n"
"v_cvt_pk_bf16_f32 v22, v28, v29\nv_cvt_pk_bf16_f32 v23, v30, v31\nM 52,180,28\nds_write_addtid_b32 v98 offset:512\nv_mul_f32 v0, v0, v214\nv_mul_f32 v1, v1, v214\n"
"v_mul_f32 v2, v2, v214\nv_mfma_f32_16x16x32_bf16 v[36:39], v[176:179], a[8:11], 0\nR 170,191,28672\nds_write_addtid_b32 v99 offset:768\nv_mul_f32 v3, v3, v214\nv_mul_f32 v4, v4, v214\n"
"M 36,180,24\nds_write_addtid_b32 v100 offset:1024\nv_mul_f32 v5, v5, v214\nv_mul_f32 v6, v6, v214\nv_mul_f32 v7, v7, v214\nv_mfma_f32_16x16x32_bf16 v[148:151], v[176:179], a[4:7], 0\n"
"R 172,191,32768\nds_write_addtid_b32 v101 offset:1280\nv_mul_f32 v8, v8, v214\nv_mul_f32 v9, v9, v214\nM 148,180,20\nds_write_addtid_b32 v102 offset:1536\n"
"v_mul_f32 v10, v10, v214\nv_mul_f32 v11, v11, v214\nv_mul_f32 v12, v12, v214\nv_mfma_f32_16x16x32_bf16 v[132:135], v[176:179], a[0:3], 0\nR 174,191,36864\nds_write_addtid_b32 v103 offset:1792\n"
"v_mul_f32 v13, v13, v214\nv_mul_f32 v14, v14, v214\nM 132,180,16\nds_write_addtid_b32 v104 offset:2048\nG 13\nv_mul_f32 v15, v15, v214\n"
"v_cvt_pk_bf16_f32 v0, v0, v1\nv_cvt_pk_bf16_f32 v1, v2, v3\nv_mfma_f32_16x16x32_bf16 v[136:139], v[160:163], a[0:3], 0\nR 176,188,40960\nds_write_addtid_b32 v105 offset:2304\nv_cvt_pk_bf16_f32 v2, v4, v5\n"
"v_cvt_pk_bf16_f32 v3, v6, v7\nM 136,164,16\nds_write_addtid_b32 v106 offset:2560\nv_cvt_pk_bf16_f32 v4, v8, v9\nv_cvt_pk_bf16_f32 v5, v10, v11\nv_cvt_pk_bf16_f32 v6, v12, v13\n"
"v_mfma_f32_16x16x32_bf16 v[152:155], v[160:163], a[4:7], 0\nR 178,188,45056\nds_write_addtid_b32 v107 offset:2816\ns_mov_b64 exec, s[66:67]\nbuffer_store_dwordx4 v[16:19], v252, s[20:23], s64 offen offset:0 sc1\nbuffer_store_dwordx4 v[20:23], v252, s[20:23], s64 offen offset:1024 sc1\n"
"s_mov_b64 exec, -1\nv_cvt_pk_bf16_f32 v7, v14, v15\nM 152,164,20\nds_write_addtid_b32 v108 offset:3072\nds_write_addtid_b32 v109 offset:3328\nv_mfma_f32_16x16x32_bf16 v[40:43], v[160:163], a[8:11], 0\n"
"R 180,188,49152\nds_write_addtid_b32 v110 offset:3584\nM 40,164,24\nds_write_addtid_b32 v111 offset:3840\nds_write_addtid_b32 v112 offset:4096\nv_mfma_f32_16x16x32_bf16 v[56:59], v[160:163], a[12:15], 0\n"
"R 182,188,53248\nds_write_addtid_b32 v113 offset:4352\nM 56,164,28\nds_write_addtid_b32 v114 offset:4608\nds_write_addtid_b32 v115 offset:4864\nG 15\n"
"v_mfma_f32_16x16x32_bf16 v[60:63], v[168:171], a[12:15], 0\nR 160,189,40960\nds_write_addtid_b32 v116 offset:5120\nM 60,172,28\nds_write_addtid_b32 v117 offset:5376\nds_write_addtid_b32 v118 offset:5632\n"
"v_mfma_f32_16x16x32_bf16 v[44:47], v[168:171], a[8:11], 0\nR 162,189,45056\nds_write_addtid_b32 v119 offset:5888\nM 44,172,24\nds_write_addtid_b32 v120 offset:6144\nds_write_addtid_b32 v121 offset:6400\n"
"ds_write_addtid_b32 v122 offset:6656\nds_write_addtid_b32 v123 offset:6912\nds_write_addtid_b32 v124 offset:7168\nds_write_addtid_b32 v125 offset:7424\nds_write_addtid_b32 v126 offset:7680\nds_write_addtid_b32 v127 offset:7936\n"
"v_mfma_f32_16x16x32_bf16 v[156:159], v[168:171], a[4:7], 0\nR 164,189,49152\ns_add_u32 m0, s50, 57344\nM 156,172,20\nbuffer_load_dwordx4 v221, s[4:7], s63 offen offset:1024 lds\nv_mfma_f32_16x16x32_bf16 v[140:143], v[168:171], a[0:3], 0\n"
"R 166,189,53248\ns_waitcnt vmcnt(10) lgkmcnt(2)\ns_barrier\nM 140,172,16\nds_read2st64_b32 v[96:97], v225 offset0:0 offset1:1\nds_read2st64_b32 v[98:99], v225 offset0:2 offset1:3\n"
"v_lshrrev_b32 v226, 8, v252\nM 128,176,32\nR 168,190,40960\ns_add_u32 s63, s52, 1966080\nds_read2st64_b32 v[100:101], v225 offset0:4 offset1:5\ns_lshl_b32 s60, s40, 7\n"
"s_add_u32 s60, s60, 161280\nv_lshl_add_u32 v226, v226, 4, s60\nM 128,180,48\ns_add_u32 m0, s50, 65536\nds_read2st64_b32 v[102:103], v225 offset0:6 offset1:7\nds_read2st64_b32 v[104:105], v225 offset0:8 offset1:9\n"
"ds_read_b128 v[230:233], v226 offset:0\nM 144,176,36\nR 170,190,45056\nds_read2st64_b32 v[106:107], v225 offset0:10 offset1:11\nds_read_b128 v[234:237], v226 offset:64\nM 144,180,52\n"
"buffer_load_dwordx4 v220, s[4:7], s63 offen lds\nds_read2st64_b32 v[108:109], v225 offset0:12 offset1:13\nM 32,176,40\nR 172,190,49152\nds_read2st64_b32 v[110:111], v225 offset0:14 offset1:15\nM 32,180,56\n"
"ds_read2st64_b32 v[112:113], v225 offset0:16 offset1:17\nG 13\nv_add_f32 v64, v64, v96\nv_add_f32 v65, v65, v97\nM 48,176,44\nR 174,190,53248\n"
"G 13\nv_add_f32 v66, v66, v98\nv_add_f32 v67, v67, v99\nM 48,180,60\nds_read2st64_b32 v[114:115], v225 offset0:18 offset1:19\nG 12\n"
"v_add_f32 v68, v68, v100\nM 52,160,44\nR 176,191,40960\nv_add_f32 v69, v69, v101\nG 12\nv_add_f32 v70, v70, v102\n"
"M 52,164,60\nbuffer_load_dwordx4 v221, s[4:7], s63 offen offset:1024 lds\nv_add_f32 v71, v71, v103\nM 36,160,40\nR 178,191,45056\nG 12\n"
"v_add_f32 v72, v72, v104\nv_add_f32 v73, v73, v105\nM 36,164,56\ns_add_u32 m0, s50, 73728\nds_read2st64_b32 v[116:117], v225 offset0:20 offset1:21\nG 10\n"
"v_add_f32 v74, v74, v106\nM 148,160,36\nR 180,191,49152\nv_add_f32 v75, v75, v107\nG 9\nv_add_f32 v76, v76, v108\n"
"M 148,164,52\ns_add_u32 s63, s52, 2162688\nds_read2st64_b32 v[118:119], v225 offset0:22 offset1:23\nv_add_f32 v77, v77, v109\nM 132,160,32\nR 182,191,53248\n"
"G 9\nv_add_f32 v78, v78, v110\nv_add_f32 v79, v79, v111\nM 132,164,48\nds_read2st64_b32 v[120:121], v225 offset0:24 offset1:25\nG 8\n"
"v_add_f32 v80, v80, v112\nM 136,168,32\nR 160,184,0\nv_add_f32 v81, v81, v113\nG 8\nv_add_f32 v82, v82, v114\n"
"M 136,172,48\nbuffer_load_dwordx4 v220, s[4:7], s63 offen lds\nv_add_f32 v83, v83, v115\nM 152,168,36\nR 162,184,4096\nG 6\n"
"v_add_f32 v84, v84, v116\nv_add_f32 v85, v85, v117\nM 152,172,52\nds_read2st64_b32 v[122:123], v225 offset0:26 offset1:27\nG 5\nv_add_f32 v86, v86, v118\n"
"v_add_f32 v87, v87, v119\nM 40,168,40\nR 164,184,8192\nds_read2st64_b32 v[124:125], v225 offset0:28 offset1:29\nM 40,172,56\nbuffer_load_dwordx4 v221, s[4:7], s63 offen offset:1024 lds\n"
"G 5\nv_add_f32 v88, v88, v120\nM 56,168,44\nR 166,184,12288\ns_add_u32 m0, s50, 81920\nM 56,172,60\n"
"ds_read2st64_b32 v[126:127], v225 offset0:30 offset1:31\nv_add_f32 v89, v89, v121\nM 60,176,44\nR 168,185,0\ns_add_u32 s63, s52, 2359296\nM 60,180,60\n"
"buffer_load_dwordx4 v220, s[4:7], s63 offen lds\nM 44,176,40\nR 170,185,4096\nG 6\nv_add_f32 v90, v90, v122\nM 44,180,56\n"
"v_add_f32 v91, v91, v123\nG 4\nv_add_f32 v92, v92, v124\nM 156,176,36\nR 172,185,8192\nv_add_f32 v93, v93, v125\n"
"M 156,180,52\nbuffer_load_dwordx4 v221, s[4:7], s63 offen offset:1024 lds\nM 140,176,32\nR 174,185,12288\ns_add_u32 m0, s50, 90112\nM 140,180,48\n"
"G 5\nG 4\nv_add_f32 v94, v94, v126\nv_add_f32 v95, v95, v127\nM 128,160,64\nR 176,186,0\n"
"s_add_u32 s63, s52, 2555904\nv_mul_f32 v64, v64, v230\nM 128,164,80\nbuffer_load_dwordx4 v220, s[4:7], s63 offen lds\nv_mul_f32 v65, v65, v231\nM 144,160,68\n"
"R 178,186,4096\nv_mul_f32 v66, v66, v232\nv_mul_f32 v67, v67, v233\nM 144,164,84\nv_mul_f32 v68, v68, v234\nv_mul_f32 v69, v69, v235\n"
"v_mul_f32 v70, v70, v236\nM 32,160,72\nR 180,186,8192\nv_mul_f32 v71, v71, v237\nv_cvt_pk_bf16_f32 v67, v66, v67\nM 32,164,88\n"
"v_cvt_pk_bf16_f32 v66, v64, v65\nv_cvt_pk_bf16_f32 v68, v68, v69\nv_cvt_pk_bf16_f32 v69, v70, v71\nM 48,160,76\nR 182,186,12288\nv_mul_f32 v72, v72, v230\n"
"v_mul_f32 v73, v73, v231\nM 48,164,92\nG 4\nv_mul_f32 v74, v74, v232\nv_mul_f32 v75, v75, v233\nv_mul_f32 v76, v76, v234\n"
"M 52,168,76\nR 160,187,0\nv_mul_f32 v77, v77, v235\nv_mul_f32 v78, v78, v236\nM 52,172,92\nbuffer_load_dwordx4 v221, s[4:7], s63 offen offset:1024 lds\n"
"v_mul_f32 v79, v79, v237\nM 36,168,72\nR 162,187,4096\ns_add_u32 s63, s52, 2752512\nv_cvt_pk_bf16_f32 v72, v72, v73\nM 36,172,88\n"
"s_add_u32 m0, s50, 98304\nv_cvt_pk_bf16_f32 v73, v74, v75\nv_cvt_pk_bf16_f32 v74, v76, v77\nv_cvt_pk_bf16_f32 v75, v78, v79\nM 148,168,68\nR 164,187,8192\n"
"v_mul_f32 v80, v80, v230\nv_mul_f32 v81, v81, v231\nM 148,172,84\nbuffer_load_dwordx4 v220, s[4:7], s63 offen lds\nv_mul_f32 v82, v82, v232\nM 132,168,64\n"
"R 166,187,12288\ns_waitcnt vmcnt(9)\ns_barrier\nv_mul_f32 v83, v83, v233\nM 132,172,80\nG 4\n"
"v_mul_f32 v84, v84, v234\nv_mul_f32 v85, v85, v235\nv_mul_f32 v86, v86, v236\nM 136,176,64\nR 168,184,16384\nv_mul_f32 v87, v87, v237\n"
"v_cvt_pk_bf16_f32 v78, v80, v81\nM 136,180,80\ns_mov_b64 exec, s[66:67]\nbuffer_store_dwordx2 v[68:69], v227, s[20:23], s64 offen offset:2048 sc1\nbuffer_store_dwordx2 v[74:75], v227, s[20:23], s64 offen offset:2560 sc1\ns_mov_b64 exec, -1\n"
"v_cvt_pk_bf16_f32 v79, v82, v83\nv_cvt_pk_bf16_f32 v80, v84, v85\nM 152,176,68\nR 170,184,20480\nv_cvt_pk_bf16_f32 v81, v86, v87\nv_mul_f32 v88, v88, v230\n"
"M 152,180,84\nv_mul_f32 v89, v89, v231\nv_mul_f32 v90, v90, v232\nv_mul_f32 v91, v91, v233\nM 40,176,72\nR 172,184,24576\n"
"s_mov_b32 s60, 155648\nv_bfe_u32 v246, v252, 4, 4\nv_lshl_add_u32 v246, v246, 3, s60\nv_mul_f32 v92, v92, v234\nM 40,180,88\nbuffer_load_dwordx4 v221, s[4:7], s63 offen offset:1024 lds\n"
"v_mul_f32 v93, v93, v235\nM 56,176,76\nR 174,184,28672\nds_read_b64 v[238:239], v246 offset:0\nv_mul_f32 v94, v94, v236\nM 56,180,92\n"
"G 5\nv_mul_f32 v95, v95, v237\nv_cvt_pk_bf16_f32 v84, v88, v89\nv_cvt_pk_bf16_f32 v85, v90, v91\nM 60,160,76\nR 176,185,16384\n"
"ds_read_b64 v[240:241], v246 offset:128\nv_cvt_pk_bf16_f32 v86, v92, v93\nM 60,164,92\ns_add_u32 m0, s50, 106496\nv_cvt_pk_bf16_f32 v87, v94, v95\ns_mov_b64 exec, s[66:67]\n"
"buffer_store_dwordx2 v[80:81], v227, s[20:23], s64 offen offset:3072 sc1\nbuffer_store_dwordx2 v[86:87], v227, s[20:23], s64 offen offset:3584 sc1\ns_mov_b64 exec, -1\nv_add_u32 v108, 161792, v252\nM 44,160,72\nR 178,185,20480\n"
"ds_read_b64 v[242:243], v246 offset:256\nM 44,164,88\ns_add_u32 s63, s52, 2949120\nds_read_b128 v[112:115], v108\nM 156,160,68\nR 180,185,24576\n"
"ds_read_b64 v[244:245], v246 offset:384\nM 156,164,84\nbuffer_load_dwordx4 v220, s[4:7], s63 offen lds\nM 140,160,64\nR 182,185,28672\nds_read_b64 v[246:247], v246 offset:0\n"
"M 140,164,80\nds_read_b128 v[104:107], v108 offset:1024\nG 11\nM 128,168,96\nR 160,186,16384\nM 128,172,112\n"
"s_lshl_b32 s60, s40, 12\ns_add_u32 s60, s60, 69632\nv_add_u32 v230, s60, v252\nv_add_u32 v231, s60, v227\nM 144,168,100\nR 162,186,20480\n"
"M 144,172,116\ns_not_b64 exec, s[66:67]\nds_write_b128 v230, v[16:19] offset:0\nds_write_b128 v230, v[20:23] offset:1024\ns_mov_b64 exec, -1\nM 32,168,104\n"
"R 164,186,24576\nG 13\nv_mfma_f32_16x16x32_bf16 v[128:131], v[238:241], v[208:211], v[128:131]\nv_mov_b32 v124, s76\nM 32,172,120\nbuffer_load_dwordx4 v221, s[4:7], s63 offen offset:1024 lds\n"
"M 48,168,108\nR 166,186,28672\nG 12\nv_mfma_f32_16x16x32_bf16 v[132:135], v[240:243], v[208:211], v[132:135]\nG 6\nv_lshlrev_b32 v100, 16, v104\n"
"M 48,172,124\nv_and_b32 v101, s77, v104\nv_lshlrev_b32 v102, 16, v105\nM 52,176,108\nR 168,187,16384\nv_mfma_f32_16x16x32_bf16 v[144:147], v[244:247], v[210:213], v[144:147]\n"
"v_and_b32 v103, s77, v105\nM 52,180,124\nv_lshlrev_b32 v96, 16, v106\nv_and_b32 v97, s77, v106\nM 36,176,104\nR 170,187,20480\n"
"v_mfma_f32_16x16x32_bf16 v[148:151], v[238:241], v[210:213], v[148:151]\nv_lshlrev_b32 v98, 16, v107\nM 36,180,120\ns_movk_i32 s63, 2048\nv_mad_u32_u24 v220, v228, s63, v229\nv_add_u32 v221, 1047552, v220\n"
"s_add_u32 m0, s50, 114688\ns_add_u32 s63, s53, 0\nbuffer_load_dwordx4 v220, s[8:11], s63 offen lds\nM 148,176,100\nR 172,187,24576\nv_and_b32 v99, s77, v107\n"
"M 148,180,116\nv_add_f32 v104, s78, v100\nv_add_f32 v105, s78, v101\nM 132,176,96\nR 174,187,28672\nv_add_f32 v106, s78, v102\n"
"M 132,180,112\nG 4\nv_add_f32 v107, s78, v103\nv_mov_b32 v125, s76\nM 136,160,96\nR 176,184,49152\n"
"v_mov_b32 v126, s76\nM 136,164,112\ns_not_b64 exec, s[66:67]\nds_write_b64 v231, v[68:69] offset:2048\nds_write_b64 v231, v[74:75] offset:2560\ns_mov_b64 exec, -1\n"
"v_mov_b32 v127, s76\nM 152,160,100\nR 178,184,53248\nM 152,164,116\nbuffer_load_dwordx4 v221, s[8:11], s63 offen offset:1024 lds\nM 40,160,104\n"
"R 180,184,57344\nM 40,164,120\nM 56,160,108\nR 182,184,61440\nM 56,164,124\nG 6\n"
"M 60,168,108\nR 160,185,49152\ns_not_b64 exec, s[66:67]\nds_write_b64 v231, v[80:81] offset:3072\nds_write_b64 v231, v[86:87] offset:3584\ns_mov_b64 exec, -1\n"
"M 60,172,124\nM 44,168,104\nR 162,185,53248\nM 44,172,120\ns_add_u32 m0, s50, 0\nM 156,168,100\n"
"R 164,185,57344\nM 156,172,116\ns_add_u32 s63, s53, 65536\nM 140,168,96\nR 166,185,61440\nM 140,172,112\n"
"G 6\nM 128,176,128\nR 168,186,49152\ns_waitcnt vmcnt(12) lgkmcnt(4)\ns_barrier\nM 128,180,144\n"
"buffer_load_dwordx4 v220, s[8:11], s63 offen lds\nM 144,176,132\nR 170,186,53248\nM 144,180,148\ns_cmp_eq_u32 s78, 0\ns_cbranch_scc1 Lq660\n"
"v_mov_b32 v230, 0\nv_mov_b32 v231, 0\nv_mov_b32 v232, 0\nv_mov_b32 v233, 0\nv_mov_b32 v64, 0\nv_mov_b32 v65, 0\n"
"v_mov_b32 v70, 0\nv_mov_b32 v71, 0\nv_mov_b32 v234, 0\nv_mov_b32 v235, 0\nv_mov_b32 v236, 0\nv_mov_b32 v237, 0\n"
"v_mov_b32 v76, 0\nv_mov_b32 v77, 0\nv_mov_b32 v82, 0\nv_mov_b32 v83, 0\nLq660:\ns_lshl4_add_u32 s65, s41, s42\n"
"M 32,176,136\nR 172,186,57344\ns_lshl_b32 s65, s65, 12\nM 32,180,152\ns_add_u32 s65, s65, 78577664\nM 48,176,140\n"
"R 174,186,61440\nM 48,180,156\nG 4\nM 52,160,140\nR 176,187,49152\nM 52,164,156\n"
"buffer_load_dwordx4 v221, s[8:11], s63 offen offset:1024 lds\nM 36,160,136\nR 178,187,53248\nM 36,164,152\ns_add_u32 m0, s50, 8192\nM 148,160,132\n"
"R 180,187,57344\nM 148,164,148\ns_add_u32 s63, s53, 131072\nM 132,160,128\nR 182,187,61440\nM 132,164,144\n"
"G 4\nM 136,168,128\nR 160,188,0\nM 136,172,144\nbuffer_load_dwordx4 v220, s[8:11], s63 offen lds\nM 152,168,132\n"
"R 162,188,4096\nM 152,172,148\nM 40,168,136\nR 164,188,8192\nM 40,172,152\nM 56,168,140\n"
"R 166,188,12288\nv_mfma_f32_16x16x32_bf16 v[136:139], v[242:245], v[208:211], v[136:139]\nM 56,172,156\nG 4\nM 60,176,140\nR 168,189,0\n"
"v_mfma_f32_16x16x32_bf16 v[140:143], v[244:247], v[208:211], v[140:143]\nM 60,180,156\nbuffer_load_dwordx4 v221, s[8:11], s63 offen offset:1024 lds\nM 44,176,136\nR 170,189,4096\nv_mfma_f32_16x16x32_bf16 v[152:155], v[240:243], v[210:213], v[152:155]\n"
"s_add_u32 s63, s53, 196608\nM 44,180,152\ns_add_u32 m0, s50, 16384\nM 156,176,132\nR 172,189,8192\nv_mfma_f32_16x16x32_bf16 v[156:159], v[242:245], v[210:213], v[156:159]\n"
"M 156,180,148\nbuffer_load_dwordx4 v220, s[8:11], s63 offen lds\nM 140,176,128\nR 174,189,12288\nM 140,180,144\nG 4\n"
"M 128,160,160\nR 176,190,0\nM 128,164,176\nM 144,160,164\nR 178,190,4096\nM 144,164,180\n"
"buffer_load_dwordx4 v221, s[8:11], s63 offen offset:1024 lds\nM 32,160,168\nR 180,190,8192\ns_add_u32 s63, s53, 262144\nM 32,164,184\ns_add_u32 m0, s50, 24576\n"
"M 48,160,172\nR 182,190,12288\nM 48,164,188\nG 4\nM 52,168,172\nR 160,191,0\n"
"M 52,172,188\nbuffer_load_dwordx4 v220, s[8:11], s63 offen lds\nM 36,168,168\nR 162,191,4096\nM 36,172,184\nM 148,168,164\n"
"R 164,191,8192\ns_lshl_b32 s60, s40, 12\ns_add_u32 s60, s60, 65536\nv_add_u32 v226, s60, v252\nv_add_u32 v111, s60, v227\nM 148,172,180\n"
"M 132,168,160\nR 166,191,12288\ns_cmp_eq_u32 s40, 0\ns_cselect_b64 s[88:89], 0, -1\ns_cselect_b32 s60, s41, 0\ns_cmp_lg_u32 s60, 0\n"
"s_cselect_b64 s[66:67], -1, 0\nM 132,172,176\nG 4\nM 136,176,160\nR 168,188,16384\nM 136,180,176\n"
"s_mov_b64 exec, s[88:89]\nds_read_b128 v[230:233], v226 offset:0\nds_read_b64 v[64:65], v111 offset:2048\ns_mov_b64 exec, -1\nM 152,176,164\nR 170,188,20480\n"
"s_waitcnt vmcnt(9) lgkmcnt(1)\ns_barrier\nM 152,180,180\nbuffer_load_dwordx4 v221, s[8:11], s63 offen offset:1024 lds\nM 40,176,168\nR 172,188,24576\n"
"M 40,180,184\ns_mov_b64 exec, s[88:89]\nds_read_b64 v[70:71], v111 offset:2560\nds_read_b128 v[234:237], v226 offset:1024\ns_mov_b64 exec, -1\nM 56,176,172\n"
"R 174,188,28672\nM 56,180,188\nM 60,160,172\nR 176,189,16384\nM 60,164,188\ns_mov_b64 exec, s[88:89]\n"
"ds_read_b64 v[76:77], v111 offset:3072\nds_read_b64 v[82:83], v111 offset:3584\ns_mov_b64 exec, -1\nM 44,160,168\nR 178,189,20480\nM 44,164,184\n"
"M 156,160,164\nR 180,189,24576\nM 156,164,180\nM 140,160,160\nR 182,189,28672\nM 140,164,176\n"
"G 6\nM 32,168,200\nR 160,190,16384\ns_mov_b64 exec, s[66:67]\nbuffer_load_dwordx4 v[230:233], v252, s[20:23], s65 offen offset:0 sc1\nbuffer_load_dwordx2 v[64:65], v227, s[20:23], s65 offen offset:2048 sc1\n"
"s_mov_b64 exec, -1\nM 32,172,216\nR 162,190,20480\nM 48,168,204\nR 164,190,24576\nM 48,172,220\n"
"R 166,190,28672\nG 4\nM 52,176,204\nR 168,191,16384\nM 52,180,220\nR 170,191,20480\n"
"s_mov_b64 exec, s[66:67]\nbuffer_load_dwordx2 v[70:71], v227, s[20:23], s65 offen offset:2560 sc1\nbuffer_load_dwordx4 v[234:237], v252, s[20:23], s65 offen offset:1024 sc1\ns_mov_b64 exec, -1\nM 36,176,200\nR 172,191,24576\n"
"M 36,180,216\nR 174,191,28672\nG 4\nM 40,160,200\nR 176,188,32768\nM 40,164,216\n"
"R 178,188,36864\nM 56,160,204\nR 180,188,40960\ns_mov_b64 exec, s[66:67]\nbuffer_load_dwordx2 v[76:77], v227, s[20:23], s65 offen offset:3072 sc1\nbuffer_load_dwordx2 v[82:83], v227, s[20:23], s65 offen offset:3584 sc1\n"
"s_mov_b64 exec, -1\nM 56,164,220\nR 182,188,45056\nG 4\nM 60,168,204\nR 160,189,32768\n"
"M 60,172,220\nR 162,189,36864\nM 44,168,200\nR 164,189,40960\nM 44,172,216\nR 166,189,45056\n"
"G 4\nM 32,176,232\nR 168,190,32768\nLq691:\ns_lshr_b32 s60, s45, 3\ns_lshl2_add_u32 s60, s60, s40\n"
"s_lshl_b32 s60, s60, 8\nv_lshl_add_u32 v224, v252, 3, s60\nv_add_u32 v224, Lq691+4096-LqPC, v224\ns_mov_b64 exec, 3\nglobal_load_dword v224, v224, s[98:99]\ns_mov_b64 exec, -1\n"
"M 32,180,248\nR 170,190,36864\nM 48,176,236\nR 172,190,40960\nM 48,180,252\nR 174,190,45056\n"
"G 4\nM 52,160,236\nR 176,191,32768\nM 52,164,252\nR 178,191,36864\nM 36,160,232\n"
"R 180,191,40960\nM 36,164,248\nR 182,191,45056\nG 4\nM 40,168,232\nR 160,188,16384\n"
"M 40,172,248\nR 162,188,20480\nM 56,168,236\nR 164,188,24576\nM 56,172,252\nR 166,188,28672\n"
"G 4\nM 60,176,236\nR 168,189,16384\nM 60,180,252\nR 170,189,20480\nM 44,176,232\n"
"R 172,189,24576\nM 44,180,248\nR 174,189,28672\nG 4\nM 128,160,192\nR 176,190,16384\n"
"M 128,164,208\nR 178,190,20480\ns_mov_b32 m0, s51\nM 144,160,196\nR 180,190,24576\nds_write_addtid_b32 v32 offset:0\n"
"M 144,164,212\nR 182,190,28672\nds_write_addtid_b32 v33 offset:256\nG 6\nM 148,168,196\nR 160,191,16384\n"
"ds_write_addtid_b32 v34 offset:512\nM 148,172,212\nR 162,191,20480\nds_write_addtid_b32 v35 offset:768\nM 132,168,192\nR 164,191,24576\n"
"ds_write_addtid_b32 v36 offset:1024\nM 132,172,208\nR 166,191,28672\nds_write_addtid_b32 v37 offset:1280\nG 9\nM 136,176,192\n"
"R 168,188,32768\nds_write_addtid_b32 v38 offset:1536\nM 136,180,208\nR 170,188,36864\nds_write_addtid_b32 v39 offset:1792\nM 152,176,196\n"
"R 172,188,40960\nds_write_addtid_b32 v40 offset:2048\nM 152,180,212\nR 174,188,45056\nds_write_addtid_b32 v41 offset:2304\nG 9\n"
"M 156,160,196\nR 176,189,32768\nds_write_addtid_b32 v42 offset:2560\nM 156,164,212\nR 178,189,36864\nds_write_addtid_b32 v43 offset:2816\n"
"M 140,160,192\nR 180,189,40960\nds_write_addtid_b32 v44 offset:3072\nds_write_addtid_b32 v45 offset:3328\nds_write_addtid_b32 v46 offset:3584\nds_write_addtid_b32 v47 offset:3840\n"
"ds_write_addtid_b32 v48 offset:4096\nds_write_addtid_b32 v49 offset:4352\nds_write_addtid_b32 v50 offset:4608\nds_write_addtid_b32 v51 offset:4864\nds_write_addtid_b32 v52 offset:5120\nds_write_addtid_b32 v53 offset:5376\n"
"ds_write_addtid_b32 v54 offset:5632\nds_write_addtid_b32 v55 offset:5888\nds_write_addtid_b32 v56 offset:6144\nds_write_addtid_b32 v57 offset:6400\nds_write_addtid_b32 v58 offset:6656\nds_write_addtid_b32 v59 offset:6912\n"
"ds_write_addtid_b32 v60 offset:7168\nds_write_addtid_b32 v61 offset:7424\nds_write_addtid_b32 v62 offset:7680\nds_write_addtid_b32 v63 offset:7936\nM 140,164,208\nR 182,189,45056\n"
"G 15\nM 128,168,224\nR 160,190,32768\ns_add_u32 m0, s50, 32768\nM 128,172,240\nR 162,190,36864\n"
"G 3\ns_barrier\ns_add_u32 s63, s53, 327680\nM 144,168,228\nR 164,190,40960\nbuffer_load_dwordx4 v220, s[8:11], s63 offen lds\n"
"ds_read2st64_b32 v[32:33], v225 offset0:0 offset1:1\nM 144,172,244\nR 166,190,45056\nbuffer_load_dwordx4 v221, s[8:11], s63 offen offset:1024 lds\nds_read2st64_b32 v[34:35], v225 offset0:2 offset1:3\nG 6\n"
"M 148,176,228\nR 168,191,32768\ns_add_u32 m0, s50, 40960\nds_read2st64_b32 v[36:37], v225 offset0:4 offset1:5\nM 148,180,244\nR 170,191,36864\n"
"s_add_u32 s63, s53, 393216\nM 132,176,224\nR 172,191,40960\nds_read2st64_b32 v[38:39], v225 offset0:6 offset1:7\nM 132,180,240\nR 174,191,45056\n"
"G 7\nM 136,160,224\nbuffer_load_dwordx4 v220, s[8:11], s63 offen lds\nM 136,164,240\nds_read2st64_b32 v[40:41], v225 offset0:8 offset1:9\nds_read2st64_b32 v[42:43], v225 offset0:10 offset1:11\n"
"M 152,160,228\nbuffer_load_dwordx4 v221, s[8:11], s63 offen offset:1024 lds\nM 152,164,244\nds_read2st64_b32 v[44:45], v225 offset0:12 offset1:13\nds_read2st64_b32 v[46:47], v225 offset0:14 offset1:15\nG 4\n"
"M 156,168,228\nds_read2st64_b32 v[48:49], v225 offset0:16 offset1:17\nds_read2st64_b32 v[50:51], v225 offset0:18 offset1:19\nM 156,172,244\nds_read2st64_b32 v[52:53], v225 offset0:20 offset1:21\nds_read2st64_b32 v[54:55], v225 offset0:22 offset1:23\n"
"M 140,168,224\nds_read2st64_b32 v[56:57], v225 offset0:24 offset1:25\nds_read2st64_b32 v[58:59], v225 offset0:26 offset1:27\nM 140,172,240\nds_read2st64_b32 v[60:61], v225 offset0:28 offset1:29\nds_read2st64_b32 v[62:63], v225 offset0:30 offset1:31\n"
"G 0\ns_barrier\ns_waitcnt vmcnt(8)\nv_max3_u32 v248, v233, v65, v71\nv_cmp_eq_u32 vcc, -1, v248\ns_nop 0\n"
"s_cbranch_vccnz Lq693\nLq694:\ns_waitcnt vmcnt(5)\nv_max3_u32 v248, v237, v77, v83\nv_cmp_eq_u32 vcc, -1, v248\ns_nop 0\n"
"s_cbranch_vccnz Lq695\nLq696:\ns_lshl_b32 s84, s42, 11\ns_add_u32 s84, s84, s49\ns_add_u32 s84, s84, 6291456\ns_add_u32 s85, s84, 32768\n"
"v_mov_b32 v238, s46\nv_mov_b32 v239, s47\nv_pk_add_f32 v[144:145], v[144:145], v[48:49]\nv_pk_add_f32 v[146:147], v[146:147], v[50:51]\nv_pk_add_f32 v[148:149], v[148:149], v[52:53]\nv_pk_add_f32 v[150:151], v[150:151], v[54:55]\n"
"v_cvt_pk_bf16_f32 v144, v144, v145\nv_cvt_pk_bf16_f32 v145, v146, v147\nv_cvt_pk_bf16_f32 v146, v148, v149\nv_cvt_pk_bf16_f32 v147, v150, v151\ns_nop 1\nv_mfma_f32_16x16x32_bf16 v[52:55], v[16:19], v[144:147], v[96:99]\n"
"v_mfma_f32_16x16x32_bf16 v[48:51], v[0:3], v[144:147], v[100:103]\nv_pk_add_f32 v[152:153], v[152:153], v[56:57]\nv_pk_add_f32 v[154:155], v[154:155], v[58:59]\nv_pk_add_f32 v[156:157], v[156:157], v[60:61]\nv_pk_add_f32 v[158:159], v[158:159], v[62:63]\nv_cvt_pk_bf16_f32 v152, v152, v153\n"
"v_cvt_pk_bf16_f32 v153, v154, v155\nv_cvt_pk_bf16_f32 v154, v156, v157\nv_cvt_pk_bf16_f32 v155, v158, v159\ns_nop 1\nv_mfma_f32_16x16x32_bf16 v[60:63], v[20:23], v[152:155], v[96:99]\nv_mfma_f32_16x16x32_bf16 v[56:59], v[4:7], v[152:155], v[100:103]\n"
"v_pk_add_f32 v[128:129], v[128:129], v[32:33]\nv_pk_add_f32 v[130:131], v[130:131], v[34:35]\nv_pk_add_f32 v[132:133], v[132:133], v[36:37]\nv_pk_add_f32 v[134:135], v[134:135], v[38:39]\nv_cvt_pk_bf16_f32 v128, v128, v129\nv_cvt_pk_bf16_f32 v129, v130, v131\n"
"v_cvt_pk_bf16_f32 v130, v132, v133\nv_cvt_pk_bf16_f32 v131, v134, v135\ns_nop 1\nv_mfma_f32_16x16x32_bf16 v[36:39], v[0:3], v[128:131], v[96:99]\nv_mfma_f32_16x16x32_bf16 v[32:35], v[230:233], v[128:131], v[104:107]\nv_pk_add_f32 v[136:137], v[136:137], v[40:41]\n"
"v_pk_add_f32 v[138:139], v[138:139], v[42:43]\nv_pk_add_f32 v[140:141], v[140:141], v[44:45]\nv_pk_add_f32 v[142:143], v[142:143], v[46:47]\nv_cvt_pk_bf16_f32 v136, v136, v137\nv_cvt_pk_bf16_f32 v137, v138, v139\nv_cvt_pk_bf16_f32 v138, v140, v141\n"
"v_cvt_pk_bf16_f32 v139, v142, v143\ns_nop 1\nv_mfma_f32_16x16x32_bf16 v[44:47], v[4:7], v[136:139], v[96:99]\nv_mfma_f32_16x16x32_bf16 v[40:43], v[234:237], v[136:139], v[104:107]\nv_max3_f32 v52, v52, v53, v48\nv_max3_f32 v60, v60, v61, v56\n"
"v_max3_f32 v36, v36, v37, v32\ns_nop 4\nv_max3_f32 v44, v44, v45, v40\nv_max_f32 v52, v52, v49\nv_max_f32 v60, v60, v57\nv_max_f32 v36, v36, v33\n"
"v_max_f32 v44, v44, v41\nv_max3_f32 v54, v54, v55, v50\nv_max3_f32 v62, v62, v63, v58\nv_max3_f32 v38, v38, v39, v34\nv_max3_f32 v46, v46, v47, v42\nv_max_f32 v54, v54, v51\n"
"v_max_f32 v62, v62, v59\nv_max_f32 v38, v38, v35\nv_max_f32 v46, v46, v43\nv_max_f32 v118, v52, v54\nv_max_f32 v119, v60, v62\nv_max_f32 v116, v36, v38\n"
"v_max_f32 v117, v44, v46\nv_mov_b32 v120, v118\nv_mov_b32 v121, v119\nv_mov_b32 v122, v116\nv_mov_b32 v123, v117\nv_permlane32_swap_b32 v120, v118\n"
"v_permlane32_swap_b32 v121, v119\nv_permlane32_swap_b32 v122, v116\nv_permlane32_swap_b32 v123, v117\nv_max_f32 v118, v118, v120\nv_max_f32 v119, v119, v121\nv_max_f32 v116, v116, v122\n"
"v_max_f32 v117, v117, v123\nv_mov_b32 v120, v118\nv_mov_b32 v121, v119\nv_mov_b32 v122, v116\nv_mov_b32 v123, v117\nv_permlane16_swap_b32 v120, v118\n"
"v_permlane16_swap_b32 v121, v119\nv_permlane16_swap_b32 v122, v116\nv_permlane16_swap_b32 v123, v117\nv_max_f32 v118, v118, v120\nv_max_f32 v119, v119, v121\nv_max_f32 v116, v116, v122\n"
"v_max_f32 v117, v117, v123\nv_mul_f32 v118, v118, v218\nv_mul_f32 v119, v119, v218\nv_mul_f32 v116, v116, v215\nv_mul_f32 v117, v117, v215\nv_fma_f32 v52, v52, v218, -v118\n"
"v_fma_f32 v54, v54, v218, -v118\nv_fma_f32 v60, v60, v218, -v119\nv_fma_f32 v62, v62, v218, -v119\nv_fma_f32 v36, v36, v215, -v116\nv_fma_f32 v38, v38, v215, -v116\nv_fma_f32 v44, v44, v215, -v117\n"
"v_fma_f32 v46, v46, v215, -v117\nv_exp_f32 v52, v52\nv_exp_f32 v54, v54\nv_exp_f32 v60, v60\nv_exp_f32 v62, v62\nv_exp_f32 v36, v36\n"
"v_exp_f32 v38, v38\nv_exp_f32 v44, v44\nv_exp_f32 v46, v46\nv_cvt_pk_bf16_f32 v52, v52, v52\nv_cvt_pk_bf16_f32 v54, v54, v54\nv_and_b32 v48, v52, v112\n"
"v_and_b32 v49, v54, v113\nv_and_b32 v50, v52, v114\nv_and_b32 v51, v54, v115\ns_nop 1\nv_mfma_f32_16x16x32_bf16 v[24:27], v[124:127], v[48:51], 0\nv_mfma_f32_16x16x32_bf16 v[144:147], v[66:69], v[48:51], 0\n"
"v_mfma_f32_16x16x32_bf16 v[148:151], v[72:75], v[48:51], 0\nv_cvt_pk_bf16_f32 v60, v60, v60\nv_cvt_pk_bf16_f32 v62, v62, v62\nv_and_b32 v56, v60, v112\nv_and_b32 v57, v62, v113\nv_and_b32 v58, v60, v114\n"
"v_and_b32 v59, v62, v115\ns_nop 1\nv_mfma_f32_16x16x32_bf16 v[28:31], v[124:127], v[56:59], 0\nv_mfma_f32_16x16x32_bf16 v[152:155], v[78:81], v[56:59], 0\nv_mfma_f32_16x16x32_bf16 v[156:159], v[84:87], v[56:59], 0\nv_cvt_pk_bf16_f32 v36, v36, v36\n"
"v_cvt_pk_bf16_f32 v38, v38, v38\nv_and_b32 v32, v36, v112\nv_and_b32 v33, v38, v113\nv_and_b32 v34, v36, v114\nv_and_b32 v35, v38, v115\ns_nop 1\n"
"v_mfma_f32_16x16x32_bf16 v[8:11], v[124:127], v[32:35], 0\nv_mfma_f32_16x16x32_bf16 v[128:131], v[64:67], v[32:35], 0\nv_mfma_f32_16x16x32_bf16 v[132:135], v[70:73], v[32:35], 0\nv_cvt_pk_bf16_f32 v44, v44, v44\nv_cvt_pk_bf16_f32 v46, v46, v46\nv_and_b32 v40, v44, v112\n"
"v_and_b32 v41, v46, v113\nv_and_b32 v42, v44, v114\nv_and_b32 v43, v46, v115\ns_nop 1\nv_mfma_f32_16x16x32_bf16 v[12:15], v[124:127], v[40:43], 0\nv_mfma_f32_16x16x32_bf16 v[136:139], v[76:79], v[40:43], 0\n"
"v_mfma_f32_16x16x32_bf16 v[140:143], v[82:85], v[40:43], 0\nv_rcp_f32 v24, v24\nv_rcp_f32 v28, v28\nv_rcp_f32 v8, v8\ns_nop 2\nv_rcp_f32 v12, v12\n"
"v_pk_mul_f32 v[144:145], v[144:145], v[24:25] op_sel_hi:[1,0]\nv_pk_mul_f32 v[146:147], v[146:147], v[24:25] op_sel_hi:[1,0]\nv_pk_mul_f32 v[148:149], v[148:149], v[24:25] op_sel_hi:[1,0]\nv_pk_mul_f32 v[150:151], v[150:151], v[24:25] op_sel_hi:[1,0]\nv_cvt_pk_bf16_f32 v144, v144, v145\nv_cvt_pk_bf16_f32 v145, v146, v147\n"
"v_cvt_pk_bf16_f32 v146, v148, v149\nv_cvt_pk_bf16_f32 v147, v150, v151\nbuffer_store_dwordx4 v[144:147], v252, s[20:23], s85 offen offset:0 sc1\nv_pk_mul_f32 v[152:153], v[152:153], v[28:29] op_sel_hi:[1,0]\nv_pk_mul_f32 v[154:155], v[154:155], v[28:29] op_sel_hi:[1,0]\nv_pk_mul_f32 v[156:157], v[156:157], v[28:29] op_sel_hi:[1,0]\n"
"v_pk_mul_f32 v[158:159], v[158:159], v[28:29] op_sel_hi:[1,0]\nv_cvt_pk_bf16_f32 v152, v152, v153\nv_cvt_pk_bf16_f32 v153, v154, v155\nv_cvt_pk_bf16_f32 v154, v156, v157\nv_cvt_pk_bf16_f32 v155, v158, v159\nbuffer_store_dwordx4 v[152:155], v252, s[20:23], s85 offen offset:1024 sc1\n"
"v_pk_mul_f32 v[128:129], v[128:129], v[8:9] op_sel_hi:[1,0]\nv_pk_mul_f32 v[130:131], v[130:131], v[8:9] op_sel_hi:[1,0]\nv_pk_mul_f32 v[132:133], v[132:133], v[8:9] op_sel_hi:[1,0]\nv_pk_mul_f32 v[134:135], v[134:135], v[8:9] op_sel_hi:[1,0]\nv_cvt_pk_bf16_f32 v128, v128, v129\nv_cvt_pk_bf16_f32 v129, v130, v131\n"
"v_cvt_pk_bf16_f32 v130, v132, v133\nv_cvt_pk_bf16_f32 v131, v134, v135\nbuffer_store_dwordx4 v[128:131], v252, s[20:23], s84 offen offset:0 sc1\nv_pk_mul_f32 v[136:137], v[136:137], v[12:13] op_sel_hi:[1,0]\nv_pk_mul_f32 v[138:139], v[138:139], v[12:13] op_sel_hi:[1,0]\nv_pk_mul_f32 v[140:141], v[140:141], v[12:13] op_sel_hi:[1,0]\n"
"v_pk_mul_f32 v[142:143], v[142:143], v[12:13] op_sel_hi:[1,0]\nv_cvt_pk_bf16_f32 v136, v136, v137\nv_cvt_pk_bf16_f32 v137, v138, v139\nv_cvt_pk_bf16_f32 v138, v140, v141\nv_cvt_pk_bf16_f32 v139, v142, v143\nbuffer_store_dwordx4 v[136:139], v252, s[20:23], s84 offen offset:1024 sc1\n"
"s_add_u32 s61, s79, 88498176\ns_lshl_b32 s62, s42, 3\ns_add_u32 s61, s61, s62\ns_waitcnt vmcnt(0)\ns_mov_b64 exec, 1\nbuffer_atomic_swap_x2 v[238:239], off, s[20:23], s61 sc1\n"
"s_mov_b64 exec, -1\nv_bfe_u32 v250, v252, 7, 1\nv_mul_u32_u24 v250, 2176, v250\nv_bfe_u32 v251, v252, 4, 3\nv_lshl_add_u32 v250, v251, 3, v250\ns_lshl_b32 s60, s90, 6\n"
"v_add_u32 v250, s60, v250\ns_sleep 5\nv_mov_b32 v248, 0\nv_mov_b32 v249, 0\ns_add_u32 s61, s80, 88498176\ns_mov_b64 exec, 0xffff\n"
"buffer_atomic_or_x2 v[248:249], v250, s[20:23], s61 offen sc0 sc1\ns_mov_b64 exec, -1\ns_add_u32 m0, s50, 49152\ns_add_u32 s63, s53, 458752\nbuffer_load_dwordx4 v220, s[8:11], s63 offen lds\nbuffer_load_dwordx4 v221, s[8:11], s63 offen offset:1024 lds\n"
"s_add_u32 m0, s50, 57344\ns_add_u32 s63, s53, 524288\nbuffer_load_dwordx4 v220, s[8:11], s63 offen lds\nbuffer_load_dwordx4 v221, s[8:11], s63 offen offset:1024 lds\ns_add_u32 m0, s50, 65536\ns_add_u32 s63, s53, 589824\n"
"buffer_load_dwordx4 v220, s[8:11], s63 offen lds\nbuffer_load_dwordx4 v221, s[8:11], s63 offen offset:1024 lds\ns_add_u32 m0, s50, 73728\ns_add_u32 s63, s53, 655360\nbuffer_load_dwordx4 v220, s[8:11], s63 offen lds\nbuffer_load_dwordx4 v221, s[8:11], s63 offen offset:1024 lds\n"
"s_add_u32 m0, s50, 81920\ns_add_u32 s63, s53, 720896\nbuffer_load_dwordx4 v220, s[8:11], s63 offen lds\nbuffer_load_dwordx4 v221, s[8:11], s63 offen offset:1024 lds\ns_add_u32 m0, s50, 90112\ns_add_u32 s63, s53, 786432\n"
"buffer_load_dwordx4 v220, s[8:11], s63 offen lds\nbuffer_load_dwordx4 v221, s[8:11], s63 offen offset:1024 lds\nv_bfe_u32 v234, v252, 4, 4\nv_lshrrev_b32 v235, 8, v252\nv_lshlrev_b32 v235, 2, v235\nv_sub_u32 v234, v234, v235\n"
"v_lshlrev_b32 v235, 4, v234\ns_mov_b64 s[60:61], 0x3f80\nv_lshlrev_b64 v[208:209], v235, s[60:61]\nv_cmp_gt_u32 s[60:61], 4, v234\ns_nop 1\nv_cndmask_b32 v212, 0, v208, s[60:61]\n"
"v_cndmask_b32 v208, 0, v208, s[60:61]\nv_cndmask_b32 v213, 0, v209, s[60:61]\nv_cndmask_b32 v209, 0, v209, s[60:61]\ns_nop 1\nv_mfma_f32_16x16x32_bf16 v[0:3], v[208:211], v[192:195], 0\nv_mfma_f32_16x16x32_bf16 v[4:7], v[210:213], v[192:195], 0\n"
"v_mfma_f32_16x16x32_bf16 v[8:11], v[208:211], v[196:199], 0\nv_mfma_f32_16x16x32_bf16 v[12:15], v[210:213], v[196:199], 0\nv_mfma_f32_16x16x32_bf16 v[16:19], v[208:211], v[200:203], 0\nv_mfma_f32_16x16x32_bf16 v[20:23], v[210:213], v[200:203], 0\nv_mfma_f32_16x16x32_bf16 v[24:27], v[208:211], v[204:207], 0\nv_mfma_f32_16x16x32_bf16 v[28:31], v[210:213], v[204:207], 0\n"
"s_waitcnt vmcnt(12)\nv_cmp_eq_u64 s[86:87], s[46:47], v[248:249]\ns_nop 0\ns_and_b32 s62, s86, 0xffff\ns_cmp_eq_u32 s62, 0xffff\ns_cbranch_scc0 Lq698\n"
"Lq699:\ns_add_u32 s64, s70, 6291456\nbuffer_load_dwordx4 a[0:3], v252, s[20:23], s64 offen offset:0 sc1\ns_add_u32 s65, s70, 6324224\nbuffer_load_dwordx4 a[4:7], v252, s[20:23], s65 offen offset:0 sc1\ns_add_u32 s66, s71, 6291456\n"
"buffer_load_dwordx4 a[8:11], v252, s[20:23], s66 offen offset:0 sc1\ns_add_u32 s67, s71, 6324224\nbuffer_load_dwordx4 a[12:15], v252, s[20:23], s67 offen offset:0 sc1\nbuffer_load_dwordx4 a[16:19], v252, s[20:23], s64 offen offset:1024 sc1\nbuffer_load_dwordx4 a[20:23], v252, s[20:23], s65 offen offset:1024 sc1\nbuffer_load_dwordx4 a[24:27], v252, s[20:23], s66 offen offset:1024 sc1\n"
"buffer_load_dwordx4 a[28:31], v252, s[20:23], s67 offen offset:1024 sc1\ns_barrier\nR 160,188,49152\nR 162,188,53248\nR 164,184,0\nR 166,184,4096\n"
"R 168,189,49152\nR 170,189,53248\nR 172,185,0\nR 174,185,4096\nR 176,190,49152\nR 178,190,53248\n"
"R 180,186,0\nR 182,186,4096\nbuffer_load_dwordx4 a[32:35], v252, s[20:23], s64 offen offset:2048 sc1\nbuffer_load_dwordx4 a[36:39], v252, s[20:23], s65 offen offset:2048 sc1\nbuffer_load_dwordx4 a[40:43], v252, s[20:23], s66 offen offset:2048 sc1\nbuffer_load_dwordx4 a[44:47], v252, s[20:23], s67 offen offset:2048 sc1\n"
"buffer_load_dwordx4 a[48:51], v252, s[20:23], s64 offen offset:3072 sc1\nbuffer_load_dwordx4 a[52:55], v252, s[20:23], s65 offen offset:3072 sc1\nbuffer_load_dwordx4 a[56:59], v252, s[20:23], s66 offen offset:3072 sc1\nbuffer_load_dwordx4 a[60:63], v252, s[20:23], s67 offen offset:3072 sc1\ns_add_u32 s64, s70, 6295552\nbuffer_load_dwordx4 a[64:67], v252, s[20:23], s64 offen offset:0 sc1\n"
"s_add_u32 s65, s70, 6328320\nbuffer_load_dwordx4 a[68:71], v252, s[20:23], s65 offen offset:0 sc1\ns_add_u32 s66, s71, 6295552\nbuffer_load_dwordx4 a[72:75], v252, s[20:23], s66 offen offset:0 sc1\ns_add_u32 s67, s71, 6328320\nbuffer_load_dwordx4 a[76:79], v252, s[20:23], s67 offen offset:0 sc1\n"
"s_waitcnt vmcnt(19) lgkmcnt(10)\nM 0,160,0\nbuffer_load_dwordx4 a[80:83], v252, s[20:23], s64 offen offset:1024 sc1\ns_waitcnt vmcnt(16) lgkmcnt(8)\nM 0,164,16\nM 16,160,4\n"
"s_waitcnt vmcnt(15)\nM 16,164,20\nv_mfma_f32_16x16x32_bf16 v[32:35], v[160:163], a[8:11], 0\nbuffer_load_dwordx4 a[84:87], v252, s[20:23], s65 offen offset:1024 sc1\ns_waitcnt vmcnt(15)\nM 32,164,24\n"
"v_mfma_f32_16x16x32_bf16 v[48:51], v[160:163], a[12:15], 0\ns_waitcnt vmcnt(14)\nM 48,164,28\nG 4\nv_mfma_f32_16x16x32_bf16 v[52:55], v[168:171], a[12:15], 0\nR 160,191,49152\n"
"M 52,172,28\nbuffer_load_dwordx4 a[88:91], v252, s[20:23], s66 offen offset:1024 sc1\nv_mfma_f32_16x16x32_bf16 v[36:39], v[168:171], a[8:11], 0\nR 162,191,53248\nM 36,172,24\nM 20,168,4\n"
"R 164,187,0\nM 20,172,20\nbuffer_load_dwordx4 a[92:95], v252, s[20:23], s67 offen offset:1024 sc1\nM 4,168,0\nR 166,187,4096\nM 4,172,16\n"
"G 4\nM 8,176,0\nR 168,184,8192\nM 8,180,16\nbuffer_load_dwordx4 a[96:99], v252, s[20:23], s64 offen offset:2048 sc1\nM 24,176,4\n"
"R 170,184,12288\nM 24,180,20\nv_mfma_f32_16x16x32_bf16 v[40:43], v[176:179], a[8:11], 0\nR 172,184,16384\nM 40,180,24\nbuffer_load_dwordx4 a[100:103], v252, s[20:23], s65 offen offset:2048 sc1\n"
"v_mfma_f32_16x16x32_bf16 v[56:59], v[176:179], a[12:15], 0\nR 174,184,20480\nM 56,180,28\nG 4\nv_mfma_f32_16x16x32_bf16 v[60:63], v[160:163], a[12:15], 0\nR 176,185,8192\n"
"M 60,164,28\nbuffer_load_dwordx4 a[104:107], v252, s[20:23], s66 offen offset:2048 sc1\nv_mfma_f32_16x16x32_bf16 v[44:47], v[160:163], a[8:11], 0\nR 178,185,12288\nM 44,164,24\nM 28,160,4\n"
"R 180,185,16384\nM 28,164,20\nbuffer_load_dwordx4 a[108:111], v252, s[20:23], s67 offen offset:2048 sc1\nM 12,160,0\nR 182,185,20480\nM 12,164,16\n"
"G 4\ns_waitcnt vmcnt(19)\nM 0,168,32\nR 160,186,8192\ns_waitcnt vmcnt(15)\nM 0,172,48\n"
"buffer_load_dwordx4 a[112:115], v252, s[20:23], s64 offen offset:3072 sc1\nM 16,168,36\nR 162,186,12288\ns_waitcnt vmcnt(15)\nM 16,172,52\nM 32,168,40\n"
"R 164,186,16384\ns_waitcnt vmcnt(14)\nM 32,172,56\nbuffer_load_dwordx4 a[116:119], v252, s[20:23], s65 offen offset:3072 sc1\nM 48,168,44\nR 166,186,20480\n"
"s_waitcnt vmcnt(14)\nM 48,172,60\nG 4\nM 52,176,44\nR 168,187,8192\nM 52,180,60\n"
"buffer_load_dwordx4 a[120:123], v252, s[20:23], s66 offen offset:3072 sc1\nM 36,176,40\nR 170,187,12288\nM 36,180,56\nM 20,176,36\nR 172,187,16384\n"
"M 20,180,52\nbuffer_load_dwordx4 a[124:127], v252, s[20:23], s67 offen offset:3072 sc1\nM 4,176,32\nR 174,187,20480\nM 4,180,48\nG 4\n"
"M 8,160,32\nR 176,184,24576\nM 8,164,48\ns_add_u32 s64, s70, 6299648\nbuffer_load_dwordx4 a[128:131], v252, s[20:23], s64 offen offset:0 sc1\nM 24,160,36\n"
"R 178,184,28672\nM 24,164,52\nM 40,160,40\nR 180,184,32768\nM 40,164,56\ns_add_u32 s65, s70, 6332416\n"
"buffer_load_dwordx4 a[132:135], v252, s[20:23], s65 offen offset:0 sc1\nM 56,160,44\nR 182,184,36864\nM 56,164,60\nG 4\nM 60,168,44\n"
"R 160,185,24576\nM 60,172,60\ns_add_u32 s66, s71, 6299648\nbuffer_load_dwordx4 a[136:139], v252, s[20:23], s66 offen offset:0 sc1\nM 44,168,40\nR 162,185,28672\n"
"M 44,172,56\nM 28,168,36\nR 164,185,32768\nM 28,172,52\ns_add_u32 s67, s71, 6332416\nbuffer_load_dwordx4 a[140:143], v252, s[20:23], s67 offen offset:0 sc1\n"
"M 12,168,32\nR 166,185,36864\nM 12,172,48\ns_barrier\nG 4\ns_waitcnt vmcnt(19)\n"
"M 0,176,64\nR 168,186,24576\ns_waitcnt vmcnt(15)\nM 0,180,80\nbuffer_load_dwordx4 a[144:147], v252, s[20:23], s64 offen offset:1024 sc1\nM 16,176,68\n"
"R 170,186,28672\ns_waitcnt vmcnt(15)\nM 16,180,84\nM 32,176,72\nR 172,186,32768\ns_waitcnt vmcnt(14)\n"
"M 32,180,88\nbuffer_load_dwordx4 a[148:151], v252, s[20:23], s65 offen offset:1024 sc1\nM 48,176,76\nR 174,186,36864\ns_waitcnt vmcnt(14)\nM 48,180,92\n"
"G 4\nM 52,160,76\nR 176,187,24576\nM 52,164,92\nbuffer_load_dwordx4 a[152:155], v252, s[20:23], s66 offen offset:1024 sc1\nM 36,160,72\n"
"R 178,187,28672\nM 36,164,88\nM 20,160,68\nR 180,187,32768\nM 20,164,84\nbuffer_load_dwordx4 a[156:159], v252, s[20:23], s67 offen offset:1024 sc1\n"
"M 4,160,64\nR 182,187,36864\nM 4,164,80\nG 4\nM 8,168,64\nR 160,184,40960\n"
"M 8,172,80\nbuffer_load_dwordx4 a[160:163], v252, s[20:23], s64 offen offset:2048 sc1\nM 24,168,68\nR 162,184,45056\nM 24,172,84\nM 40,168,72\n"
"R 164,184,49152\nM 40,172,88\nbuffer_load_dwordx4 a[164:167], v252, s[20:23], s65 offen offset:2048 sc1\nM 56,168,76\nR 166,184,53248\nM 56,172,92\n"
"G 4\nM 60,176,76\nR 168,185,40960\nM 60,180,92\nbuffer_load_dwordx4 a[168:171], v252, s[20:23], s66 offen offset:2048 sc1\nM 44,176,72\n"
"R 170,185,45056\nM 44,180,88\nM 28,176,68\nR 172,185,49152\nM 28,180,84\nbuffer_load_dwordx4 a[172:175], v252, s[20:23], s67 offen offset:2048 sc1\n"
"M 12,176,64\nR 174,185,53248\nM 12,180,80\nG 4\ns_waitcnt vmcnt(19)\nM 0,160,96\n"
"R 176,186,40960\ns_waitcnt vmcnt(15)\nM 0,164,112\nbuffer_load_dwordx4 a[176:179], v252, s[20:23], s64 offen offset:3072 sc1\nM 16,160,100\nR 178,186,45056\n"
"s_add_u32 s63, s53, 851968\ns_waitcnt vmcnt(15)\nM 16,164,116\ns_add_u32 m0, s50, 98304\nM 32,160,104\nR 180,186,49152\n"
"s_waitcnt vmcnt(14)\nM 32,164,120\nbuffer_load_dwordx4 a[180:183], v252, s[20:23], s65 offen offset:3072 sc1\nM 48,160,108\nR 182,186,53248\ns_waitcnt vmcnt(14)\n"
"M 48,164,124\nbuffer_load_dwordx4 v220, s[8:11], s63 offen lds\nG 4\nM 52,168,108\nR 160,187,40960\nM 52,172,124\n"
"buffer_load_dwordx4 a[184:187], v252, s[20:23], s66 offen offset:3072 sc1\nM 36,168,104\nR 162,187,45056\nM 36,172,120\nbuffer_load_dwordx4 v221, s[8:11], s63 offen offset:1024 lds\nM 20,168,100\n"
"R 164,187,49152\ns_add_u32 m0, s50, 106496\nM 20,172,116\nbuffer_load_dwordx4 a[188:191], v252, s[20:23], s67 offen offset:3072 sc1\nM 4,168,96\nR 166,187,53248\n"
"s_add_u32 s63, s53, 917504\nM 4,172,112\nbuffer_load_dwordx4 v220, s[8:11], s63 offen lds\nG 4\nM 8,176,96\nR 168,184,57344\n"
"M 8,180,112\ns_add_u32 s64, s70, 6303744\nbuffer_load_dwordx4 a[192:195], v252, s[20:23], s64 offen offset:0 sc1\nM 24,176,100\nR 170,184,61440\nM 24,180,116\n"
"buffer_load_dwordx4 v221, s[8:11], s63 offen offset:1024 lds\nM 40,176,104\nR 172,188,0\ns_add_u32 m0, s50, 114688\nM 40,180,120\ns_add_u32 s65, s70, 6336512\n"
"buffer_load_dwordx4 a[196:199], v252, s[20:23], s65 offen offset:0 sc1\nM 56,176,108\nR 174,188,4096\ns_add_u32 s63, s53, 983040\nM 56,180,124\nbuffer_load_dwordx4 v220, s[8:11], s63 offen lds\n"
"G 4\nM 60,160,108\nR 176,185,57344\nM 60,164,124\ns_add_u32 s66, s71, 6303744\nbuffer_load_dwordx4 a[200:203], v252, s[20:23], s66 offen offset:0 sc1\n"
"M 44,160,104\nR 178,185,61440\nM 44,164,120\nbuffer_load_dwordx4 v221, s[8:11], s63 offen offset:1024 lds\nM 28,160,100\nR 180,189,0\n"
"M 28,164,116\ns_add_u32 s67, s71, 6336512\nbuffer_load_dwordx4 a[204:207], v252, s[20:23], s67 offen offset:0 sc1\nM 12,160,96\nR 182,189,4096\nM 12,164,112\n"
"s_barrier\nG 4\ns_waitcnt vmcnt(25)\nM 0,168,128\nR 160,186,57344\ns_waitcnt vmcnt(21)\n"
"M 0,172,144\nbuffer_load_dwordx4 a[208:211], v252, s[20:23], s64 offen offset:1024 sc1\nM 16,168,132\nR 162,186,61440\ns_waitcnt vmcnt(21)\nM 16,172,148\n"
"M 32,168,136\nR 164,190,0\ns_waitcnt vmcnt(20)\nM 32,172,152\nbuffer_load_dwordx4 a[212:215], v252, s[20:23], s65 offen offset:1024 sc1\nM 48,168,140\n"
"R 166,190,4096\ns_waitcnt vmcnt(20)\nM 48,172,156\nG 4\nM 52,176,140\nR 168,187,57344\n"
"M 52,180,156\nbuffer_load_dwordx4 a[216:219], v252, s[20:23], s66 offen offset:1024 sc1\nM 36,176,136\nR 170,187,61440\nM 36,180,152\nM 20,176,132\n"
"R 172,191,0\nM 20,180,148\nbuffer_load_dwordx4 a[220:223], v252, s[20:23], s67 offen offset:1024 sc1\nM 4,176,128\nR 174,191,4096\nM 4,180,144\n"
"G 4\nM 8,160,128\nR 176,188,8192\nM 8,164,144\nbuffer_load_dwordx4 a[224:227], v252, s[20:23], s64 offen offset:2048 sc1\nM 24,160,132\n"
"R 178,188,12288\nM 24,164,148\nM 40,160,136\nR 180,188,16384\nM 40,164,152\nbuffer_load_dwordx4 a[228:231], v252, s[20:23], s65 offen offset:2048 sc1\n"
"M 56,160,140\nR 182,188,20480\nM 56,164,156\nG 4\nM 60,168,140\nR 160,189,8192\n"
"M 60,172,156\nbuffer_load_dwordx4 a[232:235], v252, s[20:23], s66 offen offset:2048 sc1\nM 44,168,136\nR 162,189,12288\nM 44,172,152\nM 28,168,132\n"
"R 164,189,16384\nM 28,172,148\nbuffer_load_dwordx4 a[236:239], v252, s[20:23], s67 offen offset:2048 sc1\nM 12,168,128\nR 166,189,20480\nM 12,172,144\n"
"G 4\ns_waitcnt vmcnt(25)\nM 0,176,160\nR 168,190,8192\ns_waitcnt vmcnt(21)\nM 0,180,176\n"
"buffer_load_dwordx4 a[240:243], v252, s[20:23], s64 offen offset:3072 sc1\nM 16,176,164\nR 170,190,12288\ns_waitcnt vmcnt(21)\nM 16,180,180\nM 32,176,168\n"
"R 172,190,16384\ns_waitcnt vmcnt(19)\nM 32,180,184\nbuffer_load_dwordx4 a[244:247], v252, s[20:23], s65 offen offset:3072 sc1\nM 48,176,172\nR 174,190,20480\n"
"s_waitcnt vmcnt(18)\nM 48,180,188\nG 4\nM 52,160,172\nR 176,191,8192\nM 52,164,188\n"
"buffer_load_dwordx4 a[248:251], v252, s[20:23], s66 offen offset:3072 sc1\nM 36,160,168\nR 178,191,12288\ns_waitcnt vmcnt(12)\ns_barrier\nM 36,164,184\n"
"M 20,160,164\nR 180,191,16384\nM 20,164,180\nbuffer_load_dwordx4 a[252:255], v252, s[20:23], s67 offen offset:3072 sc1\nM 4,160,160\nR 182,191,20480\n"
"M 4,164,176\nG 4\nM 8,168,160\nR 160,188,24576\nM 8,172,176\ns_movk_i32 s63, 7168\n"
"v_mad_u32_u24 v220, v228, s63, v229\nv_add_u32 v221, 3668992, v220\ns_add_u32 m0, s50, 0\ns_add_u32 s63, s54, 0\nbuffer_load_dwordx4 v220, s[12:15], s63 offen lds\nM 24,168,164\n"
"R 162,188,28672\nM 24,172,180\nM 40,168,168\nR 164,188,32768\nM 40,172,184\nbuffer_load_dwordx4 v221, s[12:15], s63 offen offset:1024 lds\n"
"M 56,168,172\nR 166,188,36864\ns_add_u32 m0, s50, 8192\nM 56,172,188\nG 4\nM 60,176,172\n"
"R 168,189,24576\ns_add_u32 s63, s54, 229376\nM 60,180,188\nbuffer_load_dwordx4 v220, s[12:15], s63 offen lds\nM 44,176,168\nR 170,189,28672\n"
"M 44,180,184\nM 28,176,164\nR 172,189,32768\nM 28,180,180\nbuffer_load_dwordx4 v221, s[12:15], s63 offen offset:1024 lds\nM 12,176,160\n"
"R 174,189,36864\ns_add_u32 m0, s50, 16384\nM 12,180,176\nG 4\nM 32,160,200\nR 176,190,24576\n"
"s_add_u32 s63, s54, 128\ns_waitcnt vmcnt(13)\nM 32,164,216\nR 178,190,28672\nM 48,160,204\nR 180,190,32768\n"
"s_waitcnt vmcnt(12)\nM 48,164,220\nR 182,190,36864\nG 4\nM 52,168,204\nR 160,191,24576\n"
"buffer_load_dwordx4 v220, s[12:15], s63 offen lds\nM 52,172,220\nR 162,191,28672\nM 36,168,200\nR 164,191,32768\nM 36,172,216\n"
"R 166,191,36864\nG 4\nM 40,176,200\nR 168,188,40960\nbuffer_load_dwordx4 v221, s[12:15], s63 offen offset:1024 lds\nM 40,180,216\n"
"R 170,188,45056\ns_add_u32 m0, s50, 24576\nM 56,176,204\nR 172,188,49152\ns_add_u32 s63, s54, 229504\nM 56,180,220\n"
"R 174,188,53248\nG 4\nM 60,160,204\nR 176,189,40960\nbuffer_load_dwordx4 v220, s[12:15], s63 offen lds\nM 60,164,220\n"
"R 178,189,45056\nM 44,160,200\nR 180,189,49152\nM 44,164,216\nR 182,189,53248\nG 4\n"
"s_waitcnt vmcnt(12)\nM 32,168,232\nR 160,190,40960\nbuffer_load_dwordx4 v221, s[12:15], s63 offen offset:1024 lds\ns_waitcnt vmcnt(9)\nM 32,172,248\n"
"R 162,190,45056\nM 48,168,236\nR 164,190,49152\ns_waitcnt vmcnt(8)\nM 48,172,252\nR 166,190,53248\n"
"G 4\nM 52,176,236\nR 168,191,40960\nM 52,180,252\nR 170,191,45056\nM 36,176,232\n"
"R 172,191,49152\nM 36,180,248\nR 174,191,53248\nG 4\nM 40,160,232\nR 176,188,24576\n"
"M 40,164,248\nR 178,188,28672\nM 56,160,236\nR 180,188,32768\ns_add_u32 m0, s50, 32768\nM 56,164,252\n"
"R 182,188,36864\nG 4\nM 60,168,236\nR 160,189,24576\ns_add_u32 s63, s54, 458752\nM 60,172,252\n"
"R 162,189,28672\nM 44,168,232\nR 164,189,32768\nM 44,172,248\nR 166,189,36864\nG 4\n"
"M 0,176,192\nR 168,190,24576\nbuffer_load_dwordx4 v220, s[12:15], s63 offen lds\nM 0,180,208\nR 170,190,28672\ns_mov_b32 m0, s51\n"
"M 16,176,196\nR 172,190,32768\nds_write_addtid_b32 v32 offset:0\nM 16,180,212\nR 174,190,36864\nds_write_addtid_b32 v33 offset:256\n"
"G 6\nM 20,160,196\nR 176,191,24576\nds_write_addtid_b32 v34 offset:512\nM 20,164,212\nR 178,191,28672\n"
"ds_write_addtid_b32 v35 offset:768\nM 4,160,192\nR 180,191,32768\nds_write_addtid_b32 v36 offset:1024\nM 4,164,208\nR 182,191,36864\n"
"ds_write_addtid_b32 v37 offset:1280\nG 9\nM 8,168,192\nR 160,188,40960\nds_write_addtid_b32 v38 offset:1536\nM 8,172,208\n"
"R 162,188,45056\nds_write_addtid_b32 v39 offset:1792\nM 24,168,196\nR 164,188,49152\nds_write_addtid_b32 v40 offset:2048\nM 24,172,212\n"
"R 166,188,53248\nds_write_addtid_b32 v41 offset:2304\nG 9\nM 28,176,196\nR 168,189,40960\nds_write_addtid_b32 v42 offset:2560\n"
"M 28,180,212\nR 170,189,45056\nds_write_addtid_b32 v43 offset:2816\nM 12,176,192\nR 172,189,49152\nds_write_addtid_b32 v44 offset:3072\n"
"ds_write_addtid_b32 v45 offset:3328\nds_write_addtid_b32 v46 offset:3584\nds_write_addtid_b32 v47 offset:3840\nds_write_addtid_b32 v48 offset:4096\nds_write_addtid_b32 v49 offset:4352\nds_write_addtid_b32 v50 offset:4608\n"
"ds_write_addtid_b32 v51 offset:4864\nds_write_addtid_b32 v52 offset:5120\nds_write_addtid_b32 v53 offset:5376\nds_write_addtid_b32 v54 offset:5632\nds_write_addtid_b32 v55 offset:5888\nds_write_addtid_b32 v56 offset:6144\n"
"ds_write_addtid_b32 v57 offset:6400\nds_write_addtid_b32 v58 offset:6656\nds_write_addtid_b32 v59 offset:6912\nds_write_addtid_b32 v60 offset:7168\nds_write_addtid_b32 v61 offset:7424\nds_write_addtid_b32 v62 offset:7680\n"
"ds_write_addtid_b32 v63 offset:7936\nM 12,180,208\nR 174,189,53248\ns_add_u32 m0, s50, 32768\nG 15\nM 0,160,224\n"
"R 176,190,40960\nbuffer_load_dwordx4 v221, s[12:15], s63 offen offset:1024 lds\nM 0,164,240\nR 178,190,45056\nG 3\ns_barrier\n"
"s_add_u32 s63, s54, 688128\nM 16,160,228\nR 180,190,49152\ns_add_u32 m0, s50, 40960\nds_read2st64_b32 v[32:33], v225 offset0:0 offset1:1\nM 16,164,244\n"
"R 182,190,53248\nds_read2st64_b32 v[34:35], v225 offset0:2 offset1:3\nG 6\nM 20,168,228\nR 160,191,40960\nbuffer_load_dwordx4 v220, s[12:15], s63 offen lds\n"
"M 20,172,244\nR 162,191,45056\nds_read2st64_b32 v[36:37], v225 offset0:4 offset1:5\nM 4,168,224\nR 164,191,49152\nds_read2st64_b32 v[38:39], v225 offset0:6 offset1:7\n"
"M 4,172,240\nR 166,191,53248\nG 7\nM 8,176,224\nbuffer_load_dwordx4 v221, s[12:15], s63 offen offset:1024 lds\nM 8,180,240\n"
"s_add_u32 m0, s50, 49152\nds_read2st64_b32 v[40:41], v225 offset0:8 offset1:9\nds_read2st64_b32 v[42:43], v225 offset0:10 offset1:11\nM 24,176,228\ns_add_u32 s63, s54, 458880\nds_read2st64_b32 v[44:45], v225 offset0:12 offset1:13\n"
"ds_read2st64_b32 v[46:47], v225 offset0:14 offset1:15\nM 24,180,244\nds_read2st64_b32 v[48:49], v225 offset0:16 offset1:17\nds_read2st64_b32 v[50:51], v225 offset0:18 offset1:19\nG 6\nM 28,160,228\n"
"buffer_load_dwordx4 v220, s[12:15], s63 offen lds\nM 28,164,244\nds_read2st64_b32 v[52:53], v225 offset0:20 offset1:21\nds_read2st64_b32 v[54:55], v225 offset0:22 offset1:23\nM 12,160,224\nds_read2st64_b32 v[56:57], v225 offset0:24 offset1:25\n"
"ds_read2st64_b32 v[58:59], v225 offset0:26 offset1:27\nM 12,164,240\nds_read2st64_b32 v[60:61], v225 offset0:28 offset1:29\nds_read2st64_b32 v[62:63], v225 offset0:30 offset1:31\nG 0\ns_barrier\n"
"s_lshl_b32 s84, s75, 11\ns_add_u32 s84, s84, s49\ns_add_u32 s84, s84, 14680064\ns_add_u32 s85, s84, 32768\nv_mov_b32 v238, s46\nv_mov_b32 v239, s47\n"
"v_pk_add_f32 v[0:1], v[0:1], v[32:33]\nv_pk_add_f32 v[2:3], v[2:3], v[34:35]\nv_pk_add_f32 v[4:5], v[4:5], v[36:37]\nv_pk_add_f32 v[6:7], v[6:7], v[38:39]\nv_cvt_pk_bf16_f32 v192, v0, v1\nv_cvt_pk_bf16_f32 v193, v2, v3\n"
"v_cvt_pk_bf16_f32 v194, v4, v5\nv_cvt_pk_bf16_f32 v195, v6, v7\nbuffer_store_dwordx4 v[192:195], v252, s[20:23], s84 offen offset:0 sc1\nv_pk_add_f32 v[8:9], v[8:9], v[40:41]\nv_pk_add_f32 v[10:11], v[10:11], v[42:43]\nv_pk_add_f32 v[12:13], v[12:13], v[44:45]\n"
"v_pk_add_f32 v[14:15], v[14:15], v[46:47]\nv_cvt_pk_bf16_f32 v196, v8, v9\nv_cvt_pk_bf16_f32 v197, v10, v11\nv_cvt_pk_bf16_f32 v198, v12, v13\nv_cvt_pk_bf16_f32 v199, v14, v15\nbuffer_store_dwordx4 v[196:199], v252, s[20:23], s84 offen offset:1024 sc1\n"
"v_pk_add_f32 v[16:17], v[16:17], v[48:49]\nv_pk_add_f32 v[18:19], v[18:19], v[50:51]\nv_pk_add_f32 v[20:21], v[20:21], v[52:53]\nv_pk_add_f32 v[22:23], v[22:23], v[54:55]\nv_cvt_pk_bf16_f32 v200, v16, v17\nv_cvt_pk_bf16_f32 v201, v18, v19\n"
"v_cvt_pk_bf16_f32 v202, v20, v21\nv_cvt_pk_bf16_f32 v203, v22, v23\nbuffer_store_dwordx4 v[200:203], v252, s[20:23], s85 offen offset:0 sc1\nv_pk_add_f32 v[24:25], v[24:25], v[56:57]\nv_pk_add_f32 v[26:27], v[26:27], v[58:59]\nv_pk_add_f32 v[28:29], v[28:29], v[60:61]\n"
"v_pk_add_f32 v[30:31], v[30:31], v[62:63]\nv_cvt_pk_bf16_f32 v204, v24, v25\nv_cvt_pk_bf16_f32 v205, v26, v27\nv_cvt_pk_bf16_f32 v206, v28, v29\nv_cvt_pk_bf16_f32 v207, v30, v31\nbuffer_store_dwordx4 v[204:207], v252, s[20:23], s85 offen offset:1024 sc1\n"
"s_add_u32 s61, s79, 88637440\ns_lshl_b32 s62, s75, 3\ns_add_u32 s61, s61, s62\ns_waitcnt vmcnt(0)\ns_mov_b64 exec, 1\nbuffer_atomic_swap_x2 v[238:239], off, s[20:23], s61 sc1\n"
"s_mov_b64 exec, -1\nLqw1:\nv_bfe_u32 v250, v252, 7, 1\nv_mul_u32_u24 v250, 2176, v250\nv_bfe_u32 v251, v252, 4, 3\nv_lshl_add_u32 v250, v251, 3, v250\n"
"s_lshl_b32 s60, s90, 6\nv_add_u32 v250, s60, v250\ns_sleep 5\nv_mov_b32 v248, 0\nv_mov_b32 v249, 0\ns_add_u32 s61, s80, 88637440\n"
"s_mov_b64 exec, 0xffff\nbuffer_atomic_or_x2 v[248:249], v250, s[20:23], s61 offen sc0 sc1\ns_mov_b64 exec, -1\nbuffer_load_dwordx4 v221, s[12:15], s63 offen offset:1024 lds\ns_add_u32 m0, s50, 57344\ns_add_u32 s63, s54, 688256\n"
"buffer_load_dwordx4 v220, s[12:15], s63 offen lds\nbuffer_load_dwordx4 v221, s[12:15], s63 offen offset:1024 lds\ns_add_u32 m0, s50, 65536\ns_add_u32 s63, s54, 917504\nbuffer_load_dwordx4 v220, s[12:15], s63 offen lds\nbuffer_load_dwordx4 v221, s[12:15], s63 offen offset:1024 lds\n"
"s_add_u32 m0, s50, 73728\ns_add_u32 s63, s54, 1146880\nbuffer_load_dwordx4 v220, s[12:15], s63 offen lds\nbuffer_load_dwordx4 v221, s[12:15], s63 offen offset:1024 lds\ns_add_u32 m0, s50, 81920\ns_add_u32 s63, s54, 917632\n"
"buffer_load_dwordx4 v220, s[12:15], s63 offen lds\nbuffer_load_dwordx4 v221, s[12:15], s63 offen offset:1024 lds\ns_add_u32 m0, s50, 90112\ns_add_u32 s63, s54, 1147008\nbuffer_load_dwordx4 v220, s[12:15], s63 offen lds\nbuffer_load_dwordx4 v221, s[12:15], s63 offen offset:1024 lds\n"
"s_add_u32 m0, s50, 98304\ns_add_u32 s63, s54, 1376256\nbuffer_load_dwordx4 v220, s[12:15], s63 offen lds\nv_mov_b32 v230, 0\nv_mov_b32 v231, 0\nv_mov_b32 v232, 0\n"
"v_mov_b32 v233, 0\nv_dot2c_f32_bf16 v230, s76, v192\nv_dot2c_f32_bf16 v231, v192, v192\nv_dot2c_f32_bf16 v232, s76, v200\nv_dot2c_f32_bf16 v233, v200, v200\nv_dot2c_f32_bf16 v230, s76, v193\n"
"v_dot2c_f32_bf16 v231, v193, v193\nv_dot2c_f32_bf16 v232, s76, v201\nv_dot2c_f32_bf16 v233, v201, v201\nv_dot2c_f32_bf16 v230, s76, v194\nv_dot2c_f32_bf16 v231, v194, v194\nv_dot2c_f32_bf16 v232, s76, v202\n"
"v_dot2c_f32_bf16 v233, v202, v202\nv_dot2c_f32_bf16 v230, s76, v195\nv_dot2c_f32_bf16 v231, v195, v195\nv_dot2c_f32_bf16 v232, s76, v203\nv_dot2c_f32_bf16 v233, v203, v203\nv_dot2c_f32_bf16 v230, s76, v196\n"
"v_dot2c_f32_bf16 v231, v196, v196\nv_dot2c_f32_bf16 v232, s76, v204\nv_dot2c_f32_bf16 v233, v204, v204\nv_dot2c_f32_bf16 v230, s76, v197\nv_dot2c_f32_bf16 v231, v197, v197\nv_dot2c_f32_bf16 v232, s76, v205\n"
"v_dot2c_f32_bf16 v233, v205, v205\nv_dot2c_f32_bf16 v230, s76, v198\nv_dot2c_f32_bf16 v231, v198, v198\nv_dot2c_f32_bf16 v232, s76, v206\nv_dot2c_f32_bf16 v233, v206, v206\nv_dot2c_f32_bf16 v230, s76, v199\n"
"v_dot2c_f32_bf16 v231, v199, v199\nv_dot2c_f32_bf16 v232, s76, v207\nv_dot2c_f32_bf16 v233, v207, v207\nv_lshrrev_b32 v234, 1, v252\nv_permlane32_swap_b32 v230, v231\ns_nop 0\n"
"v_permlane32_swap_b32 v232, v233\nv_add_f32 v230, v230, v231\nv_add_f32 v232, v232, v233\nv_mov_b32 v231, s46\ns_nop 0\nv_permlane16_swap_b32 v230, v232\n"
"v_add_f32 v230, v230, v232\ns_lshr_b32 s61, s49, 3\ns_lshl2_add_u32 s61, s53, s61\ns_add_u32 s61, s61, 83361792\nbuffer_store_dwordx2 v[230:231], v234, s[20:23], s61 offen sc1\ns_waitcnt vmcnt(13)\n"
"v_cmp_eq_u64 s[86:87], s[46:47], v[248:249]\ns_nop 0\ns_and_b32 s62, s86, 0xffff\ns_cmp_eq_u32 s62, 0xffff\ns_cbranch_scc0 Lq750\nLq751:\n"
"s_add_u32 s64, s70, 14680064\nbuffer_load_dwordx4 a[0:3], v252, s[20:23], s64 offen offset:0 sc1\ns_add_u32 s65, s70, 14712832\nbuffer_load_dwordx4 a[4:7], v252, s[20:23], s65 offen offset:0 sc1\ns_add_u32 s66, s71, 14680064\nbuffer_load_dwordx4 a[8:11], v252, s[20:23], s66 offen offset:0 sc1\n"
"s_add_u32 s67, s71, 14712832\nbuffer_load_dwordx4 a[12:15], v252, s[20:23], s67 offen offset:0 sc1\nbuffer_load_dwordx4 a[16:19], v252, s[20:23], s64 offen offset:1024 sc1\nbuffer_load_dwordx4 a[20:23], v252, s[20:23], s65 offen offset:1024 sc1\nbuffer_load_dwordx4 a[24:27], v252, s[20:23], s66 offen offset:1024 sc1\nbuffer_load_dwordx4 a[28:31], v252, s[20:23], s67 offen offset:1024 sc1\n"
"s_barrier\nR 160,184,0\nR 162,184,4096\nR 164,184,8192\nR 166,184,12288\nR 168,185,0\n"
"R 170,185,4096\nR 172,185,8192\nR 174,185,12288\nR 176,186,0\nR 178,186,4096\nR 180,186,8192\n"
"R 182,186,12288\nbuffer_load_dwordx4 a[32:35], v252, s[20:23], s64 offen offset:2048 sc1\nbuffer_load_dwordx4 a[36:39], v252, s[20:23], s65 offen offset:2048 sc1\nbuffer_load_dwordx4 a[40:43], v252, s[20:23], s66 offen offset:2048 sc1\nbuffer_load_dwordx4 a[44:47], v252, s[20:23], s67 offen offset:2048 sc1\nbuffer_load_dwordx4 a[48:51], v252, s[20:23], s64 offen offset:3072 sc1\n"
"buffer_load_dwordx4 a[52:55], v252, s[20:23], s65 offen offset:3072 sc1\nbuffer_load_dwordx4 a[56:59], v252, s[20:23], s66 offen offset:3072 sc1\nbuffer_load_dwordx4 a[60:63], v252, s[20:23], s67 offen offset:3072 sc1\ns_waitcnt vmcnt(15) lgkmcnt(10)\nv_mfma_f32_16x16x32_bf16 v[32:35], v[160:163], a[0:3], 0\ns_add_u32 s64, s70, 14684160\n"
"buffer_load_dwordx4 a[64:67], v252, s[20:23], s64 offen offset:0 sc1\ns_waitcnt vmcnt(12) lgkmcnt(8)\nM 32,164,16\nv_mfma_f32_16x16x32_bf16 v[48:51], v[160:163], a[4:7], 0\ns_waitcnt vmcnt(11)\nM 48,164,20\n"
"v_mfma_f32_16x16x32_bf16 v[64:67], v[160:163], a[8:11], 0\ns_waitcnt vmcnt(10)\nM 64,164,24\ns_add_u32 s65, s70, 14716928\nbuffer_load_dwordx4 a[68:71], v252, s[20:23], s65 offen offset:0 sc1\nv_mfma_f32_16x16x32_bf16 v[80:83], v[160:163], a[12:15], 0\n"
"s_waitcnt vmcnt(10)\nM 80,164,28\nG 4\nv_mfma_f32_16x16x32_bf16 v[84:87], v[168:171], a[12:15], 0\nR 160,187,0\nM 84,172,28\n"
"v_mfma_f32_16x16x32_bf16 v[68:71], v[168:171], a[8:11], 0\nR 162,187,4096\nM 68,172,24\ns_add_u32 s66, s71, 14684160\nbuffer_load_dwordx4 a[72:75], v252, s[20:23], s66 offen offset:0 sc1\nv_mfma_f32_16x16x32_bf16 v[52:55], v[168:171], a[4:7], 0\n"
"R 164,187,8192\nM 52,172,20\nv_mfma_f32_16x16x32_bf16 v[36:39], v[168:171], a[0:3], 0\nR 166,187,12288\nM 36,172,16\nG 4\n"
"v_mfma_f32_16x16x32_bf16 v[40:43], v[176:179], a[0:3], 0\nR 168,184,16384\nM 40,180,16\ns_add_u32 s67, s71, 14716928\nbuffer_load_dwordx4 a[76:79], v252, s[20:23], s67 offen offset:0 sc1\nv_mfma_f32_16x16x32_bf16 v[56:59], v[176:179], a[4:7], 0\n"
"R 170,184,20480\nM 56,180,20\nv_mfma_f32_16x16x32_bf16 v[72:75], v[176:179], a[8:11], 0\nR 172,184,24576\nM 72,180,24\nv_mfma_f32_16x16x32_bf16 v[88:91], v[176:179], a[12:15], 0\n"
"R 174,184,28672\nM 88,180,28\nG 4\nv_mfma_f32_16x16x32_bf16 v[92:95], v[160:163], a[12:15], 0\nR 176,185,16384\nM 92,164,28\n"
"buffer_load_dwordx4 a[80:83], v252, s[20:23], s64 offen offset:1024 sc1\nv_mfma_f32_16x16x32_bf16 v[76:79], v[160:163], a[8:11], 0\nR 178,185,20480\nM 76,164,24\nv_mfma_f32_16x16x32_bf16 v[60:63], v[160:163], a[4:7], 0\nR 180,185,24576\n"
"M 60,164,20\nbuffer_load_dwordx4 a[84:87], v252, s[20:23], s65 offen offset:1024 sc1\nv_mfma_f32_16x16x32_bf16 v[44:47], v[160:163], a[0:3], 0\nR 182,185,28672\nM 44,164,16\nG 4\n"
"v_mfma_f32_16x16x32_bf16 v[96:99], v[168:171], a[0:3], 0\nR 160,186,16384\nM 96,172,16\nv_mfma_f32_16x16x32_bf16 v[112:115], v[168:171], a[4:7], 0\nR 162,186,20480\nM 112,172,20\n"
"buffer_load_dwordx4 a[88:91], v252, s[20:23], s66 offen offset:1024 sc1\nv_mfma_f32_16x16x32_bf16 v[128:131], v[168:171], a[8:11], 0\nR 164,186,24576\nM 128,172,24\nv_mfma_f32_16x16x32_bf16 v[144:147], v[168:171], a[12:15], 0\nR 166,186,28672\n"
"M 144,172,28\ns_barrier\nG 4\nv_mfma_f32_16x16x32_bf16 v[148:151], v[176:179], a[12:15], 0\nR 168,187,16384\nM 148,180,28\n"
"buffer_load_dwordx4 a[92:95], v252, s[20:23], s67 offen offset:1024 sc1\nv_mfma_f32_16x16x32_bf16 v[132:135], v[176:179], a[8:11], 0\nR 170,187,20480\nM 132,180,24\nv_mfma_f32_16x16x32_bf16 v[116:119], v[176:179], a[4:7], 0\nR 172,187,24576\n"
"M 116,180,20\nbuffer_load_dwordx4 v221, s[12:15], s63 offen offset:1024 lds\nv_mfma_f32_16x16x32_bf16 v[100:103], v[176:179], a[0:3], 0\nR 174,187,28672\nM 100,180,16\nG 4\n"
"v_mfma_f32_16x16x32_bf16 v[104:107], v[160:163], a[0:3], 0\nR 176,184,32768\nM 104,164,16\nbuffer_load_dwordx4 a[96:99], v252, s[20:23], s64 offen offset:2048 sc1\nv_mfma_f32_16x16x32_bf16 v[120:123], v[160:163], a[4:7], 0\nR 178,184,36864\n"
"M 120,164,20\nbuffer_load_dwordx4 a[100:103], v252, s[20:23], s65 offen offset:2048 sc1\nv_mfma_f32_16x16x32_bf16 v[136:139], v[160:163], a[8:11], 0\nR 180,184,40960\nM 136,164,24\nv_mfma_f32_16x16x32_bf16 v[152:155], v[160:163], a[12:15], 0\n"
"R 182,184,45056\nM 152,164,28\nG 4\nv_mfma_f32_16x16x32_bf16 v[156:159], v[168:171], a[12:15], 0\nR 160,185,32768\nM 156,172,28\n"
"buffer_load_dwordx4 a[104:107], v252, s[20:23], s66 offen offset:2048 sc1\nv_mfma_f32_16x16x32_bf16 v[140:143], v[168:171], a[8:11], 0\nR 162,185,36864\nM 140,172,24\nv_mfma_f32_16x16x32_bf16 v[124:127], v[168:171], a[4:7], 0\nR 164,185,40960\n"
"M 124,172,20\nv_mfma_f32_16x16x32_bf16 v[108:111], v[168:171], a[0:3], 0\nR 166,185,45056\nM 108,172,16\nG 4\ns_waitcnt vmcnt(19)\n"
"M 32,176,32\nR 168,186,32768\ns_waitcnt vmcnt(15)\nM 32,180,48\nbuffer_load_dwordx4 a[108:111], v252, s[20:23], s67 offen offset:2048 sc1\nM 48,176,36\n"
"R 170,186,36864\ns_waitcnt vmcnt(15)\nM 48,180,52\ns_add_u32 m0, s50, 106496\nM 64,176,40\nR 172,186,40960\n"
"s_add_u32 s63, s54, 1605632\ns_waitcnt vmcnt(14)\nM 64,180,56\nbuffer_load_dwordx4 a[112:115], v252, s[20:23], s64 offen offset:3072 sc1\nM 80,176,44\nR 174,186,45056\n"
"s_waitcnt vmcnt(14)\nM 80,180,60\ns_barrier\nG 4\nM 84,160,44\nR 176,187,32768\n"
"M 84,164,60\nbuffer_load_dwordx4 v220, s[12:15], s63 offen lds\nM 68,160,40\nR 178,187,36864\nM 68,164,56\nbuffer_load_dwordx4 a[116:119], v252, s[20:23], s65 offen offset:3072 sc1\n"
"M 52,160,36\nR 180,187,40960\nM 52,164,52\nM 36,160,32\nR 182,187,45056\nM 36,164,48\n"
"G 4\nM 40,168,32\nR 160,184,49152\nM 40,172,48\nbuffer_load_dwordx4 a[120:123], v252, s[20:23], s66 offen offset:3072 sc1\nM 56,168,36\n"
"R 162,184,53248\nM 56,172,52\nM 72,168,40\nR 164,184,57344\nM 72,172,56\nbuffer_load_dwordx4 v221, s[12:15], s63 offen offset:1024 lds\n"
"M 88,168,44\nR 166,184,61440\nM 88,172,60\nG 4\nM 92,176,44\nR 168,185,49152\n"
"M 92,180,60\nbuffer_load_dwordx4 a[124:127], v252, s[20:23], s67 offen offset:3072 sc1\nM 76,176,40\nR 170,185,53248\nM 76,180,56\nM 60,176,36\n"
"R 172,185,57344\nM 60,180,52\ns_add_u32 s64, s70, 14688256\nbuffer_load_dwordx4 a[128:131], v252, s[20:23], s64 offen offset:0 sc1\nM 44,176,32\nR 174,185,61440\n"
"M 44,180,48\nG 4\nM 96,160,32\nR 176,186,49152\nM 96,164,48\ns_add_u32 s65, s70, 14721024\n"
"buffer_load_dwordx4 a[132:135], v252, s[20:23], s65 offen offset:0 sc1\nM 112,160,36\nR 178,186,53248\nM 112,164,52\nM 128,160,40\nR 180,186,57344\n"
"M 128,164,56\nM 144,160,44\nR 182,186,61440\nM 144,164,60\ns_barrier\nG 4\n"
"M 148,168,44\nR 160,187,49152\nM 148,172,60\ns_add_u32 s66, s71, 14688256\nbuffer_load_dwordx4 a[136:139], v252, s[20:23], s66 offen offset:0 sc1\nM 132,168,40\n"
"R 162,187,53248\nM 132,172,56\ns_add_u32 m0, s50, 114688\nM 116,168,36\nR 164,187,57344\ns_add_u32 s63, s54, 1376384\n"
"M 116,172,52\ns_add_u32 s67, s71, 14721024\nbuffer_load_dwordx4 a[140:143], v252, s[20:23], s67 offen offset:0 sc1\nM 100,168,32\nR 166,187,61440\nM 100,172,48\n"
"G 4\nM 104,176,32\nR 168,188,0\nM 104,180,48\nbuffer_load_dwordx4 v220, s[12:15], s63 offen lds\nM 120,176,36\n"
"R 170,188,4096\nM 120,180,52\nbuffer_load_dwordx4 a[144:147], v252, s[20:23], s64 offen offset:1024 sc1\nM 136,176,40\nR 172,188,8192\nM 136,180,56\n"
"M 152,176,44\nR 174,188,12288\nM 152,180,60\nG 4\nM 156,160,44\nR 176,189,0\n"
"M 156,164,60\nbuffer_load_dwordx4 a[148:151], v252, s[20:23], s65 offen offset:1024 sc1\nM 140,160,40\nR 178,189,4096\nM 140,164,56\nM 124,160,36\n"
"R 180,189,8192\nM 124,164,52\nbuffer_load_dwordx4 v221, s[12:15], s63 offen offset:1024 lds\nM 108,160,32\nR 182,189,12288\nM 108,164,48\n"
"G 4\ns_waitcnt vmcnt(26)\nM 32,168,64\nR 160,190,0\ns_waitcnt vmcnt(22)\nM 32,172,80\n"
"buffer_load_dwordx4 a[152:155], v252, s[20:23], s66 offen offset:1024 sc1\nM 48,168,68\nR 162,190,4096\ns_waitcnt vmcnt(22)\nM 48,172,84\nM 64,168,72\n"
"R 164,190,8192\ns_waitcnt vmcnt(21)\nM 64,172,88\nbuffer_load_dwordx4 a[156:159], v252, s[20:23], s67 offen offset:1024 sc1\nM 80,168,76\nR 166,190,12288\n"
"s_waitcnt vmcnt(21)\nM 80,172,92\ns_barrier\nG 4\nM 84,176,76\nR 168,191,0\n"
"M 84,180,92\nM 68,176,72\nR 170,191,4096\nM 68,180,88\nbuffer_load_dwordx4 a[160:163], v252, s[20:23], s64 offen offset:2048 sc1\nM 52,176,68\n"
"R 172,191,8192\nM 52,180,84\ns_add_u32 m0, s50, 0\nM 36,176,64\nR 174,191,12288\ns_add_u32 s63, s54, 1605760\n"
"M 36,180,80\nG 4\nM 40,160,64\nR 176,188,16384\nM 40,164,80\nbuffer_load_dwordx4 a[164:167], v252, s[20:23], s65 offen offset:2048 sc1\n"
"M 56,160,68\nR 178,188,20480\nM 56,164,84\nbuffer_load_dwordx4 v220, s[12:15], s63 offen lds\nM 72,160,72\nR 180,188,24576\n"
"M 72,164,88\nbuffer_load_dwordx4 a[168:171], v252, s[20:23], s66 offen offset:2048 sc1\nM 88,160,76\nR 182,188,28672\nM 88,164,92\nG 4\n"
"M 92,168,76\nR 160,189,16384\nbuffer_load_dwordx4 v221, s[12:15], s63 offen offset:1024 lds\nM 92,172,92\nM 76,168,72\nR 162,189,20480\n"
"M 76,172,88\nbuffer_load_dwordx4 a[172:175], v252, s[20:23], s67 offen offset:2048 sc1\nM 60,168,68\nR 164,189,24576\nM 60,172,84\nM 44,168,64\n"
"R 166,189,28672\nM 44,172,80\nG 4\nM 96,176,64\nR 168,190,16384\nM 96,180,80\n"
"buffer_load_dwordx4 a[176:179], v252, s[20:23], s64 offen offset:3072 sc1\nM 112,176,68\nR 170,190,20480\nM 112,180,84\nM 128,176,72\nR 172,190,24576\n"
"M 128,180,88\ns_add_u32 m0, s50, 8192\nM 144,176,76\nR 174,190,28672\ns_add_u32 s63, s54, 1835008\nM 144,180,92\n"
"s_waitcnt vmcnt(18)\ns_barrier\nG 4\nM 148,160,76\nR 176,191,16384\nM 148,164,92\n"
"buffer_load_dwordx4 a[180:183], v252, s[20:23], s65 offen offset:3072 sc1\nM 132,160,72\nR 178,191,20480\nM 132,164,88\nbuffer_load_dwordx4 v220, s[12:15], s63 offen lds\nM 116,160,68\n"
"R 180,191,24576\nM 116,164,84\nbuffer_load_dwordx4 a[184:187], v252, s[20:23], s66 offen offset:3072 sc1\nM 100,160,64\nR 182,191,28672\nM 100,164,80\n"
"G 4\nM 104,168,64\nR 160,188,32768\nbuffer_load_dwordx4 v221, s[12:15], s63 offen offset:1024 lds\nM 104,172,80\ns_add_u32 m0, s50, 16384\n"
"s_add_u32 s63, s54, 2064384\nbuffer_load_dwordx4 v220, s[12:15], s63 offen lds\nM 120,168,68\nR 162,188,36864\nM 120,172,84\nbuffer_load_dwordx4 a[188:191], v252, s[20:23], s67 offen offset:3072 sc1\n"
"M 136,168,72\nR 164,188,40960\nM 136,172,88\nbuffer_load_dwordx4 v221, s[12:15], s63 offen offset:1024 lds\nM 152,168,76\nR 166,188,45056\n"
"M 152,172,92\nG 4\nM 156,176,76\nR 168,189,32768\nM 156,180,92\ns_add_u32 s64, s70, 14692352\n"
"buffer_load_dwordx4 a[192:195], v252, s[20:23], s64 offen offset:0 sc1\nM 140,176,72\nR 170,189,36864\nM 140,180,88\nM 124,176,68\nR 172,189,40960\n"
"M 124,180,84\ns_add_u32 s65, s70, 14725120\nbuffer_load_dwordx4 a[196:199], v252, s[20:23], s65 offen offset:0 sc1\nM 108,176,64\nR 174,189,45056\nM 108,180,80\n"
"G 4\nM 32,160,96\nR 176,190,32768\nM 32,164,112\nM 48,160,100\nR 178,190,36864\n"
"M 48,164,116\ns_add_u32 s66, s71, 14692352\nbuffer_load_dwordx4 a[200:203], v252, s[20:23], s66 offen offset:0 sc1\nM 64,160,104\nR 180,190,40960\nM 64,164,120\n"
"s_add_u32 m0, s50, 24576\nM 80,160,108\nR 182,190,45056\ns_add_u32 s63, s54, 2293760\ns_waitcnt vmcnt(27)\nM 80,164,124\n"
"s_waitcnt vmcnt(12)\ns_barrier\nG 4\nM 84,168,108\nR 160,191,32768\nM 84,172,124\n"
"s_add_u32 s67, s71, 14725120\nbuffer_load_dwordx4 a[204:207], v252, s[20:23], s67 offen offset:0 sc1\nM 68,168,104\nR 162,191,36864\nM 68,172,120\nbuffer_load_dwordx4 v220, s[12:15], s63 offen lds\n"
"M 52,168,100\nR 164,191,40960\nM 52,172,116\nbuffer_load_dwordx4 v221, s[12:15], s63 offen offset:1024 lds\nM 36,168,96\nR 166,191,45056\n"
"s_add_u32 m0, s50, 32768\nM 36,172,112\nG 4\nM 40,176,96\nR 168,188,49152\ns_add_u32 s63, s54, 2523136\n"
"M 40,180,112\nbuffer_load_dwordx4 a[208:211], v252, s[20:23], s64 offen offset:1024 sc1\nM 56,176,100\nR 170,188,53248\nM 56,180,116\nbuffer_load_dwordx4 v220, s[12:15], s63 offen lds\n"
"M 72,176,104\nR 172,184,0\nM 72,180,120\nbuffer_load_dwordx4 a[212:215], v252, s[20:23], s65 offen offset:1024 sc1\nM 88,176,108\nR 174,184,4096\n"
"M 88,180,124\nG 4\nM 92,160,108\nR 176,189,49152\nbuffer_load_dwordx4 v221, s[12:15], s63 offen offset:1024 lds\nM 92,164,124\n"
"M 76,160,104\nR 178,189,53248\nM 76,164,120\nbuffer_load_dwordx4 a[216:219], v252, s[20:23], s66 offen offset:1024 sc1\nM 60,160,100\nR 180,185,0\n"
"Lq1025:\ns_lshr_b32 s60, s45, 3\ns_lshl2_add_u32 s60, s60, s40\ns_lshl_b32 s60, s60, 8\nv_lshl_add_u32 v224, v252, 3, s60\nv_add_u32 v224, Lq1025+4096-LqPC, v224\n"
"s_mov_b64 exec, 3\nglobal_load_dword v224, v224, s[98:99]\ns_mov_b64 exec, -1\nM 60,164,116\nM 44,160,96\nR 182,185,4096\n"
"M 44,164,112\nG 4\nM 96,168,96\nR 160,190,49152\nM 96,172,112\nbuffer_load_dwordx4 a[220:223], v252, s[20:23], s67 offen offset:1024 sc1\n"
"M 112,168,100\nR 162,190,53248\nM 112,172,116\nM 128,168,104\nR 164,186,0\nM 128,172,120\n"
"M 144,168,108\nR 166,186,4096\ns_add_u32 m0, s50, 40960\nM 144,172,124\ns_waitcnt vmcnt(13)\ns_barrier\n"
"G 4\nM 148,176,108\nR 168,191,49152\ns_add_u32 s63, s54, 2752512\nM 148,180,124\nbuffer_load_dwordx4 a[224:227], v252, s[20:23], s64 offen offset:2048 sc1\n"
"M 132,176,104\nR 170,191,53248\nM 132,180,120\nbuffer_load_dwordx4 a[228:231], v252, s[20:23], s65 offen offset:2048 sc1\nM 116,176,100\nR 172,187,0\n"
"buffer_load_dwordx4 v220, s[12:15], s63 offen lds\nM 116,180,116\nbuffer_load_dwordx4 v221, s[12:15], s63 offen offset:1024 lds\nM 100,176,96\nR 174,187,4096\ns_add_u32 m0, s50, 49152\n"
"M 100,180,112\nG 4\nM 104,160,96\nR 176,184,8192\ns_add_u32 s63, s54, 2981888\nM 104,164,112\n"
"buffer_load_dwordx4 a[232:235], v252, s[20:23], s66 offen offset:2048 sc1\nM 120,160,100\nR 178,184,12288\nM 120,164,116\nbuffer_load_dwordx4 v220, s[12:15], s63 offen lds\nM 136,160,104\n"
"R 180,184,16384\nM 136,164,120\nbuffer_load_dwordx4 v221, s[12:15], s63 offen offset:1024 lds\nM 152,160,108\nR 182,184,20480\nM 152,164,124\n"
"G 4\nM 156,168,108\nR 160,185,8192\nM 156,172,124\nbuffer_load_dwordx4 a[236:239], v252, s[20:23], s67 offen offset:2048 sc1\nM 140,168,104\n"
"R 162,185,12288\nM 140,172,120\nM 124,168,100\nR 164,185,16384\nM 124,172,116\nbuffer_load_dwordx4 a[240:243], v252, s[20:23], s64 offen offset:3072 sc1\n"
"M 108,168,96\nR 166,185,20480\nM 108,172,112\nG 4\nM 32,176,128\nR 168,186,8192\n"
"M 32,180,144\nM 48,176,132\nR 170,186,12288\nM 48,180,148\nbuffer_load_dwordx4 a[244:247], v252, s[20:23], s65 offen offset:3072 sc1\nM 64,176,136\n"
"R 172,186,16384\nM 64,180,152\nM 80,176,140\nR 174,186,20480\ns_add_u32 m0, s50, 57344\nM 80,180,156\n"
"s_waitcnt vmcnt(13)\ns_barrier\nG 4\nM 84,160,140\nR 176,187,8192\ns_add_u32 s63, s54, 3211264\n"
"M 84,164,156\nbuffer_load_dwordx4 a[248:251], v252, s[20:23], s66 offen offset:3072 sc1\nM 68,160,136\nR 178,187,12288\nM 68,164,152\ns_lshr_b32 s84, s49, 3\n"
"M 52,160,132\nR 180,187,16384\nbuffer_load_dwordx4 v220, s[12:15], s63 offen lds\nM 52,164,148\ns_add_u32 s84, s84, 83361792\nM 36,160,128\n"
"R 182,187,20480\nM 36,164,144\nbuffer_load_dwordx4 v[0:3], v252, s[20:23], s84 offen offset:0 sc1\nG 4\nM 40,168,128\nR 160,184,24576\n"
"buffer_load_dwordx4 v221, s[12:15], s63 offen offset:1024 lds\nM 40,172,144\nbuffer_load_dwordx4 a[252:255], v252, s[20:23], s67 offen offset:3072 sc1\ns_add_u32 s63, s54, 3440640\nM 56,168,132\nR 162,184,28672\n"
"s_add_u32 m0, s50, 65536\nM 56,172,148\nbuffer_load_dwordx4 v[4:7], v252, s[20:23], s84 offen offset:1024 sc1\nM 72,168,136\nR 164,184,32768\ns_lshr_b32 s85, s49, 3\n"
"buffer_load_dwordx4 v220, s[12:15], s63 offen lds\nM 72,172,152\nbuffer_load_dwordx4 v[8:11], v252, s[20:23], s84 offen offset:2048 sc1\nM 88,168,140\nR 166,184,36864\ns_add_u32 s85, s85, 83365888\n"
"M 88,172,156\nG 4\nM 92,176,140\nR 168,185,24576\nbuffer_load_dwordx4 v221, s[12:15], s63 offen offset:1024 lds\nM 92,180,156\n"
"buffer_load_dwordx4 v[12:15], v252, s[20:23], s84 offen offset:3072 sc1\nM 76,176,136\nR 170,185,28672\nM 76,180,152\nbuffer_load_dwordx4 v[16:19], v252, s[20:23], s85 offen offset:0 sc1\nM 60,176,132\n"
"R 172,185,32768\nM 60,180,148\nbuffer_load_dwordx4 v[20:23], v252, s[20:23], s85 offen offset:1024 sc1\nM 44,176,128\nR 174,185,36864\nM 44,180,144\n"
"G 4\nM 32,160,160\nR 176,186,24576\nM 32,164,176\nbuffer_load_dwordx4 v[24:27], v252, s[20:23], s85 offen offset:2048 sc1\nM 48,160,164\n"
"R 178,186,28672\nM 48,164,180\nbuffer_load_dwordx4 v[28:31], v252, s[20:23], s85 offen offset:3072 sc1\nM 64,160,168\nR 180,186,32768\ns_add_u32 s63, s54, 1835136\n"
"M 64,164,184\ns_add_u32 m0, s50, 73728\nM 80,160,172\nR 182,186,36864\nM 80,164,188\ns_waitcnt vmcnt(17)\n"
"s_barrier\nG 4\nM 84,168,172\nR 160,187,24576\nM 84,172,188\nbuffer_load_dwordx4 v220, s[12:15], s63 offen lds\n"
"M 68,168,168\nR 162,187,28672\nM 68,172,184\nM 52,168,164\nR 164,187,32768\nv_bfe_u32 v250, v252, 5, 1\n"
"v_mul_u32_u24 v250, 2560, v250\nM 52,172,180\nbuffer_load_dwordx4 v221, s[12:15], s63 offen offset:1024 lds\nv_and_b32 v251, 16, v252\nM 36,168,160\nR 166,187,36864\n"
"s_add_u32 m0, s50, 81920\nv_lshl_add_u32 v250, v251, 1, v250\nM 36,172,176\ns_lshl_b32 s60, s90, 6\nv_add_u32 v250, s60, v250\nG 4\n"
"M 40,176,160\nR 168,184,40960\ns_add_u32 s63, s54, 2064512\nM 40,180,176\nbuffer_load_dwordx4 v220, s[12:15], s63 offen lds\nM 56,176,164\n"
"R 170,184,45056\nM 56,180,180\nM 72,176,168\nR 172,184,49152\nM 72,180,184\nbuffer_load_dwordx4 v221, s[12:15], s63 offen offset:1024 lds\n"
"M 88,176,172\nR 174,184,53248\nM 88,180,188\nG 4\nM 92,160,172\nR 176,185,40960\n"
"M 92,164,188\nM 76,160,168\nR 178,185,45056\nM 76,164,184\ns_add_u32 m0, s50, 90112\nM 60,160,164\n"
"R 180,185,49152\nM 60,164,180\ns_add_u32 s63, s54, 2293888\nM 44,160,160\nR 182,185,53248\nM 44,164,176\n"
"G 4\nM 32,168,192\nR 160,186,40960\nM 32,172,208\nbuffer_load_dwordx4 v220, s[12:15], s63 offen lds\nM 48,168,196\n"
"R 162,186,45056\nM 48,172,212\nM 64,168,200\nR 164,186,49152\nM 64,172,216\nbuffer_load_dwordx4 v221, s[12:15], s63 offen offset:1024 lds\n"
"M 80,168,204\nR 166,186,53248\ns_add_u32 m0, s50, 98304\nM 80,172,220\ns_waitcnt vmcnt(11)\ns_barrier\n"
"G 4\nM 84,176,204\nR 168,187,40960\ns_add_u32 s63, s54, 2523264\nM 84,180,220\nbuffer_load_dwordx4 v220, s[12:15], s63 offen lds\n"
"M 68,176,200\nR 170,187,45056\nv_min3_u32 v230, v1, v3, v5\nv_min3_u32 v230, v230, v7, v9\nM 68,180,216\ns_waitcnt vmcnt(11)\n"
"v_min3_u32 v230, v230, v11, v13\ns_waitcnt vmcnt(10)\nv_min3_u32 v230, v230, v15, v17\ns_waitcnt vmcnt(9)\nv_min3_u32 v230, v230, v19, v21\nM 52,176,196\n"
"R 172,187,49152\ns_waitcnt vmcnt(8)\nv_min3_u32 v230, v230, v23, v25\ns_waitcnt vmcnt(7)\nv_min3_u32 v230, v230, v27, v29\nM 52,180,212\n"
"buffer_load_dwordx4 v221, s[12:15], s63 offen offset:1024 lds\nv_min3_u32 v230, v230, v31, v31\nM 36,176,192\nR 174,187,53248\ns_add_u32 s63, s54, 2752640\nv_max3_u32 v231, v1, v3, v5\n"
"M 36,180,208\nG 4\nv_max3_u32 v231, v231, v7, v9\nv_max3_u32 v231, v231, v11, v13\nv_max3_u32 v231, v231, v15, v17\nM 40,160,192\n"
"R 176,184,57344\nv_max3_u32 v231, v231, v19, v21\nv_max3_u32 v231, v231, v23, v25\nM 40,164,208\ns_add_u32 m0, s50, 106496\nv_max3_u32 v231, v231, v27, v29\n"
"v_max3_u32 v231, v231, v31, v31\nv_cmp_ne_u32 s[86:87], s46, v230\nM 56,160,196\nR 178,184,61440\nv_cmp_ne_u32 s[88:89], s46, v231\nM 56,164,212\n"
"buffer_load_dwordx4 v220, s[12:15], s63 offen lds\ns_or_b64 s[86:87], s[86:87], s[88:89]\ns_cbranch_scc1 Lq800\nLq801:\nM 72,160,200\nR 180,188,0\n"
"v_add_f32 v0, v0, v16\nv_add_f32 v2, v2, v18\nM 72,164,216\nv_add_f32 v4, v4, v20\nv_add_f32 v6, v6, v22\nv_add_f32 v8, v8, v24\n"
"M 88,160,204\nR 182,188,4096\nv_add_f32 v10, v10, v26\nv_add_f32 v12, v12, v28\nM 88,164,220\nG 4\n"
"v_add_f32 v14, v14, v30\nv_add_f32 v0, v0, v8\nv_add_f32 v2, v2, v10\nM 92,168,204\nR 160,185,57344\nv_add_f32 v4, v4, v12\n"
"v_add_f32 v6, v6, v14\nM 92,172,220\nbuffer_load_dwordx4 v221, s[12:15], s63 offen offset:1024 lds\nv_add_f32 v0, v0, v4\nM 76,168,200\nR 162,185,61440\n"
"s_add_u32 s63, s54, 2982016\nv_add_f32 v2, v2, v6\nM 76,172,216\ns_add_u32 m0, s50, 114688\nv_lshrrev_b32 v231, 1, v252\nv_lshrrev_b32 v230, 2, v252\n"
"s_lshl_b32 s62, s40, 9\ns_add_u32 s62, s62, 159232\nv_add_u32 v231, s62, v231\nM 60,168,196\nR 164,189,0\nds_write2_b32 v231, v0, v2 offset1:1\n"
"M 60,172,212\nbuffer_load_dwordx4 v220, s[12:15], s63 offen lds\ns_lshl_b32 s62, s40, 9\ns_add_u32 s62, s62, 159232\nv_add_u32 v230, s62, v230\nM 44,168,192\n"
"R 166,189,4096\nds_read2st64_b32 v[232:233], v230 offset1:1\nM 44,172,208\nG 6\nM 32,176,224\nR 168,186,57344\n"
"M 32,180,240\nbuffer_load_dwordx4 v221, s[12:15], s63 offen offset:1024 lds\nM 48,176,228\nR 170,186,61440\ns_add_u32 s63, s54, 3211392\nM 48,180,244\n"
"s_add_u32 m0, s50, 0\nM 64,176,232\nR 172,190,0\nG 3\nv_add_f32 v232, v232, v233\nv_mov_b32 v233, v232\n"
"M 64,180,248\nbuffer_load_dwordx4 v220, s[12:15], s63 offen lds\nv_permlane32_swap_b32 v232, v233\nM 80,176,236\nR 174,190,4096\nv_mov_b32 v230, v232\n"
"v_mov_b32 v231, v233\nM 80,180,252\ns_waitcnt vmcnt(9)\ns_barrier\nv_permlane16_swap_b32 v230, v232\nM 84,160,236\n"
"R 176,187,57344\nv_permlane16_swap_b32 v231, v233\nv_mul_f32 v230, 0xba800000, v230\nM 84,164,252\nbuffer_load_dwordx4 v221, s[12:15], s63 offen offset:1024 lds\nv_mul_f32 v214, v230, v230\n"
"M 68,160,232\nR 178,187,61440\ns_add_u32 s63, s54, 3440768\nv_mul_f32 v231, 0x3a800000, v231\nM 68,164,248\ns_add_u32 m0, s50, 8192\n"
"v_sub_f32 v231, v231, v214\nv_max_f32 v231, 0, v231\nv_add_f32 v231, 0x3727c5ac, v231\nM 52,160,228\nR 180,191,0\nv_and_b32 v208, s77, v230\n"
"v_rsq_f32 v234, v231\nM 52,164,244\nbuffer_load_dwordx4 v220, s[12:15], s63 offen lds\nv_mul_f32 v216, v231, v234\nM 36,160,224\nR 182,191,4096\n"
"v_sub_f32 v230, v230, v208\nv_cvt_pk_bf16_f32 v209, v230, v216\nM 36,164,240\nG 4\nv_cvt_pk_bf16_f32 v208, v208, v208\nv_mul_f32 v214, 0xc0135761, v234\n"
"v_mul_f32 v215, v234, v234\nM 40,168,224\nR 160,188,8192\nv_mul_f32 v215, v215, v214\nM 40,172,240\ns_mov_b64 s[60:61], 0xffff\n"
"v_cndmask_b32 v208, 0, v208, s[60:61]\nv_mul_f32 v215, 0x3d372713, v215\nM 56,168,228\nR 162,188,12288\nv_mul_f32 v232, 0xba800000, v232\nM 56,172,244\n"
"buffer_load_dwordx4 v221, s[12:15], s63 offen offset:1024 lds\nM 72,168,232\nR 164,188,16384\ns_add_u32 s63, s55, 0\nM 72,172,248\ns_add_u32 m0, s50, 16384\n"
"s_mov_b64 s[60:61], 0xffff\nv_cndmask_b32 v209, 0, v209, s[60:61]\nv_mul_f32 v217, v232, v232\nM 88,168,236\nR 166,188,20480\nv_mul_f32 v233, 0x3a800000, v233\n"
"M 88,172,252\nG 4\nv_sub_f32 v233, v233, v217\nv_max_f32 v233, 0, v233\nM 92,176,236\nR 168,189,8192\n"
"v_add_f32 v233, 0x3727c5ac, v233\nM 92,180,252\nbuffer_load_dwordx4 v222, s[12:15], s63 offen lds\nM 76,176,232\nR 170,189,12288\nv_and_b32 v212, s77, v232\n"
"M 76,180,248\nv_rsq_f32 v235, v233\nM 60,176,228\nR 172,189,16384\nv_mul_f32 v219, v233, v235\nM 60,180,244\n"
"buffer_load_dwordx4 v223, s[12:15], s63 offen offset:1024 lds\nM 44,176,224\nR 174,189,20480\nv_sub_f32 v232, v232, v212\nM 44,180,240\ns_waitcnt vmcnt(10)\n"
"s_barrier\nG 4\nM 96,160,128\nR 176,190,8192\nv_cvt_pk_bf16_f32 v213, v232, v219\nM 96,164,144\n"
"s_mov_b32 m0, s51\nv_cvt_pk_bf16_f32 v212, v212, v212\nv_mul_f32 v217, 0xc0135761, v235\nM 112,160,132\nR 178,190,12288\nv_mul_f32 v218, v235, v235\n"
"M 112,164,148\ns_mov_b64 s[60:61], 0xffff\nv_cndmask_b32 v212, 0, v212, s[60:61]\nv_mul_f32 v218, v218, v217\nM 128,160,136\nR 180,190,16384\n"
"v_mul_f32 v218, 0x3d372713, v218\nM 128,164,152\nds_write_addtid_b32 v64 offset:0\nM 144,160,140\nR 182,190,20480\nM 144,164,156\n"
"ds_write_addtid_b32 v65 offset:256\nG 6\nM 148,168,140\nR 160,191,8192\nM 148,172,156\nds_write_addtid_b32 v66 offset:512\n"
"M 132,168,136\nR 162,191,12288\nM 132,172,152\nds_write_addtid_b32 v67 offset:768\nM 116,168,132\nR 164,191,16384\n"
"M 116,172,148\nds_write_addtid_b32 v68 offset:1024\nM 100,168,128\nR 166,191,20480\nds_write_addtid_b32 v69 offset:1280\nM 100,172,144\n"
"ds_write_addtid_b32 v70 offset:1536\nds_write_addtid_b32 v71 offset:1792\nG 11\nM 104,176,128\nR 168,188,24576\nds_write_addtid_b32 v72 offset:2048\n"
"M 104,180,144\nds_write_addtid_b32 v73 offset:2304\nds_write_addtid_b32 v74 offset:2560\nM 120,176,132\nR 170,188,28672\nds_write_addtid_b32 v75 offset:2816\n"
"M 120,180,148\nds_write_addtid_b32 v76 offset:3072\nds_write_addtid_b32 v77 offset:3328\nM 136,176,136\nR 172,188,32768\nds_write_addtid_b32 v78 offset:3584\n"
"M 136,180,152\nds_write_addtid_b32 v79 offset:3840\nds_write_addtid_b32 v80 offset:4096\nM 152,176,140\nR 174,188,36864\nds_write_addtid_b32 v81 offset:4352\n"
"M 152,180,156\nds_write_addtid_b32 v82 offset:4608\nds_write_addtid_b32 v83 offset:4864\nG 15\nM 156,160,140\nR 176,189,24576\n"
"ds_write_addtid_b32 v84 offset:5120\nM 156,164,156\nds_write_addtid_b32 v85 offset:5376\nds_write_addtid_b32 v86 offset:5632\nM 140,160,136\nR 178,189,28672\n"
"ds_write_addtid_b32 v87 offset:5888\nM 140,164,152\nds_write_addtid_b32 v88 offset:6144\nds_write_addtid_b32 v89 offset:6400\nM 124,160,132\nR 180,189,32768\n"
"ds_write_addtid_b32 v90 offset:6656\nM 124,164,148\nds_write_addtid_b32 v91 offset:6912\nds_write_addtid_b32 v92 offset:7168\nM 108,160,128\nR 182,189,36864\n"
"ds_write_addtid_b32 v93 offset:7424\nM 108,164,144\nds_write_addtid_b32 v94 offset:7680\nds_write_addtid_b32 v95 offset:7936\nG 15\nM 96,168,160\n"
"R 160,190,24576\ns_add_u32 m0, s50, 24576\ns_add_u32 s63, s55, 458752\nM 96,172,176\nbuffer_load_dwordx4 v222, s[12:15], s63 offen lds\nM 112,168,164\n"
"R 162,190,28672\ns_waitcnt vmcnt(7) lgkmcnt(2)\ns_barrier\nM 112,172,180\nds_read2st64_b32 v[64:65], v225 offset0:0 offset1:1\nds_read2st64_b32 v[66:67], v225 offset0:2 offset1:3\n"
"M 128,168,168\nR 164,190,32768\nds_read2st64_b32 v[68:69], v225 offset0:4 offset1:5\nM 128,172,184\nds_read2st64_b32 v[70:71], v225 offset0:6 offset1:7\nds_read2st64_b32 v[72:73], v225 offset0:8 offset1:9\n"
"M 144,168,172\nR 166,190,36864\nds_read2st64_b32 v[74:75], v225 offset0:10 offset1:11\nM 144,172,188\nds_read2st64_b32 v[76:77], v225 offset0:12 offset1:13\ns_mov_b64 s[60:61], 0xffff\n"
"v_cndmask_b32 v213, 0, v213, s[60:61]\nds_read2st64_b32 v[78:79], v225 offset0:14 offset1:15\nM 148,176,172\nR 168,191,24576\nds_read2st64_b32 v[80:81], v225 offset0:16 offset1:17\nM 148,180,188\n"
"buffer_load_dwordx4 v223, s[12:15], s63 offen offset:1024 lds\nG 11\nv_add_f32 v32, v32, v64\nM 132,176,168\nR 170,191,28672\nv_add_f32 v33, v33, v65\n"
"G 11\nv_add_f32 v34, v34, v66\nM 132,180,184\ns_add_u32 m0, s50, 32768\nds_read2st64_b32 v[82:83], v225 offset0:18 offset1:19\nv_add_f32 v35, v35, v67\n"
"M 116,176,164\nR 172,191,32768\nG 11\nv_add_f32 v36, v36, v68\nv_add_f32 v37, v37, v69\nM 116,180,180\n"
"s_add_u32 s63, s55, 917504\nds_read2st64_b32 v[84:85], v225 offset0:20 offset1:21\nG 11\nv_add_f32 v38, v38, v70\nM 100,176,160\nR 174,191,36864\n"
"v_add_f32 v39, v39, v71\nG 11\nv_add_f32 v40, v40, v72\nM 100,180,176\nds_read2st64_b32 v[86:87], v225 offset0:22 offset1:23\nG 11\n"
"v_add_f32 v41, v41, v73\nM 104,160,160\nR 176,188,40960\nG 11\nv_add_f32 v42, v42, v74\nv_add_f32 v43, v43, v75\n"
"M 104,164,176\nbuffer_load_dwordx4 v222, s[12:15], s63 offen lds\nG 10\nv_add_f32 v44, v44, v76\nM 120,160,164\nR 178,188,45056\n"
"v_add_f32 v45, v45, v77\nG 10\nv_add_f32 v46, v46, v78\nM 120,164,180\nds_read2st64_b32 v[88:89], v225 offset0:24 offset1:25\nv_add_f32 v47, v47, v79\n"
"G 9\nv_add_f32 v48, v48, v80\nM 136,160,168\nR 180,188,49152\nv_add_f32 v49, v49, v81\nG 8\n"
"v_add_f32 v50, v50, v82\nM 136,164,184\nds_read2st64_b32 v[90:91], v225 offset0:26 offset1:27\nv_add_f32 v51, v51, v83\nG 7\nv_add_f32 v52, v52, v84\n"
"M 152,160,172\nR 182,188,53248\nv_add_f32 v53, v53, v85\nM 152,164,188\nds_read2st64_b32 v[92:93], v225 offset0:28 offset1:29\nG 8\n"
"G 7\nv_add_f32 v54, v54, v86\nM 156,168,172\nR 160,189,40960\nv_add_f32 v55, v55, v87\nM 156,172,188\n"
"buffer_load_dwordx4 v223, s[12:15], s63 offen offset:1024 lds\nM 140,168,168\nR 162,189,45056\ns_mov_b32 s60, 157184\nv_bfe_u32 v246, v252, 4, 4\nv_lshl_add_u32 v246, v246, 3, s60\n"
"M 140,172,184\nds_read_b64 v[238:239], v246 offset:0\ns_add_u32 m0, s50, 40960\nM 124,168,164\nR 164,189,49152\nds_read_b64 v[240:241], v246 offset:128\n"
"M 124,172,180\nds_read_b64 v[242:243], v246 offset:256\ns_add_u32 s63, s55, 1376256\nM 108,168,160\nR 166,189,53248\nds_read_b64 v[244:245], v246 offset:384\n"
"M 108,172,176\nds_read_b64 v[246:247], v246 offset:0\nG 10\nM 96,176,192\nR 168,190,40960\nv_add_f32 v56, v56, v88\n"
"M 96,180,208\nbuffer_load_dwordx4 v222, s[12:15], s63 offen lds\nM 112,176,196\nR 170,190,45056\ns_waitcnt vmcnt(5) lgkmcnt(2)\ns_barrier\n"
"M 112,180,212\nds_read2st64_b32 v[94:95], v225 offset0:30 offset1:31\nv_add_f32 v57, v57, v89\nM 128,176,200\nR 172,190,49152\nv_add_f32 v58, v58, v90\n"
"M 128,180,216\nv_add_f32 v59, v59, v91\nv_add_f32 v60, v60, v92\nM 144,176,204\nR 174,190,53248\nv_add_f32 v61, v61, v93\n"
"M 144,180,220\nG 2\nv_add_f32 v62, v62, v94\nv_add_f32 v63, v63, v95\nM 148,160,204\nR 176,191,40960\n"
"v_mfma_f32_16x16x32_bf16 v[32:35], v[238:241], v[208:211], v[32:35]\nM 148,164,220\nv_mfma_f32_16x16x32_bf16 v[36:39], v[240:243], v[208:211], v[36:39]\nbuffer_load_dwordx4 v223, s[12:15], s63 offen offset:1024 lds\nM 132,160,200\nR 178,191,45056\n"
"v_mfma_f32_16x16x32_bf16 v[40:43], v[242:245], v[208:211], v[40:43]\ns_add_u32 s63, s55, 1835008\nM 132,164,216\nv_mfma_f32_16x16x32_bf16 v[44:47], v[244:247], v[208:211], v[44:47]\ns_add_u32 m0, s50, 49152\nM 116,160,196\n"
"R 180,191,49152\nv_mfma_f32_16x16x32_bf16 v[48:51], v[244:247], v[210:213], v[48:51]\nM 116,164,212\nv_mfma_f32_16x16x32_bf16 v[52:55], v[238:241], v[210:213], v[52:55]\nbuffer_load_dwordx4 v222, s[12:15], s63 offen lds\nM 100,160,192\n"
"R 182,191,53248\nv_mfma_f32_16x16x32_bf16 v[56:59], v[240:243], v[210:213], v[56:59]\nM 100,164,208\nv_mfma_f32_16x16x32_bf16 v[60:63], v[242:245], v[210:213], v[60:63]\nG 4\nM 104,168,192\n"
"R 160,184,0\nM 104,172,208\nbuffer_load_dwordx4 v223, s[12:15], s63 offen offset:1024 lds\nM 120,168,196\nR 162,184,4096\nM 120,172,212\n"
"s_add_u32 m0, s50, 57344\nM 136,168,200\nR 164,184,8192\nM 136,172,216\ns_add_u32 s63, s55, 2293760\nM 152,168,204\n"
"R 166,184,12288\nv_mul_f32 v230, v32, v32\nv_mul_f32 v231, v33, v33\nM 152,172,220\ns_add_u32 s84, s73, 37748736\ns_add_u32 s85, s73, 37863424\n"
"G 4\nv_mul_f32 v232, v34, v34\nM 156,176,204\nR 168,185,0\nv_mul_f32 v233, v35, v35\nv_fma_f32 v230, v230, v215, v214\n"
"M 156,180,220\nbuffer_load_dwordx4 v222, s[12:15], s63 offen lds\nv_fma_f32 v231, v231, v215, v214\nM 140,176,200\nR 170,185,4096\nv_fma_f32 v232, v232, v215, v214\n"
"v_fma_f32 v233, v233, v215, v214\nM 140,180,216\nv_mul_f32 v230, v32, v230\nv_mul_f32 v231, v33, v231\nv_mul_f32 v232, v34, v232\nM 124,176,196\n"
"R 172,185,8192\nv_mul_f32 v233, v35, v233\nv_exp_f32 v230, v230\nM 124,180,212\nv_exp_f32 v231, v231\nv_exp_f32 v232, v232\n"
"M 108,176,192\nR 174,185,12288\ns_mov_b32 s60, 157696\nv_bfe_u32 v246, v252, 4, 4\nv_lshl_add_u32 v246, v246, 3, s60\nv_exp_f32 v233, v233\n"
"M 108,180,208\nds_read_b64 v[238:239], v246 offset:0\nG 5\nv_fma_f32 v230, v230, v216, v216\nM 96,160,224\nR 176,186,0\n"
"ds_read_b64 v[240:241], v246 offset:128\nv_fma_f32 v231, v231, v216, v216\nM 96,164,240\nds_read_b64 v[242:243], v246 offset:256\nv_fma_f32 v232, v232, v216, v216\nv_fma_f32 v233, v233, v216, v216\n"
"M 112,160,228\nR 178,186,4096\nds_read_b64 v[244:245], v246 offset:384\nv_rcp_f32 v230, v230\nM 112,164,244\nds_read_b64 v[246:247], v246 offset:0\n"
"v_mul_f32 v230, v32, v230\nv_rcp_f32 v231, v231\nM 128,160,232\nR 180,186,8192\nv_mul_f32 v231, v33, v231\nv_rcp_f32 v232, v232\n"
"M 128,164,248\nbuffer_load_dwordx4 v223, s[12:15], s63 offen offset:1024 lds\nv_rcp_f32 v233, v233\nM 144,160,236\nR 182,186,12288\ns_add_u32 s63, s55, 2752512\n"
"v_mul_f32 v232, v34, v232\nM 144,164,252\nG 9\nv_mul_f32 v233, v35, v233\nv_cvt_pk_bf16_f32 v32, v230, v231\nv_cvt_pk_bf16_f32 v33, v232, v233\n"
"M 148,168,236\nR 160,187,0\nv_mul_f32 v234, v36, v36\nv_mul_f32 v235, v37, v37\nM 148,172,252\ns_add_u32 m0, s50, 65536\n"
"v_mul_f32 v236, v38, v38\nv_mul_f32 v237, v39, v39\nv_fma_f32 v234, v234, v215, v214\nM 132,168,232\nR 162,187,4096\nv_fma_f32 v235, v235, v215, v214\n"
"v_fma_f32 v236, v236, v215, v214\nM 132,172,248\nbuffer_load_dwordx4 v222, s[12:15], s63 offen lds\nv_fma_f32 v237, v237, v215, v214\nM 116,168,228\nR 164,187,8192\n"
"G 9\nv_mfma_f32_16x16x32_bf16 v[96:99], v[238:241], v[208:211], v[96:99]\nv_mul_f32 v234, v36, v234\nv_mul_f32 v235, v37, v235\nM 116,172,244\nv_exp_f32 v234, v234\n"
"v_mul_f32 v236, v38, v236\nM 100,168,224\nR 166,187,12288\nG 9\nv_mfma_f32_16x16x32_bf16 v[100:103], v[240:243], v[208:211], v[100:103]\nv_mul_f32 v237, v39, v237\n"
"v_exp_f32 v235, v235\nM 100,172,240\nG 4\nv_exp_f32 v236, v236\nv_exp_f32 v237, v237\nM 104,176,224\n"
"R 168,184,16384\nv_mfma_f32_16x16x32_bf16 v[104:107], v[242:245], v[208:211], v[104:107]\nv_fma_f32 v234, v234, v216, v216\nv_fma_f32 v235, v235, v216, v216\nM 104,180,240\nbuffer_load_dwordx4 v223, s[12:15], s63 offen offset:1024 lds\n"
"v_fma_f32 v236, v236, v216, v216\nM 120,176,228\nR 170,184,20480\nv_mfma_f32_16x16x32_bf16 v[108:111], v[244:247], v[208:211], v[108:111]\nv_fma_f32 v237, v237, v216, v216\nv_rcp_f32 v234, v234\n"
"M 120,180,244\ns_add_u32 m0, s50, 73728\nv_rcp_f32 v235, v235\nv_rcp_f32 v236, v236\nM 136,176,232\nR 172,186,16384\n"
"v_mfma_f32_16x16x32_bf16 v[112:115], v[244:247], v[210:213], v[112:115]\nv_mul_f32 v234, v36, v234\nv_rcp_f32 v237, v237\nM 136,180,248\ns_add_u32 s63, s55, 3211264\nv_mul_f32 v235, v37, v235\n"
"v_mul_f32 v236, v38, v236\nv_mul_f32 v237, v39, v237\nM 152,176,236\nR 174,186,20480\nv_mfma_f32_16x16x32_bf16 v[116:119], v[238:241], v[210:213], v[116:119]\nv_cvt_pk_bf16_f32 v34, v234, v235\n"
"v_cvt_pk_bf16_f32 v35, v236, v237\nM 152,180,252\nG 4\nv_mul_f32 v230, v40, v40\nv_mul_f32 v231, v41, v41\nv_mul_f32 v232, v42, v42\n"
"M 156,160,236\nR 176,185,16384\nv_mfma_f32_16x16x32_bf16 v[120:123], v[240:243], v[210:213], v[120:123]\nv_mul_f32 v233, v43, v43\nv_fma_f32 v230, v230, v215, v214\nM 156,164,252\n"
"buffer_load_dwordx4 v222, s[12:15], s63 offen lds\nv_fma_f32 v231, v231, v215, v214\nM 140,160,232\nR 178,185,20480\nv_mfma_f32_16x16x32_bf16 v[124:127], v[242:245], v[210:213], v[124:127]\nv_fma_f32 v232, v232, v215, v214\n"
"v_fma_f32 v233, v233, v215, v214\nM 140,164,248\nbuffer_load_dwordx4 v223, s[12:15], s63 offen offset:1024 lds\nv_mul_f32 v230, v40, v230\nM 124,160,228\nR 180,187,16384\n"
"s_add_u32 m0, s50, 81920\ns_add_u32 s63, s54, 256\nv_mul_f32 v231, v41, v231\nM 124,164,244\nbuffer_load_dwordx4 v220, s[12:15], s63 offen lds\nv_mul_f32 v232, v42, v232\n"
"M 108,160,224\nR 182,187,20480\nv_mul_f32 v233, v43, v233\nv_exp_f32 v230, v230\nM 108,164,240\ns_waitcnt vmcnt(9)\n"
"s_barrier\nG 4\nv_exp_f32 v231, v231\nv_mfma_f32_16x16x32_bf16 v[0:3], v[168:171], a[0:3], 0\nR 160,184,24576\nv_fma_f32 v230, v230, v216, v216\n"
"v_exp_f32 v232, v232\nM 0,172,16\ns_mov_b32 m0, s51\nv_exp_f32 v233, v233\nv_fma_f32 v231, v231, v216, v216\nv_mfma_f32_16x16x32_bf16 v[16:19], v[168:171], a[4:7], 0\n"
"R 162,184,28672\nv_fma_f32 v232, v232, v216, v216\nv_fma_f32 v233, v233, v216, v216\nM 16,172,20\nbuffer_store_dwordx4 v[32:35], v252, s[20:23], s84 offen offset:0 sc1\nv_rcp_f32 v230, v230\n"
"v_mfma_f32_16x16x32_bf16 v[8:11], v[168:171], a[8:11], 0\nR 164,186,24576\nv_mul_f32 v230, v40, v230\nv_rcp_f32 v231, v231\nM 8,172,24\nv_rcp_f32 v232, v232\n"
"v_rcp_f32 v233, v233\nv_mfma_f32_16x16x32_bf16 v[24:27], v[168:171], a[12:15], 0\nR 166,186,28672\nv_mul_f32 v231, v41, v231\nv_mul_f32 v232, v42, v232\nM 24,172,28\n"
"G 4\nv_mul_f32 v233, v43, v233\nv_cvt_pk_bf16_f32 v40, v230, v231\nv_cvt_pk_bf16_f32 v41, v232, v233\nv_mfma_f32_16x16x32_bf16 v[28:31], v[176:179], a[12:15], 0\nR 168,185,24576\n"
"v_mul_f32 v234, v44, v44\nv_mul_f32 v235, v45, v45\nM 28,180,28\nv_mul_f32 v236, v46, v46\nv_mul_f32 v237, v47, v47\nv_fma_f32 v234, v234, v215, v214\n"
"v_mfma_f32_16x16x32_bf16 v[12:15], v[176:179], a[8:11], 0\nR 170,185,28672\nv_fma_f32 v235, v235, v215, v214\nv_fma_f32 v236, v236, v215, v214\nM 12,180,24\nds_write_addtid_b32 v128 offset:0\n"
"v_fma_f32 v237, v237, v215, v214\nv_mul_f32 v234, v44, v234\nv_mul_f32 v235, v45, v235\nv_mfma_f32_16x16x32_bf16 v[20:23], v[176:179], a[4:7], 0\nR 172,187,24576\nds_write_addtid_b32 v129 offset:256\n"
"v_mul_f32 v236, v46, v236\nv_mul_f32 v237, v47, v237\nM 20,180,20\nds_write_addtid_b32 v130 offset:512\nv_exp_f32 v234, v234\nv_exp_f32 v235, v235\n"
"v_mfma_f32_16x16x32_bf16 v[4:7], v[176:179], a[0:3], 0\nR 174,187,28672\nds_write_addtid_b32 v131 offset:768\nv_fma_f32 v234, v234, v216, v216\nv_exp_f32 v236, v236\nM 4,180,16\n"
"ds_write_addtid_b32 v132 offset:1024\nG 9\nv_exp_f32 v237, v237\nv_fma_f32 v235, v235, v216, v216\nM 0,160,32\nR 176,184,32768\n"
"ds_write_addtid_b32 v133 offset:1280\nv_fma_f32 v236, v236, v216, v216\nv_fma_f32 v237, v237, v216, v216\nM 0,164,48\nds_write_addtid_b32 v134 offset:1536\nv_rcp_f32 v234, v234\n"
"v_rcp_f32 v235, v235\nM 16,160,36\nR 178,184,36864\nds_write_addtid_b32 v135 offset:1792\nv_mul_f32 v234, v44, v234\nv_rcp_f32 v236, v236\n"
"M 16,164,52\nds_write_addtid_b32 v136 offset:2048\nv_rcp_f32 v237, v237\nv_mul_f32 v235, v45, v235\nM 8,160,40\nR 180,186,32768\n"
"ds_write_addtid_b32 v137 offset:2304\nv_mul_f32 v236, v46, v236\nv_mul_f32 v237, v47, v237\nM 8,164,56\nds_write_addtid_b32 v138 offset:2560\nv_cvt_pk_bf16_f32 v42, v234, v235\n"
"v_cvt_pk_bf16_f32 v43, v236, v237\nv_mul_f32 v230, v48, v48\nM 24,160,44\nR 182,186,36864\nds_write_addtid_b32 v139 offset:2816\nv_mul_f32 v231, v49, v49\n"
"v_mul_f32 v232, v50, v50\nM 24,164,60\nds_write_addtid_b32 v140 offset:3072\nG 14\nv_mul_f32 v233, v51, v51\nv_fma_f32 v230, v230, v218, v217\n"
"v_fma_f32 v231, v231, v218, v217\nM 28,168,44\nR 160,185,32768\nds_write_addtid_b32 v141 offset:3328\nv_fma_f32 v232, v232, v218, v217\nv_fma_f32 v233, v233, v218, v217\n"
"M 28,172,60\nbuffer_store_dwordx4 v[40:43], v252, s[20:23], s84 offen offset:1024 sc1\nds_write_addtid_b32 v142 offset:3584\nv_mul_f32 v230, v48, v230\nM 12,168,40\nR 162,185,36864\n"
"ds_write_addtid_b32 v143 offset:3840\nv_mul_f32 v231, v49, v231\nv_mul_f32 v232, v50, v232\nM 12,172,56\nds_write_addtid_b32 v144 offset:4096\nv_exp_f32 v230, v230\n"
"v_mul_f32 v233, v51, v233\nM 20,168,36\nR 164,187,32768\nds_write_addtid_b32 v145 offset:4352\nv_fma_f32 v230, v230, v219, v219\nv_exp_f32 v231, v231\n"
"M 20,172,52\nds_write_addtid_b32 v146 offset:4608\nv_exp_f32 v232, v232\nv_exp_f32 v233, v233\nM 4,168,32\nR 166,187,36864\n"
"ds_write_addtid_b32 v147 offset:4864\nv_fma_f32 v231, v231, v219, v219\nv_fma_f32 v232, v232, v219, v219\nM 4,172,48\nds_write_addtid_b32 v148 offset:5120\nds_write_addtid_b32 v149 offset:5376\n"
"ds_write_addtid_b32 v150 offset:5632\nds_write_addtid_b32 v151 offset:5888\nds_write_addtid_b32 v152 offset:6144\nds_write_addtid_b32 v153 offset:6400\nds_write_addtid_b32 v154 offset:6656\nds_write_addtid_b32 v155 offset:6912\n"
"ds_write_addtid_b32 v156 offset:7168\nds_write_addtid_b32 v157 offset:7424\nds_write_addtid_b32 v158 offset:7680\nds_write_addtid_b32 v159 offset:7936\nG 15\nv_rcp_f32 v230, v230\n"
"v_fma_f32 v233, v233, v219, v219\nM 0,176,64\nR 168,184,40960\ns_add_u32 m0, s50, 81920\nv_rcp_f32 v231, v231\nM 0,180,80\n"
"buffer_load_dwordx4 v221, s[12:15], s63 offen offset:1024 lds\nv_rcp_f32 v232, v232\nM 16,176,68\nR 170,184,45056\ns_waitcnt vmcnt(6) lgkmcnt(2)\ns_barrier\n"
"s_add_u32 m0, s50, 90112\ns_add_u32 s63, s54, 229632\nv_rcp_f32 v233, v233\nM 16,180,84\nbuffer_load_dwordx4 v220, s[12:15], s63 offen lds\nds_read2st64_b32 v[128:129], v225 offset0:0 offset1:1\n"
"v_mul_f32 v230, v48, v230\nM 8,176,72\nR 172,186,40960\nds_read2st64_b32 v[130:131], v225 offset0:2 offset1:3\nv_mul_f32 v231, v49, v231\nv_mul_f32 v232, v50, v232\n"
"M 8,180,88\nds_read2st64_b32 v[132:133], v225 offset0:4 offset1:5\nv_mul_f32 v233, v51, v233\nv_cvt_pk_bf16_f32 v48, v230, v231\nv_cvt_pk_bf16_f32 v49, v232, v233\nM 24,176,76\n"
"R 174,186,45056\nds_read2st64_b32 v[134:135], v225 offset0:6 offset1:7\nv_mul_f32 v234, v52, v52\nv_mul_f32 v235, v53, v53\nM 24,180,92\nds_read2st64_b32 v[136:137], v225 offset0:8 offset1:9\n"
"v_mul_f32 v236, v54, v54\nv_mul_f32 v237, v55, v55\nv_fma_f32 v234, v234, v218, v217\nM 28,160,76\nR 176,185,40960\nds_read2st64_b32 v[138:139], v225 offset0:10 offset1:11\n"
"v_fma_f32 v235, v235, v218, v217\nv_fma_f32 v236, v236, v218, v217\nM 28,164,92\nbuffer_load_dwordx4 v221, s[12:15], s63 offen offset:1024 lds\nG 8\nv_add_f32 v96, v96, v128\n"
"v_fma_f32 v237, v237, v218, v217\nM 12,160,72\nR 178,185,45056\ns_add_u32 s63, s54, 459008\nv_add_f32 v97, v97, v129\nv_mul_f32 v234, v52, v234\n"
"M 12,164,88\ns_add_u32 m0, s50, 98304\nv_exp_f32 v234, v234\nG 7\nv_add_f32 v98, v98, v130\nv_mul_f32 v235, v53, v235\n"
"M 20,160,68\nR 180,187,40960\nv_mul_f32 v236, v54, v236\nv_add_f32 v99, v99, v131\nv_mul_f32 v237, v55, v237\nM 20,164,84\n"
"buffer_load_dwordx4 v220, s[12:15], s63 offen lds\nG 7\nv_add_f32 v100, v100, v132\nv_exp_f32 v235, v235\nM 4,160,64\nR 182,187,45056\n"
"v_fma_f32 v234, v234, v219, v219\nv_add_f32 v101, v101, v133\nv_exp_f32 v236, v236\nM 4,164,80\nG 7\nv_exp_f32 v237, v237\n"
"G 6\nv_add_f32 v102, v102, v134\nv_fma_f32 v235, v235, v219, v219\nM 0,168,96\nR 160,184,49152\nv_fma_f32 v236, v236, v219, v219\n"
"v_add_f32 v103, v103, v135\nv_fma_f32 v237, v237, v219, v219\nM 0,172,112\nbuffer_load_dwordx4 v221, s[12:15], s63 offen offset:1024 lds\nG 6\nv_add_f32 v104, v104, v136\n"
"v_rcp_f32 v234, v234\nM 16,168,100\nR 162,184,53248\ns_add_u32 s63, s54, 688384\nv_add_f32 v105, v105, v137\nv_rcp_f32 v235, v235\n"
"M 16,172,116\ns_add_u32 m0, s50, 106496\nv_rcp_f32 v236, v236\nG 5\nv_add_f32 v106, v106, v138\nv_rcp_f32 v237, v237\n"
"M 8,168,104\nR 164,186,49152\nv_mul_f32 v234, v52, v234\nv_add_f32 v107, v107, v139\nv_mul_f32 v235, v53, v235\nM 8,172,120\n"
"buffer_load_dwordx4 v220, s[12:15], s63 offen lds\nds_read2st64_b32 v[140:141], v225 offset0:12 offset1:13\nv_mul_f32 v236, v54, v236\nM 24,168,108\nR 166,186,53248\nds_read2st64_b32 v[142:143], v225 offset0:14 offset1:15\n"
"v_mul_f32 v237, v55, v237\nv_cvt_pk_bf16_f32 v50, v234, v235\nM 24,172,124\nds_read2st64_b32 v[144:145], v225 offset0:16 offset1:17\nG 7\nv_cvt_pk_bf16_f32 v51, v236, v237\n"
"v_mul_f32 v230, v56, v56\nv_mul_f32 v231, v57, v57\nM 28,176,108\nR 168,185,49152\nds_read2st64_b32 v[146:147], v225 offset0:18 offset1:19\nv_mul_f32 v232, v58, v58\n"
"v_mul_f32 v233, v59, v59\nM 28,180,124\nds_read2st64_b32 v[148:149], v225 offset0:20 offset1:21\nv_fma_f32 v230, v230, v218, v217\nv_fma_f32 v231, v231, v218, v217\nv_fma_f32 v232, v232, v218, v217\n"
"M 12,176,104\nR 170,185,53248\nds_read2st64_b32 v[150:151], v225 offset0:22 offset1:23\nv_fma_f32 v233, v233, v218, v217\nv_mul_f32 v230, v56, v230\nM 12,180,120\n"
"buffer_load_dwordx4 v221, s[12:15], s63 offen offset:1024 lds\nG 8\nv_add_f32 v108, v108, v140\nv_mul_f32 v231, v57, v231\nM 20,176,100\nR 172,187,49152\n"
"s_add_u32 s63, s54, 917760\nv_add_f32 v109, v109, v141\nv_mul_f32 v232, v58, v232\nM 20,180,116\ns_add_u32 m0, s50, 114688\nv_exp_f32 v230, v230\n"
"G 7\nv_add_f32 v110, v110, v142\nv_mul_f32 v233, v59, v233\nM 4,176,96\nR 174,187,53248\nv_fma_f32 v230, v230, v219, v219\n"
"v_add_f32 v111, v111, v143\nv_exp_f32 v231, v231\nM 4,180,112\nv_exp_f32 v232, v232\nG 7\nv_add_f32 v112, v112, v144\n"
"v_exp_f32 v233, v233\nM 0,160,128\nR 176,184,57344\nv_fma_f32 v231, v231, v219, v219\nv_add_f32 v113, v113, v145\nv_fma_f32 v232, v232, v219, v219\n"
"M 0,164,144\nbuffer_load_dwordx4 v220, s[12:15], s63 offen lds\nG 6\nv_add_f32 v114, v114, v146\nv_fma_f32 v233, v233, v219, v219\nM 16,160,132\n"
"R 178,184,61440\nv_mul_f32 v234, v60, v60\nv_add_f32 v115, v115, v147\nv_rcp_f32 v230, v230\nM 16,164,148\nds_read2st64_b32 v[152:153], v225 offset0:24 offset1:25\n"
"v_rcp_f32 v231, v231\nv_rcp_f32 v232, v232\nM 8,160,136\nR 180,186,57344\nLq1027:\ns_lshr_b32 s60, s45, 3\n"
"s_lshl2_add_u32 s60, s60, s40\ns_lshl_b32 s60, s60, 8\nv_lshl_add_u32 v224, v252, 3, s60\nv_add_u32 v224, Lq1027+4096-LqPC, v224\ns_mov_b64 exec, 3\nglobal_load_dword v224, v224, s[98:99]\n"
"s_mov_b64 exec, -1\nG 8\nv_add_f32 v116, v116, v148\nv_rcp_f32 v233, v233\nM 8,164,152\nds_read2st64_b32 v[154:155], v225 offset0:26 offset1:27\n"
"v_mul_f32 v230, v56, v230\nv_mul_f32 v231, v57, v231\nv_mul_f32 v232, v58, v232\nM 24,160,140\nR 182,186,61440\nv_mul_f32 v233, v59, v233\n"
"v_add_f32 v117, v117, v149\nv_cvt_pk_bf16_f32 v56, v230, v231\nM 24,164,156\nds_read2st64_b32 v[156:157], v225 offset0:28 offset1:29\nds_read2st64_b32 v[158:159], v225 offset0:30 offset1:31\nG 8\n"
"v_cvt_pk_bf16_f32 v57, v232, v233\nv_mul_f32 v235, v61, v61\nv_add_f32 v118, v118, v150\nv_add_f32 v119, v119, v151\nG 5\nv_add_f32 v120, v120, v152\n"
"v_add_f32 v121, v121, v153\nG 3\nv_add_f32 v122, v122, v154\nv_add_f32 v123, v123, v155\nG 1\nv_add_f32 v124, v124, v156\n"
"v_add_f32 v125, v125, v157\nG 0\nv_add_f32 v126, v126, v158\nv_add_f32 v127, v127, v159\nv_mul_f32 v236, v62, v62\nM 28,168,140\n"
"R 160,185,57344\nv_mul_f32 v237, v63, v63\nv_fma_f32 v234, v234, v218, v217\nM 28,172,156\nbuffer_load_dwordx4 v221, s[12:15], s63 offen offset:1024 lds\nv_fma_f32 v235, v235, v218, v217\n"
"M 12,168,136\nR 162,185,61440\ns_add_u32 m0, s50, 0\ns_add_u32 s63, s54, 1147136\nv_fma_f32 v236, v236, v218, v217\nM 12,172,152\n"
"buffer_load_dwordx4 v220, s[12:15], s63 offen lds\nv_fma_f32 v237, v237, v218, v217\nM 20,168,132\nR 164,187,57344\nv_mul_f32 v234, v60, v234\nv_mul_f32 v235, v61, v235\n"
"M 20,172,148\nv_exp_f32 v234, v234\nv_mul_f32 v236, v62, v236\nM 4,168,128\nR 166,187,61440\nv_mul_f32 v237, v63, v237\n"
"v_exp_f32 v235, v235\nM 4,172,144\nv_exp_f32 v236, v236\nv_exp_f32 v237, v237\nM 0,176,160\nR 168,188,0\n"
"v_fma_f32 v234, v234, v219, v219\nv_fma_f32 v235, v235, v219, v219\nM 0,180,176\nbuffer_load_dwordx4 v221, s[12:15], s63 offen offset:1024 lds\nv_fma_f32 v236, v236, v219, v219\nM 16,176,164\n"
"R 170,188,4096\ns_add_u32 m0, s50, 8192\ns_add_u32 s63, s54, 1376512\nv_fma_f32 v237, v237, v219, v219\nM 16,180,180\nbuffer_load_dwordx4 v220, s[12:15], s63 offen lds\n"
"v_rcp_f32 v234, v234\nM 8,176,168\nR 172,190,0\nv_mul_f32 v234, v60, v234\nv_rcp_f32 v235, v235\nM 8,180,184\n"
"buffer_store_dwordx4 v[48:51], v252, s[20:23], s85 offen offset:0 sc1\nv_rcp_f32 v236, v236\nM 24,176,172\nR 174,190,4096\nv_mul_f32 v235, v61, v235\nv_rcp_f32 v237, v237\n"
"M 24,180,188\nv_mul_f32 v236, v62, v236\nv_mul_f32 v237, v63, v237\nv_cvt_pk_bf16_f32 v58, v234, v235\nv_cvt_pk_bf16_f32 v59, v236, v237\nbuffer_store_dwordx4 v[56:59], v252, s[20:23], s85 offen offset:1024 sc1\n"
"G 4\nM 28,160,172\nR 176,189,0\ns_waitcnt vmcnt(12)\ns_barrier\nv_mul_f32 v230, v96, v96\n"
"M 28,164,188\nbuffer_load_dwordx4 v221, s[12:15], s63 offen offset:1024 lds\nv_mul_f32 v231, v97, v97\nM 12,160,168\nR 178,189,4096\ns_add_u32 m0, s50, 16384\n"
"s_add_u32 s63, s54, 1605888\nv_mul_f32 v232, v98, v98\nM 12,164,184\nbuffer_load_dwordx4 v220, s[12:15], s63 offen lds\nv_mul_f32 v233, v99, v99\nM 20,160,164\n"
"R 180,191,0\nv_fma_f32 v230, v230, v215, v214\nv_fma_f32 v231, v231, v215, v214\nM 20,164,180\ns_add_u32 s84, s73, 37750784\ns_add_u32 s85, s73, 37865472\n"
"v_fma_f32 v232, v232, v215, v214\nv_fma_f32 v233, v233, v215, v214\nM 4,160,160\nR 182,191,4096\ns_mov_b32 s60, 158720\nv_bfe_u32 v242, v252, 4, 4\n"
"v_lshl_add_u32 v242, v242, 3, s60\nv_mul_f32 v230, v96, v230\nM 4,164,176\nds_read_b64 v[238:239], v242 offset:0\nG 5\nv_mul_f32 v231, v97, v231\n"
"M 0,168,192\nR 160,188,8192\nds_read_b64 v[240:241], v242 offset:128\nv_mul_f32 v232, v98, v232\nM 0,172,208\nds_read_b64 v[242:243], v242 offset:0\n"
"v_mul_f32 v233, v99, v233\nv_exp_f32 v230, v230\nM 16,168,196\nR 162,188,12288\nv_fma_f32 v230, v230, v216, v216\nv_exp_f32 v231, v231\n"
"M 16,172,212\nv_exp_f32 v232, v232\nv_exp_f32 v233, v233\nM 8,168,200\nR 164,190,8192\nv_fma_f32 v231, v231, v216, v216\n"
"v_fma_f32 v232, v232, v216, v216\nM 8,172,216\nv_rcp_f32 v230, v230\nv_fma_f32 v233, v233, v216, v216\nM 24,168,204\nR 166,190,12288\n"
"v_mul_f32 v230, v96, v230\nv_rcp_f32 v231, v231\nM 24,172,220\nG 7\nv_rcp_f32 v232, v232\nv_rcp_f32 v233, v233\n"
"M 28,176,204\nR 168,189,8192\nv_mul_f32 v231, v97, v231\nv_mul_f32 v232, v98, v232\nM 28,180,220\nv_mul_f32 v233, v99, v233\n"
"v_cvt_pk_bf16_f32 v96, v230, v231\nv_cvt_pk_bf16_f32 v97, v232, v233\nM 12,176,200\nR 170,189,12288\nv_mul_f32 v234, v100, v100\nv_mul_f32 v235, v101, v101\n"
"M 12,180,216\nv_mul_f32 v236, v102, v102\nv_mul_f32 v237, v103, v103\nv_fma_f32 v234, v234, v215, v214\nM 20,176,196\nR 172,191,8192\n"
"G 7\nv_mfma_f32_16x16x32_bf16 v[0:3], v[238:241], v[208:211], v[0:3]\nv_fma_f32 v235, v235, v215, v214\nv_fma_f32 v236, v236, v215, v214\nM 20,180,212\nv_fma_f32 v237, v237, v215, v214\n"
"v_mul_f32 v234, v100, v234\nv_mul_f32 v235, v101, v235\nM 4,176,192\nR 174,191,12288\nG 7\nv_mfma_f32_16x16x32_bf16 v[4:7], v[240:243], v[208:211], v[4:7]\n"
"v_mul_f32 v236, v102, v236\nv_mul_f32 v237, v103, v237\nM 4,180,208\nG 4\nv_exp_f32 v234, v234\nv_exp_f32 v235, v235\n"
"M 0,160,224\nR 176,188,16384\nv_mfma_f32_16x16x32_bf16 v[16:19], v[240:243], v[210:213], v[16:19]\nv_fma_f32 v234, v234, v216, v216\nv_exp_f32 v236, v236\nM 0,164,240\n"
"v_exp_f32 v237, v237\nv_fma_f32 v235, v235, v216, v216\nM 16,160,228\nR 178,188,20480\nv_mfma_f32_16x16x32_bf16 v[20:23], v[238:241], v[210:213], v[20:23]\nv_fma_f32 v236, v236, v216, v216\n"
"v_fma_f32 v237, v237, v216, v216\nM 16,164,244\nv_rcp_f32 v234, v234\nv_rcp_f32 v235, v235\nM 8,160,232\nR 180,188,24576\n"
"v_mul_f32 v234, v100, v234\nv_rcp_f32 v236, v236\nM 8,164,248\nv_rcp_f32 v237, v237\nv_mul_f32 v235, v101, v235\nM 24,160,236\n"
"R 182,188,28672\nv_mul_f32 v236, v102, v236\nv_mul_f32 v237, v103, v237\nM 24,164,252\nG 4\nv_cvt_pk_bf16_f32 v98, v234, v235\n"
"v_cvt_pk_bf16_f32 v99, v236, v237\nv_mul_f32 v230, v104, v104\nM 28,168,236\nR 160,189,16384\nv_mul_f32 v231, v105, v105\nv_mul_f32 v232, v106, v106\n"
"M 28,172,252\nv_mul_f32 v233, v107, v107\nv_fma_f32 v230, v230, v215, v214\nv_fma_f32 v231, v231, v215, v214\nM 12,168,232\nR 162,189,20480\n"
"v_fma_f32 v232, v232, v215, v214\nv_fma_f32 v233, v233, v215, v214\nM 12,172,248\nv_mul_f32 v230, v104, v230\nv_mul_f32 v231, v105, v231\nv_mul_f32 v232, v106, v232\n"
"M 20,168,228\nR 164,189,24576\nv_mul_f32 v233, v107, v233\nv_exp_f32 v230, v230\nM 20,172,244\nv_exp_f32 v231, v231\n"
"v_exp_f32 v232, v232\nM 4,168,224\nR 166,189,28672\nv_fma_f32 v230, v230, v216, v216\nv_exp_f32 v233, v233\nM 4,172,240\n"
"s_waitcnt vmcnt(12)\ns_barrier\nG 4\nv_fma_f32 v231, v231, v216, v216\nv_mfma_f32_16x16x32_bf16 v[32:35], v[176:179], a[0:3], 0\nR 168,190,16384\n"
"v_fma_f32 v232, v232, v216, v216\nv_fma_f32 v233, v233, v216, v216\nM 32,180,16\ns_mov_b32 m0, s51\nv_rcp_f32 v230, v230\nv_rcp_f32 v231, v231\n"
"v_mfma_f32_16x16x32_bf16 v[48:51], v[176:179], a[4:7], 0\nR 170,190,20480\nv_mul_f32 v230, v104, v230\nv_rcp_f32 v232, v232\nM 48,180,20\nv_rcp_f32 v233, v233\n"
"v_mul_f32 v231, v105, v231\nv_mfma_f32_16x16x32_bf16 v[64:67], v[176:179], a[8:11], 0\nR 172,190,24576\nv_mov_b32 v226, s46\nv_mul_f32 v232, v106, v232\nM 64,180,24\n"
"ds_write_addtid_b32 v8 offset:0\nv_mov_b32 v227, s47\nv_mul_f32 v233, v107, v233\nv_cvt_pk_bf16_f32 v104, v230, v231\nv_mfma_f32_16x16x32_bf16 v[80:83], v[176:179], a[12:15], 0\nR 174,190,28672\n"
"s_add_u32 s61, s81, 845824\ns_lshl_b32 s62, s44, 5\ns_add_u32 s61, s61, s62\ns_waitcnt vmcnt(0)\ns_mov_b64 exec, 1\nbuffer_atomic_swap_x2 v[226:227], off, s[20:23], s61 sc1\n"
"s_mov_b64 exec, -1\nv_cvt_pk_bf16_f32 v105, v232, v233\nM 80,180,28\nds_write_addtid_b32 v9 offset:256\nG 6\nv_mul_f32 v234, v108, v108\n"
"v_mul_f32 v235, v109, v109\nv_mul_f32 v236, v110, v110\nv_mfma_f32_16x16x32_bf16 v[84:87], v[160:163], a[12:15], 0\nR 176,191,16384\nds_write_addtid_b32 v10 offset:512\nv_mul_f32 v237, v111, v111\n"
"v_fma_f32 v234, v234, v215, v214\nM 84,164,28\nds_write_addtid_b32 v11 offset:768\nv_fma_f32 v235, v235, v215, v214\nv_fma_f32 v236, v236, v215, v214\nv_fma_f32 v237, v237, v215, v214\n"
"v_mfma_f32_16x16x32_bf16 v[68:71], v[160:163], a[8:11], 0\nR 178,191,20480\nds_write_addtid_b32 v12 offset:1024\nv_mul_f32 v234, v108, v234\nv_mul_f32 v235, v109, v235\nM 68,164,24\n"
"buffer_store_dwordx4 v[96:99], v252, s[20:23], s84 offen offset:0 sc1\nds_write_addtid_b32 v13 offset:1280\nv_mul_f32 v236, v110, v236\nv_mfma_f32_16x16x32_bf16 v[52:55], v[160:163], a[4:7], 0\nR 180,191,24576\nds_write_addtid_b32 v14 offset:1536\n"
"v_mul_f32 v237, v111, v237\nv_exp_f32 v234, v234\nM 52,164,20\nds_write_addtid_b32 v15 offset:1792\nv_exp_f32 v235, v235\nv_exp_f32 v236, v236\n"
"v_mfma_f32_16x16x32_bf16 v[36:39], v[160:163], a[0:3], 0\nR 182,191,28672\nds_write_addtid_b32 v24 offset:4096\nv_fma_f32 v234, v234, v216, v216\nv_exp_f32 v237, v237\nM 36,164,16\n"
"ds_write_addtid_b32 v25 offset:4352\nG 13\nv_rcp_f32 v234, v234\nv_fma_f32 v235, v235, v216, v216\nv_mfma_f32_16x16x32_bf16 v[40:43], v[168:171], a[0:3], 0\nR 160,188,32768\n"
"ds_write_addtid_b32 v26 offset:4608\nds_write_addtid_b32 v27 offset:4864\nds_write_addtid_b32 v28 offset:5120\nds_write_addtid_b32 v29 offset:5376\nds_write_addtid_b32 v30 offset:5632\nds_write_addtid_b32 v31 offset:5888\n"
"v_fma_f32 v236, v236, v216, v216\nv_fma_f32 v237, v237, v216, v216\nM 40,172,16\ns_add_u32 m0, s50, 16384\ns_nop 0\nbuffer_load_dwordx4 v221, s[12:15], s63 offen offset:1024 lds\n"
"v_rcp_f32 v235, v235\nv_mfma_f32_16x16x32_bf16 v[56:59], v[168:171], a[4:7], 0\nR 162,188,36864\ns_add_u32 m0, s50, 24576\nv_rcp_f32 v236, v236\nM 56,172,20\n"
"G 1\ns_barrier\ns_add_u32 s63, s54, 1835264\nv_rcp_f32 v237, v237\nv_mfma_f32_16x16x32_bf16 v[72:75], v[168:171], a[8:11], 0\nR 164,188,40960\n"
"ds_read2st64_b32 v[8:9], v225 offset0:0 offset1:1\nv_mov_b32 v248, 0\nv_mul_f32 v234, v108, v234\nM 72,172,24\nbuffer_load_dwordx4 v220, s[12:15], s63 offen lds\nds_read2st64_b32 v[10:11], v225 offset0:2 offset1:3\n"
"v_mov_b32 v249, 0\nv_mul_f32 v235, v109, v235\nv_mfma_f32_16x16x32_bf16 v[88:91], v[168:171], a[12:15], 0\nR 166,188,45056\ns_add_u32 s61, s82, 845824\ns_mov_b64 exec, 0xf\n"
"buffer_atomic_or_x2 v[248:249], v250, s[20:23], s61 offen sc0 sc1\ns_mov_b64 exec, -1\nv_mul_f32 v236, v110, v236\nM 88,172,28\nds_read2st64_b32 v[12:13], v225 offset0:4 offset1:5\nv_mul_f32 v237, v111, v237\n"
"v_cvt_pk_bf16_f32 v106, v234, v235\nv_cvt_pk_bf16_f32 v107, v236, v237\nv_mfma_f32_16x16x32_bf16 v[92:95], v[176:179], a[12:15], 0\nR 168,189,32768\nds_read2st64_b32 v[14:15], v225 offset0:6 offset1:7\nv_mul_f32 v230, v112, v112\n"
"v_mul_f32 v231, v113, v113\nM 92,180,28\nbuffer_load_dwordx4 v221, s[12:15], s63 offen offset:1024 lds\nds_read2st64_b32 v[24:25], v225 offset0:16 offset1:17\nv_mul_f32 v232, v114, v114\nv_mfma_f32_16x16x32_bf16 v[76:79], v[176:179], a[8:11], 0\n"
"R 170,189,36864\ns_add_u32 m0, s50, 32768\ns_add_u32 s63, s54, 2064640\nv_mul_f32 v233, v115, v115\nM 76,180,24\nbuffer_load_dwordx4 v220, s[12:15], s63 offen lds\n"
"v_fma_f32 v230, v230, v218, v217\nv_mfma_f32_16x16x32_bf16 v[60:63], v[176:179], a[4:7], 0\nR 172,189,40960\nv_fma_f32 v231, v231, v218, v217\nG 8\nv_add_f32 v0, v0, v8\n"
"v_fma_f32 v232, v232, v218, v217\nM 60,180,20\nds_read2st64_b32 v[26:27], v225 offset0:18 offset1:19\nv_fma_f32 v233, v233, v218, v217\nv_mul_f32 v230, v112, v230\nv_mul_f32 v231, v113, v231\n"
"v_mfma_f32_16x16x32_bf16 v[44:47], v[176:179], a[0:3], 0\nR 174,189,45056\nv_mul_f32 v232, v114, v232\nv_add_f32 v1, v1, v9\nv_mul_f32 v233, v115, v233\nM 44,180,16\n"
"G 8\nv_exp_f32 v230, v230\nv_add_f32 v2, v2, v10\nv_exp_f32 v231, v231\nM 32,160,32\nR 176,190,32768\n"
"v_fma_f32 v230, v230, v219, v219\nv_add_f32 v3, v3, v11\nv_exp_f32 v232, v232\nM 32,164,48\nbuffer_load_dwordx4 v221, s[12:15], s63 offen offset:1024 lds\nG 8\n"
"v_add_f32 v4, v4, v12\nv_exp_f32 v233, v233\nM 48,160,36\nR 178,190,36864\nv_fma_f32 v231, v231, v219, v219\nv_add_f32 v5, v5, v13\n"
"v_fma_f32 v232, v232, v219, v219\nM 48,164,52\nds_read2st64_b32 v[28:29], v225 offset0:20 offset1:21\nv_rcp_f32 v230, v230\nv_fma_f32 v233, v233, v219, v219\nM 64,160,40\n"
"R 180,190,40960\nv_mul_f32 v230, v112, v230\nG 9\nv_add_f32 v6, v6, v14\nv_rcp_f32 v231, v231\nM 64,164,56\n"
"s_add_u32 m0, s50, 40960\nv_rcp_f32 v232, v232\nv_add_f32 v7, v7, v15\nv_rcp_f32 v233, v233\nM 80,160,44\nR 182,190,45056\n"
"v_mul_f32 v231, v113, v231\nG 9\nv_add_f32 v16, v16, v24\nv_mul_f32 v232, v114, v232\nM 80,164,60\nG 5\n"
"v_mul_f32 v233, v115, v233\nv_cvt_pk_bf16_f32 v112, v230, v231\nv_add_f32 v17, v17, v25\nv_cvt_pk_bf16_f32 v113, v232, v233\nM 84,168,44\nR 160,191,32768\n"
"v_mul_f32 v234, v116, v116\nv_add_f32 v18, v18, v26\nv_mul_f32 v235, v117, v117\nM 84,172,60\ns_add_u32 s63, s54, 2294016\nds_read2st64_b32 v[30:31], v225 offset0:22 offset1:23\n"
"v_mul_f32 v236, v118, v118\nv_add_f32 v19, v19, v27\nG 4\nv_add_f32 v20, v20, v28\nv_add_f32 v21, v21, v29\nG 0\n"
"v_add_f32 v22, v22, v30\nv_add_f32 v23, v23, v31\nM 68,168,40\nR 162,191,36864\nv_mul_f32 v237, v119, v119\nM 68,172,56\n"
"buffer_load_dwordx4 v220, s[12:15], s63 offen lds\nM 52,168,36\nR 164,191,40960\nv_fma_f32 v234, v234, v218, v217\nM 52,172,52\nv_fma_f32 v235, v235, v218, v217\n"
"v_fma_f32 v236, v236, v218, v217\nM 36,168,32\nR 166,191,45056\nv_fma_f32 v237, v237, v218, v217\nM 36,172,48\nv_mul_f32 v234, v116, v234\n"
"v_mul_f32 v235, v117, v235\nM 40,176,32\nR 168,188,49152\nv_mul_f32 v236, v118, v236\nM 40,180,48\nbuffer_load_dwordx4 v221, s[12:15], s63 offen offset:1024 lds\n"
"M 56,176,36\nR 170,188,53248\nv_mul_f32 v237, v119, v237\nM 56,180,52\ns_add_u32 m0, s50, 49152\nv_exp_f32 v234, v234\n"
"M 72,176,40\nR 172,184,0\ns_add_u32 s63, s54, 2523392\nM 72,180,56\ns_add_u32 s64, s68, 37748736\nv_exp_f32 v235, v235\n"
"M 88,176,44\nR 174,184,4096\nv_fma_f32 v234, v234, v219, v219\nM 88,180,60\nG 4\nv_exp_f32 v236, v236\n"
"M 92,160,44\nR 176,189,49152\ns_waitcnt vmcnt(5)\nv_cmp_eq_u64 s[86:87], s[46:47], v[248:249]\nM 92,164,60\nbuffer_load_dwordx4 v220, s[12:15], s63 offen lds\n"
"M 76,160,40\nR 178,189,53248\ns_and_b32 s62, s86, 0xf\ns_cmp_eq_u32 s62, 0xf\ns_cbranch_scc0 Lq826\nLq827:\n"
"M 76,164,56\ns_add_u32 s65, s68, 37863424\nv_exp_f32 v237, v237\nM 60,160,36\nR 180,185,0\nv_fma_f32 v235, v235, v219, v219\n"
"M 60,164,52\nbuffer_load_dwordx4 a[0:3], v252, s[20:23], s64 offen sc1\nM 44,160,32\nR 182,185,4096\nv_fma_f32 v236, v236, v219, v219\nM 44,164,48\n"
"G 4\nv_rcp_f32 v234, v234\nM 32,168,64\nR 160,190,49152\nv_fma_f32 v237, v237, v219, v219\nM 32,172,80\n"
"s_add_u32 s66, s69, 37748736\nv_rcp_f32 v235, v235\nM 48,168,68\nR 162,190,53248\nv_mul_f32 v234, v116, v234\nM 48,172,84\n"
"buffer_load_dwordx4 a[4:7], v252, s[20:23], s65 offen sc1\nM 64,168,72\nR 164,186,0\nv_mul_f32 v235, v117, v235\nM 64,172,88\nv_rcp_f32 v236, v236\n"
"M 80,168,76\nR 166,186,4096\nv_mul_f32 v236, v118, v236\nM 80,172,92\nG 4\nv_rcp_f32 v237, v237\n"
"M 84,176,76\nR 168,191,49152\nv_mul_f32 v237, v119, v237\nM 84,180,92\nbuffer_load_dwordx4 a[8:11], v252, s[20:23], s66 offen sc1\nM 68,176,72\n"
"R 170,191,53248\nv_cvt_pk_bf16_f32 v114, v234, v235\nM 68,180,88\ns_add_u32 s67, s69, 37863424\nv_cvt_pk_bf16_f32 v115, v236, v237\nv_mul_f32 v230, v120, v120\n"
"M 52,176,68\nR 172,187,0\nv_mul_f32 v231, v121, v121\nM 52,180,84\ns_add_u32 s64, s68, 37755904\nbuffer_load_dwordx4 v221, s[12:15], s63 offen offset:1024 lds\n"
"M 36,176,64\nR 174,187,4096\ns_add_u32 m0, s50, 57344\nM 36,180,80\ns_add_u32 s63, s54, 2752768\nG 4\n"
"v_mul_f32 v232, v122, v122\nM 40,160,64\nR 176,184,8192\ns_waitcnt vmcnt(7)\ns_barrier\nM 40,164,80\n"
"buffer_load_dwordx4 a[12:15], v252, s[20:23], s67 offen sc1\nbuffer_load_dwordx4 v220, s[12:15], s63 offen lds\nM 56,160,68\nR 178,184,12288\nv_mul_f32 v233, v123, v123\nM 56,164,84\n"
"s_add_u32 s65, s68, 37870592\nv_fma_f32 v230, v230, v218, v217\nv_fma_f32 v231, v231, v218, v217\nM 72,160,72\nR 180,184,16384\nv_fma_f32 v232, v232, v218, v217\n"
"M 72,164,88\nbuffer_load_dwordx4 a[16:19], v252, s[20:23], s64 offen sc1\nbuffer_load_dwordx4 v221, s[12:15], s63 offen offset:1024 lds\nM 88,160,76\nR 182,184,20480\ns_add_u32 m0, s50, 65536\n"
"M 88,164,92\nG 4\nv_fma_f32 v233, v233, v218, v217\nv_mul_f32 v230, v120, v230\nM 92,168,76\nR 160,185,8192\n"
"s_add_u32 s63, s54, 2982144\nM 92,172,92\ns_add_u32 s66, s69, 37755904\nbuffer_load_dwordx4 v220, s[12:15], s63 offen lds\nM 76,168,72\nR 162,185,12288\n"
"v_mul_f32 v231, v121, v231\nM 76,172,88\nbuffer_load_dwordx4 a[20:23], v252, s[20:23], s65 offen sc1\nM 60,168,68\nR 164,185,16384\nv_mul_f32 v232, v122, v232\n"
"M 60,172,84\nbuffer_load_dwordx4 v221, s[12:15], s63 offen offset:1024 lds\nM 44,168,64\nR 166,185,20480\nv_mul_f32 v233, v123, v233\nM 44,172,80\n"
"G 4\nv_exp_f32 v230, v230\nM 32,176,96\nR 168,186,8192\nv_fma_f32 v230, v230, v219, v219\nM 32,180,112\n"
"buffer_load_dwordx4 a[24:27], v252, s[20:23], s66 offen sc1\nM 48,176,100\nR 170,186,12288\nv_mul_f32 v234, v124, v124\nM 48,180,116\ns_add_u32 s67, s69, 37870592\n"
"v_exp_f32 v231, v231\nM 64,176,104\nR 172,186,16384\nv_fma_f32 v231, v231, v219, v219\nM 64,180,120\nbuffer_store_dwordx4 v[104:107], v252, s[20:23], s84 offen offset:1024 sc1\n"
"M 80,176,108\nR 174,186,20480\nv_mul_f32 v235, v125, v125\nM 80,180,124\nG 4\nv_exp_f32 v232, v232\n"
"M 84,160,108\nR 176,187,8192\nv_fma_f32 v232, v232, v219, v219\nM 84,164,124\nbuffer_load_dwordx4 a[28:31], v252, s[20:23], s67 offen sc1\nM 68,160,104\n"
"R 178,187,12288\nv_mul_f32 v236, v126, v126\nM 68,164,120\ns_add_u32 s64, s68, 37749760\nv_exp_f32 v233, v233\nM 52,160,100\n"
"R 180,187,16384\nv_fma_f32 v233, v233, v219, v219\nM 52,164,116\ns_add_u32 s65, s68, 37864448\nv_rcp_f32 v230, v230\nM 36,160,96\n"
"R 182,187,20480\nv_mul_f32 v230, v120, v230\nM 36,164,112\nG 4\nv_rcp_f32 v231, v231\nM 40,168,96\n"
"R 160,184,24576\nv_mul_f32 v231, v121, v231\nM 40,172,112\nbuffer_load_dwordx4 a[32:35], v252, s[20:23], s64 offen sc1\nM 56,168,100\nR 162,184,28672\n"
"v_cvt_pk_bf16_f32 v120, v230, v231\nM 56,172,116\ns_add_u32 s66, s69, 37749760\nv_rcp_f32 v232, v232\nM 72,168,104\nR 164,184,32768\n"
"v_mul_f32 v232, v122, v232\nM 72,172,120\nbuffer_load_dwordx4 a[36:39], v252, s[20:23], s65 offen sc1\nM 88,168,108\nR 166,184,36864\nv_mul_f32 v237, v127, v127\n"
"M 88,172,124\nG 4\nv_rcp_f32 v233, v233\nM 92,176,108\nR 168,185,24576\ns_add_u32 m0, s50, 73728\n"
"M 92,180,124\ns_add_u32 s67, s69, 37864448\nv_mul_f32 v233, v123, v233\nv_cvt_pk_bf16_f32 v121, v232, v233\nM 76,176,104\nR 170,185,28672\n"
"s_add_u32 s63, s54, 3211520\nM 76,180,120\nbuffer_load_dwordx4 a[40:43], v252, s[20:23], s66 offen sc1\nM 60,176,100\nR 172,185,32768\nv_fma_f32 v234, v234, v218, v217\n"
"M 60,180,116\nbuffer_load_dwordx4 v220, s[12:15], s63 offen lds\nM 44,176,96\nR 174,185,36864\nv_fma_f32 v235, v235, v218, v217\nM 44,180,112\n"
"G 4\nv_fma_f32 v236, v236, v218, v217\nv_fma_f32 v237, v237, v218, v217\nM 32,160,128\nR 176,186,24576\nv_mul_f32 v234, v124, v234\n"
"M 32,164,144\nbuffer_load_dwordx4 a[44:47], v252, s[20:23], s67 offen sc1\nbuffer_load_dwordx4 v221, s[12:15], s63 offen offset:1024 lds\nM 48,160,132\nR 178,186,28672\ns_add_u32 m0, s50, 81920\n"
"M 48,164,148\ns_add_u32 s64, s68, 37756928\nv_exp_f32 v234, v234\nM 64,160,136\nR 180,186,32768\ns_add_u32 s63, s54, 3440896\n"
"M 64,164,152\ns_add_u32 s65, s68, 37871616\nbuffer_load_dwordx4 v220, s[12:15], s63 offen lds\nM 80,160,140\nR 182,186,36864\nv_mul_f32 v235, v125, v235\n"
"M 80,164,156\nG 4\nv_exp_f32 v235, v235\nM 84,168,140\nR 160,187,24576\nv_mul_f32 v236, v126, v236\n"
"M 84,172,156\nbuffer_load_dwordx4 a[48:51], v252, s[20:23], s64 offen sc1\nbuffer_load_dwordx4 v221, s[12:15], s63 offen offset:1024 lds\nM 68,168,136\nR 162,187,28672\nv_mul_f32 v237, v127, v237\n"
"M 68,172,152\ns_add_u32 s66, s69, 37756928\nv_exp_f32 v236, v236\nM 52,168,132\nR 164,187,32768\ns_waitcnt vmcnt(12)\n"
"s_barrier\nM 52,172,148\nbuffer_load_dwordx4 a[52:55], v252, s[20:23], s65 offen sc1\ns_movk_i32 s63, 2048\nv_mad_u32_u24 v220, v228, s63, v229\nv_add_u32 v221, 916480, v220\n"
"s_add_u32 m0, s50, 90112\ns_add_u32 s63, s56, 0\nbuffer_load_dwordx4 v220, s[16:19], s63 offen lds\nM 36,168,128\nR 166,187,36864\nv_fma_f32 v234, v234, v219, v219\n"
"M 36,172,144\nG 4\nv_exp_f32 v237, v237\nM 40,176,128\nR 168,184,40960\nv_fma_f32 v235, v235, v219, v219\n"
"M 40,180,144\ns_add_u32 s67, s69, 37871616\nbuffer_load_dwordx4 v221, s[16:19], s63 offen offset:1024 lds\nM 56,176,132\nR 170,184,45056\ns_add_u32 m0, s50, 98304\n"
"M 56,180,148\nbuffer_load_dwordx4 a[56:59], v252, s[20:23], s66 offen sc1\nM 72,176,136\nR 172,184,49152\ns_add_u32 s63, s56, 524288\nM 72,180,152\n"
"buffer_load_dwordx4 v220, s[16:19], s63 offen lds\nM 88,176,140\nR 174,184,53248\nv_fma_f32 v236, v236, v219, v219\nM 88,180,156\nG 4\n"
"v_rcp_f32 v234, v234\nM 92,160,140\nR 176,185,40960\nv_fma_f32 v237, v237, v219, v219\nM 92,164,156\nbuffer_load_dwordx4 a[60:63], v252, s[20:23], s67 offen sc1\n"
"buffer_load_dwordx4 v221, s[16:19], s63 offen offset:1024 lds\nM 76,160,136\nR 178,185,45056\nv_mul_f32 v234, v124, v234\nM 76,164,152\ns_add_u32 m0, s50, 106496\n"
"v_rcp_f32 v235, v235\nM 60,160,132\nR 180,185,49152\nv_mul_f32 v235, v125, v235\nM 60,164,148\ns_add_u32 s63, s56, 65536\n"
"v_rcp_f32 v236, v236\nM 44,160,128\nR 182,185,53248\nv_mul_f32 v236, v126, v236\nM 44,164,144\nG 4\n"
"v_rcp_f32 v237, v237\nM 32,168,160\nR 160,186,40960\nv_mul_f32 v237, v127, v237\nM 32,172,176\nbuffer_load_dwordx4 v220, s[16:19], s63 offen lds\n"
"M 48,168,164\nR 162,186,45056\nv_cvt_pk_bf16_f32 v122, v234, v235\nM 48,172,180\nbuffer_store_dwordx4 v[112:115], v252, s[20:23], s85 offen offset:0 sc1\nM 64,168,168\n"
"R 164,186,49152\nv_cvt_pk_bf16_f32 v123, v236, v237\nM 64,172,184\nbuffer_load_dwordx4 v221, s[16:19], s63 offen offset:1024 lds\nM 80,168,172\nR 166,186,53248\n"
"s_add_u32 m0, s50, 114688\nM 80,172,188\nbuffer_store_dwordx4 v[120:123], v252, s[20:23], s85 offen offset:1024 sc1\nG 4\nM 84,176,172\nR 168,187,40960\n"
"s_add_u32 s63, s56, 589824\nM 84,180,188\nbuffer_load_dwordx4 v220, s[16:19], s63 offen lds\nM 68,176,168\nR 170,187,45056\nv_mul_f32 v230, v0, v0\n"
"v_mul_f32 v231, v1, v1\nM 68,180,184\ns_add_u32 s84, s73, 37754880\ns_add_u32 s85, s73, 37869568\nv_mul_f32 v232, v2, v2\nv_mul_f32 v233, v3, v3\n"
"M 52,176,164\nR 172,187,49152\nv_fma_f32 v230, v230, v215, v214\nv_fma_f32 v231, v231, v215, v214\nM 52,180,180\nbuffer_load_dwordx4 v221, s[16:19], s63 offen offset:1024 lds\n"
"v_fma_f32 v232, v232, v215, v214\nM 36,176,160\nR 174,187,53248\nv_fma_f32 v233, v233, v215, v214\nv_mul_f32 v230, v0, v230\nM 36,180,176\n"
"G 4\nv_exp_f32 v230, v230\nv_mul_f32 v231, v1, v231\nM 40,160,160\nR 176,184,57344\nv_mul_f32 v232, v2, v232\n"
"v_mul_f32 v233, v3, v233\nM 40,164,176\nv_exp_f32 v231, v231\nv_exp_f32 v232, v232\nM 56,160,164\nR 178,184,61440\n"
"v_fma_f32 v230, v230, v216, v216\nv_exp_f32 v233, v233\nM 56,164,180\nv_rcp_f32 v230, v230\nv_fma_f32 v231, v231, v216, v216\nM 72,160,168\n"
"R 180,188,0\nv_fma_f32 v232, v232, v216, v216\nv_fma_f32 v233, v233, v216, v216\nM 72,164,184\nv_rcp_f32 v231, v231\nv_rcp_f32 v232, v232\n"
"M 88,160,172\nR 182,188,4096\nv_mul_f32 v230, v0, v230\nv_rcp_f32 v233, v233\nM 88,164,188\nG 4\n"
"v_mul_f32 v231, v1, v231\nv_mul_f32 v232, v2, v232\nv_mul_f32 v233, v3, v233\nM 92,168,172\nR 160,185,57344\nv_cvt_pk_bf16_f32 v0, v230, v231\n"
"v_cvt_pk_bf16_f32 v1, v232, v233\nM 92,172,188\nv_mul_f32 v234, v4, v4\nv_mul_f32 v235, v5, v5\nv_mul_f32 v236, v6, v6\nM 76,168,168\n"
"R 162,185,61440\nv_mul_f32 v237, v7, v7\nv_fma_f32 v234, v234, v215, v214\nM 76,172,184\nv_fma_f32 v235, v235, v215, v214\nv_fma_f32 v236, v236, v215, v214\n"
"v_fma_f32 v237, v237, v215, v214\nM 60,168,164\nR 164,189,0\nv_mul_f32 v234, v4, v234\nv_mul_f32 v235, v5, v235\nM 60,172,180\n"
"v_exp_f32 v234, v234\nv_mul_f32 v236, v6, v236\nM 44,168,160\nR 166,189,4096\nv_mul_f32 v237, v7, v237\nv_exp_f32 v235, v235\n"
"M 44,172,176\nG 4\nv_exp_f32 v236, v236\nv_exp_f32 v237, v237\nM 32,176,192\nR 168,186,57344\n"
"v_fma_f32 v234, v234, v216, v216\nv_fma_f32 v235, v235, v216, v216\nM 32,180,208\nv_rcp_f32 v234, v234\nv_fma_f32 v236, v236, v216, v216\nM 48,176,196\n"
"R 170,186,61440\nv_fma_f32 v237, v237, v216, v216\nv_rcp_f32 v235, v235\nM 48,180,212\nv_rcp_f32 v236, v236\nv_rcp_f32 v237, v237\n"
"M 64,176,200\nR 172,190,0\nv_mul_f32 v234, v4, v234\nv_mul_f32 v235, v5, v235\nM 64,180,216\nv_mul_f32 v236, v6, v236\n"
"v_mul_f32 v237, v7, v237\nv_cvt_pk_bf16_f32 v2, v234, v235\nM 80,176,204\nR 174,190,4096\nv_cvt_pk_bf16_f32 v3, v236, v237\nv_mul_f32 v230, v16, v16\n"
"M 80,180,220\nG 4\nv_mul_f32 v231, v17, v17\nv_mul_f32 v232, v18, v18\nv_mul_f32 v233, v19, v19\nM 84,160,204\n"
"R 176,187,57344\ns_waitcnt vmcnt(6)\ns_barrier\nv_fma_f32 v230, v230, v218, v217\nM 84,164,220\nv_fma_f32 v231, v231, v218, v217\n"
"v_fma_f32 v232, v232, v218, v217\nv_fma_f32 v233, v233, v218, v217\nM 68,160,200\nR 178,187,61440\nv_mul_f32 v230, v16, v230\nv_mul_f32 v231, v17, v231\n"
"M 68,164,216\nv_exp_f32 v230, v230\nv_mul_f32 v232, v18, v232\nM 52,160,196\nR 180,191,0\nv_mul_f32 v233, v19, v233\n"
"v_exp_f32 v231, v231\nM 52,164,212\nv_exp_f32 v232, v232\nv_exp_f32 v233, v233\nM 36,160,192\nR 182,191,4096\n"
"v_fma_f32 v230, v230, v219, v219\nv_fma_f32 v231, v231, v219, v219\nM 36,164,208\nG 4\nv_rcp_f32 v230, v230\nv_fma_f32 v232, v232, v219, v219\n"
"M 40,168,192\nR 160,188,8192\nv_fma_f32 v233, v233, v219, v219\nv_rcp_f32 v231, v231\nM 40,172,208\nv_rcp_f32 v232, v232\n"
"v_rcp_f32 v233, v233\nM 56,168,196\nR 162,188,12288\nv_mul_f32 v230, v16, v230\nv_mul_f32 v231, v17, v231\nM 56,172,212\n"
"s_add_u32 m0, s50, 0\nv_mul_f32 v232, v18, v232\nv_mul_f32 v233, v19, v233\nv_cvt_pk_bf16_f32 v16, v230, v231\nM 72,168,200\nR 164,188,16384\n"
"v_cvt_pk_bf16_f32 v17, v232, v233\nv_mul_f32 v234, v20, v20\nM 72,172,216\ns_add_u32 s63, s57, 0\nv_mul_f32 v235, v21, v21\nv_mul_f32 v236, v22, v22\n"
"v_mul_f32 v237, v23, v23\nM 88,168,204\nR 166,188,20480\nv_mov_b32 v226, s46\nv_fma_f32 v234, v234, v218, v217\nM 88,172,220\n"
"G 4\nv_mov_b32 v227, s47\nv_fma_f32 v235, v235, v218, v217\nv_fma_f32 v236, v236, v218, v217\nM 92,176,204\nR 168,189,8192\n"
"s_add_u32 s61, s81, 845832\ns_lshl_b32 s62, s44, 5\ns_add_u32 s61, s61, s62\ns_waitcnt vmcnt(0)\ns_mov_b64 exec, 1\nbuffer_atomic_swap_x2 v[226:227], off, s[20:23], s61 sc1\n"
"s_mov_b64 exec, -1\nv_fma_f32 v237, v237, v218, v217\nM 92,180,220\nbuffer_load_dwordx4 v220, s[16:19], s63 offen lds\nv_mul_f32 v234, v20, v234\nM 76,176,200\n"
"R 170,189,12288\nv_mul_f32 v235, v21, v235\nv_mul_f32 v236, v22, v236\nM 76,180,216\nv_exp_f32 v234, v234\nv_mul_f32 v237, v23, v237\n"
"M 60,176,196\nR 172,189,16384\nv_fma_f32 v234, v234, v219, v219\nv_exp_f32 v235, v235\nM 60,180,212\nbuffer_load_dwordx4 v221, s[16:19], s63 offen offset:1024 lds\n"
"v_exp_f32 v236, v236\nM 44,176,192\nR 174,189,20480\ns_mov_b32 s60, 158208\nv_bfe_u32 v246, v252, 4, 4\nv_lshl_add_u32 v246, v246, 3, s60\n"
"v_exp_f32 v237, v237\nM 44,180,208\nds_read_b64 v[238:239], v246 offset:0\nG 5\nv_fma_f32 v235, v235, v219, v219\nM 32,160,224\n"
"R 176,190,8192\nds_read_b64 v[240:241], v246 offset:128\ns_add_u32 s63, s57, 524288\nv_fma_f32 v236, v236, v219, v219\nM 32,164,240\nds_read_b64 v[242:243], v246 offset:256\n"
"s_add_u32 m0, s50, 8192\nv_fma_f32 v237, v237, v219, v219\nM 48,160,228\nR 178,190,12288\nds_read_b64 v[244:245], v246 offset:384\nv_rcp_f32 v234, v234\n"
"M 48,164,244\nds_read_b64 v[246:247], v246 offset:0\nbuffer_load_dwordx4 v220, s[16:19], s63 offen lds\nv_rcp_f32 v235, v235\nM 64,160,232\nR 180,190,16384\n"
"v_mul_f32 v234, v20, v234\nv_rcp_f32 v236, v236\nM 64,164,248\nbuffer_load_dwordx4 v221, s[16:19], s63 offen offset:1024 lds\nv_rcp_f32 v237, v237\nM 80,160,236\n"
"R 182,190,20480\ns_add_u32 m0, s50, 16384\nM 80,164,252\nG 9\nv_mul_f32 v235, v21, v235\nv_mul_f32 v236, v22, v236\n"
"M 84,168,236\nR 160,191,8192\ns_add_u32 s63, s57, 65536\nM 84,172,252\nbuffer_load_dwordx4 v220, s[16:19], s63 offen lds\nM 68,168,232\n"
"R 162,191,12288\nv_mul_f32 v237, v23, v237\nM 68,172,248\nbuffer_store_dwordx4 v[0:3], v252, s[20:23], s84 offen offset:0 sc1\nM 52,168,228\nR 164,191,16384\n"
"G 9\nv_mfma_f32_16x16x32_bf16 v[32:35], v[238:241], v[208:211], v[32:35]\nv_cvt_pk_bf16_f32 v18, v234, v235\nM 52,172,244\nbuffer_load_dwordx4 v221, s[16:19], s63 offen offset:1024 lds\nM 36,168,224\n"
"R 166,191,20480\nG 9\nv_mfma_f32_16x16x32_bf16 v[36:39], v[240:243], v[208:211], v[36:39]\ns_add_u32 m0, s50, 24576\nM 36,172,240\nG 4\n"
"v_cvt_pk_bf16_f32 v19, v236, v237\nM 40,176,224\nR 168,188,24576\nv_mfma_f32_16x16x32_bf16 v[40:43], v[242:245], v[208:211], v[40:43]\ns_add_u32 s63, s57, 589824\nbuffer_store_dwordx4 v[16:19], v252, s[20:23], s85 offen offset:0 sc1\n"
"M 40,180,240\nbuffer_load_dwordx4 v220, s[16:19], s63 offen lds\nM 56,176,228\nR 170,188,28672\nv_mfma_f32_16x16x32_bf16 v[44:47], v[244:247], v[208:211], v[44:47]\nM 56,180,244\n"
"buffer_load_dwordx4 v221, s[16:19], s63 offen offset:1024 lds\nM 72,176,232\nR 172,188,32768\nv_mfma_f32_16x16x32_bf16 v[48:51], v[244:247], v[210:213], v[48:51]\ns_add_u32 m0, s50, 32768\ns_add_u32 s63, s56, 131072\n"
"M 72,180,248\nbuffer_load_dwordx4 v220, s[16:19], s63 offen lds\nM 88,176,236\nR 174,188,36864\nv_mfma_f32_16x16x32_bf16 v[52:55], v[238:241], v[210:213], v[52:55]\nM 88,180,252\n"
"G 4\nM 92,160,236\nR 176,189,24576\nv_mfma_f32_16x16x32_bf16 v[56:59], v[240:243], v[210:213], v[56:59]\nM 92,164,252\nbuffer_load_dwordx4 v221, s[16:19], s63 offen offset:1024 lds\n"
"M 76,160,232\nR 178,189,28672\nv_mfma_f32_16x16x32_bf16 v[60:63], v[242:245], v[210:213], v[60:63]\nv_mov_b32 v248, 0\nM 76,164,248\nv_mov_b32 v249, 0\n"
"M 60,160,228\nR 180,189,32768\ns_add_u32 s61, s82, 845832\ns_mov_b64 exec, 0xf\nbuffer_atomic_or_x2 v[248:249], v250, s[20:23], s61 offen sc0 sc1\ns_mov_b64 exec, -1\n"
"M 60,164,244\nM 44,160,224\nR 182,189,36864\nM 44,164,240\ns_barrier\nG 4\n"
"v_mfma_f32_16x16x32_bf16 v[96:99], v[168:171], a[0:3], 0\nR 160,190,24576\nM 96,172,16\ns_mov_b32 m0, s51\nv_mfma_f32_16x16x32_bf16 v[112:115], v[168:171], a[4:7], 0\nR 162,190,28672\n"
"M 112,172,20\nds_write_addtid_b32 v64 offset:0\nv_mfma_f32_16x16x32_bf16 v[128:131], v[168:171], a[8:11], 0\nR 164,190,32768\nM 128,172,24\nds_write_addtid_b32 v65 offset:256\n"
"v_mfma_f32_16x16x32_bf16 v[144:147], v[168:171], a[12:15], 0\nR 166,190,36864\nM 144,172,28\nds_write_addtid_b32 v66 offset:512\nG 7\nv_mfma_f32_16x16x32_bf16 v[148:151], v[176:179], a[12:15], 0\n"
"R 168,191,24576\nM 148,180,28\nds_write_addtid_b32 v67 offset:768\nv_mfma_f32_16x16x32_bf16 v[132:135], v[176:179], a[8:11], 0\nR 170,191,28672\nM 132,180,24\n"
"ds_write_addtid_b32 v68 offset:1024\nv_mfma_f32_16x16x32_bf16 v[116:119], v[176:179], a[4:7], 0\nR 172,191,32768\nM 116,180,20\nds_write_addtid_b32 v69 offset:1280\nv_mfma_f32_16x16x32_bf16 v[100:103], v[176:179], a[0:3], 0\n"
"R 174,191,36864\nds_write_addtid_b32 v70 offset:1536\nM 100,180,16\nds_write_addtid_b32 v71 offset:1792\nds_write_addtid_b32 v72 offset:2048\nG 11\n"
"v_mfma_f32_16x16x32_bf16 v[104:107], v[160:163], a[0:3], 0\nR 176,188,40960\nds_write_addtid_b32 v73 offset:2304\nM 104,164,16\nds_write_addtid_b32 v74 offset:2560\nds_write_addtid_b32 v75 offset:2816\n"
"v_mfma_f32_16x16x32_bf16 v[120:123], v[160:163], a[4:7], 0\nR 178,188,45056\nds_write_addtid_b32 v76 offset:3072\nM 120,164,20\nds_write_addtid_b32 v77 offset:3328\nds_write_addtid_b32 v78 offset:3584\n"
"v_mfma_f32_16x16x32_bf16 v[136:139], v[160:163], a[8:11], 0\nR 180,188,49152\nds_write_addtid_b32 v79 offset:3840\nM 136,164,24\nds_write_addtid_b32 v80 offset:4096\nds_write_addtid_b32 v81 offset:4352\n"
"v_mfma_f32_16x16x32_bf16 v[152:155], v[160:163], a[12:15], 0\nR 182,188,53248\nds_write_addtid_b32 v82 offset:4608\nM 152,164,28\nds_write_addtid_b32 v83 offset:4864\nds_write_addtid_b32 v84 offset:5120\n"
"G 15\nv_mfma_f32_16x16x32_bf16 v[156:159], v[168:171], a[12:15], 0\nR 160,189,40960\nds_write_addtid_b32 v85 offset:5376\nM 156,172,28\nds_write_addtid_b32 v86 offset:5632\n"
"ds_write_addtid_b32 v87 offset:5888\nv_mfma_f32_16x16x32_bf16 v[140:143], v[168:171], a[8:11], 0\nR 162,189,45056\nds_write_addtid_b32 v88 offset:6144\nM 140,172,24\nds_write_addtid_b32 v89 offset:6400\n"
"ds_write_addtid_b32 v90 offset:6656\nv_mfma_f32_16x16x32_bf16 v[124:127], v[168:171], a[4:7], 0\nR 164,189,49152\nds_write_addtid_b32 v91 offset:6912\nM 124,172,20\ns_add_u32 s64, s68, 37750784\n"
"ds_write_addtid_b32 v92 offset:7168\nds_write_addtid_b32 v93 offset:7424\nv_mfma_f32_16x16x32_bf16 v[108:111], v[168:171], a[0:3], 0\nR 166,189,53248\ns_waitcnt vmcnt(0)\nv_cmp_eq_u64 s[86:87], s[46:47], v[248:249]\n"
"M 108,172,16\nds_write_addtid_b32 v94 offset:7680\nds_write_addtid_b32 v95 offset:7936\nG 15\nM 96,176,32\nR 168,190,40960\n"
"s_and_b32 s62, s86, 0xf\ns_cmp_eq_u32 s62, 0xf\ns_cbranch_scc0 Lq861\nLq862:\nM 96,180,48\ns_add_u32 s65, s68, 37865472\n"
"s_add_u32 m0, s50, 40960\nv_mov_b32 v226, s46\nM 112,176,36\nR 170,190,45056\nG 2\ns_barrier\n"
"v_mov_b32 v227, s47\nM 112,180,52\ns_add_u32 s61, s81, 845848\ns_lshl_b32 s62, s44, 5\ns_add_u32 s61, s61, s62\ns_waitcnt vmcnt(0)\n"
"s_mov_b64 exec, 1\nbuffer_atomic_swap_x2 v[226:227], off, s[20:23], s61 sc1\ns_mov_b64 exec, -1\ns_add_u32 s63, s56, 655360\nds_read2st64_b32 v[64:65], v225 offset0:0 offset1:1\nM 128,176,40\n"
"R 172,190,49152\nbuffer_load_dwordx4 v220, s[16:19], s63 offen lds\nds_read2st64_b32 v[66:67], v225 offset0:2 offset1:3\nM 128,180,56\nbuffer_load_dwordx4 a[64:67], v252, s[20:23], s64 offen sc1\nds_read2st64_b32 v[68:69], v225 offset0:4 offset1:5\n"
"M 144,176,44\nR 174,190,53248\nds_read2st64_b32 v[70:71], v225 offset0:6 offset1:7\nM 144,180,60\nds_read2st64_b32 v[72:73], v225 offset0:8 offset1:9\nds_read2st64_b32 v[74:75], v225 offset0:10 offset1:11\n"
"M 148,160,44\nR 176,191,40960\nbuffer_load_dwordx4 v221, s[16:19], s63 offen offset:1024 lds\nds_read2st64_b32 v[76:77], v225 offset0:12 offset1:13\nM 148,164,60\nbuffer_load_dwordx4 a[68:71], v252, s[20:23], s65 offen sc1\n"
"G 9\nv_add_f32 v32, v32, v64\nM 132,160,40\nR 178,191,45056\ns_add_u32 s67, s69, 37865472\nv_add_f32 v33, v33, v65\n"
"M 132,164,56\ns_add_u32 s66, s69, 37750784\ns_add_u32 m0, s50, 49152\ns_add_u32 s63, s56, 196608\nG 8\nv_add_f32 v34, v34, v66\n"
"M 116,160,36\nR 180,191,49152\nbuffer_load_dwordx4 v220, s[16:19], s63 offen lds\nv_add_f32 v35, v35, v67\nM 116,164,52\nbuffer_load_dwordx4 a[72:75], v252, s[20:23], s66 offen sc1\n"
"G 8\nv_add_f32 v36, v36, v68\nM 100,160,32\nR 182,191,53248\nv_add_f32 v37, v37, v69\nG 7\n"
"v_add_f32 v38, v38, v70\nM 100,164,48\nds_read2st64_b32 v[78:79], v225 offset0:14 offset1:15\nv_add_f32 v39, v39, v71\nM 104,168,32\nR 160,184,0\n"
"buffer_load_dwordx4 v221, s[16:19], s63 offen offset:1024 lds\nG 8\nv_add_f32 v40, v40, v72\nM 104,172,48\nbuffer_load_dwordx4 a[76:79], v252, s[20:23], s67 offen sc1\nv_add_f32 v41, v41, v73\n"
"M 120,168,36\nR 162,184,4096\nG 8\nv_add_f32 v42, v42, v74\nv_add_f32 v43, v43, v75\nM 120,172,52\n"
"s_add_u32 s64, s68, 37757952\nds_read2st64_b32 v[80:81], v225 offset0:16 offset1:17\nG 7\nv_add_f32 v44, v44, v76\nM 136,168,40\nR 164,184,8192\n"
"ds_read2st64_b32 v[82:83], v225 offset0:18 offset1:19\nv_add_f32 v45, v45, v77\nM 136,172,56\ns_add_u32 s65, s68, 37872640\nds_read2st64_b32 v[84:85], v225 offset0:20 offset1:21\nG 6\n"
"v_add_f32 v46, v46, v78\nM 152,168,44\nR 166,184,12288\nds_read2st64_b32 v[86:87], v225 offset0:22 offset1:23\nv_add_f32 v47, v47, v79\nM 152,172,60\n"
"ds_read2st64_b32 v[88:89], v225 offset0:24 offset1:25\nds_read2st64_b32 v[90:91], v225 offset0:26 offset1:27\nM 156,176,44\nR 168,185,0\nds_read2st64_b32 v[92:93], v225 offset0:28 offset1:29\nM 156,180,60\n"
"buffer_load_dwordx4 a[80:83], v252, s[20:23], s64 offen sc1\nG 9\nv_add_f32 v48, v48, v80\nM 140,176,40\nR 170,185,4096\nv_add_f32 v49, v49, v81\n"
"G 8\nv_add_f32 v50, v50, v82\nM 140,180,56\nbuffer_load_dwordx4 a[84:87], v252, s[20:23], s65 offen sc1\nv_add_f32 v51, v51, v83\nM 124,176,36\n"
"R 172,185,8192\nG 8\nv_add_f32 v52, v52, v84\nv_add_f32 v53, v53, v85\nM 124,180,52\ns_add_u32 s66, s69, 37757952\n"
"ds_read2st64_b32 v[94:95], v225 offset0:30 offset1:31\nG 7\nv_add_f32 v54, v54, v86\nM 108,176,32\nR 174,185,12288\nv_add_f32 v55, v55, v87\n"
"M 108,180,48\nG 7\nv_add_f32 v56, v56, v88\nv_add_f32 v57, v57, v89\nv_mfma_f32_16x16x32_bf16 v[0:3], v[160:163], a[0:3], 0\nR 176,186,0\n"
"s_lshr_b32 s60, s45, 3\ns_lshl2_add_u32 s60, s60, s40\ns_lshl_b32 s60, s60, 8\nv_lshl_add_u32 v224, v252, 3, s60\nv_add_u32 v224, LqF-32768-LqPC, v224\ns_mov_b64 exec, 3\n"
"global_load_dword v224, v224, s[98:99]\ns_mov_b64 exec, -1\nM 0,164,16\nbuffer_load_dwordx4 a[88:91], v252, s[20:23], s66 offen sc1\nv_mfma_f32_16x16x32_bf16 v[16:19], v[160:163], a[4:7], 0\nR 178,186,4096\n"
"G 8\nv_add_f32 v58, v58, v90\nM 16,164,20\ns_add_u32 s67, s69, 37872640\nv_add_f32 v59, v59, v91\nG 6\n"
"v_add_f32 v60, v60, v92\nv_mfma_f32_16x16x32_bf16 a[224:227], v[160:163], a[8:11], 0\nR 180,186,8192\nv_add_f32 v61, v61, v93\nv_mfma_f32_16x16x32_bf16 a[224:227], v[164:167], a[24:27], a[224:227]\nbuffer_load_dwordx4 a[92:95], v252, s[20:23], s67 offen sc1\n"
"v_mfma_f32_16x16x32_bf16 a[240:243], v[160:163], a[12:15], 0\nR 182,186,12288\nG 5\nv_add_f32 v62, v62, v94\nv_mfma_f32_16x16x32_bf16 a[240:243], v[164:167], a[28:31], a[240:243]\ns_add_u32 s63, s56, 720896\n"
"G 4\nv_add_f32 v63, v63, v95\nv_mfma_f32_16x16x32_bf16 a[244:247], v[168:171], a[12:15], 0\nR 160,187,0\ns_add_u32 m0, s50, 57344\nv_mul_f32 v230, v32, v32\n"
"v_mfma_f32_16x16x32_bf16 a[244:247], v[172:175], a[28:31], a[244:247]\ns_add_u32 s64, s68, 37751808\ns_add_u32 s84, s73, 37752832\ns_add_u32 s85, s73, 37867520\nv_mul_f32 v231, v33, v33\nv_mfma_f32_16x16x32_bf16 a[228:231], v[168:171], a[8:11], 0\n"
"R 162,187,4096\nv_mov_b32 v248, 0\nv_mul_f32 v232, v34, v34\nv_mfma_f32_16x16x32_bf16 a[228:231], v[172:175], a[24:27], a[228:231]\nbuffer_load_dwordx4 a[96:99], v252, s[20:23], s64 offen sc1\nv_mov_b32 v249, 0\n"
"v_mul_f32 v233, v35, v35\nv_mfma_f32_16x16x32_bf16 v[20:23], v[168:171], a[4:7], 0\nR 164,187,8192\ns_add_u32 s61, s82, 845848\ns_mov_b64 exec, 0xf\nbuffer_atomic_or_x2 v[248:249], v250, s[20:23], s61 offen sc0 sc1\n"
"s_mov_b64 exec, -1\nv_fma_f32 v230, v230, v215, v214\nM 20,172,20\ns_add_u32 s65, s68, 37866496\nbuffer_load_dwordx4 v220, s[16:19], s63 offen lds\nv_fma_f32 v231, v231, v215, v214\n"
"v_mfma_f32_16x16x32_bf16 v[4:7], v[168:171], a[0:3], 0\nR 166,187,12288\nv_fma_f32 v232, v232, v215, v214\nv_fma_f32 v233, v233, v215, v214\nM 4,172,16\nG 4\n"
"v_mul_f32 v230, v32, v230\nv_mul_f32 v231, v33, v231\nv_mul_f32 v232, v34, v232\nv_mfma_f32_16x16x32_bf16 v[8:11], v[176:179], a[0:3], 0\nR 168,184,16384\nv_mul_f32 v233, v35, v233\n"
"v_exp_f32 v230, v230\nM 8,180,16\nbuffer_load_dwordx4 a[100:103], v252, s[20:23], s65 offen sc1\nbuffer_load_dwordx4 v221, s[16:19], s63 offen offset:1024 lds\nv_exp_f32 v231, v231\nv_mfma_f32_16x16x32_bf16 v[24:27], v[176:179], a[4:7], 0\n"
"R 170,184,20480\ns_add_u32 s67, s69, 37866496\nv_exp_f32 v232, v232\nM 24,180,20\ns_add_u32 s66, s69, 37751808\ns_add_u32 s63, s56, 393216\n"
"v_fma_f32 v230, v230, v216, v216\nv_exp_f32 v233, v233\nv_mfma_f32_16x16x32_bf16 a[232:235], v[176:179], a[8:11], 0\nR 172,184,24576\ns_add_u32 m0, s50, 65536\nv_fma_f32 v231, v231, v216, v216\n"
"v_mfma_f32_16x16x32_bf16 a[232:235], v[180:183], a[24:27], a[232:235]\nbuffer_load_dwordx4 a[104:107], v252, s[20:23], s66 offen sc1\nbuffer_load_dwordx4 v220, s[16:19], s63 offen lds\nv_fma_f32 v232, v232, v216, v216\nv_mfma_f32_16x16x32_bf16 a[248:251], v[176:179], a[12:15], 0\nR 174,184,28672\n"
"v_fma_f32 v233, v233, v216, v216\nv_rcp_f32 v230, v230\nv_mfma_f32_16x16x32_bf16 a[248:251], v[180:183], a[28:31], a[248:251]\nG 4\nv_rcp_f32 v231, v231\nv_rcp_f32 v232, v232\n"
"v_mfma_f32_16x16x32_bf16 a[252:255], v[160:163], a[12:15], 0\nR 176,185,16384\nv_mul_f32 v230, v32, v230\nv_rcp_f32 v233, v233\nv_mfma_f32_16x16x32_bf16 a[252:255], v[164:167], a[28:31], a[252:255]\nbuffer_load_dwordx4 a[108:111], v252, s[20:23], s67 offen sc1\n"
"buffer_load_dwordx4 v221, s[16:19], s63 offen offset:1024 lds\nv_mul_f32 v231, v33, v231\nv_mfma_f32_16x16x32_bf16 a[236:239], v[160:163], a[8:11], 0\nR 178,185,20480\ns_add_u32 m0, s50, 73728\nv_mul_f32 v232, v34, v232\n"
"v_mfma_f32_16x16x32_bf16 a[236:239], v[164:167], a[24:27], a[236:239]\ns_add_u32 s64, s68, 37758976\nv_mul_f32 v233, v35, v233\nv_cvt_pk_bf16_f32 v32, v230, v231\nv_cvt_pk_bf16_f32 v33, v232, v233\nv_mfma_f32_16x16x32_bf16 v[28:31], v[160:163], a[4:7], 0\n"
"R 180,185,24576\ns_add_u32 s63, s56, 458752\nv_mul_f32 v234, v36, v36\nM 28,164,20\ns_add_u32 s65, s68, 37873664\nbuffer_load_dwordx4 v220, s[16:19], s63 offen lds\n"
"v_mul_f32 v235, v37, v37\nv_mfma_f32_16x16x32_bf16 v[12:15], v[160:163], a[0:3], 0\nR 182,185,28672\nv_mul_f32 v236, v38, v38\nv_mul_f32 v237, v39, v39\nM 12,164,16\n"
"s_waitcnt vmcnt(16)\ns_barrier\nG 4\nv_fma_f32 v234, v234, v215, v214\nM 0,168,32\nR 160,186,16384\n"
"v_fma_f32 v235, v235, v215, v214\nv_fma_f32 v236, v236, v215, v214\nM 0,172,48\nbuffer_load_dwordx4 a[112:115], v252, s[20:23], s64 offen sc1\nbuffer_load_dwordx4 v221, s[16:19], s63 offen offset:1024 lds\nv_fma_f32 v237, v237, v215, v214\n"
"M 16,168,36\nR 162,186,20480\nv_mul_f32 v234, v36, v234\nv_mul_f32 v235, v37, v235\nM 16,172,52\nbuffer_load_dwordx4 a[116:119], v252, s[20:23], s65 offen sc1\n"
"v_mul_f32 v236, v38, v236\nv_mfma_f32_16x16x32_bf16 a[224:227], v[168:171], a[40:43], a[224:227]\nR 164,186,24576\nv_mul_f32 v237, v39, v237\nv_exp_f32 v234, v234\nv_mfma_f32_16x16x32_bf16 a[224:227], v[172:175], a[56:59], a[224:227]\n"
"s_add_u32 s66, s69, 37758976\nv_exp_f32 v235, v235\nv_exp_f32 v236, v236\nv_mfma_f32_16x16x32_bf16 a[240:243], v[168:171], a[44:47], a[240:243]\nR 166,186,28672\nv_fma_f32 v234, v234, v216, v216\n"
"v_exp_f32 v237, v237\nv_mfma_f32_16x16x32_bf16 a[240:243], v[172:175], a[60:63], a[240:243]\nG 4\nv_rcp_f32 v234, v234\nv_fma_f32 v235, v235, v216, v216\nv_mfma_f32_16x16x32_bf16 a[244:247], v[176:179], a[44:47], a[244:247]\n"
"R 168,187,16384\nv_fma_f32 v236, v236, v216, v216\nv_fma_f32 v237, v237, v216, v216\nv_mfma_f32_16x16x32_bf16 a[244:247], v[180:183], a[60:63], a[244:247]\nbuffer_load_dwordx4 a[120:123], v252, s[20:23], s66 offen sc1\nv_rcp_f32 v235, v235\n"
"v_mfma_f32_16x16x32_bf16 a[228:231], v[176:179], a[40:43], a[228:231]\nR 170,187,20480\nv_mul_f32 v234, v36, v234\nv_rcp_f32 v236, v236\nv_mfma_f32_16x16x32_bf16 a[228:231], v[180:183], a[56:59], a[228:231]\ns_add_u32 s67, s69, 37873664\n"
"v_rcp_f32 v237, v237\nv_mul_f32 v235, v37, v235\nM 20,176,36\nR 172,187,24576\nv_mul_f32 v236, v38, v236\nv_mul_f32 v237, v39, v237\n"
"M 20,180,52\nbuffer_load_dwordx4 a[124:127], v252, s[20:23], s67 offen sc1\nv_cvt_pk_bf16_f32 v34, v234, v235\nM 4,176,32\nR 174,187,28672\ns_waitcnt vmcnt(13)\n"
"v_cmp_eq_u64 s[86:87], s[46:47], v[248:249]\nv_cvt_pk_bf16_f32 v35, v236, v237\nM 4,180,48\ns_add_u32 s63, s57, 131072\nG 4\nv_mul_f32 v230, v40, v40\n"
"v_mul_f32 v231, v41, v41\nM 8,160,32\nR 176,184,32768\ns_and_b32 s62, s86, 0xf\ns_cmp_eq_u32 s62, 0xf\ns_cbranch_scc0 Lq896\n"
"Lq897:\nv_mul_f32 v232, v42, v42\nM 8,164,48\ns_add_u32 s64, s68, 37754880\nv_mul_f32 v233, v43, v43\nv_fma_f32 v230, v230, v215, v214\n"
"v_fma_f32 v231, v231, v215, v214\nM 24,160,36\nR 178,184,36864\ns_add_u32 m0, s50, 81920\nv_fma_f32 v232, v232, v215, v214\nM 24,164,52\n"
"buffer_load_dwordx4 a[128:131], v252, s[20:23], s64 offen sc1\nbuffer_load_dwordx4 v220, s[16:19], s63 offen lds\nv_fma_f32 v233, v233, v215, v214\nv_mfma_f32_16x16x32_bf16 a[232:235], v[160:163], a[40:43], a[232:235]\nR 180,184,40960\ns_add_u32 s66, s69, 37754880\n"
"v_mul_f32 v230, v40, v230\nv_mfma_f32_16x16x32_bf16 a[232:235], v[164:167], a[56:59], a[232:235]\ns_add_u32 s65, s68, 37869568\nv_exp_f32 v230, v230\nv_mul_f32 v231, v41, v231\nv_mfma_f32_16x16x32_bf16 a[248:251], v[160:163], a[44:47], a[248:251]\n"
"R 182,184,45056\nv_mul_f32 v232, v42, v232\nv_mul_f32 v233, v43, v233\nv_mfma_f32_16x16x32_bf16 a[248:251], v[164:167], a[60:63], a[248:251]\nbuffer_load_dwordx4 v221, s[16:19], s63 offen offset:1024 lds\nG 4\n"
"v_exp_f32 v231, v231\nv_mfma_f32_16x16x32_bf16 a[252:255], v[168:171], a[44:47], a[252:255]\nR 160,185,32768\ns_add_u32 m0, s50, 90112\nv_exp_f32 v232, v232\nv_mfma_f32_16x16x32_bf16 a[252:255], v[172:175], a[60:63], a[252:255]\n"
"buffer_load_dwordx4 a[132:135], v252, s[20:23], s65 offen sc1\nv_exp_f32 v233, v233\nv_mfma_f32_16x16x32_bf16 a[236:239], v[168:171], a[40:43], a[236:239]\nR 162,185,36864\ns_add_u32 s63, s57, 655360\nv_fma_f32 v230, v230, v216, v216\n"
"v_mfma_f32_16x16x32_bf16 a[236:239], v[172:175], a[56:59], a[236:239]\nbuffer_load_dwordx4 a[136:139], v252, s[20:23], s66 offen sc1\nbuffer_load_dwordx4 v220, s[16:19], s63 offen lds\nv_fma_f32 v231, v231, v216, v216\nM 28,168,36\nR 164,185,40960\n"
"s_add_u32 s64, s68, 37762048\nv_fma_f32 v232, v232, v216, v216\nM 28,172,52\ns_add_u32 s67, s69, 37869568\nv_rcp_f32 v230, v230\nv_fma_f32 v233, v233, v216, v216\n"
"M 12,168,32\nR 166,185,45056\nv_mul_f32 v230, v40, v230\nv_rcp_f32 v231, v231\nM 12,172,48\nbuffer_load_dwordx4 v221, s[16:19], s63 offen offset:1024 lds\n"
"G 4\nv_rcp_f32 v232, v232\nM 96,176,64\nR 168,186,32768\ns_add_u32 m0, s50, 98304\nv_rcp_f32 v233, v233\n"
"M 96,180,80\nbuffer_load_dwordx4 a[140:143], v252, s[20:23], s67 offen sc1\nv_mul_f32 v231, v41, v231\nM 112,176,68\nR 170,186,36864\ns_add_u32 s63, s57, 196608\n"
"v_mul_f32 v232, v42, v232\nM 112,180,84\nbuffer_load_dwordx4 a[144:147], v252, s[20:23], s64 offen sc1\nbuffer_load_dwordx4 v220, s[16:19], s63 offen lds\nv_mul_f32 v233, v43, v233\nM 128,176,72\n"
"R 172,186,40960\ns_add_u32 s66, s69, 37762048\nv_cvt_pk_bf16_f32 v40, v230, v231\nM 128,180,88\ns_add_u32 s65, s68, 37876736\nv_cvt_pk_bf16_f32 v41, v232, v233\n"
"v_mul_f32 v234, v44, v44\nv_mul_f32 v235, v45, v45\nM 144,176,76\nR 174,186,45056\nv_mul_f32 v236, v46, v46\nv_mul_f32 v237, v47, v47\n"
"M 144,180,92\nbuffer_load_dwordx4 v221, s[16:19], s63 offen offset:1024 lds\nG 4\nv_fma_f32 v234, v234, v215, v214\nM 148,160,76\nR 176,187,32768\n"
"s_add_u32 m0, s50, 106496\nv_fma_f32 v235, v235, v215, v214\nM 148,164,92\nbuffer_load_dwordx4 a[148:151], v252, s[20:23], s65 offen sc1\nv_fma_f32 v236, v236, v215, v214\nM 132,160,72\n"
"R 178,187,36864\ns_add_u32 s63, s57, 720896\nv_fma_f32 v237, v237, v215, v214\nM 132,164,88\nbuffer_load_dwordx4 a[152:155], v252, s[20:23], s66 offen sc1\nbuffer_load_dwordx4 v220, s[16:19], s63 offen lds\n"
"v_mul_f32 v234, v44, v234\nM 116,160,68\nR 180,187,40960\nv_mul_f32 v235, v45, v235\nv_mul_f32 v236, v46, v236\nM 116,164,84\n"
"s_add_u32 s67, s69, 37876736\nv_exp_f32 v234, v234\nv_mul_f32 v237, v47, v237\nM 100,160,64\nR 182,187,45056\nv_fma_f32 v234, v234, v216, v216\n"
"v_exp_f32 v235, v235\nM 100,164,80\nbuffer_load_dwordx4 v221, s[16:19], s63 offen offset:1024 lds\nG 4\nv_exp_f32 v236, v236\nM 104,168,64\n"
"R 160,184,49152\ns_waitcnt vmcnt(18)\ns_barrier\nv_exp_f32 v237, v237\nM 104,172,80\nbuffer_load_dwordx4 a[156:159], v252, s[20:23], s67 offen sc1\n"
"v_fma_f32 v235, v235, v216, v216\nM 120,168,68\nR 162,184,53248\nv_fma_f32 v236, v236, v216, v216\nv_fma_f32 v237, v237, v216, v216\nM 120,172,84\n"
"v_rcp_f32 v234, v234\nv_rcp_f32 v235, v235\nM 136,168,72\nR 164,184,57344\nv_mul_f32 v234, v44, v234\nv_rcp_f32 v236, v236\n"
"M 136,172,88\nbuffer_store_dwordx4 v[32:35], v252, s[20:23], s84 offen offset:0 sc1\nv_rcp_f32 v237, v237\nM 152,168,76\nR 166,184,61440\nv_mul_f32 v235, v45, v235\n"
"v_mul_f32 v236, v46, v236\nM 152,172,92\nG 4\nv_mul_f32 v237, v47, v237\nv_cvt_pk_bf16_f32 v42, v234, v235\nv_cvt_pk_bf16_f32 v43, v236, v237\n"
"M 156,176,76\nR 168,185,49152\nv_mul_f32 v230, v48, v48\nv_mul_f32 v231, v49, v49\nM 156,180,92\ns_add_u32 m0, s50, 114688\n"
"v_mul_f32 v232, v50, v50\nv_mul_f32 v233, v51, v51\nv_fma_f32 v230, v230, v218, v217\nM 140,176,72\nR 170,185,53248\nv_fma_f32 v231, v231, v218, v217\n"
"v_fma_f32 v232, v232, v218, v217\nM 140,180,88\ns_add_u32 s63, s57, 393216\nv_fma_f32 v233, v233, v218, v217\nv_mul_f32 v230, v48, v230\nv_mul_f32 v231, v49, v231\n"
"M 124,176,68\nR 172,185,57344\nv_mul_f32 v232, v50, v232\nv_mul_f32 v233, v51, v233\nM 124,180,84\nbuffer_load_dwordx4 v220, s[16:19], s63 offen lds\n"
"v_exp_f32 v230, v230\nM 108,176,64\nR 174,185,61440\nv_fma_f32 v230, v230, v219, v219\nv_exp_f32 v231, v231\nM 108,180,80\n"
"G 4\nv_exp_f32 v232, v232\nv_exp_f32 v233, v233\nM 96,160,96\nR 176,186,49152\nv_fma_f32 v231, v231, v219, v219\n"
"v_fma_f32 v232, v232, v219, v219\nM 96,164,112\nbuffer_load_dwordx4 v221, s[16:19], s63 offen offset:1024 lds\nv_fma_f32 v233, v233, v219, v219\nM 112,160,100\nR 178,186,53248\n"
"s_add_u32 s63, s57, 458752\nv_rcp_f32 v230, v230\ns_waitcnt vmcnt(21)\nM 112,164,116\ns_add_u32 m0, s50, 0\nv_rcp_f32 v231, v231\n"
"v_rcp_f32 v232, v232\nM 128,160,104\nR 180,186,57344\nv_mul_f32 v230, v48, v230\nv_rcp_f32 v233, v233\ns_waitcnt vmcnt(20)\n"
"M 128,164,120\nbuffer_load_dwordx4 v220, s[16:19], s63 offen lds\nv_mul_f32 v231, v49, v231\nM 144,160,108\nR 182,186,61440\nv_mul_f32 v232, v50, v232\n"
"v_mul_f32 v233, v51, v233\ns_waitcnt vmcnt(20)\nM 144,164,124\nG 4\nv_cvt_pk_bf16_f32 v48, v230, v231\nv_cvt_pk_bf16_f32 v49, v232, v233\n"
"v_mul_f32 v234, v52, v52\nM 148,168,108\nR 160,187,49152\nv_mul_f32 v235, v53, v53\nv_mul_f32 v236, v54, v54\nM 148,172,124\n"
"v_mul_f32 v237, v55, v55\nv_fma_f32 v234, v234, v218, v217\nv_fma_f32 v235, v235, v218, v217\nM 132,168,104\nR 162,187,53248\nv_fma_f32 v236, v236, v218, v217\n"
"v_fma_f32 v237, v237, v218, v217\nM 132,172,120\nbuffer_load_dwordx4 v221, s[16:19], s63 offen offset:1024 lds\nv_mul_f32 v234, v52, v234\nM 116,168,100\nR 164,187,57344\n"
"s_add_u32 s63, s58, 0\nv_mul_f32 v235, v53, v235\nM 116,172,116\ns_add_u32 m0, s50, 8192\nv_exp_f32 v234, v234\nv_mul_f32 v236, v54, v236\n"
"M 100,168,96\nR 166,187,61440\nv_mul_f32 v237, v55, v237\nv_exp_f32 v235, v235\nM 100,172,112\nG 4\n"
"v_exp_f32 v236, v236\nv_exp_f32 v237, v237\nM 104,176,96\nR 168,188,0\nv_fma_f32 v234, v234, v219, v219\nv_fma_f32 v235, v235, v219, v219\n"
"M 104,180,112\nbuffer_load_dwordx4 v220, s[16:19], s63 offen lds\nv_fma_f32 v236, v236, v219, v219\nM 120,176,100\nR 170,188,4096\nv_fma_f32 v237, v237, v219, v219\n"
"v_rcp_f32 v234, v234\nM 120,180,116\nv_rcp_f32 v235, v235\nv_rcp_f32 v236, v236\nM 136,176,104\nR 172,188,8192\n"
"v_mul_f32 v234, v52, v234\nv_rcp_f32 v237, v237\nM 136,180,120\nbuffer_load_dwordx4 v221, s[16:19], s63 offen offset:1024 lds\nv_mul_f32 v235, v53, v235\nM 152,176,108\n"
"R 174,188,12288\ns_add_u32 m0, s50, 16384\nv_mul_f32 v236, v54, v236\nM 152,180,124\nG 4\nv_mul_f32 v237, v55, v237\n"
"v_cvt_pk_bf16_f32 v50, v234, v235\nv_cvt_pk_bf16_f32 v51, v236, v237\nM 156,160,108\nR 176,189,0\ns_add_u32 s63, s58, 524288\nv_mul_f32 v230, v56, v56\n"
"M 156,164,124\nbuffer_load_dwordx4 v220, s[16:19], s63 offen lds\nv_mul_f32 v231, v57, v57\nM 140,160,104\nR 178,189,4096\nv_mul_f32 v232, v58, v58\n"
"v_mul_f32 v233, v59, v59\nM 140,164,120\nv_fma_f32 v230, v230, v218, v217\nv_fma_f32 v231, v231, v218, v217\nv_fma_f32 v232, v232, v218, v217\nM 124,160,100\n"
"R 180,189,8192\nv_fma_f32 v233, v233, v218, v217\nv_mul_f32 v230, v56, v230\nM 124,164,116\nbuffer_load_dwordx4 v221, s[16:19], s63 offen offset:1024 lds\nv_mul_f32 v231, v57, v231\n"
"M 108,160,96\nR 182,189,12288\ns_add_u32 s63, s58, 65536\nv_mul_f32 v232, v58, v232\nM 108,164,112\ns_waitcnt vmcnt(10)\n"
"s_barrier\nG 4\nM 96,168,128\nR 160,190,0\nv_mul_f32 v233, v59, v233\nM 96,172,144\n"
"s_add_u32 m0, s50, 24576\nv_exp_f32 v230, v230\nM 112,168,132\nR 162,190,4096\nv_fma_f32 v230, v230, v219, v219\nM 112,172,148\n"
"buffer_load_dwordx4 v220, s[16:19], s63 offen lds\nM 128,168,136\nR 164,190,8192\nv_mul_f32 v234, v60, v60\nM 128,172,152\nv_exp_f32 v231, v231\n"
"M 144,168,140\nR 166,190,12288\nv_fma_f32 v231, v231, v219, v219\ns_waitcnt vmcnt(10)\nM 144,172,156\nG 4\n"
"v_exp_f32 v232, v232\nM 148,176,140\nR 168,191,0\nv_fma_f32 v232, v232, v219, v219\nM 148,180,156\nbuffer_load_dwordx4 v221, s[16:19], s63 offen offset:1024 lds\n"
"M 132,176,136\nR 170,191,4096\nv_mul_f32 v235, v61, v61\nM 132,180,152\ns_add_u32 m0, s50, 32768\nv_exp_f32 v233, v233\n"
"M 116,176,132\nR 172,191,8192\nv_fma_f32 v233, v233, v219, v219\nM 116,180,148\ns_add_u32 s63, s58, 589824\nv_rcp_f32 v230, v230\n"
"M 100,176,128\nR 174,191,12288\nv_mul_f32 v230, v56, v230\nM 100,180,144\nG 4\nv_rcp_f32 v231, v231\n"
"M 104,160,128\nR 176,188,16384\nv_mul_f32 v231, v57, v231\nM 104,164,144\nbuffer_load_dwordx4 v220, s[16:19], s63 offen lds\nM 120,160,132\n"
"R 178,188,20480\nv_cvt_pk_bf16_f32 v56, v230, v231\nM 120,164,148\nv_rcp_f32 v232, v232\nM 136,160,136\nR 180,188,24576\n"
"v_mul_f32 v232, v58, v232\nM 136,164,152\nbuffer_store_dwordx4 v[40:43], v252, s[20:23], s84 offen offset:1024 sc1\nM 152,160,140\nR 182,188,28672\nv_mul_f32 v236, v62, v62\n"
"M 152,164,156\nG 4\nv_rcp_f32 v233, v233\nM 156,168,140\nR 160,189,16384\nv_mul_f32 v233, v59, v233\n"
"M 156,172,156\nbuffer_load_dwordx4 v221, s[16:19], s63 offen offset:1024 lds\nM 140,168,136\nR 162,189,20480\nv_cvt_pk_bf16_f32 v57, v232, v233\nM 140,172,152\n"
"s_add_u32 m0, s50, 40960\nv_mul_f32 v237, v63, v63\nv_fma_f32 v234, v234, v218, v217\nM 124,168,132\nR 164,189,24576\nv_fma_f32 v235, v235, v218, v217\n"
"M 124,172,148\ns_add_u32 s63, s58, 131072\nv_fma_f32 v236, v236, v218, v217\nv_fma_f32 v237, v237, v218, v217\nM 108,168,128\nR 166,189,28672\n"
"v_mul_f32 v234, v60, v234\nM 108,172,144\nG 4\nv_exp_f32 v234, v234\nM 0,176,64\nR 168,190,16384\n"
"v_mul_f32 v235, v61, v235\nM 0,180,80\nbuffer_load_dwordx4 v220, s[16:19], s63 offen lds\nM 16,176,68\nR 170,190,20480\nv_mul_f32 v236, v62, v236\n"
"M 16,180,84\nv_exp_f32 v235, v235\nv_mfma_f32_16x16x32_bf16 a[224:227], v[176:179], a[72:75], a[224:227]\nR 172,190,24576\nv_mul_f32 v237, v63, v237\nv_mfma_f32_16x16x32_bf16 a[224:227], v[180:183], a[88:91], a[224:227]\n"
"buffer_store_dwordx4 v[48:51], v252, s[20:23], s85 offen offset:0 sc1\nv_mfma_f32_16x16x32_bf16 a[240:243], v[176:179], a[76:79], a[240:243]\nR 174,190,28672\nv_fma_f32 v234, v234, v219, v219\nv_mfma_f32_16x16x32_bf16 a[240:243], v[180:183], a[92:95], a[240:243]\nG 4\n"
"v_exp_f32 v236, v236\nv_mfma_f32_16x16x32_bf16 a[244:247], v[160:163], a[76:79], a[244:247]\nR 176,191,16384\nv_fma_f32 v235, v235, v219, v219\nv_mfma_f32_16x16x32_bf16 a[244:247], v[164:167], a[92:95], a[244:247]\nbuffer_load_dwordx4 v221, s[16:19], s63 offen offset:1024 lds\n"
"v_mfma_f32_16x16x32_bf16 a[228:231], v[160:163], a[72:75], a[228:231]\nR 178,191,20480\nv_fma_f32 v236, v236, v219, v219\nv_mfma_f32_16x16x32_bf16 a[228:231], v[164:167], a[88:91], a[228:231]\ns_add_u32 m0, s50, 49152\nv_exp_f32 v237, v237\n"
"M 20,160,68\nR 180,191,24576\nv_fma_f32 v237, v237, v219, v219\nM 20,164,84\ns_add_u32 s63, s58, 655360\nv_rcp_f32 v234, v234\n"
"M 4,160,64\nR 182,191,28672\nv_mul_f32 v234, v60, v234\nM 4,164,80\nG 4\nv_rcp_f32 v235, v235\n"
"M 8,168,64\nR 160,188,32768\nv_mul_f32 v235, v61, v235\nM 8,172,80\nbuffer_load_dwordx4 v220, s[16:19], s63 offen lds\nM 24,168,68\n"
"R 162,188,36864\nv_cvt_pk_bf16_f32 v58, v234, v235\nM 24,172,84\nv_rcp_f32 v236, v236\nv_mfma_f32_16x16x32_bf16 a[232:235], v[168:171], a[72:75], a[232:235]\nR 164,188,40960\n"
"v_mul_f32 v236, v62, v236\nv_mfma_f32_16x16x32_bf16 a[232:235], v[172:175], a[88:91], a[232:235]\nbuffer_load_dwordx4 v221, s[16:19], s63 offen offset:1024 lds\nv_mfma_f32_16x16x32_bf16 a[248:251], v[168:171], a[76:79], a[248:251]\nR 166,188,45056\ns_add_u32 m0, s50, 57344\n"
"v_mfma_f32_16x16x32_bf16 a[248:251], v[172:175], a[92:95], a[248:251]\nG 4\nv_rcp_f32 v237, v237\nv_mfma_f32_16x16x32_bf16 a[252:255], v[176:179], a[76:79], a[252:255]\nR 168,189,32768\ns_add_u32 s63, s58, 196608\n"
"v_mfma_f32_16x16x32_bf16 a[252:255], v[180:183], a[92:95], a[252:255]\nbuffer_load_dwordx4 v220, s[16:19], s63 offen lds\nv_mfma_f32_16x16x32_bf16 a[236:239], v[176:179], a[72:75], a[236:239]\nR 170,189,36864\nv_mul_f32 v237, v63, v237\nv_mfma_f32_16x16x32_bf16 a[236:239], v[180:183], a[88:91], a[236:239]\n"
"v_cvt_pk_bf16_f32 v59, v236, v237\nbuffer_store_dwordx4 v[56:59], v252, s[20:23], s85 offen offset:1024 sc1\nM 28,176,68\nR 172,189,40960\nM 28,180,84\nbuffer_load_dwordx4 v221, s[16:19], s63 offen offset:1024 lds\n"
"M 12,176,64\nR 174,189,45056\nM 12,180,80\ns_waitcnt vmcnt(13)\ns_barrier\nG 4\n"
"M 0,160,96\nR 176,190,32768\nM 0,164,112\nM 16,160,100\nR 178,190,36864\nM 16,164,116\n"
"v_mfma_f32_16x16x32_bf16 a[224:227], v[160:163], a[104:107], a[224:227]\nR 180,190,40960\nv_mfma_f32_16x16x32_bf16 a[224:227], v[164:167], a[120:123], a[224:227]\nv_mfma_f32_16x16x32_bf16 a[240:243], v[160:163], a[108:111], a[240:243]\nR 182,190,45056\nv_mfma_f32_16x16x32_bf16 a[240:243], v[164:167], a[124:127], a[240:243]\n"
"G 4\nv_mfma_f32_16x16x32_bf16 a[244:247], v[168:171], a[108:111], a[244:247]\nR 160,191,32768\nv_mfma_f32_16x16x32_bf16 a[244:247], v[172:175], a[124:127], a[244:247]\nv_mfma_f32_16x16x32_bf16 a[228:231], v[168:171], a[104:107], a[228:231]\nR 162,191,36864\n"
"v_mfma_f32_16x16x32_bf16 a[228:231], v[172:175], a[120:123], a[228:231]\nM 20,168,100\nR 164,191,40960\nM 20,172,116\nM 4,168,96\nR 166,191,45056\n"
"M 4,172,112\nG 4\nM 8,176,96\nR 168,188,49152\nM 8,180,112\nM 24,176,100\n"
"R 170,188,53248\nM 24,180,116\nv_mfma_f32_16x16x32_bf16 a[232:235], v[176:179], a[104:107], a[232:235]\nR 172,184,0\nv_mfma_f32_16x16x32_bf16 a[232:235], v[180:183], a[120:123], a[232:235]\nv_mfma_f32_16x16x32_bf16 a[248:251], v[176:179], a[108:111], a[248:251]\n"
"R 174,184,4096\nv_mfma_f32_16x16x32_bf16 a[248:251], v[180:183], a[124:127], a[248:251]\nG 4\nv_mfma_f32_16x16x32_bf16 a[252:255], v[160:163], a[108:111], a[252:255]\nR 176,189,49152\nv_mfma_f32_16x16x32_bf16 a[252:255], v[164:167], a[124:127], a[252:255]\n"
"v_mfma_f32_16x16x32_bf16 a[236:239], v[160:163], a[104:107], a[236:239]\nR 178,189,53248\nv_mfma_f32_16x16x32_bf16 a[236:239], v[164:167], a[120:123], a[236:239]\nM 28,160,100\nR 180,185,0\nM 28,164,116\n"
"M 12,160,96\nR 182,185,4096\nM 12,164,112\nG 4\nM 0,168,128\nR 160,190,49152\n"
"M 0,172,144\nM 16,168,132\nR 162,190,53248\nM 16,172,148\nv_mfma_f32_16x16x32_bf16 a[224:227], v[168:171], a[136:139], a[224:227]\nR 164,186,0\n"
"v_mfma_f32_16x16x32_bf16 a[224:227], v[172:175], a[152:155], a[224:227]\nv_mfma_f32_16x16x32_bf16 a[240:243], v[168:171], a[140:143], a[240:243]\nR 166,186,4096\nv_mfma_f32_16x16x32_bf16 a[240:243], v[172:175], a[156:159], a[240:243]\nG 4\nv_mfma_f32_16x16x32_bf16 a[244:247], v[176:179], a[140:143], a[244:247]\n"
"R 168,191,49152\nv_mfma_f32_16x16x32_bf16 a[244:247], v[180:183], a[156:159], a[244:247]\nv_mfma_f32_16x16x32_bf16 a[228:231], v[176:179], a[136:139], a[228:231]\nR 170,191,53248\nv_mov_b32 v226, s46\nv_mfma_f32_16x16x32_bf16 a[228:231], v[180:183], a[152:155], a[228:231]\n"
"s_add_u32 m0, s50, 65536\nv_mov_b32 v227, s47\nM 20,176,132\nR 172,187,0\ns_add_u32 s61, s81, 845840\ns_lshl_b32 s62, s44, 5\n"
"s_add_u32 s61, s61, s62\ns_waitcnt vmcnt(0)\ns_mov_b64 exec, 1\nbuffer_atomic_swap_x2 v[226:227], off, s[20:23], s61 sc1\ns_mov_b64 exec, -1\nM 20,180,148\n"
"s_add_u32 s63, s58, 720896\nM 4,176,128\nR 174,187,4096\nM 4,180,144\nG 4\nM 8,160,128\n"
"R 176,184,8192\nM 8,164,144\nbuffer_load_dwordx4 v220, s[16:19], s63 offen lds\nM 24,160,132\nR 178,184,12288\nM 24,164,148\n"
"v_mfma_f32_16x16x32_bf16 a[232:235], v[160:163], a[136:139], a[232:235]\nR 180,184,16384\nv_mfma_f32_16x16x32_bf16 a[232:235], v[164:167], a[152:155], a[232:235]\nbuffer_load_dwordx4 v221, s[16:19], s63 offen offset:1024 lds\nv_mfma_f32_16x16x32_bf16 a[248:251], v[160:163], a[140:143], a[248:251]\nR 182,184,20480\n"
"s_add_u32 m0, s50, 73728\nv_mfma_f32_16x16x32_bf16 a[248:251], v[164:167], a[156:159], a[248:251]\nG 4\nv_mfma_f32_16x16x32_bf16 a[252:255], v[168:171], a[140:143], a[252:255]\nR 160,185,8192\ns_add_u32 s63, s58, 393216\n"
"v_mfma_f32_16x16x32_bf16 a[252:255], v[172:175], a[156:159], a[252:255]\nbuffer_load_dwordx4 v220, s[16:19], s63 offen lds\nv_mfma_f32_16x16x32_bf16 a[236:239], v[168:171], a[136:139], a[236:239]\nR 162,185,12288\nv_mfma_f32_16x16x32_bf16 a[236:239], v[172:175], a[152:155], a[236:239]\nM 28,168,132\n"
"R 164,185,16384\nM 28,172,148\nbuffer_load_dwordx4 v221, s[16:19], s63 offen offset:1024 lds\nM 12,168,128\nR 166,185,20480\ns_add_u32 s63, s58, 458752\n"
"M 12,172,144\ns_barrier\nG 4\nv_mfma_f32_16x16x32_bf16 v[32:35], v[176:179], a[0:3], 0\nR 168,186,8192\nM 32,180,16\n"
"s_add_u32 m0, s50, 81920\nv_mfma_f32_16x16x32_bf16 v[48:51], v[176:179], a[4:7], 0\nR 170,186,12288\nM 48,180,20\nbuffer_load_dwordx4 v220, s[16:19], s63 offen lds\nv_mfma_f32_16x16x32_bf16 v[64:67], v[176:179], a[8:11], 0\n"
"R 172,186,16384\nM 64,180,24\nv_mfma_f32_16x16x32_bf16 v[80:83], v[176:179], a[12:15], 0\nR 174,186,20480\nM 80,180,28\nG 4\n"
"v_mfma_f32_16x16x32_bf16 v[84:87], v[160:163], a[12:15], 0\nR 176,187,8192\nM 84,164,28\nbuffer_load_dwordx4 v221, s[16:19], s63 offen offset:1024 lds\nv_mfma_f32_16x16x32_bf16 v[68:71], v[160:163], a[8:11], 0\nR 178,187,12288\n"
"M 68,164,24\nv_mfma_f32_16x16x32_bf16 v[52:55], v[160:163], a[4:7], 0\nR 180,187,16384\nM 52,164,20\ns_add_u32 m0, s50, 90112\nv_mfma_f32_16x16x32_bf16 v[36:39], v[160:163], a[0:3], 0\n"
"R 182,187,20480\nv_mov_b32 v248, 0\nM 36,164,16\nG 4\nv_mov_b32 v249, 0\nv_mfma_f32_16x16x32_bf16 v[40:43], v[168:171], a[0:3], 0\n"
"R 160,184,24576\ns_add_u32 s61, s82, 845840\ns_mov_b64 exec, 0xf\nbuffer_atomic_or_x2 v[248:249], v250, s[20:23], s61 offen sc0 sc1\ns_mov_b64 exec, -1\nM 40,172,16\n"
"s_add_u32 s63, s56, 262144\nv_mfma_f32_16x16x32_bf16 v[56:59], v[168:171], a[4:7], 0\nR 162,184,28672\nM 56,172,20\nbuffer_load_dwordx4 v220, s[16:19], s63 offen lds\nv_mfma_f32_16x16x32_bf16 v[72:75], v[168:171], a[8:11], 0\n"
"R 164,184,32768\nM 72,172,24\nv_mfma_f32_16x16x32_bf16 v[88:91], v[168:171], a[12:15], 0\nR 166,184,36864\nM 88,172,28\nG 4\n"
"v_mfma_f32_16x16x32_bf16 v[92:95], v[176:179], a[12:15], 0\nR 168,185,24576\nM 92,180,28\nbuffer_load_dwordx4 v221, s[16:19], s63 offen offset:1024 lds\nv_mfma_f32_16x16x32_bf16 v[76:79], v[176:179], a[8:11], 0\nR 170,185,28672\n"
"s_add_u32 s63, s56, 786432\nM 76,180,24\ns_add_u32 m0, s50, 98304\nv_mfma_f32_16x16x32_bf16 v[60:63], v[176:179], a[4:7], 0\nR 172,185,32768\nM 60,180,20\n"
"buffer_load_dwordx4 v220, s[16:19], s63 offen lds\nv_mfma_f32_16x16x32_bf16 v[44:47], v[176:179], a[0:3], 0\nR 174,185,36864\nM 44,180,16\nG 4\nM 32,160,32\n"
"R 176,186,24576\nM 32,164,48\nbuffer_load_dwordx4 v221, s[16:19], s63 offen offset:1024 lds\nM 48,160,36\nR 178,186,28672\ns_add_u32 s63, s56, 327680\n"
"M 48,164,52\ns_add_u32 m0, s50, 106496\nM 64,160,40\nR 180,186,32768\nM 64,164,56\nbuffer_load_dwordx4 v220, s[16:19], s63 offen lds\n"
"M 80,160,44\nR 182,186,36864\nM 80,164,60\nG 4\nM 84,168,44\nR 160,187,24576\n"
"M 84,172,60\nbuffer_load_dwordx4 v221, s[16:19], s63 offen offset:1024 lds\nM 68,168,40\nR 162,187,28672\ns_add_u32 s63, s56, 851968\nM 68,172,56\n"
"s_add_u32 m0, s50, 114688\nM 52,168,36\nR 164,187,32768\nM 52,172,52\nbuffer_load_dwordx4 v220, s[16:19], s63 offen lds\nM 36,168,32\n"
"R 166,187,36864\nM 36,172,48\nG 4\nM 40,176,32\nR 168,184,40960\nM 40,180,48\n"
"s_add_u32 s64, s68, 37752832\nM 56,176,36\nR 170,184,45056\ns_waitcnt vmcnt(7)\nv_cmp_eq_u64 s[86:87], s[46:47], v[248:249]\nM 56,180,52\n"
"buffer_load_dwordx4 v221, s[16:19], s63 offen offset:1024 lds\nM 72,176,40\nR 172,184,49152\ns_and_b32 s62, s86, 0xf\ns_cmp_eq_u32 s62, 0xf\ns_cbranch_scc0 Lq915\n"
"Lq916:\nM 72,180,56\ns_add_u32 s65, s68, 37867520\nM 88,176,44\nR 174,184,53248\ns_barrier\n"
"M 88,180,60\nG 4\nM 92,160,44\nR 176,185,40960\nM 92,164,60\nbuffer_load_dwordx4 a[160:163], v252, s[20:23], s64 offen sc1\n"
"M 76,160,40\nR 178,185,45056\nM 76,164,56\ns_add_u32 s66, s69, 37752832\nM 60,160,36\nR 180,185,49152\n"
"M 60,164,52\nbuffer_load_dwordx4 a[164:167], v252, s[20:23], s65 offen sc1\nM 44,160,32\nR 182,185,53248\nM 44,164,48\nG 4\n"
"M 32,168,64\nR 160,186,40960\nM 32,172,80\ns_add_u32 s67, s69, 37867520\nM 48,168,68\nR 162,186,45056\n"
"M 48,172,84\nbuffer_load_dwordx4 a[168:171], v252, s[20:23], s66 offen sc1\nM 64,168,72\nR 164,186,49152\nM 64,172,88\nM 80,168,76\n"
"R 166,186,53248\nM 80,172,92\nG 4\nM 84,176,76\nR 168,187,40960\nM 84,180,92\n"
"buffer_load_dwordx4 a[172:175], v252, s[20:23], s67 offen sc1\nM 68,176,72\nR 170,187,45056\nM 68,180,88\ns_add_u32 s64, s68, 37760000\nM 52,176,68\n"
"R 172,187,49152\nM 52,180,84\ns_add_u32 s65, s68, 37874688\nM 36,176,64\nR 174,187,53248\ns_add_u32 m0, s50, 0\n"
"M 36,180,80\nG 4\nM 40,160,64\nR 176,184,57344\ns_add_u32 s63, s57, 262144\nM 40,164,80\n"
"buffer_load_dwordx4 a[176:179], v252, s[20:23], s64 offen sc1\nM 56,160,68\nR 178,184,61440\nM 56,164,84\ns_add_u32 s66, s69, 37760000\nbuffer_load_dwordx4 v220, s[16:19], s63 offen lds\n"
"M 72,160,72\nR 180,188,0\nM 72,164,88\nbuffer_load_dwordx4 a[180:183], v252, s[20:23], s65 offen sc1\nM 88,160,76\nR 182,188,4096\n"
"M 88,164,92\nbuffer_load_dwordx4 v221, s[16:19], s63 offen offset:1024 lds\nG 4\nM 92,168,76\nR 160,185,57344\ns_add_u32 m0, s50, 8192\n"
"M 92,172,92\ns_add_u32 s67, s69, 37874688\nM 76,168,72\nR 162,185,61440\ns_add_u32 s63, s57, 786432\nM 76,172,88\n"
"buffer_load_dwordx4 a[184:187], v252, s[20:23], s66 offen sc1\nbuffer_load_dwordx4 v220, s[16:19], s63 offen lds\nM 60,168,68\nR 164,189,0\nM 60,172,84\nbuffer_load_dwordx4 v221, s[16:19], s63 offen offset:1024 lds\n"
"M 44,168,64\nR 166,189,4096\ns_add_u32 m0, s50, 16384\nM 44,172,80\nG 4\nM 32,176,96\n"
"R 168,186,57344\ns_add_u32 s63, s57, 327680\nM 32,180,112\nbuffer_load_dwordx4 a[188:191], v252, s[20:23], s67 offen sc1\nM 48,176,100\nR 170,186,61440\n"
"M 48,180,116\ns_add_u32 s64, s68, 37753856\nbuffer_load_dwordx4 v220, s[16:19], s63 offen lds\nM 64,176,104\nR 172,190,0\nM 64,180,120\n"
"buffer_load_dwordx4 v221, s[16:19], s63 offen offset:1024 lds\nM 80,176,108\nR 174,190,4096\ns_add_u32 m0, s50, 24576\nM 80,180,124\nG 4\n"
"M 84,160,108\nR 176,187,57344\ns_waitcnt vmcnt(18)\ns_barrier\nM 84,164,124\nbuffer_load_dwordx4 a[192:195], v252, s[20:23], s64 offen sc1\n"
"M 68,160,104\nR 178,187,61440\ns_add_u32 s63, s57, 851968\nM 68,164,120\ns_add_u32 s65, s68, 37868544\nM 52,160,100\n"
"R 180,191,0\nbuffer_load_dwordx4 v220, s[16:19], s63 offen lds\nM 52,164,116\ns_add_u32 s66, s69, 37753856\nM 36,160,96\nR 182,191,4096\n"
"M 36,164,112\nG 4\nM 40,168,96\nR 160,188,8192\nbuffer_load_dwordx4 v221, s[16:19], s63 offen offset:1024 lds\nM 40,172,112\n"
"buffer_load_dwordx4 a[196:199], v252, s[20:23], s65 offen sc1\nM 56,168,100\nR 162,188,12288\nM 56,172,116\ns_add_u32 s67, s69, 37868544\nM 72,168,104\n"
"R 164,188,16384\nM 72,172,120\nbuffer_load_dwordx4 a[200:203], v252, s[20:23], s66 offen sc1\nM 88,168,108\nR 166,188,20480\nM 88,172,124\n"
"G 4\nM 92,176,108\nR 168,189,8192\nM 92,180,124\ns_add_u32 s64, s68, 37761024\nM 76,176,104\n"
"R 170,189,12288\nM 76,180,120\nbuffer_load_dwordx4 a[204:207], v252, s[20:23], s67 offen sc1\nM 60,176,100\nR 172,189,16384\nM 60,180,116\n"
"M 44,176,96\nR 174,189,20480\nM 44,180,112\nG 4\nM 32,160,128\nR 176,190,8192\n"
"M 32,164,144\nbuffer_load_dwordx4 a[208:211], v252, s[20:23], s64 offen sc1\nM 48,160,132\nR 178,190,12288\nM 48,164,148\ns_add_u32 s65, s68, 37875712\n"
"M 64,160,136\nR 180,190,16384\nM 64,164,152\ns_add_u32 s66, s69, 37761024\nM 80,160,140\nR 182,190,20480\n"
"M 80,164,156\nG 4\nM 84,168,140\nR 160,191,8192\nM 84,172,156\nbuffer_load_dwordx4 a[212:215], v252, s[20:23], s65 offen sc1\n"
"M 68,168,136\nR 162,191,12288\nM 68,172,152\ns_add_u32 s67, s69, 37875712\nM 52,168,132\nR 164,191,16384\n"
"M 52,172,148\nbuffer_load_dwordx4 a[216:219], v252, s[20:23], s66 offen sc1\nM 36,168,128\nR 166,191,20480\nM 36,172,144\nG 4\n"
"M 40,176,128\nR 168,188,24576\nM 40,180,144\nM 56,176,132\nR 170,188,28672\nM 56,180,148\n"
"buffer_load_dwordx4 a[220:223], v252, s[20:23], s67 offen sc1\nM 72,176,136\nR 172,188,32768\ns_add_u32 s63, s58, 262144\nM 72,180,152\ns_add_u32 m0, s50, 32768\n"
"M 88,176,140\nR 174,188,36864\nM 88,180,156\nG 4\nM 92,160,140\nR 176,189,24576\n"
"M 92,164,156\nbuffer_load_dwordx4 v220, s[16:19], s63 offen lds\nM 76,160,136\nR 178,189,28672\nM 76,164,152\nM 60,160,132\n"
"R 180,189,32768\nM 60,164,148\nbuffer_load_dwordx4 v221, s[16:19], s63 offen offset:1024 lds\nM 44,160,128\nR 182,189,36864\nM 44,164,144\n"
"s_waitcnt vmcnt(9)\ns_barrier\nG 4\nM 96,168,160\nR 160,190,24576\nM 96,172,176\n"
"s_add_u32 m0, s50, 40960\nM 112,168,164\nR 162,190,28672\nM 112,172,180\ns_add_u32 s63, s58, 786432\nM 128,168,168\n"
"R 164,190,32768\nv_lshrrev_b32 v230, 8, v252\nM 128,172,184\nbuffer_load_dwordx4 v220, s[16:19], s63 offen lds\nM 144,168,172\nR 166,190,36864\n"
"v_lshlrev_b32 v230, 3, v230\nM 144,172,188\nG 4\nM 148,176,172\nR 168,191,24576\nM 148,180,188\n"
"buffer_load_dwordx2 v[232:233], v230, s[36:39], s53 offen offset:0\nM 132,176,168\nR 170,191,28672\nM 132,180,184\nbuffer_load_dwordx2 v[234:235], v230, s[36:39], s53 offen offset:32\nM 116,176,164\n"
"R 172,191,32768\nM 116,180,180\nbuffer_load_dwordx4 v221, s[16:19], s63 offen offset:1024 lds\nM 100,176,160\nR 174,191,36864\nM 100,180,176\n"
"G 4\nM 104,160,160\nR 176,188,40960\nM 104,164,176\ns_add_u32 m0, s50, 49152\nM 120,160,164\n"
"R 178,188,45056\nM 120,164,180\ns_add_u32 s63, s58, 327680\nM 136,160,168\nR 180,188,49152\nbuffer_load_dwordx2 v[236:237], v230, s[36:39], s53 offen offset:64\n"
"M 136,164,184\nbuffer_load_dwordx4 v220, s[16:19], s63 offen lds\nM 152,160,172\nR 182,188,53248\nM 152,164,188\nbuffer_load_dwordx2 v[238:239], v230, s[36:39], s53 offen offset:96\n"
"G 4\nM 156,168,172\nR 160,189,40960\nv_bfe_u32 v242, v252, 4, 4\nM 156,172,188\nv_lshrrev_b32 v243, 8, v252\n"
"v_lshlrev_b32 v243, 2, v243\nM 140,168,168\nR 162,189,45056\nv_sub_u32 v242, v242, v243\nM 140,172,184\nv_lshlrev_b32 v243, 4, v242\n"
"s_mov_b64 s[60:61], 0x3f80\nv_lshlrev_b64 v[208:209], v243, s[60:61]\nM 124,168,164\nR 164,189,49152\nM 124,172,180\nbuffer_load_dwordx4 v221, s[16:19], s63 offen offset:1024 lds\n"
"M 108,168,160\nR 166,189,53248\nM 108,172,176\nG 4\nM 96,176,192\nR 168,190,40960\n"
"s_waitcnt vmcnt(13)\nM 96,180,208\ns_add_u32 m0, s50, 57344\nM 112,176,196\nR 170,190,45056\ns_waitcnt vmcnt(12)\n"
"M 112,180,212\ns_add_u32 s63, s58, 851968\nM 128,176,200\nR 172,190,49152\ns_waitcnt vmcnt(11)\nM 128,180,216\n"
"buffer_load_dwordx4 v220, s[16:19], s63 offen lds\nM 144,176,204\nR 174,190,53248\ns_waitcnt vmcnt(11)\nM 144,180,220\nG 4\n"
"M 148,160,204\nR 176,191,40960\nM 148,164,220\nv_cmp_gt_u32 s[60:61], 4, v242\ns_nop 1\nv_cndmask_b32 v212, 0, v208, s[60:61]\n"
"v_cndmask_b32 v208, 0, v208, s[60:61]\nv_cndmask_b32 v213, 0, v209, s[60:61]\nv_cndmask_b32 v209, 0, v209, s[60:61]\nM 132,160,200\nR 178,191,45056\nM 132,164,216\n"
"M 116,160,196\nR 180,191,49152\nM 116,164,212\nbuffer_load_dwordx4 v221, s[16:19], s63 offen offset:1024 lds\nM 100,160,192\nR 182,191,53248\n"
"M 100,164,208\nG 4\nM 104,168,192\nR 160,184,0\nM 104,172,208\ns_add_u32 m0, s50, 65536\n"
"M 120,168,196\nR 162,184,4096\nM 120,172,212\ns_add_u32 s63, s59, 0\nM 136,168,200\nR 164,184,8192\n"
"M 136,172,216\nbuffer_load_dwordx4 v220, s[16:19], s63 offen lds\nM 152,168,204\nR 166,184,12288\nM 152,172,220\nG 4\n"
"M 156,176,204\nR 168,185,0\nM 156,180,220\nM 140,176,200\nR 170,185,4096\nM 140,180,216\n"
"buffer_load_dwordx4 v221, s[16:19], s63 offen offset:1024 lds\nM 124,176,196\nR 172,185,8192\nM 124,180,212\nM 108,176,192\nR 174,185,12288\n"
"M 108,180,208\nG 4\nM 0,160,160\nR 176,186,0\nM 0,164,176\ns_mov_b32 m0, s51\n"
"M 16,160,164\nR 178,186,4096\nds_write_addtid_b32 v128 offset:0\nM 16,164,180\nds_write_addtid_b32 v129 offset:256\nds_write_addtid_b32 v130 offset:512\n"
"v_mfma_f32_16x16x32_bf16 a[224:227], v[160:163], a[168:171], a[224:227]\nR 180,186,8192\nds_write_addtid_b32 v131 offset:768\nv_mfma_f32_16x16x32_bf16 a[224:227], v[164:167], a[184:187], a[224:227]\nds_write_addtid_b32 v132 offset:1024\nds_write_addtid_b32 v133 offset:1280\n"
"v_mfma_f32_16x16x32_bf16 a[240:243], v[160:163], a[172:175], a[240:243]\nR 182,186,12288\nds_write_addtid_b32 v134 offset:1536\nv_mfma_f32_16x16x32_bf16 a[240:243], v[164:167], a[188:191], a[240:243]\nds_write_addtid_b32 v135 offset:1792\nds_write_addtid_b32 v136 offset:2048\n"
"G 13\nv_mfma_f32_16x16x32_bf16 a[244:247], v[168:171], a[172:175], a[244:247]\nR 160,187,0\nds_write_addtid_b32 v137 offset:2304\nv_mfma_f32_16x16x32_bf16 a[244:247], v[172:175], a[188:191], a[244:247]\nds_write_addtid_b32 v138 offset:2560\n"
"ds_write_addtid_b32 v139 offset:2816\nv_mfma_f32_16x16x32_bf16 a[228:231], v[168:171], a[168:171], a[228:231]\nR 162,187,4096\nds_write_addtid_b32 v140 offset:3072\nv_mfma_f32_16x16x32_bf16 a[228:231], v[172:175], a[184:187], a[228:231]\nds_write_addtid_b32 v141 offset:3328\n"
"ds_write_addtid_b32 v142 offset:3584\nM 20,168,164\nR 164,187,8192\nds_write_addtid_b32 v143 offset:3840\nM 20,172,180\nds_write_addtid_b32 v144 offset:4096\n"
"ds_write_addtid_b32 v145 offset:4352\nM 4,168,160\nR 166,187,12288\nds_write_addtid_b32 v146 offset:4608\nM 4,172,176\nds_write_addtid_b32 v147 offset:4864\n"
"ds_write_addtid_b32 v148 offset:5120\nG 15\nM 8,176,160\nR 168,184,16384\nds_write_addtid_b32 v149 offset:5376\nM 8,180,176\n"
"ds_write_addtid_b32 v150 offset:5632\nds_write_addtid_b32 v151 offset:5888\nM 24,176,164\nR 170,184,20480\nds_write_addtid_b32 v152 offset:6144\nM 24,180,180\n"
"ds_write_addtid_b32 v153 offset:6400\nds_write_addtid_b32 v154 offset:6656\nv_mfma_f32_16x16x32_bf16 a[232:235], v[176:179], a[168:171], a[232:235]\nR 172,184,24576\nds_write_addtid_b32 v155 offset:6912\nv_mfma_f32_16x16x32_bf16 a[232:235], v[180:183], a[184:187], a[232:235]\n"
"ds_write_addtid_b32 v156 offset:7168\nds_write_addtid_b32 v157 offset:7424\nds_write_addtid_b32 v158 offset:7680\nds_write_addtid_b32 v159 offset:7936\nv_mfma_f32_16x16x32_bf16 a[248:251], v[176:179], a[172:175], a[248:251]\nR 174,184,28672\n"
"s_add_u32 m0, s50, 73728\nv_mfma_f32_16x16x32_bf16 a[248:251], v[180:183], a[188:191], a[248:251]\ns_add_u32 s63, s59, 524288\nG 15\nv_mfma_f32_16x16x32_bf16 a[252:255], v[160:163], a[172:175], a[252:255]\nR 176,185,16384\n"
"s_waitcnt vmcnt(8) lgkmcnt(2)\ns_barrier\nv_mfma_f32_16x16x32_bf16 a[252:255], v[164:167], a[188:191], a[252:255]\nbuffer_load_dwordx4 v220, s[16:19], s63 offen lds\nds_read2st64_b32 v[128:129], v225 offset0:0 offset1:1\nv_mfma_f32_16x16x32_bf16 a[236:239], v[160:163], a[168:171], a[236:239]\n"
"R 178,185,20480\nds_read2st64_b32 v[130:131], v225 offset0:2 offset1:3\nv_mfma_f32_16x16x32_bf16 a[236:239], v[164:167], a[184:187], a[236:239]\nds_read2st64_b32 v[132:133], v225 offset0:4 offset1:5\nds_read2st64_b32 v[134:135], v225 offset0:6 offset1:7\nM 28,160,164\n"
"R 180,185,24576\nds_read2st64_b32 v[136:137], v225 offset0:8 offset1:9\nM 28,164,180\nbuffer_load_dwordx4 v221, s[16:19], s63 offen offset:1024 lds\nds_read2st64_b32 v[138:139], v225 offset0:10 offset1:11\nM 12,160,160\n"
"R 182,185,28672\ns_add_u32 m0, s50, 81920\nds_read2st64_b32 v[140:141], v225 offset0:12 offset1:13\nM 12,164,176\nds_read2st64_b32 v[142:143], v225 offset0:14 offset1:15\nG 12\n"
"G 10\nv_add_f32 v96, v96, v128\nM 0,168,192\nR 160,186,16384\ns_add_u32 s63, s59, 65536\nv_add_f32 v97, v97, v129\n"
"M 0,172,208\nbuffer_load_dwordx4 v220, s[16:19], s63 offen lds\nG 9\nv_add_f32 v98, v98, v130\nM 16,168,196\nR 162,186,20480\n"
"v_add_f32 v99, v99, v131\nG 9\nv_add_f32 v100, v100, v132\nM 16,172,212\nds_read2st64_b32 v[144:145], v225 offset0:16 offset1:17\nv_add_f32 v101, v101, v133\n"
"G 9\nv_add_f32 v102, v102, v134\nv_mfma_f32_16x16x32_bf16 a[224:227], v[168:171], a[200:203], a[224:227]\nR 164,186,24576\nv_add_f32 v103, v103, v135\nG 8\n"
"v_add_f32 v104, v104, v136\nv_mfma_f32_16x16x32_bf16 a[224:227], v[172:175], a[216:219], a[224:227]\nbuffer_load_dwordx4 v221, s[16:19], s63 offen offset:1024 lds\nv_add_f32 v105, v105, v137\nv_mfma_f32_16x16x32_bf16 a[240:243], v[168:171], a[204:207], a[240:243]\nR 166,186,28672\n"
"s_add_u32 m0, s50, 90112\nG 8\nv_add_f32 v106, v106, v138\nv_mfma_f32_16x16x32_bf16 a[240:243], v[172:175], a[220:223], a[240:243]\nds_read2st64_b32 v[146:147], v225 offset0:18 offset1:19\nG 8\n"
"v_add_f32 v107, v107, v139\nv_mfma_f32_16x16x32_bf16 a[244:247], v[176:179], a[204:207], a[244:247]\nR 168,187,16384\ns_add_u32 s63, s59, 589824\nG 8\nv_add_f32 v108, v108, v140\n"
"v_mfma_f32_16x16x32_bf16 a[244:247], v[180:183], a[220:223], a[244:247]\nbuffer_load_dwordx4 v220, s[16:19], s63 offen lds\nv_add_f32 v109, v109, v141\nv_mfma_f32_16x16x32_bf16 a[228:231], v[176:179], a[200:203], a[228:231]\nR 170,187,20480\nG 8\n"
"v_add_f32 v110, v110, v142\nv_add_f32 v111, v111, v143\nv_mfma_f32_16x16x32_bf16 a[228:231], v[180:183], a[216:219], a[228:231]\nds_read2st64_b32 v[148:149], v225 offset0:20 offset1:21\nG 6\nv_add_f32 v112, v112, v144\n"
"v_add_f32 v113, v113, v145\nM 20,176,196\nR 172,187,24576\nds_read2st64_b32 v[150:151], v225 offset0:22 offset1:23\nM 20,180,212\nbuffer_load_dwordx4 v221, s[16:19], s63 offen offset:1024 lds\n"
"G 5\nv_add_f32 v114, v114, v146\nM 4,176,192\nR 174,187,28672\ns_add_u32 s63, s59, 131072\nv_add_f32 v115, v115, v147\n"
"M 4,180,208\nds_read2st64_b32 v[152:153], v225 offset0:24 offset1:25\nds_read2st64_b32 v[154:155], v225 offset0:26 offset1:27\nM 8,160,192\nR 176,184,32768\nds_read2st64_b32 v[156:157], v225 offset0:28 offset1:29\n"
"M 8,164,208\ns_add_u32 m0, s50, 98304\nds_read2st64_b32 v[158:159], v225 offset0:30 offset1:31\nG 8\nv_add_f32 v116, v116, v148\nM 24,160,196\n"
"R 178,184,36864\nv_add_f32 v117, v117, v149\nG 7\nv_add_f32 v118, v118, v150\nM 24,164,212\nbuffer_load_dwordx4 v220, s[16:19], s63 offen lds\n"
"v_add_f32 v119, v119, v151\nG 5\nv_add_f32 v120, v120, v152\nv_add_f32 v121, v121, v153\nG 4\nv_add_f32 v122, v122, v154\n"
"v_add_f32 v123, v123, v155\nG 2\nv_add_f32 v124, v124, v156\nv_add_f32 v125, v125, v157\nG 1\nv_add_f32 v126, v126, v158\n"
"v_add_f32 v127, v127, v159\nv_mfma_f32_16x16x32_bf16 a[232:235], v[160:163], a[200:203], a[232:235]\nR 180,184,40960\nv_cvt_pk_bf16_f32 v96, v96, v97\nv_cvt_pk_bf16_f32 v97, v98, v99\nv_mfma_f32_16x16x32_bf16 a[232:235], v[164:167], a[216:219], a[232:235]\n"
"v_cvt_pk_bf16_f32 v98, v100, v101\nv_cvt_pk_bf16_f32 v99, v102, v103\nv_cvt_pk_bf16_f32 v104, v104, v105\nv_mfma_f32_16x16x32_bf16 a[248:251], v[160:163], a[204:207], a[248:251]\nR 182,184,45056\nv_cvt_pk_bf16_f32 v105, v106, v107\n"
"v_cvt_pk_bf16_f32 v106, v108, v109\nv_mfma_f32_16x16x32_bf16 a[248:251], v[164:167], a[220:223], a[248:251]\nv_cvt_pk_bf16_f32 v107, v110, v111\nv_cvt_pk_bf16_f32 v112, v112, v113\nv_cvt_pk_bf16_f32 v113, v114, v115\nv_mfma_f32_16x16x32_bf16 a[252:255], v[168:171], a[204:207], a[252:255]\n"
"R 160,185,32768\nv_cvt_pk_bf16_f32 v114, v116, v117\nv_cvt_pk_bf16_f32 v115, v118, v119\nv_mfma_f32_16x16x32_bf16 a[252:255], v[172:175], a[220:223], a[252:255]\nbuffer_load_dwordx4 v221, s[16:19], s63 offen offset:1024 lds\nv_cvt_pk_bf16_f32 v120, v120, v121\n"
"v_mfma_f32_16x16x32_bf16 a[236:239], v[168:171], a[200:203], a[236:239]\nR 162,185,36864\ns_add_u32 s63, s59, 655360\nv_cvt_pk_bf16_f32 v121, v122, v123\nv_mfma_f32_16x16x32_bf16 a[236:239], v[172:175], a[216:219], a[236:239]\ns_add_u32 m0, s50, 106496\n"
"s_bfe_u32 s100, s56, 0x20007\ns_mul_i32 s100, s100, 49152\ns_add_u32 s100, s100, s72\ns_add_u32 s100, s100, 65011712\nv_cvt_pk_bf16_f32 v122, v124, v125\nv_cvt_pk_bf16_f32 v123, v126, v127\n"
"M 28,168,196\nR 164,185,40960\nbuffer_store_dwordx4 v[96:99], v252, s[20:23], s100 offen offset:0 sc1\nM 28,172,212\nbuffer_load_dwordx4 v220, s[16:19], s63 offen lds\nbuffer_store_dwordx4 v[104:107], v252, s[20:23], s100 offen offset:1024 sc1\n"
"M 12,168,192\nR 166,185,45056\nbuffer_store_dwordx4 v[112:115], v252, s[20:23], s100 offen offset:2048 sc1\nM 12,172,208\ns_waitcnt vmcnt(14)\ns_barrier\n"
"buffer_store_dwordx4 v[120:123], v252, s[20:23], s100 offen offset:3072 sc1\nG 4\nM 32,176,160\nR 168,186,32768\nM 32,180,176\nbuffer_load_dwordx4 v221, s[16:19], s63 offen offset:1024 lds\n"
"v_xor_b32 v240, 0x2000, v225\nM 48,176,164\nR 170,186,36864\ns_add_u32 s63, s59, 196608\nds_write2st64_b32 v240, a224, a225 offset0:0 offset1:1\nM 48,180,180\n"
"s_add_u32 m0, s50, 114688\nds_write2st64_b32 v240, a226, a227 offset0:2 offset1:3\nM 64,176,168\nR 172,186,40960\nds_write2st64_b32 v240, a228, a229 offset0:4 offset1:5\nM 64,180,184\n"
"buffer_load_dwordx4 v220, s[16:19], s63 offen lds\nds_write2st64_b32 v240, a230, a231 offset0:6 offset1:7\nM 80,176,172\nR 174,186,45056\nv_mfma_f32_16x16x32_bf16 v[96:99], v[208:211], v[192:195], 0\nds_write2st64_b32 v240, a232, a233 offset0:8 offset1:9\n"
"M 80,180,188\nv_mfma_f32_16x16x32_bf16 v[100:103], v[210:213], v[192:195], 0\nds_write2st64_b32 v240, a234, a235 offset0:10 offset1:11\nG 10\nM 84,160,172\nR 176,187,32768\n"
"v_mfma_f32_16x16x32_bf16 v[104:107], v[208:211], v[196:199], 0\nds_write2st64_b32 v240, a236, a237 offset0:12 offset1:13\nM 84,164,188\nv_mfma_f32_16x16x32_bf16 v[108:111], v[210:213], v[196:199], 0\nbuffer_load_dwordx4 v221, s[16:19], s63 offen offset:1024 lds\nds_write2st64_b32 v240, a238, a239 offset0:14 offset1:15\n"
"M 68,160,168\nR 178,187,36864\nv_mfma_f32_16x16x32_bf16 v[112:115], v[208:211], v[200:203], 0\ns_add_u32 m0, s50, 0\ns_add_u32 s63, s59, 720896\nds_write2st64_b32 v240, a240, a241 offset0:16 offset1:17\n"
"M 68,164,184\nv_mfma_f32_16x16x32_bf16 v[116:119], v[210:213], v[200:203], 0\nbuffer_load_dwordx4 v220, s[16:19], s63 offen lds\nds_write2st64_b32 v240, a242, a243 offset0:18 offset1:19\nM 52,160,164\nR 180,187,40960\n"
"v_mfma_f32_16x16x32_bf16 v[120:123], v[208:211], v[204:207], 0\nds_write2st64_b32 v240, a244, a245 offset0:20 offset1:21\nM 52,164,180\nv_mfma_f32_16x16x32_bf16 v[124:127], v[210:213], v[204:207], 0\nbuffer_load_dwordx4 v221, s[16:19], s63 offen offset:1024 lds\nds_write2st64_b32 v240, a246, a247 offset0:22 offset1:23\n"
"M 36,160,160\nR 182,187,45056\nv_mfma_f32_16x16x32_bf16 v[96:99], v[208:211], v[232:235], v[96:99]\ns_add_u32 m0, s50, 8192\nds_write2st64_b32 v240, a248, a249 offset0:24 offset1:25\nM 36,164,176\n"
"v_mfma_f32_16x16x32_bf16 v[100:103], v[210:213], v[232:235], v[100:103]\nds_write2st64_b32 v240, a250, a251 offset0:26 offset1:27\nG 14\nM 40,168,160\nR 160,184,49152\nv_mfma_f32_16x16x32_bf16 v[104:107], v[208:211], v[236:239], v[104:107]\n"
"s_add_u32 s63, s59, 393216\nds_write2st64_b32 v240, a252, a253 offset0:28 offset1:29\nM 40,172,176\nv_mfma_f32_16x16x32_bf16 v[108:111], v[210:213], v[236:239], v[108:111]\nbuffer_load_dwordx4 v220, s[16:19], s63 offen lds\nds_write2st64_b32 v240, a254, a255 offset0:30 offset1:31\n"
"M 56,168,164\nR 162,184,53248\nv_mfma_f32_16x16x32_bf16 v[112:115], v[208:211], v[232:235], v[112:115]\nM 56,172,180\nv_mfma_f32_16x16x32_bf16 v[116:119], v[210:213], v[232:235], v[116:119]\nbuffer_load_dwordx4 v221, s[16:19], s63 offen offset:1024 lds\n"
"M 72,168,168\nR 164,184,57344\nv_mfma_f32_16x16x32_bf16 v[120:123], v[208:211], v[236:239], v[120:123]\ns_add_u32 m0, s50, 16384\ns_add_u32 s63, s59, 458752\nM 72,172,184\n"
"v_mfma_f32_16x16x32_bf16 v[124:127], v[210:213], v[236:239], v[124:127]\nbuffer_load_dwordx4 v220, s[16:19], s63 offen lds\nM 88,168,172\nR 166,184,61440\nM 88,172,188\nG 8\n"
"M 92,176,172\nR 168,185,49152\ns_waitcnt vmcnt(19) lgkmcnt(4)\ns_barrier\nM 92,180,188\nds_read2st64_b32 v[128:129], v225 offset0:0 offset1:1\n"
"ds_read2st64_b32 v[130:131], v225 offset0:2 offset1:3\nM 76,176,168\nR 170,185,53248\nds_read2st64_b32 v[132:133], v225 offset0:4 offset1:5\nM 76,180,184\nds_read2st64_b32 v[134:135], v225 offset0:6 offset1:7\n"
"ds_read2st64_b32 v[136:137], v225 offset0:8 offset1:9\nM 60,176,164\nR 172,185,57344\nds_read2st64_b32 v[138:139], v225 offset0:10 offset1:11\nM 60,180,180\nbuffer_load_dwordx4 v221, s[16:19], s63 offen offset:1024 lds\n"
"ds_read2st64_b32 v[140:141], v225 offset0:12 offset1:13\nM 44,176,160\nR 174,185,61440\nds_read2st64_b32 v[142:143], v225 offset0:14 offset1:15\nM 44,180,176\nds_read2st64_b32 v[144:145], v225 offset0:16 offset1:17\n"
"G 13\nG 11\nv_add_f32 v0, v0, v128\nM 32,160,192\nR 176,186,49152\nv_add_f32 v1, v1, v129\n"
"G 11\nv_add_f32 v2, v2, v130\nM 32,164,208\ns_add_u32 m0, s50, 24576\nds_read2st64_b32 v[146:147], v225 offset0:18 offset1:19\nv_add_f32 v3, v3, v131\n"
"M 48,160,196\nR 178,186,53248\nG 11\nv_add_f32 v4, v4, v132\nv_add_f32 v5, v5, v133\nM 48,164,212\n"
"s_add_u32 s63, s59, 262144\nds_read2st64_b32 v[148:149], v225 offset0:20 offset1:21\nG 11\nv_add_f32 v6, v6, v134\nM 64,160,200\nR 180,186,57344\n"
"v_add_f32 v7, v7, v135\nG 11\nv_add_f32 v8, v8, v136\nM 64,164,216\nbuffer_load_dwordx4 v220, s[16:19], s63 offen lds\nv_add_f32 v9, v9, v137\n"
"M 80,160,204\nR 182,186,61440\nG 10\nv_add_f32 v10, v10, v138\nv_add_f32 v11, v11, v139\nM 80,164,220\n"
"ds_read2st64_b32 v[150:151], v225 offset0:22 offset1:23\nG 9\nv_add_f32 v12, v12, v140\nM 84,168,204\nR 160,187,49152\nv_add_f32 v13, v13, v141\n"
"G 9\nv_add_f32 v14, v14, v142\nM 84,172,220\nds_read2st64_b32 v[152:153], v225 offset0:24 offset1:25\nv_add_f32 v15, v15, v143\nG 9\n"
"v_add_f32 v16, v16, v144\nM 68,168,200\nR 162,187,53248\nv_add_f32 v17, v17, v145\nG 8\nv_add_f32 v18, v18, v146\n"
"M 68,172,216\nds_read2st64_b32 v[154:155], v225 offset0:26 offset1:27\nv_add_f32 v19, v19, v147\nG 7\nv_add_f32 v20, v20, v148\nM 52,168,196\n"
"R 164,187,57344\nds_read2st64_b32 v[156:157], v225 offset0:28 offset1:29\nv_add_f32 v21, v21, v149\nM 52,172,212\nbuffer_load_dwordx4 v221, s[16:19], s63 offen offset:1024 lds\nG 6\n"
"v_add_f32 v22, v22, v150\nM 36,168,192\nR 166,187,61440\ns_add_u32 s63, s59, 786432\nv_add_f32 v23, v23, v151\nM 36,172,208\n"
"ds_read2st64_b32 v[158:159], v225 offset0:30 offset1:31\nG 6\nv_add_f32 v24, v24, v152\nM 40,176,192\nR 168,188,0\nv_add_f32 v25, v25, v153\n"
"M 40,180,208\ns_add_u32 m0, s50, 32768\nG 5\nv_add_f32 v26, v26, v154\nv_add_f32 v27, v27, v155\nM 56,176,196\n"
"R 170,188,4096\nG 4\nv_add_f32 v28, v28, v156\nv_add_f32 v29, v29, v157\nM 56,180,212\nbuffer_load_dwordx4 v220, s[16:19], s63 offen lds\n"
"G 2\nv_add_f32 v30, v30, v158\nv_add_f32 v31, v31, v159\nM 72,176,200\nR 172,188,8192\nv_cvt_pk_bf16_f32 v0, v0, v1\n"
"v_cvt_pk_bf16_f32 v1, v2, v3\nM 72,180,216\nv_cvt_pk_bf16_f32 v2, v4, v5\nv_cvt_pk_bf16_f32 v3, v6, v7\nv_cvt_pk_bf16_f32 v8, v8, v9\nM 88,176,204\n"
"R 174,188,12288\nv_cvt_pk_bf16_f32 v9, v10, v11\nv_cvt_pk_bf16_f32 v10, v12, v13\nM 88,180,220\nv_cvt_pk_bf16_f32 v11, v14, v15\nv_cvt_pk_bf16_f32 v16, v16, v17\n"
"v_cvt_pk_bf16_f32 v17, v18, v19\nM 92,160,204\nR 176,189,0\nv_cvt_pk_bf16_f32 v18, v20, v21\nv_cvt_pk_bf16_f32 v19, v22, v23\nM 92,164,220\n"
"buffer_load_dwordx4 v221, s[16:19], s63 offen offset:1024 lds\nv_cvt_pk_bf16_f32 v24, v24, v25\nM 76,160,200\nR 178,189,4096\nv_cvt_pk_bf16_f32 v25, v26, v27\nv_cvt_pk_bf16_f32 v26, v28, v29\n"
"M 76,164,216\ns_bfe_u32 s100, s57, 0x20007\ns_mul_i32 s100, s100, 49152\ns_add_u32 s100, s100, s72\ns_add_u32 s100, s100, 65028096\nv_cvt_pk_bf16_f32 v27, v30, v31\n"
"M 60,160,196\nR 180,189,8192\nbuffer_store_dwordx4 v[0:3], v252, s[20:23], s100 offen offset:0 sc1\nM 60,164,212\nbuffer_store_dwordx4 v[8:11], v252, s[20:23], s100 offen offset:1024 sc1\nbuffer_store_dwordx4 v[16:19], v252, s[20:23], s100 offen offset:2048 sc1\n"
"M 44,160,192\nR 182,189,12288\nM 44,164,208\ns_waitcnt vmcnt(23)\ns_barrier\nG 4\n"
"M 96,168,0\nR 160,190,0\nbuffer_store_dwordx4 v[24:27], v252, s[20:23], s100 offen offset:3072 sc1\nM 96,172,16\ns_mov_b32 m0, s51\nM 112,168,4\n"
"R 162,190,4096\nds_write_addtid_b32 v64 offset:0\nM 112,172,20\nds_write_addtid_b32 v65 offset:256\nds_write_addtid_b32 v66 offset:512\nv_mfma_f32_16x16x32_bf16 v[128:131], v[168:171], a[8:11], 0\n"
"R 164,190,8192\nds_write_addtid_b32 v67 offset:768\nM 128,172,24\nds_write_addtid_b32 v68 offset:1024\nds_write_addtid_b32 v69 offset:1280\nv_mfma_f32_16x16x32_bf16 v[144:147], v[168:171], a[12:15], 0\n"
"R 166,190,12288\nds_write_addtid_b32 v70 offset:1536\nM 144,172,28\nds_write_addtid_b32 v71 offset:1792\nds_write_addtid_b32 v72 offset:2048\nG 13\n"
"v_mfma_f32_16x16x32_bf16 v[148:151], v[176:179], a[12:15], 0\nR 168,191,0\nds_write_addtid_b32 v73 offset:2304\nM 148,180,28\nds_write_addtid_b32 v74 offset:2560\nds_write_addtid_b32 v75 offset:2816\n"
"v_mfma_f32_16x16x32_bf16 v[132:135], v[176:179], a[8:11], 0\nR 170,191,4096\nds_write_addtid_b32 v76 offset:3072\nM 132,180,24\nds_write_addtid_b32 v77 offset:3328\nds_write_addtid_b32 v78 offset:3584\n"
"M 116,176,4\nR 172,191,8192\nds_write_addtid_b32 v79 offset:3840\nM 116,180,20\nds_write_addtid_b32 v80 offset:4096\nds_write_addtid_b32 v81 offset:4352\n"
"M 100,176,0\nR 174,191,12288\nds_write_addtid_b32 v82 offset:4608\nM 100,180,16\nds_write_addtid_b32 v83 offset:4864\nds_write_addtid_b32 v84 offset:5120\n"
"G 15\nM 104,160,0\nR 176,188,16384\nds_write_addtid_b32 v85 offset:5376\nM 104,164,16\nds_write_addtid_b32 v86 offset:5632\n"
"ds_write_addtid_b32 v87 offset:5888\nM 120,160,4\nR 178,188,20480\nds_write_addtid_b32 v88 offset:6144\nM 120,164,20\nds_write_addtid_b32 v89 offset:6400\n"
"ds_write_addtid_b32 v90 offset:6656\nv_mfma_f32_16x16x32_bf16 v[136:139], v[160:163], a[8:11], 0\nR 180,188,24576\nds_write_addtid_b32 v91 offset:6912\nM 136,164,24\nds_write_addtid_b32 v92 offset:7168\n"
"ds_write_addtid_b32 v93 offset:7424\nds_write_addtid_b32 v94 offset:7680\nds_write_addtid_b32 v95 offset:7936\nv_mfma_f32_16x16x32_bf16 v[152:155], v[160:163], a[12:15], 0\nR 182,188,28672\nM 152,164,28\n"
"G 15\nv_mfma_f32_16x16x32_bf16 v[156:159], v[168:171], a[12:15], 0\nR 160,189,16384\ns_waitcnt vmcnt(16) lgkmcnt(2)\ns_barrier\nM 156,172,28\n"
"ds_read2st64_b32 v[64:65], v225 offset0:0 offset1:1\nds_read2st64_b32 v[66:67], v225 offset0:2 offset1:3\nv_mfma_f32_16x16x32_bf16 v[140:143], v[168:171], a[8:11], 0\nR 162,189,20480\nds_read2st64_b32 v[68:69], v225 offset0:4 offset1:5\nM 140,172,24\n"
"ds_read2st64_b32 v[70:71], v225 offset0:6 offset1:7\nds_read2st64_b32 v[72:73], v225 offset0:8 offset1:9\nM 124,168,4\nR 164,189,24576\nds_read2st64_b32 v[74:75], v225 offset0:10 offset1:11\nM 124,172,20\n"
"ds_read2st64_b32 v[76:77], v225 offset0:12 offset1:13\nds_read2st64_b32 v[78:79], v225 offset0:14 offset1:15\nM 108,168,0\nR 166,189,28672\nds_read2st64_b32 v[80:81], v225 offset0:16 offset1:17\nM 108,172,16\n"
"ds_read2st64_b32 v[82:83], v225 offset0:18 offset1:19\nG 14\nG 12\nv_add_f32 v32, v32, v64\nM 96,176,32\nR 168,190,16384\n"
"v_add_f32 v33, v33, v65\nG 12\nv_add_f32 v34, v34, v66\nM 96,180,48\nds_read2st64_b32 v[84:85], v225 offset0:20 offset1:21\nv_add_f32 v35, v35, v67\n"
"G 11\nv_add_f32 v36, v36, v68\nM 112,176,36\nR 170,190,20480\nv_add_f32 v37, v37, v69\nG 11\n"
"v_add_f32 v38, v38, v70\nM 112,180,52\nds_read2st64_b32 v[86:87], v225 offset0:22 offset1:23\nv_add_f32 v39, v39, v71\nG 11\nv_add_f32 v40, v40, v72\n"
"M 128,176,40\nR 172,190,24576\nv_add_f32 v41, v41, v73\nG 10\nv_add_f32 v42, v42, v74\nM 128,180,56\n"
"ds_read2st64_b32 v[88:89], v225 offset0:24 offset1:25\nv_add_f32 v43, v43, v75\nG 10\nv_add_f32 v44, v44, v76\nM 144,176,44\nR 174,190,28672\n"
"v_add_f32 v45, v45, v77\nG 10\nv_add_f32 v46, v46, v78\nM 144,180,60\nds_read2st64_b32 v[90:91], v225 offset0:26 offset1:27\nG 10\n"
"v_add_f32 v47, v47, v79\nM 148,160,44\nR 176,191,16384\nG 10\nv_add_f32 v48, v48, v80\nv_add_f32 v49, v49, v81\n"
"M 148,164,60\nds_read2st64_b32 v[92:93], v225 offset0:28 offset1:29\nG 10\nv_add_f32 v50, v50, v82\nv_add_f32 v51, v51, v83\nM 132,160,40\n"
"R 178,191,20480\nG 9\nv_add_f32 v52, v52, v84\nv_add_f32 v53, v53, v85\nM 132,164,56\nds_read2st64_b32 v[94:95], v225 offset0:30 offset1:31\n"
"G 8\nv_add_f32 v54, v54, v86\nv_add_f32 v55, v55, v87\nM 116,160,36\nR 180,191,24576\nG 7\n"
"v_add_f32 v56, v56, v88\nv_add_f32 v57, v57, v89\nM 116,164,52\nG 5\nv_add_f32 v58, v58, v90\nv_add_f32 v59, v59, v91\n"
"M 100,160,32\nR 182,191,28672\nv_mov_b32 v226, s46\nM 100,164,48\nv_mov_b32 v227, s47\nG 4\n"
"v_add_f32 v60, v60, v92\nM 104,168,32\nR 160,188,32768\ns_bfe_u32 s62, s56, 0x20007\ns_lshl_b32 s62, s62, 5\ns_add_u32 s61, s74, 1009664\n"
"s_add_u32 s61, s61, s62\ns_waitcnt vmcnt(0)\ns_mov_b64 exec, 1\nbuffer_atomic_swap_x2 v[226:227], off, s[20:23], s61 sc1\ns_mov_b64 exec, -1\nM 104,172,48\n"
"s_bfe_u32 s62, s57, 0x20007\ns_lshl_b32 s62, s62, 5\ns_add_u32 s61, s74, 1009672\ns_add_u32 s61, s61, s62\ns_mov_b64 exec, 1\nbuffer_atomic_swap_x2 v[226:227], off, s[20:23], s61 sc1\n"
"s_mov_b64 exec, -1\nM 120,168,36\nR 162,188,36864\nv_add_f32 v61, v61, v93\nM 120,172,52\ns_add_u32 m0, s50, 40960\n"
"G 4\nv_add_f32 v62, v62, v94\nv_add_f32 v63, v63, v95\nM 136,168,40\nR 164,188,40960\nv_cvt_pk_bf16_f32 v32, v32, v33\n"
"v_cvt_pk_bf16_f32 v33, v34, v35\nM 136,172,56\ns_add_u32 s63, s59, 327680\nv_cvt_pk_bf16_f32 v34, v36, v37\nv_cvt_pk_bf16_f32 v35, v38, v39\nv_cvt_pk_bf16_f32 v40, v40, v41\n"
"M 152,168,44\nR 166,188,45056\nv_cvt_pk_bf16_f32 v41, v42, v43\nv_cvt_pk_bf16_f32 v42, v44, v45\nM 152,172,60\nG 4\n"
"v_cvt_pk_bf16_f32 v43, v46, v47\nv_cvt_pk_bf16_f32 v48, v48, v49\nv_cvt_pk_bf16_f32 v49, v50, v51\nM 156,176,44\nR 168,189,32768\nv_cvt_pk_bf16_f32 v50, v52, v53\n"
"v_cvt_pk_bf16_f32 v51, v54, v55\nM 156,180,60\nbuffer_load_dwordx4 v220, s[16:19], s63 offen lds\nv_cvt_pk_bf16_f32 v56, v56, v57\nM 140,176,40\nR 170,189,36864\n"
"v_cvt_pk_bf16_f32 v57, v58, v59\nv_cvt_pk_bf16_f32 v58, v60, v61\nM 140,180,56\nbuffer_load_dwordx4 v221, s[16:19], s63 offen offset:1024 lds\nv_cvt_pk_bf16_f32 v59, v62, v63\nM 124,176,36\n"
"R 172,189,40960\ns_add_u32 s63, s59, 851968\ns_bfe_u32 s100, s58, 0x20007\ns_mul_i32 s100, s100, 49152\ns_add_u32 s100, s100, s72\ns_add_u32 s100, s100, 65044480\n"
"M 124,180,52\ns_add_u32 m0, s50, 49152\nbuffer_store_dwordx4 v[32:35], v252, s[20:23], s100 offen offset:0 sc1\nM 108,176,32\nR 174,189,45056\nbuffer_store_dwordx4 v[40:43], v252, s[20:23], s100 offen offset:1024 sc1\n"
"M 108,180,48\ns_barrier\nbuffer_store_dwordx4 v[48:51], v252, s[20:23], s100 offen offset:2048 sc1\nG 4\nM 96,160,64\nR 176,190,32768\n"
"buffer_store_dwordx4 v[56:59], v252, s[20:23], s100 offen offset:3072 sc1\nM 96,164,80\nbuffer_load_dwordx4 v220, s[16:19], s63 offen lds\nM 112,160,68\nR 178,190,36864\nM 112,164,84\n"
"buffer_load_dwordx4 v221, s[16:19], s63 offen offset:1024 lds\nM 128,160,72\nR 180,190,40960\nM 128,164,88\nM 144,160,76\nR 182,190,45056\n"
"M 144,164,92\nG 4\nM 148,168,76\nR 160,191,32768\nv_bfe_u32 v250, v252, 4, 2\nM 148,172,92\n"
"v_lshlrev_b32 v250, 3, v250\ns_lshl_b32 s60, s43, 5\nv_add_u32 v250, s60, v250\nM 132,168,72\nR 162,191,36864\nM 132,172,88\n"
"M 116,168,68\nR 164,191,40960\nM 116,172,84\nM 100,168,64\nR 166,191,45056\nM 100,172,80\n"
"G 4\nM 104,176,64\nR 168,188,49152\nM 104,180,80\nM 120,176,68\nR 170,188,53248\n"
"M 120,180,84\nM 136,176,72\nR 172,184,0\nM 136,180,88\nM 152,176,76\nR 174,184,4096\n"
"M 152,180,92\nG 4\nM 156,160,76\nR 176,189,49152\nM 156,164,92\nM 140,160,72\n"
"R 178,189,53248\nM 140,164,88\nM 124,160,68\nR 180,185,0\nM 124,164,84\nM 108,160,64\n"
"R 182,185,4096\nM 108,164,80\nG 4\nM 96,168,96\nR 160,190,49152\nM 96,172,112\n"
"M 112,168,100\nR 162,190,53248\nM 112,172,116\nM 128,168,104\nR 164,186,0\nM 128,172,120\n"
"M 144,168,108\nR 166,186,4096\nM 144,172,124\nG 4\nM 148,176,108\nR 168,191,49152\n"
"M 148,180,124\nM 132,176,104\nR 170,191,53248\nM 132,180,120\nM 116,176,100\nR 172,187,0\n"
"M 116,180,116\nM 100,176,96\nR 174,187,4096\nv_mov_b32 v226, s46\nM 100,180,112\nG 4\n"
"v_mov_b32 v227, s47\nM 104,160,96\nR 176,184,8192\ns_bfe_u32 s62, s58, 0x20007\ns_lshl_b32 s62, s62, 5\ns_add_u32 s61, s74, 1009680\n"
"s_add_u32 s61, s61, s62\ns_waitcnt vmcnt(0)\ns_mov_b64 exec, 1\nbuffer_atomic_swap_x2 v[226:227], off, s[20:23], s61 sc1\ns_mov_b64 exec, -1\ns_barrier\n"
"M 104,164,112\nM 120,160,100\nR 178,184,12288\nM 120,164,116\nM 136,160,104\nR 180,184,16384\n"
"v_mov_b32 v248, 0\nM 136,164,120\nv_mov_b32 v249, 0\nM 152,160,108\nR 182,184,20480\ns_add_u32 s61, s74, 1009664\n"
"s_mov_b64 exec, 3\nbuffer_atomic_or_x2 v[248:249], v250, s[20:23], s61 offen sc0 sc1\ns_mov_b64 exec, -1\nM 152,164,124\nG 4\nM 156,168,108\n"
"R 160,185,8192\nM 156,172,124\nM 140,168,104\nR 162,185,12288\nM 140,172,120\nM 124,168,100\n"
"R 164,185,16384\nM 124,172,116\nM 108,168,96\nR 166,185,20480\nM 108,172,112\nG 4\n"
"M 96,176,128\nR 168,186,8192\nM 96,180,144\nM 112,176,132\nR 170,186,12288\nM 112,180,148\n"
"M 128,176,136\nR 172,186,16384\nM 128,180,152\nM 144,176,140\nR 174,186,20480\nM 144,180,156\n"
"G 4\nM 148,160,140\nR 176,187,8192\nM 148,164,156\nM 132,160,136\nR 178,187,12288\n"
"M 132,164,152\nM 116,160,132\nR 180,187,16384\nM 116,164,148\nM 100,160,128\nR 182,187,20480\n"
"M 100,164,144\nG 4\nM 104,168,128\nR 160,184,24576\nM 104,172,144\nM 120,168,132\n"
"R 162,184,28672\nM 120,172,148\nM 136,168,136\nR 164,184,32768\nM 136,172,152\ns_mul_i32 s64, s43, 49152\n"
"M 152,168,140\nR 166,184,36864\nM 152,172,156\nG 4\nM 156,176,140\nR 168,185,24576\n"
"s_barrier\ns_waitcnt vmcnt(0)\nv_cmp_eq_u64 s[86:87], s[46:47], v[248:249]\nM 156,180,156\ns_add_u32 s64, s64, s72\nM 140,176,136\n"
"R 170,185,28672\ns_and_b32 s62, s86, 3\ns_cmp_eq_u32 s62, 3\ns_cbranch_scc0 Lq958\nLq959:\nv_mov_b32 v248, 0\n"
"M 140,180,152\ns_add_u32 s64, s64, 65011712\nv_mov_b32 v249, 0\nM 124,176,132\nR 172,185,32768\ns_add_u32 s61, s74, 1009664\n"
"s_mov_b64 exec, 4\nbuffer_atomic_or_x2 v[248:249], v250, s[20:23], s61 offen sc0 sc1\ns_mov_b64 exec, -1\nM 124,180,148\nbuffer_load_dwordx4 v[0:3], v252, s[20:23], s64 offen offset:0 sc1\nM 108,176,128\n"
"R 174,185,36864\nM 108,180,144\nbuffer_load_dwordx4 v[4:7], v252, s[20:23], s64 offen offset:1024 sc1\nG 4\nM 128,160,168\nR 176,186,24576\n"
"s_mul_i32 s65, s43, 49152\nM 128,164,184\nR 178,186,28672\ns_add_u32 s65, s65, s72\nM 144,160,172\nR 180,186,32768\n"
"buffer_load_dwordx4 v[8:11], v252, s[20:23], s64 offen offset:2048 sc1\nM 144,164,188\nR 182,186,36864\nG 4\nM 148,168,172\nR 160,187,24576\n"
"s_add_u32 s65, s65, 65028096\nM 148,172,188\nR 162,187,28672\nbuffer_load_dwordx4 v[12:15], v252, s[20:23], s64 offen offset:3072 sc1\nM 132,168,168\nR 164,187,32768\n"
"M 132,172,184\nR 166,187,36864\nG 4\nM 136,176,168\nR 168,184,40960\nbuffer_load_dwordx4 v[16:19], v252, s[20:23], s65 offen offset:0 sc1\n"
"M 136,180,184\nR 170,184,45056\nM 152,176,172\nR 172,184,49152\nM 152,180,188\nR 174,184,53248\n"
"G 4\nM 156,160,172\nR 176,185,40960\nbuffer_load_dwordx4 v[20:23], v252, s[20:23], s65 offen offset:1024 sc1\nM 156,164,188\nR 178,185,45056\n"
"M 140,160,168\nR 180,185,49152\nbuffer_load_dwordx4 v[24:27], v252, s[20:23], s65 offen offset:2048 sc1\nM 140,164,184\nR 182,185,53248\nG 4\n"
"M 128,168,200\nR 160,186,40960\nM 128,172,216\nR 162,186,45056\nbuffer_load_dwordx4 v[28:31], v252, s[20:23], s65 offen offset:3072 sc1\nM 144,168,204\n"
"R 164,186,49152\nM 144,172,220\nR 166,186,53248\nG 4\nM 148,176,204\nR 168,187,40960\n"
"M 148,180,220\nR 170,187,45056\nM 132,176,200\nR 172,187,49152\nM 132,180,216\nR 174,187,53248\n"
"G 4\nM 136,160,200\nR 176,184,24576\nM 136,164,216\nR 178,184,28672\nM 152,160,204\n"
"R 180,184,32768\ns_mul_i32 s66, s43, 49152\nM 152,164,220\nR 182,184,36864\nG 4\nM 156,168,204\n"
"R 160,185,24576\ns_add_u32 s66, s66, s72\nM 156,172,220\nR 162,185,28672\ns_add_u32 s66, s66, 65044480\nM 140,168,200\n"
"R 164,185,32768\ns_waitcnt vmcnt(8)\nv_cmp_eq_u64 s[86:87], s[46:47], v[248:249]\nM 140,172,216\nR 166,185,36864\nG 4\n"
"M 96,176,160\nR 168,186,24576\ns_and_b32 s62, s86, 4\ns_cmp_eq_u32 s62, 4\ns_cbranch_scc0 Lq974\nLq975:\n"
"M 96,180,176\nR 170,186,28672\ns_mov_b32 m0, s51\nM 112,176,164\nR 172,186,32768\nbuffer_load_dwordx4 v[64:67], v252, s[20:23], s66 offen offset:0 sc1\n"
"ds_write_addtid_b32 v128 offset:0\nM 112,180,180\nR 174,186,36864\nds_write_addtid_b32 v129 offset:256\nG 6\nM 116,160,164\n"
"R 176,187,24576\nbuffer_load_dwordx4 v[68:71], v252, s[20:23], s66 offen offset:1024 sc1\nds_write_addtid_b32 v130 offset:512\nM 116,164,180\nR 178,187,28672\nds_write_addtid_b32 v131 offset:768\n"
"M 100,160,160\nR 180,187,32768\nds_write_addtid_b32 v132 offset:1024\nM 100,164,176\nR 182,187,36864\nds_write_addtid_b32 v133 offset:1280\n"
"G 9\nM 104,168,160\nR 160,184,40960\nbuffer_load_dwordx4 v[72:75], v252, s[20:23], s66 offen offset:2048 sc1\nds_write_addtid_b32 v134 offset:1536\nM 104,172,176\n"
"R 162,184,45056\nbuffer_load_dwordx4 v[76:79], v252, s[20:23], s66 offen offset:3072 sc1\nds_write_addtid_b32 v135 offset:1792\nM 120,168,164\nR 164,184,49152\nds_write_addtid_b32 v136 offset:2048\n"
"M 120,172,180\nR 166,184,53248\nds_write_addtid_b32 v137 offset:2304\nG 9\nM 124,176,164\nR 168,185,40960\n"
"ds_write_addtid_b32 v138 offset:2560\nM 124,180,180\nR 170,185,45056\nds_write_addtid_b32 v139 offset:2816\nM 108,176,160\nR 172,185,49152\n"
"ds_write_addtid_b32 v140 offset:3072\nds_write_addtid_b32 v141 offset:3328\nds_write_addtid_b32 v142 offset:3584\nds_write_addtid_b32 v143 offset:3840\nds_write_addtid_b32 v144 offset:4096\nds_write_addtid_b32 v145 offset:4352\n"
"ds_write_addtid_b32 v146 offset:4608\nds_write_addtid_b32 v147 offset:4864\nds_write_addtid_b32 v148 offset:5120\nds_write_addtid_b32 v149 offset:5376\nds_write_addtid_b32 v150 offset:5632\nds_write_addtid_b32 v151 offset:5888\n"
"ds_write_addtid_b32 v152 offset:6144\nds_write_addtid_b32 v153 offset:6400\nds_write_addtid_b32 v154 offset:6656\nds_write_addtid_b32 v155 offset:6912\nds_write_addtid_b32 v156 offset:7168\nds_write_addtid_b32 v157 offset:7424\n"
"ds_write_addtid_b32 v158 offset:7680\nds_write_addtid_b32 v159 offset:7936\nM 108,180,176\nR 174,185,53248\nG 15\nM 96,160,192\n"
"R 176,186,40960\nM 96,164,208\nR 178,186,45056\nG 3\ns_barrier\nM 112,160,196\n"
"R 180,186,49152\nM 112,164,212\nR 182,186,53248\nG 4\nM 116,168,196\nR 160,187,40960\n"
"M 116,172,212\nR 162,187,45056\nM 100,168,192\nR 164,187,49152\nM 100,172,208\nR 166,187,53248\n"
"G 4\nM 104,176,192\nds_read2st64_b32 v[128:129], v225 offset0:0 offset1:1\nds_read2st64_b32 v[130:131], v225 offset0:2 offset1:3\nM 104,180,208\nds_read2st64_b32 v[132:133], v225 offset0:4 offset1:5\n"
"ds_read2st64_b32 v[134:135], v225 offset0:6 offset1:7\nM 120,176,196\nds_read2st64_b32 v[136:137], v225 offset0:8 offset1:9\nds_read2st64_b32 v[138:139], v225 offset0:10 offset1:11\nM 120,180,212\nds_read2st64_b32 v[140:141], v225 offset0:12 offset1:13\n"
"ds_read2st64_b32 v[142:143], v225 offset0:14 offset1:15\nG 8\nM 124,160,196\nds_read2st64_b32 v[144:145], v225 offset0:16 offset1:17\nds_read2st64_b32 v[146:147], v225 offset0:18 offset1:19\nM 124,164,212\n"
"ds_read2st64_b32 v[148:149], v225 offset0:20 offset1:21\nds_read2st64_b32 v[150:151], v225 offset0:22 offset1:23\nM 108,160,192\nds_read2st64_b32 v[152:153], v225 offset0:24 offset1:25\nds_read2st64_b32 v[154:155], v225 offset0:26 offset1:27\nM 108,164,208\n"
"ds_read2st64_b32 v[156:157], v225 offset0:28 offset1:29\nds_read2st64_b32 v[158:159], v225 offset0:30 offset1:31\ns_waitcnt vmcnt(11)\nv_mfma_f32_16x16x32_bf16 v[96:99], v[208:211], v[0:3], v[96:99]\nv_mfma_f32_16x16x32_bf16 v[100:103], v[210:213], v[0:3], v[100:103]\ns_waitcnt vmcnt(10)\n"
"v_mfma_f32_16x16x32_bf16 v[104:107], v[208:211], v[4:7], v[104:107]\nv_mfma_f32_16x16x32_bf16 v[108:111], v[210:213], v[4:7], v[108:111]\ns_waitcnt vmcnt(9)\nv_mfma_f32_16x16x32_bf16 v[112:115], v[208:211], v[8:11], v[112:115]\nv_mfma_f32_16x16x32_bf16 v[116:119], v[210:213], v[8:11], v[116:119]\ns_waitcnt vmcnt(8)\n"
"v_mfma_f32_16x16x32_bf16 v[120:123], v[208:211], v[12:15], v[120:123]\nv_mfma_f32_16x16x32_bf16 v[124:127], v[210:213], v[12:15], v[124:127]\ns_waitcnt vmcnt(7)\nv_mfma_f32_16x16x32_bf16 v[96:99], v[208:211], v[16:19], v[96:99]\nv_mfma_f32_16x16x32_bf16 v[100:103], v[210:213], v[16:19], v[100:103]\ns_waitcnt vmcnt(6)\n"
"v_mfma_f32_16x16x32_bf16 v[104:107], v[208:211], v[20:23], v[104:107]\nv_mfma_f32_16x16x32_bf16 v[108:111], v[210:213], v[20:23], v[108:111]\ns_waitcnt vmcnt(5)\nv_mfma_f32_16x16x32_bf16 v[112:115], v[208:211], v[24:27], v[112:115]\nv_mfma_f32_16x16x32_bf16 v[116:119], v[210:213], v[24:27], v[116:119]\ns_waitcnt vmcnt(4)\n"
"v_mfma_f32_16x16x32_bf16 v[120:123], v[208:211], v[28:31], v[120:123]\nv_mfma_f32_16x16x32_bf16 v[124:127], v[210:213], v[28:31], v[124:127]\ns_waitcnt vmcnt(3)\nv_mfma_f32_16x16x32_bf16 v[96:99], v[208:211], v[64:67], v[96:99]\nv_mfma_f32_16x16x32_bf16 v[100:103], v[210:213], v[64:67], v[100:103]\ns_waitcnt vmcnt(2)\n"
"v_mfma_f32_16x16x32_bf16 v[104:107], v[208:211], v[68:71], v[104:107]\nv_mfma_f32_16x16x32_bf16 v[108:111], v[210:213], v[68:71], v[108:111]\ns_waitcnt vmcnt(1)\nv_mfma_f32_16x16x32_bf16 v[112:115], v[208:211], v[72:75], v[112:115]\nv_mfma_f32_16x16x32_bf16 v[116:119], v[210:213], v[72:75], v[116:119]\ns_waitcnt vmcnt(0)\n"
"v_mfma_f32_16x16x32_bf16 v[120:123], v[208:211], v[76:79], v[120:123]\nv_mfma_f32_16x16x32_bf16 v[124:127], v[210:213], v[76:79], v[124:127]\nG 15\nv_pk_add_f32 v[96:97], v[96:97], v[128:129]\nG 14\nv_pk_add_f32 v[98:99], v[98:99], v[130:131]\n"
"G 13\nv_pk_add_f32 v[100:101], v[100:101], v[132:133]\nG 12\nv_pk_add_f32 v[102:103], v[102:103], v[134:135]\nv_cvt_pk_bf16_f32 v192, v96, v97\nv_cvt_pk_bf16_f32 v193, v98, v99\n"
"v_cvt_pk_bf16_f32 v194, v100, v101\nv_cvt_pk_bf16_f32 v195, v102, v103\nG 11\nv_pk_add_f32 v[104:105], v[104:105], v[136:137]\nG 10\nv_pk_add_f32 v[106:107], v[106:107], v[138:139]\n"
"G 9\nv_pk_add_f32 v[108:109], v[108:109], v[140:141]\nG 8\nv_pk_add_f32 v[110:111], v[110:111], v[142:143]\nv_cvt_pk_bf16_f32 v196, v104, v105\nv_cvt_pk_bf16_f32 v197, v106, v107\n"
"v_cvt_pk_bf16_f32 v198, v108, v109\nv_cvt_pk_bf16_f32 v199, v110, v111\nG 7\nv_pk_add_f32 v[112:113], v[112:113], v[144:145]\nG 6\nv_pk_add_f32 v[114:115], v[114:115], v[146:147]\n"
"G 5\nv_pk_add_f32 v[116:117], v[116:117], v[148:149]\nG 4\nv_pk_add_f32 v[118:119], v[118:119], v[150:151]\nv_cvt_pk_bf16_f32 v200, v112, v113\nv_cvt_pk_bf16_f32 v201, v114, v115\n"
"v_cvt_pk_bf16_f32 v202, v116, v117\nv_cvt_pk_bf16_f32 v203, v118, v119\nG 3\nv_pk_add_f32 v[120:121], v[120:121], v[152:153]\nG 2\nv_pk_add_f32 v[122:123], v[122:123], v[154:155]\n"
"G 1\nv_pk_add_f32 v[124:125], v[124:125], v[156:157]\nG 0\nv_pk_add_f32 v[126:127], v[126:127], v[158:159]\nv_cvt_pk_bf16_f32 v204, v120, v121\nv_cvt_pk_bf16_f32 v205, v122, v123\n"
"v_cvt_pk_bf16_f32 v206, v124, v125\nv_cvt_pk_bf16_f32 v207, v126, v127\nv_and_b32 v230, 0xf0, v252\nv_lshlrev_b32 v230, 7, v230\nv_bfe_u32 v231, v252, 8, 1\nv_lshl_add_u32 v230, v231, 5, v230\n"
"v_lshrrev_b32 v231, 9, v252\nv_lshl_add_u32 v230, v231, 4, v230\ns_add_u32 s61, s49, s53\ns_add_u32 s62, s61, 32768\nv_permlane16_swap_b32 v192, v194\nv_permlane16_swap_b32 v193, v195\n"
"buffer_store_dwordx4 v[192:195], v230, s[28:31], s61 offen offset:0\nv_permlane16_swap_b32 v196, v198\nv_permlane16_swap_b32 v197, v199\nbuffer_store_dwordx4 v[196:199], v230, s[28:31], s61 offen offset:64\nv_permlane16_swap_b32 v200, v202\nv_permlane16_swap_b32 v201, v203\n"
"buffer_store_dwordx4 v[200:203], v230, s[28:31], s62 offen offset:0\nv_permlane16_swap_b32 v204, v206\nv_permlane16_swap_b32 v205, v207\nbuffer_store_dwordx4 v[204:207], v230, s[28:31], s62 offen offset:64\ns_add_u32 s60, s83, 1\nv_mov_b32 v234, s60\n"
"s_or_b32 s60, s45, s40\ns_cmp_eq_u32 s60, 0\ns_cselect_b64 exec, 1, 0\nbuffer_store_dword v234, off, s[20:23], 0 sc1\ns_mov_b64 exec, -1\ns_endpgm\n"
"LqF:\nLq593:\nv_mov_b32 v248, 0\nv_mov_b32 v249, 0\ns_add_u32 s61, s80, 88358912\ns_mov_b64 exec, 0xffff\n"
"buffer_atomic_or_x2 v[248:249], v250, s[20:23], s61 offen sc0 sc1\ns_mov_b64 exec, -1\ns_waitcnt vmcnt(0)\nv_cmp_eq_u64 s[86:87], s[46:47], v[248:249]\ns_nop 1\ns_and_b32 s62, s86, 0xffff\n"
"s_cmp_eq_u32 s62, 0xffff\ns_cbranch_scc1 Lq594\ns_sleep 1\ns_sub_u32 s48, s48, 1\ns_cmp_gt_i32 s48, 0\ns_cbranch_scc1 Lq593\n"
"s_branch Lq594\nLq643:\ns_lshr_b32 s84, s49, 3\ns_add_u32 s84, s84, 82837504\nbuffer_load_dwordx4 v[128:131], v252, s[20:23], s84 offen offset:0 sc1\nbuffer_load_dwordx4 v[132:135], v252, s[20:23], s84 offen offset:1024 sc1\n"
"buffer_load_dwordx4 v[136:139], v252, s[20:23], s84 offen offset:2048 sc1\nbuffer_load_dwordx4 v[140:143], v252, s[20:23], s84 offen offset:3072 sc1\ns_lshr_b32 s85, s49, 3\ns_add_u32 s85, s85, 82841600\nbuffer_load_dwordx4 v[144:147], v252, s[20:23], s85 offen offset:0 sc1\nbuffer_load_dwordx4 v[148:151], v252, s[20:23], s85 offen offset:1024 sc1\n"
"buffer_load_dwordx4 v[152:155], v252, s[20:23], s85 offen offset:2048 sc1\nbuffer_load_dwordx4 v[156:159], v252, s[20:23], s85 offen offset:3072 sc1\ns_waitcnt vmcnt(6)\nv_min3_u32 v230, v129, v131, v133\ns_waitcnt vmcnt(5)\nv_min3_u32 v230, v230, v135, v137\n"
"s_waitcnt vmcnt(4)\nv_min3_u32 v230, v230, v139, v141\ns_waitcnt vmcnt(3)\nv_min3_u32 v230, v230, v143, v145\ns_waitcnt vmcnt(2)\nv_min3_u32 v230, v230, v147, v149\n"
"s_waitcnt vmcnt(1)\nv_min3_u32 v230, v230, v151, v153\ns_waitcnt vmcnt(0)\nv_min3_u32 v230, v230, v155, v157\nv_min3_u32 v230, v230, v159, v159\nv_max3_u32 v231, v129, v131, v133\n"
"v_max3_u32 v231, v231, v135, v137\nv_max3_u32 v231, v231, v139, v141\nv_max3_u32 v231, v231, v143, v145\nv_max3_u32 v231, v231, v147, v149\nv_max3_u32 v231, v231, v151, v153\nv_max3_u32 v231, v231, v155, v157\n"
"v_max3_u32 v231, v231, v159, v159\nv_cmp_ne_u32 s[86:87], s46, v230\nv_cmp_ne_u32 s[88:89], s46, v231\ns_nop 0\ns_or_b64 s[86:87], s[86:87], s[88:89]\ns_cbranch_scc0 Lq644\n"
"s_sub_u32 s48, s48, 1\ns_cmp_gt_i32 s48, 0\ns_cbranch_scc1 Lq643\ns_branch Lq644\nLq693:\ns_mov_b64 exec, s[66:67]\n"
"buffer_load_dwordx4 v[230:233], v252, s[20:23], s65 offen offset:0 sc1\nbuffer_load_dwordx2 v[64:65], v227, s[20:23], s65 offen offset:2048 sc1\nbuffer_load_dwordx2 v[70:71], v227, s[20:23], s65 offen offset:2560 sc1\ns_mov_b64 exec, -1\ns_waitcnt vmcnt(0)\nv_max3_u32 v248, v233, v65, v71\n"
"v_cmp_eq_u32 vcc, -1, v248\ns_nop 1\ns_cbranch_vccz Lq694\ns_sub_u32 s48, s48, 1\ns_cmp_gt_i32 s48, 0\ns_cbranch_scc1 Lq693\n"
"s_branch Lq694\nLq695:\ns_mov_b64 exec, s[66:67]\nbuffer_load_dwordx4 v[234:237], v252, s[20:23], s65 offen offset:1024 sc1\nbuffer_load_dwordx2 v[76:77], v227, s[20:23], s65 offen offset:3072 sc1\nbuffer_load_dwordx2 v[82:83], v227, s[20:23], s65 offen offset:3584 sc1\n"
"s_mov_b64 exec, -1\ns_waitcnt vmcnt(0)\nv_max3_u32 v248, v237, v77, v83\nv_cmp_eq_u32 vcc, -1, v248\ns_nop 1\ns_cbranch_vccz Lq696\n"
"s_sub_u32 s48, s48, 1\ns_cmp_gt_i32 s48, 0\ns_cbranch_scc1 Lq695\ns_branch Lq696\nLq698:\nv_mov_b32 v248, 0\n"
"v_mov_b32 v249, 0\ns_add_u32 s61, s80, 88498176\ns_mov_b64 exec, 0xffff\nbuffer_atomic_or_x2 v[248:249], v250, s[20:23], s61 offen sc0 sc1\ns_mov_b64 exec, -1\ns_waitcnt vmcnt(0)\n"
"v_cmp_eq_u64 s[86:87], s[46:47], v[248:249]\ns_nop 1\ns_and_b32 s62, s86, 0xffff\ns_cmp_eq_u32 s62, 0xffff\ns_cbranch_scc1 Lq699\ns_sleep 1\n"
"s_sub_u32 s48, s48, 1\ns_cmp_gt_i32 s48, 0\ns_cbranch_scc1 Lq698\ns_branch Lq699\nLq750:\nv_mov_b32 v248, 0\n"
"v_mov_b32 v249, 0\ns_add_u32 s61, s80, 88637440\ns_mov_b64 exec, 0xffff\nbuffer_atomic_or_x2 v[248:249], v250, s[20:23], s61 offen sc0 sc1\ns_mov_b64 exec, -1\ns_waitcnt vmcnt(0)\n"
"v_cmp_eq_u64 s[86:87], s[46:47], v[248:249]\ns_nop 1\ns_and_b32 s62, s86, 0xffff\ns_cmp_eq_u32 s62, 0xffff\ns_cbranch_scc1 Lq751\ns_sleep 1\n"
"s_sub_u32 s48, s48, 1\ns_cmp_gt_i32 s48, 0\ns_cbranch_scc1 Lq750\ns_branch Lq751\nLq800:\ns_lshr_b32 s84, s49, 3\n"
"s_add_u32 s84, s84, 83361792\nbuffer_load_dwordx4 v[0:3], v252, s[20:23], s84 offen offset:0 sc1\nbuffer_load_dwordx4 v[4:7], v252, s[20:23], s84 offen offset:1024 sc1\nbuffer_load_dwordx4 v[8:11], v252, s[20:23], s84 offen offset:2048 sc1\nbuffer_load_dwordx4 v[12:15], v252, s[20:23], s84 offen offset:3072 sc1\ns_lshr_b32 s85, s49, 3\n"
"s_add_u32 s85, s85, 83365888\nbuffer_load_dwordx4 v[16:19], v252, s[20:23], s85 offen offset:0 sc1\nbuffer_load_dwordx4 v[20:23], v252, s[20:23], s85 offen offset:1024 sc1\nbuffer_load_dwordx4 v[24:27], v252, s[20:23], s85 offen offset:2048 sc1\nbuffer_load_dwordx4 v[28:31], v252, s[20:23], s85 offen offset:3072 sc1\ns_waitcnt vmcnt(6)\n"
"v_min3_u32 v230, v1, v3, v5\ns_waitcnt vmcnt(5)\nv_min3_u32 v230, v230, v7, v9\ns_waitcnt vmcnt(4)\nv_min3_u32 v230, v230, v11, v13\ns_waitcnt vmcnt(3)\n"
"v_min3_u32 v230, v230, v15, v17\ns_waitcnt vmcnt(2)\nv_min3_u32 v230, v230, v19, v21\ns_waitcnt vmcnt(1)\nv_min3_u32 v230, v230, v23, v25\ns_waitcnt vmcnt(0)\n"
"v_min3_u32 v230, v230, v27, v29\nv_min3_u32 v230, v230, v31, v31\nv_max3_u32 v231, v1, v3, v5\nv_max3_u32 v231, v231, v7, v9\nv_max3_u32 v231, v231, v11, v13\nv_max3_u32 v231, v231, v15, v17\n"
"v_max3_u32 v231, v231, v19, v21\nv_max3_u32 v231, v231, v23, v25\nv_max3_u32 v231, v231, v27, v29\nv_max3_u32 v231, v231, v31, v31\nv_cmp_ne_u32 s[86:87], s46, v230\nv_cmp_ne_u32 s[88:89], s46, v231\n"
"s_nop 0\ns_or_b64 s[86:87], s[86:87], s[88:89]\ns_cbranch_scc0 Lq801\ns_sub_u32 s48, s48, 1\ns_cmp_gt_i32 s48, 0\ns_cbranch_scc1 Lq800\n"
"s_branch Lq801\nLq826:\nv_mov_b32 v248, 0\nv_mov_b32 v249, 0\ns_add_u32 s61, s82, 845824\ns_mov_b64 exec, 0xf\n"
"buffer_atomic_or_x2 v[248:249], v250, s[20:23], s61 offen sc0 sc1\ns_mov_b64 exec, -1\ns_waitcnt vmcnt(0)\nv_cmp_eq_u64 s[86:87], s[46:47], v[248:249]\ns_nop 1\ns_and_b32 s62, s86, 0xf\n"
"s_cmp_eq_u32 s62, 0xf\ns_cbranch_scc1 Lq827\ns_sleep 1\ns_sub_u32 s48, s48, 1\ns_cmp_gt_i32 s48, 0\ns_cbranch_scc1 Lq826\n"
"s_branch Lq827\nLq861:\nv_mov_b32 v248, 0\nv_mov_b32 v249, 0\ns_add_u32 s61, s82, 845832\ns_mov_b64 exec, 0xf\n"
"buffer_atomic_or_x2 v[248:249], v250, s[20:23], s61 offen sc0 sc1\ns_mov_b64 exec, -1\ns_waitcnt vmcnt(0)\nv_cmp_eq_u64 s[86:87], s[46:47], v[248:249]\ns_nop 1\ns_and_b32 s62, s86, 0xf\n"
"s_cmp_eq_u32 s62, 0xf\ns_cbranch_scc1 Lq862\ns_sleep 1\ns_sub_u32 s48, s48, 1\ns_cmp_gt_i32 s48, 0\ns_cbranch_scc1 Lq861\n"
"s_branch Lq862\nLq896:\nv_mov_b32 v248, 0\nv_mov_b32 v249, 0\ns_add_u32 s61, s82, 845848\ns_mov_b64 exec, 0xf\n"
"buffer_atomic_or_x2 v[248:249], v250, s[20:23], s61 offen sc0 sc1\ns_mov_b64 exec, -1\ns_waitcnt vmcnt(0)\nv_cmp_eq_u64 s[86:87], s[46:47], v[248:249]\ns_nop 1\ns_and_b32 s62, s86, 0xf\n"
"s_cmp_eq_u32 s62, 0xf\ns_cbranch_scc1 Lq897\ns_sleep 1\ns_sub_u32 s48, s48, 1\ns_cmp_gt_i32 s48, 0\ns_cbranch_scc1 Lq896\n"
"s_branch Lq897\nLq915:\nv_mov_b32 v248, 0\nv_mov_b32 v249, 0\ns_add_u32 s61, s82, 845840\ns_mov_b64 exec, 0xf\n"
"buffer_atomic_or_x2 v[248:249], v250, s[20:23], s61 offen sc0 sc1\ns_mov_b64 exec, -1\ns_waitcnt vmcnt(0)\nv_cmp_eq_u64 s[86:87], s[46:47], v[248:249]\ns_nop 1\ns_and_b32 s62, s86, 0xf\n"
"s_cmp_eq_u32 s62, 0xf\ns_cbranch_scc1 Lq916\ns_sleep 1\ns_sub_u32 s48, s48, 1\ns_cmp_gt_i32 s48, 0\ns_cbranch_scc1 Lq915\n"
"s_branch Lq916\nLq958:\nv_mov_b32 v248, 0\nv_mov_b32 v249, 0\ns_add_u32 s61, s74, 1009664\ns_mov_b64 exec, 3\n"
"buffer_atomic_or_x2 v[248:249], v250, s[20:23], s61 offen sc0 sc1\ns_mov_b64 exec, -1\ns_waitcnt vmcnt(0)\nv_cmp_eq_u64 s[86:87], s[46:47], v[248:249]\ns_nop 1\ns_and_b32 s62, s86, 3\n"
"s_cmp_eq_u32 s62, 3\ns_cbranch_scc1 Lq959\ns_sleep 1\ns_sub_u32 s48, s48, 1\ns_cmp_gt_i32 s48, 0\ns_cbranch_scc1 Lq958\n"
"s_branch Lq959\nLq974:\nv_mov_b32 v248, 0\nv_mov_b32 v249, 0\ns_add_u32 s61, s74, 1009664\ns_mov_b64 exec, 4\n"
"buffer_atomic_or_x2 v[248:249], v250, s[20:23], s61 offen sc0 sc1\ns_mov_b64 exec, -1\ns_waitcnt vmcnt(0)\nv_cmp_eq_u64 s[86:87], s[46:47], v[248:249]\ns_nop 1\ns_and_b32 s62, s86, 4\n"
"s_cmp_eq_u32 s62, 4\ns_cbranch_scc1 Lq975\ns_sleep 1\ns_sub_u32 s48, s48, 1\ns_cmp_gt_i32 s48, 0\ns_cbranch_scc1 Lq974\n"
"s_branch Lq975\nLqE:\n"
  : "+{v0}"(tid), "+{s[92:93]}"(ka), "+{s45}"(bid) :
  : "a0", "a1", "a2", "a3", "a4", "a5", "a6", "a7", "a8", "a9", "a10", "a11", "a12", "a13", "a14", "a15", "a16", "a17", "a18", "a19", "a20", "a21", "a22", "a23", "a24", "a25", "a26", "a27", "a28", "a29", "a30", "a31", "a32", "a33", "a34", "a35", "a36", "a37", "a38", "a39", "a40", "a41", "a42", "a43", "a44", "a45", "a46", "a47", "a48", "a49", "a50", "a51", "a52", "a53", "a54", "a55", "a56", "a57", "a58", "a59", "a60", "a61", "a62", "a63", "a64", "a65", "a66", "a67", "a68", "a69", "a70", "a71", "a72", "a73", "a74", "a75", "a76", "a77", "a78", "a79", "a80", "a81", "a82", "a83", "a84", "a85", "a86", "a87", "a88", "a89", "a90", "a91", "a92", "a93", "a94", "a95", "a96", "a97", "a98", "a99", "a100", "a101", "a102", "a103", "a104", "a105", "a106", "a107", "a108", "a109", "a110", "a111", "a112", "a113", "a114", "a115", "a116", "a117", "a118", "a119", "a120", "a121", "a122", "a123", "a124", "a125", "a126", "a127", "a128", "a129", "a130", "a131", "a132", "a133", "a134", "a135", "a136", "a137", "a138", "a139", "a140", "a141", "a142", "a143", "a144", "a145", "a146", "a147", "a148", "a149", "a150", "a151", "a152", "a153", "a154", "a155", "a156", "a157", "a158", "a159", "a160", "a161", "a162", "a163", "a164", "a165", "a166", "a167", "a168", "a169", "a170", "a171", "a172", "a173", "a174", "a175", "a176", "a177", "a178", "a179", "a180", "a181", "a182", "a183", "a184", "a185", "a186", "a187", "a188", "a189", "a190", "a191", "a192", "a193", "a194", "a195", "a196", "a197", "a198", "a199", "a200", "a201", "a202", "a203", "a204", "a205", "a206", "a207", "a208", "a209", "a210", "a211", "a212", "a213", "a214", "a215", "a216", "a217", "a218", "a219", "a220", "a221", "a222", "a223", "a224", "a225", "a226", "a227", "a228", "a229", "a230", "a231", "a232", "a233", "a234", "a235", "a236", "a237", "a238", "a239", "a240", "a241", "a242", "a243", "a244", "a245", "a246", "a247", "a248", "a249", "a250", "a251", "a252", "a253", "a254", "a255", "v1", "v2", "v3", "v4", "v5", "v6", "v7", "v8", "v9", "v10", "v11", "v12", "v13", "v14", "v15", "v16", "v17", "v18", "v19", "v20", "v21", "v22", "v23", "v24", "v25", "v26", "v27", "v28", "v29", "v30", "v31", "v32", "v33", "v34", "v35", "v36", "v37", "v38", "v39", "v40", "v41", "v42", "v43", "v44", "v45", "v46", "v47", "v48", "v49", "v50", "v51", "v52", "v53", "v54", "v55", "v56", "v57", "v58", "v59", "v60", "v61", "v62", "v63", "v64", "v65", "v66", "v67", "v68", "v69", "v70", "v71", "v72", "v73", "v74", "v75", "v76", "v77", "v78", "v79", "v80", "v81", "v82", "v83", "v84", "v85", "v86", "v87", "v88", "v89", "v90", "v91", "v92", "v93", "v94", "v95", "v96", "v97", "v98", "v99", "v100", "v101", "v102", "v103", "v104", "v105", "v106", "v107", "v108", "v109", "v110", "v111", "v112", "v113", "v114", "v115", "v116", "v117", "v118", "v119", "v120", "v121", "v122", "v123", "v124", "v125", "v126", "v127", "v128", "v129", "v130", "v131", "v132", "v133", "v134", "v135", "v136", "v137", "v138", "v139", "v140", "v141", "v142", "v143", "v144", "v145", "v146", "v147", "v148", "v149", "v150", "v151", "v152", "v153", "v154", "v155", "v156", "v157", "v158", "v159", "v160", "v161", "v162", "v163", "v164", "v165", "v166", "v167", "v168", "v169", "v170", "v171", "v172", "v173", "v174", "v175", "v176", "v177", "v178", "v179", "v180", "v181", "v182", "v183", "v184", "v185", "v186", "v187", "v188", "v189", "v190", "v191", "v192", "v193", "v194", "v195", "v196", "v197", "v198", "v199", "v200", "v201", "v202", "v203", "v204", "v205", "v206", "v207", "v208", "v209", "v210", "v211", "v212", "v213", "v214", "v215", "v216", "v217", "v218", "v219", "v220", "v221", "v222", "v223", "v224", "v225", "v226", "v227", "v228", "v229", "v230", "v231", "v232", "v233", "v234", "v235", "v236", "v237", "v238", "v239", "v240", "v241", "v242", "v243", "v244", "v245", "v246", "v247", "v248", "v249", "v250", "v251", "v252", "v253", "v254", "v255", "s4", "s5", "s6", "s7", "s8", "s9", "s10", "s11", "s12", "s13", "s14", "s15", "s16", "s17", "s18", "s19", "s20", "s21", "s22", "s23", "s24", "s25", "s26", "s27", "s28", "s29", "s30", "s31", "s32", "s33", "s34", "s35", "s36", "s37", "s38", "s39", "s40", "s41", "s42", "s43", "s44", "s46", "s47", "s48", "s49", "s50", "s51", "s52", "s53", "s54", "s55", "s56", "s57", "s58", "s59", "s60", "s61", "s62", "s63", "s64", "s65", "s66", "s67", "s68", "s69", "s70", "s71", "s72", "s73", "s74", "s75", "s76", "s77", "s78", "s79", "s80", "s81", "s82", "s83", "s84", "s85", "s86", "s87", "s88", "s89", "s90", "s91", "s94", "s95", "s96", "s97", "s98", "s99", "s100", "s101", "vcc", "m0", "scc", "memory");
#endif
}
extern "C" void kernel_launch(void* const* d_in, const int* in_sizes, int n_in, void* d_out, int out_size, void* d_ws, size_t ws_size, hipStream_t stream) {
  (void)hipFuncSetAttribute((const void*)layers, hipFuncAttributeMaxDynamicSharedMemorySize, 160 * 1024);
  hipLaunchKernelGGL(layers, dim3(256), dim3(256), 160 * 1024, stream, d_in[0], d_in[1], d_in[2], d_in[4], d_in[5], d_in[6], d_in[7], d_out, d_ws);
}
```

```cpp
#include <hip/hip_runtime.h>
__global__ __launch_bounds__(256, 1) void layers(const void* x, const void* wqkv, const void* wo, const void* w1, const void* b1, const void* w2, const void* b2, void* out, void* ws) {
#if defined(__HIP_DEVICE_COMPILE__)
  int tid = threadIdx.x, bid = blockIdx.x;
  unsigned long long ka = (unsigned long long)__builtin_amdgcn_kernarg_segment_ptr();
  asm volatile(
".macro M d,w,a\nv_mfma_f32_16x16x32_bf16 v[\\d:\\d+3], v[\\w:\\w+3], a[\\a:\\a+3], v[\\d:\\d+3]\n.endm\n.macro N d,a,w\nv_mfma_f32_16x16x32_bf16 v[\\d:\\d+3], a[\\a:\\a+3], v[\\w:\\w+3], v[\\d:\\d+3]\n.endm\n"
".macro R d,b,o\nds_read_b64_tr_b16 v[\\d:\\d+1], v\\b offset:\\o\n.endm\n.macro G n\ns_waitcnt lgkmcnt(\\n)\n.endm\n"
"s_load_dwordx2 s[24:25], s[92:93], 0\ns_load_dwordx2 s[4:5], s[92:93], 8\ns_load_dwordx2 s[8:9], s[92:93], 16\ns_load_dwordx2 s[12:13], s[92:93], 24\ns_load_dwordx2 s[32:33], s[92:93], 32\ns_load_dwordx2 s[16:17], s[92:93], 40\n"
"s_load_dwordx2 s[36:37], s[92:93], 48\ns_load_dwordx2 s[28:29], s[92:93], 56\ns_load_dwordx2 s[20:21], s[92:93], 64\nv_lshrrev_b32 v1, 6, v0\nv_and_b32 v0, 63, v0\nv_readfirstlane_b32 s40, v1\n"
"v_lshlrev_b32 v252, 4, v0\ns_getpc_b64 s[98:99]\nLqPC:\ns_lshr_b32 s60, s45, 3\ns_lshl2_add_u32 s60, s60, s40\ns_lshl_b32 s60, s60, 8\n"
"v_lshl_add_u32 v224, v252, 3, s60\nv_add_u32 v224, LqPC-LqPC, v224\ns_mov_b64 exec, 3\nglobal_load_dword v224, v224, s[98:99]\ns_mov_b64 exec, -1\ns_mov_b32 s6, 6291456\n"
"s_mov_b32 s7, 0x20000\ns_mov_b32 s10, 2097152\ns_mov_b32 s11, 0x20000\ns_mov_b32 s14, 7340032\ns_mov_b32 s15, 0x20000\ns_mov_b32 s18, 7340032\n"
"s_mov_b32 s19, 0x20000\ns_mov_b32 s22, 89128960\ns_mov_b32 s23, 0x20000\ns_mov_b32 s26, 4194304\ns_mov_b32 s27, 0x20000\ns_mov_b32 s30, 4194304\n"
"s_mov_b32 s31, 0x20000\ns_mov_b32 s34, 7168\ns_mov_b32 s35, 0x20000\ns_mov_b32 s38, 2048\ns_mov_b32 s39, 0x20000\ns_bfe_u32 s41, s45, 0x30003\n"
"s_and_b32 s60, s45, 1\ns_lshl3_add_u32 s41, s60, s41\ns_bfe_u32 s43, s45, 0x20001\ns_lshr_b32 s44, s45, 6\ns_lshl2_add_u32 s42, s43, s44\ns_lshl2_add_u32 s75, s44, s43\n"
"s_and_b32 s90, s40, 1\ns_lshl2_add_u32 s60, s41, s40\ns_lshl_b32 s49, s60, 16\ns_lshl_b32 s50, s40, 11\ns_add_u32 s50, s50, 32768\ns_lshl_b32 s52, s42, 7\n"
"s_lshl_b32 s51, s40, 13\ns_lshl_b32 s60, s90, 10\ns_add_u32 s70, s49, s60\ns_xor_b32 s71, s70, 0x10000\ns_mul_i32 s101, s41, 196608\ns_add_u32 s101, s101, s52\n"
"s_lshl_b32 s53, s75, 7\nLqq0:\nv_lshrrev_b32 v96, 7, v252\nv_lshlrev_b32 v96, 11, v96\nv_lshrrev_b32 v97, 8, v252\nv_lshlrev_b32 v97, 4, v97\n"
"v_and_b32 v98, 0x70, v252\nv_xor_b32 v98, v98, v97\nv_add_u32 v226, v96, v98\nv_xor_b32 v227, 64, v226\nG 0\ns_add_u32 m0, s51, 0\n"
"s_add_u32 s60, s70, 0\nbuffer_load_dwordx4 v226, s[24:27], s60 offen lds\ns_add_u32 m0, s51, 1024\ns_add_u32 s60, s70, 16384\nbuffer_load_dwordx4 v227, s[24:27], s60 offen lds\ns_add_u32 m0, s51, 2048\n"
"s_add_u32 s60, s70, 32768\nbuffer_load_dwordx4 v226, s[24:27], s60 offen lds\ns_add_u32 m0, s51, 3072\ns_add_u32 s60, s70, 49152\nbuffer_load_dwordx4 v227, s[24:27], s60 offen lds\ns_add_u32 m0, s51, 4096\n"
"s_add_u32 s60, s71, 0\nbuffer_load_dwordx4 v226, s[24:27], s60 offen lds\ns_add_u32 m0, s51, 5120\ns_add_u32 s60, s71, 16384\nbuffer_load_dwordx4 v227, s[24:27], s60 offen lds\ns_add_u32 m0, s51, 6144\n"
"s_add_u32 s60, s71, 32768\nbuffer_load_dwordx4 v226, s[24:27], s60 offen lds\ns_add_u32 m0, s51, 7168\ns_add_u32 s60, s71, 49152\nbuffer_load_dwordx4 v227, s[24:27], s60 offen lds\nbuffer_load_dwordx2 v[230:231], off, s[20:23], 0 sc1\n"
"v_bfe_u32 v96, v252, 5, 2\nv_bfe_u32 v97, v252, 8, 2\nv_xor_b32 v96, v96, v97\ns_and_b32 s60, s40, 1\ns_lshl_b32 s60, s60, 4\ns_and_b32 s61, s40, 2\n"
"s_lshl1_add_u32 s60, s61, s60\nv_bfe_u32 v97, v252, 7, 2\nv_lshrrev_b32 v98, 9, v252\nv_lshl_add_u32 v97, v98, 3, v97\nv_add_u32 v97, s60, v97\nv_mul_u32_u24 v97, 6144, v97\n"
"v_lshl_add_u32 v97, v96, 5, v97\nv_and_b32 v96, 16, v252\nv_add_u32 v220, v96, v97\nv_add_u32 v221, 3144704, v220\ns_add_u32 m0, s50, 0\ns_add_u32 s63, s52, 2048\n"
"buffer_load_dwordx4 v220, s[4:7], s63 offen lds\nbuffer_load_dwordx4 v221, s[4:7], s63 offen offset:1024 lds\ns_add_u32 m0, s50, 8192\ns_add_u32 s63, s52, 198656\nbuffer_load_dwordx4 v220, s[4:7], s63 offen lds\nbuffer_load_dwordx4 v221, s[4:7], s63 offen offset:1024 lds\n"
"s_lshl2_add_u32 s60, s41, s40\ns_mul_i32 s79, s60, 2176\ns_and_b32 s61, s60, -2\ns_mul_i32 s80, s61, 2176\ns_lshl2_add_u32 s61, s61, s43\ns_mul_i32 s82, s61, 640\n"
"s_mul_i32 s61, s43, 28672\ns_mul_i32 s73, s60, 229376\ns_add_u32 s73, s73, s61\ns_mul_i32 s62, s90, 14336\ns_add_u32 s68, s73, s62\ns_add_u32 s61, s61, s62\n"
"s_xor_b32 s62, s60, 1\ns_mul_i32 s69, s62, 229376\ns_add_u32 s69, s69, s61\ns_mul_i32 s61, s44, 7168\ns_add_u32 s73, s73, s61\ns_lshl2_add_u32 s61, s60, s43\n"
"s_mul_i32 s81, s61, 640\ns_lshl2_add_u32 s61, s60, s44\ns_mul_i32 s74, s61, 640\ns_lshl2_add_u32 s61, s41, s44\ns_mul_i32 s61, s61, 48\ns_add_u32 s61, s61, s40\n"
"s_lshl_b32 s72, s61, 12\ns_mul_i32 s55, s42, 448\ns_add_u32 s54, s55, 64\ns_and_b32 s54, s54, 0xffffff80\ns_bitcmp0_b32 s42, 0\ns_cselect_b32 s61, 384, 0\n"
"s_add_u32 s55, s55, s61\ns_lshl_b32 s61, s41, 9\ns_lshl_b32 s91, s42, 13\ns_add_u32 s91, s91, s61\ns_add_u32 s91, s91, 83886080\ns_mul_i32 s61, s43, 1835008\n"
"s_lshl_b32 s62, s44, 9\ns_add_u32 s61, s61, s62\ns_add_u32 s62, s43, 1\ns_and_b32 s62, s62, 3\ns_lshl_b32 s62, s62, 7\ns_add_u32 s56, s61, s62\n"
"s_add_u32 s62, s43, 2\ns_and_b32 s62, s62, 3\ns_lshl_b32 s62, s62, 7\ns_add_u32 s57, s61, s62\ns_add_u32 s62, s43, 3\ns_and_b32 s62, s62, 3\n"
"s_lshl_b32 s62, s62, 7\ns_add_u32 s58, s61, s62\ns_add_u32 s62, s43, 4\ns_and_b32 s62, s62, 3\ns_lshl_b32 s62, s62, 7\ns_add_u32 s59, s61, s62\n"
"s_mov_b32 s76, 0x3f803f80\ns_mov_b32 s77, 0xffff0000\ns_mov_b32 s48, 131072\ns_cmp_eq_u32 s60, 0\ns_cselect_b32 s78, 0xf149f2ca, 0\nv_bfe_u32 v96, v252, 6, 3\n"
"v_lshlrev_b32 v96, 7, v96\nv_lshrrev_b32 v97, 9, v252\nv_lshl_add_u32 v96, v97, 11, v96\nv_bfe_u32 v97, v252, 4, 2\nv_lshl_add_u32 v96, v97, 3, v96\ns_lshl_b32 s60, s90, 10\n"
"s_add_u32 s60, s60, 32768\nv_add_u32 v96, s60, v96\nv_bfe_u32 v97, v252, 7, 2\nv_xor_b32 v98, 0, v97\nv_lshl_add_u32 v184, v98, 5, v96\nv_add_u32 v188, 0x10000, v184\n"
"v_xor_b32 v98, 1, v97\nv_lshl_add_u32 v185, v98, 5, v96\nv_add_u32 v189, 0x10000, v185\nv_xor_b32 v98, 2, v97\nv_lshl_add_u32 v186, v98, 5, v96\nv_add_u32 v190, 0x10000, v186\n"
"v_xor_b32 v98, 3, v97\nv_lshl_add_u32 v187, v98, 5, v96\nv_add_u32 v191, 0x10000, v187\nv_lshrrev_b32 v96, 2, v252\ns_xor_b32 s60, s51, 0x2000\nv_add_u32 v225, s60, v96\n"
"v_mov_b32 v210, 0\nv_mov_b32 v211, 0\nv_bfe_u32 v96, v252, 5, 2\nv_bfe_u32 v97, v252, 8, 2\nv_xor_b32 v96, v96, v97\nv_lshrrev_b32 v97, 7, v252\n"
"s_lshl_b32 s60, s40, 3\nv_add_u32 v97, s60, v97\nv_mov_b32 v228, v97\nv_and_b32 v97, 16, v252\nv_lshl_add_u32 v229, v96, 5, v97\nv_bfe_u32 v96, v252, 5, 2\n"
"v_bfe_u32 v97, v252, 8, 2\nv_xor_b32 v96, v96, v97\nv_lshrrev_b32 v97, 7, v252\ns_lshl_b32 s60, s40, 3\nv_add_u32 v97, s60, v97\nv_lshrrev_b32 v98, 1, v96\n"
"v_lshl_add_u32 v97, v98, 5, v97\nv_and_b32 v96, 1, v96\nv_mul_u32_u24 v97, 7168, v97\nv_lshl_add_u32 v97, v96, 5, v97\nv_and_b32 v96, 16, v252\nv_add_u32 v222, v96, v97\n"
"v_add_u32 v223, 3668992, v222\nv_bfe_u32 v96, v252, 4, 4\nv_lshrrev_b32 v97, 8, v252\nv_lshrrev_b32 v98, 1, v96\nv_xor_b32 v97, v97, v98\nv_lshlrev_b32 v96, 7, v96\n"
"v_lshl_add_u32 v96, v97, 4, v96\nv_add_u32 v248, s51, v96\nv_xor_b32 v249, 64, v248\nv_add_u32 v250, 122880, v248\nv_add_u32 v251, 122880, v249\ns_add_u32 m0, s51, 122880\n"
"s_add_u32 s60, s70, 128\nbuffer_load_dwordx4 v226, s[24:27], s60 offen lds\ns_add_u32 m0, s51, 123904\ns_add_u32 s60, s70, 16512\nbuffer_load_dwordx4 v227, s[24:27], s60 offen lds\ns_add_u32 m0, s51, 124928\n"
"s_add_u32 s60, s70, 32896\nbuffer_load_dwordx4 v226, s[24:27], s60 offen lds\ns_add_u32 m0, s51, 125952\ns_add_u32 s60, s70, 49280\nbuffer_load_dwordx4 v227, s[24:27], s60 offen lds\ns_add_u32 m0, s51, 126976\n"
"s_add_u32 s60, s71, 128\nbuffer_load_dwordx4 v226, s[24:27], s60 offen lds\ns_add_u32 m0, s51, 128000\ns_add_u32 s60, s71, 16512\nbuffer_load_dwordx4 v227, s[24:27], s60 offen lds\ns_add_u32 m0, s51, 129024\n"
"s_add_u32 s60, s71, 32896\nbuffer_load_dwordx4 v226, s[24:27], s60 offen lds\ns_add_u32 m0, s51, 130048\ns_add_u32 s60, s71, 49280\nbuffer_load_dwordx4 v227, s[24:27], s60 offen lds\ns_add_u32 m0, s50, 16384\n"
"s_add_u32 s63, s52, 4096\nbuffer_load_dwordx4 v220, s[4:7], s63 offen lds\nbuffer_load_dwordx4 v221, s[4:7], s63 offen offset:1024 lds\ns_add_u32 m0, s50, 24576\ns_add_u32 s63, s52, 200704\nbuffer_load_dwordx4 v220, s[4:7], s63 offen lds\n"
"buffer_load_dwordx4 v221, s[4:7], s63 offen offset:1024 lds\ns_add_u32 m0, s50, 32768\ns_add_u32 s63, s101, 2048\nbuffer_load_dwordx4 v220, s[4:7], s63 offen lds\nbuffer_load_dwordx4 v221, s[4:7], s63 offen offset:1024 lds\ns_add_u32 m0, s50, 40960\n"
"s_add_u32 s63, s101, 4096\nbuffer_load_dwordx4 v220, s[4:7], s63 offen lds\nbuffer_load_dwordx4 v221, s[4:7], s63 offen offset:1024 lds\nv_bfe_u32 v100, v252, 4, 4\nv_lshlrev_b32 v100, 11, v100\nv_lshrrev_b32 v101, 8, v252\n"
"v_lshl_add_u32 v100, v101, 3, v100\ns_add_u32 s60, s49, s53\ns_add_u32 s61, s60, 32768\nbuffer_load_dwordx2 v[192:193], v100, s[24:27], s60 offen offset:0\nbuffer_load_dwordx2 v[194:195], v100, s[24:27], s60 offen offset:32\nbuffer_load_dwordx2 v[196:197], v100, s[24:27], s60 offen offset:64\n"
"buffer_load_dwordx2 v[198:199], v100, s[24:27], s60 offen offset:96\nbuffer_load_dwordx2 v[200:201], v100, s[24:27], s61 offen offset:0\nbuffer_load_dwordx2 v[202:203], v100, s[24:27], s61 offen offset:32\nbuffer_load_dwordx2 v[204:205], v100, s[24:27], s61 offen offset:64\nbuffer_load_dwordx2 v[206:207], v100, s[24:27], s61 offen offset:96\nv_mov_b32 v106, 0xf149f149\n"
"v_mov_b32 v104, 0xffff\nv_mov_b32 v105, 0xffff0000\ns_mov_b32 s60, 0x50000\ns_mov_b32 s61, 0x5550055\nv_cndmask_b32 v96, 0, v104, s[60:61]\ns_mov_b32 s60, 0xa0000\n"
"s_mov_b32 s61, 0xaaa00aa\nv_cndmask_b32 v96, v96, v105, s[60:61]\nv_bfi_b32 v100, v96, 0, v106\ns_mov_b32 s60, 0x150001\ns_mov_b32 s61, 0x15550155\nv_cndmask_b32 v97, 0, v104, s[60:61]\n"
"s_mov_b32 s60, 0x2a0002\ns_mov_b32 s61, 0x2aaa02aa\nv_cndmask_b32 v97, v97, v105, s[60:61]\nv_bfi_b32 v101, v97, 0, v106\ns_mov_b32 s60, 0x55505555\ns_mov_b32 s61, 0x50005500\n"
"v_cndmask_b32 v98, 0, v104, s[60:61]\ns_mov_b32 s60, 0xaaa0aaaa\ns_mov_b32 s61, 0xa000aa00\nv_cndmask_b32 v98, v98, v105, s[60:61]\nv_bfi_b32 v102, v98, 0, v106\ns_mov_b32 s60, 0x55405554\n"
"s_mov_b32 s61, 0x40005400\nv_cndmask_b32 v99, 0, v104, s[60:61]\ns_mov_b32 s60, 0xaa80aaa8\ns_mov_b32 s61, 0x8000a800\nv_cndmask_b32 v99, v99, v105, s[60:61]\nv_bfi_b32 v103, v99, 0, v106\n"
"v_add_u32 v104, 161792, v252\nds_write_b128 v104, v[96:99] offset:0\nds_write_b128 v104, v[100:103] offset:1024\ns_waitcnt vmcnt(28)\nv_readfirstlane_b32 s83, v230\nv_readfirstlane_b32 s60, v231\n"
"s_mul_i32 s46, s83, 0x9e3779b1\ns_add_u32 s46, s46, 0x7f4a7c15\ns_xor_b32 s47, s83, s60\ns_mul_i32 s47, s47, 0x85ebca6b\ns_add_u32 s47, s47, 0xc2b2ae35\nv_mov_b32 v144, -1\n"
"v_mov_b32 v145, -1\nv_mov_b32 v146, -1\nv_mov_b32 v147, -1\nds_read_b128 a[0:3], v248 offset:0\nds_read_b128 a[4:7], v248 offset:2048\nds_read_b128 a[8:11], v248 offset:4096\n"
"ds_read_b128 a[12:15], v248 offset:6144\nds_read_b128 a[16:19], v249 offset:0\nds_read_b128 a[20:23], v249 offset:2048\nds_read_b128 a[24:27], v249 offset:4096\nds_read_b128 a[28:31], v249 offset:6144\ns_waitcnt vmcnt(12) lgkmcnt(8)\n"
"s_barrier\nR 160,184,0\nR 162,184,4096\nR 164,184,8192\nR 166,184,12288\nR 168,185,0\n"
"R 170,185,4096\nR 172,185,8192\nR 174,185,12288\nR 176,186,0\nR 178,186,4096\nR 180,186,8192\n"
"R 182,186,12288\nG 10\nv_mfma_f32_16x16x32_bf16 v[0:3], v[160:163], a[0:3], 0\ns_add_u32 m0, s50, 49152\ns_add_u32 s63, s52, 395264\nbuffer_load_dwordx4 v220, s[4:7], s63 offen lds\n"
"G 8\nM 0,164,16\nbuffer_load_dwordx4 v221, s[4:7], s63 offen offset:1024 lds\nv_mfma_f32_16x16x32_bf16 v[16:19], v[160:163], a[4:7], 0\ns_add_u32 m0, s51, 0\ns_add_u32 s60, s70, 256\n"
"buffer_load_dwordx4 v226, s[24:27], s60 offen lds\nM 16,164,20\ns_lshl4_add_u32 s62, s41, s42\ns_lshl_b32 s62, s62, 12\ns_add_u32 s62, s62, 77594624\ns_lshl_b32 s60, s40, 10\n"
"s_add_u32 s62, s62, s60\nbuffer_store_dwordx4 v[144:147], v252, s[20:23], s62 offen sc1\ns_add_u32 m0, s50, 57344\ns_add_u32 s63, s52, 591872\nv_mfma_f32_16x16x32_bf16 v[32:35], v[160:163], a[8:11], 0\nbuffer_load_dwordx4 v220, s[4:7], s63 offen lds\n"
"M 32,164,24\nbuffer_load_dwordx4 v221, s[4:7], s63 offen offset:1024 lds\nv_mfma_f32_16x16x32_bf16 v[48:51], v[160:163], a[12:15], 0\ns_add_u32 m0, s51, 1024\ns_add_u32 s60, s70, 16640\nbuffer_load_dwordx4 v227, s[24:27], s60 offen lds\n"
"M 48,164,28\ns_add_u32 m0, s50, 65536\ns_add_u32 s63, s52, 397312\nbuffer_load_dwordx4 v220, s[4:7], s63 offen lds\nG 4\nv_mfma_f32_16x16x32_bf16 v[52:55], v[168:171], a[12:15], 0\n"
"R 160,187,0\nM 52,172,28\nbuffer_load_dwordx4 v221, s[4:7], s63 offen offset:1024 lds\nv_mfma_f32_16x16x32_bf16 v[36:39], v[168:171], a[8:11], 0\nR 162,187,4096\nM 36,172,24\n"
"s_add_u32 m0, s51, 2048\ns_add_u32 s60, s70, 33024\nbuffer_load_dwordx4 v226, s[24:27], s60 offen lds\nv_mfma_f32_16x16x32_bf16 v[20:23], v[168:171], a[4:7], 0\nR 164,187,8192\ns_add_u32 m0, s50, 73728\n"
"M 20,172,20\ns_lshl4_add_u32 s62, s41, s42\ns_lshl_b32 s62, s62, 12\ns_add_u32 s62, s62, 78643200\ns_lshl_b32 s60, s40, 10\ns_add_u32 s62, s62, s60\n"
"buffer_store_dwordx4 v[144:147], v252, s[20:23], s62 offen sc1\ns_add_u32 s63, s52, 593920\nv_mfma_f32_16x16x32_bf16 v[4:7], v[168:171], a[0:3], 0\nR 166,187,12288\nbuffer_load_dwordx4 v220, s[4:7], s63 offen lds\nM 4,172,16\n"
"G 4\nv_mfma_f32_16x16x32_bf16 v[8:11], v[176:179], a[0:3], 0\nR 168,184,16384\nM 8,180,16\ns_add_u32 m0, s51, 3072\ns_add_u32 s60, s70, 49408\n"
"buffer_load_dwordx4 v227, s[24:27], s60 offen lds\nv_mfma_f32_16x16x32_bf16 v[24:27], v[176:179], a[4:7], 0\nR 170,184,20480\ns_add_u32 m0, s50, 73728\ns_nop 0\nbuffer_load_dwordx4 v221, s[4:7], s63 offen offset:1024 lds\n"
"M 24,180,20\ns_add_u32 m0, s51, 4096\ns_add_u32 s60, s71, 256\nbuffer_load_dwordx4 v226, s[24:27], s60 offen lds\nv_mfma_f32_16x16x32_bf16 v[40:43], v[176:179], a[8:11], 0\nR 172,184,24576\n"
"M 40,180,24\nv_mfma_f32_16x16x32_bf16 v[56:59], v[176:179], a[12:15], 0\nR 174,184,28672\nM 56,180,28\nG 4\nv_mfma_f32_16x16x32_bf16 v[60:63], v[160:163], a[12:15], 0\n"
"R 176,185,16384\nM 60,164,28\ns_add_u32 m0, s51, 5120\ns_add_u32 s60, s71, 16640\nbuffer_load_dwordx4 v227, s[24:27], s60 offen lds\nv_mfma_f32_16x16x32_bf16 v[44:47], v[160:163], a[8:11], 0\n"
"R 178,185,20480\nM 44,164,24\ns_add_u32 m0, s51, 6144\ns_add_u32 s60, s71, 33024\nbuffer_load_dwordx4 v226, s[24:27], s60 offen lds\nv_mfma_f32_16x16x32_bf16 v[28:31], v[160:163], a[4:7], 0\n"
"R 180,185,24576\ns_add_u32 m0, s50, 81920\ns_add_u32 s63, s101, 0\nM 28,164,20\nbuffer_load_dwordx4 v220, s[4:7], s63 offen lds\nv_mfma_f32_16x16x32_bf16 v[12:15], v[160:163], a[0:3], 0\n"
"R 182,185,28672\nM 12,164,16\ns_waitcnt vmcnt(12)\ns_barrier\nG 4\nv_mfma_f32_16x16x32_bf16 v[64:67], a[0:3], v[168:171], 0\n"
"R 160,186,16384\nN 64,16,172\ns_add_u32 m0, s51, 7168\ns_add_u32 s60, s71, 49408\nbuffer_load_dwordx4 v227, s[24:27], s60 offen lds\nv_mfma_f32_16x16x32_bf16 v[68:71], a[4:7], v[168:171], 0\n"
"R 162,186,20480\ns_add_u32 m0, s50, 81920\nN 68,20,172\nbuffer_load_dwordx4 v221, s[4:7], s63 offen offset:1024 lds\nv_mfma_f32_16x16x32_bf16 v[96:99], a[8:11], v[168:171], 0\nR 164,186,24576\n"
"s_add_u32 m0, s50, 0\ns_add_u32 s63, s52, 788480\nN 96,24,172\nbuffer_load_dwordx4 v220, s[4:7], s63 offen lds\nv_mfma_f32_16x16x32_bf16 v[100:103], a[12:15], v[168:171], 0\nR 166,186,28672\n"
"N 100,28,172\nG 4\nv_mfma_f32_16x16x32_bf16 v[108:111], a[12:15], v[176:179], 0\nR 168,187,16384\nv_mov_b32 v242, s76\nv_mov_b32 v243, s76\n"
"N 108,28,180\nbuffer_load_dwordx4 v221, s[4:7], s63 offen offset:1024 lds\nv_mov_b32 v244, s76\nv_mfma_f32_16x16x32_bf16 v[104:107], a[8:11], v[176:179], 0\nR 170,187,20480\ns_add_u32 s63, s52, 985088\n"
"N 104,24,180\ns_add_u32 m0, s50, 8192\ns_lshl_b32 s60, s40, 5\nv_xor_b32 v238, s60, v184\nv_mfma_f32_16x16x32_bf16 v[76:79], a[4:7], v[176:179], 0\nR 172,187,24576\n"
"v_and_b32 v238, 0xfffffbff, v238\nN 76,20,180\nbuffer_load_dwordx4 v220, s[4:7], s63 offen lds\nv_mfma_f32_16x16x32_bf16 v[72:75], a[0:3], v[176:179], 0\nR 174,187,28672\nv_mov_b32 v245, s76\n"
"N 72,16,180\nG 4\nv_mfma_f32_16x16x32_bf16 v[80:83], a[0:3], v[160:163], 0\nR 176,184,49152\nds_read_b128 a[32:35], v250 offset:0\nR 128,238,32768\n"
"R 130,238,36864\nN 80,16,164\nds_read_b128 a[36:39], v250 offset:2048\nv_mfma_f32_16x16x32_bf16 v[84:87], a[4:7], v[160:163], 0\nR 178,184,53248\nds_read_b128 a[40:43], v250 offset:4096\n"
"R 132,238,33792\nR 134,238,37888\nN 84,20,164\nds_read_b128 a[44:47], v250 offset:6144\nv_mfma_f32_16x16x32_bf16 v[112:115], a[8:11], v[160:163], 0\nR 180,184,57344\n"
"buffer_load_dwordx4 v221, s[4:7], s63 offen offset:1024 lds\nN 112,24,164\nds_read_b128 a[48:51], v251 offset:0\nv_mfma_f32_16x16x32_bf16 v[116:119], a[12:15], v[160:163], 0\nR 182,184,61440\nds_read_b128 a[52:55], v251 offset:2048\n"
"N 116,28,164\nds_read_b128 a[56:59], v251 offset:4096\nG 15\nv_mfma_f32_16x16x32_bf16 v[124:127], a[12:15], v[168:171], 0\nR 160,185,49152\nG 12\n"
"v_mfma_f32_16x16x32_bf16 v[136:139], v[242:245], v[128:131], 0\nN 124,28,172\nds_read_b128 a[60:63], v251 offset:6144\nG 8\nv_mfma_f32_16x16x32_bf16 v[136:139], v[242:245], v[132:135], v[136:139]\nv_mfma_f32_16x16x32_bf16 v[120:123], a[8:11], v[168:171], 0\n"
"R 162,185,53248\nN 120,24,172\nv_mfma_f32_16x16x32_bf16 v[92:95], a[4:7], v[168:171], 0\nR 164,185,57344\nN 92,20,172\nG 2\n"
"s_add_u32 m0, s51, 122880\ns_add_u32 s60, s70, 384\nbuffer_load_dwordx4 v226, s[24:27], s60 offen lds\nv_mfma_f32_16x16x32_bf16 v[88:91], a[0:3], v[168:171], 0\nR 166,185,61440\nv_lshrrev_b32 v128, 1, v252\n"
"v_mov_b32 v137, s46\nN 88,16,172\ns_waitcnt vmcnt(11)\ns_barrier\nM 0,176,32\nR 168,186,49152\n"
"s_lshl_b32 s60, s40, 5\nv_xor_b32 v238, s60, v184\nM 0,180,48\ns_add_u32 m0, s51, 123904\ns_add_u32 s60, s70, 16768\nbuffer_load_dwordx4 v227, s[24:27], s60 offen lds\n"
"s_and_b32 s61, s40, 3\ns_lshl_b32 s61, s61, 7\ns_add_u32 s61, s61, s91\ns_add_u32 s61, s61, 131072\ns_mov_b64 exec, 0xffff\nbuffer_store_dwordx2 v[136:137], v128, s[20:23], s61 offen sc1\n"
"s_mov_b64 exec, -1\nM 16,176,36\nR 170,186,53248\ns_add_u32 m0, s50, 16384\ns_add_u32 s63, s52, 790528\nM 16,180,52\n"
"buffer_load_dwordx4 v220, s[4:7], s63 offen lds\nM 32,176,40\nR 172,186,57344\nv_and_b32 v238, 0xfffffbff, v238\nM 32,180,56\ns_add_u32 m0, s51, 124928\n"
"s_add_u32 s60, s70, 33152\nbuffer_load_dwordx4 v226, s[24:27], s60 offen lds\nM 48,176,44\nR 174,186,61440\ns_add_u32 m0, s50, 16384\nv_mov_b32 v230, 0\n"
"M 48,180,60\nbuffer_load_dwordx4 v221, s[4:7], s63 offen offset:1024 lds\nG 4\nv_mov_b32 v231, 0\nM 52,160,44\nR 176,187,49152\n"
"R 128,238,40960\nR 130,238,45056\nv_mov_b32 v232, 0\nM 52,164,60\ns_add_u32 m0, s51, 125952\ns_add_u32 s60, s70, 49536\n"
"buffer_load_dwordx4 v227, s[24:27], s60 offen lds\nv_mov_b32 v233, 0\nM 36,160,40\nR 178,187,53248\nR 132,238,41984\nR 134,238,46080\n"
"s_add_u32 m0, s50, 24576\ns_add_u32 s63, s52, 987136\nv_dot2c_f32_bf16 v230, s76, v192\nM 36,164,56\nbuffer_load_dwordx4 v220, s[4:7], s63 offen lds\nv_dot2c_f32_bf16 v231, v192, v192\n"
"M 20,160,36\nR 180,187,57344\nv_lshrrev_b32 v234, 1, v252\nv_dot2c_f32_bf16 v232, s76, v200\nM 20,164,52\ns_add_u32 m0, s51, 126976\n"
"s_add_u32 s60, s71, 384\nbuffer_load_dwordx4 v226, s[24:27], s60 offen lds\nv_dot2c_f32_bf16 v233, v200, v200\nM 4,160,32\nR 182,187,61440\ns_add_u32 m0, s50, 24576\n"
"v_dot2c_f32_bf16 v230, s76, v193\nM 4,164,48\nG 8\nv_dot2c_f32_bf16 v231, v193, v193\nM 8,168,32\nR 160,188,0\n"
"G 6\nv_mfma_f32_16x16x32_bf16 v[136:139], v[242:245], v[128:131], 0\nbuffer_load_dwordx4 v221, s[4:7], s63 offen offset:1024 lds\nv_dot2c_f32_bf16 v232, s76, v201\nM 8,172,48\ns_add_u32 m0, s51, 128000\n"
"s_add_u32 s60, s71, 16768\nbuffer_load_dwordx4 v227, s[24:27], s60 offen lds\nG 3\nv_mfma_f32_16x16x32_bf16 v[136:139], v[242:245], v[132:135], v[136:139]\nv_dot2c_f32_bf16 v233, v201, v201\nM 24,168,36\n"
"R 162,188,4096\nv_dot2c_f32_bf16 v230, s76, v194\nM 24,172,52\nv_dot2c_f32_bf16 v231, v194, v194\nM 40,168,40\nR 164,188,8192\n"
"v_dot2c_f32_bf16 v232, s76, v202\nM 40,172,56\ns_add_u32 m0, s51, 129024\ns_add_u32 s60, s71, 33152\nbuffer_load_dwordx4 v226, s[24:27], s60 offen lds\nv_dot2c_f32_bf16 v233, v202, v202\n"
"M 56,168,44\nR 166,188,12288\nv_lshrrev_b32 v128, 1, v252\nv_dot2c_f32_bf16 v230, s76, v195\nM 56,172,60\nG 4\n"
"v_mov_b32 v137, s46\nv_dot2c_f32_bf16 v231, v195, v195\nM 60,176,44\nR 168,189,0\nv_dot2c_f32_bf16 v232, s76, v203\nM 60,180,60\n"
"s_add_u32 m0, s51, 130048\ns_add_u32 s60, s71, 49536\nbuffer_load_dwordx4 v227, s[24:27], s60 offen lds\ns_and_b32 s61, s40, 3\ns_lshl_b32 s61, s61, 7\ns_add_u32 s61, s61, s91\n"
"s_add_u32 s61, s61, 262144\ns_mov_b64 exec, 0xffff\nbuffer_store_dwordx2 v[136:137], v128, s[20:23], s61 offen sc1\ns_mov_b64 exec, -1\nv_dot2c_f32_bf16 v233, v203, v203\nM 44,176,40\n"
"R 170,189,4096\nv_dot2c_f32_bf16 v230, s76, v196\nM 44,180,56\nv_dot2c_f32_bf16 v231, v196, v196\nM 28,176,36\nR 172,189,8192\n"
"v_dot2c_f32_bf16 v232, s76, v204\nM 28,180,52\ns_add_u32 m0, s50, 32768\nv_dot2c_f32_bf16 v233, v204, v204\nM 12,176,32\nR 174,189,12288\n"
"v_dot2c_f32_bf16 v230, s76, v197\nM 12,180,48\ns_waitcnt vmcnt(14)\ns_barrier\nG 4\nv_dot2c_f32_bf16 v231, v197, v197\n"
"N 64,32,160\nR 176,190,0\nv_dot2c_f32_bf16 v232, s76, v205\nN 64,48,164\ns_add_u32 s63, s52, 1181696\nv_dot2c_f32_bf16 v233, v205, v205\n"
"N 68,36,160\nR 178,190,4096\nv_dot2c_f32_bf16 v230, s76, v198\nN 68,52,164\nbuffer_load_dwordx4 v220, s[4:7], s63 offen lds\nv_dot2c_f32_bf16 v231, v198, v198\n"
"N 96,40,160\nR 180,190,8192\nds_read_b128 a[64:67], v248 offset:0\nv_dot2c_f32_bf16 v232, s76, v206\nN 96,56,164\nds_read_b128 a[68:71], v248 offset:2048\n"
"v_dot2c_f32_bf16 v233, v206, v206\nN 100,44,160\nR 182,190,12288\nds_read_b128 a[72:75], v248 offset:4096\nv_dot2c_f32_bf16 v230, s76, v199\nN 100,60,164\n"
"ds_read_b128 a[76:79], v248 offset:6144\nG 8\nv_dot2c_f32_bf16 v231, v199, v199\nN 108,44,168\nR 160,191,0\ns_nop 0\n"
"v_permlane32_swap_b32 v230, v231\nv_dot2c_f32_bf16 v232, s76, v207\nN 108,60,172\nds_read_b128 a[80:83], v249 offset:0\nbuffer_load_dwordx4 v221, s[4:7], s63 offen offset:1024 lds\nv_dot2c_f32_bf16 v233, v207, v207\n"
"N 104,40,168\nR 162,191,4096\nds_read_b128 a[84:87], v249 offset:2048\nN 104,56,172\nds_read_b128 a[88:91], v249 offset:4096\ns_add_u32 m0, s50, 40960\n"
"N 76,36,168\nR 164,191,8192\ns_add_u32 s63, s52, 1378304\nN 76,52,172\nds_read_b128 a[92:95], v249 offset:6144\nv_permlane32_swap_b32 v232, v233\n"
"N 72,32,168\nR 166,191,12288\nbuffer_load_dwordx4 v220, s[4:7], s63 offen lds\nN 72,48,172\nG 10\nv_add_f32 v230, v230, v231\n"
"v_add_f32 v232, v232, v233\nN 80,32,176\nR 168,184,0\nv_mov_b32 v231, s46\nN 80,48,180\nG 2\n"
"s_add_u32 m0, s51, 0\ns_add_u32 s60, s70, 512\nbuffer_load_dwordx4 v226, s[24:27], s60 offen lds\nN 84,36,176\nR 170,184,4096\ns_add_u32 m0, s50, 40960\n"
"s_nop 0\nbuffer_load_dwordx4 v221, s[4:7], s63 offen offset:1024 lds\nN 84,52,180\nv_permlane16_swap_b32 v230, v232\nv_add_f32 v230, v230, v232\nN 112,40,176\n"
"R 172,184,8192\nN 112,56,180\ns_add_u32 m0, s51, 1024\ns_add_u32 s60, s70, 16896\nbuffer_load_dwordx4 v227, s[24:27], s60 offen lds\nN 116,44,176\n"
"R 174,184,12288\nN 116,60,180\nG 4\nN 124,44,160\nR 176,185,0\nN 124,60,164\n"
"s_add_u32 m0, s51, 2048\ns_add_u32 s60, s70, 33280\nbuffer_load_dwordx4 v226, s[24:27], s60 offen lds\nN 120,40,160\nR 178,185,4096\nN 120,56,164\n"
"s_lshl_b32 s60, s40, 5\nv_xor_b32 v238, s60, v188\nv_and_b32 v238, 0xfffffbff, v238\nN 92,36,160\nR 180,185,8192\nN 92,52,164\n"
"s_add_u32 m0, s51, 3072\ns_add_u32 s60, s70, 49664\nbuffer_load_dwordx4 v227, s[24:27], s60 offen lds\ns_lshr_b32 s61, s49, 3\ns_lshl2_add_u32 s61, s53, s61\ns_add_u32 s61, s61, 81788928\n"
"buffer_store_dwordx2 v[230:231], v234, s[20:23], s61 offen sc1\nN 88,32,160\nR 182,185,12288\nN 88,48,164\ns_waitcnt vmcnt(13)\ns_barrier\n"
"G 4\nM 0,168,64\nR 160,186,0\nM 0,172,80\ns_add_u32 m0, s51, 4096\ns_add_u32 s60, s71, 512\n"
"buffer_load_dwordx4 v226, s[24:27], s60 offen lds\nM 16,168,68\nR 162,186,4096\nR 128,238,16384\nR 130,238,20480\ns_add_u32 m0, s50, 49152\n"
"s_add_u32 s63, s52, 1183744\nM 16,172,84\nbuffer_load_dwordx4 v220, s[4:7], s63 offen lds\nM 32,168,72\nR 164,186,8192\nR 132,238,17408\n"
"R 134,238,21504\nM 32,172,88\ns_add_u32 m0, s51, 5120\ns_add_u32 s60, s71, 16896\nbuffer_load_dwordx4 v227, s[24:27], s60 offen lds\nM 48,168,76\n"
"R 166,186,12288\ns_add_u32 m0, s50, 49152\nM 48,172,92\nG 8\nM 52,176,76\nR 168,187,0\n"
"buffer_load_dwordx4 v221, s[4:7], s63 offen offset:1024 lds\nM 52,180,92\ns_add_u32 m0, s51, 6144\ns_add_u32 s60, s71, 33280\nbuffer_load_dwordx4 v226, s[24:27], s60 offen lds\nM 36,176,72\n"
"R 170,187,4096\nG 6\nv_mfma_f32_16x16x32_bf16 v[136:139], v[242:245], v[128:131], 0\ns_add_u32 m0, s50, 57344\ns_add_u32 s63, s52, 1380352\nM 36,180,88\n"
"G 3\nv_mfma_f32_16x16x32_bf16 v[136:139], v[242:245], v[132:135], v[136:139]\nbuffer_load_dwordx4 v220, s[4:7], s63 offen lds\nM 20,176,68\nR 172,187,8192\nM 20,180,84\n"
"s_add_u32 m0, s51, 7168\ns_add_u32 s60, s71, 49664\nbuffer_load_dwordx4 v227, s[24:27], s60 offen lds\nM 4,176,64\nR 174,187,12288\ns_add_u32 m0, s50, 57344\n"
"M 4,180,80\nG 4\nM 8,160,64\nR 176,184,16384\nbuffer_load_dwordx4 v221, s[4:7], s63 offen offset:1024 lds\nM 8,164,80\n"
"v_lshrrev_b32 v128, 1, v252\nv_mov_b32 v137, s46\nM 24,160,68\nR 178,184,20480\nM 24,164,84\ns_and_b32 s61, s40, 3\n"
"s_lshl_b32 s61, s61, 7\ns_add_u32 s61, s61, s91\ns_add_u32 s61, s61, 0\ns_mov_b64 exec, 0xffff\nbuffer_store_dwordx2 v[136:137], v128, s[20:23], s61 offen sc1\ns_mov_b64 exec, -1\n"
"M 40,160,72\nR 180,184,24576\nM 40,164,88\nM 56,160,76\nR 182,184,28672\nM 56,164,92\n"
"G 4\nM 60,168,76\nR 160,185,16384\nM 60,172,92\ns_add_u32 m0, s50, 65536\nM 44,168,72\n"
"R 162,185,20480\nM 44,172,88\ns_add_u32 s63, s52, 1574912\nM 28,168,68\nR 164,185,24576\nM 28,172,84\n"
"buffer_load_dwordx4 v220, s[4:7], s63 offen lds\nM 12,168,64\nR 166,185,28672\nM 12,172,80\ns_waitcnt vmcnt(14)\ns_barrier\n"
"G 4\nN 64,64,176\nR 168,186,16384\nN 64,80,180\nv_lshrrev_b32 v230, 7, v252\nN 68,68,176\n"
"R 170,186,20480\nN 68,84,180\nbuffer_load_dwordx4 v221, s[4:7], s63 offen offset:1024 lds\nv_lshlrev_b32 v230, 9, v230\nN 96,72,176\nR 172,186,24576\n"
"s_add_u32 m0, s50, 73728\nN 96,88,180\nds_read_b128 a[96:99], v250 offset:0\nN 100,76,176\nR 174,186,28672\nds_read_b128 a[100:103], v250 offset:2048\n"
"s_add_u32 s63, s52, 1771520\nN 100,92,180\nds_read_b128 a[104:107], v250 offset:4096\nG 7\nN 108,76,160\nR 176,187,16384\n"
"v_and_b32 v231, 0x70, v252\nN 108,92,164\nds_read_b128 a[108:111], v250 offset:6144\nbuffer_load_dwordx4 v220, s[4:7], s63 offen lds\nN 104,72,160\nR 178,187,20480\n"
"ds_read_b128 a[112:115], v251 offset:0\nN 104,88,164\nds_read_b128 a[116:119], v251 offset:2048\ns_and_b32 s60, s40, 3\ns_lshl_b32 s60, s60, 7\nv_add3_u32 v230, v230, v231, s60\n"
"N 76,68,160\nR 180,187,24576\nds_read_b128 a[120:123], v251 offset:4096\nN 76,84,164\nds_read_b128 a[124:127], v251 offset:6144\nN 72,64,160\n"
"R 182,187,28672\nN 72,80,164\ns_lshl_b32 s61, s42, 13\ns_add_u32 s61, s61, 84017152\nbuffer_load_dwordx4 v[128:131], v230, s[20:23], s61 offen sc1\nG 11\n"
"N 80,64,168\nR 160,184,32768\nbuffer_load_dwordx4 v221, s[4:7], s63 offen offset:1024 lds\nN 80,80,172\ns_lshl_b32 s61, s42, 13\ns_add_u32 s61, s61, 84021248\n"
"buffer_load_dwordx4 v[132:135], v230, s[20:23], s61 offen sc1\nN 84,68,168\nR 162,184,36864\nN 84,84,172\nN 112,72,168\nR 164,184,40960\n"
"s_waitcnt vmcnt(8)\nds_read_b128 a[128:131], v248 offset:0\nN 112,88,172\nds_read_b128 a[132:135], v248 offset:2048\nN 116,76,168\nR 166,184,45056\n"
"ds_read_b128 a[136:139], v248 offset:4096\nN 116,92,172\nds_read_b128 a[140:143], v248 offset:6144\nG 8\nN 124,76,176\nR 168,185,32768\n"
"N 124,92,180\nds_read_b128 a[144:147], v249 offset:0\nN 120,72,176\nR 170,185,36864\nds_read_b128 a[148:151], v249 offset:2048\nN 120,88,180\n"
"ds_read_b128 a[152:155], v249 offset:4096\nN 92,68,176\nR 172,185,40960\nN 92,84,180\nds_read_b128 a[156:159], v249 offset:6144\nN 88,64,176\n"
"R 174,185,45056\nN 88,80,180\ns_waitcnt vmcnt(7)\ns_barrier\nG 10\nM 0,160,96\n"
"R 176,186,32768\nM 0,164,112\nG 2\ns_add_u32 m0, s51, 0\ns_add_u32 s60, s70, 640\nbuffer_load_dwordx4 v226, s[24:27], s60 offen lds\n"
"M 16,160,100\nR 178,186,36864\nM 16,164,116\ns_add_u32 m0, s51, 1024\ns_add_u32 s60, s70, 17024\nbuffer_load_dwordx4 v227, s[24:27], s60 offen lds\n"
"M 32,160,104\nR 180,186,40960\ns_add_u32 m0, s50, 81920\ns_add_u32 s63, s52, 1576960\nbuffer_load_dwordx4 v220, s[4:7], s63 offen lds\nv_lshrrev_b32 v234, 7, v252\n"
"M 32,164,120\ns_add_u32 m0, s51, 2048\ns_add_u32 s60, s70, 33408\nbuffer_load_dwordx4 v226, s[24:27], s60 offen lds\nM 48,160,108\nR 182,186,45056\n"
"M 48,164,124\ns_add_u32 m0, s51, 3072\ns_add_u32 s60, s70, 49792\nbuffer_load_dwordx4 v227, s[24:27], s60 offen lds\nG 4\nM 52,168,108\n"
"R 160,187,32768\ns_add_u32 m0, s50, 81920\ns_nop 0\nbuffer_load_dwordx4 v221, s[4:7], s63 offen offset:1024 lds\nv_lshlrev_b32 v234, 9, v234\nM 52,172,124\n"
"s_add_u32 m0, s51, 4096\ns_add_u32 s60, s71, 640\nbuffer_load_dwordx4 v226, s[24:27], s60 offen lds\nM 36,168,104\nR 162,187,36864\nv_and_b32 v235, 0x70, v252\n"
"M 36,172,120\ns_add_u32 m0, s51, 5120\ns_add_u32 s60, s71, 17024\nbuffer_load_dwordx4 v227, s[24:27], s60 offen lds\nM 20,168,100\nR 164,187,40960\n"
"s_add_u32 m0, s50, 90112\ns_add_u32 s63, s52, 1773568\nbuffer_load_dwordx4 v220, s[4:7], s63 offen lds\nM 20,172,116\ns_add_u32 m0, s51, 6144\ns_add_u32 s60, s71, 33408\n"
"buffer_load_dwordx4 v226, s[24:27], s60 offen lds\nM 4,168,96\nR 166,187,45056\ns_and_b32 s60, s40, 3\ns_lshl_b32 s60, s60, 7\nv_add3_u32 v234, v234, v235, s60\n"
"M 4,172,112\ns_add_u32 m0, s51, 7168\ns_add_u32 s60, s71, 49792\nbuffer_load_dwordx4 v227, s[24:27], s60 offen lds\nG 4\nM 8,176,96\n"
"R 168,184,49152\ns_add_u32 m0, s50, 90112\ns_nop 0\nbuffer_load_dwordx4 v221, s[4:7], s63 offen offset:1024 lds\nM 8,180,112\ns_lshl_b32 s61, s42, 13\n"
"s_add_u32 s61, s61, 84148224\nbuffer_load_dwordx4 v[144:147], v234, s[20:23], s61 offen sc1\nM 24,176,100\nR 170,184,53248\nM 24,180,116\nM 40,176,104\n"
"R 172,184,57344\nM 40,180,120\ns_lshl_b32 s61, s42, 13\ns_add_u32 s61, s61, 84152320\nbuffer_load_dwordx4 v[148:151], v234, s[20:23], s61 offen sc1\nM 56,176,108\n"
"R 174,184,61440\nM 56,180,124\nG 4\nM 60,160,108\nR 176,185,49152\nM 60,164,124\n"
"M 44,160,104\nR 178,185,53248\nM 44,164,120\ns_add_u32 m0, s50, 98304\nM 28,160,100\nR 180,185,57344\n"
"M 28,164,116\ns_add_u32 s63, s52, 1968128\nM 12,160,96\nR 182,185,61440\nM 12,164,112\ns_waitcnt vmcnt(15)\n"
"s_barrier\nG 4\nN 64,96,168\nR 160,186,49152\ns_waitcnt vmcnt(14)\nv_min3_u32 v232, v129, v131, v133\n"
"v_min3_u32 v232, v232, v135, v135\nN 64,112,172\nbuffer_load_dwordx4 v220, s[4:7], s63 offen lds\nv_max3_u32 v233, v129, v131, v133\nN 68,100,168\nR 162,186,53248\n"
"v_max3_u32 v233, v233, v135, v135\nv_cmp_ne_u32 s[86:87], s46, v232\nN 68,116,172\nv_cmp_ne_u32 s[88:89], s46, v233\nN 96,104,168\nR 164,186,57344\n"
"s_or_b64 s[86:87], s[86:87], s[88:89]\ns_cbranch_scc1 Lq116\nLq117:\nN 96,120,172\nbuffer_load_dwordx4 v221, s[4:7], s63 offen offset:1024 lds\nv_add_f32 v128, v128, v132\n"
"N 100,108,168\nR 166,186,61440\ns_add_u32 m0, s50, 106496\nv_add_f32 v130, v130, v134\nN 100,124,172\nG 4\n"
"v_add_f32_dpp v128, v128, v128 row_ror:8 row_mask:0xf bank_mask:0xf\nv_add_f32_dpp v130, v130, v130 row_ror:8 row_mask:0xf bank_mask:0xf\nv_mov_b32 v132, v128\nN 108,108,176\nR 168,187,49152\ns_add_u32 s63, s52, 2164736\n"
"v_mov_b32 v133, v130\nN 108,124,180\nbuffer_load_dwordx4 v220, s[4:7], s63 offen lds\nv_permlane32_swap_b32 v132, v128\nN 104,104,176\nR 170,187,53248\n"
"v_permlane32_swap_b32 v133, v130\nv_add_f32 v128, v128, v132\nN 104,120,180\nv_add_f32 v130, v130, v133\nv_mov_b32 v132, v128\nv_mov_b32 v133, v130\n"
"N 76,100,176\nR 172,187,57344\nv_permlane16_swap_b32 v132, v128\nv_permlane16_swap_b32 v133, v130\nN 76,116,180\nbuffer_load_dwordx4 v221, s[4:7], s63 offen offset:1024 lds\n"
"v_add_f32 v128, v128, v132\nN 72,96,176\nR 174,187,61440\ns_add_u32 s63, s52, 1970176\nN 72,112,180\nG 4\n"
"v_add_f32 v130, v130, v133\nv_and_b32 v136, s77, v128\nN 80,96,160\nR 176,188,0\nv_sub_f32 v137, v128, v136\nN 80,112,164\n"
"s_add_u32 m0, s50, 114688\nv_cvt_pk_bf16_f32 v132, v136, v137\nv_cvt_pk_bf16_f32 v133, v136, 0\nN 84,100,160\nR 178,188,4096\nv_and_b32 v136, s77, v130\n"
"N 84,116,164\nbuffer_load_dwordx4 v220, s[4:7], s63 offen lds\nN 112,104,160\nR 180,188,8192\nv_sub_f32 v137, v130, v136\nN 112,120,164\n"
"v_cvt_pk_bf16_f32 v134, v136, v137\nv_cvt_pk_bf16_f32 v135, v136, 0\nN 116,108,160\nR 182,188,12288\nv_and_b32 v230, 0x1ff, v230\nN 116,124,164\n"
"v_add_u32 v230, 156160, v230\nds_write_b128 v230, v[132:135]\nG 5\nN 124,108,168\nR 160,189,0\nN 124,124,172\n"
"buffer_load_dwordx4 v221, s[4:7], s63 offen offset:1024 lds\nN 120,104,168\nR 162,189,4096\ns_add_u32 s63, s52, 2166784\nN 120,120,172\ns_add_u32 m0, s50, 0\n"
"N 92,100,168\nR 164,189,8192\nN 92,116,172\nbuffer_load_dwordx4 v220, s[4:7], s63 offen lds\nN 88,96,168\nR 166,189,12288\n"
"N 88,112,172\ns_waitcnt vmcnt(10)\nds_read_b128 a[160:163], v248 offset:0\ns_waitcnt vmcnt(9) lgkmcnt(5)\ns_barrier\nM 0,176,128\n"
"R 168,190,0\nds_read_b128 a[164:167], v248 offset:2048\nM 0,180,144\nds_read_b128 a[168:171], v248 offset:4096\nM 16,176,132\nR 170,190,4096\n"
"s_waitcnt vmcnt(7)\nv_min3_u32 v236, v145, v147, v149\nv_min3_u32 v236, v236, v151, v151\nM 16,180,148\nds_read_b128 a[172:175], v248 offset:6144\nv_max3_u32 v237, v145, v147, v149\n"
"v_max3_u32 v237, v237, v151, v151\nM 32,176,136\nR 172,190,8192\nds_read_b128 a[176:179], v249 offset:0\nv_cmp_ne_u32 s[86:87], s46, v236\nM 32,180,152\n"
"ds_read_b128 a[180:183], v249 offset:2048\nv_lshrrev_b32 v230, 7, v252\nv_cmp_ne_u32 s[88:89], s46, v237\nM 48,176,140\nR 174,190,12288\nds_read_b128 a[184:187], v249 offset:4096\n"
"s_or_b64 s[86:87], s[86:87], s[88:89]\ns_cbranch_scc1 Lq124\nLq125:\nM 48,180,156\nds_read_b128 a[188:191], v249 offset:6144\nG 12\n"
"v_add_f32 v144, v144, v148\nM 52,160,140\nR 176,191,0\nv_add_f32 v146, v146, v150\nv_add_f32_dpp v144, v144, v144 row_ror:8 row_mask:0xf bank_mask:0xf\nM 52,164,156\n"
"buffer_load_dwordx4 v221, s[4:7], s63 offen offset:1024 lds\nv_lshlrev_b32 v230, 9, v230\nv_add_f32_dpp v146, v146, v146 row_ror:8 row_mask:0xf bank_mask:0xf\nM 36,160,136\nR 178,191,4096\nv_and_b32 v231, 0x70, v252\n"
"v_mov_b32 v148, v144\nM 36,164,152\nG 2\ns_add_u32 m0, s51, 0\ns_add_u32 s60, s70, 768\nbuffer_load_dwordx4 v226, s[24:27], s60 offen lds\n"
"v_mov_b32 v149, v146\nM 20,160,132\nR 180,191,8192\nv_permlane32_swap_b32 v148, v144\nv_permlane32_swap_b32 v149, v146\nM 20,164,148\n"
"s_add_u32 m0, s51, 1024\ns_add_u32 s60, s70, 17152\nbuffer_load_dwordx4 v227, s[24:27], s60 offen lds\nv_add_f32 v144, v144, v148\nM 4,160,128\nR 182,191,12288\n"
"s_and_b32 s60, s40, 3\ns_lshl_b32 s60, s60, 7\nv_add3_u32 v230, v230, v231, s60\nv_add_f32 v146, v146, v149\nM 4,164,144\ns_add_u32 m0, s51, 2048\n"
"s_add_u32 s60, s70, 33536\nbuffer_load_dwordx4 v226, s[24:27], s60 offen lds\nv_mov_b32 v148, v144\nM 8,168,128\nR 160,188,16384\nv_mov_b32 v149, v146\n"
"v_permlane16_swap_b32 v148, v144\nM 8,172,144\ns_add_u32 m0, s51, 3072\ns_add_u32 s60, s70, 49920\nbuffer_load_dwordx4 v227, s[24:27], s60 offen lds\nv_permlane16_swap_b32 v149, v146\n"
"M 24,168,132\nR 162,188,20480\ns_lshl_b32 s61, s42, 13\ns_add_u32 s61, s61, 83886080\nbuffer_load_dwordx4 v[128:131], v230, s[20:23], s61 offen sc1\nv_add_f32 v144, v144, v148\n"
"M 24,172,148\ns_add_u32 m0, s51, 4096\ns_add_u32 s60, s71, 768\nbuffer_load_dwordx4 v226, s[24:27], s60 offen lds\nv_add_f32 v146, v146, v149\nM 40,168,136\n"
"R 164,188,24576\ns_lshl_b32 s61, s42, 13\ns_add_u32 s61, s61, 83890176\nbuffer_load_dwordx4 v[132:135], v230, s[20:23], s61 offen sc1\nv_and_b32 v152, s77, v144\nM 40,172,152\n"
"s_add_u32 m0, s51, 5120\ns_add_u32 s60, s71, 17152\nbuffer_load_dwordx4 v227, s[24:27], s60 offen lds\nM 56,168,140\nR 166,188,28672\nv_sub_f32 v153, v144, v152\n"
"M 56,172,156\ns_add_u32 m0, s51, 6144\ns_add_u32 s60, s71, 33536\nbuffer_load_dwordx4 v226, s[24:27], s60 offen lds\nG 4\nM 60,176,140\n"
"R 168,189,16384\nv_cvt_pk_bf16_f32 v148, v152, v153\nM 60,180,156\ns_add_u32 m0, s51, 7168\ns_add_u32 s60, s71, 49920\nbuffer_load_dwordx4 v227, s[24:27], s60 offen lds\n"
"M 44,176,136\nR 170,189,20480\nv_cvt_pk_bf16_f32 v149, v152, 0\nM 44,180,152\ns_add_u32 m0, s50, 8192\nv_and_b32 v152, s77, v146\n"
"v_sub_f32 v153, v146, v152\nM 28,176,132\nR 172,189,24576\nv_cvt_pk_bf16_f32 v150, v152, v153\nM 28,180,148\ns_add_u32 s63, s52, 2361344\n"
"v_cvt_pk_bf16_f32 v151, v152, 0\nv_and_b32 v234, 0x1ff, v234\nM 12,176,128\nR 174,189,28672\nv_add_u32 v234, 156672, v234\nM 12,180,144\n"
"s_waitcnt vmcnt(14)\ns_barrier\nG 4\nN 64,128,160\nR 176,190,16384\nds_write_b128 v234, v[148:151]\n"
"N 64,144,164\nbuffer_load_dwordx4 v220, s[4:7], s63 offen lds\nN 68,132,160\nR 178,190,20480\nN 68,148,164\nN 96,136,160\n"
"R 180,190,24576\nN 96,152,164\nbuffer_load_dwordx4 v221, s[4:7], s63 offen offset:1024 lds\nN 100,140,160\nR 182,190,28672\ns_add_u32 m0, s50, 16384\n"
"N 100,156,164\nG 5\nN 108,140,168\nR 160,191,16384\ns_add_u32 s63, s52, 2557952\nN 108,156,172\n"
"buffer_load_dwordx4 v220, s[4:7], s63 offen lds\nN 104,136,168\nR 162,191,20480\nN 104,152,172\nN 76,132,168\nR 164,191,24576\n"
"Lq186:\ns_lshr_b32 s60, s45, 3\ns_lshl2_add_u32 s60, s60, s40\ns_lshl_b32 s60, s60, 8\nv_lshl_add_u32 v224, v252, 3, s60\nv_add_u32 v224, Lq186+4096-LqPC, v224\n"
"s_mov_b64 exec, 3\nglobal_load_dword v224, v224, s[98:99]\ns_mov_b64 exec, -1\nN 76,148,172\nbuffer_load_dwordx4 v221, s[4:7], s63 offen offset:1024 lds\nN 72,128,168\n"
"R 166,191,28672\nN 72,144,172\nG 4\nN 80,128,176\nR 168,188,32768\nN 80,144,180\n"
"N 84,132,176\nR 170,188,36864\nN 84,148,180\ns_add_u32 m0, s50, 24576\nN 112,136,176\nR 172,188,40960\n"
"N 112,152,180\ns_add_u32 s63, s52, 2754560\nN 116,140,176\nR 174,188,45056\nN 116,156,180\nG 4\n"
"N 124,140,160\nR 176,189,32768\nN 124,156,164\nbuffer_load_dwordx4 v220, s[4:7], s63 offen lds\nN 120,136,160\nR 178,189,36864\n"
"N 120,152,164\nN 92,132,160\nR 180,189,40960\ns_waitcnt vmcnt(9)\nv_min3_u32 v232, v129, v131, v133\nv_min3_u32 v232, v232, v135, v135\n"
"N 92,148,164\nbuffer_load_dwordx4 v221, s[4:7], s63 offen offset:1024 lds\nv_max3_u32 v233, v129, v131, v133\nN 88,128,160\nR 182,189,45056\ns_add_u32 m0, s50, 32768\n"
"v_max3_u32 v233, v233, v135, v135\nN 88,144,164\ns_barrier\nG 4\nv_cmp_ne_u32 s[86:87], s46, v232\nM 0,168,160\n"
"R 160,190,32768\ns_add_u32 s63, s52, 2951168\nv_cmp_ne_u32 s[88:89], s46, v233\nM 0,172,176\nbuffer_load_dwordx4 v220, s[4:7], s63 offen lds\ns_or_b64 s[86:87], s[86:87], s[88:89]\n"
"s_cbranch_scc1 Lq132\nLq133:\nM 16,168,164\nR 162,190,36864\nv_add_f32 v128, v128, v132\nv_add_f32 v130, v130, v134\n"
"M 16,172,180\nv_add_f32_dpp v128, v128, v128 row_ror:8 row_mask:0xf bank_mask:0xf\nv_add_f32_dpp v130, v130, v130 row_ror:8 row_mask:0xf bank_mask:0xf\nv_mov_b32 v132, v128\nM 32,168,168\nR 164,190,40960\n"
"v_mov_b32 v133, v130\nv_permlane32_swap_b32 v132, v128\nM 32,172,184\nv_permlane32_swap_b32 v133, v130\nv_add_f32 v128, v128, v132\nv_add_f32 v130, v130, v133\n"
"M 48,168,172\nR 166,190,45056\nv_mov_b32 v132, v128\nv_mov_b32 v133, v130\nM 48,172,188\nG 4\n"
"v_permlane16_swap_b32 v132, v128\nv_permlane16_swap_b32 v133, v130\nv_add_f32 v128, v128, v132\nM 52,176,172\nR 168,191,32768\nv_add_f32 v130, v130, v133\n"
"v_and_b32 v136, s77, v128\nM 52,180,188\nbuffer_load_dwordx4 v221, s[4:7], s63 offen offset:1024 lds\nv_sub_f32 v137, v128, v136\nM 36,176,168\nR 170,191,36864\n"
"s_waitcnt vmcnt(9)\nds_read_b128 a[192:195], v248 offset:0\nv_cvt_pk_bf16_f32 v132, v136, v137\nM 36,180,184\nds_read_b128 a[196:199], v248 offset:2048\nv_cvt_pk_bf16_f32 v133, v136, 0\n"
"v_and_b32 v136, s77, v130\nM 20,176,164\nR 172,191,40960\nds_read_b128 a[200:203], v248 offset:4096\nv_sub_f32 v137, v130, v136\nM 20,180,180\n"
"ds_read_b128 a[204:207], v248 offset:6144\nv_cvt_pk_bf16_f32 v134, v136, v137\nv_cvt_pk_bf16_f32 v135, v136, 0\nM 4,176,160\nR 174,191,45056\nv_and_b32 v230, 0x1ff, v230\n"
"v_add_u32 v230, 155648, v230\nM 4,180,176\nds_read_b128 a[208:211], v249 offset:0\nds_write_b128 v230, v[132:135]\nG 10\nM 8,160,160\n"
"R 176,188,49152\nds_read_b128 a[212:215], v249 offset:2048\nM 8,164,176\nds_read_b128 a[216:219], v249 offset:4096\nM 24,160,164\nR 178,188,53248\n"
"M 24,164,180\nds_read_b128 a[220:223], v249 offset:6144\nM 40,160,168\nR 180,184,0\nM 40,164,184\ns_lshr_b32 s84, s49, 3\n"
"M 56,160,172\nR 182,184,4096\ns_add_u32 s84, s84, 81788928\nM 56,164,188\nG 2\ns_add_u32 m0, s51, 0\n"
"s_add_u32 s60, s70, 896\nbuffer_load_dwordx4 v226, s[24:27], s60 offen lds\nM 60,168,172\nR 160,189,49152\nbuffer_load_dwordx4 v[128:131], v252, s[20:23], s84 offen offset:0 sc1\nM 60,172,188\n"
"s_add_u32 m0, s51, 1024\ns_add_u32 s60, s70, 17280\nbuffer_load_dwordx4 v227, s[24:27], s60 offen lds\nM 44,168,168\nR 162,189,53248\nM 44,172,184\n"
"s_add_u32 m0, s51, 2048\ns_add_u32 s60, s70, 33664\nbuffer_load_dwordx4 v226, s[24:27], s60 offen lds\nM 28,168,164\nR 164,185,0\nbuffer_load_dwordx4 v[132:135], v252, s[20:23], s84 offen offset:1024 sc1\n"
"M 28,172,180\ns_add_u32 m0, s51, 3072\ns_add_u32 s60, s70, 50048\nbuffer_load_dwordx4 v227, s[24:27], s60 offen lds\nM 12,168,160\nR 166,185,4096\n"
"buffer_load_dwordx4 v[136:139], v252, s[20:23], s84 offen offset:2048 sc1\nM 12,172,176\ns_add_u32 m0, s51, 4096\ns_add_u32 s60, s71, 896\nbuffer_load_dwordx4 v226, s[24:27], s60 offen lds\ns_waitcnt vmcnt(12)\n"
"s_barrier\nG 4\nN 64,160,176\nR 168,190,49152\ns_lshr_b32 s85, s49, 3\ns_add_u32 m0, s50, 40960\n"
"s_add_u32 s63, s52, 2363392\nbuffer_load_dwordx4 v220, s[4:7], s63 offen lds\nN 64,176,180\ns_add_u32 m0, s51, 5120\ns_add_u32 s60, s71, 17280\nbuffer_load_dwordx4 v227, s[24:27], s60 offen lds\n"
"s_add_u32 s85, s85, 81793024\nN 68,164,176\nR 170,190,53248\nbuffer_load_dwordx4 v[140:143], v252, s[20:23], s84 offen offset:3072 sc1\nN 68,180,180\ns_add_u32 m0, s51, 6144\n"
"s_add_u32 s60, s71, 33664\nbuffer_load_dwordx4 v226, s[24:27], s60 offen lds\nN 96,168,176\nR 172,186,0\nbuffer_load_dwordx4 v[144:147], v252, s[20:23], s85 offen offset:0 sc1\nN 96,184,180\n"
"s_add_u32 m0, s51, 7168\ns_add_u32 s60, s71, 50048\nbuffer_load_dwordx4 v227, s[24:27], s60 offen lds\nN 100,172,176\nR 174,186,4096\ns_add_u32 m0, s50, 40960\n"
"N 100,188,180\nG 4\nN 108,172,160\nR 176,191,49152\nbuffer_load_dwordx4 v221, s[4:7], s63 offen offset:1024 lds\nN 108,188,164\n"
"buffer_load_dwordx4 v[148:151], v252, s[20:23], s85 offen offset:1024 sc1\ns_add_u32 s63, s52, 2560000\nN 104,168,160\nR 178,191,53248\ns_add_u32 m0, s50, 49152\nN 104,184,164\n"
"buffer_load_dwordx4 v[152:155], v252, s[20:23], s85 offen offset:2048 sc1\nN 76,164,160\nR 180,187,0\nbuffer_load_dwordx4 v220, s[4:7], s63 offen lds\nN 76,180,164\nbuffer_load_dwordx4 v[156:159], v252, s[20:23], s85 offen offset:3072 sc1\n"
"N 72,160,160\nR 182,187,4096\nN 72,176,164\nG 4\nN 80,160,168\nR 160,184,8192\n"
"buffer_load_dwordx4 v221, s[4:7], s63 offen offset:1024 lds\nN 80,176,172\nN 84,164,168\nR 162,184,12288\nN 84,180,172\nN 112,168,168\n"
"R 164,184,16384\nN 112,184,172\nN 116,172,168\nR 166,184,20480\nN 116,188,172\nG 4\n"
"N 124,172,176\nR 168,185,8192\nN 124,188,180\ns_add_u32 m0, s50, 57344\nN 120,168,176\nR 170,185,12288\n"
"N 120,184,180\ns_add_u32 s63, s52, 2756608\nN 92,164,176\nR 172,185,16384\nN 92,180,180\nbuffer_load_dwordx4 v220, s[4:7], s63 offen lds\n"
"N 88,160,176\nR 174,185,20480\nN 88,176,180\ns_waitcnt vmcnt(21)\ns_barrier\nG 4\n"
"M 0,160,192\nR 176,186,8192\nM 0,164,208\nbuffer_load_dwordx4 v221, s[4:7], s63 offen offset:1024 lds\nM 16,160,196\nR 178,186,12288\n"
"M 16,164,212\ns_add_u32 m0, s50, 65536\nM 32,160,200\nR 180,186,16384\nM 32,164,216\ns_add_u32 s63, s52, 2953216\n"
"M 48,160,204\nR 182,186,20480\ns_lshl2_add_u32 s60, s45, s40\nv_lshlrev_b32 v224, 3, v252\ns_mul_i32 s60, s60, 2048\ns_bfm_b64 exec, 16, 0\n"
"buffer_load_dword v224, v224, s[8:11], s60 offen\ns_mov_b64 exec, -1\nM 48,164,220\nG 4\nM 52,168,204\nR 160,187,8192\n"
"M 52,172,220\nbuffer_load_dwordx4 v220, s[4:7], s63 offen lds\nM 36,168,200\nR 162,187,12288\nM 36,172,216\nM 20,168,196\n"
"R 164,187,16384\nM 20,172,212\nbuffer_load_dwordx4 v221, s[4:7], s63 offen offset:1024 lds\nM 4,168,192\nR 166,187,20480\nM 4,172,208\n"
"G 4\nM 8,176,192\nR 168,184,24576\nM 8,180,208\nM 24,176,196\nR 170,184,28672\n"
"s_waitcnt vmcnt(11)\nds_read_b128 a[224:227], v248 offset:0\nM 24,180,212\nds_read_b128 a[228:231], v248 offset:2048\nM 40,176,200\nR 172,184,32768\n"
"ds_read_b128 a[232:235], v248 offset:4096\nM 40,180,216\nds_read_b128 a[236:239], v248 offset:6144\nM 56,176,204\nR 174,184,36864\nM 56,180,220\n"
"ds_read_b128 a[240:243], v249 offset:0\nG 9\nM 60,160,204\nR 176,185,24576\nds_read_b128 a[244:247], v249 offset:2048\nv_min3_u32 v230, v129, v131, v133\n"
"M 60,164,220\nds_read_b128 a[248:251], v249 offset:4096\ns_add_u32 m0, s50, 73728\nv_min3_u32 v230, v230, v135, v137\nM 44,160,200\nR 178,185,28672\n"
"s_add_u32 s63, s52, 0\nv_min3_u32 v230, v230, v139, v141\nM 44,164,216\nds_read_b128 a[252:255], v249 offset:6144\nv_min3_u32 v230, v230, v143, v145\ns_waitcnt vmcnt(9)\n"
"v_min3_u32 v230, v230, v147, v149\nM 28,160,196\nR 180,185,32768\ns_waitcnt vmcnt(8)\nv_min3_u32 v230, v230, v151, v153\ns_waitcnt vmcnt(6)\n"
"v_min3_u32 v230, v230, v155, v157\nM 28,164,212\nbuffer_load_dwordx4 v220, s[4:7], s63 offen lds\nv_min3_u32 v230, v230, v159, v159\nM 12,160,192\nR 182,185,36864\n"
"v_max3_u32 v231, v129, v131, v133\nv_max3_u32 v231, v231, v135, v137\nM 12,164,208\ns_waitcnt vmcnt(6)\ns_barrier\nG 8\n"
"v_max3_u32 v231, v231, v139, v141\nM 0,168,224\nR 160,186,24576\nv_max3_u32 v231, v231, v143, v145\nv_max3_u32 v231, v231, v147, v149\nG 8\n"
"M 0,172,240\nbuffer_load_dwordx4 v221, s[4:7], s63 offen offset:1024 lds\nv_max3_u32 v231, v231, v151, v153\nM 16,168,228\nR 162,186,28672\ns_add_u32 s63, s52, 196608\n"
"v_max3_u32 v231, v231, v155, v157\nG 7\nM 16,172,244\ns_add_u32 m0, s50, 81920\nv_max3_u32 v231, v231, v159, v159\nv_cmp_ne_u32 s[86:87], s46, v230\n"
"v_cmp_ne_u32 s[88:89], s46, v231\nM 32,168,232\nR 164,186,32768\ns_or_b64 s[86:87], s[86:87], s[88:89]\ns_cbranch_scc1 Lq140\nLq141:\n"
"G 7\nM 32,172,248\nbuffer_load_dwordx4 v220, s[4:7], s63 offen lds\nv_add_f32 v128, v128, v144\nM 48,168,236\nR 166,186,36864\n"
"s_lshl_b32 s60, s90, 14\ns_add_u32 s70, s49, s60\ns_xor_b32 s71, s70, 0x10000\nv_add_f32 v130, v130, v146\nG 6\nM 48,172,252\n"
"G 4\nv_add_f32 v132, v132, v148\nv_add_f32 v134, v134, v150\nv_add_f32 v136, v136, v152\nM 52,176,236\nR 168,187,24576\n"
"v_add_f32 v138, v138, v154\nv_add_f32 v140, v140, v156\nM 52,180,252\nbuffer_load_dwordx4 v221, s[4:7], s63 offen offset:1024 lds\nv_add_f32 v142, v142, v158\nM 36,176,232\n"
"R 170,187,28672\ns_add_u32 s63, s52, 393216\nv_add_f32 v128, v128, v136\nM 36,180,248\ns_add_u32 m0, s50, 90112\nv_add_f32 v130, v130, v138\n"
"v_add_f32 v132, v132, v140\nv_add_f32 v134, v134, v142\nM 20,176,228\nR 172,187,32768\nv_add_f32 v128, v128, v132\nv_add_f32 v130, v130, v134\n"
"M 20,180,244\nbuffer_load_dwordx4 v220, s[4:7], s63 offen lds\nv_lshrrev_b32 v231, 1, v252\nM 4,176,224\nR 174,187,36864\nv_lshrrev_b32 v230, 2, v252\n"
"s_lshl_b32 s62, s40, 9\ns_add_u32 s62, s62, 159232\nv_add_u32 v231, s62, v231\nM 4,180,240\nds_write2_b32 v231, v128, v130 offset1:1\nG 5\n"
"M 8,160,224\nR 176,184,40960\ns_lshl_b32 s62, s40, 9\ns_add_u32 s62, s62, 159232\nv_add_u32 v230, s62, v230\nM 8,164,240\n"
"buffer_load_dwordx4 v221, s[4:7], s63 offen offset:1024 lds\nds_read2st64_b32 v[232:233], v230 offset1:1\nM 24,160,228\nR 178,184,45056\nM 24,164,244\ns_add_u32 m0, s50, 98304\n"
"M 40,160,232\nR 180,184,49152\nM 40,164,248\ns_add_u32 s63, s52, 589824\nM 56,160,236\nR 182,184,53248\n"
"M 56,164,252\nG 6\nG 3\nv_add_f32 v232, v232, v233\nv_mov_b32 v233, v232\ns_nop 1\n"
"v_permlane32_swap_b32 v232, v233\nM 60,168,236\nR 160,185,40960\nv_mov_b32 v230, v232\nv_mov_b32 v231, v233\nM 60,172,252\n"
"buffer_load_dwordx4 v220, s[4:7], s63 offen lds\nv_permlane16_swap_b32 v230, v232\nM 44,168,232\nR 162,185,45056\nv_permlane16_swap_b32 v231, v233\nv_mul_f32 v230, 0xba800000, v230\n"
"M 44,172,248\nv_mul_f32 v214, v230, v230\nv_mul_f32 v231, 0x3a800000, v231\nv_sub_f32 v231, v231, v214\nM 28,168,228\nR 164,185,49152\n"
"v_max_f32 v231, 0, v231\nv_add_f32 v231, 0x3727c5ac, v231\nM 28,172,244\nbuffer_load_dwordx4 v221, s[4:7], s63 offen offset:1024 lds\nv_rsq_f32 v234, v231\nM 12,168,224\n"
"R 166,185,53248\ns_mov_b32 s60, 156160\nv_bfe_u32 v246, v252, 4, 4\nv_lshl_add_u32 v246, v246, 3, s60\nv_and_b32 v208, s77, v230\nM 12,172,240\n"
"ds_read_b64 v[238:239], v246 offset:0\ns_waitcnt vmcnt(8) lgkmcnt(0)\ns_barrier\nv_mul_f32 v216, v231, v234\nN 64,192,176\nR 168,186,40960\n"
"ds_read_b64 v[240:241], v246 offset:128\nv_sub_f32 v230, v230, v208\nN 64,208,180\ns_mov_b32 m0, s51\nds_read_b64 v[242:243], v246 offset:256\nv_cvt_pk_bf16_f32 v209, v230, v216\n"
"N 68,196,176\nR 170,186,45056\ns_lshl2_add_u32 s60, s45, s40\nv_lshlrev_b32 v224, 3, v252\ns_mul_i32 s60, s60, 7168\ns_bfm_b64 exec, 14, 0\n"
"buffer_load_dword v224, v224, s[12:15], s60 offen\ns_mov_b64 exec, -1\nds_read_b64 v[244:245], v246 offset:384\nds_write_addtid_b32 v32 offset:0\nv_cvt_pk_bf16_f32 v208, v208, v208\nN 68,212,180\n"
"ds_read_b64 v[246:247], v246 offset:0\nds_write_addtid_b32 v33 offset:256\nv_mov_b32 v214, v234\nv_mul_f32 v215, 0x3e8293ee, v234\nN 96,200,176\nR 172,186,49152\n"
"ds_write_addtid_b32 v34 offset:512\nv_mul_f32 v232, 0xba800000, v232\nv_mul_f32 v217, v232, v232\nN 96,216,180\nds_write_addtid_b32 v35 offset:768\nds_write_addtid_b32 v36 offset:1024\n"
"v_mul_f32 v233, 0x3a800000, v233\nv_sub_f32 v233, v233, v217\nN 100,204,176\nR 174,186,53248\nds_write_addtid_b32 v37 offset:1280\nv_max_f32 v233, 0, v233\n"
"v_add_f32 v233, 0x3727c5ac, v233\nN 100,220,180\nds_write_addtid_b32 v38 offset:1536\nv_rsq_f32 v235, v233\nv_and_b32 v212, s77, v232\nv_mul_f32 v219, v233, v235\n"
"v_sub_f32 v232, v232, v212\nv_cvt_pk_bf16_f32 v213, v232, v219\nv_cvt_pk_bf16_f32 v212, v212, v212\nv_mov_b32 v217, v235\nv_mul_f32 v218, 0x3e8293ee, v235\ns_mov_b64 s[60:61], 0xffff\n"
"v_cndmask_b32 v208, 0, v208, s[60:61]\ns_mov_b64 s[60:61], 0xffff\nv_cndmask_b32 v209, 0, v209, s[60:61]\ns_mov_b64 s[60:61], 0xffff\nv_cndmask_b32 v212, 0, v212, s[60:61]\ns_mov_b64 s[60:61], 0xffff\n"
"v_cndmask_b32 v213, 0, v213, s[60:61]\nv_bfe_u32 v230, v252, 2, 6\ns_lshl_b32 s60, s40, 7\ns_add_u32 s60, s60, 161280\nv_add_u32 v230, s60, v230\nds_write_b32 v230, v214 offset:0\n"
"ds_write_b32 v230, v217 offset:64\nds_write_addtid_b32 v39 offset:1792\nN 108,204,160\nR 176,187,40960\nds_write_addtid_b32 v40 offset:2048\nN 108,220,164\n"
"ds_write_addtid_b32 v41 offset:2304\nds_write_addtid_b32 v42 offset:2560\nN 104,200,160\nR 178,187,45056\nds_write_addtid_b32 v43 offset:2816\nN 104,216,164\n"
"ds_write_addtid_b32 v44 offset:3072\nds_write_addtid_b32 v45 offset:3328\nN 76,196,160\nR 180,187,49152\nG 15\nv_mfma_f32_16x16x32_bf16 v[0:3], v[238:241], v[208:211], v[0:3]\n"
"ds_write_addtid_b32 v46 offset:3584\nN 76,212,164\nv_mfma_f32_16x16x32_bf16 v[4:7], v[240:243], v[208:211], v[4:7]\nds_write_addtid_b32 v47 offset:3840\nds_write_addtid_b32 v48 offset:4096\nN 72,192,160\n"
"R 182,187,53248\nv_mfma_f32_16x16x32_bf16 v[8:11], v[242:245], v[208:211], v[8:11]\nds_write_addtid_b32 v49 offset:4352\nN 72,208,164\nv_mfma_f32_16x16x32_bf16 v[12:15], v[244:247], v[208:211], v[12:15]\nds_write_addtid_b32 v50 offset:4608\n"
"ds_write_addtid_b32 v51 offset:4864\nG 15\nN 80,192,168\nR 160,184,57344\nv_mfma_f32_16x16x32_bf16 v[16:19], v[244:247], v[210:213], v[16:19]\nds_write_addtid_b32 v52 offset:5120\n"
"N 80,208,172\nv_mfma_f32_16x16x32_bf16 v[20:23], v[238:241], v[210:213], v[20:23]\nds_write_addtid_b32 v53 offset:5376\nds_write_addtid_b32 v54 offset:5632\nN 84,196,168\nR 162,184,61440\n"
"v_mfma_f32_16x16x32_bf16 v[24:27], v[240:243], v[210:213], v[24:27]\nds_write_addtid_b32 v55 offset:5888\nN 84,212,172\nv_mfma_f32_16x16x32_bf16 v[28:31], v[242:245], v[210:213], v[28:31]\nds_write_addtid_b32 v56 offset:6144\nds_write_addtid_b32 v57 offset:6400\n"
"N 112,200,168\nR 164,188,0\nds_write_addtid_b32 v58 offset:6656\nN 112,216,172\nds_write_addtid_b32 v59 offset:6912\nds_write_addtid_b32 v60 offset:7168\n"
"N 116,204,168\nR 166,188,4096\nds_write_addtid_b32 v61 offset:7424\nN 116,220,172\nds_write_addtid_b32 v62 offset:7680\nds_write_addtid_b32 v63 offset:7936\n"
"G 15\nN 124,204,176\nR 168,185,57344\ns_add_u32 m0, s50, 106496\ns_add_u32 s63, s52, 786432\nN 124,220,180\n"
"buffer_load_dwordx4 v220, s[4:7], s63 offen lds\nN 120,200,176\nR 170,185,61440\ns_waitcnt vmcnt(6) lgkmcnt(2)\ns_barrier\nN 120,216,180\n"
"ds_read2st64_b32 v[32:33], v225 offset0:0 offset1:1\nds_read2st64_b32 v[34:35], v225 offset0:2 offset1:3\nN 92,196,176\nR 172,189,0\nds_read2st64_b32 v[36:37], v225 offset0:4 offset1:5\nN 92,212,180\n"
"buffer_load_dwordx4 v221, s[4:7], s63 offen offset:1024 lds\nds_read2st64_b32 v[38:39], v225 offset0:6 offset1:7\nN 88,192,176\nR 174,189,4096\nds_read2st64_b32 v[40:41], v225 offset0:8 offset1:9\nN 88,208,180\n"
"ds_read2st64_b32 v[42:43], v225 offset0:10 offset1:11\nds_read2st64_b32 v[44:45], v225 offset0:12 offset1:13\nN 64,224,160\nR 176,186,57344\ns_mov_b32 s60, 156672\nv_bfe_u32 v246, v252, 4, 4\n"
"v_lshl_add_u32 v246, v246, 3, s60\nds_read2st64_b32 v[46:47], v225 offset0:14 offset1:15\nN 64,240,164\nds_read_b64 v[238:239], v246 offset:0\ns_add_u32 m0, s50, 114688\nG 11\n"
"v_add_f32 v0, v0, v32\nN 68,228,160\nR 178,186,61440\nds_read_b64 v[240:241], v246 offset:128\nv_add_f32 v1, v1, v33\nN 68,244,164\n"
"ds_read_b64 v[242:243], v246 offset:256\ns_add_u32 s63, s52, 983040\nG 13\nv_add_f32 v2, v2, v34\nN 96,232,160\nR 180,190,0\n"
"ds_read_b64 v[244:245], v246 offset:384\nv_add_f32 v3, v3, v35\nN 96,248,164\nds_read_b64 v[246:247], v246 offset:0\nG 14\nv_add_f32 v4, v4, v36\n"
"v_add_f32 v5, v5, v37\nN 100,236,160\nR 182,190,4096\nbuffer_load_dwordx4 v220, s[4:7], s63 offen lds\nG 14\nv_add_f32 v6, v6, v38\n"
"N 100,252,164\nds_read2st64_b32 v[48:49], v225 offset0:16 offset1:17\nG 14\nv_add_f32 v7, v7, v39\nN 108,236,168\nR 160,187,57344\n"
"G 14\nv_add_f32 v8, v8, v40\nv_add_f32 v9, v9, v41\nN 108,252,172\nbuffer_load_dwordx4 v221, s[4:7], s63 offen offset:1024 lds\nG 13\n"
"v_add_f32 v10, v10, v42\nN 104,232,168\nR 162,187,61440\ns_add_u32 m0, s50, 0\ns_add_u32 s63, s52, 1179648\nv_add_f32 v11, v11, v43\n"
"N 104,248,172\nbuffer_load_dwordx4 v220, s[4:7], s63 offen lds\nG 13\nv_add_f32 v12, v12, v44\nN 76,228,168\nR 164,191,0\n"
"v_add_f32 v13, v13, v45\nG 12\nv_add_f32 v14, v14, v46\nN 76,244,172\nG 9\nv_mfma_f32_16x16x32_bf16 v[64:67], v[208:211], v[238:241], v[64:67]\n"
"ds_read2st64_b32 v[50:51], v225 offset0:18 offset1:19\nv_add_f32 v15, v15, v47\nG 4\nv_add_f32 v16, v16, v48\nN 72,224,168\nR 166,191,4096\n"
"ds_read2st64_b32 v[52:53], v225 offset0:20 offset1:21\nv_add_f32 v17, v17, v49\nN 72,240,172\nv_mfma_f32_16x16x32_bf16 v[72:75], v[208:211], v[240:243], v[72:75]\nds_read2st64_b32 v[54:55], v225 offset0:22 offset1:23\nds_read2st64_b32 v[56:57], v225 offset0:24 offset1:25\n"
"N 80,224,176\nR 168,188,8192\nbuffer_load_dwordx4 v221, s[4:7], s63 offen offset:1024 lds\nds_read2st64_b32 v[58:59], v225 offset0:26 offset1:27\nN 80,240,180\nv_mfma_f32_16x16x32_bf16 v[80:83], v[208:211], v[242:245], v[80:83]\n"
"s_add_u32 m0, s50, 8192\ns_add_u32 s63, s52, 1376256\nbuffer_load_dwordx4 v220, s[4:7], s63 offen lds\nds_read2st64_b32 v[60:61], v225 offset0:28 offset1:29\nN 84,228,176\nR 170,188,12288\n"
"ds_read2st64_b32 v[62:63], v225 offset0:30 offset1:31\nN 84,244,180\nv_mfma_f32_16x16x32_bf16 v[88:91], v[208:211], v[244:247], v[88:91]\nG 9\nv_add_f32 v18, v18, v50\nv_add_f32 v19, v19, v51\n"
"N 112,232,176\nR 172,188,16384\nG 8\nv_add_f32 v20, v20, v52\nv_add_f32 v21, v21, v53\nN 112,248,180\n"
"v_mfma_f32_16x16x32_bf16 v[68:71], v[210:213], v[244:247], v[68:71]\nbuffer_load_dwordx4 v221, s[4:7], s63 offen offset:1024 lds\nG 7\nv_add_f32 v22, v22, v54\nN 116,236,176\nR 174,188,20480\n"
"s_add_u32 m0, s50, 32768\nv_add_f32 v23, v23, v55\nN 116,252,180\nv_mfma_f32_16x16x32_bf16 v[76:79], v[210:213], v[238:241], v[76:79]\nG 7\nv_add_f32 v24, v24, v56\n"
"v_add_f32 v25, v25, v57\nG 5\nv_add_f32 v26, v26, v58\nN 124,236,160\nR 176,189,8192\ns_add_u32 s63, s52, 1572864\n"
"v_add_f32 v27, v27, v59\nN 124,252,164\nv_mfma_f32_16x16x32_bf16 v[84:87], v[210:213], v[240:243], v[84:87]\nbuffer_load_dwordx4 v220, s[4:7], s63 offen lds\nG 5\nv_add_f32 v28, v28, v60\n"
"v_add_f32 v29, v29, v61\nG 3\nv_add_f32 v30, v30, v62\nv_add_f32 v31, v31, v63\nN 120,232,160\nR 178,189,12288\n"
"s_lshl4_add_u32 s64, s41, s42\nv_lshrrev_b32 v227, 1, v252\nN 120,248,164\nv_mfma_f32_16x16x32_bf16 v[92:95], v[210:213], v[242:245], v[92:95]\nbuffer_load_dwordx4 v221, s[4:7], s63 offen offset:1024 lds\nv_mul_f32 v16, v16, v217\n"
"N 92,228,160\nR 180,189,16384\ns_add_u32 m0, s50, 40960\ns_add_u32 s63, s52, 1769472\nv_mul_f32 v17, v17, v217\nN 92,244,164\n"
"buffer_load_dwordx4 v220, s[4:7], s63 offen lds\nv_mul_f32 v18, v18, v217\nN 88,224,160\nR 182,189,20480\ns_lshl_b32 s64, s64, 12\nv_mul_f32 v19, v19, v217\n"
"N 88,240,164\ns_waitcnt vmcnt(12)\ns_barrier\nG 4\nv_mul_f32 v20, v20, v217\nv_mfma_f32_16x16x32_bf16 v[128:131], v[168:171], a[0:3], 0\n"
"R 160,190,8192\ns_add_u32 s64, s64, 77594624\nv_mul_f32 v21, v21, v217\nM 128,172,16\ns_mov_b32 m0, s51\ns_cmp_eq_u32 s40, 3\n"
"s_cselect_b64 s[66:67], -1, 0\nv_mul_f32 v22, v22, v217\nv_mfma_f32_16x16x32_bf16 v[144:147], v[168:171], a[4:7], 0\nR 162,190,12288\ns_lshl2_add_u32 s60, s45, s40\ns_waitcnt vmcnt(11)\n"
"v_lshlrev_b32 v224, 3, v252\ns_mul_i32 s60, s60, 7168\ns_bfm_b64 exec, 14, 14\nbuffer_load_dword v224, v224, s[12:15], s60 offen\ns_mov_b64 exec, -1\nv_mul_f32 v23, v23, v217\n"
"M 144,172,20\nv_mul_f32 v24, v24, v217\nv_mul_f32 v25, v25, v217\nv_mul_f32 v26, v26, v217\nv_mfma_f32_16x16x32_bf16 v[32:35], v[168:171], a[8:11], 0\nR 164,190,16384\n"
"v_mul_f32 v27, v27, v217\nv_mul_f32 v28, v28, v217\nM 32,172,24\nv_mul_f32 v29, v29, v217\nv_mul_f32 v30, v30, v217\nv_mul_f32 v31, v31, v217\n"
"v_mfma_f32_16x16x32_bf16 v[48:51], v[168:171], a[12:15], 0\nR 166,190,20480\nds_write_addtid_b32 v96 offset:0\nv_cvt_pk_bf16_f32 v16, v16, v17\nv_cvt_pk_bf16_f32 v17, v18, v19\nM 48,172,28\n"
"ds_write_addtid_b32 v97 offset:256\nG 6\nv_cvt_pk_bf16_f32 v18, v20, v21\nv_cvt_pk_bf16_f32 v19, v22, v23\nv_cvt_pk_bf16_f32 v20, v24, v25\nv_mfma_f32_16x16x32_bf16 v[52:55], v[176:179], a[12:15], 0\n"
"R 168,191,8192\nds_write_addtid_b32 v98 offset:512\ns_lshr_b32 s100, s41, 1\ns_mul_i32 s100, s100, 458752\ns_add_u32 s100, s100, s55\nv_cvt_pk_bf16_f32 v21, v26, v27\n"
"v_cvt_pk_bf16_f32 v22, v28, v29\nM 52,180,28\nds_write_addtid_b32 v99 offset:768\ns_mul_i32 s101, s41, 229376\ns_add_u32 s101, s101, s54\nv_cvt_pk_bf16_f32 v23, v30, v31\n"
"v_mul_f32 v0, v0, v214\nv_mul_f32 v1, v1, v214\nv_mfma_f32_16x16x32_bf16 v[36:39], v[176:179], a[8:11], 0\nR 170,191,12288\nds_write_addtid_b32 v100 offset:1024\nv_mul_f32 v2, v2, v214\n"
"v_mul_f32 v3, v3, v214\nM 36,180,24\nds_write_addtid_b32 v101 offset:1280\nv_mul_f32 v4, v4, v214\nv_mul_f32 v5, v5, v214\nv_mul_f32 v6, v6, v214\n"
"v_mfma_f32_16x16x32_bf16 v[148:151], v[176:179], a[4:7], 0\nR 172,191,16384\nds_write_addtid_b32 v102 offset:1536\nv_mul_f32 v7, v7, v214\nv_mul_f32 v8, v8, v214\nM 148,180,20\n"
"ds_write_addtid_b32 v103 offset:1792\nv_mul_f32 v9, v9, v214\nv_mul_f32 v10, v10, v214\nv_mul_f32 v11, v11, v214\nv_mfma_f32_16x16x32_bf16 v[132:135], v[176:179], a[0:3], 0\nR 174,191,20480\n"
"ds_write_addtid_b32 v104 offset:2048\nv_mul_f32 v12, v12, v214\nv_mul_f32 v13, v13, v214\nM 132,180,16\nds_write_addtid_b32 v105 offset:2304\nG 14\n"
"v_mul_f32 v14, v14, v214\nv_mul_f32 v15, v15, v214\nv_cvt_pk_bf16_f32 v0, v0, v1\nv_mfma_f32_16x16x32_bf16 v[136:139], v[160:163], a[0:3], 0\nR 176,188,24576\nds_write_addtid_b32 v106 offset:2560\n"
"v_cvt_pk_bf16_f32 v1, v2, v3\nv_cvt_pk_bf16_f32 v2, v4, v5\nM 136,164,16\nds_write_addtid_b32 v107 offset:2816\nv_cvt_pk_bf16_f32 v3, v6, v7\nv_cvt_pk_bf16_f32 v4, v8, v9\n"
"v_cvt_pk_bf16_f32 v5, v10, v11\nv_mfma_f32_16x16x32_bf16 v[152:155], v[160:163], a[4:7], 0\nR 178,188,28672\nds_write_addtid_b32 v108 offset:3072\nv_cvt_pk_bf16_f32 v6, v12, v13\nv_cvt_pk_bf16_f32 v7, v14, v15\n"
"M 152,164,20\nds_write_addtid_b32 v109 offset:3328\nds_write_addtid_b32 v110 offset:3584\ns_mov_b64 exec, s[66:67]\nbuffer_store_dwordx4 v[16:19], v252, s[20:23], s64 offen offset:0 sc1\nbuffer_store_dwordx4 v[20:23], v252, s[20:23], s64 offen offset:1024 sc1\n"
"s_mov_b64 exec, -1\nv_mfma_f32_16x16x32_bf16 v[40:43], v[160:163], a[8:11], 0\nR 180,188,32768\nds_write_addtid_b32 v111 offset:3840\nM 40,164,24\nds_write_addtid_b32 v112 offset:4096\n"
"ds_write_addtid_b32 v113 offset:4352\nv_mfma_f32_16x16x32_bf16 v[56:59], v[160:163], a[12:15], 0\nR 182,188,36864\nds_write_addtid_b32 v114 offset:4608\nM 56,164,28\nds_write_addtid_b32 v115 offset:4864\n"
"ds_write_addtid_b32 v116 offset:5120\nG 15\nv_mfma_f32_16x16x32_bf16 v[60:63], v[168:171], a[12:15], 0\nR 160,189,24576\nds_write_addtid_b32 v117 offset:5376\nM 60,172,28\n"
"ds_write_addtid_b32 v118 offset:5632\nds_write_addtid_b32 v119 offset:5888\nv_mfma_f32_16x16x32_bf16 v[44:47], v[168:171], a[8:11], 0\nR 162,189,28672\nds_write_addtid_b32 v120 offset:6144\nM 44,172,24\n"
"ds_write_addtid_b32 v121 offset:6400\nds_write_addtid_b32 v122 offset:6656\nds_write_addtid_b32 v123 offset:6912\nds_write_addtid_b32 v124 offset:7168\nds_write_addtid_b32 v125 offset:7424\nds_write_addtid_b32 v126 offset:7680\n"
"ds_write_addtid_b32 v127 offset:7936\nv_mfma_f32_16x16x32_bf16 v[156:159], v[168:171], a[4:7], 0\nR 164,189,32768\ns_add_u32 m0, s50, 40960\nM 156,172,20\nbuffer_load_dwordx4 v221, s[4:7], s63 offen offset:1024 lds\n"
"v_mfma_f32_16x16x32_bf16 v[140:143], v[168:171], a[0:3], 0\nR 166,189,36864\ns_waitcnt vmcnt(11) lgkmcnt(2)\ns_barrier\nM 140,172,16\nds_read2st64_b32 v[96:97], v225 offset0:0 offset1:1\n"
"ds_read2st64_b32 v[98:99], v225 offset0:2 offset1:3\nv_lshrrev_b32 v226, 8, v252\nM 128,176,32\nR 168,190,24576\ns_add_u32 s63, s52, 1966080\nds_read2st64_b32 v[100:101], v225 offset0:4 offset1:5\n"
"s_lshl_b32 s60, s40, 7\ns_add_u32 s60, s60, 161280\nv_lshl_add_u32 v226, v226, 4, s60\nM 128,180,48\ns_add_u32 m0, s50, 49152\nds_read2st64_b32 v[102:103], v225 offset0:6 offset1:7\n"
"ds_read2st64_b32 v[104:105], v225 offset0:8 offset1:9\nds_read_b128 v[230:233], v226 offset:0\nM 144,176,36\nR 170,190,28672\nds_read2st64_b32 v[106:107], v225 offset0:10 offset1:11\nds_read_b128 v[234:237], v226 offset:64\n"
"M 144,180,52\nbuffer_load_dwordx4 v220, s[4:7], s63 offen lds\nds_read2st64_b32 v[108:109], v225 offset0:12 offset1:13\nM 32,176,40\nR 172,190,32768\nds_read2st64_b32 v[110:111], v225 offset0:14 offset1:15\n"
"M 32,180,56\nds_read2st64_b32 v[112:113], v225 offset0:16 offset1:17\nG 13\nv_add_f32 v64, v64, v96\nv_add_f32 v65, v65, v97\nM 48,176,44\n"
"R 174,190,36864\nG 13\nv_add_f32 v66, v66, v98\nv_add_f32 v67, v67, v99\nM 48,180,60\nds_read2st64_b32 v[114:115], v225 offset0:18 offset1:19\n"
"G 12\nv_add_f32 v68, v68, v100\nM 52,160,44\nR 176,191,24576\nv_add_f32 v69, v69, v101\nG 12\n"
"v_add_f32 v70, v70, v102\nM 52,164,60\nbuffer_load_dwordx4 v221, s[4:7], s63 offen offset:1024 lds\nv_add_f32 v71, v71, v103\nM 36,160,40\nR 178,191,28672\n"
"G 12\nv_add_f32 v72, v72, v104\nv_add_f32 v73, v73, v105\nM 36,164,56\ns_add_u32 m0, s50, 57344\nds_read2st64_b32 v[116:117], v225 offset0:20 offset1:21\n"
"G 10\nv_add_f32 v74, v74, v106\nM 148,160,36\nR 180,191,32768\nv_add_f32 v75, v75, v107\nG 9\n"
"v_add_f32 v76, v76, v108\nM 148,164,52\ns_add_u32 s63, s52, 2162688\nds_read2st64_b32 v[118:119], v225 offset0:22 offset1:23\nv_add_f32 v77, v77, v109\nM 132,160,32\n"
"R 182,191,36864\nG 9\nv_add_f32 v78, v78, v110\nv_add_f32 v79, v79, v111\nM 132,164,48\nds_read2st64_b32 v[120:121], v225 offset0:24 offset1:25\n"
"G 8\nv_add_f32 v80, v80, v112\nM 136,168,32\nR 160,188,40960\nv_add_f32 v81, v81, v113\nG 8\n"
"v_add_f32 v82, v82, v114\nM 136,172,48\nbuffer_load_dwordx4 v220, s[4:7], s63 offen lds\nv_add_f32 v83, v83, v115\nM 152,168,36\nR 162,188,45056\n"
"G 6\nv_add_f32 v84, v84, v116\nv_add_f32 v85, v85, v117\nM 152,172,52\nds_read2st64_b32 v[122:123], v225 offset0:26 offset1:27\nG 5\n"
"v_add_f32 v86, v86, v118\nv_add_f32 v87, v87, v119\nM 40,168,40\nR 164,188,49152\nds_read2st64_b32 v[124:125], v225 offset0:28 offset1:29\nM 40,172,56\n"
"buffer_load_dwordx4 v221, s[4:7], s63 offen offset:1024 lds\nG 5\nv_add_f32 v88, v88, v120\nM 56,168,44\nR 166,188,53248\ns_add_u32 m0, s50, 65536\n"
"M 56,172,60\nds_read2st64_b32 v[126:127], v225 offset0:30 offset1:31\nv_add_f32 v89, v89, v121\nM 60,176,44\nR 168,189,40960\ns_add_u32 s63, s52, 2359296\n"
"M 60,180,60\nbuffer_load_dwordx4 v220, s[4:7], s63 offen lds\nM 44,176,40\nR 170,189,45056\nG 6\nv_add_f32 v90, v90, v122\n"
"M 44,180,56\nv_add_f32 v91, v91, v123\nG 4\nv_add_f32 v92, v92, v124\nM 156,176,36\nR 172,189,49152\n"
"v_add_f32 v93, v93, v125\nM 156,180,52\nbuffer_load_dwordx4 v221, s[4:7], s63 offen offset:1024 lds\nM 140,176,32\nR 174,189,53248\ns_add_u32 m0, s50, 73728\n"
"M 140,180,48\nG 5\nG 4\nv_add_f32 v94, v94, v126\nv_add_f32 v95, v95, v127\nM 128,160,64\n"
"R 176,190,40960\ns_add_u32 s63, s52, 2555904\nv_mul_f32 v64, v64, v230\nM 128,164,80\nbuffer_load_dwordx4 v220, s[4:7], s63 offen lds\nv_mul_f32 v65, v65, v231\n"
"M 144,160,68\nR 178,190,45056\nv_mul_f32 v66, v66, v232\nv_mul_f32 v67, v67, v233\nM 144,164,84\nv_mul_f32 v68, v68, v234\n"
"v_mul_f32 v69, v69, v235\nv_mul_f32 v70, v70, v236\nM 32,160,72\nR 180,190,49152\nv_mul_f32 v71, v71, v237\nv_cvt_pk_bf16_f32 v67, v66, v67\n"
"M 32,164,88\nbuffer_load_dwordx4 v221, s[4:7], s63 offen offset:1024 lds\nv_cvt_pk_bf16_f32 v66, v64, v65\nM 48,160,76\nR 182,190,53248\ns_add_u32 s63, s52, 2752512\n"
"v_cvt_pk_bf16_f32 v68, v68, v69\nM 48,164,92\nG 4\nv_cvt_pk_bf16_f32 v69, v70, v71\nv_mul_f32 v72, v72, v230\nv_mul_f32 v73, v73, v231\n"
"M 52,168,76\nR 160,191,40960\nv_mul_f32 v74, v74, v232\nv_mul_f32 v75, v75, v233\nM 52,172,92\ns_add_u32 m0, s50, 81920\n"
"v_mul_f32 v76, v76, v234\nv_mul_f32 v77, v77, v235\nv_mul_f32 v78, v78, v236\nM 36,168,72\nR 162,191,45056\nv_mul_f32 v79, v79, v237\n"
"v_cvt_pk_bf16_f32 v72, v72, v73\nM 36,172,88\nbuffer_load_dwordx4 v220, s[4:7], s63 offen lds\nv_cvt_pk_bf16_f32 v73, v74, v75\nM 148,168,68\nR 164,191,49152\n"
"v_cvt_pk_bf16_f32 v74, v76, v77\nv_cvt_pk_bf16_f32 v75, v78, v79\nM 148,172,84\nv_mul_f32 v80, v80, v230\nv_mul_f32 v81, v81, v231\nv_mul_f32 v82, v82, v232\n"
"M 132,168,64\nR 166,191,53248\ns_waitcnt vmcnt(9)\ns_barrier\nv_mul_f32 v83, v83, v233\nM 132,172,80\n"
"G 4\nv_mul_f32 v84, v84, v234\nv_mul_f32 v85, v85, v235\nv_mul_f32 v86, v86, v236\nM 136,176,64\nR 168,184,0\n"
"s_lshl2_add_u32 s60, s45, s40\nv_lshlrev_b32 v224, 3, v252\ns_mul_i32 s60, s60, 7168\ns_bfm_b64 exec, 14, 28\nbuffer_load_dword v224, v224, s[12:15], s60 offen\ns_mov_b64 exec, -1\n"
"v_mul_f32 v87, v87, v237\nM 136,180,80\nv_cvt_pk_bf16_f32 v78, v80, v81\nv_cvt_pk_bf16_f32 v79, v82, v83\nv_cvt_pk_bf16_f32 v80, v84, v85\nM 152,176,68\n"
"R 170,184,4096\nv_cvt_pk_bf16_f32 v81, v86, v87\nv_mul_f32 v88, v88, v230\nM 152,180,84\nbuffer_load_dwordx4 v221, s[4:7], s63 offen offset:1024 lds\nv_mul_f32 v89, v89, v231\n"
"M 40,176,72\nR 172,184,8192\ns_mov_b32 s60, 155648\nv_bfe_u32 v246, v252, 4, 4\nv_lshl_add_u32 v246, v246, 3, s60\nv_mul_f32 v90, v90, v232\n"
"M 40,180,88\ns_add_u32 m0, s50, 90112\nv_mul_f32 v91, v91, v233\nv_mul_f32 v92, v92, v234\nv_mul_f32 v93, v93, v235\nM 56,176,76\n"
"R 174,184,12288\nds_read_b64 v[238:239], v246 offset:0\nv_mul_f32 v94, v94, v236\nM 56,180,92\nG 5\nv_mul_f32 v95, v95, v237\n"
"v_cvt_pk_bf16_f32 v84, v88, v89\nv_cvt_pk_bf16_f32 v85, v90, v91\nM 60,160,76\nR 176,185,0\nds_read_b64 v[240:241], v246 offset:128\nv_cvt_pk_bf16_f32 v86, v92, v93\n"
"M 60,164,92\ns_add_u32 s63, s52, 2949120\nv_cvt_pk_bf16_f32 v87, v94, v95\nv_add_u32 v108, 161792, v252\ns_lshl_b32 s60, s40, 12\ns_add_u32 s60, s60, 53248\n"
"v_add_u32 v230, s60, v252\nv_add_u32 v231, s60, v227\nM 44,160,72\nR 178,185,4096\nds_read_b64 v[242:243], v246 offset:256\nM 44,164,88\n"
"buffer_load_dwordx4 v220, s[4:7], s63 offen lds\nM 156,160,68\nR 180,185,8192\nds_read_b64 v[244:245], v246 offset:384\nM 156,164,84\nds_read_b128 v[112:115], v108\n"
"M 140,160,64\nR 182,185,12288\nds_read_b64 v[246:247], v246 offset:0\nM 140,164,80\nds_read_b128 v[104:107], v108 offset:1024\ns_mov_b64 exec, s[66:67]\n"
"buffer_store_dwordx2 v[68:69], v227, s[20:23], s64 offen offset:2048 sc1\nbuffer_store_dwordx2 v[74:75], v227, s[20:23], s64 offen offset:2560 sc1\ns_mov_b64 exec, -1\nG 11\nM 128,168,96\nR 160,186,0\n"
"M 128,172,112\nM 144,168,100\nR 162,186,4096\nM 144,172,116\nbuffer_load_dwordx4 v221, s[4:7], s63 offen offset:1024 lds\nM 32,168,104\n"
"R 164,186,8192\nG 11\nv_mfma_f32_16x16x32_bf16 v[128:131], v[238:241], v[208:211], v[128:131]\nM 32,172,120\nG 3\nv_lshlrev_b32 v100, 16, v104\n"
"v_and_b32 v101, s77, v104\nM 48,168,108\nR 166,186,12288\nv_mfma_f32_16x16x32_bf16 v[132:135], v[240:243], v[208:211], v[132:135]\nv_lshlrev_b32 v102, 16, v105\nM 48,172,124\n"
"v_and_b32 v103, s77, v105\nv_lshlrev_b32 v96, 16, v106\nM 52,176,108\nR 168,187,0\nv_mfma_f32_16x16x32_bf16 v[144:147], v[244:247], v[210:213], v[144:147]\nv_and_b32 v97, s77, v106\n"
"M 52,180,124\ns_movk_i32 s63, 7168\nv_mad_u32_u24 v220, v228, s63, v229\nv_add_u32 v221, 3668992, v220\ns_add_u32 m0, s50, 98304\ns_add_u32 s63, s101, 0\n"
"buffer_load_dwordx4 v220, s[12:15], s63 offen lds\nM 36,176,104\nR 170,187,4096\nv_mfma_f32_16x16x32_bf16 v[148:151], v[238:241], v[210:213], v[148:151]\nv_lshlrev_b32 v98, 16, v107\nM 36,180,120\n"
"v_and_b32 v99, s77, v107\nv_add_f32 v104, s78, v100\nM 148,176,100\nR 172,187,8192\nv_add_f32 v105, s78, v101\nM 148,180,116\n"
"s_mov_b64 exec, s[66:67]\nbuffer_store_dwordx2 v[80:81], v227, s[20:23], s64 offen offset:3072 sc1\nbuffer_store_dwordx2 v[86:87], v227, s[20:23], s64 offen offset:3584 sc1\ns_mov_b64 exec, -1\nv_add_f32 v106, s78, v102\nv_add_f32 v107, s78, v103\n"
"M 132,176,96\nR 174,187,12288\ns_not_b64 exec, s[66:67]\nds_write_b128 v230, v[16:19] offset:0\nds_write_b128 v230, v[20:23] offset:1024\ns_mov_b64 exec, -1\n"
"M 132,180,112\nG 6\nM 136,160,96\nR 176,184,32768\nM 136,164,112\nbuffer_load_dwordx4 v221, s[12:15], s63 offen offset:1024 lds\n"
"M 152,160,100\nR 178,184,36864\ns_not_b64 exec, s[66:67]\nds_write_b64 v231, v[68:69] offset:2048\nds_write_b64 v231, v[74:75] offset:2560\ns_mov_b64 exec, -1\n"
"M 152,164,116\nM 40,160,104\nR 180,184,40960\nM 40,164,120\nM 56,160,108\nR 182,184,45056\n"
"M 56,164,124\nG 8\nM 60,168,108\nR 160,185,32768\ns_not_b64 exec, s[66:67]\nds_write_b64 v231, v[80:81] offset:3072\n"
"ds_write_b64 v231, v[86:87] offset:3584\ns_mov_b64 exec, -1\nM 60,172,124\nM 44,168,104\nR 162,185,36864\nM 44,172,120\n"
"s_add_u32 m0, s50, 106496\nM 156,168,100\nR 164,185,40960\nM 156,172,116\ns_add_u32 s63, s101, 128\nM 140,168,96\n"
"R 166,185,45056\nM 140,172,112\nG 6\nM 128,176,128\nR 168,186,32768\ns_waitcnt vmcnt(13) lgkmcnt(4)\n"
"s_barrier\nM 128,180,144\nbuffer_load_dwordx4 v220, s[12:15], s63 offen lds\nM 144,176,132\nR 170,186,36864\nM 144,180,148\n"
"s_cmp_eq_u32 s78, 0\ns_cbranch_scc1 Lq157\nv_mov_b32 v230, 0\nv_mov_b32 v231, 0\nv_mov_b32 v232, 0\nv_mov_b32 v233, 0\n"
"v_mov_b32 v64, 0\nv_mov_b32 v65, 0\nv_mov_b32 v70, 0\nv_mov_b32 v71, 0\nv_mov_b32 v234, 0\nv_mov_b32 v235, 0\n"
"v_mov_b32 v236, 0\nv_mov_b32 v237, 0\nv_mov_b32 v76, 0\nv_mov_b32 v77, 0\nv_mov_b32 v82, 0\nv_mov_b32 v83, 0\n"
"Lq157:\ns_lshl4_add_u32 s65, s41, s42\nM 32,176,136\nR 172,186,40960\ns_lshl_b32 s65, s65, 12\nM 32,180,152\n"
"s_add_u32 s65, s65, 77529088\nM 48,176,140\nR 174,186,45056\nM 48,180,156\nG 4\nM 52,160,140\n"
"R 176,187,32768\nM 52,164,156\nbuffer_load_dwordx4 v221, s[12:15], s63 offen offset:1024 lds\nM 36,160,136\nR 178,187,36864\nM 36,164,152\n"
"s_add_u32 m0, s50, 114688\nM 148,160,132\nR 180,187,40960\nM 148,164,148\ns_add_u32 s63, s101, 256\nM 132,160,128\n"
"R 182,187,45056\nM 132,164,144\nG 4\nM 136,168,128\nR 160,184,49152\nM 136,172,144\n"
"buffer_load_dwordx4 v220, s[12:15], s63 offen lds\nM 152,168,132\nR 162,184,53248\nM 152,172,148\nM 40,168,136\nR 164,184,57344\n"
"s_lshl2_add_u32 s60, s45, s40\ns_waitcnt vmcnt(12)\nv_lshlrev_b32 v224, 3, v252\ns_mul_i32 s60, s60, 7168\ns_bfm_b64 exec, 14, 42\nbuffer_load_dword v224, v224, s[12:15], s60 offen\n"
"s_mov_b64 exec, -1\nM 40,172,152\nM 56,168,140\nR 166,184,61440\nv_mfma_f32_16x16x32_bf16 v[136:139], v[242:245], v[208:211], v[136:139]\nM 56,172,156\n"
"G 4\nM 60,176,140\nR 168,185,49152\nv_mfma_f32_16x16x32_bf16 v[140:143], v[244:247], v[208:211], v[140:143]\nM 60,180,156\nbuffer_load_dwordx4 v221, s[12:15], s63 offen offset:1024 lds\n"
"M 44,176,136\nR 170,185,53248\nv_mfma_f32_16x16x32_bf16 v[152:155], v[240:243], v[210:213], v[152:155]\ns_add_u32 s63, s100, 0\nM 44,180,152\ns_add_u32 m0, s50, 0\n"
"M 156,176,132\nR 172,185,57344\nv_mfma_f32_16x16x32_bf16 v[156:159], v[242:245], v[210:213], v[156:159]\nM 156,180,148\nbuffer_load_dwordx4 v222, s[12:15], s63 offen lds\nM 140,176,128\n"
"R 174,185,61440\nM 140,180,144\nG 4\nM 128,160,160\nR 176,186,49152\nM 128,164,176\n"
"M 144,160,164\nR 178,186,53248\nM 144,164,180\nbuffer_load_dwordx4 v223, s[12:15], s63 offen offset:1024 lds\nM 32,160,168\nR 180,186,57344\n"
"M 32,164,184\nM 48,160,172\nR 182,186,61440\nM 48,164,188\nG 4\nM 52,168,172\n"
"R 160,187,49152\nM 52,172,188\nM 36,168,168\nR 162,187,53248\nM 36,172,184\ns_movk_i32 s63, 2048\n"
"v_mad_u32_u24 v220, v228, s63, v229\nv_add_u32 v221, 1047552, v220\ns_add_u32 m0, s50, 8192\ns_add_u32 s63, s53, 0\nbuffer_load_dwordx4 v220, s[8:11], s63 offen lds\nM 148,168,164\n"
"R 164,187,57344\ns_lshl_b32 s60, s40, 12\ns_add_u32 s60, s60, 49152\nv_add_u32 v226, s60, v252\nv_add_u32 v111, s60, v227\nM 148,172,180\n"
"M 132,168,160\nR 166,187,61440\ns_cmp_eq_u32 s40, 0\ns_cselect_b64 s[88:89], 0, -1\ns_cselect_b32 s60, s41, 0\ns_cmp_lg_u32 s60, 0\n"
"s_cselect_b64 s[66:67], -1, 0\nM 132,172,176\nG 4\nM 136,176,160\nR 168,188,0\nM 136,180,176\n"
"s_mov_b64 exec, s[88:89]\nds_read_b128 v[230:233], v226 offset:0\nds_read_b64 v[64:65], v111 offset:2048\ns_mov_b64 exec, -1\nM 152,176,164\nR 170,188,4096\n"
"s_waitcnt vmcnt(12) lgkmcnt(1)\ns_barrier\nM 152,180,180\nbuffer_load_dwordx4 v221, s[8:11], s63 offen offset:1024 lds\nM 40,176,168\nR 172,188,8192\n"
"M 40,180,184\ns_mov_b64 exec, s[88:89]\nds_read_b64 v[70:71], v111 offset:2560\nds_read_b128 v[234:237], v226 offset:1024\ns_mov_b64 exec, -1\nM 56,176,172\n"
"R 174,188,12288\nM 56,180,188\nM 60,160,172\nR 176,189,0\nM 60,164,188\ns_mov_b64 exec, s[88:89]\n"
"ds_read_b64 v[76:77], v111 offset:3072\nds_read_b64 v[82:83], v111 offset:3584\ns_mov_b64 exec, -1\nM 44,160,168\nR 178,189,4096\nM 44,164,184\n"
"M 156,160,164\nR 180,189,8192\nM 156,164,180\nM 140,160,160\nR 182,189,12288\nM 140,164,176\n"
"G 6\nM 32,168,200\nR 160,190,0\ns_mov_b64 exec, s[66:67]\nbuffer_load_dwordx4 v[230:233], v252, s[20:23], s65 offen offset:0 sc1\nbuffer_load_dwordx2 v[64:65], v227, s[20:23], s65 offen offset:2048 sc1\n"
"s_mov_b64 exec, -1\nM 32,172,216\nR 162,190,4096\nM 48,168,204\nR 164,190,8192\nM 48,172,220\n"
"R 166,190,12288\nG 4\nM 52,176,204\nR 168,191,0\nM 52,180,220\nR 170,191,4096\n"
"s_mov_b64 exec, s[66:67]\nbuffer_load_dwordx2 v[70:71], v227, s[20:23], s65 offen offset:2560 sc1\nbuffer_load_dwordx4 v[234:237], v252, s[20:23], s65 offen offset:1024 sc1\ns_mov_b64 exec, -1\nM 36,176,200\nR 172,191,8192\n"
"M 36,180,216\nR 174,191,12288\nG 4\nM 40,160,200\nR 176,188,16384\nM 40,164,216\n"
"R 178,188,20480\nM 56,160,204\nR 180,188,24576\ns_mov_b64 exec, s[66:67]\nbuffer_load_dwordx2 v[76:77], v227, s[20:23], s65 offen offset:3072 sc1\nbuffer_load_dwordx2 v[82:83], v227, s[20:23], s65 offen offset:3584 sc1\n"
"s_mov_b64 exec, -1\nM 56,164,220\nR 182,188,28672\nG 4\nM 60,168,204\nR 160,189,16384\n"
"M 60,172,220\nR 162,189,20480\nM 44,168,200\nR 164,189,24576\nM 44,172,216\nR 166,189,28672\n"
"G 4\nM 32,176,232\nR 168,190,16384\nLq188:\ns_lshr_b32 s60, s45, 3\ns_lshl2_add_u32 s60, s60, s40\n"
"s_lshl_b32 s60, s60, 8\ns_waitcnt vmcnt(11)\nv_lshl_add_u32 v224, v252, 3, s60\nv_add_u32 v224, Lq188+4096-LqPC, v224\ns_mov_b64 exec, 3\nglobal_load_dword v224, v224, s[98:99]\n"
"s_mov_b64 exec, -1\nM 32,180,248\nR 170,190,20480\nM 48,176,236\nR 172,190,24576\nM 48,180,252\n"
"R 174,190,28672\nG 4\nM 52,160,236\nR 176,191,16384\nM 52,164,252\nR 178,191,20480\n"
"M 36,160,232\nR 180,191,24576\nM 36,164,248\nR 182,191,28672\nG 4\nM 40,168,232\n"
"R 160,188,0\nM 40,172,248\nR 162,188,4096\nM 56,168,236\nR 164,188,8192\nM 56,172,252\n"
"R 166,188,12288\nG 4\nM 60,176,236\nR 168,189,0\nM 60,180,252\nR 170,189,4096\n"
"M 44,176,232\nR 172,189,8192\nM 44,180,248\nR 174,189,12288\nG 4\nM 128,160,192\n"
"R 176,190,0\nM 128,164,208\nR 178,190,4096\ns_mov_b32 m0, s51\nM 144,160,196\nR 180,190,8192\n"
"ds_write_addtid_b32 v32 offset:0\nM 144,164,212\nR 182,190,12288\nds_write_addtid_b32 v33 offset:256\nG 6\nM 148,168,196\n"
"R 160,191,0\nds_write_addtid_b32 v34 offset:512\nM 148,172,212\nR 162,191,4096\nds_write_addtid_b32 v35 offset:768\nM 132,168,192\n"
"R 164,191,8192\nds_write_addtid_b32 v36 offset:1024\nM 132,172,208\nR 166,191,12288\nds_write_addtid_b32 v37 offset:1280\nG 9\n"
"M 136,176,192\nR 168,188,16384\nds_write_addtid_b32 v38 offset:1536\nM 136,180,208\nR 170,188,20480\nds_write_addtid_b32 v39 offset:1792\n"
"M 152,176,196\nR 172,188,24576\nds_write_addtid_b32 v40 offset:2048\nM 152,180,212\nR 174,188,28672\nds_write_addtid_b32 v41 offset:2304\n"
"G 9\nM 156,160,196\nR 176,189,16384\nds_write_addtid_b32 v42 offset:2560\nM 156,164,212\nR 178,189,20480\n"
"ds_write_addtid_b32 v43 offset:2816\nM 140,160,192\nR 180,189,24576\nds_write_addtid_b32 v44 offset:3072\nds_write_addtid_b32 v45 offset:3328\nds_write_addtid_b32 v46 offset:3584\n"
"ds_write_addtid_b32 v47 offset:3840\nds_write_addtid_b32 v48 offset:4096\nds_write_addtid_b32 v49 offset:4352\nds_write_addtid_b32 v50 offset:4608\nds_write_addtid_b32 v51 offset:4864\nds_write_addtid_b32 v52 offset:5120\n"
"ds_write_addtid_b32 v53 offset:5376\nds_write_addtid_b32 v54 offset:5632\nds_write_addtid_b32 v55 offset:5888\nds_write_addtid_b32 v56 offset:6144\nds_write_addtid_b32 v57 offset:6400\nds_write_addtid_b32 v58 offset:6656\n"
"ds_write_addtid_b32 v59 offset:6912\nds_write_addtid_b32 v60 offset:7168\nds_write_addtid_b32 v61 offset:7424\nds_write_addtid_b32 v62 offset:7680\nds_write_addtid_b32 v63 offset:7936\nM 140,164,208\n"
"R 182,189,28672\nG 15\nM 128,168,224\nR 160,190,16384\ns_add_u32 m0, s50, 16384\nM 128,172,240\n"
"R 162,190,20480\nG 3\ns_barrier\ns_add_u32 s63, s53, 65536\nM 144,168,228\nR 164,190,24576\n"
"buffer_load_dwordx4 v220, s[8:11], s63 offen lds\nds_read2st64_b32 v[32:33], v225 offset0:0 offset1:1\nM 144,172,244\nR 166,190,28672\nbuffer_load_dwordx4 v221, s[8:11], s63 offen offset:1024 lds\nds_read2st64_b32 v[34:35], v225 offset0:2 offset1:3\n"
"G 6\nM 148,176,228\nR 168,191,16384\ns_add_u32 m0, s50, 24576\ns_add_u32 s63, s53, 131072\nbuffer_load_dwordx4 v220, s[8:11], s63 offen lds\n"
"ds_read2st64_b32 v[36:37], v225 offset0:4 offset1:5\nM 148,180,244\nR 170,191,20480\nbuffer_load_dwordx4 v221, s[8:11], s63 offen offset:1024 lds\nds_read2st64_b32 v[38:39], v225 offset0:6 offset1:7\nM 132,176,224\n"
"R 172,191,24576\ns_add_u32 m0, s50, 32768\ns_add_u32 s63, s53, 196608\nbuffer_load_dwordx4 v220, s[8:11], s63 offen lds\nds_read2st64_b32 v[40:41], v225 offset0:8 offset1:9\nM 132,180,240\n"
"R 174,191,28672\nbuffer_load_dwordx4 v221, s[8:11], s63 offen offset:1024 lds\nds_read2st64_b32 v[42:43], v225 offset0:10 offset1:11\nG 9\nM 136,160,224\ns_add_u32 m0, s50, 40960\n"
"s_add_u32 s63, s53, 262144\nbuffer_load_dwordx4 v220, s[8:11], s63 offen lds\nM 136,164,240\nbuffer_load_dwordx4 v221, s[8:11], s63 offen offset:1024 lds\nM 152,160,228\ns_add_u32 m0, s50, 49152\n"
"s_add_u32 s63, s53, 327680\nbuffer_load_dwordx4 v220, s[8:11], s63 offen lds\nM 152,164,244\nds_read2st64_b32 v[44:45], v225 offset0:12 offset1:13\nds_read2st64_b32 v[46:47], v225 offset0:14 offset1:15\nG 3\n"
"M 156,168,228\nds_read2st64_b32 v[48:49], v225 offset0:16 offset1:17\nds_read2st64_b32 v[50:51], v225 offset0:18 offset1:19\nM 156,172,244\nds_read2st64_b32 v[52:53], v225 offset0:20 offset1:21\nds_read2st64_b32 v[54:55], v225 offset0:22 offset1:23\n"
"M 140,168,224\nds_read2st64_b32 v[56:57], v225 offset0:24 offset1:25\nds_read2st64_b32 v[58:59], v225 offset0:26 offset1:27\nM 140,172,240\nds_read2st64_b32 v[60:61], v225 offset0:28 offset1:29\nds_read2st64_b32 v[62:63], v225 offset0:30 offset1:31\n"
"G 0\ns_barrier\nv_pk_add_f32 v[144:145], v[144:145], v[48:49]\nv_pk_add_f32 v[146:147], v[146:147], v[50:51]\nv_pk_add_f32 v[148:149], v[148:149], v[52:53]\nv_pk_add_f32 v[150:151], v[150:151], v[54:55]\n"
"v_pk_add_f32 v[152:153], v[152:153], v[56:57]\nv_pk_add_f32 v[154:155], v[154:155], v[58:59]\nv_pk_add_f32 v[156:157], v[156:157], v[60:61]\nv_pk_add_f32 v[158:159], v[158:159], v[62:63]\nv_pk_add_f32 v[128:129], v[128:129], v[32:33]\nv_pk_add_f32 v[130:131], v[130:131], v[34:35]\n"
"v_pk_add_f32 v[132:133], v[132:133], v[36:37]\nv_pk_add_f32 v[134:135], v[134:135], v[38:39]\nv_pk_add_f32 v[136:137], v[136:137], v[40:41]\nv_pk_add_f32 v[138:139], v[138:139], v[42:43]\nv_pk_add_f32 v[140:141], v[140:141], v[44:45]\nv_pk_add_f32 v[142:143], v[142:143], v[46:47]\n"
"v_cvt_pk_bf16_f32 v144, v144, v145\nv_cvt_pk_bf16_f32 v145, v146, v147\nv_cvt_pk_bf16_f32 v146, v148, v149\nv_cvt_pk_bf16_f32 v147, v150, v151\nv_cvt_pk_bf16_f32 v152, v152, v153\nv_cvt_pk_bf16_f32 v153, v154, v155\n"
"v_cvt_pk_bf16_f32 v154, v156, v157\nv_cvt_pk_bf16_f32 v155, v158, v159\nv_cvt_pk_bf16_f32 v128, v128, v129\nv_cvt_pk_bf16_f32 v129, v130, v131\nv_cvt_pk_bf16_f32 v130, v132, v133\nv_cvt_pk_bf16_f32 v131, v134, v135\n"
"v_cvt_pk_bf16_f32 v136, v136, v137\nv_cvt_pk_bf16_f32 v137, v138, v139\nv_cvt_pk_bf16_f32 v138, v140, v141\nv_cvt_pk_bf16_f32 v139, v142, v143\nv_mfma_f32_16x16x32_bf16 v[52:55], v[16:19], v[144:147], v[96:99]\nv_mfma_f32_16x16x32_bf16 v[48:51], v[0:3], v[144:147], v[100:103]\n"
"v_mfma_f32_16x16x32_bf16 v[60:63], v[20:23], v[152:155], v[96:99]\nv_mfma_f32_16x16x32_bf16 v[56:59], v[4:7], v[152:155], v[100:103]\ns_waitcnt vmcnt(13)\nv_max3_u32 v248, v233, v65, v71\nv_cmp_eq_u32 vcc, -1, v248\ns_nop 0\n"
"s_cbranch_vccnz Lq190\nLq191:\ns_waitcnt vmcnt(10)\nv_max3_u32 v248, v237, v77, v83\nv_cmp_eq_u32 vcc, -1, v248\ns_nop 0\n"
"s_cbranch_vccnz Lq192\nLq193:\nv_mfma_f32_16x16x32_bf16 v[36:39], v[0:3], v[128:131], v[96:99]\nv_mfma_f32_16x16x32_bf16 v[32:35], v[230:233], v[128:131], v[104:107]\nv_mfma_f32_16x16x32_bf16 v[44:47], v[4:7], v[136:139], v[96:99]\nv_mfma_f32_16x16x32_bf16 v[40:43], v[234:237], v[136:139], v[104:107]\n"
"s_lshl_b32 s84, s42, 11\ns_add_u32 s84, s84, s49\ns_add_u32 s84, s84, 2097152\ns_add_u32 s85, s84, 32768\ns_lshl2_add_u32 s60, s45, s40\ns_waitcnt vmcnt(9)\n"
"v_lshlrev_b32 v224, 3, v252\ns_mul_i32 s60, s60, 7168\ns_bfm_b64 exec, 28, 28\nbuffer_load_dword v224, v224, s[16:19], s60 offen\ns_mov_b64 exec, -1\nv_mov_b32 v238, s46\n"
"v_mov_b32 v239, s47\nv_max_f32 v52, v52, v53\nv_max_f32 v54, v54, v55\nv_max3_f32 v52, v52, v48, v49\nv_max3_f32 v54, v54, v50, v51\nv_max_f32 v118, v52, v54\n"
"v_max_f32 v60, v60, v61\nv_max_f32 v62, v62, v63\nv_max3_f32 v60, v60, v56, v57\nv_max3_f32 v62, v62, v58, v59\nv_max_f32 v119, v60, v62\nv_max_f32 v36, v36, v37\n"
"v_max_f32 v38, v38, v39\nv_max3_f32 v36, v36, v32, v33\nv_max3_f32 v38, v38, v34, v35\nv_max_f32 v116, v36, v38\nv_max_f32 v44, v44, v45\nv_max_f32 v46, v46, v47\n"
"v_max3_f32 v44, v44, v40, v41\nv_max3_f32 v46, v46, v42, v43\nv_max_f32 v117, v44, v46\nv_permlane32_swap_b32 v116, v118\ns_nop 0\nv_permlane32_swap_b32 v117, v119\n"
"v_max_f32 v116, v116, v118\nv_max_f32 v117, v117, v119\ns_nop 1\nv_permlane16_swap_b32 v116, v117\nv_max_f32 v116, v116, v117\nv_mov_b32 v117, v116\n"
"s_nop 1\nv_permlane16_swap_b32 v116, v117\nv_mov_b32 v118, v116\nv_mov_b32 v119, v117\ns_nop 0\nv_permlane32_swap_b32 v116, v118\n"
"v_permlane32_swap_b32 v117, v119\nv_mul_f32 v116, v116, v215\nv_mul_f32 v118, v118, v218\nv_mul_f32 v117, v117, v215\nv_mul_f32 v119, v119, v218\nv_fma_f32 v52, v52, v218, -v118\n"
"v_fma_f32 v54, v54, v218, -v118\nv_fma_f32 v60, v60, v218, -v119\nv_fma_f32 v62, v62, v218, -v119\nv_fma_f32 v36, v36, v215, -v116\nv_fma_f32 v38, v38, v215, -v116\nv_fma_f32 v44, v44, v215, -v117\n"
"v_fma_f32 v46, v46, v215, -v117\nv_exp_f32 v52, v52\nv_exp_f32 v54, v54\nv_exp_f32 v60, v60\nv_exp_f32 v62, v62\nv_exp_f32 v36, v36\n"
"v_exp_f32 v38, v38\nv_exp_f32 v44, v44\nv_exp_f32 v46, v46\nv_add_f32 v118, v52, v54\nv_add_f32 v119, v60, v62\nv_add_f32 v116, v36, v38\n"
"v_add_f32 v117, v44, v46\ns_nop 0\nv_permlane32_swap_b32 v116, v118\nv_permlane32_swap_b32 v117, v119\nv_add_f32 v116, v116, v118\nv_add_f32 v117, v117, v119\n"
"s_nop 1\nv_permlane16_swap_b32 v116, v117\nv_add_f32 v116, v116, v117\nv_rcp_f32 v116, v116\ns_nop 0\nv_mov_b32 v117, v116\n"
"s_nop 1\nv_permlane16_swap_b32 v116, v117\nv_mov_b32 v118, v116\nv_mov_b32 v119, v117\ns_nop 0\nv_permlane32_swap_b32 v116, v118\n"
"v_permlane32_swap_b32 v117, v119\nv_mul_f32 v52, v52, v118\nv_mul_f32 v54, v54, v118\nv_mul_f32 v60, v60, v119\nv_mul_f32 v62, v62, v119\nv_mul_f32 v36, v36, v116\n"
"v_mul_f32 v38, v38, v116\nv_mul_f32 v44, v44, v117\nv_mul_f32 v46, v46, v117\nv_cvt_pk_bf16_f32 v52, v52, v52\nv_cvt_pk_bf16_f32 v54, v54, v54\nv_cvt_pk_bf16_f32 v60, v60, v60\n"
"v_cvt_pk_bf16_f32 v62, v62, v62\nv_cvt_pk_bf16_f32 v36, v36, v36\nv_cvt_pk_bf16_f32 v38, v38, v38\nv_cvt_pk_bf16_f32 v44, v44, v44\nv_cvt_pk_bf16_f32 v46, v46, v46\nv_and_b32 v48, v52, v112\n"
"v_and_b32 v49, v54, v113\nv_and_b32 v50, v52, v114\nv_and_b32 v51, v54, v115\nv_and_b32 v56, v60, v112\nv_and_b32 v57, v62, v113\nv_and_b32 v58, v60, v114\n"
"v_and_b32 v59, v62, v115\nv_mfma_f32_16x16x32_bf16 v[144:147], v[66:69], v[48:51], 0\nv_mfma_f32_16x16x32_bf16 v[148:151], v[72:75], v[48:51], 0\nv_and_b32 v32, v36, v112\nv_and_b32 v33, v38, v113\nv_and_b32 v34, v36, v114\n"
"v_and_b32 v35, v38, v115\nv_mfma_f32_16x16x32_bf16 v[152:155], v[78:81], v[56:59], 0\nv_mfma_f32_16x16x32_bf16 v[156:159], v[84:87], v[56:59], 0\nv_and_b32 v40, v44, v112\nv_and_b32 v41, v46, v113\nv_and_b32 v42, v44, v114\n"
"v_and_b32 v43, v46, v115\nv_mfma_f32_16x16x32_bf16 v[128:131], v[64:67], v[32:35], 0\nv_mfma_f32_16x16x32_bf16 v[132:135], v[70:73], v[32:35], 0\nv_mfma_f32_16x16x32_bf16 v[136:139], v[76:79], v[40:43], 0\nv_mfma_f32_16x16x32_bf16 v[140:143], v[82:85], v[40:43], 0\nv_cvt_pk_bf16_f32 v144, v144, v145\n"
"v_cvt_pk_bf16_f32 v145, v146, v147\nv_cvt_pk_bf16_f32 v146, v148, v149\nv_cvt_pk_bf16_f32 v147, v150, v151\nbuffer_store_dwordx4 v[144:147], v252, s[20:23], s85 offen offset:0 sc1\nv_cvt_pk_bf16_f32 v152, v152, v153\nv_cvt_pk_bf16_f32 v153, v154, v155\n"
"v_cvt_pk_bf16_f32 v154, v156, v157\nv_cvt_pk_bf16_f32 v155, v158, v159\nbuffer_store_dwordx4 v[152:155], v252, s[20:23], s85 offen offset:1024 sc1\nv_cvt_pk_bf16_f32 v128, v128, v129\nv_cvt_pk_bf16_f32 v129, v130, v131\nv_cvt_pk_bf16_f32 v130, v132, v133\n"
"v_cvt_pk_bf16_f32 v131, v134, v135\nbuffer_store_dwordx4 v[128:131], v252, s[20:23], s84 offen offset:0 sc1\nv_cvt_pk_bf16_f32 v136, v136, v137\nv_cvt_pk_bf16_f32 v137, v138, v139\nv_cvt_pk_bf16_f32 v138, v140, v141\nv_cvt_pk_bf16_f32 v139, v142, v143\n"
"buffer_store_dwordx4 v[136:139], v252, s[20:23], s84 offen offset:1024 sc1\ns_add_u32 s61, s79, 88080384\ns_lshl_b32 s62, s42, 3\ns_add_u32 s61, s61, s62\ns_waitcnt vmcnt(0)\ns_mov_b64 exec, 1\n"
"buffer_atomic_swap_x2 v[238:239], off, s[20:23], s61 sc1\ns_mov_b64 exec, -1\nv_bfe_u32 v250, v252, 7, 1\nv_mul_u32_u24 v250, 2176, v250\nv_bfe_u32 v251, v252, 4, 3\nv_lshl_add_u32 v250, v251, 3, v250\n"
"s_lshl_b32 s60, s90, 6\nv_add_u32 v250, s60, v250\ns_sleep 5\nv_mov_b32 v248, 0\nv_mov_b32 v249, 0\ns_add_u32 s61, s80, 88080384\n"
"s_mov_b64 exec, 0xffff\nbuffer_atomic_or_x2 v[248:249], v250, s[20:23], s61 offen sc0 sc1\ns_mov_b64 exec, -1\nbuffer_load_dwordx4 v221, s[8:11], s63 offen offset:1024 lds\ns_add_u32 m0, s50, 57344\ns_add_u32 s63, s53, 393216\n"
"buffer_load_dwordx4 v220, s[8:11], s63 offen lds\nbuffer_load_dwordx4 v221, s[8:11], s63 offen offset:1024 lds\ns_add_u32 m0, s50, 65536\ns_add_u32 s63, s53, 458752\nbuffer_load_dwordx4 v220, s[8:11], s63 offen lds\nbuffer_load_dwordx4 v221, s[8:11], s63 offen offset:1024 lds\n"
"s_add_u32 m0, s50, 73728\ns_add_u32 s63, s53, 524288\nbuffer_load_dwordx4 v220, s[8:11], s63 offen lds\nbuffer_load_dwordx4 v221, s[8:11], s63 offen offset:1024 lds\ns_add_u32 m0, s50, 81920\ns_add_u32 s63, s53, 589824\n"
"buffer_load_dwordx4 v220, s[8:11], s63 offen lds\nbuffer_load_dwordx4 v221, s[8:11], s63 offen offset:1024 lds\ns_add_u32 m0, s50, 90112\ns_add_u32 s63, s53, 655360\nbuffer_load_dwordx4 v220, s[8:11], s63 offen lds\nbuffer_load_dwordx4 v221, s[8:11], s63 offen offset:1024 lds\n"
"v_bfe_u32 v234, v252, 4, 4\nv_lshrrev_b32 v235, 8, v252\nv_lshlrev_b32 v235, 2, v235\nv_sub_u32 v234, v234, v235\nv_lshlrev_b32 v235, 4, v234\ns_mov_b64 s[60:61], 0x3f80\n"
"v_lshlrev_b64 v[208:209], v235, s[60:61]\nv_cmp_gt_u32 s[60:61], 4, v234\ns_nop 1\nv_cndmask_b32 v212, 0, v208, s[60:61]\nv_cndmask_b32 v208, 0, v208, s[60:61]\nv_cndmask_b32 v213, 0, v209, s[60:61]\n"
"v_cndmask_b32 v209, 0, v209, s[60:61]\ns_nop 1\nv_mfma_f32_16x16x32_bf16 v[0:3], v[208:211], v[192:195], 0\nv_mfma_f32_16x16x32_bf16 v[4:7], v[210:213], v[192:195], 0\nv_mfma_f32_16x16x32_bf16 v[8:11], v[208:211], v[196:199], 0\nv_mfma_f32_16x16x32_bf16 v[12:15], v[210:213], v[196:199], 0\n"
"v_mfma_f32_16x16x32_bf16 v[16:19], v[208:211], v[200:203], 0\nv_mfma_f32_16x16x32_bf16 v[20:23], v[210:213], v[200:203], 0\nv_mfma_f32_16x16x32_bf16 v[24:27], v[208:211], v[204:207], 0\nv_mfma_f32_16x16x32_bf16 v[28:31], v[210:213], v[204:207], 0\ns_waitcnt vmcnt(11)\nv_cmp_eq_u64 s[86:87], s[46:47], v[248:249]\n"
"s_nop 0\ns_and_b32 s62, s86, 0xffff\ns_cmp_eq_u32 s62, 0xffff\ns_cbranch_scc0 Lq195\nLq196:\ns_add_u32 s64, s70, 2097152\n"
"buffer_load_dwordx4 a[0:3], v252, s[20:23], s64 offen offset:0 sc1\ns_add_u32 s65, s70, 2129920\nbuffer_load_dwordx4 a[4:7], v252, s[20:23], s65 offen offset:0 sc1\ns_add_u32 s66, s71, 2097152\nbuffer_load_dwordx4 a[8:11], v252, s[20:23], s66 offen offset:0 sc1\ns_add_u32 s67, s71, 2129920\n"
"buffer_load_dwordx4 a[12:15], v252, s[20:23], s67 offen offset:0 sc1\nbuffer_load_dwordx4 a[16:19], v252, s[20:23], s64 offen offset:1024 sc1\nbuffer_load_dwordx4 a[20:23], v252, s[20:23], s65 offen offset:1024 sc1\nbuffer_load_dwordx4 a[24:27], v252, s[20:23], s66 offen offset:1024 sc1\nbuffer_load_dwordx4 a[28:31], v252, s[20:23], s67 offen offset:1024 sc1\ns_barrier\n"
"R 160,184,8192\nR 162,184,12288\nR 164,184,16384\nR 166,184,20480\nR 168,185,8192\nR 170,185,12288\n"
"R 172,185,16384\nR 174,185,20480\nR 176,186,8192\nR 178,186,12288\nR 180,186,16384\nR 182,186,20480\n"
"buffer_load_dwordx4 a[32:35], v252, s[20:23], s64 offen offset:2048 sc1\nbuffer_load_dwordx4 a[36:39], v252, s[20:23], s65 offen offset:2048 sc1\nbuffer_load_dwordx4 a[40:43], v252, s[20:23], s66 offen offset:2048 sc1\nbuffer_load_dwordx4 a[44:47], v252, s[20:23], s67 offen offset:2048 sc1\nbuffer_load_dwordx4 a[48:51], v252, s[20:23], s64 offen offset:3072 sc1\nbuffer_load_dwordx4 a[52:55], v252, s[20:23], s65 offen offset:3072 sc1\n"
"buffer_load_dwordx4 a[56:59], v252, s[20:23], s66 offen offset:3072 sc1\nbuffer_load_dwordx4 a[60:63], v252, s[20:23], s67 offen offset:3072 sc1\ns_add_u32 s64, s70, 2101248\nbuffer_load_dwordx4 a[64:67], v252, s[20:23], s64 offen offset:0 sc1\ns_add_u32 s65, s70, 2134016\nbuffer_load_dwordx4 a[68:71], v252, s[20:23], s65 offen offset:0 sc1\n"
"s_add_u32 s66, s71, 2101248\nbuffer_load_dwordx4 a[72:75], v252, s[20:23], s66 offen offset:0 sc1\ns_add_u32 s67, s71, 2134016\nbuffer_load_dwordx4 a[76:79], v252, s[20:23], s67 offen offset:0 sc1\ns_waitcnt vmcnt(19) lgkmcnt(10)\nM 0,160,0\n"
"buffer_load_dwordx4 a[80:83], v252, s[20:23], s64 offen offset:1024 sc1\ns_waitcnt vmcnt(16) lgkmcnt(8)\nM 0,164,16\nM 16,160,4\ns_lshl_b32 s60, s40, 5\nv_xor_b32 v238, s60, v188\n"
"v_and_b32 v238, 0xfffffbff, v238\ns_waitcnt vmcnt(15)\nM 16,164,20\nv_mov_b32 v242, s76\nv_mov_b32 v243, s76\nv_mfma_f32_16x16x32_bf16 v[32:35], v[160:163], a[8:11], 0\n"
"buffer_load_dwordx4 a[84:87], v252, s[20:23], s65 offen offset:1024 sc1\ns_waitcnt vmcnt(15)\nM 32,164,24\nv_mov_b32 v244, s76\nv_mov_b32 v245, s76\nv_mfma_f32_16x16x32_bf16 v[48:51], v[160:163], a[12:15], 0\n"
"s_waitcnt vmcnt(14)\nM 48,164,28\nG 4\nv_mfma_f32_16x16x32_bf16 v[52:55], v[168:171], a[12:15], 0\nR 160,187,8192\nM 52,172,28\n"
"buffer_load_dwordx4 a[88:91], v252, s[20:23], s66 offen offset:1024 sc1\nv_mfma_f32_16x16x32_bf16 v[36:39], v[168:171], a[8:11], 0\nR 162,187,12288\nM 36,172,24\nM 20,168,4\nR 164,187,16384\n"
"R 64,238,32768\nR 66,238,36864\nM 20,172,20\nbuffer_load_dwordx4 a[92:95], v252, s[20:23], s67 offen offset:1024 sc1\nM 4,168,0\nR 166,187,20480\n"
"R 68,238,33792\nR 70,238,37888\nM 4,172,16\nG 8\nM 8,176,0\nR 168,184,24576\n"
"M 8,180,16\nbuffer_load_dwordx4 a[96:99], v252, s[20:23], s64 offen offset:2048 sc1\nM 24,176,4\nR 170,184,28672\nM 24,180,20\nv_mfma_f32_16x16x32_bf16 v[40:43], v[176:179], a[8:11], 0\n"
"R 172,184,32768\nG 6\nv_mfma_f32_16x16x32_bf16 v[72:75], v[242:245], v[64:67], 0\nM 40,180,24\nG 3\nv_mfma_f32_16x16x32_bf16 v[72:75], v[242:245], v[68:71], v[72:75]\n"
"buffer_load_dwordx4 a[100:103], v252, s[20:23], s65 offen offset:2048 sc1\nv_mfma_f32_16x16x32_bf16 v[56:59], v[176:179], a[12:15], 0\nR 174,184,36864\nM 56,180,28\nv_mfma_f32_16x16x32_bf16 v[60:63], v[160:163], a[12:15], 0\nR 176,185,24576\n"
"M 60,164,28\nbuffer_load_dwordx4 a[104:107], v252, s[20:23], s66 offen offset:2048 sc1\nv_lshrrev_b32 v64, 1, v252\nv_mfma_f32_16x16x32_bf16 v[44:47], v[160:163], a[8:11], 0\nR 178,185,28672\nv_mov_b32 v73, s46\n"
"s_and_b32 s61, s40, 3\ns_lshl_b32 s61, s61, 7\ns_add_u32 s61, s61, s91\ns_add_u32 s61, s61, 393216\ns_mov_b64 exec, 0xffff\nbuffer_store_dwordx2 v[72:73], v64, s[20:23], s61 offen sc1\n"
"s_mov_b64 exec, -1\nM 44,164,24\nM 28,160,4\nR 180,185,32768\nM 28,164,20\nbuffer_load_dwordx4 a[108:111], v252, s[20:23], s67 offen offset:2048 sc1\n"
"M 12,160,0\nR 182,185,36864\nM 12,164,16\ns_lshl_b32 s60, s40, 5\nv_xor_b32 v238, s60, v188\nG 4\n"
"s_waitcnt vmcnt(20)\nM 0,168,32\nR 160,186,24576\nv_and_b32 v238, 0xfffffbff, v238\ns_waitcnt vmcnt(16)\nM 0,172,48\n"
"buffer_load_dwordx4 a[112:115], v252, s[20:23], s64 offen offset:3072 sc1\nM 16,168,36\nR 162,186,28672\ns_waitcnt vmcnt(16)\nM 16,172,52\nM 32,168,40\n"
"R 164,186,32768\ns_waitcnt vmcnt(15)\nM 32,172,56\nbuffer_load_dwordx4 a[116:119], v252, s[20:23], s65 offen offset:3072 sc1\ns_barrier\nM 48,168,44\n"
"R 166,186,36864\nR 64,238,40960\nR 66,238,45056\ns_waitcnt vmcnt(15)\nM 48,172,60\nG 6\n"
"M 52,176,44\nR 168,187,24576\nR 68,238,41984\nR 70,238,46080\nM 52,180,60\nbuffer_load_dwordx4 a[120:123], v252, s[20:23], s66 offen offset:3072 sc1\n"
"M 36,176,40\nR 170,187,28672\nM 36,180,56\nM 20,176,36\nR 172,187,32768\nM 20,180,52\n"
"buffer_load_dwordx4 a[124:127], v252, s[20:23], s67 offen offset:3072 sc1\nM 4,176,32\nR 174,187,36864\nG 6\nv_mfma_f32_16x16x32_bf16 v[72:75], v[242:245], v[64:67], 0\nM 4,180,48\n"
"G 3\nv_mfma_f32_16x16x32_bf16 v[72:75], v[242:245], v[68:71], v[72:75]\nM 8,160,32\nR 176,184,40960\nM 8,164,48\ns_add_u32 s64, s70, 2105344\n"
"buffer_load_dwordx4 a[128:131], v252, s[20:23], s64 offen offset:0 sc1\nM 24,160,36\nR 178,184,45056\nM 24,164,52\nv_lshrrev_b32 v64, 1, v252\nv_mov_b32 v73, s46\n"
"s_and_b32 s61, s40, 3\ns_lshl_b32 s61, s61, 7\ns_add_u32 s61, s61, s91\ns_add_u32 s61, s61, 524288\ns_mov_b64 exec, 0xffff\nbuffer_store_dwordx2 v[72:73], v64, s[20:23], s61 offen sc1\n"
"s_mov_b64 exec, -1\nM 40,160,40\nR 180,184,49152\nM 40,164,56\ns_add_u32 s65, s70, 2138112\nbuffer_load_dwordx4 a[132:135], v252, s[20:23], s65 offen offset:0 sc1\n"
"M 56,160,44\nR 182,184,53248\nM 56,164,60\ns_lshl_b32 s60, s40, 5\nv_xor_b32 v238, s60, v188\nG 4\n"
"M 60,168,44\nR 160,185,40960\nv_and_b32 v238, 0xfffffbff, v238\nM 60,172,60\ns_add_u32 s66, s71, 2105344\nbuffer_load_dwordx4 a[136:139], v252, s[20:23], s66 offen offset:0 sc1\n"
"M 44,168,40\nR 162,185,45056\nM 44,172,56\nM 28,168,36\nR 164,185,49152\nM 28,172,52\n"
"s_add_u32 s67, s71, 2138112\nbuffer_load_dwordx4 a[140:143], v252, s[20:23], s67 offen offset:0 sc1\nM 12,168,32\nR 166,185,53248\nM 12,172,48\ns_barrier\n"
"G 4\ns_waitcnt vmcnt(21)\nM 0,176,64\nR 168,186,40960\nR 64,238,49152\nR 66,238,53248\n"
"s_waitcnt vmcnt(17)\nM 0,180,80\nbuffer_load_dwordx4 a[144:147], v252, s[20:23], s64 offen offset:1024 sc1\nM 16,176,68\nR 170,186,45056\nR 68,238,50176\n"
"R 70,238,54272\ns_waitcnt vmcnt(17)\nM 16,180,84\nM 32,176,72\nR 172,186,49152\ns_waitcnt vmcnt(16)\n"
"M 32,180,88\nbuffer_load_dwordx4 a[148:151], v252, s[20:23], s65 offen offset:1024 sc1\nM 48,176,76\nR 174,186,53248\ns_add_u32 m0, s50, 98304\ns_waitcnt vmcnt(16)\n"
"M 48,180,92\nG 8\nM 52,160,76\nR 176,187,40960\nG 6\nv_mfma_f32_16x16x32_bf16 v[72:75], v[242:245], v[64:67], 0\n"
"s_add_u32 s63, s53, 720896\nM 52,164,92\nG 3\nv_mfma_f32_16x16x32_bf16 v[72:75], v[242:245], v[68:71], v[72:75]\nbuffer_load_dwordx4 a[152:155], v252, s[20:23], s66 offen offset:1024 sc1\nM 36,160,72\n"
"R 178,187,45056\nM 36,164,88\nbuffer_load_dwordx4 v220, s[8:11], s63 offen lds\nM 20,160,68\nR 180,187,49152\nM 20,164,84\n"
"buffer_load_dwordx4 a[156:159], v252, s[20:23], s67 offen offset:1024 sc1\nv_lshrrev_b32 v64, 1, v252\nM 4,160,64\nR 182,187,53248\nv_mov_b32 v73, s46\ns_and_b32 s61, s40, 3\n"
"s_lshl_b32 s61, s61, 7\ns_add_u32 s61, s61, s91\ns_add_u32 s61, s61, 655360\ns_mov_b64 exec, 0xffff\nbuffer_store_dwordx2 v[72:73], v64, s[20:23], s61 offen sc1\ns_mov_b64 exec, -1\n"
"M 4,164,80\nG 4\nM 8,168,64\nR 160,184,57344\nM 8,172,80\nbuffer_load_dwordx4 a[160:163], v252, s[20:23], s64 offen offset:2048 sc1\n"
"M 24,168,68\nR 162,184,61440\nbuffer_load_dwordx4 v221, s[8:11], s63 offen offset:1024 lds\nM 24,172,84\nM 40,168,72\nR 164,188,0\n"
"M 40,172,88\nbuffer_load_dwordx4 a[164:167], v252, s[20:23], s65 offen offset:2048 sc1\nM 56,168,76\nR 166,188,4096\nM 56,172,92\ns_and_b32 s60, s41, 1\n"
"s_lshl1_add_u32 s60, s60, s90\ns_lshl_b32 s60, s60, 5\nv_xor_b32 v238, s60, v184\nG 4\nM 60,176,76\nR 168,185,57344\n"
"v_and_b32 v238, 0xfffffbff, v238\nM 60,180,92\nbuffer_load_dwordx4 a[168:171], v252, s[20:23], s66 offen offset:2048 sc1\nM 44,176,72\nR 170,185,61440\nR 64,238,0\n"
"R 66,238,4096\nM 44,180,88\ns_add_u32 m0, s50, 106496\nM 28,176,68\nR 172,189,0\nR 68,238,1024\n"
"R 70,238,5120\nM 28,180,84\nbuffer_load_dwordx4 a[172:175], v252, s[20:23], s67 offen offset:2048 sc1\nM 12,176,64\nR 174,189,4096\ns_add_u32 s63, s53, 786432\n"
"M 12,180,80\ns_barrier\nG 8\ns_waitcnt vmcnt(24)\nM 0,160,96\nR 176,186,57344\n"
"s_waitcnt vmcnt(19)\nM 0,164,112\nbuffer_load_dwordx4 a[176:179], v252, s[20:23], s64 offen offset:3072 sc1\nbuffer_load_dwordx4 v220, s[8:11], s63 offen lds\nM 16,160,100\nR 178,186,61440\n"
"G 6\nv_mfma_f32_16x16x32_bf16 v[72:75], v[242:245], v[64:67], 0\ns_waitcnt vmcnt(20)\nM 16,164,116\nG 3\nv_mfma_f32_16x16x32_bf16 v[72:75], v[242:245], v[68:71], v[72:75]\n"
"buffer_load_dwordx4 v221, s[8:11], s63 offen offset:1024 lds\nM 32,160,104\nR 180,190,0\ns_add_u32 m0, s50, 114688\ns_waitcnt vmcnt(20)\nM 32,164,120\n"
"buffer_load_dwordx4 a[180:183], v252, s[20:23], s65 offen offset:3072 sc1\nM 48,160,108\nR 182,190,4096\ns_add_u32 s63, s53, 851968\ns_waitcnt vmcnt(20)\nM 48,164,124\n"
"buffer_load_dwordx4 v220, s[8:11], s63 offen lds\nG 4\nv_lshrrev_b32 v64, 1, v252\nM 52,168,108\nR 160,187,57344\nv_mov_b32 v73, s46\n"
"s_and_b32 s61, s40, 1\ns_lshl_b32 s61, s61, 7\ns_add_u32 s61, s61, s91\ns_add_u32 s61, s61, 786432\ns_mov_b64 exec, 0xffff\nbuffer_store_dwordx2 v[72:73], v64, s[20:23], s61 offen sc1\n"
"s_mov_b64 exec, -1\nM 52,172,124\nbuffer_load_dwordx4 a[184:187], v252, s[20:23], s66 offen offset:3072 sc1\nM 36,168,104\nR 162,187,61440\nM 36,172,120\n"
"buffer_load_dwordx4 v221, s[8:11], s63 offen offset:1024 lds\nM 20,168,100\nR 164,191,0\nM 20,172,116\nbuffer_load_dwordx4 a[188:191], v252, s[20:23], s67 offen offset:3072 sc1\nM 4,168,96\n"
"R 166,191,4096\nM 4,172,112\nG 4\nM 8,176,96\nR 168,188,8192\nM 8,180,112\n"
"s_add_u32 s64, s70, 2109440\nbuffer_load_dwordx4 a[192:195], v252, s[20:23], s64 offen offset:0 sc1\nM 24,176,100\nR 170,188,12288\nM 24,180,116\nM 40,176,104\n"
"R 172,188,16384\nM 40,180,120\ns_add_u32 s65, s70, 2142208\nbuffer_load_dwordx4 a[196:199], v252, s[20:23], s65 offen offset:0 sc1\nM 56,176,108\nR 174,188,20480\n"
"M 56,180,124\nG 4\nM 60,160,108\nR 176,189,8192\nM 60,164,124\ns_add_u32 s66, s71, 2109440\n"
"buffer_load_dwordx4 a[200:203], v252, s[20:23], s66 offen offset:0 sc1\nM 44,160,104\nR 178,189,12288\ns_add_u32 s63, s53, 917504\nM 44,164,120\ns_add_u32 m0, s50, 0\n"
"M 28,160,100\nR 180,189,16384\nM 28,164,116\ns_add_u32 s67, s71, 2142208\nbuffer_load_dwordx4 a[204:207], v252, s[20:23], s67 offen offset:0 sc1\nM 12,160,96\n"
"R 182,189,20480\nM 12,164,112\ns_waitcnt vmcnt(16)\ns_barrier\nbuffer_load_dwordx4 v220, s[8:11], s63 offen lds\nG 4\n"
"M 0,168,128\nR 160,190,8192\nbuffer_load_dwordx4 v221, s[8:11], s63 offen offset:1024 lds\nM 0,172,144\nbuffer_load_dwordx4 a[208:211], v252, s[20:23], s64 offen offset:1024 sc1\ns_add_u32 m0, s50, 8192\n"
"s_add_u32 s63, s53, 983040\nbuffer_load_dwordx4 v220, s[8:11], s63 offen lds\nM 16,168,132\nR 162,190,12288\nbuffer_load_dwordx4 v221, s[8:11], s63 offen offset:1024 lds\nM 16,172,148\n"
"M 32,168,136\nR 164,190,16384\nM 32,172,152\nbuffer_load_dwordx4 a[212:215], v252, s[20:23], s65 offen offset:1024 sc1\nM 48,168,140\nR 166,190,20480\n"
"M 48,172,156\nG 4\nM 52,176,140\nR 168,191,8192\nM 52,180,156\nbuffer_load_dwordx4 a[216:219], v252, s[20:23], s66 offen offset:1024 sc1\n"
"M 36,176,136\nR 170,191,12288\nM 36,180,152\nM 20,176,132\nR 172,191,16384\nM 20,180,148\n"
"buffer_load_dwordx4 a[220:223], v252, s[20:23], s67 offen offset:1024 sc1\nM 4,176,128\nR 174,191,20480\nM 4,180,144\nG 4\nM 8,160,128\n"
"R 176,188,24576\nM 8,164,144\nbuffer_load_dwordx4 a[224:227], v252, s[20:23], s64 offen offset:2048 sc1\nM 24,160,132\nR 178,188,28672\nM 24,164,148\n"
"M 40,160,136\nR 180,188,32768\nM 40,164,152\nbuffer_load_dwordx4 a[228:231], v252, s[20:23], s65 offen offset:2048 sc1\nM 56,160,140\nR 182,188,36864\n"
"M 56,164,156\nG 4\nM 60,168,140\nR 160,189,24576\nM 60,172,156\nbuffer_load_dwordx4 a[232:235], v252, s[20:23], s66 offen offset:2048 sc1\n"
"M 44,168,136\nR 162,189,28672\nM 44,172,152\nM 28,168,132\nR 164,189,32768\nM 28,172,148\n"
"buffer_load_dwordx4 a[236:239], v252, s[20:23], s67 offen offset:2048 sc1\nM 12,168,128\nR 166,189,36864\nM 12,172,144\nG 4\nM 0,176,160\n"
"R 168,190,24576\ns_waitcnt vmcnt(24)\nM 0,180,176\nbuffer_load_dwordx4 a[240:243], v252, s[20:23], s64 offen offset:3072 sc1\nM 16,176,164\nR 170,190,28672\n"
"s_waitcnt vmcnt(22)\nM 16,180,180\nM 32,176,168\nR 172,190,32768\ns_waitcnt vmcnt(19)\nM 32,180,184\n"
"buffer_load_dwordx4 a[244:247], v252, s[20:23], s65 offen offset:3072 sc1\nM 48,176,172\nR 174,190,36864\ns_waitcnt vmcnt(18)\nM 48,180,188\nG 4\n"
"M 52,160,172\nR 176,191,24576\nM 52,164,188\nbuffer_load_dwordx4 a[248:251], v252, s[20:23], s66 offen offset:3072 sc1\nM 36,160,168\nR 178,191,28672\n"
"s_waitcnt vmcnt(10)\ns_barrier\nM 36,164,184\nM 20,160,164\nR 180,191,32768\nM 20,164,180\n"
"buffer_load_dwordx4 a[252:255], v252, s[20:23], s67 offen offset:3072 sc1\nM 4,160,160\nR 182,191,36864\nM 4,164,176\nG 4\nM 8,168,160\n"
"R 160,188,40960\nM 8,172,176\ns_movk_i32 s63, 7168\nv_mad_u32_u24 v220, v228, s63, v229\nv_add_u32 v221, 3668992, v220\ns_add_u32 m0, s50, 16384\n"
"s_add_u32 s63, s54, 0\nbuffer_load_dwordx4 v220, s[12:15], s63 offen lds\nM 24,168,164\nR 162,188,45056\nM 24,172,180\nM 40,168,168\n"
"R 164,188,49152\nM 40,172,184\nbuffer_load_dwordx4 v221, s[12:15], s63 offen offset:1024 lds\nM 56,168,172\nR 166,188,53248\ns_add_u32 m0, s50, 24576\n"
"M 56,172,188\nG 4\nM 60,176,172\nR 168,189,40960\ns_add_u32 s63, s54, 229376\nM 60,180,188\n"
"buffer_load_dwordx4 v220, s[12:15], s63 offen lds\nM 44,176,168\nR 170,189,45056\nM 44,180,184\nM 28,176,164\nR 172,189,49152\n"
"M 28,180,180\nbuffer_load_dwordx4 v221, s[12:15], s63 offen offset:1024 lds\nM 12,176,160\nR 174,189,53248\ns_add_u32 m0, s50, 32768\nM 12,180,176\n"
"G 4\nM 32,160,200\nR 176,190,40960\ns_add_u32 s63, s54, 128\ns_waitcnt vmcnt(13)\nM 32,164,216\n"
"R 178,190,45056\nM 48,160,204\nR 180,190,49152\ns_waitcnt vmcnt(12)\nM 48,164,220\nR 182,190,53248\n"
"G 4\nM 52,168,204\nR 160,191,40960\nbuffer_load_dwordx4 v220, s[12:15], s63 offen lds\nM 52,172,220\nR 162,191,45056\n"
"M 36,168,200\nR 164,191,49152\nM 36,172,216\nR 166,191,53248\nG 4\nM 40,176,200\n"
"R 168,184,0\nbuffer_load_dwordx4 v221, s[12:15], s63 offen offset:1024 lds\nM 40,180,216\nR 170,184,4096\ns_add_u32 m0, s50, 40960\nM 56,176,204\n"
"R 172,184,8192\ns_add_u32 s63, s54, 229504\nM 56,180,220\nR 174,184,12288\nG 4\nM 60,160,204\n"
"R 176,185,0\nbuffer_load_dwordx4 v220, s[12:15], s63 offen lds\nM 60,164,220\nR 178,185,4096\nM 44,160,200\nR 180,185,8192\n"
"M 44,164,216\nR 182,185,12288\nG 4\ns_waitcnt vmcnt(12)\nM 32,168,232\nR 160,186,0\n"
"buffer_load_dwordx4 v221, s[12:15], s63 offen offset:1024 lds\ns_waitcnt vmcnt(9)\nM 32,172,248\nR 162,186,4096\nM 48,168,236\nR 164,186,8192\n"
"s_waitcnt vmcnt(8)\nM 48,172,252\nR 166,186,12288\nG 4\nM 52,176,236\nR 168,187,0\n"
"M 52,180,252\nR 170,187,4096\nM 36,176,232\nR 172,187,8192\nM 36,180,248\nR 174,187,12288\n"
"G 4\nM 40,160,232\nR 176,188,40960\nM 40,164,248\nR 178,188,45056\nM 56,160,236\n"
"R 180,188,49152\ns_add_u32 m0, s50, 49152\nM 56,164,252\nR 182,188,53248\nG 4\nM 60,168,236\n"
"R 160,189,40960\ns_add_u32 s63, s54, 458752\nM 60,172,252\nR 162,189,45056\nM 44,168,232\nR 164,189,49152\n"
"M 44,172,248\nR 166,189,53248\nG 4\nM 0,176,192\nR 168,190,40960\nbuffer_load_dwordx4 v220, s[12:15], s63 offen lds\n"
"M 0,180,208\nR 170,190,45056\ns_mov_b32 m0, s51\nM 16,176,196\nR 172,190,49152\nds_write_addtid_b32 v32 offset:0\n"
"M 16,180,212\nR 174,190,53248\nds_write_addtid_b32 v33 offset:256\nG 6\nM 20,160,196\nR 176,191,40960\n"
"ds_write_addtid_b32 v34 offset:512\nM 20,164,212\nR 178,191,45056\nds_write_addtid_b32 v35 offset:768\nM 4,160,192\nR 180,191,49152\n"
"ds_write_addtid_b32 v36 offset:1024\nM 4,164,208\nR 182,191,53248\nds_write_addtid_b32 v37 offset:1280\nG 9\nM 8,168,192\n"
"R 160,184,0\nds_write_addtid_b32 v38 offset:1536\nM 8,172,208\nR 162,184,4096\nds_write_addtid_b32 v39 offset:1792\nM 24,168,196\n"
"R 164,184,8192\nds_write_addtid_b32 v40 offset:2048\nM 24,172,212\nR 166,184,12288\nds_write_addtid_b32 v41 offset:2304\nG 9\n"
"M 28,176,196\nR 168,185,0\nds_write_addtid_b32 v42 offset:2560\nM 28,180,212\nR 170,185,4096\nds_write_addtid_b32 v43 offset:2816\n"
"M 12,176,192\nR 172,185,8192\nds_write_addtid_b32 v44 offset:3072\nds_write_addtid_b32 v45 offset:3328\nds_write_addtid_b32 v46 offset:3584\nds_write_addtid_b32 v47 offset:3840\n"
"ds_write_addtid_b32 v48 offset:4096\nds_write_addtid_b32 v49 offset:4352\nds_write_addtid_b32 v50 offset:4608\nds_write_addtid_b32 v51 offset:4864\nds_write_addtid_b32 v52 offset:5120\nds_write_addtid_b32 v53 offset:5376\n"
"ds_write_addtid_b32 v54 offset:5632\nds_write_addtid_b32 v55 offset:5888\nds_write_addtid_b32 v56 offset:6144\nds_write_addtid_b32 v57 offset:6400\nds_write_addtid_b32 v58 offset:6656\nds_write_addtid_b32 v59 offset:6912\n"
"ds_write_addtid_b32 v60 offset:7168\nds_write_addtid_b32 v61 offset:7424\nds_write_addtid_b32 v62 offset:7680\nds_write_addtid_b32 v63 offset:7936\nM 12,180,208\nR 174,185,12288\n"
"s_add_u32 m0, s50, 49152\nG 15\nM 0,160,224\nR 176,186,0\nbuffer_load_dwordx4 v221, s[12:15], s63 offen offset:1024 lds\nM 0,164,240\n"
"R 178,186,4096\nG 3\ns_barrier\ns_add_u32 s63, s54, 688128\nM 16,160,228\nR 180,186,8192\n"
"s_add_u32 m0, s50, 57344\nds_read2st64_b32 v[32:33], v225 offset0:0 offset1:1\nM 16,164,244\nR 182,186,12288\nds_read2st64_b32 v[34:35], v225 offset0:2 offset1:3\nG 6\n"
"M 20,168,228\nR 160,187,0\nbuffer_load_dwordx4 v220, s[12:15], s63 offen lds\nM 20,172,244\nR 162,187,4096\nds_read2st64_b32 v[36:37], v225 offset0:4 offset1:5\n"
"M 4,168,224\nR 164,187,8192\nds_read2st64_b32 v[38:39], v225 offset0:6 offset1:7\nM 4,172,240\nR 166,187,12288\nG 7\n"
"M 8,176,224\nbuffer_load_dwordx4 v221, s[12:15], s63 offen offset:1024 lds\nM 8,180,240\ns_add_u32 m0, s50, 65536\nds_read2st64_b32 v[40:41], v225 offset0:8 offset1:9\nds_read2st64_b32 v[42:43], v225 offset0:10 offset1:11\n"
"M 24,176,228\ns_add_u32 s63, s54, 458880\nds_read2st64_b32 v[44:45], v225 offset0:12 offset1:13\nds_read2st64_b32 v[46:47], v225 offset0:14 offset1:15\nM 24,180,244\nds_read2st64_b32 v[48:49], v225 offset0:16 offset1:17\n"
"ds_read2st64_b32 v[50:51], v225 offset0:18 offset1:19\nG 6\nM 28,160,228\nbuffer_load_dwordx4 v220, s[12:15], s63 offen lds\nM 28,164,244\nds_read2st64_b32 v[52:53], v225 offset0:20 offset1:21\n"
"ds_read2st64_b32 v[54:55], v225 offset0:22 offset1:23\nM 12,160,224\nds_read2st64_b32 v[56:57], v225 offset0:24 offset1:25\nds_read2st64_b32 v[58:59], v225 offset0:26 offset1:27\nM 12,164,240\nds_read2st64_b32 v[60:61], v225 offset0:28 offset1:29\n"
"ds_read2st64_b32 v[62:63], v225 offset0:30 offset1:31\nG 0\ns_barrier\ns_lshl_b32 s84, s75, 11\ns_add_u32 s84, s84, s49\ns_add_u32 s84, s84, 10485760\n"
"s_add_u32 s85, s84, 32768\nv_mov_b32 v238, s46\nv_mov_b32 v239, s47\nv_pk_add_f32 v[0:1], v[0:1], v[32:33]\nv_pk_add_f32 v[2:3], v[2:3], v[34:35]\nv_pk_add_f32 v[4:5], v[4:5], v[36:37]\n"
"v_pk_add_f32 v[6:7], v[6:7], v[38:39]\nv_cvt_pk_bf16_f32 v192, v0, v1\nv_cvt_pk_bf16_f32 v193, v2, v3\nv_cvt_pk_bf16_f32 v194, v4, v5\nv_cvt_pk_bf16_f32 v195, v6, v7\nbuffer_store_dwordx4 v[192:195], v252, s[20:23], s84 offen offset:0 sc1\n"
"v_pk_add_f32 v[8:9], v[8:9], v[40:41]\nv_pk_add_f32 v[10:11], v[10:11], v[42:43]\nv_pk_add_f32 v[12:13], v[12:13], v[44:45]\nv_pk_add_f32 v[14:15], v[14:15], v[46:47]\nv_cvt_pk_bf16_f32 v196, v8, v9\nv_cvt_pk_bf16_f32 v197, v10, v11\n"
"v_cvt_pk_bf16_f32 v198, v12, v13\nv_cvt_pk_bf16_f32 v199, v14, v15\nbuffer_store_dwordx4 v[196:199], v252, s[20:23], s84 offen offset:1024 sc1\nv_pk_add_f32 v[16:17], v[16:17], v[48:49]\nv_pk_add_f32 v[18:19], v[18:19], v[50:51]\nv_pk_add_f32 v[20:21], v[20:21], v[52:53]\n"
"v_pk_add_f32 v[22:23], v[22:23], v[54:55]\nv_cvt_pk_bf16_f32 v200, v16, v17\nv_cvt_pk_bf16_f32 v201, v18, v19\nv_cvt_pk_bf16_f32 v202, v20, v21\nv_cvt_pk_bf16_f32 v203, v22, v23\nbuffer_store_dwordx4 v[200:203], v252, s[20:23], s85 offen offset:0 sc1\n"
"v_pk_add_f32 v[24:25], v[24:25], v[56:57]\nv_pk_add_f32 v[26:27], v[26:27], v[58:59]\nv_pk_add_f32 v[28:29], v[28:29], v[60:61]\nv_pk_add_f32 v[30:31], v[30:31], v[62:63]\nv_cvt_pk_bf16_f32 v204, v24, v25\nv_cvt_pk_bf16_f32 v205, v26, v27\n"
"v_cvt_pk_bf16_f32 v206, v28, v29\nv_cvt_pk_bf16_f32 v207, v30, v31\nbuffer_store_dwordx4 v[204:207], v252, s[20:23], s85 offen offset:1024 sc1\ns_add_u32 s61, s79, 88219648\ns_lshl_b32 s62, s75, 3\ns_add_u32 s61, s61, s62\n"
"s_waitcnt vmcnt(0)\ns_mov_b64 exec, 1\nbuffer_atomic_swap_x2 v[238:239], off, s[20:23], s61 sc1\ns_mov_b64 exec, -1\nLqw0:\nv_bfe_u32 v250, v252, 7, 1\n"
"v_mul_u32_u24 v250, 2176, v250\nv_bfe_u32 v251, v252, 4, 3\nv_lshl_add_u32 v250, v251, 3, v250\ns_lshl_b32 s60, s90, 6\nv_add_u32 v250, s60, v250\ns_sleep 5\n"
"v_mov_b32 v248, 0\nv_mov_b32 v249, 0\ns_add_u32 s61, s80, 88219648\ns_mov_b64 exec, 0xffff\nbuffer_atomic_or_x2 v[248:249], v250, s[20:23], s61 offen sc0 sc1\ns_mov_b64 exec, -1\n"
"buffer_load_dwordx4 v221, s[12:15], s63 offen offset:1024 lds\ns_add_u32 m0, s50, 73728\ns_add_u32 s63, s54, 688256\nbuffer_load_dwordx4 v220, s[12:15], s63 offen lds\nbuffer_load_dwordx4 v221, s[12:15], s63 offen offset:1024 lds\ns_add_u32 m0, s50, 81920\n"
"s_add_u32 s63, s54, 917504\nbuffer_load_dwordx4 v220, s[12:15], s63 offen lds\nbuffer_load_dwordx4 v221, s[12:15], s63 offen offset:1024 lds\ns_add_u32 m0, s50, 90112\ns_add_u32 s63, s54, 1146880\nbuffer_load_dwordx4 v220, s[12:15], s63 offen lds\n"
"buffer_load_dwordx4 v221, s[12:15], s63 offen offset:1024 lds\ns_add_u32 m0, s50, 98304\ns_add_u32 s63, s54, 917632\nbuffer_load_dwordx4 v220, s[12:15], s63 offen lds\nbuffer_load_dwordx4 v221, s[12:15], s63 offen offset:1024 lds\ns_add_u32 m0, s50, 106496\n"
"s_add_u32 s63, s54, 1147008\nbuffer_load_dwordx4 v220, s[12:15], s63 offen lds\nbuffer_load_dwordx4 v221, s[12:15], s63 offen offset:1024 lds\ns_add_u32 m0, s50, 114688\ns_add_u32 s63, s54, 1376256\nbuffer_load_dwordx4 v220, s[12:15], s63 offen lds\n"
"v_mov_b32 v230, 0\nv_mov_b32 v231, 0\nv_mov_b32 v232, 0\nv_mov_b32 v233, 0\nv_dot2c_f32_bf16 v230, s76, v192\nv_dot2c_f32_bf16 v231, v192, v192\n"
"v_dot2c_f32_bf16 v232, s76, v200\nv_dot2c_f32_bf16 v233, v200, v200\nv_dot2c_f32_bf16 v230, s76, v193\nv_dot2c_f32_bf16 v231, v193, v193\nv_dot2c_f32_bf16 v232, s76, v201\nv_dot2c_f32_bf16 v233, v201, v201\n"
"v_dot2c_f32_bf16 v230, s76, v194\nv_dot2c_f32_bf16 v231, v194, v194\nv_dot2c_f32_bf16 v232, s76, v202\nv_dot2c_f32_bf16 v233, v202, v202\nv_dot2c_f32_bf16 v230, s76, v195\nv_dot2c_f32_bf16 v231, v195, v195\n"
"v_dot2c_f32_bf16 v232, s76, v203\nv_dot2c_f32_bf16 v233, v203, v203\nv_dot2c_f32_bf16 v230, s76, v196\nv_dot2c_f32_bf16 v231, v196, v196\nv_dot2c_f32_bf16 v232, s76, v204\nv_dot2c_f32_bf16 v233, v204, v204\n"
"v_dot2c_f32_bf16 v230, s76, v197\nv_dot2c_f32_bf16 v231, v197, v197\nv_dot2c_f32_bf16 v232, s76, v205\nv_dot2c_f32_bf16 v233, v205, v205\nv_dot2c_f32_bf16 v230, s76, v198\nv_dot2c_f32_bf16 v231, v198, v198\n"
"v_dot2c_f32_bf16 v232, s76, v206\nv_dot2c_f32_bf16 v233, v206, v206\nv_dot2c_f32_bf16 v230, s76, v199\nv_dot2c_f32_bf16 v231, v199, v199\nv_dot2c_f32_bf16 v232, s76, v207\nv_dot2c_f32_bf16 v233, v207, v207\n"
"v_lshrrev_b32 v234, 1, v252\nv_permlane32_swap_b32 v230, v231\ns_nop 0\nv_permlane32_swap_b32 v232, v233\nv_add_f32 v230, v230, v231\nv_add_f32 v232, v232, v233\n"
"v_mov_b32 v231, s46\ns_nop 0\nv_permlane16_swap_b32 v230, v232\nv_add_f32 v230, v230, v232\ns_lshr_b32 s61, s49, 3\ns_lshl2_add_u32 s61, s53, s61\n"
"s_add_u32 s61, s61, 82313216\nbuffer_store_dwordx2 v[230:231], v234, s[20:23], s61 offen sc1\ns_waitcnt vmcnt(13)\nv_cmp_eq_u64 s[86:87], s[46:47], v[248:249]\ns_nop 0\ns_and_b32 s62, s86, 0xffff\n"
"s_cmp_eq_u32 s62, 0xffff\ns_cbranch_scc0 Lq249\nLq250:\ns_add_u32 s64, s70, 10485760\nbuffer_load_dwordx4 a[0:3], v252, s[20:23], s64 offen offset:0 sc1\ns_add_u32 s65, s70, 10518528\n"
"buffer_load_dwordx4 a[4:7], v252, s[20:23], s65 offen offset:0 sc1\ns_add_u32 s66, s71, 10485760\nbuffer_load_dwordx4 a[8:11], v252, s[20:23], s66 offen offset:0 sc1\ns_add_u32 s67, s71, 10518528\nbuffer_load_dwordx4 a[12:15], v252, s[20:23], s67 offen offset:0 sc1\nbuffer_load_dwordx4 a[16:19], v252, s[20:23], s64 offen offset:1024 sc1\n"
"buffer_load_dwordx4 a[20:23], v252, s[20:23], s65 offen offset:1024 sc1\nbuffer_load_dwordx4 a[24:27], v252, s[20:23], s66 offen offset:1024 sc1\nbuffer_load_dwordx4 a[28:31], v252, s[20:23], s67 offen offset:1024 sc1\ns_barrier\nR 160,184,16384\nR 162,184,20480\n"
"R 164,184,24576\nR 166,184,28672\nR 168,185,16384\nR 170,185,20480\nR 172,185,24576\nR 174,185,28672\n"
"R 176,186,16384\nR 178,186,20480\nR 180,186,24576\nR 182,186,28672\nbuffer_load_dwordx4 a[32:35], v252, s[20:23], s64 offen offset:2048 sc1\nbuffer_load_dwordx4 a[36:39], v252, s[20:23], s65 offen offset:2048 sc1\n"
"buffer_load_dwordx4 a[40:43], v252, s[20:23], s66 offen offset:2048 sc1\nbuffer_load_dwordx4 a[44:47], v252, s[20:23], s67 offen offset:2048 sc1\nbuffer_load_dwordx4 a[48:51], v252, s[20:23], s64 offen offset:3072 sc1\nbuffer_load_dwordx4 a[52:55], v252, s[20:23], s65 offen offset:3072 sc1\nbuffer_load_dwordx4 a[56:59], v252, s[20:23], s66 offen offset:3072 sc1\nbuffer_load_dwordx4 a[60:63], v252, s[20:23], s67 offen offset:3072 sc1\n"
"s_waitcnt vmcnt(15) lgkmcnt(10)\nv_mfma_f32_16x16x32_bf16 v[32:35], v[160:163], a[0:3], 0\ns_add_u32 s64, s70, 10489856\nbuffer_load_dwordx4 a[64:67], v252, s[20:23], s64 offen offset:0 sc1\ns_waitcnt vmcnt(12) lgkmcnt(8)\nM 32,164,16\n"
"v_mfma_f32_16x16x32_bf16 v[48:51], v[160:163], a[4:7], 0\ns_waitcnt vmcnt(11)\nM 48,164,20\nv_mfma_f32_16x16x32_bf16 v[64:67], v[160:163], a[8:11], 0\nv_lshrrev_b32 v238, 7, v252\ns_waitcnt vmcnt(10)\n"
"M 64,164,24\ns_add_u32 s65, s70, 10522624\nbuffer_load_dwordx4 a[68:71], v252, s[20:23], s65 offen offset:0 sc1\nv_mfma_f32_16x16x32_bf16 v[80:83], v[160:163], a[12:15], 0\nv_lshlrev_b32 v238, 9, v238\ns_waitcnt vmcnt(10)\n"
"M 80,164,28\nG 4\nv_and_b32 v239, 0x70, v252\nv_mfma_f32_16x16x32_bf16 v[84:87], v[168:171], a[12:15], 0\nR 160,187,16384\nM 84,172,28\n"
"s_and_b32 s60, s40, 3\ns_lshl_b32 s60, s60, 7\nv_add3_u32 v238, v238, v239, s60\nv_mfma_f32_16x16x32_bf16 v[68:71], v[168:171], a[8:11], 0\nR 162,187,20480\ns_lshl_b32 s61, s42, 13\n"
"s_add_u32 s61, s61, 84279296\nbuffer_load_dwordx4 v[0:3], v238, s[20:23], s61 offen sc1\nM 68,172,24\ns_add_u32 s66, s71, 10489856\nbuffer_load_dwordx4 a[72:75], v252, s[20:23], s66 offen offset:0 sc1\nv_mfma_f32_16x16x32_bf16 v[52:55], v[168:171], a[4:7], 0\n"
"R 164,187,24576\ns_lshl_b32 s61, s42, 13\ns_add_u32 s61, s61, 84283392\nbuffer_load_dwordx4 v[4:7], v238, s[20:23], s61 offen sc1\nM 52,172,20\nv_bfe_u32 v239, v238, 2, 7\n"
"v_mfma_f32_16x16x32_bf16 v[36:39], v[168:171], a[0:3], 0\nR 166,187,28672\nM 36,172,16\nbuffer_load_dword v239, v239, s[32:35], s54 offen offset:0\nG 4\nv_mfma_f32_16x16x32_bf16 v[40:43], v[176:179], a[0:3], 0\n"
"R 168,184,32768\nM 40,180,16\ns_add_u32 s67, s71, 10522624\nbuffer_load_dwordx4 a[76:79], v252, s[20:23], s67 offen offset:0 sc1\nv_mfma_f32_16x16x32_bf16 v[56:59], v[176:179], a[4:7], 0\nR 170,184,36864\n"
"M 56,180,20\nv_mfma_f32_16x16x32_bf16 v[72:75], v[176:179], a[8:11], 0\nR 172,184,40960\nM 72,180,24\nv_mfma_f32_16x16x32_bf16 v[88:91], v[176:179], a[12:15], 0\nR 174,184,45056\n"
"M 88,180,28\nG 4\nv_mfma_f32_16x16x32_bf16 v[92:95], v[160:163], a[12:15], 0\nR 176,185,32768\nM 92,164,28\nbuffer_load_dwordx4 a[80:83], v252, s[20:23], s64 offen offset:1024 sc1\n"
"v_mfma_f32_16x16x32_bf16 v[76:79], v[160:163], a[8:11], 0\nR 178,185,36864\nM 76,164,24\nv_mfma_f32_16x16x32_bf16 v[60:63], v[160:163], a[4:7], 0\nR 180,185,40960\nM 60,164,20\n"
"buffer_load_dwordx4 a[84:87], v252, s[20:23], s65 offen offset:1024 sc1\nv_mfma_f32_16x16x32_bf16 v[44:47], v[160:163], a[0:3], 0\nR 182,185,45056\nM 44,164,16\nG 4\nv_mfma_f32_16x16x32_bf16 v[96:99], v[168:171], a[0:3], 0\n"
"R 160,186,32768\nM 96,172,16\nv_mfma_f32_16x16x32_bf16 v[112:115], v[168:171], a[4:7], 0\nR 162,186,36864\nM 112,172,20\nbuffer_load_dwordx4 a[88:91], v252, s[20:23], s66 offen offset:1024 sc1\n"
"v_mfma_f32_16x16x32_bf16 v[128:131], v[168:171], a[8:11], 0\nR 164,186,40960\nM 128,172,24\nv_mfma_f32_16x16x32_bf16 v[144:147], v[168:171], a[12:15], 0\nR 166,186,45056\nM 144,172,28\n"
"s_barrier\nG 4\nv_mfma_f32_16x16x32_bf16 v[148:151], v[176:179], a[12:15], 0\nR 168,187,32768\nM 148,180,28\nbuffer_load_dwordx4 a[92:95], v252, s[20:23], s67 offen offset:1024 sc1\n"
"v_mfma_f32_16x16x32_bf16 v[132:135], v[176:179], a[8:11], 0\nR 170,187,36864\nM 132,180,24\nv_mfma_f32_16x16x32_bf16 v[116:119], v[176:179], a[4:7], 0\nR 172,187,40960\nM 116,180,20\n"
"v_mfma_f32_16x16x32_bf16 v[100:103], v[176:179], a[0:3], 0\nR 174,187,45056\nM 100,180,16\nG 4\nv_mfma_f32_16x16x32_bf16 v[104:107], v[160:163], a[0:3], 0\nR 176,184,49152\n"
"v_lshrrev_b32 v242, 7, v252\nM 104,164,16\nbuffer_load_dwordx4 a[96:99], v252, s[20:23], s64 offen offset:2048 sc1\nv_mfma_f32_16x16x32_bf16 v[120:123], v[160:163], a[4:7], 0\nR 178,184,53248\nv_lshlrev_b32 v242, 9, v242\n"
"s_waitcnt vmcnt(7)\nv_min3_u32 v240, v1, v3, v5\nM 120,164,20\nbuffer_load_dwordx4 a[100:103], v252, s[20:23], s65 offen offset:2048 sc1\nv_and_b32 v243, 0x70, v252\nv_min3_u32 v240, v240, v7, v7\n"
"v_mfma_f32_16x16x32_bf16 v[136:139], v[160:163], a[8:11], 0\nR 180,184,57344\nv_max3_u32 v241, v1, v3, v5\nv_max3_u32 v241, v241, v7, v7\nM 136,164,24\ns_and_b32 s60, s40, 3\n"
"s_lshl_b32 s60, s60, 7\nv_add3_u32 v242, v242, v243, s60\nv_cmp_ne_u32 s[86:87], s46, v240\nv_mfma_f32_16x16x32_bf16 v[152:155], v[160:163], a[12:15], 0\nR 182,184,61440\ns_lshl_b32 s61, s42, 13\n"
"s_add_u32 s61, s61, 84410368\nbuffer_load_dwordx4 v[16:19], v242, s[20:23], s61 offen sc1\nv_cmp_ne_u32 s[88:89], s46, v241\nM 152,164,28\ns_lshl_b32 s61, s42, 13\ns_add_u32 s61, s61, 84414464\n"
"buffer_load_dwordx4 v[20:23], v242, s[20:23], s61 offen sc1\ns_or_b64 s[86:87], s[86:87], s[88:89]\ns_cbranch_scc1 Lq303\nLq304:\nG 4\nv_mfma_f32_16x16x32_bf16 v[156:159], v[168:171], a[12:15], 0\n"
"R 160,185,49152\nv_add_f32 v0, v0, v4\nM 156,172,28\nbuffer_load_dwordx4 a[104:107], v252, s[20:23], s66 offen offset:2048 sc1\nv_bfe_u32 v243, v242, 2, 7\nv_mfma_f32_16x16x32_bf16 v[140:143], v[168:171], a[8:11], 0\n"
"R 162,185,53248\nv_add_f32 v2, v2, v6\nM 140,172,24\nbuffer_load_dword v243, v243, s[32:35], s54 offen offset:128\nv_mfma_f32_16x16x32_bf16 v[124:127], v[168:171], a[4:7], 0\nR 164,185,57344\n"
"v_add_f32_dpp v0, v0, v0 row_ror:8 row_mask:0xf bank_mask:0xf\nM 124,172,20\nv_add_f32_dpp v2, v2, v2 row_ror:8 row_mask:0xf bank_mask:0xf\nv_mov_b32 v4, v0\nv_mfma_f32_16x16x32_bf16 v[108:111], v[168:171], a[0:3], 0\nR 166,185,61440\n"
"v_mov_b32 v5, v2\nM 108,172,16\nG 4\nv_permlane32_swap_b32 v4, v0\nv_permlane32_swap_b32 v5, v2\nM 32,176,32\n"
"R 168,186,49152\nv_add_f32 v0, v0, v4\nM 32,180,48\nbuffer_load_dwordx4 a[108:111], v252, s[20:23], s67 offen offset:2048 sc1\nM 48,176,36\nR 170,186,53248\n"
"v_add_f32 v2, v2, v5\nM 48,180,52\nv_mov_b32 v4, v0\nv_mov_b32 v5, v2\nM 64,176,40\nR 172,186,57344\n"
"v_permlane16_swap_b32 v4, v0\nM 64,180,56\nbuffer_load_dwordx4 a[112:115], v252, s[20:23], s64 offen offset:3072 sc1\nM 80,176,44\nR 174,186,61440\nv_permlane16_swap_b32 v5, v2\n"
"M 80,180,60\ns_barrier\nG 4\nM 84,160,44\nR 176,187,49152\nv_add_f32 v0, v0, v4\n"
"M 84,164,60\nv_add_f32 v2, v2, v5\nv_and_b32 v8, s77, v0\nM 68,160,40\nR 178,187,53248\nv_sub_f32 v9, v0, v8\n"
"M 68,164,56\nbuffer_load_dwordx4 a[116:119], v252, s[20:23], s65 offen offset:3072 sc1\nM 52,160,36\nR 180,187,57344\ns_waitcnt vmcnt(14)\nv_lshlrev_b32 v10, 16, v239\n"
"M 52,164,52\nv_cvt_pk_bf16_f32 v4, v8, v9\nv_cvt_pk_bf16_f32 v5, v8, v10\nM 36,160,32\nR 182,187,61440\nv_and_b32 v8, s77, v2\n"
"M 36,164,48\nG 4\nv_sub_f32 v9, v2, v8\nv_and_b32 v10, s77, v239\nM 40,168,32\nR 160,188,0\n"
"v_cvt_pk_bf16_f32 v6, v8, v9\nM 40,172,48\nbuffer_load_dwordx4 a[120:123], v252, s[20:23], s66 offen offset:3072 sc1\nM 56,168,36\nR 162,188,4096\nv_cvt_pk_bf16_f32 v7, v8, v10\n"
"M 56,172,52\nv_and_b32 v238, 0x1ff, v238\nv_add_u32 v238, 157184, v238\nM 72,168,40\nR 164,188,8192\nds_write_b128 v238, v[4:7]\n"
"M 72,172,56\nM 88,168,44\nR 166,188,12288\nM 88,172,60\nG 5\nM 92,176,44\n"
"R 168,189,0\nM 92,180,60\nbuffer_load_dwordx4 a[124:127], v252, s[20:23], s67 offen offset:3072 sc1\nM 76,176,40\nR 170,189,4096\nM 76,180,56\n"
"M 60,176,36\nR 172,189,8192\nv_lshrrev_b32 v238, 7, v252\nM 60,180,52\ns_add_u32 s64, s70, 10493952\nbuffer_load_dwordx4 a[128:131], v252, s[20:23], s64 offen offset:0 sc1\n"
"M 44,176,32\nR 174,189,12288\nv_lshlrev_b32 v238, 9, v238\ns_waitcnt vmcnt(8)\nv_min3_u32 v244, v17, v19, v21\nM 44,180,48\n"
"G 4\nv_and_b32 v239, 0x70, v252\nv_min3_u32 v244, v244, v23, v23\nv_max3_u32 v245, v17, v19, v21\nM 96,160,32\nR 176,190,0\n"
"s_and_b32 s60, s40, 3\ns_lshl_b32 s60, s60, 7\nv_add3_u32 v238, v238, v239, s60\nv_max3_u32 v245, v245, v23, v23\nM 96,164,48\ns_add_u32 s65, s70, 10526720\n"
"buffer_load_dwordx4 a[132:135], v252, s[20:23], s65 offen offset:0 sc1\nv_cmp_ne_u32 s[86:87], s46, v244\nM 112,160,36\nR 178,190,4096\ns_lshl_b32 s61, s42, 13\ns_add_u32 s61, s61, 84541440\n"
"buffer_load_dwordx4 v[0:3], v238, s[20:23], s61 offen sc1\nv_cmp_ne_u32 s[88:89], s46, v245\nM 112,164,52\ns_lshl_b32 s61, s42, 13\ns_add_u32 s61, s61, 84545536\nbuffer_load_dwordx4 v[4:7], v238, s[20:23], s61 offen sc1\n"
"s_or_b64 s[86:87], s[86:87], s[88:89]\ns_cbranch_scc1 Lq313\nLq314:\nM 128,160,40\nR 180,190,8192\nv_add_f32 v16, v16, v20\n"
"v_add_f32 v18, v18, v22\nM 128,164,56\nv_bfe_u32 v239, v238, 2, 7\nv_add_f32_dpp v16, v16, v16 row_ror:8 row_mask:0xf bank_mask:0xf\nv_add_f32_dpp v18, v18, v18 row_ror:8 row_mask:0xf bank_mask:0xf\nM 144,160,44\n"
"R 182,190,12288\nv_mov_b32 v20, v16\nv_mov_b32 v21, v18\nM 144,164,60\nbuffer_load_dword v239, v239, s[32:35], s54 offen offset:256\ns_barrier\n"
"G 4\nv_permlane32_swap_b32 v20, v16\nM 148,168,44\nR 160,191,0\nv_permlane32_swap_b32 v21, v18\nv_add_f32 v16, v16, v20\n"
"M 148,172,60\ns_add_u32 s66, s71, 10493952\nbuffer_load_dwordx4 a[136:139], v252, s[20:23], s66 offen offset:0 sc1\nM 132,168,40\nR 162,191,4096\nv_add_f32 v18, v18, v21\n"
"M 132,172,56\nv_mov_b32 v20, v16\nv_mov_b32 v21, v18\nM 116,168,36\nR 164,191,8192\nv_permlane16_swap_b32 v20, v16\n"
"M 116,172,52\ns_add_u32 s67, s71, 10526720\nbuffer_load_dwordx4 a[140:143], v252, s[20:23], s67 offen offset:0 sc1\nM 100,168,32\nR 166,191,12288\nv_permlane16_swap_b32 v21, v18\n"
"M 100,172,48\nG 4\nv_add_f32 v16, v16, v20\nv_add_f32 v18, v18, v21\nM 104,176,32\nR 168,188,16384\n"
"buffer_load_dwordx4 v221, s[12:15], s63 offen offset:1024 lds\nM 104,180,48\ns_add_u32 m0, s50, 0\ns_add_u32 s63, s54, 1605632\nbuffer_load_dwordx4 v220, s[12:15], s63 offen lds\nM 120,176,36\n"
"R 170,188,20480\nv_and_b32 v24, s77, v16\nM 120,180,52\nbuffer_load_dwordx4 a[144:147], v252, s[20:23], s64 offen offset:1024 sc1\nM 136,176,40\nR 172,188,24576\n"
"v_sub_f32 v25, v16, v24\nM 136,180,56\nbuffer_load_dwordx4 v221, s[12:15], s63 offen offset:1024 lds\nM 152,176,44\nR 174,188,28672\ns_waitcnt vmcnt(16)\n"
"v_lshlrev_b32 v26, 16, v243\nM 152,180,60\nG 4\nv_cvt_pk_bf16_f32 v20, v24, v25\nv_cvt_pk_bf16_f32 v21, v24, v26\nM 156,160,44\n"
"R 176,189,16384\nv_and_b32 v24, s77, v18\nM 156,164,60\nbuffer_load_dwordx4 a[148:151], v252, s[20:23], s65 offen offset:1024 sc1\nM 140,160,40\nR 178,189,20480\n"
"v_sub_f32 v25, v18, v24\nM 140,164,56\nv_and_b32 v26, s77, v243\nv_cvt_pk_bf16_f32 v22, v24, v25\nM 124,160,36\nR 180,189,24576\n"
"v_cvt_pk_bf16_f32 v23, v24, v26\nM 124,164,52\nv_and_b32 v242, 0x1ff, v242\nv_add_u32 v242, 157696, v242\nM 108,160,32\nR 182,189,28672\n"
"ds_write_b128 v242, v[20:23]\nM 108,164,48\nG 5\nM 32,168,64\nR 160,190,16384\nM 32,172,80\n"
"buffer_load_dwordx4 a[152:155], v252, s[20:23], s66 offen offset:1024 sc1\nM 48,168,68\nR 162,190,20480\nM 48,172,84\ns_add_u32 m0, s50, 8192\nM 64,168,72\n"
"R 164,190,24576\ns_add_u32 s63, s54, 1376384\nM 64,172,88\nbuffer_load_dwordx4 a[156:159], v252, s[20:23], s67 offen offset:1024 sc1\nM 80,168,76\nR 166,190,28672\n"
"M 80,172,92\nG 4\ns_barrier\nM 84,176,76\nR 168,191,16384\nv_lshrrev_b32 v242, 7, v252\n"
"M 84,180,92\nbuffer_load_dwordx4 v220, s[12:15], s63 offen lds\nv_lshlrev_b32 v242, 9, v242\nM 68,176,72\nR 170,191,20480\ns_waitcnt vmcnt(11)\n"
"v_min3_u32 v240, v1, v3, v5\nv_min3_u32 v240, v240, v7, v7\nM 68,180,88\nbuffer_load_dwordx4 a[160:163], v252, s[20:23], s64 offen offset:2048 sc1\nv_and_b32 v243, 0x70, v252\nv_max3_u32 v241, v1, v3, v5\n"
"M 52,176,68\nR 172,191,24576\nv_max3_u32 v241, v241, v7, v7\nv_cmp_ne_u32 s[86:87], s46, v240\nM 52,180,84\ns_and_b32 s60, s40, 1\n"
"s_lshl_b32 s60, s60, 7\nv_add3_u32 v242, v242, v243, s60\nv_cmp_ne_u32 s[88:89], s46, v241\nM 36,176,64\nR 174,191,28672\ns_lshl_b32 s61, s42, 13\n"
"s_add_u32 s61, s61, 84672512\nbuffer_load_dwordx4 v[16:19], v242, s[20:23], s61 offen sc1\ns_or_b64 s[86:87], s[86:87], s[88:89]\ns_cbranch_scc1 Lq323\nLq324:\nM 36,180,80\n"
"s_lshl_b32 s61, s42, 13\ns_add_u32 s61, s61, 84676608\nbuffer_load_dwordx4 v[20:23], v242, s[20:23], s61 offen sc1\nG 4\nv_add_f32 v0, v0, v4\nM 40,160,64\n"
"R 176,188,32768\nbuffer_load_dwordx4 v221, s[12:15], s63 offen offset:1024 lds\nv_add_f32 v2, v2, v6\nM 40,164,80\nbuffer_load_dwordx4 a[164:167], v252, s[20:23], s65 offen offset:2048 sc1\ns_add_u32 s63, s54, 1605760\n"
"v_bfe_u32 v243, v242, 2, 7\nv_add_f32_dpp v0, v0, v0 row_ror:8 row_mask:0xf bank_mask:0xf\nM 56,160,68\nR 178,188,36864\ns_add_u32 m0, s50, 16384\nv_add_f32_dpp v2, v2, v2 row_ror:8 row_mask:0xf bank_mask:0xf\n"
"M 56,164,84\nbuffer_load_dword v243, v243, s[32:35], s55 offen offset:0\nv_mov_b32 v4, v0\nM 72,160,72\nR 180,188,40960\nbuffer_load_dwordx4 v220, s[12:15], s63 offen lds\n"
"v_mov_b32 v5, v2\nM 72,164,88\nbuffer_load_dwordx4 a[168:171], v252, s[20:23], s66 offen offset:2048 sc1\nv_permlane32_swap_b32 v4, v0\nM 88,160,76\nR 182,188,45056\n"
"v_permlane32_swap_b32 v5, v2\nv_add_f32 v0, v0, v4\nM 88,164,92\nG 4\nv_add_f32 v2, v2, v5\nv_mov_b32 v4, v0\n"
"v_mov_b32 v5, v2\nM 92,168,76\nR 160,189,32768\nbuffer_load_dwordx4 v221, s[12:15], s63 offen offset:1024 lds\nv_permlane16_swap_b32 v4, v0\nM 92,172,92\n"
"v_permlane16_swap_b32 v5, v2\nv_add_f32 v0, v0, v4\nM 76,168,72\nR 162,189,36864\nv_add_f32 v2, v2, v5\nM 76,172,88\n"
"buffer_load_dwordx4 a[172:175], v252, s[20:23], s67 offen offset:2048 sc1\nM 60,168,68\nR 164,189,40960\nv_and_b32 v8, s77, v0\nM 60,172,84\nv_sub_f32 v9, v0, v8\n"
"s_waitcnt vmcnt(20)\nv_lshlrev_b32 v10, 16, v239\nM 44,168,64\nR 166,189,45056\nv_cvt_pk_bf16_f32 v4, v8, v9\nM 44,172,80\n"
"G 4\nv_cvt_pk_bf16_f32 v5, v8, v10\nv_and_b32 v8, s77, v2\nM 96,176,64\nR 168,190,32768\nv_sub_f32 v9, v2, v8\n"
"M 96,180,80\nbuffer_load_dwordx4 a[176:179], v252, s[20:23], s64 offen offset:3072 sc1\nM 112,176,68\nR 170,190,36864\nv_and_b32 v10, s77, v239\nM 112,180,84\n"
"v_cvt_pk_bf16_f32 v6, v8, v9\nv_cvt_pk_bf16_f32 v7, v8, v10\nM 128,176,72\nR 172,190,40960\nv_and_b32 v238, 0x1ff, v238\nM 128,180,88\n"
"s_add_u32 m0, s50, 24576\nv_add_u32 v238, 158208, v238\nds_write_b128 v238, v[4:7]\nM 144,176,76\nR 174,190,45056\ns_add_u32 s63, s54, 1835008\n"
"M 144,180,92\ns_waitcnt vmcnt(15) lgkmcnt(1)\ns_barrier\nM 148,160,76\nR 176,191,32768\nM 148,164,92\n"
"buffer_load_dwordx4 a[180:183], v252, s[20:23], s65 offen offset:3072 sc1\nM 132,160,72\nR 178,191,36864\nM 132,164,88\nbuffer_load_dwordx4 v220, s[12:15], s63 offen lds\nM 116,160,68\n"
"R 180,191,40960\nM 116,164,84\nbuffer_load_dwordx4 a[184:187], v252, s[20:23], s66 offen offset:3072 sc1\nM 100,160,64\nR 182,191,45056\nM 100,164,80\n"
"G 4\nM 104,168,64\nR 160,188,49152\nbuffer_load_dwordx4 v221, s[12:15], s63 offen offset:1024 lds\nM 104,172,80\ns_add_u32 m0, s50, 32768\n"
"s_add_u32 s63, s54, 2064384\nbuffer_load_dwordx4 v220, s[12:15], s63 offen lds\nM 120,168,68\nR 162,188,53248\nM 120,172,84\nbuffer_load_dwordx4 a[188:191], v252, s[20:23], s67 offen offset:3072 sc1\n"
"M 136,168,72\nR 164,184,0\nM 136,172,88\nbuffer_load_dwordx4 v221, s[12:15], s63 offen offset:1024 lds\nM 152,168,76\nR 166,184,4096\n"
"s_waitcnt vmcnt(15)\nv_min3_u32 v244, v17, v19, v21\nv_min3_u32 v244, v244, v23, v23\nM 152,172,92\nG 4\nv_max3_u32 v245, v17, v19, v21\n"
"v_max3_u32 v245, v245, v23, v23\nv_cmp_ne_u32 s[86:87], s46, v244\nM 156,176,76\nR 168,189,49152\nv_cmp_ne_u32 s[88:89], s46, v245\nM 156,180,92\n"
"s_add_u32 s64, s70, 10498048\nbuffer_load_dwordx4 a[192:195], v252, s[20:23], s64 offen offset:0 sc1\ns_or_b64 s[86:87], s[86:87], s[88:89]\ns_cbranch_scc1 Lq333\nLq334:\nM 140,176,72\n"
"R 170,189,53248\nv_add_f32 v16, v16, v20\nv_add_f32 v18, v18, v22\nM 140,180,88\nv_add_f32_dpp v16, v16, v16 row_ror:8 row_mask:0xf bank_mask:0xf\nv_add_f32_dpp v18, v18, v18 row_ror:8 row_mask:0xf bank_mask:0xf\n"
"v_mov_b32 v20, v16\nM 124,176,68\nR 172,185,0\nv_mov_b32 v21, v18\nv_permlane32_swap_b32 v20, v16\nM 124,180,84\n"
"s_add_u32 s65, s70, 10530816\nbuffer_load_dwordx4 a[196:199], v252, s[20:23], s65 offen offset:0 sc1\nv_permlane32_swap_b32 v21, v18\nM 108,176,64\nR 174,185,4096\nv_add_f32 v16, v16, v20\n"
"v_add_f32 v18, v18, v21\nM 108,180,80\nG 4\nv_mov_b32 v20, v16\nv_mov_b32 v21, v18\ns_nop 0\n"
"v_permlane16_swap_b32 v20, v16\nM 32,160,96\nR 176,190,49152\nv_permlane16_swap_b32 v21, v18\nM 32,164,112\nv_add_f32 v16, v16, v20\n"
"v_add_f32 v18, v18, v21\nM 48,160,100\nR 178,190,53248\nv_and_b32 v24, s77, v16\nM 48,164,116\ns_add_u32 s66, s71, 10498048\n"
"buffer_load_dwordx4 a[200:203], v252, s[20:23], s66 offen offset:0 sc1\nM 64,160,104\nR 180,186,0\nv_sub_f32 v25, v16, v24\nM 64,164,120\ns_add_u32 m0, s50, 40960\n"
"s_waitcnt vmcnt(15)\nv_lshlrev_b32 v26, 16, v243\nv_cvt_pk_bf16_f32 v20, v24, v25\nM 80,160,108\nR 182,186,4096\ns_add_u32 s63, s54, 2293760\n"
"M 80,164,124\ns_waitcnt vmcnt(12)\ns_barrier\nG 4\nM 84,168,108\nR 160,191,49152\n"
"v_cvt_pk_bf16_f32 v21, v24, v26\nM 84,172,124\ns_add_u32 s67, s71, 10530816\nbuffer_load_dwordx4 a[204:207], v252, s[20:23], s67 offen offset:0 sc1\nM 68,168,104\nR 162,191,53248\n"
"v_and_b32 v24, s77, v18\nM 68,172,120\nbuffer_load_dwordx4 v220, s[12:15], s63 offen lds\nM 52,168,100\nR 164,187,0\nv_sub_f32 v25, v18, v24\n"
"M 52,172,116\nbuffer_load_dwordx4 v221, s[12:15], s63 offen offset:1024 lds\nM 36,168,96\nR 166,187,4096\ns_add_u32 m0, s50, 49152\nM 36,172,112\n"
"G 4\nv_and_b32 v26, s77, v243\nv_cvt_pk_bf16_f32 v22, v24, v25\nM 40,176,96\nR 168,184,8192\ns_add_u32 s63, s54, 2523136\n"
"M 40,180,112\nbuffer_load_dwordx4 a[208:211], v252, s[20:23], s64 offen offset:1024 sc1\nM 56,176,100\nR 170,184,12288\nv_cvt_pk_bf16_f32 v23, v24, v26\nM 56,180,116\n"
"buffer_load_dwordx4 v220, s[12:15], s63 offen lds\nM 72,176,104\nR 172,184,16384\nv_and_b32 v242, 0x1ff, v242\nM 72,180,120\nbuffer_load_dwordx4 a[212:215], v252, s[20:23], s65 offen offset:1024 sc1\n"
"M 88,176,108\nR 174,184,20480\nv_add_u32 v242, 158720, v242\nM 88,180,124\nds_write_b128 v242, v[20:23]\nG 5\n"
"M 92,160,108\nR 176,185,8192\nbuffer_load_dwordx4 v221, s[12:15], s63 offen offset:1024 lds\nM 92,164,124\nM 76,160,104\nR 178,185,12288\n"
"M 76,164,120\nbuffer_load_dwordx4 a[216:219], v252, s[20:23], s66 offen offset:1024 sc1\nM 60,160,100\nR 180,185,16384\nLq566:\ns_lshr_b32 s60, s45, 3\n"
"s_lshl2_add_u32 s60, s60, s40\ns_lshl_b32 s60, s60, 8\nv_lshl_add_u32 v224, v252, 3, s60\nv_add_u32 v224, Lq566+4096-LqPC, v224\ns_mov_b64 exec, 3\nglobal_load_dword v224, v224, s[98:99]\n"
"s_mov_b64 exec, -1\nM 60,164,116\nM 44,160,96\nR 182,185,20480\nM 44,164,112\nG 5\n"
"M 96,168,96\nR 160,186,8192\nM 96,172,112\nbuffer_load_dwordx4 a[220:223], v252, s[20:23], s67 offen offset:1024 sc1\nM 112,168,100\nR 162,186,12288\n"
"M 112,172,116\nM 128,168,104\nR 164,186,16384\nM 128,172,120\nM 144,168,108\nR 166,186,20480\n"
"s_add_u32 m0, s50, 57344\nM 144,172,124\ns_waitcnt vmcnt(13) lgkmcnt(8)\ns_barrier\nG 4\nM 148,176,108\n"
"R 168,187,8192\ns_add_u32 s63, s54, 2752512\nM 148,180,124\nbuffer_load_dwordx4 a[224:227], v252, s[20:23], s64 offen offset:2048 sc1\nM 132,176,104\nR 170,187,12288\n"
"M 132,180,120\nbuffer_load_dwordx4 a[228:231], v252, s[20:23], s65 offen offset:2048 sc1\nM 116,176,100\nR 172,187,16384\nbuffer_load_dwordx4 v220, s[12:15], s63 offen lds\nM 116,180,116\n"
"buffer_load_dwordx4 v221, s[12:15], s63 offen offset:1024 lds\nM 100,176,96\nR 174,187,20480\ns_add_u32 m0, s50, 65536\nM 100,180,112\nG 4\n"
"M 104,160,96\nR 176,184,24576\ns_add_u32 s63, s54, 2981888\nM 104,164,112\nbuffer_load_dwordx4 a[232:235], v252, s[20:23], s66 offen offset:2048 sc1\nM 120,160,100\n"
"R 178,184,28672\nM 120,164,116\nbuffer_load_dwordx4 v220, s[12:15], s63 offen lds\nM 136,160,104\nR 180,184,32768\nM 136,164,120\n"
"buffer_load_dwordx4 v221, s[12:15], s63 offen offset:1024 lds\nM 152,160,108\nR 182,184,36864\nM 152,164,124\nG 4\nM 156,168,108\n"
"R 160,185,24576\nM 156,172,124\nbuffer_load_dwordx4 a[236:239], v252, s[20:23], s67 offen offset:2048 sc1\nM 140,168,104\nR 162,185,28672\nM 140,172,120\n"
"M 124,168,100\nR 164,185,32768\nM 124,172,116\nbuffer_load_dwordx4 a[240:243], v252, s[20:23], s64 offen offset:3072 sc1\nM 108,168,96\nR 166,185,36864\n"
"M 108,172,112\nG 4\nM 32,176,128\nR 168,186,24576\nM 32,180,144\nM 48,176,132\n"
"R 170,186,28672\nM 48,180,148\nbuffer_load_dwordx4 a[244:247], v252, s[20:23], s65 offen offset:3072 sc1\nM 64,176,136\nR 172,186,32768\nM 64,180,152\n"
"M 80,176,140\nR 174,186,36864\ns_add_u32 m0, s50, 73728\nM 80,180,156\ns_waitcnt vmcnt(13)\ns_barrier\n"
"G 4\nM 84,160,140\nR 176,187,24576\ns_add_u32 s63, s54, 3211264\nM 84,164,156\nbuffer_load_dwordx4 a[248:251], v252, s[20:23], s66 offen offset:3072 sc1\n"
"M 68,160,136\nR 178,187,28672\nM 68,164,152\ns_lshr_b32 s84, s49, 3\nM 52,160,132\nR 180,187,32768\n"
"buffer_load_dwordx4 v220, s[12:15], s63 offen lds\nM 52,164,148\ns_add_u32 s84, s84, 82313216\nM 36,160,128\nR 182,187,36864\nM 36,164,144\n"
"buffer_load_dwordx4 v[0:3], v252, s[20:23], s84 offen offset:0 sc1\nG 4\nM 40,168,128\nR 160,184,40960\nbuffer_load_dwordx4 v221, s[12:15], s63 offen offset:1024 lds\nM 40,172,144\n"
"buffer_load_dwordx4 a[252:255], v252, s[20:23], s67 offen offset:3072 sc1\ns_add_u32 s63, s54, 3440640\nM 56,168,132\nR 162,184,45056\ns_add_u32 m0, s50, 81920\nM 56,172,148\n"
"buffer_load_dwordx4 v[4:7], v252, s[20:23], s84 offen offset:1024 sc1\nM 72,168,136\nR 164,184,49152\ns_lshr_b32 s85, s49, 3\nbuffer_load_dwordx4 v220, s[12:15], s63 offen lds\nM 72,172,152\n"
"buffer_load_dwordx4 v[8:11], v252, s[20:23], s84 offen offset:2048 sc1\nM 88,168,140\nR 166,184,53248\ns_add_u32 s85, s85, 82317312\nM 88,172,156\nG 4\n"
"M 92,176,140\nR 168,185,40960\nbuffer_load_dwordx4 v221, s[12:15], s63 offen offset:1024 lds\nM 92,180,156\nbuffer_load_dwordx4 v[12:15], v252, s[20:23], s84 offen offset:3072 sc1\nM 76,176,136\n"
"R 170,185,45056\nM 76,180,152\nbuffer_load_dwordx4 v[16:19], v252, s[20:23], s85 offen offset:0 sc1\nM 60,176,132\nR 172,185,49152\nM 60,180,148\n"
"buffer_load_dwordx4 v[20:23], v252, s[20:23], s85 offen offset:1024 sc1\nM 44,176,128\nR 174,185,53248\nM 44,180,144\nG 4\nM 32,160,160\n"
"R 176,186,40960\nM 32,164,176\nbuffer_load_dwordx4 v[24:27], v252, s[20:23], s85 offen offset:2048 sc1\nM 48,160,164\nR 178,186,45056\nM 48,164,180\n"
"buffer_load_dwordx4 v[28:31], v252, s[20:23], s85 offen offset:3072 sc1\nM 64,160,168\nR 180,186,49152\ns_add_u32 s63, s54, 1835136\nM 64,164,184\ns_add_u32 m0, s50, 90112\n"
"M 80,160,172\nR 182,186,53248\nM 80,164,188\ns_waitcnt vmcnt(17)\ns_barrier\nG 4\n"
"M 84,168,172\nR 160,187,40960\nM 84,172,188\nbuffer_load_dwordx4 v220, s[12:15], s63 offen lds\nM 68,168,168\nR 162,187,45056\n"
"M 68,172,184\nM 52,168,164\nR 164,187,49152\nv_bfe_u32 v250, v252, 5, 1\nv_mul_u32_u24 v250, 2560, v250\nM 52,172,180\n"
"buffer_load_dwordx4 v221, s[12:15], s63 offen offset:1024 lds\nv_and_b32 v251, 16, v252\nM 36,168,160\nR 166,187,53248\ns_add_u32 m0, s50, 98304\nv_lshl_add_u32 v250, v251, 1, v250\n"
"M 36,172,176\ns_lshl_b32 s60, s90, 6\nv_add_u32 v250, s60, v250\nG 4\nM 40,176,160\nR 168,184,57344\n"
"s_add_u32 s63, s54, 2064512\nM 40,180,176\nbuffer_load_dwordx4 v220, s[12:15], s63 offen lds\nM 56,176,164\nR 170,184,61440\nM 56,180,180\n"
"M 72,176,168\nR 172,188,0\nM 72,180,184\nbuffer_load_dwordx4 v221, s[12:15], s63 offen offset:1024 lds\nM 88,176,172\nR 174,188,4096\n"
"M 88,180,188\nG 4\nM 92,160,172\nR 176,185,57344\nM 92,164,188\nM 76,160,168\n"
"R 178,185,61440\nM 76,164,184\ns_add_u32 m0, s50, 106496\nM 60,160,164\nR 180,189,0\nM 60,164,180\n"
"s_add_u32 s63, s54, 2293888\nM 44,160,160\nR 182,189,4096\nM 44,164,176\nG 4\nM 32,168,192\n"
"R 160,186,57344\nM 32,172,208\nbuffer_load_dwordx4 v220, s[12:15], s63 offen lds\nM 48,168,196\nR 162,186,61440\nM 48,172,212\n"
"M 64,168,200\nR 164,190,0\nM 64,172,216\nbuffer_load_dwordx4 v221, s[12:15], s63 offen offset:1024 lds\nM 80,168,204\nR 166,190,4096\n"
"s_add_u32 m0, s50, 114688\nM 80,172,220\ns_waitcnt vmcnt(11)\ns_barrier\nG 4\nM 84,176,204\n"
"R 168,187,57344\ns_add_u32 s63, s54, 2523264\nM 84,180,220\nbuffer_load_dwordx4 v220, s[12:15], s63 offen lds\nM 68,176,200\nR 170,187,61440\n"
"v_min3_u32 v230, v1, v3, v5\nv_min3_u32 v230, v230, v7, v9\nM 68,180,216\ns_waitcnt vmcnt(11)\nv_min3_u32 v230, v230, v11, v13\ns_waitcnt vmcnt(10)\n"
"v_min3_u32 v230, v230, v15, v17\ns_waitcnt vmcnt(9)\nv_min3_u32 v230, v230, v19, v21\nM 52,176,196\nR 172,191,0\ns_waitcnt vmcnt(8)\n"
"v_min3_u32 v230, v230, v23, v25\ns_waitcnt vmcnt(7)\nv_min3_u32 v230, v230, v27, v29\nM 52,180,212\nbuffer_load_dwordx4 v221, s[12:15], s63 offen offset:1024 lds\nv_min3_u32 v230, v230, v31, v31\n"
"M 36,176,192\nR 174,191,4096\ns_add_u32 s63, s54, 2752640\nv_max3_u32 v231, v1, v3, v5\nM 36,180,208\nG 4\n"
"v_max3_u32 v231, v231, v7, v9\nv_max3_u32 v231, v231, v11, v13\nv_max3_u32 v231, v231, v15, v17\nM 40,160,192\nR 176,188,8192\nv_max3_u32 v231, v231, v19, v21\n"
"v_max3_u32 v231, v231, v23, v25\nM 40,164,208\ns_add_u32 m0, s50, 0\nv_max3_u32 v231, v231, v27, v29\nv_max3_u32 v231, v231, v31, v31\nv_cmp_ne_u32 s[86:87], s46, v230\n"
"M 56,160,196\nR 178,188,12288\nv_cmp_ne_u32 s[88:89], s46, v231\nM 56,164,212\nbuffer_load_dwordx4 v220, s[12:15], s63 offen lds\ns_or_b64 s[86:87], s[86:87], s[88:89]\n"
"s_cbranch_scc1 Lq301\nLq302:\nM 72,160,200\nR 180,188,16384\nv_add_f32 v0, v0, v16\nv_add_f32 v2, v2, v18\n"
"M 72,164,216\nv_add_f32 v4, v4, v20\nv_add_f32 v6, v6, v22\nv_add_f32 v8, v8, v24\nM 88,160,204\nR 182,188,20480\n"
"v_add_f32 v10, v10, v26\nv_add_f32 v12, v12, v28\nM 88,164,220\nG 4\nv_add_f32 v14, v14, v30\nv_add_f32 v0, v0, v8\n"
"v_add_f32 v2, v2, v10\nM 92,168,204\nR 160,189,8192\nv_add_f32 v4, v4, v12\nv_add_f32 v6, v6, v14\nM 92,172,220\n"
"buffer_load_dwordx4 v221, s[12:15], s63 offen offset:1024 lds\nv_add_f32 v0, v0, v4\nM 76,168,200\nR 162,189,12288\ns_add_u32 s63, s54, 2982016\nv_add_f32 v2, v2, v6\n"
"M 76,172,216\ns_add_u32 m0, s50, 8192\nv_lshrrev_b32 v231, 1, v252\nv_lshrrev_b32 v230, 2, v252\ns_lshl_b32 s62, s40, 9\ns_add_u32 s62, s62, 159232\n"
"v_add_u32 v231, s62, v231\nM 60,168,196\nR 164,189,16384\nds_write2_b32 v231, v0, v2 offset1:1\nM 60,172,212\nbuffer_load_dwordx4 v220, s[12:15], s63 offen lds\n"
"s_lshl_b32 s62, s40, 9\ns_add_u32 s62, s62, 159232\nv_add_u32 v230, s62, v230\nM 44,168,192\nR 166,189,20480\nds_read2st64_b32 v[232:233], v230 offset1:1\n"
"M 44,172,208\nG 6\nM 32,176,224\nR 168,190,8192\nM 32,180,240\nbuffer_load_dwordx4 v221, s[12:15], s63 offen offset:1024 lds\n"
"M 48,176,228\nR 170,190,12288\ns_add_u32 s63, s54, 3211392\nM 48,180,244\ns_add_u32 m0, s50, 16384\nM 64,176,232\n"
"R 172,190,16384\nG 3\nv_add_f32 v232, v232, v233\nv_mov_b32 v233, v232\nM 64,180,248\nbuffer_load_dwordx4 v220, s[12:15], s63 offen lds\n"
"v_permlane32_swap_b32 v232, v233\nM 80,176,236\nR 174,190,20480\nv_mov_b32 v230, v232\nv_mov_b32 v231, v233\nM 80,180,252\n"
"s_waitcnt vmcnt(9)\ns_barrier\nv_permlane16_swap_b32 v230, v232\nM 84,160,236\nR 176,191,8192\nv_permlane16_swap_b32 v231, v233\n"
"v_mul_f32 v230, 0xba800000, v230\nM 84,164,252\nbuffer_load_dwordx4 v221, s[12:15], s63 offen offset:1024 lds\nv_mul_f32 v214, v230, v230\nM 68,160,232\nR 178,191,12288\n"
"v_mul_f32 v231, 0x3a800000, v231\nv_sub_f32 v231, v231, v214\nM 68,164,248\ns_add_u32 m0, s50, 24576\nv_max_f32 v231, 0, v231\nv_add_f32 v231, 0x3727c5ac, v231\n"
"v_rsq_f32 v234, v231\nM 52,160,228\nR 180,191,16384\nv_and_b32 v208, s77, v230\nv_mul_f32 v216, v231, v234\nM 52,164,244\n"
"s_add_u32 s63, s54, 3440768\nv_sub_f32 v230, v230, v208\nv_cvt_pk_bf16_f32 v209, v230, v216\nv_cvt_pk_bf16_f32 v208, v208, v208\nM 36,160,224\nR 182,191,20480\n"
"v_mul_f32 v214, 0xc0135761, v234\nv_mul_f32 v215, v234, v234\nM 36,164,240\ns_mov_b64 s[60:61], 0xffff\nv_cndmask_b32 v208, 0, v208, s[60:61]\nG 4\n"
"v_mul_f32 v215, v215, v214\nM 40,168,224\nR 160,188,24576\nv_mul_f32 v215, 0x3d372713, v215\nv_mul_f32 v232, 0xba800000, v232\nM 40,172,240\n"
"buffer_load_dwordx4 v220, s[12:15], s63 offen lds\nv_mul_f32 v217, v232, v232\nM 56,168,228\nR 162,188,28672\nv_mul_f32 v233, 0x3a800000, v233\nM 56,172,244\n"
"s_mov_b64 s[60:61], 0xffff\nv_cndmask_b32 v209, 0, v209, s[60:61]\nv_sub_f32 v233, v233, v217\nM 72,168,232\nR 164,188,32768\nv_max_f32 v233, 0, v233\n"
"M 72,172,248\nbuffer_load_dwordx4 v221, s[12:15], s63 offen offset:1024 lds\nM 88,168,236\nR 166,188,36864\ns_add_u32 m0, s50, 32768\nM 88,172,252\n"
"G 4\nv_add_f32 v233, 0x3727c5ac, v233\nv_and_b32 v212, s77, v232\nM 92,176,236\nR 168,189,24576\ns_add_u32 s63, s55, 0\n"
"M 92,180,252\nbuffer_load_dwordx4 v222, s[12:15], s63 offen lds\nM 76,176,232\nR 170,189,28672\nv_sub_f32 v232, v232, v212\nM 76,180,248\n"
"v_rsq_f32 v235, v233\nM 60,176,228\nR 172,189,32768\nv_mul_f32 v219, v233, v235\nM 60,180,244\nbuffer_load_dwordx4 v223, s[12:15], s63 offen offset:1024 lds\n"
"M 44,176,224\nR 174,189,36864\nv_cvt_pk_bf16_f32 v213, v232, v219\nM 44,180,240\ns_waitcnt vmcnt(10)\ns_barrier\n"
"G 4\nM 96,160,128\nR 176,190,24576\nv_cvt_pk_bf16_f32 v212, v212, v212\nM 96,164,144\ns_mov_b32 m0, s51\n"
"s_mov_b64 s[60:61], 0xffff\nv_cndmask_b32 v212, 0, v212, s[60:61]\nv_mul_f32 v217, 0xc0135761, v235\nM 112,160,132\nR 178,190,28672\ns_lshl2_add_u32 s60, s45, s40\n"
"v_lshlrev_b32 v224, 3, v252\ns_mul_i32 s60, s60, 7168\ns_bfm_b64 exec, 14, 0\nbuffer_load_dword v224, v224, s[16:19], s60 offen\ns_mov_b64 exec, -1\nM 112,164,148\n"
"s_mov_b64 s[60:61], 0xffff\nv_cndmask_b32 v213, 0, v213, s[60:61]\nv_mul_f32 v218, v235, v235\nM 128,160,136\nR 180,190,32768\nv_mul_f32 v218, v218, v217\n"
"M 128,164,152\nds_write_addtid_b32 v64 offset:0\nv_mul_f32 v218, 0x3d372713, v218\nM 144,160,140\nR 182,190,36864\nM 144,164,156\n"
"ds_write_addtid_b32 v65 offset:256\nG 6\nM 148,168,140\nR 160,191,24576\nM 148,172,156\nds_write_addtid_b32 v66 offset:512\n"
"M 132,168,136\nR 162,191,28672\nM 132,172,152\nds_write_addtid_b32 v67 offset:768\nM 116,168,132\nR 164,191,32768\n"
"M 116,172,148\nds_write_addtid_b32 v68 offset:1024\nM 100,168,128\nR 166,191,36864\nds_write_addtid_b32 v69 offset:1280\nM 100,172,144\n"
"ds_write_addtid_b32 v70 offset:1536\nds_write_addtid_b32 v71 offset:1792\nG 11\nM 104,176,128\nR 168,188,40960\nds_write_addtid_b32 v72 offset:2048\n"
"M 104,180,144\nds_write_addtid_b32 v73 offset:2304\nds_write_addtid_b32 v74 offset:2560\nM 120,176,132\nR 170,188,45056\nds_write_addtid_b32 v75 offset:2816\n"
"M 120,180,148\nds_write_addtid_b32 v76 offset:3072\nds_write_addtid_b32 v77 offset:3328\nM 136,176,136\nR 172,188,49152\nds_write_addtid_b32 v78 offset:3584\n"
"M 136,180,152\nds_write_addtid_b32 v79 offset:3840\nds_write_addtid_b32 v80 offset:4096\nM 152,176,140\nR 174,188,53248\nds_write_addtid_b32 v81 offset:4352\n"
"M 152,180,156\nds_write_addtid_b32 v82 offset:4608\nds_write_addtid_b32 v83 offset:4864\nG 15\nM 156,160,140\nR 176,189,40960\n"
"ds_write_addtid_b32 v84 offset:5120\nM 156,164,156\nds_write_addtid_b32 v85 offset:5376\nds_write_addtid_b32 v86 offset:5632\nM 140,160,136\nR 178,189,45056\n"
"ds_write_addtid_b32 v87 offset:5888\nM 140,164,152\nds_write_addtid_b32 v88 offset:6144\nds_write_addtid_b32 v89 offset:6400\nM 124,160,132\nR 180,189,49152\n"
"ds_write_addtid_b32 v90 offset:6656\nM 124,164,148\nds_write_addtid_b32 v91 offset:6912\nds_write_addtid_b32 v92 offset:7168\nM 108,160,128\nR 182,189,53248\n"
"ds_write_addtid_b32 v93 offset:7424\nM 108,164,144\nds_write_addtid_b32 v94 offset:7680\nds_write_addtid_b32 v95 offset:7936\nG 15\nM 96,168,160\n"
"R 160,190,40960\ns_add_u32 m0, s50, 40960\ns_add_u32 s63, s55, 458752\nM 96,172,176\nbuffer_load_dwordx4 v222, s[12:15], s63 offen lds\nM 112,168,164\n"
"R 162,190,45056\ns_waitcnt vmcnt(8) lgkmcnt(2)\ns_barrier\nM 112,172,180\nds_read2st64_b32 v[64:65], v225 offset0:0 offset1:1\nds_read2st64_b32 v[66:67], v225 offset0:2 offset1:3\n"
"M 128,168,168\nR 164,190,49152\nds_read2st64_b32 v[68:69], v225 offset0:4 offset1:5\nM 128,172,184\nds_read2st64_b32 v[70:71], v225 offset0:6 offset1:7\nds_read2st64_b32 v[72:73], v225 offset0:8 offset1:9\n"
"M 144,168,172\nR 166,190,53248\nds_read2st64_b32 v[74:75], v225 offset0:10 offset1:11\nM 144,172,188\nds_read2st64_b32 v[76:77], v225 offset0:12 offset1:13\nds_read2st64_b32 v[78:79], v225 offset0:14 offset1:15\n"
"M 148,176,172\nR 168,191,40960\nds_read2st64_b32 v[80:81], v225 offset0:16 offset1:17\nM 148,180,188\nbuffer_load_dwordx4 v223, s[12:15], s63 offen offset:1024 lds\nG 11\n"
"v_add_f32 v32, v32, v64\nM 132,176,168\nR 170,191,45056\nv_add_f32 v33, v33, v65\nG 11\nv_add_f32 v34, v34, v66\n"
"M 132,180,184\ns_add_u32 m0, s50, 49152\nds_read2st64_b32 v[82:83], v225 offset0:18 offset1:19\nv_add_f32 v35, v35, v67\nM 116,176,164\nR 172,191,49152\n"
"G 11\nv_add_f32 v36, v36, v68\nv_add_f32 v37, v37, v69\nM 116,180,180\ns_add_u32 s63, s55, 917504\nds_read2st64_b32 v[84:85], v225 offset0:20 offset1:21\n"
"G 11\nv_add_f32 v38, v38, v70\nM 100,176,160\nR 174,191,53248\nv_add_f32 v39, v39, v71\nG 11\n"
"v_add_f32 v40, v40, v72\nM 100,180,176\nds_read2st64_b32 v[86:87], v225 offset0:22 offset1:23\nG 11\nv_add_f32 v41, v41, v73\nM 104,160,160\n"
"R 176,184,0\nG 11\nv_add_f32 v42, v42, v74\nv_add_f32 v43, v43, v75\nM 104,164,176\nbuffer_load_dwordx4 v222, s[12:15], s63 offen lds\n"
"G 10\nv_add_f32 v44, v44, v76\nM 120,160,164\nR 178,184,4096\nv_add_f32 v45, v45, v77\nG 10\n"
"v_add_f32 v46, v46, v78\nM 120,164,180\nds_read2st64_b32 v[88:89], v225 offset0:24 offset1:25\nv_add_f32 v47, v47, v79\nG 9\nv_add_f32 v48, v48, v80\n"
"M 136,160,168\nR 180,184,8192\nv_add_f32 v49, v49, v81\nM 136,164,184\nds_read2st64_b32 v[90:91], v225 offset0:26 offset1:27\nG 9\n"
"v_add_f32 v50, v50, v82\nM 152,160,172\nR 182,184,12288\nv_add_f32 v51, v51, v83\nM 152,164,188\nds_read2st64_b32 v[92:93], v225 offset0:28 offset1:29\n"
"G 8\nv_add_f32 v52, v52, v84\nM 156,168,172\nR 160,185,0\nv_add_f32 v53, v53, v85\nM 156,172,188\n"
"buffer_load_dwordx4 v223, s[12:15], s63 offen offset:1024 lds\nM 140,168,168\nR 162,185,4096\ns_mov_b32 s60, 157184\nv_bfe_u32 v246, v252, 4, 4\nv_lshl_add_u32 v246, v246, 3, s60\n"
"M 140,172,184\nds_read_b64 v[238:239], v246 offset:0\nG 10\nv_add_f32 v54, v54, v86\nM 124,168,164\nR 164,185,8192\n"
"ds_read_b64 v[240:241], v246 offset:128\nM 124,172,180\nds_read_b64 v[242:243], v246 offset:256\nv_add_f32 v55, v55, v87\nM 108,168,160\nR 166,185,12288\n"
"ds_read_b64 v[244:245], v246 offset:384\nM 108,172,176\nds_read_b64 v[246:247], v246 offset:0\nG 10\nM 96,176,192\nR 168,186,0\n"
"v_add_f32 v56, v56, v88\nM 96,180,208\ns_add_u32 m0, s50, 57344\nds_read2st64_b32 v[94:95], v225 offset0:30 offset1:31\nv_add_f32 v57, v57, v89\nM 112,176,196\n"
"R 170,186,4096\ns_lshl2_add_u32 s60, s45, s40\ns_waitcnt vmcnt(4)\nv_lshlrev_b32 v224, 3, v252\ns_mul_i32 s60, s60, 7168\ns_bfm_b64 exec, 14, 14\n"
"buffer_load_dword v224, v224, s[16:19], s60 offen\ns_mov_b64 exec, -1\nG 1\ns_barrier\nM 112,180,212\ns_add_u32 s63, s55, 1376256\n"
"v_add_f32 v58, v58, v90\nv_add_f32 v59, v59, v91\nM 128,176,200\nR 172,186,8192\nv_add_f32 v60, v60, v92\nM 128,180,216\n"
"buffer_load_dwordx4 v222, s[12:15], s63 offen lds\nM 144,176,204\nR 174,186,12288\nv_add_f32 v61, v61, v93\nM 144,180,220\nv_add_f32 v62, v62, v94\n"
"v_add_f32 v63, v63, v95\nM 148,160,204\nR 176,187,0\nv_mfma_f32_16x16x32_bf16 v[32:35], v[238:241], v[208:211], v[32:35]\nM 148,164,220\nv_mfma_f32_16x16x32_bf16 v[36:39], v[240:243], v[208:211], v[36:39]\n"
"M 132,160,200\nR 178,187,4096\nv_mfma_f32_16x16x32_bf16 v[40:43], v[242:245], v[208:211], v[40:43]\nM 132,164,216\nv_mfma_f32_16x16x32_bf16 v[44:47], v[244:247], v[208:211], v[44:47]\nbuffer_load_dwordx4 v223, s[12:15], s63 offen offset:1024 lds\n"
"M 116,160,196\nR 180,187,8192\nv_mfma_f32_16x16x32_bf16 v[48:51], v[244:247], v[210:213], v[48:51]\ns_add_u32 s63, s55, 1835008\nM 116,164,212\nv_mfma_f32_16x16x32_bf16 v[52:55], v[238:241], v[210:213], v[52:55]\n"
"s_add_u32 m0, s50, 65536\nM 100,160,192\nR 182,187,12288\nv_mfma_f32_16x16x32_bf16 v[56:59], v[240:243], v[210:213], v[56:59]\nM 100,164,208\nv_mfma_f32_16x16x32_bf16 v[60:63], v[242:245], v[210:213], v[60:63]\n"
"G 4\nM 104,168,192\nR 160,184,16384\nM 104,172,208\nbuffer_load_dwordx4 v222, s[12:15], s63 offen lds\nM 120,168,196\n"
"R 162,184,20480\nM 120,172,212\nM 136,168,200\nR 164,184,24576\nM 136,172,216\nM 152,168,204\n"
"R 166,184,28672\nv_mul_f32 v230, v32, v32\nv_mul_f32 v231, v33, v33\nM 152,172,220\ns_add_u32 s84, s73, 23068672\ns_add_u32 s85, s73, 23183360\n"
"G 4\nv_mul_f32 v232, v34, v34\nM 156,176,204\nR 168,185,16384\nv_mul_f32 v233, v35, v35\nv_fma_f32 v230, v230, v215, v214\n"
"M 156,180,220\nbuffer_load_dwordx4 v223, s[12:15], s63 offen offset:1024 lds\nv_fma_f32 v231, v231, v215, v214\nM 140,176,200\nR 170,185,20480\nv_fma_f32 v232, v232, v215, v214\n"
"v_fma_f32 v233, v233, v215, v214\nM 140,180,216\nv_mul_f32 v230, v32, v230\nv_mul_f32 v231, v33, v231\nv_mul_f32 v232, v34, v232\nM 124,176,196\n"
"R 172,185,24576\nv_mul_f32 v233, v35, v233\nv_exp_f32 v230, v230\nM 124,180,212\nv_exp_f32 v231, v231\nv_exp_f32 v232, v232\n"
"M 108,176,192\nR 174,185,28672\ns_mov_b32 s60, 157696\nv_bfe_u32 v246, v252, 4, 4\nv_lshl_add_u32 v246, v246, 3, s60\nv_exp_f32 v233, v233\n"
"M 108,180,208\nds_read_b64 v[238:239], v246 offset:0\nG 5\nv_fma_f32 v230, v230, v216, v216\nM 96,160,224\nR 176,186,16384\n"
"ds_read_b64 v[240:241], v246 offset:128\nv_fma_f32 v231, v231, v216, v216\nM 96,164,240\nds_read_b64 v[242:243], v246 offset:256\ns_add_u32 m0, s50, 73728\nv_fma_f32 v232, v232, v216, v216\n"
"M 112,160,228\nR 178,186,20480\nds_read_b64 v[244:245], v246 offset:384\nv_fma_f32 v233, v233, v216, v216\nM 112,164,244\nds_read_b64 v[246:247], v246 offset:0\n"
"s_add_u32 s63, s55, 2293760\nv_rcp_f32 v230, v230\nM 128,160,232\nR 180,186,24576\nv_mul_f32 v230, v32, v230\nv_rcp_f32 v231, v231\n"
"M 128,164,248\nbuffer_load_dwordx4 v222, s[12:15], s63 offen lds\nv_rcp_f32 v232, v232\nM 144,160,236\nR 182,186,28672\nv_mul_f32 v231, v33, v231\n"
"v_rcp_f32 v233, v233\nM 144,164,252\nG 9\nv_mul_f32 v232, v34, v232\nv_mul_f32 v233, v35, v233\nv_cvt_pk_bf16_f32 v32, v230, v231\n"
"M 148,168,236\nR 160,187,16384\nv_cvt_pk_bf16_f32 v33, v232, v233\nv_mul_f32 v234, v36, v36\nM 148,172,252\nv_mul_f32 v235, v37, v37\n"
"v_mul_f32 v236, v38, v38\nv_mul_f32 v237, v39, v39\nM 132,168,232\nR 162,187,20480\nv_fma_f32 v234, v234, v215, v214\nv_fma_f32 v235, v235, v215, v214\n"
"M 132,172,248\nbuffer_load_dwordx4 v223, s[12:15], s63 offen offset:1024 lds\nv_fma_f32 v236, v236, v215, v214\nM 116,168,228\nR 164,187,24576\nG 9\n"
"v_mfma_f32_16x16x32_bf16 v[96:99], v[238:241], v[208:211], v[96:99]\ns_add_u32 s63, s55, 2752512\nv_fma_f32 v237, v237, v215, v214\nM 116,172,244\ns_add_u32 m0, s50, 81920\nv_mul_f32 v234, v36, v234\n"
"v_mul_f32 v235, v37, v235\nv_mul_f32 v236, v38, v236\nM 100,168,224\nR 166,187,28672\nG 9\nv_mfma_f32_16x16x32_bf16 v[100:103], v[240:243], v[208:211], v[100:103]\n"
"v_mul_f32 v237, v39, v237\nv_exp_f32 v234, v234\nM 100,172,240\nG 4\nv_exp_f32 v235, v235\nv_exp_f32 v236, v236\n"
"M 104,176,224\nR 168,184,32768\nv_mfma_f32_16x16x32_bf16 v[104:107], v[242:245], v[208:211], v[104:107]\nv_fma_f32 v234, v234, v216, v216\nv_exp_f32 v237, v237\nM 104,180,240\n"
"buffer_load_dwordx4 v222, s[12:15], s63 offen lds\nv_fma_f32 v235, v235, v216, v216\nM 120,176,228\nR 170,184,36864\nv_mfma_f32_16x16x32_bf16 v[108:111], v[244:247], v[208:211], v[108:111]\nv_fma_f32 v236, v236, v216, v216\n"
"v_fma_f32 v237, v237, v216, v216\nM 120,180,244\nbuffer_load_dwordx4 v223, s[12:15], s63 offen offset:1024 lds\nv_rcp_f32 v234, v234\nM 136,176,232\nR 172,186,32768\n"
"v_mfma_f32_16x16x32_bf16 v[112:115], v[244:247], v[210:213], v[112:115]\ns_add_u32 s63, s55, 3211264\nv_rcp_f32 v235, v235\nM 136,180,248\ns_add_u32 m0, s50, 90112\nv_rcp_f32 v236, v236\n"
"v_rcp_f32 v237, v237\nM 152,176,236\nR 174,186,36864\nv_mfma_f32_16x16x32_bf16 v[116:119], v[238:241], v[210:213], v[116:119]\nv_mul_f32 v234, v36, v234\nv_mul_f32 v235, v37, v235\n"
"M 152,180,252\nG 4\nv_mul_f32 v236, v38, v236\nv_mul_f32 v237, v39, v237\nv_cvt_pk_bf16_f32 v34, v234, v235\nM 156,160,236\n"
"R 176,185,32768\nv_mfma_f32_16x16x32_bf16 v[120:123], v[240:243], v[210:213], v[120:123]\nv_cvt_pk_bf16_f32 v35, v236, v237\nv_mul_f32 v230, v40, v40\nM 156,164,252\nbuffer_load_dwordx4 v222, s[12:15], s63 offen lds\n"
"v_mul_f32 v231, v41, v41\nM 140,160,232\nR 178,185,36864\nv_mfma_f32_16x16x32_bf16 v[124:127], v[242:245], v[210:213], v[124:127]\nv_mul_f32 v232, v42, v42\nv_mul_f32 v233, v43, v43\n"
"M 140,164,248\nbuffer_load_dwordx4 v223, s[12:15], s63 offen offset:1024 lds\nv_fma_f32 v230, v230, v215, v214\nM 124,160,228\nR 180,187,32768\ns_add_u32 m0, s50, 98304\n"
"s_add_u32 s63, s54, 256\nv_fma_f32 v231, v231, v215, v214\nM 124,164,244\nbuffer_load_dwordx4 v220, s[12:15], s63 offen lds\nv_fma_f32 v232, v232, v215, v214\nM 108,160,224\n"
"R 182,187,36864\nv_fma_f32 v233, v233, v215, v214\nv_mul_f32 v230, v40, v230\nM 108,164,240\ns_waitcnt vmcnt(9)\ns_barrier\n"
"G 4\nv_mul_f32 v231, v41, v231\nv_mfma_f32_16x16x32_bf16 v[0:3], v[168:171], a[0:3], 0\nR 160,184,40960\nv_mul_f32 v232, v42, v232\nv_mul_f32 v233, v43, v233\n"
"M 0,172,16\ns_mov_b32 m0, s51\nv_exp_f32 v230, v230\nv_exp_f32 v231, v231\nv_mfma_f32_16x16x32_bf16 v[16:19], v[168:171], a[4:7], 0\nR 162,184,45056\n"
"v_fma_f32 v230, v230, v216, v216\nv_exp_f32 v232, v232\nM 16,172,20\nbuffer_store_dwordx4 v[32:35], v252, s[20:23], s84 offen offset:0 sc1\nv_exp_f32 v233, v233\nv_mfma_f32_16x16x32_bf16 v[8:11], v[168:171], a[8:11], 0\n"
"R 164,186,40960\nv_fma_f32 v231, v231, v216, v216\nv_fma_f32 v232, v232, v216, v216\nM 8,172,24\nv_rcp_f32 v230, v230\nv_fma_f32 v233, v233, v216, v216\n"
"v_mfma_f32_16x16x32_bf16 v[24:27], v[168:171], a[12:15], 0\nR 166,186,45056\nv_mul_f32 v230, v40, v230\nv_rcp_f32 v231, v231\nM 24,172,28\nG 4\n"
"v_rcp_f32 v232, v232\nv_rcp_f32 v233, v233\nv_mfma_f32_16x16x32_bf16 v[28:31], v[176:179], a[12:15], 0\nR 168,185,40960\nv_mul_f32 v231, v41, v231\nv_mul_f32 v232, v42, v232\n"
"M 28,180,28\nv_mul_f32 v233, v43, v233\nv_cvt_pk_bf16_f32 v40, v230, v231\nv_cvt_pk_bf16_f32 v41, v232, v233\nv_mfma_f32_16x16x32_bf16 v[12:15], v[176:179], a[8:11], 0\nR 170,185,45056\n"
"v_mul_f32 v234, v44, v44\nv_mul_f32 v235, v45, v45\nM 12,180,24\nds_write_addtid_b32 v128 offset:0\nv_mul_f32 v236, v46, v46\nv_mul_f32 v237, v47, v47\n"
"v_fma_f32 v234, v234, v215, v214\nv_mfma_f32_16x16x32_bf16 v[20:23], v[176:179], a[4:7], 0\nR 172,187,40960\nds_write_addtid_b32 v129 offset:256\nv_fma_f32 v235, v235, v215, v214\nv_fma_f32 v236, v236, v215, v214\n"
"M 20,180,20\nds_write_addtid_b32 v130 offset:512\nv_fma_f32 v237, v237, v215, v214\nv_mul_f32 v234, v44, v234\nv_mul_f32 v235, v45, v235\nv_mfma_f32_16x16x32_bf16 v[4:7], v[176:179], a[0:3], 0\n"
"R 174,187,45056\nds_write_addtid_b32 v131 offset:768\nv_mul_f32 v236, v46, v236\nv_mul_f32 v237, v47, v237\nM 4,180,16\nds_write_addtid_b32 v132 offset:1024\n"
"G 9\nv_exp_f32 v234, v234\nv_exp_f32 v235, v235\nM 0,160,32\nR 176,184,49152\nds_write_addtid_b32 v133 offset:1280\n"
"v_fma_f32 v234, v234, v216, v216\nv_exp_f32 v236, v236\nM 0,164,48\nds_write_addtid_b32 v134 offset:1536\nv_exp_f32 v237, v237\nv_fma_f32 v235, v235, v216, v216\n"
"M 16,160,36\nR 178,184,53248\nds_write_addtid_b32 v135 offset:1792\nv_fma_f32 v236, v236, v216, v216\nv_fma_f32 v237, v237, v216, v216\nM 16,164,52\n"
"ds_write_addtid_b32 v136 offset:2048\nv_rcp_f32 v234, v234\nv_rcp_f32 v235, v235\nM 8,160,40\nR 180,186,49152\nds_write_addtid_b32 v137 offset:2304\n"
"v_mul_f32 v234, v44, v234\nv_rcp_f32 v236, v236\nM 8,164,56\nds_write_addtid_b32 v138 offset:2560\nv_rcp_f32 v237, v237\nv_mul_f32 v235, v45, v235\n"
"M 24,160,44\nR 182,186,53248\nds_write_addtid_b32 v139 offset:2816\nv_mul_f32 v236, v46, v236\nv_mul_f32 v237, v47, v237\nM 24,164,60\n"
"ds_write_addtid_b32 v140 offset:3072\nG 14\nv_cvt_pk_bf16_f32 v42, v234, v235\nv_cvt_pk_bf16_f32 v43, v236, v237\nv_mul_f32 v230, v48, v48\nM 28,168,44\n"
"R 160,185,49152\nds_write_addtid_b32 v141 offset:3328\nv_mul_f32 v231, v49, v49\nv_mul_f32 v232, v50, v50\nM 28,172,60\nbuffer_store_dwordx4 v[40:43], v252, s[20:23], s84 offen offset:1024 sc1\n"
"ds_write_addtid_b32 v142 offset:3584\nv_mul_f32 v233, v51, v51\nM 12,168,40\nR 162,185,53248\nds_write_addtid_b32 v143 offset:3840\nv_fma_f32 v230, v230, v218, v217\n"
"v_fma_f32 v231, v231, v218, v217\nM 12,172,56\nds_write_addtid_b32 v144 offset:4096\nv_fma_f32 v232, v232, v218, v217\nv_fma_f32 v233, v233, v218, v217\nv_mul_f32 v230, v48, v230\n"
"M 20,168,36\nR 164,187,49152\nds_write_addtid_b32 v145 offset:4352\nv_mul_f32 v231, v49, v231\nv_mul_f32 v232, v50, v232\nM 20,172,52\n"
"ds_write_addtid_b32 v146 offset:4608\nv_exp_f32 v230, v230\nv_mul_f32 v233, v51, v233\nM 4,168,32\nR 166,187,53248\nds_write_addtid_b32 v147 offset:4864\n"
"v_fma_f32 v230, v230, v219, v219\nv_exp_f32 v231, v231\nM 4,172,48\nds_write_addtid_b32 v148 offset:5120\nds_write_addtid_b32 v149 offset:5376\nds_write_addtid_b32 v150 offset:5632\n"
"ds_write_addtid_b32 v151 offset:5888\nds_write_addtid_b32 v152 offset:6144\nds_write_addtid_b32 v153 offset:6400\nds_write_addtid_b32 v154 offset:6656\nds_write_addtid_b32 v155 offset:6912\nds_write_addtid_b32 v156 offset:7168\n"
"ds_write_addtid_b32 v157 offset:7424\nds_write_addtid_b32 v158 offset:7680\nds_write_addtid_b32 v159 offset:7936\nG 15\nv_exp_f32 v232, v232\nv_exp_f32 v233, v233\n"
"M 0,176,64\nR 168,184,57344\ns_add_u32 m0, s50, 98304\nv_fma_f32 v231, v231, v219, v219\nM 0,180,80\nbuffer_load_dwordx4 v221, s[12:15], s63 offen offset:1024 lds\n"
"v_fma_f32 v232, v232, v219, v219\nM 16,176,68\nR 170,184,61440\ns_waitcnt vmcnt(6) lgkmcnt(2)\ns_barrier\ns_add_u32 m0, s50, 106496\n"
"s_add_u32 s63, s54, 229632\nv_fma_f32 v233, v233, v219, v219\nM 16,180,84\nbuffer_load_dwordx4 v220, s[12:15], s63 offen lds\nds_read2st64_b32 v[128:129], v225 offset0:0 offset1:1\nv_rcp_f32 v230, v230\n"
"M 8,176,72\nR 172,186,57344\nds_read2st64_b32 v[130:131], v225 offset0:2 offset1:3\nv_mul_f32 v230, v48, v230\nv_rcp_f32 v231, v231\nM 8,180,88\n"
"ds_read2st64_b32 v[132:133], v225 offset0:4 offset1:5\nv_rcp_f32 v232, v232\nv_rcp_f32 v233, v233\nM 24,176,76\nR 174,186,61440\nds_read2st64_b32 v[134:135], v225 offset0:6 offset1:7\n"
"v_mul_f32 v231, v49, v231\nv_mul_f32 v232, v50, v232\nM 24,180,92\nds_read2st64_b32 v[136:137], v225 offset0:8 offset1:9\nv_mul_f32 v233, v51, v233\nv_cvt_pk_bf16_f32 v48, v230, v231\n"
"v_cvt_pk_bf16_f32 v49, v232, v233\nM 28,160,76\nR 176,185,57344\nds_read2st64_b32 v[138:139], v225 offset0:10 offset1:11\nv_mul_f32 v234, v52, v52\nv_mul_f32 v235, v53, v53\n"
"M 28,164,92\nbuffer_load_dwordx4 v221, s[12:15], s63 offen offset:1024 lds\nG 8\nv_add_f32 v96, v96, v128\nv_mul_f32 v236, v54, v54\nM 12,160,72\n"
"R 178,185,61440\ns_add_u32 s63, s54, 459008\nv_add_f32 v97, v97, v129\nv_mul_f32 v237, v55, v55\nM 12,164,88\ns_add_u32 m0, s50, 114688\n"
"v_fma_f32 v234, v234, v218, v217\nv_fma_f32 v235, v235, v218, v217\nG 7\nv_add_f32 v98, v98, v130\nv_fma_f32 v236, v236, v218, v217\nM 20,160,68\n"
"R 180,187,57344\nv_fma_f32 v237, v237, v218, v217\nv_add_f32 v99, v99, v131\nv_mul_f32 v234, v52, v234\nM 20,164,84\nbuffer_load_dwordx4 v220, s[12:15], s63 offen lds\n"
"G 7\nv_add_f32 v100, v100, v132\nv_mul_f32 v235, v53, v235\nM 4,160,64\nR 182,187,61440\nv_mul_f32 v236, v54, v236\n"
"v_add_f32 v101, v101, v133\nv_mul_f32 v237, v55, v237\nM 4,164,80\nG 7\nv_exp_f32 v234, v234\nG 6\n"
"v_add_f32 v102, v102, v134\nv_exp_f32 v235, v235\nM 0,168,96\nR 160,188,0\nv_fma_f32 v234, v234, v219, v219\nv_add_f32 v103, v103, v135\n"
"v_exp_f32 v236, v236\nM 0,172,112\nbuffer_load_dwordx4 v221, s[12:15], s63 offen offset:1024 lds\nG 6\nv_add_f32 v104, v104, v136\nv_exp_f32 v237, v237\n"
"M 16,168,100\nR 162,188,4096\nv_fma_f32 v235, v235, v219, v219\nv_add_f32 v105, v105, v137\nv_fma_f32 v236, v236, v219, v219\nM 16,172,116\n"
"s_add_u32 m0, s50, 0\nv_rcp_f32 v234, v234\nG 5\nv_add_f32 v106, v106, v138\nv_fma_f32 v237, v237, v219, v219\nM 8,168,104\n"
"R 164,190,0\nv_mul_f32 v234, v52, v234\nv_add_f32 v107, v107, v139\nv_rcp_f32 v235, v235\nM 8,172,120\ns_add_u32 s63, s54, 688384\n"
"ds_read2st64_b32 v[140:141], v225 offset0:12 offset1:13\nv_rcp_f32 v236, v236\nv_rcp_f32 v237, v237\nM 24,168,108\nR 166,190,4096\nds_read2st64_b32 v[142:143], v225 offset0:14 offset1:15\n"
"v_mul_f32 v235, v53, v235\nv_mul_f32 v236, v54, v236\nM 24,172,124\nds_read2st64_b32 v[144:145], v225 offset0:16 offset1:17\nG 7\nv_mul_f32 v237, v55, v237\n"
"v_cvt_pk_bf16_f32 v50, v234, v235\nv_cvt_pk_bf16_f32 v51, v236, v237\nM 28,176,108\nR 168,189,0\nds_read2st64_b32 v[146:147], v225 offset0:18 offset1:19\nv_mul_f32 v230, v56, v56\n"
"v_mul_f32 v231, v57, v57\nM 28,180,124\nbuffer_load_dwordx4 v220, s[12:15], s63 offen lds\nds_read2st64_b32 v[148:149], v225 offset0:20 offset1:21\nv_mul_f32 v232, v58, v58\nM 12,176,104\n"
"R 170,189,4096\nds_read2st64_b32 v[150:151], v225 offset0:22 offset1:23\nv_mul_f32 v233, v59, v59\nv_fma_f32 v230, v230, v218, v217\nM 12,180,120\nds_read2st64_b32 v[152:153], v225 offset0:24 offset1:25\n"
"v_fma_f32 v231, v231, v218, v217\nv_fma_f32 v232, v232, v218, v217\nv_fma_f32 v233, v233, v218, v217\nM 20,176,100\nR 172,191,0\nv_mul_f32 v230, v56, v230\n"
"G 10\nv_add_f32 v108, v108, v140\nv_mul_f32 v231, v57, v231\nM 20,180,116\nbuffer_load_dwordx4 v221, s[12:15], s63 offen offset:1024 lds\nv_add_f32 v109, v109, v141\n"
"v_mul_f32 v232, v58, v232\nM 4,176,96\nR 174,191,4096\ns_add_u32 m0, s50, 8192\nG 9\nv_add_f32 v110, v110, v142\n"
"v_mul_f32 v233, v59, v233\nM 4,180,112\nv_exp_f32 v230, v230\nv_add_f32 v111, v111, v143\nv_exp_f32 v231, v231\nM 0,160,128\n"
"R 176,188,8192\ns_add_u32 s63, s54, 917760\nG 9\nv_add_f32 v112, v112, v144\nv_exp_f32 v232, v232\nM 0,164,144\n"
"buffer_load_dwordx4 v220, s[12:15], s63 offen lds\nv_add_f32 v113, v113, v145\nv_exp_f32 v233, v233\nM 16,160,132\nR 178,188,12288\nv_fma_f32 v230, v230, v219, v219\n"
"G 8\nv_add_f32 v114, v114, v146\nv_fma_f32 v231, v231, v219, v219\nM 16,164,148\nds_read2st64_b32 v[154:155], v225 offset0:26 offset1:27\nv_rcp_f32 v230, v230\n"
"v_fma_f32 v232, v232, v219, v219\nM 8,160,136\nR 180,190,8192\nLq568:\ns_lshr_b32 s60, s45, 3\ns_lshl2_add_u32 s60, s60, s40\n"
"s_lshl_b32 s60, s60, 8\nv_lshl_add_u32 v224, v252, 3, s60\nv_add_u32 v224, Lq568+4096-LqPC, v224\ns_mov_b64 exec, 3\nglobal_load_dword v224, v224, s[98:99]\ns_mov_b64 exec, -1\n"
"v_add_f32 v115, v115, v147\nv_fma_f32 v233, v233, v219, v219\nM 8,164,152\nds_read2st64_b32 v[156:157], v225 offset0:28 offset1:29\nv_rcp_f32 v231, v231\nv_rcp_f32 v232, v232\n"
"M 24,160,140\nR 182,190,12288\nv_mul_f32 v230, v56, v230\nG 11\nv_add_f32 v116, v116, v148\nv_rcp_f32 v233, v233\n"
"M 24,164,156\nds_read2st64_b32 v[158:159], v225 offset0:30 offset1:31\nG 7\nv_mul_f32 v231, v57, v231\nv_mul_f32 v232, v58, v232\nv_add_f32 v117, v117, v149\n"
"v_add_f32 v118, v118, v150\nv_add_f32 v119, v119, v151\nv_add_f32 v120, v120, v152\nv_add_f32 v121, v121, v153\nG 4\nv_add_f32 v122, v122, v154\n"
"v_add_f32 v123, v123, v155\nG 2\nv_add_f32 v124, v124, v156\nv_add_f32 v125, v125, v157\nG 0\nv_add_f32 v126, v126, v158\n"
"v_add_f32 v127, v127, v159\nv_mul_f32 v233, v59, v233\nM 28,168,140\nR 160,189,8192\nv_cvt_pk_bf16_f32 v56, v230, v231\nv_cvt_pk_bf16_f32 v57, v232, v233\n"
"M 28,172,156\nbuffer_load_dwordx4 v221, s[12:15], s63 offen offset:1024 lds\nv_mul_f32 v234, v60, v60\nM 12,168,136\nR 162,189,12288\ns_add_u32 m0, s50, 16384\n"
"s_add_u32 s63, s54, 1147136\nv_mul_f32 v235, v61, v61\nM 12,172,152\nbuffer_load_dwordx4 v220, s[12:15], s63 offen lds\nv_mul_f32 v236, v62, v62\nM 20,168,132\n"
"R 164,191,8192\nv_mul_f32 v237, v63, v63\nv_fma_f32 v234, v234, v218, v217\nM 20,172,148\nv_fma_f32 v235, v235, v218, v217\nv_fma_f32 v236, v236, v218, v217\n"
"v_fma_f32 v237, v237, v218, v217\nM 4,168,128\nR 166,191,12288\nv_mul_f32 v234, v60, v234\nv_mul_f32 v235, v61, v235\nM 4,172,144\n"
"v_exp_f32 v234, v234\nv_mul_f32 v236, v62, v236\nM 0,176,160\nR 168,188,16384\nv_mul_f32 v237, v63, v237\nv_exp_f32 v235, v235\n"
"M 0,180,176\nbuffer_load_dwordx4 v221, s[12:15], s63 offen offset:1024 lds\nv_exp_f32 v236, v236\nM 16,176,164\nR 170,188,20480\ns_add_u32 m0, s50, 24576\n"
"s_add_u32 s63, s54, 1376512\nv_exp_f32 v237, v237\nM 16,180,180\nbuffer_load_dwordx4 v220, s[12:15], s63 offen lds\nv_fma_f32 v234, v234, v219, v219\nM 8,176,168\n"
"R 172,190,16384\nv_fma_f32 v235, v235, v219, v219\nv_fma_f32 v236, v236, v219, v219\nM 8,180,184\nbuffer_store_dwordx4 v[48:51], v252, s[20:23], s85 offen offset:0 sc1\nv_fma_f32 v237, v237, v219, v219\n"
"M 24,176,172\nR 174,190,20480\nv_rcp_f32 v234, v234\nM 24,180,188\nv_rcp_f32 v235, v235\nv_rcp_f32 v236, v236\n"
"v_rcp_f32 v237, v237\nv_mul_f32 v234, v60, v234\nv_mul_f32 v235, v61, v235\nv_mul_f32 v236, v62, v236\nv_mul_f32 v237, v63, v237\nv_cvt_pk_bf16_f32 v58, v234, v235\n"
"v_cvt_pk_bf16_f32 v59, v236, v237\nbuffer_store_dwordx4 v[56:59], v252, s[20:23], s85 offen offset:1024 sc1\nG 4\nM 28,160,172\nR 176,189,16384\ns_waitcnt vmcnt(12)\n"
"s_barrier\nv_mul_f32 v230, v96, v96\nM 28,164,188\nbuffer_load_dwordx4 v221, s[12:15], s63 offen offset:1024 lds\nv_mul_f32 v231, v97, v97\nM 12,160,168\n"
"R 178,189,20480\ns_add_u32 m0, s50, 32768\ns_add_u32 s63, s54, 1605888\nv_mul_f32 v232, v98, v98\nM 12,164,184\nbuffer_load_dwordx4 v220, s[12:15], s63 offen lds\n"
"v_mul_f32 v233, v99, v99\nM 20,160,164\nR 180,191,16384\nv_fma_f32 v230, v230, v215, v214\nv_fma_f32 v231, v231, v215, v214\nM 20,164,180\n"
"s_add_u32 s84, s73, 23070720\ns_add_u32 s85, s73, 23185408\nv_fma_f32 v232, v232, v215, v214\nv_fma_f32 v233, v233, v215, v214\nM 4,160,160\nR 182,191,20480\n"
"s_mov_b32 s60, 158720\nv_bfe_u32 v242, v252, 4, 4\nv_lshl_add_u32 v242, v242, 3, s60\nv_mul_f32 v230, v96, v230\nM 4,164,176\nds_read_b64 v[238:239], v242 offset:0\n"
"G 5\nv_mul_f32 v231, v97, v231\nM 0,168,192\nR 160,188,24576\nds_read_b64 v[240:241], v242 offset:128\nv_mul_f32 v232, v98, v232\n"
"M 0,172,208\nds_read_b64 v[242:243], v242 offset:0\nv_mul_f32 v233, v99, v233\nv_exp_f32 v230, v230\nM 16,168,196\nR 162,188,28672\n"
"v_fma_f32 v230, v230, v216, v216\nv_exp_f32 v231, v231\nM 16,172,212\nv_exp_f32 v232, v232\nv_exp_f32 v233, v233\nM 8,168,200\n"
"R 164,190,24576\nv_fma_f32 v231, v231, v216, v216\nv_fma_f32 v232, v232, v216, v216\nM 8,172,216\nv_rcp_f32 v230, v230\nv_fma_f32 v233, v233, v216, v216\n"
"M 24,168,204\nR 166,190,28672\nv_mul_f32 v230, v96, v230\nv_rcp_f32 v231, v231\nM 24,172,220\nG 7\n"
"v_rcp_f32 v232, v232\nv_rcp_f32 v233, v233\nM 28,176,204\nR 168,189,24576\nv_mul_f32 v231, v97, v231\nv_mul_f32 v232, v98, v232\n"
"M 28,180,220\nv_mul_f32 v233, v99, v233\nv_cvt_pk_bf16_f32 v96, v230, v231\nv_cvt_pk_bf16_f32 v97, v232, v233\nM 12,176,200\nR 170,189,28672\n"
"v_mul_f32 v234, v100, v100\nv_mul_f32 v235, v101, v101\nM 12,180,216\nv_mul_f32 v236, v102, v102\nv_mul_f32 v237, v103, v103\nv_fma_f32 v234, v234, v215, v214\n"
"M 20,176,196\nR 172,191,24576\nG 7\nv_mfma_f32_16x16x32_bf16 v[0:3], v[238:241], v[208:211], v[0:3]\nv_fma_f32 v235, v235, v215, v214\nv_fma_f32 v236, v236, v215, v214\n"
"M 20,180,212\nv_fma_f32 v237, v237, v215, v214\nv_mul_f32 v234, v100, v234\nv_mul_f32 v235, v101, v235\nM 4,176,192\nR 174,191,28672\n"
"G 7\nv_mfma_f32_16x16x32_bf16 v[4:7], v[240:243], v[208:211], v[4:7]\nv_mul_f32 v236, v102, v236\nv_mul_f32 v237, v103, v237\nM 4,180,208\nG 4\n"
"v_exp_f32 v234, v234\nv_exp_f32 v235, v235\nM 0,160,224\nR 176,188,32768\nv_mfma_f32_16x16x32_bf16 v[16:19], v[240:243], v[210:213], v[16:19]\nv_fma_f32 v234, v234, v216, v216\n"
"v_exp_f32 v236, v236\nM 0,164,240\nv_exp_f32 v237, v237\nv_fma_f32 v235, v235, v216, v216\nM 16,160,228\nR 178,188,36864\n"
"v_mfma_f32_16x16x32_bf16 v[20:23], v[238:241], v[210:213], v[20:23]\nv_fma_f32 v236, v236, v216, v216\nv_fma_f32 v237, v237, v216, v216\nM 16,164,244\nv_rcp_f32 v234, v234\nv_rcp_f32 v235, v235\n"
"M 8,160,232\nR 180,188,40960\nv_mul_f32 v234, v100, v234\nv_rcp_f32 v236, v236\nM 8,164,248\nv_rcp_f32 v237, v237\n"
"v_mul_f32 v235, v101, v235\nM 24,160,236\nR 182,188,45056\nv_mul_f32 v236, v102, v236\nv_mul_f32 v237, v103, v237\nM 24,164,252\n"
"G 4\nv_cvt_pk_bf16_f32 v98, v234, v235\nv_cvt_pk_bf16_f32 v99, v236, v237\nv_mul_f32 v230, v104, v104\nM 28,168,236\nR 160,189,32768\n"
"v_mul_f32 v231, v105, v105\nv_mul_f32 v232, v106, v106\nM 28,172,252\nv_mul_f32 v233, v107, v107\nv_fma_f32 v230, v230, v215, v214\nv_fma_f32 v231, v231, v215, v214\n"
"M 12,168,232\nR 162,189,36864\nv_fma_f32 v232, v232, v215, v214\nv_fma_f32 v233, v233, v215, v214\nM 12,172,248\nv_mul_f32 v230, v104, v230\n"
"v_mul_f32 v231, v105, v231\nv_mul_f32 v232, v106, v232\nM 20,168,228\nR 164,189,40960\nv_mul_f32 v233, v107, v233\nv_exp_f32 v230, v230\n"
"M 20,172,244\nv_exp_f32 v231, v231\nv_exp_f32 v232, v232\nM 4,168,224\nR 166,189,45056\nv_fma_f32 v230, v230, v216, v216\n"
"v_exp_f32 v233, v233\nM 4,172,240\ns_waitcnt vmcnt(12)\ns_barrier\nG 4\nv_fma_f32 v231, v231, v216, v216\n"
"v_mfma_f32_16x16x32_bf16 v[32:35], v[176:179], a[0:3], 0\nR 168,190,32768\nv_fma_f32 v232, v232, v216, v216\nv_fma_f32 v233, v233, v216, v216\nM 32,180,16\ns_mov_b32 m0, s51\n"
"v_rcp_f32 v230, v230\nv_rcp_f32 v231, v231\nv_mfma_f32_16x16x32_bf16 v[48:51], v[176:179], a[4:7], 0\nR 170,190,36864\nv_mul_f32 v230, v104, v230\nv_rcp_f32 v232, v232\n"
"M 48,180,20\nv_rcp_f32 v233, v233\nv_mul_f32 v231, v105, v231\nv_mfma_f32_16x16x32_bf16 v[64:67], v[176:179], a[8:11], 0\nR 172,190,40960\nv_mov_b32 v226, s46\n"
"v_mul_f32 v232, v106, v232\nM 64,180,24\nds_write_addtid_b32 v8 offset:0\nv_mov_b32 v227, s47\nv_mul_f32 v233, v107, v233\nv_cvt_pk_bf16_f32 v104, v230, v231\n"
"v_mfma_f32_16x16x32_bf16 v[80:83], v[176:179], a[12:15], 0\nR 174,190,45056\ns_add_u32 s61, s81, 385024\ns_lshl_b32 s62, s44, 5\ns_add_u32 s61, s61, s62\ns_waitcnt vmcnt(0)\n"
"s_mov_b64 exec, 1\nbuffer_atomic_swap_x2 v[226:227], off, s[20:23], s61 sc1\ns_mov_b64 exec, -1\nv_cvt_pk_bf16_f32 v105, v232, v233\nM 80,180,28\nds_write_addtid_b32 v9 offset:256\n"
"G 6\nv_mul_f32 v234, v108, v108\nv_mul_f32 v235, v109, v109\nv_mul_f32 v236, v110, v110\nv_mfma_f32_16x16x32_bf16 v[84:87], v[160:163], a[12:15], 0\nR 176,191,32768\n"
"ds_write_addtid_b32 v10 offset:512\nv_mul_f32 v237, v111, v111\nv_fma_f32 v234, v234, v215, v214\nM 84,164,28\nds_write_addtid_b32 v11 offset:768\nv_fma_f32 v235, v235, v215, v214\n"
"v_fma_f32 v236, v236, v215, v214\nv_fma_f32 v237, v237, v215, v214\nv_mfma_f32_16x16x32_bf16 v[68:71], v[160:163], a[8:11], 0\nR 178,191,36864\nds_write_addtid_b32 v12 offset:1024\nv_mul_f32 v234, v108, v234\n"
"v_mul_f32 v235, v109, v235\nM 68,164,24\nbuffer_store_dwordx4 v[96:99], v252, s[20:23], s84 offen offset:0 sc1\nds_write_addtid_b32 v13 offset:1280\nv_mul_f32 v236, v110, v236\nv_mfma_f32_16x16x32_bf16 v[52:55], v[160:163], a[4:7], 0\n"
"R 180,191,40960\nds_write_addtid_b32 v14 offset:1536\nv_mul_f32 v237, v111, v237\nv_exp_f32 v234, v234\nM 52,164,20\nds_write_addtid_b32 v15 offset:1792\n"
"v_exp_f32 v235, v235\nv_exp_f32 v236, v236\nv_mfma_f32_16x16x32_bf16 v[36:39], v[160:163], a[0:3], 0\nR 182,191,45056\nds_write_addtid_b32 v24 offset:4096\nv_fma_f32 v234, v234, v216, v216\n"
"v_exp_f32 v237, v237\nM 36,164,16\nds_write_addtid_b32 v25 offset:4352\nG 13\nv_rcp_f32 v234, v234\nv_fma_f32 v235, v235, v216, v216\n"
"v_mfma_f32_16x16x32_bf16 v[40:43], v[168:171], a[0:3], 0\nR 160,188,49152\nds_write_addtid_b32 v26 offset:4608\nds_write_addtid_b32 v27 offset:4864\nds_write_addtid_b32 v28 offset:5120\nds_write_addtid_b32 v29 offset:5376\n"
"ds_write_addtid_b32 v30 offset:5632\nds_write_addtid_b32 v31 offset:5888\nv_fma_f32 v236, v236, v216, v216\nv_fma_f32 v237, v237, v216, v216\nM 40,172,16\ns_add_u32 m0, s50, 32768\n"
"s_nop 0\nbuffer_load_dwordx4 v221, s[12:15], s63 offen offset:1024 lds\nv_rcp_f32 v235, v235\nv_mfma_f32_16x16x32_bf16 v[56:59], v[168:171], a[4:7], 0\nR 162,188,53248\ns_add_u32 m0, s50, 40960\n"
"v_rcp_f32 v236, v236\nM 56,172,20\nG 1\ns_barrier\ns_add_u32 s63, s54, 1835264\nv_rcp_f32 v237, v237\n"
"v_mfma_f32_16x16x32_bf16 v[72:75], v[168:171], a[8:11], 0\nR 164,184,0\nds_read2st64_b32 v[8:9], v225 offset0:0 offset1:1\nv_mov_b32 v248, 0\nv_mul_f32 v234, v108, v234\nM 72,172,24\n"
"buffer_load_dwordx4 v220, s[12:15], s63 offen lds\nds_read2st64_b32 v[10:11], v225 offset0:2 offset1:3\nv_mov_b32 v249, 0\nv_mul_f32 v235, v109, v235\nv_mfma_f32_16x16x32_bf16 v[88:91], v[168:171], a[12:15], 0\nR 166,184,4096\n"
"s_add_u32 s61, s82, 385024\ns_mov_b64 exec, 0xf\nbuffer_atomic_or_x2 v[248:249], v250, s[20:23], s61 offen sc0 sc1\ns_mov_b64 exec, -1\nv_mul_f32 v236, v110, v236\nM 88,172,28\n"
"ds_read2st64_b32 v[12:13], v225 offset0:4 offset1:5\nv_mul_f32 v237, v111, v237\nv_cvt_pk_bf16_f32 v106, v234, v235\nv_cvt_pk_bf16_f32 v107, v236, v237\nv_mfma_f32_16x16x32_bf16 v[92:95], v[176:179], a[12:15], 0\nR 168,189,49152\n"
"ds_read2st64_b32 v[14:15], v225 offset0:6 offset1:7\nv_mul_f32 v230, v112, v112\nv_mul_f32 v231, v113, v113\nM 92,180,28\nbuffer_load_dwordx4 v221, s[12:15], s63 offen offset:1024 lds\nds_read2st64_b32 v[24:25], v225 offset0:16 offset1:17\n"
"v_mul_f32 v232, v114, v114\nv_mfma_f32_16x16x32_bf16 v[76:79], v[176:179], a[8:11], 0\nR 170,189,53248\ns_add_u32 m0, s50, 49152\ns_add_u32 s63, s54, 2064640\nv_mul_f32 v233, v115, v115\n"
"M 76,180,24\nbuffer_load_dwordx4 v220, s[12:15], s63 offen lds\nv_fma_f32 v230, v230, v218, v217\nv_mfma_f32_16x16x32_bf16 v[60:63], v[176:179], a[4:7], 0\nR 172,185,0\nv_fma_f32 v231, v231, v218, v217\n"
"G 8\nv_add_f32 v0, v0, v8\nv_fma_f32 v232, v232, v218, v217\nM 60,180,20\nds_read2st64_b32 v[26:27], v225 offset0:18 offset1:19\nv_fma_f32 v233, v233, v218, v217\n"
"v_mul_f32 v230, v112, v230\nv_mul_f32 v231, v113, v231\nv_mfma_f32_16x16x32_bf16 v[44:47], v[176:179], a[0:3], 0\nR 174,185,4096\nv_mul_f32 v232, v114, v232\nv_add_f32 v1, v1, v9\n"
"v_mul_f32 v233, v115, v233\nM 44,180,16\nG 8\nv_exp_f32 v230, v230\nv_add_f32 v2, v2, v10\nv_exp_f32 v231, v231\n"
"M 32,160,32\nR 176,190,49152\nv_fma_f32 v230, v230, v219, v219\nv_add_f32 v3, v3, v11\nv_exp_f32 v232, v232\nM 32,164,48\n"
"buffer_load_dwordx4 v221, s[12:15], s63 offen offset:1024 lds\nG 8\nv_add_f32 v4, v4, v12\nv_exp_f32 v233, v233\nM 48,160,36\nR 178,190,53248\n"
"v_fma_f32 v231, v231, v219, v219\nv_add_f32 v5, v5, v13\nv_fma_f32 v232, v232, v219, v219\nM 48,164,52\nds_read2st64_b32 v[28:29], v225 offset0:20 offset1:21\nv_rcp_f32 v230, v230\n"
"v_fma_f32 v233, v233, v219, v219\nM 64,160,40\nR 180,186,0\nv_mul_f32 v230, v112, v230\nG 9\nv_add_f32 v6, v6, v14\n"
"v_rcp_f32 v231, v231\nM 64,164,56\ns_add_u32 m0, s50, 57344\nv_rcp_f32 v232, v232\nv_add_f32 v7, v7, v15\nv_rcp_f32 v233, v233\n"
"M 80,160,44\nR 182,186,4096\nv_mul_f32 v231, v113, v231\nG 9\nv_add_f32 v16, v16, v24\nv_mul_f32 v232, v114, v232\n"
"M 80,164,60\nG 5\nv_mul_f32 v233, v115, v233\nv_cvt_pk_bf16_f32 v112, v230, v231\nv_add_f32 v17, v17, v25\nv_cvt_pk_bf16_f32 v113, v232, v233\n"
"M 84,168,44\nR 160,191,49152\nv_mul_f32 v234, v116, v116\nv_add_f32 v18, v18, v26\nv_mul_f32 v235, v117, v117\nM 84,172,60\n"
"s_add_u32 s63, s54, 2294016\nds_read2st64_b32 v[30:31], v225 offset0:22 offset1:23\nv_mul_f32 v236, v118, v118\nv_add_f32 v19, v19, v27\nG 4\nv_add_f32 v20, v20, v28\n"
"v_add_f32 v21, v21, v29\nG 0\nv_add_f32 v22, v22, v30\nv_add_f32 v23, v23, v31\nM 68,168,40\nR 162,191,53248\n"
"v_mul_f32 v237, v119, v119\nM 68,172,56\nbuffer_load_dwordx4 v220, s[12:15], s63 offen lds\nM 52,168,36\nR 164,187,0\nv_fma_f32 v234, v234, v218, v217\n"
"M 52,172,52\nv_fma_f32 v235, v235, v218, v217\nv_fma_f32 v236, v236, v218, v217\nM 36,168,32\nR 166,187,4096\nv_fma_f32 v237, v237, v218, v217\n"
"M 36,172,48\nv_mul_f32 v234, v116, v234\nv_mul_f32 v235, v117, v235\nM 40,176,32\nR 168,184,8192\nv_mul_f32 v236, v118, v236\n"
"M 40,180,48\nbuffer_load_dwordx4 v221, s[12:15], s63 offen offset:1024 lds\nM 56,176,36\nR 170,184,12288\nv_mul_f32 v237, v119, v237\nM 56,180,52\n"
"s_add_u32 m0, s50, 65536\nv_exp_f32 v234, v234\nM 72,176,40\nR 172,184,16384\ns_add_u32 s63, s54, 2523392\nM 72,180,56\n"
"s_add_u32 s64, s68, 23068672\nv_exp_f32 v235, v235\nM 88,176,44\nR 174,184,20480\nv_fma_f32 v234, v234, v219, v219\nM 88,180,60\n"
"G 4\nv_exp_f32 v236, v236\nM 92,160,44\nR 176,185,8192\ns_waitcnt vmcnt(5)\nv_cmp_eq_u64 s[86:87], s[46:47], v[248:249]\n"
"M 92,164,60\nbuffer_load_dwordx4 v220, s[12:15], s63 offen lds\nM 76,160,40\nR 178,185,12288\ns_and_b32 s62, s86, 0xf\ns_cmp_eq_u32 s62, 0xf\n"
"s_cbranch_scc0 Lq367\nLq368:\nM 76,164,56\ns_add_u32 s65, s68, 23183360\nv_exp_f32 v237, v237\nM 60,160,36\n"
"R 180,185,16384\nv_fma_f32 v235, v235, v219, v219\nM 60,164,52\nbuffer_load_dwordx4 a[0:3], v252, s[20:23], s64 offen sc1\nM 44,160,32\nR 182,185,20480\n"
"v_fma_f32 v236, v236, v219, v219\nM 44,164,48\nG 4\nv_rcp_f32 v234, v234\nM 32,168,64\nR 160,186,8192\n"
"v_fma_f32 v237, v237, v219, v219\nM 32,172,80\ns_add_u32 s66, s69, 23068672\nv_rcp_f32 v235, v235\nM 48,168,68\nR 162,186,12288\n"
"v_mul_f32 v234, v116, v234\nM 48,172,84\nbuffer_load_dwordx4 a[4:7], v252, s[20:23], s65 offen sc1\nM 64,168,72\nR 164,186,16384\nv_mul_f32 v235, v117, v235\n"
"M 64,172,88\nv_rcp_f32 v236, v236\nM 80,168,76\nR 166,186,20480\nv_mul_f32 v236, v118, v236\nM 80,172,92\n"
"G 4\nv_rcp_f32 v237, v237\nM 84,176,76\nR 168,187,8192\nv_mul_f32 v237, v119, v237\nM 84,180,92\n"
"buffer_load_dwordx4 a[8:11], v252, s[20:23], s66 offen sc1\nM 68,176,72\nR 170,187,12288\nv_cvt_pk_bf16_f32 v114, v234, v235\nM 68,180,88\ns_add_u32 s67, s69, 23183360\n"
"v_cvt_pk_bf16_f32 v115, v236, v237\nv_mul_f32 v230, v120, v120\nM 52,176,68\nR 172,187,16384\nv_mul_f32 v231, v121, v121\nM 52,180,84\n"
"s_add_u32 s64, s68, 23075840\nbuffer_load_dwordx4 v221, s[12:15], s63 offen offset:1024 lds\nM 36,176,64\nR 174,187,20480\ns_add_u32 m0, s50, 73728\nM 36,180,80\n"
"s_add_u32 s63, s54, 2752768\nG 4\nv_mul_f32 v232, v122, v122\nM 40,160,64\nR 176,184,24576\ns_waitcnt vmcnt(7)\n"
"s_barrier\nM 40,164,80\nbuffer_load_dwordx4 a[12:15], v252, s[20:23], s67 offen sc1\nbuffer_load_dwordx4 v220, s[12:15], s63 offen lds\nM 56,160,68\nR 178,184,28672\n"
"v_mul_f32 v233, v123, v123\nM 56,164,84\ns_add_u32 s65, s68, 23190528\nv_fma_f32 v230, v230, v218, v217\nv_fma_f32 v231, v231, v218, v217\nM 72,160,72\n"
"R 180,184,32768\nv_fma_f32 v232, v232, v218, v217\nM 72,164,88\nbuffer_load_dwordx4 a[16:19], v252, s[20:23], s64 offen sc1\nbuffer_load_dwordx4 v221, s[12:15], s63 offen offset:1024 lds\nM 88,160,76\n"
"R 182,184,36864\ns_add_u32 m0, s50, 81920\nM 88,164,92\nG 4\nv_fma_f32 v233, v233, v218, v217\nv_mul_f32 v230, v120, v230\n"
"M 92,168,76\nR 160,185,24576\ns_add_u32 s63, s54, 2982144\nM 92,172,92\ns_add_u32 s66, s69, 23075840\nbuffer_load_dwordx4 v220, s[12:15], s63 offen lds\n"
"M 76,168,72\nR 162,185,28672\nv_mul_f32 v231, v121, v231\nM 76,172,88\nbuffer_load_dwordx4 a[20:23], v252, s[20:23], s65 offen sc1\nM 60,168,68\n"
"R 164,185,32768\nv_mul_f32 v232, v122, v232\nM 60,172,84\nbuffer_load_dwordx4 v221, s[12:15], s63 offen offset:1024 lds\nM 44,168,64\nR 166,185,36864\n"
"v_mul_f32 v233, v123, v233\nM 44,172,80\nG 4\nv_exp_f32 v230, v230\nM 32,176,96\nR 168,186,24576\n"
"v_fma_f32 v230, v230, v219, v219\nM 32,180,112\nbuffer_load_dwordx4 a[24:27], v252, s[20:23], s66 offen sc1\nM 48,176,100\nR 170,186,28672\nv_mul_f32 v234, v124, v124\n"
"M 48,180,116\ns_add_u32 s67, s69, 23190528\nv_exp_f32 v231, v231\nM 64,176,104\nR 172,186,32768\nv_fma_f32 v231, v231, v219, v219\n"
"M 64,180,120\nbuffer_store_dwordx4 v[104:107], v252, s[20:23], s84 offen offset:1024 sc1\nM 80,176,108\nR 174,186,36864\nv_mul_f32 v235, v125, v125\nM 80,180,124\n"
"G 4\nv_exp_f32 v232, v232\nM 84,160,108\nR 176,187,24576\nv_fma_f32 v232, v232, v219, v219\nM 84,164,124\n"
"buffer_load_dwordx4 a[28:31], v252, s[20:23], s67 offen sc1\nM 68,160,104\nR 178,187,28672\nv_mul_f32 v236, v126, v126\nM 68,164,120\ns_add_u32 s64, s68, 23069696\n"
"v_exp_f32 v233, v233\nM 52,160,100\nR 180,187,32768\nv_fma_f32 v233, v233, v219, v219\nM 52,164,116\ns_add_u32 s65, s68, 23184384\n"
"v_rcp_f32 v230, v230\nM 36,160,96\nR 182,187,36864\nv_mul_f32 v230, v120, v230\nM 36,164,112\nG 4\n"
"v_rcp_f32 v231, v231\nM 40,168,96\nR 160,184,40960\nv_mul_f32 v231, v121, v231\nM 40,172,112\nbuffer_load_dwordx4 a[32:35], v252, s[20:23], s64 offen sc1\n"
"M 56,168,100\nR 162,184,45056\nv_cvt_pk_bf16_f32 v120, v230, v231\nM 56,172,116\ns_add_u32 s66, s69, 23069696\nv_rcp_f32 v232, v232\n"
"M 72,168,104\nR 164,184,49152\nv_mul_f32 v232, v122, v232\nM 72,172,120\nbuffer_load_dwordx4 a[36:39], v252, s[20:23], s65 offen sc1\nM 88,168,108\n"
"R 166,184,53248\nv_mul_f32 v237, v127, v127\nM 88,172,124\nG 4\nv_rcp_f32 v233, v233\nM 92,176,108\n"
"R 168,185,40960\ns_add_u32 m0, s50, 90112\nM 92,180,124\ns_add_u32 s67, s69, 23184384\nv_mul_f32 v233, v123, v233\nv_cvt_pk_bf16_f32 v121, v232, v233\n"
"M 76,176,104\nR 170,185,45056\ns_add_u32 s63, s54, 3211520\nM 76,180,120\nbuffer_load_dwordx4 a[40:43], v252, s[20:23], s66 offen sc1\nM 60,176,100\n"
"R 172,185,49152\nv_fma_f32 v234, v234, v218, v217\nM 60,180,116\nbuffer_load_dwordx4 v220, s[12:15], s63 offen lds\nM 44,176,96\nR 174,185,53248\n"
"v_fma_f32 v235, v235, v218, v217\nM 44,180,112\nG 4\nv_fma_f32 v236, v236, v218, v217\nv_fma_f32 v237, v237, v218, v217\nM 32,160,128\n"
"R 176,186,40960\nv_mul_f32 v234, v124, v234\nM 32,164,144\nbuffer_load_dwordx4 a[44:47], v252, s[20:23], s67 offen sc1\nbuffer_load_dwordx4 v221, s[12:15], s63 offen offset:1024 lds\nM 48,160,132\n"
"R 178,186,45056\ns_add_u32 m0, s50, 98304\nM 48,164,148\ns_add_u32 s64, s68, 23076864\nv_exp_f32 v234, v234\nM 64,160,136\n"
"R 180,186,49152\ns_add_u32 s63, s54, 3440896\nM 64,164,152\ns_add_u32 s65, s68, 23191552\nbuffer_load_dwordx4 v220, s[12:15], s63 offen lds\nM 80,160,140\n"
"R 182,186,53248\nv_mul_f32 v235, v125, v235\nM 80,164,156\nG 4\nv_exp_f32 v235, v235\nM 84,168,140\n"
"R 160,187,40960\nv_mul_f32 v236, v126, v236\nM 84,172,156\nbuffer_load_dwordx4 a[48:51], v252, s[20:23], s64 offen sc1\nbuffer_load_dwordx4 v221, s[12:15], s63 offen offset:1024 lds\nM 68,168,136\n"
"R 162,187,45056\nv_mul_f32 v237, v127, v237\nM 68,172,152\ns_add_u32 s66, s69, 23076864\nv_exp_f32 v236, v236\nM 52,168,132\n"
"R 164,187,49152\ns_waitcnt vmcnt(12)\ns_barrier\nM 52,172,148\nbuffer_load_dwordx4 a[52:55], v252, s[20:23], s65 offen sc1\ns_movk_i32 s63, 2048\n"
"v_mad_u32_u24 v220, v228, s63, v229\nv_add_u32 v221, 916480, v220\ns_add_u32 m0, s50, 106496\ns_add_u32 s63, s56, 0\nbuffer_load_dwordx4 v220, s[16:19], s63 offen lds\nM 36,168,128\n"
"R 166,187,53248\nv_fma_f32 v234, v234, v219, v219\nM 36,172,144\nG 4\nv_exp_f32 v237, v237\nM 40,176,128\n"
"R 168,184,57344\nv_fma_f32 v235, v235, v219, v219\nM 40,180,144\ns_add_u32 s67, s69, 23191552\nbuffer_load_dwordx4 v221, s[16:19], s63 offen offset:1024 lds\nM 56,176,132\n"
"R 170,184,61440\ns_add_u32 m0, s50, 114688\nM 56,180,148\nbuffer_load_dwordx4 a[56:59], v252, s[20:23], s66 offen sc1\nM 72,176,136\nR 172,188,0\n"
"s_add_u32 s63, s56, 524288\nM 72,180,152\nbuffer_load_dwordx4 v220, s[16:19], s63 offen lds\nM 88,176,140\nR 174,188,4096\nv_fma_f32 v236, v236, v219, v219\n"
"M 88,180,156\nG 4\nv_rcp_f32 v234, v234\nM 92,160,140\nR 176,185,57344\nv_fma_f32 v237, v237, v219, v219\n"
"M 92,164,156\nbuffer_load_dwordx4 a[60:63], v252, s[20:23], s67 offen sc1\nbuffer_load_dwordx4 v221, s[16:19], s63 offen offset:1024 lds\nM 76,160,136\nR 178,185,61440\nv_mul_f32 v234, v124, v234\n"
"M 76,164,152\ns_add_u32 m0, s50, 0\nv_rcp_f32 v235, v235\nM 60,160,132\nR 180,189,0\nv_mul_f32 v235, v125, v235\n"
"M 60,164,148\ns_add_u32 s63, s56, 65536\nv_rcp_f32 v236, v236\nM 44,160,128\nR 182,189,4096\nv_mul_f32 v236, v126, v236\n"
"M 44,164,144\nG 4\nv_rcp_f32 v237, v237\nM 32,168,160\nR 160,186,57344\nv_mul_f32 v237, v127, v237\n"
"M 32,172,176\nbuffer_load_dwordx4 v220, s[16:19], s63 offen lds\nM 48,168,164\nR 162,186,61440\nv_cvt_pk_bf16_f32 v122, v234, v235\nM 48,172,180\n"
"buffer_store_dwordx4 v[112:115], v252, s[20:23], s85 offen offset:0 sc1\nM 64,168,168\nR 164,190,0\nv_cvt_pk_bf16_f32 v123, v236, v237\nM 64,172,184\nbuffer_load_dwordx4 v221, s[16:19], s63 offen offset:1024 lds\n"
"M 80,168,172\nR 166,190,4096\ns_add_u32 m0, s50, 8192\nM 80,172,188\nbuffer_store_dwordx4 v[120:123], v252, s[20:23], s85 offen offset:1024 sc1\nG 4\n"
"M 84,176,172\nR 168,187,57344\ns_add_u32 s63, s56, 589824\nM 84,180,188\nbuffer_load_dwordx4 v220, s[16:19], s63 offen lds\nM 68,176,168\n"
"R 170,187,61440\nv_mul_f32 v230, v0, v0\nv_mul_f32 v231, v1, v1\nM 68,180,184\ns_add_u32 s84, s73, 23074816\ns_add_u32 s85, s73, 23189504\n"
"v_mul_f32 v232, v2, v2\nv_mul_f32 v233, v3, v3\nM 52,176,164\nR 172,191,0\nv_fma_f32 v230, v230, v215, v214\nv_fma_f32 v231, v231, v215, v214\n"
"M 52,180,180\nbuffer_load_dwordx4 v221, s[16:19], s63 offen offset:1024 lds\nv_fma_f32 v232, v232, v215, v214\nM 36,176,160\nR 174,191,4096\nv_fma_f32 v233, v233, v215, v214\n"
"v_mul_f32 v230, v0, v230\nM 36,180,176\nG 4\nv_exp_f32 v230, v230\nv_mul_f32 v231, v1, v231\nM 40,160,160\n"
"R 176,188,8192\nv_mul_f32 v232, v2, v232\nv_mul_f32 v233, v3, v233\nM 40,164,176\nv_exp_f32 v231, v231\nv_exp_f32 v232, v232\n"
"M 56,160,164\nR 178,188,12288\nv_fma_f32 v230, v230, v216, v216\nv_exp_f32 v233, v233\nM 56,164,180\nv_rcp_f32 v230, v230\n"
"v_fma_f32 v231, v231, v216, v216\nM 72,160,168\nR 180,188,16384\nv_fma_f32 v232, v232, v216, v216\nv_fma_f32 v233, v233, v216, v216\nM 72,164,184\n"
"v_rcp_f32 v231, v231\nv_rcp_f32 v232, v232\nM 88,160,172\nR 182,188,20480\nv_mul_f32 v230, v0, v230\nv_rcp_f32 v233, v233\n"
"M 88,164,188\nG 4\nv_mul_f32 v231, v1, v231\nv_mul_f32 v232, v2, v232\nv_mul_f32 v233, v3, v233\nM 92,168,172\n"
"R 160,189,8192\nv_cvt_pk_bf16_f32 v0, v230, v231\nv_cvt_pk_bf16_f32 v1, v232, v233\nM 92,172,188\nv_mul_f32 v234, v4, v4\nv_mul_f32 v235, v5, v5\n"
"v_mul_f32 v236, v6, v6\nM 76,168,168\nR 162,189,12288\nv_mul_f32 v237, v7, v7\nv_fma_f32 v234, v234, v215, v214\nM 76,172,184\n"
"v_fma_f32 v235, v235, v215, v214\nv_fma_f32 v236, v236, v215, v214\nv_fma_f32 v237, v237, v215, v214\nM 60,168,164\nR 164,189,16384\nv_mul_f32 v234, v4, v234\n"
"v_mul_f32 v235, v5, v235\nM 60,172,180\nv_exp_f32 v234, v234\nv_mul_f32 v236, v6, v236\nM 44,168,160\nR 166,189,20480\n"
"v_mul_f32 v237, v7, v237\nv_exp_f32 v235, v235\nM 44,172,176\nG 4\nv_exp_f32 v236, v236\nv_exp_f32 v237, v237\n"
"M 32,176,192\nR 168,190,8192\nv_fma_f32 v234, v234, v216, v216\nv_fma_f32 v235, v235, v216, v216\nM 32,180,208\nv_rcp_f32 v234, v234\n"
"v_fma_f32 v236, v236, v216, v216\nM 48,176,196\nR 170,190,12288\nv_fma_f32 v237, v237, v216, v216\nv_rcp_f32 v235, v235\nM 48,180,212\n"
"v_rcp_f32 v236, v236\nv_rcp_f32 v237, v237\nM 64,176,200\nR 172,190,16384\nv_mul_f32 v234, v4, v234\nv_mul_f32 v235, v5, v235\n"
"M 64,180,216\nv_mul_f32 v236, v6, v236\nv_mul_f32 v237, v7, v237\nv_cvt_pk_bf16_f32 v2, v234, v235\nM 80,176,204\nR 174,190,20480\n"
"v_cvt_pk_bf16_f32 v3, v236, v237\nv_mul_f32 v230, v16, v16\nM 80,180,220\nG 4\nv_mul_f32 v231, v17, v17\nv_mul_f32 v232, v18, v18\n"
"v_mul_f32 v233, v19, v19\nM 84,160,204\nR 176,191,8192\ns_waitcnt vmcnt(6)\ns_barrier\nv_fma_f32 v230, v230, v218, v217\n"
"M 84,164,220\nv_fma_f32 v231, v231, v218, v217\nv_fma_f32 v232, v232, v218, v217\nv_fma_f32 v233, v233, v218, v217\nM 68,160,200\nR 178,191,12288\n"
"v_mul_f32 v230, v16, v230\nv_mul_f32 v231, v17, v231\nM 68,164,216\nv_exp_f32 v230, v230\nv_mul_f32 v232, v18, v232\nM 52,160,196\n"
"R 180,191,16384\nv_mul_f32 v233, v19, v233\nv_exp_f32 v231, v231\nM 52,164,212\nv_exp_f32 v232, v232\nv_exp_f32 v233, v233\n"
"M 36,160,192\nR 182,191,20480\nv_fma_f32 v230, v230, v219, v219\nv_fma_f32 v231, v231, v219, v219\nM 36,164,208\nG 4\n"
"v_rcp_f32 v230, v230\nv_fma_f32 v232, v232, v219, v219\nM 40,168,192\nR 160,188,24576\nv_fma_f32 v233, v233, v219, v219\nv_rcp_f32 v231, v231\n"
"M 40,172,208\nv_rcp_f32 v232, v232\nv_rcp_f32 v233, v233\nM 56,168,196\nR 162,188,28672\nv_mul_f32 v230, v16, v230\n"
"v_mul_f32 v231, v17, v231\nM 56,172,212\ns_add_u32 m0, s50, 16384\nv_mul_f32 v232, v18, v232\nv_mul_f32 v233, v19, v233\nv_cvt_pk_bf16_f32 v16, v230, v231\n"
"M 72,168,200\nR 164,188,32768\nv_cvt_pk_bf16_f32 v17, v232, v233\nv_mul_f32 v234, v20, v20\nM 72,172,216\ns_add_u32 s63, s57, 0\n"
"v_mul_f32 v235, v21, v21\nv_mul_f32 v236, v22, v22\nv_mul_f32 v237, v23, v23\nM 88,168,204\nR 166,188,36864\nv_mov_b32 v226, s46\n"
"v_fma_f32 v234, v234, v218, v217\nM 88,172,220\nG 4\nv_mov_b32 v227, s47\nv_fma_f32 v235, v235, v218, v217\nv_fma_f32 v236, v236, v218, v217\n"
"M 92,176,204\nR 168,189,24576\ns_add_u32 s61, s81, 385032\ns_lshl_b32 s62, s44, 5\ns_add_u32 s61, s61, s62\ns_waitcnt vmcnt(0)\n"
"s_mov_b64 exec, 1\nbuffer_atomic_swap_x2 v[226:227], off, s[20:23], s61 sc1\ns_mov_b64 exec, -1\nv_fma_f32 v237, v237, v218, v217\nM 92,180,220\nbuffer_load_dwordx4 v220, s[16:19], s63 offen lds\n"
"v_mul_f32 v234, v20, v234\nM 76,176,200\nR 170,189,28672\nv_mul_f32 v235, v21, v235\nv_mul_f32 v236, v22, v236\nM 76,180,216\n"
"v_exp_f32 v234, v234\nv_mul_f32 v237, v23, v237\nM 60,176,196\nR 172,189,32768\nv_fma_f32 v234, v234, v219, v219\nv_exp_f32 v235, v235\n"
"M 60,180,212\nbuffer_load_dwordx4 v221, s[16:19], s63 offen offset:1024 lds\nv_exp_f32 v236, v236\nM 44,176,192\nR 174,189,36864\ns_mov_b32 s60, 158208\n"
"v_bfe_u32 v246, v252, 4, 4\nv_lshl_add_u32 v246, v246, 3, s60\nv_exp_f32 v237, v237\nM 44,180,208\nds_read_b64 v[238:239], v246 offset:0\nG 5\n"
"v_fma_f32 v235, v235, v219, v219\nM 32,160,224\nR 176,190,24576\nds_read_b64 v[240:241], v246 offset:128\ns_add_u32 s63, s57, 524288\nv_fma_f32 v236, v236, v219, v219\n"
"M 32,164,240\nds_read_b64 v[242:243], v246 offset:256\ns_add_u32 m0, s50, 24576\nv_fma_f32 v237, v237, v219, v219\nM 48,160,228\nR 178,190,28672\n"
"ds_read_b64 v[244:245], v246 offset:384\nv_rcp_f32 v234, v234\nM 48,164,244\nds_read_b64 v[246:247], v246 offset:0\nbuffer_load_dwordx4 v220, s[16:19], s63 offen lds\nv_rcp_f32 v235, v235\n"
"M 64,160,232\nR 180,190,32768\nv_mul_f32 v234, v20, v234\nv_rcp_f32 v236, v236\nM 64,164,248\nbuffer_load_dwordx4 v221, s[16:19], s63 offen offset:1024 lds\n"
"v_rcp_f32 v237, v237\nM 80,160,236\nR 182,190,36864\ns_add_u32 m0, s50, 32768\nM 80,164,252\nG 9\n"
"v_mul_f32 v235, v21, v235\nv_mul_f32 v236, v22, v236\nM 84,168,236\nR 160,191,24576\ns_add_u32 s63, s57, 65536\nM 84,172,252\n"
"buffer_load_dwordx4 v220, s[16:19], s63 offen lds\nM 68,168,232\nR 162,191,28672\nv_mul_f32 v237, v23, v237\nM 68,172,248\nbuffer_store_dwordx4 v[0:3], v252, s[20:23], s84 offen offset:0 sc1\n"
"M 52,168,228\nR 164,191,32768\nG 9\nv_mfma_f32_16x16x32_bf16 v[32:35], v[238:241], v[208:211], v[32:35]\nv_cvt_pk_bf16_f32 v18, v234, v235\nM 52,172,244\n"
"buffer_load_dwordx4 v221, s[16:19], s63 offen offset:1024 lds\nM 36,168,224\nR 166,191,36864\nG 9\nv_mfma_f32_16x16x32_bf16 v[36:39], v[240:243], v[208:211], v[36:39]\ns_add_u32 m0, s50, 40960\n"
"M 36,172,240\nG 4\nv_cvt_pk_bf16_f32 v19, v236, v237\nM 40,176,224\nR 168,188,40960\nv_mfma_f32_16x16x32_bf16 v[40:43], v[242:245], v[208:211], v[40:43]\n"
"s_add_u32 s63, s57, 589824\nbuffer_store_dwordx4 v[16:19], v252, s[20:23], s85 offen offset:0 sc1\nM 40,180,240\nbuffer_load_dwordx4 v220, s[16:19], s63 offen lds\nM 56,176,228\nR 170,188,45056\n"
"v_mfma_f32_16x16x32_bf16 v[44:47], v[244:247], v[208:211], v[44:47]\nM 56,180,244\nbuffer_load_dwordx4 v221, s[16:19], s63 offen offset:1024 lds\nM 72,176,232\nR 172,188,49152\nv_mfma_f32_16x16x32_bf16 v[48:51], v[244:247], v[210:213], v[48:51]\n"
"s_add_u32 m0, s50, 49152\ns_add_u32 s63, s56, 131072\nM 72,180,248\nbuffer_load_dwordx4 v220, s[16:19], s63 offen lds\nM 88,176,236\nR 174,188,53248\n"
"v_mfma_f32_16x16x32_bf16 v[52:55], v[238:241], v[210:213], v[52:55]\nM 88,180,252\nG 4\nM 92,160,236\nR 176,189,40960\nv_mfma_f32_16x16x32_bf16 v[56:59], v[240:243], v[210:213], v[56:59]\n"
"M 92,164,252\nbuffer_load_dwordx4 v221, s[16:19], s63 offen offset:1024 lds\nM 76,160,232\nR 178,189,45056\nv_mfma_f32_16x16x32_bf16 v[60:63], v[242:245], v[210:213], v[60:63]\nv_mov_b32 v248, 0\n"
"M 76,164,248\nv_mov_b32 v249, 0\nM 60,160,228\nR 180,189,49152\ns_add_u32 s61, s82, 385032\ns_mov_b64 exec, 0xf\n"
"buffer_atomic_or_x2 v[248:249], v250, s[20:23], s61 offen sc0 sc1\ns_mov_b64 exec, -1\nM 60,164,244\nM 44,160,224\nR 182,189,53248\nM 44,164,240\n"
"s_barrier\nG 4\nv_mfma_f32_16x16x32_bf16 v[96:99], v[168:171], a[0:3], 0\nR 160,190,40960\nM 96,172,16\ns_mov_b32 m0, s51\n"
"v_mfma_f32_16x16x32_bf16 v[112:115], v[168:171], a[4:7], 0\nR 162,190,45056\nM 112,172,20\nds_write_addtid_b32 v64 offset:0\nv_mfma_f32_16x16x32_bf16 v[128:131], v[168:171], a[8:11], 0\nR 164,190,49152\n"
"M 128,172,24\nds_write_addtid_b32 v65 offset:256\nv_mfma_f32_16x16x32_bf16 v[144:147], v[168:171], a[12:15], 0\nR 166,190,53248\nM 144,172,28\nds_write_addtid_b32 v66 offset:512\n"
"G 7\nv_mfma_f32_16x16x32_bf16 v[148:151], v[176:179], a[12:15], 0\nR 168,191,40960\nM 148,180,28\nds_write_addtid_b32 v67 offset:768\nv_mfma_f32_16x16x32_bf16 v[132:135], v[176:179], a[8:11], 0\n"
"R 170,191,45056\nM 132,180,24\nds_write_addtid_b32 v68 offset:1024\nv_mfma_f32_16x16x32_bf16 v[116:119], v[176:179], a[4:7], 0\nR 172,191,49152\nM 116,180,20\n"
"ds_write_addtid_b32 v69 offset:1280\nv_mfma_f32_16x16x32_bf16 v[100:103], v[176:179], a[0:3], 0\nR 174,191,53248\nds_write_addtid_b32 v70 offset:1536\nM 100,180,16\nds_write_addtid_b32 v71 offset:1792\n"
"ds_write_addtid_b32 v72 offset:2048\nG 11\nv_mfma_f32_16x16x32_bf16 v[104:107], v[160:163], a[0:3], 0\nR 176,184,0\nds_write_addtid_b32 v73 offset:2304\nM 104,164,16\n"
"ds_write_addtid_b32 v74 offset:2560\nds_write_addtid_b32 v75 offset:2816\nv_mfma_f32_16x16x32_bf16 v[120:123], v[160:163], a[4:7], 0\nR 178,184,4096\nds_write_addtid_b32 v76 offset:3072\nM 120,164,20\n"
"ds_write_addtid_b32 v77 offset:3328\nds_write_addtid_b32 v78 offset:3584\nv_mfma_f32_16x16x32_bf16 v[136:139], v[160:163], a[8:11], 0\nR 180,184,8192\nds_write_addtid_b32 v79 offset:3840\nM 136,164,24\n"
"ds_write_addtid_b32 v80 offset:4096\nds_write_addtid_b32 v81 offset:4352\nv_mfma_f32_16x16x32_bf16 v[152:155], v[160:163], a[12:15], 0\nR 182,184,12288\nds_write_addtid_b32 v82 offset:4608\nM 152,164,28\n"
"ds_write_addtid_b32 v83 offset:4864\nds_write_addtid_b32 v84 offset:5120\nG 15\nv_mfma_f32_16x16x32_bf16 v[156:159], v[168:171], a[12:15], 0\nR 160,185,0\nds_write_addtid_b32 v85 offset:5376\n"
"M 156,172,28\nds_write_addtid_b32 v86 offset:5632\nds_write_addtid_b32 v87 offset:5888\nv_mfma_f32_16x16x32_bf16 v[140:143], v[168:171], a[8:11], 0\nR 162,185,4096\nds_write_addtid_b32 v88 offset:6144\n"
"M 140,172,24\nds_write_addtid_b32 v89 offset:6400\nds_write_addtid_b32 v90 offset:6656\nv_mfma_f32_16x16x32_bf16 v[124:127], v[168:171], a[4:7], 0\nR 164,185,8192\nds_write_addtid_b32 v91 offset:6912\n"
"M 124,172,20\ns_add_u32 s64, s68, 23070720\nds_write_addtid_b32 v92 offset:7168\nds_write_addtid_b32 v93 offset:7424\nv_mfma_f32_16x16x32_bf16 v[108:111], v[168:171], a[0:3], 0\nR 166,185,12288\n"
"s_waitcnt vmcnt(0)\nv_cmp_eq_u64 s[86:87], s[46:47], v[248:249]\nM 108,172,16\nds_write_addtid_b32 v94 offset:7680\nds_write_addtid_b32 v95 offset:7936\nG 15\n"
"M 96,176,32\nR 168,186,0\ns_and_b32 s62, s86, 0xf\ns_cmp_eq_u32 s62, 0xf\ns_cbranch_scc0 Lq402\nLq403:\n"
"M 96,180,48\ns_add_u32 s65, s68, 23185408\ns_add_u32 m0, s50, 57344\nv_mov_b32 v226, s46\nM 112,176,36\nR 170,186,4096\n"
"G 2\ns_barrier\nv_mov_b32 v227, s47\nM 112,180,52\ns_add_u32 s61, s81, 385048\ns_lshl_b32 s62, s44, 5\n"
"s_add_u32 s61, s61, s62\ns_waitcnt vmcnt(0)\ns_mov_b64 exec, 1\nbuffer_atomic_swap_x2 v[226:227], off, s[20:23], s61 sc1\ns_mov_b64 exec, -1\ns_add_u32 s63, s56, 655360\n"
"ds_read2st64_b32 v[64:65], v225 offset0:0 offset1:1\nM 128,176,40\nR 172,186,8192\nbuffer_load_dwordx4 v220, s[16:19], s63 offen lds\nds_read2st64_b32 v[66:67], v225 offset0:2 offset1:3\nM 128,180,56\n"
"buffer_load_dwordx4 a[64:67], v252, s[20:23], s64 offen sc1\nds_read2st64_b32 v[68:69], v225 offset0:4 offset1:5\nM 144,176,44\nR 174,186,12288\nds_read2st64_b32 v[70:71], v225 offset0:6 offset1:7\nM 144,180,60\n"
"ds_read2st64_b32 v[72:73], v225 offset0:8 offset1:9\nds_read2st64_b32 v[74:75], v225 offset0:10 offset1:11\nM 148,160,44\nR 176,187,0\nbuffer_load_dwordx4 v221, s[16:19], s63 offen offset:1024 lds\nds_read2st64_b32 v[76:77], v225 offset0:12 offset1:13\n"
"M 148,164,60\nbuffer_load_dwordx4 a[68:71], v252, s[20:23], s65 offen sc1\nG 9\nv_add_f32 v32, v32, v64\nM 132,160,40\nR 178,187,4096\n"
"s_add_u32 s67, s69, 23185408\nv_add_f32 v33, v33, v65\nM 132,164,56\ns_add_u32 s66, s69, 23070720\ns_add_u32 m0, s50, 65536\ns_add_u32 s63, s56, 196608\n"
"G 8\nv_add_f32 v34, v34, v66\nM 116,160,36\nR 180,187,8192\nbuffer_load_dwordx4 v220, s[16:19], s63 offen lds\nv_add_f32 v35, v35, v67\n"
"M 116,164,52\nbuffer_load_dwordx4 a[72:75], v252, s[20:23], s66 offen sc1\nG 8\nv_add_f32 v36, v36, v68\nM 100,160,32\nR 182,187,12288\n"
"v_add_f32 v37, v37, v69\nG 7\nv_add_f32 v38, v38, v70\nM 100,164,48\nds_read2st64_b32 v[78:79], v225 offset0:14 offset1:15\nv_add_f32 v39, v39, v71\n"
"M 104,168,32\nR 160,184,16384\nbuffer_load_dwordx4 v221, s[16:19], s63 offen offset:1024 lds\nG 8\nv_add_f32 v40, v40, v72\nM 104,172,48\n"
"buffer_load_dwordx4 a[76:79], v252, s[20:23], s67 offen sc1\nv_add_f32 v41, v41, v73\nM 120,168,36\nR 162,184,20480\nG 8\nv_add_f32 v42, v42, v74\n"
"v_add_f32 v43, v43, v75\nM 120,172,52\ns_add_u32 s64, s68, 23077888\nds_read2st64_b32 v[80:81], v225 offset0:16 offset1:17\nG 7\nv_add_f32 v44, v44, v76\n"
"M 136,168,40\nR 164,184,24576\nds_read2st64_b32 v[82:83], v225 offset0:18 offset1:19\nv_add_f32 v45, v45, v77\nM 136,172,56\ns_add_u32 s65, s68, 23192576\n"
"ds_read2st64_b32 v[84:85], v225 offset0:20 offset1:21\nG 6\nv_add_f32 v46, v46, v78\nM 152,168,44\nR 166,184,28672\nds_read2st64_b32 v[86:87], v225 offset0:22 offset1:23\n"
"v_add_f32 v47, v47, v79\nM 152,172,60\nds_read2st64_b32 v[88:89], v225 offset0:24 offset1:25\nds_read2st64_b32 v[90:91], v225 offset0:26 offset1:27\nM 156,176,44\nR 168,185,16384\n"
"ds_read2st64_b32 v[92:93], v225 offset0:28 offset1:29\nM 156,180,60\nbuffer_load_dwordx4 a[80:83], v252, s[20:23], s64 offen sc1\nG 9\nv_add_f32 v48, v48, v80\nM 140,176,40\n"
"R 170,185,20480\nv_add_f32 v49, v49, v81\nG 8\nv_add_f32 v50, v50, v82\nM 140,180,56\nbuffer_load_dwordx4 a[84:87], v252, s[20:23], s65 offen sc1\n"
"v_add_f32 v51, v51, v83\nM 124,176,36\nR 172,185,24576\nG 8\nv_add_f32 v52, v52, v84\nv_add_f32 v53, v53, v85\n"
"M 124,180,52\ns_add_u32 s66, s69, 23077888\nds_read2st64_b32 v[94:95], v225 offset0:30 offset1:31\nG 7\nv_add_f32 v54, v54, v86\nM 108,176,32\n"
"R 174,185,28672\nv_add_f32 v55, v55, v87\nM 108,180,48\nG 7\nv_add_f32 v56, v56, v88\nv_add_f32 v57, v57, v89\n"
"v_mfma_f32_16x16x32_bf16 v[0:3], v[160:163], a[0:3], 0\nR 176,186,16384\nLq570:\ns_lshr_b32 s60, s45, 3\ns_lshl2_add_u32 s60, s60, s40\ns_lshl_b32 s60, s60, 8\n"
"v_lshl_add_u32 v224, v252, 3, s60\nv_add_u32 v224, Lq570+4096-LqPC, v224\ns_mov_b64 exec, 3\nglobal_load_dword v224, v224, s[98:99]\ns_mov_b64 exec, -1\nM 0,164,16\n"
"buffer_load_dwordx4 a[88:91], v252, s[20:23], s66 offen sc1\nv_mfma_f32_16x16x32_bf16 v[16:19], v[160:163], a[4:7], 0\nR 178,186,20480\nG 8\nv_add_f32 v58, v58, v90\nM 16,164,20\n"
"s_add_u32 s67, s69, 23192576\nv_add_f32 v59, v59, v91\nG 6\nv_add_f32 v60, v60, v92\nv_mfma_f32_16x16x32_bf16 a[224:227], v[160:163], a[8:11], 0\nR 180,186,24576\n"
"v_add_f32 v61, v61, v93\nv_mfma_f32_16x16x32_bf16 a[224:227], v[164:167], a[24:27], a[224:227]\nbuffer_load_dwordx4 a[92:95], v252, s[20:23], s67 offen sc1\nv_mfma_f32_16x16x32_bf16 a[240:243], v[160:163], a[12:15], 0\nR 182,186,28672\nG 5\n"
"v_add_f32 v62, v62, v94\nv_mfma_f32_16x16x32_bf16 a[240:243], v[164:167], a[28:31], a[240:243]\ns_add_u32 s63, s56, 720896\nG 4\nv_add_f32 v63, v63, v95\nv_mfma_f32_16x16x32_bf16 a[244:247], v[168:171], a[12:15], 0\n"
"R 160,187,16384\ns_add_u32 m0, s50, 73728\nv_mul_f32 v230, v32, v32\nv_mfma_f32_16x16x32_bf16 a[244:247], v[172:175], a[28:31], a[244:247]\ns_add_u32 s64, s68, 23071744\ns_add_u32 s84, s73, 23072768\n"
"s_add_u32 s85, s73, 23187456\nv_mul_f32 v231, v33, v33\nv_mfma_f32_16x16x32_bf16 a[228:231], v[168:171], a[8:11], 0\nR 162,187,20480\nv_mov_b32 v248, 0\nv_mul_f32 v232, v34, v34\n"
"v_mfma_f32_16x16x32_bf16 a[228:231], v[172:175], a[24:27], a[228:231]\nbuffer_load_dwordx4 a[96:99], v252, s[20:23], s64 offen sc1\nv_mov_b32 v249, 0\nv_mul_f32 v233, v35, v35\nv_mfma_f32_16x16x32_bf16 v[20:23], v[168:171], a[4:7], 0\nR 164,187,24576\n"
"s_add_u32 s61, s82, 385048\ns_mov_b64 exec, 0xf\nbuffer_atomic_or_x2 v[248:249], v250, s[20:23], s61 offen sc0 sc1\ns_mov_b64 exec, -1\nv_fma_f32 v230, v230, v215, v214\nM 20,172,20\n"
"s_add_u32 s65, s68, 23186432\nbuffer_load_dwordx4 v220, s[16:19], s63 offen lds\nv_fma_f32 v231, v231, v215, v214\nv_mfma_f32_16x16x32_bf16 v[4:7], v[168:171], a[0:3], 0\nR 166,187,28672\nv_fma_f32 v232, v232, v215, v214\n"
"v_fma_f32 v233, v233, v215, v214\nM 4,172,16\nG 4\nv_mul_f32 v230, v32, v230\nv_mul_f32 v231, v33, v231\nv_mul_f32 v232, v34, v232\n"
"v_mfma_f32_16x16x32_bf16 v[8:11], v[176:179], a[0:3], 0\nR 168,184,32768\nv_mul_f32 v233, v35, v233\nv_exp_f32 v230, v230\nM 8,180,16\nbuffer_load_dwordx4 a[100:103], v252, s[20:23], s65 offen sc1\n"
"buffer_load_dwordx4 v221, s[16:19], s63 offen offset:1024 lds\nv_exp_f32 v231, v231\nv_mfma_f32_16x16x32_bf16 v[24:27], v[176:179], a[4:7], 0\nR 170,184,36864\ns_add_u32 s67, s69, 23186432\nv_exp_f32 v232, v232\n"
"M 24,180,20\ns_add_u32 s66, s69, 23071744\ns_add_u32 s63, s56, 393216\nv_fma_f32 v230, v230, v216, v216\nv_exp_f32 v233, v233\nv_mfma_f32_16x16x32_bf16 a[232:235], v[176:179], a[8:11], 0\n"
"R 172,184,40960\ns_add_u32 m0, s50, 81920\nv_fma_f32 v231, v231, v216, v216\nv_mfma_f32_16x16x32_bf16 a[232:235], v[180:183], a[24:27], a[232:235]\nbuffer_load_dwordx4 a[104:107], v252, s[20:23], s66 offen sc1\nbuffer_load_dwordx4 v220, s[16:19], s63 offen lds\n"
"v_fma_f32 v232, v232, v216, v216\nv_mfma_f32_16x16x32_bf16 a[248:251], v[176:179], a[12:15], 0\nR 174,184,45056\nv_fma_f32 v233, v233, v216, v216\nv_rcp_f32 v230, v230\nv_mfma_f32_16x16x32_bf16 a[248:251], v[180:183], a[28:31], a[248:251]\n"
"G 4\nv_rcp_f32 v231, v231\nv_rcp_f32 v232, v232\nv_mfma_f32_16x16x32_bf16 a[252:255], v[160:163], a[12:15], 0\nR 176,185,32768\nv_mul_f32 v230, v32, v230\n"
"v_rcp_f32 v233, v233\nv_mfma_f32_16x16x32_bf16 a[252:255], v[164:167], a[28:31], a[252:255]\nbuffer_load_dwordx4 a[108:111], v252, s[20:23], s67 offen sc1\nbuffer_load_dwordx4 v221, s[16:19], s63 offen offset:1024 lds\nv_mul_f32 v231, v33, v231\nv_mfma_f32_16x16x32_bf16 a[236:239], v[160:163], a[8:11], 0\n"
"R 178,185,36864\ns_add_u32 m0, s50, 90112\nv_mul_f32 v232, v34, v232\nv_mfma_f32_16x16x32_bf16 a[236:239], v[164:167], a[24:27], a[236:239]\ns_add_u32 s64, s68, 23078912\nv_mul_f32 v233, v35, v233\n"
"v_cvt_pk_bf16_f32 v32, v230, v231\nv_cvt_pk_bf16_f32 v33, v232, v233\nv_mfma_f32_16x16x32_bf16 v[28:31], v[160:163], a[4:7], 0\nR 180,185,40960\ns_add_u32 s63, s56, 458752\nv_mul_f32 v234, v36, v36\n"
"M 28,164,20\ns_add_u32 s65, s68, 23193600\nbuffer_load_dwordx4 v220, s[16:19], s63 offen lds\nv_mul_f32 v235, v37, v37\nv_mfma_f32_16x16x32_bf16 v[12:15], v[160:163], a[0:3], 0\nR 182,185,45056\n"
"v_mul_f32 v236, v38, v38\nv_mul_f32 v237, v39, v39\nM 12,164,16\ns_waitcnt vmcnt(16)\ns_barrier\nG 4\n"
"v_fma_f32 v234, v234, v215, v214\nM 0,168,32\nR 160,186,32768\nv_fma_f32 v235, v235, v215, v214\nv_fma_f32 v236, v236, v215, v214\nM 0,172,48\n"
"buffer_load_dwordx4 a[112:115], v252, s[20:23], s64 offen sc1\nbuffer_load_dwordx4 v221, s[16:19], s63 offen offset:1024 lds\nv_fma_f32 v237, v237, v215, v214\nM 16,168,36\nR 162,186,36864\nv_mul_f32 v234, v36, v234\n"
"v_mul_f32 v235, v37, v235\nM 16,172,52\nbuffer_load_dwordx4 a[116:119], v252, s[20:23], s65 offen sc1\nv_mul_f32 v236, v38, v236\nv_mfma_f32_16x16x32_bf16 a[224:227], v[168:171], a[40:43], a[224:227]\nR 164,186,40960\n"
"v_mul_f32 v237, v39, v237\nv_exp_f32 v234, v234\nv_mfma_f32_16x16x32_bf16 a[224:227], v[172:175], a[56:59], a[224:227]\ns_add_u32 s66, s69, 23078912\nv_exp_f32 v235, v235\nv_exp_f32 v236, v236\n"
"v_mfma_f32_16x16x32_bf16 a[240:243], v[168:171], a[44:47], a[240:243]\nR 166,186,45056\nv_fma_f32 v234, v234, v216, v216\nv_exp_f32 v237, v237\nv_mfma_f32_16x16x32_bf16 a[240:243], v[172:175], a[60:63], a[240:243]\nG 4\n"
"v_rcp_f32 v234, v234\nv_fma_f32 v235, v235, v216, v216\nv_mfma_f32_16x16x32_bf16 a[244:247], v[176:179], a[44:47], a[244:247]\nR 168,187,32768\nv_fma_f32 v236, v236, v216, v216\nv_fma_f32 v237, v237, v216, v216\n"
"v_mfma_f32_16x16x32_bf16 a[244:247], v[180:183], a[60:63], a[244:247]\nbuffer_load_dwordx4 a[120:123], v252, s[20:23], s66 offen sc1\nv_rcp_f32 v235, v235\nv_mfma_f32_16x16x32_bf16 a[228:231], v[176:179], a[40:43], a[228:231]\nR 170,187,36864\nv_mul_f32 v234, v36, v234\n"
"v_rcp_f32 v236, v236\nv_mfma_f32_16x16x32_bf16 a[228:231], v[180:183], a[56:59], a[228:231]\ns_add_u32 s67, s69, 23193600\nv_rcp_f32 v237, v237\nv_mul_f32 v235, v37, v235\nM 20,176,36\n"
"R 172,187,40960\nv_mul_f32 v236, v38, v236\nv_mul_f32 v237, v39, v237\nM 20,180,52\nbuffer_load_dwordx4 a[124:127], v252, s[20:23], s67 offen sc1\nv_cvt_pk_bf16_f32 v34, v234, v235\n"
"M 4,176,32\nR 174,187,45056\ns_waitcnt vmcnt(13)\nv_cmp_eq_u64 s[86:87], s[46:47], v[248:249]\nv_cvt_pk_bf16_f32 v35, v236, v237\nM 4,180,48\n"
"s_add_u32 s63, s57, 131072\nG 4\nv_mul_f32 v230, v40, v40\nv_mul_f32 v231, v41, v41\nM 8,160,32\nR 176,184,49152\n"
"s_and_b32 s62, s86, 0xf\ns_cmp_eq_u32 s62, 0xf\ns_cbranch_scc0 Lq437\nLq438:\nv_mul_f32 v232, v42, v42\nM 8,164,48\n"
"s_add_u32 s64, s68, 23074816\nv_mul_f32 v233, v43, v43\nv_fma_f32 v230, v230, v215, v214\nv_fma_f32 v231, v231, v215, v214\nM 24,160,36\nR 178,184,53248\n"
"s_add_u32 m0, s50, 98304\nv_fma_f32 v232, v232, v215, v214\nM 24,164,52\nbuffer_load_dwordx4 a[128:131], v252, s[20:23], s64 offen sc1\nbuffer_load_dwordx4 v220, s[16:19], s63 offen lds\nv_fma_f32 v233, v233, v215, v214\n"
"v_mfma_f32_16x16x32_bf16 a[232:235], v[160:163], a[40:43], a[232:235]\nR 180,184,57344\ns_add_u32 s66, s69, 23074816\nv_mul_f32 v230, v40, v230\nv_mfma_f32_16x16x32_bf16 a[232:235], v[164:167], a[56:59], a[232:235]\ns_add_u32 s65, s68, 23189504\n"
"v_exp_f32 v230, v230\nv_mul_f32 v231, v41, v231\nv_mfma_f32_16x16x32_bf16 a[248:251], v[160:163], a[44:47], a[248:251]\nR 182,184,61440\nv_mul_f32 v232, v42, v232\nv_mul_f32 v233, v43, v233\n"
"v_mfma_f32_16x16x32_bf16 a[248:251], v[164:167], a[60:63], a[248:251]\nbuffer_load_dwordx4 v221, s[16:19], s63 offen offset:1024 lds\nG 4\nv_exp_f32 v231, v231\nv_mfma_f32_16x16x32_bf16 a[252:255], v[168:171], a[44:47], a[252:255]\nR 160,185,49152\n"
"s_add_u32 m0, s50, 106496\nv_exp_f32 v232, v232\nv_mfma_f32_16x16x32_bf16 a[252:255], v[172:175], a[60:63], a[252:255]\nbuffer_load_dwordx4 a[132:135], v252, s[20:23], s65 offen sc1\nv_exp_f32 v233, v233\nv_mfma_f32_16x16x32_bf16 a[236:239], v[168:171], a[40:43], a[236:239]\n"
"R 162,185,53248\ns_add_u32 s63, s57, 655360\nv_fma_f32 v230, v230, v216, v216\nv_mfma_f32_16x16x32_bf16 a[236:239], v[172:175], a[56:59], a[236:239]\nbuffer_load_dwordx4 a[136:139], v252, s[20:23], s66 offen sc1\nbuffer_load_dwordx4 v220, s[16:19], s63 offen lds\n"
"v_fma_f32 v231, v231, v216, v216\nM 28,168,36\nR 164,185,57344\ns_add_u32 s64, s68, 23081984\nv_fma_f32 v232, v232, v216, v216\nM 28,172,52\n"
"s_add_u32 s67, s69, 23189504\nv_rcp_f32 v230, v230\nv_fma_f32 v233, v233, v216, v216\nM 12,168,32\nR 166,185,61440\nv_mul_f32 v230, v40, v230\n"
"v_rcp_f32 v231, v231\nM 12,172,48\nbuffer_load_dwordx4 v221, s[16:19], s63 offen offset:1024 lds\nG 4\nv_rcp_f32 v232, v232\nM 96,176,64\n"
"R 168,186,49152\ns_add_u32 m0, s50, 114688\nv_rcp_f32 v233, v233\nM 96,180,80\nbuffer_load_dwordx4 a[140:143], v252, s[20:23], s67 offen sc1\nv_mul_f32 v231, v41, v231\n"
"M 112,176,68\nR 170,186,53248\ns_add_u32 s63, s57, 196608\nv_mul_f32 v232, v42, v232\nM 112,180,84\nbuffer_load_dwordx4 a[144:147], v252, s[20:23], s64 offen sc1\n"
"buffer_load_dwordx4 v220, s[16:19], s63 offen lds\nv_mul_f32 v233, v43, v233\nM 128,176,72\nR 172,186,57344\ns_add_u32 s66, s69, 23081984\nv_cvt_pk_bf16_f32 v40, v230, v231\n"
"M 128,180,88\ns_add_u32 s65, s68, 23196672\nv_cvt_pk_bf16_f32 v41, v232, v233\nv_mul_f32 v234, v44, v44\nv_mul_f32 v235, v45, v45\nM 144,176,76\n"
"R 174,186,61440\nv_mul_f32 v236, v46, v46\nv_mul_f32 v237, v47, v47\nM 144,180,92\nbuffer_load_dwordx4 v221, s[16:19], s63 offen offset:1024 lds\nG 4\n"
"v_fma_f32 v234, v234, v215, v214\nM 148,160,76\nR 176,187,49152\ns_add_u32 m0, s50, 0\nv_fma_f32 v235, v235, v215, v214\nM 148,164,92\n"
"buffer_load_dwordx4 a[148:151], v252, s[20:23], s65 offen sc1\nv_fma_f32 v236, v236, v215, v214\nM 132,160,72\nR 178,187,53248\ns_add_u32 s63, s57, 720896\nv_fma_f32 v237, v237, v215, v214\n"
"M 132,164,88\nbuffer_load_dwordx4 a[152:155], v252, s[20:23], s66 offen sc1\nbuffer_load_dwordx4 v220, s[16:19], s63 offen lds\nv_mul_f32 v234, v44, v234\nM 116,160,68\nR 180,187,57344\n"
"v_mul_f32 v235, v45, v235\nv_mul_f32 v236, v46, v236\nM 116,164,84\ns_add_u32 s67, s69, 23196672\nv_exp_f32 v234, v234\nv_mul_f32 v237, v47, v237\n"
"M 100,160,64\nR 182,187,61440\nv_fma_f32 v234, v234, v216, v216\nv_exp_f32 v235, v235\nM 100,164,80\nbuffer_load_dwordx4 v221, s[16:19], s63 offen offset:1024 lds\n"
"G 4\nv_exp_f32 v236, v236\nM 104,168,64\nR 160,188,0\ns_waitcnt vmcnt(18)\ns_barrier\n"
"v_exp_f32 v237, v237\nM 104,172,80\nbuffer_load_dwordx4 a[156:159], v252, s[20:23], s67 offen sc1\nv_fma_f32 v235, v235, v216, v216\nM 120,168,68\nR 162,188,4096\n"
"v_fma_f32 v236, v236, v216, v216\nv_fma_f32 v237, v237, v216, v216\nM 120,172,84\nv_rcp_f32 v234, v234\nv_rcp_f32 v235, v235\nM 136,168,72\n"
"R 164,188,8192\nv_mul_f32 v234, v44, v234\nv_rcp_f32 v236, v236\nM 136,172,88\nbuffer_store_dwordx4 v[32:35], v252, s[20:23], s84 offen offset:0 sc1\nv_rcp_f32 v237, v237\n"
"M 152,168,76\nR 166,188,12288\nv_mul_f32 v235, v45, v235\nv_mul_f32 v236, v46, v236\nM 152,172,92\nG 4\n"
"v_mul_f32 v237, v47, v237\nv_cvt_pk_bf16_f32 v42, v234, v235\nv_cvt_pk_bf16_f32 v43, v236, v237\nM 156,176,76\nR 168,189,0\nv_mul_f32 v230, v48, v48\n"
"v_mul_f32 v231, v49, v49\nM 156,180,92\ns_add_u32 m0, s50, 8192\nv_mul_f32 v232, v50, v50\nv_mul_f32 v233, v51, v51\nv_fma_f32 v230, v230, v218, v217\n"
"M 140,176,72\nR 170,189,4096\nv_fma_f32 v231, v231, v218, v217\nv_fma_f32 v232, v232, v218, v217\nM 140,180,88\ns_add_u32 s63, s57, 393216\n"
"v_fma_f32 v233, v233, v218, v217\nv_mul_f32 v230, v48, v230\nv_mul_f32 v231, v49, v231\nM 124,176,68\nR 172,189,8192\nv_mul_f32 v232, v50, v232\n"
"v_mul_f32 v233, v51, v233\nM 124,180,84\nbuffer_load_dwordx4 v220, s[16:19], s63 offen lds\nv_exp_f32 v230, v230\nM 108,176,64\nR 174,189,12288\n"
"v_fma_f32 v230, v230, v219, v219\nv_exp_f32 v231, v231\nM 108,180,80\nG 4\nv_exp_f32 v232, v232\nv_exp_f32 v233, v233\n"
"M 96,160,96\nR 176,190,0\nv_fma_f32 v231, v231, v219, v219\nv_fma_f32 v232, v232, v219, v219\nM 96,164,112\nbuffer_load_dwordx4 v221, s[16:19], s63 offen offset:1024 lds\n"
"v_fma_f32 v233, v233, v219, v219\nM 112,160,100\nR 178,190,4096\ns_add_u32 s63, s57, 458752\nv_rcp_f32 v230, v230\ns_waitcnt vmcnt(21)\n"
"M 112,164,116\ns_add_u32 m0, s50, 16384\nv_rcp_f32 v231, v231\nv_rcp_f32 v232, v232\nM 128,160,104\nR 180,190,8192\n"
"v_mul_f32 v230, v48, v230\nv_rcp_f32 v233, v233\ns_waitcnt vmcnt(20)\nM 128,164,120\nbuffer_load_dwordx4 v220, s[16:19], s63 offen lds\nv_mul_f32 v231, v49, v231\n"
"M 144,160,108\nR 182,190,12288\nv_mul_f32 v232, v50, v232\nv_mul_f32 v233, v51, v233\ns_waitcnt vmcnt(20)\nM 144,164,124\n"
"G 4\nv_cvt_pk_bf16_f32 v48, v230, v231\nv_cvt_pk_bf16_f32 v49, v232, v233\nv_mul_f32 v234, v52, v52\nM 148,168,108\nR 160,191,0\n"
"v_mul_f32 v235, v53, v53\nv_mul_f32 v236, v54, v54\nM 148,172,124\nv_mul_f32 v237, v55, v55\nv_fma_f32 v234, v234, v218, v217\nv_fma_f32 v235, v235, v218, v217\n"
"M 132,168,104\nR 162,191,4096\nv_fma_f32 v236, v236, v218, v217\nv_fma_f32 v237, v237, v218, v217\nM 132,172,120\nbuffer_load_dwordx4 v221, s[16:19], s63 offen offset:1024 lds\n"
"v_mul_f32 v234, v52, v234\nM 116,168,100\nR 164,191,8192\ns_add_u32 s63, s58, 0\nv_mul_f32 v235, v53, v235\nM 116,172,116\n"
"s_add_u32 m0, s50, 24576\nv_exp_f32 v234, v234\nv_mul_f32 v236, v54, v236\nM 100,168,96\nR 166,191,12288\nv_mul_f32 v237, v55, v237\n"
"v_exp_f32 v235, v235\nM 100,172,112\nG 4\nv_exp_f32 v236, v236\nv_exp_f32 v237, v237\nM 104,176,96\n"
"R 168,188,16384\nv_fma_f32 v234, v234, v219, v219\nv_fma_f32 v235, v235, v219, v219\nM 104,180,112\nbuffer_load_dwordx4 v220, s[16:19], s63 offen lds\nv_fma_f32 v236, v236, v219, v219\n"
"M 120,176,100\nR 170,188,20480\nv_fma_f32 v237, v237, v219, v219\nv_rcp_f32 v234, v234\nM 120,180,116\nv_rcp_f32 v235, v235\n"
"v_rcp_f32 v236, v236\nM 136,176,104\nR 172,188,24576\nv_mul_f32 v234, v52, v234\nv_rcp_f32 v237, v237\nM 136,180,120\n"
"buffer_load_dwordx4 v221, s[16:19], s63 offen offset:1024 lds\nv_mul_f32 v235, v53, v235\nM 152,176,108\nR 174,188,28672\ns_add_u32 m0, s50, 32768\nv_mul_f32 v236, v54, v236\n"
"M 152,180,124\nG 4\nv_mul_f32 v237, v55, v237\nv_cvt_pk_bf16_f32 v50, v234, v235\nv_cvt_pk_bf16_f32 v51, v236, v237\nM 156,160,108\n"
"R 176,189,16384\ns_add_u32 s63, s58, 524288\nv_mul_f32 v230, v56, v56\nM 156,164,124\nbuffer_load_dwordx4 v220, s[16:19], s63 offen lds\nv_mul_f32 v231, v57, v57\n"
"M 140,160,104\nR 178,189,20480\nv_mul_f32 v232, v58, v58\nv_mul_f32 v233, v59, v59\nM 140,164,120\nv_fma_f32 v230, v230, v218, v217\n"
"v_fma_f32 v231, v231, v218, v217\nv_fma_f32 v232, v232, v218, v217\nM 124,160,100\nR 180,189,24576\nv_fma_f32 v233, v233, v218, v217\nv_mul_f32 v230, v56, v230\n"
"M 124,164,116\nbuffer_load_dwordx4 v221, s[16:19], s63 offen offset:1024 lds\nv_mul_f32 v231, v57, v231\nM 108,160,96\nR 182,189,28672\ns_add_u32 s63, s58, 65536\n"
"v_mul_f32 v232, v58, v232\nM 108,164,112\ns_waitcnt vmcnt(10)\ns_barrier\nG 4\nM 96,168,128\n"
"R 160,190,16384\nv_mul_f32 v233, v59, v233\nM 96,172,144\ns_add_u32 m0, s50, 40960\nv_exp_f32 v230, v230\nM 112,168,132\n"
"R 162,190,20480\nv_fma_f32 v230, v230, v219, v219\nM 112,172,148\nbuffer_load_dwordx4 v220, s[16:19], s63 offen lds\nM 128,168,136\nR 164,190,24576\n"
"v_mul_f32 v234, v60, v60\nM 128,172,152\nv_exp_f32 v231, v231\nM 144,168,140\nR 166,190,28672\nv_fma_f32 v231, v231, v219, v219\n"
"s_waitcnt vmcnt(10)\nM 144,172,156\nG 4\nv_exp_f32 v232, v232\nM 148,176,140\nR 168,191,16384\n"
"v_fma_f32 v232, v232, v219, v219\nM 148,180,156\nbuffer_load_dwordx4 v221, s[16:19], s63 offen offset:1024 lds\nM 132,176,136\nR 170,191,20480\nv_mul_f32 v235, v61, v61\n"
"M 132,180,152\ns_add_u32 m0, s50, 49152\nv_exp_f32 v233, v233\nM 116,176,132\nR 172,191,24576\nv_fma_f32 v233, v233, v219, v219\n"
"M 116,180,148\ns_add_u32 s63, s58, 589824\nv_rcp_f32 v230, v230\nM 100,176,128\nR 174,191,28672\nv_mul_f32 v230, v56, v230\n"
"M 100,180,144\nG 4\nv_rcp_f32 v231, v231\nM 104,160,128\nR 176,188,32768\nv_mul_f32 v231, v57, v231\n"
"M 104,164,144\nbuffer_load_dwordx4 v220, s[16:19], s63 offen lds\nM 120,160,132\nR 178,188,36864\nv_cvt_pk_bf16_f32 v56, v230, v231\nM 120,164,148\n"
"v_rcp_f32 v232, v232\nM 136,160,136\nR 180,188,40960\nv_mul_f32 v232, v58, v232\nM 136,164,152\nbuffer_store_dwordx4 v[40:43], v252, s[20:23], s84 offen offset:1024 sc1\n"
"M 152,160,140\nR 182,188,45056\nv_mul_f32 v236, v62, v62\nM 152,164,156\nG 4\nv_rcp_f32 v233, v233\n"
"M 156,168,140\nR 160,189,32768\nv_mul_f32 v233, v59, v233\nM 156,172,156\nbuffer_load_dwordx4 v221, s[16:19], s63 offen offset:1024 lds\nM 140,168,136\n"
"R 162,189,36864\nv_cvt_pk_bf16_f32 v57, v232, v233\nM 140,172,152\ns_add_u32 m0, s50, 57344\nv_mul_f32 v237, v63, v63\nv_fma_f32 v234, v234, v218, v217\n"
"M 124,168,132\nR 164,189,40960\nv_fma_f32 v235, v235, v218, v217\nM 124,172,148\ns_add_u32 s63, s58, 131072\nv_fma_f32 v236, v236, v218, v217\n"
"v_fma_f32 v237, v237, v218, v217\nM 108,168,128\nR 166,189,45056\nv_mul_f32 v234, v60, v234\nM 108,172,144\nG 4\n"
"v_exp_f32 v234, v234\nM 0,176,64\nR 168,190,32768\nv_mul_f32 v235, v61, v235\nM 0,180,80\nbuffer_load_dwordx4 v220, s[16:19], s63 offen lds\n"
"M 16,176,68\nR 170,190,36864\nv_mul_f32 v236, v62, v236\nM 16,180,84\nv_exp_f32 v235, v235\nv_mfma_f32_16x16x32_bf16 a[224:227], v[176:179], a[72:75], a[224:227]\n"
"R 172,190,40960\nv_mul_f32 v237, v63, v237\nv_mfma_f32_16x16x32_bf16 a[224:227], v[180:183], a[88:91], a[224:227]\nbuffer_store_dwordx4 v[48:51], v252, s[20:23], s85 offen offset:0 sc1\nv_mfma_f32_16x16x32_bf16 a[240:243], v[176:179], a[76:79], a[240:243]\nR 174,190,45056\n"
"v_fma_f32 v234, v234, v219, v219\nv_mfma_f32_16x16x32_bf16 a[240:243], v[180:183], a[92:95], a[240:243]\nG 4\nv_exp_f32 v236, v236\nv_mfma_f32_16x16x32_bf16 a[244:247], v[160:163], a[76:79], a[244:247]\nR 176,191,32768\n"
"v_fma_f32 v235, v235, v219, v219\nv_mfma_f32_16x16x32_bf16 a[244:247], v[164:167], a[92:95], a[244:247]\nbuffer_load_dwordx4 v221, s[16:19], s63 offen offset:1024 lds\nv_mfma_f32_16x16x32_bf16 a[228:231], v[160:163], a[72:75], a[228:231]\nR 178,191,36864\nv_fma_f32 v236, v236, v219, v219\n"
"v_mfma_f32_16x16x32_bf16 a[228:231], v[164:167], a[88:91], a[228:231]\ns_add_u32 m0, s50, 65536\nv_exp_f32 v237, v237\nM 20,160,68\nR 180,191,40960\nv_fma_f32 v237, v237, v219, v219\n"
"M 20,164,84\ns_add_u32 s63, s58, 655360\nv_rcp_f32 v234, v234\nM 4,160,64\nR 182,191,45056\nv_mul_f32 v234, v60, v234\n"
"M 4,164,80\nG 4\nv_rcp_f32 v235, v235\nM 8,168,64\nR 160,188,49152\nv_mul_f32 v235, v61, v235\n"
"M 8,172,80\nbuffer_load_dwordx4 v220, s[16:19], s63 offen lds\nM 24,168,68\nR 162,188,53248\nv_cvt_pk_bf16_f32 v58, v234, v235\nM 24,172,84\n"
"v_rcp_f32 v236, v236\nv_mfma_f32_16x16x32_bf16 a[232:235], v[168:171], a[72:75], a[232:235]\nR 164,184,0\nv_mul_f32 v236, v62, v236\nv_mfma_f32_16x16x32_bf16 a[232:235], v[172:175], a[88:91], a[232:235]\nbuffer_load_dwordx4 v221, s[16:19], s63 offen offset:1024 lds\n"
"v_mfma_f32_16x16x32_bf16 a[248:251], v[168:171], a[76:79], a[248:251]\nR 166,184,4096\ns_add_u32 m0, s50, 73728\nv_mfma_f32_16x16x32_bf16 a[248:251], v[172:175], a[92:95], a[248:251]\nG 4\nv_rcp_f32 v237, v237\n"
"v_mfma_f32_16x16x32_bf16 a[252:255], v[176:179], a[76:79], a[252:255]\nR 168,189,49152\ns_add_u32 s63, s58, 196608\nv_mfma_f32_16x16x32_bf16 a[252:255], v[180:183], a[92:95], a[252:255]\nbuffer_load_dwordx4 v220, s[16:19], s63 offen lds\nv_mfma_f32_16x16x32_bf16 a[236:239], v[176:179], a[72:75], a[236:239]\n"
"R 170,189,53248\nv_mul_f32 v237, v63, v237\nv_mfma_f32_16x16x32_bf16 a[236:239], v[180:183], a[88:91], a[236:239]\nv_cvt_pk_bf16_f32 v59, v236, v237\nbuffer_store_dwordx4 v[56:59], v252, s[20:23], s85 offen offset:1024 sc1\nM 28,176,68\n"
"R 172,185,0\nM 28,180,84\nbuffer_load_dwordx4 v221, s[16:19], s63 offen offset:1024 lds\nM 12,176,64\nR 174,185,4096\nM 12,180,80\n"
"s_waitcnt vmcnt(13)\ns_barrier\nG 4\nM 0,160,96\nR 176,190,49152\nM 0,164,112\n"
"M 16,160,100\nR 178,190,53248\nM 16,164,116\nv_mfma_f32_16x16x32_bf16 a[224:227], v[160:163], a[104:107], a[224:227]\nR 180,186,0\nv_mfma_f32_16x16x32_bf16 a[224:227], v[164:167], a[120:123], a[224:227]\n"
"v_mfma_f32_16x16x32_bf16 a[240:243], v[160:163], a[108:111], a[240:243]\nR 182,186,4096\nv_mfma_f32_16x16x32_bf16 a[240:243], v[164:167], a[124:127], a[240:243]\nG 4\nv_mfma_f32_16x16x32_bf16 a[244:247], v[168:171], a[108:111], a[244:247]\nR 160,191,49152\n"
"v_mfma_f32_16x16x32_bf16 a[244:247], v[172:175], a[124:127], a[244:247]\nv_mfma_f32_16x16x32_bf16 a[228:231], v[168:171], a[104:107], a[228:231]\nR 162,191,53248\nv_mfma_f32_16x16x32_bf16 a[228:231], v[172:175], a[120:123], a[228:231]\nM 20,168,100\nR 164,187,0\n"
"M 20,172,116\nM 4,168,96\nR 166,187,4096\nM 4,172,112\nG 4\nM 8,176,96\n"
"R 168,184,8192\nM 8,180,112\nM 24,176,100\nR 170,184,12288\nM 24,180,116\nv_mfma_f32_16x16x32_bf16 a[232:235], v[176:179], a[104:107], a[232:235]\n"
"R 172,184,16384\nv_mfma_f32_16x16x32_bf16 a[232:235], v[180:183], a[120:123], a[232:235]\nv_mfma_f32_16x16x32_bf16 a[248:251], v[176:179], a[108:111], a[248:251]\nR 174,184,20480\nv_mfma_f32_16x16x32_bf16 a[248:251], v[180:183], a[124:127], a[248:251]\nG 4\n"
"v_mfma_f32_16x16x32_bf16 a[252:255], v[160:163], a[108:111], a[252:255]\nR 176,185,8192\nv_mfma_f32_16x16x32_bf16 a[252:255], v[164:167], a[124:127], a[252:255]\nv_mfma_f32_16x16x32_bf16 a[236:239], v[160:163], a[104:107], a[236:239]\nR 178,185,12288\nv_mfma_f32_16x16x32_bf16 a[236:239], v[164:167], a[120:123], a[236:239]\n"
"M 28,160,100\nR 180,185,16384\nM 28,164,116\nM 12,160,96\nR 182,185,20480\nM 12,164,112\n"
"G 4\nM 0,168,128\nR 160,186,8192\nM 0,172,144\nM 16,168,132\nR 162,186,12288\n"
"M 16,172,148\nv_mfma_f32_16x16x32_bf16 a[224:227], v[168:171], a[136:139], a[224:227]\nR 164,186,16384\nv_mfma_f32_16x16x32_bf16 a[224:227], v[172:175], a[152:155], a[224:227]\nv_mfma_f32_16x16x32_bf16 a[240:243], v[168:171], a[140:143], a[240:243]\nR 166,186,20480\n"
"v_mfma_f32_16x16x32_bf16 a[240:243], v[172:175], a[156:159], a[240:243]\nG 4\nv_mfma_f32_16x16x32_bf16 a[244:247], v[176:179], a[140:143], a[244:247]\nR 168,187,8192\nv_mfma_f32_16x16x32_bf16 a[244:247], v[180:183], a[156:159], a[244:247]\nv_mfma_f32_16x16x32_bf16 a[228:231], v[176:179], a[136:139], a[228:231]\n"
"R 170,187,12288\nv_mov_b32 v226, s46\nv_mfma_f32_16x16x32_bf16 a[228:231], v[180:183], a[152:155], a[228:231]\ns_add_u32 m0, s50, 81920\nv_mov_b32 v227, s47\nM 20,176,132\n"
"R 172,187,16384\ns_add_u32 s61, s81, 385040\ns_lshl_b32 s62, s44, 5\ns_add_u32 s61, s61, s62\ns_waitcnt vmcnt(0)\ns_mov_b64 exec, 1\n"
"buffer_atomic_swap_x2 v[226:227], off, s[20:23], s61 sc1\ns_mov_b64 exec, -1\nM 20,180,148\ns_add_u32 s63, s58, 720896\nM 4,176,128\nR 174,187,20480\n"
"M 4,180,144\nG 4\nM 8,160,128\nR 176,184,24576\nM 8,164,144\nbuffer_load_dwordx4 v220, s[16:19], s63 offen lds\n"
"M 24,160,132\nR 178,184,28672\nM 24,164,148\nv_mfma_f32_16x16x32_bf16 a[232:235], v[160:163], a[136:139], a[232:235]\nR 180,184,32768\nv_mfma_f32_16x16x32_bf16 a[232:235], v[164:167], a[152:155], a[232:235]\n"
"buffer_load_dwordx4 v221, s[16:19], s63 offen offset:1024 lds\nv_mfma_f32_16x16x32_bf16 a[248:251], v[160:163], a[140:143], a[248:251]\nR 182,184,36864\ns_add_u32 m0, s50, 90112\nv_mfma_f32_16x16x32_bf16 a[248:251], v[164:167], a[156:159], a[248:251]\nG 4\n"
"v_mfma_f32_16x16x32_bf16 a[252:255], v[168:171], a[140:143], a[252:255]\nR 160,185,24576\ns_add_u32 s63, s58, 393216\nv_mfma_f32_16x16x32_bf16 a[252:255], v[172:175], a[156:159], a[252:255]\nbuffer_load_dwordx4 v220, s[16:19], s63 offen lds\nv_mfma_f32_16x16x32_bf16 a[236:239], v[168:171], a[136:139], a[236:239]\n"
"R 162,185,28672\nv_mfma_f32_16x16x32_bf16 a[236:239], v[172:175], a[152:155], a[236:239]\nM 28,168,132\nR 164,185,32768\nM 28,172,148\nbuffer_load_dwordx4 v221, s[16:19], s63 offen offset:1024 lds\n"
"M 12,168,128\nR 166,185,36864\ns_add_u32 s63, s58, 458752\nM 12,172,144\ns_barrier\nG 4\n"
"v_mfma_f32_16x16x32_bf16 v[32:35], v[176:179], a[0:3], 0\nR 168,186,24576\nM 32,180,16\ns_add_u32 m0, s50, 98304\nv_mfma_f32_16x16x32_bf16 v[48:51], v[176:179], a[4:7], 0\nR 170,186,28672\n"
"M 48,180,20\nbuffer_load_dwordx4 v220, s[16:19], s63 offen lds\nv_mfma_f32_16x16x32_bf16 v[64:67], v[176:179], a[8:11], 0\nR 172,186,32768\nM 64,180,24\nv_mfma_f32_16x16x32_bf16 v[80:83], v[176:179], a[12:15], 0\n"
"R 174,186,36864\nM 80,180,28\nG 4\nv_mfma_f32_16x16x32_bf16 v[84:87], v[160:163], a[12:15], 0\nR 176,187,24576\nM 84,164,28\n"
"buffer_load_dwordx4 v221, s[16:19], s63 offen offset:1024 lds\nv_mfma_f32_16x16x32_bf16 v[68:71], v[160:163], a[8:11], 0\nR 178,187,28672\nM 68,164,24\nv_mfma_f32_16x16x32_bf16 v[52:55], v[160:163], a[4:7], 0\nR 180,187,32768\n"
"M 52,164,20\ns_add_u32 m0, s50, 106496\nv_mfma_f32_16x16x32_bf16 v[36:39], v[160:163], a[0:3], 0\nR 182,187,36864\nv_mov_b32 v248, 0\nM 36,164,16\n"
"G 4\nv_mov_b32 v249, 0\nv_mfma_f32_16x16x32_bf16 v[40:43], v[168:171], a[0:3], 0\nR 160,184,40960\ns_add_u32 s61, s82, 385040\ns_mov_b64 exec, 0xf\n"
"buffer_atomic_or_x2 v[248:249], v250, s[20:23], s61 offen sc0 sc1\ns_mov_b64 exec, -1\nM 40,172,16\ns_add_u32 s63, s56, 262144\nv_mfma_f32_16x16x32_bf16 v[56:59], v[168:171], a[4:7], 0\nR 162,184,45056\n"
"M 56,172,20\nbuffer_load_dwordx4 v220, s[16:19], s63 offen lds\nv_mfma_f32_16x16x32_bf16 v[72:75], v[168:171], a[8:11], 0\nR 164,184,49152\nM 72,172,24\nv_mfma_f32_16x16x32_bf16 v[88:91], v[168:171], a[12:15], 0\n"
"R 166,184,53248\nM 88,172,28\nG 4\nv_mfma_f32_16x16x32_bf16 v[92:95], v[176:179], a[12:15], 0\nR 168,185,40960\nM 92,180,28\n"
"buffer_load_dwordx4 v221, s[16:19], s63 offen offset:1024 lds\nv_mfma_f32_16x16x32_bf16 v[76:79], v[176:179], a[8:11], 0\nR 170,185,45056\ns_add_u32 s63, s56, 786432\nM 76,180,24\ns_add_u32 m0, s50, 114688\n"
"v_mfma_f32_16x16x32_bf16 v[60:63], v[176:179], a[4:7], 0\nR 172,185,49152\nM 60,180,20\nbuffer_load_dwordx4 v220, s[16:19], s63 offen lds\nv_mfma_f32_16x16x32_bf16 v[44:47], v[176:179], a[0:3], 0\nR 174,185,53248\n"
"M 44,180,16\nG 4\nM 32,160,32\nR 176,186,40960\nM 32,164,48\nbuffer_load_dwordx4 v221, s[16:19], s63 offen offset:1024 lds\n"
"M 48,160,36\nR 178,186,45056\ns_add_u32 s63, s56, 327680\nM 48,164,52\ns_add_u32 m0, s50, 0\nM 64,160,40\n"
"R 180,186,49152\nM 64,164,56\nbuffer_load_dwordx4 v220, s[16:19], s63 offen lds\nM 80,160,44\nR 182,186,53248\nM 80,164,60\n"
"G 4\nM 84,168,44\nR 160,187,40960\nM 84,172,60\nbuffer_load_dwordx4 v221, s[16:19], s63 offen offset:1024 lds\nM 68,168,40\n"
"R 162,187,45056\nM 68,172,56\nM 52,168,36\nR 164,187,49152\nM 52,172,52\ns_add_u32 m0, s50, 8192\n"
"M 36,168,32\nR 166,187,53248\ns_add_u32 s63, s56, 851968\nM 36,172,48\nG 4\nM 40,176,32\n"
"R 168,184,57344\nM 40,180,48\ns_add_u32 s64, s68, 23072768\nM 56,176,36\nR 170,184,61440\ns_waitcnt vmcnt(6)\n"
"v_cmp_eq_u64 s[86:87], s[46:47], v[248:249]\nM 56,180,52\nbuffer_load_dwordx4 v220, s[16:19], s63 offen lds\nM 72,176,40\nR 172,188,0\ns_and_b32 s62, s86, 0xf\n"
"s_cmp_eq_u32 s62, 0xf\ns_cbranch_scc0 Lq456\nLq457:\nM 72,180,56\ns_add_u32 s65, s68, 23187456\nM 88,176,44\n"
"R 174,188,4096\ns_barrier\nM 88,180,60\nG 4\nM 92,160,44\nR 176,185,57344\n"
"M 92,164,60\nbuffer_load_dwordx4 a[160:163], v252, s[20:23], s64 offen sc1\nM 76,160,40\nR 178,185,61440\nM 76,164,56\ns_add_u32 s66, s69, 23072768\n"
"M 60,160,36\nR 180,189,0\nM 60,164,52\nbuffer_load_dwordx4 a[164:167], v252, s[20:23], s65 offen sc1\nM 44,160,32\nR 182,189,4096\n"
"M 44,164,48\nG 4\nM 32,168,64\nR 160,186,57344\nM 32,172,80\ns_add_u32 s67, s69, 23187456\n"
"M 48,168,68\nR 162,186,61440\nM 48,172,84\nbuffer_load_dwordx4 a[168:171], v252, s[20:23], s66 offen sc1\nM 64,168,72\nR 164,190,0\n"
"M 64,172,88\nM 80,168,76\nR 166,190,4096\nM 80,172,92\nG 4\nM 84,176,76\n"
"R 168,187,57344\nM 84,180,92\nbuffer_load_dwordx4 a[172:175], v252, s[20:23], s67 offen sc1\nM 68,176,72\nR 170,187,61440\nM 68,180,88\n"
"s_add_u32 s64, s68, 23079936\nM 52,176,68\nR 172,191,0\nM 52,180,84\ns_add_u32 s65, s68, 23194624\nM 36,176,64\n"
"R 174,191,4096\nM 36,180,80\nbuffer_load_dwordx4 v221, s[16:19], s63 offen offset:1024 lds\nG 4\nM 40,160,64\nR 176,188,8192\n"
"s_add_u32 m0, s50, 16384\nM 40,164,80\nbuffer_load_dwordx4 a[176:179], v252, s[20:23], s64 offen sc1\nM 56,160,68\nR 178,188,12288\ns_add_u32 s63, s57, 262144\n"
"M 56,164,84\ns_add_u32 s66, s69, 23079936\nbuffer_load_dwordx4 v220, s[16:19], s63 offen lds\nM 72,160,72\nR 180,188,16384\nLq572:\n"
"s_lshr_b32 s60, s45, 3\ns_lshl2_add_u32 s60, s60, s40\ns_lshl_b32 s60, s60, 8\nv_lshl_add_u32 v224, v252, 3, s60\nv_add_u32 v224, Lq572+4096-LqPC, v224\ns_mov_b64 exec, 3\n"
"global_load_dword v224, v224, s[98:99]\ns_mov_b64 exec, -1\nM 72,164,88\nbuffer_load_dwordx4 a[180:183], v252, s[20:23], s65 offen sc1\nM 88,160,76\nR 182,188,20480\n"
"M 88,164,92\nbuffer_load_dwordx4 v221, s[16:19], s63 offen offset:1024 lds\nG 4\nM 92,168,76\nR 160,189,8192\ns_add_u32 m0, s50, 24576\n"
"M 92,172,92\ns_add_u32 s67, s69, 23194624\nM 76,168,72\nR 162,189,12288\ns_add_u32 s63, s57, 786432\nM 76,172,88\n"
"buffer_load_dwordx4 a[184:187], v252, s[20:23], s66 offen sc1\nbuffer_load_dwordx4 v220, s[16:19], s63 offen lds\nM 60,168,68\nR 164,189,16384\nM 60,172,84\nbuffer_load_dwordx4 v221, s[16:19], s63 offen offset:1024 lds\n"
"M 44,168,64\nR 166,189,20480\ns_add_u32 m0, s50, 32768\nM 44,172,80\nG 4\nM 32,176,96\n"
"R 168,190,8192\ns_add_u32 s63, s57, 327680\nM 32,180,112\nbuffer_load_dwordx4 a[188:191], v252, s[20:23], s67 offen sc1\nM 48,176,100\nR 170,190,12288\n"
"M 48,180,116\ns_add_u32 s64, s68, 23073792\nbuffer_load_dwordx4 v220, s[16:19], s63 offen lds\nM 64,176,104\nR 172,190,16384\nM 64,180,120\n"
"buffer_load_dwordx4 v221, s[16:19], s63 offen offset:1024 lds\nM 80,176,108\nR 174,190,20480\ns_add_u32 m0, s50, 40960\nM 80,180,124\nG 4\n"
"M 84,160,108\nR 176,191,8192\ns_waitcnt vmcnt(19)\ns_barrier\nM 84,164,124\nbuffer_load_dwordx4 a[192:195], v252, s[20:23], s64 offen sc1\n"
"M 68,160,104\nR 178,191,12288\ns_add_u32 s63, s57, 851968\nM 68,164,120\ns_add_u32 s65, s68, 23188480\nM 52,160,100\n"
"R 180,191,16384\nbuffer_load_dwordx4 v220, s[16:19], s63 offen lds\nM 52,164,116\ns_add_u32 s66, s69, 23073792\nM 36,160,96\nR 182,191,20480\n"
"M 36,164,112\nG 4\nM 40,168,96\nR 160,188,24576\nbuffer_load_dwordx4 v221, s[16:19], s63 offen offset:1024 lds\nM 40,172,112\n"
"buffer_load_dwordx4 a[196:199], v252, s[20:23], s65 offen sc1\nM 56,168,100\nR 162,188,28672\nM 56,172,116\ns_add_u32 s67, s69, 23188480\nM 72,168,104\n"
"R 164,188,32768\nM 72,172,120\nbuffer_load_dwordx4 a[200:203], v252, s[20:23], s66 offen sc1\nM 88,168,108\nR 166,188,36864\nM 88,172,124\n"
"G 4\nM 92,176,108\nR 168,189,24576\nM 92,180,124\ns_add_u32 s64, s68, 23080960\nM 76,176,104\n"
"R 170,189,28672\nM 76,180,120\nbuffer_load_dwordx4 a[204:207], v252, s[20:23], s67 offen sc1\nM 60,176,100\nR 172,189,32768\nM 60,180,116\n"
"M 44,176,96\nR 174,189,36864\nM 44,180,112\nG 4\nM 32,160,128\nR 176,190,24576\n"
"M 32,164,144\nbuffer_load_dwordx4 a[208:211], v252, s[20:23], s64 offen sc1\nM 48,160,132\nR 178,190,28672\nM 48,164,148\ns_add_u32 s65, s68, 23195648\n"
"M 64,160,136\nR 180,190,32768\nM 64,164,152\ns_add_u32 s66, s69, 23080960\nM 80,160,140\nR 182,190,36864\n"
"M 80,164,156\nG 4\nM 84,168,140\nR 160,191,24576\nM 84,172,156\nbuffer_load_dwordx4 a[212:215], v252, s[20:23], s65 offen sc1\n"
"M 68,168,136\nR 162,191,28672\nM 68,172,152\ns_add_u32 s67, s69, 23195648\nM 52,168,132\nR 164,191,32768\n"
"M 52,172,148\nbuffer_load_dwordx4 a[216:219], v252, s[20:23], s66 offen sc1\nM 36,168,128\nR 166,191,36864\nM 36,172,144\nG 4\n"
"M 40,176,128\nR 168,188,40960\nM 40,180,144\nM 56,176,132\nR 170,188,45056\nM 56,180,148\n"
"buffer_load_dwordx4 a[220:223], v252, s[20:23], s67 offen sc1\nM 72,176,136\nR 172,188,49152\ns_add_u32 s63, s58, 262144\nM 72,180,152\ns_add_u32 m0, s50, 49152\n"
"M 88,176,140\nR 174,188,53248\nM 88,180,156\nG 4\nM 92,160,140\nR 176,189,40960\n"
"M 92,164,156\nbuffer_load_dwordx4 v220, s[16:19], s63 offen lds\nM 76,160,136\nR 178,189,45056\nM 76,164,152\nM 60,160,132\n"
"R 180,189,49152\nM 60,164,148\nbuffer_load_dwordx4 v221, s[16:19], s63 offen offset:1024 lds\nM 44,160,128\nR 182,189,53248\nM 44,164,144\n"
"s_waitcnt vmcnt(9)\ns_barrier\nG 4\nM 96,168,160\nR 160,190,40960\nM 96,172,176\n"
"s_add_u32 m0, s50, 57344\nM 112,168,164\nR 162,190,45056\nM 112,172,180\ns_add_u32 s63, s58, 786432\nM 128,168,168\n"
"R 164,190,49152\nv_lshrrev_b32 v230, 8, v252\nM 128,172,184\nbuffer_load_dwordx4 v220, s[16:19], s63 offen lds\nM 144,168,172\nR 166,190,53248\n"
"v_lshlrev_b32 v230, 3, v230\nM 144,172,188\nG 4\nM 148,176,172\nR 168,191,40960\nM 148,180,188\n"
"buffer_load_dwordx2 v[232:233], v230, s[36:39], s53 offen offset:0\nM 132,176,168\nR 170,191,45056\nM 132,180,184\nbuffer_load_dwordx2 v[234:235], v230, s[36:39], s53 offen offset:32\nM 116,176,164\n"
"R 172,191,49152\nM 116,180,180\nbuffer_load_dwordx4 v221, s[16:19], s63 offen offset:1024 lds\nM 100,176,160\nR 174,191,53248\nM 100,180,176\n"
"G 4\nM 104,160,160\nR 176,184,0\nM 104,164,176\ns_add_u32 m0, s50, 65536\nM 120,160,164\n"
"R 178,184,4096\nM 120,164,180\ns_add_u32 s63, s58, 327680\nM 136,160,168\nR 180,184,8192\nbuffer_load_dwordx2 v[236:237], v230, s[36:39], s53 offen offset:64\n"
"M 136,164,184\nbuffer_load_dwordx4 v220, s[16:19], s63 offen lds\nM 152,160,172\nR 182,184,12288\nM 152,164,188\nbuffer_load_dwordx2 v[238:239], v230, s[36:39], s53 offen offset:96\n"
"G 4\nM 156,168,172\nR 160,185,0\nv_bfe_u32 v242, v252, 4, 4\nM 156,172,188\nv_lshrrev_b32 v243, 8, v252\n"
"v_lshlrev_b32 v243, 2, v243\nM 140,168,168\nR 162,185,4096\nv_sub_u32 v242, v242, v243\nM 140,172,184\nv_lshlrev_b32 v243, 4, v242\n"
"s_mov_b64 s[60:61], 0x3f80\nv_lshlrev_b64 v[208:209], v243, s[60:61]\nM 124,168,164\nR 164,185,8192\nM 124,172,180\nbuffer_load_dwordx4 v221, s[16:19], s63 offen offset:1024 lds\n"
"M 108,168,160\nR 166,185,12288\nM 108,172,176\nG 4\nM 96,176,192\nR 168,186,0\n"
"s_waitcnt vmcnt(13)\nM 96,180,208\ns_add_u32 m0, s50, 73728\nM 112,176,196\nR 170,186,4096\ns_waitcnt vmcnt(12)\n"
"M 112,180,212\ns_add_u32 s63, s58, 851968\nM 128,176,200\nR 172,186,8192\ns_waitcnt vmcnt(11)\nM 128,180,216\n"
"buffer_load_dwordx4 v220, s[16:19], s63 offen lds\nM 144,176,204\nR 174,186,12288\ns_waitcnt vmcnt(11)\nM 144,180,220\nG 4\n"
"M 148,160,204\nR 176,187,0\nM 148,164,220\nv_cmp_gt_u32 s[60:61], 4, v242\ns_nop 1\nv_cndmask_b32 v212, 0, v208, s[60:61]\n"
"v_cndmask_b32 v208, 0, v208, s[60:61]\nv_cndmask_b32 v213, 0, v209, s[60:61]\nv_cndmask_b32 v209, 0, v209, s[60:61]\nM 132,160,200\nR 178,187,4096\nM 132,164,216\n"
"M 116,160,196\nR 180,187,8192\nM 116,164,212\nbuffer_load_dwordx4 v221, s[16:19], s63 offen offset:1024 lds\nM 100,160,192\nR 182,187,12288\n"
"M 100,164,208\nG 4\nM 104,168,192\nR 160,184,16384\nM 104,172,208\ns_add_u32 m0, s50, 81920\n"
"M 120,168,196\nR 162,184,20480\nM 120,172,212\ns_add_u32 s63, s59, 0\nM 136,168,200\nR 164,184,24576\n"
"M 136,172,216\nbuffer_load_dwordx4 v220, s[16:19], s63 offen lds\nM 152,168,204\nR 166,184,28672\nM 152,172,220\nG 4\n"
"M 156,176,204\nR 168,185,16384\nM 156,180,220\nM 140,176,200\nR 170,185,20480\nM 140,180,216\n"
"buffer_load_dwordx4 v221, s[16:19], s63 offen offset:1024 lds\nM 124,176,196\nR 172,185,24576\nM 124,180,212\nM 108,176,192\nR 174,185,28672\n"
"M 108,180,208\nG 4\nM 0,160,160\nR 176,186,16384\nM 0,164,176\ns_mov_b32 m0, s51\n"
"M 16,160,164\nR 178,186,20480\nds_write_addtid_b32 v128 offset:0\nM 16,164,180\nds_write_addtid_b32 v129 offset:256\nds_write_addtid_b32 v130 offset:512\n"
"v_mfma_f32_16x16x32_bf16 a[224:227], v[160:163], a[168:171], a[224:227]\nR 180,186,24576\nds_write_addtid_b32 v131 offset:768\nv_mfma_f32_16x16x32_bf16 a[224:227], v[164:167], a[184:187], a[224:227]\nds_write_addtid_b32 v132 offset:1024\nds_write_addtid_b32 v133 offset:1280\n"
"v_mfma_f32_16x16x32_bf16 a[240:243], v[160:163], a[172:175], a[240:243]\nR 182,186,28672\nds_write_addtid_b32 v134 offset:1536\nv_mfma_f32_16x16x32_bf16 a[240:243], v[164:167], a[188:191], a[240:243]\nds_write_addtid_b32 v135 offset:1792\nds_write_addtid_b32 v136 offset:2048\n"
"G 13\nv_mfma_f32_16x16x32_bf16 a[244:247], v[168:171], a[172:175], a[244:247]\nR 160,187,16384\nds_write_addtid_b32 v137 offset:2304\nv_mfma_f32_16x16x32_bf16 a[244:247], v[172:175], a[188:191], a[244:247]\nds_write_addtid_b32 v138 offset:2560\n"
"ds_write_addtid_b32 v139 offset:2816\nv_mfma_f32_16x16x32_bf16 a[228:231], v[168:171], a[168:171], a[228:231]\nR 162,187,20480\nds_write_addtid_b32 v140 offset:3072\nv_mfma_f32_16x16x32_bf16 a[228:231], v[172:175], a[184:187], a[228:231]\nds_write_addtid_b32 v141 offset:3328\n"
"ds_write_addtid_b32 v142 offset:3584\nM 20,168,164\nR 164,187,24576\nds_write_addtid_b32 v143 offset:3840\nM 20,172,180\nds_write_addtid_b32 v144 offset:4096\n"
"ds_write_addtid_b32 v145 offset:4352\nM 4,168,160\nR 166,187,28672\nds_write_addtid_b32 v146 offset:4608\nM 4,172,176\nds_write_addtid_b32 v147 offset:4864\n"
"ds_write_addtid_b32 v148 offset:5120\nG 15\nM 8,176,160\nR 168,184,32768\nds_write_addtid_b32 v149 offset:5376\nM 8,180,176\n"
"ds_write_addtid_b32 v150 offset:5632\nds_write_addtid_b32 v151 offset:5888\nM 24,176,164\nR 170,184,36864\nds_write_addtid_b32 v152 offset:6144\nM 24,180,180\n"
"ds_write_addtid_b32 v153 offset:6400\nds_write_addtid_b32 v154 offset:6656\nv_mfma_f32_16x16x32_bf16 a[232:235], v[176:179], a[168:171], a[232:235]\nR 172,184,40960\nds_write_addtid_b32 v155 offset:6912\nv_mfma_f32_16x16x32_bf16 a[232:235], v[180:183], a[184:187], a[232:235]\n"
"ds_write_addtid_b32 v156 offset:7168\nds_write_addtid_b32 v157 offset:7424\nds_write_addtid_b32 v158 offset:7680\nds_write_addtid_b32 v159 offset:7936\nv_mfma_f32_16x16x32_bf16 a[248:251], v[176:179], a[172:175], a[248:251]\nR 174,184,45056\n"
"s_add_u32 m0, s50, 90112\nv_mfma_f32_16x16x32_bf16 a[248:251], v[180:183], a[188:191], a[248:251]\ns_add_u32 s63, s59, 524288\nG 15\nv_mfma_f32_16x16x32_bf16 a[252:255], v[160:163], a[172:175], a[252:255]\nR 176,185,32768\n"
"s_waitcnt vmcnt(8) lgkmcnt(2)\ns_barrier\nv_mfma_f32_16x16x32_bf16 a[252:255], v[164:167], a[188:191], a[252:255]\nbuffer_load_dwordx4 v220, s[16:19], s63 offen lds\nds_read2st64_b32 v[128:129], v225 offset0:0 offset1:1\nv_mfma_f32_16x16x32_bf16 a[236:239], v[160:163], a[168:171], a[236:239]\n"
"R 178,185,36864\nds_read2st64_b32 v[130:131], v225 offset0:2 offset1:3\nv_mfma_f32_16x16x32_bf16 a[236:239], v[164:167], a[184:187], a[236:239]\nds_read2st64_b32 v[132:133], v225 offset0:4 offset1:5\nds_read2st64_b32 v[134:135], v225 offset0:6 offset1:7\nM 28,160,164\n"
"R 180,185,40960\nds_read2st64_b32 v[136:137], v225 offset0:8 offset1:9\nM 28,164,180\nbuffer_load_dwordx4 v221, s[16:19], s63 offen offset:1024 lds\nds_read2st64_b32 v[138:139], v225 offset0:10 offset1:11\nM 12,160,160\n"
"R 182,185,45056\ns_add_u32 m0, s50, 98304\nds_read2st64_b32 v[140:141], v225 offset0:12 offset1:13\nM 12,164,176\nds_read2st64_b32 v[142:143], v225 offset0:14 offset1:15\nG 12\n"
"G 10\nv_add_f32 v96, v96, v128\nM 0,168,192\nR 160,186,32768\ns_add_u32 s63, s59, 65536\nv_add_f32 v97, v97, v129\n"
"M 0,172,208\nbuffer_load_dwordx4 v220, s[16:19], s63 offen lds\nG 9\nv_add_f32 v98, v98, v130\nM 16,168,196\nR 162,186,36864\n"
"v_add_f32 v99, v99, v131\nG 9\nv_add_f32 v100, v100, v132\nM 16,172,212\nds_read2st64_b32 v[144:145], v225 offset0:16 offset1:17\nv_add_f32 v101, v101, v133\n"
"G 9\nv_add_f32 v102, v102, v134\nv_mfma_f32_16x16x32_bf16 a[224:227], v[168:171], a[200:203], a[224:227]\nR 164,186,40960\nv_add_f32 v103, v103, v135\nG 8\n"
"v_add_f32 v104, v104, v136\nv_mfma_f32_16x16x32_bf16 a[224:227], v[172:175], a[216:219], a[224:227]\nbuffer_load_dwordx4 v221, s[16:19], s63 offen offset:1024 lds\nv_add_f32 v105, v105, v137\nv_mfma_f32_16x16x32_bf16 a[240:243], v[168:171], a[204:207], a[240:243]\nR 166,186,45056\n"
"s_add_u32 m0, s50, 106496\nG 8\nv_add_f32 v106, v106, v138\nv_mfma_f32_16x16x32_bf16 a[240:243], v[172:175], a[220:223], a[240:243]\nds_read2st64_b32 v[146:147], v225 offset0:18 offset1:19\nG 8\n"
"v_add_f32 v107, v107, v139\nv_mfma_f32_16x16x32_bf16 a[244:247], v[176:179], a[204:207], a[244:247]\nR 168,187,32768\ns_add_u32 s63, s59, 589824\nG 8\nv_add_f32 v108, v108, v140\n"
"v_mfma_f32_16x16x32_bf16 a[244:247], v[180:183], a[220:223], a[244:247]\nbuffer_load_dwordx4 v220, s[16:19], s63 offen lds\nv_add_f32 v109, v109, v141\nv_mfma_f32_16x16x32_bf16 a[228:231], v[176:179], a[200:203], a[228:231]\nR 170,187,36864\nG 8\n"
"v_add_f32 v110, v110, v142\nv_add_f32 v111, v111, v143\nv_mfma_f32_16x16x32_bf16 a[228:231], v[180:183], a[216:219], a[228:231]\nds_read2st64_b32 v[148:149], v225 offset0:20 offset1:21\nG 6\nv_add_f32 v112, v112, v144\n"
"v_add_f32 v113, v113, v145\nM 20,176,196\nR 172,187,40960\nds_read2st64_b32 v[150:151], v225 offset0:22 offset1:23\nM 20,180,212\nds_read2st64_b32 v[152:153], v225 offset0:24 offset1:25\n"
"G 6\nv_add_f32 v114, v114, v146\nv_add_f32 v115, v115, v147\nM 4,176,192\nR 174,187,45056\nds_read2st64_b32 v[154:155], v225 offset0:26 offset1:27\n"
"M 4,180,208\nds_read2st64_b32 v[156:157], v225 offset0:28 offset1:29\nds_read2st64_b32 v[158:159], v225 offset0:30 offset1:31\nM 8,160,192\nR 176,184,49152\nM 8,164,208\n"
"buffer_load_dwordx4 v221, s[16:19], s63 offen offset:1024 lds\nG 8\nv_add_f32 v116, v116, v148\nM 24,160,196\nR 178,184,53248\ns_add_u32 s63, s59, 131072\n"
"v_add_f32 v117, v117, v149\nM 24,164,212\ns_add_u32 m0, s50, 114688\nG 7\nv_add_f32 v118, v118, v150\nv_add_f32 v119, v119, v151\n"
"G 6\nv_add_f32 v120, v120, v152\nv_add_f32 v121, v121, v153\nG 4\nv_add_f32 v122, v122, v154\nv_add_f32 v123, v123, v155\n"
"G 3\nv_add_f32 v124, v124, v156\nv_add_f32 v125, v125, v157\nG 2\nv_add_f32 v126, v126, v158\nv_add_f32 v127, v127, v159\n"
"v_mfma_f32_16x16x32_bf16 a[232:235], v[160:163], a[200:203], a[232:235]\nR 180,184,57344\nv_cvt_pk_bf16_f32 v96, v96, v97\nv_cvt_pk_bf16_f32 v97, v98, v99\nv_mfma_f32_16x16x32_bf16 a[232:235], v[164:167], a[216:219], a[232:235]\nbuffer_load_dwordx4 v220, s[16:19], s63 offen lds\n"
"v_cvt_pk_bf16_f32 v98, v100, v101\nv_mfma_f32_16x16x32_bf16 a[248:251], v[160:163], a[204:207], a[248:251]\nR 182,184,61440\nv_cvt_pk_bf16_f32 v99, v102, v103\nv_cvt_pk_bf16_f32 v104, v104, v105\nv_mfma_f32_16x16x32_bf16 a[248:251], v[164:167], a[220:223], a[248:251]\n"
"v_cvt_pk_bf16_f32 v105, v106, v107\nv_cvt_pk_bf16_f32 v106, v108, v109\nv_cvt_pk_bf16_f32 v107, v110, v111\nv_mfma_f32_16x16x32_bf16 a[252:255], v[168:171], a[204:207], a[252:255]\nR 160,185,49152\nv_cvt_pk_bf16_f32 v112, v112, v113\n"
"v_cvt_pk_bf16_f32 v113, v114, v115\nv_mfma_f32_16x16x32_bf16 a[252:255], v[172:175], a[220:223], a[252:255]\nbuffer_load_dwordx4 v221, s[16:19], s63 offen offset:1024 lds\nv_cvt_pk_bf16_f32 v114, v116, v117\nv_mfma_f32_16x16x32_bf16 a[236:239], v[168:171], a[200:203], a[236:239]\nR 162,185,53248\n"
"s_add_u32 s63, s59, 655360\nv_cvt_pk_bf16_f32 v115, v118, v119\nv_mfma_f32_16x16x32_bf16 a[236:239], v[172:175], a[216:219], a[236:239]\ns_add_u32 m0, s50, 0\nv_cvt_pk_bf16_f32 v120, v120, v121\nv_cvt_pk_bf16_f32 v121, v122, v123\n"
"v_cvt_pk_bf16_f32 v122, v124, v125\nM 28,168,196\nR 164,185,57344\ns_bfe_u32 s100, s56, 0x20007\ns_mul_i32 s100, s100, 49152\ns_add_u32 s100, s100, s72\n"
"s_add_u32 s100, s100, 52428800\nv_cvt_pk_bf16_f32 v123, v126, v127\nM 28,172,212\nbuffer_load_dwordx4 v220, s[16:19], s63 offen lds\nbuffer_store_dwordx4 v[96:99], v252, s[20:23], s100 offen offset:0 sc1\nM 12,168,192\n"
"R 166,185,61440\nbuffer_store_dwordx4 v[104:107], v252, s[20:23], s100 offen offset:1024 sc1\nM 12,172,208\ns_waitcnt vmcnt(13)\ns_barrier\nbuffer_store_dwordx4 v[112:115], v252, s[20:23], s100 offen offset:2048 sc1\n"
"G 4\nM 32,176,160\nR 168,186,49152\nbuffer_store_dwordx4 v[120:123], v252, s[20:23], s100 offen offset:3072 sc1\nM 32,180,176\nbuffer_load_dwordx4 v221, s[16:19], s63 offen offset:1024 lds\n"
"v_xor_b32 v240, 0x2000, v225\nM 48,176,164\nR 170,186,53248\nds_write2st64_b32 v240, a224, a225 offset0:0 offset1:1\nM 48,180,180\ns_add_u32 m0, s50, 8192\n"
"ds_write2st64_b32 v240, a226, a227 offset0:2 offset1:3\nM 64,176,168\nR 172,186,57344\nds_write2st64_b32 v240, a228, a229 offset0:4 offset1:5\nM 64,180,184\ns_add_u32 s63, s59, 196608\n"
"ds_write2st64_b32 v240, a230, a231 offset0:6 offset1:7\nM 80,176,172\nR 174,186,61440\nv_mfma_f32_16x16x32_bf16 v[96:99], v[208:211], v[192:195], 0\nds_write2st64_b32 v240, a232, a233 offset0:8 offset1:9\nM 80,180,188\n"
"v_mfma_f32_16x16x32_bf16 v[100:103], v[210:213], v[192:195], 0\nds_write2st64_b32 v240, a234, a235 offset0:10 offset1:11\nG 10\nM 84,160,172\nR 176,187,49152\nv_mfma_f32_16x16x32_bf16 v[104:107], v[208:211], v[196:199], 0\n"
"ds_write2st64_b32 v240, a236, a237 offset0:12 offset1:13\nM 84,164,188\nv_mfma_f32_16x16x32_bf16 v[108:111], v[210:213], v[196:199], 0\nbuffer_load_dwordx4 v220, s[16:19], s63 offen lds\nds_write2st64_b32 v240, a238, a239 offset0:14 offset1:15\nM 68,160,168\n"
"R 178,187,53248\nv_mfma_f32_16x16x32_bf16 v[112:115], v[208:211], v[200:203], 0\nds_write2st64_b32 v240, a240, a241 offset0:16 offset1:17\nM 68,164,184\nv_mfma_f32_16x16x32_bf16 v[116:119], v[210:213], v[200:203], 0\nbuffer_load_dwordx4 v221, s[16:19], s63 offen offset:1024 lds\n"
"ds_write2st64_b32 v240, a242, a243 offset0:18 offset1:19\nM 52,160,164\nR 180,187,57344\nv_mfma_f32_16x16x32_bf16 v[120:123], v[208:211], v[204:207], 0\ns_add_u32 m0, s50, 16384\ns_add_u32 s63, s59, 720896\n"
"ds_write2st64_b32 v240, a244, a245 offset0:20 offset1:21\nM 52,164,180\nv_mfma_f32_16x16x32_bf16 v[124:127], v[210:213], v[204:207], 0\nbuffer_load_dwordx4 v220, s[16:19], s63 offen lds\nds_write2st64_b32 v240, a246, a247 offset0:22 offset1:23\nM 36,160,160\n"
"R 182,187,61440\nv_mfma_f32_16x16x32_bf16 v[96:99], v[208:211], v[232:235], v[96:99]\nds_write2st64_b32 v240, a248, a249 offset0:24 offset1:25\nM 36,164,176\nv_mfma_f32_16x16x32_bf16 v[100:103], v[210:213], v[232:235], v[100:103]\nds_write2st64_b32 v240, a250, a251 offset0:26 offset1:27\n"
"G 14\nM 40,168,160\nR 160,188,0\nv_mfma_f32_16x16x32_bf16 v[104:107], v[208:211], v[236:239], v[104:107]\nds_write2st64_b32 v240, a252, a253 offset0:28 offset1:29\nM 40,172,176\n"
"v_mfma_f32_16x16x32_bf16 v[108:111], v[210:213], v[236:239], v[108:111]\nbuffer_load_dwordx4 v221, s[16:19], s63 offen offset:1024 lds\nds_write2st64_b32 v240, a254, a255 offset0:30 offset1:31\nM 56,168,164\nR 162,188,4096\nv_mfma_f32_16x16x32_bf16 v[112:115], v[208:211], v[232:235], v[112:115]\n"
"s_add_u32 m0, s50, 24576\ns_add_u32 s63, s59, 393216\nM 56,172,180\nv_mfma_f32_16x16x32_bf16 v[116:119], v[210:213], v[232:235], v[116:119]\nbuffer_load_dwordx4 v220, s[16:19], s63 offen lds\nM 72,168,168\n"
"R 164,188,8192\nv_mfma_f32_16x16x32_bf16 v[120:123], v[208:211], v[236:239], v[120:123]\nM 72,172,184\nv_mfma_f32_16x16x32_bf16 v[124:127], v[210:213], v[236:239], v[124:127]\nbuffer_load_dwordx4 v221, s[16:19], s63 offen offset:1024 lds\nM 88,168,172\n"
"R 166,188,12288\ns_add_u32 s63, s59, 458752\nM 88,172,188\nG 8\nM 92,176,172\nR 168,189,0\n"
"s_waitcnt vmcnt(18) lgkmcnt(4)\ns_barrier\nM 92,180,188\ns_add_u32 m0, s50, 32768\nds_read2st64_b32 v[128:129], v225 offset0:0 offset1:1\nds_read2st64_b32 v[130:131], v225 offset0:2 offset1:3\n"
"M 76,176,168\nR 170,189,4096\nds_read2st64_b32 v[132:133], v225 offset0:4 offset1:5\nM 76,180,184\nbuffer_load_dwordx4 v220, s[16:19], s63 offen lds\nds_read2st64_b32 v[134:135], v225 offset0:6 offset1:7\n"
"M 60,176,164\nR 172,189,8192\nds_read2st64_b32 v[136:137], v225 offset0:8 offset1:9\nM 60,180,180\nds_read2st64_b32 v[138:139], v225 offset0:10 offset1:11\nds_read2st64_b32 v[140:141], v225 offset0:12 offset1:13\n"
"M 44,176,160\nR 174,189,12288\nds_read2st64_b32 v[142:143], v225 offset0:14 offset1:15\nM 44,180,176\nds_read2st64_b32 v[144:145], v225 offset0:16 offset1:17\nG 13\n"
"G 11\nv_add_f32 v0, v0, v128\nM 32,160,192\nR 176,190,0\nv_add_f32 v1, v1, v129\nG 11\n"
"v_add_f32 v2, v2, v130\nM 32,164,208\nbuffer_load_dwordx4 v221, s[16:19], s63 offen offset:1024 lds\nv_add_f32 v3, v3, v131\nM 48,160,196\nR 178,190,4096\n"
"G 10\nv_add_f32 v4, v4, v132\nv_add_f32 v5, v5, v133\nM 48,164,212\ns_add_u32 m0, s50, 40960\nds_read2st64_b32 v[146:147], v225 offset0:18 offset1:19\n"
"G 10\nv_add_f32 v6, v6, v134\nM 64,160,200\nR 180,190,8192\nv_add_f32 v7, v7, v135\nG 9\n"
"v_add_f32 v8, v8, v136\nM 64,164,216\ns_add_u32 s63, s59, 262144\nds_read2st64_b32 v[148:149], v225 offset0:20 offset1:21\nv_add_f32 v9, v9, v137\nM 80,160,204\n"
"R 182,190,12288\nG 10\nv_add_f32 v10, v10, v138\nv_add_f32 v11, v11, v139\nM 80,164,220\nds_read2st64_b32 v[150:151], v225 offset0:22 offset1:23\n"
"G 9\nv_add_f32 v12, v12, v140\nM 84,168,204\nR 160,191,0\nv_add_f32 v13, v13, v141\nG 9\n"
"v_add_f32 v14, v14, v142\nM 84,172,220\nbuffer_load_dwordx4 v220, s[16:19], s63 offen lds\nv_add_f32 v15, v15, v143\nM 68,168,200\nR 162,191,4096\n"
"G 9\nv_add_f32 v16, v16, v144\nv_add_f32 v17, v17, v145\nM 68,172,216\nds_read2st64_b32 v[152:153], v225 offset0:24 offset1:25\nG 7\n"
"v_add_f32 v18, v18, v146\nv_add_f32 v19, v19, v147\nM 52,168,196\nR 164,191,8192\nG 6\nv_add_f32 v20, v20, v148\n"
"v_add_f32 v21, v21, v149\nM 52,172,212\nds_read2st64_b32 v[154:155], v225 offset0:26 offset1:27\nG 5\nv_add_f32 v22, v22, v150\nv_add_f32 v23, v23, v151\n"
"M 36,168,192\nR 166,191,12288\nds_read2st64_b32 v[156:157], v225 offset0:28 offset1:29\nM 36,172,208\nds_read2st64_b32 v[158:159], v225 offset0:30 offset1:31\nM 40,176,192\n"
"R 168,188,16384\nM 40,180,208\nbuffer_load_dwordx4 v221, s[16:19], s63 offen offset:1024 lds\nG 6\nv_add_f32 v24, v24, v152\nM 56,176,196\n"
"R 170,188,20480\nv_add_f32 v25, v25, v153\nM 56,180,212\ns_add_u32 m0, s50, 49152\nG 5\nv_add_f32 v26, v26, v154\n"
"v_add_f32 v27, v27, v155\nG 3\nv_add_f32 v28, v28, v156\nv_add_f32 v29, v29, v157\nG 2\nv_add_f32 v30, v30, v158\n"
"v_add_f32 v31, v31, v159\nM 72,176,200\nR 172,188,24576\nv_cvt_pk_bf16_f32 v0, v0, v1\nv_cvt_pk_bf16_f32 v1, v2, v3\nM 72,180,216\n"
"s_add_u32 s63, s59, 786432\nv_cvt_pk_bf16_f32 v2, v4, v5\nv_cvt_pk_bf16_f32 v3, v6, v7\nv_cvt_pk_bf16_f32 v8, v8, v9\nM 88,176,204\nR 174,188,28672\n"
"v_cvt_pk_bf16_f32 v9, v10, v11\nv_cvt_pk_bf16_f32 v10, v12, v13\nM 88,180,220\nv_cvt_pk_bf16_f32 v11, v14, v15\nv_cvt_pk_bf16_f32 v16, v16, v17\nv_cvt_pk_bf16_f32 v17, v18, v19\n"
"M 92,160,204\nR 176,189,16384\nv_cvt_pk_bf16_f32 v18, v20, v21\nv_cvt_pk_bf16_f32 v19, v22, v23\nM 92,164,220\nbuffer_load_dwordx4 v220, s[16:19], s63 offen lds\n"
"v_cvt_pk_bf16_f32 v24, v24, v25\nM 76,160,200\nR 178,189,20480\nv_cvt_pk_bf16_f32 v25, v26, v27\nv_cvt_pk_bf16_f32 v26, v28, v29\nM 76,164,216\n"
"s_bfe_u32 s100, s57, 0x20007\ns_mul_i32 s100, s100, 49152\ns_add_u32 s100, s100, s72\ns_add_u32 s100, s100, 52445184\nv_cvt_pk_bf16_f32 v27, v30, v31\nM 60,160,196\n"
"R 180,189,24576\nbuffer_store_dwordx4 v[0:3], v252, s[20:23], s100 offen offset:0 sc1\nM 60,164,212\nbuffer_load_dwordx4 v221, s[16:19], s63 offen offset:1024 lds\nbuffer_store_dwordx4 v[8:11], v252, s[20:23], s100 offen offset:1024 sc1\nM 44,160,192\n"
"R 182,189,28672\nbuffer_store_dwordx4 v[16:19], v252, s[20:23], s100 offen offset:2048 sc1\nM 44,164,208\ns_waitcnt vmcnt(23)\ns_barrier\nG 4\n"
"M 96,168,0\nR 160,190,16384\nbuffer_store_dwordx4 v[24:27], v252, s[20:23], s100 offen offset:3072 sc1\nM 96,172,16\ns_mov_b32 m0, s51\nM 112,168,4\n"
"R 162,190,20480\nds_write_addtid_b32 v64 offset:0\nM 112,172,20\nds_write_addtid_b32 v65 offset:256\nds_write_addtid_b32 v66 offset:512\nv_mfma_f32_16x16x32_bf16 v[128:131], v[168:171], a[8:11], 0\n"
"R 164,190,24576\nds_write_addtid_b32 v67 offset:768\nM 128,172,24\nds_write_addtid_b32 v68 offset:1024\nds_write_addtid_b32 v69 offset:1280\nv_mfma_f32_16x16x32_bf16 v[144:147], v[168:171], a[12:15], 0\n"
"R 166,190,28672\nds_write_addtid_b32 v70 offset:1536\nM 144,172,28\nds_write_addtid_b32 v71 offset:1792\nds_write_addtid_b32 v72 offset:2048\nG 13\n"
"v_mfma_f32_16x16x32_bf16 v[148:151], v[176:179], a[12:15], 0\nR 168,191,16384\nds_write_addtid_b32 v73 offset:2304\nM 148,180,28\nds_write_addtid_b32 v74 offset:2560\nds_write_addtid_b32 v75 offset:2816\n"
"v_mfma_f32_16x16x32_bf16 v[132:135], v[176:179], a[8:11], 0\nR 170,191,20480\nds_write_addtid_b32 v76 offset:3072\nM 132,180,24\nds_write_addtid_b32 v77 offset:3328\nds_write_addtid_b32 v78 offset:3584\n"
"M 116,176,4\nR 172,191,24576\nds_write_addtid_b32 v79 offset:3840\nM 116,180,20\nds_write_addtid_b32 v80 offset:4096\nds_write_addtid_b32 v81 offset:4352\n"
"M 100,176,0\nR 174,191,28672\nds_write_addtid_b32 v82 offset:4608\nM 100,180,16\nds_write_addtid_b32 v83 offset:4864\nds_write_addtid_b32 v84 offset:5120\n"
"G 15\nM 104,160,0\nR 176,188,32768\nds_write_addtid_b32 v85 offset:5376\nM 104,164,16\nds_write_addtid_b32 v86 offset:5632\n"
"ds_write_addtid_b32 v87 offset:5888\nM 120,160,4\nR 178,188,36864\nds_write_addtid_b32 v88 offset:6144\nM 120,164,20\nds_write_addtid_b32 v89 offset:6400\n"
"ds_write_addtid_b32 v90 offset:6656\nv_mfma_f32_16x16x32_bf16 v[136:139], v[160:163], a[8:11], 0\nR 180,188,40960\nds_write_addtid_b32 v91 offset:6912\nM 136,164,24\nds_write_addtid_b32 v92 offset:7168\n"
"ds_write_addtid_b32 v93 offset:7424\nds_write_addtid_b32 v94 offset:7680\nds_write_addtid_b32 v95 offset:7936\nv_mfma_f32_16x16x32_bf16 v[152:155], v[160:163], a[12:15], 0\nR 182,188,45056\nM 152,164,28\n"
"G 15\nv_mfma_f32_16x16x32_bf16 v[156:159], v[168:171], a[12:15], 0\nR 160,189,32768\ns_waitcnt vmcnt(16) lgkmcnt(2)\ns_barrier\nM 156,172,28\n"
"ds_read2st64_b32 v[64:65], v225 offset0:0 offset1:1\nds_read2st64_b32 v[66:67], v225 offset0:2 offset1:3\nv_mfma_f32_16x16x32_bf16 v[140:143], v[168:171], a[8:11], 0\nR 162,189,36864\nds_read2st64_b32 v[68:69], v225 offset0:4 offset1:5\nM 140,172,24\n"
"ds_read2st64_b32 v[70:71], v225 offset0:6 offset1:7\nds_read2st64_b32 v[72:73], v225 offset0:8 offset1:9\nM 124,168,4\nR 164,189,40960\nds_read2st64_b32 v[74:75], v225 offset0:10 offset1:11\nM 124,172,20\n"
"ds_read2st64_b32 v[76:77], v225 offset0:12 offset1:13\nds_read2st64_b32 v[78:79], v225 offset0:14 offset1:15\nM 108,168,0\nR 166,189,45056\nds_read2st64_b32 v[80:81], v225 offset0:16 offset1:17\nM 108,172,16\n"
"ds_read2st64_b32 v[82:83], v225 offset0:18 offset1:19\nG 14\nG 12\nv_add_f32 v32, v32, v64\nM 96,176,32\nR 168,190,32768\n"
"v_add_f32 v33, v33, v65\nG 12\nv_add_f32 v34, v34, v66\nM 96,180,48\nds_read2st64_b32 v[84:85], v225 offset0:20 offset1:21\nv_add_f32 v35, v35, v67\n"
"G 11\nv_add_f32 v36, v36, v68\nM 112,176,36\nR 170,190,36864\nv_add_f32 v37, v37, v69\nG 11\n"
"v_add_f32 v38, v38, v70\nM 112,180,52\nds_read2st64_b32 v[86:87], v225 offset0:22 offset1:23\nv_add_f32 v39, v39, v71\nG 11\nv_add_f32 v40, v40, v72\n"
"M 128,176,40\nR 172,190,40960\nv_add_f32 v41, v41, v73\nG 10\nv_add_f32 v42, v42, v74\nM 128,180,56\n"
"ds_read2st64_b32 v[88:89], v225 offset0:24 offset1:25\nv_add_f32 v43, v43, v75\nG 10\nv_add_f32 v44, v44, v76\nM 144,176,44\nR 174,190,45056\n"
"v_add_f32 v45, v45, v77\nG 10\nv_add_f32 v46, v46, v78\nM 144,180,60\nds_read2st64_b32 v[90:91], v225 offset0:26 offset1:27\nG 10\n"
"v_add_f32 v47, v47, v79\nM 148,160,44\nR 176,191,32768\nG 10\nv_add_f32 v48, v48, v80\nv_add_f32 v49, v49, v81\n"
"M 148,164,60\nds_read2st64_b32 v[92:93], v225 offset0:28 offset1:29\nG 10\nv_add_f32 v50, v50, v82\nv_add_f32 v51, v51, v83\nM 132,160,40\n"
"R 178,191,36864\nG 9\nv_add_f32 v52, v52, v84\nv_add_f32 v53, v53, v85\nM 132,164,56\nds_read2st64_b32 v[94:95], v225 offset0:30 offset1:31\n"
"G 8\nv_add_f32 v54, v54, v86\nv_add_f32 v55, v55, v87\nM 116,160,36\nR 180,191,40960\nG 7\n"
"v_add_f32 v56, v56, v88\nv_add_f32 v57, v57, v89\nM 116,164,52\nG 5\nv_add_f32 v58, v58, v90\nv_add_f32 v59, v59, v91\n"
"M 100,160,32\nR 182,191,45056\nv_mov_b32 v226, s46\nM 100,164,48\nv_mov_b32 v227, s47\nG 4\n"
"v_add_f32 v60, v60, v92\nM 104,168,32\nR 160,188,49152\ns_bfe_u32 s62, s56, 0x20007\ns_lshl_b32 s62, s62, 5\ns_add_u32 s61, s74, 548864\n"
"s_add_u32 s61, s61, s62\ns_waitcnt vmcnt(0)\ns_mov_b64 exec, 1\nbuffer_atomic_swap_x2 v[226:227], off, s[20:23], s61 sc1\ns_mov_b64 exec, -1\nM 104,172,48\n"
"s_bfe_u32 s62, s57, 0x20007\ns_lshl_b32 s62, s62, 5\ns_add_u32 s61, s74, 548872\ns_add_u32 s61, s61, s62\ns_mov_b64 exec, 1\nbuffer_atomic_swap_x2 v[226:227], off, s[20:23], s61 sc1\n"
"s_mov_b64 exec, -1\nM 120,168,36\nR 162,188,53248\nv_add_f32 v61, v61, v93\nM 120,172,52\ns_add_u32 m0, s50, 57344\n"
"G 4\nv_add_f32 v62, v62, v94\nv_add_f32 v63, v63, v95\nM 136,168,40\nR 164,184,0\nv_cvt_pk_bf16_f32 v32, v32, v33\n"
"v_cvt_pk_bf16_f32 v33, v34, v35\nM 136,172,56\ns_add_u32 s63, s59, 327680\nv_cvt_pk_bf16_f32 v34, v36, v37\nv_cvt_pk_bf16_f32 v35, v38, v39\nv_cvt_pk_bf16_f32 v40, v40, v41\n"
"M 152,168,44\nR 166,184,4096\nv_cvt_pk_bf16_f32 v41, v42, v43\nv_cvt_pk_bf16_f32 v42, v44, v45\nM 152,172,60\nG 4\n"
"v_cvt_pk_bf16_f32 v43, v46, v47\nv_cvt_pk_bf16_f32 v48, v48, v49\nv_cvt_pk_bf16_f32 v49, v50, v51\nM 156,176,44\nR 168,189,49152\nv_cvt_pk_bf16_f32 v50, v52, v53\n"
"v_cvt_pk_bf16_f32 v51, v54, v55\nM 156,180,60\nbuffer_load_dwordx4 v220, s[16:19], s63 offen lds\nv_cvt_pk_bf16_f32 v56, v56, v57\nM 140,176,40\nR 170,189,53248\n"
"v_cvt_pk_bf16_f32 v57, v58, v59\nv_cvt_pk_bf16_f32 v58, v60, v61\nM 140,180,56\nbuffer_load_dwordx4 v221, s[16:19], s63 offen offset:1024 lds\nv_cvt_pk_bf16_f32 v59, v62, v63\nM 124,176,36\n"
"R 172,185,0\ns_add_u32 s63, s59, 851968\ns_bfe_u32 s100, s58, 0x20007\ns_mul_i32 s100, s100, 49152\ns_add_u32 s100, s100, s72\ns_add_u32 s100, s100, 52461568\n"
"M 124,180,52\ns_add_u32 m0, s50, 65536\nbuffer_store_dwordx4 v[32:35], v252, s[20:23], s100 offen offset:0 sc1\nM 108,176,32\nR 174,185,4096\nbuffer_store_dwordx4 v[40:43], v252, s[20:23], s100 offen offset:1024 sc1\n"
"M 108,180,48\ns_barrier\nbuffer_store_dwordx4 v[48:51], v252, s[20:23], s100 offen offset:2048 sc1\nG 4\nM 96,160,64\nR 176,190,49152\n"
"buffer_store_dwordx4 v[56:59], v252, s[20:23], s100 offen offset:3072 sc1\nM 96,164,80\nbuffer_load_dwordx4 v220, s[16:19], s63 offen lds\nM 112,160,68\nR 178,190,53248\nM 112,164,84\n"
"buffer_load_dwordx4 v221, s[16:19], s63 offen offset:1024 lds\nM 128,160,72\nR 180,186,0\nM 128,164,88\nM 144,160,76\nR 182,186,4096\n"
"M 144,164,92\nG 4\nM 148,168,76\nR 160,191,49152\nLq574:\ns_lshr_b32 s60, s45, 3\n"
"s_lshl2_add_u32 s60, s60, s40\ns_lshl_b32 s60, s60, 8\nv_lshl_add_u32 v224, v252, 3, s60\nv_add_u32 v224, Lq574+4096-LqPC, v224\ns_mov_b64 exec, 3\nglobal_load_dword v224, v224, s[98:99]\n"
"s_mov_b64 exec, -1\nM 148,172,92\nv_bfe_u32 v250, v252, 4, 2\nv_lshlrev_b32 v250, 3, v250\nM 132,168,72\nR 162,191,53248\n"
"M 132,172,88\ns_lshl_b32 s60, s43, 5\nv_add_u32 v250, s60, v250\nM 116,168,68\nR 164,187,0\nM 116,172,84\n"
"M 100,168,64\nR 166,187,4096\nM 100,172,80\nG 4\nM 104,176,64\nR 168,184,8192\n"
"M 104,180,80\nM 120,176,68\nR 170,184,12288\nM 120,180,84\nM 136,176,72\nR 172,184,16384\n"
"M 136,180,88\nM 152,176,76\nR 174,184,20480\nM 152,180,92\nG 4\nM 156,160,76\n"
"R 176,185,8192\nM 156,164,92\nM 140,160,72\nR 178,185,12288\nM 140,164,88\nM 124,160,68\n"
"R 180,185,16384\nM 124,164,84\nM 108,160,64\nR 182,185,20480\nM 108,164,80\nG 4\n"
"M 96,168,96\nR 160,186,8192\nM 96,172,112\nM 112,168,100\nR 162,186,12288\nM 112,172,116\n"
"M 128,168,104\nR 164,186,16384\nM 128,172,120\nM 144,168,108\nR 166,186,20480\nM 144,172,124\n"
"G 4\nM 148,176,108\nR 168,187,8192\nM 148,180,124\nM 132,176,104\nR 170,187,12288\n"
"M 132,180,120\nM 116,176,100\nR 172,187,16384\nM 116,180,116\nM 100,176,96\nR 174,187,20480\n"
"v_mov_b32 v226, s46\nM 100,180,112\nG 4\nv_mov_b32 v227, s47\nM 104,160,96\nR 176,184,24576\n"
"s_bfe_u32 s62, s58, 0x20007\ns_lshl_b32 s62, s62, 5\ns_add_u32 s61, s74, 548880\ns_add_u32 s61, s61, s62\ns_waitcnt vmcnt(0)\ns_mov_b64 exec, 1\n"
"buffer_atomic_swap_x2 v[226:227], off, s[20:23], s61 sc1\ns_mov_b64 exec, -1\ns_barrier\nM 104,164,112\nM 120,160,100\nR 178,184,28672\n"
"M 120,164,116\ns_movk_i32 s63, 6144\nv_mad_u32_u24 v220, v228, s63, v229\nv_add_u32 v221, 3144704, v220\ns_add_u32 m0, s50, 73728\ns_add_u32 s63, s52, 2048\n"
"buffer_load_dwordx4 v220, s[4:7], s63 offen lds\nM 136,160,104\nR 180,184,32768\nv_mov_b32 v248, 0\nM 136,164,120\nv_mov_b32 v249, 0\n"
"M 152,160,108\nR 182,184,36864\ns_add_u32 s61, s74, 548864\ns_mov_b64 exec, 3\nbuffer_atomic_or_x2 v[248:249], v250, s[20:23], s61 offen sc0 sc1\ns_mov_b64 exec, -1\n"
"M 152,164,124\nG 4\nM 156,168,108\nR 160,185,24576\nM 156,172,124\nM 140,168,104\n"
"R 162,185,28672\nM 140,172,120\nM 124,168,100\nR 164,185,32768\nM 124,172,116\nbuffer_load_dwordx4 v221, s[4:7], s63 offen offset:1024 lds\n"
"M 108,168,96\nR 166,185,36864\ns_add_u32 m0, s50, 81920\nM 108,172,112\nG 4\nM 96,176,128\n"
"R 168,186,24576\ns_add_u32 s63, s52, 198656\nM 96,180,144\nbuffer_load_dwordx4 v220, s[4:7], s63 offen lds\nM 112,176,132\nR 170,186,28672\n"
"M 112,180,148\nM 128,176,136\nR 172,186,32768\ns_lshr_b32 s60, s45, 3\ns_lshl2_add_u32 s60, s60, s40\ns_lshl_b32 s60, s60, 8\n"
"v_lshl_add_u32 v224, v252, 3, s60\nv_add_u32 v224, LqM-LqPC, v224\ns_mov_b64 exec, 3\nglobal_load_dword v224, v224, s[98:99]\ns_mov_b64 exec, -1\nM 128,180,152\n"
"M 144,176,140\nR 174,186,36864\nM 144,180,156\nG 4\nM 148,160,140\nR 176,187,24576\n"
"M 148,164,156\nbuffer_load_dwordx4 v221, s[4:7], s63 offen offset:1024 lds\nM 132,160,136\nR 178,187,28672\ns_add_u32 s63, s52, 4096\nM 132,164,152\n"
"s_add_u32 m0, s50, 90112\nM 116,160,132\nR 180,187,32768\nM 116,164,148\nbuffer_load_dwordx4 v220, s[4:7], s63 offen lds\nM 100,160,128\n"
"R 182,187,36864\nM 100,164,144\nG 4\nM 104,168,128\nR 160,184,40960\nM 104,172,144\n"
"M 120,168,132\nR 162,184,45056\nM 120,172,148\nbuffer_load_dwordx4 v221, s[4:7], s63 offen offset:1024 lds\nM 136,168,136\nR 164,184,49152\n"
"M 136,172,152\ns_mul_i32 s64, s43, 49152\nM 152,168,140\nR 166,184,53248\nM 152,172,156\nG 4\n"
"M 156,176,140\nR 168,185,40960\ns_barrier\ns_waitcnt vmcnt(6)\nv_cmp_eq_u64 s[86:87], s[46:47], v[248:249]\nM 156,180,156\n"
"s_add_u32 s64, s64, s72\nM 140,176,136\nR 170,185,45056\ns_and_b32 s62, s86, 3\ns_cmp_eq_u32 s62, 3\ns_cbranch_scc0 Lq499\n"
"Lq500:\nv_mov_b32 v248, 0\nM 140,180,152\ns_add_u32 s64, s64, 52428800\nv_mov_b32 v249, 0\nM 124,176,132\n"
"R 172,185,49152\ns_add_u32 s61, s74, 548864\ns_mov_b64 exec, 4\nbuffer_atomic_or_x2 v[248:249], v250, s[20:23], s61 offen sc0 sc1\ns_mov_b64 exec, -1\nM 124,180,148\n"
"buffer_load_dwordx4 v[0:3], v252, s[20:23], s64 offen offset:0 sc1\nM 108,176,128\nR 174,185,53248\nM 108,180,144\nbuffer_load_dwordx4 v[4:7], v252, s[20:23], s64 offen offset:1024 sc1\nG 4\n"
"M 128,160,168\nR 176,186,40960\ns_mul_i32 s65, s43, 49152\nM 128,164,184\nR 178,186,45056\ns_add_u32 s65, s65, s72\n"
"M 144,160,172\nR 180,186,49152\nbuffer_load_dwordx4 v[8:11], v252, s[20:23], s64 offen offset:2048 sc1\nM 144,164,188\nR 182,186,53248\nG 4\n"
"M 148,168,172\nR 160,187,40960\ns_add_u32 s65, s65, 52445184\nM 148,172,188\nR 162,187,45056\nbuffer_load_dwordx4 v[12:15], v252, s[20:23], s64 offen offset:3072 sc1\n"
"M 132,168,168\nR 164,187,49152\nM 132,172,184\nR 166,187,53248\nG 4\nM 136,176,168\n"
"R 168,184,57344\nbuffer_load_dwordx4 v[16:19], v252, s[20:23], s65 offen offset:0 sc1\nM 136,180,184\nR 170,184,61440\nM 152,176,172\nR 172,188,0\n"
"M 152,180,188\nR 174,188,4096\nG 4\nM 156,160,172\nR 176,185,57344\nbuffer_load_dwordx4 v[20:23], v252, s[20:23], s65 offen offset:1024 sc1\n"
"M 156,164,188\nR 178,185,61440\nM 140,160,168\nR 180,189,0\nbuffer_load_dwordx4 v[24:27], v252, s[20:23], s65 offen offset:2048 sc1\nM 140,164,184\n"
"R 182,189,4096\nG 4\nM 128,168,200\nR 160,186,57344\nM 128,172,216\nR 162,186,61440\n"
"buffer_load_dwordx4 v[28:31], v252, s[20:23], s65 offen offset:3072 sc1\nM 144,168,204\nR 164,190,0\nM 144,172,220\nR 166,190,4096\nG 4\n"
"M 148,176,204\nR 168,187,57344\nM 148,180,220\nR 170,187,61440\ns_add_u32 m0, s50, 98304\nM 132,176,200\n"
"R 172,191,0\ns_add_u32 s63, s52, 200704\nM 132,180,216\nR 174,191,4096\nG 4\nM 136,160,200\n"
"R 176,184,40960\nM 136,164,216\nR 178,184,45056\nM 152,160,204\nR 180,184,49152\ns_mul_i32 s66, s43, 49152\n"
"M 152,164,220\nR 182,184,53248\nbuffer_load_dwordx4 v220, s[4:7], s63 offen lds\nG 4\nM 156,168,204\nR 160,185,40960\n"
"s_add_u32 s66, s66, s72\nM 156,172,220\nR 162,185,45056\ns_add_u32 s66, s66, 52461568\nM 140,168,200\nR 164,185,49152\n"
"s_waitcnt vmcnt(9)\nv_cmp_eq_u64 s[86:87], s[46:47], v[248:249]\nM 140,172,216\nR 166,185,53248\nbuffer_load_dwordx4 v221, s[4:7], s63 offen offset:1024 lds\nG 4\n"
"M 96,176,160\nR 168,186,40960\ns_and_b32 s62, s86, 4\ns_cmp_eq_u32 s62, 4\ns_cbranch_scc0 Lq515\nLq516:\n"
"M 96,180,176\nR 170,186,45056\ns_mov_b32 m0, s51\nM 112,176,164\nR 172,186,49152\nbuffer_load_dwordx4 v[64:67], v252, s[20:23], s66 offen offset:0 sc1\n"
"ds_write_addtid_b32 v128 offset:0\nM 112,180,180\nR 174,186,53248\nds_write_addtid_b32 v129 offset:256\nG 6\nM 116,160,164\n"
"R 176,187,40960\nbuffer_load_dwordx4 v[68:71], v252, s[20:23], s66 offen offset:1024 sc1\nds_write_addtid_b32 v130 offset:512\nM 116,164,180\nR 178,187,45056\nds_write_addtid_b32 v131 offset:768\n"
"M 100,160,160\nR 180,187,49152\nds_write_addtid_b32 v132 offset:1024\nM 100,164,176\nR 182,187,53248\nds_write_addtid_b32 v133 offset:1280\n"
"G 9\nM 104,168,160\nR 160,184,57344\nbuffer_load_dwordx4 v[72:75], v252, s[20:23], s66 offen offset:2048 sc1\nds_write_addtid_b32 v134 offset:1536\nM 104,172,176\n"
"R 162,184,61440\nbuffer_load_dwordx4 v[76:79], v252, s[20:23], s66 offen offset:3072 sc1\nds_write_addtid_b32 v135 offset:1792\nM 120,168,164\nR 164,188,0\nds_write_addtid_b32 v136 offset:2048\n"
"M 120,172,180\nR 166,188,4096\nds_write_addtid_b32 v137 offset:2304\nG 9\nM 124,176,164\nR 168,185,57344\n"
"ds_write_addtid_b32 v138 offset:2560\nM 124,180,180\nR 170,185,61440\nds_write_addtid_b32 v139 offset:2816\nM 108,176,160\nR 172,189,0\n"
"ds_write_addtid_b32 v140 offset:3072\nds_write_addtid_b32 v141 offset:3328\nds_write_addtid_b32 v142 offset:3584\nds_write_addtid_b32 v143 offset:3840\nds_write_addtid_b32 v144 offset:4096\nds_write_addtid_b32 v145 offset:4352\n"
"ds_write_addtid_b32 v146 offset:4608\nds_write_addtid_b32 v147 offset:4864\nds_write_addtid_b32 v148 offset:5120\nds_write_addtid_b32 v149 offset:5376\nds_write_addtid_b32 v150 offset:5632\nds_write_addtid_b32 v151 offset:5888\n"
"ds_write_addtid_b32 v152 offset:6144\nds_write_addtid_b32 v153 offset:6400\nds_write_addtid_b32 v154 offset:6656\nds_write_addtid_b32 v155 offset:6912\nds_write_addtid_b32 v156 offset:7168\nds_write_addtid_b32 v157 offset:7424\n"
"ds_write_addtid_b32 v158 offset:7680\nds_write_addtid_b32 v159 offset:7936\nM 108,180,176\nR 174,189,4096\ns_add_u32 m0, s50, 106496\ns_add_u32 s63, s52, 395264\n"
"G 15\nM 96,160,192\nR 176,186,57344\nbuffer_load_dwordx4 v220, s[4:7], s63 offen lds\nM 96,164,208\nR 178,186,61440\n"
"G 3\ns_barrier\nM 112,160,196\nR 180,190,0\nds_read2st64_b32 v[128:129], v225 offset0:0 offset1:1\nM 112,164,212\n"
"R 182,190,4096\nds_read2st64_b32 v[130:131], v225 offset0:2 offset1:3\nG 6\nM 116,168,196\nR 160,187,57344\nds_read2st64_b32 v[132:133], v225 offset0:4 offset1:5\n"
"M 116,172,212\nR 162,187,61440\nbuffer_load_dwordx4 v221, s[4:7], s63 offen offset:1024 lds\nds_read2st64_b32 v[134:135], v225 offset0:6 offset1:7\nM 100,168,192\nR 164,191,0\n"
"s_add_u32 m0, s50, 114688\ns_add_u32 s63, s52, 591872\nM 100,172,208\nR 166,191,4096\nG 7\nM 104,176,192\n"
"buffer_load_dwordx4 v220, s[4:7], s63 offen lds\nM 104,180,208\nds_read2st64_b32 v[136:137], v225 offset0:8 offset1:9\nds_read2st64_b32 v[138:139], v225 offset0:10 offset1:11\nM 120,176,196\nbuffer_load_dwordx4 v221, s[4:7], s63 offen offset:1024 lds\n"
"M 120,180,212\nds_read2st64_b32 v[140:141], v225 offset0:12 offset1:13\nds_read2st64_b32 v[142:143], v225 offset0:14 offset1:15\nG 4\nM 124,160,196\nds_read2st64_b32 v[144:145], v225 offset0:16 offset1:17\n"
"ds_read2st64_b32 v[146:147], v225 offset0:18 offset1:19\nM 124,164,212\nds_read2st64_b32 v[148:149], v225 offset0:20 offset1:21\nds_read2st64_b32 v[150:151], v225 offset0:22 offset1:23\nM 108,160,192\nds_read2st64_b32 v[152:153], v225 offset0:24 offset1:25\n"
"ds_read2st64_b32 v[154:155], v225 offset0:26 offset1:27\nM 108,164,208\nds_read2st64_b32 v[156:157], v225 offset0:28 offset1:29\nds_read2st64_b32 v[158:159], v225 offset0:30 offset1:31\nG 0\ns_barrier\n"
"s_lshl_b32 s84, s75, 11\ns_add_u32 s84, s84, s49\ns_add_u32 s84, s84, 18874368\ns_add_u32 s85, s84, 32768\nv_mov_b32 v238, s46\nv_mov_b32 v239, s47\n"
"s_waitcnt vmcnt(17)\nv_mfma_f32_16x16x32_bf16 v[96:99], v[208:211], v[0:3], v[96:99]\nv_mfma_f32_16x16x32_bf16 v[100:103], v[210:213], v[0:3], v[100:103]\ns_waitcnt vmcnt(16)\nv_mfma_f32_16x16x32_bf16 v[104:107], v[208:211], v[4:7], v[104:107]\nv_mfma_f32_16x16x32_bf16 v[108:111], v[210:213], v[4:7], v[108:111]\n"
"s_waitcnt vmcnt(15)\nv_mfma_f32_16x16x32_bf16 v[112:115], v[208:211], v[8:11], v[112:115]\nv_mfma_f32_16x16x32_bf16 v[116:119], v[210:213], v[8:11], v[116:119]\ns_waitcnt vmcnt(14)\nv_mfma_f32_16x16x32_bf16 v[120:123], v[208:211], v[12:15], v[120:123]\nv_mfma_f32_16x16x32_bf16 v[124:127], v[210:213], v[12:15], v[124:127]\n"
"s_waitcnt vmcnt(13)\nv_mfma_f32_16x16x32_bf16 v[96:99], v[208:211], v[16:19], v[96:99]\nv_mfma_f32_16x16x32_bf16 v[100:103], v[210:213], v[16:19], v[100:103]\ns_waitcnt vmcnt(12)\nv_mfma_f32_16x16x32_bf16 v[104:107], v[208:211], v[20:23], v[104:107]\nv_mfma_f32_16x16x32_bf16 v[108:111], v[210:213], v[20:23], v[108:111]\n"
"s_waitcnt vmcnt(11)\nv_mfma_f32_16x16x32_bf16 v[112:115], v[208:211], v[24:27], v[112:115]\nv_mfma_f32_16x16x32_bf16 v[116:119], v[210:213], v[24:27], v[116:119]\ns_waitcnt vmcnt(10)\nv_mfma_f32_16x16x32_bf16 v[120:123], v[208:211], v[28:31], v[120:123]\nv_mfma_f32_16x16x32_bf16 v[124:127], v[210:213], v[28:31], v[124:127]\n"
"s_waitcnt vmcnt(7)\nv_mfma_f32_16x16x32_bf16 v[96:99], v[208:211], v[64:67], v[96:99]\nv_mfma_f32_16x16x32_bf16 v[100:103], v[210:213], v[64:67], v[100:103]\ns_waitcnt vmcnt(6)\nv_mfma_f32_16x16x32_bf16 v[104:107], v[208:211], v[68:71], v[104:107]\nv_mfma_f32_16x16x32_bf16 v[108:111], v[210:213], v[68:71], v[108:111]\n"
"s_waitcnt vmcnt(5)\nv_mfma_f32_16x16x32_bf16 v[112:115], v[208:211], v[72:75], v[112:115]\nv_mfma_f32_16x16x32_bf16 v[116:119], v[210:213], v[72:75], v[116:119]\ns_waitcnt vmcnt(4)\nv_mfma_f32_16x16x32_bf16 v[120:123], v[208:211], v[76:79], v[120:123]\nv_mfma_f32_16x16x32_bf16 v[124:127], v[210:213], v[76:79], v[124:127]\n"
"v_pk_add_f32 v[96:97], v[96:97], v[128:129]\nv_pk_add_f32 v[98:99], v[98:99], v[130:131]\nv_pk_add_f32 v[100:101], v[100:101], v[132:133]\nv_pk_add_f32 v[102:103], v[102:103], v[134:135]\nv_cvt_pk_bf16_f32 v192, v96, v97\nv_cvt_pk_bf16_f32 v193, v98, v99\n"
"v_cvt_pk_bf16_f32 v194, v100, v101\nv_cvt_pk_bf16_f32 v195, v102, v103\nbuffer_store_dwordx4 v[192:195], v252, s[20:23], s84 offen offset:0 sc1\nv_pk_add_f32 v[104:105], v[104:105], v[136:137]\nv_pk_add_f32 v[106:107], v[106:107], v[138:139]\nv_pk_add_f32 v[108:109], v[108:109], v[140:141]\n"
"v_pk_add_f32 v[110:111], v[110:111], v[142:143]\nv_cvt_pk_bf16_f32 v196, v104, v105\nv_cvt_pk_bf16_f32 v197, v106, v107\nv_cvt_pk_bf16_f32 v198, v108, v109\nv_cvt_pk_bf16_f32 v199, v110, v111\nbuffer_store_dwordx4 v[196:199], v252, s[20:23], s84 offen offset:1024 sc1\n"
"v_pk_add_f32 v[112:113], v[112:113], v[144:145]\nv_pk_add_f32 v[114:115], v[114:115], v[146:147]\nv_pk_add_f32 v[116:117], v[116:117], v[148:149]\nv_pk_add_f32 v[118:119], v[118:119], v[150:151]\nv_cvt_pk_bf16_f32 v200, v112, v113\nv_cvt_pk_bf16_f32 v201, v114, v115\n"
"v_cvt_pk_bf16_f32 v202, v116, v117\nv_cvt_pk_bf16_f32 v203, v118, v119\nbuffer_store_dwordx4 v[200:203], v252, s[20:23], s85 offen offset:0 sc1\nv_pk_add_f32 v[120:121], v[120:121], v[152:153]\nv_pk_add_f32 v[122:123], v[122:123], v[154:155]\nv_pk_add_f32 v[124:125], v[124:125], v[156:157]\n"
"v_pk_add_f32 v[126:127], v[126:127], v[158:159]\nv_cvt_pk_bf16_f32 v204, v120, v121\nv_cvt_pk_bf16_f32 v205, v122, v123\nv_cvt_pk_bf16_f32 v206, v124, v125\nv_cvt_pk_bf16_f32 v207, v126, v127\nbuffer_store_dwordx4 v[204:207], v252, s[20:23], s85 offen offset:1024 sc1\n"
"s_add_u32 s61, s79, 88358912\ns_lshl_b32 s62, s75, 3\ns_add_u32 s61, s61, s62\ns_waitcnt vmcnt(0)\ns_mov_b64 exec, 1\nbuffer_atomic_swap_x2 v[238:239], off, s[20:23], s61 sc1\n"
"s_mov_b64 exec, -1\ns_branch LqM\nLq116:\ns_lshl_b32 s61, s42, 13\ns_add_u32 s61, s61, 84017152\nbuffer_load_dwordx4 v[128:131], v230, s[20:23], s61 offen sc1\n"
"s_lshl_b32 s61, s42, 13\ns_add_u32 s61, s61, 84021248\nbuffer_load_dwordx4 v[132:135], v230, s[20:23], s61 offen sc1\ns_waitcnt vmcnt(0)\nv_min3_u32 v232, v129, v131, v133\nv_min3_u32 v232, v232, v135, v135\n"
"v_max3_u32 v233, v129, v131, v133\nv_max3_u32 v233, v233, v135, v135\nv_cmp_ne_u32 s[86:87], s46, v232\nv_cmp_ne_u32 s[88:89], s46, v233\ns_nop 0\ns_or_b64 s[86:87], s[86:87], s[88:89]\n"
"s_cbranch_scc0 Lq117\ns_sub_u32 s48, s48, 1\ns_cmp_gt_i32 s48, 0\ns_cbranch_scc1 Lq116\ns_branch Lq117\nLq124:\n"
"s_lshl_b32 s61, s42, 13\ns_add_u32 s61, s61, 84148224\nbuffer_load_dwordx4 v[144:147], v234, s[20:23], s61 offen sc1\ns_lshl_b32 s61, s42, 13\ns_add_u32 s61, s61, 84152320\nbuffer_load_dwordx4 v[148:151], v234, s[20:23], s61 offen sc1\n"
"s_waitcnt vmcnt(0)\nv_min3_u32 v236, v145, v147, v149\nv_min3_u32 v236, v236, v151, v151\nv_max3_u32 v237, v145, v147, v149\nv_max3_u32 v237, v237, v151, v151\nv_cmp_ne_u32 s[86:87], s46, v236\n"
"v_cmp_ne_u32 s[88:89], s46, v237\ns_nop 0\ns_or_b64 s[86:87], s[86:87], s[88:89]\ns_cbranch_scc0 Lq125\ns_sub_u32 s48, s48, 1\ns_cmp_gt_i32 s48, 0\n"
"s_cbranch_scc1 Lq124\ns_branch Lq125\nLq132:\ns_lshl_b32 s61, s42, 13\ns_add_u32 s61, s61, 83886080\nbuffer_load_dwordx4 v[128:131], v230, s[20:23], s61 offen sc1\n"
"s_lshl_b32 s61, s42, 13\ns_add_u32 s61, s61, 83890176\nbuffer_load_dwordx4 v[132:135], v230, s[20:23], s61 offen sc1\ns_waitcnt vmcnt(0)\nv_min3_u32 v232, v129, v131, v133\nv_min3_u32 v232, v232, v135, v135\n"
"v_max3_u32 v233, v129, v131, v133\nv_max3_u32 v233, v233, v135, v135\nv_cmp_ne_u32 s[86:87], s46, v232\nv_cmp_ne_u32 s[88:89], s46, v233\ns_nop 0\ns_or_b64 s[86:87], s[86:87], s[88:89]\n"
"s_cbranch_scc0 Lq133\ns_sub_u32 s48, s48, 1\ns_cmp_gt_i32 s48, 0\ns_cbranch_scc1 Lq132\ns_branch Lq133\nLq140:\n"
"s_lshr_b32 s84, s49, 3\ns_add_u32 s84, s84, 81788928\nbuffer_load_dwordx4 v[128:131], v252, s[20:23], s84 offen offset:0 sc1\nbuffer_load_dwordx4 v[132:135], v252, s[20:23], s84 offen offset:1024 sc1\nbuffer_load_dwordx4 v[136:139], v252, s[20:23], s84 offen offset:2048 sc1\nbuffer_load_dwordx4 v[140:143], v252, s[20:23], s84 offen offset:3072 sc1\n"
"s_lshr_b32 s85, s49, 3\ns_add_u32 s85, s85, 81793024\nbuffer_load_dwordx4 v[144:147], v252, s[20:23], s85 offen offset:0 sc1\nbuffer_load_dwordx4 v[148:151], v252, s[20:23], s85 offen offset:1024 sc1\nbuffer_load_dwordx4 v[152:155], v252, s[20:23], s85 offen offset:2048 sc1\nbuffer_load_dwordx4 v[156:159], v252, s[20:23], s85 offen offset:3072 sc1\n"
"s_waitcnt vmcnt(6)\nv_min3_u32 v230, v129, v131, v133\ns_waitcnt vmcnt(5)\nv_min3_u32 v230, v230, v135, v137\ns_waitcnt vmcnt(4)\nv_min3_u32 v230, v230, v139, v141\n"
"s_waitcnt vmcnt(3)\nv_min3_u32 v230, v230, v143, v145\ns_waitcnt vmcnt(2)\nv_min3_u32 v230, v230, v147, v149\ns_waitcnt vmcnt(1)\nv_min3_u32 v230, v230, v151, v153\n"
"s_waitcnt vmcnt(0)\nv_min3_u32 v230, v230, v155, v157\nv_min3_u32 v230, v230, v159, v159\nv_max3_u32 v231, v129, v131, v133\nv_max3_u32 v231, v231, v135, v137\nv_max3_u32 v231, v231, v139, v141\n"
"v_max3_u32 v231, v231, v143, v145\nv_max3_u32 v231, v231, v147, v149\nv_max3_u32 v231, v231, v151, v153\nv_max3_u32 v231, v231, v155, v157\nv_max3_u32 v231, v231, v159, v159\nv_cmp_ne_u32 s[86:87], s46, v230\n"
"v_cmp_ne_u32 s[88:89], s46, v231\ns_nop 0\ns_or_b64 s[86:87], s[86:87], s[88:89]\ns_cbranch_scc0 Lq141\ns_sub_u32 s48, s48, 1\ns_cmp_gt_i32 s48, 0\n"
"s_cbranch_scc1 Lq140\ns_branch Lq141\nLq190:\ns_mov_b64 exec, s[66:67]\nbuffer_load_dwordx4 v[230:233], v252, s[20:23], s65 offen offset:0 sc1\nbuffer_load_dwordx2 v[64:65], v227, s[20:23], s65 offen offset:2048 sc1\n"
"buffer_load_dwordx2 v[70:71], v227, s[20:23], s65 offen offset:2560 sc1\ns_mov_b64 exec, -1\ns_waitcnt vmcnt(0)\nv_max3_u32 v248, v233, v65, v71\nv_cmp_eq_u32 vcc, -1, v248\ns_nop 1\n"
"s_cbranch_vccz Lq191\ns_sub_u32 s48, s48, 1\ns_cmp_gt_i32 s48, 0\ns_cbranch_scc1 Lq190\ns_branch Lq191\nLq192:\n"
"s_mov_b64 exec, s[66:67]\nbuffer_load_dwordx4 v[234:237], v252, s[20:23], s65 offen offset:1024 sc1\nbuffer_load_dwordx2 v[76:77], v227, s[20:23], s65 offen offset:3072 sc1\nbuffer_load_dwordx2 v[82:83], v227, s[20:23], s65 offen offset:3584 sc1\ns_mov_b64 exec, -1\ns_waitcnt vmcnt(0)\n"
"v_max3_u32 v248, v237, v77, v83\nv_cmp_eq_u32 vcc, -1, v248\ns_nop 1\ns_cbranch_vccz Lq193\ns_sub_u32 s48, s48, 1\ns_cmp_gt_i32 s48, 0\n"
"s_cbranch_scc1 Lq192\ns_branch Lq193\nLq195:\nv_mov_b32 v248, 0\nv_mov_b32 v249, 0\ns_add_u32 s61, s80, 88080384\n"
"s_mov_b64 exec, 0xffff\nbuffer_atomic_or_x2 v[248:249], v250, s[20:23], s61 offen sc0 sc1\ns_mov_b64 exec, -1\ns_waitcnt vmcnt(0)\nv_cmp_eq_u64 s[86:87], s[46:47], v[248:249]\ns_nop 1\n"
"s_and_b32 s62, s86, 0xffff\ns_cmp_eq_u32 s62, 0xffff\ns_cbranch_scc1 Lq196\ns_sleep 1\ns_sub_u32 s48, s48, 1\ns_cmp_gt_i32 s48, 0\n"
"s_cbranch_scc1 Lq195\ns_branch Lq196\nLq249:\nv_mov_b32 v248, 0\nv_mov_b32 v249, 0\ns_add_u32 s61, s80, 88219648\n"
"s_mov_b64 exec, 0xffff\nbuffer_atomic_or_x2 v[248:249], v250, s[20:23], s61 offen sc0 sc1\ns_mov_b64 exec, -1\ns_waitcnt vmcnt(0)\nv_cmp_eq_u64 s[86:87], s[46:47], v[248:249]\ns_nop 1\n"
"s_and_b32 s62, s86, 0xffff\ns_cmp_eq_u32 s62, 0xffff\ns_cbranch_scc1 Lq250\ns_sleep 1\ns_sub_u32 s48, s48, 1\ns_cmp_gt_i32 s48, 0\n"
"s_cbranch_scc1 Lq249\ns_branch Lq250\nLq301:\ns_lshr_b32 s84, s49, 3\ns_add_u32 s84, s84, 82313216\nbuffer_load_dwordx4 v[0:3], v252, s[20:23], s84 offen offset:0 sc1\n"
"buffer_load_dwordx4 v[4:7], v252, s[20:23], s84 offen offset:1024 sc1\nbuffer_load_dwordx4 v[8:11], v252, s[20:23], s84 offen offset:2048 sc1\nbuffer_load_dwordx4 v[12:15], v252, s[20:23], s84 offen offset:3072 sc1\ns_lshr_b32 s85, s49, 3\ns_add_u32 s85, s85, 82317312\nbuffer_load_dwordx4 v[16:19], v252, s[20:23], s85 offen offset:0 sc1\n"
"buffer_load_dwordx4 v[20:23], v252, s[20:23], s85 offen offset:1024 sc1\nbuffer_load_dwordx4 v[24:27], v252, s[20:23], s85 offen offset:2048 sc1\nbuffer_load_dwordx4 v[28:31], v252, s[20:23], s85 offen offset:3072 sc1\ns_waitcnt vmcnt(6)\nv_min3_u32 v230, v1, v3, v5\ns_waitcnt vmcnt(5)\n"
"v_min3_u32 v230, v230, v7, v9\ns_waitcnt vmcnt(4)\nv_min3_u32 v230, v230, v11, v13\ns_waitcnt vmcnt(3)\nv_min3_u32 v230, v230, v15, v17\ns_waitcnt vmcnt(2)\n"
"v_min3_u32 v230, v230, v19, v21\ns_waitcnt vmcnt(1)\nv_min3_u32 v230, v230, v23, v25\ns_waitcnt vmcnt(0)\nv_min3_u32 v230, v230, v27, v29\nv_min3_u32 v230, v230, v31, v31\n"
"v_max3_u32 v231, v1, v3, v5\nv_max3_u32 v231, v231, v7, v9\nv_max3_u32 v231, v231, v11, v13\nv_max3_u32 v231, v231, v15, v17\nv_max3_u32 v231, v231, v19, v21\nv_max3_u32 v231, v231, v23, v25\n"
"v_max3_u32 v231, v231, v27, v29\nv_max3_u32 v231, v231, v31, v31\nv_cmp_ne_u32 s[86:87], s46, v230\nv_cmp_ne_u32 s[88:89], s46, v231\ns_nop 0\ns_or_b64 s[86:87], s[86:87], s[88:89]\n"
"s_cbranch_scc0 Lq302\ns_sub_u32 s48, s48, 1\ns_cmp_gt_i32 s48, 0\ns_cbranch_scc1 Lq301\ns_branch Lq302\nLq303:\n"
"s_lshl_b32 s61, s42, 13\ns_add_u32 s61, s61, 84279296\nbuffer_load_dwordx4 v[0:3], v238, s[20:23], s61 offen sc1\ns_lshl_b32 s61, s42, 13\ns_add_u32 s61, s61, 84283392\nbuffer_load_dwordx4 v[4:7], v238, s[20:23], s61 offen sc1\n"
"s_waitcnt vmcnt(0)\nv_min3_u32 v240, v1, v3, v5\nv_min3_u32 v240, v240, v7, v7\nv_max3_u32 v241, v1, v3, v5\nv_max3_u32 v241, v241, v7, v7\nv_cmp_ne_u32 s[86:87], s46, v240\n"
"v_cmp_ne_u32 s[88:89], s46, v241\ns_nop 0\ns_or_b64 s[86:87], s[86:87], s[88:89]\ns_cbranch_scc0 Lq304\ns_sub_u32 s48, s48, 1\ns_cmp_gt_i32 s48, 0\n"
"s_cbranch_scc1 Lq303\ns_branch Lq304\nLq313:\ns_lshl_b32 s61, s42, 13\ns_add_u32 s61, s61, 84410368\nbuffer_load_dwordx4 v[16:19], v242, s[20:23], s61 offen sc1\n"
"s_lshl_b32 s61, s42, 13\ns_add_u32 s61, s61, 84414464\nbuffer_load_dwordx4 v[20:23], v242, s[20:23], s61 offen sc1\ns_waitcnt vmcnt(0)\nv_min3_u32 v244, v17, v19, v21\nv_min3_u32 v244, v244, v23, v23\n"
"v_max3_u32 v245, v17, v19, v21\nv_max3_u32 v245, v245, v23, v23\nv_cmp_ne_u32 s[86:87], s46, v244\nv_cmp_ne_u32 s[88:89], s46, v245\ns_nop 0\ns_or_b64 s[86:87], s[86:87], s[88:89]\n"
"s_cbranch_scc0 Lq314\ns_sub_u32 s48, s48, 1\ns_cmp_gt_i32 s48, 0\ns_cbranch_scc1 Lq313\ns_branch Lq314\nLq323:\n"
"s_lshl_b32 s61, s42, 13\ns_add_u32 s61, s61, 84541440\nbuffer_load_dwordx4 v[0:3], v238, s[20:23], s61 offen sc1\ns_lshl_b32 s61, s42, 13\ns_add_u32 s61, s61, 84545536\nbuffer_load_dwordx4 v[4:7], v238, s[20:23], s61 offen sc1\n"
"s_waitcnt vmcnt(0)\nv_min3_u32 v240, v1, v3, v5\nv_min3_u32 v240, v240, v7, v7\nv_max3_u32 v241, v1, v3, v5\nv_max3_u32 v241, v241, v7, v7\nv_cmp_ne_u32 s[86:87], s46, v240\n"
"v_cmp_ne_u32 s[88:89], s46, v241\ns_nop 0\ns_or_b64 s[86:87], s[86:87], s[88:89]\ns_cbranch_scc0 Lq324\ns_sub_u32 s48, s48, 1\ns_cmp_gt_i32 s48, 0\n"
"s_cbranch_scc1 Lq323\ns_branch Lq324\nLq333:\ns_lshl_b32 s61, s42, 13\ns_add_u32 s61, s61, 84672512\nbuffer_load_dwordx4 v[16:19], v242, s[20:23], s61 offen sc1\n"
"s_lshl_b32 s61, s42, 13\ns_add_u32 s61, s61, 84676608\nbuffer_load_dwordx4 v[20:23], v242, s[20:23], s61 offen sc1\ns_waitcnt vmcnt(0)\nv_min3_u32 v244, v17, v19, v21\nv_min3_u32 v244, v244, v23, v23\n"
"v_max3_u32 v245, v17, v19, v21\nv_max3_u32 v245, v245, v23, v23\nv_cmp_ne_u32 s[86:87], s46, v244\nv_cmp_ne_u32 s[88:89], s46, v245\ns_nop 0\ns_or_b64 s[86:87], s[86:87], s[88:89]\n"
"s_cbranch_scc0 Lq334\ns_sub_u32 s48, s48, 1\ns_cmp_gt_i32 s48, 0\ns_cbranch_scc1 Lq333\ns_branch Lq334\nLq367:\n"
"v_mov_b32 v248, 0\nv_mov_b32 v249, 0\ns_add_u32 s61, s82, 385024\ns_mov_b64 exec, 0xf\nbuffer_atomic_or_x2 v[248:249], v250, s[20:23], s61 offen sc0 sc1\ns_mov_b64 exec, -1\n"
"s_waitcnt vmcnt(0)\nv_cmp_eq_u64 s[86:87], s[46:47], v[248:249]\ns_nop 1\ns_and_b32 s62, s86, 0xf\ns_cmp_eq_u32 s62, 0xf\ns_cbranch_scc1 Lq368\n"
"s_sleep 1\ns_sub_u32 s48, s48, 1\ns_cmp_gt_i32 s48, 0\ns_cbranch_scc1 Lq367\ns_branch Lq368\nLq402:\n"
"v_mov_b32 v248, 0\nv_mov_b32 v249, 0\ns_add_u32 s61, s82, 385032\ns_mov_b64 exec, 0xf\nbuffer_atomic_or_x2 v[248:249], v250, s[20:23], s61 offen sc0 sc1\ns_mov_b64 exec, -1\n"
"s_waitcnt vmcnt(0)\nv_cmp_eq_u64 s[86:87], s[46:47], v[248:249]\ns_nop 1\ns_and_b32 s62, s86, 0xf\ns_cmp_eq_u32 s62, 0xf\ns_cbranch_scc1 Lq403\n"
"s_sleep 1\ns_sub_u32 s48, s48, 1\ns_cmp_gt_i32 s48, 0\ns_cbranch_scc1 Lq402\ns_branch Lq403\nLq437:\n"
"v_mov_b32 v248, 0\nv_mov_b32 v249, 0\ns_add_u32 s61, s82, 385048\ns_mov_b64 exec, 0xf\nbuffer_atomic_or_x2 v[248:249], v250, s[20:23], s61 offen sc0 sc1\ns_mov_b64 exec, -1\n"
"s_waitcnt vmcnt(0)\nv_cmp_eq_u64 s[86:87], s[46:47], v[248:249]\ns_nop 1\ns_and_b32 s62, s86, 0xf\ns_cmp_eq_u32 s62, 0xf\ns_cbranch_scc1 Lq438\n"
"s_sleep 1\ns_sub_u32 s48, s48, 1\ns_cmp_gt_i32 s48, 0\ns_cbranch_scc1 Lq437\ns_branch Lq438\nLq456:\n"
"v_mov_b32 v248, 0\nv_mov_b32 v249, 0\ns_add_u32 s61, s82, 385040\ns_mov_b64 exec, 0xf\nbuffer_atomic_or_x2 v[248:249], v250, s[20:23], s61 offen sc0 sc1\ns_mov_b64 exec, -1\n"
"s_waitcnt vmcnt(0)\nv_cmp_eq_u64 s[86:87], s[46:47], v[248:249]\ns_nop 1\ns_and_b32 s62, s86, 0xf\ns_cmp_eq_u32 s62, 0xf\ns_cbranch_scc1 Lq457\n"
"s_sleep 1\ns_sub_u32 s48, s48, 1\ns_cmp_gt_i32 s48, 0\ns_cbranch_scc1 Lq456\ns_branch Lq457\nLq499:\n"
"v_mov_b32 v248, 0\nv_mov_b32 v249, 0\ns_add_u32 s61, s74, 548864\ns_mov_b64 exec, 3\nbuffer_atomic_or_x2 v[248:249], v250, s[20:23], s61 offen sc0 sc1\ns_mov_b64 exec, -1\n"
"s_waitcnt vmcnt(0)\nv_cmp_eq_u64 s[86:87], s[46:47], v[248:249]\ns_nop 1\ns_and_b32 s62, s86, 3\ns_cmp_eq_u32 s62, 3\ns_cbranch_scc1 Lq500\n"
"s_sleep 1\ns_sub_u32 s48, s48, 1\ns_cmp_gt_i32 s48, 0\ns_cbranch_scc1 Lq499\ns_branch Lq500\nLq515:\n"
"v_mov_b32 v248, 0\nv_mov_b32 v249, 0\ns_add_u32 s61, s74, 548864\ns_mov_b64 exec, 4\nbuffer_atomic_or_x2 v[248:249], v250, s[20:23], s61 offen sc0 sc1\ns_mov_b64 exec, -1\n"
"s_waitcnt vmcnt(0)\nv_cmp_eq_u64 s[86:87], s[46:47], v[248:249]\ns_nop 1\ns_and_b32 s62, s86, 4\ns_cmp_eq_u32 s62, 4\ns_cbranch_scc1 Lq516\n"
"s_sleep 1\ns_sub_u32 s48, s48, 1\ns_cmp_gt_i32 s48, 0\ns_cbranch_scc1 Lq515\ns_branch Lq516\n.p2align 6\n"
"LqM:\nLqq1:\nv_bfe_u32 v250, v252, 7, 1\nv_mul_u32_u24 v250, 2176, v250\nv_bfe_u32 v251, v252, 4, 3\nv_lshl_add_u32 v250, v251, 3, v250\n"
"s_lshl_b32 s60, s90, 6\nv_add_u32 v250, s60, v250\ns_sleep 5\nv_mov_b32 v248, 0\nv_mov_b32 v249, 0\ns_add_u32 s61, s80, 88358912\n"
"s_mov_b64 exec, 0xffff\nbuffer_atomic_or_x2 v[248:249], v250, s[20:23], s61 offen sc0 sc1\ns_mov_b64 exec, -1\ns_add_u32 m0, s50, 0\ns_add_u32 s63, s52, 397312\nbuffer_load_dwordx4 v220, s[4:7], s63 offen lds\n"
"buffer_load_dwordx4 v221, s[4:7], s63 offen offset:1024 lds\ns_add_u32 m0, s50, 8192\ns_add_u32 s63, s52, 593920\nbuffer_load_dwordx4 v220, s[4:7], s63 offen lds\nbuffer_load_dwordx4 v221, s[4:7], s63 offen offset:1024 lds\ns_add_u32 m0, s50, 16384\n"
"s_add_u32 s63, s52, 788480\nbuffer_load_dwordx4 v220, s[4:7], s63 offen lds\nbuffer_load_dwordx4 v221, s[4:7], s63 offen offset:1024 lds\ns_add_u32 m0, s50, 24576\ns_add_u32 s63, s52, 985088\nbuffer_load_dwordx4 v220, s[4:7], s63 offen lds\n"
"buffer_load_dwordx4 v221, s[4:7], s63 offen offset:1024 lds\ns_add_u32 m0, s50, 32768\ns_add_u32 s63, s52, 790528\nbuffer_load_dwordx4 v220, s[4:7], s63 offen lds\nbuffer_load_dwordx4 v221, s[4:7], s63 offen offset:1024 lds\ns_add_u32 m0, s50, 40960\n"
"s_add_u32 s63, s52, 987136\nbuffer_load_dwordx4 v220, s[4:7], s63 offen lds\nbuffer_load_dwordx4 v221, s[4:7], s63 offen offset:1024 lds\nv_mov_b32 v230, 0\nv_mov_b32 v231, 0\nv_mov_b32 v232, 0\n"
"v_mov_b32 v233, 0\nv_dot2c_f32_bf16 v230, s76, v192\nv_dot2c_f32_bf16 v231, v192, v192\nv_dot2c_f32_bf16 v232, s76, v200\nv_dot2c_f32_bf16 v233, v200, v200\nv_dot2c_f32_bf16 v230, s76, v193\n"
"v_dot2c_f32_bf16 v231, v193, v193\nv_dot2c_f32_bf16 v232, s76, v201\nv_dot2c_f32_bf16 v233, v201, v201\nv_dot2c_f32_bf16 v230, s76, v194\nv_dot2c_f32_bf16 v231, v194, v194\nv_dot2c_f32_bf16 v232, s76, v202\n"
"v_dot2c_f32_bf16 v233, v202, v202\nv_dot2c_f32_bf16 v230, s76, v195\nv_dot2c_f32_bf16 v231, v195, v195\nv_dot2c_f32_bf16 v232, s76, v203\nv_dot2c_f32_bf16 v233, v203, v203\nv_dot2c_f32_bf16 v230, s76, v196\n"
"v_dot2c_f32_bf16 v231, v196, v196\nv_dot2c_f32_bf16 v232, s76, v204\nv_dot2c_f32_bf16 v233, v204, v204\nv_dot2c_f32_bf16 v230, s76, v197\nv_dot2c_f32_bf16 v231, v197, v197\nv_dot2c_f32_bf16 v232, s76, v205\n"
"v_dot2c_f32_bf16 v233, v205, v205\nv_dot2c_f32_bf16 v230, s76, v198\nv_dot2c_f32_bf16 v231, v198, v198\nv_dot2c_f32_bf16 v232, s76, v206\nv_dot2c_f32_bf16 v233, v206, v206\nv_dot2c_f32_bf16 v230, s76, v199\n"
"v_dot2c_f32_bf16 v231, v199, v199\nv_dot2c_f32_bf16 v232, s76, v207\nv_dot2c_f32_bf16 v233, v207, v207\nv_lshrrev_b32 v234, 1, v252\nv_permlane32_swap_b32 v230, v231\ns_nop 0\n"
"v_permlane32_swap_b32 v232, v233\nv_add_f32 v230, v230, v231\nv_add_f32 v232, v232, v233\nv_mov_b32 v231, s46\ns_nop 0\nv_permlane16_swap_b32 v230, v232\n"
"v_add_f32 v230, v230, v232\ns_lshr_b32 s61, s49, 3\ns_lshl2_add_u32 s61, s53, s61\ns_add_u32 s61, s61, 82837504\nbuffer_store_dwordx2 v[230:231], v234, s[20:23], s61 offen sc1\ns_waitcnt vmcnt(13)\n"
"v_cmp_eq_u64 s[86:87], s[46:47], v[248:249]\ns_nop 0\ns_and_b32 s62, s86, 0xffff\ns_cmp_eq_u32 s62, 0xffff\ns_cbranch_scc0 Lq579\nLq580:\n"
"s_add_u32 s64, s70, 18874368\nbuffer_load_dwordx4 a[0:3], v252, s[20:23], s64 offen offset:0 sc1\ns_add_u32 s65, s70, 18907136\nbuffer_load_dwordx4 a[4:7], v252, s[20:23], s65 offen offset:0 sc1\ns_add_u32 s66, s71, 18874368\nbuffer_load_dwordx4 a[8:11], v252, s[20:23], s66 offen offset:0 sc1\n"
"s_add_u32 s67, s71, 18907136\nbuffer_load_dwordx4 a[12:15], v252, s[20:23], s67 offen offset:0 sc1\nbuffer_load_dwordx4 a[16:19], v252, s[20:23], s64 offen offset:1024 sc1\nbuffer_load_dwordx4 a[20:23], v252, s[20:23], s65 offen offset:1024 sc1\nbuffer_load_dwordx4 a[24:27], v252, s[20:23], s66 offen offset:1024 sc1\nbuffer_load_dwordx4 a[28:31], v252, s[20:23], s67 offen offset:1024 sc1\n"
"s_barrier\nR 160,188,8192\nR 162,188,12288\nR 164,188,16384\nR 166,188,20480\nR 168,189,8192\n"
"R 170,189,12288\nR 172,189,16384\nR 174,189,20480\nR 176,190,8192\nR 178,190,12288\nR 180,190,16384\n"
"R 182,190,20480\nbuffer_load_dwordx4 a[32:35], v252, s[20:23], s64 offen offset:2048 sc1\nbuffer_load_dwordx4 a[36:39], v252, s[20:23], s65 offen offset:2048 sc1\nbuffer_load_dwordx4 a[40:43], v252, s[20:23], s66 offen offset:2048 sc1\nbuffer_load_dwordx4 a[44:47], v252, s[20:23], s67 offen offset:2048 sc1\nbuffer_load_dwordx4 a[48:51], v252, s[20:23], s64 offen offset:3072 sc1\n"
"buffer_load_dwordx4 a[52:55], v252, s[20:23], s65 offen offset:3072 sc1\nbuffer_load_dwordx4 a[56:59], v252, s[20:23], s66 offen offset:3072 sc1\nbuffer_load_dwordx4 a[60:63], v252, s[20:23], s67 offen offset:3072 sc1\ns_waitcnt vmcnt(15) lgkmcnt(10)\nv_mfma_f32_16x16x32_bf16 v[0:3], v[160:163], a[0:3], 0\ns_add_u32 s64, s70, 18878464\n"
"buffer_load_dwordx4 a[64:67], v252, s[20:23], s64 offen offset:0 sc1\ns_waitcnt vmcnt(12) lgkmcnt(8)\nM 0,164,16\nv_mfma_f32_16x16x32_bf16 v[16:19], v[160:163], a[4:7], 0\ns_waitcnt vmcnt(11)\nM 16,164,20\n"
"v_mfma_f32_16x16x32_bf16 v[32:35], v[160:163], a[8:11], 0\ns_waitcnt vmcnt(10)\nM 32,164,24\nv_mfma_f32_16x16x32_bf16 v[48:51], v[160:163], a[12:15], 0\ns_add_u32 s65, s70, 18911232\nbuffer_load_dwordx4 a[68:71], v252, s[20:23], s65 offen offset:0 sc1\n"
"s_waitcnt vmcnt(10)\nM 48,164,28\nG 4\nv_mfma_f32_16x16x32_bf16 v[52:55], v[168:171], a[12:15], 0\nR 160,191,8192\nM 52,172,28\n"
"v_mfma_f32_16x16x32_bf16 v[36:39], v[168:171], a[8:11], 0\nR 162,191,12288\nM 36,172,24\nv_mfma_f32_16x16x32_bf16 v[20:23], v[168:171], a[4:7], 0\nR 164,191,16384\nM 20,172,20\n"
"s_add_u32 s66, s71, 18878464\nbuffer_load_dwordx4 a[72:75], v252, s[20:23], s66 offen offset:0 sc1\nv_mfma_f32_16x16x32_bf16 v[4:7], v[168:171], a[0:3], 0\nR 166,191,20480\nM 4,172,16\nG 4\n"
"v_mfma_f32_16x16x32_bf16 v[8:11], v[176:179], a[0:3], 0\nR 168,188,24576\nM 8,180,16\nv_mfma_f32_16x16x32_bf16 v[24:27], v[176:179], a[4:7], 0\nR 170,188,28672\nM 24,180,20\n"
"s_add_u32 m0, s50, 49152\nv_mfma_f32_16x16x32_bf16 v[40:43], v[176:179], a[8:11], 0\nR 172,188,32768\ns_add_u32 s63, s52, 1181696\nM 40,180,24\ns_add_u32 s67, s71, 18911232\n"
"buffer_load_dwordx4 a[76:79], v252, s[20:23], s67 offen offset:0 sc1\nv_mfma_f32_16x16x32_bf16 v[56:59], v[176:179], a[12:15], 0\nR 174,188,36864\nM 56,180,28\nG 4\nv_mfma_f32_16x16x32_bf16 v[60:63], v[160:163], a[12:15], 0\n"
"R 176,189,24576\nM 60,164,28\nbuffer_load_dwordx4 v220, s[4:7], s63 offen lds\nv_mfma_f32_16x16x32_bf16 v[44:47], v[160:163], a[8:11], 0\nR 178,189,28672\nM 44,164,24\n"
"buffer_load_dwordx4 a[80:83], v252, s[20:23], s64 offen offset:1024 sc1\nv_mfma_f32_16x16x32_bf16 v[28:31], v[160:163], a[4:7], 0\nR 180,189,32768\nM 28,164,20\nv_mfma_f32_16x16x32_bf16 v[12:15], v[160:163], a[0:3], 0\nR 182,189,36864\n"
"M 12,164,16\nG 4\nv_mfma_f32_16x16x32_bf16 v[64:67], a[0:3], v[168:171], 0\nR 160,190,24576\nN 64,16,172\nbuffer_load_dwordx4 a[84:87], v252, s[20:23], s65 offen offset:1024 sc1\n"
"v_mfma_f32_16x16x32_bf16 v[68:71], a[4:7], v[168:171], 0\nR 162,190,28672\nN 68,20,172\nv_mfma_f32_16x16x32_bf16 v[96:99], a[8:11], v[168:171], 0\nR 164,190,32768\nN 96,24,172\n"
"v_mfma_f32_16x16x32_bf16 v[100:103], a[12:15], v[168:171], 0\nR 166,190,36864\nN 100,28,172\ns_barrier\nG 4\nv_mfma_f32_16x16x32_bf16 v[108:111], a[12:15], v[176:179], 0\n"
"R 168,191,24576\nN 108,28,180\nbuffer_load_dwordx4 a[88:91], v252, s[20:23], s66 offen offset:1024 sc1\nv_mfma_f32_16x16x32_bf16 v[104:107], a[8:11], v[176:179], 0\nR 170,191,28672\nN 104,24,180\n"
"v_mfma_f32_16x16x32_bf16 v[76:79], a[4:7], v[176:179], 0\nR 172,191,32768\nN 76,20,180\nbuffer_load_dwordx4 v221, s[4:7], s63 offen offset:1024 lds\nv_mfma_f32_16x16x32_bf16 v[72:75], a[0:3], v[176:179], 0\nR 174,191,36864\n"
"N 72,16,180\nG 4\nv_mfma_f32_16x16x32_bf16 v[80:83], a[0:3], v[160:163], 0\nR 176,188,40960\ns_add_u32 s63, s52, 1378304\nN 80,16,164\n"
"buffer_load_dwordx4 a[92:95], v252, s[20:23], s67 offen offset:1024 sc1\nv_mfma_f32_16x16x32_bf16 v[84:87], a[4:7], v[160:163], 0\nR 178,188,45056\nN 84,20,164\ns_add_u32 m0, s50, 57344\nv_mfma_f32_16x16x32_bf16 v[112:115], a[8:11], v[160:163], 0\n"
"R 180,188,49152\nN 112,24,164\nbuffer_load_dwordx4 v220, s[4:7], s63 offen lds\nv_mfma_f32_16x16x32_bf16 v[116:119], a[12:15], v[160:163], 0\nR 182,188,53248\nN 116,28,164\n"
"G 4\nv_mfma_f32_16x16x32_bf16 v[124:127], a[12:15], v[168:171], 0\nR 160,189,40960\nN 124,28,172\nbuffer_load_dwordx4 a[96:99], v252, s[20:23], s64 offen offset:2048 sc1\nv_mfma_f32_16x16x32_bf16 v[120:123], a[8:11], v[168:171], 0\n"
"R 162,189,45056\nN 120,24,172\nv_mfma_f32_16x16x32_bf16 v[92:95], a[4:7], v[168:171], 0\nR 164,189,49152\nN 92,20,172\nbuffer_load_dwordx4 a[100:103], v252, s[20:23], s65 offen offset:2048 sc1\n"
"v_mfma_f32_16x16x32_bf16 v[88:91], a[0:3], v[168:171], 0\nR 166,189,53248\nN 88,16,172\nG 4\ns_waitcnt vmcnt(20)\nM 0,176,32\n"
"R 168,190,40960\ns_waitcnt vmcnt(16)\nM 0,180,48\nM 16,176,36\nR 170,190,45056\ns_waitcnt vmcnt(15)\n"
"M 16,180,52\nbuffer_load_dwordx4 a[104:107], v252, s[20:23], s66 offen offset:2048 sc1\nM 32,176,40\nR 172,190,49152\ns_waitcnt vmcnt(15)\nM 32,180,56\n"
"M 48,176,44\nR 174,190,53248\ns_waitcnt vmcnt(14)\nM 48,180,60\ns_barrier\nG 4\n"
"M 52,160,44\nR 176,191,40960\nM 52,164,60\nbuffer_load_dwordx4 v221, s[4:7], s63 offen offset:1024 lds\nM 36,160,40\nR 178,191,45056\n"
"M 36,164,56\nbuffer_load_dwordx4 a[108:111], v252, s[20:23], s67 offen offset:2048 sc1\nM 20,160,36\nR 180,191,49152\nM 20,164,52\nM 4,160,32\n"
"R 182,191,53248\nM 4,164,48\nG 4\nM 8,168,32\nR 160,184,0\ns_add_u32 s63, s52, 1183744\n"
"M 8,172,48\nbuffer_load_dwordx4 a[112:115], v252, s[20:23], s64 offen offset:3072 sc1\nM 24,168,36\nR 162,184,4096\nM 24,172,52\ns_add_u32 m0, s50, 65536\n"
"M 40,168,40\nR 164,184,8192\nM 40,172,56\nbuffer_load_dwordx4 v220, s[4:7], s63 offen lds\nM 56,168,44\nR 166,184,12288\n"
"M 56,172,60\nG 4\nM 60,176,44\nR 168,185,0\nM 60,180,60\nbuffer_load_dwordx4 a[116:119], v252, s[20:23], s65 offen offset:3072 sc1\n"
"M 44,176,40\nR 170,185,4096\nM 44,180,56\nM 28,176,36\nR 172,185,8192\nM 28,180,52\n"
"M 12,176,32\nR 174,185,12288\nM 12,180,48\nG 4\nN 64,32,160\nR 176,186,0\n"
"N 64,48,164\nbuffer_load_dwordx4 a[120:123], v252, s[20:23], s66 offen offset:3072 sc1\nN 68,36,160\nR 178,186,4096\nN 68,52,164\nN 96,40,160\n"
"R 180,186,8192\nN 96,56,164\nbuffer_load_dwordx4 a[124:127], v252, s[20:23], s67 offen offset:3072 sc1\nN 100,44,160\nR 182,186,12288\nN 100,60,164\n"
"s_barrier\nG 4\nN 108,44,168\nR 160,187,0\nN 108,60,172\nbuffer_load_dwordx4 v221, s[4:7], s63 offen offset:1024 lds\n"
"N 104,40,168\nR 162,187,4096\nN 104,56,172\ns_add_u32 m0, s50, 73728\nN 76,36,168\nR 164,187,8192\n"
"s_add_u32 s63, s52, 1380352\nN 76,52,172\ns_add_u32 s64, s70, 18882560\nbuffer_load_dwordx4 a[128:131], v252, s[20:23], s64 offen offset:0 sc1\nN 72,32,168\nR 166,187,12288\n"
"N 72,48,172\nG 4\nN 80,32,176\nR 168,184,16384\nN 80,48,180\nbuffer_load_dwordx4 v220, s[4:7], s63 offen lds\n"
"N 84,36,176\nR 170,184,20480\nN 84,52,180\ns_add_u32 s65, s70, 18915328\nbuffer_load_dwordx4 a[132:135], v252, s[20:23], s65 offen offset:0 sc1\nN 112,40,176\n"
"R 172,184,24576\nN 112,56,180\nN 116,44,176\nR 174,184,28672\nN 116,60,180\nG 4\n"
"N 124,44,160\nR 176,185,16384\nN 124,60,164\ns_add_u32 s66, s71, 18882560\nbuffer_load_dwordx4 a[136:139], v252, s[20:23], s66 offen offset:0 sc1\nN 120,40,160\n"
"R 178,185,20480\nN 120,56,164\nN 92,36,160\nR 180,185,24576\nN 92,52,164\nN 88,32,160\n"
"R 182,185,28672\nN 88,48,164\nG 4\ns_waitcnt vmcnt(25)\nM 0,168,64\nR 160,186,16384\n"
"s_waitcnt vmcnt(20)\nM 0,172,80\ns_add_u32 s67, s71, 18915328\nbuffer_load_dwordx4 a[140:143], v252, s[20:23], s67 offen offset:0 sc1\nM 16,168,68\nR 162,186,20480\n"
"s_waitcnt vmcnt(20)\nM 16,172,84\nM 32,168,72\nR 164,186,24576\ns_waitcnt vmcnt(19)\nM 32,172,88\n"
"buffer_load_dwordx4 v221, s[4:7], s63 offen offset:1024 lds\nM 48,168,76\nR 166,186,28672\ns_waitcnt vmcnt(18)\nM 48,172,92\ns_barrier\n"
"G 4\nM 52,176,76\nR 168,187,16384\nM 52,180,92\nbuffer_load_dwordx4 a[144:147], v252, s[20:23], s64 offen offset:1024 sc1\nM 36,176,72\n"
"R 170,187,20480\nM 36,180,88\ns_add_u32 m0, s50, 81920\nM 20,176,68\nR 172,187,24576\ns_add_u32 s63, s52, 1574912\n"
"M 20,180,84\nbuffer_load_dwordx4 a[148:151], v252, s[20:23], s65 offen offset:1024 sc1\nM 4,176,64\nR 174,187,28672\nM 4,180,80\nG 4\n"
"M 8,160,64\nR 176,184,32768\nM 8,164,80\nbuffer_load_dwordx4 v220, s[4:7], s63 offen lds\nM 24,160,68\nR 178,184,36864\n"
"M 24,164,84\nM 40,160,72\nR 180,184,40960\nM 40,164,88\nbuffer_load_dwordx4 a[152:155], v252, s[20:23], s66 offen offset:1024 sc1\nM 56,160,76\n"
"R 182,184,45056\nM 56,164,92\nG 4\nM 60,168,76\nR 160,185,32768\nM 60,172,92\n"
"M 44,168,72\nR 162,185,36864\nM 44,172,88\nbuffer_load_dwordx4 a[156:159], v252, s[20:23], s67 offen offset:1024 sc1\nM 28,168,68\nR 164,185,40960\n"
"M 28,172,84\nM 12,168,64\nR 166,185,45056\nM 12,172,80\nG 4\nN 64,64,176\n"
"R 168,186,32768\nN 64,80,180\nbuffer_load_dwordx4 v221, s[4:7], s63 offen offset:1024 lds\nN 68,68,176\nR 170,186,36864\nN 68,84,180\n"
"buffer_load_dwordx4 a[160:163], v252, s[20:23], s64 offen offset:2048 sc1\nN 96,72,176\nR 172,186,40960\nN 96,88,180\nN 100,76,176\nR 174,186,45056\n"
"N 100,92,180\ns_waitcnt vmcnt(20)\ns_barrier\nG 4\nN 108,76,160\nR 176,187,32768\n"
"s_add_u32 s63, s52, 1771520\nN 108,92,164\nbuffer_load_dwordx4 a[164:167], v252, s[20:23], s65 offen offset:2048 sc1\nN 104,72,160\nR 178,187,36864\nN 104,88,164\n"
"s_add_u32 m0, s50, 90112\nN 76,68,160\nR 180,187,40960\nN 76,84,164\nbuffer_load_dwordx4 v220, s[4:7], s63 offen lds\nN 72,64,160\n"
"R 182,187,45056\nN 72,80,164\nG 4\nN 80,64,168\nR 160,184,49152\nN 80,80,172\n"
"buffer_load_dwordx4 a[168:171], v252, s[20:23], s66 offen offset:2048 sc1\nN 84,68,168\nR 162,184,53248\nN 84,84,172\nN 112,72,168\nR 164,184,57344\n"
"N 112,88,172\nbuffer_load_dwordx4 v221, s[4:7], s63 offen offset:1024 lds\nN 116,76,168\nR 166,184,61440\nN 116,92,172\nG 4\n"
"N 124,76,176\nR 168,185,49152\nN 124,92,180\nbuffer_load_dwordx4 a[172:175], v252, s[20:23], s67 offen offset:2048 sc1\nN 120,72,176\nR 170,185,53248\n"
"N 120,88,180\nN 92,68,176\nR 172,185,57344\nN 92,84,180\nbuffer_load_dwordx4 a[176:179], v252, s[20:23], s64 offen offset:3072 sc1\nN 88,64,176\n"
"R 174,185,61440\nN 88,80,180\nG 4\nM 0,160,96\nR 176,186,49152\ns_waitcnt vmcnt(24)\n"
"M 0,164,112\nM 16,160,100\nR 178,186,53248\ns_waitcnt vmcnt(22)\nM 16,164,116\nbuffer_load_dwordx4 a[180:183], v252, s[20:23], s65 offen offset:3072 sc1\n"
"M 32,160,104\nR 180,186,57344\ns_add_u32 s63, s52, 1576960\ns_waitcnt vmcnt(22)\nM 32,164,120\ns_add_u32 m0, s50, 98304\n"
"M 48,160,108\nR 182,186,61440\ns_waitcnt vmcnt(21)\nM 48,164,124\ns_waitcnt vmcnt(14)\ns_barrier\n"
"G 4\nM 52,168,108\nR 160,187,49152\nM 52,172,124\nbuffer_load_dwordx4 v220, s[4:7], s63 offen lds\nM 36,168,104\n"
"R 162,187,53248\nM 36,172,120\nbuffer_load_dwordx4 a[184:187], v252, s[20:23], s66 offen offset:3072 sc1\nM 20,168,100\nR 164,187,57344\nM 20,172,116\n"
"buffer_load_dwordx4 v221, s[4:7], s63 offen offset:1024 lds\nM 4,168,96\nR 166,187,61440\ns_add_u32 m0, s50, 106496\nM 4,172,112\nG 4\n"
"M 8,176,96\nR 168,188,0\ns_add_u32 s63, s52, 1773568\nM 8,180,112\nbuffer_load_dwordx4 a[188:191], v252, s[20:23], s67 offen offset:3072 sc1\nM 24,176,100\n"
"R 170,188,4096\nM 24,180,116\nbuffer_load_dwordx4 v220, s[4:7], s63 offen lds\nM 40,176,104\nR 172,188,8192\nM 40,180,120\n"
"buffer_load_dwordx4 v221, s[4:7], s63 offen offset:1024 lds\nM 56,176,108\nR 174,188,12288\nM 56,180,124\nG 4\nM 60,160,108\n"
"R 176,189,0\nM 60,164,124\ns_add_u32 s64, s70, 18886656\nbuffer_load_dwordx4 a[192:195], v252, s[20:23], s64 offen offset:0 sc1\nM 44,160,104\nR 178,189,4096\n"
"M 44,164,120\nM 28,160,100\nR 180,189,8192\nM 28,164,116\nM 12,160,96\nR 182,189,12288\n"
"M 12,164,112\nG 4\nN 64,96,168\nR 160,190,0\nN 64,112,172\ns_add_u32 s65, s70, 18919424\n"
"buffer_load_dwordx4 a[196:199], v252, s[20:23], s65 offen offset:0 sc1\nN 68,100,168\nR 162,190,4096\nN 68,116,172\ns_add_u32 m0, s50, 114688\nN 96,104,168\n"
"R 164,190,8192\ns_add_u32 s63, s52, 1968128\nN 96,120,172\ns_add_u32 s66, s71, 18886656\nbuffer_load_dwordx4 a[200:203], v252, s[20:23], s66 offen offset:0 sc1\nN 100,108,168\n"
"R 166,190,12288\nN 100,124,172\ns_waitcnt vmcnt(12)\ns_barrier\nG 4\nN 108,108,176\n"
"R 168,191,0\nN 108,124,180\nbuffer_load_dwordx4 v220, s[4:7], s63 offen lds\nN 104,104,176\nR 170,191,4096\nN 104,120,180\n"
"N 76,100,176\nR 172,191,8192\nN 76,116,180\ns_add_u32 s67, s71, 18919424\nbuffer_load_dwordx4 a[204:207], v252, s[20:23], s67 offen offset:0 sc1\nN 72,96,176\n"
"R 174,191,12288\nN 72,112,180\nG 4\nN 80,96,160\nR 176,188,16384\nbuffer_load_dwordx4 v221, s[4:7], s63 offen offset:1024 lds\n"
"N 80,112,164\ns_add_u32 m0, s50, 0\ns_add_u32 s63, s52, 2164736\nbuffer_load_dwordx4 v220, s[4:7], s63 offen lds\nN 84,100,160\nR 178,188,20480\n"
"N 84,116,164\nbuffer_load_dwordx4 a[208:211], v252, s[20:23], s64 offen offset:1024 sc1\nN 112,104,160\nR 180,188,24576\nN 112,120,164\nbuffer_load_dwordx4 v221, s[4:7], s63 offen offset:1024 lds\n"
"N 116,108,160\nR 182,188,28672\nN 116,124,164\nG 4\nN 124,108,168\nR 160,189,16384\n"
"N 124,124,172\nbuffer_load_dwordx4 a[212:215], v252, s[20:23], s65 offen offset:1024 sc1\nN 120,104,168\nR 162,189,20480\nN 120,120,172\nN 92,100,168\n"
"R 164,189,24576\nN 92,116,172\nN 88,96,168\nR 166,189,28672\nN 88,112,172\nG 4\n"
"M 0,176,128\nR 168,190,16384\nM 0,180,144\nbuffer_load_dwordx4 a[216:219], v252, s[20:23], s66 offen offset:1024 sc1\nM 16,176,132\nR 170,190,20480\n"
"M 16,180,148\nM 32,176,136\nR 172,190,24576\nM 32,180,152\ns_add_u32 m0, s50, 8192\nM 48,176,140\n"
"R 174,190,28672\ns_add_u32 s63, s52, 1970176\nM 48,180,156\ns_waitcnt vmcnt(11)\ns_barrier\nG 4\n"
"M 52,160,140\nR 176,191,16384\nM 52,164,156\nbuffer_load_dwordx4 a[220:223], v252, s[20:23], s67 offen offset:1024 sc1\nM 36,160,136\nR 178,191,20480\n"
"M 36,164,152\nbuffer_load_dwordx4 v220, s[4:7], s63 offen lds\nM 20,160,132\nR 180,191,24576\nM 20,164,148\nbuffer_load_dwordx4 v221, s[4:7], s63 offen offset:1024 lds\n"
"M 4,160,128\nR 182,191,28672\ns_add_u32 m0, s50, 16384\nM 4,164,144\nG 4\nM 8,168,128\n"
"R 160,188,32768\ns_add_u32 s63, s52, 2166784\nM 8,172,144\nbuffer_load_dwordx4 a[224:227], v252, s[20:23], s64 offen offset:2048 sc1\nM 24,168,132\nR 162,188,36864\n"
"M 24,172,148\nbuffer_load_dwordx4 v220, s[4:7], s63 offen lds\nM 40,168,136\nR 164,188,40960\nM 40,172,152\nbuffer_load_dwordx4 a[228:231], v252, s[20:23], s65 offen offset:2048 sc1\n"
"M 56,168,140\nR 166,188,45056\nM 56,172,156\nG 4\nM 60,176,140\nR 168,189,32768\n"
"buffer_load_dwordx4 v221, s[4:7], s63 offen offset:1024 lds\nM 60,180,156\nM 44,176,136\nR 170,189,36864\nM 44,180,152\nbuffer_load_dwordx4 a[232:235], v252, s[20:23], s66 offen offset:2048 sc1\n"
"M 28,176,132\nR 172,189,40960\nM 28,180,148\nM 12,176,128\nR 174,189,45056\nM 12,180,144\n"
"G 4\nN 64,128,160\nR 176,190,32768\nN 64,144,164\nN 68,132,160\nR 178,190,36864\n"
"N 68,148,164\nbuffer_load_dwordx4 a[236:239], v252, s[20:23], s67 offen offset:2048 sc1\nN 96,136,160\nR 180,190,40960\nN 96,152,164\ns_add_u32 m0, s50, 24576\n"
"N 100,140,160\nR 182,190,45056\ns_add_u32 s63, s52, 2361344\nN 100,156,164\ns_waitcnt vmcnt(11)\ns_barrier\n"
"G 4\nN 108,140,168\nR 160,191,32768\nN 108,156,172\nbuffer_load_dwordx4 a[240:243], v252, s[20:23], s64 offen offset:3072 sc1\nN 104,136,168\n"
"R 162,191,36864\nN 104,152,172\nbuffer_load_dwordx4 v220, s[4:7], s63 offen lds\nN 76,132,168\nR 164,191,40960\nLq675:\n"
"s_lshr_b32 s60, s45, 3\ns_lshl2_add_u32 s60, s60, s40\ns_lshl_b32 s60, s60, 8\nv_lshl_add_u32 v224, v252, 3, s60\nv_add_u32 v224, Lq675+4096-LqPC, v224\ns_mov_b64 exec, 3\n"
"global_load_dword v224, v224, s[98:99]\ns_mov_b64 exec, -1\nN 76,148,172\nbuffer_load_dwordx4 v221, s[4:7], s63 offen offset:1024 lds\nN 72,128,168\nR 166,191,45056\n"
"s_add_u32 m0, s50, 32768\nN 72,144,172\nG 4\nN 80,128,176\nR 168,188,49152\ns_add_u32 s63, s52, 2557952\n"
"N 80,144,180\nbuffer_load_dwordx4 a[244:247], v252, s[20:23], s65 offen offset:3072 sc1\nN 84,132,176\nR 170,188,53248\nN 84,148,180\nbuffer_load_dwordx4 v220, s[4:7], s63 offen lds\n"
"N 112,136,176\nR 172,184,0\nN 112,152,180\nbuffer_load_dwordx4 v221, s[4:7], s63 offen offset:1024 lds\nN 116,140,176\nR 174,184,4096\n"
"N 116,156,180\nG 4\nN 124,140,160\nR 176,189,49152\nN 124,156,164\nbuffer_load_dwordx4 a[248:251], v252, s[20:23], s66 offen offset:3072 sc1\n"
"N 120,136,160\nR 178,189,53248\nN 120,152,164\nN 92,132,160\nR 180,185,0\nN 92,148,164\n"
"buffer_load_dwordx4 a[252:255], v252, s[20:23], s67 offen offset:3072 sc1\nN 88,128,160\nR 182,185,4096\nN 88,144,164\nG 4\nM 0,168,160\n"
"R 160,190,49152\nM 0,172,176\nM 16,168,164\nR 162,190,53248\nM 16,172,180\ns_lshr_b32 s84, s49, 3\n"
"M 32,168,168\nR 164,186,0\ns_add_u32 m0, s50, 40960\nM 32,172,184\ns_add_u32 s84, s84, 82837504\nM 48,168,172\n"
"R 166,186,4096\ns_add_u32 s63, s52, 2754560\nM 48,172,188\ns_waitcnt vmcnt(11)\ns_barrier\nG 4\n"
"M 52,176,172\nR 168,191,49152\nM 52,180,188\nbuffer_load_dwordx4 v220, s[4:7], s63 offen lds\nM 36,176,168\nR 170,191,53248\n"
"M 36,180,184\nbuffer_load_dwordx4 v[128:131], v252, s[20:23], s84 offen offset:0 sc1\nM 20,176,164\nR 172,187,0\nM 20,180,180\nbuffer_load_dwordx4 v[132:135], v252, s[20:23], s84 offen offset:1024 sc1\n"
"M 4,176,160\nR 174,187,4096\nM 4,180,176\nG 4\nM 8,160,160\nR 176,184,8192\n"
"buffer_load_dwordx4 v221, s[4:7], s63 offen offset:1024 lds\nM 8,164,176\nbuffer_load_dwordx4 v[136:139], v252, s[20:23], s84 offen offset:2048 sc1\ns_add_u32 s63, s52, 2951168\nM 24,160,164\nR 178,184,12288\n"
"s_add_u32 m0, s50, 49152\nM 24,164,180\nbuffer_load_dwordx4 v[140:143], v252, s[20:23], s84 offen offset:3072 sc1\nM 40,160,168\nR 180,184,16384\nbuffer_load_dwordx4 v220, s[4:7], s63 offen lds\n"
"M 40,164,184\ns_lshr_b32 s85, s49, 3\nM 56,160,172\nR 182,184,20480\ns_add_u32 s85, s85, 82841600\nM 56,164,188\n"
"buffer_load_dwordx4 v[144:147], v252, s[20:23], s85 offen offset:0 sc1\nG 4\nM 60,168,172\nR 160,185,8192\nbuffer_load_dwordx4 v221, s[4:7], s63 offen offset:1024 lds\nM 60,172,188\n"
"buffer_load_dwordx4 v[148:151], v252, s[20:23], s85 offen offset:1024 sc1\nM 44,168,168\nR 162,185,12288\nM 44,172,184\nM 28,168,164\nR 164,185,16384\n"
"M 28,172,180\nbuffer_load_dwordx4 v[152:155], v252, s[20:23], s85 offen offset:2048 sc1\nM 12,168,160\nR 166,185,20480\nM 12,172,176\nG 4\n"
"N 64,160,176\nR 168,186,8192\nN 64,176,180\nbuffer_load_dwordx4 v[156:159], v252, s[20:23], s85 offen offset:3072 sc1\nN 68,164,176\nR 170,186,12288\n"
"N 68,180,180\ns_add_u32 m0, s50, 57344\nN 96,168,176\nR 172,186,16384\nN 96,184,180\ns_add_u32 s63, s52, 2363392\n"
"N 100,172,176\nR 174,186,20480\nN 100,188,180\ns_waitcnt vmcnt(14)\ns_barrier\nG 4\n"
"N 108,172,160\nR 176,187,8192\nN 108,188,164\nbuffer_load_dwordx4 v220, s[4:7], s63 offen lds\nN 104,168,160\nR 178,187,12288\n"
"N 104,184,164\nN 76,164,160\nR 180,187,16384\nN 76,180,164\nbuffer_load_dwordx4 v221, s[4:7], s63 offen offset:1024 lds\nN 72,160,160\n"
"R 182,187,20480\ns_add_u32 m0, s50, 65536\nN 72,176,164\nG 4\nN 80,160,168\nR 160,184,24576\n"
"s_add_u32 s63, s52, 2560000\nN 80,176,172\nbuffer_load_dwordx4 v220, s[4:7], s63 offen lds\nN 84,164,168\nR 162,184,28672\nN 84,180,172\n"
"N 112,168,168\nR 164,184,32768\nN 112,184,172\nbuffer_load_dwordx4 v221, s[4:7], s63 offen offset:1024 lds\nN 116,172,168\nR 166,184,36864\n"
"N 116,188,172\nG 4\nN 124,172,176\nR 168,185,24576\nN 124,188,180\nN 120,168,176\n"
"R 170,185,28672\nN 120,184,180\ns_add_u32 m0, s50, 73728\nN 92,164,176\nR 172,185,32768\nN 92,180,180\n"
"s_add_u32 s63, s52, 2756608\nN 88,160,176\nR 174,185,36864\nN 88,176,180\nG 4\nM 0,160,192\n"
"R 176,186,24576\nM 0,164,208\nbuffer_load_dwordx4 v220, s[4:7], s63 offen lds\nM 16,160,196\nR 178,186,28672\nM 16,164,212\n"
"M 32,160,200\nR 180,186,32768\nM 32,164,216\nbuffer_load_dwordx4 v221, s[4:7], s63 offen offset:1024 lds\nM 48,160,204\nR 182,186,36864\n"
"s_add_u32 m0, s50, 81920\nM 48,164,220\ns_waitcnt vmcnt(9)\ns_barrier\nG 4\nM 52,168,204\n"
"R 160,187,24576\ns_add_u32 s63, s52, 2953216\nM 52,172,220\nbuffer_load_dwordx4 v220, s[4:7], s63 offen lds\nM 36,168,200\nR 162,187,28672\n"
"v_min3_u32 v230, v129, v131, v133\nv_min3_u32 v230, v230, v135, v137\nM 36,172,216\nv_min3_u32 v230, v230, v139, v141\nv_min3_u32 v230, v230, v143, v145\ns_waitcnt vmcnt(9)\n"
"v_min3_u32 v230, v230, v147, v149\nM 20,168,196\nR 164,187,32768\ns_waitcnt vmcnt(8)\nv_min3_u32 v230, v230, v151, v153\ns_waitcnt vmcnt(7)\n"
"v_min3_u32 v230, v230, v155, v157\nM 20,172,212\nbuffer_load_dwordx4 v221, s[4:7], s63 offen offset:1024 lds\nv_min3_u32 v230, v230, v159, v159\nM 4,168,192\nR 166,187,36864\n"
"s_add_u32 m0, s50, 90112\nv_max3_u32 v231, v129, v131, v133\nM 4,172,208\nG 4\nv_max3_u32 v231, v231, v135, v137\nv_max3_u32 v231, v231, v139, v141\n"
"v_max3_u32 v231, v231, v143, v145\nM 8,176,192\nR 168,184,40960\ns_add_u32 s63, s52, 0\nv_max3_u32 v231, v231, v147, v149\nM 8,180,208\n"
"buffer_load_dwordx4 v220, s[4:7], s63 offen lds\nv_max3_u32 v231, v231, v151, v153\nM 24,176,196\nR 170,184,45056\nv_max3_u32 v231, v231, v155, v157\nv_max3_u32 v231, v231, v159, v159\n"
"M 24,180,212\nv_cmp_ne_u32 s[86:87], s46, v230\nv_cmp_ne_u32 s[88:89], s46, v231\nM 40,176,200\nR 172,184,49152\ns_or_b64 s[86:87], s[86:87], s[88:89]\n"
"s_cbranch_scc1 Lq629\nLq630:\nM 40,180,216\nv_add_f32 v128, v128, v144\nv_add_f32 v130, v130, v146\nv_add_f32 v132, v132, v148\n"
"M 56,176,204\nR 174,184,53248\nv_add_f32 v134, v134, v150\nv_add_f32 v136, v136, v152\nM 56,180,220\nG 4\n"
"v_add_f32 v138, v138, v154\nv_add_f32 v140, v140, v156\nv_add_f32 v142, v142, v158\nM 60,160,204\nR 176,185,40960\nv_add_f32 v128, v128, v136\n"
"v_add_f32 v130, v130, v138\nM 60,164,220\nbuffer_load_dwordx4 v221, s[4:7], s63 offen offset:1024 lds\nv_add_f32 v132, v132, v140\nM 44,160,200\nR 178,185,45056\n"
"s_add_u32 s63, s52, 196608\nv_add_f32 v134, v134, v142\nM 44,164,216\ns_add_u32 m0, s50, 98304\nv_add_f32 v128, v128, v132\nv_add_f32 v130, v130, v134\n"
"v_lshrrev_b32 v231, 1, v252\nM 28,160,196\nR 180,185,49152\nv_lshrrev_b32 v230, 2, v252\ns_lshl_b32 s62, s40, 9\ns_add_u32 s62, s62, 159232\n"
"v_add_u32 v231, s62, v231\nM 28,164,212\nbuffer_load_dwordx4 v220, s[4:7], s63 offen lds\nds_write2_b32 v231, v128, v130 offset1:1\nM 12,160,192\nR 182,185,53248\n"
"s_lshl_b32 s62, s40, 9\ns_add_u32 s62, s62, 159232\nv_add_u32 v230, s62, v230\nM 12,164,208\nds_read2st64_b32 v[232:233], v230 offset1:1\nG 6\n"
"M 0,168,224\nR 160,186,40960\nM 0,172,240\nbuffer_load_dwordx4 v221, s[4:7], s63 offen offset:1024 lds\nM 16,168,228\nR 162,186,45056\n"
"s_add_u32 s63, s52, 393216\nM 16,172,244\ns_add_u32 m0, s50, 106496\nM 32,168,232\nR 164,186,49152\nM 32,172,248\n"
"buffer_load_dwordx4 v220, s[4:7], s63 offen lds\nG 3\nv_add_f32 v232, v232, v233\nM 48,168,236\nR 166,186,53248\nv_mov_b32 v233, v232\n"
"s_nop 1\nv_permlane32_swap_b32 v232, v233\nM 48,172,252\ns_waitcnt vmcnt(9)\ns_barrier\nv_mov_b32 v230, v232\n"
"M 52,176,236\nR 168,187,40960\nv_mov_b32 v231, v233\nv_permlane16_swap_b32 v230, v232\nM 52,180,252\nbuffer_load_dwordx4 v221, s[4:7], s63 offen offset:1024 lds\n"
"v_permlane16_swap_b32 v231, v233\nM 36,176,232\nR 170,187,45056\nv_mul_f32 v230, 0xba800000, v230\nv_mul_f32 v214, v230, v230\nM 36,180,248\n"
"s_add_u32 m0, s50, 114688\nv_mul_f32 v231, 0x3a800000, v231\nv_sub_f32 v231, v231, v214\nv_max_f32 v231, 0, v231\nM 20,176,228\nR 172,187,49152\n"
"v_add_f32 v231, 0x3727c5ac, v231\nv_rsq_f32 v234, v231\nM 20,180,244\ns_add_u32 s63, s52, 589824\nv_and_b32 v208, s77, v230\nv_mul_f32 v216, v231, v234\n"
"v_sub_f32 v230, v230, v208\nM 4,176,224\nR 174,187,53248\nv_cvt_pk_bf16_f32 v209, v230, v216\nv_cvt_pk_bf16_f32 v208, v208, v208\nM 4,180,240\n"
"s_mov_b64 s[60:61], 0xffff\nv_cndmask_b32 v208, 0, v208, s[60:61]\nG 4\nv_mov_b32 v214, v234\nM 8,160,224\nR 176,184,57344\n"
"v_mul_f32 v215, 0x3e8293ee, v234\nv_mul_f32 v232, 0xba800000, v232\nM 8,164,240\nbuffer_load_dwordx4 v220, s[4:7], s63 offen lds\nv_mul_f32 v217, v232, v232\nM 24,160,228\n"
"R 178,184,61440\nv_mul_f32 v233, 0x3a800000, v233\nv_sub_f32 v233, v233, v217\nM 24,164,244\ns_mov_b64 s[60:61], 0xffff\nv_cndmask_b32 v209, 0, v209, s[60:61]\n"
"v_max_f32 v233, 0, v233\nv_add_f32 v233, 0x3727c5ac, v233\nM 40,160,232\nR 180,188,0\nv_and_b32 v212, s77, v232\nv_rsq_f32 v235, v233\n"
"M 40,164,248\nbuffer_load_dwordx4 v221, s[4:7], s63 offen offset:1024 lds\nv_mul_f32 v219, v233, v235\nM 56,160,236\nR 182,188,4096\ns_add_u32 m0, s50, 0\n"
"v_sub_f32 v232, v232, v212\nM 56,164,252\nG 4\nv_cvt_pk_bf16_f32 v213, v232, v219\nv_cvt_pk_bf16_f32 v212, v212, v212\nv_mov_b32 v217, v235\n"
"M 60,168,236\nR 160,185,57344\ns_add_u32 s63, s52, 786432\nv_mul_f32 v218, 0x3e8293ee, v235\nM 60,172,252\nbuffer_load_dwordx4 v220, s[4:7], s63 offen lds\n"
"v_bfe_u32 v230, v252, 2, 6\nM 44,168,232\nR 162,185,61440\ns_mov_b64 s[60:61], 0xffff\nv_cndmask_b32 v212, 0, v212, s[60:61]\nM 44,172,248\n"
"s_mov_b64 s[60:61], 0xffff\nv_cndmask_b32 v213, 0, v213, s[60:61]\ns_lshl_b32 s60, s40, 7\ns_add_u32 s60, s60, 161280\nv_add_u32 v230, s60, v230\nM 28,168,228\n"
"R 164,189,0\nds_write_b32 v230, v214 offset:0\nM 28,172,244\nbuffer_load_dwordx4 v221, s[4:7], s63 offen offset:1024 lds\nds_write_b32 v230, v217 offset:64\nM 12,168,224\n"
"R 166,189,4096\ns_mov_b32 s60, 156160\nv_bfe_u32 v246, v252, 4, 4\nv_lshl_add_u32 v246, v246, 3, s60\nM 12,172,240\nds_read_b64 v[238:239], v246 offset:0\n"
"s_waitcnt vmcnt(10) lgkmcnt(0)\ns_barrier\nN 64,192,176\nR 168,186,57344\nds_read_b64 v[240:241], v246 offset:128\nN 64,208,180\n"
"s_mov_b32 m0, s51\nds_read_b64 v[242:243], v246 offset:256\nN 68,196,176\nR 170,186,61440\nds_read_b64 v[244:245], v246 offset:384\nds_write_addtid_b32 v32 offset:0\n"
"N 68,212,180\nds_read_b64 v[246:247], v246 offset:0\nds_write_addtid_b32 v33 offset:256\nN 96,200,176\nR 172,190,0\nds_write_addtid_b32 v34 offset:512\n"
"N 96,216,180\nds_write_addtid_b32 v35 offset:768\nds_write_addtid_b32 v36 offset:1024\nN 100,204,176\nR 174,190,4096\nds_write_addtid_b32 v37 offset:1280\n"
"N 100,220,180\nds_write_addtid_b32 v38 offset:1536\nds_write_addtid_b32 v39 offset:1792\nN 108,204,160\nR 176,187,57344\nds_write_addtid_b32 v40 offset:2048\n"
"N 108,220,164\nds_write_addtid_b32 v41 offset:2304\nds_write_addtid_b32 v42 offset:2560\nN 104,200,160\nR 178,187,61440\nds_write_addtid_b32 v43 offset:2816\n"
"N 104,216,164\nds_write_addtid_b32 v44 offset:3072\nds_write_addtid_b32 v45 offset:3328\nN 76,196,160\nR 180,191,0\nG 15\n"
"v_mfma_f32_16x16x32_bf16 v[0:3], v[238:241], v[208:211], v[0:3]\nds_write_addtid_b32 v46 offset:3584\nN 76,212,164\nv_mfma_f32_16x16x32_bf16 v[4:7], v[240:243], v[208:211], v[4:7]\nds_write_addtid_b32 v47 offset:3840\nds_write_addtid_b32 v48 offset:4096\n"
"N 72,192,160\nR 182,191,4096\nv_mfma_f32_16x16x32_bf16 v[8:11], v[242:245], v[208:211], v[8:11]\nds_write_addtid_b32 v49 offset:4352\nN 72,208,164\nv_mfma_f32_16x16x32_bf16 v[12:15], v[244:247], v[208:211], v[12:15]\n"
"ds_write_addtid_b32 v50 offset:4608\nds_write_addtid_b32 v51 offset:4864\nG 15\nN 80,192,168\nR 160,188,8192\nv_mfma_f32_16x16x32_bf16 v[16:19], v[244:247], v[210:213], v[16:19]\n"
"ds_write_addtid_b32 v52 offset:5120\nN 80,208,172\nv_mfma_f32_16x16x32_bf16 v[20:23], v[238:241], v[210:213], v[20:23]\nds_write_addtid_b32 v53 offset:5376\nds_write_addtid_b32 v54 offset:5632\nN 84,196,168\n"
"R 162,188,12288\nv_mfma_f32_16x16x32_bf16 v[24:27], v[240:243], v[210:213], v[24:27]\nds_write_addtid_b32 v55 offset:5888\nN 84,212,172\nv_mfma_f32_16x16x32_bf16 v[28:31], v[242:245], v[210:213], v[28:31]\nds_write_addtid_b32 v56 offset:6144\n"
"ds_write_addtid_b32 v57 offset:6400\nN 112,200,168\nR 164,188,16384\nds_write_addtid_b32 v58 offset:6656\nN 112,216,172\nds_write_addtid_b32 v59 offset:6912\n"
"ds_write_addtid_b32 v60 offset:7168\nN 116,204,168\nR 166,188,20480\nds_write_addtid_b32 v61 offset:7424\nN 116,220,172\nds_write_addtid_b32 v62 offset:7680\n"
"ds_write_addtid_b32 v63 offset:7936\nG 15\nN 124,204,176\nR 168,189,8192\ns_add_u32 m0, s50, 8192\ns_add_u32 s63, s52, 983040\n"
"N 124,220,180\nbuffer_load_dwordx4 v220, s[4:7], s63 offen lds\nN 120,200,176\nR 170,189,12288\ns_waitcnt vmcnt(7) lgkmcnt(2)\ns_barrier\n"
"N 120,216,180\nds_read2st64_b32 v[32:33], v225 offset0:0 offset1:1\nds_read2st64_b32 v[34:35], v225 offset0:2 offset1:3\nN 92,196,176\nR 172,189,16384\nds_read2st64_b32 v[36:37], v225 offset0:4 offset1:5\n"
"N 92,212,180\nbuffer_load_dwordx4 v221, s[4:7], s63 offen offset:1024 lds\nds_read2st64_b32 v[38:39], v225 offset0:6 offset1:7\nN 88,192,176\nR 174,189,20480\nds_read2st64_b32 v[40:41], v225 offset0:8 offset1:9\n"
"N 88,208,180\nds_read2st64_b32 v[42:43], v225 offset0:10 offset1:11\nds_read2st64_b32 v[44:45], v225 offset0:12 offset1:13\nN 64,224,160\nR 176,190,8192\ns_mov_b32 s60, 156672\n"
"v_bfe_u32 v246, v252, 4, 4\nv_lshl_add_u32 v246, v246, 3, s60\nds_read2st64_b32 v[46:47], v225 offset0:14 offset1:15\nN 64,240,164\nds_read_b64 v[238:239], v246 offset:0\nG 11\n"
"v_add_f32 v0, v0, v32\nv_add_f32 v1, v1, v33\nN 68,228,160\nR 178,190,12288\nds_read_b64 v[240:241], v246 offset:128\nG 12\n"
"v_add_f32 v2, v2, v34\nN 68,244,164\nds_read_b64 v[242:243], v246 offset:256\ns_add_u32 m0, s50, 16384\nv_add_f32 v3, v3, v35\nN 96,232,160\n"
"R 180,190,16384\nds_read_b64 v[244:245], v246 offset:384\nG 13\nv_add_f32 v4, v4, v36\nN 96,248,164\nds_read_b64 v[246:247], v246 offset:0\n"
"s_add_u32 s63, s52, 1179648\nv_add_f32 v5, v5, v37\nN 100,236,160\nR 182,190,20480\nG 14\nv_add_f32 v6, v6, v38\n"
"v_add_f32 v7, v7, v39\nN 100,252,164\nds_read2st64_b32 v[48:49], v225 offset0:16 offset1:17\nG 14\nG 13\nv_add_f32 v8, v8, v40\n"
"N 108,236,168\nR 160,191,8192\nv_add_f32 v9, v9, v41\nG 13\nv_add_f32 v10, v10, v42\nN 108,252,172\n"
"buffer_load_dwordx4 v220, s[4:7], s63 offen lds\nv_add_f32 v11, v11, v43\nN 104,232,168\nR 162,191,12288\nG 13\nv_add_f32 v12, v12, v44\n"
"v_add_f32 v13, v13, v45\nN 104,248,172\nds_read2st64_b32 v[50:51], v225 offset0:18 offset1:19\nG 12\nv_add_f32 v14, v14, v46\nv_add_f32 v15, v15, v47\n"
"N 76,228,168\nR 164,191,16384\nds_read2st64_b32 v[52:53], v225 offset0:20 offset1:21\nN 76,244,172\nG 11\nv_mfma_f32_16x16x32_bf16 v[64:67], v[208:211], v[238:241], v[64:67]\n"
"buffer_load_dwordx4 v221, s[4:7], s63 offen offset:1024 lds\nG 5\nv_add_f32 v16, v16, v48\nN 72,224,168\nR 166,191,20480\ns_add_u32 m0, s50, 24576\n"
"v_add_f32 v17, v17, v49\nN 72,240,172\nv_mfma_f32_16x16x32_bf16 v[72:75], v[208:211], v[240:243], v[72:75]\nds_read2st64_b32 v[54:55], v225 offset0:22 offset1:23\nds_read2st64_b32 v[56:57], v225 offset0:24 offset1:25\nN 80,224,176\n"
"R 168,188,24576\ns_add_u32 s63, s52, 1376256\nds_read2st64_b32 v[58:59], v225 offset0:26 offset1:27\nN 80,240,180\nv_mfma_f32_16x16x32_bf16 v[80:83], v[208:211], v[242:245], v[80:83]\nbuffer_load_dwordx4 v220, s[4:7], s63 offen lds\n"
"G 7\nv_add_f32 v18, v18, v50\nN 84,228,176\nR 170,188,28672\nv_add_f32 v19, v19, v51\nG 6\n"
"v_add_f32 v20, v20, v52\nN 84,244,180\nv_mfma_f32_16x16x32_bf16 v[88:91], v[208:211], v[244:247], v[88:91]\nbuffer_load_dwordx4 v221, s[4:7], s63 offen offset:1024 lds\nv_add_f32 v21, v21, v53\nN 112,232,176\n"
"R 172,188,32768\ns_add_u32 s63, s52, 1572864\nds_read2st64_b32 v[60:61], v225 offset0:28 offset1:29\nN 112,248,180\nv_mfma_f32_16x16x32_bf16 v[68:71], v[210:213], v[244:247], v[68:71]\ns_add_u32 m0, s50, 49152\n"
"ds_read2st64_b32 v[62:63], v225 offset0:30 offset1:31\nG 7\nv_add_f32 v22, v22, v54\nN 116,236,176\nR 174,188,36864\nv_add_f32 v23, v23, v55\n"
"G 7\nv_add_f32 v24, v24, v56\nN 116,252,180\nv_mfma_f32_16x16x32_bf16 v[76:79], v[210:213], v[238:241], v[76:79]\nv_add_f32 v25, v25, v57\nG 5\n"
"v_add_f32 v26, v26, v58\nv_add_f32 v27, v27, v59\nN 124,236,160\nR 176,189,24576\nN 124,252,164\nv_mfma_f32_16x16x32_bf16 v[84:87], v[210:213], v[240:243], v[84:87]\n"
"buffer_load_dwordx4 v220, s[4:7], s63 offen lds\nG 3\nv_add_f32 v28, v28, v60\nv_add_f32 v29, v29, v61\nG 2\nv_add_f32 v30, v30, v62\n"
"v_add_f32 v31, v31, v63\nN 120,232,160\nR 178,189,28672\ns_lshl4_add_u32 s64, s41, s42\nv_lshrrev_b32 v227, 1, v252\nN 120,248,164\n"
"v_mfma_f32_16x16x32_bf16 v[92:95], v[210:213], v[242:245], v[92:95]\nbuffer_load_dwordx4 v221, s[4:7], s63 offen offset:1024 lds\nv_mul_f32 v16, v16, v217\nN 92,228,160\nR 180,189,32768\ns_add_u32 m0, s50, 57344\n"
"s_add_u32 s63, s52, 1769472\nv_mul_f32 v17, v17, v217\nN 92,244,164\nbuffer_load_dwordx4 v220, s[4:7], s63 offen lds\nv_mul_f32 v18, v18, v217\nN 88,224,160\n"
"R 182,189,36864\ns_lshl_b32 s64, s64, 12\nv_mul_f32 v19, v19, v217\nN 88,240,164\ns_waitcnt vmcnt(11)\ns_barrier\n"
"G 4\nv_mul_f32 v20, v20, v217\nv_mfma_f32_16x16x32_bf16 v[128:131], v[168:171], a[0:3], 0\nR 160,190,24576\ns_add_u32 s64, s64, 78643200\nv_mul_f32 v21, v21, v217\n"
"M 128,172,16\ns_mov_b32 m0, s51\ns_cmp_eq_u32 s40, 3\ns_cselect_b64 s[66:67], -1, 0\nv_mul_f32 v22, v22, v217\nv_mfma_f32_16x16x32_bf16 v[144:147], v[168:171], a[4:7], 0\n"
"R 162,190,28672\nv_mul_f32 v23, v23, v217\nv_mul_f32 v24, v24, v217\nM 144,172,20\nv_mul_f32 v25, v25, v217\nv_mul_f32 v26, v26, v217\n"
"v_mul_f32 v27, v27, v217\nv_mfma_f32_16x16x32_bf16 v[32:35], v[168:171], a[8:11], 0\nR 164,190,32768\nv_mul_f32 v28, v28, v217\nv_mul_f32 v29, v29, v217\nM 32,172,24\n"
"v_mul_f32 v30, v30, v217\nv_mul_f32 v31, v31, v217\nv_cvt_pk_bf16_f32 v16, v16, v17\nv_mfma_f32_16x16x32_bf16 v[48:51], v[168:171], a[12:15], 0\nR 166,190,36864\nv_cvt_pk_bf16_f32 v17, v18, v19\n"
"v_cvt_pk_bf16_f32 v18, v20, v21\nM 48,172,28\nds_write_addtid_b32 v96 offset:0\nG 5\nv_cvt_pk_bf16_f32 v19, v22, v23\nv_cvt_pk_bf16_f32 v20, v24, v25\n"
"v_cvt_pk_bf16_f32 v21, v26, v27\nv_mfma_f32_16x16x32_bf16 v[52:55], v[176:179], a[12:15], 0\nR 168,191,24576\nds_write_addtid_b32 v97 offset:256\nv_cvt_pk_bf16_f32 v22, v28, v29\nv_cvt_pk_bf16_f32 v23, v30, v31\n"
"M 52,180,28\nds_write_addtid_b32 v98 offset:512\nv_mul_f32 v0, v0, v214\nv_mul_f32 v1, v1, v214\nv_mul_f32 v2, v2, v214\nv_mfma_f32_16x16x32_bf16 v[36:39], v[176:179], a[8:11], 0\n"
"R 170,191,28672\nds_write_addtid_b32 v99 offset:768\nv_mul_f32 v3, v3, v214\nv_mul_f32 v4, v4, v214\nM 36,180,24\nds_write_addtid_b32 v100 offset:1024\n"
"v_mul_f32 v5, v5, v214\nv_mul_f32 v6, v6, v214\nv_mul_f32 v7, v7, v214\nv_mfma_f32_16x16x32_bf16 v[148:151], v[176:179], a[4:7], 0\nR 172,191,32768\nds_write_addtid_b32 v101 offset:1280\n"
"v_mul_f32 v8, v8, v214\nv_mul_f32 v9, v9, v214\nM 148,180,20\nds_write_addtid_b32 v102 offset:1536\nv_mul_f32 v10, v10, v214\nv_mul_f32 v11, v11, v214\n"
"v_mul_f32 v12, v12, v214\nv_mfma_f32_16x16x32_bf16 v[132:135], v[176:179], a[0:3], 0\nR 174,191,36864\nds_write_addtid_b32 v103 offset:1792\nv_mul_f32 v13, v13, v214\nv_mul_f32 v14, v14, v214\n"
"M 132,180,16\nds_write_addtid_b32 v104 offset:2048\nG 13\nv_mul_f32 v15, v15, v214\nv_cvt_pk_bf16_f32 v0, v0, v1\nv_cvt_pk_bf16_f32 v1, v2, v3\n"
"v_mfma_f32_16x16x32_bf16 v[136:139], v[160:163], a[0:3], 0\nR 176,188,40960\nds_write_addtid_b32 v105 offset:2304\nv_cvt_pk_bf16_f32 v2, v4, v5\nv_cvt_pk_bf16_f32 v3, v6, v7\nM 136,164,16\n"
"ds_write_addtid_b32 v106 offset:2560\nv_cvt_pk_bf16_f32 v4, v8, v9\nv_cvt_pk_bf16_f32 v5, v10, v11\nv_cvt_pk_bf16_f32 v6, v12, v13\nv_mfma_f32_16x16x32_bf16 v[152:155], v[160:163], a[4:7], 0\nR 178,188,45056\n"
"ds_write_addtid_b32 v107 offset:2816\ns_mov_b64 exec, s[66:67]\nbuffer_store_dwordx4 v[16:19], v252, s[20:23], s64 offen offset:0 sc1\nbuffer_store_dwordx4 v[20:23], v252, s[20:23], s64 offen offset:1024 sc1\ns_mov_b64 exec, -1\nv_cvt_pk_bf16_f32 v7, v14, v15\n"
"M 152,164,20\nds_write_addtid_b32 v108 offset:3072\nds_write_addtid_b32 v109 offset:3328\nv_mfma_f32_16x16x32_bf16 v[40:43], v[160:163], a[8:11], 0\nR 180,188,49152\nds_write_addtid_b32 v110 offset:3584\n"
"M 40,164,24\nds_write_addtid_b32 v111 offset:3840\nds_write_addtid_b32 v112 offset:4096\nv_mfma_f32_16x16x32_bf16 v[56:59], v[160:163], a[12:15], 0\nR 182,188,53248\nds_write_addtid_b32 v113 offset:4352\n"
"M 56,164,28\nds_write_addtid_b32 v114 offset:4608\nds_write_addtid_b32 v115 offset:4864\nG 15\nv_mfma_f32_16x16x32_bf16 v[60:63], v[168:171], a[12:15], 0\nR 160,189,40960\n"
"ds_write_addtid_b32 v116 offset:5120\nM 60,172,28\nds_write_addtid_b32 v117 offset:5376\nds_write_addtid_b32 v118 offset:5632\nv_mfma_f32_16x16x32_bf16 v[44:47], v[168:171], a[8:11], 0\nR 162,189,45056\n"
"ds_write_addtid_b32 v119 offset:5888\nM 44,172,24\nds_write_addtid_b32 v120 offset:6144\nds_write_addtid_b32 v121 offset:6400\nds_write_addtid_b32 v122 offset:6656\nds_write_addtid_b32 v123 offset:6912\n"
"ds_write_addtid_b32 v124 offset:7168\nds_write_addtid_b32 v125 offset:7424\nds_write_addtid_b32 v126 offset:7680\nds_write_addtid_b32 v127 offset:7936\nv_mfma_f32_16x16x32_bf16 v[156:159], v[168:171], a[4:7], 0\nR 164,189,49152\n"
"s_add_u32 m0, s50, 57344\nM 156,172,20\nbuffer_load_dwordx4 v221, s[4:7], s63 offen offset:1024 lds\nv_mfma_f32_16x16x32_bf16 v[140:143], v[168:171], a[0:3], 0\nR 166,189,53248\ns_waitcnt vmcnt(10) lgkmcnt(2)\n"
"s_barrier\nM 140,172,16\nds_read2st64_b32 v[96:97], v225 offset0:0 offset1:1\nds_read2st64_b32 v[98:99], v225 offset0:2 offset1:3\nv_lshrrev_b32 v226, 8, v252\nM 128,176,32\n"
"R 168,190,40960\ns_add_u32 s63, s52, 1966080\nds_read2st64_b32 v[100:101], v225 offset0:4 offset1:5\ns_lshl_b32 s60, s40, 7\ns_add_u32 s60, s60, 161280\nv_lshl_add_u32 v226, v226, 4, s60\n"
"M 128,180,48\ns_add_u32 m0, s50, 65536\nds_read2st64_b32 v[102:103], v225 offset0:6 offset1:7\nds_read2st64_b32 v[104:105], v225 offset0:8 offset1:9\nds_read_b128 v[230:233], v226 offset:0\nM 144,176,36\n"
"R 170,190,45056\nds_read2st64_b32 v[106:107], v225 offset0:10 offset1:11\nds_read_b128 v[234:237], v226 offset:64\nM 144,180,52\nbuffer_load_dwordx4 v220, s[4:7], s63 offen lds\nds_read2st64_b32 v[108:109], v225 offset0:12 offset1:13\n"
"M 32,176,40\nR 172,190,49152\nds_read2st64_b32 v[110:111], v225 offset0:14 offset1:15\nM 32,180,56\nds_read2st64_b32 v[112:113], v225 offset0:16 offset1:17\nG 13\n"
"v_add_f32 v64, v64, v96\nv_add_f32 v65, v65, v97\nM 48,176,44\nR 174,190,53248\nG 13\nv_add_f32 v66, v66, v98\n"
"v_add_f32 v67, v67, v99\nM 48,180,60\nds_read2st64_b32 v[114:115], v225 offset0:18 offset1:19\nG 12\nv_add_f32 v68, v68, v100\nM 52,160,44\n"
"R 176,191,40960\nv_add_f32 v69, v69, v101\nG 12\nv_add_f32 v70, v70, v102\nM 52,164,60\nbuffer_load_dwordx4 v221, s[4:7], s63 offen offset:1024 lds\n"
"v_add_f32 v71, v71, v103\nM 36,160,40\nR 178,191,45056\nG 12\nv_add_f32 v72, v72, v104\nv_add_f32 v73, v73, v105\n"
"M 36,164,56\ns_add_u32 m0, s50, 73728\nds_read2st64_b32 v[116:117], v225 offset0:20 offset1:21\nG 10\nv_add_f32 v74, v74, v106\nM 148,160,36\n"
"R 180,191,49152\nv_add_f32 v75, v75, v107\nG 9\nv_add_f32 v76, v76, v108\nM 148,164,52\ns_add_u32 s63, s52, 2162688\n"
"ds_read2st64_b32 v[118:119], v225 offset0:22 offset1:23\nv_add_f32 v77, v77, v109\nM 132,160,32\nR 182,191,53248\nG 9\nv_add_f32 v78, v78, v110\n"
"v_add_f32 v79, v79, v111\nM 132,164,48\nds_read2st64_b32 v[120:121], v225 offset0:24 offset1:25\nG 8\nv_add_f32 v80, v80, v112\nM 136,168,32\n"
"R 160,184,0\nv_add_f32 v81, v81, v113\nG 8\nv_add_f32 v82, v82, v114\nM 136,172,48\nbuffer_load_dwordx4 v220, s[4:7], s63 offen lds\n"
"v_add_f32 v83, v83, v115\nM 152,168,36\nR 162,184,4096\nG 6\nv_add_f32 v84, v84, v116\nv_add_f32 v85, v85, v117\n"
"M 152,172,52\nds_read2st64_b32 v[122:123], v225 offset0:26 offset1:27\nG 5\nv_add_f32 v86, v86, v118\nv_add_f32 v87, v87, v119\nM 40,168,40\n"
"R 164,184,8192\nds_read2st64_b32 v[124:125], v225 offset0:28 offset1:29\nM 40,172,56\nbuffer_load_dwordx4 v221, s[4:7], s63 offen offset:1024 lds\nG 5\nv_add_f32 v88, v88, v120\n"
"M 56,168,44\nR 166,184,12288\ns_add_u32 m0, s50, 81920\nM 56,172,60\nds_read2st64_b32 v[126:127], v225 offset0:30 offset1:31\nv_add_f32 v89, v89, v121\n"
"M 60,176,44\nR 168,185,0\ns_add_u32 s63, s52, 2359296\nM 60,180,60\nbuffer_load_dwordx4 v220, s[4:7], s63 offen lds\nM 44,176,40\n"
"R 170,185,4096\nG 6\nv_add_f32 v90, v90, v122\nM 44,180,56\nv_add_f32 v91, v91, v123\nG 4\n"
"v_add_f32 v92, v92, v124\nM 156,176,36\nR 172,185,8192\nv_add_f32 v93, v93, v125\nM 156,180,52\nbuffer_load_dwordx4 v221, s[4:7], s63 offen offset:1024 lds\n"
"M 140,176,32\nR 174,185,12288\ns_add_u32 m0, s50, 90112\nM 140,180,48\nG 5\nG 4\n"
"v_add_f32 v94, v94, v126\nv_add_f32 v95, v95, v127\nM 128,160,64\nR 176,186,0\ns_add_u32 s63, s52, 2555904\nv_mul_f32 v64, v64, v230\n"
"M 128,164,80\nbuffer_load_dwordx4 v220, s[4:7], s63 offen lds\nv_mul_f32 v65, v65, v231\nM 144,160,68\nR 178,186,4096\nv_mul_f32 v66, v66, v232\n"
"v_mul_f32 v67, v67, v233\nM 144,164,84\nv_mul_f32 v68, v68, v234\nv_mul_f32 v69, v69, v235\nv_mul_f32 v70, v70, v236\nM 32,160,72\n"
"R 180,186,8192\nv_mul_f32 v71, v71, v237\nv_cvt_pk_bf16_f32 v67, v66, v67\nM 32,164,88\nv_cvt_pk_bf16_f32 v66, v64, v65\nv_cvt_pk_bf16_f32 v68, v68, v69\n"
"v_cvt_pk_bf16_f32 v69, v70, v71\nM 48,160,76\nR 182,186,12288\nv_mul_f32 v72, v72, v230\nv_mul_f32 v73, v73, v231\nM 48,164,92\n"
"G 4\nv_mul_f32 v74, v74, v232\nv_mul_f32 v75, v75, v233\nv_mul_f32 v76, v76, v234\nM 52,168,76\nR 160,187,0\n"
"v_mul_f32 v77, v77, v235\nv_mul_f32 v78, v78, v236\nM 52,172,92\nbuffer_load_dwordx4 v221, s[4:7], s63 offen offset:1024 lds\nv_mul_f32 v79, v79, v237\nM 36,168,72\n"
"R 162,187,4096\ns_add_u32 s63, s52, 2752512\nv_cvt_pk_bf16_f32 v72, v72, v73\nM 36,172,88\ns_add_u32 m0, s50, 98304\nv_cvt_pk_bf16_f32 v73, v74, v75\n"
"v_cvt_pk_bf16_f32 v74, v76, v77\nv_cvt_pk_bf16_f32 v75, v78, v79\nM 148,168,68\nR 164,187,8192\nv_mul_f32 v80, v80, v230\nv_mul_f32 v81, v81, v231\n"
"M 148,172,84\nbuffer_load_dwordx4 v220, s[4:7], s63 offen lds\nv_mul_f32 v82, v82, v232\nM 132,168,64\nR 166,187,12288\ns_waitcnt vmcnt(9)\n"
"s_barrier\nv_mul_f32 v83, v83, v233\nM 132,172,80\nG 4\nv_mul_f32 v84, v84, v234\nv_mul_f32 v85, v85, v235\n"
"v_mul_f32 v86, v86, v236\nM 136,176,64\nR 168,184,16384\nv_mul_f32 v87, v87, v237\nv_cvt_pk_bf16_f32 v78, v80, v81\nM 136,180,80\n"
"s_mov_b64 exec, s[66:67]\nbuffer_store_dwordx2 v[68:69], v227, s[20:23], s64 offen offset:2048 sc1\nbuffer_store_dwordx2 v[74:75], v227, s[20:23], s64 offen offset:2560 sc1\ns_mov_b64 exec, -1\nv_cvt_pk_bf16_f32 v79, v82, v83\nv_cvt_pk_bf16_f32 v80, v84, v85\n"
"M 152,176,68\nR 170,184,20480\nv_cvt_pk_bf16_f32 v81, v86, v87\nv_mul_f32 v88, v88, v230\nM 152,180,84\nv_mul_f32 v89, v89, v231\n"
"v_mul_f32 v90, v90, v232\nv_mul_f32 v91, v91, v233\nM 40,176,72\nR 172,184,24576\ns_mov_b32 s60, 155648\nv_bfe_u32 v246, v252, 4, 4\n"
"v_lshl_add_u32 v246, v246, 3, s60\nv_mul_f32 v92, v92, v234\nM 40,180,88\nbuffer_load_dwordx4 v221, s[4:7], s63 offen offset:1024 lds\nv_mul_f32 v93, v93, v235\nM 56,176,76\n"
"R 174,184,28672\nds_read_b64 v[238:239], v246 offset:0\nv_mul_f32 v94, v94, v236\nM 56,180,92\nG 5\nv_mul_f32 v95, v95, v237\n"
"v_cvt_pk_bf16_f32 v84, v88, v89\nv_cvt_pk_bf16_f32 v85, v90, v91\nM 60,160,76\nR 176,185,16384\nds_read_b64 v[240:241], v246 offset:128\nv_cvt_pk_bf16_f32 v86, v92, v93\n"
"M 60,164,92\ns_add_u32 m0, s50, 106496\nv_cvt_pk_bf16_f32 v87, v94, v95\ns_mov_b64 exec, s[66:67]\nbuffer_store_dwordx2 v[80:81], v227, s[20:23], s64 offen offset:3072 sc1\nbuffer_store_dwordx2 v[86:87], v227, s[20:23], s64 offen offset:3584 sc1\n"
"s_mov_b64 exec, -1\nv_add_u32 v108, 161792, v252\nM 44,160,72\nR 178,185,20480\nds_read_b64 v[242:243], v246 offset:256\nM 44,164,88\n"
"s_add_u32 s63, s52, 2949120\nds_read_b128 v[112:115], v108\nM 156,160,68\nR 180,185,24576\nds_read_b64 v[244:245], v246 offset:384\nM 156,164,84\n"
"buffer_load_dwordx4 v220, s[4:7], s63 offen lds\nM 140,160,64\nR 182,185,28672\nds_read_b64 v[246:247], v246 offset:0\nM 140,164,80\nds_read_b128 v[104:107], v108 offset:1024\n"
"G 11\nM 128,168,96\nR 160,186,16384\nM 128,172,112\ns_lshl_b32 s60, s40, 12\ns_add_u32 s60, s60, 69632\n"
"v_add_u32 v230, s60, v252\nv_add_u32 v231, s60, v227\nM 144,168,100\nR 162,186,20480\nM 144,172,116\ns_not_b64 exec, s[66:67]\n"
"ds_write_b128 v230, v[16:19] offset:0\nds_write_b128 v230, v[20:23] offset:1024\ns_mov_b64 exec, -1\nM 32,168,104\nR 164,186,24576\nG 13\n"
"v_mfma_f32_16x16x32_bf16 v[128:131], v[238:241], v[208:211], v[128:131]\nM 32,172,120\nbuffer_load_dwordx4 v221, s[4:7], s63 offen offset:1024 lds\nM 48,168,108\nR 166,186,28672\nG 12\n"
"v_mfma_f32_16x16x32_bf16 v[132:135], v[240:243], v[208:211], v[132:135]\nG 6\nv_lshlrev_b32 v100, 16, v104\nM 48,172,124\nv_and_b32 v101, s77, v104\nv_lshlrev_b32 v102, 16, v105\n"
"M 52,176,108\nR 168,187,16384\nv_mfma_f32_16x16x32_bf16 v[144:147], v[244:247], v[210:213], v[144:147]\nv_and_b32 v103, s77, v105\nM 52,180,124\nv_lshlrev_b32 v96, 16, v106\n"
"v_and_b32 v97, s77, v106\nM 36,176,104\nR 170,187,20480\nv_mfma_f32_16x16x32_bf16 v[148:151], v[238:241], v[210:213], v[148:151]\nv_lshlrev_b32 v98, 16, v107\nM 36,180,120\n"
"s_movk_i32 s63, 2048\nv_mad_u32_u24 v220, v228, s63, v229\nv_add_u32 v221, 1047552, v220\ns_add_u32 m0, s50, 114688\ns_add_u32 s63, s53, 0\nbuffer_load_dwordx4 v220, s[8:11], s63 offen lds\n"
"M 148,176,100\nR 172,187,24576\nv_and_b32 v99, s77, v107\nM 148,180,116\nv_add_f32 v104, s78, v100\nv_add_f32 v105, s78, v101\n"
"M 132,176,96\nR 174,187,28672\nv_add_f32 v106, s78, v102\nM 132,180,112\nG 4\nv_add_f32 v107, s78, v103\n"
"M 136,160,96\nR 176,184,49152\nM 136,164,112\ns_not_b64 exec, s[66:67]\nds_write_b64 v231, v[68:69] offset:2048\nds_write_b64 v231, v[74:75] offset:2560\n"
"s_mov_b64 exec, -1\nM 152,160,100\nR 178,184,53248\nM 152,164,116\nbuffer_load_dwordx4 v221, s[8:11], s63 offen offset:1024 lds\nM 40,160,104\n"
"R 180,184,57344\nM 40,164,120\nM 56,160,108\nR 182,184,61440\nM 56,164,124\nG 6\n"
"M 60,168,108\nR 160,185,49152\ns_not_b64 exec, s[66:67]\nds_write_b64 v231, v[80:81] offset:3072\nds_write_b64 v231, v[86:87] offset:3584\ns_mov_b64 exec, -1\n"
"M 60,172,124\nM 44,168,104\nR 162,185,53248\nM 44,172,120\ns_add_u32 m0, s50, 0\nM 156,168,100\n"
"R 164,185,57344\nM 156,172,116\ns_add_u32 s63, s53, 65536\nM 140,168,96\nR 166,185,61440\nM 140,172,112\n"
"G 6\nM 128,176,128\nR 168,186,49152\ns_waitcnt vmcnt(12) lgkmcnt(4)\ns_barrier\nM 128,180,144\n"
"buffer_load_dwordx4 v220, s[8:11], s63 offen lds\nM 144,176,132\nR 170,186,53248\nM 144,180,148\ns_cmp_eq_u32 s78, 0\ns_cbranch_scc1 Lq646\n"
"v_mov_b32 v230, 0\nv_mov_b32 v231, 0\nv_mov_b32 v232, 0\nv_mov_b32 v233, 0\nv_mov_b32 v64, 0\nv_mov_b32 v65, 0\n"
"v_mov_b32 v70, 0\nv_mov_b32 v71, 0\nv_mov_b32 v234, 0\nv_mov_b32 v235, 0\nv_mov_b32 v236, 0\nv_mov_b32 v237, 0\n"
"v_mov_b32 v76, 0\nv_mov_b32 v77, 0\nv_mov_b32 v82, 0\nv_mov_b32 v83, 0\nLq646:\ns_lshl4_add_u32 s65, s41, s42\n"
"M 32,176,136\nR 172,186,57344\ns_lshl_b32 s65, s65, 12\nM 32,180,152\ns_add_u32 s65, s65, 78577664\nM 48,176,140\n"
"R 174,186,61440\nM 48,180,156\nG 4\nM 52,160,140\nR 176,187,49152\nM 52,164,156\n"
"buffer_load_dwordx4 v221, s[8:11], s63 offen offset:1024 lds\nM 36,160,136\nR 178,187,53248\nM 36,164,152\ns_add_u32 m0, s50, 8192\nM 148,160,132\n"
"R 180,187,57344\nM 148,164,148\ns_add_u32 s63, s53, 131072\nM 132,160,128\nR 182,187,61440\nM 132,164,144\n"
"G 4\nM 136,168,128\nR 160,188,0\nM 136,172,144\nbuffer_load_dwordx4 v220, s[8:11], s63 offen lds\nM 152,168,132\n"
"R 162,188,4096\nM 152,172,148\nM 40,168,136\nR 164,188,8192\nM 40,172,152\nM 56,168,140\n"
"R 166,188,12288\nv_mfma_f32_16x16x32_bf16 v[136:139], v[242:245], v[208:211], v[136:139]\nM 56,172,156\nG 4\nM 60,176,140\nR 168,189,0\n"
"v_mfma_f32_16x16x32_bf16 v[140:143], v[244:247], v[208:211], v[140:143]\nM 60,180,156\nbuffer_load_dwordx4 v221, s[8:11], s63 offen offset:1024 lds\nM 44,176,136\nR 170,189,4096\nv_mfma_f32_16x16x32_bf16 v[152:155], v[240:243], v[210:213], v[152:155]\n"
"s_add_u32 s63, s53, 196608\nM 44,180,152\ns_add_u32 m0, s50, 16384\nM 156,176,132\nR 172,189,8192\nv_mfma_f32_16x16x32_bf16 v[156:159], v[242:245], v[210:213], v[156:159]\n"
"M 156,180,148\nbuffer_load_dwordx4 v220, s[8:11], s63 offen lds\nM 140,176,128\nR 174,189,12288\nM 140,180,144\nG 4\n"
"M 128,160,160\nR 176,190,0\nM 128,164,176\nM 144,160,164\nR 178,190,4096\nM 144,164,180\n"
"buffer_load_dwordx4 v221, s[8:11], s63 offen offset:1024 lds\nM 32,160,168\nR 180,190,8192\ns_add_u32 s63, s53, 262144\nM 32,164,184\ns_add_u32 m0, s50, 24576\n"
"M 48,160,172\nR 182,190,12288\nM 48,164,188\nG 4\nM 52,168,172\nR 160,191,0\n"
"M 52,172,188\nbuffer_load_dwordx4 v220, s[8:11], s63 offen lds\nM 36,168,168\nR 162,191,4096\nM 36,172,184\nM 148,168,164\n"
"R 164,191,8192\ns_lshl_b32 s60, s40, 12\ns_add_u32 s60, s60, 65536\nv_add_u32 v226, s60, v252\nv_add_u32 v111, s60, v227\nM 148,172,180\n"
"M 132,168,160\nR 166,191,12288\ns_cmp_eq_u32 s40, 0\ns_cselect_b64 s[88:89], 0, -1\ns_cselect_b32 s60, s41, 0\ns_cmp_lg_u32 s60, 0\n"
"s_cselect_b64 s[66:67], -1, 0\nM 132,172,176\nG 4\nM 136,176,160\nR 168,188,16384\nM 136,180,176\n"
"s_mov_b64 exec, s[88:89]\nds_read_b128 v[230:233], v226 offset:0\nds_read_b64 v[64:65], v111 offset:2048\ns_mov_b64 exec, -1\nM 152,176,164\nR 170,188,20480\n"
"s_waitcnt vmcnt(9) lgkmcnt(1)\ns_barrier\nM 152,180,180\nbuffer_load_dwordx4 v221, s[8:11], s63 offen offset:1024 lds\nM 40,176,168\nR 172,188,24576\n"
"M 40,180,184\ns_mov_b64 exec, s[88:89]\nds_read_b64 v[70:71], v111 offset:2560\nds_read_b128 v[234:237], v226 offset:1024\ns_mov_b64 exec, -1\nM 56,176,172\n"
"R 174,188,28672\nM 56,180,188\nM 60,160,172\nR 176,189,16384\nM 60,164,188\ns_mov_b64 exec, s[88:89]\n"
"ds_read_b64 v[76:77], v111 offset:3072\nds_read_b64 v[82:83], v111 offset:3584\ns_mov_b64 exec, -1\nM 44,160,168\nR 178,189,20480\nM 44,164,184\n"
"M 156,160,164\nR 180,189,24576\nM 156,164,180\nM 140,160,160\nR 182,189,28672\nM 140,164,176\n"
"G 6\nM 32,168,200\nR 160,190,16384\ns_mov_b64 exec, s[66:67]\nbuffer_load_dwordx4 v[230:233], v252, s[20:23], s65 offen offset:0 sc1\nbuffer_load_dwordx2 v[64:65], v227, s[20:23], s65 offen offset:2048 sc1\n"
"s_mov_b64 exec, -1\nM 32,172,216\nR 162,190,20480\nM 48,168,204\nR 164,190,24576\nM 48,172,220\n"
"R 166,190,28672\nG 4\nM 52,176,204\nR 168,191,16384\nM 52,180,220\nR 170,191,20480\n"
"s_mov_b64 exec, s[66:67]\nbuffer_load_dwordx2 v[70:71], v227, s[20:23], s65 offen offset:2560 sc1\nbuffer_load_dwordx4 v[234:237], v252, s[20:23], s65 offen offset:1024 sc1\ns_mov_b64 exec, -1\nM 36,176,200\nR 172,191,24576\n"
"M 36,180,216\nR 174,191,28672\nG 4\nM 40,160,200\nR 176,188,32768\nM 40,164,216\n"
"R 178,188,36864\nM 56,160,204\nR 180,188,40960\ns_mov_b64 exec, s[66:67]\nbuffer_load_dwordx2 v[76:77], v227, s[20:23], s65 offen offset:3072 sc1\nbuffer_load_dwordx2 v[82:83], v227, s[20:23], s65 offen offset:3584 sc1\n"
"s_mov_b64 exec, -1\nM 56,164,220\nR 182,188,45056\nG 4\nM 60,168,204\nR 160,189,32768\n"
"M 60,172,220\nR 162,189,36864\nM 44,168,200\nR 164,189,40960\nM 44,172,216\nR 166,189,45056\n"
"G 4\nM 32,176,232\nR 168,190,32768\nLq677:\ns_lshr_b32 s60, s45, 3\ns_lshl2_add_u32 s60, s60, s40\n"
"s_lshl_b32 s60, s60, 8\nv_lshl_add_u32 v224, v252, 3, s60\nv_add_u32 v224, Lq677+4096-LqPC, v224\ns_mov_b64 exec, 3\nglobal_load_dword v224, v224, s[98:99]\ns_mov_b64 exec, -1\n"
"M 32,180,248\nR 170,190,36864\nM 48,176,236\nR 172,190,40960\nM 48,180,252\nR 174,190,45056\n"
"G 4\nM 52,160,236\nR 176,191,32768\nM 52,164,252\nR 178,191,36864\nM 36,160,232\n"
"R 180,191,40960\nM 36,164,248\nR 182,191,45056\nG 4\nM 40,168,232\nR 160,188,16384\n"
"M 40,172,248\nR 162,188,20480\nM 56,168,236\nR 164,188,24576\nM 56,172,252\nR 166,188,28672\n"
"G 4\nM 60,176,236\nR 168,189,16384\nM 60,180,252\nR 170,189,20480\nM 44,176,232\n"
"R 172,189,24576\nM 44,180,248\nR 174,189,28672\nG 4\nM 128,160,192\nR 176,190,16384\n"
"M 128,164,208\nR 178,190,20480\ns_mov_b32 m0, s51\nM 144,160,196\nR 180,190,24576\nds_write_addtid_b32 v32 offset:0\n"
"M 144,164,212\nR 182,190,28672\nds_write_addtid_b32 v33 offset:256\nG 6\nM 148,168,196\nR 160,191,16384\n"
"ds_write_addtid_b32 v34 offset:512\nM 148,172,212\nR 162,191,20480\nds_write_addtid_b32 v35 offset:768\nM 132,168,192\nR 164,191,24576\n"
"ds_write_addtid_b32 v36 offset:1024\nM 132,172,208\nR 166,191,28672\nds_write_addtid_b32 v37 offset:1280\nG 9\nM 136,176,192\n"
"R 168,188,32768\nds_write_addtid_b32 v38 offset:1536\nM 136,180,208\nR 170,188,36864\nds_write_addtid_b32 v39 offset:1792\nM 152,176,196\n"
"R 172,188,40960\nds_write_addtid_b32 v40 offset:2048\nM 152,180,212\nR 174,188,45056\nds_write_addtid_b32 v41 offset:2304\nG 9\n"
"M 156,160,196\nR 176,189,32768\nds_write_addtid_b32 v42 offset:2560\nM 156,164,212\nR 178,189,36864\nds_write_addtid_b32 v43 offset:2816\n"
"M 140,160,192\nR 180,189,40960\nds_write_addtid_b32 v44 offset:3072\nds_write_addtid_b32 v45 offset:3328\nds_write_addtid_b32 v46 offset:3584\nds_write_addtid_b32 v47 offset:3840\n"
"ds_write_addtid_b32 v48 offset:4096\nds_write_addtid_b32 v49 offset:4352\nds_write_addtid_b32 v50 offset:4608\nds_write_addtid_b32 v51 offset:4864\nds_write_addtid_b32 v52 offset:5120\nds_write_addtid_b32 v53 offset:5376\n"
"ds_write_addtid_b32 v54 offset:5632\nds_write_addtid_b32 v55 offset:5888\nds_write_addtid_b32 v56 offset:6144\nds_write_addtid_b32 v57 offset:6400\nds_write_addtid_b32 v58 offset:6656\nds_write_addtid_b32 v59 offset:6912\n"
"ds_write_addtid_b32 v60 offset:7168\nds_write_addtid_b32 v61 offset:7424\nds_write_addtid_b32 v62 offset:7680\nds_write_addtid_b32 v63 offset:7936\nM 140,164,208\nR 182,189,45056\n"
"G 15\nM 128,168,224\nR 160,190,32768\ns_add_u32 m0, s50, 32768\nM 128,172,240\nR 162,190,36864\n"
"G 3\ns_barrier\ns_add_u32 s63, s53, 327680\nM 144,168,228\nR 164,190,40960\nbuffer_load_dwordx4 v220, s[8:11], s63 offen lds\n"
"ds_read2st64_b32 v[32:33], v225 offset0:0 offset1:1\nM 144,172,244\nR 166,190,45056\nbuffer_load_dwordx4 v221, s[8:11], s63 offen offset:1024 lds\nds_read2st64_b32 v[34:35], v225 offset0:2 offset1:3\nG 6\n"
"M 148,176,228\nR 168,191,32768\ns_add_u32 m0, s50, 40960\nds_read2st64_b32 v[36:37], v225 offset0:4 offset1:5\nM 148,180,244\nR 170,191,36864\n"
"s_add_u32 s63, s53, 393216\nM 132,176,224\nR 172,191,40960\nds_read2st64_b32 v[38:39], v225 offset0:6 offset1:7\nM 132,180,240\nR 174,191,45056\n"
"G 7\nM 136,160,224\nbuffer_load_dwordx4 v220, s[8:11], s63 offen lds\nM 136,164,240\nds_read2st64_b32 v[40:41], v225 offset0:8 offset1:9\nds_read2st64_b32 v[42:43], v225 offset0:10 offset1:11\n"
"M 152,160,228\nbuffer_load_dwordx4 v221, s[8:11], s63 offen offset:1024 lds\nM 152,164,244\nds_read2st64_b32 v[44:45], v225 offset0:12 offset1:13\nds_read2st64_b32 v[46:47], v225 offset0:14 offset1:15\nG 4\n"
"M 156,168,228\nds_read2st64_b32 v[48:49], v225 offset0:16 offset1:17\nds_read2st64_b32 v[50:51], v225 offset0:18 offset1:19\nM 156,172,244\nds_read2st64_b32 v[52:53], v225 offset0:20 offset1:21\nds_read2st64_b32 v[54:55], v225 offset0:22 offset1:23\n"
"M 140,168,224\nds_read2st64_b32 v[56:57], v225 offset0:24 offset1:25\nds_read2st64_b32 v[58:59], v225 offset0:26 offset1:27\nM 140,172,240\nds_read2st64_b32 v[60:61], v225 offset0:28 offset1:29\nds_read2st64_b32 v[62:63], v225 offset0:30 offset1:31\n"
"G 0\ns_barrier\nv_pk_add_f32 v[144:145], v[144:145], v[48:49]\nv_pk_add_f32 v[146:147], v[146:147], v[50:51]\nv_pk_add_f32 v[148:149], v[148:149], v[52:53]\nv_pk_add_f32 v[150:151], v[150:151], v[54:55]\n"
"v_pk_add_f32 v[152:153], v[152:153], v[56:57]\nv_pk_add_f32 v[154:155], v[154:155], v[58:59]\nv_pk_add_f32 v[156:157], v[156:157], v[60:61]\nv_pk_add_f32 v[158:159], v[158:159], v[62:63]\nv_pk_add_f32 v[128:129], v[128:129], v[32:33]\nv_pk_add_f32 v[130:131], v[130:131], v[34:35]\n"
"v_pk_add_f32 v[132:133], v[132:133], v[36:37]\nv_pk_add_f32 v[134:135], v[134:135], v[38:39]\nv_pk_add_f32 v[136:137], v[136:137], v[40:41]\nv_pk_add_f32 v[138:139], v[138:139], v[42:43]\nv_pk_add_f32 v[140:141], v[140:141], v[44:45]\nv_pk_add_f32 v[142:143], v[142:143], v[46:47]\n"
"v_cvt_pk_bf16_f32 v144, v144, v145\nv_cvt_pk_bf16_f32 v145, v146, v147\nv_cvt_pk_bf16_f32 v146, v148, v149\nv_cvt_pk_bf16_f32 v147, v150, v151\nv_cvt_pk_bf16_f32 v152, v152, v153\nv_cvt_pk_bf16_f32 v153, v154, v155\n"
"v_cvt_pk_bf16_f32 v154, v156, v157\nv_cvt_pk_bf16_f32 v155, v158, v159\nv_cvt_pk_bf16_f32 v128, v128, v129\nv_cvt_pk_bf16_f32 v129, v130, v131\nv_cvt_pk_bf16_f32 v130, v132, v133\nv_cvt_pk_bf16_f32 v131, v134, v135\n"
"v_cvt_pk_bf16_f32 v136, v136, v137\nv_cvt_pk_bf16_f32 v137, v138, v139\nv_cvt_pk_bf16_f32 v138, v140, v141\nv_cvt_pk_bf16_f32 v139, v142, v143\nv_mfma_f32_16x16x32_bf16 v[52:55], v[16:19], v[144:147], v[96:99]\nv_mfma_f32_16x16x32_bf16 v[48:51], v[0:3], v[144:147], v[100:103]\n"
"v_mfma_f32_16x16x32_bf16 v[60:63], v[20:23], v[152:155], v[96:99]\nv_mfma_f32_16x16x32_bf16 v[56:59], v[4:7], v[152:155], v[100:103]\ns_waitcnt vmcnt(8)\nv_max3_u32 v248, v233, v65, v71\nv_cmp_eq_u32 vcc, -1, v248\ns_nop 0\n"
"s_cbranch_vccnz Lq679\nLq680:\ns_waitcnt vmcnt(5)\nv_max3_u32 v248, v237, v77, v83\nv_cmp_eq_u32 vcc, -1, v248\ns_nop 0\n"
"s_cbranch_vccnz Lq681\nLq682:\nv_mfma_f32_16x16x32_bf16 v[36:39], v[0:3], v[128:131], v[96:99]\nv_mfma_f32_16x16x32_bf16 v[32:35], v[230:233], v[128:131], v[104:107]\nv_mfma_f32_16x16x32_bf16 v[44:47], v[4:7], v[136:139], v[96:99]\nv_mfma_f32_16x16x32_bf16 v[40:43], v[234:237], v[136:139], v[104:107]\n"
"s_lshl_b32 s84, s42, 11\ns_add_u32 s84, s84, s49\ns_add_u32 s84, s84, 6291456\ns_add_u32 s85, s84, 32768\nv_mov_b32 v238, s46\nv_mov_b32 v239, s47\n"
"v_max_f32 v52, v52, v53\nv_max_f32 v54, v54, v55\nv_max3_f32 v52, v52, v48, v49\nv_max3_f32 v54, v54, v50, v51\nv_max_f32 v118, v52, v54\nv_max_f32 v60, v60, v61\n"
"v_max_f32 v62, v62, v63\nv_max3_f32 v60, v60, v56, v57\nv_max3_f32 v62, v62, v58, v59\nv_max_f32 v119, v60, v62\nv_max_f32 v36, v36, v37\nv_max_f32 v38, v38, v39\n"
"v_max3_f32 v36, v36, v32, v33\nv_max3_f32 v38, v38, v34, v35\nv_max_f32 v116, v36, v38\nv_max_f32 v44, v44, v45\nv_max_f32 v46, v46, v47\nv_max3_f32 v44, v44, v40, v41\n"
"v_max3_f32 v46, v46, v42, v43\nv_max_f32 v117, v44, v46\nv_permlane32_swap_b32 v116, v118\ns_nop 0\nv_permlane32_swap_b32 v117, v119\nv_max_f32 v116, v116, v118\n"
"v_max_f32 v117, v117, v119\ns_nop 1\nv_permlane16_swap_b32 v116, v117\nv_max_f32 v116, v116, v117\nv_mov_b32 v117, v116\ns_nop 1\n"
"v_permlane16_swap_b32 v116, v117\nv_mov_b32 v118, v116\nv_mov_b32 v119, v117\ns_nop 0\nv_permlane32_swap_b32 v116, v118\nv_permlane32_swap_b32 v117, v119\n"
"v_mul_f32 v116, v116, v215\nv_mul_f32 v118, v118, v218\nv_mul_f32 v117, v117, v215\nv_mul_f32 v119, v119, v218\nv_fma_f32 v52, v52, v218, -v118\nv_fma_f32 v54, v54, v218, -v118\n"
"v_fma_f32 v60, v60, v218, -v119\nv_fma_f32 v62, v62, v218, -v119\nv_fma_f32 v36, v36, v215, -v116\nv_fma_f32 v38, v38, v215, -v116\nv_fma_f32 v44, v44, v215, -v117\nv_fma_f32 v46, v46, v215, -v117\n"
"v_exp_f32 v52, v52\nv_exp_f32 v54, v54\nv_exp_f32 v60, v60\nv_exp_f32 v62, v62\nv_exp_f32 v36, v36\nv_exp_f32 v38, v38\n"
"v_exp_f32 v44, v44\nv_exp_f32 v46, v46\nv_add_f32 v118, v52, v54\nv_add_f32 v119, v60, v62\nv_add_f32 v116, v36, v38\nv_add_f32 v117, v44, v46\n"
"s_nop 0\nv_permlane32_swap_b32 v116, v118\nv_permlane32_swap_b32 v117, v119\nv_add_f32 v116, v116, v118\nv_add_f32 v117, v117, v119\ns_nop 1\n"
"v_permlane16_swap_b32 v116, v117\nv_add_f32 v116, v116, v117\nv_rcp_f32 v116, v116\ns_nop 0\nv_mov_b32 v117, v116\ns_nop 1\n"
"v_permlane16_swap_b32 v116, v117\nv_mov_b32 v118, v116\nv_mov_b32 v119, v117\ns_nop 0\nv_permlane32_swap_b32 v116, v118\nv_permlane32_swap_b32 v117, v119\n"
"v_mul_f32 v52, v52, v118\nv_mul_f32 v54, v54, v118\nv_mul_f32 v60, v60, v119\nv_mul_f32 v62, v62, v119\nv_mul_f32 v36, v36, v116\nv_mul_f32 v38, v38, v116\n"
"v_mul_f32 v44, v44, v117\nv_mul_f32 v46, v46, v117\nv_cvt_pk_bf16_f32 v52, v52, v52\nv_cvt_pk_bf16_f32 v54, v54, v54\nv_cvt_pk_bf16_f32 v60, v60, v60\nv_cvt_pk_bf16_f32 v62, v62, v62\n"
"v_cvt_pk_bf16_f32 v36, v36, v36\nv_cvt_pk_bf16_f32 v38, v38, v38\nv_cvt_pk_bf16_f32 v44, v44, v44\nv_cvt_pk_bf16_f32 v46, v46, v46\nv_and_b32 v48, v52, v112\nv_and_b32 v49, v54, v113\n"
"v_and_b32 v50, v52, v114\nv_and_b32 v51, v54, v115\nv_and_b32 v56, v60, v112\nv_and_b32 v57, v62, v113\nv_and_b32 v58, v60, v114\nv_and_b32 v59, v62, v115\n"
"v_mfma_f32_16x16x32_bf16 v[144:147], v[66:69], v[48:51], 0\nv_mfma_f32_16x16x32_bf16 v[148:151], v[72:75], v[48:51], 0\nv_and_b32 v32, v36, v112\nv_and_b32 v33, v38, v113\nv_and_b32 v34, v36, v114\nv_and_b32 v35, v38, v115\n"
"v_mfma_f32_16x16x32_bf16 v[152:155], v[78:81], v[56:59], 0\nv_mfma_f32_16x16x32_bf16 v[156:159], v[84:87], v[56:59], 0\nv_and_b32 v40, v44, v112\nv_and_b32 v41, v46, v113\nv_and_b32 v42, v44, v114\nv_and_b32 v43, v46, v115\n"
"v_mfma_f32_16x16x32_bf16 v[128:131], v[64:67], v[32:35], 0\nv_mfma_f32_16x16x32_bf16 v[132:135], v[70:73], v[32:35], 0\nv_mfma_f32_16x16x32_bf16 v[136:139], v[76:79], v[40:43], 0\nv_mfma_f32_16x16x32_bf16 v[140:143], v[82:85], v[40:43], 0\nv_cvt_pk_bf16_f32 v144, v144, v145\nv_cvt_pk_bf16_f32 v145, v146, v147\n"
"v_cvt_pk_bf16_f32 v146, v148, v149\nv_cvt_pk_bf16_f32 v147, v150, v151\nbuffer_store_dwordx4 v[144:147], v252, s[20:23], s85 offen offset:0 sc1\nv_cvt_pk_bf16_f32 v152, v152, v153\nv_cvt_pk_bf16_f32 v153, v154, v155\nv_cvt_pk_bf16_f32 v154, v156, v157\n"
"v_cvt_pk_bf16_f32 v155, v158, v159\nbuffer_store_dwordx4 v[152:155], v252, s[20:23], s85 offen offset:1024 sc1\nv_cvt_pk_bf16_f32 v128, v128, v129\nv_cvt_pk_bf16_f32 v129, v130, v131\nv_cvt_pk_bf16_f32 v130, v132, v133\nv_cvt_pk_bf16_f32 v131, v134, v135\n"
"buffer_store_dwordx4 v[128:131], v252, s[20:23], s84 offen offset:0 sc1\nv_cvt_pk_bf16_f32 v136, v136, v137\nv_cvt_pk_bf16_f32 v137, v138, v139\nv_cvt_pk_bf16_f32 v138, v140, v141\nv_cvt_pk_bf16_f32 v139, v142, v143\nbuffer_store_dwordx4 v[136:139], v252, s[20:23], s84 offen offset:1024 sc1\n"
"s_add_u32 s61, s79, 88498176\ns_lshl_b32 s62, s42, 3\ns_add_u32 s61, s61, s62\ns_waitcnt vmcnt(0)\ns_mov_b64 exec, 1\nbuffer_atomic_swap_x2 v[238:239], off, s[20:23], s61 sc1\n"
"s_mov_b64 exec, -1\nv_bfe_u32 v250, v252, 7, 1\nv_mul_u32_u24 v250, 2176, v250\nv_bfe_u32 v251, v252, 4, 3\nv_lshl_add_u32 v250, v251, 3, v250\ns_lshl_b32 s60, s90, 6\n"
"v_add_u32 v250, s60, v250\ns_sleep 5\nv_mov_b32 v248, 0\nv_mov_b32 v249, 0\ns_add_u32 s61, s80, 88498176\ns_mov_b64 exec, 0xffff\n"
"buffer_atomic_or_x2 v[248:249], v250, s[20:23], s61 offen sc0 sc1\ns_mov_b64 exec, -1\ns_add_u32 m0, s50, 49152\ns_add_u32 s63, s53, 458752\nbuffer_load_dwordx4 v220, s[8:11], s63 offen lds\nbuffer_load_dwordx4 v221, s[8:11], s63 offen offset:1024 lds\n"
"s_add_u32 m0, s50, 57344\ns_add_u32 s63, s53, 524288\nbuffer_load_dwordx4 v220, s[8:11], s63 offen lds\nbuffer_load_dwordx4 v221, s[8:11], s63 offen offset:1024 lds\ns_add_u32 m0, s50, 65536\ns_add_u32 s63, s53, 589824\n"
"buffer_load_dwordx4 v220, s[8:11], s63 offen lds\nbuffer_load_dwordx4 v221, s[8:11], s63 offen offset:1024 lds\ns_add_u32 m0, s50, 73728\ns_add_u32 s63, s53, 655360\nbuffer_load_dwordx4 v220, s[8:11], s63 offen lds\nbuffer_load_dwordx4 v221, s[8:11], s63 offen offset:1024 lds\n"
"s_add_u32 m0, s50, 81920\ns_add_u32 s63, s53, 720896\nbuffer_load_dwordx4 v220, s[8:11], s63 offen lds\nbuffer_load_dwordx4 v221, s[8:11], s63 offen offset:1024 lds\ns_add_u32 m0, s50, 90112\ns_add_u32 s63, s53, 786432\n"
"buffer_load_dwordx4 v220, s[8:11], s63 offen lds\nbuffer_load_dwordx4 v221, s[8:11], s63 offen offset:1024 lds\nv_bfe_u32 v234, v252, 4, 4\nv_lshrrev_b32 v235, 8, v252\nv_lshlrev_b32 v235, 2, v235\nv_sub_u32 v234, v234, v235\n"
"v_lshlrev_b32 v235, 4, v234\ns_mov_b64 s[60:61], 0x3f80\nv_lshlrev_b64 v[208:209], v235, s[60:61]\nv_cmp_gt_u32 s[60:61], 4, v234\ns_nop 1\nv_cndmask_b32 v212, 0, v208, s[60:61]\n"
"v_cndmask_b32 v208, 0, v208, s[60:61]\nv_cndmask_b32 v213, 0, v209, s[60:61]\nv_cndmask_b32 v209, 0, v209, s[60:61]\ns_nop 1\nv_mfma_f32_16x16x32_bf16 v[0:3], v[208:211], v[192:195], 0\nv_mfma_f32_16x16x32_bf16 v[4:7], v[210:213], v[192:195], 0\n"
"v_mfma_f32_16x16x32_bf16 v[8:11], v[208:211], v[196:199], 0\nv_mfma_f32_16x16x32_bf16 v[12:15], v[210:213], v[196:199], 0\nv_mfma_f32_16x16x32_bf16 v[16:19], v[208:211], v[200:203], 0\nv_mfma_f32_16x16x32_bf16 v[20:23], v[210:213], v[200:203], 0\nv_mfma_f32_16x16x32_bf16 v[24:27], v[208:211], v[204:207], 0\nv_mfma_f32_16x16x32_bf16 v[28:31], v[210:213], v[204:207], 0\n"
"s_waitcnt vmcnt(12)\nv_cmp_eq_u64 s[86:87], s[46:47], v[248:249]\ns_nop 0\ns_and_b32 s62, s86, 0xffff\ns_cmp_eq_u32 s62, 0xffff\ns_cbranch_scc0 Lq684\n"
"Lq685:\ns_add_u32 s64, s70, 6291456\nbuffer_load_dwordx4 a[0:3], v252, s[20:23], s64 offen offset:0 sc1\ns_add_u32 s65, s70, 6324224\nbuffer_load_dwordx4 a[4:7], v252, s[20:23], s65 offen offset:0 sc1\ns_add_u32 s66, s71, 6291456\n"
"buffer_load_dwordx4 a[8:11], v252, s[20:23], s66 offen offset:0 sc1\ns_add_u32 s67, s71, 6324224\nbuffer_load_dwordx4 a[12:15], v252, s[20:23], s67 offen offset:0 sc1\nbuffer_load_dwordx4 a[16:19], v252, s[20:23], s64 offen offset:1024 sc1\nbuffer_load_dwordx4 a[20:23], v252, s[20:23], s65 offen offset:1024 sc1\nbuffer_load_dwordx4 a[24:27], v252, s[20:23], s66 offen offset:1024 sc1\n"
"buffer_load_dwordx4 a[28:31], v252, s[20:23], s67 offen offset:1024 sc1\ns_barrier\nR 160,188,49152\nR 162,188,53248\nR 164,184,0\nR 166,184,4096\n"
"R 168,189,49152\nR 170,189,53248\nR 172,185,0\nR 174,185,4096\nR 176,190,49152\nR 178,190,53248\n"
"R 180,186,0\nR 182,186,4096\nbuffer_load_dwordx4 a[32:35], v252, s[20:23], s64 offen offset:2048 sc1\nbuffer_load_dwordx4 a[36:39], v252, s[20:23], s65 offen offset:2048 sc1\nbuffer_load_dwordx4 a[40:43], v252, s[20:23], s66 offen offset:2048 sc1\nbuffer_load_dwordx4 a[44:47], v252, s[20:23], s67 offen offset:2048 sc1\n"
"buffer_load_dwordx4 a[48:51], v252, s[20:23], s64 offen offset:3072 sc1\nbuffer_load_dwordx4 a[52:55], v252, s[20:23], s65 offen offset:3072 sc1\nbuffer_load_dwordx4 a[56:59], v252, s[20:23], s66 offen offset:3072 sc1\nbuffer_load_dwordx4 a[60:63], v252, s[20:23], s67 offen offset:3072 sc1\ns_add_u32 s64, s70, 6295552\nbuffer_load_dwordx4 a[64:67], v252, s[20:23], s64 offen offset:0 sc1\n"
"s_add_u32 s65, s70, 6328320\nbuffer_load_dwordx4 a[68:71], v252, s[20:23], s65 offen offset:0 sc1\ns_add_u32 s66, s71, 6295552\nbuffer_load_dwordx4 a[72:75], v252, s[20:23], s66 offen offset:0 sc1\ns_add_u32 s67, s71, 6328320\nbuffer_load_dwordx4 a[76:79], v252, s[20:23], s67 offen offset:0 sc1\n"
"s_waitcnt vmcnt(19) lgkmcnt(10)\nM 0,160,0\nbuffer_load_dwordx4 a[80:83], v252, s[20:23], s64 offen offset:1024 sc1\ns_waitcnt vmcnt(16) lgkmcnt(8)\nM 0,164,16\nM 16,160,4\n"
"s_waitcnt vmcnt(15)\nM 16,164,20\nv_mfma_f32_16x16x32_bf16 v[32:35], v[160:163], a[8:11], 0\nbuffer_load_dwordx4 a[84:87], v252, s[20:23], s65 offen offset:1024 sc1\ns_waitcnt vmcnt(15)\nM 32,164,24\n"
"v_mfma_f32_16x16x32_bf16 v[48:51], v[160:163], a[12:15], 0\ns_waitcnt vmcnt(14)\nM 48,164,28\nG 4\nv_mfma_f32_16x16x32_bf16 v[52:55], v[168:171], a[12:15], 0\nR 160,191,49152\n"
"M 52,172,28\nbuffer_load_dwordx4 a[88:91], v252, s[20:23], s66 offen offset:1024 sc1\nv_mfma_f32_16x16x32_bf16 v[36:39], v[168:171], a[8:11], 0\nR 162,191,53248\nM 36,172,24\nM 20,168,4\n"
"R 164,187,0\nM 20,172,20\nbuffer_load_dwordx4 a[92:95], v252, s[20:23], s67 offen offset:1024 sc1\nM 4,168,0\nR 166,187,4096\nM 4,172,16\n"
"G 4\nM 8,176,0\nR 168,184,8192\nM 8,180,16\nbuffer_load_dwordx4 a[96:99], v252, s[20:23], s64 offen offset:2048 sc1\nM 24,176,4\n"
"R 170,184,12288\nM 24,180,20\nv_mfma_f32_16x16x32_bf16 v[40:43], v[176:179], a[8:11], 0\nR 172,184,16384\nM 40,180,24\nbuffer_load_dwordx4 a[100:103], v252, s[20:23], s65 offen offset:2048 sc1\n"
"v_mfma_f32_16x16x32_bf16 v[56:59], v[176:179], a[12:15], 0\nR 174,184,20480\nM 56,180,28\nG 4\nv_mfma_f32_16x16x32_bf16 v[60:63], v[160:163], a[12:15], 0\nR 176,185,8192\n"
"M 60,164,28\nbuffer_load_dwordx4 a[104:107], v252, s[20:23], s66 offen offset:2048 sc1\nv_mfma_f32_16x16x32_bf16 v[44:47], v[160:163], a[8:11], 0\nR 178,185,12288\nM 44,164,24\nM 28,160,4\n"
"R 180,185,16384\nM 28,164,20\nbuffer_load_dwordx4 a[108:111], v252, s[20:23], s67 offen offset:2048 sc1\nM 12,160,0\nR 182,185,20480\nM 12,164,16\n"
"G 4\ns_waitcnt vmcnt(19)\nM 0,168,32\nR 160,186,8192\ns_waitcnt vmcnt(15)\nM 0,172,48\n"
"buffer_load_dwordx4 a[112:115], v252, s[20:23], s64 offen offset:3072 sc1\nM 16,168,36\nR 162,186,12288\ns_waitcnt vmcnt(15)\nM 16,172,52\nM 32,168,40\n"
"R 164,186,16384\ns_waitcnt vmcnt(14)\nM 32,172,56\nbuffer_load_dwordx4 a[116:119], v252, s[20:23], s65 offen offset:3072 sc1\nM 48,168,44\nR 166,186,20480\n"
"s_waitcnt vmcnt(14)\nM 48,172,60\nG 4\nM 52,176,44\nR 168,187,8192\nM 52,180,60\n"
"buffer_load_dwordx4 a[120:123], v252, s[20:23], s66 offen offset:3072 sc1\nM 36,176,40\nR 170,187,12288\nM 36,180,56\nM 20,176,36\nR 172,187,16384\n"
"M 20,180,52\nbuffer_load_dwordx4 a[124:127], v252, s[20:23], s67 offen offset:3072 sc1\nM 4,176,32\nR 174,187,20480\nM 4,180,48\nG 4\n"
"M 8,160,32\nR 176,184,24576\nM 8,164,48\ns_add_u32 s64, s70, 6299648\nbuffer_load_dwordx4 a[128:131], v252, s[20:23], s64 offen offset:0 sc1\nM 24,160,36\n"
"R 178,184,28672\nM 24,164,52\nM 40,160,40\nR 180,184,32768\nM 40,164,56\ns_add_u32 s65, s70, 6332416\n"
"buffer_load_dwordx4 a[132:135], v252, s[20:23], s65 offen offset:0 sc1\nM 56,160,44\nR 182,184,36864\nM 56,164,60\nG 4\nM 60,168,44\n"
"R 160,185,24576\nM 60,172,60\ns_add_u32 s66, s71, 6299648\nbuffer_load_dwordx4 a[136:139], v252, s[20:23], s66 offen offset:0 sc1\nM 44,168,40\nR 162,185,28672\n"
"M 44,172,56\nM 28,168,36\nR 164,185,32768\nM 28,172,52\ns_add_u32 s67, s71, 6332416\nbuffer_load_dwordx4 a[140:143], v252, s[20:23], s67 offen offset:0 sc1\n"
"M 12,168,32\nR 166,185,36864\nM 12,172,48\ns_barrier\nG 4\ns_waitcnt vmcnt(19)\n"
"M 0,176,64\nR 168,186,24576\ns_waitcnt vmcnt(15)\nM 0,180,80\nbuffer_load_dwordx4 a[144:147], v252, s[20:23], s64 offen offset:1024 sc1\nM 16,176,68\n"
"R 170,186,28672\ns_waitcnt vmcnt(15)\nM 16,180,84\nM 32,176,72\nR 172,186,32768\ns_waitcnt vmcnt(14)\n"
"M 32,180,88\nbuffer_load_dwordx4 a[148:151], v252, s[20:23], s65 offen offset:1024 sc1\nM 48,176,76\nR 174,186,36864\ns_waitcnt vmcnt(14)\nM 48,180,92\n"
"G 4\nM 52,160,76\nR 176,187,24576\nM 52,164,92\nbuffer_load_dwordx4 a[152:155], v252, s[20:23], s66 offen offset:1024 sc1\nM 36,160,72\n"
"R 178,187,28672\nM 36,164,88\nM 20,160,68\nR 180,187,32768\nM 20,164,84\nbuffer_load_dwordx4 a[156:159], v252, s[20:23], s67 offen offset:1024 sc1\n"
"M 4,160,64\nR 182,187,36864\nM 4,164,80\nG 4\nM 8,168,64\nR 160,184,40960\n"
"M 8,172,80\nbuffer_load_dwordx4 a[160:163], v252, s[20:23], s64 offen offset:2048 sc1\nM 24,168,68\nR 162,184,45056\nM 24,172,84\nM 40,168,72\n"
"R 164,184,49152\nM 40,172,88\nbuffer_load_dwordx4 a[164:167], v252, s[20:23], s65 offen offset:2048 sc1\nM 56,168,76\nR 166,184,53248\nM 56,172,92\n"
"G 4\nM 60,176,76\nR 168,185,40960\nM 60,180,92\nbuffer_load_dwordx4 a[168:171], v252, s[20:23], s66 offen offset:2048 sc1\nM 44,176,72\n"
"R 170,185,45056\nM 44,180,88\nM 28,176,68\nR 172,185,49152\nM 28,180,84\nbuffer_load_dwordx4 a[172:175], v252, s[20:23], s67 offen offset:2048 sc1\n"
"M 12,176,64\nR 174,185,53248\nM 12,180,80\nG 4\ns_waitcnt vmcnt(19)\nM 0,160,96\n"
"R 176,186,40960\ns_waitcnt vmcnt(15)\nM 0,164,112\nbuffer_load_dwordx4 a[176:179], v252, s[20:23], s64 offen offset:3072 sc1\nM 16,160,100\nR 178,186,45056\n"
"s_add_u32 s63, s53, 851968\ns_waitcnt vmcnt(15)\nM 16,164,116\ns_add_u32 m0, s50, 98304\nM 32,160,104\nR 180,186,49152\n"
"s_waitcnt vmcnt(14)\nM 32,164,120\nbuffer_load_dwordx4 a[180:183], v252, s[20:23], s65 offen offset:3072 sc1\nM 48,160,108\nR 182,186,53248\ns_waitcnt vmcnt(14)\n"
"M 48,164,124\nbuffer_load_dwordx4 v220, s[8:11], s63 offen lds\nG 4\nM 52,168,108\nR 160,187,40960\nM 52,172,124\n"
"buffer_load_dwordx4 a[184:187], v252, s[20:23], s66 offen offset:3072 sc1\nM 36,168,104\nR 162,187,45056\nM 36,172,120\nbuffer_load_dwordx4 v221, s[8:11], s63 offen offset:1024 lds\nM 20,168,100\n"
"R 164,187,49152\ns_add_u32 m0, s50, 106496\nM 20,172,116\nbuffer_load_dwordx4 a[188:191], v252, s[20:23], s67 offen offset:3072 sc1\nM 4,168,96\nR 166,187,53248\n"
"s_add_u32 s63, s53, 917504\nM 4,172,112\nbuffer_load_dwordx4 v220, s[8:11], s63 offen lds\nG 4\nM 8,176,96\nR 168,184,57344\n"
"M 8,180,112\ns_add_u32 s64, s70, 6303744\nbuffer_load_dwordx4 a[192:195], v252, s[20:23], s64 offen offset:0 sc1\nM 24,176,100\nR 170,184,61440\nM 24,180,116\n"
"buffer_load_dwordx4 v221, s[8:11], s63 offen offset:1024 lds\nM 40,176,104\nR 172,188,0\ns_add_u32 m0, s50, 114688\nM 40,180,120\ns_add_u32 s65, s70, 6336512\n"
"buffer_load_dwordx4 a[196:199], v252, s[20:23], s65 offen offset:0 sc1\nM 56,176,108\nR 174,188,4096\ns_add_u32 s63, s53, 983040\nM 56,180,124\nbuffer_load_dwordx4 v220, s[8:11], s63 offen lds\n"
"G 4\nM 60,160,108\nR 176,185,57344\nM 60,164,124\ns_add_u32 s66, s71, 6303744\nbuffer_load_dwordx4 a[200:203], v252, s[20:23], s66 offen offset:0 sc1\n"
"M 44,160,104\nR 178,185,61440\nM 44,164,120\nbuffer_load_dwordx4 v221, s[8:11], s63 offen offset:1024 lds\nM 28,160,100\nR 180,189,0\n"
"M 28,164,116\ns_add_u32 s67, s71, 6336512\nbuffer_load_dwordx4 a[204:207], v252, s[20:23], s67 offen offset:0 sc1\nM 12,160,96\nR 182,189,4096\nM 12,164,112\n"
"s_barrier\nG 4\ns_waitcnt vmcnt(25)\nM 0,168,128\nR 160,186,57344\ns_waitcnt vmcnt(21)\n"
"M 0,172,144\nbuffer_load_dwordx4 a[208:211], v252, s[20:23], s64 offen offset:1024 sc1\nM 16,168,132\nR 162,186,61440\ns_waitcnt vmcnt(21)\nM 16,172,148\n"
"M 32,168,136\nR 164,190,0\ns_waitcnt vmcnt(20)\nM 32,172,152\nbuffer_load_dwordx4 a[212:215], v252, s[20:23], s65 offen offset:1024 sc1\nM 48,168,140\n"
"R 166,190,4096\ns_waitcnt vmcnt(20)\nM 48,172,156\nG 4\nM 52,176,140\nR 168,187,57344\n"
"M 52,180,156\nbuffer_load_dwordx4 a[216:219], v252, s[20:23], s66 offen offset:1024 sc1\nM 36,176,136\nR 170,187,61440\nM 36,180,152\nM 20,176,132\n"
"R 172,191,0\nM 20,180,148\nbuffer_load_dwordx4 a[220:223], v252, s[20:23], s67 offen offset:1024 sc1\nM 4,176,128\nR 174,191,4096\nM 4,180,144\n"
"G 4\nM 8,160,128\nR 176,188,8192\nM 8,164,144\nbuffer_load_dwordx4 a[224:227], v252, s[20:23], s64 offen offset:2048 sc1\nM 24,160,132\n"
"R 178,188,12288\nM 24,164,148\nM 40,160,136\nR 180,188,16384\nM 40,164,152\nbuffer_load_dwordx4 a[228:231], v252, s[20:23], s65 offen offset:2048 sc1\n"
"M 56,160,140\nR 182,188,20480\nM 56,164,156\nG 4\nM 60,168,140\nR 160,189,8192\n"
"M 60,172,156\nbuffer_load_dwordx4 a[232:235], v252, s[20:23], s66 offen offset:2048 sc1\nM 44,168,136\nR 162,189,12288\nM 44,172,152\nM 28,168,132\n"
"R 164,189,16384\nM 28,172,148\nbuffer_load_dwordx4 a[236:239], v252, s[20:23], s67 offen offset:2048 sc1\nM 12,168,128\nR 166,189,20480\nM 12,172,144\n"
"G 4\ns_waitcnt vmcnt(25)\nM 0,176,160\nR 168,190,8192\ns_waitcnt vmcnt(21)\nM 0,180,176\n"
"buffer_load_dwordx4 a[240:243], v252, s[20:23], s64 offen offset:3072 sc1\nM 16,176,164\nR 170,190,12288\ns_waitcnt vmcnt(21)\nM 16,180,180\nM 32,176,168\n"
"R 172,190,16384\ns_waitcnt vmcnt(19)\nM 32,180,184\nbuffer_load_dwordx4 a[244:247], v252, s[20:23], s65 offen offset:3072 sc1\nM 48,176,172\nR 174,190,20480\n"
"s_waitcnt vmcnt(18)\nM 48,180,188\nG 4\nM 52,160,172\nR 176,191,8192\nM 52,164,188\n"
"buffer_load_dwordx4 a[248:251], v252, s[20:23], s66 offen offset:3072 sc1\nM 36,160,168\nR 178,191,12288\ns_waitcnt vmcnt(12)\ns_barrier\nM 36,164,184\n"
"M 20,160,164\nR 180,191,16384\nM 20,164,180\nbuffer_load_dwordx4 a[252:255], v252, s[20:23], s67 offen offset:3072 sc1\nM 4,160,160\nR 182,191,20480\n"
"M 4,164,176\nG 4\nM 8,168,160\nR 160,188,24576\nM 8,172,176\ns_movk_i32 s63, 7168\n"
"v_mad_u32_u24 v220, v228, s63, v229\nv_add_u32 v221, 3668992, v220\ns_add_u32 m0, s50, 0\ns_add_u32 s63, s54, 0\nbuffer_load_dwordx4 v220, s[12:15], s63 offen lds\nM 24,168,164\n"
"R 162,188,28672\nM 24,172,180\nM 40,168,168\nR 164,188,32768\nM 40,172,184\nbuffer_load_dwordx4 v221, s[12:15], s63 offen offset:1024 lds\n"
"M 56,168,172\nR 166,188,36864\ns_add_u32 m0, s50, 8192\nM 56,172,188\nG 4\nM 60,176,172\n"
"R 168,189,24576\ns_add_u32 s63, s54, 229376\nM 60,180,188\nbuffer_load_dwordx4 v220, s[12:15], s63 offen lds\nM 44,176,168\nR 170,189,28672\n"
"M 44,180,184\nM 28,176,164\nR 172,189,32768\nM 28,180,180\nbuffer_load_dwordx4 v221, s[12:15], s63 offen offset:1024 lds\nM 12,176,160\n"
"R 174,189,36864\ns_add_u32 m0, s50, 16384\nM 12,180,176\nG 4\nM 32,160,200\nR 176,190,24576\n"
"s_add_u32 s63, s54, 128\ns_waitcnt vmcnt(13)\nM 32,164,216\nR 178,190,28672\nM 48,160,204\nR 180,190,32768\n"
"s_waitcnt vmcnt(12)\nM 48,164,220\nR 182,190,36864\nG 4\nM 52,168,204\nR 160,191,24576\n"
"buffer_load_dwordx4 v220, s[12:15], s63 offen lds\nM 52,172,220\nR 162,191,28672\nM 36,168,200\nR 164,191,32768\nM 36,172,216\n"
"R 166,191,36864\nG 4\nM 40,176,200\nR 168,188,40960\nbuffer_load_dwordx4 v221, s[12:15], s63 offen offset:1024 lds\nM 40,180,216\n"
"R 170,188,45056\ns_add_u32 m0, s50, 24576\nM 56,176,204\nR 172,188,49152\ns_add_u32 s63, s54, 229504\nM 56,180,220\n"
"R 174,188,53248\nG 4\nM 60,160,204\nR 176,189,40960\nbuffer_load_dwordx4 v220, s[12:15], s63 offen lds\nM 60,164,220\n"
"R 178,189,45056\nM 44,160,200\nR 180,189,49152\nM 44,164,216\nR 182,189,53248\nG 4\n"
"s_waitcnt vmcnt(12)\nM 32,168,232\nR 160,190,40960\nbuffer_load_dwordx4 v221, s[12:15], s63 offen offset:1024 lds\ns_waitcnt vmcnt(9)\nM 32,172,248\n"
"R 162,190,45056\nM 48,168,236\nR 164,190,49152\ns_waitcnt vmcnt(8)\nM 48,172,252\nR 166,190,53248\n"
"G 4\nM 52,176,236\nR 168,191,40960\nM 52,180,252\nR 170,191,45056\nM 36,176,232\n"
"R 172,191,49152\nM 36,180,248\nR 174,191,53248\nG 4\nM 40,160,232\nR 176,188,24576\n"
"M 40,164,248\nR 178,188,28672\nM 56,160,236\nR 180,188,32768\ns_add_u32 m0, s50, 32768\nM 56,164,252\n"
"R 182,188,36864\nG 4\nM 60,168,236\nR 160,189,24576\ns_add_u32 s63, s54, 458752\nM 60,172,252\n"
"R 162,189,28672\nM 44,168,232\nR 164,189,32768\nM 44,172,248\nR 166,189,36864\nG 4\n"
"M 0,176,192\nR 168,190,24576\nbuffer_load_dwordx4 v220, s[12:15], s63 offen lds\nM 0,180,208\nR 170,190,28672\ns_mov_b32 m0, s51\n"
"M 16,176,196\nR 172,190,32768\nds_write_addtid_b32 v32 offset:0\nM 16,180,212\nR 174,190,36864\nds_write_addtid_b32 v33 offset:256\n"
"G 6\nM 20,160,196\nR 176,191,24576\nds_write_addtid_b32 v34 offset:512\nM 20,164,212\nR 178,191,28672\n"
"ds_write_addtid_b32 v35 offset:768\nM 4,160,192\nR 180,191,32768\nds_write_addtid_b32 v36 offset:1024\nM 4,164,208\nR 182,191,36864\n"
"ds_write_addtid_b32 v37 offset:1280\nG 9\nM 8,168,192\nR 160,188,40960\nds_write_addtid_b32 v38 offset:1536\nM 8,172,208\n"
"R 162,188,45056\nds_write_addtid_b32 v39 offset:1792\nM 24,168,196\nR 164,188,49152\nds_write_addtid_b32 v40 offset:2048\nM 24,172,212\n"
"R 166,188,53248\nds_write_addtid_b32 v41 offset:2304\nG 9\nM 28,176,196\nR 168,189,40960\nds_write_addtid_b32 v42 offset:2560\n"
"M 28,180,212\nR 170,189,45056\nds_write_addtid_b32 v43 offset:2816\nM 12,176,192\nR 172,189,49152\nds_write_addtid_b32 v44 offset:3072\n"
"ds_write_addtid_b32 v45 offset:3328\nds_write_addtid_b32 v46 offset:3584\nds_write_addtid_b32 v47 offset:3840\nds_write_addtid_b32 v48 offset:4096\nds_write_addtid_b32 v49 offset:4352\nds_write_addtid_b32 v50 offset:4608\n"
"ds_write_addtid_b32 v51 offset:4864\nds_write_addtid_b32 v52 offset:5120\nds_write_addtid_b32 v53 offset:5376\nds_write_addtid_b32 v54 offset:5632\nds_write_addtid_b32 v55 offset:5888\nds_write_addtid_b32 v56 offset:6144\n"
"ds_write_addtid_b32 v57 offset:6400\nds_write_addtid_b32 v58 offset:6656\nds_write_addtid_b32 v59 offset:6912\nds_write_addtid_b32 v60 offset:7168\nds_write_addtid_b32 v61 offset:7424\nds_write_addtid_b32 v62 offset:7680\n"
"ds_write_addtid_b32 v63 offset:7936\nM 12,180,208\nR 174,189,53248\ns_add_u32 m0, s50, 32768\nG 15\nM 0,160,224\n"
"R 176,190,40960\nbuffer_load_dwordx4 v221, s[12:15], s63 offen offset:1024 lds\nM 0,164,240\nR 178,190,45056\nG 3\ns_barrier\n"
"s_add_u32 s63, s54, 688128\nM 16,160,228\nR 180,190,49152\ns_add_u32 m0, s50, 40960\nds_read2st64_b32 v[32:33], v225 offset0:0 offset1:1\nM 16,164,244\n"
"R 182,190,53248\nds_read2st64_b32 v[34:35], v225 offset0:2 offset1:3\nG 6\nM 20,168,228\nR 160,191,40960\nbuffer_load_dwordx4 v220, s[12:15], s63 offen lds\n"
"M 20,172,244\nR 162,191,45056\nds_read2st64_b32 v[36:37], v225 offset0:4 offset1:5\nM 4,168,224\nR 164,191,49152\nds_read2st64_b32 v[38:39], v225 offset0:6 offset1:7\n"
"M 4,172,240\nR 166,191,53248\nG 7\nM 8,176,224\nbuffer_load_dwordx4 v221, s[12:15], s63 offen offset:1024 lds\nM 8,180,240\n"
"s_add_u32 m0, s50, 49152\nds_read2st64_b32 v[40:41], v225 offset0:8 offset1:9\nds_read2st64_b32 v[42:43], v225 offset0:10 offset1:11\nM 24,176,228\ns_add_u32 s63, s54, 458880\nds_read2st64_b32 v[44:45], v225 offset0:12 offset1:13\n"
"ds_read2st64_b32 v[46:47], v225 offset0:14 offset1:15\nM 24,180,244\nds_read2st64_b32 v[48:49], v225 offset0:16 offset1:17\nds_read2st64_b32 v[50:51], v225 offset0:18 offset1:19\nG 6\nM 28,160,228\n"
"buffer_load_dwordx4 v220, s[12:15], s63 offen lds\nM 28,164,244\nds_read2st64_b32 v[52:53], v225 offset0:20 offset1:21\nds_read2st64_b32 v[54:55], v225 offset0:22 offset1:23\nM 12,160,224\nds_read2st64_b32 v[56:57], v225 offset0:24 offset1:25\n"
"ds_read2st64_b32 v[58:59], v225 offset0:26 offset1:27\nM 12,164,240\nds_read2st64_b32 v[60:61], v225 offset0:28 offset1:29\nds_read2st64_b32 v[62:63], v225 offset0:30 offset1:31\nG 0\ns_barrier\n"
"s_lshl_b32 s84, s75, 11\ns_add_u32 s84, s84, s49\ns_add_u32 s84, s84, 14680064\ns_add_u32 s85, s84, 32768\nv_mov_b32 v238, s46\nv_mov_b32 v239, s47\n"
"v_pk_add_f32 v[0:1], v[0:1], v[32:33]\nv_pk_add_f32 v[2:3], v[2:3], v[34:35]\nv_pk_add_f32 v[4:5], v[4:5], v[36:37]\nv_pk_add_f32 v[6:7], v[6:7], v[38:39]\nv_cvt_pk_bf16_f32 v192, v0, v1\nv_cvt_pk_bf16_f32 v193, v2, v3\n"
"v_cvt_pk_bf16_f32 v194, v4, v5\nv_cvt_pk_bf16_f32 v195, v6, v7\nbuffer_store_dwordx4 v[192:195], v252, s[20:23], s84 offen offset:0 sc1\nv_pk_add_f32 v[8:9], v[8:9], v[40:41]\nv_pk_add_f32 v[10:11], v[10:11], v[42:43]\nv_pk_add_f32 v[12:13], v[12:13], v[44:45]\n"
"v_pk_add_f32 v[14:15], v[14:15], v[46:47]\nv_cvt_pk_bf16_f32 v196, v8, v9\nv_cvt_pk_bf16_f32 v197, v10, v11\nv_cvt_pk_bf16_f32 v198, v12, v13\nv_cvt_pk_bf16_f32 v199, v14, v15\nbuffer_store_dwordx4 v[196:199], v252, s[20:23], s84 offen offset:1024 sc1\n"
"v_pk_add_f32 v[16:17], v[16:17], v[48:49]\nv_pk_add_f32 v[18:19], v[18:19], v[50:51]\nv_pk_add_f32 v[20:21], v[20:21], v[52:53]\nv_pk_add_f32 v[22:23], v[22:23], v[54:55]\nv_cvt_pk_bf16_f32 v200, v16, v17\nv_cvt_pk_bf16_f32 v201, v18, v19\n"
"v_cvt_pk_bf16_f32 v202, v20, v21\nv_cvt_pk_bf16_f32 v203, v22, v23\nbuffer_store_dwordx4 v[200:203], v252, s[20:23], s85 offen offset:0 sc1\nv_pk_add_f32 v[24:25], v[24:25], v[56:57]\nv_pk_add_f32 v[26:27], v[26:27], v[58:59]\nv_pk_add_f32 v[28:29], v[28:29], v[60:61]\n"
"v_pk_add_f32 v[30:31], v[30:31], v[62:63]\nv_cvt_pk_bf16_f32 v204, v24, v25\nv_cvt_pk_bf16_f32 v205, v26, v27\nv_cvt_pk_bf16_f32 v206, v28, v29\nv_cvt_pk_bf16_f32 v207, v30, v31\nbuffer_store_dwordx4 v[204:207], v252, s[20:23], s85 offen offset:1024 sc1\n"
"s_add_u32 s61, s79, 88637440\ns_lshl_b32 s62, s75, 3\ns_add_u32 s61, s61, s62\ns_waitcnt vmcnt(0)\ns_mov_b64 exec, 1\nbuffer_atomic_swap_x2 v[238:239], off, s[20:23], s61 sc1\n"
"s_mov_b64 exec, -1\nLqw1:\nv_bfe_u32 v250, v252, 7, 1\nv_mul_u32_u24 v250, 2176, v250\nv_bfe_u32 v251, v252, 4, 3\nv_lshl_add_u32 v250, v251, 3, v250\n"
"s_lshl_b32 s60, s90, 6\nv_add_u32 v250, s60, v250\ns_sleep 5\nv_mov_b32 v248, 0\nv_mov_b32 v249, 0\ns_add_u32 s61, s80, 88637440\n"
"s_mov_b64 exec, 0xffff\nbuffer_atomic_or_x2 v[248:249], v250, s[20:23], s61 offen sc0 sc1\ns_mov_b64 exec, -1\nbuffer_load_dwordx4 v221, s[12:15], s63 offen offset:1024 lds\ns_add_u32 m0, s50, 57344\ns_add_u32 s63, s54, 688256\n"
"buffer_load_dwordx4 v220, s[12:15], s63 offen lds\nbuffer_load_dwordx4 v221, s[12:15], s63 offen offset:1024 lds\ns_add_u32 m0, s50, 65536\ns_add_u32 s63, s54, 917504\nbuffer_load_dwordx4 v220, s[12:15], s63 offen lds\nbuffer_load_dwordx4 v221, s[12:15], s63 offen offset:1024 lds\n"
"s_add_u32 m0, s50, 73728\ns_add_u32 s63, s54, 1146880\nbuffer_load_dwordx4 v220, s[12:15], s63 offen lds\nbuffer_load_dwordx4 v221, s[12:15], s63 offen offset:1024 lds\ns_add_u32 m0, s50, 81920\ns_add_u32 s63, s54, 917632\n"
"buffer_load_dwordx4 v220, s[12:15], s63 offen lds\nbuffer_load_dwordx4 v221, s[12:15], s63 offen offset:1024 lds\ns_add_u32 m0, s50, 90112\ns_add_u32 s63, s54, 1147008\nbuffer_load_dwordx4 v220, s[12:15], s63 offen lds\nbuffer_load_dwordx4 v221, s[12:15], s63 offen offset:1024 lds\n"
"s_add_u32 m0, s50, 98304\ns_add_u32 s63, s54, 1376256\nbuffer_load_dwordx4 v220, s[12:15], s63 offen lds\nv_mov_b32 v230, 0\nv_mov_b32 v231, 0\nv_mov_b32 v232, 0\n"
"v_mov_b32 v233, 0\nv_dot2c_f32_bf16 v230, s76, v192\nv_dot2c_f32_bf16 v231, v192, v192\nv_dot2c_f32_bf16 v232, s76, v200\nv_dot2c_f32_bf16 v233, v200, v200\nv_dot2c_f32_bf16 v230, s76, v193\n"
"v_dot2c_f32_bf16 v231, v193, v193\nv_dot2c_f32_bf16 v232, s76, v201\nv_dot2c_f32_bf16 v233, v201, v201\nv_dot2c_f32_bf16 v230, s76, v194\nv_dot2c_f32_bf16 v231, v194, v194\nv_dot2c_f32_bf16 v232, s76, v202\n"
"v_dot2c_f32_bf16 v233, v202, v202\nv_dot2c_f32_bf16 v230, s76, v195\nv_dot2c_f32_bf16 v231, v195, v195\nv_dot2c_f32_bf16 v232, s76, v203\nv_dot2c_f32_bf16 v233, v203, v203\nv_dot2c_f32_bf16 v230, s76, v196\n"
"v_dot2c_f32_bf16 v231, v196, v196\nv_dot2c_f32_bf16 v232, s76, v204\nv_dot2c_f32_bf16 v233, v204, v204\nv_dot2c_f32_bf16 v230, s76, v197\nv_dot2c_f32_bf16 v231, v197, v197\nv_dot2c_f32_bf16 v232, s76, v205\n"
"v_dot2c_f32_bf16 v233, v205, v205\nv_dot2c_f32_bf16 v230, s76, v198\nv_dot2c_f32_bf16 v231, v198, v198\nv_dot2c_f32_bf16 v232, s76, v206\nv_dot2c_f32_bf16 v233, v206, v206\nv_dot2c_f32_bf16 v230, s76, v199\n"
"v_dot2c_f32_bf16 v231, v199, v199\nv_dot2c_f32_bf16 v232, s76, v207\nv_dot2c_f32_bf16 v233, v207, v207\nv_lshrrev_b32 v234, 1, v252\nv_permlane32_swap_b32 v230, v231\ns_nop 0\n"
"v_permlane32_swap_b32 v232, v233\nv_add_f32 v230, v230, v231\nv_add_f32 v232, v232, v233\nv_mov_b32 v231, s46\ns_nop 0\nv_permlane16_swap_b32 v230, v232\n"
"v_add_f32 v230, v230, v232\ns_lshr_b32 s61, s49, 3\ns_lshl2_add_u32 s61, s53, s61\ns_add_u32 s61, s61, 83361792\nbuffer_store_dwordx2 v[230:231], v234, s[20:23], s61 offen sc1\ns_waitcnt vmcnt(13)\n"
"v_cmp_eq_u64 s[86:87], s[46:47], v[248:249]\ns_nop 0\ns_and_b32 s62, s86, 0xffff\ns_cmp_eq_u32 s62, 0xffff\ns_cbranch_scc0 Lq736\nLq737:\n"
"s_add_u32 s64, s70, 14680064\nbuffer_load_dwordx4 a[0:3], v252, s[20:23], s64 offen offset:0 sc1\ns_add_u32 s65, s70, 14712832\nbuffer_load_dwordx4 a[4:7], v252, s[20:23], s65 offen offset:0 sc1\ns_add_u32 s66, s71, 14680064\nbuffer_load_dwordx4 a[8:11], v252, s[20:23], s66 offen offset:0 sc1\n"
"s_add_u32 s67, s71, 14712832\nbuffer_load_dwordx4 a[12:15], v252, s[20:23], s67 offen offset:0 sc1\nbuffer_load_dwordx4 a[16:19], v252, s[20:23], s64 offen offset:1024 sc1\nbuffer_load_dwordx4 a[20:23], v252, s[20:23], s65 offen offset:1024 sc1\nbuffer_load_dwordx4 a[24:27], v252, s[20:23], s66 offen offset:1024 sc1\nbuffer_load_dwordx4 a[28:31], v252, s[20:23], s67 offen offset:1024 sc1\n"
"s_barrier\nR 160,184,0\nR 162,184,4096\nR 164,184,8192\nR 166,184,12288\nR 168,185,0\n"
"R 170,185,4096\nR 172,185,8192\nR 174,185,12288\nR 176,186,0\nR 178,186,4096\nR 180,186,8192\n"
"R 182,186,12288\nbuffer_load_dwordx4 a[32:35], v252, s[20:23], s64 offen offset:2048 sc1\nbuffer_load_dwordx4 a[36:39], v252, s[20:23], s65 offen offset:2048 sc1\nbuffer_load_dwordx4 a[40:43], v252, s[20:23], s66 offen offset:2048 sc1\nbuffer_load_dwordx4 a[44:47], v252, s[20:23], s67 offen offset:2048 sc1\nbuffer_load_dwordx4 a[48:51], v252, s[20:23], s64 offen offset:3072 sc1\n"
"buffer_load_dwordx4 a[52:55], v252, s[20:23], s65 offen offset:3072 sc1\nbuffer_load_dwordx4 a[56:59], v252, s[20:23], s66 offen offset:3072 sc1\nbuffer_load_dwordx4 a[60:63], v252, s[20:23], s67 offen offset:3072 sc1\ns_waitcnt vmcnt(15) lgkmcnt(10)\nv_mfma_f32_16x16x32_bf16 v[32:35], v[160:163], a[0:3], 0\ns_add_u32 s64, s70, 14684160\n"
"buffer_load_dwordx4 a[64:67], v252, s[20:23], s64 offen offset:0 sc1\ns_waitcnt vmcnt(12) lgkmcnt(8)\nM 32,164,16\nv_mfma_f32_16x16x32_bf16 v[48:51], v[160:163], a[4:7], 0\ns_waitcnt vmcnt(11)\nM 48,164,20\n"
"v_mfma_f32_16x16x32_bf16 v[64:67], v[160:163], a[8:11], 0\ns_waitcnt vmcnt(10)\nM 64,164,24\ns_add_u32 s65, s70, 14716928\nbuffer_load_dwordx4 a[68:71], v252, s[20:23], s65 offen offset:0 sc1\nv_mfma_f32_16x16x32_bf16 v[80:83], v[160:163], a[12:15], 0\n"
"s_waitcnt vmcnt(10)\nM 80,164,28\nG 4\nv_mfma_f32_16x16x32_bf16 v[84:87], v[168:171], a[12:15], 0\nR 160,187,0\nM 84,172,28\n"
"v_mfma_f32_16x16x32_bf16 v[68:71], v[168:171], a[8:11], 0\nR 162,187,4096\nM 68,172,24\ns_add_u32 s66, s71, 14684160\nbuffer_load_dwordx4 a[72:75], v252, s[20:23], s66 offen offset:0 sc1\nv_mfma_f32_16x16x32_bf16 v[52:55], v[168:171], a[4:7], 0\n"
"R 164,187,8192\nM 52,172,20\nv_mfma_f32_16x16x32_bf16 v[36:39], v[168:171], a[0:3], 0\nR 166,187,12288\nM 36,172,16\nG 4\n"
"v_mfma_f32_16x16x32_bf16 v[40:43], v[176:179], a[0:3], 0\nR 168,184,16384\nM 40,180,16\ns_add_u32 s67, s71, 14716928\nbuffer_load_dwordx4 a[76:79], v252, s[20:23], s67 offen offset:0 sc1\nv_mfma_f32_16x16x32_bf16 v[56:59], v[176:179], a[4:7], 0\n"
"R 170,184,20480\nM 56,180,20\nv_mfma_f32_16x16x32_bf16 v[72:75], v[176:179], a[8:11], 0\nR 172,184,24576\nM 72,180,24\nv_mfma_f32_16x16x32_bf16 v[88:91], v[176:179], a[12:15], 0\n"
"R 174,184,28672\nM 88,180,28\nG 4\nv_mfma_f32_16x16x32_bf16 v[92:95], v[160:163], a[12:15], 0\nR 176,185,16384\nM 92,164,28\n"
"buffer_load_dwordx4 a[80:83], v252, s[20:23], s64 offen offset:1024 sc1\nv_mfma_f32_16x16x32_bf16 v[76:79], v[160:163], a[8:11], 0\nR 178,185,20480\nM 76,164,24\nv_mfma_f32_16x16x32_bf16 v[60:63], v[160:163], a[4:7], 0\nR 180,185,24576\n"
"M 60,164,20\nbuffer_load_dwordx4 a[84:87], v252, s[20:23], s65 offen offset:1024 sc1\nv_mfma_f32_16x16x32_bf16 v[44:47], v[160:163], a[0:3], 0\nR 182,185,28672\nM 44,164,16\nG 4\n"
"v_mfma_f32_16x16x32_bf16 v[96:99], v[168:171], a[0:3], 0\nR 160,186,16384\nM 96,172,16\nv_mfma_f32_16x16x32_bf16 v[112:115], v[168:171], a[4:7], 0\nR 162,186,20480\nM 112,172,20\n"
"buffer_load_dwordx4 a[88:91], v252, s[20:23], s66 offen offset:1024 sc1\nv_mfma_f32_16x16x32_bf16 v[128:131], v[168:171], a[8:11], 0\nR 164,186,24576\nM 128,172,24\nv_mfma_f32_16x16x32_bf16 v[144:147], v[168:171], a[12:15], 0\nR 166,186,28672\n"
"M 144,172,28\ns_barrier\nG 4\nv_mfma_f32_16x16x32_bf16 v[148:151], v[176:179], a[12:15], 0\nR 168,187,16384\nM 148,180,28\n"
"buffer_load_dwordx4 a[92:95], v252, s[20:23], s67 offen offset:1024 sc1\nv_mfma_f32_16x16x32_bf16 v[132:135], v[176:179], a[8:11], 0\nR 170,187,20480\nM 132,180,24\nv_mfma_f32_16x16x32_bf16 v[116:119], v[176:179], a[4:7], 0\nR 172,187,24576\n"
"M 116,180,20\nbuffer_load_dwordx4 v221, s[12:15], s63 offen offset:1024 lds\nv_mfma_f32_16x16x32_bf16 v[100:103], v[176:179], a[0:3], 0\nR 174,187,28672\nM 100,180,16\nG 4\n"
"v_mfma_f32_16x16x32_bf16 v[104:107], v[160:163], a[0:3], 0\nR 176,184,32768\nM 104,164,16\nbuffer_load_dwordx4 a[96:99], v252, s[20:23], s64 offen offset:2048 sc1\nv_mfma_f32_16x16x32_bf16 v[120:123], v[160:163], a[4:7], 0\nR 178,184,36864\n"
"M 120,164,20\nbuffer_load_dwordx4 a[100:103], v252, s[20:23], s65 offen offset:2048 sc1\nv_mfma_f32_16x16x32_bf16 v[136:139], v[160:163], a[8:11], 0\nR 180,184,40960\nM 136,164,24\nv_mfma_f32_16x16x32_bf16 v[152:155], v[160:163], a[12:15], 0\n"
"R 182,184,45056\nM 152,164,28\nG 4\nv_mfma_f32_16x16x32_bf16 v[156:159], v[168:171], a[12:15], 0\nR 160,185,32768\nM 156,172,28\n"
"buffer_load_dwordx4 a[104:107], v252, s[20:23], s66 offen offset:2048 sc1\nv_mfma_f32_16x16x32_bf16 v[140:143], v[168:171], a[8:11], 0\nR 162,185,36864\nM 140,172,24\nv_mfma_f32_16x16x32_bf16 v[124:127], v[168:171], a[4:7], 0\nR 164,185,40960\n"
"M 124,172,20\nv_mfma_f32_16x16x32_bf16 v[108:111], v[168:171], a[0:3], 0\nR 166,185,45056\nM 108,172,16\nG 4\ns_waitcnt vmcnt(19)\n"
"M 32,176,32\nR 168,186,32768\ns_waitcnt vmcnt(15)\nM 32,180,48\nbuffer_load_dwordx4 a[108:111], v252, s[20:23], s67 offen offset:2048 sc1\nM 48,176,36\n"
"R 170,186,36864\ns_waitcnt vmcnt(15)\nM 48,180,52\ns_add_u32 m0, s50, 106496\nM 64,176,40\nR 172,186,40960\n"
"s_add_u32 s63, s54, 1605632\ns_waitcnt vmcnt(14)\nM 64,180,56\nbuffer_load_dwordx4 a[112:115], v252, s[20:23], s64 offen offset:3072 sc1\nM 80,176,44\nR 174,186,45056\n"
"s_waitcnt vmcnt(14)\nM 80,180,60\ns_barrier\nG 4\nM 84,160,44\nR 176,187,32768\n"
"M 84,164,60\nbuffer_load_dwordx4 v220, s[12:15], s63 offen lds\nM 68,160,40\nR 178,187,36864\nM 68,164,56\nbuffer_load_dwordx4 a[116:119], v252, s[20:23], s65 offen offset:3072 sc1\n"
"M 52,160,36\nR 180,187,40960\nM 52,164,52\nM 36,160,32\nR 182,187,45056\nM 36,164,48\n"
"G 4\nM 40,168,32\nR 160,184,49152\nM 40,172,48\nbuffer_load_dwordx4 a[120:123], v252, s[20:23], s66 offen offset:3072 sc1\nM 56,168,36\n"
"R 162,184,53248\nM 56,172,52\nM 72,168,40\nR 164,184,57344\nM 72,172,56\nbuffer_load_dwordx4 v221, s[12:15], s63 offen offset:1024 lds\n"
"M 88,168,44\nR 166,184,61440\nM 88,172,60\nG 4\nM 92,176,44\nR 168,185,49152\n"
"M 92,180,60\nbuffer_load_dwordx4 a[124:127], v252, s[20:23], s67 offen offset:3072 sc1\nM 76,176,40\nR 170,185,53248\nM 76,180,56\nM 60,176,36\n"
"R 172,185,57344\nM 60,180,52\ns_add_u32 s64, s70, 14688256\nbuffer_load_dwordx4 a[128:131], v252, s[20:23], s64 offen offset:0 sc1\nM 44,176,32\nR 174,185,61440\n"
"M 44,180,48\nG 4\nM 96,160,32\nR 176,186,49152\nM 96,164,48\ns_add_u32 s65, s70, 14721024\n"
"buffer_load_dwordx4 a[132:135], v252, s[20:23], s65 offen offset:0 sc1\nM 112,160,36\nR 178,186,53248\nM 112,164,52\nM 128,160,40\nR 180,186,57344\n"
"M 128,164,56\nM 144,160,44\nR 182,186,61440\nM 144,164,60\ns_barrier\nG 4\n"
"M 148,168,44\nR 160,187,49152\nM 148,172,60\ns_add_u32 s66, s71, 14688256\nbuffer_load_dwordx4 a[136:139], v252, s[20:23], s66 offen offset:0 sc1\nM 132,168,40\n"
"R 162,187,53248\nM 132,172,56\ns_add_u32 m0, s50, 114688\nM 116,168,36\nR 164,187,57344\ns_add_u32 s63, s54, 1376384\n"
"M 116,172,52\ns_add_u32 s67, s71, 14721024\nbuffer_load_dwordx4 a[140:143], v252, s[20:23], s67 offen offset:0 sc1\nM 100,168,32\nR 166,187,61440\nM 100,172,48\n"
"G 4\nM 104,176,32\nR 168,188,0\nM 104,180,48\nbuffer_load_dwordx4 v220, s[12:15], s63 offen lds\nM 120,176,36\n"
"R 170,188,4096\nM 120,180,52\nbuffer_load_dwordx4 a[144:147], v252, s[20:23], s64 offen offset:1024 sc1\nM 136,176,40\nR 172,188,8192\nM 136,180,56\n"
"M 152,176,44\nR 174,188,12288\nM 152,180,60\nG 4\nM 156,160,44\nR 176,189,0\n"
"M 156,164,60\nbuffer_load_dwordx4 a[148:151], v252, s[20:23], s65 offen offset:1024 sc1\nM 140,160,40\nR 178,189,4096\nM 140,164,56\nM 124,160,36\n"
"R 180,189,8192\nM 124,164,52\nbuffer_load_dwordx4 v221, s[12:15], s63 offen offset:1024 lds\nM 108,160,32\nR 182,189,12288\nM 108,164,48\n"
"G 4\ns_waitcnt vmcnt(26)\nM 32,168,64\nR 160,190,0\ns_waitcnt vmcnt(22)\nM 32,172,80\n"
"buffer_load_dwordx4 a[152:155], v252, s[20:23], s66 offen offset:1024 sc1\nM 48,168,68\nR 162,190,4096\ns_waitcnt vmcnt(22)\nM 48,172,84\nM 64,168,72\n"
"R 164,190,8192\ns_waitcnt vmcnt(21)\nM 64,172,88\nbuffer_load_dwordx4 a[156:159], v252, s[20:23], s67 offen offset:1024 sc1\nM 80,168,76\nR 166,190,12288\n"
"s_waitcnt vmcnt(21)\nM 80,172,92\ns_barrier\nG 4\nM 84,176,76\nR 168,191,0\n"
"M 84,180,92\nM 68,176,72\nR 170,191,4096\nM 68,180,88\nbuffer_load_dwordx4 a[160:163], v252, s[20:23], s64 offen offset:2048 sc1\nM 52,176,68\n"
"R 172,191,8192\nM 52,180,84\ns_add_u32 m0, s50, 0\nM 36,176,64\nR 174,191,12288\ns_add_u32 s63, s54, 1605760\n"
"M 36,180,80\nG 4\nM 40,160,64\nR 176,188,16384\nM 40,164,80\nbuffer_load_dwordx4 a[164:167], v252, s[20:23], s65 offen offset:2048 sc1\n"
"M 56,160,68\nR 178,188,20480\nM 56,164,84\nbuffer_load_dwordx4 v220, s[12:15], s63 offen lds\nM 72,160,72\nR 180,188,24576\n"
"M 72,164,88\nbuffer_load_dwordx4 a[168:171], v252, s[20:23], s66 offen offset:2048 sc1\nM 88,160,76\nR 182,188,28672\nM 88,164,92\nG 4\n"
"M 92,168,76\nR 160,189,16384\nbuffer_load_dwordx4 v221, s[12:15], s63 offen offset:1024 lds\nM 92,172,92\nM 76,168,72\nR 162,189,20480\n"
"M 76,172,88\nbuffer_load_dwordx4 a[172:175], v252, s[20:23], s67 offen offset:2048 sc1\nM 60,168,68\nR 164,189,24576\nM 60,172,84\nM 44,168,64\n"
"R 166,189,28672\nM 44,172,80\nG 4\nM 96,176,64\nR 168,190,16384\nM 96,180,80\n"
"buffer_load_dwordx4 a[176:179], v252, s[20:23], s64 offen offset:3072 sc1\nM 112,176,68\nR 170,190,20480\nM 112,180,84\nM 128,176,72\nR 172,190,24576\n"
"M 128,180,88\ns_add_u32 m0, s50, 8192\nM 144,176,76\nR 174,190,28672\ns_add_u32 s63, s54, 1835008\nM 144,180,92\n"
"s_waitcnt vmcnt(18)\ns_barrier\nG 4\nM 148,160,76\nR 176,191,16384\nM 148,164,92\n"
"buffer_load_dwordx4 a[180:183], v252, s[20:23], s65 offen offset:3072 sc1\nM 132,160,72\nR 178,191,20480\nM 132,164,88\nbuffer_load_dwordx4 v220, s[12:15], s63 offen lds\nM 116,160,68\n"
"R 180,191,24576\nM 116,164,84\nbuffer_load_dwordx4 a[184:187], v252, s[20:23], s66 offen offset:3072 sc1\nM 100,160,64\nR 182,191,28672\nM 100,164,80\n"
"G 4\nM 104,168,64\nR 160,188,32768\nbuffer_load_dwordx4 v221, s[12:15], s63 offen offset:1024 lds\nM 104,172,80\ns_add_u32 m0, s50, 16384\n"
"s_add_u32 s63, s54, 2064384\nbuffer_load_dwordx4 v220, s[12:15], s63 offen lds\nM 120,168,68\nR 162,188,36864\nM 120,172,84\nbuffer_load_dwordx4 a[188:191], v252, s[20:23], s67 offen offset:3072 sc1\n"
"M 136,168,72\nR 164,188,40960\nM 136,172,88\nbuffer_load_dwordx4 v221, s[12:15], s63 offen offset:1024 lds\nM 152,168,76\nR 166,188,45056\n"
"M 152,172,92\nG 4\nM 156,176,76\nR 168,189,32768\nM 156,180,92\ns_add_u32 s64, s70, 14692352\n"
"buffer_load_dwordx4 a[192:195], v252, s[20:23], s64 offen offset:0 sc1\nM 140,176,72\nR 170,189,36864\nM 140,180,88\nM 124,176,68\nR 172,189,40960\n"
"M 124,180,84\ns_add_u32 s65, s70, 14725120\nbuffer_load_dwordx4 a[196:199], v252, s[20:23], s65 offen offset:0 sc1\nM 108,176,64\nR 174,189,45056\nM 108,180,80\n"
"G 4\nM 32,160,96\nR 176,190,32768\nM 32,164,112\nM 48,160,100\nR 178,190,36864\n"
"M 48,164,116\ns_add_u32 s66, s71, 14692352\nbuffer_load_dwordx4 a[200:203], v252, s[20:23], s66 offen offset:0 sc1\nM 64,160,104\nR 180,190,40960\nM 64,164,120\n"
"s_add_u32 m0, s50, 24576\nM 80,160,108\nR 182,190,45056\ns_add_u32 s63, s54, 2293760\ns_waitcnt vmcnt(27)\nM 80,164,124\n"
"s_waitcnt vmcnt(12)\ns_barrier\nG 4\nM 84,168,108\nR 160,191,32768\nM 84,172,124\n"
"s_add_u32 s67, s71, 14725120\nbuffer_load_dwordx4 a[204:207], v252, s[20:23], s67 offen offset:0 sc1\nM 68,168,104\nR 162,191,36864\nM 68,172,120\nbuffer_load_dwordx4 v220, s[12:15], s63 offen lds\n"
"M 52,168,100\nR 164,191,40960\nM 52,172,116\nbuffer_load_dwordx4 v221, s[12:15], s63 offen offset:1024 lds\nM 36,168,96\nR 166,191,45056\n"
"s_add_u32 m0, s50, 32768\nM 36,172,112\nG 4\nM 40,176,96\nR 168,188,49152\ns_add_u32 s63, s54, 2523136\n"
"M 40,180,112\nbuffer_load_dwordx4 a[208:211], v252, s[20:23], s64 offen offset:1024 sc1\nM 56,176,100\nR 170,188,53248\nM 56,180,116\nbuffer_load_dwordx4 v220, s[12:15], s63 offen lds\n"
"M 72,176,104\nR 172,184,0\nM 72,180,120\nbuffer_load_dwordx4 a[212:215], v252, s[20:23], s65 offen offset:1024 sc1\nM 88,176,108\nR 174,184,4096\n"
"M 88,180,124\nG 4\nM 92,160,108\nR 176,189,49152\nbuffer_load_dwordx4 v221, s[12:15], s63 offen offset:1024 lds\nM 92,164,124\n"
"M 76,160,104\nR 178,189,53248\nM 76,164,120\nbuffer_load_dwordx4 a[216:219], v252, s[20:23], s66 offen offset:1024 sc1\nM 60,160,100\nR 180,185,0\n"
"Lq1011:\ns_lshr_b32 s60, s45, 3\ns_lshl2_add_u32 s60, s60, s40\ns_lshl_b32 s60, s60, 8\nv_lshl_add_u32 v224, v252, 3, s60\nv_add_u32 v224, Lq1011+4096-LqPC, v224\n"
"s_mov_b64 exec, 3\nglobal_load_dword v224, v224, s[98:99]\ns_mov_b64 exec, -1\nM 60,164,116\nM 44,160,96\nR 182,185,4096\n"
"M 44,164,112\nG 4\nM 96,168,96\nR 160,190,49152\nM 96,172,112\nbuffer_load_dwordx4 a[220:223], v252, s[20:23], s67 offen offset:1024 sc1\n"
"M 112,168,100\nR 162,190,53248\nM 112,172,116\nM 128,168,104\nR 164,186,0\nM 128,172,120\n"
"M 144,168,108\nR 166,186,4096\ns_add_u32 m0, s50, 40960\nM 144,172,124\ns_waitcnt vmcnt(13)\ns_barrier\n"
"G 4\nM 148,176,108\nR 168,191,49152\ns_add_u32 s63, s54, 2752512\nM 148,180,124\nbuffer_load_dwordx4 a[224:227], v252, s[20:23], s64 offen offset:2048 sc1\n"
"M 132,176,104\nR 170,191,53248\nM 132,180,120\nbuffer_load_dwordx4 a[228:231], v252, s[20:23], s65 offen offset:2048 sc1\nM 116,176,100\nR 172,187,0\n"
"buffer_load_dwordx4 v220, s[12:15], s63 offen lds\nM 116,180,116\nbuffer_load_dwordx4 v221, s[12:15], s63 offen offset:1024 lds\nM 100,176,96\nR 174,187,4096\ns_add_u32 m0, s50, 49152\n"
"M 100,180,112\nG 4\nM 104,160,96\nR 176,184,8192\ns_add_u32 s63, s54, 2981888\nM 104,164,112\n"
"buffer_load_dwordx4 a[232:235], v252, s[20:23], s66 offen offset:2048 sc1\nM 120,160,100\nR 178,184,12288\nM 120,164,116\nbuffer_load_dwordx4 v220, s[12:15], s63 offen lds\nM 136,160,104\n"
"R 180,184,16384\nM 136,164,120\nbuffer_load_dwordx4 v221, s[12:15], s63 offen offset:1024 lds\nM 152,160,108\nR 182,184,20480\nM 152,164,124\n"
"G 4\nM 156,168,108\nR 160,185,8192\nM 156,172,124\nbuffer_load_dwordx4 a[236:239], v252, s[20:23], s67 offen offset:2048 sc1\nM 140,168,104\n"
"R 162,185,12288\nM 140,172,120\nM 124,168,100\nR 164,185,16384\nM 124,172,116\nbuffer_load_dwordx4 a[240:243], v252, s[20:23], s64 offen offset:3072 sc1\n"
"M 108,168,96\nR 166,185,20480\nM 108,172,112\nG 4\nM 32,176,128\nR 168,186,8192\n"
"M 32,180,144\nM 48,176,132\nR 170,186,12288\nM 48,180,148\nbuffer_load_dwordx4 a[244:247], v252, s[20:23], s65 offen offset:3072 sc1\nM 64,176,136\n"
"R 172,186,16384\nM 64,180,152\nM 80,176,140\nR 174,186,20480\ns_add_u32 m0, s50, 57344\nM 80,180,156\n"
"s_waitcnt vmcnt(13)\ns_barrier\nG 4\nM 84,160,140\nR 176,187,8192\ns_add_u32 s63, s54, 3211264\n"
"M 84,164,156\nbuffer_load_dwordx4 a[248:251], v252, s[20:23], s66 offen offset:3072 sc1\nM 68,160,136\nR 178,187,12288\nM 68,164,152\ns_lshr_b32 s84, s49, 3\n"
"M 52,160,132\nR 180,187,16384\nbuffer_load_dwordx4 v220, s[12:15], s63 offen lds\nM 52,164,148\ns_add_u32 s84, s84, 83361792\nM 36,160,128\n"
"R 182,187,20480\nM 36,164,144\nbuffer_load_dwordx4 v[0:3], v252, s[20:23], s84 offen offset:0 sc1\nG 4\nM 40,168,128\nR 160,184,24576\n"
"buffer_load_dwordx4 v221, s[12:15], s63 offen offset:1024 lds\nM 40,172,144\nbuffer_load_dwordx4 a[252:255], v252, s[20:23], s67 offen offset:3072 sc1\ns_add_u32 s63, s54, 3440640\nM 56,168,132\nR 162,184,28672\n"
"s_add_u32 m0, s50, 65536\nM 56,172,148\nbuffer_load_dwordx4 v[4:7], v252, s[20:23], s84 offen offset:1024 sc1\nM 72,168,136\nR 164,184,32768\ns_lshr_b32 s85, s49, 3\n"
"buffer_load_dwordx4 v220, s[12:15], s63 offen lds\nM 72,172,152\nbuffer_load_dwordx4 v[8:11], v252, s[20:23], s84 offen offset:2048 sc1\nM 88,168,140\nR 166,184,36864\ns_add_u32 s85, s85, 83365888\n"
"M 88,172,156\nG 4\nM 92,176,140\nR 168,185,24576\nbuffer_load_dwordx4 v221, s[12:15], s63 offen offset:1024 lds\nM 92,180,156\n"
"buffer_load_dwordx4 v[12:15], v252, s[20:23], s84 offen offset:3072 sc1\nM 76,176,136\nR 170,185,28672\nM 76,180,152\nbuffer_load_dwordx4 v[16:19], v252, s[20:23], s85 offen offset:0 sc1\nM 60,176,132\n"
"R 172,185,32768\nM 60,180,148\nbuffer_load_dwordx4 v[20:23], v252, s[20:23], s85 offen offset:1024 sc1\nM 44,176,128\nR 174,185,36864\nM 44,180,144\n"
"G 4\nM 32,160,160\nR 176,186,24576\nM 32,164,176\nbuffer_load_dwordx4 v[24:27], v252, s[20:23], s85 offen offset:2048 sc1\nM 48,160,164\n"
"R 178,186,28672\nM 48,164,180\nbuffer_load_dwordx4 v[28:31], v252, s[20:23], s85 offen offset:3072 sc1\nM 64,160,168\nR 180,186,32768\ns_add_u32 s63, s54, 1835136\n"
"M 64,164,184\ns_add_u32 m0, s50, 73728\nM 80,160,172\nR 182,186,36864\nM 80,164,188\ns_waitcnt vmcnt(17)\n"
"s_barrier\nG 4\nM 84,168,172\nR 160,187,24576\nM 84,172,188\nbuffer_load_dwordx4 v220, s[12:15], s63 offen lds\n"
"M 68,168,168\nR 162,187,28672\nM 68,172,184\nM 52,168,164\nR 164,187,32768\nv_bfe_u32 v250, v252, 5, 1\n"
"v_mul_u32_u24 v250, 2560, v250\nM 52,172,180\nbuffer_load_dwordx4 v221, s[12:15], s63 offen offset:1024 lds\nv_and_b32 v251, 16, v252\nM 36,168,160\nR 166,187,36864\n"
"s_add_u32 m0, s50, 81920\nv_lshl_add_u32 v250, v251, 1, v250\nM 36,172,176\ns_lshl_b32 s60, s90, 6\nv_add_u32 v250, s60, v250\nG 4\n"
"M 40,176,160\nR 168,184,40960\ns_add_u32 s63, s54, 2064512\nM 40,180,176\nbuffer_load_dwordx4 v220, s[12:15], s63 offen lds\nM 56,176,164\n"
"R 170,184,45056\nM 56,180,180\nM 72,176,168\nR 172,184,49152\nM 72,180,184\nbuffer_load_dwordx4 v221, s[12:15], s63 offen offset:1024 lds\n"
"M 88,176,172\nR 174,184,53248\nM 88,180,188\nG 4\nM 92,160,172\nR 176,185,40960\n"
"M 92,164,188\nM 76,160,168\nR 178,185,45056\nM 76,164,184\ns_add_u32 m0, s50, 90112\nM 60,160,164\n"
"R 180,185,49152\nM 60,164,180\ns_add_u32 s63, s54, 2293888\nM 44,160,160\nR 182,185,53248\nM 44,164,176\n"
"G 4\nM 32,168,192\nR 160,186,40960\nM 32,172,208\nbuffer_load_dwordx4 v220, s[12:15], s63 offen lds\nM 48,168,196\n"
"R 162,186,45056\nM 48,172,212\nM 64,168,200\nR 164,186,49152\nM 64,172,216\nbuffer_load_dwordx4 v221, s[12:15], s63 offen offset:1024 lds\n"
"M 80,168,204\nR 166,186,53248\ns_add_u32 m0, s50, 98304\nM 80,172,220\ns_waitcnt vmcnt(11)\ns_barrier\n"
"G 4\nM 84,176,204\nR 168,187,40960\ns_add_u32 s63, s54, 2523264\nM 84,180,220\nbuffer_load_dwordx4 v220, s[12:15], s63 offen lds\n"
"M 68,176,200\nR 170,187,45056\nv_min3_u32 v230, v1, v3, v5\nv_min3_u32 v230, v230, v7, v9\nM 68,180,216\ns_waitcnt vmcnt(11)\n"
"v_min3_u32 v230, v230, v11, v13\ns_waitcnt vmcnt(10)\nv_min3_u32 v230, v230, v15, v17\ns_waitcnt vmcnt(9)\nv_min3_u32 v230, v230, v19, v21\nM 52,176,196\n"
"R 172,187,49152\ns_waitcnt vmcnt(8)\nv_min3_u32 v230, v230, v23, v25\ns_waitcnt vmcnt(7)\nv_min3_u32 v230, v230, v27, v29\nM 52,180,212\n"
"buffer_load_dwordx4 v221, s[12:15], s63 offen offset:1024 lds\nv_min3_u32 v230, v230, v31, v31\nM 36,176,192\nR 174,187,53248\ns_add_u32 s63, s54, 2752640\nv_max3_u32 v231, v1, v3, v5\n"
"M 36,180,208\nG 4\nv_max3_u32 v231, v231, v7, v9\nv_max3_u32 v231, v231, v11, v13\nv_max3_u32 v231, v231, v15, v17\nM 40,160,192\n"
"R 176,184,57344\nv_max3_u32 v231, v231, v19, v21\nv_max3_u32 v231, v231, v23, v25\nM 40,164,208\ns_add_u32 m0, s50, 106496\nv_max3_u32 v231, v231, v27, v29\n"
"v_max3_u32 v231, v231, v31, v31\nv_cmp_ne_u32 s[86:87], s46, v230\nM 56,160,196\nR 178,184,61440\nv_cmp_ne_u32 s[88:89], s46, v231\nM 56,164,212\n"
"buffer_load_dwordx4 v220, s[12:15], s63 offen lds\ns_or_b64 s[86:87], s[86:87], s[88:89]\ns_cbranch_scc1 Lq786\nLq787:\nM 72,160,200\nR 180,188,0\n"
"v_add_f32 v0, v0, v16\nv_add_f32 v2, v2, v18\nM 72,164,216\nv_add_f32 v4, v4, v20\nv_add_f32 v6, v6, v22\nv_add_f32 v8, v8, v24\n"
"M 88,160,204\nR 182,188,4096\nv_add_f32 v10, v10, v26\nv_add_f32 v12, v12, v28\nM 88,164,220\nG 4\n"
"v_add_f32 v14, v14, v30\nv_add_f32 v0, v0, v8\nv_add_f32 v2, v2, v10\nM 92,168,204\nR 160,185,57344\nv_add_f32 v4, v4, v12\n"
"v_add_f32 v6, v6, v14\nM 92,172,220\nbuffer_load_dwordx4 v221, s[12:15], s63 offen offset:1024 lds\nv_add_f32 v0, v0, v4\nM 76,168,200\nR 162,185,61440\n"
"s_add_u32 s63, s54, 2982016\nv_add_f32 v2, v2, v6\nM 76,172,216\ns_add_u32 m0, s50, 114688\nv_lshrrev_b32 v231, 1, v252\nv_lshrrev_b32 v230, 2, v252\n"
"s_lshl_b32 s62, s40, 9\ns_add_u32 s62, s62, 159232\nv_add_u32 v231, s62, v231\nM 60,168,196\nR 164,189,0\nds_write2_b32 v231, v0, v2 offset1:1\n"
"M 60,172,212\nbuffer_load_dwordx4 v220, s[12:15], s63 offen lds\ns_lshl_b32 s62, s40, 9\ns_add_u32 s62, s62, 159232\nv_add_u32 v230, s62, v230\nM 44,168,192\n"
"R 166,189,4096\nds_read2st64_b32 v[232:233], v230 offset1:1\nM 44,172,208\nG 6\nM 32,176,224\nR 168,186,57344\n"
"M 32,180,240\nbuffer_load_dwordx4 v221, s[12:15], s63 offen offset:1024 lds\nM 48,176,228\nR 170,186,61440\ns_add_u32 s63, s54, 3211392\nM 48,180,244\n"
"s_add_u32 m0, s50, 0\nM 64,176,232\nR 172,190,0\nG 3\nv_add_f32 v232, v232, v233\nv_mov_b32 v233, v232\n"
"M 64,180,248\nbuffer_load_dwordx4 v220, s[12:15], s63 offen lds\nv_permlane32_swap_b32 v232, v233\nM 80,176,236\nR 174,190,4096\nv_mov_b32 v230, v232\n"
"v_mov_b32 v231, v233\nM 80,180,252\ns_waitcnt vmcnt(9)\ns_barrier\nv_permlane16_swap_b32 v230, v232\nM 84,160,236\n"
"R 176,187,57344\nv_permlane16_swap_b32 v231, v233\nv_mul_f32 v230, 0xba800000, v230\nM 84,164,252\nbuffer_load_dwordx4 v221, s[12:15], s63 offen offset:1024 lds\nv_mul_f32 v214, v230, v230\n"
"M 68,160,232\nR 178,187,61440\ns_add_u32 s63, s54, 3440768\nv_mul_f32 v231, 0x3a800000, v231\nM 68,164,248\ns_add_u32 m0, s50, 8192\n"
"v_sub_f32 v231, v231, v214\nv_max_f32 v231, 0, v231\nv_add_f32 v231, 0x3727c5ac, v231\nM 52,160,228\nR 180,191,0\nv_and_b32 v208, s77, v230\n"
"v_rsq_f32 v234, v231\nM 52,164,244\nbuffer_load_dwordx4 v220, s[12:15], s63 offen lds\nv_mul_f32 v216, v231, v234\nM 36,160,224\nR 182,191,4096\n"
"v_sub_f32 v230, v230, v208\nv_cvt_pk_bf16_f32 v209, v230, v216\nM 36,164,240\nG 4\nv_cvt_pk_bf16_f32 v208, v208, v208\nv_mul_f32 v214, 0xc0135761, v234\n"
"v_mul_f32 v215, v234, v234\nM 40,168,224\nR 160,188,8192\nv_mul_f32 v215, v215, v214\nM 40,172,240\ns_mov_b64 s[60:61], 0xffff\n"
"v_cndmask_b32 v208, 0, v208, s[60:61]\nv_mul_f32 v215, 0x3d372713, v215\nM 56,168,228\nR 162,188,12288\nv_mul_f32 v232, 0xba800000, v232\nM 56,172,244\n"
"buffer_load_dwordx4 v221, s[12:15], s63 offen offset:1024 lds\nM 72,168,232\nR 164,188,16384\ns_add_u32 s63, s55, 0\nM 72,172,248\ns_add_u32 m0, s50, 16384\n"
"s_mov_b64 s[60:61], 0xffff\nv_cndmask_b32 v209, 0, v209, s[60:61]\nv_mul_f32 v217, v232, v232\nM 88,168,236\nR 166,188,20480\nv_mul_f32 v233, 0x3a800000, v233\n"
"M 88,172,252\nG 4\nv_sub_f32 v233, v233, v217\nv_max_f32 v233, 0, v233\nM 92,176,236\nR 168,189,8192\n"
"v_add_f32 v233, 0x3727c5ac, v233\nM 92,180,252\nbuffer_load_dwordx4 v222, s[12:15], s63 offen lds\nM 76,176,232\nR 170,189,12288\nv_and_b32 v212, s77, v232\n"
"M 76,180,248\nv_rsq_f32 v235, v233\nM 60,176,228\nR 172,189,16384\nv_mul_f32 v219, v233, v235\nM 60,180,244\n"
"buffer_load_dwordx4 v223, s[12:15], s63 offen offset:1024 lds\nM 44,176,224\nR 174,189,20480\nv_sub_f32 v232, v232, v212\nM 44,180,240\ns_waitcnt vmcnt(10)\n"
"s_barrier\nG 4\nM 96,160,128\nR 176,190,8192\nv_cvt_pk_bf16_f32 v213, v232, v219\nM 96,164,144\n"
"s_mov_b32 m0, s51\nv_cvt_pk_bf16_f32 v212, v212, v212\nv_mul_f32 v217, 0xc0135761, v235\nM 112,160,132\nR 178,190,12288\nv_mul_f32 v218, v235, v235\n"
"M 112,164,148\ns_mov_b64 s[60:61], 0xffff\nv_cndmask_b32 v212, 0, v212, s[60:61]\nv_mul_f32 v218, v218, v217\nM 128,160,136\nR 180,190,16384\n"
"v_mul_f32 v218, 0x3d372713, v218\nM 128,164,152\nds_write_addtid_b32 v64 offset:0\nM 144,160,140\nR 182,190,20480\nM 144,164,156\n"
"ds_write_addtid_b32 v65 offset:256\nG 6\nM 148,168,140\nR 160,191,8192\nM 148,172,156\nds_write_addtid_b32 v66 offset:512\n"
"M 132,168,136\nR 162,191,12288\nM 132,172,152\nds_write_addtid_b32 v67 offset:768\nM 116,168,132\nR 164,191,16384\n"
"M 116,172,148\nds_write_addtid_b32 v68 offset:1024\nM 100,168,128\nR 166,191,20480\nds_write_addtid_b32 v69 offset:1280\nM 100,172,144\n"
"ds_write_addtid_b32 v70 offset:1536\nds_write_addtid_b32 v71 offset:1792\nG 11\nM 104,176,128\nR 168,188,24576\nds_write_addtid_b32 v72 offset:2048\n"
"M 104,180,144\nds_write_addtid_b32 v73 offset:2304\nds_write_addtid_b32 v74 offset:2560\nM 120,176,132\nR 170,188,28672\nds_write_addtid_b32 v75 offset:2816\n"
"M 120,180,148\nds_write_addtid_b32 v76 offset:3072\nds_write_addtid_b32 v77 offset:3328\nM 136,176,136\nR 172,188,32768\nds_write_addtid_b32 v78 offset:3584\n"
"M 136,180,152\nds_write_addtid_b32 v79 offset:3840\nds_write_addtid_b32 v80 offset:4096\nM 152,176,140\nR 174,188,36864\nds_write_addtid_b32 v81 offset:4352\n"
"M 152,180,156\nds_write_addtid_b32 v82 offset:4608\nds_write_addtid_b32 v83 offset:4864\nG 15\nM 156,160,140\nR 176,189,24576\n"
"ds_write_addtid_b32 v84 offset:5120\nM 156,164,156\nds_write_addtid_b32 v85 offset:5376\nds_write_addtid_b32 v86 offset:5632\nM 140,160,136\nR 178,189,28672\n"
"ds_write_addtid_b32 v87 offset:5888\nM 140,164,152\nds_write_addtid_b32 v88 offset:6144\nds_write_addtid_b32 v89 offset:6400\nM 124,160,132\nR 180,189,32768\n"
"ds_write_addtid_b32 v90 offset:6656\nM 124,164,148\nds_write_addtid_b32 v91 offset:6912\nds_write_addtid_b32 v92 offset:7168\nM 108,160,128\nR 182,189,36864\n"
"ds_write_addtid_b32 v93 offset:7424\nM 108,164,144\nds_write_addtid_b32 v94 offset:7680\nds_write_addtid_b32 v95 offset:7936\nG 15\nM 96,168,160\n"
"R 160,190,24576\ns_add_u32 m0, s50, 24576\ns_add_u32 s63, s55, 458752\nM 96,172,176\nbuffer_load_dwordx4 v222, s[12:15], s63 offen lds\nM 112,168,164\n"
"R 162,190,28672\ns_waitcnt vmcnt(7) lgkmcnt(2)\ns_barrier\nM 112,172,180\nds_read2st64_b32 v[64:65], v225 offset0:0 offset1:1\nds_read2st64_b32 v[66:67], v225 offset0:2 offset1:3\n"
"M 128,168,168\nR 164,190,32768\nds_read2st64_b32 v[68:69], v225 offset0:4 offset1:5\nM 128,172,184\nds_read2st64_b32 v[70:71], v225 offset0:6 offset1:7\nds_read2st64_b32 v[72:73], v225 offset0:8 offset1:9\n"
"M 144,168,172\nR 166,190,36864\nds_read2st64_b32 v[74:75], v225 offset0:10 offset1:11\nM 144,172,188\nds_read2st64_b32 v[76:77], v225 offset0:12 offset1:13\ns_mov_b64 s[60:61], 0xffff\n"
"v_cndmask_b32 v213, 0, v213, s[60:61]\nds_read2st64_b32 v[78:79], v225 offset0:14 offset1:15\nM 148,176,172\nR 168,191,24576\nds_read2st64_b32 v[80:81], v225 offset0:16 offset1:17\nM 148,180,188\n"
"buffer_load_dwordx4 v223, s[12:15], s63 offen offset:1024 lds\nG 11\nv_add_f32 v32, v32, v64\nM 132,176,168\nR 170,191,28672\nv_add_f32 v33, v33, v65\n"
"G 11\nv_add_f32 v34, v34, v66\nM 132,180,184\ns_add_u32 m0, s50, 32768\nds_read2st64_b32 v[82:83], v225 offset0:18 offset1:19\nv_add_f32 v35, v35, v67\n"
"M 116,176,164\nR 172,191,32768\nG 11\nv_add_f32 v36, v36, v68\nv_add_f32 v37, v37, v69\nM 116,180,180\n"
"s_add_u32 s63, s55, 917504\nds_read2st64_b32 v[84:85], v225 offset0:20 offset1:21\nG 11\nv_add_f32 v38, v38, v70\nM 100,176,160\nR 174,191,36864\n"
"v_add_f32 v39, v39, v71\nG 11\nv_add_f32 v40, v40, v72\nM 100,180,176\nds_read2st64_b32 v[86:87], v225 offset0:22 offset1:23\nG 11\n"
"v_add_f32 v41, v41, v73\nM 104,160,160\nR 176,188,40960\nG 11\nv_add_f32 v42, v42, v74\nv_add_f32 v43, v43, v75\n"
"M 104,164,176\nbuffer_load_dwordx4 v222, s[12:15], s63 offen lds\nG 10\nv_add_f32 v44, v44, v76\nM 120,160,164\nR 178,188,45056\n"
"v_add_f32 v45, v45, v77\nG 10\nv_add_f32 v46, v46, v78\nM 120,164,180\nds_read2st64_b32 v[88:89], v225 offset0:24 offset1:25\nv_add_f32 v47, v47, v79\n"
"G 9\nv_add_f32 v48, v48, v80\nM 136,160,168\nR 180,188,49152\nv_add_f32 v49, v49, v81\nG 8\n"
"v_add_f32 v50, v50, v82\nM 136,164,184\nds_read2st64_b32 v[90:91], v225 offset0:26 offset1:27\nv_add_f32 v51, v51, v83\nG 7\nv_add_f32 v52, v52, v84\n"
"M 152,160,172\nR 182,188,53248\nv_add_f32 v53, v53, v85\nM 152,164,188\nds_read2st64_b32 v[92:93], v225 offset0:28 offset1:29\nG 8\n"
"G 7\nv_add_f32 v54, v54, v86\nM 156,168,172\nR 160,189,40960\nv_add_f32 v55, v55, v87\nM 156,172,188\n"
"buffer_load_dwordx4 v223, s[12:15], s63 offen offset:1024 lds\nM 140,168,168\nR 162,189,45056\ns_mov_b32 s60, 157184\nv_bfe_u32 v246, v252, 4, 4\nv_lshl_add_u32 v246, v246, 3, s60\n"
"M 140,172,184\nds_read_b64 v[238:239], v246 offset:0\ns_add_u32 m0, s50, 40960\nM 124,168,164\nR 164,189,49152\nds_read_b64 v[240:241], v246 offset:128\n"
"M 124,172,180\nds_read_b64 v[242:243], v246 offset:256\ns_add_u32 s63, s55, 1376256\nM 108,168,160\nR 166,189,53248\nds_read_b64 v[244:245], v246 offset:384\n"
"M 108,172,176\nds_read_b64 v[246:247], v246 offset:0\nG 10\nM 96,176,192\nR 168,190,40960\nv_add_f32 v56, v56, v88\n"
"M 96,180,208\nbuffer_load_dwordx4 v222, s[12:15], s63 offen lds\nM 112,176,196\nR 170,190,45056\ns_waitcnt vmcnt(5) lgkmcnt(2)\ns_barrier\n"
"M 112,180,212\nds_read2st64_b32 v[94:95], v225 offset0:30 offset1:31\nv_add_f32 v57, v57, v89\nM 128,176,200\nR 172,190,49152\nv_add_f32 v58, v58, v90\n"
"M 128,180,216\nv_add_f32 v59, v59, v91\nv_add_f32 v60, v60, v92\nM 144,176,204\nR 174,190,53248\nv_add_f32 v61, v61, v93\n"
"M 144,180,220\nG 2\nv_add_f32 v62, v62, v94\nv_add_f32 v63, v63, v95\nM 148,160,204\nR 176,191,40960\n"
"v_mfma_f32_16x16x32_bf16 v[32:35], v[238:241], v[208:211], v[32:35]\nM 148,164,220\nv_mfma_f32_16x16x32_bf16 v[36:39], v[240:243], v[208:211], v[36:39]\nbuffer_load_dwordx4 v223, s[12:15], s63 offen offset:1024 lds\nM 132,160,200\nR 178,191,45056\n"
"v_mfma_f32_16x16x32_bf16 v[40:43], v[242:245], v[208:211], v[40:43]\ns_add_u32 s63, s55, 1835008\nM 132,164,216\nv_mfma_f32_16x16x32_bf16 v[44:47], v[244:247], v[208:211], v[44:47]\ns_add_u32 m0, s50, 49152\nM 116,160,196\n"
"R 180,191,49152\nv_mfma_f32_16x16x32_bf16 v[48:51], v[244:247], v[210:213], v[48:51]\nM 116,164,212\nv_mfma_f32_16x16x32_bf16 v[52:55], v[238:241], v[210:213], v[52:55]\nbuffer_load_dwordx4 v222, s[12:15], s63 offen lds\nM 100,160,192\n"
"R 182,191,53248\nv_mfma_f32_16x16x32_bf16 v[56:59], v[240:243], v[210:213], v[56:59]\nM 100,164,208\nv_mfma_f32_16x16x32_bf16 v[60:63], v[242:245], v[210:213], v[60:63]\nG 4\nM 104,168,192\n"
"R 160,184,0\nM 104,172,208\nbuffer_load_dwordx4 v223, s[12:15], s63 offen offset:1024 lds\nM 120,168,196\nR 162,184,4096\nM 120,172,212\n"
"s_add_u32 m0, s50, 57344\nM 136,168,200\nR 164,184,8192\nM 136,172,216\ns_add_u32 s63, s55, 2293760\nM 152,168,204\n"
"R 166,184,12288\nv_mul_f32 v230, v32, v32\nv_mul_f32 v231, v33, v33\nM 152,172,220\ns_add_u32 s84, s73, 37748736\ns_add_u32 s85, s73, 37863424\n"
"G 4\nv_mul_f32 v232, v34, v34\nM 156,176,204\nR 168,185,0\nv_mul_f32 v233, v35, v35\nv_fma_f32 v230, v230, v215, v214\n"
"M 156,180,220\nbuffer_load_dwordx4 v222, s[12:15], s63 offen lds\nv_fma_f32 v231, v231, v215, v214\nM 140,176,200\nR 170,185,4096\nv_fma_f32 v232, v232, v215, v214\n"
"v_fma_f32 v233, v233, v215, v214\nM 140,180,216\nv_mul_f32 v230, v32, v230\nv_mul_f32 v231, v33, v231\nv_mul_f32 v232, v34, v232\nM 124,176,196\n"
"R 172,185,8192\nv_mul_f32 v233, v35, v233\nv_exp_f32 v230, v230\nM 124,180,212\nv_exp_f32 v231, v231\nv_exp_f32 v232, v232\n"
"M 108,176,192\nR 174,185,12288\ns_mov_b32 s60, 157696\nv_bfe_u32 v246, v252, 4, 4\nv_lshl_add_u32 v246, v246, 3, s60\nv_exp_f32 v233, v233\n"
"M 108,180,208\nds_read_b64 v[238:239], v246 offset:0\nG 5\nv_fma_f32 v230, v230, v216, v216\nM 96,160,224\nR 176,186,0\n"
"ds_read_b64 v[240:241], v246 offset:128\nv_fma_f32 v231, v231, v216, v216\nM 96,164,240\nds_read_b64 v[242:243], v246 offset:256\nv_fma_f32 v232, v232, v216, v216\nv_fma_f32 v233, v233, v216, v216\n"
"M 112,160,228\nR 178,186,4096\nds_read_b64 v[244:245], v246 offset:384\nv_rcp_f32 v230, v230\nM 112,164,244\nds_read_b64 v[246:247], v246 offset:0\n"
"v_mul_f32 v230, v32, v230\nv_rcp_f32 v231, v231\nM 128,160,232\nR 180,186,8192\nv_mul_f32 v231, v33, v231\nv_rcp_f32 v232, v232\n"
"M 128,164,248\nbuffer_load_dwordx4 v223, s[12:15], s63 offen offset:1024 lds\nv_rcp_f32 v233, v233\nM 144,160,236\nR 182,186,12288\ns_add_u32 s63, s55, 2752512\n"
"v_mul_f32 v232, v34, v232\nM 144,164,252\nG 9\nv_mul_f32 v233, v35, v233\nv_cvt_pk_bf16_f32 v32, v230, v231\nv_cvt_pk_bf16_f32 v33, v232, v233\n"
"M 148,168,236\nR 160,187,0\nv_mul_f32 v234, v36, v36\nv_mul_f32 v235, v37, v37\nM 148,172,252\ns_add_u32 m0, s50, 65536\n"
"v_mul_f32 v236, v38, v38\nv_mul_f32 v237, v39, v39\nv_fma_f32 v234, v234, v215, v214\nM 132,168,232\nR 162,187,4096\nv_fma_f32 v235, v235, v215, v214\n"
"v_fma_f32 v236, v236, v215, v214\nM 132,172,248\nbuffer_load_dwordx4 v222, s[12:15], s63 offen lds\nv_fma_f32 v237, v237, v215, v214\nM 116,168,228\nR 164,187,8192\n"
"G 9\nv_mfma_f32_16x16x32_bf16 v[96:99], v[238:241], v[208:211], v[96:99]\nv_mul_f32 v234, v36, v234\nv_mul_f32 v235, v37, v235\nM 116,172,244\nv_exp_f32 v234, v234\n"
"v_mul_f32 v236, v38, v236\nM 100,168,224\nR 166,187,12288\nG 9\nv_mfma_f32_16x16x32_bf16 v[100:103], v[240:243], v[208:211], v[100:103]\nv_mul_f32 v237, v39, v237\n"
"v_exp_f32 v235, v235\nM 100,172,240\nG 4\nv_exp_f32 v236, v236\nv_exp_f32 v237, v237\nM 104,176,224\n"
"R 168,184,16384\nv_mfma_f32_16x16x32_bf16 v[104:107], v[242:245], v[208:211], v[104:107]\nv_fma_f32 v234, v234, v216, v216\nv_fma_f32 v235, v235, v216, v216\nM 104,180,240\nbuffer_load_dwordx4 v223, s[12:15], s63 offen offset:1024 lds\n"
"v_fma_f32 v236, v236, v216, v216\nM 120,176,228\nR 170,184,20480\nv_mfma_f32_16x16x32_bf16 v[108:111], v[244:247], v[208:211], v[108:111]\nv_fma_f32 v237, v237, v216, v216\nv_rcp_f32 v234, v234\n"
"M 120,180,244\ns_add_u32 m0, s50, 73728\nv_rcp_f32 v235, v235\nv_rcp_f32 v236, v236\nM 136,176,232\nR 172,186,16384\n"
"v_mfma_f32_16x16x32_bf16 v[112:115], v[244:247], v[210:213], v[112:115]\nv_mul_f32 v234, v36, v234\nv_rcp_f32 v237, v237\nM 136,180,248\ns_add_u32 s63, s55, 3211264\nv_mul_f32 v235, v37, v235\n"
"v_mul_f32 v236, v38, v236\nv_mul_f32 v237, v39, v237\nM 152,176,236\nR 174,186,20480\nv_mfma_f32_16x16x32_bf16 v[116:119], v[238:241], v[210:213], v[116:119]\nv_cvt_pk_bf16_f32 v34, v234, v235\n"
"v_cvt_pk_bf16_f32 v35, v236, v237\nM 152,180,252\nG 4\nv_mul_f32 v230, v40, v40\nv_mul_f32 v231, v41, v41\nv_mul_f32 v232, v42, v42\n"
"M 156,160,236\nR 176,185,16384\nv_mfma_f32_16x16x32_bf16 v[120:123], v[240:243], v[210:213], v[120:123]\nv_mul_f32 v233, v43, v43\nv_fma_f32 v230, v230, v215, v214\nM 156,164,252\n"
"buffer_load_dwordx4 v222, s[12:15], s63 offen lds\nv_fma_f32 v231, v231, v215, v214\nM 140,160,232\nR 178,185,20480\nv_mfma_f32_16x16x32_bf16 v[124:127], v[242:245], v[210:213], v[124:127]\nv_fma_f32 v232, v232, v215, v214\n"
"v_fma_f32 v233, v233, v215, v214\nM 140,164,248\nbuffer_load_dwordx4 v223, s[12:15], s63 offen offset:1024 lds\nv_mul_f32 v230, v40, v230\nM 124,160,228\nR 180,187,16384\n"
"s_add_u32 m0, s50, 81920\ns_add_u32 s63, s54, 256\nv_mul_f32 v231, v41, v231\nM 124,164,244\nbuffer_load_dwordx4 v220, s[12:15], s63 offen lds\nv_mul_f32 v232, v42, v232\n"
"M 108,160,224\nR 182,187,20480\nv_mul_f32 v233, v43, v233\nv_exp_f32 v230, v230\nM 108,164,240\ns_waitcnt vmcnt(9)\n"
"s_barrier\nG 4\nv_exp_f32 v231, v231\nv_mfma_f32_16x16x32_bf16 v[0:3], v[168:171], a[0:3], 0\nR 160,184,24576\nv_fma_f32 v230, v230, v216, v216\n"
"v_exp_f32 v232, v232\nM 0,172,16\ns_mov_b32 m0, s51\nv_exp_f32 v233, v233\nv_fma_f32 v231, v231, v216, v216\nv_mfma_f32_16x16x32_bf16 v[16:19], v[168:171], a[4:7], 0\n"
"R 162,184,28672\nv_fma_f32 v232, v232, v216, v216\nv_fma_f32 v233, v233, v216, v216\nM 16,172,20\nbuffer_store_dwordx4 v[32:35], v252, s[20:23], s84 offen offset:0 sc1\nv_rcp_f32 v230, v230\n"
"v_mfma_f32_16x16x32_bf16 v[8:11], v[168:171], a[8:11], 0\nR 164,186,24576\nv_mul_f32 v230, v40, v230\nv_rcp_f32 v231, v231\nM 8,172,24\nv_rcp_f32 v232, v232\n"
"v_rcp_f32 v233, v233\nv_mfma_f32_16x16x32_bf16 v[24:27], v[168:171], a[12:15], 0\nR 166,186,28672\nv_mul_f32 v231, v41, v231\nv_mul_f32 v232, v42, v232\nM 24,172,28\n"
"G 4\nv_mul_f32 v233, v43, v233\nv_cvt_pk_bf16_f32 v40, v230, v231\nv_cvt_pk_bf16_f32 v41, v232, v233\nv_mfma_f32_16x16x32_bf16 v[28:31], v[176:179], a[12:15], 0\nR 168,185,24576\n"
"v_mul_f32 v234, v44, v44\nv_mul_f32 v235, v45, v45\nM 28,180,28\nv_mul_f32 v236, v46, v46\nv_mul_f32 v237, v47, v47\nv_fma_f32 v234, v234, v215, v214\n"
"v_mfma_f32_16x16x32_bf16 v[12:15], v[176:179], a[8:11], 0\nR 170,185,28672\nv_fma_f32 v235, v235, v215, v214\nv_fma_f32 v236, v236, v215, v214\nM 12,180,24\nds_write_addtid_b32 v128 offset:0\n"
"v_fma_f32 v237, v237, v215, v214\nv_mul_f32 v234, v44, v234\nv_mul_f32 v235, v45, v235\nv_mfma_f32_16x16x32_bf16 v[20:23], v[176:179], a[4:7], 0\nR 172,187,24576\nds_write_addtid_b32 v129 offset:256\n"
"v_mul_f32 v236, v46, v236\nv_mul_f32 v237, v47, v237\nM 20,180,20\nds_write_addtid_b32 v130 offset:512\nv_exp_f32 v234, v234\nv_exp_f32 v235, v235\n"
"v_mfma_f32_16x16x32_bf16 v[4:7], v[176:179], a[0:3], 0\nR 174,187,28672\nds_write_addtid_b32 v131 offset:768\nv_fma_f32 v234, v234, v216, v216\nv_exp_f32 v236, v236\nM 4,180,16\n"
"ds_write_addtid_b32 v132 offset:1024\nG 9\nv_exp_f32 v237, v237\nv_fma_f32 v235, v235, v216, v216\nM 0,160,32\nR 176,184,32768\n"
"ds_write_addtid_b32 v133 offset:1280\nv_fma_f32 v236, v236, v216, v216\nv_fma_f32 v237, v237, v216, v216\nM 0,164,48\nds_write_addtid_b32 v134 offset:1536\nv_rcp_f32 v234, v234\n"
"v_rcp_f32 v235, v235\nM 16,160,36\nR 178,184,36864\nds_write_addtid_b32 v135 offset:1792\nv_mul_f32 v234, v44, v234\nv_rcp_f32 v236, v236\n"
"M 16,164,52\nds_write_addtid_b32 v136 offset:2048\nv_rcp_f32 v237, v237\nv_mul_f32 v235, v45, v235\nM 8,160,40\nR 180,186,32768\n"
"ds_write_addtid_b32 v137 offset:2304\nv_mul_f32 v236, v46, v236\nv_mul_f32 v237, v47, v237\nM 8,164,56\nds_write_addtid_b32 v138 offset:2560\nv_cvt_pk_bf16_f32 v42, v234, v235\n"
"v_cvt_pk_bf16_f32 v43, v236, v237\nv_mul_f32 v230, v48, v48\nM 24,160,44\nR 182,186,36864\nds_write_addtid_b32 v139 offset:2816\nv_mul_f32 v231, v49, v49\n"
"v_mul_f32 v232, v50, v50\nM 24,164,60\nds_write_addtid_b32 v140 offset:3072\nG 14\nv_mul_f32 v233, v51, v51\nv_fma_f32 v230, v230, v218, v217\n"
"v_fma_f32 v231, v231, v218, v217\nM 28,168,44\nR 160,185,32768\nds_write_addtid_b32 v141 offset:3328\nv_fma_f32 v232, v232, v218, v217\nv_fma_f32 v233, v233, v218, v217\n"
"M 28,172,60\nbuffer_store_dwordx4 v[40:43], v252, s[20:23], s84 offen offset:1024 sc1\nds_write_addtid_b32 v142 offset:3584\nv_mul_f32 v230, v48, v230\nM 12,168,40\nR 162,185,36864\n"
"ds_write_addtid_b32 v143 offset:3840\nv_mul_f32 v231, v49, v231\nv_mul_f32 v232, v50, v232\nM 12,172,56\nds_write_addtid_b32 v144 offset:4096\nv_exp_f32 v230, v230\n"
"v_mul_f32 v233, v51, v233\nM 20,168,36\nR 164,187,32768\nds_write_addtid_b32 v145 offset:4352\nv_fma_f32 v230, v230, v219, v219\nv_exp_f32 v231, v231\n"
"M 20,172,52\nds_write_addtid_b32 v146 offset:4608\nv_exp_f32 v232, v232\nv_exp_f32 v233, v233\nM 4,168,32\nR 166,187,36864\n"
"ds_write_addtid_b32 v147 offset:4864\nv_fma_f32 v231, v231, v219, v219\nv_fma_f32 v232, v232, v219, v219\nM 4,172,48\nds_write_addtid_b32 v148 offset:5120\nds_write_addtid_b32 v149 offset:5376\n"
"ds_write_addtid_b32 v150 offset:5632\nds_write_addtid_b32 v151 offset:5888\nds_write_addtid_b32 v152 offset:6144\nds_write_addtid_b32 v153 offset:6400\nds_write_addtid_b32 v154 offset:6656\nds_write_addtid_b32 v155 offset:6912\n"
"ds_write_addtid_b32 v156 offset:7168\nds_write_addtid_b32 v157 offset:7424\nds_write_addtid_b32 v158 offset:7680\nds_write_addtid_b32 v159 offset:7936\nG 15\nv_rcp_f32 v230, v230\n"
"v_fma_f32 v233, v233, v219, v219\nM 0,176,64\nR 168,184,40960\ns_add_u32 m0, s50, 81920\nv_rcp_f32 v231, v231\nM 0,180,80\n"
"buffer_load_dwordx4 v221, s[12:15], s63 offen offset:1024 lds\nv_rcp_f32 v232, v232\nM 16,176,68\nR 170,184,45056\ns_waitcnt vmcnt(6) lgkmcnt(2)\ns_barrier\n"
"s_add_u32 m0, s50, 90112\ns_add_u32 s63, s54, 229632\nv_rcp_f32 v233, v233\nM 16,180,84\nbuffer_load_dwordx4 v220, s[12:15], s63 offen lds\nds_read2st64_b32 v[128:129], v225 offset0:0 offset1:1\n"
"v_mul_f32 v230, v48, v230\nM 8,176,72\nR 172,186,40960\nds_read2st64_b32 v[130:131], v225 offset0:2 offset1:3\nv_mul_f32 v231, v49, v231\nv_mul_f32 v232, v50, v232\n"
"M 8,180,88\nds_read2st64_b32 v[132:133], v225 offset0:4 offset1:5\nv_mul_f32 v233, v51, v233\nv_cvt_pk_bf16_f32 v48, v230, v231\nv_cvt_pk_bf16_f32 v49, v232, v233\nM 24,176,76\n"
"R 174,186,45056\nds_read2st64_b32 v[134:135], v225 offset0:6 offset1:7\nv_mul_f32 v234, v52, v52\nv_mul_f32 v235, v53, v53\nM 24,180,92\nds_read2st64_b32 v[136:137], v225 offset0:8 offset1:9\n"
"v_mul_f32 v236, v54, v54\nv_mul_f32 v237, v55, v55\nv_fma_f32 v234, v234, v218, v217\nM 28,160,76\nR 176,185,40960\nds_read2st64_b32 v[138:139], v225 offset0:10 offset1:11\n"
"v_fma_f32 v235, v235, v218, v217\nv_fma_f32 v236, v236, v218, v217\nM 28,164,92\nbuffer_load_dwordx4 v221, s[12:15], s63 offen offset:1024 lds\nG 8\nv_add_f32 v96, v96, v128\n"
"v_fma_f32 v237, v237, v218, v217\nM 12,160,72\nR 178,185,45056\ns_add_u32 s63, s54, 459008\nv_add_f32 v97, v97, v129\nv_mul_f32 v234, v52, v234\n"
"M 12,164,88\ns_add_u32 m0, s50, 98304\nv_exp_f32 v234, v234\nG 7\nv_add_f32 v98, v98, v130\nv_mul_f32 v235, v53, v235\n"
"M 20,160,68\nR 180,187,40960\nv_mul_f32 v236, v54, v236\nv_add_f32 v99, v99, v131\nv_mul_f32 v237, v55, v237\nM 20,164,84\n"
"buffer_load_dwordx4 v220, s[12:15], s63 offen lds\nG 7\nv_add_f32 v100, v100, v132\nv_exp_f32 v235, v235\nM 4,160,64\nR 182,187,45056\n"
"v_fma_f32 v234, v234, v219, v219\nv_add_f32 v101, v101, v133\nv_exp_f32 v236, v236\nM 4,164,80\nG 7\nv_exp_f32 v237, v237\n"
"G 6\nv_add_f32 v102, v102, v134\nv_fma_f32 v235, v235, v219, v219\nM 0,168,96\nR 160,184,49152\nv_fma_f32 v236, v236, v219, v219\n"
"v_add_f32 v103, v103, v135\nv_fma_f32 v237, v237, v219, v219\nM 0,172,112\nbuffer_load_dwordx4 v221, s[12:15], s63 offen offset:1024 lds\nG 6\nv_add_f32 v104, v104, v136\n"
"v_rcp_f32 v234, v234\nM 16,168,100\nR 162,184,53248\ns_add_u32 s63, s54, 688384\nv_add_f32 v105, v105, v137\nv_rcp_f32 v235, v235\n"
"M 16,172,116\ns_add_u32 m0, s50, 106496\nv_rcp_f32 v236, v236\nG 5\nv_add_f32 v106, v106, v138\nv_rcp_f32 v237, v237\n"
"M 8,168,104\nR 164,186,49152\nv_mul_f32 v234, v52, v234\nv_add_f32 v107, v107, v139\nv_mul_f32 v235, v53, v235\nM 8,172,120\n"
"buffer_load_dwordx4 v220, s[12:15], s63 offen lds\nds_read2st64_b32 v[140:141], v225 offset0:12 offset1:13\nv_mul_f32 v236, v54, v236\nM 24,168,108\nR 166,186,53248\nds_read2st64_b32 v[142:143], v225 offset0:14 offset1:15\n"
"v_mul_f32 v237, v55, v237\nv_cvt_pk_bf16_f32 v50, v234, v235\nM 24,172,124\nds_read2st64_b32 v[144:145], v225 offset0:16 offset1:17\nG 7\nv_cvt_pk_bf16_f32 v51, v236, v237\n"
"v_mul_f32 v230, v56, v56\nv_mul_f32 v231, v57, v57\nM 28,176,108\nR 168,185,49152\nds_read2st64_b32 v[146:147], v225 offset0:18 offset1:19\nv_mul_f32 v232, v58, v58\n"
"v_mul_f32 v233, v59, v59\nM 28,180,124\nds_read2st64_b32 v[148:149], v225 offset0:20 offset1:21\nv_fma_f32 v230, v230, v218, v217\nv_fma_f32 v231, v231, v218, v217\nv_fma_f32 v232, v232, v218, v217\n"
"M 12,176,104\nR 170,185,53248\nds_read2st64_b32 v[150:151], v225 offset0:22 offset1:23\nv_fma_f32 v233, v233, v218, v217\nv_mul_f32 v230, v56, v230\nM 12,180,120\n"
"buffer_load_dwordx4 v221, s[12:15], s63 offen offset:1024 lds\nG 8\nv_add_f32 v108, v108, v140\nv_mul_f32 v231, v57, v231\nM 20,176,100\nR 172,187,49152\n"
"s_add_u32 s63, s54, 917760\nv_add_f32 v109, v109, v141\nv_mul_f32 v232, v58, v232\nM 20,180,116\ns_add_u32 m0, s50, 114688\nv_exp_f32 v230, v230\n"
"G 7\nv_add_f32 v110, v110, v142\nv_mul_f32 v233, v59, v233\nM 4,176,96\nR 174,187,53248\nv_fma_f32 v230, v230, v219, v219\n"
"v_add_f32 v111, v111, v143\nv_exp_f32 v231, v231\nM 4,180,112\nv_exp_f32 v232, v232\nG 7\nv_add_f32 v112, v112, v144\n"
"v_exp_f32 v233, v233\nM 0,160,128\nR 176,184,57344\nv_fma_f32 v231, v231, v219, v219\nv_add_f32 v113, v113, v145\nv_fma_f32 v232, v232, v219, v219\n"
"M 0,164,144\nbuffer_load_dwordx4 v220, s[12:15], s63 offen lds\nG 6\nv_add_f32 v114, v114, v146\nv_fma_f32 v233, v233, v219, v219\nM 16,160,132\n"
"R 178,184,61440\nv_mul_f32 v234, v60, v60\nv_add_f32 v115, v115, v147\nv_rcp_f32 v230, v230\nM 16,164,148\nds_read2st64_b32 v[152:153], v225 offset0:24 offset1:25\n"
"v_rcp_f32 v231, v231\nv_rcp_f32 v232, v232\nM 8,160,136\nR 180,186,57344\nLq1013:\ns_lshr_b32 s60, s45, 3\n"
"s_lshl2_add_u32 s60, s60, s40\ns_lshl_b32 s60, s60, 8\nv_lshl_add_u32 v224, v252, 3, s60\nv_add_u32 v224, Lq1013+4096-LqPC, v224\ns_mov_b64 exec, 3\nglobal_load_dword v224, v224, s[98:99]\n"
"s_mov_b64 exec, -1\nG 8\nv_add_f32 v116, v116, v148\nv_rcp_f32 v233, v233\nM 8,164,152\nds_read2st64_b32 v[154:155], v225 offset0:26 offset1:27\n"
"v_mul_f32 v230, v56, v230\nv_mul_f32 v231, v57, v231\nv_mul_f32 v232, v58, v232\nM 24,160,140\nR 182,186,61440\nv_mul_f32 v233, v59, v233\n"
"v_add_f32 v117, v117, v149\nv_cvt_pk_bf16_f32 v56, v230, v231\nM 24,164,156\nds_read2st64_b32 v[156:157], v225 offset0:28 offset1:29\nds_read2st64_b32 v[158:159], v225 offset0:30 offset1:31\nG 8\n"
"v_cvt_pk_bf16_f32 v57, v232, v233\nv_mul_f32 v235, v61, v61\nv_add_f32 v118, v118, v150\nv_add_f32 v119, v119, v151\nG 5\nv_add_f32 v120, v120, v152\n"
"v_add_f32 v121, v121, v153\nG 3\nv_add_f32 v122, v122, v154\nv_add_f32 v123, v123, v155\nG 1\nv_add_f32 v124, v124, v156\n"
"v_add_f32 v125, v125, v157\nG 0\nv_add_f32 v126, v126, v158\nv_add_f32 v127, v127, v159\nv_mul_f32 v236, v62, v62\nM 28,168,140\n"
"R 160,185,57344\nv_mul_f32 v237, v63, v63\nv_fma_f32 v234, v234, v218, v217\nM 28,172,156\nbuffer_load_dwordx4 v221, s[12:15], s63 offen offset:1024 lds\nv_fma_f32 v235, v235, v218, v217\n"
"M 12,168,136\nR 162,185,61440\ns_add_u32 m0, s50, 0\ns_add_u32 s63, s54, 1147136\nv_fma_f32 v236, v236, v218, v217\nM 12,172,152\n"
"buffer_load_dwordx4 v220, s[12:15], s63 offen lds\nv_fma_f32 v237, v237, v218, v217\nM 20,168,132\nR 164,187,57344\nv_mul_f32 v234, v60, v234\nv_mul_f32 v235, v61, v235\n"
"M 20,172,148\nv_exp_f32 v234, v234\nv_mul_f32 v236, v62, v236\nM 4,168,128\nR 166,187,61440\nv_mul_f32 v237, v63, v237\n"
"v_exp_f32 v235, v235\nM 4,172,144\nv_exp_f32 v236, v236\nv_exp_f32 v237, v237\nM 0,176,160\nR 168,188,0\n"
"v_fma_f32 v234, v234, v219, v219\nv_fma_f32 v235, v235, v219, v219\nM 0,180,176\nbuffer_load_dwordx4 v221, s[12:15], s63 offen offset:1024 lds\nv_fma_f32 v236, v236, v219, v219\nM 16,176,164\n"
"R 170,188,4096\ns_add_u32 m0, s50, 8192\ns_add_u32 s63, s54, 1376512\nv_fma_f32 v237, v237, v219, v219\nM 16,180,180\nbuffer_load_dwordx4 v220, s[12:15], s63 offen lds\n"
"v_rcp_f32 v234, v234\nM 8,176,168\nR 172,190,0\nv_mul_f32 v234, v60, v234\nv_rcp_f32 v235, v235\nM 8,180,184\n"
"buffer_store_dwordx4 v[48:51], v252, s[20:23], s85 offen offset:0 sc1\nv_rcp_f32 v236, v236\nM 24,176,172\nR 174,190,4096\nv_mul_f32 v235, v61, v235\nv_rcp_f32 v237, v237\n"
"M 24,180,188\nv_mul_f32 v236, v62, v236\nv_mul_f32 v237, v63, v237\nv_cvt_pk_bf16_f32 v58, v234, v235\nv_cvt_pk_bf16_f32 v59, v236, v237\nbuffer_store_dwordx4 v[56:59], v252, s[20:23], s85 offen offset:1024 sc1\n"
"G 4\nM 28,160,172\nR 176,189,0\ns_waitcnt vmcnt(12)\ns_barrier\nv_mul_f32 v230, v96, v96\n"
"M 28,164,188\nbuffer_load_dwordx4 v221, s[12:15], s63 offen offset:1024 lds\nv_mul_f32 v231, v97, v97\nM 12,160,168\nR 178,189,4096\ns_add_u32 m0, s50, 16384\n"
"s_add_u32 s63, s54, 1605888\nv_mul_f32 v232, v98, v98\nM 12,164,184\nbuffer_load_dwordx4 v220, s[12:15], s63 offen lds\nv_mul_f32 v233, v99, v99\nM 20,160,164\n"
"R 180,191,0\nv_fma_f32 v230, v230, v215, v214\nv_fma_f32 v231, v231, v215, v214\nM 20,164,180\ns_add_u32 s84, s73, 37750784\ns_add_u32 s85, s73, 37865472\n"
"v_fma_f32 v232, v232, v215, v214\nv_fma_f32 v233, v233, v215, v214\nM 4,160,160\nR 182,191,4096\ns_mov_b32 s60, 158720\nv_bfe_u32 v242, v252, 4, 4\n"
"v_lshl_add_u32 v242, v242, 3, s60\nv_mul_f32 v230, v96, v230\nM 4,164,176\nds_read_b64 v[238:239], v242 offset:0\nG 5\nv_mul_f32 v231, v97, v231\n"
"M 0,168,192\nR 160,188,8192\nds_read_b64 v[240:241], v242 offset:128\nv_mul_f32 v232, v98, v232\nM 0,172,208\nds_read_b64 v[242:243], v242 offset:0\n"
"v_mul_f32 v233, v99, v233\nv_exp_f32 v230, v230\nM 16,168,196\nR 162,188,12288\nv_fma_f32 v230, v230, v216, v216\nv_exp_f32 v231, v231\n"
"M 16,172,212\nv_exp_f32 v232, v232\nv_exp_f32 v233, v233\nM 8,168,200\nR 164,190,8192\nv_fma_f32 v231, v231, v216, v216\n"
"v_fma_f32 v232, v232, v216, v216\nM 8,172,216\nv_rcp_f32 v230, v230\nv_fma_f32 v233, v233, v216, v216\nM 24,168,204\nR 166,190,12288\n"
"v_mul_f32 v230, v96, v230\nv_rcp_f32 v231, v231\nM 24,172,220\nG 7\nv_rcp_f32 v232, v232\nv_rcp_f32 v233, v233\n"
"M 28,176,204\nR 168,189,8192\nv_mul_f32 v231, v97, v231\nv_mul_f32 v232, v98, v232\nM 28,180,220\nv_mul_f32 v233, v99, v233\n"
"v_cvt_pk_bf16_f32 v96, v230, v231\nv_cvt_pk_bf16_f32 v97, v232, v233\nM 12,176,200\nR 170,189,12288\nv_mul_f32 v234, v100, v100\nv_mul_f32 v235, v101, v101\n"
"M 12,180,216\nv_mul_f32 v236, v102, v102\nv_mul_f32 v237, v103, v103\nv_fma_f32 v234, v234, v215, v214\nM 20,176,196\nR 172,191,8192\n"
"G 7\nv_mfma_f32_16x16x32_bf16 v[0:3], v[238:241], v[208:211], v[0:3]\nv_fma_f32 v235, v235, v215, v214\nv_fma_f32 v236, v236, v215, v214\nM 20,180,212\nv_fma_f32 v237, v237, v215, v214\n"
"v_mul_f32 v234, v100, v234\nv_mul_f32 v235, v101, v235\nM 4,176,192\nR 174,191,12288\nG 7\nv_mfma_f32_16x16x32_bf16 v[4:7], v[240:243], v[208:211], v[4:7]\n"
"v_mul_f32 v236, v102, v236\nv_mul_f32 v237, v103, v237\nM 4,180,208\nG 4\nv_exp_f32 v234, v234\nv_exp_f32 v235, v235\n"
"M 0,160,224\nR 176,188,16384\nv_mfma_f32_16x16x32_bf16 v[16:19], v[240:243], v[210:213], v[16:19]\nv_fma_f32 v234, v234, v216, v216\nv_exp_f32 v236, v236\nM 0,164,240\n"
"v_exp_f32 v237, v237\nv_fma_f32 v235, v235, v216, v216\nM 16,160,228\nR 178,188,20480\nv_mfma_f32_16x16x32_bf16 v[20:23], v[238:241], v[210:213], v[20:23]\nv_fma_f32 v236, v236, v216, v216\n"
"v_fma_f32 v237, v237, v216, v216\nM 16,164,244\nv_rcp_f32 v234, v234\nv_rcp_f32 v235, v235\nM 8,160,232\nR 180,188,24576\n"
"v_mul_f32 v234, v100, v234\nv_rcp_f32 v236, v236\nM 8,164,248\nv_rcp_f32 v237, v237\nv_mul_f32 v235, v101, v235\nM 24,160,236\n"
"R 182,188,28672\nv_mul_f32 v236, v102, v236\nv_mul_f32 v237, v103, v237\nM 24,164,252\nG 4\nv_cvt_pk_bf16_f32 v98, v234, v235\n"
"v_cvt_pk_bf16_f32 v99, v236, v237\nv_mul_f32 v230, v104, v104\nM 28,168,236\nR 160,189,16384\nv_mul_f32 v231, v105, v105\nv_mul_f32 v232, v106, v106\n"
"M 28,172,252\nv_mul_f32 v233, v107, v107\nv_fma_f32 v230, v230, v215, v214\nv_fma_f32 v231, v231, v215, v214\nM 12,168,232\nR 162,189,20480\n"
"v_fma_f32 v232, v232, v215, v214\nv_fma_f32 v233, v233, v215, v214\nM 12,172,248\nv_mul_f32 v230, v104, v230\nv_mul_f32 v231, v105, v231\nv_mul_f32 v232, v106, v232\n"
"M 20,168,228\nR 164,189,24576\nv_mul_f32 v233, v107, v233\nv_exp_f32 v230, v230\nM 20,172,244\nv_exp_f32 v231, v231\n"
"v_exp_f32 v232, v232\nM 4,168,224\nR 166,189,28672\nv_fma_f32 v230, v230, v216, v216\nv_exp_f32 v233, v233\nM 4,172,240\n"
"s_waitcnt vmcnt(12)\ns_barrier\nG 4\nv_fma_f32 v231, v231, v216, v216\nv_mfma_f32_16x16x32_bf16 v[32:35], v[176:179], a[0:3], 0\nR 168,190,16384\n"
"v_fma_f32 v232, v232, v216, v216\nv_fma_f32 v233, v233, v216, v216\nM 32,180,16\ns_mov_b32 m0, s51\nv_rcp_f32 v230, v230\nv_rcp_f32 v231, v231\n"
"v_mfma_f32_16x16x32_bf16 v[48:51], v[176:179], a[4:7], 0\nR 170,190,20480\nv_mul_f32 v230, v104, v230\nv_rcp_f32 v232, v232\nM 48,180,20\nv_rcp_f32 v233, v233\n"
"v_mul_f32 v231, v105, v231\nv_mfma_f32_16x16x32_bf16 v[64:67], v[176:179], a[8:11], 0\nR 172,190,24576\nv_mov_b32 v226, s46\nv_mul_f32 v232, v106, v232\nM 64,180,24\n"
"ds_write_addtid_b32 v8 offset:0\nv_mov_b32 v227, s47\nv_mul_f32 v233, v107, v233\nv_cvt_pk_bf16_f32 v104, v230, v231\nv_mfma_f32_16x16x32_bf16 v[80:83], v[176:179], a[12:15], 0\nR 174,190,28672\n"
"s_add_u32 s61, s81, 845824\ns_lshl_b32 s62, s44, 5\ns_add_u32 s61, s61, s62\ns_waitcnt vmcnt(0)\ns_mov_b64 exec, 1\nbuffer_atomic_swap_x2 v[226:227], off, s[20:23], s61 sc1\n"
"s_mov_b64 exec, -1\nv_cvt_pk_bf16_f32 v105, v232, v233\nM 80,180,28\nds_write_addtid_b32 v9 offset:256\nG 6\nv_mul_f32 v234, v108, v108\n"
"v_mul_f32 v235, v109, v109\nv_mul_f32 v236, v110, v110\nv_mfma_f32_16x16x32_bf16 v[84:87], v[160:163], a[12:15], 0\nR 176,191,16384\nds_write_addtid_b32 v10 offset:512\nv_mul_f32 v237, v111, v111\n"
"v_fma_f32 v234, v234, v215, v214\nM 84,164,28\nds_write_addtid_b32 v11 offset:768\nv_fma_f32 v235, v235, v215, v214\nv_fma_f32 v236, v236, v215, v214\nv_fma_f32 v237, v237, v215, v214\n"
"v_mfma_f32_16x16x32_bf16 v[68:71], v[160:163], a[8:11], 0\nR 178,191,20480\nds_write_addtid_b32 v12 offset:1024\nv_mul_f32 v234, v108, v234\nv_mul_f32 v235, v109, v235\nM 68,164,24\n"
"buffer_store_dwordx4 v[96:99], v252, s[20:23], s84 offen offset:0 sc1\nds_write_addtid_b32 v13 offset:1280\nv_mul_f32 v236, v110, v236\nv_mfma_f32_16x16x32_bf16 v[52:55], v[160:163], a[4:7], 0\nR 180,191,24576\nds_write_addtid_b32 v14 offset:1536\n"
"v_mul_f32 v237, v111, v237\nv_exp_f32 v234, v234\nM 52,164,20\nds_write_addtid_b32 v15 offset:1792\nv_exp_f32 v235, v235\nv_exp_f32 v236, v236\n"
"v_mfma_f32_16x16x32_bf16 v[36:39], v[160:163], a[0:3], 0\nR 182,191,28672\nds_write_addtid_b32 v24 offset:4096\nv_fma_f32 v234, v234, v216, v216\nv_exp_f32 v237, v237\nM 36,164,16\n"
"ds_write_addtid_b32 v25 offset:4352\nG 13\nv_rcp_f32 v234, v234\nv_fma_f32 v235, v235, v216, v216\nv_mfma_f32_16x16x32_bf16 v[40:43], v[168:171], a[0:3], 0\nR 160,188,32768\n"
"ds_write_addtid_b32 v26 offset:4608\nds_write_addtid_b32 v27 offset:4864\nds_write_addtid_b32 v28 offset:5120\nds_write_addtid_b32 v29 offset:5376\nds_write_addtid_b32 v30 offset:5632\nds_write_addtid_b32 v31 offset:5888\n"
"v_fma_f32 v236, v236, v216, v216\nv_fma_f32 v237, v237, v216, v216\nM 40,172,16\ns_add_u32 m0, s50, 16384\ns_nop 0\nbuffer_load_dwordx4 v221, s[12:15], s63 offen offset:1024 lds\n"
"v_rcp_f32 v235, v235\nv_mfma_f32_16x16x32_bf16 v[56:59], v[168:171], a[4:7], 0\nR 162,188,36864\ns_add_u32 m0, s50, 24576\nv_rcp_f32 v236, v236\nM 56,172,20\n"
"G 1\ns_barrier\ns_add_u32 s63, s54, 1835264\nv_rcp_f32 v237, v237\nv_mfma_f32_16x16x32_bf16 v[72:75], v[168:171], a[8:11], 0\nR 164,188,40960\n"
"ds_read2st64_b32 v[8:9], v225 offset0:0 offset1:1\nv_mov_b32 v248, 0\nv_mul_f32 v234, v108, v234\nM 72,172,24\nbuffer_load_dwordx4 v220, s[12:15], s63 offen lds\nds_read2st64_b32 v[10:11], v225 offset0:2 offset1:3\n"
"v_mov_b32 v249, 0\nv_mul_f32 v235, v109, v235\nv_mfma_f32_16x16x32_bf16 v[88:91], v[168:171], a[12:15], 0\nR 166,188,45056\ns_add_u32 s61, s82, 845824\ns_mov_b64 exec, 0xf\n"
"buffer_atomic_or_x2 v[248:249], v250, s[20:23], s61 offen sc0 sc1\ns_mov_b64 exec, -1\nv_mul_f32 v236, v110, v236\nM 88,172,28\nds_read2st64_b32 v[12:13], v225 offset0:4 offset1:5\nv_mul_f32 v237, v111, v237\n"
"v_cvt_pk_bf16_f32 v106, v234, v235\nv_cvt_pk_bf16_f32 v107, v236, v237\nv_mfma_f32_16x16x32_bf16 v[92:95], v[176:179], a[12:15], 0\nR 168,189,32768\nds_read2st64_b32 v[14:15], v225 offset0:6 offset1:7\nv_mul_f32 v230, v112, v112\n"
"v_mul_f32 v231, v113, v113\nM 92,180,28\nbuffer_load_dwordx4 v221, s[12:15], s63 offen offset:1024 lds\nds_read2st64_b32 v[24:25], v225 offset0:16 offset1:17\nv_mul_f32 v232, v114, v114\nv_mfma_f32_16x16x32_bf16 v[76:79], v[176:179], a[8:11], 0\n"
"R 170,189,36864\ns_add_u32 m0, s50, 32768\ns_add_u32 s63, s54, 2064640\nv_mul_f32 v233, v115, v115\nM 76,180,24\nbuffer_load_dwordx4 v220, s[12:15], s63 offen lds\n"
"v_fma_f32 v230, v230, v218, v217\nv_mfma_f32_16x16x32_bf16 v[60:63], v[176:179], a[4:7], 0\nR 172,189,40960\nv_fma_f32 v231, v231, v218, v217\nG 8\nv_add_f32 v0, v0, v8\n"
"v_fma_f32 v232, v232, v218, v217\nM 60,180,20\nds_read2st64_b32 v[26:27], v225 offset0:18 offset1:19\nv_fma_f32 v233, v233, v218, v217\nv_mul_f32 v230, v112, v230\nv_mul_f32 v231, v113, v231\n"
"v_mfma_f32_16x16x32_bf16 v[44:47], v[176:179], a[0:3], 0\nR 174,189,45056\nv_mul_f32 v232, v114, v232\nv_add_f32 v1, v1, v9\nv_mul_f32 v233, v115, v233\nM 44,180,16\n"
"G 8\nv_exp_f32 v230, v230\nv_add_f32 v2, v2, v10\nv_exp_f32 v231, v231\nM 32,160,32\nR 176,190,32768\n"
"v_fma_f32 v230, v230, v219, v219\nv_add_f32 v3, v3, v11\nv_exp_f32 v232, v232\nM 32,164,48\nbuffer_load_dwordx4 v221, s[12:15], s63 offen offset:1024 lds\nG 8\n"
"v_add_f32 v4, v4, v12\nv_exp_f32 v233, v233\nM 48,160,36\nR 178,190,36864\nv_fma_f32 v231, v231, v219, v219\nv_add_f32 v5, v5, v13\n"
"v_fma_f32 v232, v232, v219, v219\nM 48,164,52\nds_read2st64_b32 v[28:29], v225 offset0:20 offset1:21\nv_rcp_f32 v230, v230\nv_fma_f32 v233, v233, v219, v219\nM 64,160,40\n"
"R 180,190,40960\nv_mul_f32 v230, v112, v230\nG 9\nv_add_f32 v6, v6, v14\nv_rcp_f32 v231, v231\nM 64,164,56\n"
"s_add_u32 m0, s50, 40960\nv_rcp_f32 v232, v232\nv_add_f32 v7, v7, v15\nv_rcp_f32 v233, v233\nM 80,160,44\nR 182,190,45056\n"
"v_mul_f32 v231, v113, v231\nG 9\nv_add_f32 v16, v16, v24\nv_mul_f32 v232, v114, v232\nM 80,164,60\nG 5\n"
"v_mul_f32 v233, v115, v233\nv_cvt_pk_bf16_f32 v112, v230, v231\nv_add_f32 v17, v17, v25\nv_cvt_pk_bf16_f32 v113, v232, v233\nM 84,168,44\nR 160,191,32768\n"
"v_mul_f32 v234, v116, v116\nv_add_f32 v18, v18, v26\nv_mul_f32 v235, v117, v117\nM 84,172,60\ns_add_u32 s63, s54, 2294016\nds_read2st64_b32 v[30:31], v225 offset0:22 offset1:23\n"
"v_mul_f32 v236, v118, v118\nv_add_f32 v19, v19, v27\nG 4\nv_add_f32 v20, v20, v28\nv_add_f32 v21, v21, v29\nG 0\n"
"v_add_f32 v22, v22, v30\nv_add_f32 v23, v23, v31\nM 68,168,40\nR 162,191,36864\nv_mul_f32 v237, v119, v119\nM 68,172,56\n"
"buffer_load_dwordx4 v220, s[12:15], s63 offen lds\nM 52,168,36\nR 164,191,40960\nv_fma_f32 v234, v234, v218, v217\nM 52,172,52\nv_fma_f32 v235, v235, v218, v217\n"
"v_fma_f32 v236, v236, v218, v217\nM 36,168,32\nR 166,191,45056\nv_fma_f32 v237, v237, v218, v217\nM 36,172,48\nv_mul_f32 v234, v116, v234\n"
"v_mul_f32 v235, v117, v235\nM 40,176,32\nR 168,188,49152\nv_mul_f32 v236, v118, v236\nM 40,180,48\nbuffer_load_dwordx4 v221, s[12:15], s63 offen offset:1024 lds\n"
"M 56,176,36\nR 170,188,53248\nv_mul_f32 v237, v119, v237\nM 56,180,52\ns_add_u32 m0, s50, 49152\nv_exp_f32 v234, v234\n"
"M 72,176,40\nR 172,184,0\ns_add_u32 s63, s54, 2523392\nM 72,180,56\ns_add_u32 s64, s68, 37748736\nv_exp_f32 v235, v235\n"
"M 88,176,44\nR 174,184,4096\nv_fma_f32 v234, v234, v219, v219\nM 88,180,60\nG 4\nv_exp_f32 v236, v236\n"
"M 92,160,44\nR 176,189,49152\ns_waitcnt vmcnt(5)\nv_cmp_eq_u64 s[86:87], s[46:47], v[248:249]\nM 92,164,60\nbuffer_load_dwordx4 v220, s[12:15], s63 offen lds\n"
"M 76,160,40\nR 178,189,53248\ns_and_b32 s62, s86, 0xf\ns_cmp_eq_u32 s62, 0xf\ns_cbranch_scc0 Lq812\nLq813:\n"
"M 76,164,56\ns_add_u32 s65, s68, 37863424\nv_exp_f32 v237, v237\nM 60,160,36\nR 180,185,0\nv_fma_f32 v235, v235, v219, v219\n"
"M 60,164,52\nbuffer_load_dwordx4 a[0:3], v252, s[20:23], s64 offen sc1\nM 44,160,32\nR 182,185,4096\nv_fma_f32 v236, v236, v219, v219\nM 44,164,48\n"
"G 4\nv_rcp_f32 v234, v234\nM 32,168,64\nR 160,190,49152\nv_fma_f32 v237, v237, v219, v219\nM 32,172,80\n"
"s_add_u32 s66, s69, 37748736\nv_rcp_f32 v235, v235\nM 48,168,68\nR 162,190,53248\nv_mul_f32 v234, v116, v234\nM 48,172,84\n"
"buffer_load_dwordx4 a[4:7], v252, s[20:23], s65 offen sc1\nM 64,168,72\nR 164,186,0\nv_mul_f32 v235, v117, v235\nM 64,172,88\nv_rcp_f32 v236, v236\n"
"M 80,168,76\nR 166,186,4096\nv_mul_f32 v236, v118, v236\nM 80,172,92\nG 4\nv_rcp_f32 v237, v237\n"
"M 84,176,76\nR 168,191,49152\nv_mul_f32 v237, v119, v237\nM 84,180,92\nbuffer_load_dwordx4 a[8:11], v252, s[20:23], s66 offen sc1\nM 68,176,72\n"
"R 170,191,53248\nv_cvt_pk_bf16_f32 v114, v234, v235\nM 68,180,88\ns_add_u32 s67, s69, 37863424\nv_cvt_pk_bf16_f32 v115, v236, v237\nv_mul_f32 v230, v120, v120\n"
"M 52,176,68\nR 172,187,0\nv_mul_f32 v231, v121, v121\nM 52,180,84\ns_add_u32 s64, s68, 37755904\nbuffer_load_dwordx4 v221, s[12:15], s63 offen offset:1024 lds\n"
"M 36,176,64\nR 174,187,4096\ns_add_u32 m0, s50, 57344\nM 36,180,80\ns_add_u32 s63, s54, 2752768\nG 4\n"
"v_mul_f32 v232, v122, v122\nM 40,160,64\nR 176,184,8192\ns_waitcnt vmcnt(7)\ns_barrier\nM 40,164,80\n"
"buffer_load_dwordx4 a[12:15], v252, s[20:23], s67 offen sc1\nbuffer_load_dwordx4 v220, s[12:15], s63 offen lds\nM 56,160,68\nR 178,184,12288\nv_mul_f32 v233, v123, v123\nM 56,164,84\n"
"s_add_u32 s65, s68, 37870592\nv_fma_f32 v230, v230, v218, v217\nv_fma_f32 v231, v231, v218, v217\nM 72,160,72\nR 180,184,16384\nv_fma_f32 v232, v232, v218, v217\n"
"M 72,164,88\nbuffer_load_dwordx4 a[16:19], v252, s[20:23], s64 offen sc1\nbuffer_load_dwordx4 v221, s[12:15], s63 offen offset:1024 lds\nM 88,160,76\nR 182,184,20480\ns_add_u32 m0, s50, 65536\n"
"M 88,164,92\nG 4\nv_fma_f32 v233, v233, v218, v217\nv_mul_f32 v230, v120, v230\nM 92,168,76\nR 160,185,8192\n"
"s_add_u32 s63, s54, 2982144\nM 92,172,92\ns_add_u32 s66, s69, 37755904\nbuffer_load_dwordx4 v220, s[12:15], s63 offen lds\nM 76,168,72\nR 162,185,12288\n"
"v_mul_f32 v231, v121, v231\nM 76,172,88\nbuffer_load_dwordx4 a[20:23], v252, s[20:23], s65 offen sc1\nM 60,168,68\nR 164,185,16384\nv_mul_f32 v232, v122, v232\n"
"M 60,172,84\nbuffer_load_dwordx4 v221, s[12:15], s63 offen offset:1024 lds\nM 44,168,64\nR 166,185,20480\nv_mul_f32 v233, v123, v233\nM 44,172,80\n"
"G 4\nv_exp_f32 v230, v230\nM 32,176,96\nR 168,186,8192\nv_fma_f32 v230, v230, v219, v219\nM 32,180,112\n"
"buffer_load_dwordx4 a[24:27], v252, s[20:23], s66 offen sc1\nM 48,176,100\nR 170,186,12288\nv_mul_f32 v234, v124, v124\nM 48,180,116\ns_add_u32 s67, s69, 37870592\n"
"v_exp_f32 v231, v231\nM 64,176,104\nR 172,186,16384\nv_fma_f32 v231, v231, v219, v219\nM 64,180,120\nbuffer_store_dwordx4 v[104:107], v252, s[20:23], s84 offen offset:1024 sc1\n"
"M 80,176,108\nR 174,186,20480\nv_mul_f32 v235, v125, v125\nM 80,180,124\nG 4\nv_exp_f32 v232, v232\n"
"M 84,160,108\nR 176,187,8192\nv_fma_f32 v232, v232, v219, v219\nM 84,164,124\nbuffer_load_dwordx4 a[28:31], v252, s[20:23], s67 offen sc1\nM 68,160,104\n"
"R 178,187,12288\nv_mul_f32 v236, v126, v126\nM 68,164,120\ns_add_u32 s64, s68, 37749760\nv_exp_f32 v233, v233\nM 52,160,100\n"
"R 180,187,16384\nv_fma_f32 v233, v233, v219, v219\nM 52,164,116\ns_add_u32 s65, s68, 37864448\nv_rcp_f32 v230, v230\nM 36,160,96\n"
"R 182,187,20480\nv_mul_f32 v230, v120, v230\nM 36,164,112\nG 4\nv_rcp_f32 v231, v231\nM 40,168,96\n"
"R 160,184,24576\nv_mul_f32 v231, v121, v231\nM 40,172,112\nbuffer_load_dwordx4 a[32:35], v252, s[20:23], s64 offen sc1\nM 56,168,100\nR 162,184,28672\n"
"v_cvt_pk_bf16_f32 v120, v230, v231\nM 56,172,116\ns_add_u32 s66, s69, 37749760\nv_rcp_f32 v232, v232\nM 72,168,104\nR 164,184,32768\n"
"v_mul_f32 v232, v122, v232\nM 72,172,120\nbuffer_load_dwordx4 a[36:39], v252, s[20:23], s65 offen sc1\nM 88,168,108\nR 166,184,36864\nv_mul_f32 v237, v127, v127\n"
"M 88,172,124\nG 4\nv_rcp_f32 v233, v233\nM 92,176,108\nR 168,185,24576\ns_add_u32 m0, s50, 73728\n"
"M 92,180,124\ns_add_u32 s67, s69, 37864448\nv_mul_f32 v233, v123, v233\nv_cvt_pk_bf16_f32 v121, v232, v233\nM 76,176,104\nR 170,185,28672\n"
"s_add_u32 s63, s54, 3211520\nM 76,180,120\nbuffer_load_dwordx4 a[40:43], v252, s[20:23], s66 offen sc1\nM 60,176,100\nR 172,185,32768\nv_fma_f32 v234, v234, v218, v217\n"
"M 60,180,116\nbuffer_load_dwordx4 v220, s[12:15], s63 offen lds\nM 44,176,96\nR 174,185,36864\nv_fma_f32 v235, v235, v218, v217\nM 44,180,112\n"
"G 4\nv_fma_f32 v236, v236, v218, v217\nv_fma_f32 v237, v237, v218, v217\nM 32,160,128\nR 176,186,24576\nv_mul_f32 v234, v124, v234\n"
"M 32,164,144\nbuffer_load_dwordx4 a[44:47], v252, s[20:23], s67 offen sc1\nbuffer_load_dwordx4 v221, s[12:15], s63 offen offset:1024 lds\nM 48,160,132\nR 178,186,28672\ns_add_u32 m0, s50, 81920\n"
"M 48,164,148\ns_add_u32 s64, s68, 37756928\nv_exp_f32 v234, v234\nM 64,160,136\nR 180,186,32768\ns_add_u32 s63, s54, 3440896\n"
"M 64,164,152\ns_add_u32 s65, s68, 37871616\nbuffer_load_dwordx4 v220, s[12:15], s63 offen lds\nM 80,160,140\nR 182,186,36864\nv_mul_f32 v235, v125, v235\n"
"M 80,164,156\nG 4\nv_exp_f32 v235, v235\nM 84,168,140\nR 160,187,24576\nv_mul_f32 v236, v126, v236\n"
"M 84,172,156\nbuffer_load_dwordx4 a[48:51], v252, s[20:23], s64 offen sc1\nbuffer_load_dwordx4 v221, s[12:15], s63 offen offset:1024 lds\nM 68,168,136\nR 162,187,28672\nv_mul_f32 v237, v127, v237\n"
"M 68,172,152\ns_add_u32 s66, s69, 37756928\nv_exp_f32 v236, v236\nM 52,168,132\nR 164,187,32768\ns_waitcnt vmcnt(12)\n"
"s_barrier\nM 52,172,148\nbuffer_load_dwordx4 a[52:55], v252, s[20:23], s65 offen sc1\ns_movk_i32 s63, 2048\nv_mad_u32_u24 v220, v228, s63, v229\nv_add_u32 v221, 916480, v220\n"
"s_add_u32 m0, s50, 90112\ns_add_u32 s63, s56, 0\nbuffer_load_dwordx4 v220, s[16:19], s63 offen lds\nM 36,168,128\nR 166,187,36864\nv_fma_f32 v234, v234, v219, v219\n"
"M 36,172,144\nG 4\nv_exp_f32 v237, v237\nM 40,176,128\nR 168,184,40960\nv_fma_f32 v235, v235, v219, v219\n"
"M 40,180,144\ns_add_u32 s67, s69, 37871616\nbuffer_load_dwordx4 v221, s[16:19], s63 offen offset:1024 lds\nM 56,176,132\nR 170,184,45056\ns_add_u32 m0, s50, 98304\n"
"M 56,180,148\nbuffer_load_dwordx4 a[56:59], v252, s[20:23], s66 offen sc1\nM 72,176,136\nR 172,184,49152\ns_add_u32 s63, s56, 524288\nM 72,180,152\n"
"buffer_load_dwordx4 v220, s[16:19], s63 offen lds\nM 88,176,140\nR 174,184,53248\nv_fma_f32 v236, v236, v219, v219\nM 88,180,156\nG 4\n"
"v_rcp_f32 v234, v234\nM 92,160,140\nR 176,185,40960\nv_fma_f32 v237, v237, v219, v219\nM 92,164,156\nbuffer_load_dwordx4 a[60:63], v252, s[20:23], s67 offen sc1\n"
"buffer_load_dwordx4 v221, s[16:19], s63 offen offset:1024 lds\nM 76,160,136\nR 178,185,45056\nv_mul_f32 v234, v124, v234\nM 76,164,152\ns_add_u32 m0, s50, 106496\n"
"v_rcp_f32 v235, v235\nM 60,160,132\nR 180,185,49152\nv_mul_f32 v235, v125, v235\nM 60,164,148\ns_add_u32 s63, s56, 65536\n"
"v_rcp_f32 v236, v236\nM 44,160,128\nR 182,185,53248\nv_mul_f32 v236, v126, v236\nM 44,164,144\nG 4\n"
"v_rcp_f32 v237, v237\nM 32,168,160\nR 160,186,40960\nv_mul_f32 v237, v127, v237\nM 32,172,176\nbuffer_load_dwordx4 v220, s[16:19], s63 offen lds\n"
"M 48,168,164\nR 162,186,45056\nv_cvt_pk_bf16_f32 v122, v234, v235\nM 48,172,180\nbuffer_store_dwordx4 v[112:115], v252, s[20:23], s85 offen offset:0 sc1\nM 64,168,168\n"
"R 164,186,49152\nv_cvt_pk_bf16_f32 v123, v236, v237\nM 64,172,184\nbuffer_load_dwordx4 v221, s[16:19], s63 offen offset:1024 lds\nM 80,168,172\nR 166,186,53248\n"
"s_add_u32 m0, s50, 114688\nM 80,172,188\nbuffer_store_dwordx4 v[120:123], v252, s[20:23], s85 offen offset:1024 sc1\nG 4\nM 84,176,172\nR 168,187,40960\n"
"s_add_u32 s63, s56, 589824\nM 84,180,188\nbuffer_load_dwordx4 v220, s[16:19], s63 offen lds\nM 68,176,168\nR 170,187,45056\nv_mul_f32 v230, v0, v0\n"
"v_mul_f32 v231, v1, v1\nM 68,180,184\ns_add_u32 s84, s73, 37754880\ns_add_u32 s85, s73, 37869568\nv_mul_f32 v232, v2, v2\nv_mul_f32 v233, v3, v3\n"
"M 52,176,164\nR 172,187,49152\nv_fma_f32 v230, v230, v215, v214\nv_fma_f32 v231, v231, v215, v214\nM 52,180,180\nbuffer_load_dwordx4 v221, s[16:19], s63 offen offset:1024 lds\n"
"v_fma_f32 v232, v232, v215, v214\nM 36,176,160\nR 174,187,53248\nv_fma_f32 v233, v233, v215, v214\nv_mul_f32 v230, v0, v230\nM 36,180,176\n"
"G 4\nv_exp_f32 v230, v230\nv_mul_f32 v231, v1, v231\nM 40,160,160\nR 176,184,57344\nv_mul_f32 v232, v2, v232\n"
"v_mul_f32 v233, v3, v233\nM 40,164,176\nv_exp_f32 v231, v231\nv_exp_f32 v232, v232\nM 56,160,164\nR 178,184,61440\n"
"v_fma_f32 v230, v230, v216, v216\nv_exp_f32 v233, v233\nM 56,164,180\nv_rcp_f32 v230, v230\nv_fma_f32 v231, v231, v216, v216\nM 72,160,168\n"
"R 180,188,0\nv_fma_f32 v232, v232, v216, v216\nv_fma_f32 v233, v233, v216, v216\nM 72,164,184\nv_rcp_f32 v231, v231\nv_rcp_f32 v232, v232\n"
"M 88,160,172\nR 182,188,4096\nv_mul_f32 v230, v0, v230\nv_rcp_f32 v233, v233\nM 88,164,188\nG 4\n"
"v_mul_f32 v231, v1, v231\nv_mul_f32 v232, v2, v232\nv_mul_f32 v233, v3, v233\nM 92,168,172\nR 160,185,57344\nv_cvt_pk_bf16_f32 v0, v230, v231\n"
"v_cvt_pk_bf16_f32 v1, v232, v233\nM 92,172,188\nv_mul_f32 v234, v4, v4\nv_mul_f32 v235, v5, v5\nv_mul_f32 v236, v6, v6\nM 76,168,168\n"
"R 162,185,61440\nv_mul_f32 v237, v7, v7\nv_fma_f32 v234, v234, v215, v214\nM 76,172,184\nv_fma_f32 v235, v235, v215, v214\nv_fma_f32 v236, v236, v215, v214\n"
"v_fma_f32 v237, v237, v215, v214\nM 60,168,164\nR 164,189,0\nv_mul_f32 v234, v4, v234\nv_mul_f32 v235, v5, v235\nM 60,172,180\n"
"v_exp_f32 v234, v234\nv_mul_f32 v236, v6, v236\nM 44,168,160\nR 166,189,4096\nv_mul_f32 v237, v7, v237\nv_exp_f32 v235, v235\n"
"M 44,172,176\nG 4\nv_exp_f32 v236, v236\nv_exp_f32 v237, v237\nM 32,176,192\nR 168,186,57344\n"
"v_fma_f32 v234, v234, v216, v216\nv_fma_f32 v235, v235, v216, v216\nM 32,180,208\nv_rcp_f32 v234, v234\nv_fma_f32 v236, v236, v216, v216\nM 48,176,196\n"
"R 170,186,61440\nv_fma_f32 v237, v237, v216, v216\nv_rcp_f32 v235, v235\nM 48,180,212\nv_rcp_f32 v236, v236\nv_rcp_f32 v237, v237\n"
"M 64,176,200\nR 172,190,0\nv_mul_f32 v234, v4, v234\nv_mul_f32 v235, v5, v235\nM 64,180,216\nv_mul_f32 v236, v6, v236\n"
"v_mul_f32 v237, v7, v237\nv_cvt_pk_bf16_f32 v2, v234, v235\nM 80,176,204\nR 174,190,4096\nv_cvt_pk_bf16_f32 v3, v236, v237\nv_mul_f32 v230, v16, v16\n"
"M 80,180,220\nG 4\nv_mul_f32 v231, v17, v17\nv_mul_f32 v232, v18, v18\nv_mul_f32 v233, v19, v19\nM 84,160,204\n"
"R 176,187,57344\ns_waitcnt vmcnt(6)\ns_barrier\nv_fma_f32 v230, v230, v218, v217\nM 84,164,220\nv_fma_f32 v231, v231, v218, v217\n"
"v_fma_f32 v232, v232, v218, v217\nv_fma_f32 v233, v233, v218, v217\nM 68,160,200\nR 178,187,61440\nv_mul_f32 v230, v16, v230\nv_mul_f32 v231, v17, v231\n"
"M 68,164,216\nv_exp_f32 v230, v230\nv_mul_f32 v232, v18, v232\nM 52,160,196\nR 180,191,0\nv_mul_f32 v233, v19, v233\n"
"v_exp_f32 v231, v231\nM 52,164,212\nv_exp_f32 v232, v232\nv_exp_f32 v233, v233\nM 36,160,192\nR 182,191,4096\n"
"v_fma_f32 v230, v230, v219, v219\nv_fma_f32 v231, v231, v219, v219\nM 36,164,208\nG 4\nv_rcp_f32 v230, v230\nv_fma_f32 v232, v232, v219, v219\n"
"M 40,168,192\nR 160,188,8192\nv_fma_f32 v233, v233, v219, v219\nv_rcp_f32 v231, v231\nM 40,172,208\nv_rcp_f32 v232, v232\n"
"v_rcp_f32 v233, v233\nM 56,168,196\nR 162,188,12288\nv_mul_f32 v230, v16, v230\nv_mul_f32 v231, v17, v231\nM 56,172,212\n"
"s_add_u32 m0, s50, 0\nv_mul_f32 v232, v18, v232\nv_mul_f32 v233, v19, v233\nv_cvt_pk_bf16_f32 v16, v230, v231\nM 72,168,200\nR 164,188,16384\n"
"v_cvt_pk_bf16_f32 v17, v232, v233\nv_mul_f32 v234, v20, v20\nM 72,172,216\ns_add_u32 s63, s57, 0\nv_mul_f32 v235, v21, v21\nv_mul_f32 v236, v22, v22\n"
"v_mul_f32 v237, v23, v23\nM 88,168,204\nR 166,188,20480\nv_mov_b32 v226, s46\nv_fma_f32 v234, v234, v218, v217\nM 88,172,220\n"
"G 4\nv_mov_b32 v227, s47\nv_fma_f32 v235, v235, v218, v217\nv_fma_f32 v236, v236, v218, v217\nM 92,176,204\nR 168,189,8192\n"
"s_add_u32 s61, s81, 845832\ns_lshl_b32 s62, s44, 5\ns_add_u32 s61, s61, s62\ns_waitcnt vmcnt(0)\ns_mov_b64 exec, 1\nbuffer_atomic_swap_x2 v[226:227], off, s[20:23], s61 sc1\n"
"s_mov_b64 exec, -1\nv_fma_f32 v237, v237, v218, v217\nM 92,180,220\nbuffer_load_dwordx4 v220, s[16:19], s63 offen lds\nv_mul_f32 v234, v20, v234\nM 76,176,200\n"
"R 170,189,12288\nv_mul_f32 v235, v21, v235\nv_mul_f32 v236, v22, v236\nM 76,180,216\nv_exp_f32 v234, v234\nv_mul_f32 v237, v23, v237\n"
"M 60,176,196\nR 172,189,16384\nv_fma_f32 v234, v234, v219, v219\nv_exp_f32 v235, v235\nM 60,180,212\nbuffer_load_dwordx4 v221, s[16:19], s63 offen offset:1024 lds\n"
"v_exp_f32 v236, v236\nM 44,176,192\nR 174,189,20480\ns_mov_b32 s60, 158208\nv_bfe_u32 v246, v252, 4, 4\nv_lshl_add_u32 v246, v246, 3, s60\n"
"v_exp_f32 v237, v237\nM 44,180,208\nds_read_b64 v[238:239], v246 offset:0\nG 5\nv_fma_f32 v235, v235, v219, v219\nM 32,160,224\n"
"R 176,190,8192\nds_read_b64 v[240:241], v246 offset:128\ns_add_u32 s63, s57, 524288\nv_fma_f32 v236, v236, v219, v219\nM 32,164,240\nds_read_b64 v[242:243], v246 offset:256\n"
"s_add_u32 m0, s50, 8192\nv_fma_f32 v237, v237, v219, v219\nM 48,160,228\nR 178,190,12288\nds_read_b64 v[244:245], v246 offset:384\nv_rcp_f32 v234, v234\n"
"M 48,164,244\nds_read_b64 v[246:247], v246 offset:0\nbuffer_load_dwordx4 v220, s[16:19], s63 offen lds\nv_rcp_f32 v235, v235\nM 64,160,232\nR 180,190,16384\n"
"v_mul_f32 v234, v20, v234\nv_rcp_f32 v236, v236\nM 64,164,248\nbuffer_load_dwordx4 v221, s[16:19], s63 offen offset:1024 lds\nv_rcp_f32 v237, v237\nM 80,160,236\n"
"R 182,190,20480\ns_add_u32 m0, s50, 16384\nM 80,164,252\nG 9\nv_mul_f32 v235, v21, v235\nv_mul_f32 v236, v22, v236\n"
"M 84,168,236\nR 160,191,8192\ns_add_u32 s63, s57, 65536\nM 84,172,252\nbuffer_load_dwordx4 v220, s[16:19], s63 offen lds\nM 68,168,232\n"
"R 162,191,12288\nv_mul_f32 v237, v23, v237\nM 68,172,248\nbuffer_store_dwordx4 v[0:3], v252, s[20:23], s84 offen offset:0 sc1\nM 52,168,228\nR 164,191,16384\n"
"G 9\nv_mfma_f32_16x16x32_bf16 v[32:35], v[238:241], v[208:211], v[32:35]\nv_cvt_pk_bf16_f32 v18, v234, v235\nM 52,172,244\nbuffer_load_dwordx4 v221, s[16:19], s63 offen offset:1024 lds\nM 36,168,224\n"
"R 166,191,20480\nG 9\nv_mfma_f32_16x16x32_bf16 v[36:39], v[240:243], v[208:211], v[36:39]\ns_add_u32 m0, s50, 24576\nM 36,172,240\nG 4\n"
"v_cvt_pk_bf16_f32 v19, v236, v237\nM 40,176,224\nR 168,188,24576\nv_mfma_f32_16x16x32_bf16 v[40:43], v[242:245], v[208:211], v[40:43]\ns_add_u32 s63, s57, 589824\nbuffer_store_dwordx4 v[16:19], v252, s[20:23], s85 offen offset:0 sc1\n"
"M 40,180,240\nbuffer_load_dwordx4 v220, s[16:19], s63 offen lds\nM 56,176,228\nR 170,188,28672\nv_mfma_f32_16x16x32_bf16 v[44:47], v[244:247], v[208:211], v[44:47]\nM 56,180,244\n"
"buffer_load_dwordx4 v221, s[16:19], s63 offen offset:1024 lds\nM 72,176,232\nR 172,188,32768\nv_mfma_f32_16x16x32_bf16 v[48:51], v[244:247], v[210:213], v[48:51]\ns_add_u32 m0, s50, 32768\ns_add_u32 s63, s56, 131072\n"
"M 72,180,248\nbuffer_load_dwordx4 v220, s[16:19], s63 offen lds\nM 88,176,236\nR 174,188,36864\nv_mfma_f32_16x16x32_bf16 v[52:55], v[238:241], v[210:213], v[52:55]\nM 88,180,252\n"
"G 4\nM 92,160,236\nR 176,189,24576\nv_mfma_f32_16x16x32_bf16 v[56:59], v[240:243], v[210:213], v[56:59]\nM 92,164,252\nbuffer_load_dwordx4 v221, s[16:19], s63 offen offset:1024 lds\n"
"M 76,160,232\nR 178,189,28672\nv_mfma_f32_16x16x32_bf16 v[60:63], v[242:245], v[210:213], v[60:63]\nv_mov_b32 v248, 0\nM 76,164,248\nv_mov_b32 v249, 0\n"
"M 60,160,228\nR 180,189,32768\ns_add_u32 s61, s82, 845832\ns_mov_b64 exec, 0xf\nbuffer_atomic_or_x2 v[248:249], v250, s[20:23], s61 offen sc0 sc1\ns_mov_b64 exec, -1\n"
"M 60,164,244\nM 44,160,224\nR 182,189,36864\nM 44,164,240\ns_barrier\nG 4\n"
"v_mfma_f32_16x16x32_bf16 v[96:99], v[168:171], a[0:3], 0\nR 160,190,24576\nM 96,172,16\ns_mov_b32 m0, s51\nv_mfma_f32_16x16x32_bf16 v[112:115], v[168:171], a[4:7], 0\nR 162,190,28672\n"
"M 112,172,20\nds_write_addtid_b32 v64 offset:0\nv_mfma_f32_16x16x32_bf16 v[128:131], v[168:171], a[8:11], 0\nR 164,190,32768\nM 128,172,24\nds_write_addtid_b32 v65 offset:256\n"
"v_mfma_f32_16x16x32_bf16 v[144:147], v[168:171], a[12:15], 0\nR 166,190,36864\nM 144,172,28\nds_write_addtid_b32 v66 offset:512\nG 7\nv_mfma_f32_16x16x32_bf16 v[148:151], v[176:179], a[12:15], 0\n"
"R 168,191,24576\nM 148,180,28\nds_write_addtid_b32 v67 offset:768\nv_mfma_f32_16x16x32_bf16 v[132:135], v[176:179], a[8:11], 0\nR 170,191,28672\nM 132,180,24\n"
"ds_write_addtid_b32 v68 offset:1024\nv_mfma_f32_16x16x32_bf16 v[116:119], v[176:179], a[4:7], 0\nR 172,191,32768\nM 116,180,20\nds_write_addtid_b32 v69 offset:1280\nv_mfma_f32_16x16x32_bf16 v[100:103], v[176:179], a[0:3], 0\n"
"R 174,191,36864\nds_write_addtid_b32 v70 offset:1536\nM 100,180,16\nds_write_addtid_b32 v71 offset:1792\nds_write_addtid_b32 v72 offset:2048\nG 11\n"
"v_mfma_f32_16x16x32_bf16 v[104:107], v[160:163], a[0:3], 0\nR 176,188,40960\nds_write_addtid_b32 v73 offset:2304\nM 104,164,16\nds_write_addtid_b32 v74 offset:2560\nds_write_addtid_b32 v75 offset:2816\n"
"v_mfma_f32_16x16x32_bf16 v[120:123], v[160:163], a[4:7], 0\nR 178,188,45056\nds_write_addtid_b32 v76 offset:3072\nM 120,164,20\nds_write_addtid_b32 v77 offset:3328\nds_write_addtid_b32 v78 offset:3584\n"
"v_mfma_f32_16x16x32_bf16 v[136:139], v[160:163], a[8:11], 0\nR 180,188,49152\nds_write_addtid_b32 v79 offset:3840\nM 136,164,24\nds_write_addtid_b32 v80 offset:4096\nds_write_addtid_b32 v81 offset:4352\n"
"v_mfma_f32_16x16x32_bf16 v[152:155], v[160:163], a[12:15], 0\nR 182,188,53248\nds_write_addtid_b32 v82 offset:4608\nM 152,164,28\nds_write_addtid_b32 v83 offset:4864\nds_write_addtid_b32 v84 offset:5120\n"
"G 15\nv_mfma_f32_16x16x32_bf16 v[156:159], v[168:171], a[12:15], 0\nR 160,189,40960\nds_write_addtid_b32 v85 offset:5376\nM 156,172,28\nds_write_addtid_b32 v86 offset:5632\n"
"ds_write_addtid_b32 v87 offset:5888\nv_mfma_f32_16x16x32_bf16 v[140:143], v[168:171], a[8:11], 0\nR 162,189,45056\nds_write_addtid_b32 v88 offset:6144\nM 140,172,24\nds_write_addtid_b32 v89 offset:6400\n"
"ds_write_addtid_b32 v90 offset:6656\nv_mfma_f32_16x16x32_bf16 v[124:127], v[168:171], a[4:7], 0\nR 164,189,49152\nds_write_addtid_b32 v91 offset:6912\nM 124,172,20\ns_add_u32 s64, s68, 37750784\n"
"ds_write_addtid_b32 v92 offset:7168\nds_write_addtid_b32 v93 offset:7424\nv_mfma_f32_16x16x32_bf16 v[108:111], v[168:171], a[0:3], 0\nR 166,189,53248\ns_waitcnt vmcnt(0)\nv_cmp_eq_u64 s[86:87], s[46:47], v[248:249]\n"
"M 108,172,16\nds_write_addtid_b32 v94 offset:7680\nds_write_addtid_b32 v95 offset:7936\nG 15\nM 96,176,32\nR 168,190,40960\n"
"s_and_b32 s62, s86, 0xf\ns_cmp_eq_u32 s62, 0xf\ns_cbranch_scc0 Lq847\nLq848:\nM 96,180,48\ns_add_u32 s65, s68, 37865472\n"
"s_add_u32 m0, s50, 40960\nv_mov_b32 v226, s46\nM 112,176,36\nR 170,190,45056\nG 2\ns_barrier\n"
"v_mov_b32 v227, s47\nM 112,180,52\ns_add_u32 s61, s81, 845848\ns_lshl_b32 s62, s44, 5\ns_add_u32 s61, s61, s62\ns_waitcnt vmcnt(0)\n"
"s_mov_b64 exec, 1\nbuffer_atomic_swap_x2 v[226:227], off, s[20:23], s61 sc1\ns_mov_b64 exec, -1\ns_add_u32 s63, s56, 655360\nds_read2st64_b32 v[64:65], v225 offset0:0 offset1:1\nM 128,176,40\n"
"R 172,190,49152\nbuffer_load_dwordx4 v220, s[16:19], s63 offen lds\nds_read2st64_b32 v[66:67], v225 offset0:2 offset1:3\nM 128,180,56\nbuffer_load_dwordx4 a[64:67], v252, s[20:23], s64 offen sc1\nds_read2st64_b32 v[68:69], v225 offset0:4 offset1:5\n"
"M 144,176,44\nR 174,190,53248\nds_read2st64_b32 v[70:71], v225 offset0:6 offset1:7\nM 144,180,60\nds_read2st64_b32 v[72:73], v225 offset0:8 offset1:9\nds_read2st64_b32 v[74:75], v225 offset0:10 offset1:11\n"
"M 148,160,44\nR 176,191,40960\nbuffer_load_dwordx4 v221, s[16:19], s63 offen offset:1024 lds\nds_read2st64_b32 v[76:77], v225 offset0:12 offset1:13\nM 148,164,60\nbuffer_load_dwordx4 a[68:71], v252, s[20:23], s65 offen sc1\n"
"G 9\nv_add_f32 v32, v32, v64\nM 132,160,40\nR 178,191,45056\ns_add_u32 s67, s69, 37865472\nv_add_f32 v33, v33, v65\n"
"M 132,164,56\ns_add_u32 s66, s69, 37750784\ns_add_u32 m0, s50, 49152\ns_add_u32 s63, s56, 196608\nG 8\nv_add_f32 v34, v34, v66\n"
"M 116,160,36\nR 180,191,49152\nbuffer_load_dwordx4 v220, s[16:19], s63 offen lds\nv_add_f32 v35, v35, v67\nM 116,164,52\nbuffer_load_dwordx4 a[72:75], v252, s[20:23], s66 offen sc1\n"
"G 8\nv_add_f32 v36, v36, v68\nM 100,160,32\nR 182,191,53248\nv_add_f32 v37, v37, v69\nG 7\n"
"v_add_f32 v38, v38, v70\nM 100,164,48\nds_read2st64_b32 v[78:79], v225 offset0:14 offset1:15\nv_add_f32 v39, v39, v71\nM 104,168,32\nR 160,184,0\n"
"buffer_load_dwordx4 v221, s[16:19], s63 offen offset:1024 lds\nG 8\nv_add_f32 v40, v40, v72\nM 104,172,48\nbuffer_load_dwordx4 a[76:79], v252, s[20:23], s67 offen sc1\nv_add_f32 v41, v41, v73\n"
"M 120,168,36\nR 162,184,4096\nG 8\nv_add_f32 v42, v42, v74\nv_add_f32 v43, v43, v75\nM 120,172,52\n"
"s_add_u32 s64, s68, 37757952\nds_read2st64_b32 v[80:81], v225 offset0:16 offset1:17\nG 7\nv_add_f32 v44, v44, v76\nM 136,168,40\nR 164,184,8192\n"
"ds_read2st64_b32 v[82:83], v225 offset0:18 offset1:19\nv_add_f32 v45, v45, v77\nM 136,172,56\ns_add_u32 s65, s68, 37872640\nds_read2st64_b32 v[84:85], v225 offset0:20 offset1:21\nG 6\n"
"v_add_f32 v46, v46, v78\nM 152,168,44\nR 166,184,12288\nds_read2st64_b32 v[86:87], v225 offset0:22 offset1:23\nv_add_f32 v47, v47, v79\nM 152,172,60\n"
"ds_read2st64_b32 v[88:89], v225 offset0:24 offset1:25\nds_read2st64_b32 v[90:91], v225 offset0:26 offset1:27\nM 156,176,44\nR 168,185,0\nds_read2st64_b32 v[92:93], v225 offset0:28 offset1:29\nM 156,180,60\n"
"buffer_load_dwordx4 a[80:83], v252, s[20:23], s64 offen sc1\nG 9\nv_add_f32 v48, v48, v80\nM 140,176,40\nR 170,185,4096\nv_add_f32 v49, v49, v81\n"
"G 8\nv_add_f32 v50, v50, v82\nM 140,180,56\nbuffer_load_dwordx4 a[84:87], v252, s[20:23], s65 offen sc1\nv_add_f32 v51, v51, v83\nM 124,176,36\n"
"R 172,185,8192\nG 8\nv_add_f32 v52, v52, v84\nv_add_f32 v53, v53, v85\nM 124,180,52\ns_add_u32 s66, s69, 37757952\n"
"ds_read2st64_b32 v[94:95], v225 offset0:30 offset1:31\nG 7\nv_add_f32 v54, v54, v86\nM 108,176,32\nR 174,185,12288\nv_add_f32 v55, v55, v87\n"
"M 108,180,48\nG 7\nv_add_f32 v56, v56, v88\nv_add_f32 v57, v57, v89\nv_mfma_f32_16x16x32_bf16 v[0:3], v[160:163], a[0:3], 0\nR 176,186,0\n"
"s_lshr_b32 s60, s45, 3\ns_lshl2_add_u32 s60, s60, s40\ns_lshl_b32 s60, s60, 8\nv_lshl_add_u32 v224, v252, 3, s60\nv_add_u32 v224, LqF-32768-LqPC, v224\ns_mov_b64 exec, 3\n"
"global_load_dword v224, v224, s[98:99]\ns_mov_b64 exec, -1\nM 0,164,16\nbuffer_load_dwordx4 a[88:91], v252, s[20:23], s66 offen sc1\nv_mfma_f32_16x16x32_bf16 v[16:19], v[160:163], a[4:7], 0\nR 178,186,4096\n"
"G 8\nv_add_f32 v58, v58, v90\nM 16,164,20\ns_add_u32 s67, s69, 37872640\nv_add_f32 v59, v59, v91\nG 6\n"
"v_add_f32 v60, v60, v92\nv_mfma_f32_16x16x32_bf16 a[224:227], v[160:163], a[8:11], 0\nR 180,186,8192\nv_add_f32 v61, v61, v93\nv_mfma_f32_16x16x32_bf16 a[224:227], v[164:167], a[24:27], a[224:227]\nbuffer_load_dwordx4 a[92:95], v252, s[20:23], s67 offen sc1\n"
"v_mfma_f32_16x16x32_bf16 a[240:243], v[160:163], a[12:15], 0\nR 182,186,12288\nG 5\nv_add_f32 v62, v62, v94\nv_mfma_f32_16x16x32_bf16 a[240:243], v[164:167], a[28:31], a[240:243]\ns_add_u32 s63, s56, 720896\n"
"G 4\nv_add_f32 v63, v63, v95\nv_mfma_f32_16x16x32_bf16 a[244:247], v[168:171], a[12:15], 0\nR 160,187,0\ns_add_u32 m0, s50, 57344\nv_mul_f32 v230, v32, v32\n"
"v_mfma_f32_16x16x32_bf16 a[244:247], v[172:175], a[28:31], a[244:247]\ns_add_u32 s64, s68, 37751808\ns_add_u32 s84, s73, 37752832\ns_add_u32 s85, s73, 37867520\nv_mul_f32 v231, v33, v33\nv_mfma_f32_16x16x32_bf16 a[228:231], v[168:171], a[8:11], 0\n"
"R 162,187,4096\nv_mov_b32 v248, 0\nv_mul_f32 v232, v34, v34\nv_mfma_f32_16x16x32_bf16 a[228:231], v[172:175], a[24:27], a[228:231]\nbuffer_load_dwordx4 a[96:99], v252, s[20:23], s64 offen sc1\nv_mov_b32 v249, 0\n"
"v_mul_f32 v233, v35, v35\nv_mfma_f32_16x16x32_bf16 v[20:23], v[168:171], a[4:7], 0\nR 164,187,8192\ns_add_u32 s61, s82, 845848\ns_mov_b64 exec, 0xf\nbuffer_atomic_or_x2 v[248:249], v250, s[20:23], s61 offen sc0 sc1\n"
"s_mov_b64 exec, -1\nv_fma_f32 v230, v230, v215, v214\nM 20,172,20\ns_add_u32 s65, s68, 37866496\nbuffer_load_dwordx4 v220, s[16:19], s63 offen lds\nv_fma_f32 v231, v231, v215, v214\n"
"v_mfma_f32_16x16x32_bf16 v[4:7], v[168:171], a[0:3], 0\nR 166,187,12288\nv_fma_f32 v232, v232, v215, v214\nv_fma_f32 v233, v233, v215, v214\nM 4,172,16\nG 4\n"
"v_mul_f32 v230, v32, v230\nv_mul_f32 v231, v33, v231\nv_mul_f32 v232, v34, v232\nv_mfma_f32_16x16x32_bf16 v[8:11], v[176:179], a[0:3], 0\nR 168,184,16384\nv_mul_f32 v233, v35, v233\n"
"v_exp_f32 v230, v230\nM 8,180,16\nbuffer_load_dwordx4 a[100:103], v252, s[20:23], s65 offen sc1\nbuffer_load_dwordx4 v221, s[16:19], s63 offen offset:1024 lds\nv_exp_f32 v231, v231\nv_mfma_f32_16x16x32_bf16 v[24:27], v[176:179], a[4:7], 0\n"
"R 170,184,20480\ns_add_u32 s67, s69, 37866496\nv_exp_f32 v232, v232\nM 24,180,20\ns_add_u32 s66, s69, 37751808\ns_add_u32 s63, s56, 393216\n"
"v_fma_f32 v230, v230, v216, v216\nv_exp_f32 v233, v233\nv_mfma_f32_16x16x32_bf16 a[232:235], v[176:179], a[8:11], 0\nR 172,184,24576\ns_add_u32 m0, s50, 65536\nv_fma_f32 v231, v231, v216, v216\n"
"v_mfma_f32_16x16x32_bf16 a[232:235], v[180:183], a[24:27], a[232:235]\nbuffer_load_dwordx4 a[104:107], v252, s[20:23], s66 offen sc1\nbuffer_load_dwordx4 v220, s[16:19], s63 offen lds\nv_fma_f32 v232, v232, v216, v216\nv_mfma_f32_16x16x32_bf16 a[248:251], v[176:179], a[12:15], 0\nR 174,184,28672\n"
"v_fma_f32 v233, v233, v216, v216\nv_rcp_f32 v230, v230\nv_mfma_f32_16x16x32_bf16 a[248:251], v[180:183], a[28:31], a[248:251]\nG 4\nv_rcp_f32 v231, v231\nv_rcp_f32 v232, v232\n"
"v_mfma_f32_16x16x32_bf16 a[252:255], v[160:163], a[12:15], 0\nR 176,185,16384\nv_mul_f32 v230, v32, v230\nv_rcp_f32 v233, v233\nv_mfma_f32_16x16x32_bf16 a[252:255], v[164:167], a[28:31], a[252:255]\nbuffer_load_dwordx4 a[108:111], v252, s[20:23], s67 offen sc1\n"
"buffer_load_dwordx4 v221, s[16:19], s63 offen offset:1024 lds\nv_mul_f32 v231, v33, v231\nv_mfma_f32_16x16x32_bf16 a[236:239], v[160:163], a[8:11], 0\nR 178,185,20480\ns_add_u32 m0, s50, 73728\nv_mul_f32 v232, v34, v232\n"
"v_mfma_f32_16x16x32_bf16 a[236:239], v[164:167], a[24:27], a[236:239]\ns_add_u32 s64, s68, 37758976\nv_mul_f32 v233, v35, v233\nv_cvt_pk_bf16_f32 v32, v230, v231\nv_cvt_pk_bf16_f32 v33, v232, v233\nv_mfma_f32_16x16x32_bf16 v[28:31], v[160:163], a[4:7], 0\n"
"R 180,185,24576\ns_add_u32 s63, s56, 458752\nv_mul_f32 v234, v36, v36\nM 28,164,20\ns_add_u32 s65, s68, 37873664\nbuffer_load_dwordx4 v220, s[16:19], s63 offen lds\n"
"v_mul_f32 v235, v37, v37\nv_mfma_f32_16x16x32_bf16 v[12:15], v[160:163], a[0:3], 0\nR 182,185,28672\nv_mul_f32 v236, v38, v38\nv_mul_f32 v237, v39, v39\nM 12,164,16\n"
"s_waitcnt vmcnt(16)\ns_barrier\nG 4\nv_fma_f32 v234, v234, v215, v214\nM 0,168,32\nR 160,186,16384\n"
"v_fma_f32 v235, v235, v215, v214\nv_fma_f32 v236, v236, v215, v214\nM 0,172,48\nbuffer_load_dwordx4 a[112:115], v252, s[20:23], s64 offen sc1\nbuffer_load_dwordx4 v221, s[16:19], s63 offen offset:1024 lds\nv_fma_f32 v237, v237, v215, v214\n"
"M 16,168,36\nR 162,186,20480\nv_mul_f32 v234, v36, v234\nv_mul_f32 v235, v37, v235\nM 16,172,52\nbuffer_load_dwordx4 a[116:119], v252, s[20:23], s65 offen sc1\n"
"v_mul_f32 v236, v38, v236\nv_mfma_f32_16x16x32_bf16 a[224:227], v[168:171], a[40:43], a[224:227]\nR 164,186,24576\nv_mul_f32 v237, v39, v237\nv_exp_f32 v234, v234\nv_mfma_f32_16x16x32_bf16 a[224:227], v[172:175], a[56:59], a[224:227]\n"
"s_add_u32 s66, s69, 37758976\nv_exp_f32 v235, v235\nv_exp_f32 v236, v236\nv_mfma_f32_16x16x32_bf16 a[240:243], v[168:171], a[44:47], a[240:243]\nR 166,186,28672\nv_fma_f32 v234, v234, v216, v216\n"
"v_exp_f32 v237, v237\nv_mfma_f32_16x16x32_bf16 a[240:243], v[172:175], a[60:63], a[240:243]\nG 4\nv_rcp_f32 v234, v234\nv_fma_f32 v235, v235, v216, v216\nv_mfma_f32_16x16x32_bf16 a[244:247], v[176:179], a[44:47], a[244:247]\n"
"R 168,187,16384\nv_fma_f32 v236, v236, v216, v216\nv_fma_f32 v237, v237, v216, v216\nv_mfma_f32_16x16x32_bf16 a[244:247], v[180:183], a[60:63], a[244:247]\nbuffer_load_dwordx4 a[120:123], v252, s[20:23], s66 offen sc1\nv_rcp_f32 v235, v235\n"
"v_mfma_f32_16x16x32_bf16 a[228:231], v[176:179], a[40:43], a[228:231]\nR 170,187,20480\nv_mul_f32 v234, v36, v234\nv_rcp_f32 v236, v236\nv_mfma_f32_16x16x32_bf16 a[228:231], v[180:183], a[56:59], a[228:231]\ns_add_u32 s67, s69, 37873664\n"
"v_rcp_f32 v237, v237\nv_mul_f32 v235, v37, v235\nM 20,176,36\nR 172,187,24576\nv_mul_f32 v236, v38, v236\nv_mul_f32 v237, v39, v237\n"
"M 20,180,52\nbuffer_load_dwordx4 a[124:127], v252, s[20:23], s67 offen sc1\nv_cvt_pk_bf16_f32 v34, v234, v235\nM 4,176,32\nR 174,187,28672\ns_waitcnt vmcnt(13)\n"
"v_cmp_eq_u64 s[86:87], s[46:47], v[248:249]\nv_cvt_pk_bf16_f32 v35, v236, v237\nM 4,180,48\ns_add_u32 s63, s57, 131072\nG 4\nv_mul_f32 v230, v40, v40\n"
"v_mul_f32 v231, v41, v41\nM 8,160,32\nR 176,184,32768\ns_and_b32 s62, s86, 0xf\ns_cmp_eq_u32 s62, 0xf\ns_cbranch_scc0 Lq882\n"
"Lq883:\nv_mul_f32 v232, v42, v42\nM 8,164,48\ns_add_u32 s64, s68, 37754880\nv_mul_f32 v233, v43, v43\nv_fma_f32 v230, v230, v215, v214\n"
"v_fma_f32 v231, v231, v215, v214\nM 24,160,36\nR 178,184,36864\ns_add_u32 m0, s50, 81920\nv_fma_f32 v232, v232, v215, v214\nM 24,164,52\n"
"buffer_load_dwordx4 a[128:131], v252, s[20:23], s64 offen sc1\nbuffer_load_dwordx4 v220, s[16:19], s63 offen lds\nv_fma_f32 v233, v233, v215, v214\nv_mfma_f32_16x16x32_bf16 a[232:235], v[160:163], a[40:43], a[232:235]\nR 180,184,40960\ns_add_u32 s66, s69, 37754880\n"
"v_mul_f32 v230, v40, v230\nv_mfma_f32_16x16x32_bf16 a[232:235], v[164:167], a[56:59], a[232:235]\ns_add_u32 s65, s68, 37869568\nv_exp_f32 v230, v230\nv_mul_f32 v231, v41, v231\nv_mfma_f32_16x16x32_bf16 a[248:251], v[160:163], a[44:47], a[248:251]\n"
"R 182,184,45056\nv_mul_f32 v232, v42, v232\nv_mul_f32 v233, v43, v233\nv_mfma_f32_16x16x32_bf16 a[248:251], v[164:167], a[60:63], a[248:251]\nbuffer_load_dwordx4 v221, s[16:19], s63 offen offset:1024 lds\nG 4\n"
"v_exp_f32 v231, v231\nv_mfma_f32_16x16x32_bf16 a[252:255], v[168:171], a[44:47], a[252:255]\nR 160,185,32768\ns_add_u32 m0, s50, 90112\nv_exp_f32 v232, v232\nv_mfma_f32_16x16x32_bf16 a[252:255], v[172:175], a[60:63], a[252:255]\n"
"buffer_load_dwordx4 a[132:135], v252, s[20:23], s65 offen sc1\nv_exp_f32 v233, v233\nv_mfma_f32_16x16x32_bf16 a[236:239], v[168:171], a[40:43], a[236:239]\nR 162,185,36864\ns_add_u32 s63, s57, 655360\nv_fma_f32 v230, v230, v216, v216\n"
"v_mfma_f32_16x16x32_bf16 a[236:239], v[172:175], a[56:59], a[236:239]\nbuffer_load_dwordx4 a[136:139], v252, s[20:23], s66 offen sc1\nbuffer_load_dwordx4 v220, s[16:19], s63 offen lds\nv_fma_f32 v231, v231, v216, v216\nM 28,168,36\nR 164,185,40960\n"
"s_add_u32 s64, s68, 37762048\nv_fma_f32 v232, v232, v216, v216\nM 28,172,52\ns_add_u32 s67, s69, 37869568\nv_rcp_f32 v230, v230\nv_fma_f32 v233, v233, v216, v216\n"
"M 12,168,32\nR 166,185,45056\nv_mul_f32 v230, v40, v230\nv_rcp_f32 v231, v231\nM 12,172,48\nbuffer_load_dwordx4 v221, s[16:19], s63 offen offset:1024 lds\n"
"G 4\nv_rcp_f32 v232, v232\nM 96,176,64\nR 168,186,32768\ns_add_u32 m0, s50, 98304\nv_rcp_f32 v233, v233\n"
"M 96,180,80\nbuffer_load_dwordx4 a[140:143], v252, s[20:23], s67 offen sc1\nv_mul_f32 v231, v41, v231\nM 112,176,68\nR 170,186,36864\ns_add_u32 s63, s57, 196608\n"
"v_mul_f32 v232, v42, v232\nM 112,180,84\nbuffer_load_dwordx4 a[144:147], v252, s[20:23], s64 offen sc1\nbuffer_load_dwordx4 v220, s[16:19], s63 offen lds\nv_mul_f32 v233, v43, v233\nM 128,176,72\n"
"R 172,186,40960\ns_add_u32 s66, s69, 37762048\nv_cvt_pk_bf16_f32 v40, v230, v231\nM 128,180,88\ns_add_u32 s65, s68, 37876736\nv_cvt_pk_bf16_f32 v41, v232, v233\n"
"v_mul_f32 v234, v44, v44\nv_mul_f32 v235, v45, v45\nM 144,176,76\nR 174,186,45056\nv_mul_f32 v236, v46, v46\nv_mul_f32 v237, v47, v47\n"
"M 144,180,92\nbuffer_load_dwordx4 v221, s[16:19], s63 offen offset:1024 lds\nG 4\nv_fma_f32 v234, v234, v215, v214\nM 148,160,76\nR 176,187,32768\n"
"s_add_u32 m0, s50, 106496\nv_fma_f32 v235, v235, v215, v214\nM 148,164,92\nbuffer_load_dwordx4 a[148:151], v252, s[20:23], s65 offen sc1\nv_fma_f32 v236, v236, v215, v214\nM 132,160,72\n"
"R 178,187,36864\ns_add_u32 s63, s57, 720896\nv_fma_f32 v237, v237, v215, v214\nM 132,164,88\nbuffer_load_dwordx4 a[152:155], v252, s[20:23], s66 offen sc1\nbuffer_load_dwordx4 v220, s[16:19], s63 offen lds\n"
"v_mul_f32 v234, v44, v234\nM 116,160,68\nR 180,187,40960\nv_mul_f32 v235, v45, v235\nv_mul_f32 v236, v46, v236\nM 116,164,84\n"
"s_add_u32 s67, s69, 37876736\nv_exp_f32 v234, v234\nv_mul_f32 v237, v47, v237\nM 100,160,64\nR 182,187,45056\nv_fma_f32 v234, v234, v216, v216\n"
"v_exp_f32 v235, v235\nM 100,164,80\nbuffer_load_dwordx4 v221, s[16:19], s63 offen offset:1024 lds\nG 4\nv_exp_f32 v236, v236\nM 104,168,64\n"
"R 160,184,49152\ns_waitcnt vmcnt(18)\ns_barrier\nv_exp_f32 v237, v237\nM 104,172,80\nbuffer_load_dwordx4 a[156:159], v252, s[20:23], s67 offen sc1\n"
"v_fma_f32 v235, v235, v216, v216\nM 120,168,68\nR 162,184,53248\nv_fma_f32 v236, v236, v216, v216\nv_fma_f32 v237, v237, v216, v216\nM 120,172,84\n"
"v_rcp_f32 v234, v234\nv_rcp_f32 v235, v235\nM 136,168,72\nR 164,184,57344\nv_mul_f32 v234, v44, v234\nv_rcp_f32 v236, v236\n"
"M 136,172,88\nbuffer_store_dwordx4 v[32:35], v252, s[20:23], s84 offen offset:0 sc1\nv_rcp_f32 v237, v237\nM 152,168,76\nR 166,184,61440\nv_mul_f32 v235, v45, v235\n"
"v_mul_f32 v236, v46, v236\nM 152,172,92\nG 4\nv_mul_f32 v237, v47, v237\nv_cvt_pk_bf16_f32 v42, v234, v235\nv_cvt_pk_bf16_f32 v43, v236, v237\n"
"M 156,176,76\nR 168,185,49152\nv_mul_f32 v230, v48, v48\nv_mul_f32 v231, v49, v49\nM 156,180,92\ns_add_u32 m0, s50, 114688\n"
"v_mul_f32 v232, v50, v50\nv_mul_f32 v233, v51, v51\nv_fma_f32 v230, v230, v218, v217\nM 140,176,72\nR 170,185,53248\nv_fma_f32 v231, v231, v218, v217\n"
"v_fma_f32 v232, v232, v218, v217\nM 140,180,88\ns_add_u32 s63, s57, 393216\nv_fma_f32 v233, v233, v218, v217\nv_mul_f32 v230, v48, v230\nv_mul_f32 v231, v49, v231\n"
"M 124,176,68\nR 172,185,57344\nv_mul_f32 v232, v50, v232\nv_mul_f32 v233, v51, v233\nM 124,180,84\nbuffer_load_dwordx4 v220, s[16:19], s63 offen lds\n"
"v_exp_f32 v230, v230\nM 108,176,64\nR 174,185,61440\nv_fma_f32 v230, v230, v219, v219\nv_exp_f32 v231, v231\nM 108,180,80\n"
"G 4\nv_exp_f32 v232, v232\nv_exp_f32 v233, v233\nM 96,160,96\nR 176,186,49152\nv_fma_f32 v231, v231, v219, v219\n"
"v_fma_f32 v232, v232, v219, v219\nM 96,164,112\nbuffer_load_dwordx4 v221, s[16:19], s63 offen offset:1024 lds\nv_fma_f32 v233, v233, v219, v219\nM 112,160,100\nR 178,186,53248\n"
"s_add_u32 s63, s57, 458752\nv_rcp_f32 v230, v230\ns_waitcnt vmcnt(21)\nM 112,164,116\ns_add_u32 m0, s50, 0\nv_rcp_f32 v231, v231\n"
"v_rcp_f32 v232, v232\nM 128,160,104\nR 180,186,57344\nv_mul_f32 v230, v48, v230\nv_rcp_f32 v233, v233\ns_waitcnt vmcnt(20)\n"
"M 128,164,120\nbuffer_load_dwordx4 v220, s[16:19], s63 offen lds\nv_mul_f32 v231, v49, v231\nM 144,160,108\nR 182,186,61440\nv_mul_f32 v232, v50, v232\n"
"v_mul_f32 v233, v51, v233\ns_waitcnt vmcnt(20)\nM 144,164,124\nG 4\nv_cvt_pk_bf16_f32 v48, v230, v231\nv_cvt_pk_bf16_f32 v49, v232, v233\n"
"v_mul_f32 v234, v52, v52\nM 148,168,108\nR 160,187,49152\nv_mul_f32 v235, v53, v53\nv_mul_f32 v236, v54, v54\nM 148,172,124\n"
"v_mul_f32 v237, v55, v55\nv_fma_f32 v234, v234, v218, v217\nv_fma_f32 v235, v235, v218, v217\nM 132,168,104\nR 162,187,53248\nv_fma_f32 v236, v236, v218, v217\n"
"v_fma_f32 v237, v237, v218, v217\nM 132,172,120\nbuffer_load_dwordx4 v221, s[16:19], s63 offen offset:1024 lds\nv_mul_f32 v234, v52, v234\nM 116,168,100\nR 164,187,57344\n"
"s_add_u32 s63, s58, 0\nv_mul_f32 v235, v53, v235\nM 116,172,116\ns_add_u32 m0, s50, 8192\nv_exp_f32 v234, v234\nv_mul_f32 v236, v54, v236\n"
"M 100,168,96\nR 166,187,61440\nv_mul_f32 v237, v55, v237\nv_exp_f32 v235, v235\nM 100,172,112\nG 4\n"
"v_exp_f32 v236, v236\nv_exp_f32 v237, v237\nM 104,176,96\nR 168,188,0\nv_fma_f32 v234, v234, v219, v219\nv_fma_f32 v235, v235, v219, v219\n"
"M 104,180,112\nbuffer_load_dwordx4 v220, s[16:19], s63 offen lds\nv_fma_f32 v236, v236, v219, v219\nM 120,176,100\nR 170,188,4096\nv_fma_f32 v237, v237, v219, v219\n"
"v_rcp_f32 v234, v234\nM 120,180,116\nv_rcp_f32 v235, v235\nv_rcp_f32 v236, v236\nM 136,176,104\nR 172,188,8192\n"
"v_mul_f32 v234, v52, v234\nv_rcp_f32 v237, v237\nM 136,180,120\nbuffer_load_dwordx4 v221, s[16:19], s63 offen offset:1024 lds\nv_mul_f32 v235, v53, v235\nM 152,176,108\n"
"R 174,188,12288\ns_add_u32 m0, s50, 16384\nv_mul_f32 v236, v54, v236\nM 152,180,124\nG 4\nv_mul_f32 v237, v55, v237\n"
"v_cvt_pk_bf16_f32 v50, v234, v235\nv_cvt_pk_bf16_f32 v51, v236, v237\nM 156,160,108\nR 176,189,0\ns_add_u32 s63, s58, 524288\nv_mul_f32 v230, v56, v56\n"
"M 156,164,124\nbuffer_load_dwordx4 v220, s[16:19], s63 offen lds\nv_mul_f32 v231, v57, v57\nM 140,160,104\nR 178,189,4096\nv_mul_f32 v232, v58, v58\n"
"v_mul_f32 v233, v59, v59\nM 140,164,120\nv_fma_f32 v230, v230, v218, v217\nv_fma_f32 v231, v231, v218, v217\nv_fma_f32 v232, v232, v218, v217\nM 124,160,100\n"
"R 180,189,8192\nv_fma_f32 v233, v233, v218, v217\nv_mul_f32 v230, v56, v230\nM 124,164,116\nbuffer_load_dwordx4 v221, s[16:19], s63 offen offset:1024 lds\nv_mul_f32 v231, v57, v231\n"
"M 108,160,96\nR 182,189,12288\ns_add_u32 s63, s58, 65536\nv_mul_f32 v232, v58, v232\nM 108,164,112\ns_waitcnt vmcnt(10)\n"
"s_barrier\nG 4\nM 96,168,128\nR 160,190,0\nv_mul_f32 v233, v59, v233\nM 96,172,144\n"
"s_add_u32 m0, s50, 24576\nv_exp_f32 v230, v230\nM 112,168,132\nR 162,190,4096\nv_fma_f32 v230, v230, v219, v219\nM 112,172,148\n"
"buffer_load_dwordx4 v220, s[16:19], s63 offen lds\nM 128,168,136\nR 164,190,8192\nv_mul_f32 v234, v60, v60\nM 128,172,152\nv_exp_f32 v231, v231\n"
"M 144,168,140\nR 166,190,12288\nv_fma_f32 v231, v231, v219, v219\ns_waitcnt vmcnt(10)\nM 144,172,156\nG 4\n"
"v_exp_f32 v232, v232\nM 148,176,140\nR 168,191,0\nv_fma_f32 v232, v232, v219, v219\nM 148,180,156\nbuffer_load_dwordx4 v221, s[16:19], s63 offen offset:1024 lds\n"
"M 132,176,136\nR 170,191,4096\nv_mul_f32 v235, v61, v61\nM 132,180,152\ns_add_u32 m0, s50, 32768\nv_exp_f32 v233, v233\n"
"M 116,176,132\nR 172,191,8192\nv_fma_f32 v233, v233, v219, v219\nM 116,180,148\ns_add_u32 s63, s58, 589824\nv_rcp_f32 v230, v230\n"
"M 100,176,128\nR 174,191,12288\nv_mul_f32 v230, v56, v230\nM 100,180,144\nG 4\nv_rcp_f32 v231, v231\n"
"M 104,160,128\nR 176,188,16384\nv_mul_f32 v231, v57, v231\nM 104,164,144\nbuffer_load_dwordx4 v220, s[16:19], s63 offen lds\nM 120,160,132\n"
"R 178,188,20480\nv_cvt_pk_bf16_f32 v56, v230, v231\nM 120,164,148\nv_rcp_f32 v232, v232\nM 136,160,136\nR 180,188,24576\n"
"v_mul_f32 v232, v58, v232\nM 136,164,152\nbuffer_store_dwordx4 v[40:43], v252, s[20:23], s84 offen offset:1024 sc1\nM 152,160,140\nR 182,188,28672\nv_mul_f32 v236, v62, v62\n"
"M 152,164,156\nG 4\nv_rcp_f32 v233, v233\nM 156,168,140\nR 160,189,16384\nv_mul_f32 v233, v59, v233\n"
"M 156,172,156\nbuffer_load_dwordx4 v221, s[16:19], s63 offen offset:1024 lds\nM 140,168,136\nR 162,189,20480\nv_cvt_pk_bf16_f32 v57, v232, v233\nM 140,172,152\n"
"s_add_u32 m0, s50, 40960\nv_mul_f32 v237, v63, v63\nv_fma_f32 v234, v234, v218, v217\nM 124,168,132\nR 164,189,24576\nv_fma_f32 v235, v235, v218, v217\n"
"M 124,172,148\ns_add_u32 s63, s58, 131072\nv_fma_f32 v236, v236, v218, v217\nv_fma_f32 v237, v237, v218, v217\nM 108,168,128\nR 166,189,28672\n"
"v_mul_f32 v234, v60, v234\nM 108,172,144\nG 4\nv_exp_f32 v234, v234\nM 0,176,64\nR 168,190,16384\n"
"v_mul_f32 v235, v61, v235\nM 0,180,80\nbuffer_load_dwordx4 v220, s[16:19], s63 offen lds\nM 16,176,68\nR 170,190,20480\nv_mul_f32 v236, v62, v236\n"
"M 16,180,84\nv_exp_f32 v235, v235\nv_mfma_f32_16x16x32_bf16 a[224:227], v[176:179], a[72:75], a[224:227]\nR 172,190,24576\nv_mul_f32 v237, v63, v237\nv_mfma_f32_16x16x32_bf16 a[224:227], v[180:183], a[88:91], a[224:227]\n"
"buffer_store_dwordx4 v[48:51], v252, s[20:23], s85 offen offset:0 sc1\nv_mfma_f32_16x16x32_bf16 a[240:243], v[176:179], a[76:79], a[240:243]\nR 174,190,28672\nv_fma_f32 v234, v234, v219, v219\nv_mfma_f32_16x16x32_bf16 a[240:243], v[180:183], a[92:95], a[240:243]\nG 4\n"
"v_exp_f32 v236, v236\nv_mfma_f32_16x16x32_bf16 a[244:247], v[160:163], a[76:79], a[244:247]\nR 176,191,16384\nv_fma_f32 v235, v235, v219, v219\nv_mfma_f32_16x16x32_bf16 a[244:247], v[164:167], a[92:95], a[244:247]\nbuffer_load_dwordx4 v221, s[16:19], s63 offen offset:1024 lds\n"
"v_mfma_f32_16x16x32_bf16 a[228:231], v[160:163], a[72:75], a[228:231]\nR 178,191,20480\nv_fma_f32 v236, v236, v219, v219\nv_mfma_f32_16x16x32_bf16 a[228:231], v[164:167], a[88:91], a[228:231]\ns_add_u32 m0, s50, 49152\nv_exp_f32 v237, v237\n"
"M 20,160,68\nR 180,191,24576\nv_fma_f32 v237, v237, v219, v219\nM 20,164,84\ns_add_u32 s63, s58, 655360\nv_rcp_f32 v234, v234\n"
"M 4,160,64\nR 182,191,28672\nv_mul_f32 v234, v60, v234\nM 4,164,80\nG 4\nv_rcp_f32 v235, v235\n"
"M 8,168,64\nR 160,188,32768\nv_mul_f32 v235, v61, v235\nM 8,172,80\nbuffer_load_dwordx4 v220, s[16:19], s63 offen lds\nM 24,168,68\n"
"R 162,188,36864\nv_cvt_pk_bf16_f32 v58, v234, v235\nM 24,172,84\nv_rcp_f32 v236, v236\nv_mfma_f32_16x16x32_bf16 a[232:235], v[168:171], a[72:75], a[232:235]\nR 164,188,40960\n"
"v_mul_f32 v236, v62, v236\nv_mfma_f32_16x16x32_bf16 a[232:235], v[172:175], a[88:91], a[232:235]\nbuffer_load_dwordx4 v221, s[16:19], s63 offen offset:1024 lds\nv_mfma_f32_16x16x32_bf16 a[248:251], v[168:171], a[76:79], a[248:251]\nR 166,188,45056\ns_add_u32 m0, s50, 57344\n"
"v_mfma_f32_16x16x32_bf16 a[248:251], v[172:175], a[92:95], a[248:251]\nG 4\nv_rcp_f32 v237, v237\nv_mfma_f32_16x16x32_bf16 a[252:255], v[176:179], a[76:79], a[252:255]\nR 168,189,32768\ns_add_u32 s63, s58, 196608\n"
"v_mfma_f32_16x16x32_bf16 a[252:255], v[180:183], a[92:95], a[252:255]\nbuffer_load_dwordx4 v220, s[16:19], s63 offen lds\nv_mfma_f32_16x16x32_bf16 a[236:239], v[176:179], a[72:75], a[236:239]\nR 170,189,36864\nv_mul_f32 v237, v63, v237\nv_mfma_f32_16x16x32_bf16 a[236:239], v[180:183], a[88:91], a[236:239]\n"
"v_cvt_pk_bf16_f32 v59, v236, v237\nbuffer_store_dwordx4 v[56:59], v252, s[20:23], s85 offen offset:1024 sc1\nM 28,176,68\nR 172,189,40960\nM 28,180,84\nbuffer_load_dwordx4 v221, s[16:19], s63 offen offset:1024 lds\n"
"M 12,176,64\nR 174,189,45056\nM 12,180,80\ns_waitcnt vmcnt(13)\ns_barrier\nG 4\n"
"M 0,160,96\nR 176,190,32768\nM 0,164,112\nM 16,160,100\nR 178,190,36864\nM 16,164,116\n"
"v_mfma_f32_16x16x32_bf16 a[224:227], v[160:163], a[104:107], a[224:227]\nR 180,190,40960\nv_mfma_f32_16x16x32_bf16 a[224:227], v[164:167], a[120:123], a[224:227]\nv_mfma_f32_16x16x32_bf16 a[240:243], v[160:163], a[108:111], a[240:243]\nR 182,190,45056\nv_mfma_f32_16x16x32_bf16 a[240:243], v[164:167], a[124:127], a[240:243]\n"
"G 4\nv_mfma_f32_16x16x32_bf16 a[244:247], v[168:171], a[108:111], a[244:247]\nR 160,191,32768\nv_mfma_f32_16x16x32_bf16 a[244:247], v[172:175], a[124:127], a[244:247]\nv_mfma_f32_16x16x32_bf16 a[228:231], v[168:171], a[104:107], a[228:231]\nR 162,191,36864\n"
"v_mfma_f32_16x16x32_bf16 a[228:231], v[172:175], a[120:123], a[228:231]\nM 20,168,100\nR 164,191,40960\nM 20,172,116\nM 4,168,96\nR 166,191,45056\n"
"M 4,172,112\nG 4\nM 8,176,96\nR 168,188,49152\nM 8,180,112\nM 24,176,100\n"
"R 170,188,53248\nM 24,180,116\nv_mfma_f32_16x16x32_bf16 a[232:235], v[176:179], a[104:107], a[232:235]\nR 172,184,0\nv_mfma_f32_16x16x32_bf16 a[232:235], v[180:183], a[120:123], a[232:235]\nv_mfma_f32_16x16x32_bf16 a[248:251], v[176:179], a[108:111], a[248:251]\n"
"R 174,184,4096\nv_mfma_f32_16x16x32_bf16 a[248:251], v[180:183], a[124:127], a[248:251]\nG 4\nv_mfma_f32_16x16x32_bf16 a[252:255], v[160:163], a[108:111], a[252:255]\nR 176,189,49152\nv_mfma_f32_16x16x32_bf16 a[252:255], v[164:167], a[124:127], a[252:255]\n"
"v_mfma_f32_16x16x32_bf16 a[236:239], v[160:163], a[104:107], a[236:239]\nR 178,189,53248\nv_mfma_f32_16x16x32_bf16 a[236:239], v[164:167], a[120:123], a[236:239]\nM 28,160,100\nR 180,185,0\nM 28,164,116\n"
"M 12,160,96\nR 182,185,4096\nM 12,164,112\nG 4\nM 0,168,128\nR 160,190,49152\n"
"M 0,172,144\nM 16,168,132\nR 162,190,53248\nM 16,172,148\nv_mfma_f32_16x16x32_bf16 a[224:227], v[168:171], a[136:139], a[224:227]\nR 164,186,0\n"
"v_mfma_f32_16x16x32_bf16 a[224:227], v[172:175], a[152:155], a[224:227]\nv_mfma_f32_16x16x32_bf16 a[240:243], v[168:171], a[140:143], a[240:243]\nR 166,186,4096\nv_mfma_f32_16x16x32_bf16 a[240:243], v[172:175], a[156:159], a[240:243]\nG 4\nv_mfma_f32_16x16x32_bf16 a[244:247], v[176:179], a[140:143], a[244:247]\n"
"R 168,191,49152\nv_mfma_f32_16x16x32_bf16 a[244:247], v[180:183], a[156:159], a[244:247]\nv_mfma_f32_16x16x32_bf16 a[228:231], v[176:179], a[136:139], a[228:231]\nR 170,191,53248\nv_mov_b32 v226, s46\nv_mfma_f32_16x16x32_bf16 a[228:231], v[180:183], a[152:155], a[228:231]\n"
"s_add_u32 m0, s50, 65536\nv_mov_b32 v227, s47\nM 20,176,132\nR 172,187,0\ns_add_u32 s61, s81, 845840\ns_lshl_b32 s62, s44, 5\n"
"s_add_u32 s61, s61, s62\ns_waitcnt vmcnt(0)\ns_mov_b64 exec, 1\nbuffer_atomic_swap_x2 v[226:227], off, s[20:23], s61 sc1\ns_mov_b64 exec, -1\nM 20,180,148\n"
"s_add_u32 s63, s58, 720896\nM 4,176,128\nR 174,187,4096\nM 4,180,144\nG 4\nM 8,160,128\n"
"R 176,184,8192\nM 8,164,144\nbuffer_load_dwordx4 v220, s[16:19], s63 offen lds\nM 24,160,132\nR 178,184,12288\nM 24,164,148\n"
"v_mfma_f32_16x16x32_bf16 a[232:235], v[160:163], a[136:139], a[232:235]\nR 180,184,16384\nv_mfma_f32_16x16x32_bf16 a[232:235], v[164:167], a[152:155], a[232:235]\nbuffer_load_dwordx4 v221, s[16:19], s63 offen offset:1024 lds\nv_mfma_f32_16x16x32_bf16 a[248:251], v[160:163], a[140:143], a[248:251]\nR 182,184,20480\n"
"s_add_u32 m0, s50, 73728\nv_mfma_f32_16x16x32_bf16 a[248:251], v[164:167], a[156:159], a[248:251]\nG 4\nv_mfma_f32_16x16x32_bf16 a[252:255], v[168:171], a[140:143], a[252:255]\nR 160,185,8192\ns_add_u32 s63, s58, 393216\n"
"v_mfma_f32_16x16x32_bf16 a[252:255], v[172:175], a[156:159], a[252:255]\nbuffer_load_dwordx4 v220, s[16:19], s63 offen lds\nv_mfma_f32_16x16x32_bf16 a[236:239], v[168:171], a[136:139], a[236:239]\nR 162,185,12288\nv_mfma_f32_16x16x32_bf16 a[236:239], v[172:175], a[152:155], a[236:239]\nM 28,168,132\n"
"R 164,185,16384\nM 28,172,148\nbuffer_load_dwordx4 v221, s[16:19], s63 offen offset:1024 lds\nM 12,168,128\nR 166,185,20480\ns_add_u32 s63, s58, 458752\n"
"M 12,172,144\ns_barrier\nG 4\nv_mfma_f32_16x16x32_bf16 v[32:35], v[176:179], a[0:3], 0\nR 168,186,8192\nM 32,180,16\n"
"s_add_u32 m0, s50, 81920\nv_mfma_f32_16x16x32_bf16 v[48:51], v[176:179], a[4:7], 0\nR 170,186,12288\nM 48,180,20\nbuffer_load_dwordx4 v220, s[16:19], s63 offen lds\nv_mfma_f32_16x16x32_bf16 v[64:67], v[176:179], a[8:11], 0\n"
"R 172,186,16384\nM 64,180,24\nv_mfma_f32_16x16x32_bf16 v[80:83], v[176:179], a[12:15], 0\nR 174,186,20480\nM 80,180,28\nG 4\n"
"v_mfma_f32_16x16x32_bf16 v[84:87], v[160:163], a[12:15], 0\nR 176,187,8192\nM 84,164,28\nbuffer_load_dwordx4 v221, s[16:19], s63 offen offset:1024 lds\nv_mfma_f32_16x16x32_bf16 v[68:71], v[160:163], a[8:11], 0\nR 178,187,12288\n"
"M 68,164,24\nv_mfma_f32_16x16x32_bf16 v[52:55], v[160:163], a[4:7], 0\nR 180,187,16384\nM 52,164,20\ns_add_u32 m0, s50, 90112\nv_mfma_f32_16x16x32_bf16 v[36:39], v[160:163], a[0:3], 0\n"
"R 182,187,20480\nv_mov_b32 v248, 0\nM 36,164,16\nG 4\nv_mov_b32 v249, 0\nv_mfma_f32_16x16x32_bf16 v[40:43], v[168:171], a[0:3], 0\n"
"R 160,184,24576\ns_add_u32 s61, s82, 845840\ns_mov_b64 exec, 0xf\nbuffer_atomic_or_x2 v[248:249], v250, s[20:23], s61 offen sc0 sc1\ns_mov_b64 exec, -1\nM 40,172,16\n"
"s_add_u32 s63, s56, 262144\nv_mfma_f32_16x16x32_bf16 v[56:59], v[168:171], a[4:7], 0\nR 162,184,28672\nM 56,172,20\nbuffer_load_dwordx4 v220, s[16:19], s63 offen lds\nv_mfma_f32_16x16x32_bf16 v[72:75], v[168:171], a[8:11], 0\n"
"R 164,184,32768\nM 72,172,24\nv_mfma_f32_16x16x32_bf16 v[88:91], v[168:171], a[12:15], 0\nR 166,184,36864\nM 88,172,28\nG 4\n"
"v_mfma_f32_16x16x32_bf16 v[92:95], v[176:179], a[12:15], 0\nR 168,185,24576\nM 92,180,28\nbuffer_load_dwordx4 v221, s[16:19], s63 offen offset:1024 lds\nv_mfma_f32_16x16x32_bf16 v[76:79], v[176:179], a[8:11], 0\nR 170,185,28672\n"
"s_add_u32 s63, s56, 786432\nM 76,180,24\ns_add_u32 m0, s50, 98304\nv_mfma_f32_16x16x32_bf16 v[60:63], v[176:179], a[4:7], 0\nR 172,185,32768\nM 60,180,20\n"
"buffer_load_dwordx4 v220, s[16:19], s63 offen lds\nv_mfma_f32_16x16x32_bf16 v[44:47], v[176:179], a[0:3], 0\nR 174,185,36864\nM 44,180,16\nG 4\nM 32,160,32\n"
"R 176,186,24576\nM 32,164,48\nbuffer_load_dwordx4 v221, s[16:19], s63 offen offset:1024 lds\nM 48,160,36\nR 178,186,28672\ns_add_u32 s63, s56, 327680\n"
"M 48,164,52\ns_add_u32 m0, s50, 106496\nM 64,160,40\nR 180,186,32768\nM 64,164,56\nbuffer_load_dwordx4 v220, s[16:19], s63 offen lds\n"
"M 80,160,44\nR 182,186,36864\nM 80,164,60\nG 4\nM 84,168,44\nR 160,187,24576\n"
"M 84,172,60\nbuffer_load_dwordx4 v221, s[16:19], s63 offen offset:1024 lds\nM 68,168,40\nR 162,187,28672\ns_add_u32 s63, s56, 851968\nM 68,172,56\n"
"s_add_u32 m0, s50, 114688\nM 52,168,36\nR 164,187,32768\nM 52,172,52\nbuffer_load_dwordx4 v220, s[16:19], s63 offen lds\nM 36,168,32\n"
"R 166,187,36864\nM 36,172,48\nG 4\nM 40,176,32\nR 168,184,40960\nM 40,180,48\n"
"s_add_u32 s64, s68, 37752832\nM 56,176,36\nR 170,184,45056\ns_waitcnt vmcnt(7)\nv_cmp_eq_u64 s[86:87], s[46:47], v[248:249]\nM 56,180,52\n"
"buffer_load_dwordx4 v221, s[16:19], s63 offen offset:1024 lds\nM 72,176,40\nR 172,184,49152\ns_and_b32 s62, s86, 0xf\ns_cmp_eq_u32 s62, 0xf\ns_cbranch_scc0 Lq901\n"
"Lq902:\nM 72,180,56\ns_add_u32 s65, s68, 37867520\nM 88,176,44\nR 174,184,53248\ns_barrier\n"
"M 88,180,60\nG 4\nM 92,160,44\nR 176,185,40960\nM 92,164,60\nbuffer_load_dwordx4 a[160:163], v252, s[20:23], s64 offen sc1\n"
"M 76,160,40\nR 178,185,45056\nM 76,164,56\ns_add_u32 s66, s69, 37752832\nM 60,160,36\nR 180,185,49152\n"
"M 60,164,52\nbuffer_load_dwordx4 a[164:167], v252, s[20:23], s65 offen sc1\nM 44,160,32\nR 182,185,53248\nM 44,164,48\nG 4\n"
"M 32,168,64\nR 160,186,40960\nM 32,172,80\ns_add_u32 s67, s69, 37867520\nM 48,168,68\nR 162,186,45056\n"
"M 48,172,84\nbuffer_load_dwordx4 a[168:171], v252, s[20:23], s66 offen sc1\nM 64,168,72\nR 164,186,49152\nM 64,172,88\nM 80,168,76\n"
"R 166,186,53248\nM 80,172,92\nG 4\nM 84,176,76\nR 168,187,40960\nM 84,180,92\n"
"buffer_load_dwordx4 a[172:175], v252, s[20:23], s67 offen sc1\nM 68,176,72\nR 170,187,45056\nM 68,180,88\ns_add_u32 s64, s68, 37760000\nM 52,176,68\n"
"R 172,187,49152\nM 52,180,84\ns_add_u32 s65, s68, 37874688\nM 36,176,64\nR 174,187,53248\ns_add_u32 m0, s50, 0\n"
"M 36,180,80\nG 4\nM 40,160,64\nR 176,184,57344\ns_add_u32 s63, s57, 262144\nM 40,164,80\n"
"buffer_load_dwordx4 a[176:179], v252, s[20:23], s64 offen sc1\nM 56,160,68\nR 178,184,61440\nM 56,164,84\ns_add_u32 s66, s69, 37760000\nbuffer_load_dwordx4 v220, s[16:19], s63 offen lds\n"
"M 72,160,72\nR 180,188,0\nM 72,164,88\nbuffer_load_dwordx4 a[180:183], v252, s[20:23], s65 offen sc1\nM 88,160,76\nR 182,188,4096\n"
"M 88,164,92\nbuffer_load_dwordx4 v221, s[16:19], s63 offen offset:1024 lds\nG 4\nM 92,168,76\nR 160,185,57344\ns_add_u32 m0, s50, 8192\n"
"M 92,172,92\ns_add_u32 s67, s69, 37874688\nM 76,168,72\nR 162,185,61440\ns_add_u32 s63, s57, 786432\nM 76,172,88\n"
"buffer_load_dwordx4 a[184:187], v252, s[20:23], s66 offen sc1\nbuffer_load_dwordx4 v220, s[16:19], s63 offen lds\nM 60,168,68\nR 164,189,0\nM 60,172,84\nbuffer_load_dwordx4 v221, s[16:19], s63 offen offset:1024 lds\n"
"M 44,168,64\nR 166,189,4096\ns_add_u32 m0, s50, 16384\nM 44,172,80\nG 4\nM 32,176,96\n"
"R 168,186,57344\ns_add_u32 s63, s57, 327680\nM 32,180,112\nbuffer_load_dwordx4 a[188:191], v252, s[20:23], s67 offen sc1\nM 48,176,100\nR 170,186,61440\n"
"M 48,180,116\ns_add_u32 s64, s68, 37753856\nbuffer_load_dwordx4 v220, s[16:19], s63 offen lds\nM 64,176,104\nR 172,190,0\nM 64,180,120\n"
"buffer_load_dwordx4 v221, s[16:19], s63 offen offset:1024 lds\nM 80,176,108\nR 174,190,4096\ns_add_u32 m0, s50, 24576\nM 80,180,124\nG 4\n"
"M 84,160,108\nR 176,187,57344\ns_waitcnt vmcnt(18)\ns_barrier\nM 84,164,124\nbuffer_load_dwordx4 a[192:195], v252, s[20:23], s64 offen sc1\n"
"M 68,160,104\nR 178,187,61440\ns_add_u32 s63, s57, 851968\nM 68,164,120\ns_add_u32 s65, s68, 37868544\nM 52,160,100\n"
"R 180,191,0\nbuffer_load_dwordx4 v220, s[16:19], s63 offen lds\nM 52,164,116\ns_add_u32 s66, s69, 37753856\nM 36,160,96\nR 182,191,4096\n"
"M 36,164,112\nG 4\nM 40,168,96\nR 160,188,8192\nbuffer_load_dwordx4 v221, s[16:19], s63 offen offset:1024 lds\nM 40,172,112\n"
"buffer_load_dwordx4 a[196:199], v252, s[20:23], s65 offen sc1\nM 56,168,100\nR 162,188,12288\nM 56,172,116\ns_add_u32 s67, s69, 37868544\nM 72,168,104\n"
"R 164,188,16384\nM 72,172,120\nbuffer_load_dwordx4 a[200:203], v252, s[20:23], s66 offen sc1\nM 88,168,108\nR 166,188,20480\nM 88,172,124\n"
"G 4\nM 92,176,108\nR 168,189,8192\nM 92,180,124\ns_add_u32 s64, s68, 37761024\nM 76,176,104\n"
"R 170,189,12288\nM 76,180,120\nbuffer_load_dwordx4 a[204:207], v252, s[20:23], s67 offen sc1\nM 60,176,100\nR 172,189,16384\nM 60,180,116\n"
"M 44,176,96\nR 174,189,20480\nM 44,180,112\nG 4\nM 32,160,128\nR 176,190,8192\n"
"M 32,164,144\nbuffer_load_dwordx4 a[208:211], v252, s[20:23], s64 offen sc1\nM 48,160,132\nR 178,190,12288\nM 48,164,148\ns_add_u32 s65, s68, 37875712\n"
"M 64,160,136\nR 180,190,16384\nM 64,164,152\ns_add_u32 s66, s69, 37761024\nM 80,160,140\nR 182,190,20480\n"
"M 80,164,156\nG 4\nM 84,168,140\nR 160,191,8192\nM 84,172,156\nbuffer_load_dwordx4 a[212:215], v252, s[20:23], s65 offen sc1\n"
"M 68,168,136\nR 162,191,12288\nM 68,172,152\ns_add_u32 s67, s69, 37875712\nM 52,168,132\nR 164,191,16384\n"
"M 52,172,148\nbuffer_load_dwordx4 a[216:219], v252, s[20:23], s66 offen sc1\nM 36,168,128\nR 166,191,20480\nM 36,172,144\nG 4\n"
"M 40,176,128\nR 168,188,24576\nM 40,180,144\nM 56,176,132\nR 170,188,28672\nM 56,180,148\n"
"buffer_load_dwordx4 a[220:223], v252, s[20:23], s67 offen sc1\nM 72,176,136\nR 172,188,32768\ns_add_u32 s63, s58, 262144\nM 72,180,152\ns_add_u32 m0, s50, 32768\n"
"M 88,176,140\nR 174,188,36864\nM 88,180,156\nG 4\nM 92,160,140\nR 176,189,24576\n"
"M 92,164,156\nbuffer_load_dwordx4 v220, s[16:19], s63 offen lds\nM 76,160,136\nR 178,189,28672\nM 76,164,152\nM 60,160,132\n"
"R 180,189,32768\nM 60,164,148\nbuffer_load_dwordx4 v221, s[16:19], s63 offen offset:1024 lds\nM 44,160,128\nR 182,189,36864\nM 44,164,144\n"
"s_waitcnt vmcnt(9)\ns_barrier\nG 4\nM 96,168,160\nR 160,190,24576\nM 96,172,176\n"
"s_add_u32 m0, s50, 40960\nM 112,168,164\nR 162,190,28672\nM 112,172,180\ns_add_u32 s63, s58, 786432\nM 128,168,168\n"
"R 164,190,32768\nv_lshrrev_b32 v230, 8, v252\nM 128,172,184\nbuffer_load_dwordx4 v220, s[16:19], s63 offen lds\nM 144,168,172\nR 166,190,36864\n"
"v_lshlrev_b32 v230, 3, v230\nM 144,172,188\nG 4\nM 148,176,172\nR 168,191,24576\nM 148,180,188\n"
"buffer_load_dwordx2 v[232:233], v230, s[36:39], s53 offen offset:0\nM 132,176,168\nR 170,191,28672\nM 132,180,184\nbuffer_load_dwordx2 v[234:235], v230, s[36:39], s53 offen offset:32\nM 116,176,164\n"
"R 172,191,32768\nM 116,180,180\nbuffer_load_dwordx4 v221, s[16:19], s63 offen offset:1024 lds\nM 100,176,160\nR 174,191,36864\nM 100,180,176\n"
"G 4\nM 104,160,160\nR 176,188,40960\nM 104,164,176\ns_add_u32 m0, s50, 49152\nM 120,160,164\n"
"R 178,188,45056\nM 120,164,180\ns_add_u32 s63, s58, 327680\nM 136,160,168\nR 180,188,49152\nbuffer_load_dwordx2 v[236:237], v230, s[36:39], s53 offen offset:64\n"
"M 136,164,184\nbuffer_load_dwordx4 v220, s[16:19], s63 offen lds\nM 152,160,172\nR 182,188,53248\nM 152,164,188\nbuffer_load_dwordx2 v[238:239], v230, s[36:39], s53 offen offset:96\n"
"G 4\nM 156,168,172\nR 160,189,40960\nv_bfe_u32 v242, v252, 4, 4\nM 156,172,188\nv_lshrrev_b32 v243, 8, v252\n"
"v_lshlrev_b32 v243, 2, v243\nM 140,168,168\nR 162,189,45056\nv_sub_u32 v242, v242, v243\nM 140,172,184\nv_lshlrev_b32 v243, 4, v242\n"
"s_mov_b64 s[60:61], 0x3f80\nv_lshlrev_b64 v[208:209], v243, s[60:61]\nM 124,168,164\nR 164,189,49152\nM 124,172,180\nbuffer_load_dwordx4 v221, s[16:19], s63 offen offset:1024 lds\n"
"M 108,168,160\nR 166,189,53248\nM 108,172,176\nG 4\nM 96,176,192\nR 168,190,40960\n"
"s_waitcnt vmcnt(13)\nM 96,180,208\ns_add_u32 m0, s50, 57344\nM 112,176,196\nR 170,190,45056\ns_waitcnt vmcnt(12)\n"
"M 112,180,212\ns_add_u32 s63, s58, 851968\nM 128,176,200\nR 172,190,49152\ns_waitcnt vmcnt(11)\nM 128,180,216\n"
"buffer_load_dwordx4 v220, s[16:19], s63 offen lds\nM 144,176,204\nR 174,190,53248\ns_waitcnt vmcnt(11)\nM 144,180,220\nG 4\n"
"M 148,160,204\nR 176,191,40960\nM 148,164,220\nv_cmp_gt_u32 s[60:61], 4, v242\ns_nop 1\nv_cndmask_b32 v212, 0, v208, s[60:61]\n"
"v_cndmask_b32 v208, 0, v208, s[60:61]\nv_cndmask_b32 v213, 0, v209, s[60:61]\nv_cndmask_b32 v209, 0, v209, s[60:61]\nM 132,160,200\nR 178,191,45056\nM 132,164,216\n"
"M 116,160,196\nR 180,191,49152\nM 116,164,212\nbuffer_load_dwordx4 v221, s[16:19], s63 offen offset:1024 lds\nM 100,160,192\nR 182,191,53248\n"
"M 100,164,208\nG 4\nM 104,168,192\nR 160,184,0\nM 104,172,208\ns_add_u32 m0, s50, 65536\n"
"M 120,168,196\nR 162,184,4096\nM 120,172,212\ns_add_u32 s63, s59, 0\nM 136,168,200\nR 164,184,8192\n"
"M 136,172,216\nbuffer_load_dwordx4 v220, s[16:19], s63 offen lds\nM 152,168,204\nR 166,184,12288\nM 152,172,220\nG 4\n"
"M 156,176,204\nR 168,185,0\nM 156,180,220\nM 140,176,200\nR 170,185,4096\nM 140,180,216\n"
"buffer_load_dwordx4 v221, s[16:19], s63 offen offset:1024 lds\nM 124,176,196\nR 172,185,8192\nM 124,180,212\nM 108,176,192\nR 174,185,12288\n"
"M 108,180,208\nG 4\nM 0,160,160\nR 176,186,0\nM 0,164,176\ns_mov_b32 m0, s51\n"
"M 16,160,164\nR 178,186,4096\nds_write_addtid_b32 v128 offset:0\nM 16,164,180\nds_write_addtid_b32 v129 offset:256\nds_write_addtid_b32 v130 offset:512\n"
"v_mfma_f32_16x16x32_bf16 a[224:227], v[160:163], a[168:171], a[224:227]\nR 180,186,8192\nds_write_addtid_b32 v131 offset:768\nv_mfma_f32_16x16x32_bf16 a[224:227], v[164:167], a[184:187], a[224:227]\nds_write_addtid_b32 v132 offset:1024\nds_write_addtid_b32 v133 offset:1280\n"
"v_mfma_f32_16x16x32_bf16 a[240:243], v[160:163], a[172:175], a[240:243]\nR 182,186,12288\nds_write_addtid_b32 v134 offset:1536\nv_mfma_f32_16x16x32_bf16 a[240:243], v[164:167], a[188:191], a[240:243]\nds_write_addtid_b32 v135 offset:1792\nds_write_addtid_b32 v136 offset:2048\n"
"G 13\nv_mfma_f32_16x16x32_bf16 a[244:247], v[168:171], a[172:175], a[244:247]\nR 160,187,0\nds_write_addtid_b32 v137 offset:2304\nv_mfma_f32_16x16x32_bf16 a[244:247], v[172:175], a[188:191], a[244:247]\nds_write_addtid_b32 v138 offset:2560\n"
"ds_write_addtid_b32 v139 offset:2816\nv_mfma_f32_16x16x32_bf16 a[228:231], v[168:171], a[168:171], a[228:231]\nR 162,187,4096\nds_write_addtid_b32 v140 offset:3072\nv_mfma_f32_16x16x32_bf16 a[228:231], v[172:175], a[184:187], a[228:231]\nds_write_addtid_b32 v141 offset:3328\n"
"ds_write_addtid_b32 v142 offset:3584\nM 20,168,164\nR 164,187,8192\nds_write_addtid_b32 v143 offset:3840\nM 20,172,180\nds_write_addtid_b32 v144 offset:4096\n"
"ds_write_addtid_b32 v145 offset:4352\nM 4,168,160\nR 166,187,12288\nds_write_addtid_b32 v146 offset:4608\nM 4,172,176\nds_write_addtid_b32 v147 offset:4864\n"
"ds_write_addtid_b32 v148 offset:5120\nG 15\nM 8,176,160\nR 168,184,16384\nds_write_addtid_b32 v149 offset:5376\nM 8,180,176\n"
"ds_write_addtid_b32 v150 offset:5632\nds_write_addtid_b32 v151 offset:5888\nM 24,176,164\nR 170,184,20480\nds_write_addtid_b32 v152 offset:6144\nM 24,180,180\n"
"ds_write_addtid_b32 v153 offset:6400\nds_write_addtid_b32 v154 offset:6656\nv_mfma_f32_16x16x32_bf16 a[232:235], v[176:179], a[168:171], a[232:235]\nR 172,184,24576\nds_write_addtid_b32 v155 offset:6912\nv_mfma_f32_16x16x32_bf16 a[232:235], v[180:183], a[184:187], a[232:235]\n"
"ds_write_addtid_b32 v156 offset:7168\nds_write_addtid_b32 v157 offset:7424\nds_write_addtid_b32 v158 offset:7680\nds_write_addtid_b32 v159 offset:7936\nv_mfma_f32_16x16x32_bf16 a[248:251], v[176:179], a[172:175], a[248:251]\nR 174,184,28672\n"
"s_add_u32 m0, s50, 73728\nv_mfma_f32_16x16x32_bf16 a[248:251], v[180:183], a[188:191], a[248:251]\ns_add_u32 s63, s59, 524288\nG 15\nv_mfma_f32_16x16x32_bf16 a[252:255], v[160:163], a[172:175], a[252:255]\nR 176,185,16384\n"
"s_waitcnt vmcnt(8) lgkmcnt(2)\ns_barrier\nv_mfma_f32_16x16x32_bf16 a[252:255], v[164:167], a[188:191], a[252:255]\nbuffer_load_dwordx4 v220, s[16:19], s63 offen lds\nds_read2st64_b32 v[128:129], v225 offset0:0 offset1:1\nv_mfma_f32_16x16x32_bf16 a[236:239], v[160:163], a[168:171], a[236:239]\n"
"R 178,185,20480\nds_read2st64_b32 v[130:131], v225 offset0:2 offset1:3\nv_mfma_f32_16x16x32_bf16 a[236:239], v[164:167], a[184:187], a[236:239]\nds_read2st64_b32 v[132:133], v225 offset0:4 offset1:5\nds_read2st64_b32 v[134:135], v225 offset0:6 offset1:7\nM 28,160,164\n"
"R 180,185,24576\nds_read2st64_b32 v[136:137], v225 offset0:8 offset1:9\nM 28,164,180\nbuffer_load_dwordx4 v221, s[16:19], s63 offen offset:1024 lds\nds_read2st64_b32 v[138:139], v225 offset0:10 offset1:11\nM 12,160,160\n"
"R 182,185,28672\ns_add_u32 m0, s50, 81920\nds_read2st64_b32 v[140:141], v225 offset0:12 offset1:13\nM 12,164,176\nds_read2st64_b32 v[142:143], v225 offset0:14 offset1:15\nG 12\n"
"G 10\nv_add_f32 v96, v96, v128\nM 0,168,192\nR 160,186,16384\ns_add_u32 s63, s59, 65536\nv_add_f32 v97, v97, v129\n"
"M 0,172,208\nbuffer_load_dwordx4 v220, s[16:19], s63 offen lds\nG 9\nv_add_f32 v98, v98, v130\nM 16,168,196\nR 162,186,20480\n"
"v_add_f32 v99, v99, v131\nG 9\nv_add_f32 v100, v100, v132\nM 16,172,212\nds_read2st64_b32 v[144:145], v225 offset0:16 offset1:17\nv_add_f32 v101, v101, v133\n"
"G 9\nv_add_f32 v102, v102, v134\nv_mfma_f32_16x16x32_bf16 a[224:227], v[168:171], a[200:203], a[224:227]\nR 164,186,24576\nv_add_f32 v103, v103, v135\nG 8\n"
"v_add_f32 v104, v104, v136\nv_mfma_f32_16x16x32_bf16 a[224:227], v[172:175], a[216:219], a[224:227]\nbuffer_load_dwordx4 v221, s[16:19], s63 offen offset:1024 lds\nv_add_f32 v105, v105, v137\nv_mfma_f32_16x16x32_bf16 a[240:243], v[168:171], a[204:207], a[240:243]\nR 166,186,28672\n"
"s_add_u32 m0, s50, 90112\nG 8\nv_add_f32 v106, v106, v138\nv_mfma_f32_16x16x32_bf16 a[240:243], v[172:175], a[220:223], a[240:243]\nds_read2st64_b32 v[146:147], v225 offset0:18 offset1:19\nG 8\n"
"v_add_f32 v107, v107, v139\nv_mfma_f32_16x16x32_bf16 a[244:247], v[176:179], a[204:207], a[244:247]\nR 168,187,16384\ns_add_u32 s63, s59, 589824\nG 8\nv_add_f32 v108, v108, v140\n"
"v_mfma_f32_16x16x32_bf16 a[244:247], v[180:183], a[220:223], a[244:247]\nbuffer_load_dwordx4 v220, s[16:19], s63 offen lds\nv_add_f32 v109, v109, v141\nv_mfma_f32_16x16x32_bf16 a[228:231], v[176:179], a[200:203], a[228:231]\nR 170,187,20480\nG 8\n"
"v_add_f32 v110, v110, v142\nv_add_f32 v111, v111, v143\nv_mfma_f32_16x16x32_bf16 a[228:231], v[180:183], a[216:219], a[228:231]\nds_read2st64_b32 v[148:149], v225 offset0:20 offset1:21\nG 6\nv_add_f32 v112, v112, v144\n"
"v_add_f32 v113, v113, v145\nM 20,176,196\nR 172,187,24576\nds_read2st64_b32 v[150:151], v225 offset0:22 offset1:23\nM 20,180,212\nbuffer_load_dwordx4 v221, s[16:19], s63 offen offset:1024 lds\n"
"G 5\nv_add_f32 v114, v114, v146\nM 4,176,192\nR 174,187,28672\ns_add_u32 s63, s59, 131072\nv_add_f32 v115, v115, v147\n"
"M 4,180,208\nds_read2st64_b32 v[152:153], v225 offset0:24 offset1:25\nds_read2st64_b32 v[154:155], v225 offset0:26 offset1:27\nM 8,160,192\nR 176,184,32768\nds_read2st64_b32 v[156:157], v225 offset0:28 offset1:29\n"
"M 8,164,208\ns_add_u32 m0, s50, 98304\nds_read2st64_b32 v[158:159], v225 offset0:30 offset1:31\nG 8\nv_add_f32 v116, v116, v148\nM 24,160,196\n"
"R 178,184,36864\nv_add_f32 v117, v117, v149\nG 7\nv_add_f32 v118, v118, v150\nM 24,164,212\nbuffer_load_dwordx4 v220, s[16:19], s63 offen lds\n"
"v_add_f32 v119, v119, v151\nG 5\nv_add_f32 v120, v120, v152\nv_add_f32 v121, v121, v153\nG 4\nv_add_f32 v122, v122, v154\n"
"v_add_f32 v123, v123, v155\nG 2\nv_add_f32 v124, v124, v156\nv_add_f32 v125, v125, v157\nG 1\nv_add_f32 v126, v126, v158\n"
"v_add_f32 v127, v127, v159\nv_mfma_f32_16x16x32_bf16 a[232:235], v[160:163], a[200:203], a[232:235]\nR 180,184,40960\nv_cvt_pk_bf16_f32 v96, v96, v97\nv_cvt_pk_bf16_f32 v97, v98, v99\nv_mfma_f32_16x16x32_bf16 a[232:235], v[164:167], a[216:219], a[232:235]\n"
"v_cvt_pk_bf16_f32 v98, v100, v101\nv_cvt_pk_bf16_f32 v99, v102, v103\nv_cvt_pk_bf16_f32 v104, v104, v105\nv_mfma_f32_16x16x32_bf16 a[248:251], v[160:163], a[204:207], a[248:251]\nR 182,184,45056\nv_cvt_pk_bf16_f32 v105, v106, v107\n"
"v_cvt_pk_bf16_f32 v106, v108, v109\nv_mfma_f32_16x16x32_bf16 a[248:251], v[164:167], a[220:223], a[248:251]\nv_cvt_pk_bf16_f32 v107, v110, v111\nv_cvt_pk_bf16_f32 v112, v112, v113\nv_cvt_pk_bf16_f32 v113, v114, v115\nv_mfma_f32_16x16x32_bf16 a[252:255], v[168:171], a[204:207], a[252:255]\n"
"R 160,185,32768\nv_cvt_pk_bf16_f32 v114, v116, v117\nv_cvt_pk_bf16_f32 v115, v118, v119\nv_mfma_f32_16x16x32_bf16 a[252:255], v[172:175], a[220:223], a[252:255]\nbuffer_load_dwordx4 v221, s[16:19], s63 offen offset:1024 lds\nv_cvt_pk_bf16_f32 v120, v120, v121\n"
"v_mfma_f32_16x16x32_bf16 a[236:239], v[168:171], a[200:203], a[236:239]\nR 162,185,36864\ns_add_u32 s63, s59, 655360\nv_cvt_pk_bf16_f32 v121, v122, v123\nv_mfma_f32_16x16x32_bf16 a[236:239], v[172:175], a[216:219], a[236:239]\ns_add_u32 m0, s50, 106496\n"
"s_bfe_u32 s100, s56, 0x20007\ns_mul_i32 s100, s100, 49152\ns_add_u32 s100, s100, s72\ns_add_u32 s100, s100, 65011712\nv_cvt_pk_bf16_f32 v122, v124, v125\nv_cvt_pk_bf16_f32 v123, v126, v127\n"
"M 28,168,196\nR 164,185,40960\nbuffer_store_dwordx4 v[96:99], v252, s[20:23], s100 offen offset:0 sc1\nM 28,172,212\nbuffer_load_dwordx4 v220, s[16:19], s63 offen lds\nbuffer_store_dwordx4 v[104:107], v252, s[20:23], s100 offen offset:1024 sc1\n"
"M 12,168,192\nR 166,185,45056\nbuffer_store_dwordx4 v[112:115], v252, s[20:23], s100 offen offset:2048 sc1\nM 12,172,208\ns_waitcnt vmcnt(14)\ns_barrier\n"
"buffer_store_dwordx4 v[120:123], v252, s[20:23], s100 offen offset:3072 sc1\nG 4\nM 32,176,160\nR 168,186,32768\nM 32,180,176\nbuffer_load_dwordx4 v221, s[16:19], s63 offen offset:1024 lds\n"
"v_xor_b32 v240, 0x2000, v225\nM 48,176,164\nR 170,186,36864\ns_add_u32 s63, s59, 196608\nds_write2st64_b32 v240, a224, a225 offset0:0 offset1:1\nM 48,180,180\n"
"s_add_u32 m0, s50, 114688\nds_write2st64_b32 v240, a226, a227 offset0:2 offset1:3\nM 64,176,168\nR 172,186,40960\nds_write2st64_b32 v240, a228, a229 offset0:4 offset1:5\nM 64,180,184\n"
"buffer_load_dwordx4 v220, s[16:19], s63 offen lds\nds_write2st64_b32 v240, a230, a231 offset0:6 offset1:7\nM 80,176,172\nR 174,186,45056\nv_mfma_f32_16x16x32_bf16 v[96:99], v[208:211], v[192:195], 0\nds_write2st64_b32 v240, a232, a233 offset0:8 offset1:9\n"
"M 80,180,188\nv_mfma_f32_16x16x32_bf16 v[100:103], v[210:213], v[192:195], 0\nds_write2st64_b32 v240, a234, a235 offset0:10 offset1:11\nG 10\nM 84,160,172\nR 176,187,32768\n"
"v_mfma_f32_16x16x32_bf16 v[104:107], v[208:211], v[196:199], 0\nds_write2st64_b32 v240, a236, a237 offset0:12 offset1:13\nM 84,164,188\nv_mfma_f32_16x16x32_bf16 v[108:111], v[210:213], v[196:199], 0\nbuffer_load_dwordx4 v221, s[16:19], s63 offen offset:1024 lds\nds_write2st64_b32 v240, a238, a239 offset0:14 offset1:15\n"
"M 68,160,168\nR 178,187,36864\nv_mfma_f32_16x16x32_bf16 v[112:115], v[208:211], v[200:203], 0\ns_add_u32 m0, s50, 0\ns_add_u32 s63, s59, 720896\nds_write2st64_b32 v240, a240, a241 offset0:16 offset1:17\n"
"M 68,164,184\nv_mfma_f32_16x16x32_bf16 v[116:119], v[210:213], v[200:203], 0\nbuffer_load_dwordx4 v220, s[16:19], s63 offen lds\nds_write2st64_b32 v240, a242, a243 offset0:18 offset1:19\nM 52,160,164\nR 180,187,40960\n"
"v_mfma_f32_16x16x32_bf16 v[120:123], v[208:211], v[204:207], 0\nds_write2st64_b32 v240, a244, a245 offset0:20 offset1:21\nM 52,164,180\nv_mfma_f32_16x16x32_bf16 v[124:127], v[210:213], v[204:207], 0\nbuffer_load_dwordx4 v221, s[16:19], s63 offen offset:1024 lds\nds_write2st64_b32 v240, a246, a247 offset0:22 offset1:23\n"
"M 36,160,160\nR 182,187,45056\nv_mfma_f32_16x16x32_bf16 v[96:99], v[208:211], v[232:235], v[96:99]\ns_add_u32 m0, s50, 8192\nds_write2st64_b32 v240, a248, a249 offset0:24 offset1:25\nM 36,164,176\n"
"v_mfma_f32_16x16x32_bf16 v[100:103], v[210:213], v[232:235], v[100:103]\nds_write2st64_b32 v240, a250, a251 offset0:26 offset1:27\nG 14\nM 40,168,160\nR 160,184,49152\nv_mfma_f32_16x16x32_bf16 v[104:107], v[208:211], v[236:239], v[104:107]\n"
"s_add_u32 s63, s59, 393216\nds_write2st64_b32 v240, a252, a253 offset0:28 offset1:29\nM 40,172,176\nv_mfma_f32_16x16x32_bf16 v[108:111], v[210:213], v[236:239], v[108:111]\nbuffer_load_dwordx4 v220, s[16:19], s63 offen lds\nds_write2st64_b32 v240, a254, a255 offset0:30 offset1:31\n"
"M 56,168,164\nR 162,184,53248\nv_mfma_f32_16x16x32_bf16 v[112:115], v[208:211], v[232:235], v[112:115]\nM 56,172,180\nv_mfma_f32_16x16x32_bf16 v[116:119], v[210:213], v[232:235], v[116:119]\nbuffer_load_dwordx4 v221, s[16:19], s63 offen offset:1024 lds\n"
"M 72,168,168\nR 164,184,57344\nv_mfma_f32_16x16x32_bf16 v[120:123], v[208:211], v[236:239], v[120:123]\ns_add_u32 m0, s50, 16384\ns_add_u32 s63, s59, 458752\nM 72,172,184\n"
"v_mfma_f32_16x16x32_bf16 v[124:127], v[210:213], v[236:239], v[124:127]\nbuffer_load_dwordx4 v220, s[16:19], s63 offen lds\nM 88,168,172\nR 166,184,61440\nM 88,172,188\nG 8\n"
"M 92,176,172\nR 168,185,49152\ns_waitcnt vmcnt(19) lgkmcnt(4)\ns_barrier\nM 92,180,188\nds_read2st64_b32 v[128:129], v225 offset0:0 offset1:1\n"
"ds_read2st64_b32 v[130:131], v225 offset0:2 offset1:3\nM 76,176,168\nR 170,185,53248\nds_read2st64_b32 v[132:133], v225 offset0:4 offset1:5\nM 76,180,184\nds_read2st64_b32 v[134:135], v225 offset0:6 offset1:7\n"
"ds_read2st64_b32 v[136:137], v225 offset0:8 offset1:9\nM 60,176,164\nR 172,185,57344\nds_read2st64_b32 v[138:139], v225 offset0:10 offset1:11\nM 60,180,180\nbuffer_load_dwordx4 v221, s[16:19], s63 offen offset:1024 lds\n"
"ds_read2st64_b32 v[140:141], v225 offset0:12 offset1:13\nM 44,176,160\nR 174,185,61440\nds_read2st64_b32 v[142:143], v225 offset0:14 offset1:15\nM 44,180,176\nds_read2st64_b32 v[144:145], v225 offset0:16 offset1:17\n"
"G 13\nG 11\nv_add_f32 v0, v0, v128\nM 32,160,192\nR 176,186,49152\nv_add_f32 v1, v1, v129\n"
"G 11\nv_add_f32 v2, v2, v130\nM 32,164,208\ns_add_u32 m0, s50, 24576\nds_read2st64_b32 v[146:147], v225 offset0:18 offset1:19\nv_add_f32 v3, v3, v131\n"
"M 48,160,196\nR 178,186,53248\nG 11\nv_add_f32 v4, v4, v132\nv_add_f32 v5, v5, v133\nM 48,164,212\n"
"s_add_u32 s63, s59, 262144\nds_read2st64_b32 v[148:149], v225 offset0:20 offset1:21\nG 11\nv_add_f32 v6, v6, v134\nM 64,160,200\nR 180,186,57344\n"
"v_add_f32 v7, v7, v135\nG 11\nv_add_f32 v8, v8, v136\nM 64,164,216\nbuffer_load_dwordx4 v220, s[16:19], s63 offen lds\nv_add_f32 v9, v9, v137\n"
"M 80,160,204\nR 182,186,61440\nG 10\nv_add_f32 v10, v10, v138\nv_add_f32 v11, v11, v139\nM 80,164,220\n"
"ds_read2st64_b32 v[150:151], v225 offset0:22 offset1:23\nG 9\nv_add_f32 v12, v12, v140\nM 84,168,204\nR 160,187,49152\nv_add_f32 v13, v13, v141\n"
"G 9\nv_add_f32 v14, v14, v142\nM 84,172,220\nds_read2st64_b32 v[152:153], v225 offset0:24 offset1:25\nv_add_f32 v15, v15, v143\nG 9\n"
"v_add_f32 v16, v16, v144\nM 68,168,200\nR 162,187,53248\nv_add_f32 v17, v17, v145\nG 8\nv_add_f32 v18, v18, v146\n"
"M 68,172,216\nds_read2st64_b32 v[154:155], v225 offset0:26 offset1:27\nv_add_f32 v19, v19, v147\nG 7\nv_add_f32 v20, v20, v148\nM 52,168,196\n"
"R 164,187,57344\nds_read2st64_b32 v[156:157], v225 offset0:28 offset1:29\nv_add_f32 v21, v21, v149\nM 52,172,212\nbuffer_load_dwordx4 v221, s[16:19], s63 offen offset:1024 lds\nG 6\n"
"v_add_f32 v22, v22, v150\nM 36,168,192\nR 166,187,61440\ns_add_u32 s63, s59, 786432\nv_add_f32 v23, v23, v151\nM 36,172,208\n"
"ds_read2st64_b32 v[158:159], v225 offset0:30 offset1:31\nG 6\nv_add_f32 v24, v24, v152\nM 40,176,192\nR 168,188,0\nv_add_f32 v25, v25, v153\n"
"M 40,180,208\ns_add_u32 m0, s50, 32768\nG 5\nv_add_f32 v26, v26, v154\nv_add_f32 v27, v27, v155\nM 56,176,196\n"
"R 170,188,4096\nG 4\nv_add_f32 v28, v28, v156\nv_add_f32 v29, v29, v157\nM 56,180,212\nbuffer_load_dwordx4 v220, s[16:19], s63 offen lds\n"
"G 2\nv_add_f32 v30, v30, v158\nv_add_f32 v31, v31, v159\nM 72,176,200\nR 172,188,8192\nv_cvt_pk_bf16_f32 v0, v0, v1\n"
"v_cvt_pk_bf16_f32 v1, v2, v3\nM 72,180,216\nv_cvt_pk_bf16_f32 v2, v4, v5\nv_cvt_pk_bf16_f32 v3, v6, v7\nv_cvt_pk_bf16_f32 v8, v8, v9\nM 88,176,204\n"
"R 174,188,12288\nv_cvt_pk_bf16_f32 v9, v10, v11\nv_cvt_pk_bf16_f32 v10, v12, v13\nM 88,180,220\nv_cvt_pk_bf16_f32 v11, v14, v15\nv_cvt_pk_bf16_f32 v16, v16, v17\n"
"v_cvt_pk_bf16_f32 v17, v18, v19\nM 92,160,204\nR 176,189,0\nv_cvt_pk_bf16_f32 v18, v20, v21\nv_cvt_pk_bf16_f32 v19, v22, v23\nM 92,164,220\n"
"buffer_load_dwordx4 v221, s[16:19], s63 offen offset:1024 lds\nv_cvt_pk_bf16_f32 v24, v24, v25\nM 76,160,200\nR 178,189,4096\nv_cvt_pk_bf16_f32 v25, v26, v27\nv_cvt_pk_bf16_f32 v26, v28, v29\n"
"M 76,164,216\ns_bfe_u32 s100, s57, 0x20007\ns_mul_i32 s100, s100, 49152\ns_add_u32 s100, s100, s72\ns_add_u32 s100, s100, 65028096\nv_cvt_pk_bf16_f32 v27, v30, v31\n"
"M 60,160,196\nR 180,189,8192\nbuffer_store_dwordx4 v[0:3], v252, s[20:23], s100 offen offset:0 sc1\nM 60,164,212\nbuffer_store_dwordx4 v[8:11], v252, s[20:23], s100 offen offset:1024 sc1\nbuffer_store_dwordx4 v[16:19], v252, s[20:23], s100 offen offset:2048 sc1\n"
"M 44,160,192\nR 182,189,12288\nM 44,164,208\ns_waitcnt vmcnt(23)\ns_barrier\nG 4\n"
"M 96,168,0\nR 160,190,0\nbuffer_store_dwordx4 v[24:27], v252, s[20:23], s100 offen offset:3072 sc1\nM 96,172,16\ns_mov_b32 m0, s51\nM 112,168,4\n"
"R 162,190,4096\nds_write_addtid_b32 v64 offset:0\nM 112,172,20\nds_write_addtid_b32 v65 offset:256\nds_write_addtid_b32 v66 offset:512\nv_mfma_f32_16x16x32_bf16 v[128:131], v[168:171], a[8:11], 0\n"
"R 164,190,8192\nds_write_addtid_b32 v67 offset:768\nM 128,172,24\nds_write_addtid_b32 v68 offset:1024\nds_write_addtid_b32 v69 offset:1280\nv_mfma_f32_16x16x32_bf16 v[144:147], v[168:171], a[12:15], 0\n"
"R 166,190,12288\nds_write_addtid_b32 v70 offset:1536\nM 144,172,28\nds_write_addtid_b32 v71 offset:1792\nds_write_addtid_b32 v72 offset:2048\nG 13\n"
"v_mfma_f32_16x16x32_bf16 v[148:151], v[176:179], a[12:15], 0\nR 168,191,0\nds_write_addtid_b32 v73 offset:2304\nM 148,180,28\nds_write_addtid_b32 v74 offset:2560\nds_write_addtid_b32 v75 offset:2816\n"
"v_mfma_f32_16x16x32_bf16 v[132:135], v[176:179], a[8:11], 0\nR 170,191,4096\nds_write_addtid_b32 v76 offset:3072\nM 132,180,24\nds_write_addtid_b32 v77 offset:3328\nds_write_addtid_b32 v78 offset:3584\n"
"M 116,176,4\nR 172,191,8192\nds_write_addtid_b32 v79 offset:3840\nM 116,180,20\nds_write_addtid_b32 v80 offset:4096\nds_write_addtid_b32 v81 offset:4352\n"
"M 100,176,0\nR 174,191,12288\nds_write_addtid_b32 v82 offset:4608\nM 100,180,16\nds_write_addtid_b32 v83 offset:4864\nds_write_addtid_b32 v84 offset:5120\n"
"G 15\nM 104,160,0\nR 176,188,16384\nds_write_addtid_b32 v85 offset:5376\nM 104,164,16\nds_write_addtid_b32 v86 offset:5632\n"
"ds_write_addtid_b32 v87 offset:5888\nM 120,160,4\nR 178,188,20480\nds_write_addtid_b32 v88 offset:6144\nM 120,164,20\nds_write_addtid_b32 v89 offset:6400\n"
"ds_write_addtid_b32 v90 offset:6656\nv_mfma_f32_16x16x32_bf16 v[136:139], v[160:163], a[8:11], 0\nR 180,188,24576\nds_write_addtid_b32 v91 offset:6912\nM 136,164,24\nds_write_addtid_b32 v92 offset:7168\n"
"ds_write_addtid_b32 v93 offset:7424\nds_write_addtid_b32 v94 offset:7680\nds_write_addtid_b32 v95 offset:7936\nv_mfma_f32_16x16x32_bf16 v[152:155], v[160:163], a[12:15], 0\nR 182,188,28672\nM 152,164,28\n"
"G 15\nv_mfma_f32_16x16x32_bf16 v[156:159], v[168:171], a[12:15], 0\nR 160,189,16384\ns_waitcnt vmcnt(16) lgkmcnt(2)\ns_barrier\nM 156,172,28\n"
"ds_read2st64_b32 v[64:65], v225 offset0:0 offset1:1\nds_read2st64_b32 v[66:67], v225 offset0:2 offset1:3\nv_mfma_f32_16x16x32_bf16 v[140:143], v[168:171], a[8:11], 0\nR 162,189,20480\nds_read2st64_b32 v[68:69], v225 offset0:4 offset1:5\nM 140,172,24\n"
"ds_read2st64_b32 v[70:71], v225 offset0:6 offset1:7\nds_read2st64_b32 v[72:73], v225 offset0:8 offset1:9\nM 124,168,4\nR 164,189,24576\nds_read2st64_b32 v[74:75], v225 offset0:10 offset1:11\nM 124,172,20\n"
"ds_read2st64_b32 v[76:77], v225 offset0:12 offset1:13\nds_read2st64_b32 v[78:79], v225 offset0:14 offset1:15\nM 108,168,0\nR 166,189,28672\nds_read2st64_b32 v[80:81], v225 offset0:16 offset1:17\nM 108,172,16\n"
"ds_read2st64_b32 v[82:83], v225 offset0:18 offset1:19\nG 14\nG 12\nv_add_f32 v32, v32, v64\nM 96,176,32\nR 168,190,16384\n"
"v_add_f32 v33, v33, v65\nG 12\nv_add_f32 v34, v34, v66\nM 96,180,48\nds_read2st64_b32 v[84:85], v225 offset0:20 offset1:21\nv_add_f32 v35, v35, v67\n"
"G 11\nv_add_f32 v36, v36, v68\nM 112,176,36\nR 170,190,20480\nv_add_f32 v37, v37, v69\nG 11\n"
"v_add_f32 v38, v38, v70\nM 112,180,52\nds_read2st64_b32 v[86:87], v225 offset0:22 offset1:23\nv_add_f32 v39, v39, v71\nG 11\nv_add_f32 v40, v40, v72\n"
"M 128,176,40\nR 172,190,24576\nv_add_f32 v41, v41, v73\nG 10\nv_add_f32 v42, v42, v74\nM 128,180,56\n"
"ds_read2st64_b32 v[88:89], v225 offset0:24 offset1:25\nv_add_f32 v43, v43, v75\nG 10\nv_add_f32 v44, v44, v76\nM 144,176,44\nR 174,190,28672\n"
"v_add_f32 v45, v45, v77\nG 10\nv_add_f32 v46, v46, v78\nM 144,180,60\nds_read2st64_b32 v[90:91], v225 offset0:26 offset1:27\nG 10\n"
"v_add_f32 v47, v47, v79\nM 148,160,44\nR 176,191,16384\nG 10\nv_add_f32 v48, v48, v80\nv_add_f32 v49, v49, v81\n"
"M 148,164,60\nds_read2st64_b32 v[92:93], v225 offset0:28 offset1:29\nG 10\nv_add_f32 v50, v50, v82\nv_add_f32 v51, v51, v83\nM 132,160,40\n"
"R 178,191,20480\nG 9\nv_add_f32 v52, v52, v84\nv_add_f32 v53, v53, v85\nM 132,164,56\nds_read2st64_b32 v[94:95], v225 offset0:30 offset1:31\n"
"G 8\nv_add_f32 v54, v54, v86\nv_add_f32 v55, v55, v87\nM 116,160,36\nR 180,191,24576\nG 7\n"
"v_add_f32 v56, v56, v88\nv_add_f32 v57, v57, v89\nM 116,164,52\nG 5\nv_add_f32 v58, v58, v90\nv_add_f32 v59, v59, v91\n"
"M 100,160,32\nR 182,191,28672\nv_mov_b32 v226, s46\nM 100,164,48\nv_mov_b32 v227, s47\nG 4\n"
"v_add_f32 v60, v60, v92\nM 104,168,32\nR 160,188,32768\ns_bfe_u32 s62, s56, 0x20007\ns_lshl_b32 s62, s62, 5\ns_add_u32 s61, s74, 1009664\n"
"s_add_u32 s61, s61, s62\ns_waitcnt vmcnt(0)\ns_mov_b64 exec, 1\nbuffer_atomic_swap_x2 v[226:227], off, s[20:23], s61 sc1\ns_mov_b64 exec, -1\nM 104,172,48\n"
"s_bfe_u32 s62, s57, 0x20007\ns_lshl_b32 s62, s62, 5\ns_add_u32 s61, s74, 1009672\ns_add_u32 s61, s61, s62\ns_mov_b64 exec, 1\nbuffer_atomic_swap_x2 v[226:227], off, s[20:23], s61 sc1\n"
"s_mov_b64 exec, -1\nM 120,168,36\nR 162,188,36864\nv_add_f32 v61, v61, v93\nM 120,172,52\ns_add_u32 m0, s50, 40960\n"
"G 4\nv_add_f32 v62, v62, v94\nv_add_f32 v63, v63, v95\nM 136,168,40\nR 164,188,40960\nv_cvt_pk_bf16_f32 v32, v32, v33\n"
"v_cvt_pk_bf16_f32 v33, v34, v35\nM 136,172,56\ns_add_u32 s63, s59, 327680\nv_cvt_pk_bf16_f32 v34, v36, v37\nv_cvt_pk_bf16_f32 v35, v38, v39\nv_cvt_pk_bf16_f32 v40, v40, v41\n"
"M 152,168,44\nR 166,188,45056\nv_cvt_pk_bf16_f32 v41, v42, v43\nv_cvt_pk_bf16_f32 v42, v44, v45\nM 152,172,60\nG 4\n"
"v_cvt_pk_bf16_f32 v43, v46, v47\nv_cvt_pk_bf16_f32 v48, v48, v49\nv_cvt_pk_bf16_f32 v49, v50, v51\nM 156,176,44\nR 168,189,32768\nv_cvt_pk_bf16_f32 v50, v52, v53\n"
"v_cvt_pk_bf16_f32 v51, v54, v55\nM 156,180,60\nbuffer_load_dwordx4 v220, s[16:19], s63 offen lds\nv_cvt_pk_bf16_f32 v56, v56, v57\nM 140,176,40\nR 170,189,36864\n"
"v_cvt_pk_bf16_f32 v57, v58, v59\nv_cvt_pk_bf16_f32 v58, v60, v61\nM 140,180,56\nbuffer_load_dwordx4 v221, s[16:19], s63 offen offset:1024 lds\nv_cvt_pk_bf16_f32 v59, v62, v63\nM 124,176,36\n"
"R 172,189,40960\ns_add_u32 s63, s59, 851968\ns_bfe_u32 s100, s58, 0x20007\ns_mul_i32 s100, s100, 49152\ns_add_u32 s100, s100, s72\ns_add_u32 s100, s100, 65044480\n"
"M 124,180,52\ns_add_u32 m0, s50, 49152\nbuffer_store_dwordx4 v[32:35], v252, s[20:23], s100 offen offset:0 sc1\nM 108,176,32\nR 174,189,45056\nbuffer_store_dwordx4 v[40:43], v252, s[20:23], s100 offen offset:1024 sc1\n"
"M 108,180,48\ns_barrier\nbuffer_store_dwordx4 v[48:51], v252, s[20:23], s100 offen offset:2048 sc1\nG 4\nM 96,160,64\nR 176,190,32768\n"
"buffer_store_dwordx4 v[56:59], v252, s[20:23], s100 offen offset:3072 sc1\nM 96,164,80\nbuffer_load_dwordx4 v220, s[16:19], s63 offen lds\nM 112,160,68\nR 178,190,36864\nM 112,164,84\n"
"buffer_load_dwordx4 v221, s[16:19], s63 offen offset:1024 lds\nM 128,160,72\nR 180,190,40960\nM 128,164,88\nM 144,160,76\nR 182,190,45056\n"
"M 144,164,92\nG 4\nM 148,168,76\nR 160,191,32768\nv_bfe_u32 v250, v252, 4, 2\nM 148,172,92\n"
"v_lshlrev_b32 v250, 3, v250\ns_lshl_b32 s60, s43, 5\nv_add_u32 v250, s60, v250\nM 132,168,72\nR 162,191,36864\nM 132,172,88\n"
"M 116,168,68\nR 164,191,40960\nM 116,172,84\nM 100,168,64\nR 166,191,45056\nM 100,172,80\n"
"G 4\nM 104,176,64\nR 168,188,49152\nM 104,180,80\nM 120,176,68\nR 170,188,53248\n"
"M 120,180,84\nM 136,176,72\nR 172,184,0\nM 136,180,88\nM 152,176,76\nR 174,184,4096\n"
"M 152,180,92\nG 4\nM 156,160,76\nR 176,189,49152\nM 156,164,92\nM 140,160,72\n"
"R 178,189,53248\nM 140,164,88\nM 124,160,68\nR 180,185,0\nM 124,164,84\nM 108,160,64\n"
"R 182,185,4096\nM 108,164,80\nG 4\nM 96,168,96\nR 160,190,49152\nM 96,172,112\n"
"M 112,168,100\nR 162,190,53248\nM 112,172,116\nM 128,168,104\nR 164,186,0\nM 128,172,120\n"
"M 144,168,108\nR 166,186,4096\nM 144,172,124\nG 4\nM 148,176,108\nR 168,191,49152\n"
"M 148,180,124\nM 132,176,104\nR 170,191,53248\nM 132,180,120\nM 116,176,100\nR 172,187,0\n"
"M 116,180,116\nM 100,176,96\nR 174,187,4096\nv_mov_b32 v226, s46\nM 100,180,112\nG 4\n"
"v_mov_b32 v227, s47\nM 104,160,96\nR 176,184,8192\ns_bfe_u32 s62, s58, 0x20007\ns_lshl_b32 s62, s62, 5\ns_add_u32 s61, s74, 1009680\n"
"s_add_u32 s61, s61, s62\ns_waitcnt vmcnt(0)\ns_mov_b64 exec, 1\nbuffer_atomic_swap_x2 v[226:227], off, s[20:23], s61 sc1\ns_mov_b64 exec, -1\ns_barrier\n"
"M 104,164,112\nM 120,160,100\nR 178,184,12288\nM 120,164,116\nM 136,160,104\nR 180,184,16384\n"
"v_mov_b32 v248, 0\nM 136,164,120\nv_mov_b32 v249, 0\nM 152,160,108\nR 182,184,20480\ns_add_u32 s61, s74, 1009664\n"
"s_mov_b64 exec, 3\nbuffer_atomic_or_x2 v[248:249], v250, s[20:23], s61 offen sc0 sc1\ns_mov_b64 exec, -1\nM 152,164,124\nG 4\nM 156,168,108\n"
"R 160,185,8192\nM 156,172,124\nM 140,168,104\nR 162,185,12288\nM 140,172,120\nM 124,168,100\n"
"R 164,185,16384\nM 124,172,116\nM 108,168,96\nR 166,185,20480\nM 108,172,112\nG 4\n"
"M 96,176,128\nR 168,186,8192\nM 96,180,144\nM 112,176,132\nR 170,186,12288\nM 112,180,148\n"
"M 128,176,136\nR 172,186,16384\nM 128,180,152\nM 144,176,140\nR 174,186,20480\nM 144,180,156\n"
"G 4\nM 148,160,140\nR 176,187,8192\nM 148,164,156\nM 132,160,136\nR 178,187,12288\n"
"M 132,164,152\nM 116,160,132\nR 180,187,16384\nM 116,164,148\nM 100,160,128\nR 182,187,20480\n"
"M 100,164,144\nG 4\nM 104,168,128\nR 160,184,24576\nM 104,172,144\nM 120,168,132\n"
"R 162,184,28672\nM 120,172,148\nM 136,168,136\nR 164,184,32768\nM 136,172,152\ns_mul_i32 s64, s43, 49152\n"
"M 152,168,140\nR 166,184,36864\nM 152,172,156\nG 4\nM 156,176,140\nR 168,185,24576\n"
"s_barrier\ns_waitcnt vmcnt(0)\nv_cmp_eq_u64 s[86:87], s[46:47], v[248:249]\nM 156,180,156\ns_add_u32 s64, s64, s72\nM 140,176,136\n"
"R 170,185,28672\ns_and_b32 s62, s86, 3\ns_cmp_eq_u32 s62, 3\ns_cbranch_scc0 Lq944\nLq945:\nv_mov_b32 v248, 0\n"
"M 140,180,152\ns_add_u32 s64, s64, 65011712\nv_mov_b32 v249, 0\nM 124,176,132\nR 172,185,32768\ns_add_u32 s61, s74, 1009664\n"
"s_mov_b64 exec, 4\nbuffer_atomic_or_x2 v[248:249], v250, s[20:23], s61 offen sc0 sc1\ns_mov_b64 exec, -1\nM 124,180,148\nbuffer_load_dwordx4 v[0:3], v252, s[20:23], s64 offen offset:0 sc1\nM 108,176,128\n"
"R 174,185,36864\nM 108,180,144\nbuffer_load_dwordx4 v[4:7], v252, s[20:23], s64 offen offset:1024 sc1\nG 4\nM 128,160,168\nR 176,186,24576\n"
"s_mul_i32 s65, s43, 49152\nM 128,164,184\nR 178,186,28672\ns_add_u32 s65, s65, s72\nM 144,160,172\nR 180,186,32768\n"
"buffer_load_dwordx4 v[8:11], v252, s[20:23], s64 offen offset:2048 sc1\nM 144,164,188\nR 182,186,36864\nG 4\nM 148,168,172\nR 160,187,24576\n"
"s_add_u32 s65, s65, 65028096\nM 148,172,188\nR 162,187,28672\nbuffer_load_dwordx4 v[12:15], v252, s[20:23], s64 offen offset:3072 sc1\nM 132,168,168\nR 164,187,32768\n"
"M 132,172,184\nR 166,187,36864\nG 4\nM 136,176,168\nR 168,184,40960\nbuffer_load_dwordx4 v[16:19], v252, s[20:23], s65 offen offset:0 sc1\n"
"M 136,180,184\nR 170,184,45056\nM 152,176,172\nR 172,184,49152\nM 152,180,188\nR 174,184,53248\n"
"G 4\nM 156,160,172\nR 176,185,40960\nbuffer_load_dwordx4 v[20:23], v252, s[20:23], s65 offen offset:1024 sc1\nM 156,164,188\nR 178,185,45056\n"
"M 140,160,168\nR 180,185,49152\nbuffer_load_dwordx4 v[24:27], v252, s[20:23], s65 offen offset:2048 sc1\nM 140,164,184\nR 182,185,53248\nG 4\n"
"M 128,168,200\nR 160,186,40960\nM 128,172,216\nR 162,186,45056\nbuffer_load_dwordx4 v[28:31], v252, s[20:23], s65 offen offset:3072 sc1\nM 144,168,204\n"
"R 164,186,49152\nM 144,172,220\nR 166,186,53248\nG 4\nM 148,176,204\nR 168,187,40960\n"
"M 148,180,220\nR 170,187,45056\nM 132,176,200\nR 172,187,49152\nM 132,180,216\nR 174,187,53248\n"
"G 4\nM 136,160,200\nR 176,184,24576\nM 136,164,216\nR 178,184,28672\nM 152,160,204\n"
"R 180,184,32768\ns_mul_i32 s66, s43, 49152\nM 152,164,220\nR 182,184,36864\nG 4\nM 156,168,204\n"
"R 160,185,24576\ns_add_u32 s66, s66, s72\nM 156,172,220\nR 162,185,28672\ns_add_u32 s66, s66, 65044480\nM 140,168,200\n"
"R 164,185,32768\ns_waitcnt vmcnt(8)\nv_cmp_eq_u64 s[86:87], s[46:47], v[248:249]\nM 140,172,216\nR 166,185,36864\nG 4\n"
"M 96,176,160\nR 168,186,24576\ns_and_b32 s62, s86, 4\ns_cmp_eq_u32 s62, 4\ns_cbranch_scc0 Lq960\nLq961:\n"
"M 96,180,176\nR 170,186,28672\ns_mov_b32 m0, s51\nM 112,176,164\nR 172,186,32768\nbuffer_load_dwordx4 v[64:67], v252, s[20:23], s66 offen offset:0 sc1\n"
"ds_write_addtid_b32 v128 offset:0\nM 112,180,180\nR 174,186,36864\nds_write_addtid_b32 v129 offset:256\nG 6\nM 116,160,164\n"
"R 176,187,24576\nbuffer_load_dwordx4 v[68:71], v252, s[20:23], s66 offen offset:1024 sc1\nds_write_addtid_b32 v130 offset:512\nM 116,164,180\nR 178,187,28672\nds_write_addtid_b32 v131 offset:768\n"
"M 100,160,160\nR 180,187,32768\nds_write_addtid_b32 v132 offset:1024\nM 100,164,176\nR 182,187,36864\nds_write_addtid_b32 v133 offset:1280\n"
"G 9\nM 104,168,160\nR 160,184,40960\nbuffer_load_dwordx4 v[72:75], v252, s[20:23], s66 offen offset:2048 sc1\nds_write_addtid_b32 v134 offset:1536\nM 104,172,176\n"
"R 162,184,45056\nbuffer_load_dwordx4 v[76:79], v252, s[20:23], s66 offen offset:3072 sc1\nds_write_addtid_b32 v135 offset:1792\nM 120,168,164\nR 164,184,49152\nds_write_addtid_b32 v136 offset:2048\n"
"M 120,172,180\nR 166,184,53248\nds_write_addtid_b32 v137 offset:2304\nG 9\nM 124,176,164\nR 168,185,40960\n"
"ds_write_addtid_b32 v138 offset:2560\nM 124,180,180\nR 170,185,45056\nds_write_addtid_b32 v139 offset:2816\nM 108,176,160\nR 172,185,49152\n"
"ds_write_addtid_b32 v140 offset:3072\nds_write_addtid_b32 v141 offset:3328\nds_write_addtid_b32 v142 offset:3584\nds_write_addtid_b32 v143 offset:3840\nds_write_addtid_b32 v144 offset:4096\nds_write_addtid_b32 v145 offset:4352\n"
"ds_write_addtid_b32 v146 offset:4608\nds_write_addtid_b32 v147 offset:4864\nds_write_addtid_b32 v148 offset:5120\nds_write_addtid_b32 v149 offset:5376\nds_write_addtid_b32 v150 offset:5632\nds_write_addtid_b32 v151 offset:5888\n"
"ds_write_addtid_b32 v152 offset:6144\nds_write_addtid_b32 v153 offset:6400\nds_write_addtid_b32 v154 offset:6656\nds_write_addtid_b32 v155 offset:6912\nds_write_addtid_b32 v156 offset:7168\nds_write_addtid_b32 v157 offset:7424\n"
"ds_write_addtid_b32 v158 offset:7680\nds_write_addtid_b32 v159 offset:7936\nM 108,180,176\nR 174,185,53248\nG 15\nM 96,160,192\n"
"R 176,186,40960\nM 96,164,208\nR 178,186,45056\nG 3\ns_barrier\nM 112,160,196\n"
"R 180,186,49152\nM 112,164,212\nR 182,186,53248\nG 4\nM 116,168,196\nR 160,187,40960\n"
"M 116,172,212\nR 162,187,45056\nM 100,168,192\nR 164,187,49152\nM 100,172,208\nR 166,187,53248\n"
"G 4\nM 104,176,192\nds_read2st64_b32 v[128:129], v225 offset0:0 offset1:1\nds_read2st64_b32 v[130:131], v225 offset0:2 offset1:3\nM 104,180,208\nds_read2st64_b32 v[132:133], v225 offset0:4 offset1:5\n"
"ds_read2st64_b32 v[134:135], v225 offset0:6 offset1:7\nM 120,176,196\nds_read2st64_b32 v[136:137], v225 offset0:8 offset1:9\nds_read2st64_b32 v[138:139], v225 offset0:10 offset1:11\nM 120,180,212\nds_read2st64_b32 v[140:141], v225 offset0:12 offset1:13\n"
"ds_read2st64_b32 v[142:143], v225 offset0:14 offset1:15\nG 8\nM 124,160,196\nds_read2st64_b32 v[144:145], v225 offset0:16 offset1:17\nds_read2st64_b32 v[146:147], v225 offset0:18 offset1:19\nM 124,164,212\n"
"ds_read2st64_b32 v[148:149], v225 offset0:20 offset1:21\nds_read2st64_b32 v[150:151], v225 offset0:22 offset1:23\nM 108,160,192\nds_read2st64_b32 v[152:153], v225 offset0:24 offset1:25\nds_read2st64_b32 v[154:155], v225 offset0:26 offset1:27\nM 108,164,208\n"
"ds_read2st64_b32 v[156:157], v225 offset0:28 offset1:29\nds_read2st64_b32 v[158:159], v225 offset0:30 offset1:31\ns_waitcnt vmcnt(11)\nv_mfma_f32_16x16x32_bf16 v[96:99], v[208:211], v[0:3], v[96:99]\nv_mfma_f32_16x16x32_bf16 v[100:103], v[210:213], v[0:3], v[100:103]\ns_waitcnt vmcnt(10)\n"
"v_mfma_f32_16x16x32_bf16 v[104:107], v[208:211], v[4:7], v[104:107]\nv_mfma_f32_16x16x32_bf16 v[108:111], v[210:213], v[4:7], v[108:111]\ns_waitcnt vmcnt(9)\nv_mfma_f32_16x16x32_bf16 v[112:115], v[208:211], v[8:11], v[112:115]\nv_mfma_f32_16x16x32_bf16 v[116:119], v[210:213], v[8:11], v[116:119]\ns_waitcnt vmcnt(8)\n"
"v_mfma_f32_16x16x32_bf16 v[120:123], v[208:211], v[12:15], v[120:123]\nv_mfma_f32_16x16x32_bf16 v[124:127], v[210:213], v[12:15], v[124:127]\ns_waitcnt vmcnt(7)\nv_mfma_f32_16x16x32_bf16 v[96:99], v[208:211], v[16:19], v[96:99]\nv_mfma_f32_16x16x32_bf16 v[100:103], v[210:213], v[16:19], v[100:103]\ns_waitcnt vmcnt(6)\n"
"v_mfma_f32_16x16x32_bf16 v[104:107], v[208:211], v[20:23], v[104:107]\nv_mfma_f32_16x16x32_bf16 v[108:111], v[210:213], v[20:23], v[108:111]\ns_waitcnt vmcnt(5)\nv_mfma_f32_16x16x32_bf16 v[112:115], v[208:211], v[24:27], v[112:115]\nv_mfma_f32_16x16x32_bf16 v[116:119], v[210:213], v[24:27], v[116:119]\ns_waitcnt vmcnt(4)\n"
"v_mfma_f32_16x16x32_bf16 v[120:123], v[208:211], v[28:31], v[120:123]\nv_mfma_f32_16x16x32_bf16 v[124:127], v[210:213], v[28:31], v[124:127]\ns_waitcnt vmcnt(3)\nv_mfma_f32_16x16x32_bf16 v[96:99], v[208:211], v[64:67], v[96:99]\nv_mfma_f32_16x16x32_bf16 v[100:103], v[210:213], v[64:67], v[100:103]\ns_waitcnt vmcnt(2)\n"
"v_mfma_f32_16x16x32_bf16 v[104:107], v[208:211], v[68:71], v[104:107]\nv_mfma_f32_16x16x32_bf16 v[108:111], v[210:213], v[68:71], v[108:111]\ns_waitcnt vmcnt(1)\nv_mfma_f32_16x16x32_bf16 v[112:115], v[208:211], v[72:75], v[112:115]\nv_mfma_f32_16x16x32_bf16 v[116:119], v[210:213], v[72:75], v[116:119]\ns_waitcnt vmcnt(0)\n"
"v_mfma_f32_16x16x32_bf16 v[120:123], v[208:211], v[76:79], v[120:123]\nv_mfma_f32_16x16x32_bf16 v[124:127], v[210:213], v[76:79], v[124:127]\nG 15\nv_pk_add_f32 v[96:97], v[96:97], v[128:129]\nG 14\nv_pk_add_f32 v[98:99], v[98:99], v[130:131]\n"
"G 13\nv_pk_add_f32 v[100:101], v[100:101], v[132:133]\nG 12\nv_pk_add_f32 v[102:103], v[102:103], v[134:135]\nv_cvt_pk_bf16_f32 v192, v96, v97\nv_cvt_pk_bf16_f32 v193, v98, v99\n"
"v_cvt_pk_bf16_f32 v194, v100, v101\nv_cvt_pk_bf16_f32 v195, v102, v103\nG 11\nv_pk_add_f32 v[104:105], v[104:105], v[136:137]\nG 10\nv_pk_add_f32 v[106:107], v[106:107], v[138:139]\n"
"G 9\nv_pk_add_f32 v[108:109], v[108:109], v[140:141]\nG 8\nv_pk_add_f32 v[110:111], v[110:111], v[142:143]\nv_cvt_pk_bf16_f32 v196, v104, v105\nv_cvt_pk_bf16_f32 v197, v106, v107\n"
"v_cvt_pk_bf16_f32 v198, v108, v109\nv_cvt_pk_bf16_f32 v199, v110, v111\nG 7\nv_pk_add_f32 v[112:113], v[112:113], v[144:145]\nG 6\nv_pk_add_f32 v[114:115], v[114:115], v[146:147]\n"
"G 5\nv_pk_add_f32 v[116:117], v[116:117], v[148:149]\nG 4\nv_pk_add_f32 v[118:119], v[118:119], v[150:151]\nv_cvt_pk_bf16_f32 v200, v112, v113\nv_cvt_pk_bf16_f32 v201, v114, v115\n"
"v_cvt_pk_bf16_f32 v202, v116, v117\nv_cvt_pk_bf16_f32 v203, v118, v119\nG 3\nv_pk_add_f32 v[120:121], v[120:121], v[152:153]\nG 2\nv_pk_add_f32 v[122:123], v[122:123], v[154:155]\n"
"G 1\nv_pk_add_f32 v[124:125], v[124:125], v[156:157]\nG 0\nv_pk_add_f32 v[126:127], v[126:127], v[158:159]\nv_cvt_pk_bf16_f32 v204, v120, v121\nv_cvt_pk_bf16_f32 v205, v122, v123\n"
"v_cvt_pk_bf16_f32 v206, v124, v125\nv_cvt_pk_bf16_f32 v207, v126, v127\nv_and_b32 v230, 0xf0, v252\nv_lshlrev_b32 v230, 7, v230\nv_bfe_u32 v231, v252, 8, 1\nv_lshl_add_u32 v230, v231, 5, v230\n"
"v_lshrrev_b32 v231, 9, v252\nv_lshl_add_u32 v230, v231, 4, v230\ns_add_u32 s61, s49, s53\ns_add_u32 s62, s61, 32768\nv_permlane16_swap_b32 v192, v194\nv_permlane16_swap_b32 v193, v195\n"
"buffer_store_dwordx4 v[192:195], v230, s[28:31], s61 offen offset:0\nv_permlane16_swap_b32 v196, v198\nv_permlane16_swap_b32 v197, v199\nbuffer_store_dwordx4 v[196:199], v230, s[28:31], s61 offen offset:64\nv_permlane16_swap_b32 v200, v202\nv_permlane16_swap_b32 v201, v203\n"
"buffer_store_dwordx4 v[200:203], v230, s[28:31], s62 offen offset:0\nv_permlane16_swap_b32 v204, v206\nv_permlane16_swap_b32 v205, v207\nbuffer_store_dwordx4 v[204:207], v230, s[28:31], s62 offen offset:64\ns_add_u32 s60, s83, 1\nv_mov_b32 v234, s60\n"
"s_or_b32 s60, s45, s40\ns_cmp_eq_u32 s60, 0\ns_cselect_b64 exec, 1, 0\nbuffer_store_dword v234, off, s[20:23], 0 sc1\ns_mov_b64 exec, -1\ns_endpgm\n"
"LqF:\nLq579:\nv_mov_b32 v248, 0\nv_mov_b32 v249, 0\ns_add_u32 s61, s80, 88358912\ns_mov_b64 exec, 0xffff\n"
"buffer_atomic_or_x2 v[248:249], v250, s[20:23], s61 offen sc0 sc1\ns_mov_b64 exec, -1\ns_waitcnt vmcnt(0)\nv_cmp_eq_u64 s[86:87], s[46:47], v[248:249]\ns_nop 1\ns_and_b32 s62, s86, 0xffff\n"
"s_cmp_eq_u32 s62, 0xffff\ns_cbranch_scc1 Lq580\ns_sleep 1\ns_sub_u32 s48, s48, 1\ns_cmp_gt_i32 s48, 0\ns_cbranch_scc1 Lq579\n"
"s_branch Lq580\nLq629:\ns_lshr_b32 s84, s49, 3\ns_add_u32 s84, s84, 82837504\nbuffer_load_dwordx4 v[128:131], v252, s[20:23], s84 offen offset:0 sc1\nbuffer_load_dwordx4 v[132:135], v252, s[20:23], s84 offen offset:1024 sc1\n"
"buffer_load_dwordx4 v[136:139], v252, s[20:23], s84 offen offset:2048 sc1\nbuffer_load_dwordx4 v[140:143], v252, s[20:23], s84 offen offset:3072 sc1\ns_lshr_b32 s85, s49, 3\ns_add_u32 s85, s85, 82841600\nbuffer_load_dwordx4 v[144:147], v252, s[20:23], s85 offen offset:0 sc1\nbuffer_load_dwordx4 v[148:151], v252, s[20:23], s85 offen offset:1024 sc1\n"
"buffer_load_dwordx4 v[152:155], v252, s[20:23], s85 offen offset:2048 sc1\nbuffer_load_dwordx4 v[156:159], v252, s[20:23], s85 offen offset:3072 sc1\ns_waitcnt vmcnt(6)\nv_min3_u32 v230, v129, v131, v133\ns_waitcnt vmcnt(5)\nv_min3_u32 v230, v230, v135, v137\n"
"s_waitcnt vmcnt(4)\nv_min3_u32 v230, v230, v139, v141\ns_waitcnt vmcnt(3)\nv_min3_u32 v230, v230, v143, v145\ns_waitcnt vmcnt(2)\nv_min3_u32 v230, v230, v147, v149\n"
"s_waitcnt vmcnt(1)\nv_min3_u32 v230, v230, v151, v153\ns_waitcnt vmcnt(0)\nv_min3_u32 v230, v230, v155, v157\nv_min3_u32 v230, v230, v159, v159\nv_max3_u32 v231, v129, v131, v133\n"
"v_max3_u32 v231, v231, v135, v137\nv_max3_u32 v231, v231, v139, v141\nv_max3_u32 v231, v231, v143, v145\nv_max3_u32 v231, v231, v147, v149\nv_max3_u32 v231, v231, v151, v153\nv_max3_u32 v231, v231, v155, v157\n"
"v_max3_u32 v231, v231, v159, v159\nv_cmp_ne_u32 s[86:87], s46, v230\nv_cmp_ne_u32 s[88:89], s46, v231\ns_nop 0\ns_or_b64 s[86:87], s[86:87], s[88:89]\ns_cbranch_scc0 Lq630\n"
"s_sub_u32 s48, s48, 1\ns_cmp_gt_i32 s48, 0\ns_cbranch_scc1 Lq629\ns_branch Lq630\nLq679:\ns_mov_b64 exec, s[66:67]\n"
"buffer_load_dwordx4 v[230:233], v252, s[20:23], s65 offen offset:0 sc1\nbuffer_load_dwordx2 v[64:65], v227, s[20:23], s65 offen offset:2048 sc1\nbuffer_load_dwordx2 v[70:71], v227, s[20:23], s65 offen offset:2560 sc1\ns_mov_b64 exec, -1\ns_waitcnt vmcnt(0)\nv_max3_u32 v248, v233, v65, v71\n"
"v_cmp_eq_u32 vcc, -1, v248\ns_nop 1\ns_cbranch_vccz Lq680\ns_sub_u32 s48, s48, 1\ns_cmp_gt_i32 s48, 0\ns_cbranch_scc1 Lq679\n"
"s_branch Lq680\nLq681:\ns_mov_b64 exec, s[66:67]\nbuffer_load_dwordx4 v[234:237], v252, s[20:23], s65 offen offset:1024 sc1\nbuffer_load_dwordx2 v[76:77], v227, s[20:23], s65 offen offset:3072 sc1\nbuffer_load_dwordx2 v[82:83], v227, s[20:23], s65 offen offset:3584 sc1\n"
"s_mov_b64 exec, -1\ns_waitcnt vmcnt(0)\nv_max3_u32 v248, v237, v77, v83\nv_cmp_eq_u32 vcc, -1, v248\ns_nop 1\ns_cbranch_vccz Lq682\n"
"s_sub_u32 s48, s48, 1\ns_cmp_gt_i32 s48, 0\ns_cbranch_scc1 Lq681\ns_branch Lq682\nLq684:\nv_mov_b32 v248, 0\n"
"v_mov_b32 v249, 0\ns_add_u32 s61, s80, 88498176\ns_mov_b64 exec, 0xffff\nbuffer_atomic_or_x2 v[248:249], v250, s[20:23], s61 offen sc0 sc1\ns_mov_b64 exec, -1\ns_waitcnt vmcnt(0)\n"
"v_cmp_eq_u64 s[86:87], s[46:47], v[248:249]\ns_nop 1\ns_and_b32 s62, s86, 0xffff\ns_cmp_eq_u32 s62, 0xffff\ns_cbranch_scc1 Lq685\ns_sleep 1\n"
"s_sub_u32 s48, s48, 1\ns_cmp_gt_i32 s48, 0\ns_cbranch_scc1 Lq684\ns_branch Lq685\nLq736:\nv_mov_b32 v248, 0\n"
"v_mov_b32 v249, 0\ns_add_u32 s61, s80, 88637440\ns_mov_b64 exec, 0xffff\nbuffer_atomic_or_x2 v[248:249], v250, s[20:23], s61 offen sc0 sc1\ns_mov_b64 exec, -1\ns_waitcnt vmcnt(0)\n"
"v_cmp_eq_u64 s[86:87], s[46:47], v[248:249]\ns_nop 1\ns_and_b32 s62, s86, 0xffff\ns_cmp_eq_u32 s62, 0xffff\ns_cbranch_scc1 Lq737\ns_sleep 1\n"
"s_sub_u32 s48, s48, 1\ns_cmp_gt_i32 s48, 0\ns_cbranch_scc1 Lq736\ns_branch Lq737\nLq786:\ns_lshr_b32 s84, s49, 3\n"
"s_add_u32 s84, s84, 83361792\nbuffer_load_dwordx4 v[0:3], v252, s[20:23], s84 offen offset:0 sc1\nbuffer_load_dwordx4 v[4:7], v252, s[20:23], s84 offen offset:1024 sc1\nbuffer_load_dwordx4 v[8:11], v252, s[20:23], s84 offen offset:2048 sc1\nbuffer_load_dwordx4 v[12:15], v252, s[20:23], s84 offen offset:3072 sc1\ns_lshr_b32 s85, s49, 3\n"
"s_add_u32 s85, s85, 83365888\nbuffer_load_dwordx4 v[16:19], v252, s[20:23], s85 offen offset:0 sc1\nbuffer_load_dwordx4 v[20:23], v252, s[20:23], s85 offen offset:1024 sc1\nbuffer_load_dwordx4 v[24:27], v252, s[20:23], s85 offen offset:2048 sc1\nbuffer_load_dwordx4 v[28:31], v252, s[20:23], s85 offen offset:3072 sc1\ns_waitcnt vmcnt(6)\n"
"v_min3_u32 v230, v1, v3, v5\ns_waitcnt vmcnt(5)\nv_min3_u32 v230, v230, v7, v9\ns_waitcnt vmcnt(4)\nv_min3_u32 v230, v230, v11, v13\ns_waitcnt vmcnt(3)\n"
"v_min3_u32 v230, v230, v15, v17\ns_waitcnt vmcnt(2)\nv_min3_u32 v230, v230, v19, v21\ns_waitcnt vmcnt(1)\nv_min3_u32 v230, v230, v23, v25\ns_waitcnt vmcnt(0)\n"
"v_min3_u32 v230, v230, v27, v29\nv_min3_u32 v230, v230, v31, v31\nv_max3_u32 v231, v1, v3, v5\nv_max3_u32 v231, v231, v7, v9\nv_max3_u32 v231, v231, v11, v13\nv_max3_u32 v231, v231, v15, v17\n"
"v_max3_u32 v231, v231, v19, v21\nv_max3_u32 v231, v231, v23, v25\nv_max3_u32 v231, v231, v27, v29\nv_max3_u32 v231, v231, v31, v31\nv_cmp_ne_u32 s[86:87], s46, v230\nv_cmp_ne_u32 s[88:89], s46, v231\n"
"s_nop 0\ns_or_b64 s[86:87], s[86:87], s[88:89]\ns_cbranch_scc0 Lq787\ns_sub_u32 s48, s48, 1\ns_cmp_gt_i32 s48, 0\ns_cbranch_scc1 Lq786\n"
"s_branch Lq787\nLq812:\nv_mov_b32 v248, 0\nv_mov_b32 v249, 0\ns_add_u32 s61, s82, 845824\ns_mov_b64 exec, 0xf\n"
"buffer_atomic_or_x2 v[248:249], v250, s[20:23], s61 offen sc0 sc1\ns_mov_b64 exec, -1\ns_waitcnt vmcnt(0)\nv_cmp_eq_u64 s[86:87], s[46:47], v[248:249]\ns_nop 1\ns_and_b32 s62, s86, 0xf\n"
"s_cmp_eq_u32 s62, 0xf\ns_cbranch_scc1 Lq813\ns_sleep 1\ns_sub_u32 s48, s48, 1\ns_cmp_gt_i32 s48, 0\ns_cbranch_scc1 Lq812\n"
"s_branch Lq813\nLq847:\nv_mov_b32 v248, 0\nv_mov_b32 v249, 0\ns_add_u32 s61, s82, 845832\ns_mov_b64 exec, 0xf\n"
"buffer_atomic_or_x2 v[248:249], v250, s[20:23], s61 offen sc0 sc1\ns_mov_b64 exec, -1\ns_waitcnt vmcnt(0)\nv_cmp_eq_u64 s[86:87], s[46:47], v[248:249]\ns_nop 1\ns_and_b32 s62, s86, 0xf\n"
"s_cmp_eq_u32 s62, 0xf\ns_cbranch_scc1 Lq848\ns_sleep 1\ns_sub_u32 s48, s48, 1\ns_cmp_gt_i32 s48, 0\ns_cbranch_scc1 Lq847\n"
"s_branch Lq848\nLq882:\nv_mov_b32 v248, 0\nv_mov_b32 v249, 0\ns_add_u32 s61, s82, 845848\ns_mov_b64 exec, 0xf\n"
"buffer_atomic_or_x2 v[248:249], v250, s[20:23], s61 offen sc0 sc1\ns_mov_b64 exec, -1\ns_waitcnt vmcnt(0)\nv_cmp_eq_u64 s[86:87], s[46:47], v[248:249]\ns_nop 1\ns_and_b32 s62, s86, 0xf\n"
"s_cmp_eq_u32 s62, 0xf\ns_cbranch_scc1 Lq883\ns_sleep 1\ns_sub_u32 s48, s48, 1\ns_cmp_gt_i32 s48, 0\ns_cbranch_scc1 Lq882\n"
"s_branch Lq883\nLq901:\nv_mov_b32 v248, 0\nv_mov_b32 v249, 0\ns_add_u32 s61, s82, 845840\ns_mov_b64 exec, 0xf\n"
"buffer_atomic_or_x2 v[248:249], v250, s[20:23], s61 offen sc0 sc1\ns_mov_b64 exec, -1\ns_waitcnt vmcnt(0)\nv_cmp_eq_u64 s[86:87], s[46:47], v[248:249]\ns_nop 1\ns_and_b32 s62, s86, 0xf\n"
"s_cmp_eq_u32 s62, 0xf\ns_cbranch_scc1 Lq902\ns_sleep 1\ns_sub_u32 s48, s48, 1\ns_cmp_gt_i32 s48, 0\ns_cbranch_scc1 Lq901\n"
"s_branch Lq902\nLq944:\nv_mov_b32 v248, 0\nv_mov_b32 v249, 0\ns_add_u32 s61, s74, 1009664\ns_mov_b64 exec, 3\n"
"buffer_atomic_or_x2 v[248:249], v250, s[20:23], s61 offen sc0 sc1\ns_mov_b64 exec, -1\ns_waitcnt vmcnt(0)\nv_cmp_eq_u64 s[86:87], s[46:47], v[248:249]\ns_nop 1\ns_and_b32 s62, s86, 3\n"
"s_cmp_eq_u32 s62, 3\ns_cbranch_scc1 Lq945\ns_sleep 1\ns_sub_u32 s48, s48, 1\ns_cmp_gt_i32 s48, 0\ns_cbranch_scc1 Lq944\n"
"s_branch Lq945\nLq960:\nv_mov_b32 v248, 0\nv_mov_b32 v249, 0\ns_add_u32 s61, s74, 1009664\ns_mov_b64 exec, 4\n"
"buffer_atomic_or_x2 v[248:249], v250, s[20:23], s61 offen sc0 sc1\ns_mov_b64 exec, -1\ns_waitcnt vmcnt(0)\nv_cmp_eq_u64 s[86:87], s[46:47], v[248:249]\ns_nop 1\ns_and_b32 s62, s86, 4\n"
"s_cmp_eq_u32 s62, 4\ns_cbranch_scc1 Lq961\ns_sleep 1\ns_sub_u32 s48, s48, 1\ns_cmp_gt_i32 s48, 0\ns_cbranch_scc1 Lq960\n"
"s_branch Lq961\nLqE:\n"
  : "+{v0}"(tid), "+{s[92:93]}"(ka), "+{s45}"(bid) :
  : "a0", "a1", "a2", "a3", "a4", "a5", "a6", "a7", "a8", "a9", "a10", "a11", "a12", "a13", "a14", "a15", "a16", "a17", "a18", "a19", "a20", "a21", "a22", "a23", "a24", "a25", "a26", "a27", "a28", "a29", "a30", "a31", "a32", "a33", "a34", "a35", "a36", "a37", "a38", "a39", "a40", "a41", "a42", "a43", "a44", "a45", "a46", "a47", "a48", "a49", "a50", "a51", "a52", "a53", "a54", "a55", "a56", "a57", "a58", "a59", "a60", "a61", "a62", "a63", "a64", "a65", "a66", "a67", "a68", "a69", "a70", "a71", "a72", "a73", "a74", "a75", "a76", "a77", "a78", "a79", "a80", "a81", "a82", "a83", "a84", "a85", "a86", "a87", "a88", "a89", "a90", "a91", "a92", "a93", "a94", "a95", "a96", "a97", "a98", "a99", "a100", "a101", "a102", "a103", "a104", "a105", "a106", "a107", "a108", "a109", "a110", "a111", "a112", "a113", "a114", "a115", "a116", "a117", "a118", "a119", "a120", "a121", "a122", "a123", "a124", "a125", "a126", "a127", "a128", "a129", "a130", "a131", "a132", "a133", "a134", "a135", "a136", "a137", "a138", "a139", "a140", "a141", "a142", "a143", "a144", "a145", "a146", "a147", "a148", "a149", "a150", "a151", "a152", "a153", "a154", "a155", "a156", "a157", "a158", "a159", "a160", "a161", "a162", "a163", "a164", "a165", "a166", "a167", "a168", "a169", "a170", "a171", "a172", "a173", "a174", "a175", "a176", "a177", "a178", "a179", "a180", "a181", "a182", "a183", "a184", "a185", "a186", "a187", "a188", "a189", "a190", "a191", "a192", "a193", "a194", "a195", "a196", "a197", "a198", "a199", "a200", "a201", "a202", "a203", "a204", "a205", "a206", "a207", "a208", "a209", "a210", "a211", "a212", "a213", "a214", "a215", "a216", "a217", "a218", "a219", "a220", "a221", "a222", "a223", "a224", "a225", "a226", "a227", "a228", "a229", "a230", "a231", "a232", "a233", "a234", "a235", "a236", "a237", "a238", "a239", "a240", "a241", "a242", "a243", "a244", "a245", "a246", "a247", "a248", "a249", "a250", "a251", "a252", "a253", "a254", "a255", "v1", "v2", "v3", "v4", "v5", "v6", "v7", "v8", "v9", "v10", "v11", "v12", "v13", "v14", "v15", "v16", "v17", "v18", "v19", "v20", "v21", "v22", "v23", "v24", "v25", "v26", "v27", "v28", "v29", "v30", "v31", "v32", "v33", "v34", "v35", "v36", "v37", "v38", "v39", "v40", "v41", "v42", "v43", "v44", "v45", "v46", "v47", "v48", "v49", "v50", "v51", "v52", "v53", "v54", "v55", "v56", "v57", "v58", "v59", "v60", "v61", "v62", "v63", "v64", "v65", "v66", "v67", "v68", "v69", "v70", "v71", "v72", "v73", "v74", "v75", "v76", "v77", "v78", "v79", "v80", "v81", "v82", "v83", "v84", "v85", "v86", "v87", "v88", "v89", "v90", "v91", "v92", "v93", "v94", "v95", "v96", "v97", "v98", "v99", "v100", "v101", "v102", "v103", "v104", "v105", "v106", "v107", "v108", "v109", "v110", "v111", "v112", "v113", "v114", "v115", "v116", "v117", "v118", "v119", "v120", "v121", "v122", "v123", "v124", "v125", "v126", "v127", "v128", "v129", "v130", "v131", "v132", "v133", "v134", "v135", "v136", "v137", "v138", "v139", "v140", "v141", "v142", "v143", "v144", "v145", "v146", "v147", "v148", "v149", "v150", "v151", "v152", "v153", "v154", "v155", "v156", "v157", "v158", "v159", "v160", "v161", "v162", "v163", "v164", "v165", "v166", "v167", "v168", "v169", "v170", "v171", "v172", "v173", "v174", "v175", "v176", "v177", "v178", "v179", "v180", "v181", "v182", "v183", "v184", "v185", "v186", "v187", "v188", "v189", "v190", "v191", "v192", "v193", "v194", "v195", "v196", "v197", "v198", "v199", "v200", "v201", "v202", "v203", "v204", "v205", "v206", "v207", "v208", "v209", "v210", "v211", "v212", "v213", "v214", "v215", "v216", "v217", "v218", "v219", "v220", "v221", "v222", "v223", "v224", "v225", "v226", "v227", "v228", "v229", "v230", "v231", "v232", "v233", "v234", "v235", "v236", "v237", "v238", "v239", "v240", "v241", "v242", "v243", "v244", "v245", "v246", "v247", "v248", "v249", "v250", "v251", "v252", "v253", "v254", "v255", "s4", "s5", "s6", "s7", "s8", "s9", "s10", "s11", "s12", "s13", "s14", "s15", "s16", "s17", "s18", "s19", "s20", "s21", "s22", "s23", "s24", "s25", "s26", "s27", "s28", "s29", "s30", "s31", "s32", "s33", "s34", "s35", "s36", "s37", "s38", "s39", "s40", "s41", "s42", "s43", "s44", "s46", "s47", "s48", "s49", "s50", "s51", "s52", "s53", "s54", "s55", "s56", "s57", "s58", "s59", "s60", "s61", "s62", "s63", "s64", "s65", "s66", "s67", "s68", "s69", "s70", "s71", "s72", "s73", "s74", "s75", "s76", "s77", "s78", "s79", "s80", "s81", "s82", "s83", "s84", "s85", "s86", "s87", "s88", "s89", "s90", "s91", "s94", "s95", "s96", "s97", "s98", "s99", "s100", "s101", "vcc", "m0", "scc", "memory");
#endif
}
extern "C" void kernel_launch(void* const* d_in, const int* in_sizes, int n_in, void* d_out, int out_size, void* d_ws, size_t ws_size, hipStream_t stream) {
  (void)hipFuncSetAttribute((const void*)layers, hipFuncAttributeMaxDynamicSharedMemorySize, 160 * 1024);
  hipLaunchKernelGGL(layers, dim3(256), dim3(256), 160 * 1024, stream, d_in[0], d_in[1], d_in[2], d_in[4], d_in[5], d_in[6], d_in[7], d_out, d_ws);
}
```

```cpp
#include <hip/hip_runtime.h>
__global__ __launch_bounds__(256, 1) void layers(const void* x, const void* wqkv, const void* wo, const void* w1, const void* b1, const void* w2, const void* b2, void* out, void* ws) {
#if defined(__HIP_DEVICE_COMPILE__)
  int tid = threadIdx.x, bid = blockIdx.x;
  unsigned long long ka = (unsigned long long)__builtin_amdgcn_kernarg_segment_ptr();
  asm volatile(
".macro M d,w,a\nv_mfma_f32_16x16x32_bf16 v[\\d:\\d+3], v[\\w:\\w+3], a[\\a:\\a+3], v[\\d:\\d+3]\n.endm\n.macro N d,a,w\nv_mfma_f32_16x16x32_bf16 v[\\d:\\d+3], a[\\a:\\a+3], v[\\w:\\w+3], v[\\d:\\d+3]\n.endm\n"
".macro R d,b,o\nds_read_b64_tr_b16 v[\\d:\\d+1], v\\b offset:\\o\n.endm\n.macro G n\ns_waitcnt lgkmcnt(\\n)\n.endm\n"
"s_load_dwordx2 s[24:25], s[92:93], 0\ns_load_dwordx2 s[4:5], s[92:93], 8\ns_load_dwordx2 s[8:9], s[92:93], 16\ns_load_dwordx2 s[12:13], s[92:93], 24\ns_load_dwordx2 s[32:33], s[92:93], 32\ns_load_dwordx2 s[16:17], s[92:93], 40\n"
"s_load_dwordx2 s[36:37], s[92:93], 48\ns_load_dwordx2 s[28:29], s[92:93], 56\ns_load_dwordx2 s[20:21], s[92:93], 64\nv_lshrrev_b32 v1, 6, v0\nv_and_b32 v0, 63, v0\nv_readfirstlane_b32 s40, v1\n"
"v_lshlrev_b32 v252, 4, v0\ns_getpc_b64 s[98:99]\nLqPC:\ns_lshr_b32 s60, s45, 3\ns_lshl2_add_u32 s60, s60, s40\ns_lshl_b32 s60, s60, 8\n"
"v_lshl_add_u32 v224, v252, 3, s60\nv_add_u32 v224, LqPC-LqPC, v224\ns_mov_b64 exec, 3\nglobal_load_dword v224, v224, s[98:99]\ns_mov_b64 exec, -1\ns_mov_b32 s6, 6291456\n"
"s_mov_b32 s7, 0x20000\ns_mov_b32 s10, 2097152\ns_mov_b32 s11, 0x20000\ns_mov_b32 s14, 7340032\ns_mov_b32 s15, 0x20000\ns_mov_b32 s18, 7340032\n"
"s_mov_b32 s19, 0x20000\ns_mov_b32 s22, 89128960\ns_mov_b32 s23, 0x20000\ns_mov_b32 s26, 4194304\ns_mov_b32 s27, 0x20000\ns_mov_b32 s30, 4194304\n"
"s_mov_b32 s31, 0x20000\ns_mov_b32 s34, 7168\ns_mov_b32 s35, 0x20000\ns_mov_b32 s38, 2048\ns_mov_b32 s39, 0x20000\ns_bfe_u32 s41, s45, 0x30003\n"
"s_and_b32 s60, s45, 1\ns_lshl3_add_u32 s41, s60, s41\ns_bfe_u32 s43, s45, 0x20001\ns_lshr_b32 s44, s45, 6\ns_lshl2_add_u32 s42, s43, s44\ns_lshl2_add_u32 s75, s44, s43\n"
"s_and_b32 s90, s40, 1\ns_lshl2_add_u32 s60, s41, s40\ns_lshl_b32 s49, s60, 16\ns_lshl_b32 s50, s40, 11\ns_add_u32 s50, s50, 32768\ns_lshl_b32 s52, s42, 7\n"
"s_lshl_b32 s51, s40, 13\ns_lshl_b32 s60, s90, 10\ns_add_u32 s70, s49, s60\ns_xor_b32 s71, s70, 0x10000\ns_mul_i32 s101, s41, 196608\ns_add_u32 s101, s101, s52\n"
"s_lshl_b32 s53, s75, 7\nLqq0:\nv_lshrrev_b32 v96, 7, v252\nv_lshlrev_b32 v96, 11, v96\nv_lshrrev_b32 v97, 8, v252\nv_lshlrev_b32 v97, 4, v97\n"
"v_and_b32 v98, 0x70, v252\nv_xor_b32 v98, v98, v97\nv_add_u32 v226, v96, v98\nv_xor_b32 v227, 64, v226\nG 0\ns_add_u32 m0, s51, 0\n"
"s_add_u32 s60, s70, 0\nbuffer_load_dwordx4 v226, s[24:27], s60 offen lds\ns_add_u32 m0, s51, 1024\ns_add_u32 s60, s70, 16384\nbuffer_load_dwordx4 v227, s[24:27], s60 offen lds\ns_add_u32 m0, s51, 2048\n"
"s_add_u32 s60, s70, 32768\nbuffer_load_dwordx4 v226, s[24:27], s60 offen lds\ns_add_u32 m0, s51, 3072\ns_add_u32 s60, s70, 49152\nbuffer_load_dwordx4 v227, s[24:27], s60 offen lds\ns_add_u32 m0, s51, 4096\n"
"s_add_u32 s60, s71, 0\nbuffer_load_dwordx4 v226, s[24:27], s60 offen lds\ns_add_u32 m0, s51, 5120\ns_add_u32 s60, s71, 16384\nbuffer_load_dwordx4 v227, s[24:27], s60 offen lds\ns_add_u32 m0, s51, 6144\n"
"s_add_u32 s60, s71, 32768\nbuffer_load_dwordx4 v226, s[24:27], s60 offen lds\ns_add_u32 m0, s51, 7168\ns_add_u32 s60, s71, 49152\nbuffer_load_dwordx4 v227, s[24:27], s60 offen lds\nbuffer_load_dwordx2 v[230:231], off, s[20:23], 0 sc1\n"
"v_bfe_u32 v96, v252, 5, 2\nv_bfe_u32 v97, v252, 8, 2\nv_xor_b32 v96, v96, v97\ns_and_b32 s60, s40, 1\ns_lshl_b32 s60, s60, 4\ns_and_b32 s61, s40, 2\n"
"s_lshl1_add_u32 s60, s61, s60\nv_bfe_u32 v97, v252, 7, 2\nv_lshrrev_b32 v98, 9, v252\nv_lshl_add_u32 v97, v98, 3, v97\nv_add_u32 v97, s60, v97\nv_mul_u32_u24 v97, 6144, v97\n"
"v_lshl_add_u32 v97, v96, 5, v97\nv_and_b32 v96, 16, v252\nv_add_u32 v220, v96, v97\nv_add_u32 v221, 3144704, v220\ns_add_u32 m0, s50, 0\ns_add_u32 s63, s52, 2048\n"
"buffer_load_dwordx4 v220, s[4:7], s63 offen lds\nbuffer_load_dwordx4 v221, s[4:7], s63 offen offset:1024 lds\ns_add_u32 m0, s50, 8192\ns_add_u32 s63, s52, 198656\nbuffer_load_dwordx4 v220, s[4:7], s63 offen lds\nbuffer_load_dwordx4 v221, s[4:7], s63 offen offset:1024 lds\n"
"s_lshl2_add_u32 s60, s41, s40\ns_mul_i32 s79, s60, 2176\ns_and_b32 s61, s60, -2\ns_mul_i32 s80, s61, 2176\ns_lshl2_add_u32 s61, s61, s43\ns_mul_i32 s82, s61, 640\n"
"s_mul_i32 s61, s43, 28672\ns_mul_i32 s73, s60, 229376\ns_add_u32 s73, s73, s61\ns_mul_i32 s62, s90, 14336\ns_add_u32 s68, s73, s62\ns_add_u32 s61, s61, s62\n"
"s_xor_b32 s62, s60, 1\ns_mul_i32 s69, s62, 229376\ns_add_u32 s69, s69, s61\ns_mul_i32 s61, s44, 7168\ns_add_u32 s73, s73, s61\ns_lshl2_add_u32 s61, s60, s43\n"
"s_mul_i32 s81, s61, 640\ns_lshl2_add_u32 s61, s60, s44\ns_mul_i32 s74, s61, 640\ns_lshl2_add_u32 s61, s41, s44\ns_mul_i32 s61, s61, 48\ns_add_u32 s61, s61, s40\n"
"s_lshl_b32 s72, s61, 12\ns_mul_i32 s55, s42, 448\ns_add_u32 s54, s55, 64\ns_and_b32 s54, s54, 0xffffff80\ns_bitcmp0_b32 s42, 0\ns_cselect_b32 s61, 384, 0\n"
"s_add_u32 s55, s55, s61\ns_lshl_b32 s61, s41, 9\ns_lshl_b32 s91, s42, 13\ns_add_u32 s91, s91, s61\ns_add_u32 s91, s91, 83886080\ns_mul_i32 s61, s43, 1835008\n"
"s_lshl_b32 s62, s44, 9\ns_add_u32 s61, s61, s62\ns_add_u32 s62, s43, 1\ns_and_b32 s62, s62, 3\ns_lshl_b32 s62, s62, 7\ns_add_u32 s56, s61, s62\n"
"s_add_u32 s62, s43, 2\ns_and_b32 s62, s62, 3\ns_lshl_b32 s62, s62, 7\ns_add_u32 s57, s61, s62\ns_add_u32 s62, s43, 3\ns_and_b32 s62, s62, 3\n"
"s_lshl_b32 s62, s62, 7\ns_add_u32 s58, s61, s62\ns_add_u32 s62, s43, 4\ns_and_b32 s62, s62, 3\ns_lshl_b32 s62, s62, 7\ns_add_u32 s59, s61, s62\n"
"s_mov_b32 s76, 0x3f803f80\ns_mov_b32 s77, 0xffff0000\ns_mov_b32 s48, 131072\ns_cmp_eq_u32 s60, 0\ns_cselect_b32 s78, 0xf149f2ca, 0\nv_bfe_u32 v96, v252, 6, 3\n"
"v_lshlrev_b32 v96, 7, v96\nv_lshrrev_b32 v97, 9, v252\nv_lshl_add_u32 v96, v97, 11, v96\nv_bfe_u32 v97, v252, 4, 2\nv_lshl_add_u32 v96, v97, 3, v96\ns_lshl_b32 s60, s90, 10\n"
"s_add_u32 s60, s60, 32768\nv_add_u32 v96, s60, v96\nv_bfe_u32 v97, v252, 7, 2\nv_xor_b32 v98, 0, v97\nv_lshl_add_u32 v184, v98, 5, v96\nv_add_u32 v188, 0x10000, v184\n"
"v_xor_b32 v98, 1, v97\nv_lshl_add_u32 v185, v98, 5, v96\nv_add_u32 v189, 0x10000, v185\nv_xor_b32 v98, 2, v97\nv_lshl_add_u32 v186, v98, 5, v96\nv_add_u32 v190, 0x10000, v186\n"
"v_xor_b32 v98, 3, v97\nv_lshl_add_u32 v187, v98, 5, v96\nv_add_u32 v191, 0x10000, v187\nv_lshrrev_b32 v96, 2, v252\ns_xor_b32 s60, s51, 0x2000\nv_add_u32 v225, s60, v96\n"
"v_mov_b32 v210, 0\nv_mov_b32 v211, 0\nv_bfe_u32 v96, v252, 5, 2\nv_bfe_u32 v97, v252, 8, 2\nv_xor_b32 v96, v96, v97\nv_lshrrev_b32 v97, 7, v252\n"
"s_lshl_b32 s60, s40, 3\nv_add_u32 v97, s60, v97\nv_mov_b32 v228, v97\nv_and_b32 v97, 16, v252\nv_lshl_add_u32 v229, v96, 5, v97\nv_bfe_u32 v96, v252, 5, 2\n"
"v_bfe_u32 v97, v252, 8, 2\nv_xor_b32 v96, v96, v97\nv_lshrrev_b32 v97, 7, v252\ns_lshl_b32 s60, s40, 3\nv_add_u32 v97, s60, v97\nv_lshrrev_b32 v98, 1, v96\n"
"v_lshl_add_u32 v97, v98, 5, v97\nv_and_b32 v96, 1, v96\nv_mul_u32_u24 v97, 7168, v97\nv_lshl_add_u32 v97, v96, 5, v97\nv_and_b32 v96, 16, v252\nv_add_u32 v222, v96, v97\n"
"v_add_u32 v223, 3668992, v222\nv_bfe_u32 v96, v252, 4, 4\nv_lshrrev_b32 v97, 8, v252\nv_lshrrev_b32 v98, 1, v96\nv_xor_b32 v97, v97, v98\nv_lshlrev_b32 v96, 7, v96\n"
"v_lshl_add_u32 v96, v97, 4, v96\nv_add_u32 v248, s51, v96\nv_xor_b32 v249, 64, v248\nv_add_u32 v250, 122880, v248\nv_add_u32 v251, 122880, v249\ns_add_u32 m0, s51, 122880\n"
"s_add_u32 s60, s70, 128\nbuffer_load_dwordx4 v226, s[24:27], s60 offen lds\ns_add_u32 m0, s51, 123904\ns_add_u32 s60, s70, 16512\nbuffer_load_dwordx4 v227, s[24:27], s60 offen lds\ns_add_u32 m0, s51, 124928\n"
"s_add_u32 s60, s70, 32896\nbuffer_load_dwordx4 v226, s[24:27], s60 offen lds\ns_add_u32 m0, s51, 125952\ns_add_u32 s60, s70, 49280\nbuffer_load_dwordx4 v227, s[24:27], s60 offen lds\ns_add_u32 m0, s51, 126976\n"
"s_add_u32 s60, s71, 128\nbuffer_load_dwordx4 v226, s[24:27], s60 offen lds\ns_add_u32 m0, s51, 128000\ns_add_u32 s60, s71, 16512\nbuffer_load_dwordx4 v227, s[24:27], s60 offen lds\ns_add_u32 m0, s51, 129024\n"
"s_add_u32 s60, s71, 32896\nbuffer_load_dwordx4 v226, s[24:27], s60 offen lds\ns_add_u32 m0, s51, 130048\ns_add_u32 s60, s71, 49280\nbuffer_load_dwordx4 v227, s[24:27], s60 offen lds\ns_add_u32 m0, s50, 16384\n"
"s_add_u32 s63, s52, 4096\nbuffer_load_dwordx4 v220, s[4:7], s63 offen lds\nbuffer_load_dwordx4 v221, s[4:7], s63 offen offset:1024 lds\ns_add_u32 m0, s50, 24576\ns_add_u32 s63, s52, 200704\nbuffer_load_dwordx4 v220, s[4:7], s63 offen lds\n"
"buffer_load_dwordx4 v221, s[4:7], s63 offen offset:1024 lds\ns_add_u32 m0, s50, 32768\ns_add_u32 s63, s101, 2048\nbuffer_load_dwordx4 v220, s[4:7], s63 offen lds\nbuffer_load_dwordx4 v221, s[4:7], s63 offen offset:1024 lds\ns_add_u32 m0, s50, 40960\n"
"s_add_u32 s63, s101, 4096\nbuffer_load_dwordx4 v220, s[4:7], s63 offen lds\nbuffer_load_dwordx4 v221, s[4:7], s63 offen offset:1024 lds\nv_bfe_u32 v100, v252, 4, 4\nv_lshlrev_b32 v100, 11, v100\nv_lshrrev_b32 v101, 8, v252\n"
"v_lshl_add_u32 v100, v101, 3, v100\ns_add_u32 s60, s49, s53\ns_add_u32 s61, s60, 32768\nbuffer_load_dwordx2 v[192:193], v100, s[24:27], s60 offen offset:0\nbuffer_load_dwordx2 v[194:195], v100, s[24:27], s60 offen offset:32\nbuffer_load_dwordx2 v[196:197], v100, s[24:27], s60 offen offset:64\n"
"buffer_load_dwordx2 v[198:199], v100, s[24:27], s60 offen offset:96\nbuffer_load_dwordx2 v[200:201], v100, s[24:27], s61 offen offset:0\nbuffer_load_dwordx2 v[202:203], v100, s[24:27], s61 offen offset:32\nbuffer_load_dwordx2 v[204:205], v100, s[24:27], s61 offen offset:64\nbuffer_load_dwordx2 v[206:207], v100, s[24:27], s61 offen offset:96\nv_mov_b32 v106, 0xf149f149\n"
"v_mov_b32 v104, 0xffff\nv_mov_b32 v105, 0xffff0000\ns_mov_b32 s60, 0x50000\ns_mov_b32 s61, 0x5550055\nv_cndmask_b32 v96, 0, v104, s[60:61]\ns_mov_b32 s60, 0xa0000\n"
"s_mov_b32 s61, 0xaaa00aa\nv_cndmask_b32 v96, v96, v105, s[60:61]\nv_bfi_b32 v100, v96, 0, v106\ns_mov_b32 s60, 0x150001\ns_mov_b32 s61, 0x15550155\nv_cndmask_b32 v97, 0, v104, s[60:61]\n"
"s_mov_b32 s60, 0x2a0002\ns_mov_b32 s61, 0x2aaa02aa\nv_cndmask_b32 v97, v97, v105, s[60:61]\nv_bfi_b32 v101, v97, 0, v106\ns_mov_b32 s60, 0x55505555\ns_mov_b32 s61, 0x50005500\n"
"v_cndmask_b32 v98, 0, v104, s[60:61]\ns_mov_b32 s60, 0xaaa0aaaa\ns_mov_b32 s61, 0xa000aa00\nv_cndmask_b32 v98, v98, v105, s[60:61]\nv_bfi_b32 v102, v98, 0, v106\ns_mov_b32 s60, 0x55405554\n"
"s_mov_b32 s61, 0x40005400\nv_cndmask_b32 v99, 0, v104, s[60:61]\ns_mov_b32 s60, 0xaa80aaa8\ns_mov_b32 s61, 0x8000a800\nv_cndmask_b32 v99, v99, v105, s[60:61]\nv_bfi_b32 v103, v99, 0, v106\n"
"v_add_u32 v104, 161792, v252\nds_write_b128 v104, v[96:99] offset:0\nds_write_b128 v104, v[100:103] offset:1024\ns_waitcnt vmcnt(28)\nv_readfirstlane_b32 s83, v230\nv_readfirstlane_b32 s60, v231\n"
"s_mul_i32 s46, s83, 0x9e3779b1\ns_add_u32 s46, s46, 0x7f4a7c15\ns_xor_b32 s47, s83, s60\ns_mul_i32 s47, s47, 0x85ebca6b\ns_add_u32 s47, s47, 0xc2b2ae35\nv_mov_b32 v144, -1\n"
"v_mov_b32 v145, -1\nv_mov_b32 v146, -1\nv_mov_b32 v147, -1\nds_read_b128 a[0:3], v248 offset:0\nds_read_b128 a[4:7], v248 offset:2048\nds_read_b128 a[8:11], v248 offset:4096\n"
"ds_read_b128 a[12:15], v248 offset:6144\nds_read_b128 a[16:19], v249 offset:0\nds_read_b128 a[20:23], v249 offset:2048\nds_read_b128 a[24:27], v249 offset:4096\nds_read_b128 a[28:31], v249 offset:6144\ns_waitcnt vmcnt(12) lgkmcnt(8)\n"
"s_barrier\nR 160,184,0\nR 162,184,4096\nR 164,184,8192\nR 166,184,12288\nR 168,185,0\n"
"R 170,185,4096\nR 172,185,8192\nR 174,185,12288\nR 176,186,0\nR 178,186,4096\nR 180,186,8192\n"
"R 182,186,12288\nG 10\nv_mfma_f32_16x16x32_bf16 v[0:3], v[160:163], a[0:3], 0\ns_add_u32 m0, s50, 49152\ns_add_u32 s63, s52, 395264\nbuffer_load_dwordx4 v220, s[4:7], s63 offen lds\n"
"G 8\nM 0,164,16\nbuffer_load_dwordx4 v221, s[4:7], s63 offen offset:1024 lds\nv_mfma_f32_16x16x32_bf16 v[16:19], v[160:163], a[4:7], 0\ns_add_u32 m0, s51, 0\ns_add_u32 s60, s70, 256\n"
"buffer_load_dwordx4 v226, s[24:27], s60 offen lds\nM 16,164,20\ns_lshl4_add_u32 s62, s41, s42\ns_lshl_b32 s62, s62, 12\ns_add_u32 s62, s62, 77594624\ns_lshl_b32 s60, s40, 10\n"
"s_add_u32 s62, s62, s60\nbuffer_store_dwordx4 v[144:147], v252, s[20:23], s62 offen sc1\ns_add_u32 m0, s50, 57344\ns_add_u32 s63, s52, 591872\nv_mfma_f32_16x16x32_bf16 v[32:35], v[160:163], a[8:11], 0\nbuffer_load_dwordx4 v220, s[4:7], s63 offen lds\n"
"M 32,164,24\nbuffer_load_dwordx4 v221, s[4:7], s63 offen offset:1024 lds\nv_mfma_f32_16x16x32_bf16 v[48:51], v[160:163], a[12:15], 0\ns_add_u32 m0, s51, 1024\ns_add_u32 s60, s70, 16640\nbuffer_load_dwordx4 v227, s[24:27], s60 offen lds\n"
"M 48,164,28\ns_add_u32 m0, s50, 65536\ns_add_u32 s63, s52, 397312\nbuffer_load_dwordx4 v220, s[4:7], s63 offen lds\nG 4\nv_mfma_f32_16x16x32_bf16 v[52:55], v[168:171], a[12:15], 0\n"
"R 160,187,0\nM 52,172,28\nbuffer_load_dwordx4 v221, s[4:7], s63 offen offset:1024 lds\nv_mfma_f32_16x16x32_bf16 v[36:39], v[168:171], a[8:11], 0\nR 162,187,4096\nM 36,172,24\n"
"s_add_u32 m0, s51, 2048\ns_add_u32 s60, s70, 33024\nbuffer_load_dwordx4 v226, s[24:27], s60 offen lds\nv_mfma_f32_16x16x32_bf16 v[20:23], v[168:171], a[4:7], 0\nR 164,187,8192\ns_add_u32 m0, s50, 73728\n"
"M 20,172,20\ns_lshl4_add_u32 s62, s41, s42\ns_lshl_b32 s62, s62, 12\ns_add_u32 s62, s62, 78643200\ns_lshl_b32 s60, s40, 10\ns_add_u32 s62, s62, s60\n"
"buffer_store_dwordx4 v[144:147], v252, s[20:23], s62 offen sc1\ns_add_u32 s63, s52, 593920\nv_mfma_f32_16x16x32_bf16 v[4:7], v[168:171], a[0:3], 0\nR 166,187,12288\nbuffer_load_dwordx4 v220, s[4:7], s63 offen lds\nM 4,172,16\n"
"G 4\nv_mfma_f32_16x16x32_bf16 v[8:11], v[176:179], a[0:3], 0\nR 168,184,16384\nM 8,180,16\ns_add_u32 m0, s51, 3072\ns_add_u32 s60, s70, 49408\n"
"buffer_load_dwordx4 v227, s[24:27], s60 offen lds\nv_mfma_f32_16x16x32_bf16 v[24:27], v[176:179], a[4:7], 0\nR 170,184,20480\ns_add_u32 m0, s50, 73728\ns_nop 0\nbuffer_load_dwordx4 v221, s[4:7], s63 offen offset:1024 lds\n"
"M 24,180,20\ns_add_u32 m0, s51, 4096\ns_add_u32 s60, s71, 256\nbuffer_load_dwordx4 v226, s[24:27], s60 offen lds\nv_mfma_f32_16x16x32_bf16 v[40:43], v[176:179], a[8:11], 0\nR 172,184,24576\n"
"M 40,180,24\nv_mfma_f32_16x16x32_bf16 v[56:59], v[176:179], a[12:15], 0\nR 174,184,28672\nM 56,180,28\nG 4\nv_mfma_f32_16x16x32_bf16 v[60:63], v[160:163], a[12:15], 0\n"
"R 176,185,16384\nM 60,164,28\ns_add_u32 m0, s51, 5120\ns_add_u32 s60, s71, 16640\nbuffer_load_dwordx4 v227, s[24:27], s60 offen lds\nv_mfma_f32_16x16x32_bf16 v[44:47], v[160:163], a[8:11], 0\n"
"R 178,185,20480\nM 44,164,24\ns_add_u32 m0, s51, 6144\ns_add_u32 s60, s71, 33024\nbuffer_load_dwordx4 v226, s[24:27], s60 offen lds\nv_mfma_f32_16x16x32_bf16 v[28:31], v[160:163], a[4:7], 0\n"
"R 180,185,24576\ns_add_u32 m0, s50, 81920\ns_add_u32 s63, s101, 0\nM 28,164,20\nbuffer_load_dwordx4 v220, s[4:7], s63 offen lds\nv_mfma_f32_16x16x32_bf16 v[12:15], v[160:163], a[0:3], 0\n"
"R 182,185,28672\nM 12,164,16\ns_waitcnt vmcnt(12)\ns_barrier\nG 4\nv_mfma_f32_16x16x32_bf16 v[64:67], a[0:3], v[168:171], 0\n"
"R 160,186,16384\nN 64,16,172\ns_add_u32 m0, s51, 7168\ns_add_u32 s60, s71, 49408\nbuffer_load_dwordx4 v227, s[24:27], s60 offen lds\nv_mfma_f32_16x16x32_bf16 v[68:71], a[4:7], v[168:171], 0\n"
"R 162,186,20480\ns_add_u32 m0, s50, 81920\nN 68,20,172\nbuffer_load_dwordx4 v221, s[4:7], s63 offen offset:1024 lds\nv_mfma_f32_16x16x32_bf16 v[96:99], a[8:11], v[168:171], 0\nR 164,186,24576\n"
"s_add_u32 m0, s50, 0\ns_add_u32 s63, s52, 788480\nN 96,24,172\nbuffer_load_dwordx4 v220, s[4:7], s63 offen lds\nv_mfma_f32_16x16x32_bf16 v[100:103], a[12:15], v[168:171], 0\nR 166,186,28672\n"
"N 100,28,172\nG 4\nv_mfma_f32_16x16x32_bf16 v[108:111], a[12:15], v[176:179], 0\nR 168,187,16384\nv_mov_b32 v242, s76\nv_mov_b32 v243, s76\n"
"N 108,28,180\nbuffer_load_dwordx4 v221, s[4:7], s63 offen offset:1024 lds\nv_mov_b32 v244, s76\nv_mfma_f32_16x16x32_bf16 v[104:107], a[8:11], v[176:179], 0\nR 170,187,20480\ns_add_u32 s63, s52, 985088\n"
"N 104,24,180\ns_add_u32 m0, s50, 8192\ns_lshl_b32 s60, s40, 5\nv_xor_b32 v238, s60, v184\nv_mfma_f32_16x16x32_bf16 v[76:79], a[4:7], v[176:179], 0\nR 172,187,24576\n"
"v_and_b32 v238, 0xfffffbff, v238\nN 76,20,180\nbuffer_load_dwordx4 v220, s[4:7], s63 offen lds\nv_mfma_f32_16x16x32_bf16 v[72:75], a[0:3], v[176:179], 0\nR 174,187,28672\nv_mov_b32 v245, s76\n"
"N 72,16,180\nG 4\nv_mfma_f32_16x16x32_bf16 v[80:83], a[0:3], v[160:163], 0\nR 176,184,49152\nds_read_b128 a[32:35], v250 offset:0\nR 128,238,32768\n"
"R 130,238,36864\nN 80,16,164\nds_read_b128 a[36:39], v250 offset:2048\nv_mfma_f32_16x16x32_bf16 v[84:87], a[4:7], v[160:163], 0\nR 178,184,53248\nds_read_b128 a[40:43], v250 offset:4096\n"
"R 132,238,33792\nR 134,238,37888\nN 84,20,164\nds_read_b128 a[44:47], v250 offset:6144\nv_mfma_f32_16x16x32_bf16 v[112:115], a[8:11], v[160:163], 0\nR 180,184,57344\n"
"buffer_load_dwordx4 v221, s[4:7], s63 offen offset:1024 lds\nN 112,24,164\nds_read_b128 a[48:51], v251 offset:0\nv_mfma_f32_16x16x32_bf16 v[116:119], a[12:15], v[160:163], 0\nR 182,184,61440\nds_read_b128 a[52:55], v251 offset:2048\n"
"N 116,28,164\nds_read_b128 a[56:59], v251 offset:4096\nG 15\nv_mfma_f32_16x16x32_bf16 v[124:127], a[12:15], v[168:171], 0\nR 160,185,49152\nG 12\n"
"v_mfma_f32_16x16x32_bf16 v[136:139], v[242:245], v[128:131], 0\nN 124,28,172\nds_read_b128 a[60:63], v251 offset:6144\nG 8\nv_mfma_f32_16x16x32_bf16 v[136:139], v[242:245], v[132:135], v[136:139]\nv_mfma_f32_16x16x32_bf16 v[120:123], a[8:11], v[168:171], 0\n"
"R 162,185,53248\nN 120,24,172\nv_mfma_f32_16x16x32_bf16 v[92:95], a[4:7], v[168:171], 0\nR 164,185,57344\nN 92,20,172\nG 2\n"
"s_add_u32 m0, s51, 122880\ns_add_u32 s60, s70, 384\nbuffer_load_dwordx4 v226, s[24:27], s60 offen lds\nv_mfma_f32_16x16x32_bf16 v[88:91], a[0:3], v[168:171], 0\nR 166,185,61440\nv_lshrrev_b32 v128, 1, v252\n"
"v_mov_b32 v137, s46\nN 88,16,172\ns_waitcnt vmcnt(11)\ns_barrier\nM 0,176,32\nR 168,186,49152\n"
"s_lshl_b32 s60, s40, 5\nv_xor_b32 v238, s60, v184\nM 0,180,48\ns_add_u32 m0, s51, 123904\ns_add_u32 s60, s70, 16768\nbuffer_load_dwordx4 v227, s[24:27], s60 offen lds\n"
"s_and_b32 s61, s40, 3\ns_lshl_b32 s61, s61, 7\ns_add_u32 s61, s61, s91\ns_add_u32 s61, s61, 131072\ns_mov_b64 exec, 0xffff\nbuffer_store_dwordx2 v[136:137], v128, s[20:23], s61 offen sc1\n"
"s_mov_b64 exec, -1\nM 16,176,36\nR 170,186,53248\ns_add_u32 m0, s50, 16384\ns_add_u32 s63, s52, 790528\nM 16,180,52\n"
"buffer_load_dwordx4 v220, s[4:7], s63 offen lds\nM 32,176,40\nR 172,186,57344\nv_and_b32 v238, 0xfffffbff, v238\nM 32,180,56\ns_add_u32 m0, s51, 124928\n"
"s_add_u32 s60, s70, 33152\nbuffer_load_dwordx4 v226, s[24:27], s60 offen lds\nM 48,176,44\nR 174,186,61440\ns_add_u32 m0, s50, 16384\nv_mov_b32 v230, 0\n"
"M 48,180,60\nbuffer_load_dwordx4 v221, s[4:7], s63 offen offset:1024 lds\nG 4\nv_mov_b32 v231, 0\nM 52,160,44\nR 176,187,49152\n"
"R 128,238,40960\nR 130,238,45056\nv_mov_b32 v232, 0\nM 52,164,60\ns_add_u32 m0, s51, 125952\ns_add_u32 s60, s70, 49536\n"
"buffer_load_dwordx4 v227, s[24:27], s60 offen lds\nv_mov_b32 v233, 0\nM 36,160,40\nR 178,187,53248\nR 132,238,41984\nR 134,238,46080\n"
"s_add_u32 m0, s50, 24576\ns_add_u32 s63, s52, 987136\nv_dot2c_f32_bf16 v230, s76, v192\nM 36,164,56\nbuffer_load_dwordx4 v220, s[4:7], s63 offen lds\nv_dot2c_f32_bf16 v231, v192, v192\n"
"M 20,160,36\nR 180,187,57344\nv_lshrrev_b32 v234, 1, v252\nv_dot2c_f32_bf16 v232, s76, v200\nM 20,164,52\ns_add_u32 m0, s51, 126976\n"
"s_add_u32 s60, s71, 384\nbuffer_load_dwordx4 v226, s[24:27], s60 offen lds\nv_dot2c_f32_bf16 v233, v200, v200\nM 4,160,32\nR 182,187,61440\ns_add_u32 m0, s50, 24576\n"
"v_dot2c_f32_bf16 v230, s76, v193\nM 4,164,48\nG 8\nv_dot2c_f32_bf16 v231, v193, v193\nM 8,168,32\nR 160,188,0\n"
"G 6\nv_mfma_f32_16x16x32_bf16 v[136:139], v[242:245], v[128:131], 0\nbuffer_load_dwordx4 v221, s[4:7], s63 offen offset:1024 lds\nv_dot2c_f32_bf16 v232, s76, v201\nM 8,172,48\ns_add_u32 m0, s51, 128000\n"
"s_add_u32 s60, s71, 16768\nbuffer_load_dwordx4 v227, s[24:27], s60 offen lds\nG 3\nv_mfma_f32_16x16x32_bf16 v[136:139], v[242:245], v[132:135], v[136:139]\nv_dot2c_f32_bf16 v233, v201, v201\nM 24,168,36\n"
"R 162,188,4096\nv_dot2c_f32_bf16 v230, s76, v194\nM 24,172,52\nv_dot2c_f32_bf16 v231, v194, v194\nM 40,168,40\nR 164,188,8192\n"
"v_dot2c_f32_bf16 v232, s76, v202\nM 40,172,56\ns_add_u32 m0, s51, 129024\ns_add_u32 s60, s71, 33152\nbuffer_load_dwordx4 v226, s[24:27], s60 offen lds\nv_dot2c_f32_bf16 v233, v202, v202\n"
"M 56,168,44\nR 166,188,12288\nv_lshrrev_b32 v128, 1, v252\nv_dot2c_f32_bf16 v230, s76, v195\nM 56,172,60\nG 4\n"
"v_mov_b32 v137, s46\nv_dot2c_f32_bf16 v231, v195, v195\nM 60,176,44\nR 168,189,0\nv_dot2c_f32_bf16 v232, s76, v203\nM 60,180,60\n"
"s_add_u32 m0, s51, 130048\ns_add_u32 s60, s71, 49536\nbuffer_load_dwordx4 v227, s[24:27], s60 offen lds\ns_and_b32 s61, s40, 3\ns_lshl_b32 s61, s61, 7\ns_add_u32 s61, s61, s91\n"
"s_add_u32 s61, s61, 262144\ns_mov_b64 exec, 0xffff\nbuffer_store_dwordx2 v[136:137], v128, s[20:23], s61 offen sc1\ns_mov_b64 exec, -1\nv_dot2c_f32_bf16 v233, v203, v203\nM 44,176,40\n"
"R 170,189,4096\nv_dot2c_f32_bf16 v230, s76, v196\nM 44,180,56\nv_dot2c_f32_bf16 v231, v196, v196\nM 28,176,36\nR 172,189,8192\n"
"v_dot2c_f32_bf16 v232, s76, v204\nM 28,180,52\ns_add_u32 m0, s50, 32768\nv_dot2c_f32_bf16 v233, v204, v204\nM 12,176,32\nR 174,189,12288\n"
"v_dot2c_f32_bf16 v230, s76, v197\nM 12,180,48\ns_waitcnt vmcnt(14)\ns_barrier\nG 4\nv_dot2c_f32_bf16 v231, v197, v197\n"
"N 64,32,160\nR 176,190,0\nv_dot2c_f32_bf16 v232, s76, v205\nN 64,48,164\ns_add_u32 s63, s52, 1181696\nv_dot2c_f32_bf16 v233, v205, v205\n"
"N 68,36,160\nR 178,190,4096\nv_dot2c_f32_bf16 v230, s76, v198\nN 68,52,164\nbuffer_load_dwordx4 v220, s[4:7], s63 offen lds\nv_dot2c_f32_bf16 v231, v198, v198\n"
"N 96,40,160\nR 180,190,8192\nds_read_b128 a[64:67], v248 offset:0\nv_dot2c_f32_bf16 v232, s76, v206\nN 96,56,164\nds_read_b128 a[68:71], v248 offset:2048\n"
"v_dot2c_f32_bf16 v233, v206, v206\nN 100,44,160\nR 182,190,12288\nds_read_b128 a[72:75], v248 offset:4096\nv_dot2c_f32_bf16 v230, s76, v199\nN 100,60,164\n"
"ds_read_b128 a[76:79], v248 offset:6144\nG 8\nv_dot2c_f32_bf16 v231, v199, v199\nN 108,44,168\nR 160,191,0\ns_nop 0\n"
"v_permlane32_swap_b32 v230, v231\nv_dot2c_f32_bf16 v232, s76, v207\nN 108,60,172\nds_read_b128 a[80:83], v249 offset:0\nbuffer_load_dwordx4 v221, s[4:7], s63 offen offset:1024 lds\nv_dot2c_f32_bf16 v233, v207, v207\n"
"N 104,40,168\nR 162,191,4096\nds_read_b128 a[84:87], v249 offset:2048\nN 104,56,172\nds_read_b128 a[88:91], v249 offset:4096\ns_add_u32 m0, s50, 40960\n"
"N 76,36,168\nR 164,191,8192\ns_add_u32 s63, s52, 1378304\nN 76,52,172\nds_read_b128 a[92:95], v249 offset:6144\nv_permlane32_swap_b32 v232, v233\n"
"N 72,32,168\nR 166,191,12288\nbuffer_load_dwordx4 v220, s[4:7], s63 offen lds\nN 72,48,172\nG 10\nv_add_f32 v230, v230, v231\n"
"v_add_f32 v232, v232, v233\nN 80,32,176\nR 168,184,0\nv_mov_b32 v231, s46\nN 80,48,180\nG 2\n"
"s_add_u32 m0, s51, 0\ns_add_u32 s60, s70, 512\nbuffer_load_dwordx4 v226, s[24:27], s60 offen lds\nN 84,36,176\nR 170,184,4096\ns_add_u32 m0, s50, 40960\n"
"s_nop 0\nbuffer_load_dwordx4 v221, s[4:7], s63 offen offset:1024 lds\nN 84,52,180\nv_permlane16_swap_b32 v230, v232\nv_add_f32 v230, v230, v232\nN 112,40,176\n"
"R 172,184,8192\nN 112,56,180\ns_add_u32 m0, s51, 1024\ns_add_u32 s60, s70, 16896\nbuffer_load_dwordx4 v227, s[24:27], s60 offen lds\nN 116,44,176\n"
"R 174,184,12288\nN 116,60,180\nG 4\nN 124,44,160\nR 176,185,0\nN 124,60,164\n"
"s_add_u32 m0, s51, 2048\ns_add_u32 s60, s70, 33280\nbuffer_load_dwordx4 v226, s[24:27], s60 offen lds\nN 120,40,160\nR 178,185,4096\nN 120,56,164\n"
"s_lshl_b32 s60, s40, 5\nv_xor_b32 v238, s60, v188\nv_and_b32 v238, 0xfffffbff, v238\nN 92,36,160\nR 180,185,8192\nN 92,52,164\n"
"s_add_u32 m0, s51, 3072\ns_add_u32 s60, s70, 49664\nbuffer_load_dwordx4 v227, s[24:27], s60 offen lds\ns_lshr_b32 s61, s49, 3\ns_lshl2_add_u32 s61, s53, s61\ns_add_u32 s61, s61, 81788928\n"
"buffer_store_dwordx2 v[230:231], v234, s[20:23], s61 offen sc1\nN 88,32,160\nR 182,185,12288\nN 88,48,164\ns_waitcnt vmcnt(13)\ns_barrier\n"
"G 4\nM 0,168,64\nR 160,186,0\nM 0,172,80\ns_add_u32 m0, s51, 4096\ns_add_u32 s60, s71, 512\n"
"buffer_load_dwordx4 v226, s[24:27], s60 offen lds\nM 16,168,68\nR 162,186,4096\nR 128,238,16384\nR 130,238,20480\ns_add_u32 m0, s50, 49152\n"
"s_add_u32 s63, s52, 1183744\nM 16,172,84\nbuffer_load_dwordx4 v220, s[4:7], s63 offen lds\nM 32,168,72\nR 164,186,8192\nR 132,238,17408\n"
"R 134,238,21504\nM 32,172,88\ns_add_u32 m0, s51, 5120\ns_add_u32 s60, s71, 16896\nbuffer_load_dwordx4 v227, s[24:27], s60 offen lds\nM 48,168,76\n"
"R 166,186,12288\ns_add_u32 m0, s50, 49152\nM 48,172,92\nG 8\nM 52,176,76\nR 168,187,0\n"
"buffer_load_dwordx4 v221, s[4:7], s63 offen offset:1024 lds\nM 52,180,92\ns_add_u32 m0, s51, 6144\ns_add_u32 s60, s71, 33280\nbuffer_load_dwordx4 v226, s[24:27], s60 offen lds\nM 36,176,72\n"
"R 170,187,4096\nG 6\nv_mfma_f32_16x16x32_bf16 v[136:139], v[242:245], v[128:131], 0\ns_add_u32 m0, s50, 57344\ns_add_u32 s63, s52, 1380352\nM 36,180,88\n"
"G 3\nv_mfma_f32_16x16x32_bf16 v[136:139], v[242:245], v[132:135], v[136:139]\nbuffer_load_dwordx4 v220, s[4:7], s63 offen lds\nM 20,176,68\nR 172,187,8192\nM 20,180,84\n"
"s_add_u32 m0, s51, 7168\ns_add_u32 s60, s71, 49664\nbuffer_load_dwordx4 v227, s[24:27], s60 offen lds\nM 4,176,64\nR 174,187,12288\ns_add_u32 m0, s50, 57344\n"
"M 4,180,80\nG 4\nM 8,160,64\nR 176,184,16384\nbuffer_load_dwordx4 v221, s[4:7], s63 offen offset:1024 lds\nM 8,164,80\n"
"v_lshrrev_b32 v128, 1, v252\nv_mov_b32 v137, s46\nM 24,160,68\nR 178,184,20480\nM 24,164,84\ns_and_b32 s61, s40, 3\n"
"s_lshl_b32 s61, s61, 7\ns_add_u32 s61, s61, s91\ns_add_u32 s61, s61, 0\ns_mov_b64 exec, 0xffff\nbuffer_store_dwordx2 v[136:137], v128, s[20:23], s61 offen sc1\ns_mov_b64 exec, -1\n"
"M 40,160,72\nR 180,184,24576\nM 40,164,88\nM 56,160,76\nR 182,184,28672\nM 56,164,92\n"
"G 4\nM 60,168,76\nR 160,185,16384\nM 60,172,92\ns_add_u32 m0, s50, 65536\nM 44,168,72\n"
"R 162,185,20480\nM 44,172,88\ns_add_u32 s63, s52, 1574912\nM 28,168,68\nR 164,185,24576\nM 28,172,84\n"
"buffer_load_dwordx4 v220, s[4:7], s63 offen lds\nM 12,168,64\nR 166,185,28672\nM 12,172,80\ns_waitcnt vmcnt(14)\ns_barrier\n"
"G 4\nN 64,64,176\nR 168,186,16384\nN 64,80,180\nv_lshrrev_b32 v230, 7, v252\nN 68,68,176\n"
"R 170,186,20480\nN 68,84,180\nbuffer_load_dwordx4 v221, s[4:7], s63 offen offset:1024 lds\nv_lshlrev_b32 v230, 9, v230\nN 96,72,176\nR 172,186,24576\n"
"s_add_u32 m0, s50, 73728\nN 96,88,180\nds_read_b128 a[96:99], v250 offset:0\nN 100,76,176\nR 174,186,28672\nds_read_b128 a[100:103], v250 offset:2048\n"
"s_add_u32 s63, s52, 1771520\nN 100,92,180\nds_read_b128 a[104:107], v250 offset:4096\nG 7\nN 108,76,160\nR 176,187,16384\n"
"v_and_b32 v231, 0x70, v252\nN 108,92,164\nds_read_b128 a[108:111], v250 offset:6144\nbuffer_load_dwordx4 v220, s[4:7], s63 offen lds\nN 104,72,160\nR 178,187,20480\n"
"ds_read_b128 a[112:115], v251 offset:0\nN 104,88,164\nds_read_b128 a[116:119], v251 offset:2048\ns_and_b32 s60, s40, 3\ns_lshl_b32 s60, s60, 7\nv_add3_u32 v230, v230, v231, s60\n"
"N 76,68,160\nR 180,187,24576\nds_read_b128 a[120:123], v251 offset:4096\nN 76,84,164\nds_read_b128 a[124:127], v251 offset:6144\nN 72,64,160\n"
"R 182,187,28672\nN 72,80,164\ns_lshl_b32 s61, s42, 13\ns_add_u32 s61, s61, 84017152\nbuffer_load_dwordx4 v[128:131], v230, s[20:23], s61 offen sc1\nG 11\n"
"N 80,64,168\nR 160,184,32768\nbuffer_load_dwordx4 v221, s[4:7], s63 offen offset:1024 lds\nN 80,80,172\ns_lshl_b32 s61, s42, 13\ns_add_u32 s61, s61, 84021248\n"
"buffer_load_dwordx4 v[132:135], v230, s[20:23], s61 offen sc1\nN 84,68,168\nR 162,184,36864\nN 84,84,172\nN 112,72,168\nR 164,184,40960\n"
"s_waitcnt vmcnt(8)\nds_read_b128 a[128:131], v248 offset:0\nN 112,88,172\nds_read_b128 a[132:135], v248 offset:2048\nN 116,76,168\nR 166,184,45056\n"
"ds_read_b128 a[136:139], v248 offset:4096\nN 116,92,172\nds_read_b128 a[140:143], v248 offset:6144\nG 8\nN 124,76,176\nR 168,185,32768\n"
"N 124,92,180\nds_read_b128 a[144:147], v249 offset:0\nN 120,72,176\nR 170,185,36864\nds_read_b128 a[148:151], v249 offset:2048\nN 120,88,180\n"
"ds_read_b128 a[152:155], v249 offset:4096\nN 92,68,176\nR 172,185,40960\nN 92,84,180\nds_read_b128 a[156:159], v249 offset:6144\nN 88,64,176\n"
"R 174,185,45056\nN 88,80,180\ns_waitcnt vmcnt(7)\ns_barrier\nG 10\nM 0,160,96\n"
"R 176,186,32768\nM 0,164,112\nG 2\ns_add_u32 m0, s51, 0\ns_add_u32 s60, s70, 640\nbuffer_load_dwordx4 v226, s[24:27], s60 offen lds\n"
"M 16,160,100\nR 178,186,36864\nM 16,164,116\ns_add_u32 m0, s51, 1024\ns_add_u32 s60, s70, 17024\nbuffer_load_dwordx4 v227, s[24:27], s60 offen lds\n"
"M 32,160,104\nR 180,186,40960\ns_add_u32 m0, s50, 81920\ns_add_u32 s63, s52, 1576960\nbuffer_load_dwordx4 v220, s[4:7], s63 offen lds\nv_lshrrev_b32 v234, 7, v252\n"
"M 32,164,120\ns_add_u32 m0, s51, 2048\ns_add_u32 s60, s70, 33408\nbuffer_load_dwordx4 v226, s[24:27], s60 offen lds\nM 48,160,108\nR 182,186,45056\n"
"M 48,164,124\ns_add_u32 m0, s51, 3072\ns_add_u32 s60, s70, 49792\nbuffer_load_dwordx4 v227, s[24:27], s60 offen lds\nG 4\nM 52,168,108\n"
"R 160,187,32768\ns_add_u32 m0, s50, 81920\ns_nop 0\nbuffer_load_dwordx4 v221, s[4:7], s63 offen offset:1024 lds\nv_lshlrev_b32 v234, 9, v234\nM 52,172,124\n"
"s_add_u32 m0, s51, 4096\ns_add_u32 s60, s71, 640\nbuffer_load_dwordx4 v226, s[24:27], s60 offen lds\nM 36,168,104\nR 162,187,36864\nv_and_b32 v235, 0x70, v252\n"
"M 36,172,120\ns_add_u32 m0, s51, 5120\ns_add_u32 s60, s71, 17024\nbuffer_load_dwordx4 v227, s[24:27], s60 offen lds\nM 20,168,100\nR 164,187,40960\n"
"s_add_u32 m0, s50, 90112\ns_add_u32 s63, s52, 1773568\nbuffer_load_dwordx4 v220, s[4:7], s63 offen lds\nM 20,172,116\ns_add_u32 m0, s51, 6144\ns_add_u32 s60, s71, 33408\n"
"buffer_load_dwordx4 v226, s[24:27], s60 offen lds\nM 4,168,96\nR 166,187,45056\ns_and_b32 s60, s40, 3\ns_lshl_b32 s60, s60, 7\nv_add3_u32 v234, v234, v235, s60\n"
"M 4,172,112\ns_add_u32 m0, s51, 7168\ns_add_u32 s60, s71, 49792\nbuffer_load_dwordx4 v227, s[24:27], s60 offen lds\nG 4\nM 8,176,96\n"
"R 168,184,49152\ns_add_u32 m0, s50, 90112\ns_nop 0\nbuffer_load_dwordx4 v221, s[4:7], s63 offen offset:1024 lds\nM 8,180,112\ns_lshl_b32 s61, s42, 13\n"
"s_add_u32 s61, s61, 84148224\nbuffer_load_dwordx4 v[144:147], v234, s[20:23], s61 offen sc1\nM 24,176,100\nR 170,184,53248\nM 24,180,116\nM 40,176,104\n"
"R 172,184,57344\nM 40,180,120\ns_lshl_b32 s61, s42, 13\ns_add_u32 s61, s61, 84152320\nbuffer_load_dwordx4 v[148:151], v234, s[20:23], s61 offen sc1\nM 56,176,108\n"
"R 174,184,61440\nM 56,180,124\nG 4\nM 60,160,108\nR 176,185,49152\nM 60,164,124\n"
"M 44,160,104\nR 178,185,53248\nM 44,164,120\ns_add_u32 m0, s50, 98304\nM 28,160,100\nR 180,185,57344\n"
"M 28,164,116\ns_add_u32 s63, s52, 1968128\nM 12,160,96\nR 182,185,61440\nM 12,164,112\ns_waitcnt vmcnt(15)\n"
"s_barrier\nG 4\nN 64,96,168\nR 160,186,49152\ns_waitcnt vmcnt(14)\nv_min3_u32 v232, v129, v131, v133\n"
"v_min3_u32 v232, v232, v135, v135\nN 64,112,172\nbuffer_load_dwordx4 v220, s[4:7], s63 offen lds\nv_max3_u32 v233, v129, v131, v133\nN 68,100,168\nR 162,186,53248\n"
"v_max3_u32 v233, v233, v135, v135\nv_cmp_ne_u32 s[86:87], s46, v232\nN 68,116,172\nv_cmp_ne_u32 s[88:89], s46, v233\nN 96,104,168\nR 164,186,57344\n"
"s_or_b64 s[86:87], s[86:87], s[88:89]\ns_cbranch_scc1 Lq116\nLq117:\nN 96,120,172\nbuffer_load_dwordx4 v221, s[4:7], s63 offen offset:1024 lds\nv_add_f32 v128, v128, v132\n"
"N 100,108,168\nR 166,186,61440\ns_add_u32 m0, s50, 106496\nv_add_f32 v130, v130, v134\nN 100,124,172\nG 4\n"
"v_add_f32_dpp v128, v128, v128 row_ror:8 row_mask:0xf bank_mask:0xf\nv_add_f32_dpp v130, v130, v130 row_ror:8 row_mask:0xf bank_mask:0xf\nv_mov_b32 v132, v128\nN 108,108,176\nR 168,187,49152\ns_add_u32 s63, s52, 2164736\n"
"v_mov_b32 v133, v130\nN 108,124,180\nbuffer_load_dwordx4 v220, s[4:7], s63 offen lds\nv_permlane32_swap_b32 v132, v128\nN 104,104,176\nR 170,187,53248\n"
"v_permlane32_swap_b32 v133, v130\nv_add_f32 v128, v128, v132\nN 104,120,180\nv_add_f32 v130, v130, v133\nv_mov_b32 v132, v128\nv_mov_b32 v133, v130\n"
"N 76,100,176\nR 172,187,57344\nv_permlane16_swap_b32 v132, v128\nv_permlane16_swap_b32 v133, v130\nN 76,116,180\nbuffer_load_dwordx4 v221, s[4:7], s63 offen offset:1024 lds\n"
"v_add_f32 v128, v128, v132\nN 72,96,176\nR 174,187,61440\ns_add_u32 s63, s52, 1970176\nN 72,112,180\nG 4\n"
"v_add_f32 v130, v130, v133\nv_and_b32 v136, s77, v128\nN 80,96,160\nR 176,188,0\nv_sub_f32 v137, v128, v136\nN 80,112,164\n"
"s_add_u32 m0, s50, 114688\nv_cvt_pk_bf16_f32 v132, v136, v137\nv_cvt_pk_bf16_f32 v133, v136, 0\nN 84,100,160\nR 178,188,4096\nv_and_b32 v136, s77, v130\n"
"N 84,116,164\nbuffer_load_dwordx4 v220, s[4:7], s63 offen lds\nN 112,104,160\nR 180,188,8192\nv_sub_f32 v137, v130, v136\nN 112,120,164\n"
"v_cvt_pk_bf16_f32 v134, v136, v137\nv_cvt_pk_bf16_f32 v135, v136, 0\nN 116,108,160\nR 182,188,12288\nv_and_b32 v230, 0x1ff, v230\nN 116,124,164\n"
"v_add_u32 v230, 156160, v230\nds_write_b128 v230, v[132:135]\nG 5\nN 124,108,168\nR 160,189,0\nN 124,124,172\n"
"buffer_load_dwordx4 v221, s[4:7], s63 offen offset:1024 lds\nN 120,104,168\nR 162,189,4096\ns_add_u32 s63, s52, 2166784\nN 120,120,172\ns_add_u32 m0, s50, 0\n"
"N 92,100,168\nR 164,189,8192\nN 92,116,172\nbuffer_load_dwordx4 v220, s[4:7], s63 offen lds\nN 88,96,168\nR 166,189,12288\n"
"N 88,112,172\ns_waitcnt vmcnt(10)\nds_read_b128 a[160:163], v248 offset:0\ns_waitcnt vmcnt(9) lgkmcnt(5)\ns_barrier\nM 0,176,128\n"
"R 168,190,0\nds_read_b128 a[164:167], v248 offset:2048\nM 0,180,144\nds_read_b128 a[168:171], v248 offset:4096\nM 16,176,132\nR 170,190,4096\n"
"s_waitcnt vmcnt(7)\nv_min3_u32 v236, v145, v147, v149\nv_min3_u32 v236, v236, v151, v151\nM 16,180,148\nds_read_b128 a[172:175], v248 offset:6144\nv_max3_u32 v237, v145, v147, v149\n"
"v_max3_u32 v237, v237, v151, v151\nM 32,176,136\nR 172,190,8192\nds_read_b128 a[176:179], v249 offset:0\nv_cmp_ne_u32 s[86:87], s46, v236\nM 32,180,152\n"
"ds_read_b128 a[180:183], v249 offset:2048\nv_lshrrev_b32 v230, 7, v252\nv_cmp_ne_u32 s[88:89], s46, v237\nM 48,176,140\nR 174,190,12288\nds_read_b128 a[184:187], v249 offset:4096\n"
"s_or_b64 s[86:87], s[86:87], s[88:89]\ns_cbranch_scc1 Lq124\nLq125:\nM 48,180,156\nds_read_b128 a[188:191], v249 offset:6144\nG 12\n"
"v_add_f32 v144, v144, v148\nM 52,160,140\nR 176,191,0\nv_add_f32 v146, v146, v150\nv_add_f32_dpp v144, v144, v144 row_ror:8 row_mask:0xf bank_mask:0xf\nM 52,164,156\n"
"buffer_load_dwordx4 v221, s[4:7], s63 offen offset:1024 lds\nv_lshlrev_b32 v230, 9, v230\nv_add_f32_dpp v146, v146, v146 row_ror:8 row_mask:0xf bank_mask:0xf\nM 36,160,136\nR 178,191,4096\nv_and_b32 v231, 0x70, v252\n"
"v_mov_b32 v148, v144\nM 36,164,152\nG 2\ns_add_u32 m0, s51, 0\ns_add_u32 s60, s70, 768\nbuffer_load_dwordx4 v226, s[24:27], s60 offen lds\n"
"v_mov_b32 v149, v146\nM 20,160,132\nR 180,191,8192\nv_permlane32_swap_b32 v148, v144\nv_permlane32_swap_b32 v149, v146\nM 20,164,148\n"
"s_add_u32 m0, s51, 1024\ns_add_u32 s60, s70, 17152\nbuffer_load_dwordx4 v227, s[24:27], s60 offen lds\nv_add_f32 v144, v144, v148\nM 4,160,128\nR 182,191,12288\n"
"s_and_b32 s60, s40, 3\ns_lshl_b32 s60, s60, 7\nv_add3_u32 v230, v230, v231, s60\nv_add_f32 v146, v146, v149\nM 4,164,144\ns_add_u32 m0, s51, 2048\n"
"s_add_u32 s60, s70, 33536\nbuffer_load_dwordx4 v226, s[24:27], s60 offen lds\nv_mov_b32 v148, v144\nM 8,168,128\nR 160,188,16384\nv_mov_b32 v149, v146\n"
"v_permlane16_swap_b32 v148, v144\nM 8,172,144\ns_add_u32 m0, s51, 3072\ns_add_u32 s60, s70, 49920\nbuffer_load_dwordx4 v227, s[24:27], s60 offen lds\nv_permlane16_swap_b32 v149, v146\n"
"M 24,168,132\nR 162,188,20480\ns_lshl_b32 s61, s42, 13\ns_add_u32 s61, s61, 83886080\nbuffer_load_dwordx4 v[128:131], v230, s[20:23], s61 offen sc1\nv_add_f32 v144, v144, v148\n"
"M 24,172,148\ns_add_u32 m0, s51, 4096\ns_add_u32 s60, s71, 768\nbuffer_load_dwordx4 v226, s[24:27], s60 offen lds\nv_add_f32 v146, v146, v149\nM 40,168,136\n"
"R 164,188,24576\ns_lshl_b32 s61, s42, 13\ns_add_u32 s61, s61, 83890176\nbuffer_load_dwordx4 v[132:135], v230, s[20:23], s61 offen sc1\nv_and_b32 v152, s77, v144\nM 40,172,152\n"
"s_add_u32 m0, s51, 5120\ns_add_u32 s60, s71, 17152\nbuffer_load_dwordx4 v227, s[24:27], s60 offen lds\nM 56,168,140\nR 166,188,28672\nv_sub_f32 v153, v144, v152\n"
"M 56,172,156\ns_add_u32 m0, s51, 6144\ns_add_u32 s60, s71, 33536\nbuffer_load_dwordx4 v226, s[24:27], s60 offen lds\nG 4\nM 60,176,140\n"
"R 168,189,16384\nv_cvt_pk_bf16_f32 v148, v152, v153\nM 60,180,156\ns_add_u32 m0, s51, 7168\ns_add_u32 s60, s71, 49920\nbuffer_load_dwordx4 v227, s[24:27], s60 offen lds\n"
"M 44,176,136\nR 170,189,20480\nv_cvt_pk_bf16_f32 v149, v152, 0\nM 44,180,152\ns_add_u32 m0, s50, 8192\nv_and_b32 v152, s77, v146\n"
"v_sub_f32 v153, v146, v152\nM 28,176,132\nR 172,189,24576\nv_cvt_pk_bf16_f32 v150, v152, v153\nM 28,180,148\ns_add_u32 s63, s52, 2361344\n"
"v_cvt_pk_bf16_f32 v151, v152, 0\nv_and_b32 v234, 0x1ff, v234\nM 12,176,128\nR 174,189,28672\nv_add_u32 v234, 156672, v234\nM 12,180,144\n"
"s_waitcnt vmcnt(14)\ns_barrier\nG 4\nN 64,128,160\nR 176,190,16384\nds_write_b128 v234, v[148:151]\n"
"N 64,144,164\nbuffer_load_dwordx4 v220, s[4:7], s63 offen lds\nN 68,132,160\nR 178,190,20480\nN 68,148,164\nN 96,136,160\n"
"R 180,190,24576\nN 96,152,164\nbuffer_load_dwordx4 v221, s[4:7], s63 offen offset:1024 lds\nN 100,140,160\nR 182,190,28672\ns_add_u32 m0, s50, 16384\n"
"N 100,156,164\nG 5\nN 108,140,168\nR 160,191,16384\ns_add_u32 s63, s52, 2557952\nN 108,156,172\n"
"buffer_load_dwordx4 v220, s[4:7], s63 offen lds\nN 104,136,168\nR 162,191,20480\nN 104,152,172\nN 76,132,168\nR 164,191,24576\n"
"Lq186:\ns_lshr_b32 s60, s45, 3\ns_lshl2_add_u32 s60, s60, s40\ns_lshl_b32 s60, s60, 8\nv_lshl_add_u32 v224, v252, 3, s60\nv_add_u32 v224, Lq186+4096-LqPC, v224\n"
"s_mov_b64 exec, 3\nglobal_load_dword v224, v224, s[98:99]\ns_mov_b64 exec, -1\nN 76,148,172\nbuffer_load_dwordx4 v221, s[4:7], s63 offen offset:1024 lds\nN 72,128,168\n"
"R 166,191,28672\nN 72,144,172\nG 4\nN 80,128,176\nR 168,188,32768\nN 80,144,180\n"
"N 84,132,176\nR 170,188,36864\nN 84,148,180\ns_add_u32 m0, s50, 24576\nN 112,136,176\nR 172,188,40960\n"
"N 112,152,180\ns_add_u32 s63, s52, 2754560\nN 116,140,176\nR 174,188,45056\nN 116,156,180\nG 4\n"
"N 124,140,160\nR 176,189,32768\nN 124,156,164\nbuffer_load_dwordx4 v220, s[4:7], s63 offen lds\nN 120,136,160\nR 178,189,36864\n"
"N 120,152,164\nN 92,132,160\nR 180,189,40960\ns_waitcnt vmcnt(9)\nv_min3_u32 v232, v129, v131, v133\nv_min3_u32 v232, v232, v135, v135\n"
"N 92,148,164\nbuffer_load_dwordx4 v221, s[4:7], s63 offen offset:1024 lds\nv_max3_u32 v233, v129, v131, v133\nN 88,128,160\nR 182,189,45056\ns_add_u32 m0, s50, 32768\n"
"v_max3_u32 v233, v233, v135, v135\nN 88,144,164\ns_barrier\nG 4\nv_cmp_ne_u32 s[86:87], s46, v232\nM 0,168,160\n"
"R 160,190,32768\ns_add_u32 s63, s52, 2951168\nv_cmp_ne_u32 s[88:89], s46, v233\nM 0,172,176\nbuffer_load_dwordx4 v220, s[4:7], s63 offen lds\ns_or_b64 s[86:87], s[86:87], s[88:89]\n"
"s_cbranch_scc1 Lq132\nLq133:\nM 16,168,164\nR 162,190,36864\nv_add_f32 v128, v128, v132\nv_add_f32 v130, v130, v134\n"
"M 16,172,180\nv_add_f32_dpp v128, v128, v128 row_ror:8 row_mask:0xf bank_mask:0xf\nv_add_f32_dpp v130, v130, v130 row_ror:8 row_mask:0xf bank_mask:0xf\nv_mov_b32 v132, v128\nM 32,168,168\nR 164,190,40960\n"
"v_mov_b32 v133, v130\nv_permlane32_swap_b32 v132, v128\nM 32,172,184\nv_permlane32_swap_b32 v133, v130\nv_add_f32 v128, v128, v132\nv_add_f32 v130, v130, v133\n"
"M 48,168,172\nR 166,190,45056\nv_mov_b32 v132, v128\nv_mov_b32 v133, v130\nM 48,172,188\nG 4\n"
"v_permlane16_swap_b32 v132, v128\nv_permlane16_swap_b32 v133, v130\nv_add_f32 v128, v128, v132\nM 52,176,172\nR 168,191,32768\nv_add_f32 v130, v130, v133\n"
"v_and_b32 v136, s77, v128\nM 52,180,188\nbuffer_load_dwordx4 v221, s[4:7], s63 offen offset:1024 lds\nv_sub_f32 v137, v128, v136\nM 36,176,168\nR 170,191,36864\n"
"s_waitcnt vmcnt(9)\nds_read_b128 a[192:195], v248 offset:0\nv_cvt_pk_bf16_f32 v132, v136, v137\nM 36,180,184\nds_read_b128 a[196:199], v248 offset:2048\nv_cvt_pk_bf16_f32 v133, v136, 0\n"
"v_and_b32 v136, s77, v130\nM 20,176,164\nR 172,191,40960\nds_read_b128 a[200:203], v248 offset:4096\nv_sub_f32 v137, v130, v136\nM 20,180,180\n"
"ds_read_b128 a[204:207], v248 offset:6144\nv_cvt_pk_bf16_f32 v134, v136, v137\nv_cvt_pk_bf16_f32 v135, v136, 0\nM 4,176,160\nR 174,191,45056\nv_and_b32 v230, 0x1ff, v230\n"
"v_add_u32 v230, 155648, v230\nM 4,180,176\nds_read_b128 a[208:211], v249 offset:0\nds_write_b128 v230, v[132:135]\nG 10\nM 8,160,160\n"
"R 176,188,49152\nds_read_b128 a[212:215], v249 offset:2048\nM 8,164,176\nds_read_b128 a[216:219], v249 offset:4096\nM 24,160,164\nR 178,188,53248\n"
"M 24,164,180\nds_read_b128 a[220:223], v249 offset:6144\nM 40,160,168\nR 180,184,0\nM 40,164,184\ns_lshr_b32 s84, s49, 3\n"
"M 56,160,172\nR 182,184,4096\ns_add_u32 s84, s84, 81788928\nM 56,164,188\nG 2\ns_add_u32 m0, s51, 0\n"
"s_add_u32 s60, s70, 896\nbuffer_load_dwordx4 v226, s[24:27], s60 offen lds\nM 60,168,172\nR 160,189,49152\nbuffer_load_dwordx4 v[128:131], v252, s[20:23], s84 offen offset:0 sc1\nM 60,172,188\n"
"s_add_u32 m0, s51, 1024\ns_add_u32 s60, s70, 17280\nbuffer_load_dwordx4 v227, s[24:27], s60 offen lds\nM 44,168,168\nR 162,189,53248\nM 44,172,184\n"
"s_add_u32 m0, s51, 2048\ns_add_u32 s60, s70, 33664\nbuffer_load_dwordx4 v226, s[24:27], s60 offen lds\nM 28,168,164\nR 164,185,0\nbuffer_load_dwordx4 v[132:135], v252, s[20:23], s84 offen offset:1024 sc1\n"
"M 28,172,180\ns_add_u32 m0, s51, 3072\ns_add_u32 s60, s70, 50048\nbuffer_load_dwordx4 v227, s[24:27], s60 offen lds\nM 12,168,160\nR 166,185,4096\n"
"buffer_load_dwordx4 v[136:139], v252, s[20:23], s84 offen offset:2048 sc1\nM 12,172,176\ns_add_u32 m0, s51, 4096\ns_add_u32 s60, s71, 896\nbuffer_load_dwordx4 v226, s[24:27], s60 offen lds\ns_waitcnt vmcnt(12)\n"
"s_barrier\nG 4\nN 64,160,176\nR 168,190,49152\ns_lshr_b32 s85, s49, 3\ns_add_u32 m0, s50, 40960\n"
"s_add_u32 s63, s52, 2363392\nbuffer_load_dwordx4 v220, s[4:7], s63 offen lds\nN 64,176,180\ns_add_u32 m0, s51, 5120\ns_add_u32 s60, s71, 17280\nbuffer_load_dwordx4 v227, s[24:27], s60 offen lds\n"
"s_add_u32 s85, s85, 81793024\nN 68,164,176\nR 170,190,53248\nbuffer_load_dwordx4 v[140:143], v252, s[20:23], s84 offen offset:3072 sc1\nN 68,180,180\ns_add_u32 m0, s51, 6144\n"
"s_add_u32 s60, s71, 33664\nbuffer_load_dwordx4 v226, s[24:27], s60 offen lds\nN 96,168,176\nR 172,186,0\nbuffer_load_dwordx4 v[144:147], v252, s[20:23], s85 offen offset:0 sc1\nN 96,184,180\n"
"s_add_u32 m0, s51, 7168\ns_add_u32 s60, s71, 50048\nbuffer_load_dwordx4 v227, s[24:27], s60 offen lds\nN 100,172,176\nR 174,186,4096\ns_add_u32 m0, s50, 40960\n"
"N 100,188,180\nG 4\nN 108,172,160\nR 176,191,49152\nbuffer_load_dwordx4 v221, s[4:7], s63 offen offset:1024 lds\nN 108,188,164\n"
"buffer_load_dwordx4 v[148:151], v252, s[20:23], s85 offen offset:1024 sc1\ns_add_u32 s63, s52, 2560000\nN 104,168,160\nR 178,191,53248\ns_add_u32 m0, s50, 49152\nN 104,184,164\n"
"buffer_load_dwordx4 v[152:155], v252, s[20:23], s85 offen offset:2048 sc1\nN 76,164,160\nR 180,187,0\nbuffer_load_dwordx4 v220, s[4:7], s63 offen lds\nN 76,180,164\nbuffer_load_dwordx4 v[156:159], v252, s[20:23], s85 offen offset:3072 sc1\n"
"N 72,160,160\nR 182,187,4096\nN 72,176,164\nG 4\nN 80,160,168\nR 160,184,8192\n"
"buffer_load_dwordx4 v221, s[4:7], s63 offen offset:1024 lds\nN 80,176,172\nN 84,164,168\nR 162,184,12288\nN 84,180,172\nN 112,168,168\n"
"R 164,184,16384\nN 112,184,172\nN 116,172,168\nR 166,184,20480\nN 116,188,172\nG 4\n"
"N 124,172,176\nR 168,185,8192\nN 124,188,180\ns_add_u32 m0, s50, 57344\nN 120,168,176\nR 170,185,12288\n"
"N 120,184,180\ns_add_u32 s63, s52, 2756608\nN 92,164,176\nR 172,185,16384\nN 92,180,180\nbuffer_load_dwordx4 v220, s[4:7], s63 offen lds\n"
"N 88,160,176\nR 174,185,20480\nN 88,176,180\ns_waitcnt vmcnt(21)\ns_barrier\nG 4\n"
"M 0,160,192\nR 176,186,8192\nM 0,164,208\nbuffer_load_dwordx4 v221, s[4:7], s63 offen offset:1024 lds\nM 16,160,196\nR 178,186,12288\n"
"M 16,164,212\ns_add_u32 m0, s50, 65536\nM 32,160,200\nR 180,186,16384\nM 32,164,216\ns_add_u32 s63, s52, 2953216\n"
"M 48,160,204\nR 182,186,20480\ns_lshl2_add_u32 s60, s45, s40\nv_lshlrev_b32 v224, 3, v252\ns_mul_i32 s60, s60, 2048\ns_bfm_b64 exec, 16, 0\n"
"buffer_load_dword v224, v224, s[8:11], s60 offen\ns_mov_b64 exec, -1\nM 48,164,220\nG 4\nM 52,168,204\nR 160,187,8192\n"
"M 52,172,220\nbuffer_load_dwordx4 v220, s[4:7], s63 offen lds\nM 36,168,200\nR 162,187,12288\nM 36,172,216\nM 20,168,196\n"
"R 164,187,16384\nM 20,172,212\nbuffer_load_dwordx4 v221, s[4:7], s63 offen offset:1024 lds\nM 4,168,192\nR 166,187,20480\nM 4,172,208\n"
"G 4\nM 8,176,192\nR 168,184,24576\nM 8,180,208\nM 24,176,196\nR 170,184,28672\n"
"s_waitcnt vmcnt(11)\nds_read_b128 a[224:227], v248 offset:0\nM 24,180,212\nds_read_b128 a[228:231], v248 offset:2048\nM 40,176,200\nR 172,184,32768\n"
"ds_read_b128 a[232:235], v248 offset:4096\nM 40,180,216\nds_read_b128 a[236:239], v248 offset:6144\nM 56,176,204\nR 174,184,36864\nM 56,180,220\n"
"ds_read_b128 a[240:243], v249 offset:0\nG 9\nM 60,160,204\nR 176,185,24576\nds_read_b128 a[244:247], v249 offset:2048\nv_min3_u32 v230, v129, v131, v133\n"
"M 60,164,220\nds_read_b128 a[248:251], v249 offset:4096\ns_add_u32 m0, s50, 73728\nv_min3_u32 v230, v230, v135, v137\nM 44,160,200\nR 178,185,28672\n"
"s_add_u32 s63, s52, 0\nv_min3_u32 v230, v230, v139, v141\nM 44,164,216\nds_read_b128 a[252:255], v249 offset:6144\nv_min3_u32 v230, v230, v143, v145\ns_waitcnt vmcnt(9)\n"
"v_min3_u32 v230, v230, v147, v149\nM 28,160,196\nR 180,185,32768\ns_waitcnt vmcnt(8)\nv_min3_u32 v230, v230, v151, v153\ns_waitcnt vmcnt(6)\n"
"v_min3_u32 v230, v230, v155, v157\nM 28,164,212\nbuffer_load_dwordx4 v220, s[4:7], s63 offen lds\nv_min3_u32 v230, v230, v159, v159\nM 12,160,192\nR 182,185,36864\n"
"v_max3_u32 v231, v129, v131, v133\nv_max3_u32 v231, v231, v135, v137\nM 12,164,208\ns_waitcnt vmcnt(6)\ns_barrier\nG 8\n"
"v_max3_u32 v231, v231, v139, v141\nM 0,168,224\nR 160,186,24576\nv_max3_u32 v231, v231, v143, v145\nv_max3_u32 v231, v231, v147, v149\nG 8\n"
"M 0,172,240\nbuffer_load_dwordx4 v221, s[4:7], s63 offen offset:1024 lds\nv_max3_u32 v231, v231, v151, v153\nM 16,168,228\nR 162,186,28672\ns_add_u32 s63, s52, 196608\n"
"v_max3_u32 v231, v231, v155, v157\nG 7\nM 16,172,244\ns_add_u32 m0, s50, 81920\nv_max3_u32 v231, v231, v159, v159\nv_cmp_ne_u32 s[86:87], s46, v230\n"
"v_cmp_ne_u32 s[88:89], s46, v231\nM 32,168,232\nR 164,186,32768\ns_or_b64 s[86:87], s[86:87], s[88:89]\ns_cbranch_scc1 Lq140\nLq141:\n"
"G 7\nM 32,172,248\nbuffer_load_dwordx4 v220, s[4:7], s63 offen lds\nv_add_f32 v128, v128, v144\nM 48,168,236\nR 166,186,36864\n"
"s_lshl_b32 s60, s90, 14\ns_add_u32 s70, s49, s60\ns_xor_b32 s71, s70, 0x10000\nv_add_f32 v130, v130, v146\nG 6\nM 48,172,252\n"
"G 4\nv_add_f32 v132, v132, v148\nv_add_f32 v134, v134, v150\nv_add_f32 v136, v136, v152\nM 52,176,236\nR 168,187,24576\n"
"v_add_f32 v138, v138, v154\nv_add_f32 v140, v140, v156\nM 52,180,252\nbuffer_load_dwordx4 v221, s[4:7], s63 offen offset:1024 lds\nv_add_f32 v142, v142, v158\nM 36,176,232\n"
"R 170,187,28672\ns_add_u32 s63, s52, 393216\nv_add_f32 v128, v128, v136\nM 36,180,248\ns_add_u32 m0, s50, 90112\nv_add_f32 v130, v130, v138\n"
"v_add_f32 v132, v132, v140\nv_add_f32 v134, v134, v142\nM 20,176,228\nR 172,187,32768\nv_add_f32 v128, v128, v132\nv_add_f32 v130, v130, v134\n"
"M 20,180,244\nbuffer_load_dwordx4 v220, s[4:7], s63 offen lds\nv_lshrrev_b32 v231, 1, v252\nM 4,176,224\nR 174,187,36864\nv_lshrrev_b32 v230, 2, v252\n"
"s_lshl_b32 s62, s40, 9\ns_add_u32 s62, s62, 159232\nv_add_u32 v231, s62, v231\nM 4,180,240\nds_write2_b32 v231, v128, v130 offset1:1\nG 5\n"
"M 8,160,224\nR 176,184,40960\ns_lshl_b32 s62, s40, 9\ns_add_u32 s62, s62, 159232\nv_add_u32 v230, s62, v230\nM 8,164,240\n"
"buffer_load_dwordx4 v221, s[4:7], s63 offen offset:1024 lds\nds_read2st64_b32 v[232:233], v230 offset1:1\nM 24,160,228\nR 178,184,45056\nM 24,164,244\ns_add_u32 m0, s50, 98304\n"
"M 40,160,232\nR 180,184,49152\nM 40,164,248\ns_add_u32 s63, s52, 589824\nM 56,160,236\nR 182,184,53248\n"
"M 56,164,252\nG 6\nG 3\nv_add_f32 v232, v232, v233\nv_mov_b32 v233, v232\ns_nop 1\n"
"v_permlane32_swap_b32 v232, v233\nM 60,168,236\nR 160,185,40960\nv_mov_b32 v230, v232\nv_mov_b32 v231, v233\nM 60,172,252\n"
"buffer_load_dwordx4 v220, s[4:7], s63 offen lds\nv_permlane16_swap_b32 v230, v232\nM 44,168,232\nR 162,185,45056\nv_permlane16_swap_b32 v231, v233\nv_mul_f32 v230, 0xba800000, v230\n"
"M 44,172,248\nv_mul_f32 v214, v230, v230\nv_mul_f32 v231, 0x3a800000, v231\nv_sub_f32 v231, v231, v214\nM 28,168,228\nR 164,185,49152\n"
"v_max_f32 v231, 0, v231\nv_add_f32 v231, 0x3727c5ac, v231\nM 28,172,244\nbuffer_load_dwordx4 v221, s[4:7], s63 offen offset:1024 lds\nv_rsq_f32 v234, v231\nM 12,168,224\n"
"R 166,185,53248\ns_mov_b32 s60, 156160\nv_bfe_u32 v246, v252, 4, 4\nv_lshl_add_u32 v246, v246, 3, s60\nv_and_b32 v208, s77, v230\nM 12,172,240\n"
"ds_read_b64 v[238:239], v246 offset:0\ns_waitcnt vmcnt(8) lgkmcnt(0)\ns_barrier\nv_mul_f32 v216, v231, v234\nN 64,192,176\nR 168,186,40960\n"
"ds_read_b64 v[240:241], v246 offset:128\nv_sub_f32 v230, v230, v208\nN 64,208,180\ns_mov_b32 m0, s51\nds_read_b64 v[242:243], v246 offset:256\nv_cvt_pk_bf16_f32 v209, v230, v216\n"
"N 68,196,176\nR 170,186,45056\ns_lshl2_add_u32 s60, s45, s40\nv_lshlrev_b32 v224, 3, v252\ns_mul_i32 s60, s60, 7168\ns_bfm_b64 exec, 14, 0\n"
"buffer_load_dword v224, v224, s[12:15], s60 offen\ns_mov_b64 exec, -1\nds_read_b64 v[244:245], v246 offset:384\nds_write_addtid_b32 v32 offset:0\nv_cvt_pk_bf16_f32 v208, v208, v208\nN 68,212,180\n"
"ds_read_b64 v[246:247], v246 offset:0\nds_write_addtid_b32 v33 offset:256\nv_mov_b32 v214, v234\nv_mul_f32 v215, 0x3e8293ee, v234\nN 96,200,176\nR 172,186,49152\n"
"ds_write_addtid_b32 v34 offset:512\nv_mul_f32 v232, 0xba800000, v232\nv_mul_f32 v217, v232, v232\nN 96,216,180\nds_write_addtid_b32 v35 offset:768\nds_write_addtid_b32 v36 offset:1024\n"
"v_mul_f32 v233, 0x3a800000, v233\nv_sub_f32 v233, v233, v217\nN 100,204,176\nR 174,186,53248\nds_write_addtid_b32 v37 offset:1280\nv_max_f32 v233, 0, v233\n"
"v_add_f32 v233, 0x3727c5ac, v233\nN 100,220,180\nds_write_addtid_b32 v38 offset:1536\nv_rsq_f32 v235, v233\nv_and_b32 v212, s77, v232\nv_mul_f32 v219, v233, v235\n"
"v_sub_f32 v232, v232, v212\nv_cvt_pk_bf16_f32 v213, v232, v219\nv_cvt_pk_bf16_f32 v212, v212, v212\nv_mov_b32 v217, v235\nv_mul_f32 v218, 0x3e8293ee, v235\ns_mov_b64 s[60:61], 0xffff\n"
"v_cndmask_b32 v208, 0, v208, s[60:61]\ns_mov_b64 s[60:61], 0xffff\nv_cndmask_b32 v209, 0, v209, s[60:61]\ns_mov_b64 s[60:61], 0xffff\nv_cndmask_b32 v212, 0, v212, s[60:61]\ns_mov_b64 s[60:61], 0xffff\n"
"v_cndmask_b32 v213, 0, v213, s[60:61]\nv_bfe_u32 v230, v252, 2, 6\ns_lshl_b32 s60, s40, 7\ns_add_u32 s60, s60, 161280\nv_add_u32 v230, s60, v230\nds_write_b32 v230, v214 offset:0\n"
"ds_write_b32 v230, v217 offset:64\nds_write_addtid_b32 v39 offset:1792\nN 108,204,160\nR 176,187,40960\nds_write_addtid_b32 v40 offset:2048\nN 108,220,164\n"
"ds_write_addtid_b32 v41 offset:2304\nds_write_addtid_b32 v42 offset:2560\nN 104,200,160\nR 178,187,45056\nds_write_addtid_b32 v43 offset:2816\nN 104,216,164\n"
"ds_write_addtid_b32 v44 offset:3072\nds_write_addtid_b32 v45 offset:3328\nN 76,196,160\nR 180,187,49152\nG 15\nv_mfma_f32_16x16x32_bf16 v[0:3], v[238:241], v[208:211], v[0:3]\n"
"ds_write_addtid_b32 v46 offset:3584\nN 76,212,164\nv_mfma_f32_16x16x32_bf16 v[4:7], v[240:243], v[208:211], v[4:7]\nds_write_addtid_b32 v47 offset:3840\nds_write_addtid_b32 v48 offset:4096\nN 72,192,160\n"
"R 182,187,53248\nv_mfma_f32_16x16x32_bf16 v[8:11], v[242:245], v[208:211], v[8:11]\nds_write_addtid_b32 v49 offset:4352\nN 72,208,164\nv_mfma_f32_16x16x32_bf16 v[12:15], v[244:247], v[208:211], v[12:15]\nds_write_addtid_b32 v50 offset:4608\n"
"ds_write_addtid_b32 v51 offset:4864\nG 15\nN 80,192,168\nR 160,184,57344\nv_mfma_f32_16x16x32_bf16 v[16:19], v[244:247], v[210:213], v[16:19]\nds_write_addtid_b32 v52 offset:5120\n"
"N 80,208,172\nv_mfma_f32_16x16x32_bf16 v[20:23], v[238:241], v[210:213], v[20:23]\nds_write_addtid_b32 v53 offset:5376\nds_write_addtid_b32 v54 offset:5632\nN 84,196,168\nR 162,184,61440\n"
"v_mfma_f32_16x16x32_bf16 v[24:27], v[240:243], v[210:213], v[24:27]\nds_write_addtid_b32 v55 offset:5888\nN 84,212,172\nv_mfma_f32_16x16x32_bf16 v[28:31], v[242:245], v[210:213], v[28:31]\nds_write_addtid_b32 v56 offset:6144\nds_write_addtid_b32 v57 offset:6400\n"
"N 112,200,168\nR 164,188,0\nds_write_addtid_b32 v58 offset:6656\nN 112,216,172\nds_write_addtid_b32 v59 offset:6912\nds_write_addtid_b32 v60 offset:7168\n"
"N 116,204,168\nR 166,188,4096\nds_write_addtid_b32 v61 offset:7424\nN 116,220,172\nds_write_addtid_b32 v62 offset:7680\nds_write_addtid_b32 v63 offset:7936\n"
"G 15\nN 124,204,176\nR 168,185,57344\ns_add_u32 m0, s50, 106496\ns_add_u32 s63, s52, 786432\nN 124,220,180\n"
"buffer_load_dwordx4 v220, s[4:7], s63 offen lds\nN 120,200,176\nR 170,185,61440\ns_waitcnt vmcnt(6) lgkmcnt(2)\ns_barrier\nN 120,216,180\n"
"ds_read2st64_b32 v[32:33], v225 offset0:0 offset1:1\nds_read2st64_b32 v[34:35], v225 offset0:2 offset1:3\nN 92,196,176\nR 172,189,0\nds_read2st64_b32 v[36:37], v225 offset0:4 offset1:5\nN 92,212,180\n"
"buffer_load_dwordx4 v221, s[4:7], s63 offen offset:1024 lds\nds_read2st64_b32 v[38:39], v225 offset0:6 offset1:7\nN 88,192,176\nR 174,189,4096\nds_read2st64_b32 v[40:41], v225 offset0:8 offset1:9\nN 88,208,180\n"
"ds_read2st64_b32 v[42:43], v225 offset0:10 offset1:11\nds_read2st64_b32 v[44:45], v225 offset0:12 offset1:13\nN 64,224,160\nR 176,186,57344\ns_mov_b32 s60, 156672\nv_bfe_u32 v246, v252, 4, 4\n"
"v_lshl_add_u32 v246, v246, 3, s60\nds_read2st64_b32 v[46:47], v225 offset0:14 offset1:15\nN 64,240,164\nds_read_b64 v[238:239], v246 offset:0\ns_add_u32 m0, s50, 114688\nG 11\n"
"v_add_f32 v0, v0, v32\nN 68,228,160\nR 178,186,61440\nds_read_b64 v[240:241], v246 offset:128\nv_add_f32 v1, v1, v33\nN 68,244,164\n"
"ds_read_b64 v[242:243], v246 offset:256\ns_add_u32 s63, s52, 983040\nG 13\nv_add_f32 v2, v2, v34\nN 96,232,160\nR 180,190,0\n"
"ds_read_b64 v[244:245], v246 offset:384\nv_add_f32 v3, v3, v35\nN 96,248,164\nds_read_b64 v[246:247], v246 offset:0\nG 14\nv_add_f32 v4, v4, v36\n"
"v_add_f32 v5, v5, v37\nN 100,236,160\nR 182,190,4096\nbuffer_load_dwordx4 v220, s[4:7], s63 offen lds\nG 14\nv_add_f32 v6, v6, v38\n"
"N 100,252,164\nds_read2st64_b32 v[48:49], v225 offset0:16 offset1:17\nG 14\nv_add_f32 v7, v7, v39\nN 108,236,168\nR 160,187,57344\n"
"G 14\nv_add_f32 v8, v8, v40\nv_add_f32 v9, v9, v41\nN 108,252,172\nbuffer_load_dwordx4 v221, s[4:7], s63 offen offset:1024 lds\nG 13\n"
"v_add_f32 v10, v10, v42\nN 104,232,168\nR 162,187,61440\ns_add_u32 m0, s50, 0\ns_add_u32 s63, s52, 1179648\nv_add_f32 v11, v11, v43\n"
"N 104,248,172\nbuffer_load_dwordx4 v220, s[4:7], s63 offen lds\nG 13\nv_add_f32 v12, v12, v44\nN 76,228,168\nR 164,191,0\n"
"v_add_f32 v13, v13, v45\nG 12\nv_add_f32 v14, v14, v46\nN 76,244,172\nG 9\nv_mfma_f32_16x16x32_bf16 v[64:67], v[208:211], v[238:241], v[64:67]\n"
"ds_read2st64_b32 v[50:51], v225 offset0:18 offset1:19\nv_add_f32 v15, v15, v47\nG 4\nv_add_f32 v16, v16, v48\nN 72,224,168\nR 166,191,4096\n"
"ds_read2st64_b32 v[52:53], v225 offset0:20 offset1:21\nv_add_f32 v17, v17, v49\nN 72,240,172\nv_mfma_f32_16x16x32_bf16 v[72:75], v[208:211], v[240:243], v[72:75]\nds_read2st64_b32 v[54:55], v225 offset0:22 offset1:23\nds_read2st64_b32 v[56:57], v225 offset0:24 offset1:25\n"
"N 80,224,176\nR 168,188,8192\nbuffer_load_dwordx4 v221, s[4:7], s63 offen offset:1024 lds\nds_read2st64_b32 v[58:59], v225 offset0:26 offset1:27\nN 80,240,180\nv_mfma_f32_16x16x32_bf16 v[80:83], v[208:211], v[242:245], v[80:83]\n"
"s_add_u32 m0, s50, 8192\ns_add_u32 s63, s52, 1376256\nbuffer_load_dwordx4 v220, s[4:7], s63 offen lds\nds_read2st64_b32 v[60:61], v225 offset0:28 offset1:29\nN 84,228,176\nR 170,188,12288\n"
"ds_read2st64_b32 v[62:63], v225 offset0:30 offset1:31\nN 84,244,180\nv_mfma_f32_16x16x32_bf16 v[88:91], v[208:211], v[244:247], v[88:91]\nG 9\nv_add_f32 v18, v18, v50\nv_add_f32 v19, v19, v51\n"
"N 112,232,176\nR 172,188,16384\nG 8\nv_add_f32 v20, v20, v52\nv_add_f32 v21, v21, v53\nN 112,248,180\n"
"v_mfma_f32_16x16x32_bf16 v[68:71], v[210:213], v[244:247], v[68:71]\nbuffer_load_dwordx4 v221, s[4:7], s63 offen offset:1024 lds\nG 7\nv_add_f32 v22, v22, v54\nN 116,236,176\nR 174,188,20480\n"
"s_add_u32 m0, s50, 32768\nv_add_f32 v23, v23, v55\nN 116,252,180\nv_mfma_f32_16x16x32_bf16 v[76:79], v[210:213], v[238:241], v[76:79]\nG 7\nv_add_f32 v24, v24, v56\n"
"v_add_f32 v25, v25, v57\nG 5\nv_add_f32 v26, v26, v58\nN 124,236,160\nR 176,189,8192\ns_add_u32 s63, s52, 1572864\n"
"v_add_f32 v27, v27, v59\nN 124,252,164\nv_mfma_f32_16x16x32_bf16 v[84:87], v[210:213], v[240:243], v[84:87]\nbuffer_load_dwordx4 v220, s[4:7], s63 offen lds\nG 5\nv_add_f32 v28, v28, v60\n"
"v_add_f32 v29, v29, v61\nG 3\nv_add_f32 v30, v30, v62\nv_add_f32 v31, v31, v63\nN 120,232,160\nR 178,189,12288\n"
"s_lshl4_add_u32 s64, s41, s42\nv_lshrrev_b32 v227, 1, v252\nN 120,248,164\nv_mfma_f32_16x16x32_bf16 v[92:95], v[210:213], v[242:245], v[92:95]\nbuffer_load_dwordx4 v221, s[4:7], s63 offen offset:1024 lds\nv_mul_f32 v16, v16, v217\n"
"N 92,228,160\nR 180,189,16384\ns_add_u32 m0, s50, 40960\ns_add_u32 s63, s52, 1769472\nv_mul_f32 v17, v17, v217\nN 92,244,164\n"
"buffer_load_dwordx4 v220, s[4:7], s63 offen lds\nv_mul_f32 v18, v18, v217\nN 88,224,160\nR 182,189,20480\ns_lshl_b32 s64, s64, 12\nv_mul_f32 v19, v19, v217\n"
"N 88,240,164\ns_waitcnt vmcnt(12)\ns_barrier\nG 4\nv_mul_f32 v20, v20, v217\nv_mfma_f32_16x16x32_bf16 v[128:131], v[168:171], a[0:3], 0\n"
"R 160,190,8192\ns_add_u32 s64, s64, 77594624\nv_mul_f32 v21, v21, v217\nM 128,172,16\ns_mov_b32 m0, s51\ns_cmp_eq_u32 s40, 3\n"
"s_cselect_b64 s[66:67], -1, 0\nv_mul_f32 v22, v22, v217\nv_mfma_f32_16x16x32_bf16 v[144:147], v[168:171], a[4:7], 0\nR 162,190,12288\ns_lshl2_add_u32 s60, s45, s40\ns_waitcnt vmcnt(11)\n"
"v_lshlrev_b32 v224, 3, v252\ns_mul_i32 s60, s60, 7168\ns_bfm_b64 exec, 14, 14\nbuffer_load_dword v224, v224, s[12:15], s60 offen\ns_mov_b64 exec, -1\nv_mul_f32 v23, v23, v217\n"
"M 144,172,20\nv_mul_f32 v24, v24, v217\nv_mul_f32 v25, v25, v217\nv_mul_f32 v26, v26, v217\nv_mfma_f32_16x16x32_bf16 v[32:35], v[168:171], a[8:11], 0\nR 164,190,16384\n"
"v_mul_f32 v27, v27, v217\nv_mul_f32 v28, v28, v217\nM 32,172,24\nv_mul_f32 v29, v29, v217\nv_mul_f32 v30, v30, v217\nv_mul_f32 v31, v31, v217\n"
"v_mfma_f32_16x16x32_bf16 v[48:51], v[168:171], a[12:15], 0\nR 166,190,20480\nds_write_addtid_b32 v96 offset:0\nv_cvt_pk_bf16_f32 v16, v16, v17\nv_cvt_pk_bf16_f32 v17, v18, v19\nM 48,172,28\n"
"ds_write_addtid_b32 v97 offset:256\nG 6\nv_cvt_pk_bf16_f32 v18, v20, v21\nv_cvt_pk_bf16_f32 v19, v22, v23\nv_cvt_pk_bf16_f32 v20, v24, v25\nv_mfma_f32_16x16x32_bf16 v[52:55], v[176:179], a[12:15], 0\n"
"R 168,191,8192\nds_write_addtid_b32 v98 offset:512\ns_lshr_b32 s100, s41, 1\ns_mul_i32 s100, s100, 458752\ns_add_u32 s100, s100, s55\nv_cvt_pk_bf16_f32 v21, v26, v27\n"
"v_cvt_pk_bf16_f32 v22, v28, v29\nM 52,180,28\nds_write_addtid_b32 v99 offset:768\ns_mul_i32 s101, s41, 229376\ns_add_u32 s101, s101, s54\nv_cvt_pk_bf16_f32 v23, v30, v31\n"
"v_mul_f32 v0, v0, v214\nv_mul_f32 v1, v1, v214\nv_mfma_f32_16x16x32_bf16 v[36:39], v[176:179], a[8:11], 0\nR 170,191,12288\nds_write_addtid_b32 v100 offset:1024\nv_mul_f32 v2, v2, v214\n"
"v_mul_f32 v3, v3, v214\nM 36,180,24\nds_write_addtid_b32 v101 offset:1280\nv_mul_f32 v4, v4, v214\nv_mul_f32 v5, v5, v214\nv_mul_f32 v6, v6, v214\n"
"v_mfma_f32_16x16x32_bf16 v[148:151], v[176:179], a[4:7], 0\nR 172,191,16384\nds_write_addtid_b32 v102 offset:1536\nv_mul_f32 v7, v7, v214\nv_mul_f32 v8, v8, v214\nM 148,180,20\n"
"ds_write_addtid_b32 v103 offset:1792\nv_mul_f32 v9, v9, v214\nv_mul_f32 v10, v10, v214\nv_mul_f32 v11, v11, v214\nv_mfma_f32_16x16x32_bf16 v[132:135], v[176:179], a[0:3], 0\nR 174,191,20480\n"
"ds_write_addtid_b32 v104 offset:2048\nv_mul_f32 v12, v12, v214\nv_mul_f32 v13, v13, v214\nM 132,180,16\nds_write_addtid_b32 v105 offset:2304\nG 14\n"
"v_mul_f32 v14, v14, v214\nv_mul_f32 v15, v15, v214\nv_cvt_pk_bf16_f32 v0, v0, v1\nv_mfma_f32_16x16x32_bf16 v[136:139], v[160:163], a[0:3], 0\nR 176,188,24576\nds_write_addtid_b32 v106 offset:2560\n"
"v_cvt_pk_bf16_f32 v1, v2, v3\nv_cvt_pk_bf16_f32 v2, v4, v5\nM 136,164,16\nds_write_addtid_b32 v107 offset:2816\nv_cvt_pk_bf16_f32 v3, v6, v7\nv_cvt_pk_bf16_f32 v4, v8, v9\n"
"v_cvt_pk_bf16_f32 v5, v10, v11\nv_mfma_f32_16x16x32_bf16 v[152:155], v[160:163], a[4:7], 0\nR 178,188,28672\nds_write_addtid_b32 v108 offset:3072\nv_cvt_pk_bf16_f32 v6, v12, v13\nv_cvt_pk_bf16_f32 v7, v14, v15\n"
"M 152,164,20\nds_write_addtid_b32 v109 offset:3328\nds_write_addtid_b32 v110 offset:3584\ns_mov_b64 exec, s[66:67]\nbuffer_store_dwordx4 v[16:19], v252, s[20:23], s64 offen offset:0 sc1\nbuffer_store_dwordx4 v[20:23], v252, s[20:23], s64 offen offset:1024 sc1\n"
"s_mov_b64 exec, -1\nv_mfma_f32_16x16x32_bf16 v[40:43], v[160:163], a[8:11], 0\nR 180,188,32768\nds_write_addtid_b32 v111 offset:3840\nM 40,164,24\nds_write_addtid_b32 v112 offset:4096\n"
"ds_write_addtid_b32 v113 offset:4352\nv_mfma_f32_16x16x32_bf16 v[56:59], v[160:163], a[12:15], 0\nR 182,188,36864\nds_write_addtid_b32 v114 offset:4608\nM 56,164,28\nds_write_addtid_b32 v115 offset:4864\n"
"ds_write_addtid_b32 v116 offset:5120\nG 15\nv_mfma_f32_16x16x32_bf16 v[60:63], v[168:171], a[12:15], 0\nR 160,189,24576\nds_write_addtid_b32 v117 offset:5376\nM 60,172,28\n"
"ds_write_addtid_b32 v118 offset:5632\nds_write_addtid_b32 v119 offset:5888\nv_mfma_f32_16x16x32_bf16 v[44:47], v[168:171], a[8:11], 0\nR 162,189,28672\nds_write_addtid_b32 v120 offset:6144\nM 44,172,24\n"
"ds_write_addtid_b32 v121 offset:6400\nds_write_addtid_b32 v122 offset:6656\nds_write_addtid_b32 v123 offset:6912\nds_write_addtid_b32 v124 offset:7168\nds_write_addtid_b32 v125 offset:7424\nds_write_addtid_b32 v126 offset:7680\n"
"ds_write_addtid_b32 v127 offset:7936\nv_mfma_f32_16x16x32_bf16 v[156:159], v[168:171], a[4:7], 0\nR 164,189,32768\ns_add_u32 m0, s50, 40960\nM 156,172,20\nbuffer_load_dwordx4 v221, s[4:7], s63 offen offset:1024 lds\n"
"v_mfma_f32_16x16x32_bf16 v[140:143], v[168:171], a[0:3], 0\nR 166,189,36864\ns_waitcnt vmcnt(11) lgkmcnt(2)\ns_barrier\nM 140,172,16\nds_read2st64_b32 v[96:97], v225 offset0:0 offset1:1\n"
"ds_read2st64_b32 v[98:99], v225 offset0:2 offset1:3\nv_lshrrev_b32 v226, 8, v252\nM 128,176,32\nR 168,190,24576\ns_add_u32 s63, s52, 1966080\nds_read2st64_b32 v[100:101], v225 offset0:4 offset1:5\n"
"s_lshl_b32 s60, s40, 7\ns_add_u32 s60, s60, 161280\nv_lshl_add_u32 v226, v226, 4, s60\nM 128,180,48\ns_add_u32 m0, s50, 49152\nds_read2st64_b32 v[102:103], v225 offset0:6 offset1:7\n"
"ds_read2st64_b32 v[104:105], v225 offset0:8 offset1:9\nds_read_b128 v[230:233], v226 offset:0\nM 144,176,36\nR 170,190,28672\nds_read2st64_b32 v[106:107], v225 offset0:10 offset1:11\nds_read_b128 v[234:237], v226 offset:64\n"
"M 144,180,52\nbuffer_load_dwordx4 v220, s[4:7], s63 offen lds\nds_read2st64_b32 v[108:109], v225 offset0:12 offset1:13\nM 32,176,40\nR 172,190,32768\nds_read2st64_b32 v[110:111], v225 offset0:14 offset1:15\n"
"M 32,180,56\nds_read2st64_b32 v[112:113], v225 offset0:16 offset1:17\nG 13\nv_add_f32 v64, v64, v96\nv_add_f32 v65, v65, v97\nM 48,176,44\n"
"R 174,190,36864\nG 13\nv_add_f32 v66, v66, v98\nv_add_f32 v67, v67, v99\nM 48,180,60\nds_read2st64_b32 v[114:115], v225 offset0:18 offset1:19\n"
"G 12\nv_add_f32 v68, v68, v100\nM 52,160,44\nR 176,191,24576\nv_add_f32 v69, v69, v101\nG 12\n"
"v_add_f32 v70, v70, v102\nM 52,164,60\nbuffer_load_dwordx4 v221, s[4:7], s63 offen offset:1024 lds\nv_add_f32 v71, v71, v103\nM 36,160,40\nR 178,191,28672\n"
"G 12\nv_add_f32 v72, v72, v104\nv_add_f32 v73, v73, v105\nM 36,164,56\ns_add_u32 m0, s50, 57344\nds_read2st64_b32 v[116:117], v225 offset0:20 offset1:21\n"
"G 10\nv_add_f32 v74, v74, v106\nM 148,160,36\nR 180,191,32768\nv_add_f32 v75, v75, v107\nG 9\n"
"v_add_f32 v76, v76, v108\nM 148,164,52\ns_add_u32 s63, s52, 2162688\nds_read2st64_b32 v[118:119], v225 offset0:22 offset1:23\nv_add_f32 v77, v77, v109\nM 132,160,32\n"
"R 182,191,36864\nG 9\nv_add_f32 v78, v78, v110\nv_add_f32 v79, v79, v111\nM 132,164,48\nds_read2st64_b32 v[120:121], v225 offset0:24 offset1:25\n"
"G 8\nv_add_f32 v80, v80, v112\nM 136,168,32\nR 160,188,40960\nv_add_f32 v81, v81, v113\nG 8\n"
"v_add_f32 v82, v82, v114\nM 136,172,48\nbuffer_load_dwordx4 v220, s[4:7], s63 offen lds\nv_add_f32 v83, v83, v115\nM 152,168,36\nR 162,188,45056\n"
"G 6\nv_add_f32 v84, v84, v116\nv_add_f32 v85, v85, v117\nM 152,172,52\nds_read2st64_b32 v[122:123], v225 offset0:26 offset1:27\nG 5\n"
"v_add_f32 v86, v86, v118\nv_add_f32 v87, v87, v119\nM 40,168,40\nR 164,188,49152\nds_read2st64_b32 v[124:125], v225 offset0:28 offset1:29\nM 40,172,56\n"
"buffer_load_dwordx4 v221, s[4:7], s63 offen offset:1024 lds\nG 5\nv_add_f32 v88, v88, v120\nM 56,168,44\nR 166,188,53248\ns_add_u32 m0, s50, 65536\n"
"M 56,172,60\nds_read2st64_b32 v[126:127], v225 offset0:30 offset1:31\nv_add_f32 v89, v89, v121\nM 60,176,44\nR 168,189,40960\ns_add_u32 s63, s52, 2359296\n"
"M 60,180,60\nbuffer_load_dwordx4 v220, s[4:7], s63 offen lds\nM 44,176,40\nR 170,189,45056\nG 6\nv_add_f32 v90, v90, v122\n"
"M 44,180,56\nv_add_f32 v91, v91, v123\nG 4\nv_add_f32 v92, v92, v124\nM 156,176,36\nR 172,189,49152\n"
"v_add_f32 v93, v93, v125\nM 156,180,52\nbuffer_load_dwordx4 v221, s[4:7], s63 offen offset:1024 lds\nM 140,176,32\nR 174,189,53248\ns_add_u32 m0, s50, 73728\n"
"M 140,180,48\nG 5\nG 4\nv_add_f32 v94, v94, v126\nv_add_f32 v95, v95, v127\nM 128,160,64\n"
"R 176,190,40960\ns_add_u32 s63, s52, 2555904\nv_mul_f32 v64, v64, v230\nM 128,164,80\nbuffer_load_dwordx4 v220, s[4:7], s63 offen lds\nv_mul_f32 v65, v65, v231\n"
"M 144,160,68\nR 178,190,45056\nv_mul_f32 v66, v66, v232\nv_mul_f32 v67, v67, v233\nM 144,164,84\nv_mul_f32 v68, v68, v234\n"
"v_mul_f32 v69, v69, v235\nv_mul_f32 v70, v70, v236\nM 32,160,72\nR 180,190,49152\nv_mul_f32 v71, v71, v237\nv_cvt_pk_bf16_f32 v67, v66, v67\n"
"M 32,164,88\nbuffer_load_dwordx4 v221, s[4:7], s63 offen offset:1024 lds\nv_cvt_pk_bf16_f32 v66, v64, v65\nM 48,160,76\nR 182,190,53248\ns_add_u32 s63, s52, 2752512\n"
"v_cvt_pk_bf16_f32 v68, v68, v69\nM 48,164,92\nG 4\nv_cvt_pk_bf16_f32 v69, v70, v71\nv_mul_f32 v72, v72, v230\nv_mul_f32 v73, v73, v231\n"
"M 52,168,76\nR 160,191,40960\nv_mul_f32 v74, v74, v232\nv_mul_f32 v75, v75, v233\nM 52,172,92\ns_add_u32 m0, s50, 81920\n"
"v_mul_f32 v76, v76, v234\nv_mul_f32 v77, v77, v235\nv_mul_f32 v78, v78, v236\nM 36,168,72\nR 162,191,45056\nv_mul_f32 v79, v79, v237\n"
"v_cvt_pk_bf16_f32 v72, v72, v73\nM 36,172,88\nbuffer_load_dwordx4 v220, s[4:7], s63 offen lds\nv_cvt_pk_bf16_f32 v73, v74, v75\nM 148,168,68\nR 164,191,49152\n"
"v_cvt_pk_bf16_f32 v74, v76, v77\nv_cvt_pk_bf16_f32 v75, v78, v79\nM 148,172,84\nv_mul_f32 v80, v80, v230\nv_mul_f32 v81, v81, v231\nv_mul_f32 v82, v82, v232\n"
"M 132,168,64\nR 166,191,53248\ns_waitcnt vmcnt(9)\ns_barrier\nv_mul_f32 v83, v83, v233\nM 132,172,80\n"
"G 4\nv_mul_f32 v84, v84, v234\nv_mul_f32 v85, v85, v235\nv_mul_f32 v86, v86, v236\nM 136,176,64\nR 168,184,0\n"
"s_lshl2_add_u32 s60, s45, s40\nv_lshlrev_b32 v224, 3, v252\ns_mul_i32 s60, s60, 7168\ns_bfm_b64 exec, 14, 28\nbuffer_load_dword v224, v224, s[12:15], s60 offen\ns_mov_b64 exec, -1\n"
"v_mul_f32 v87, v87, v237\nM 136,180,80\nv_cvt_pk_bf16_f32 v78, v80, v81\nv_cvt_pk_bf16_f32 v79, v82, v83\nv_cvt_pk_bf16_f32 v80, v84, v85\nM 152,176,68\n"
"R 170,184,4096\nv_cvt_pk_bf16_f32 v81, v86, v87\nv_mul_f32 v88, v88, v230\nM 152,180,84\nbuffer_load_dwordx4 v221, s[4:7], s63 offen offset:1024 lds\nv_mul_f32 v89, v89, v231\n"
"M 40,176,72\nR 172,184,8192\ns_mov_b32 s60, 155648\nv_bfe_u32 v246, v252, 4, 4\nv_lshl_add_u32 v246, v246, 3, s60\nv_mul_f32 v90, v90, v232\n"
"M 40,180,88\ns_add_u32 m0, s50, 90112\nv_mul_f32 v91, v91, v233\nv_mul_f32 v92, v92, v234\nv_mul_f32 v93, v93, v235\nM 56,176,76\n"
"R 174,184,12288\nds_read_b64 v[238:239], v246 offset:0\nv_mul_f32 v94, v94, v236\nM 56,180,92\nG 5\nv_mul_f32 v95, v95, v237\n"
"v_cvt_pk_bf16_f32 v84, v88, v89\nv_cvt_pk_bf16_f32 v85, v90, v91\nM 60,160,76\nR 176,185,0\nds_read_b64 v[240:241], v246 offset:128\nv_cvt_pk_bf16_f32 v86, v92, v93\n"
"M 60,164,92\ns_add_u32 s63, s52, 2949120\nv_cvt_pk_bf16_f32 v87, v94, v95\nv_add_u32 v108, 161792, v252\ns_lshl_b32 s60, s40, 12\ns_add_u32 s60, s60, 53248\n"
"v_add_u32 v230, s60, v252\nv_add_u32 v231, s60, v227\nM 44,160,72\nR 178,185,4096\nds_read_b64 v[242:243], v246 offset:256\nM 44,164,88\n"
"buffer_load_dwordx4 v220, s[4:7], s63 offen lds\nM 156,160,68\nR 180,185,8192\nds_read_b64 v[244:245], v246 offset:384\nM 156,164,84\nds_read_b128 v[112:115], v108\n"
"M 140,160,64\nR 182,185,12288\nds_read_b64 v[246:247], v246 offset:0\nM 140,164,80\nds_read_b128 v[104:107], v108 offset:1024\ns_mov_b64 exec, s[66:67]\n"
"buffer_store_dwordx2 v[68:69], v227, s[20:23], s64 offen offset:2048 sc1\nbuffer_store_dwordx2 v[74:75], v227, s[20:23], s64 offen offset:2560 sc1\ns_mov_b64 exec, -1\nG 11\nM 128,168,96\nR 160,186,0\n"
"M 128,172,112\nM 144,168,100\nR 162,186,4096\nM 144,172,116\nbuffer_load_dwordx4 v221, s[4:7], s63 offen offset:1024 lds\nM 32,168,104\n"
"R 164,186,8192\nG 11\nv_mfma_f32_16x16x32_bf16 v[128:131], v[238:241], v[208:211], v[128:131]\nM 32,172,120\nG 3\nv_lshlrev_b32 v100, 16, v104\n"
"v_and_b32 v101, s77, v104\nM 48,168,108\nR 166,186,12288\nv_mfma_f32_16x16x32_bf16 v[132:135], v[240:243], v[208:211], v[132:135]\nv_lshlrev_b32 v102, 16, v105\nM 48,172,124\n"
"v_and_b32 v103, s77, v105\nv_lshlrev_b32 v96, 16, v106\nM 52,176,108\nR 168,187,0\nv_mfma_f32_16x16x32_bf16 v[144:147], v[244:247], v[210:213], v[144:147]\nv_and_b32 v97, s77, v106\n"
"M 52,180,124\ns_movk_i32 s63, 7168\nv_mad_u32_u24 v220, v228, s63, v229\nv_add_u32 v221, 3668992, v220\ns_add_u32 m0, s50, 98304\ns_add_u32 s63, s101, 0\n"
"buffer_load_dwordx4 v220, s[12:15], s63 offen lds\nM 36,176,104\nR 170,187,4096\nv_mfma_f32_16x16x32_bf16 v[148:151], v[238:241], v[210:213], v[148:151]\nv_lshlrev_b32 v98, 16, v107\nM 36,180,120\n"
"v_and_b32 v99, s77, v107\nv_add_f32 v104, s78, v100\nM 148,176,100\nR 172,187,8192\nv_add_f32 v105, s78, v101\nM 148,180,116\n"
"s_mov_b64 exec, s[66:67]\nbuffer_store_dwordx2 v[80:81], v227, s[20:23], s64 offen offset:3072 sc1\nbuffer_store_dwordx2 v[86:87], v227, s[20:23], s64 offen offset:3584 sc1\ns_mov_b64 exec, -1\nv_add_f32 v106, s78, v102\nv_add_f32 v107, s78, v103\n"
"M 132,176,96\nR 174,187,12288\ns_not_b64 exec, s[66:67]\nds_write_b128 v230, v[16:19] offset:0\nds_write_b128 v230, v[20:23] offset:1024\ns_mov_b64 exec, -1\n"
"M 132,180,112\nG 6\nM 136,160,96\nR 176,184,32768\nM 136,164,112\nbuffer_load_dwordx4 v221, s[12:15], s63 offen offset:1024 lds\n"
"M 152,160,100\nR 178,184,36864\ns_not_b64 exec, s[66:67]\nds_write_b64 v231, v[68:69] offset:2048\nds_write_b64 v231, v[74:75] offset:2560\ns_mov_b64 exec, -1\n"
"M 152,164,116\nM 40,160,104\nR 180,184,40960\nM 40,164,120\nM 56,160,108\nR 182,184,45056\n"
"M 56,164,124\nG 8\nM 60,168,108\nR 160,185,32768\ns_not_b64 exec, s[66:67]\nds_write_b64 v231, v[80:81] offset:3072\n"
"ds_write_b64 v231, v[86:87] offset:3584\ns_mov_b64 exec, -1\nM 60,172,124\nM 44,168,104\nR 162,185,36864\nM 44,172,120\n"
"s_add_u32 m0, s50, 106496\nM 156,168,100\nR 164,185,40960\nM 156,172,116\ns_add_u32 s63, s101, 128\nM 140,168,96\n"
"R 166,185,45056\nM 140,172,112\nG 6\nM 128,176,128\nR 168,186,32768\ns_waitcnt vmcnt(13) lgkmcnt(4)\n"
"s_barrier\nM 128,180,144\nbuffer_load_dwordx4 v220, s[12:15], s63 offen lds\nM 144,176,132\nR 170,186,36864\nM 144,180,148\n"
"s_cmp_eq_u32 s78, 0\ns_cbranch_scc1 Lq157\nv_mov_b32 v230, 0\nv_mov_b32 v231, 0\nv_mov_b32 v232, 0\nv_mov_b32 v233, 0\n"
"v_mov_b32 v64, 0\nv_mov_b32 v65, 0\nv_mov_b32 v70, 0\nv_mov_b32 v71, 0\nv_mov_b32 v234, 0\nv_mov_b32 v235, 0\n"
"v_mov_b32 v236, 0\nv_mov_b32 v237, 0\nv_mov_b32 v76, 0\nv_mov_b32 v77, 0\nv_mov_b32 v82, 0\nv_mov_b32 v83, 0\n"
"Lq157:\ns_lshl4_add_u32 s65, s41, s42\nM 32,176,136\nR 172,186,40960\ns_lshl_b32 s65, s65, 12\nM 32,180,152\n"
"s_add_u32 s65, s65, 77529088\nM 48,176,140\nR 174,186,45056\nM 48,180,156\nG 4\nM 52,160,140\n"
"R 176,187,32768\nM 52,164,156\nbuffer_load_dwordx4 v221, s[12:15], s63 offen offset:1024 lds\nM 36,160,136\nR 178,187,36864\nM 36,164,152\n"
"s_add_u32 m0, s50, 114688\nM 148,160,132\nR 180,187,40960\nM 148,164,148\ns_add_u32 s63, s101, 256\nM 132,160,128\n"
"R 182,187,45056\nM 132,164,144\nG 4\nM 136,168,128\nR 160,184,49152\nM 136,172,144\n"
"buffer_load_dwordx4 v220, s[12:15], s63 offen lds\nM 152,168,132\nR 162,184,53248\nM 152,172,148\nM 40,168,136\nR 164,184,57344\n"
"s_lshl2_add_u32 s60, s45, s40\ns_waitcnt vmcnt(12)\nv_lshlrev_b32 v224, 3, v252\ns_mul_i32 s60, s60, 7168\ns_bfm_b64 exec, 14, 42\nbuffer_load_dword v224, v224, s[12:15], s60 offen\n"
"s_mov_b64 exec, -1\nM 40,172,152\nM 56,168,140\nR 166,184,61440\nv_mfma_f32_16x16x32_bf16 v[136:139], v[242:245], v[208:211], v[136:139]\nM 56,172,156\n"
"G 4\nM 60,176,140\nR 168,185,49152\nv_mfma_f32_16x16x32_bf16 v[140:143], v[244:247], v[208:211], v[140:143]\nM 60,180,156\nbuffer_load_dwordx4 v221, s[12:15], s63 offen offset:1024 lds\n"
"M 44,176,136\nR 170,185,53248\nv_mfma_f32_16x16x32_bf16 v[152:155], v[240:243], v[210:213], v[152:155]\ns_add_u32 s63, s100, 0\nM 44,180,152\ns_add_u32 m0, s50, 0\n"
"M 156,176,132\nR 172,185,57344\nv_mfma_f32_16x16x32_bf16 v[156:159], v[242:245], v[210:213], v[156:159]\nM 156,180,148\nbuffer_load_dwordx4 v222, s[12:15], s63 offen lds\nM 140,176,128\n"
"R 174,185,61440\nM 140,180,144\nG 4\nM 128,160,160\nR 176,186,49152\nM 128,164,176\n"
"M 144,160,164\nR 178,186,53248\nM 144,164,180\nbuffer_load_dwordx4 v223, s[12:15], s63 offen offset:1024 lds\nM 32,160,168\nR 180,186,57344\n"
"M 32,164,184\nM 48,160,172\nR 182,186,61440\nM 48,164,188\nG 4\nM 52,168,172\n"
"R 160,187,49152\nM 52,172,188\nM 36,168,168\nR 162,187,53248\nM 36,172,184\ns_movk_i32 s63, 2048\n"
"v_mad_u32_u24 v220, v228, s63, v229\nv_add_u32 v221, 1047552, v220\ns_add_u32 m0, s50, 8192\ns_add_u32 s63, s53, 0\nbuffer_load_dwordx4 v220, s[8:11], s63 offen lds\nM 148,168,164\n"
"R 164,187,57344\ns_lshl_b32 s60, s40, 12\ns_add_u32 s60, s60, 49152\nv_add_u32 v226, s60, v252\nv_add_u32 v111, s60, v227\nM 148,172,180\n"
"M 132,168,160\nR 166,187,61440\ns_cmp_eq_u32 s40, 0\ns_cselect_b64 s[88:89], 0, -1\ns_cselect_b32 s60, s41, 0\ns_cmp_lg_u32 s60, 0\n"
"s_cselect_b64 s[66:67], -1, 0\nM 132,172,176\nG 4\nM 136,176,160\nR 168,188,0\nM 136,180,176\n"
"s_mov_b64 exec, s[88:89]\nds_read_b128 v[230:233], v226 offset:0\nds_read_b64 v[64:65], v111 offset:2048\ns_mov_b64 exec, -1\nM 152,176,164\nR 170,188,4096\n"
"s_waitcnt vmcnt(12) lgkmcnt(1)\ns_barrier\nM 152,180,180\nbuffer_load_dwordx4 v221, s[8:11], s63 offen offset:1024 lds\nM 40,176,168\nR 172,188,8192\n"
"M 40,180,184\ns_mov_b64 exec, s[88:89]\nds_read_b64 v[70:71], v111 offset:2560\nds_read_b128 v[234:237], v226 offset:1024\ns_mov_b64 exec, -1\nM 56,176,172\n"
"R 174,188,12288\nM 56,180,188\nM 60,160,172\nR 176,189,0\nM 60,164,188\ns_mov_b64 exec, s[88:89]\n"
"ds_read_b64 v[76:77], v111 offset:3072\nds_read_b64 v[82:83], v111 offset:3584\ns_mov_b64 exec, -1\nM 44,160,168\nR 178,189,4096\nM 44,164,184\n"
"M 156,160,164\nR 180,189,8192\nM 156,164,180\nM 140,160,160\nR 182,189,12288\nM 140,164,176\n"
"G 6\nM 32,168,200\nR 160,190,0\ns_mov_b64 exec, s[66:67]\nbuffer_load_dwordx4 v[230:233], v252, s[20:23], s65 offen offset:0 sc1\nbuffer_load_dwordx2 v[64:65], v227, s[20:23], s65 offen offset:2048 sc1\n"
"s_mov_b64 exec, -1\nM 32,172,216\nR 162,190,4096\nM 48,168,204\nR 164,190,8192\nM 48,172,220\n"
"R 166,190,12288\nG 4\nM 52,176,204\nR 168,191,0\nM 52,180,220\nR 170,191,4096\n"
"s_mov_b64 exec, s[66:67]\nbuffer_load_dwordx2 v[70:71], v227, s[20:23], s65 offen offset:2560 sc1\nbuffer_load_dwordx4 v[234:237], v252, s[20:23], s65 offen offset:1024 sc1\ns_mov_b64 exec, -1\nM 36,176,200\nR 172,191,8192\n"
"M 36,180,216\nR 174,191,12288\nG 4\nM 40,160,200\nR 176,188,16384\nM 40,164,216\n"
"R 178,188,20480\nM 56,160,204\nR 180,188,24576\ns_mov_b64 exec, s[66:67]\nbuffer_load_dwordx2 v[76:77], v227, s[20:23], s65 offen offset:3072 sc1\nbuffer_load_dwordx2 v[82:83], v227, s[20:23], s65 offen offset:3584 sc1\n"
"s_mov_b64 exec, -1\nM 56,164,220\nR 182,188,28672\nG 4\nM 60,168,204\nR 160,189,16384\n"
"M 60,172,220\nR 162,189,20480\nM 44,168,200\nR 164,189,24576\nM 44,172,216\nR 166,189,28672\n"
"G 4\nM 32,176,232\nR 168,190,16384\nLq188:\ns_lshr_b32 s60, s45, 3\ns_lshl2_add_u32 s60, s60, s40\n"
"s_lshl_b32 s60, s60, 8\ns_waitcnt vmcnt(11)\nv_lshl_add_u32 v224, v252, 3, s60\nv_add_u32 v224, Lq188+4096-LqPC, v224\ns_mov_b64 exec, 3\nglobal_load_dword v224, v224, s[98:99]\n"
"s_mov_b64 exec, -1\nM 32,180,248\nR 170,190,20480\nM 48,176,236\nR 172,190,24576\nM 48,180,252\n"
"R 174,190,28672\nG 4\nM 52,160,236\nR 176,191,16384\nM 52,164,252\nR 178,191,20480\n"
"M 36,160,232\nR 180,191,24576\nM 36,164,248\nR 182,191,28672\nG 4\nM 40,168,232\n"
"R 160,188,0\nM 40,172,248\nR 162,188,4096\nM 56,168,236\nR 164,188,8192\nM 56,172,252\n"
"R 166,188,12288\nG 4\nM 60,176,236\nR 168,189,0\nM 60,180,252\nR 170,189,4096\n"
"M 44,176,232\nR 172,189,8192\nM 44,180,248\nR 174,189,12288\nG 4\nM 128,160,192\n"
"R 176,190,0\nM 128,164,208\nR 178,190,4096\ns_mov_b32 m0, s51\nM 144,160,196\nR 180,190,8192\n"
"ds_write_addtid_b32 v32 offset:0\nM 144,164,212\nR 182,190,12288\nds_write_addtid_b32 v33 offset:256\nG 6\nM 148,168,196\n"
"R 160,191,0\nds_write_addtid_b32 v34 offset:512\nM 148,172,212\nR 162,191,4096\nds_write_addtid_b32 v35 offset:768\nM 132,168,192\n"
"R 164,191,8192\nds_write_addtid_b32 v36 offset:1024\nM 132,172,208\nR 166,191,12288\nds_write_addtid_b32 v37 offset:1280\nG 9\n"
"M 136,176,192\nR 168,188,16384\nds_write_addtid_b32 v38 offset:1536\nM 136,180,208\nR 170,188,20480\nds_write_addtid_b32 v39 offset:1792\n"
"M 152,176,196\nR 172,188,24576\nds_write_addtid_b32 v40 offset:2048\nM 152,180,212\nR 174,188,28672\nds_write_addtid_b32 v41 offset:2304\n"
"G 9\nM 156,160,196\nR 176,189,16384\nds_write_addtid_b32 v42 offset:2560\nM 156,164,212\nR 178,189,20480\n"
"ds_write_addtid_b32 v43 offset:2816\nM 140,160,192\nR 180,189,24576\nds_write_addtid_b32 v44 offset:3072\nds_write_addtid_b32 v45 offset:3328\nds_write_addtid_b32 v46 offset:3584\n"
"ds_write_addtid_b32 v47 offset:3840\nds_write_addtid_b32 v48 offset:4096\nds_write_addtid_b32 v49 offset:4352\nds_write_addtid_b32 v50 offset:4608\nds_write_addtid_b32 v51 offset:4864\nds_write_addtid_b32 v52 offset:5120\n"
"ds_write_addtid_b32 v53 offset:5376\nds_write_addtid_b32 v54 offset:5632\nds_write_addtid_b32 v55 offset:5888\nds_write_addtid_b32 v56 offset:6144\nds_write_addtid_b32 v57 offset:6400\nds_write_addtid_b32 v58 offset:6656\n"
"ds_write_addtid_b32 v59 offset:6912\nds_write_addtid_b32 v60 offset:7168\nds_write_addtid_b32 v61 offset:7424\nds_write_addtid_b32 v62 offset:7680\nds_write_addtid_b32 v63 offset:7936\nM 140,164,208\n"
"R 182,189,28672\nG 15\nM 128,168,224\nR 160,190,16384\ns_add_u32 m0, s50, 16384\nM 128,172,240\n"
"R 162,190,20480\nG 3\ns_barrier\ns_add_u32 s63, s53, 65536\nM 144,168,228\nR 164,190,24576\n"
"buffer_load_dwordx4 v220, s[8:11], s63 offen lds\nds_read2st64_b32 v[32:33], v225 offset0:0 offset1:1\nM 144,172,244\nR 166,190,28672\nbuffer_load_dwordx4 v221, s[8:11], s63 offen offset:1024 lds\nds_read2st64_b32 v[34:35], v225 offset0:2 offset1:3\n"
"G 6\nM 148,176,228\nR 168,191,16384\ns_add_u32 m0, s50, 24576\ns_add_u32 s63, s53, 131072\nbuffer_load_dwordx4 v220, s[8:11], s63 offen lds\n"
"ds_read2st64_b32 v[36:37], v225 offset0:4 offset1:5\nM 148,180,244\nR 170,191,20480\nbuffer_load_dwordx4 v221, s[8:11], s63 offen offset:1024 lds\nds_read2st64_b32 v[38:39], v225 offset0:6 offset1:7\nM 132,176,224\n"
"R 172,191,24576\ns_add_u32 m0, s50, 32768\ns_add_u32 s63, s53, 196608\nbuffer_load_dwordx4 v220, s[8:11], s63 offen lds\nds_read2st64_b32 v[40:41], v225 offset0:8 offset1:9\nM 132,180,240\n"
"R 174,191,28672\nbuffer_load_dwordx4 v221, s[8:11], s63 offen offset:1024 lds\nds_read2st64_b32 v[42:43], v225 offset0:10 offset1:11\nG 9\nM 136,160,224\ns_add_u32 m0, s50, 40960\n"
"s_add_u32 s63, s53, 262144\nbuffer_load_dwordx4 v220, s[8:11], s63 offen lds\nM 136,164,240\nbuffer_load_dwordx4 v221, s[8:11], s63 offen offset:1024 lds\nM 152,160,228\ns_add_u32 m0, s50, 49152\n"
"s_add_u32 s63, s53, 327680\nbuffer_load_dwordx4 v220, s[8:11], s63 offen lds\nM 152,164,244\nds_read2st64_b32 v[44:45], v225 offset0:12 offset1:13\nds_read2st64_b32 v[46:47], v225 offset0:14 offset1:15\nG 3\n"
"M 156,168,228\nds_read2st64_b32 v[48:49], v225 offset0:16 offset1:17\nds_read2st64_b32 v[50:51], v225 offset0:18 offset1:19\nM 156,172,244\nds_read2st64_b32 v[52:53], v225 offset0:20 offset1:21\nds_read2st64_b32 v[54:55], v225 offset0:22 offset1:23\n"
"M 140,168,224\nds_read2st64_b32 v[56:57], v225 offset0:24 offset1:25\nds_read2st64_b32 v[58:59], v225 offset0:26 offset1:27\nM 140,172,240\nds_read2st64_b32 v[60:61], v225 offset0:28 offset1:29\nds_read2st64_b32 v[62:63], v225 offset0:30 offset1:31\n"
"G 0\ns_barrier\nv_pk_add_f32 v[144:145], v[144:145], v[48:49]\nv_pk_add_f32 v[146:147], v[146:147], v[50:51]\nv_pk_add_f32 v[148:149], v[148:149], v[52:53]\nv_pk_add_f32 v[150:151], v[150:151], v[54:55]\n"
"v_pk_add_f32 v[152:153], v[152:153], v[56:57]\nv_pk_add_f32 v[154:155], v[154:155], v[58:59]\nv_pk_add_f32 v[156:157], v[156:157], v[60:61]\nv_pk_add_f32 v[158:159], v[158:159], v[62:63]\nv_pk_add_f32 v[128:129], v[128:129], v[32:33]\nv_pk_add_f32 v[130:131], v[130:131], v[34:35]\n"
"v_pk_add_f32 v[132:133], v[132:133], v[36:37]\nv_pk_add_f32 v[134:135], v[134:135], v[38:39]\nv_pk_add_f32 v[136:137], v[136:137], v[40:41]\nv_pk_add_f32 v[138:139], v[138:139], v[42:43]\nv_pk_add_f32 v[140:141], v[140:141], v[44:45]\nv_pk_add_f32 v[142:143], v[142:143], v[46:47]\n"
"v_cvt_pk_bf16_f32 v144, v144, v145\nv_cvt_pk_bf16_f32 v145, v146, v147\nv_cvt_pk_bf16_f32 v146, v148, v149\nv_cvt_pk_bf16_f32 v147, v150, v151\nv_cvt_pk_bf16_f32 v152, v152, v153\nv_cvt_pk_bf16_f32 v153, v154, v155\n"
"v_cvt_pk_bf16_f32 v154, v156, v157\nv_cvt_pk_bf16_f32 v155, v158, v159\nv_cvt_pk_bf16_f32 v128, v128, v129\nv_cvt_pk_bf16_f32 v129, v130, v131\nv_cvt_pk_bf16_f32 v130, v132, v133\nv_cvt_pk_bf16_f32 v131, v134, v135\n"
"v_cvt_pk_bf16_f32 v136, v136, v137\nv_cvt_pk_bf16_f32 v137, v138, v139\nv_cvt_pk_bf16_f32 v138, v140, v141\nv_cvt_pk_bf16_f32 v139, v142, v143\nv_mfma_f32_16x16x32_bf16 v[52:55], v[16:19], v[144:147], v[96:99]\nv_mfma_f32_16x16x32_bf16 v[48:51], v[0:3], v[144:147], v[100:103]\n"
"v_mfma_f32_16x16x32_bf16 v[60:63], v[20:23], v[152:155], v[96:99]\nv_mfma_f32_16x16x32_bf16 v[56:59], v[4:7], v[152:155], v[100:103]\ns_waitcnt vmcnt(13)\nv_max3_u32 v248, v233, v65, v71\nv_cmp_eq_u32 vcc, -1, v248\ns_nop 0\n"
"s_cbranch_vccnz Lq190\nLq191:\ns_waitcnt vmcnt(10)\nv_max3_u32 v248, v237, v77, v83\nv_cmp_eq_u32 vcc, -1, v248\ns_nop 0\n"
"s_cbranch_vccnz Lq192\nLq193:\nv_mfma_f32_16x16x32_bf16 v[36:39], v[0:3], v[128:131], v[96:99]\nv_mfma_f32_16x16x32_bf16 v[32:35], v[230:233], v[128:131], v[104:107]\nv_mfma_f32_16x16x32_bf16 v[44:47], v[4:7], v[136:139], v[96:99]\nv_mfma_f32_16x16x32_bf16 v[40:43], v[234:237], v[136:139], v[104:107]\n"
"s_lshl_b32 s84, s42, 11\ns_add_u32 s84, s84, s49\ns_add_u32 s84, s84, 2097152\ns_add_u32 s85, s84, 32768\ns_lshl2_add_u32 s60, s45, s40\ns_waitcnt vmcnt(9)\n"
"v_lshlrev_b32 v224, 3, v252\ns_mul_i32 s60, s60, 7168\ns_bfm_b64 exec, 28, 28\nbuffer_load_dword v224, v224, s[16:19], s60 offen\ns_mov_b64 exec, -1\nv_mov_b32 v238, s46\n"
"v_mov_b32 v239, s47\nv_max_f32 v52, v52, v53\nv_max_f32 v54, v54, v55\nv_max3_f32 v52, v52, v48, v49\nv_max3_f32 v54, v54, v50, v51\nv_max_f32 v118, v52, v54\n"
"v_max_f32 v60, v60, v61\nv_max_f32 v62, v62, v63\nv_max3_f32 v60, v60, v56, v57\nv_max3_f32 v62, v62, v58, v59\nv_max_f32 v119, v60, v62\nv_max_f32 v36, v36, v37\n"
"v_max_f32 v38, v38, v39\nv_max3_f32 v36, v36, v32, v33\nv_max3_f32 v38, v38, v34, v35\nv_max_f32 v116, v36, v38\nv_max_f32 v44, v44, v45\nv_max_f32 v46, v46, v47\n"
"v_max3_f32 v44, v44, v40, v41\nv_max3_f32 v46, v46, v42, v43\nv_max_f32 v117, v44, v46\nv_permlane32_swap_b32 v116, v118\ns_nop 0\nv_permlane32_swap_b32 v117, v119\n"
"v_max_f32 v116, v116, v118\nv_max_f32 v117, v117, v119\ns_nop 1\nv_permlane16_swap_b32 v116, v117\nv_max_f32 v116, v116, v117\nv_mov_b32 v117, v116\n"
"s_nop 1\nv_permlane16_swap_b32 v116, v117\nv_mov_b32 v118, v116\nv_mov_b32 v119, v117\ns_nop 0\nv_permlane32_swap_b32 v116, v118\n"
"v_permlane32_swap_b32 v117, v119\nv_mul_f32 v116, v116, v215\nv_mul_f32 v118, v118, v218\nv_mul_f32 v117, v117, v215\nv_mul_f32 v119, v119, v218\nv_fma_f32 v52, v52, v218, -v118\n"
"v_fma_f32 v54, v54, v218, -v118\nv_fma_f32 v60, v60, v218, -v119\nv_fma_f32 v62, v62, v218, -v119\nv_fma_f32 v36, v36, v215, -v116\nv_fma_f32 v38, v38, v215, -v116\nv_fma_f32 v44, v44, v215, -v117\n"
"v_fma_f32 v46, v46, v215, -v117\nv_exp_f32 v52, v52\nv_exp_f32 v54, v54\nv_exp_f32 v60, v60\nv_exp_f32 v62, v62\nv_exp_f32 v36, v36\n"
"v_exp_f32 v38, v38\nv_exp_f32 v44, v44\nv_exp_f32 v46, v46\nv_add_f32 v118, v52, v54\nv_add_f32 v119, v60, v62\nv_add_f32 v116, v36, v38\n"
"v_add_f32 v117, v44, v46\ns_nop 0\nv_permlane32_swap_b32 v116, v118\nv_permlane32_swap_b32 v117, v119\nv_add_f32 v116, v116, v118\nv_add_f32 v117, v117, v119\n"
"s_nop 1\nv_permlane16_swap_b32 v116, v117\nv_add_f32 v116, v116, v117\nv_rcp_f32 v116, v116\ns_nop 0\nv_mov_b32 v117, v116\n"
"s_nop 1\nv_permlane16_swap_b32 v116, v117\nv_mov_b32 v118, v116\nv_mov_b32 v119, v117\ns_nop 0\nv_permlane32_swap_b32 v116, v118\n"
"v_permlane32_swap_b32 v117, v119\nv_mul_f32 v52, v52, v118\nv_mul_f32 v54, v54, v118\nv_mul_f32 v60, v60, v119\nv_mul_f32 v62, v62, v119\nv_mul_f32 v36, v36, v116\n"
"v_mul_f32 v38, v38, v116\nv_mul_f32 v44, v44, v117\nv_mul_f32 v46, v46, v117\nv_cvt_pk_bf16_f32 v52, v52, v52\nv_cvt_pk_bf16_f32 v54, v54, v54\nv_cvt_pk_bf16_f32 v60, v60, v60\n"
"v_cvt_pk_bf16_f32 v62, v62, v62\nv_cvt_pk_bf16_f32 v36, v36, v36\nv_cvt_pk_bf16_f32 v38, v38, v38\nv_cvt_pk_bf16_f32 v44, v44, v44\nv_cvt_pk_bf16_f32 v46, v46, v46\nv_and_b32 v48, v52, v112\n"
"v_and_b32 v49, v54, v113\nv_and_b32 v50, v52, v114\nv_and_b32 v51, v54, v115\nv_and_b32 v56, v60, v112\nv_and_b32 v57, v62, v113\nv_and_b32 v58, v60, v114\n"
"v_and_b32 v59, v62, v115\nv_mfma_f32_16x16x32_bf16 v[144:147], v[66:69], v[48:51], 0\nv_mfma_f32_16x16x32_bf16 v[148:151], v[72:75], v[48:51], 0\nv_and_b32 v32, v36, v112\nv_and_b32 v33, v38, v113\nv_and_b32 v34, v36, v114\n"
"v_and_b32 v35, v38, v115\nv_mfma_f32_16x16x32_bf16 v[152:155], v[78:81], v[56:59], 0\nv_mfma_f32_16x16x32_bf16 v[156:159], v[84:87], v[56:59], 0\nv_and_b32 v40, v44, v112\nv_and_b32 v41, v46, v113\nv_and_b32 v42, v44, v114\n"
"v_and_b32 v43, v46, v115\nv_mfma_f32_16x16x32_bf16 v[128:131], v[64:67], v[32:35], 0\nv_mfma_f32_16x16x32_bf16 v[132:135], v[70:73], v[32:35], 0\nv_mfma_f32_16x16x32_bf16 v[136:139], v[76:79], v[40:43], 0\nv_mfma_f32_16x16x32_bf16 v[140:143], v[82:85], v[40:43], 0\nv_cvt_pk_bf16_f32 v144, v144, v145\n"
"v_cvt_pk_bf16_f32 v145, v146, v147\nv_cvt_pk_bf16_f32 v146, v148, v149\nv_cvt_pk_bf16_f32 v147, v150, v151\nbuffer_store_dwordx4 v[144:147], v252, s[20:23], s85 offen offset:0 sc1\nv_cvt_pk_bf16_f32 v152, v152, v153\nv_cvt_pk_bf16_f32 v153, v154, v155\n"
"v_cvt_pk_bf16_f32 v154, v156, v157\nv_cvt_pk_bf16_f32 v155, v158, v159\nbuffer_store_dwordx4 v[152:155], v252, s[20:23], s85 offen offset:1024 sc1\nv_cvt_pk_bf16_f32 v128, v128, v129\nv_cvt_pk_bf16_f32 v129, v130, v131\nv_cvt_pk_bf16_f32 v130, v132, v133\n"
"v_cvt_pk_bf16_f32 v131, v134, v135\nbuffer_store_dwordx4 v[128:131], v252, s[20:23], s84 offen offset:0 sc1\nv_cvt_pk_bf16_f32 v136, v136, v137\nv_cvt_pk_bf16_f32 v137, v138, v139\nv_cvt_pk_bf16_f32 v138, v140, v141\nv_cvt_pk_bf16_f32 v139, v142, v143\n"
"buffer_store_dwordx4 v[136:139], v252, s[20:23], s84 offen offset:1024 sc1\ns_add_u32 s61, s79, 88080384\ns_lshl_b32 s62, s42, 3\ns_add_u32 s61, s61, s62\ns_waitcnt vmcnt(0)\ns_mov_b64 exec, 1\n"
"buffer_atomic_swap_x2 v[238:239], off, s[20:23], s61 sc1\ns_mov_b64 exec, -1\nv_bfe_u32 v250, v252, 7, 1\nv_mul_u32_u24 v250, 2176, v250\nv_bfe_u32 v251, v252, 4, 3\nv_lshl_add_u32 v250, v251, 3, v250\n"
"s_lshl_b32 s60, s90, 6\nv_add_u32 v250, s60, v250\ns_sleep 5\nv_mov_b32 v248, 0\nv_mov_b32 v249, 0\ns_add_u32 s61, s80, 88080384\n"
"s_mov_b64 exec, 0xffff\nbuffer_atomic_or_x2 v[248:249], v250, s[20:23], s61 offen sc0 sc1\ns_mov_b64 exec, -1\nbuffer_load_dwordx4 v221, s[8:11], s63 offen offset:1024 lds\ns_add_u32 m0, s50, 57344\ns_add_u32 s63, s53, 393216\n"
"buffer_load_dwordx4 v220, s[8:11], s63 offen lds\nbuffer_load_dwordx4 v221, s[8:11], s63 offen offset:1024 lds\ns_add_u32 m0, s50, 65536\ns_add_u32 s63, s53, 458752\nbuffer_load_dwordx4 v220, s[8:11], s63 offen lds\nbuffer_load_dwordx4 v221, s[8:11], s63 offen offset:1024 lds\n"
"s_add_u32 m0, s50, 73728\ns_add_u32 s63, s53, 524288\nbuffer_load_dwordx4 v220, s[8:11], s63 offen lds\nbuffer_load_dwordx4 v221, s[8:11], s63 offen offset:1024 lds\ns_add_u32 m0, s50, 81920\ns_add_u32 s63, s53, 589824\n"
"buffer_load_dwordx4 v220, s[8:11], s63 offen lds\nbuffer_load_dwordx4 v221, s[8:11], s63 offen offset:1024 lds\ns_add_u32 m0, s50, 90112\ns_add_u32 s63, s53, 655360\nbuffer_load_dwordx4 v220, s[8:11], s63 offen lds\nbuffer_load_dwordx4 v221, s[8:11], s63 offen offset:1024 lds\n"
"v_bfe_u32 v234, v252, 4, 4\nv_lshrrev_b32 v235, 8, v252\nv_lshlrev_b32 v235, 2, v235\nv_sub_u32 v234, v234, v235\nv_lshlrev_b32 v235, 4, v234\ns_mov_b64 s[60:61], 0x3f80\n"
"v_lshlrev_b64 v[208:209], v235, s[60:61]\nv_cmp_gt_u32 s[60:61], 4, v234\ns_nop 1\nv_cndmask_b32 v212, 0, v208, s[60:61]\nv_cndmask_b32 v208, 0, v208, s[60:61]\nv_cndmask_b32 v213, 0, v209, s[60:61]\n"
"v_cndmask_b32 v209, 0, v209, s[60:61]\ns_nop 1\nv_mfma_f32_16x16x32_bf16 v[0:3], v[208:211], v[192:195], 0\nv_mfma_f32_16x16x32_bf16 v[4:7], v[210:213], v[192:195], 0\nv_mfma_f32_16x16x32_bf16 v[8:11], v[208:211], v[196:199], 0\nv_mfma_f32_16x16x32_bf16 v[12:15], v[210:213], v[196:199], 0\n"
"v_mfma_f32_16x16x32_bf16 v[16:19], v[208:211], v[200:203], 0\nv_mfma_f32_16x16x32_bf16 v[20:23], v[210:213], v[200:203], 0\nv_mfma_f32_16x16x32_bf16 v[24:27], v[208:211], v[204:207], 0\nv_mfma_f32_16x16x32_bf16 v[28:31], v[210:213], v[204:207], 0\ns_waitcnt vmcnt(11)\nv_cmp_eq_u64 s[86:87], s[46:47], v[248:249]\n"
"s_nop 0\ns_and_b32 s62, s86, 0xffff\ns_cmp_eq_u32 s62, 0xffff\ns_cbranch_scc0 Lq195\nLq196:\ns_add_u32 s64, s70, 2097152\n"
"buffer_load_dwordx4 a[0:3], v252, s[20:23], s64 offen offset:0 sc1\ns_add_u32 s65, s70, 2129920\nbuffer_load_dwordx4 a[4:7], v252, s[20:23], s65 offen offset:0 sc1\ns_add_u32 s66, s71, 2097152\nbuffer_load_dwordx4 a[8:11], v252, s[20:23], s66 offen offset:0 sc1\ns_add_u32 s67, s71, 2129920\n"
"buffer_load_dwordx4 a[12:15], v252, s[20:23], s67 offen offset:0 sc1\nbuffer_load_dwordx4 a[16:19], v252, s[20:23], s64 offen offset:1024 sc1\nbuffer_load_dwordx4 a[20:23], v252, s[20:23], s65 offen offset:1024 sc1\nbuffer_load_dwordx4 a[24:27], v252, s[20:23], s66 offen offset:1024 sc1\nbuffer_load_dwordx4 a[28:31], v252, s[20:23], s67 offen offset:1024 sc1\ns_barrier\n"
"R 160,184,8192\nR 162,184,12288\nR 164,184,16384\nR 166,184,20480\nR 168,185,8192\nR 170,185,12288\n"
"R 172,185,16384\nR 174,185,20480\nR 176,186,8192\nR 178,186,12288\nR 180,186,16384\nR 182,186,20480\n"
"buffer_load_dwordx4 a[32:35], v252, s[20:23], s64 offen offset:2048 sc1\nbuffer_load_dwordx4 a[36:39], v252, s[20:23], s65 offen offset:2048 sc1\nbuffer_load_dwordx4 a[40:43], v252, s[20:23], s66 offen offset:2048 sc1\nbuffer_load_dwordx4 a[44:47], v252, s[20:23], s67 offen offset:2048 sc1\nbuffer_load_dwordx4 a[48:51], v252, s[20:23], s64 offen offset:3072 sc1\nbuffer_load_dwordx4 a[52:55], v252, s[20:23], s65 offen offset:3072 sc1\n"
"buffer_load_dwordx4 a[56:59], v252, s[20:23], s66 offen offset:3072 sc1\nbuffer_load_dwordx4 a[60:63], v252, s[20:23], s67 offen offset:3072 sc1\ns_add_u32 s64, s70, 2101248\nbuffer_load_dwordx4 a[64:67], v252, s[20:23], s64 offen offset:0 sc1\ns_add_u32 s65, s70, 2134016\nbuffer_load_dwordx4 a[68:71], v252, s[20:23], s65 offen offset:0 sc1\n"
"s_add_u32 s66, s71, 2101248\nbuffer_load_dwordx4 a[72:75], v252, s[20:23], s66 offen offset:0 sc1\ns_add_u32 s67, s71, 2134016\nbuffer_load_dwordx4 a[76:79], v252, s[20:23], s67 offen offset:0 sc1\ns_waitcnt vmcnt(19) lgkmcnt(10)\nM 0,160,0\n"
"buffer_load_dwordx4 a[80:83], v252, s[20:23], s64 offen offset:1024 sc1\ns_waitcnt vmcnt(16) lgkmcnt(8)\nM 0,164,16\nM 16,160,4\ns_lshl_b32 s60, s40, 5\nv_xor_b32 v238, s60, v188\n"
"v_and_b32 v238, 0xfffffbff, v238\ns_waitcnt vmcnt(15)\nM 16,164,20\nv_mov_b32 v242, s76\nv_mov_b32 v243, s76\nv_mfma_f32_16x16x32_bf16 v[32:35], v[160:163], a[8:11], 0\n"
"buffer_load_dwordx4 a[84:87], v252, s[20:23], s65 offen offset:1024 sc1\ns_waitcnt vmcnt(15)\nM 32,164,24\nv_mov_b32 v244, s76\nv_mov_b32 v245, s76\nv_mfma_f32_16x16x32_bf16 v[48:51], v[160:163], a[12:15], 0\n"
"s_waitcnt vmcnt(14)\nM 48,164,28\nG 4\nv_mfma_f32_16x16x32_bf16 v[52:55], v[168:171], a[12:15], 0\nR 160,187,8192\nM 52,172,28\n"
"buffer_load_dwordx4 a[88:91], v252, s[20:23], s66 offen offset:1024 sc1\nv_mfma_f32_16x16x32_bf16 v[36:39], v[168:171], a[8:11], 0\nR 162,187,12288\nM 36,172,24\nM 20,168,4\nR 164,187,16384\n"
"R 64,238,32768\nR 66,238,36864\nM 20,172,20\nbuffer_load_dwordx4 a[92:95], v252, s[20:23], s67 offen offset:1024 sc1\nM 4,168,0\nR 166,187,20480\n"
"R 68,238,33792\nR 70,238,37888\nM 4,172,16\nG 8\nM 8,176,0\nR 168,184,24576\n"
"M 8,180,16\nbuffer_load_dwordx4 a[96:99], v252, s[20:23], s64 offen offset:2048 sc1\nM 24,176,4\nR 170,184,28672\nM 24,180,20\nv_mfma_f32_16x16x32_bf16 v[40:43], v[176:179], a[8:11], 0\n"
"R 172,184,32768\nG 6\nv_mfma_f32_16x16x32_bf16 v[72:75], v[242:245], v[64:67], 0\nM 40,180,24\nG 3\nv_mfma_f32_16x16x32_bf16 v[72:75], v[242:245], v[68:71], v[72:75]\n"
"buffer_load_dwordx4 a[100:103], v252, s[20:23], s65 offen offset:2048 sc1\nv_mfma_f32_16x16x32_bf16 v[56:59], v[176:179], a[12:15], 0\nR 174,184,36864\nM 56,180,28\nv_mfma_f32_16x16x32_bf16 v[60:63], v[160:163], a[12:15], 0\nR 176,185,24576\n"
"M 60,164,28\nbuffer_load_dwordx4 a[104:107], v252, s[20:23], s66 offen offset:2048 sc1\nv_lshrrev_b32 v64, 1, v252\nv_mfma_f32_16x16x32_bf16 v[44:47], v[160:163], a[8:11], 0\nR 178,185,28672\nv_mov_b32 v73, s46\n"
"s_and_b32 s61, s40, 3\ns_lshl_b32 s61, s61, 7\ns_add_u32 s61, s61, s91\ns_add_u32 s61, s61, 393216\ns_mov_b64 exec, 0xffff\nbuffer_store_dwordx2 v[72:73], v64, s[20:23], s61 offen sc1\n"
"s_mov_b64 exec, -1\nM 44,164,24\nM 28,160,4\nR 180,185,32768\nM 28,164,20\nbuffer_load_dwordx4 a[108:111], v252, s[20:23], s67 offen offset:2048 sc1\n"
"M 12,160,0\nR 182,185,36864\nM 12,164,16\ns_lshl_b32 s60, s40, 5\nv_xor_b32 v238, s60, v188\nG 4\n"
"s_waitcnt vmcnt(20)\nM 0,168,32\nR 160,186,24576\nv_and_b32 v238, 0xfffffbff, v238\ns_waitcnt vmcnt(16)\nM 0,172,48\n"
"buffer_load_dwordx4 a[112:115], v252, s[20:23], s64 offen offset:3072 sc1\nM 16,168,36\nR 162,186,28672\ns_waitcnt vmcnt(16)\nM 16,172,52\nM 32,168,40\n"
"R 164,186,32768\ns_waitcnt vmcnt(15)\nM 32,172,56\nbuffer_load_dwordx4 a[116:119], v252, s[20:23], s65 offen offset:3072 sc1\ns_barrier\nM 48,168,44\n"
"R 166,186,36864\nR 64,238,40960\nR 66,238,45056\ns_waitcnt vmcnt(15)\nM 48,172,60\nG 6\n"
"M 52,176,44\nR 168,187,24576\nR 68,238,41984\nR 70,238,46080\nM 52,180,60\nbuffer_load_dwordx4 a[120:123], v252, s[20:23], s66 offen offset:3072 sc1\n"
"M 36,176,40\nR 170,187,28672\nM 36,180,56\nM 20,176,36\nR 172,187,32768\nM 20,180,52\n"
"buffer_load_dwordx4 a[124:127], v252, s[20:23], s67 offen offset:3072 sc1\nM 4,176,32\nR 174,187,36864\nG 6\nv_mfma_f32_16x16x32_bf16 v[72:75], v[242:245], v[64:67], 0\nM 4,180,48\n"
"G 3\nv_mfma_f32_16x16x32_bf16 v[72:75], v[242:245], v[68:71], v[72:75]\nM 8,160,32\nR 176,184,40960\nM 8,164,48\ns_add_u32 s64, s70, 2105344\n"
"buffer_load_dwordx4 a[128:131], v252, s[20:23], s64 offen offset:0 sc1\nM 24,160,36\nR 178,184,45056\nM 24,164,52\nv_lshrrev_b32 v64, 1, v252\nv_mov_b32 v73, s46\n"
"s_and_b32 s61, s40, 3\ns_lshl_b32 s61, s61, 7\ns_add_u32 s61, s61, s91\ns_add_u32 s61, s61, 524288\ns_mov_b64 exec, 0xffff\nbuffer_store_dwordx2 v[72:73], v64, s[20:23], s61 offen sc1\n"
"s_mov_b64 exec, -1\nM 40,160,40\nR 180,184,49152\nM 40,164,56\ns_add_u32 s65, s70, 2138112\nbuffer_load_dwordx4 a[132:135], v252, s[20:23], s65 offen offset:0 sc1\n"
"M 56,160,44\nR 182,184,53248\nM 56,164,60\ns_lshl_b32 s60, s40, 5\nv_xor_b32 v238, s60, v188\nG 4\n"
"M 60,168,44\nR 160,185,40960\nv_and_b32 v238, 0xfffffbff, v238\nM 60,172,60\ns_add_u32 s66, s71, 2105344\nbuffer_load_dwordx4 a[136:139], v252, s[20:23], s66 offen offset:0 sc1\n"
"M 44,168,40\nR 162,185,45056\nM 44,172,56\nM 28,168,36\nR 164,185,49152\nM 28,172,52\n"
"s_add_u32 s67, s71, 2138112\nbuffer_load_dwordx4 a[140:143], v252, s[20:23], s67 offen offset:0 sc1\nM 12,168,32\nR 166,185,53248\nM 12,172,48\ns_barrier\n"
"G 4\ns_waitcnt vmcnt(21)\nM 0,176,64\nR 168,186,40960\nR 64,238,49152\nR 66,238,53248\n"
"s_waitcnt vmcnt(17)\nM 0,180,80\nbuffer_load_dwordx4 a[144:147], v252, s[20:23], s64 offen offset:1024 sc1\nM 16,176,68\nR 170,186,45056\nR 68,238,50176\n"
"R 70,238,54272\ns_waitcnt vmcnt(17)\nM 16,180,84\nM 32,176,72\nR 172,186,49152\ns_waitcnt vmcnt(16)\n"
"M 32,180,88\nbuffer_load_dwordx4 a[148:151], v252, s[20:23], s65 offen offset:1024 sc1\nM 48,176,76\nR 174,186,53248\ns_add_u32 m0, s50, 98304\ns_waitcnt vmcnt(16)\n"
"M 48,180,92\nG 8\nM 52,160,76\nR 176,187,40960\nG 6\nv_mfma_f32_16x16x32_bf16 v[72:75], v[242:245], v[64:67], 0\n"
"s_add_u32 s63, s53, 720896\nM 52,164,92\nG 3\nv_mfma_f32_16x16x32_bf16 v[72:75], v[242:245], v[68:71], v[72:75]\nbuffer_load_dwordx4 a[152:155], v252, s[20:23], s66 offen offset:1024 sc1\nM 36,160,72\n"
"R 178,187,45056\nM 36,164,88\nbuffer_load_dwordx4 v220, s[8:11], s63 offen lds\nM 20,160,68\nR 180,187,49152\nM 20,164,84\n"
"buffer_load_dwordx4 a[156:159], v252, s[20:23], s67 offen offset:1024 sc1\nv_lshrrev_b32 v64, 1, v252\nM 4,160,64\nR 182,187,53248\nv_mov_b32 v73, s46\ns_and_b32 s61, s40, 3\n"
"s_lshl_b32 s61, s61, 7\ns_add_u32 s61, s61, s91\ns_add_u32 s61, s61, 655360\ns_mov_b64 exec, 0xffff\nbuffer_store_dwordx2 v[72:73], v64, s[20:23], s61 offen sc1\ns_mov_b64 exec, -1\n"
"M 4,164,80\nG 4\nM 8,168,64\nR 160,184,57344\nM 8,172,80\nbuffer_load_dwordx4 a[160:163], v252, s[20:23], s64 offen offset:2048 sc1\n"
"M 24,168,68\nR 162,184,61440\nbuffer_load_dwordx4 v221, s[8:11], s63 offen offset:1024 lds\nM 24,172,84\nM 40,168,72\nR 164,188,0\n"
"M 40,172,88\nbuffer_load_dwordx4 a[164:167], v252, s[20:23], s65 offen offset:2048 sc1\nM 56,168,76\nR 166,188,4096\nM 56,172,92\ns_and_b32 s60, s41, 1\n"
"s_lshl1_add_u32 s60, s60, s90\ns_lshl_b32 s60, s60, 5\nv_xor_b32 v238, s60, v184\nG 4\nM 60,176,76\nR 168,185,57344\n"
"v_and_b32 v238, 0xfffffbff, v238\nM 60,180,92\nbuffer_load_dwordx4 a[168:171], v252, s[20:23], s66 offen offset:2048 sc1\nM 44,176,72\nR 170,185,61440\nR 64,238,0\n"
"R 66,238,4096\nM 44,180,88\ns_add_u32 m0, s50, 106496\nM 28,176,68\nR 172,189,0\nR 68,238,1024\n"
"R 70,238,5120\nM 28,180,84\nbuffer_load_dwordx4 a[172:175], v252, s[20:23], s67 offen offset:2048 sc1\nM 12,176,64\nR 174,189,4096\ns_add_u32 s63, s53, 786432\n"
"M 12,180,80\ns_barrier\nG 8\ns_waitcnt vmcnt(24)\nM 0,160,96\nR 176,186,57344\n"
"s_waitcnt vmcnt(19)\nM 0,164,112\nbuffer_load_dwordx4 a[176:179], v252, s[20:23], s64 offen offset:3072 sc1\nbuffer_load_dwordx4 v220, s[8:11], s63 offen lds\nM 16,160,100\nR 178,186,61440\n"
"G 6\nv_mfma_f32_16x16x32_bf16 v[72:75], v[242:245], v[64:67], 0\ns_waitcnt vmcnt(20)\nM 16,164,116\nG 3\nv_mfma_f32_16x16x32_bf16 v[72:75], v[242:245], v[68:71], v[72:75]\n"
"buffer_load_dwordx4 v221, s[8:11], s63 offen offset:1024 lds\nM 32,160,104\nR 180,190,0\ns_add_u32 m0, s50, 114688\ns_waitcnt vmcnt(20)\nM 32,164,120\n"
"buffer_load_dwordx4 a[180:183], v252, s[20:23], s65 offen offset:3072 sc1\nM 48,160,108\nR 182,190,4096\ns_add_u32 s63, s53, 851968\ns_waitcnt vmcnt(20)\nM 48,164,124\n"
"buffer_load_dwordx4 v220, s[8:11], s63 offen lds\nG 4\nv_lshrrev_b32 v64, 1, v252\nM 52,168,108\nR 160,187,57344\nv_mov_b32 v73, s46\n"
"s_and_b32 s61, s40, 1\ns_lshl_b32 s61, s61, 7\ns_add_u32 s61, s61, s91\ns_add_u32 s61, s61, 786432\ns_mov_b64 exec, 0xffff\nbuffer_store_dwordx2 v[72:73], v64, s[20:23], s61 offen sc1\n"
"s_mov_b64 exec, -1\nM 52,172,124\nbuffer_load_dwordx4 a[184:187], v252, s[20:23], s66 offen offset:3072 sc1\nM 36,168,104\nR 162,187,61440\nM 36,172,120\n"
"buffer_load_dwordx4 v221, s[8:11], s63 offen offset:1024 lds\nM 20,168,100\nR 164,191,0\nM 20,172,116\nbuffer_load_dwordx4 a[188:191], v252, s[20:23], s67 offen offset:3072 sc1\nM 4,168,96\n"
"R 166,191,4096\nM 4,172,112\nG 4\nM 8,176,96\nR 168,188,8192\nM 8,180,112\n"
"s_add_u32 s64, s70, 2109440\nbuffer_load_dwordx4 a[192:195], v252, s[20:23], s64 offen offset:0 sc1\nM 24,176,100\nR 170,188,12288\nM 24,180,116\nM 40,176,104\n"
"R 172,188,16384\nM 40,180,120\ns_add_u32 s65, s70, 2142208\nbuffer_load_dwordx4 a[196:199], v252, s[20:23], s65 offen offset:0 sc1\nM 56,176,108\nR 174,188,20480\n"
"M 56,180,124\nG 4\nM 60,160,108\nR 176,189,8192\nM 60,164,124\ns_add_u32 s66, s71, 2109440\n"
"buffer_load_dwordx4 a[200:203], v252, s[20:23], s66 offen offset:0 sc1\nM 44,160,104\nR 178,189,12288\ns_add_u32 s63, s53, 917504\nM 44,164,120\ns_add_u32 m0, s50, 0\n"
"M 28,160,100\nR 180,189,16384\nM 28,164,116\ns_add_u32 s67, s71, 2142208\nbuffer_load_dwordx4 a[204:207], v252, s[20:23], s67 offen offset:0 sc1\nM 12,160,96\n"
"R 182,189,20480\nM 12,164,112\ns_waitcnt vmcnt(16)\ns_barrier\nbuffer_load_dwordx4 v220, s[8:11], s63 offen lds\nG 4\n"
"M 0,168,128\nR 160,190,8192\nbuffer_load_dwordx4 v221, s[8:11], s63 offen offset:1024 lds\nM 0,172,144\nbuffer_load_dwordx4 a[208:211], v252, s[20:23], s64 offen offset:1024 sc1\ns_add_u32 m0, s50, 8192\n"
"s_add_u32 s63, s53, 983040\nbuffer_load_dwordx4 v220, s[8:11], s63 offen lds\nM 16,168,132\nR 162,190,12288\nbuffer_load_dwordx4 v221, s[8:11], s63 offen offset:1024 lds\nM 16,172,148\n"
"M 32,168,136\nR 164,190,16384\nM 32,172,152\nbuffer_load_dwordx4 a[212:215], v252, s[20:23], s65 offen offset:1024 sc1\nM 48,168,140\nR 166,190,20480\n"
"M 48,172,156\nG 4\nM 52,176,140\nR 168,191,8192\nM 52,180,156\nbuffer_load_dwordx4 a[216:219], v252, s[20:23], s66 offen offset:1024 sc1\n"
"M 36,176,136\nR 170,191,12288\nM 36,180,152\nM 20,176,132\nR 172,191,16384\nM 20,180,148\n"
"buffer_load_dwordx4 a[220:223], v252, s[20:23], s67 offen offset:1024 sc1\nM 4,176,128\nR 174,191,20480\nM 4,180,144\nG 4\nM 8,160,128\n"
"R 176,188,24576\nM 8,164,144\nbuffer_load_dwordx4 a[224:227], v252, s[20:23], s64 offen offset:2048 sc1\nM 24,160,132\nR 178,188,28672\nM 24,164,148\n"
"M 40,160,136\nR 180,188,32768\nM 40,164,152\nbuffer_load_dwordx4 a[228:231], v252, s[20:23], s65 offen offset:2048 sc1\nM 56,160,140\nR 182,188,36864\n"
"M 56,164,156\nG 4\nM 60,168,140\nR 160,189,24576\nM 60,172,156\nbuffer_load_dwordx4 a[232:235], v252, s[20:23], s66 offen offset:2048 sc1\n"
"M 44,168,136\nR 162,189,28672\nM 44,172,152\nM 28,168,132\nR 164,189,32768\nM 28,172,148\n"
"buffer_load_dwordx4 a[236:239], v252, s[20:23], s67 offen offset:2048 sc1\nM 12,168,128\nR 166,189,36864\nM 12,172,144\nG 4\nM 0,176,160\n"
"R 168,190,24576\ns_waitcnt vmcnt(24)\nM 0,180,176\nbuffer_load_dwordx4 a[240:243], v252, s[20:23], s64 offen offset:3072 sc1\nM 16,176,164\nR 170,190,28672\n"
"s_waitcnt vmcnt(22)\nM 16,180,180\nM 32,176,168\nR 172,190,32768\ns_waitcnt vmcnt(19)\nM 32,180,184\n"
"buffer_load_dwordx4 a[244:247], v252, s[20:23], s65 offen offset:3072 sc1\nM 48,176,172\nR 174,190,36864\ns_waitcnt vmcnt(18)\nM 48,180,188\nG 4\n"
"M 52,160,172\nR 176,191,24576\nM 52,164,188\nbuffer_load_dwordx4 a[248:251], v252, s[20:23], s66 offen offset:3072 sc1\nM 36,160,168\nR 178,191,28672\n"
"s_waitcnt vmcnt(10)\ns_barrier\nM 36,164,184\nM 20,160,164\nR 180,191,32768\nM 20,164,180\n"
"buffer_load_dwordx4 a[252:255], v252, s[20:23], s67 offen offset:3072 sc1\nM 4,160,160\nR 182,191,36864\nM 4,164,176\nG 4\nM 8,168,160\n"
"R 160,188,40960\nM 8,172,176\nM 24,168,164\nR 162,188,45056\nM 24,172,180\nM 40,168,168\n"
"R 164,188,49152\nM 40,172,184\nM 56,168,172\nR 166,188,53248\nM 56,172,188\nG 4\n"
"M 60,176,172\nR 168,189,40960\nM 60,180,188\ns_movk_i32 s63, 7168\nv_mad_u32_u24 v220, v228, s63, v229\nv_add_u32 v221, 3668992, v220\n"
"s_add_u32 m0, s50, 16384\ns_add_u32 s63, s54, 0\nbuffer_load_dwordx4 v220, s[12:15], s63 offen lds\nM 44,176,168\nR 170,189,45056\nM 44,180,184\n"
"M 28,176,164\nR 172,189,49152\nM 28,180,180\nbuffer_load_dwordx4 v221, s[12:15], s63 offen offset:1024 lds\nM 12,176,160\nR 174,189,53248\n"
"s_add_u32 m0, s50, 24576\nM 12,180,176\nG 4\nM 32,160,200\nR 176,190,40960\ns_add_u32 s63, s54, 229376\n"
"s_waitcnt vmcnt(11)\nM 32,164,216\nR 178,190,45056\nM 48,160,204\nR 180,190,49152\ns_waitcnt vmcnt(10)\n"
"M 48,164,220\nR 182,190,53248\nG 4\nM 52,168,204\nR 160,191,40960\nbuffer_load_dwordx4 v220, s[12:15], s63 offen lds\n"
"M 52,172,220\nR 162,191,45056\nM 36,168,200\nR 164,191,49152\nM 36,172,216\nR 166,191,53248\n"
"G 4\nM 40,176,200\nR 168,184,0\nbuffer_load_dwordx4 v221, s[12:15], s63 offen offset:1024 lds\nM 40,180,216\nR 170,184,4096\n"
"s_add_u32 m0, s50, 32768\nM 56,176,204\nR 172,184,8192\ns_add_u32 s63, s54, 128\nM 56,180,220\nR 174,184,12288\n"
"G 4\nM 60,160,204\nR 176,185,0\nbuffer_load_dwordx4 v220, s[12:15], s63 offen lds\nM 60,164,220\nR 178,185,4096\n"
"M 44,160,200\nR 180,185,8192\nM 44,164,216\nR 182,185,12288\nG 4\ns_waitcnt vmcnt(10)\n"
"M 32,168,232\nR 160,186,0\nbuffer_load_dwordx4 v221, s[12:15], s63 offen offset:1024 lds\ns_waitcnt vmcnt(7)\nM 32,172,248\nR 162,186,4096\n"
"s_add_u32 m0, s50, 40960\nM 48,168,236\nR 164,186,8192\ns_add_u32 s63, s54, 229504\ns_waitcnt vmcnt(6)\nM 48,172,252\n"
"R 166,186,12288\nG 4\nM 52,176,236\nR 168,187,0\nbuffer_load_dwordx4 v220, s[12:15], s63 offen lds\nM 52,180,252\n"
"R 170,187,4096\nM 36,176,232\nR 172,187,8192\nM 36,180,248\nR 174,187,12288\nG 4\n"
"M 40,160,232\nR 176,188,40960\nbuffer_load_dwordx4 v221, s[12:15], s63 offen offset:1024 lds\nM 40,164,248\nR 178,188,45056\nM 56,160,236\n"
"R 180,188,49152\nM 56,164,252\nR 182,188,53248\nG 4\nM 60,168,236\nR 160,189,40960\n"
"M 60,172,252\nR 162,189,45056\nM 44,168,232\nR 164,189,49152\nM 44,172,248\nR 166,189,53248\n"
"G 4\nM 0,176,192\nR 168,190,40960\nM 0,180,208\nR 170,190,45056\ns_mov_b32 m0, s51\n"
"M 16,176,196\nR 172,190,49152\nv_cvt_pk_bf16_f32 v32, v32, v33\nv_cvt_pk_bf16_f32 v33, v34, v35\nM 16,180,212\nR 174,190,53248\n"
"G 4\nv_cvt_pk_bf16_f32 v34, v36, v37\nM 20,160,196\nR 176,191,40960\nv_cvt_pk_bf16_f32 v35, v38, v39\nv_cvt_pk_bf16_f32 v36, v40, v41\n"
"M 20,164,212\nR 178,191,45056\nv_cvt_pk_bf16_f32 v37, v42, v43\nv_cvt_pk_bf16_f32 v38, v44, v45\nM 4,160,192\nR 180,191,49152\n"
"v_cvt_pk_bf16_f32 v39, v46, v47\nv_cvt_pk_bf16_f32 v40, v48, v49\nM 4,164,208\nR 182,191,53248\nG 4\nv_cvt_pk_bf16_f32 v41, v50, v51\n"
"M 8,168,192\nR 160,184,0\nv_cvt_pk_bf16_f32 v42, v52, v53\nv_cvt_pk_bf16_f32 v43, v54, v55\nM 8,172,208\nR 162,184,4096\n"
"v_cvt_pk_bf16_f32 v44, v56, v57\nv_cvt_pk_bf16_f32 v45, v58, v59\nM 24,168,196\nR 164,184,8192\nv_cvt_pk_bf16_f32 v46, v60, v61\nv_cvt_pk_bf16_f32 v47, v62, v63\n"
"M 24,172,212\nR 166,184,12288\nds_write_addtid_b32 v32 offset:0\nG 5\nM 28,176,196\nR 168,185,0\n"
"ds_write_addtid_b32 v33 offset:256\nM 28,180,212\nR 170,185,4096\nds_write_addtid_b32 v34 offset:512\nM 12,176,192\nR 172,185,8192\n"
"ds_write_addtid_b32 v35 offset:768\nds_write_addtid_b32 v36 offset:1024\nds_write_addtid_b32 v37 offset:1280\nds_write_addtid_b32 v38 offset:1536\nds_write_addtid_b32 v39 offset:1792\nds_write_addtid_b32 v40 offset:2048\n"
"ds_write_addtid_b32 v41 offset:2304\nds_write_addtid_b32 v42 offset:2560\nds_write_addtid_b32 v43 offset:2816\nds_write_addtid_b32 v44 offset:3072\nds_write_addtid_b32 v45 offset:3328\nds_write_addtid_b32 v46 offset:3584\n"
"ds_write_addtid_b32 v47 offset:3840\nM 12,180,208\nR 174,185,12288\nG 15\nM 0,160,224\nR 176,186,0\n"
"s_add_u32 m0, s50, 49152\nM 0,164,240\nR 178,186,4096\nG 3\ns_barrier\ns_add_u32 s63, s54, 458752\n"
"M 16,160,228\nR 180,186,8192\nbuffer_load_dwordx4 v220, s[12:15], s63 offen lds\nM 16,164,244\nR 182,186,12288\nG 4\n"
"M 20,168,228\nR 160,187,0\nM 20,172,244\nR 162,187,4096\nM 4,168,224\nR 164,187,8192\n"
"buffer_load_dwordx4 v221, s[12:15], s63 offen offset:1024 lds\nM 4,172,240\nR 166,187,12288\ns_add_u32 m0, s50, 57344\ns_add_u32 s63, s54, 688128\nG 4\n"
"M 8,176,224\nbuffer_load_dwordx4 v220, s[12:15], s63 offen lds\nM 8,180,240\nds_read2st64_b32 v[32:33], v225 offset0:0 offset1:1\nM 24,176,228\nds_read2st64_b32 v[34:35], v225 offset0:2 offset1:3\n"
"M 24,180,244\nds_read2st64_b32 v[36:37], v225 offset0:4 offset1:5\nG 3\nM 28,160,228\nbuffer_load_dwordx4 v221, s[12:15], s63 offen offset:1024 lds\nM 28,164,244\n"
"ds_read2st64_b32 v[38:39], v225 offset0:6 offset1:7\nM 12,160,224\nds_read2st64_b32 v[40:41], v225 offset0:8 offset1:9\nds_read2st64_b32 v[42:43], v225 offset0:10 offset1:11\nM 12,164,240\nds_read2st64_b32 v[44:45], v225 offset0:12 offset1:13\n"
"ds_read2st64_b32 v[46:47], v225 offset0:14 offset1:15\nG 0\ns_barrier\nv_mfma_f32_16x16x32_bf16 v[0:3], v[208:211], v[32:35], v[0:3]\nv_mfma_f32_16x16x32_bf16 v[4:7], v[210:213], v[32:35], v[4:7]\nv_mfma_f32_16x16x32_bf16 v[8:11], v[208:211], v[36:39], v[8:11]\n"
"v_mfma_f32_16x16x32_bf16 v[12:15], v[210:213], v[36:39], v[12:15]\nv_mfma_f32_16x16x32_bf16 v[16:19], v[208:211], v[40:43], v[16:19]\nv_mfma_f32_16x16x32_bf16 v[20:23], v[210:213], v[40:43], v[20:23]\nv_mfma_f32_16x16x32_bf16 v[24:27], v[208:211], v[44:47], v[24:27]\nv_mfma_f32_16x16x32_bf16 v[28:31], v[210:213], v[44:47], v[28:31]\ns_lshl_b32 s84, s75, 11\n"
"s_add_u32 s84, s84, s49\ns_add_u32 s84, s84, 10485760\ns_add_u32 s85, s84, 32768\nv_mov_b32 v238, s46\nv_mov_b32 v239, s47\nv_cvt_pk_bf16_f32 v192, v0, v1\n"
"v_cvt_pk_bf16_f32 v193, v2, v3\nv_cvt_pk_bf16_f32 v194, v4, v5\nv_cvt_pk_bf16_f32 v195, v6, v7\nbuffer_store_dwordx4 v[192:195], v252, s[20:23], s84 offen offset:0 sc1\nv_cvt_pk_bf16_f32 v196, v8, v9\nv_cvt_pk_bf16_f32 v197, v10, v11\n"
"v_cvt_pk_bf16_f32 v198, v12, v13\nv_cvt_pk_bf16_f32 v199, v14, v15\nbuffer_store_dwordx4 v[196:199], v252, s[20:23], s84 offen offset:1024 sc1\nv_cvt_pk_bf16_f32 v200, v16, v17\nv_cvt_pk_bf16_f32 v201, v18, v19\nv_cvt_pk_bf16_f32 v202, v20, v21\n"
"v_cvt_pk_bf16_f32 v203, v22, v23\nbuffer_store_dwordx4 v[200:203], v252, s[20:23], s85 offen offset:0 sc1\nv_cvt_pk_bf16_f32 v204, v24, v25\nv_cvt_pk_bf16_f32 v205, v26, v27\nv_cvt_pk_bf16_f32 v206, v28, v29\nv_cvt_pk_bf16_f32 v207, v30, v31\n"
"buffer_store_dwordx4 v[204:207], v252, s[20:23], s85 offen offset:1024 sc1\ns_add_u32 s61, s79, 88219648\ns_lshl_b32 s62, s75, 3\ns_add_u32 s61, s61, s62\ns_waitcnt vmcnt(0)\ns_mov_b64 exec, 1\n"
"buffer_atomic_swap_x2 v[238:239], off, s[20:23], s61 sc1\ns_mov_b64 exec, -1\nLqw0:\nv_bfe_u32 v250, v252, 7, 1\nv_mul_u32_u24 v250, 2176, v250\nv_bfe_u32 v251, v252, 4, 3\n"
"v_lshl_add_u32 v250, v251, 3, v250\ns_lshl_b32 s60, s90, 6\nv_add_u32 v250, s60, v250\ns_sleep 5\nv_mov_b32 v248, 0\nv_mov_b32 v249, 0\n"
"s_add_u32 s61, s80, 88219648\ns_mov_b64 exec, 0xffff\nbuffer_atomic_or_x2 v[248:249], v250, s[20:23], s61 offen sc0 sc1\ns_mov_b64 exec, -1\ns_add_u32 m0, s50, 65536\ns_add_u32 s63, s54, 458880\n"
"buffer_load_dwordx4 v220, s[12:15], s63 offen lds\nbuffer_load_dwordx4 v221, s[12:15], s63 offen offset:1024 lds\ns_add_u32 m0, s50, 73728\ns_add_u32 s63, s54, 688256\nbuffer_load_dwordx4 v220, s[12:15], s63 offen lds\nbuffer_load_dwordx4 v221, s[12:15], s63 offen offset:1024 lds\n"
"s_add_u32 m0, s50, 81920\ns_add_u32 s63, s54, 917504\nbuffer_load_dwordx4 v220, s[12:15], s63 offen lds\nbuffer_load_dwordx4 v221, s[12:15], s63 offen offset:1024 lds\ns_add_u32 m0, s50, 90112\ns_add_u32 s63, s54, 1146880\n"
"buffer_load_dwordx4 v220, s[12:15], s63 offen lds\nbuffer_load_dwordx4 v221, s[12:15], s63 offen offset:1024 lds\ns_add_u32 m0, s50, 98304\ns_add_u32 s63, s54, 917632\nbuffer_load_dwordx4 v220, s[12:15], s63 offen lds\nbuffer_load_dwordx4 v221, s[12:15], s63 offen offset:1024 lds\n"
"s_add_u32 m0, s50, 106496\ns_add_u32 s63, s54, 1147008\nbuffer_load_dwordx4 v220, s[12:15], s63 offen lds\nbuffer_load_dwordx4 v221, s[12:15], s63 offen offset:1024 lds\nv_mov_b32 v230, 0\nv_mov_b32 v231, 0\n"
"v_mov_b32 v232, 0\nv_mov_b32 v233, 0\nv_dot2c_f32_bf16 v230, s76, v192\nv_dot2c_f32_bf16 v231, v192, v192\nv_dot2c_f32_bf16 v232, s76, v200\nv_dot2c_f32_bf16 v233, v200, v200\n"
"v_dot2c_f32_bf16 v230, s76, v193\nv_dot2c_f32_bf16 v231, v193, v193\nv_dot2c_f32_bf16 v232, s76, v201\nv_dot2c_f32_bf16 v233, v201, v201\nv_dot2c_f32_bf16 v230, s76, v194\nv_dot2c_f32_bf16 v231, v194, v194\n"
"v_dot2c_f32_bf16 v232, s76, v202\nv_dot2c_f32_bf16 v233, v202, v202\nv_dot2c_f32_bf16 v230, s76, v195\nv_dot2c_f32_bf16 v231, v195, v195\nv_dot2c_f32_bf16 v232, s76, v203\nv_dot2c_f32_bf16 v233, v203, v203\n"
"v_dot2c_f32_bf16 v230, s76, v196\nv_dot2c_f32_bf16 v231, v196, v196\nv_dot2c_f32_bf16 v232, s76, v204\nv_dot2c_f32_bf16 v233, v204, v204\nv_dot2c_f32_bf16 v230, s76, v197\nv_dot2c_f32_bf16 v231, v197, v197\n"
"v_dot2c_f32_bf16 v232, s76, v205\nv_dot2c_f32_bf16 v233, v205, v205\nv_dot2c_f32_bf16 v230, s76, v198\nv_dot2c_f32_bf16 v231, v198, v198\nv_dot2c_f32_bf16 v232, s76, v206\nv_dot2c_f32_bf16 v233, v206, v206\n"
"v_dot2c_f32_bf16 v230, s76, v199\nv_dot2c_f32_bf16 v231, v199, v199\nv_dot2c_f32_bf16 v232, s76, v207\nv_dot2c_f32_bf16 v233, v207, v207\nv_lshrrev_b32 v234, 1, v252\nv_permlane32_swap_b32 v230, v231\n"
"s_nop 0\nv_permlane32_swap_b32 v232, v233\nv_add_f32 v230, v230, v231\nv_add_f32 v232, v232, v233\nv_mov_b32 v231, s46\ns_nop 0\n"
"v_permlane16_swap_b32 v230, v232\nv_add_f32 v230, v230, v232\ns_lshr_b32 s61, s49, 3\ns_lshl2_add_u32 s61, s53, s61\ns_add_u32 s61, s61, 82313216\nbuffer_store_dwordx2 v[230:231], v234, s[20:23], s61 offen sc1\n"
"s_waitcnt vmcnt(13)\nv_cmp_eq_u64 s[86:87], s[46:47], v[248:249]\ns_nop 0\ns_and_b32 s62, s86, 0xffff\ns_cmp_eq_u32 s62, 0xffff\ns_cbranch_scc0 Lq249\n"
"Lq250:\ns_add_u32 s64, s70, 10485760\nbuffer_load_dwordx4 a[0:3], v252, s[20:23], s64 offen offset:0 sc1\ns_add_u32 s65, s70, 10518528\nbuffer_load_dwordx4 a[4:7], v252, s[20:23], s65 offen offset:0 sc1\ns_add_u32 s66, s71, 10485760\n"
"buffer_load_dwordx4 a[8:11], v252, s[20:23], s66 offen offset:0 sc1\ns_add_u32 s67, s71, 10518528\nbuffer_load_dwordx4 a[12:15], v252, s[20:23], s67 offen offset:0 sc1\nbuffer_load_dwordx4 a[16:19], v252, s[20:23], s64 offen offset:1024 sc1\nbuffer_load_dwordx4 a[20:23], v252, s[20:23], s65 offen offset:1024 sc1\nbuffer_load_dwordx4 a[24:27], v252, s[20:23], s66 offen offset:1024 sc1\n"
"buffer_load_dwordx4 a[28:31], v252, s[20:23], s67 offen offset:1024 sc1\ns_barrier\nR 160,184,16384\nR 162,184,20480\nR 164,184,24576\nR 166,184,28672\n"
"R 168,185,16384\nR 170,185,20480\nR 172,185,24576\nR 174,185,28672\nR 176,186,16384\nR 178,186,20480\n"
"R 180,186,24576\nR 182,186,28672\nbuffer_load_dwordx4 a[32:35], v252, s[20:23], s64 offen offset:2048 sc1\nbuffer_load_dwordx4 a[36:39], v252, s[20:23], s65 offen offset:2048 sc1\nbuffer_load_dwordx4 a[40:43], v252, s[20:23], s66 offen offset:2048 sc1\nbuffer_load_dwordx4 a[44:47], v252, s[20:23], s67 offen offset:2048 sc1\n"
"buffer_load_dwordx4 a[48:51], v252, s[20:23], s64 offen offset:3072 sc1\nbuffer_load_dwordx4 a[52:55], v252, s[20:23], s65 offen offset:3072 sc1\nbuffer_load_dwordx4 a[56:59], v252, s[20:23], s66 offen offset:3072 sc1\nbuffer_load_dwordx4 a[60:63], v252, s[20:23], s67 offen offset:3072 sc1\ns_waitcnt vmcnt(15) lgkmcnt(10)\nv_mfma_f32_16x16x32_bf16 v[32:35], v[160:163], a[0:3], 0\n"
"s_add_u32 s64, s70, 10489856\nbuffer_load_dwordx4 a[64:67], v252, s[20:23], s64 offen offset:0 sc1\ns_waitcnt vmcnt(12) lgkmcnt(8)\nM 32,164,16\nv_mfma_f32_16x16x32_bf16 v[48:51], v[160:163], a[4:7], 0\ns_waitcnt vmcnt(11)\n"
"M 48,164,20\nv_mfma_f32_16x16x32_bf16 v[64:67], v[160:163], a[8:11], 0\nv_lshrrev_b32 v238, 7, v252\ns_waitcnt vmcnt(10)\nM 64,164,24\ns_add_u32 s65, s70, 10522624\n"
"buffer_load_dwordx4 a[68:71], v252, s[20:23], s65 offen offset:0 sc1\nv_mfma_f32_16x16x32_bf16 v[80:83], v[160:163], a[12:15], 0\nv_lshlrev_b32 v238, 9, v238\ns_waitcnt vmcnt(10)\nM 80,164,28\nG 4\n"
"v_and_b32 v239, 0x70, v252\nv_mfma_f32_16x16x32_bf16 v[84:87], v[168:171], a[12:15], 0\nR 160,187,16384\nM 84,172,28\ns_and_b32 s60, s40, 3\ns_lshl_b32 s60, s60, 7\n"
"v_add3_u32 v238, v238, v239, s60\nv_mfma_f32_16x16x32_bf16 v[68:71], v[168:171], a[8:11], 0\nR 162,187,20480\ns_lshl_b32 s61, s42, 13\ns_add_u32 s61, s61, 84279296\nbuffer_load_dwordx4 v[0:3], v238, s[20:23], s61 offen sc1\n"
"M 68,172,24\ns_add_u32 s66, s71, 10489856\nbuffer_load_dwordx4 a[72:75], v252, s[20:23], s66 offen offset:0 sc1\nv_mfma_f32_16x16x32_bf16 v[52:55], v[168:171], a[4:7], 0\nR 164,187,24576\ns_lshl_b32 s61, s42, 13\n"
"s_add_u32 s61, s61, 84283392\nbuffer_load_dwordx4 v[4:7], v238, s[20:23], s61 offen sc1\nM 52,172,20\nv_bfe_u32 v239, v238, 2, 7\nv_mfma_f32_16x16x32_bf16 v[36:39], v[168:171], a[0:3], 0\nR 166,187,28672\n"
"M 36,172,16\nbuffer_load_dword v239, v239, s[32:35], s54 offen offset:0\nG 4\nv_mfma_f32_16x16x32_bf16 v[40:43], v[176:179], a[0:3], 0\nR 168,184,32768\nM 40,180,16\n"
"s_add_u32 s67, s71, 10522624\nbuffer_load_dwordx4 a[76:79], v252, s[20:23], s67 offen offset:0 sc1\nv_mfma_f32_16x16x32_bf16 v[56:59], v[176:179], a[4:7], 0\nR 170,184,36864\nM 56,180,20\nv_mfma_f32_16x16x32_bf16 v[72:75], v[176:179], a[8:11], 0\n"
"R 172,184,40960\nM 72,180,24\nv_mfma_f32_16x16x32_bf16 v[88:91], v[176:179], a[12:15], 0\nR 174,184,45056\nM 88,180,28\nG 4\n"
"v_mfma_f32_16x16x32_bf16 v[92:95], v[160:163], a[12:15], 0\nR 176,185,32768\nM 92,164,28\nbuffer_load_dwordx4 a[80:83], v252, s[20:23], s64 offen offset:1024 sc1\nv_mfma_f32_16x16x32_bf16 v[76:79], v[160:163], a[8:11], 0\nR 178,185,36864\n"
"M 76,164,24\nv_mfma_f32_16x16x32_bf16 v[60:63], v[160:163], a[4:7], 0\nR 180,185,40960\nM 60,164,20\nbuffer_load_dwordx4 a[84:87], v252, s[20:23], s65 offen offset:1024 sc1\nv_mfma_f32_16x16x32_bf16 v[44:47], v[160:163], a[0:3], 0\n"
"R 182,185,45056\nM 44,164,16\nG 4\nv_mfma_f32_16x16x32_bf16 v[96:99], v[168:171], a[0:3], 0\nR 160,186,32768\nM 96,172,16\n"
"v_mfma_f32_16x16x32_bf16 v[112:115], v[168:171], a[4:7], 0\nR 162,186,36864\nM 112,172,20\nbuffer_load_dwordx4 a[88:91], v252, s[20:23], s66 offen offset:1024 sc1\nv_mfma_f32_16x16x32_bf16 v[128:131], v[168:171], a[8:11], 0\nR 164,186,40960\n"
"M 128,172,24\nv_mfma_f32_16x16x32_bf16 v[144:147], v[168:171], a[12:15], 0\nR 166,186,45056\nM 144,172,28\ns_barrier\nG 4\n"
"v_mfma_f32_16x16x32_bf16 v[148:151], v[176:179], a[12:15], 0\nR 168,187,32768\nM 148,180,28\nbuffer_load_dwordx4 a[92:95], v252, s[20:23], s67 offen offset:1024 sc1\nv_mfma_f32_16x16x32_bf16 v[132:135], v[176:179], a[8:11], 0\nR 170,187,36864\n"
"M 132,180,24\nv_mfma_f32_16x16x32_bf16 v[116:119], v[176:179], a[4:7], 0\nR 172,187,40960\nM 116,180,20\nv_mfma_f32_16x16x32_bf16 v[100:103], v[176:179], a[0:3], 0\nR 174,187,45056\n"
"M 100,180,16\nG 4\nv_mfma_f32_16x16x32_bf16 v[104:107], v[160:163], a[0:3], 0\nR 176,184,49152\nv_lshrrev_b32 v242, 7, v252\nM 104,164,16\n"
"buffer_load_dwordx4 a[96:99], v252, s[20:23], s64 offen offset:2048 sc1\nv_mfma_f32_16x16x32_bf16 v[120:123], v[160:163], a[4:7], 0\nR 178,184,53248\nv_lshlrev_b32 v242, 9, v242\ns_waitcnt vmcnt(7)\nv_min3_u32 v240, v1, v3, v5\n"
"M 120,164,20\nbuffer_load_dwordx4 a[100:103], v252, s[20:23], s65 offen offset:2048 sc1\nv_and_b32 v243, 0x70, v252\nv_min3_u32 v240, v240, v7, v7\nv_mfma_f32_16x16x32_bf16 v[136:139], v[160:163], a[8:11], 0\nR 180,184,57344\n"
"v_max3_u32 v241, v1, v3, v5\nv_max3_u32 v241, v241, v7, v7\nM 136,164,24\ns_and_b32 s60, s40, 3\ns_lshl_b32 s60, s60, 7\nv_add3_u32 v242, v242, v243, s60\n"
"v_cmp_ne_u32 s[86:87], s46, v240\nv_mfma_f32_16x16x32_bf16 v[152:155], v[160:163], a[12:15], 0\nR 182,184,61440\ns_lshl_b32 s61, s42, 13\ns_add_u32 s61, s61, 84410368\nbuffer_load_dwordx4 v[16:19], v242, s[20:23], s61 offen sc1\n"
"v_cmp_ne_u32 s[88:89], s46, v241\nM 152,164,28\ns_lshl_b32 s61, s42, 13\ns_add_u32 s61, s61, 84414464\nbuffer_load_dwordx4 v[20:23], v242, s[20:23], s61 offen sc1\ns_or_b64 s[86:87], s[86:87], s[88:89]\n"
"s_cbranch_scc1 Lq303\nLq304:\nG 4\nv_mfma_f32_16x16x32_bf16 v[156:159], v[168:171], a[12:15], 0\nR 160,185,49152\nv_add_f32 v0, v0, v4\n"
"M 156,172,28\nbuffer_load_dwordx4 a[104:107], v252, s[20:23], s66 offen offset:2048 sc1\nv_bfe_u32 v243, v242, 2, 7\nv_mfma_f32_16x16x32_bf16 v[140:143], v[168:171], a[8:11], 0\nR 162,185,53248\nv_add_f32 v2, v2, v6\n"
"M 140,172,24\nbuffer_load_dword v243, v243, s[32:35], s54 offen offset:128\nv_mfma_f32_16x16x32_bf16 v[124:127], v[168:171], a[4:7], 0\nR 164,185,57344\nv_add_f32_dpp v0, v0, v0 row_ror:8 row_mask:0xf bank_mask:0xf\nM 124,172,20\n"
"v_add_f32_dpp v2, v2, v2 row_ror:8 row_mask:0xf bank_mask:0xf\nv_mov_b32 v4, v0\nv_mfma_f32_16x16x32_bf16 v[108:111], v[168:171], a[0:3], 0\nR 166,185,61440\nv_mov_b32 v5, v2\nM 108,172,16\n"
"G 4\nv_permlane32_swap_b32 v4, v0\nv_permlane32_swap_b32 v5, v2\nM 32,176,32\nR 168,186,49152\nv_add_f32 v0, v0, v4\n"
"M 32,180,48\nbuffer_load_dwordx4 a[108:111], v252, s[20:23], s67 offen offset:2048 sc1\nM 48,176,36\nR 170,186,53248\nv_add_f32 v2, v2, v5\nM 48,180,52\n"
"v_mov_b32 v4, v0\nv_mov_b32 v5, v2\nM 64,176,40\nR 172,186,57344\nv_permlane16_swap_b32 v4, v0\nM 64,180,56\n"
"buffer_load_dwordx4 a[112:115], v252, s[20:23], s64 offen offset:3072 sc1\nM 80,176,44\nR 174,186,61440\nv_permlane16_swap_b32 v5, v2\nM 80,180,60\ns_barrier\n"
"G 4\nM 84,160,44\nR 176,187,49152\nv_add_f32 v0, v0, v4\nM 84,164,60\nv_add_f32 v2, v2, v5\n"
"v_and_b32 v8, s77, v0\nM 68,160,40\nR 178,187,53248\nv_sub_f32 v9, v0, v8\nM 68,164,56\nbuffer_load_dwordx4 a[116:119], v252, s[20:23], s65 offen offset:3072 sc1\n"
"M 52,160,36\nR 180,187,57344\ns_waitcnt vmcnt(14)\nv_lshlrev_b32 v10, 16, v239\nM 52,164,52\nv_cvt_pk_bf16_f32 v4, v8, v9\n"
"v_cvt_pk_bf16_f32 v5, v8, v10\nM 36,160,32\nR 182,187,61440\nv_and_b32 v8, s77, v2\nM 36,164,48\nG 4\n"
"v_sub_f32 v9, v2, v8\nv_and_b32 v10, s77, v239\nM 40,168,32\nR 160,188,0\nv_cvt_pk_bf16_f32 v6, v8, v9\nM 40,172,48\n"
"buffer_load_dwordx4 a[120:123], v252, s[20:23], s66 offen offset:3072 sc1\nM 56,168,36\nR 162,188,4096\nv_cvt_pk_bf16_f32 v7, v8, v10\nM 56,172,52\nv_and_b32 v238, 0x1ff, v238\n"
"v_add_u32 v238, 157184, v238\nM 72,168,40\nR 164,188,8192\nds_write_b128 v238, v[4:7]\nM 72,172,56\nM 88,168,44\n"
"R 166,188,12288\nM 88,172,60\nG 5\nM 92,176,44\nR 168,189,0\nM 92,180,60\n"
"buffer_load_dwordx4 a[124:127], v252, s[20:23], s67 offen offset:3072 sc1\nM 76,176,40\nR 170,189,4096\nM 76,180,56\nM 60,176,36\nR 172,189,8192\n"
"v_lshrrev_b32 v238, 7, v252\nM 60,180,52\ns_add_u32 s64, s70, 10493952\nbuffer_load_dwordx4 a[128:131], v252, s[20:23], s64 offen offset:0 sc1\nM 44,176,32\nR 174,189,12288\n"
"v_lshlrev_b32 v238, 9, v238\ns_waitcnt vmcnt(8)\nv_min3_u32 v244, v17, v19, v21\nM 44,180,48\nG 4\nv_and_b32 v239, 0x70, v252\n"
"v_min3_u32 v244, v244, v23, v23\nv_max3_u32 v245, v17, v19, v21\nM 96,160,32\nR 176,190,0\ns_and_b32 s60, s40, 3\ns_lshl_b32 s60, s60, 7\n"
"v_add3_u32 v238, v238, v239, s60\nv_max3_u32 v245, v245, v23, v23\nM 96,164,48\ns_add_u32 s65, s70, 10526720\nbuffer_load_dwordx4 a[132:135], v252, s[20:23], s65 offen offset:0 sc1\nv_cmp_ne_u32 s[86:87], s46, v244\n"
"M 112,160,36\nR 178,190,4096\ns_lshl_b32 s61, s42, 13\ns_add_u32 s61, s61, 84541440\nbuffer_load_dwordx4 v[0:3], v238, s[20:23], s61 offen sc1\nv_cmp_ne_u32 s[88:89], s46, v245\n"
"M 112,164,52\ns_lshl_b32 s61, s42, 13\ns_add_u32 s61, s61, 84545536\nbuffer_load_dwordx4 v[4:7], v238, s[20:23], s61 offen sc1\ns_or_b64 s[86:87], s[86:87], s[88:89]\ns_cbranch_scc1 Lq313\n"
"Lq314:\nM 128,160,40\nR 180,190,8192\nv_add_f32 v16, v16, v20\nv_add_f32 v18, v18, v22\nM 128,164,56\n"
"s_add_u32 m0, s50, 114688\nv_bfe_u32 v239, v238, 2, 7\nv_add_f32_dpp v16, v16, v16 row_ror:8 row_mask:0xf bank_mask:0xf\nv_add_f32_dpp v18, v18, v18 row_ror:8 row_mask:0xf bank_mask:0xf\nM 144,160,44\nR 182,190,12288\n"
"s_add_u32 s63, s54, 1376256\nv_mov_b32 v20, v16\nM 144,164,60\nbuffer_load_dword v239, v239, s[32:35], s54 offen offset:256\ns_barrier\nG 4\n"
"v_mov_b32 v21, v18\nM 148,168,44\nR 160,191,0\nv_permlane32_swap_b32 v20, v16\nv_permlane32_swap_b32 v21, v18\nM 148,172,60\n"
"s_add_u32 s66, s71, 10493952\nbuffer_load_dwordx4 a[136:139], v252, s[20:23], s66 offen offset:0 sc1\nv_add_f32 v16, v16, v20\nM 132,168,40\nR 162,191,4096\nv_add_f32 v18, v18, v21\n"
"v_mov_b32 v20, v16\nM 132,172,56\nbuffer_load_dwordx4 v220, s[12:15], s63 offen lds\nv_mov_b32 v21, v18\nM 116,168,36\nR 164,191,8192\n"
"v_permlane16_swap_b32 v20, v16\nM 116,172,52\ns_add_u32 s67, s71, 10526720\nbuffer_load_dwordx4 a[140:143], v252, s[20:23], s67 offen offset:0 sc1\nM 100,168,32\nR 166,191,12288\n"
"v_permlane16_swap_b32 v21, v18\nM 100,172,48\nG 4\nv_add_f32 v16, v16, v20\nv_add_f32 v18, v18, v21\nM 104,176,32\n"
"R 168,188,16384\nbuffer_load_dwordx4 v221, s[12:15], s63 offen offset:1024 lds\nM 104,180,48\ns_add_u32 m0, s50, 0\ns_add_u32 s63, s54, 1605632\nbuffer_load_dwordx4 v220, s[12:15], s63 offen lds\n"
"M 120,176,36\nR 170,188,20480\nv_and_b32 v24, s77, v16\nM 120,180,52\nbuffer_load_dwordx4 a[144:147], v252, s[20:23], s64 offen offset:1024 sc1\nM 136,176,40\n"
"R 172,188,24576\nv_sub_f32 v25, v16, v24\nM 136,180,56\nbuffer_load_dwordx4 v221, s[12:15], s63 offen offset:1024 lds\nM 152,176,44\nR 174,188,28672\n"
"s_waitcnt vmcnt(17)\nv_lshlrev_b32 v26, 16, v243\nM 152,180,60\nG 4\nv_cvt_pk_bf16_f32 v20, v24, v25\nv_cvt_pk_bf16_f32 v21, v24, v26\n"
"M 156,160,44\nR 176,189,16384\nv_and_b32 v24, s77, v18\nM 156,164,60\nbuffer_load_dwordx4 a[148:151], v252, s[20:23], s65 offen offset:1024 sc1\nM 140,160,40\n"
"R 178,189,20480\nv_sub_f32 v25, v18, v24\nM 140,164,56\nv_and_b32 v26, s77, v243\nv_cvt_pk_bf16_f32 v22, v24, v25\nM 124,160,36\n"
"R 180,189,24576\nv_cvt_pk_bf16_f32 v23, v24, v26\nM 124,164,52\nv_and_b32 v242, 0x1ff, v242\nv_add_u32 v242, 157696, v242\nM 108,160,32\n"
"R 182,189,28672\nds_write_b128 v242, v[20:23]\nM 108,164,48\nG 5\nM 32,168,64\nR 160,190,16384\n"
"M 32,172,80\nbuffer_load_dwordx4 a[152:155], v252, s[20:23], s66 offen offset:1024 sc1\nM 48,168,68\nR 162,190,20480\nM 48,172,84\ns_add_u32 m0, s50, 8192\n"
"M 64,168,72\nR 164,190,24576\ns_add_u32 s63, s54, 1376384\nM 64,172,88\nbuffer_load_dwordx4 a[156:159], v252, s[20:23], s67 offen offset:1024 sc1\nM 80,168,76\n"
"R 166,190,28672\nM 80,172,92\nG 4\ns_barrier\nM 84,176,76\nR 168,191,16384\n"
"v_lshrrev_b32 v242, 7, v252\nM 84,180,92\nbuffer_load_dwordx4 v220, s[12:15], s63 offen lds\nv_lshlrev_b32 v242, 9, v242\nM 68,176,72\nR 170,191,20480\n"
"s_waitcnt vmcnt(12)\nv_min3_u32 v240, v1, v3, v5\nv_min3_u32 v240, v240, v7, v7\nM 68,180,88\nbuffer_load_dwordx4 a[160:163], v252, s[20:23], s64 offen offset:2048 sc1\nv_and_b32 v243, 0x70, v252\n"
"v_max3_u32 v241, v1, v3, v5\nM 52,176,68\nR 172,191,24576\nv_max3_u32 v241, v241, v7, v7\nv_cmp_ne_u32 s[86:87], s46, v240\nM 52,180,84\n"
"s_and_b32 s60, s40, 1\ns_lshl_b32 s60, s60, 7\nv_add3_u32 v242, v242, v243, s60\nv_cmp_ne_u32 s[88:89], s46, v241\nM 36,176,64\nR 174,191,28672\n"
"s_lshl_b32 s61, s42, 13\ns_add_u32 s61, s61, 84672512\nbuffer_load_dwordx4 v[16:19], v242, s[20:23], s61 offen sc1\ns_or_b64 s[86:87], s[86:87], s[88:89]\ns_cbranch_scc1 Lq323\nLq324:\n"
"M 36,180,80\ns_lshl_b32 s61, s42, 13\ns_add_u32 s61, s61, 84676608\nbuffer_load_dwordx4 v[20:23], v242, s[20:23], s61 offen sc1\nG 4\nv_add_f32 v0, v0, v4\n"
"M 40,160,64\nR 176,188,32768\nbuffer_load_dwordx4 v221, s[12:15], s63 offen offset:1024 lds\nv_add_f32 v2, v2, v6\nM 40,164,80\nbuffer_load_dwordx4 a[164:167], v252, s[20:23], s65 offen offset:2048 sc1\n"
"s_add_u32 s63, s54, 1605760\nv_bfe_u32 v243, v242, 2, 7\nv_add_f32_dpp v0, v0, v0 row_ror:8 row_mask:0xf bank_mask:0xf\nM 56,160,68\nR 178,188,36864\ns_add_u32 m0, s50, 16384\n"
"v_add_f32_dpp v2, v2, v2 row_ror:8 row_mask:0xf bank_mask:0xf\nM 56,164,84\nbuffer_load_dword v243, v243, s[32:35], s55 offen offset:0\nv_mov_b32 v4, v0\nM 72,160,72\nR 180,188,40960\n"
"buffer_load_dwordx4 v220, s[12:15], s63 offen lds\nv_mov_b32 v5, v2\nM 72,164,88\nbuffer_load_dwordx4 a[168:171], v252, s[20:23], s66 offen offset:2048 sc1\nv_permlane32_swap_b32 v4, v0\nM 88,160,76\n"
"R 182,188,45056\nv_permlane32_swap_b32 v5, v2\nv_add_f32 v0, v0, v4\nM 88,164,92\nG 4\nv_add_f32 v2, v2, v5\n"
"v_mov_b32 v4, v0\nv_mov_b32 v5, v2\nM 92,168,76\nR 160,189,32768\nbuffer_load_dwordx4 v221, s[12:15], s63 offen offset:1024 lds\nv_permlane16_swap_b32 v4, v0\n"
"M 92,172,92\nv_permlane16_swap_b32 v5, v2\nv_add_f32 v0, v0, v4\nM 76,168,72\nR 162,189,36864\nv_add_f32 v2, v2, v5\n"
"M 76,172,88\nbuffer_load_dwordx4 a[172:175], v252, s[20:23], s67 offen offset:2048 sc1\nM 60,168,68\nR 164,189,40960\nv_and_b32 v8, s77, v0\nM 60,172,84\n"
"v_sub_f32 v9, v0, v8\ns_waitcnt vmcnt(21)\nv_lshlrev_b32 v10, 16, v239\nM 44,168,64\nR 166,189,45056\nv_cvt_pk_bf16_f32 v4, v8, v9\n"
"M 44,172,80\nG 4\nv_cvt_pk_bf16_f32 v5, v8, v10\nv_and_b32 v8, s77, v2\nM 96,176,64\nR 168,190,32768\n"
"v_sub_f32 v9, v2, v8\nM 96,180,80\nbuffer_load_dwordx4 a[176:179], v252, s[20:23], s64 offen offset:3072 sc1\nM 112,176,68\nR 170,190,36864\nv_and_b32 v10, s77, v239\n"
"M 112,180,84\nv_cvt_pk_bf16_f32 v6, v8, v9\nv_cvt_pk_bf16_f32 v7, v8, v10\nM 128,176,72\nR 172,190,40960\nv_and_b32 v238, 0x1ff, v238\n"
"M 128,180,88\ns_add_u32 m0, s50, 24576\nv_add_u32 v238, 158208, v238\nds_write_b128 v238, v[4:7]\nM 144,176,76\nR 174,190,45056\n"
"s_add_u32 s63, s54, 1835008\nM 144,180,92\ns_waitcnt vmcnt(15) lgkmcnt(1)\ns_barrier\nM 148,160,76\nR 176,191,32768\n"
"M 148,164,92\nbuffer_load_dwordx4 a[180:183], v252, s[20:23], s65 offen offset:3072 sc1\nM 132,160,72\nR 178,191,36864\nM 132,164,88\nbuffer_load_dwordx4 v220, s[12:15], s63 offen lds\n"
"M 116,160,68\nR 180,191,40960\nM 116,164,84\nbuffer_load_dwordx4 a[184:187], v252, s[20:23], s66 offen offset:3072 sc1\nM 100,160,64\nR 182,191,45056\n"
"M 100,164,80\nG 4\nM 104,168,64\nR 160,188,49152\nbuffer_load_dwordx4 v221, s[12:15], s63 offen offset:1024 lds\nM 104,172,80\n"
"s_add_u32 m0, s50, 32768\ns_add_u32 s63, s54, 2064384\nbuffer_load_dwordx4 v220, s[12:15], s63 offen lds\nM 120,168,68\nR 162,188,53248\nM 120,172,84\n"
"buffer_load_dwordx4 a[188:191], v252, s[20:23], s67 offen offset:3072 sc1\nM 136,168,72\nR 164,184,0\nM 136,172,88\nbuffer_load_dwordx4 v221, s[12:15], s63 offen offset:1024 lds\nM 152,168,76\n"
"R 166,184,4096\ns_waitcnt vmcnt(15)\nv_min3_u32 v244, v17, v19, v21\nv_min3_u32 v244, v244, v23, v23\nM 152,172,92\nG 4\n"
"v_max3_u32 v245, v17, v19, v21\nv_max3_u32 v245, v245, v23, v23\nv_cmp_ne_u32 s[86:87], s46, v244\nM 156,176,76\nR 168,189,49152\nv_cmp_ne_u32 s[88:89], s46, v245\n"
"M 156,180,92\ns_add_u32 s64, s70, 10498048\nbuffer_load_dwordx4 a[192:195], v252, s[20:23], s64 offen offset:0 sc1\ns_or_b64 s[86:87], s[86:87], s[88:89]\ns_cbranch_scc1 Lq333\nLq334:\n"
"M 140,176,72\nR 170,189,53248\nv_add_f32 v16, v16, v20\nv_add_f32 v18, v18, v22\nM 140,180,88\nv_add_f32_dpp v16, v16, v16 row_ror:8 row_mask:0xf bank_mask:0xf\n"
"v_add_f32_dpp v18, v18, v18 row_ror:8 row_mask:0xf bank_mask:0xf\nv_mov_b32 v20, v16\nM 124,176,68\nR 172,185,0\nv_mov_b32 v21, v18\nv_permlane32_swap_b32 v20, v16\n"
"M 124,180,84\ns_add_u32 s65, s70, 10530816\nbuffer_load_dwordx4 a[196:199], v252, s[20:23], s65 offen offset:0 sc1\nv_permlane32_swap_b32 v21, v18\nM 108,176,64\nR 174,185,4096\n"
"v_add_f32 v16, v16, v20\nv_add_f32 v18, v18, v21\nM 108,180,80\nG 4\nv_mov_b32 v20, v16\nv_mov_b32 v21, v18\n"
"s_nop 0\nv_permlane16_swap_b32 v20, v16\nM 32,160,96\nR 176,190,49152\nv_permlane16_swap_b32 v21, v18\nM 32,164,112\n"
"v_add_f32 v16, v16, v20\nv_add_f32 v18, v18, v21\nM 48,160,100\nR 178,190,53248\nv_and_b32 v24, s77, v16\nM 48,164,116\n"
"s_add_u32 s66, s71, 10498048\nbuffer_load_dwordx4 a[200:203], v252, s[20:23], s66 offen offset:0 sc1\nM 64,160,104\nR 180,186,0\nv_sub_f32 v25, v16, v24\nM 64,164,120\n"
"s_add_u32 m0, s50, 40960\ns_waitcnt vmcnt(15)\nv_lshlrev_b32 v26, 16, v243\nv_cvt_pk_bf16_f32 v20, v24, v25\nM 80,160,108\nR 182,186,4096\n"
"s_add_u32 s63, s54, 2293760\nM 80,164,124\ns_waitcnt vmcnt(12)\ns_barrier\nG 4\nM 84,168,108\n"
"R 160,191,49152\nv_cvt_pk_bf16_f32 v21, v24, v26\nM 84,172,124\ns_add_u32 s67, s71, 10530816\nbuffer_load_dwordx4 a[204:207], v252, s[20:23], s67 offen offset:0 sc1\nM 68,168,104\n"
"R 162,191,53248\nv_and_b32 v24, s77, v18\nM 68,172,120\nbuffer_load_dwordx4 v220, s[12:15], s63 offen lds\nM 52,168,100\nR 164,187,0\n"
"v_sub_f32 v25, v18, v24\nM 52,172,116\nbuffer_load_dwordx4 v221, s[12:15], s63 offen offset:1024 lds\nM 36,168,96\nR 166,187,4096\ns_add_u32 m0, s50, 49152\n"
"M 36,172,112\nG 4\nv_and_b32 v26, s77, v243\nv_cvt_pk_bf16_f32 v22, v24, v25\nM 40,176,96\nR 168,184,8192\n"
"s_add_u32 s63, s54, 2523136\nM 40,180,112\nbuffer_load_dwordx4 a[208:211], v252, s[20:23], s64 offen offset:1024 sc1\nM 56,176,100\nR 170,184,12288\nv_cvt_pk_bf16_f32 v23, v24, v26\n"
"M 56,180,116\nbuffer_load_dwordx4 v220, s[12:15], s63 offen lds\nM 72,176,104\nR 172,184,16384\nv_and_b32 v242, 0x1ff, v242\nM 72,180,120\n"
"buffer_load_dwordx4 a[212:215], v252, s[20:23], s65 offen offset:1024 sc1\nM 88,176,108\nR 174,184,20480\nv_add_u32 v242, 158720, v242\nM 88,180,124\nds_write_b128 v242, v[20:23]\n"
"G 5\nM 92,160,108\nR 176,185,8192\nbuffer_load_dwordx4 v221, s[12:15], s63 offen offset:1024 lds\nM 92,164,124\nM 76,160,104\n"
"R 178,185,12288\nM 76,164,120\nbuffer_load_dwordx4 a[216:219], v252, s[20:23], s66 offen offset:1024 sc1\nM 60,160,100\nR 180,185,16384\nLq566:\n"
"s_lshr_b32 s60, s45, 3\ns_lshl2_add_u32 s60, s60, s40\ns_lshl_b32 s60, s60, 8\nv_lshl_add_u32 v224, v252, 3, s60\nv_add_u32 v224, Lq566+4096-LqPC, v224\ns_mov_b64 exec, 3\n"
"global_load_dword v224, v224, s[98:99]\ns_mov_b64 exec, -1\nM 60,164,116\nM 44,160,96\nR 182,185,20480\nM 44,164,112\n"
"G 5\nM 96,168,96\nR 160,186,8192\nM 96,172,112\nbuffer_load_dwordx4 a[220:223], v252, s[20:23], s67 offen offset:1024 sc1\nM 112,168,100\n"
"R 162,186,12288\nM 112,172,116\nM 128,168,104\nR 164,186,16384\nM 128,172,120\nM 144,168,108\n"
"R 166,186,20480\ns_add_u32 m0, s50, 57344\nM 144,172,124\ns_waitcnt vmcnt(13) lgkmcnt(8)\ns_barrier\nG 4\n"
"M 148,176,108\nR 168,187,8192\ns_add_u32 s63, s54, 2752512\nM 148,180,124\nbuffer_load_dwordx4 a[224:227], v252, s[20:23], s64 offen offset:2048 sc1\nM 132,176,104\n"
"R 170,187,12288\nM 132,180,120\nbuffer_load_dwordx4 a[228:231], v252, s[20:23], s65 offen offset:2048 sc1\nM 116,176,100\nR 172,187,16384\nbuffer_load_dwordx4 v220, s[12:15], s63 offen lds\n"
"M 116,180,116\nbuffer_load_dwordx4 v221, s[12:15], s63 offen offset:1024 lds\nM 100,176,96\nR 174,187,20480\ns_add_u32 m0, s50, 65536\nM 100,180,112\n"
"G 4\nM 104,160,96\nR 176,184,24576\ns_add_u32 s63, s54, 2981888\nM 104,164,112\nbuffer_load_dwordx4 a[232:235], v252, s[20:23], s66 offen offset:2048 sc1\n"
"M 120,160,100\nR 178,184,28672\nM 120,164,116\nbuffer_load_dwordx4 v220, s[12:15], s63 offen lds\nM 136,160,104\nR 180,184,32768\n"
"M 136,164,120\nbuffer_load_dwordx4 v221, s[12:15], s63 offen offset:1024 lds\nM 152,160,108\nR 182,184,36864\nM 152,164,124\nG 4\n"
"M 156,168,108\nR 160,185,24576\nM 156,172,124\nbuffer_load_dwordx4 a[236:239], v252, s[20:23], s67 offen offset:2048 sc1\nM 140,168,104\nR 162,185,28672\n"
"M 140,172,120\nM 124,168,100\nR 164,185,32768\nM 124,172,116\nbuffer_load_dwordx4 a[240:243], v252, s[20:23], s64 offen offset:3072 sc1\nM 108,168,96\n"
"R 166,185,36864\nM 108,172,112\nG 4\nM 32,176,128\nR 168,186,24576\nM 32,180,144\n"
"M 48,176,132\nR 170,186,28672\nM 48,180,148\nbuffer_load_dwordx4 a[244:247], v252, s[20:23], s65 offen offset:3072 sc1\nM 64,176,136\nR 172,186,32768\n"
"M 64,180,152\nM 80,176,140\nR 174,186,36864\ns_add_u32 m0, s50, 73728\nM 80,180,156\ns_waitcnt vmcnt(13)\n"
"s_barrier\nG 4\nM 84,160,140\nR 176,187,24576\ns_add_u32 s63, s54, 3211264\nM 84,164,156\n"
"buffer_load_dwordx4 a[248:251], v252, s[20:23], s66 offen offset:3072 sc1\nM 68,160,136\nR 178,187,28672\nM 68,164,152\ns_lshr_b32 s84, s49, 3\nM 52,160,132\n"
"R 180,187,32768\nbuffer_load_dwordx4 v220, s[12:15], s63 offen lds\nM 52,164,148\ns_add_u32 s84, s84, 82313216\nM 36,160,128\nR 182,187,36864\n"
"M 36,164,144\nbuffer_load_dwordx4 v[0:3], v252, s[20:23], s84 offen offset:0 sc1\nG 4\nM 40,168,128\nR 160,184,40960\nbuffer_load_dwordx4 v221, s[12:15], s63 offen offset:1024 lds\n"
"M 40,172,144\nbuffer_load_dwordx4 a[252:255], v252, s[20:23], s67 offen offset:3072 sc1\ns_add_u32 s63, s54, 3440640\nM 56,168,132\nR 162,184,45056\ns_add_u32 m0, s50, 81920\n"
"M 56,172,148\nbuffer_load_dwordx4 v[4:7], v252, s[20:23], s84 offen offset:1024 sc1\nM 72,168,136\nR 164,184,49152\ns_lshr_b32 s85, s49, 3\nbuffer_load_dwordx4 v220, s[12:15], s63 offen lds\n"
"M 72,172,152\nbuffer_load_dwordx4 v[8:11], v252, s[20:23], s84 offen offset:2048 sc1\nM 88,168,140\nR 166,184,53248\ns_add_u32 s85, s85, 82317312\nM 88,172,156\n"
"G 4\nM 92,176,140\nR 168,185,40960\nbuffer_load_dwordx4 v221, s[12:15], s63 offen offset:1024 lds\nM 92,180,156\nbuffer_load_dwordx4 v[12:15], v252, s[20:23], s84 offen offset:3072 sc1\n"
"M 76,176,136\nR 170,185,45056\nM 76,180,152\nbuffer_load_dwordx4 v[16:19], v252, s[20:23], s85 offen offset:0 sc1\nM 60,176,132\nR 172,185,49152\n"
"M 60,180,148\nbuffer_load_dwordx4 v[20:23], v252, s[20:23], s85 offen offset:1024 sc1\nM 44,176,128\nR 174,185,53248\nM 44,180,144\nG 4\n"
"M 32,160,160\nR 176,186,40960\nM 32,164,176\nbuffer_load_dwordx4 v[24:27], v252, s[20:23], s85 offen offset:2048 sc1\nM 48,160,164\nR 178,186,45056\n"
"M 48,164,180\nbuffer_load_dwordx4 v[28:31], v252, s[20:23], s85 offen offset:3072 sc1\nM 64,160,168\nR 180,186,49152\ns_add_u32 s63, s54, 1835136\nM 64,164,184\n"
"s_add_u32 m0, s50, 90112\nM 80,160,172\nR 182,186,53248\nM 80,164,188\ns_waitcnt vmcnt(17)\ns_barrier\n"
"G 4\nM 84,168,172\nR 160,187,40960\nM 84,172,188\nbuffer_load_dwordx4 v220, s[12:15], s63 offen lds\nM 68,168,168\n"
"R 162,187,45056\nM 68,172,184\nM 52,168,164\nR 164,187,49152\nv_bfe_u32 v250, v252, 5, 1\nv_mul_u32_u24 v250, 2560, v250\n"
"M 52,172,180\nbuffer_load_dwordx4 v221, s[12:15], s63 offen offset:1024 lds\nv_and_b32 v251, 16, v252\nM 36,168,160\nR 166,187,53248\ns_add_u32 m0, s50, 98304\n"
"v_lshl_add_u32 v250, v251, 1, v250\nM 36,172,176\ns_lshl_b32 s60, s90, 6\nv_add_u32 v250, s60, v250\nG 4\nM 40,176,160\n"
"R 168,184,57344\ns_add_u32 s63, s54, 2064512\nM 40,180,176\nbuffer_load_dwordx4 v220, s[12:15], s63 offen lds\nM 56,176,164\nR 170,184,61440\n"
"M 56,180,180\nM 72,176,168\nR 172,188,0\nM 72,180,184\nbuffer_load_dwordx4 v221, s[12:15], s63 offen offset:1024 lds\nM 88,176,172\n"
"R 174,188,4096\nM 88,180,188\nG 4\nM 92,160,172\nR 176,185,57344\nM 92,164,188\n"
"M 76,160,168\nR 178,185,61440\nM 76,164,184\ns_add_u32 m0, s50, 106496\nM 60,160,164\nR 180,189,0\n"
"M 60,164,180\ns_add_u32 s63, s54, 2293888\nM 44,160,160\nR 182,189,4096\nM 44,164,176\nG 4\n"
"M 32,168,192\nR 160,186,57344\nM 32,172,208\nbuffer_load_dwordx4 v220, s[12:15], s63 offen lds\nM 48,168,196\nR 162,186,61440\n"
"M 48,172,212\nM 64,168,200\nR 164,190,0\nM 64,172,216\nbuffer_load_dwordx4 v221, s[12:15], s63 offen offset:1024 lds\nM 80,168,204\n"
"R 166,190,4096\ns_add_u32 m0, s50, 114688\nM 80,172,220\ns_waitcnt vmcnt(11)\ns_barrier\nG 4\n"
"M 84,176,204\nR 168,187,57344\ns_add_u32 s63, s54, 2523264\nM 84,180,220\nbuffer_load_dwordx4 v220, s[12:15], s63 offen lds\nM 68,176,200\n"
"R 170,187,61440\nv_min3_u32 v230, v1, v3, v5\nv_min3_u32 v230, v230, v7, v9\nM 68,180,216\ns_waitcnt vmcnt(11)\nv_min3_u32 v230, v230, v11, v13\n"
"s_waitcnt vmcnt(10)\nv_min3_u32 v230, v230, v15, v17\ns_waitcnt vmcnt(9)\nv_min3_u32 v230, v230, v19, v21\nM 52,176,196\nR 172,191,0\n"
"s_waitcnt vmcnt(8)\nv_min3_u32 v230, v230, v23, v25\ns_waitcnt vmcnt(7)\nv_min3_u32 v230, v230, v27, v29\nM 52,180,212\nbuffer_load_dwordx4 v221, s[12:15], s63 offen offset:1024 lds\n"
"v_min3_u32 v230, v230, v31, v31\nM 36,176,192\nR 174,191,4096\ns_add_u32 s63, s54, 2752640\nv_max3_u32 v231, v1, v3, v5\nM 36,180,208\n"
"G 4\nv_max3_u32 v231, v231, v7, v9\nv_max3_u32 v231, v231, v11, v13\nv_max3_u32 v231, v231, v15, v17\nM 40,160,192\nR 176,188,8192\n"
"v_max3_u32 v231, v231, v19, v21\nv_max3_u32 v231, v231, v23, v25\nM 40,164,208\ns_add_u32 m0, s50, 0\nv_max3_u32 v231, v231, v27, v29\nv_max3_u32 v231, v231, v31, v31\n"
"v_cmp_ne_u32 s[86:87], s46, v230\nM 56,160,196\nR 178,188,12288\nv_cmp_ne_u32 s[88:89], s46, v231\nM 56,164,212\nbuffer_load_dwordx4 v220, s[12:15], s63 offen lds\n"
"s_or_b64 s[86:87], s[86:87], s[88:89]\ns_cbranch_scc1 Lq301\nLq302:\nM 72,160,200\nR 180,188,16384\nv_add_f32 v0, v0, v16\n"
"v_add_f32 v2, v2, v18\nM 72,164,216\nv_add_f32 v4, v4, v20\nv_add_f32 v6, v6, v22\nv_add_f32 v8, v8, v24\nM 88,160,204\n"
"R 182,188,20480\nv_add_f32 v10, v10, v26\nv_add_f32 v12, v12, v28\nM 88,164,220\nG 4\nv_add_f32 v14, v14, v30\n"
"v_add_f32 v0, v0, v8\nv_add_f32 v2, v2, v10\nM 92,168,204\nR 160,189,8192\nv_add_f32 v4, v4, v12\nv_add_f32 v6, v6, v14\n"
"M 92,172,220\nbuffer_load_dwordx4 v221, s[12:15], s63 offen offset:1024 lds\nv_add_f32 v0, v0, v4\nM 76,168,200\nR 162,189,12288\ns_add_u32 s63, s54, 2982016\n"
"v_add_f32 v2, v2, v6\nM 76,172,216\ns_add_u32 m0, s50, 8192\nv_lshrrev_b32 v231, 1, v252\nv_lshrrev_b32 v230, 2, v252\ns_lshl_b32 s62, s40, 9\n"
"s_add_u32 s62, s62, 159232\nv_add_u32 v231, s62, v231\nM 60,168,196\nR 164,189,16384\nds_write2_b32 v231, v0, v2 offset1:1\nM 60,172,212\n"
"buffer_load_dwordx4 v220, s[12:15], s63 offen lds\ns_lshl_b32 s62, s40, 9\ns_add_u32 s62, s62, 159232\nv_add_u32 v230, s62, v230\nM 44,168,192\nR 166,189,20480\n"
"ds_read2st64_b32 v[232:233], v230 offset1:1\nM 44,172,208\nG 6\nM 32,176,224\nR 168,190,8192\nM 32,180,240\n"
"buffer_load_dwordx4 v221, s[12:15], s63 offen offset:1024 lds\nM 48,176,228\nR 170,190,12288\ns_add_u32 s63, s54, 3211392\nM 48,180,244\ns_add_u32 m0, s50, 16384\n"
"M 64,176,232\nR 172,190,16384\nG 3\nv_add_f32 v232, v232, v233\nv_mov_b32 v233, v232\nM 64,180,248\n"
"buffer_load_dwordx4 v220, s[12:15], s63 offen lds\nv_permlane32_swap_b32 v232, v233\nM 80,176,236\nR 174,190,20480\nv_mov_b32 v230, v232\nv_mov_b32 v231, v233\n"
"M 80,180,252\ns_waitcnt vmcnt(9)\ns_barrier\nv_permlane16_swap_b32 v230, v232\nM 84,160,236\nR 176,191,8192\n"
"v_permlane16_swap_b32 v231, v233\nv_mul_f32 v230, 0xba800000, v230\nM 84,164,252\nbuffer_load_dwordx4 v221, s[12:15], s63 offen offset:1024 lds\nv_mul_f32 v214, v230, v230\nM 68,160,232\n"
"R 178,191,12288\nv_mul_f32 v231, 0x3a800000, v231\nv_sub_f32 v231, v231, v214\nM 68,164,248\ns_add_u32 m0, s50, 24576\nv_max_f32 v231, 0, v231\n"
"v_add_f32 v231, 0x3727c5ac, v231\nv_rsq_f32 v234, v231\nM 52,160,228\nR 180,191,16384\nv_and_b32 v208, s77, v230\nv_mul_f32 v216, v231, v234\n"
"M 52,164,244\ns_add_u32 s63, s54, 3440768\nv_sub_f32 v230, v230, v208\nv_cvt_pk_bf16_f32 v209, v230, v216\nv_cvt_pk_bf16_f32 v208, v208, v208\nM 36,160,224\n"
"R 182,191,20480\nv_mul_f32 v214, 0xc0135761, v234\nv_mul_f32 v215, v234, v234\nM 36,164,240\ns_mov_b64 s[60:61], 0xffff\nv_cndmask_b32 v208, 0, v208, s[60:61]\n"
"G 4\nv_mul_f32 v215, v215, v214\nM 40,168,224\nR 160,188,24576\nv_mul_f32 v215, 0x3d372713, v215\nv_mul_f32 v232, 0xba800000, v232\n"
"M 40,172,240\nbuffer_load_dwordx4 v220, s[12:15], s63 offen lds\nv_mul_f32 v217, v232, v232\nM 56,168,228\nR 162,188,28672\nv_mul_f32 v233, 0x3a800000, v233\n"
"v_sub_f32 v233, v233, v217\nM 56,172,244\ns_mov_b64 s[60:61], 0xffff\nv_cndmask_b32 v209, 0, v209, s[60:61]\nv_max_f32 v233, 0, v233\nv_add_f32 v233, 0x3727c5ac, v233\n"
"M 72,168,232\nR 164,188,32768\nv_and_b32 v212, s77, v232\nv_rsq_f32 v235, v233\nM 72,172,248\nbuffer_load_dwordx4 v221, s[12:15], s63 offen offset:1024 lds\n"
"v_mul_f32 v219, v233, v235\nM 88,168,236\nR 166,188,36864\ns_add_u32 m0, s50, 32768\nv_sub_f32 v232, v232, v212\nM 88,172,252\n"
"G 4\nv_cvt_pk_bf16_f32 v213, v232, v219\nv_cvt_pk_bf16_f32 v212, v212, v212\nv_mul_f32 v217, 0xc0135761, v235\nM 92,176,236\nR 168,189,24576\n"
"s_add_u32 s63, s55, 0\nv_mul_f32 v218, v235, v235\nM 92,180,252\nbuffer_load_dwordx4 v222, s[12:15], s63 offen lds\nv_mul_f32 v218, v218, v217\nM 76,176,232\n"
"R 170,189,28672\nv_mul_f32 v218, 0x3d372713, v218\ns_mov_b64 s[60:61], 0xffff\nv_cndmask_b32 v212, 0, v212, s[60:61]\nM 76,180,248\ns_mov_b64 s[60:61], 0xffff\n"
"v_cndmask_b32 v213, 0, v213, s[60:61]\nM 60,176,228\nR 172,189,32768\nM 60,180,244\nbuffer_load_dwordx4 v223, s[12:15], s63 offen offset:1024 lds\nM 44,176,224\n"
"R 174,189,36864\nM 44,180,240\ns_waitcnt vmcnt(10)\ns_barrier\nG 4\nM 96,160,128\n"
"R 176,190,24576\nM 96,164,144\ns_mov_b32 m0, s51\nM 112,160,132\nR 178,190,28672\ns_lshl2_add_u32 s60, s45, s40\n"
"v_lshlrev_b32 v224, 3, v252\ns_mul_i32 s60, s60, 7168\ns_bfm_b64 exec, 14, 0\nbuffer_load_dword v224, v224, s[16:19], s60 offen\ns_mov_b64 exec, -1\nM 112,164,148\n"
"ds_write_addtid_b32 v64 offset:0\nM 128,160,136\nR 180,190,32768\nM 128,164,152\nds_write_addtid_b32 v65 offset:256\nM 144,160,140\n"
"R 182,190,36864\nds_write_addtid_b32 v66 offset:512\nM 144,164,156\nds_write_addtid_b32 v67 offset:768\nds_write_addtid_b32 v68 offset:1024\nG 9\n"
"M 148,168,140\nR 160,191,24576\nds_write_addtid_b32 v69 offset:1280\nM 148,172,156\nds_write_addtid_b32 v70 offset:1536\nds_write_addtid_b32 v71 offset:1792\n"
"M 132,168,136\nR 162,191,28672\nds_write_addtid_b32 v72 offset:2048\nM 132,172,152\nds_write_addtid_b32 v73 offset:2304\nds_write_addtid_b32 v74 offset:2560\n"
"M 116,168,132\nR 164,191,32768\nds_write_addtid_b32 v75 offset:2816\nM 116,172,148\nds_write_addtid_b32 v76 offset:3072\nds_write_addtid_b32 v77 offset:3328\n"
"M 100,168,128\nR 166,191,36864\nds_write_addtid_b32 v78 offset:3584\nM 100,172,144\nds_write_addtid_b32 v79 offset:3840\nds_write_addtid_b32 v80 offset:4096\n"
"G 15\nM 104,176,128\nR 168,188,40960\nds_write_addtid_b32 v81 offset:4352\nM 104,180,144\nds_write_addtid_b32 v82 offset:4608\n"
"ds_write_addtid_b32 v83 offset:4864\nM 120,176,132\nR 170,188,45056\nds_write_addtid_b32 v84 offset:5120\nM 120,180,148\nds_write_addtid_b32 v85 offset:5376\n"
"ds_write_addtid_b32 v86 offset:5632\nM 136,176,136\nR 172,188,49152\nds_write_addtid_b32 v87 offset:5888\nM 136,180,152\nds_write_addtid_b32 v88 offset:6144\n"
"ds_write_addtid_b32 v89 offset:6400\nM 152,176,140\nR 174,188,53248\nds_write_addtid_b32 v90 offset:6656\nM 152,180,156\nds_write_addtid_b32 v91 offset:6912\n"
"ds_write_addtid_b32 v92 offset:7168\nG 15\nM 156,160,140\nR 176,189,40960\nds_write_addtid_b32 v93 offset:7424\nM 156,164,156\n"
"ds_write_addtid_b32 v94 offset:7680\nds_write_addtid_b32 v95 offset:7936\nM 140,160,136\nR 178,189,45056\ns_add_u32 m0, s50, 40960\ns_add_u32 s63, s55, 458752\n"
"M 140,164,152\nbuffer_load_dwordx4 v222, s[12:15], s63 offen lds\nM 124,160,132\nR 180,189,49152\ns_waitcnt vmcnt(8) lgkmcnt(2)\ns_barrier\n"
"M 124,164,148\nds_read2st64_b32 v[64:65], v225 offset0:0 offset1:1\nds_read2st64_b32 v[66:67], v225 offset0:2 offset1:3\nM 108,160,128\nR 182,189,53248\nds_read2st64_b32 v[68:69], v225 offset0:4 offset1:5\n"
"M 108,164,144\nds_read2st64_b32 v[70:71], v225 offset0:6 offset1:7\nds_read2st64_b32 v[72:73], v225 offset0:8 offset1:9\nM 96,168,160\nR 160,190,40960\nds_read2st64_b32 v[74:75], v225 offset0:10 offset1:11\n"
"M 96,172,176\nbuffer_load_dwordx4 v223, s[12:15], s63 offen offset:1024 lds\nds_read2st64_b32 v[76:77], v225 offset0:12 offset1:13\nM 112,168,164\nR 162,190,45056\nds_read2st64_b32 v[78:79], v225 offset0:14 offset1:15\n"
"M 112,172,180\ns_add_u32 m0, s50, 49152\nds_read2st64_b32 v[80:81], v225 offset0:16 offset1:17\nG 11\nv_add_f32 v32, v32, v64\nM 128,168,168\n"
"R 164,190,49152\nv_add_f32 v33, v33, v65\nG 11\nv_add_f32 v34, v34, v66\nM 128,172,184\ns_add_u32 s63, s55, 917504\n"
"ds_read2st64_b32 v[82:83], v225 offset0:18 offset1:19\nv_add_f32 v35, v35, v67\nM 144,168,172\nR 166,190,53248\nG 11\nv_add_f32 v36, v36, v68\n"
"v_add_f32 v37, v37, v69\nM 144,172,188\nds_read2st64_b32 v[84:85], v225 offset0:20 offset1:21\nG 11\nv_add_f32 v38, v38, v70\nM 148,176,172\n"
"R 168,191,40960\nv_add_f32 v39, v39, v71\nG 11\nv_add_f32 v40, v40, v72\nM 148,180,188\nbuffer_load_dwordx4 v222, s[12:15], s63 offen lds\n"
"v_add_f32 v41, v41, v73\nM 132,176,168\nR 170,191,45056\nG 10\nv_add_f32 v42, v42, v74\nv_add_f32 v43, v43, v75\n"
"M 132,180,184\nds_read2st64_b32 v[86:87], v225 offset0:22 offset1:23\nG 10\nv_add_f32 v44, v44, v76\nv_add_f32 v45, v45, v77\nM 116,176,164\n"
"R 172,191,49152\nG 9\nv_add_f32 v46, v46, v78\nv_add_f32 v47, v47, v79\nM 116,180,180\nds_read2st64_b32 v[88:89], v225 offset0:24 offset1:25\n"
"G 9\nv_add_f32 v48, v48, v80\nv_add_f32 v49, v49, v81\nM 100,176,160\nR 174,191,53248\nG 8\n"
"v_add_f32 v50, v50, v82\nv_add_f32 v51, v51, v83\nM 100,180,176\nds_read2st64_b32 v[90:91], v225 offset0:26 offset1:27\nG 8\nG 7\n"
"v_add_f32 v52, v52, v84\nM 104,160,160\nR 176,184,0\ns_mov_b32 s60, 157184\nv_bfe_u32 v246, v252, 4, 4\nv_lshl_add_u32 v246, v246, 3, s60\n"
"M 104,164,176\nds_read_b64 v[238:239], v246 offset:0\nv_add_f32 v53, v53, v85\nM 120,160,164\nR 178,184,4096\nds_read_b64 v[240:241], v246 offset:128\n"
"M 120,164,180\nds_read_b64 v[242:243], v246 offset:256\nG 9\nv_add_f32 v54, v54, v86\nM 136,160,168\nR 180,184,8192\n"
"ds_read_b64 v[244:245], v246 offset:384\nM 136,164,184\nds_read_b64 v[246:247], v246 offset:0\nv_add_f32 v55, v55, v87\nM 152,160,172\nR 182,184,12288\n"
"G 11\nv_add_f32 v56, v56, v88\nM 152,164,188\nds_read2st64_b32 v[92:93], v225 offset0:28 offset1:29\nG 11\nv_add_f32 v57, v57, v89\n"
"M 156,168,172\nR 160,185,0\nG 11\nv_add_f32 v58, v58, v90\nM 156,172,188\nbuffer_load_dwordx4 v223, s[12:15], s63 offen offset:1024 lds\n"
"M 140,168,168\nR 162,185,4096\nv_add_f32 v59, v59, v91\nM 140,172,184\nds_read2st64_b32 v[94:95], v225 offset0:30 offset1:31\nM 124,168,164\n"
"R 164,185,8192\nM 124,172,180\ns_add_u32 m0, s50, 57344\nG 4\nv_add_f32 v60, v60, v92\nv_add_f32 v61, v61, v93\n"
"G 1\nv_add_f32 v62, v62, v94\nv_add_f32 v63, v63, v95\nM 108,168,160\nR 166,185,12288\nv_mfma_f32_16x16x32_bf16 v[32:35], v[238:241], v[208:211], v[32:35]\n"
"M 108,172,176\nv_mfma_f32_16x16x32_bf16 v[36:39], v[240:243], v[208:211], v[36:39]\nM 96,176,192\nR 168,186,0\nv_mfma_f32_16x16x32_bf16 v[40:43], v[242:245], v[208:211], v[40:43]\nM 96,180,208\n"
"v_mfma_f32_16x16x32_bf16 v[44:47], v[244:247], v[208:211], v[44:47]\ns_add_u32 s63, s55, 1376256\nM 112,176,196\nR 170,186,4096\ns_lshl2_add_u32 s60, s45, s40\ns_waitcnt vmcnt(4)\n"
"v_lshlrev_b32 v224, 3, v252\ns_mul_i32 s60, s60, 7168\ns_bfm_b64 exec, 14, 14\nbuffer_load_dword v224, v224, s[16:19], s60 offen\ns_mov_b64 exec, -1\nv_mfma_f32_16x16x32_bf16 v[48:51], v[244:247], v[210:213], v[48:51]\n"
"s_barrier\nM 112,180,212\nv_mfma_f32_16x16x32_bf16 v[52:55], v[238:241], v[210:213], v[52:55]\nbuffer_load_dwordx4 v222, s[12:15], s63 offen lds\nM 128,176,200\nR 172,186,8192\n"
"v_mfma_f32_16x16x32_bf16 v[56:59], v[240:243], v[210:213], v[56:59]\nM 128,180,216\nv_mfma_f32_16x16x32_bf16 v[60:63], v[242:245], v[210:213], v[60:63]\nM 144,176,204\nR 174,186,12288\nM 144,180,220\n"
"G 4\nM 148,160,204\nR 176,187,0\nM 148,164,220\nbuffer_load_dwordx4 v223, s[12:15], s63 offen offset:1024 lds\nM 132,160,200\n"
"R 178,187,4096\nM 132,164,216\ns_add_u32 m0, s50, 65536\nM 116,160,196\nR 180,187,8192\nv_mul_f32 v230, v32, v32\n"
"v_mul_f32 v231, v33, v33\nM 116,164,212\ns_add_u32 s63, s55, 1835008\ns_add_u32 s84, s73, 23068672\ns_add_u32 s85, s73, 23183360\nv_mul_f32 v232, v34, v34\n"
"M 100,160,192\nR 182,187,12288\nv_mul_f32 v233, v35, v35\nv_fma_f32 v230, v230, v215, v214\nM 100,164,208\nG 4\n"
"v_fma_f32 v231, v231, v215, v214\nv_fma_f32 v232, v232, v215, v214\nv_fma_f32 v233, v233, v215, v214\nM 104,168,192\nR 160,184,16384\nv_mul_f32 v230, v32, v230\n"
"v_mul_f32 v231, v33, v231\nM 104,172,208\nbuffer_load_dwordx4 v222, s[12:15], s63 offen lds\nv_mul_f32 v232, v34, v232\nM 120,168,196\nR 162,184,20480\n"
"v_mul_f32 v233, v35, v233\nv_exp_f32 v230, v230\nM 120,172,212\nv_exp_f32 v231, v231\nv_exp_f32 v232, v232\nM 136,168,200\n"
"R 164,184,24576\nv_fma_f32 v230, v230, v216, v216\nv_exp_f32 v233, v233\nM 136,172,216\nv_rcp_f32 v230, v230\nv_fma_f32 v231, v231, v216, v216\n"
"M 152,168,204\nR 166,184,28672\nv_fma_f32 v232, v232, v216, v216\nv_fma_f32 v233, v233, v216, v216\nM 152,172,220\nG 4\n"
"v_rcp_f32 v231, v231\nv_rcp_f32 v232, v232\nM 156,176,204\nR 168,185,16384\nv_mul_f32 v230, v32, v230\nv_rcp_f32 v233, v233\n"
"M 156,180,220\nbuffer_load_dwordx4 v223, s[12:15], s63 offen offset:1024 lds\nv_mul_f32 v231, v33, v231\nM 140,176,200\nR 170,185,20480\nv_mul_f32 v232, v34, v232\n"
"v_mul_f32 v233, v35, v233\nM 140,180,216\nv_cvt_pk_bf16_f32 v32, v230, v231\nv_cvt_pk_bf16_f32 v33, v232, v233\nv_mul_f32 v234, v36, v36\nM 124,176,196\n"
"R 172,185,24576\nv_mul_f32 v235, v37, v37\nv_mul_f32 v236, v38, v38\nM 124,180,212\nv_mul_f32 v237, v39, v39\nv_fma_f32 v234, v234, v215, v214\n"
"v_fma_f32 v235, v235, v215, v214\nM 108,176,192\nR 174,185,28672\ns_mov_b32 s60, 157696\nv_bfe_u32 v246, v252, 4, 4\nv_lshl_add_u32 v246, v246, 3, s60\n"
"v_fma_f32 v236, v236, v215, v214\nM 108,180,208\nds_read_b64 v[238:239], v246 offset:0\nG 5\nv_fma_f32 v237, v237, v215, v214\nM 96,160,224\n"
"R 176,186,16384\nds_read_b64 v[240:241], v246 offset:128\nv_mul_f32 v234, v36, v234\nM 96,164,240\nds_read_b64 v[242:243], v246 offset:256\ns_add_u32 m0, s50, 73728\n"
"v_mul_f32 v235, v37, v235\nM 112,160,228\nR 178,186,20480\nds_read_b64 v[244:245], v246 offset:384\nv_mul_f32 v236, v38, v236\nM 112,164,244\n"
"ds_read_b64 v[246:247], v246 offset:0\ns_add_u32 s63, s55, 2293760\nv_mul_f32 v237, v39, v237\nM 128,160,232\nR 180,186,24576\nv_mul_f32 v230, v40, v40\n"
"v_exp_f32 v234, v234\nM 128,164,248\nbuffer_load_dwordx4 v222, s[12:15], s63 offen lds\nv_exp_f32 v235, v235\nM 144,160,236\nR 182,186,28672\n"
"v_fma_f32 v234, v234, v216, v216\nv_exp_f32 v236, v236\nM 144,164,252\nG 9\nv_exp_f32 v237, v237\nv_fma_f32 v235, v235, v216, v216\n"
"M 148,168,236\nR 160,187,16384\nv_fma_f32 v236, v236, v216, v216\nv_fma_f32 v237, v237, v216, v216\nM 148,172,252\nbuffer_load_dwordx4 v223, s[12:15], s63 offen offset:1024 lds\n"
"v_rcp_f32 v234, v234\nM 132,168,232\nR 162,187,20480\ns_add_u32 s63, s55, 2752512\nv_rcp_f32 v235, v235\nM 132,172,248\n"
"s_add_u32 m0, s50, 81920\nv_rcp_f32 v236, v236\nv_rcp_f32 v237, v237\nM 116,168,228\nR 164,187,24576\nG 9\n"
"v_mfma_f32_16x16x32_bf16 v[96:99], v[238:241], v[208:211], v[96:99]\nv_mul_f32 v234, v36, v234\nv_mul_f32 v235, v37, v235\nM 116,172,244\nbuffer_load_dwordx4 v222, s[12:15], s63 offen lds\nv_mul_f32 v236, v38, v236\n"
"M 100,168,224\nR 166,187,28672\nG 9\nv_mfma_f32_16x16x32_bf16 v[100:103], v[240:243], v[208:211], v[100:103]\nv_mul_f32 v237, v39, v237\nv_cvt_pk_bf16_f32 v34, v234, v235\n"
"M 100,172,240\nG 4\nv_cvt_pk_bf16_f32 v35, v236, v237\nv_mul_f32 v231, v41, v41\nv_mul_f32 v232, v42, v42\nM 104,176,224\n"
"R 168,184,32768\nv_mfma_f32_16x16x32_bf16 v[104:107], v[242:245], v[208:211], v[104:107]\nv_mul_f32 v233, v43, v43\nv_fma_f32 v230, v230, v215, v214\nM 104,180,240\nbuffer_load_dwordx4 v223, s[12:15], s63 offen offset:1024 lds\n"
"v_fma_f32 v231, v231, v215, v214\nM 120,176,228\nR 170,184,36864\nv_mfma_f32_16x16x32_bf16 v[108:111], v[244:247], v[208:211], v[108:111]\nv_fma_f32 v232, v232, v215, v214\nv_fma_f32 v233, v233, v215, v214\n"
"M 120,180,244\ns_add_u32 m0, s50, 90112\nv_mul_f32 v230, v40, v230\nv_mul_f32 v231, v41, v231\nv_mul_f32 v232, v42, v232\nM 136,176,232\n"
"R 172,186,32768\nv_mfma_f32_16x16x32_bf16 v[112:115], v[244:247], v[210:213], v[112:115]\nv_mul_f32 v233, v43, v233\nv_exp_f32 v230, v230\nM 136,180,248\ns_add_u32 s63, s55, 3211264\n"
"v_exp_f32 v231, v231\nv_exp_f32 v232, v232\nM 152,176,236\nR 174,186,36864\nv_mfma_f32_16x16x32_bf16 v[116:119], v[238:241], v[210:213], v[116:119]\nv_fma_f32 v230, v230, v216, v216\n"
"v_exp_f32 v233, v233\nM 152,180,252\nG 4\nv_rcp_f32 v230, v230\nv_fma_f32 v231, v231, v216, v216\nM 156,160,236\n"
"R 176,185,32768\nv_mfma_f32_16x16x32_bf16 v[120:123], v[240:243], v[210:213], v[120:123]\nv_fma_f32 v232, v232, v216, v216\nv_fma_f32 v233, v233, v216, v216\nM 156,164,252\nbuffer_load_dwordx4 v222, s[12:15], s63 offen lds\n"
"v_rcp_f32 v231, v231\nM 140,160,232\nR 178,185,36864\nv_mfma_f32_16x16x32_bf16 v[124:127], v[242:245], v[210:213], v[124:127]\nv_mul_f32 v230, v40, v230\nv_rcp_f32 v232, v232\n"
"M 140,164,248\nv_rcp_f32 v233, v233\nv_mul_f32 v231, v41, v231\nM 124,160,228\nR 180,187,32768\nv_mul_f32 v232, v42, v232\n"
"v_mul_f32 v233, v43, v233\nM 124,164,244\nbuffer_load_dwordx4 v223, s[12:15], s63 offen offset:1024 lds\nv_cvt_pk_bf16_f32 v40, v230, v231\nM 108,160,224\nR 182,187,36864\n"
"v_cvt_pk_bf16_f32 v41, v232, v233\nv_mul_f32 v234, v44, v44\nM 108,164,240\ns_waitcnt vmcnt(11)\ns_barrier\nG 4\n"
"v_mul_f32 v235, v45, v45\nv_mfma_f32_16x16x32_bf16 v[0:3], v[168:171], a[0:3], 0\nR 160,184,40960\nv_mul_f32 v236, v46, v46\nv_mul_f32 v237, v47, v47\nM 0,172,16\n"
"s_mov_b32 m0, s51\nv_fma_f32 v234, v234, v215, v214\nv_fma_f32 v235, v235, v215, v214\nv_fma_f32 v236, v236, v215, v214\nv_mfma_f32_16x16x32_bf16 v[16:19], v[168:171], a[4:7], 0\nR 162,184,45056\n"
"v_fma_f32 v237, v237, v215, v214\nv_mul_f32 v234, v44, v234\nM 16,172,20\nbuffer_store_dwordx4 v[32:35], v252, s[20:23], s84 offen offset:0 sc1\nv_mul_f32 v235, v45, v235\nv_mfma_f32_16x16x32_bf16 v[8:11], v[168:171], a[8:11], 0\n"
"R 164,186,40960\nv_mul_f32 v236, v46, v236\nv_mul_f32 v237, v47, v237\nM 8,172,24\nds_write_addtid_b32 v128 offset:0\nv_exp_f32 v234, v234\n"
"v_exp_f32 v235, v235\nv_mfma_f32_16x16x32_bf16 v[24:27], v[168:171], a[12:15], 0\nR 166,186,45056\nds_write_addtid_b32 v129 offset:256\nv_fma_f32 v234, v234, v216, v216\nv_exp_f32 v236, v236\n"
"M 24,172,28\nds_write_addtid_b32 v130 offset:512\nG 7\nv_exp_f32 v237, v237\nv_fma_f32 v235, v235, v216, v216\nv_mfma_f32_16x16x32_bf16 v[28:31], v[176:179], a[12:15], 0\n"
"R 168,185,40960\nds_write_addtid_b32 v131 offset:768\nv_fma_f32 v236, v236, v216, v216\nv_fma_f32 v237, v237, v216, v216\nM 28,180,28\nds_write_addtid_b32 v132 offset:1024\n"
"v_rcp_f32 v234, v234\nv_rcp_f32 v235, v235\nv_mfma_f32_16x16x32_bf16 v[12:15], v[176:179], a[8:11], 0\nR 170,185,45056\nds_write_addtid_b32 v133 offset:1280\nv_mul_f32 v234, v44, v234\n"
"v_rcp_f32 v236, v236\nM 12,180,24\nds_write_addtid_b32 v134 offset:1536\nv_rcp_f32 v237, v237\nv_mul_f32 v235, v45, v235\nv_mfma_f32_16x16x32_bf16 v[20:23], v[176:179], a[4:7], 0\n"
"R 172,187,40960\nds_write_addtid_b32 v135 offset:1792\nv_mul_f32 v236, v46, v236\nv_mul_f32 v237, v47, v237\nM 20,180,20\nds_write_addtid_b32 v136 offset:2048\n"
"v_cvt_pk_bf16_f32 v42, v234, v235\nv_cvt_pk_bf16_f32 v43, v236, v237\nv_mul_f32 v230, v48, v48\nv_mfma_f32_16x16x32_bf16 v[4:7], v[176:179], a[0:3], 0\nR 174,187,45056\nds_write_addtid_b32 v137 offset:2304\n"
"v_mul_f32 v231, v49, v49\nv_mul_f32 v232, v50, v50\nM 4,180,16\nds_write_addtid_b32 v138 offset:2560\nG 14\nv_mul_f32 v233, v51, v51\n"
"v_fma_f32 v230, v230, v218, v217\nv_fma_f32 v231, v231, v218, v217\nM 0,160,32\nR 176,184,49152\nds_write_addtid_b32 v139 offset:2816\nv_fma_f32 v232, v232, v218, v217\n"
"v_fma_f32 v233, v233, v218, v217\nM 0,164,48\nbuffer_store_dwordx4 v[40:43], v252, s[20:23], s84 offen offset:1024 sc1\nds_write_addtid_b32 v140 offset:3072\nv_mul_f32 v230, v48, v230\nM 16,160,36\n"
"R 178,184,53248\nds_write_addtid_b32 v141 offset:3328\nv_mul_f32 v231, v49, v231\nv_mul_f32 v232, v50, v232\nM 16,164,52\nds_write_addtid_b32 v142 offset:3584\n"
"v_exp_f32 v230, v230\nv_mul_f32 v233, v51, v233\nM 8,160,40\nR 180,186,49152\nds_write_addtid_b32 v143 offset:3840\nv_fma_f32 v230, v230, v219, v219\n"
"v_exp_f32 v231, v231\nM 8,164,56\nds_write_addtid_b32 v144 offset:4096\nv_exp_f32 v232, v232\nv_exp_f32 v233, v233\nM 24,160,44\n"
"R 182,186,53248\nds_write_addtid_b32 v145 offset:4352\nv_fma_f32 v231, v231, v219, v219\nv_fma_f32 v232, v232, v219, v219\nM 24,164,60\nds_write_addtid_b32 v146 offset:4608\n"
"G 14\nv_rcp_f32 v230, v230\nv_fma_f32 v233, v233, v219, v219\nM 28,168,44\nR 160,185,49152\nds_write_addtid_b32 v147 offset:4864\n"
"v_mul_f32 v230, v48, v230\nv_rcp_f32 v231, v231\nM 28,172,60\nds_write_addtid_b32 v148 offset:5120\nds_write_addtid_b32 v149 offset:5376\nds_write_addtid_b32 v150 offset:5632\n"
"ds_write_addtid_b32 v151 offset:5888\nds_write_addtid_b32 v152 offset:6144\nds_write_addtid_b32 v153 offset:6400\nds_write_addtid_b32 v154 offset:6656\nds_write_addtid_b32 v155 offset:6912\nds_write_addtid_b32 v156 offset:7168\n"
"ds_write_addtid_b32 v157 offset:7424\nds_write_addtid_b32 v158 offset:7680\nds_write_addtid_b32 v159 offset:7936\nv_rcp_f32 v232, v232\nv_rcp_f32 v233, v233\nM 12,168,40\n"
"R 162,185,53248\ns_add_u32 m0, s50, 98304\ns_add_u32 s63, s54, 256\nv_mul_f32 v231, v49, v231\nM 12,172,56\nbuffer_load_dwordx4 v220, s[12:15], s63 offen lds\n"
"v_mul_f32 v232, v50, v232\nM 20,168,36\nR 164,187,49152\ns_waitcnt vmcnt(5) lgkmcnt(2)\ns_barrier\nv_mul_f32 v233, v51, v233\n"
"M 20,172,52\nds_read2st64_b32 v[128:129], v225 offset0:0 offset1:1\nv_cvt_pk_bf16_f32 v48, v230, v231\nv_cvt_pk_bf16_f32 v49, v232, v233\nv_mul_f32 v234, v52, v52\nM 4,168,32\n"
"R 166,187,53248\nds_read2st64_b32 v[130:131], v225 offset0:2 offset1:3\nv_mul_f32 v235, v53, v53\nv_mul_f32 v236, v54, v54\nM 4,172,48\nds_read2st64_b32 v[132:133], v225 offset0:4 offset1:5\n"
"v_mul_f32 v237, v55, v55\nv_fma_f32 v234, v234, v218, v217\nv_fma_f32 v235, v235, v218, v217\nM 0,176,64\nR 168,184,57344\nds_read2st64_b32 v[134:135], v225 offset0:6 offset1:7\n"
"v_fma_f32 v236, v236, v218, v217\nv_fma_f32 v237, v237, v218, v217\nM 0,180,80\nbuffer_load_dwordx4 v221, s[12:15], s63 offen offset:1024 lds\nds_read2st64_b32 v[136:137], v225 offset0:8 offset1:9\nv_mul_f32 v234, v52, v234\n"
"M 16,176,68\nR 170,184,61440\ns_add_u32 m0, s50, 106496\ns_add_u32 s63, s54, 229632\nds_read2st64_b32 v[138:139], v225 offset0:10 offset1:11\nv_mul_f32 v235, v53, v235\n"
"M 16,180,84\nbuffer_load_dwordx4 v220, s[12:15], s63 offen lds\nG 8\nv_add_f32 v96, v96, v128\nv_mul_f32 v236, v54, v236\nM 8,176,72\n"
"R 172,186,57344\nv_mul_f32 v237, v55, v237\nv_add_f32 v97, v97, v129\nv_exp_f32 v234, v234\nM 8,180,88\nds_read2st64_b32 v[140:141], v225 offset0:12 offset1:13\n"
"v_exp_f32 v235, v235\nv_exp_f32 v236, v236\nM 24,176,76\nR 174,186,61440\nv_fma_f32 v234, v234, v219, v219\nG 9\n"
"v_add_f32 v98, v98, v130\nv_exp_f32 v237, v237\nM 24,180,92\nv_rcp_f32 v234, v234\nv_add_f32 v99, v99, v131\nv_fma_f32 v235, v235, v219, v219\n"
"M 28,160,76\nR 176,185,57344\nv_fma_f32 v236, v236, v219, v219\nG 9\nv_add_f32 v100, v100, v132\nv_fma_f32 v237, v237, v219, v219\n"
"M 28,164,92\nbuffer_load_dwordx4 v221, s[12:15], s63 offen offset:1024 lds\nv_add_f32 v101, v101, v133\nv_rcp_f32 v235, v235\nM 12,160,72\nR 178,185,61440\n"
"s_add_u32 s63, s54, 459008\nG 8\nv_add_f32 v102, v102, v134\nv_rcp_f32 v236, v236\nM 12,164,88\ns_add_u32 m0, s50, 114688\n"
"v_rcp_f32 v237, v237\nv_add_f32 v103, v103, v135\nv_mul_f32 v234, v52, v234\nM 20,160,68\nR 180,187,57344\nv_mul_f32 v235, v53, v235\n"
"G 8\nv_add_f32 v104, v104, v136\nv_mul_f32 v236, v54, v236\nM 20,164,84\nbuffer_load_dwordx4 v220, s[12:15], s63 offen lds\nv_add_f32 v105, v105, v137\n"
"v_mul_f32 v237, v55, v237\nM 4,160,64\nR 182,187,61440\nv_cvt_pk_bf16_f32 v50, v234, v235\nG 7\nv_add_f32 v106, v106, v138\n"
"v_cvt_pk_bf16_f32 v51, v236, v237\nM 4,164,80\nG 4\nv_mul_f32 v230, v56, v56\nv_mul_f32 v231, v57, v57\nv_add_f32 v107, v107, v139\n"
"v_mul_f32 v232, v58, v58\nM 0,168,96\nR 160,188,0\nv_mul_f32 v233, v59, v59\nv_add_f32 v108, v108, v140\nv_fma_f32 v230, v230, v218, v217\n"
"M 0,172,112\nbuffer_load_dwordx4 v221, s[12:15], s63 offen offset:1024 lds\nv_add_f32 v109, v109, v141\nv_fma_f32 v231, v231, v218, v217\nM 16,168,100\nR 162,188,4096\n"
"ds_read2st64_b32 v[142:143], v225 offset0:14 offset1:15\nv_fma_f32 v232, v232, v218, v217\nv_fma_f32 v233, v233, v218, v217\nM 16,172,116\ns_add_u32 m0, s50, 0\nds_read2st64_b32 v[144:145], v225 offset0:16 offset1:17\n"
"v_mul_f32 v230, v56, v230\nv_mul_f32 v231, v57, v231\nv_mul_f32 v232, v58, v232\nM 8,168,104\nR 164,190,0\nds_read2st64_b32 v[146:147], v225 offset0:18 offset1:19\n"
"v_mul_f32 v233, v59, v233\nv_exp_f32 v230, v230\nM 8,172,120\ns_add_u32 s63, s54, 688384\nds_read2st64_b32 v[148:149], v225 offset0:20 offset1:21\nv_exp_f32 v231, v231\n"
"v_exp_f32 v232, v232\nM 24,168,108\nR 166,190,4096\nds_read2st64_b32 v[150:151], v225 offset0:22 offset1:23\nv_fma_f32 v230, v230, v219, v219\nv_exp_f32 v233, v233\n"
"M 24,172,124\nds_read2st64_b32 v[152:153], v225 offset0:24 offset1:25\nG 10\nv_rcp_f32 v230, v230\nv_fma_f32 v231, v231, v219, v219\nM 28,176,108\n"
"R 168,189,0\nv_fma_f32 v232, v232, v219, v219\nG 8\nv_add_f32 v110, v110, v142\nv_fma_f32 v233, v233, v219, v219\nM 28,180,124\n"
"buffer_load_dwordx4 v220, s[12:15], s63 offen lds\nv_add_f32 v111, v111, v143\nv_rcp_f32 v231, v231\nM 12,176,104\nR 170,189,4096\nv_mul_f32 v230, v56, v230\n"
"G 8\nv_add_f32 v112, v112, v144\nv_rcp_f32 v232, v232\nM 12,180,120\nds_read2st64_b32 v[154:155], v225 offset0:26 offset1:27\nv_rcp_f32 v233, v233\n"
"v_mul_f32 v231, v57, v231\nM 20,176,100\nR 172,191,0\nv_mul_f32 v232, v58, v232\nv_add_f32 v113, v113, v145\nv_mul_f32 v233, v59, v233\n"
"M 20,180,116\nbuffer_load_dwordx4 v221, s[12:15], s63 offen offset:1024 lds\nds_read2st64_b32 v[156:157], v225 offset0:28 offset1:29\nds_read2st64_b32 v[158:159], v225 offset0:30 offset1:31\nG 10\nv_add_f32 v114, v114, v146\n"
"v_add_f32 v115, v115, v147\nG 9\nv_add_f32 v116, v116, v148\nv_add_f32 v117, v117, v149\nG 7\nv_add_f32 v118, v118, v150\n"
"v_add_f32 v119, v119, v151\nG 6\nv_add_f32 v120, v120, v152\nv_add_f32 v121, v121, v153\nG 3\nv_add_f32 v122, v122, v154\n"
"v_add_f32 v123, v123, v155\nG 1\nv_add_f32 v124, v124, v156\nv_add_f32 v125, v125, v157\nG 0\nv_add_f32 v126, v126, v158\n"
"v_add_f32 v127, v127, v159\nv_cvt_pk_bf16_f32 v56, v230, v231\nM 4,176,96\nR 174,191,4096\ns_add_u32 m0, s50, 8192\nv_cvt_pk_bf16_f32 v57, v232, v233\n"
"M 4,180,112\nv_mul_f32 v234, v60, v60\nv_mul_f32 v235, v61, v61\nv_mul_f32 v236, v62, v62\nM 0,160,128\nR 176,188,8192\n"
"s_add_u32 s63, s54, 917760\nv_mul_f32 v237, v63, v63\nM 0,164,144\nbuffer_load_dwordx4 v220, s[12:15], s63 offen lds\nv_fma_f32 v234, v234, v218, v217\nM 16,160,132\n"
"R 178,188,12288\nv_fma_f32 v235, v235, v218, v217\nv_fma_f32 v236, v236, v218, v217\nM 16,164,148\nv_fma_f32 v237, v237, v218, v217\nv_mul_f32 v234, v60, v234\n"
"v_mul_f32 v235, v61, v235\nM 8,160,136\nR 180,190,8192\nLq568:\ns_lshr_b32 s60, s45, 3\ns_lshl2_add_u32 s60, s60, s40\n"
"s_lshl_b32 s60, s60, 8\nv_lshl_add_u32 v224, v252, 3, s60\nv_add_u32 v224, Lq568+4096-LqPC, v224\ns_mov_b64 exec, 3\nglobal_load_dword v224, v224, s[98:99]\ns_mov_b64 exec, -1\n"
"v_mul_f32 v236, v62, v236\nM 8,164,152\nv_exp_f32 v234, v234\nv_mul_f32 v237, v63, v237\nM 24,160,140\nR 182,190,12288\n"
"v_fma_f32 v234, v234, v219, v219\nv_exp_f32 v235, v235\nM 24,164,156\nG 4\nv_exp_f32 v236, v236\nv_exp_f32 v237, v237\n"
"M 28,168,140\nR 160,189,8192\nv_fma_f32 v235, v235, v219, v219\nv_fma_f32 v236, v236, v219, v219\nM 28,172,156\nbuffer_load_dwordx4 v221, s[12:15], s63 offen offset:1024 lds\n"
"v_fma_f32 v237, v237, v219, v219\nM 12,168,136\nR 162,189,12288\ns_add_u32 m0, s50, 16384\ns_add_u32 s63, s54, 1147136\nv_rcp_f32 v234, v234\n"
"M 12,172,152\nbuffer_load_dwordx4 v220, s[12:15], s63 offen lds\nv_rcp_f32 v235, v235\nM 20,168,132\nR 164,191,8192\nv_mul_f32 v234, v60, v234\n"
"v_rcp_f32 v236, v236\nM 20,172,148\nv_rcp_f32 v237, v237\nM 4,168,128\nR 166,191,12288\nv_mul_f32 v235, v61, v235\n"
"M 4,172,144\nG 4\nv_mul_f32 v236, v62, v236\nv_mul_f32 v237, v63, v237\nM 0,176,160\nR 168,188,16384\n"
"v_cvt_pk_bf16_f32 v58, v234, v235\nM 0,180,176\nbuffer_load_dwordx4 v221, s[12:15], s63 offen offset:1024 lds\nM 16,176,164\nR 170,188,20480\ns_add_u32 m0, s50, 24576\n"
"s_add_u32 s63, s54, 1376512\nM 16,180,180\nbuffer_load_dwordx4 v220, s[12:15], s63 offen lds\nM 8,176,168\nR 172,190,16384\nv_cvt_pk_bf16_f32 v59, v236, v237\n"
"M 8,180,184\nbuffer_store_dwordx4 v[48:51], v252, s[20:23], s85 offen offset:0 sc1\nM 24,176,172\nR 174,190,20480\nM 24,180,188\nbuffer_store_dwordx4 v[56:59], v252, s[20:23], s85 offen offset:1024 sc1\n"
"G 4\nM 28,160,172\nR 176,189,16384\ns_waitcnt vmcnt(12)\ns_barrier\nv_mul_f32 v230, v96, v96\n"
"M 28,164,188\nbuffer_load_dwordx4 v221, s[12:15], s63 offen offset:1024 lds\nv_mul_f32 v231, v97, v97\nM 12,160,168\nR 178,189,20480\ns_add_u32 m0, s50, 32768\n"
"s_add_u32 s63, s54, 1605888\nv_mul_f32 v232, v98, v98\nM 12,164,184\nbuffer_load_dwordx4 v220, s[12:15], s63 offen lds\nv_mul_f32 v233, v99, v99\nM 20,160,164\n"
"R 180,191,16384\nv_fma_f32 v230, v230, v215, v214\nv_fma_f32 v231, v231, v215, v214\nM 20,164,180\ns_add_u32 s84, s73, 23070720\ns_add_u32 s85, s73, 23185408\n"
"v_fma_f32 v232, v232, v215, v214\nv_fma_f32 v233, v233, v215, v214\nM 4,160,160\nR 182,191,20480\ns_mov_b32 s60, 158720\nv_bfe_u32 v242, v252, 4, 4\n"
"v_lshl_add_u32 v242, v242, 3, s60\nv_mul_f32 v230, v96, v230\nM 4,164,176\nds_read_b64 v[238:239], v242 offset:0\nG 5\nv_mul_f32 v231, v97, v231\n"
"M 0,168,192\nR 160,188,24576\nds_read_b64 v[240:241], v242 offset:128\nv_mul_f32 v232, v98, v232\nM 0,172,208\nds_read_b64 v[242:243], v242 offset:0\n"
"v_mul_f32 v233, v99, v233\nv_exp_f32 v230, v230\nM 16,168,196\nR 162,188,28672\nv_fma_f32 v230, v230, v216, v216\nv_exp_f32 v231, v231\n"
"M 16,172,212\nv_exp_f32 v232, v232\nv_exp_f32 v233, v233\nM 8,168,200\nR 164,190,24576\nv_fma_f32 v231, v231, v216, v216\n"
"v_fma_f32 v232, v232, v216, v216\nM 8,172,216\nv_rcp_f32 v230, v230\nv_fma_f32 v233, v233, v216, v216\nM 24,168,204\nR 166,190,28672\n"
"v_mul_f32 v230, v96, v230\nv_rcp_f32 v231, v231\nM 24,172,220\nG 7\nv_rcp_f32 v232, v232\nv_rcp_f32 v233, v233\n"
"M 28,176,204\nR 168,189,24576\nv_mul_f32 v231, v97, v231\nv_mul_f32 v232, v98, v232\nM 28,180,220\nv_mul_f32 v233, v99, v233\n"
"v_cvt_pk_bf16_f32 v96, v230, v231\nv_cvt_pk_bf16_f32 v97, v232, v233\nM 12,176,200\nR 170,189,28672\nv_mul_f32 v234, v100, v100\nv_mul_f32 v235, v101, v101\n"
"M 12,180,216\nv_mul_f32 v236, v102, v102\nv_mul_f32 v237, v103, v103\nv_fma_f32 v234, v234, v215, v214\nM 20,176,196\nR 172,191,24576\n"
"G 7\nv_mfma_f32_16x16x32_bf16 v[0:3], v[238:241], v[208:211], v[0:3]\nv_fma_f32 v235, v235, v215, v214\nv_fma_f32 v236, v236, v215, v214\nM 20,180,212\nv_fma_f32 v237, v237, v215, v214\n"
"v_mul_f32 v234, v100, v234\nv_mul_f32 v235, v101, v235\nM 4,176,192\nR 174,191,28672\nG 7\nv_mfma_f32_16x16x32_bf16 v[4:7], v[240:243], v[208:211], v[4:7]\n"
"v_mul_f32 v236, v102, v236\nv_mul_f32 v237, v103, v237\nM 4,180,208\nG 4\nv_exp_f32 v234, v234\nv_exp_f32 v235, v235\n"
"M 0,160,224\nR 176,188,32768\nv_mfma_f32_16x16x32_bf16 v[16:19], v[240:243], v[210:213], v[16:19]\nv_fma_f32 v234, v234, v216, v216\nv_exp_f32 v236, v236\nM 0,164,240\n"
"v_exp_f32 v237, v237\nv_fma_f32 v235, v235, v216, v216\nM 16,160,228\nR 178,188,36864\nv_mfma_f32_16x16x32_bf16 v[20:23], v[238:241], v[210:213], v[20:23]\nv_fma_f32 v236, v236, v216, v216\n"
"v_fma_f32 v237, v237, v216, v216\nM 16,164,244\nv_rcp_f32 v234, v234\nv_rcp_f32 v235, v235\nM 8,160,232\nR 180,188,40960\n"
"v_mul_f32 v234, v100, v234\nv_rcp_f32 v236, v236\nM 8,164,248\nv_rcp_f32 v237, v237\nv_mul_f32 v235, v101, v235\nM 24,160,236\n"
"R 182,188,45056\nv_mul_f32 v236, v102, v236\nv_mul_f32 v237, v103, v237\nM 24,164,252\nG 4\nv_cvt_pk_bf16_f32 v98, v234, v235\n"
"v_cvt_pk_bf16_f32 v99, v236, v237\nv_mul_f32 v230, v104, v104\nM 28,168,236\nR 160,189,32768\nv_mul_f32 v231, v105, v105\nv_mul_f32 v232, v106, v106\n"
"M 28,172,252\nv_mul_f32 v233, v107, v107\nv_fma_f32 v230, v230, v215, v214\nv_fma_f32 v231, v231, v215, v214\nM 12,168,232\nR 162,189,36864\n"
"v_fma_f32 v232, v232, v215, v214\nv_fma_f32 v233, v233, v215, v214\nM 12,172,248\nv_mul_f32 v230, v104, v230\nv_mul_f32 v231, v105, v231\nv_mul_f32 v232, v106, v232\n"
"M 20,168,228\nR 164,189,40960\nv_mul_f32 v233, v107, v233\nv_exp_f32 v230, v230\nM 20,172,244\nv_exp_f32 v231, v231\n"
"v_exp_f32 v232, v232\nM 4,168,224\nR 166,189,45056\nv_fma_f32 v230, v230, v216, v216\nv_exp_f32 v233, v233\nM 4,172,240\n"
"s_waitcnt vmcnt(12)\ns_barrier\nG 4\nv_fma_f32 v231, v231, v216, v216\nv_mfma_f32_16x16x32_bf16 v[32:35], v[176:179], a[0:3], 0\nR 168,190,32768\n"
"v_fma_f32 v232, v232, v216, v216\nv_fma_f32 v233, v233, v216, v216\nM 32,180,16\ns_mov_b32 m0, s51\nv_rcp_f32 v230, v230\nv_rcp_f32 v231, v231\n"
"v_mfma_f32_16x16x32_bf16 v[48:51], v[176:179], a[4:7], 0\nR 170,190,36864\nds_write_addtid_b32 v8 offset:0\nv_mul_f32 v230, v104, v230\nv_rcp_f32 v232, v232\nM 48,180,20\n"
"ds_write_addtid_b32 v9 offset:256\nv_rcp_f32 v233, v233\nv_mul_f32 v231, v105, v231\nv_mfma_f32_16x16x32_bf16 v[64:67], v[176:179], a[8:11], 0\nR 172,190,40960\nds_write_addtid_b32 v10 offset:512\n"
"v_mov_b32 v226, s46\nv_mul_f32 v232, v106, v232\nM 64,180,24\nds_write_addtid_b32 v11 offset:768\nv_mov_b32 v227, s47\nv_mul_f32 v233, v107, v233\n"
"v_cvt_pk_bf16_f32 v104, v230, v231\nv_mfma_f32_16x16x32_bf16 v[80:83], v[176:179], a[12:15], 0\nR 174,190,45056\ns_add_u32 s61, s81, 385024\ns_lshl_b32 s62, s44, 5\ns_add_u32 s61, s61, s62\n"
"s_waitcnt vmcnt(0)\ns_mov_b64 exec, 1\nbuffer_atomic_swap_x2 v[226:227], off, s[20:23], s61 sc1\ns_mov_b64 exec, -1\nds_write_addtid_b32 v12 offset:1024\nv_cvt_pk_bf16_f32 v105, v232, v233\n"
"M 80,180,28\nds_write_addtid_b32 v13 offset:1280\nG 10\nv_mul_f32 v234, v108, v108\nv_mul_f32 v235, v109, v109\nv_mul_f32 v236, v110, v110\n"
"v_mfma_f32_16x16x32_bf16 v[84:87], v[160:163], a[12:15], 0\nR 176,191,32768\nds_write_addtid_b32 v14 offset:1536\nv_mul_f32 v237, v111, v111\nv_fma_f32 v234, v234, v215, v214\nM 84,164,28\n"
"ds_write_addtid_b32 v15 offset:1792\nv_fma_f32 v235, v235, v215, v214\nv_fma_f32 v236, v236, v215, v214\nv_fma_f32 v237, v237, v215, v214\nv_mfma_f32_16x16x32_bf16 v[68:71], v[160:163], a[8:11], 0\nR 178,191,36864\n"
"ds_write_addtid_b32 v24 offset:4096\nv_mul_f32 v234, v108, v234\nv_mul_f32 v235, v109, v235\nM 68,164,24\nbuffer_store_dwordx4 v[96:99], v252, s[20:23], s84 offen offset:0 sc1\nds_write_addtid_b32 v25 offset:4352\n"
"v_mul_f32 v236, v110, v236\nv_mfma_f32_16x16x32_bf16 v[52:55], v[160:163], a[4:7], 0\nR 180,191,40960\nds_write_addtid_b32 v26 offset:4608\nv_mul_f32 v237, v111, v237\nv_exp_f32 v234, v234\n"
"M 52,164,20\nds_write_addtid_b32 v27 offset:4864\nds_write_addtid_b32 v28 offset:5120\nds_write_addtid_b32 v29 offset:5376\nds_write_addtid_b32 v30 offset:5632\nds_write_addtid_b32 v31 offset:5888\n"
"v_exp_f32 v235, v235\nv_exp_f32 v236, v236\nv_mfma_f32_16x16x32_bf16 v[36:39], v[160:163], a[0:3], 0\nR 182,191,45056\ns_add_u32 m0, s50, 32768\nv_exp_f32 v237, v237\n"
"M 36,164,16\nG 15\nv_fma_f32 v234, v234, v216, v216\nv_fma_f32 v235, v235, v216, v216\nv_fma_f32 v236, v236, v216, v216\nv_mfma_f32_16x16x32_bf16 v[40:43], v[168:171], a[0:3], 0\n"
"R 160,188,49152\nG 2\ns_barrier\nv_fma_f32 v237, v237, v216, v216\nM 40,172,16\nbuffer_load_dwordx4 v221, s[12:15], s63 offen offset:1024 lds\n"
"ds_read2st64_b32 v[8:9], v225 offset0:0 offset1:1\nv_rcp_f32 v234, v234\nv_mfma_f32_16x16x32_bf16 v[56:59], v[168:171], a[4:7], 0\nR 162,188,53248\ns_add_u32 m0, s50, 40960\ns_add_u32 s63, s54, 1835264\n"
"ds_read2st64_b32 v[10:11], v225 offset0:2 offset1:3\nv_rcp_f32 v235, v235\nM 56,172,20\nbuffer_load_dwordx4 v220, s[12:15], s63 offen lds\nds_read2st64_b32 v[12:13], v225 offset0:4 offset1:5\nv_rcp_f32 v236, v236\n"
"v_mfma_f32_16x16x32_bf16 v[72:75], v[168:171], a[8:11], 0\nR 164,184,0\nds_read2st64_b32 v[14:15], v225 offset0:6 offset1:7\nv_mov_b32 v248, 0\nv_rcp_f32 v237, v237\nM 72,172,24\n"
"ds_read2st64_b32 v[24:25], v225 offset0:16 offset1:17\nv_mov_b32 v249, 0\nv_mul_f32 v234, v108, v234\nv_mul_f32 v235, v109, v235\nv_mfma_f32_16x16x32_bf16 v[88:91], v[168:171], a[12:15], 0\nR 166,184,4096\n"
"s_add_u32 s61, s82, 385024\ns_mov_b64 exec, 0xf\nbuffer_atomic_or_x2 v[248:249], v250, s[20:23], s61 offen sc0 sc1\ns_mov_b64 exec, -1\nds_read2st64_b32 v[26:27], v225 offset0:18 offset1:19\nv_mul_f32 v236, v110, v236\n"
"M 88,172,28\nG 10\nv_mul_f32 v237, v111, v237\nv_cvt_pk_bf16_f32 v106, v234, v235\nG 8\nv_add_f32 v0, v0, v8\n"
"v_cvt_pk_bf16_f32 v107, v236, v237\nv_mfma_f32_16x16x32_bf16 v[92:95], v[176:179], a[12:15], 0\nR 168,189,49152\nv_mul_f32 v230, v112, v112\nv_add_f32 v1, v1, v9\nv_mul_f32 v231, v113, v113\n"
"M 92,180,28\nbuffer_load_dwordx4 v221, s[12:15], s63 offen offset:1024 lds\nG 7\nv_add_f32 v2, v2, v10\nv_mul_f32 v232, v114, v114\nv_mfma_f32_16x16x32_bf16 v[76:79], v[176:179], a[8:11], 0\n"
"R 170,189,53248\ns_add_u32 m0, s50, 49152\ns_add_u32 s63, s54, 2064640\nv_add_f32 v3, v3, v11\nv_mul_f32 v233, v115, v115\nM 76,180,24\n"
"buffer_load_dwordx4 v220, s[12:15], s63 offen lds\nG 7\nv_add_f32 v4, v4, v12\nv_fma_f32 v230, v230, v218, v217\nv_mfma_f32_16x16x32_bf16 v[60:63], v[176:179], a[4:7], 0\nR 172,185,0\n"
"v_fma_f32 v231, v231, v218, v217\nv_add_f32 v5, v5, v13\nv_fma_f32 v232, v232, v218, v217\nM 60,180,20\nds_read2st64_b32 v[28:29], v225 offset0:20 offset1:21\nv_fma_f32 v233, v233, v218, v217\n"
"v_mul_f32 v230, v112, v230\nv_mul_f32 v231, v113, v231\nv_mfma_f32_16x16x32_bf16 v[44:47], v[176:179], a[0:3], 0\nR 174,185,4096\nv_mul_f32 v232, v114, v232\nG 8\n"
"v_add_f32 v6, v6, v14\nv_mul_f32 v233, v115, v233\nM 44,180,16\nG 6\nv_exp_f32 v230, v230\nv_add_f32 v7, v7, v15\n"
"v_exp_f32 v231, v231\nM 32,160,32\nR 176,190,49152\nv_fma_f32 v230, v230, v219, v219\nv_add_f32 v16, v16, v24\nv_exp_f32 v232, v232\n"
"M 32,164,48\nbuffer_load_dwordx4 v221, s[12:15], s63 offen offset:1024 lds\nv_add_f32 v17, v17, v25\nv_exp_f32 v233, v233\nM 48,160,36\nR 178,190,53248\n"
"v_fma_f32 v231, v231, v219, v219\nG 7\nv_add_f32 v18, v18, v26\nv_fma_f32 v232, v232, v219, v219\nM 48,164,52\nds_read2st64_b32 v[30:31], v225 offset0:22 offset1:23\n"
"v_rcp_f32 v230, v230\nv_fma_f32 v233, v233, v219, v219\nM 64,160,40\nR 180,186,0\nv_mul_f32 v230, v112, v230\nv_add_f32 v19, v19, v27\n"
"G 5\nv_add_f32 v20, v20, v28\nv_add_f32 v21, v21, v29\nG 1\nv_add_f32 v22, v22, v30\nv_add_f32 v23, v23, v31\n"
"v_rcp_f32 v231, v231\nM 64,164,56\ns_add_u32 m0, s50, 57344\nv_rcp_f32 v232, v232\nv_rcp_f32 v233, v233\nM 80,160,44\n"
"R 182,186,4096\nv_mul_f32 v231, v113, v231\nv_mul_f32 v232, v114, v232\nM 80,164,60\nv_mul_f32 v233, v115, v233\nv_cvt_pk_bf16_f32 v112, v230, v231\n"
"v_cvt_pk_bf16_f32 v113, v232, v233\nM 84,168,44\nR 160,191,49152\nv_mul_f32 v234, v116, v116\nv_mul_f32 v235, v117, v117\nM 84,172,60\n"
"s_add_u32 s63, s54, 2294016\nv_mul_f32 v236, v118, v118\nv_mul_f32 v237, v119, v119\nv_fma_f32 v234, v234, v218, v217\nM 68,168,40\nR 162,191,53248\n"
"v_fma_f32 v235, v235, v218, v217\nM 68,172,56\nbuffer_load_dwordx4 v220, s[12:15], s63 offen lds\nM 52,168,36\nR 164,187,0\nv_fma_f32 v236, v236, v218, v217\n"
"M 52,172,52\nv_fma_f32 v237, v237, v218, v217\nv_mul_f32 v234, v116, v234\nM 36,168,32\nR 166,187,4096\nv_mul_f32 v235, v117, v235\n"
"M 36,172,48\nG 4\nv_exp_f32 v234, v234\nM 40,176,32\nR 168,184,8192\nv_mul_f32 v236, v118, v236\n"
"M 40,180,48\nbuffer_load_dwordx4 v221, s[12:15], s63 offen offset:1024 lds\nM 56,176,36\nR 170,184,12288\nv_mul_f32 v237, v119, v237\nM 56,180,52\n"
"s_add_u32 m0, s50, 65536\nv_exp_f32 v235, v235\nM 72,176,40\nR 172,184,16384\ns_add_u32 s63, s54, 2523392\nM 72,180,56\n"
"s_add_u32 s64, s68, 23068672\nv_exp_f32 v236, v236\nM 88,176,44\nR 174,184,20480\nv_fma_f32 v234, v234, v219, v219\nM 88,180,60\n"
"G 4\nv_exp_f32 v237, v237\nM 92,160,44\nR 176,185,8192\ns_waitcnt vmcnt(5)\nv_cmp_eq_u64 s[86:87], s[46:47], v[248:249]\n"
"M 92,164,60\nbuffer_load_dwordx4 v220, s[12:15], s63 offen lds\nM 76,160,40\nR 178,185,12288\ns_and_b32 s62, s86, 0xf\ns_cmp_eq_u32 s62, 0xf\n"
"s_cbranch_scc0 Lq367\nLq368:\nM 76,164,56\ns_add_u32 s65, s68, 23183360\nv_rcp_f32 v234, v234\nM 60,160,36\n"
"R 180,185,16384\nv_fma_f32 v235, v235, v219, v219\nM 60,164,52\nbuffer_load_dwordx4 a[0:3], v252, s[20:23], s64 offen sc1\nM 44,160,32\nR 182,185,20480\n"
"v_fma_f32 v236, v236, v219, v219\nM 44,164,48\nG 4\nv_rcp_f32 v235, v235\nM 32,168,64\nR 160,186,8192\n"
"v_fma_f32 v237, v237, v219, v219\nM 32,172,80\ns_add_u32 s66, s69, 23068672\nv_rcp_f32 v236, v236\nM 48,168,68\nR 162,186,12288\n"
"v_mul_f32 v234, v116, v234\nM 48,172,84\nbuffer_load_dwordx4 a[4:7], v252, s[20:23], s65 offen sc1\nM 64,168,72\nR 164,186,16384\nv_mul_f32 v235, v117, v235\n"
"M 64,172,88\nv_rcp_f32 v237, v237\nM 80,168,76\nR 166,186,20480\nv_mul_f32 v236, v118, v236\nM 80,172,92\n"
"G 4\nv_mul_f32 v237, v119, v237\nv_cvt_pk_bf16_f32 v114, v234, v235\nM 84,176,76\nR 168,187,8192\nv_cvt_pk_bf16_f32 v115, v236, v237\n"
"M 84,180,92\nbuffer_load_dwordx4 a[8:11], v252, s[20:23], s66 offen sc1\nM 68,176,72\nR 170,187,12288\nv_mul_f32 v230, v120, v120\nM 68,180,88\n"
"s_add_u32 s67, s69, 23183360\nv_mul_f32 v231, v121, v121\nv_mul_f32 v232, v122, v122\nM 52,176,68\nR 172,187,16384\nv_mul_f32 v233, v123, v123\n"
"M 52,180,84\ns_add_u32 s64, s68, 23075840\nbuffer_load_dwordx4 v221, s[12:15], s63 offen offset:1024 lds\nM 36,176,64\nR 174,187,20480\ns_add_u32 m0, s50, 73728\n"
"M 36,180,80\ns_add_u32 s63, s54, 2752768\nG 4\nv_fma_f32 v230, v230, v218, v217\nM 40,160,64\nR 176,184,24576\n"
"s_waitcnt vmcnt(7)\ns_barrier\nM 40,164,80\nbuffer_load_dwordx4 a[12:15], v252, s[20:23], s67 offen sc1\nbuffer_load_dwordx4 v220, s[12:15], s63 offen lds\nM 56,160,68\n"
"R 178,184,28672\nv_fma_f32 v231, v231, v218, v217\nM 56,164,84\ns_add_u32 s65, s68, 23190528\nv_fma_f32 v232, v232, v218, v217\nv_fma_f32 v233, v233, v218, v217\n"
"M 72,160,72\nR 180,184,32768\nv_mul_f32 v230, v120, v230\nM 72,164,88\nbuffer_load_dwordx4 a[16:19], v252, s[20:23], s64 offen sc1\nbuffer_load_dwordx4 v221, s[12:15], s63 offen offset:1024 lds\n"
"M 88,160,76\nR 182,184,36864\ns_add_u32 m0, s50, 81920\nM 88,164,92\nG 4\nv_exp_f32 v230, v230\n"
"M 92,168,76\nR 160,185,24576\ns_add_u32 s63, s54, 2982144\nM 92,172,92\ns_add_u32 s66, s69, 23075840\nbuffer_load_dwordx4 v220, s[12:15], s63 offen lds\n"
"M 76,168,72\nR 162,185,28672\nv_mul_f32 v231, v121, v231\nM 76,172,88\nbuffer_load_dwordx4 a[20:23], v252, s[20:23], s65 offen sc1\nM 60,168,68\n"
"R 164,185,32768\nv_mul_f32 v232, v122, v232\nM 60,172,84\nbuffer_load_dwordx4 v221, s[12:15], s63 offen offset:1024 lds\nM 44,168,64\nR 166,185,36864\n"
"v_mul_f32 v233, v123, v233\nM 44,172,80\nG 4\nv_exp_f32 v231, v231\nM 32,176,96\nR 168,186,24576\n"
"v_fma_f32 v230, v230, v219, v219\nM 32,180,112\nbuffer_load_dwordx4 a[24:27], v252, s[20:23], s66 offen sc1\nM 48,176,100\nR 170,186,28672\nv_fma_f32 v231, v231, v219, v219\n"
"M 48,180,116\ns_add_u32 s67, s69, 23190528\nv_exp_f32 v232, v232\nM 64,176,104\nR 172,186,32768\nv_fma_f32 v232, v232, v219, v219\n"
"M 64,180,120\nbuffer_store_dwordx4 v[104:107], v252, s[20:23], s84 offen offset:1024 sc1\nM 80,176,108\nR 174,186,36864\nv_mul_f32 v234, v124, v124\nM 80,180,124\n"
"G 4\nv_exp_f32 v233, v233\nM 84,160,108\nR 176,187,24576\nv_fma_f32 v233, v233, v219, v219\nM 84,164,124\n"
"buffer_load_dwordx4 a[28:31], v252, s[20:23], s67 offen sc1\nM 68,160,104\nR 178,187,28672\nv_mul_f32 v235, v125, v125\nM 68,164,120\ns_add_u32 s64, s68, 23069696\n"
"v_rcp_f32 v230, v230\nM 52,160,100\nR 180,187,32768\nv_mul_f32 v230, v120, v230\nM 52,164,116\ns_add_u32 s65, s68, 23184384\n"
"v_rcp_f32 v231, v231\nM 36,160,96\nR 182,187,36864\nv_mul_f32 v231, v121, v231\nM 36,164,112\nG 4\n"
"v_rcp_f32 v232, v232\nM 40,168,96\nR 160,184,40960\nv_mul_f32 v232, v122, v232\nM 40,172,112\nbuffer_load_dwordx4 a[32:35], v252, s[20:23], s64 offen sc1\n"
"M 56,168,100\nR 162,184,45056\nv_cvt_pk_bf16_f32 v120, v230, v231\nM 56,172,116\ns_add_u32 s66, s69, 23069696\nv_rcp_f32 v233, v233\n"
"M 72,168,104\nR 164,184,49152\nv_mul_f32 v233, v123, v233\nM 72,172,120\nbuffer_load_dwordx4 a[36:39], v252, s[20:23], s65 offen sc1\nM 88,168,108\n"
"R 166,184,53248\nv_cvt_pk_bf16_f32 v121, v232, v233\nM 88,172,124\nG 4\nv_mul_f32 v236, v126, v126\nv_mul_f32 v237, v127, v127\n"
"M 92,176,108\nR 168,185,40960\ns_add_u32 m0, s50, 90112\nM 92,180,124\ns_add_u32 s67, s69, 23184384\nv_fma_f32 v234, v234, v218, v217\n"
"v_fma_f32 v235, v235, v218, v217\nM 76,176,104\nR 170,185,45056\ns_add_u32 s63, s54, 3211520\nM 76,180,120\nbuffer_load_dwordx4 a[40:43], v252, s[20:23], s66 offen sc1\n"
"M 60,176,100\nR 172,185,49152\nv_fma_f32 v236, v236, v218, v217\nM 60,180,116\nbuffer_load_dwordx4 v220, s[12:15], s63 offen lds\nM 44,176,96\n"
"R 174,185,53248\nv_fma_f32 v237, v237, v218, v217\nM 44,180,112\nG 4\nv_mul_f32 v234, v124, v234\nv_mul_f32 v235, v125, v235\n"
"M 32,160,128\nR 176,186,40960\nv_mul_f32 v236, v126, v236\nM 32,164,144\nbuffer_load_dwordx4 a[44:47], v252, s[20:23], s67 offen sc1\nbuffer_load_dwordx4 v221, s[12:15], s63 offen offset:1024 lds\n"
"M 48,160,132\nR 178,186,45056\ns_add_u32 m0, s50, 98304\nM 48,164,148\ns_add_u32 s64, s68, 23076864\nv_exp_f32 v234, v234\n"
"M 64,160,136\nR 180,186,49152\ns_add_u32 s63, s54, 3440896\nM 64,164,152\ns_add_u32 s65, s68, 23191552\nbuffer_load_dwordx4 v220, s[12:15], s63 offen lds\n"
"M 80,160,140\nR 182,186,53248\nv_mul_f32 v237, v127, v237\nM 80,164,156\nG 4\nv_exp_f32 v235, v235\n"
"M 84,168,140\nR 160,187,40960\nv_fma_f32 v234, v234, v219, v219\nM 84,172,156\nbuffer_load_dwordx4 a[48:51], v252, s[20:23], s64 offen sc1\nbuffer_load_dwordx4 v221, s[12:15], s63 offen offset:1024 lds\n"
"M 68,168,136\nR 162,187,45056\nv_fma_f32 v235, v235, v219, v219\nM 68,172,152\ns_add_u32 s66, s69, 23076864\nv_exp_f32 v236, v236\n"
"M 52,168,132\nR 164,187,49152\ns_waitcnt vmcnt(12)\ns_barrier\nM 52,172,148\nbuffer_load_dwordx4 a[52:55], v252, s[20:23], s65 offen sc1\n"
"s_movk_i32 s63, 2048\nv_mad_u32_u24 v220, v228, s63, v229\nv_add_u32 v221, 916480, v220\ns_add_u32 m0, s50, 106496\ns_add_u32 s63, s56, 0\nbuffer_load_dwordx4 v220, s[16:19], s63 offen lds\n"
"M 36,168,128\nR 166,187,53248\nv_fma_f32 v236, v236, v219, v219\nM 36,172,144\nG 4\nv_exp_f32 v237, v237\n"
"M 40,176,128\nR 168,184,57344\nv_fma_f32 v237, v237, v219, v219\nM 40,180,144\ns_add_u32 s67, s69, 23191552\nbuffer_load_dwordx4 v221, s[16:19], s63 offen offset:1024 lds\n"
"M 56,176,132\nR 170,184,61440\ns_add_u32 m0, s50, 114688\nM 56,180,148\nbuffer_load_dwordx4 a[56:59], v252, s[20:23], s66 offen sc1\nM 72,176,136\n"
"R 172,188,0\ns_add_u32 s63, s56, 524288\nM 72,180,152\nbuffer_load_dwordx4 v220, s[16:19], s63 offen lds\nM 88,176,140\nR 174,188,4096\n"
"v_rcp_f32 v234, v234\nM 88,180,156\nG 4\nv_rcp_f32 v235, v235\nM 92,160,140\nR 176,185,57344\n"
"v_mul_f32 v234, v124, v234\nM 92,164,156\nbuffer_load_dwordx4 a[60:63], v252, s[20:23], s67 offen sc1\nbuffer_load_dwordx4 v221, s[16:19], s63 offen offset:1024 lds\nM 76,160,136\nR 178,185,61440\n"
"s_add_u32 s63, s56, 65536\nM 76,164,152\ns_add_u32 m0, s50, 0\nv_rcp_f32 v236, v236\nM 60,160,132\nR 180,189,0\n"
"v_mul_f32 v235, v125, v235\nM 60,164,148\nbuffer_load_dwordx4 v220, s[16:19], s63 offen lds\nM 44,160,128\nR 182,189,4096\nv_mul_f32 v236, v126, v236\n"
"M 44,164,144\nG 4\nv_rcp_f32 v237, v237\nM 32,168,160\nR 160,186,57344\nv_mul_f32 v237, v127, v237\n"
"M 32,172,176\nbuffer_load_dwordx4 v221, s[16:19], s63 offen offset:1024 lds\nM 48,168,164\nR 162,186,61440\ns_add_u32 s63, s56, 589824\nM 48,172,180\n"
"s_add_u32 m0, s50, 8192\nv_cvt_pk_bf16_f32 v122, v234, v235\nv_cvt_pk_bf16_f32 v123, v236, v237\nM 64,168,168\nR 164,190,0\nM 64,172,184\n"
"buffer_load_dwordx4 v220, s[16:19], s63 offen lds\nM 80,168,172\nR 166,190,4096\nbuffer_store_dwordx4 v[112:115], v252, s[20:23], s85 offen offset:0 sc1\nM 80,172,188\nbuffer_store_dwordx4 v[120:123], v252, s[20:23], s85 offen offset:1024 sc1\n"
"G 4\nM 84,176,172\nR 168,187,57344\nM 84,180,188\nbuffer_load_dwordx4 v221, s[16:19], s63 offen offset:1024 lds\nM 68,176,168\n"
"R 170,187,61440\ns_add_u32 s63, s57, 0\nv_mul_f32 v230, v0, v0\nM 68,180,184\ns_add_u32 m0, s50, 16384\ns_add_u32 s84, s73, 23074816\n"
"s_add_u32 s85, s73, 23189504\nv_mul_f32 v231, v1, v1\nM 52,176,164\nR 172,191,0\nv_mul_f32 v232, v2, v2\nv_mul_f32 v233, v3, v3\n"
"M 52,180,180\nbuffer_load_dwordx4 v220, s[16:19], s63 offen lds\nv_fma_f32 v230, v230, v215, v214\nM 36,176,160\nR 174,191,4096\nv_fma_f32 v231, v231, v215, v214\n"
"v_fma_f32 v232, v232, v215, v214\nM 36,180,176\nG 4\nv_fma_f32 v233, v233, v215, v214\nv_mul_f32 v230, v0, v230\nv_mul_f32 v231, v1, v231\n"
"M 40,160,160\nR 176,188,8192\nv_mul_f32 v232, v2, v232\nv_mul_f32 v233, v3, v233\nM 40,164,176\nv_exp_f32 v230, v230\n"
"v_exp_f32 v231, v231\nM 56,160,164\nR 178,188,12288\nv_fma_f32 v230, v230, v216, v216\nv_exp_f32 v232, v232\nM 56,164,180\n"
"v_exp_f32 v233, v233\nv_fma_f32 v231, v231, v216, v216\nM 72,160,168\nR 180,188,16384\nv_fma_f32 v232, v232, v216, v216\nv_fma_f32 v233, v233, v216, v216\n"
"M 72,164,184\nv_rcp_f32 v230, v230\nv_rcp_f32 v231, v231\nM 88,160,172\nR 182,188,20480\nv_mul_f32 v230, v0, v230\n"
"v_rcp_f32 v232, v232\nM 88,164,188\nG 4\nv_rcp_f32 v233, v233\nv_mul_f32 v231, v1, v231\nM 92,168,172\n"
"R 160,189,8192\nv_mul_f32 v232, v2, v232\nv_mul_f32 v233, v3, v233\nM 92,172,188\nv_cvt_pk_bf16_f32 v0, v230, v231\nv_cvt_pk_bf16_f32 v1, v232, v233\n"
"v_mul_f32 v234, v4, v4\nM 76,168,168\nR 162,189,12288\nv_mul_f32 v235, v5, v5\nv_mul_f32 v236, v6, v6\nM 76,172,184\n"
"v_mul_f32 v237, v7, v7\nv_fma_f32 v234, v234, v215, v214\nv_fma_f32 v235, v235, v215, v214\nM 60,168,164\nR 164,189,16384\nv_fma_f32 v236, v236, v215, v214\n"
"v_fma_f32 v237, v237, v215, v214\nM 60,172,180\nv_mul_f32 v234, v4, v234\nv_mul_f32 v235, v5, v235\nv_mul_f32 v236, v6, v236\nM 44,168,160\n"
"R 166,189,20480\nv_mul_f32 v237, v7, v237\nv_exp_f32 v234, v234\nM 44,172,176\nG 4\nv_exp_f32 v235, v235\n"
"v_exp_f32 v236, v236\nM 32,176,192\nR 168,190,8192\nv_fma_f32 v234, v234, v216, v216\nv_exp_f32 v237, v237\nM 32,180,208\n"
"v_rcp_f32 v234, v234\nv_fma_f32 v235, v235, v216, v216\nM 48,176,196\nR 170,190,12288\nv_fma_f32 v236, v236, v216, v216\nv_fma_f32 v237, v237, v216, v216\n"
"M 48,180,212\nv_rcp_f32 v235, v235\nv_rcp_f32 v236, v236\nM 64,176,200\nR 172,190,16384\nv_mul_f32 v234, v4, v234\n"
"v_rcp_f32 v237, v237\nM 64,180,216\nv_mul_f32 v235, v5, v235\nv_mul_f32 v236, v6, v236\nv_mul_f32 v237, v7, v237\nM 80,176,204\n"
"R 174,190,20480\nv_cvt_pk_bf16_f32 v2, v234, v235\nv_cvt_pk_bf16_f32 v3, v236, v237\nM 80,180,220\nG 4\nv_mul_f32 v230, v16, v16\n"
"v_mul_f32 v231, v17, v17\nv_mul_f32 v232, v18, v18\nM 84,160,204\nR 176,191,8192\ns_waitcnt vmcnt(7)\ns_barrier\n"
"v_mul_f32 v233, v19, v19\nM 84,164,220\nv_fma_f32 v230, v230, v218, v217\nv_fma_f32 v231, v231, v218, v217\nv_fma_f32 v232, v232, v218, v217\nM 68,160,200\n"
"R 178,191,12288\nv_fma_f32 v233, v233, v218, v217\nv_mul_f32 v230, v16, v230\nM 68,164,216\nv_exp_f32 v230, v230\nv_mul_f32 v231, v17, v231\n"
"M 52,160,196\nR 180,191,16384\nv_mul_f32 v232, v18, v232\nv_mul_f32 v233, v19, v233\nM 52,164,212\nv_exp_f32 v231, v231\n"
"v_exp_f32 v232, v232\nM 36,160,192\nR 182,191,20480\nv_fma_f32 v230, v230, v219, v219\nv_exp_f32 v233, v233\nM 36,164,208\n"
"G 4\nv_rcp_f32 v230, v230\nv_fma_f32 v231, v231, v219, v219\nM 40,168,192\nR 160,188,24576\nv_fma_f32 v232, v232, v219, v219\n"
"v_fma_f32 v233, v233, v219, v219\nM 40,172,208\nv_rcp_f32 v231, v231\nv_rcp_f32 v232, v232\nM 56,168,196\nR 162,188,28672\n"
"v_mul_f32 v230, v16, v230\nv_rcp_f32 v233, v233\nM 56,172,212\nv_mul_f32 v231, v17, v231\nv_mul_f32 v232, v18, v232\nv_mul_f32 v233, v19, v233\n"
"M 72,168,200\nR 164,188,32768\nv_cvt_pk_bf16_f32 v16, v230, v231\nv_cvt_pk_bf16_f32 v17, v232, v233\nM 72,172,216\nv_mul_f32 v234, v20, v20\n"
"v_mul_f32 v235, v21, v21\nv_mul_f32 v236, v22, v22\nM 88,168,204\nR 166,188,36864\nv_mov_b32 v226, s46\nv_mul_f32 v237, v23, v23\n"
"M 88,172,220\nG 4\nv_mov_b32 v227, s47\nv_fma_f32 v234, v234, v218, v217\nv_fma_f32 v235, v235, v218, v217\nM 92,176,204\n"
"R 168,189,24576\ns_add_u32 s61, s81, 385032\ns_lshl_b32 s62, s44, 5\ns_add_u32 s61, s61, s62\ns_waitcnt vmcnt(0)\ns_mov_b64 exec, 1\n"
"buffer_atomic_swap_x2 v[226:227], off, s[20:23], s61 sc1\ns_mov_b64 exec, -1\nv_fma_f32 v236, v236, v218, v217\nM 92,180,220\nbuffer_load_dwordx4 v221, s[16:19], s63 offen offset:1024 lds\nv_fma_f32 v237, v237, v218, v217\n"
"M 76,176,200\nR 170,189,28672\ns_add_u32 s63, s57, 524288\nv_mul_f32 v234, v20, v234\nM 76,180,216\ns_add_u32 m0, s50, 24576\n"
"v_exp_f32 v234, v234\nv_mul_f32 v235, v21, v235\nM 60,176,196\nR 172,189,32768\nv_mul_f32 v236, v22, v236\nv_mul_f32 v237, v23, v237\n"
"M 60,180,212\nbuffer_load_dwordx4 v220, s[16:19], s63 offen lds\nv_exp_f32 v235, v235\nM 44,176,192\nR 174,189,36864\ns_mov_b32 s60, 158208\n"
"v_bfe_u32 v246, v252, 4, 4\nv_lshl_add_u32 v246, v246, 3, s60\nv_exp_f32 v236, v236\nM 44,180,208\nds_read_b64 v[238:239], v246 offset:0\nG 5\n"
"v_exp_f32 v237, v237\nM 32,160,224\nR 176,190,24576\nds_read_b64 v[240:241], v246 offset:128\nv_fma_f32 v234, v234, v219, v219\nM 32,164,240\n"
"ds_read_b64 v[242:243], v246 offset:256\nv_fma_f32 v235, v235, v219, v219\nv_fma_f32 v236, v236, v219, v219\nM 48,160,228\nR 178,190,28672\nds_read_b64 v[244:245], v246 offset:384\n"
"v_fma_f32 v237, v237, v219, v219\nM 48,164,244\nds_read_b64 v[246:247], v246 offset:0\nbuffer_load_dwordx4 v221, s[16:19], s63 offen offset:1024 lds\nv_rcp_f32 v234, v234\nM 64,160,232\n"
"R 180,190,32768\ns_add_u32 m0, s50, 32768\ns_add_u32 s63, s57, 65536\nv_rcp_f32 v235, v235\nM 64,164,248\nbuffer_load_dwordx4 v220, s[16:19], s63 offen lds\n"
"M 80,160,236\nR 182,190,36864\nv_mul_f32 v234, v20, v234\nM 80,164,252\nG 9\nv_rcp_f32 v236, v236\n"
"M 84,168,236\nR 160,191,24576\nv_mul_f32 v235, v21, v235\nM 84,172,252\nbuffer_load_dwordx4 v221, s[16:19], s63 offen offset:1024 lds\nM 68,168,232\n"
"R 162,191,28672\ns_add_u32 s63, s57, 589824\nM 68,172,248\ns_add_u32 m0, s50, 40960\nv_rcp_f32 v237, v237\nM 52,168,228\n"
"R 164,191,32768\nG 9\nv_mfma_f32_16x16x32_bf16 v[32:35], v[238:241], v[208:211], v[32:35]\nv_mul_f32 v236, v22, v236\nM 52,172,244\nbuffer_load_dwordx4 v220, s[16:19], s63 offen lds\n"
"M 36,168,224\nR 166,191,36864\nG 9\nv_mfma_f32_16x16x32_bf16 v[36:39], v[240:243], v[208:211], v[36:39]\nv_mul_f32 v237, v23, v237\nM 36,172,240\n"
"G 4\nv_cvt_pk_bf16_f32 v18, v234, v235\nv_cvt_pk_bf16_f32 v19, v236, v237\nM 40,176,224\nR 168,188,40960\nv_mfma_f32_16x16x32_bf16 v[40:43], v[242:245], v[208:211], v[40:43]\n"
"buffer_store_dwordx4 v[0:3], v252, s[20:23], s84 offen offset:0 sc1\nM 40,180,240\nbuffer_load_dwordx4 v221, s[16:19], s63 offen offset:1024 lds\nM 56,176,228\nR 170,188,45056\nv_mfma_f32_16x16x32_bf16 v[44:47], v[244:247], v[208:211], v[44:47]\n"
"s_add_u32 s63, s56, 131072\nM 56,180,244\ns_add_u32 m0, s50, 49152\nbuffer_store_dwordx4 v[16:19], v252, s[20:23], s85 offen offset:0 sc1\nM 72,176,232\nR 172,188,49152\n"
"v_mfma_f32_16x16x32_bf16 v[48:51], v[244:247], v[210:213], v[48:51]\nM 72,180,248\nbuffer_load_dwordx4 v220, s[16:19], s63 offen lds\nM 88,176,236\nR 174,188,53248\nv_mfma_f32_16x16x32_bf16 v[52:55], v[238:241], v[210:213], v[52:55]\n"
"M 88,180,252\nG 4\nM 92,160,236\nR 176,189,40960\nv_mfma_f32_16x16x32_bf16 v[56:59], v[240:243], v[210:213], v[56:59]\nM 92,164,252\n"
"buffer_load_dwordx4 v221, s[16:19], s63 offen offset:1024 lds\nM 76,160,232\nR 178,189,45056\nv_mfma_f32_16x16x32_bf16 v[60:63], v[242:245], v[210:213], v[60:63]\nv_mov_b32 v248, 0\nM 76,164,248\n"
"v_mov_b32 v249, 0\nM 60,160,228\nR 180,189,49152\ns_add_u32 s61, s82, 385032\ns_mov_b64 exec, 0xf\nbuffer_atomic_or_x2 v[248:249], v250, s[20:23], s61 offen sc0 sc1\n"
"s_mov_b64 exec, -1\nM 60,164,244\nM 44,160,224\nR 182,189,53248\nM 44,164,240\ns_barrier\n"
"G 4\nv_mfma_f32_16x16x32_bf16 v[96:99], v[168:171], a[0:3], 0\nR 160,190,40960\nM 96,172,16\ns_mov_b32 m0, s51\nv_mfma_f32_16x16x32_bf16 v[112:115], v[168:171], a[4:7], 0\n"
"R 162,190,45056\nM 112,172,20\nds_write_addtid_b32 v64 offset:0\nv_mfma_f32_16x16x32_bf16 v[128:131], v[168:171], a[8:11], 0\nR 164,190,49152\nM 128,172,24\n"
"ds_write_addtid_b32 v65 offset:256\nv_mfma_f32_16x16x32_bf16 v[144:147], v[168:171], a[12:15], 0\nR 166,190,53248\nds_write_addtid_b32 v66 offset:512\nM 144,172,28\nds_write_addtid_b32 v67 offset:768\n"
"ds_write_addtid_b32 v68 offset:1024\nG 9\nv_mfma_f32_16x16x32_bf16 v[148:151], v[176:179], a[12:15], 0\nR 168,191,40960\nds_write_addtid_b32 v69 offset:1280\nM 148,180,28\n"
"ds_write_addtid_b32 v70 offset:1536\nds_write_addtid_b32 v71 offset:1792\nv_mfma_f32_16x16x32_bf16 v[132:135], v[176:179], a[8:11], 0\nR 170,191,45056\nds_write_addtid_b32 v72 offset:2048\nM 132,180,24\n"
"ds_write_addtid_b32 v73 offset:2304\nds_write_addtid_b32 v74 offset:2560\nv_mfma_f32_16x16x32_bf16 v[116:119], v[176:179], a[4:7], 0\nR 172,191,49152\nds_write_addtid_b32 v75 offset:2816\nM 116,180,20\n"
"ds_write_addtid_b32 v76 offset:3072\nds_write_addtid_b32 v77 offset:3328\nv_mfma_f32_16x16x32_bf16 v[100:103], v[176:179], a[0:3], 0\nR 174,191,53248\nds_write_addtid_b32 v78 offset:3584\nM 100,180,16\n"
"ds_write_addtid_b32 v79 offset:3840\nds_write_addtid_b32 v80 offset:4096\nG 15\nv_mfma_f32_16x16x32_bf16 v[104:107], v[160:163], a[0:3], 0\nR 176,184,0\nds_write_addtid_b32 v81 offset:4352\n"
"M 104,164,16\nds_write_addtid_b32 v82 offset:4608\nds_write_addtid_b32 v83 offset:4864\nv_mfma_f32_16x16x32_bf16 v[120:123], v[160:163], a[4:7], 0\nR 178,184,4096\nds_write_addtid_b32 v84 offset:5120\n"
"M 120,164,20\nds_write_addtid_b32 v85 offset:5376\nds_write_addtid_b32 v86 offset:5632\nv_mfma_f32_16x16x32_bf16 v[136:139], v[160:163], a[8:11], 0\nR 180,184,8192\nds_write_addtid_b32 v87 offset:5888\n"
"M 136,164,24\nds_write_addtid_b32 v88 offset:6144\nds_write_addtid_b32 v89 offset:6400\nv_mfma_f32_16x16x32_bf16 v[152:155], v[160:163], a[12:15], 0\nR 182,184,12288\nds_write_addtid_b32 v90 offset:6656\n"
"M 152,164,28\nds_write_addtid_b32 v91 offset:6912\nds_write_addtid_b32 v92 offset:7168\nG 15\nv_mfma_f32_16x16x32_bf16 v[156:159], v[168:171], a[12:15], 0\nR 160,185,0\n"
"ds_write_addtid_b32 v93 offset:7424\nM 156,172,28\nds_write_addtid_b32 v94 offset:7680\nds_write_addtid_b32 v95 offset:7936\nv_mfma_f32_16x16x32_bf16 v[140:143], v[168:171], a[8:11], 0\nR 162,185,4096\n"
"M 140,172,24\nv_mfma_f32_16x16x32_bf16 v[124:127], v[168:171], a[4:7], 0\nR 164,185,8192\ns_waitcnt vmcnt(4) lgkmcnt(2)\ns_barrier\nM 124,172,20\n"
"s_add_u32 s64, s68, 23070720\nds_read2st64_b32 v[64:65], v225 offset0:0 offset1:1\nds_read2st64_b32 v[66:67], v225 offset0:2 offset1:3\nv_mfma_f32_16x16x32_bf16 v[108:111], v[168:171], a[0:3], 0\nR 166,185,12288\nds_read2st64_b32 v[68:69], v225 offset0:4 offset1:5\n"
"s_waitcnt vmcnt(0)\nv_cmp_eq_u64 s[86:87], s[46:47], v[248:249]\nM 108,172,16\ns_add_u32 m0, s50, 57344\ns_add_u32 s63, s56, 655360\nds_read2st64_b32 v[70:71], v225 offset0:6 offset1:7\n"
"M 96,176,32\nR 168,186,0\ns_and_b32 s62, s86, 0xf\ns_cmp_eq_u32 s62, 0xf\ns_cbranch_scc0 Lq402\nLq403:\n"
"ds_read2st64_b32 v[72:73], v225 offset0:8 offset1:9\nM 96,180,48\ns_add_u32 s65, s68, 23185408\nds_read2st64_b32 v[74:75], v225 offset0:10 offset1:11\nv_mov_b32 v226, s46\nM 112,176,36\n"
"R 170,186,4096\nds_read2st64_b32 v[76:77], v225 offset0:12 offset1:13\nv_mov_b32 v227, s47\nM 112,180,52\ns_add_u32 s61, s81, 385048\ns_lshl_b32 s62, s44, 5\n"
"s_add_u32 s61, s61, s62\ns_waitcnt vmcnt(0)\ns_mov_b64 exec, 1\nbuffer_atomic_swap_x2 v[226:227], off, s[20:23], s61 sc1\ns_mov_b64 exec, -1\nG 9\n"
"v_add_f32 v32, v32, v64\nM 128,176,40\nR 172,186,8192\nbuffer_load_dwordx4 v220, s[16:19], s63 offen lds\nv_add_f32 v33, v33, v65\nM 128,180,56\n"
"buffer_load_dwordx4 a[64:67], v252, s[20:23], s64 offen sc1\nG 9\nv_add_f32 v34, v34, v66\nM 144,176,44\nR 174,186,12288\nv_add_f32 v35, v35, v67\n"
"G 8\nv_add_f32 v36, v36, v68\nM 144,180,60\nds_read2st64_b32 v[78:79], v225 offset0:14 offset1:15\nv_add_f32 v37, v37, v69\nM 148,160,44\n"
"R 176,187,0\nbuffer_load_dwordx4 v221, s[16:19], s63 offen offset:1024 lds\nG 9\nv_add_f32 v38, v38, v70\nM 148,164,60\nbuffer_load_dwordx4 a[68:71], v252, s[20:23], s65 offen sc1\n"
"v_add_f32 v39, v39, v71\nM 132,160,40\nR 178,187,4096\ns_add_u32 s67, s69, 23185408\nG 8\nv_add_f32 v40, v40, v72\n"
"M 132,164,56\ns_add_u32 s66, s69, 23070720\ns_add_u32 m0, s50, 65536\ns_add_u32 s63, s56, 196608\nv_add_f32 v41, v41, v73\nM 116,160,36\n"
"R 180,187,8192\nbuffer_load_dwordx4 v220, s[16:19], s63 offen lds\nG 8\nv_add_f32 v42, v42, v74\nM 116,164,52\nbuffer_load_dwordx4 a[72:75], v252, s[20:23], s66 offen sc1\n"
"v_add_f32 v43, v43, v75\nM 100,160,32\nR 182,187,12288\nG 7\nv_add_f32 v44, v44, v76\nv_add_f32 v45, v45, v77\n"
"M 100,164,48\nds_read2st64_b32 v[80:81], v225 offset0:16 offset1:17\nG 6\nG 5\nv_add_f32 v46, v46, v78\nM 104,168,32\n"
"R 160,184,16384\nbuffer_load_dwordx4 v221, s[16:19], s63 offen offset:1024 lds\nv_add_f32 v47, v47, v79\nM 104,172,48\nbuffer_load_dwordx4 a[76:79], v252, s[20:23], s67 offen sc1\nds_read2st64_b32 v[82:83], v225 offset0:18 offset1:19\n"
"M 120,168,36\nR 162,184,20480\nds_read2st64_b32 v[84:85], v225 offset0:20 offset1:21\nM 120,172,52\ns_add_u32 s64, s68, 23077888\nds_read2st64_b32 v[86:87], v225 offset0:22 offset1:23\n"
"ds_read2st64_b32 v[88:89], v225 offset0:24 offset1:25\nM 136,168,40\nR 164,184,24576\nds_read2st64_b32 v[90:91], v225 offset0:26 offset1:27\nM 136,172,56\ns_add_u32 s65, s68, 23192576\n"
"ds_read2st64_b32 v[92:93], v225 offset0:28 offset1:29\nG 9\nv_add_f32 v48, v48, v80\nM 152,168,44\nR 166,184,28672\nds_read2st64_b32 v[94:95], v225 offset0:30 offset1:31\n"
"v_add_f32 v49, v49, v81\nM 152,172,60\nG 9\nv_add_f32 v50, v50, v82\nv_add_f32 v51, v51, v83\nM 156,176,44\n"
"R 168,185,16384\nG 8\nv_add_f32 v52, v52, v84\nv_add_f32 v53, v53, v85\nM 156,180,60\nbuffer_load_dwordx4 a[80:83], v252, s[20:23], s64 offen sc1\n"
"G 7\nv_add_f32 v54, v54, v86\nM 140,176,40\nR 170,185,20480\nv_add_f32 v55, v55, v87\nG 7\n"
"v_add_f32 v56, v56, v88\nM 140,180,56\nbuffer_load_dwordx4 a[84:87], v252, s[20:23], s65 offen sc1\nv_add_f32 v57, v57, v89\nM 124,176,36\nR 172,185,24576\n"
"G 6\nv_add_f32 v58, v58, v90\nv_add_f32 v59, v59, v91\nM 124,180,52\ns_add_u32 s66, s69, 23077888\nG 5\n"
"v_add_f32 v60, v60, v92\nv_add_f32 v61, v61, v93\nG 3\nv_add_f32 v62, v62, v94\nv_add_f32 v63, v63, v95\nM 108,176,32\n"
"R 174,185,28672\nv_mul_f32 v230, v32, v32\nv_mul_f32 v231, v33, v33\nM 108,180,48\ns_add_u32 s84, s73, 23072768\ns_add_u32 s85, s73, 23187456\n"
"v_mul_f32 v232, v34, v34\nv_mfma_f32_16x16x32_bf16 v[0:3], v[160:163], a[0:3], 0\nR 176,186,16384\nLq570:\ns_lshr_b32 s60, s45, 3\ns_lshl2_add_u32 s60, s60, s40\n"
"s_lshl_b32 s60, s60, 8\nv_lshl_add_u32 v224, v252, 3, s60\nv_add_u32 v224, Lq570+4096-LqPC, v224\ns_mov_b64 exec, 3\nglobal_load_dword v224, v224, s[98:99]\ns_mov_b64 exec, -1\n"
"v_mul_f32 v233, v35, v35\nM 0,164,16\nbuffer_load_dwordx4 a[88:91], v252, s[20:23], s66 offen sc1\nv_fma_f32 v230, v230, v215, v214\nv_mfma_f32_16x16x32_bf16 v[16:19], v[160:163], a[4:7], 0\nR 178,186,20480\n"
"v_fma_f32 v231, v231, v215, v214\nv_fma_f32 v232, v232, v215, v214\nM 16,164,20\ns_add_u32 s67, s69, 23192576\nv_fma_f32 v233, v233, v215, v214\nv_mul_f32 v230, v32, v230\n"
"v_mul_f32 v231, v33, v231\nv_mfma_f32_16x16x32_bf16 a[224:227], v[160:163], a[8:11], 0\nR 180,186,24576\nv_mul_f32 v232, v34, v232\nv_mul_f32 v233, v35, v233\nv_mfma_f32_16x16x32_bf16 a[224:227], v[164:167], a[24:27], a[224:227]\n"
"buffer_load_dwordx4 a[92:95], v252, s[20:23], s67 offen sc1\nv_exp_f32 v230, v230\nv_mfma_f32_16x16x32_bf16 a[240:243], v[160:163], a[12:15], 0\nR 182,186,28672\nv_fma_f32 v230, v230, v216, v216\nv_exp_f32 v231, v231\n"
"v_mfma_f32_16x16x32_bf16 a[240:243], v[164:167], a[28:31], a[240:243]\ns_add_u32 s63, s56, 720896\nG 4\nv_fma_f32 v231, v231, v216, v216\nv_exp_f32 v232, v232\nv_mfma_f32_16x16x32_bf16 a[244:247], v[168:171], a[12:15], 0\n"
"R 160,187,16384\ns_add_u32 m0, s50, 73728\nv_exp_f32 v233, v233\nv_mfma_f32_16x16x32_bf16 a[244:247], v[172:175], a[28:31], a[244:247]\ns_add_u32 s64, s68, 23071744\nv_rcp_f32 v230, v230\n"
"v_fma_f32 v232, v232, v216, v216\nv_mfma_f32_16x16x32_bf16 a[228:231], v[168:171], a[8:11], 0\nR 162,187,20480\nv_mov_b32 v248, 0\nv_fma_f32 v233, v233, v216, v216\nv_mfma_f32_16x16x32_bf16 a[228:231], v[172:175], a[24:27], a[228:231]\n"
"buffer_load_dwordx4 a[96:99], v252, s[20:23], s64 offen sc1\nv_mov_b32 v249, 0\nv_rcp_f32 v231, v231\nv_mfma_f32_16x16x32_bf16 v[20:23], v[168:171], a[4:7], 0\nR 164,187,24576\ns_add_u32 s61, s82, 385048\n"
"s_mov_b64 exec, 0xf\nbuffer_atomic_or_x2 v[248:249], v250, s[20:23], s61 offen sc0 sc1\ns_mov_b64 exec, -1\nv_rcp_f32 v232, v232\nM 20,172,20\ns_add_u32 s65, s68, 23186432\n"
"buffer_load_dwordx4 v220, s[16:19], s63 offen lds\nv_rcp_f32 v233, v233\nv_mfma_f32_16x16x32_bf16 v[4:7], v[168:171], a[0:3], 0\nR 166,187,28672\nv_mul_f32 v230, v32, v230\nv_mul_f32 v231, v33, v231\n"
"M 4,172,16\nG 4\nv_mul_f32 v232, v34, v232\nv_mul_f32 v233, v35, v233\nv_cvt_pk_bf16_f32 v32, v230, v231\nv_mfma_f32_16x16x32_bf16 v[8:11], v[176:179], a[0:3], 0\n"
"R 168,184,32768\nv_cvt_pk_bf16_f32 v33, v232, v233\nv_mul_f32 v234, v36, v36\nM 8,180,16\nbuffer_load_dwordx4 a[100:103], v252, s[20:23], s65 offen sc1\nbuffer_load_dwordx4 v221, s[16:19], s63 offen offset:1024 lds\n"
"v_mul_f32 v235, v37, v37\nv_mfma_f32_16x16x32_bf16 v[24:27], v[176:179], a[4:7], 0\nR 170,184,36864\ns_add_u32 s67, s69, 23186432\nv_mul_f32 v236, v38, v38\nM 24,180,20\n"
"s_add_u32 s66, s69, 23071744\ns_add_u32 s63, s56, 393216\nv_mul_f32 v237, v39, v39\nv_fma_f32 v234, v234, v215, v214\nv_mfma_f32_16x16x32_bf16 a[232:235], v[176:179], a[8:11], 0\nR 172,184,40960\n"
"s_add_u32 m0, s50, 81920\nv_fma_f32 v235, v235, v215, v214\nv_mfma_f32_16x16x32_bf16 a[232:235], v[180:183], a[24:27], a[232:235]\nbuffer_load_dwordx4 a[104:107], v252, s[20:23], s66 offen sc1\nbuffer_load_dwordx4 v220, s[16:19], s63 offen lds\nv_fma_f32 v236, v236, v215, v214\n"
"v_mfma_f32_16x16x32_bf16 a[248:251], v[176:179], a[12:15], 0\nR 174,184,45056\nv_fma_f32 v237, v237, v215, v214\nv_mul_f32 v234, v36, v234\nv_mfma_f32_16x16x32_bf16 a[248:251], v[180:183], a[28:31], a[248:251]\nG 4\n"
"v_exp_f32 v234, v234\nv_mul_f32 v235, v37, v235\nv_mfma_f32_16x16x32_bf16 a[252:255], v[160:163], a[12:15], 0\nR 176,185,32768\nv_mul_f32 v236, v38, v236\nv_mul_f32 v237, v39, v237\n"
"v_mfma_f32_16x16x32_bf16 a[252:255], v[164:167], a[28:31], a[252:255]\nbuffer_load_dwordx4 a[108:111], v252, s[20:23], s67 offen sc1\nbuffer_load_dwordx4 v221, s[16:19], s63 offen offset:1024 lds\nv_exp_f32 v235, v235\nv_mfma_f32_16x16x32_bf16 a[236:239], v[160:163], a[8:11], 0\nR 178,185,36864\n"
"s_add_u32 m0, s50, 90112\nv_exp_f32 v236, v236\nv_mfma_f32_16x16x32_bf16 a[236:239], v[164:167], a[24:27], a[236:239]\ns_add_u32 s64, s68, 23078912\nv_exp_f32 v237, v237\nv_fma_f32 v234, v234, v216, v216\n"
"v_mfma_f32_16x16x32_bf16 v[28:31], v[160:163], a[4:7], 0\nR 180,185,40960\ns_add_u32 s63, s56, 458752\nv_fma_f32 v235, v235, v216, v216\nM 28,164,20\ns_add_u32 s65, s68, 23193600\n"
"buffer_load_dwordx4 v220, s[16:19], s63 offen lds\nv_fma_f32 v236, v236, v216, v216\nv_mfma_f32_16x16x32_bf16 v[12:15], v[160:163], a[0:3], 0\nR 182,185,45056\nv_fma_f32 v237, v237, v216, v216\nv_rcp_f32 v234, v234\n"
"M 12,164,16\ns_waitcnt vmcnt(16)\ns_barrier\nG 4\nv_rcp_f32 v235, v235\nM 0,168,32\n"
"R 160,186,32768\nv_mul_f32 v234, v36, v234\nv_rcp_f32 v236, v236\nM 0,172,48\nbuffer_load_dwordx4 a[112:115], v252, s[20:23], s64 offen sc1\nbuffer_load_dwordx4 v221, s[16:19], s63 offen offset:1024 lds\n"
"v_rcp_f32 v237, v237\nM 16,168,36\nR 162,186,36864\nv_mul_f32 v235, v37, v235\nv_mul_f32 v236, v38, v236\nM 16,172,52\n"
"buffer_load_dwordx4 a[116:119], v252, s[20:23], s65 offen sc1\nv_mul_f32 v237, v39, v237\nv_mfma_f32_16x16x32_bf16 a[224:227], v[168:171], a[40:43], a[224:227]\nR 164,186,40960\nv_cvt_pk_bf16_f32 v34, v234, v235\nv_cvt_pk_bf16_f32 v35, v236, v237\n"
"v_mfma_f32_16x16x32_bf16 a[224:227], v[172:175], a[56:59], a[224:227]\ns_add_u32 s66, s69, 23078912\nv_mul_f32 v230, v40, v40\nv_mul_f32 v231, v41, v41\nv_mul_f32 v232, v42, v42\nv_mfma_f32_16x16x32_bf16 a[240:243], v[168:171], a[44:47], a[240:243]\n"
"R 166,186,45056\nv_mul_f32 v233, v43, v43\nv_fma_f32 v230, v230, v215, v214\nv_mfma_f32_16x16x32_bf16 a[240:243], v[172:175], a[60:63], a[240:243]\nG 4\nv_fma_f32 v231, v231, v215, v214\n"
"v_fma_f32 v232, v232, v215, v214\nv_fma_f32 v233, v233, v215, v214\nv_mfma_f32_16x16x32_bf16 a[244:247], v[176:179], a[44:47], a[244:247]\nR 168,187,32768\nv_mul_f32 v230, v40, v230\nv_mul_f32 v231, v41, v231\n"
"v_mfma_f32_16x16x32_bf16 a[244:247], v[180:183], a[60:63], a[244:247]\nbuffer_load_dwordx4 a[120:123], v252, s[20:23], s66 offen sc1\nv_mul_f32 v232, v42, v232\nv_mfma_f32_16x16x32_bf16 a[228:231], v[176:179], a[40:43], a[228:231]\nR 170,187,36864\nv_mul_f32 v233, v43, v233\n"
"v_exp_f32 v230, v230\nv_mfma_f32_16x16x32_bf16 a[228:231], v[180:183], a[56:59], a[228:231]\ns_add_u32 s67, s69, 23193600\nv_exp_f32 v231, v231\nv_exp_f32 v232, v232\nM 20,176,36\n"
"R 172,187,40960\nv_fma_f32 v230, v230, v216, v216\nv_exp_f32 v233, v233\nM 20,180,52\nbuffer_load_dwordx4 a[124:127], v252, s[20:23], s67 offen sc1\nv_fma_f32 v231, v231, v216, v216\n"
"M 4,176,32\nR 174,187,45056\ns_waitcnt vmcnt(13)\nv_cmp_eq_u64 s[86:87], s[46:47], v[248:249]\nv_fma_f32 v232, v232, v216, v216\nM 4,180,48\n"
"s_add_u32 s63, s57, 131072\nG 4\nv_fma_f32 v233, v233, v216, v216\nv_rcp_f32 v230, v230\nM 8,160,32\nR 176,184,49152\n"
"s_and_b32 s62, s86, 0xf\ns_cmp_eq_u32 s62, 0xf\ns_cbranch_scc0 Lq437\nLq438:\nv_rcp_f32 v231, v231\nM 8,164,48\n"
"s_add_u32 s64, s68, 23074816\nv_rcp_f32 v232, v232\nv_rcp_f32 v233, v233\nM 24,160,36\nR 178,184,53248\ns_add_u32 m0, s50, 98304\n"
"v_mul_f32 v230, v40, v230\nM 24,164,52\nbuffer_load_dwordx4 a[128:131], v252, s[20:23], s64 offen sc1\nbuffer_load_dwordx4 v220, s[16:19], s63 offen lds\nv_mul_f32 v231, v41, v231\nv_mfma_f32_16x16x32_bf16 a[232:235], v[160:163], a[40:43], a[232:235]\n"
"R 180,184,57344\ns_add_u32 s66, s69, 23074816\nv_mul_f32 v232, v42, v232\nv_mfma_f32_16x16x32_bf16 a[232:235], v[164:167], a[56:59], a[232:235]\ns_add_u32 s65, s68, 23189504\nv_mul_f32 v233, v43, v233\n"
"v_cvt_pk_bf16_f32 v40, v230, v231\nv_cvt_pk_bf16_f32 v41, v232, v233\nv_mfma_f32_16x16x32_bf16 a[248:251], v[160:163], a[44:47], a[248:251]\nR 182,184,61440\nv_mul_f32 v234, v44, v44\nv_mul_f32 v235, v45, v45\n"
"v_mfma_f32_16x16x32_bf16 a[248:251], v[164:167], a[60:63], a[248:251]\nbuffer_load_dwordx4 v221, s[16:19], s63 offen offset:1024 lds\nG 4\nv_mul_f32 v236, v46, v46\nv_mfma_f32_16x16x32_bf16 a[252:255], v[168:171], a[44:47], a[252:255]\nR 160,185,49152\n"
"s_add_u32 m0, s50, 106496\nv_mul_f32 v237, v47, v47\nv_mfma_f32_16x16x32_bf16 a[252:255], v[172:175], a[60:63], a[252:255]\nbuffer_load_dwordx4 a[132:135], v252, s[20:23], s65 offen sc1\nv_fma_f32 v234, v234, v215, v214\nv_mfma_f32_16x16x32_bf16 a[236:239], v[168:171], a[40:43], a[236:239]\n"
"R 162,185,53248\ns_add_u32 s63, s57, 655360\nv_fma_f32 v235, v235, v215, v214\nv_mfma_f32_16x16x32_bf16 a[236:239], v[172:175], a[56:59], a[236:239]\nbuffer_load_dwordx4 a[136:139], v252, s[20:23], s66 offen sc1\nbuffer_load_dwordx4 v220, s[16:19], s63 offen lds\n"
"v_fma_f32 v236, v236, v215, v214\nM 28,168,36\nR 164,185,57344\ns_add_u32 s64, s68, 23081984\nv_fma_f32 v237, v237, v215, v214\nM 28,172,52\n"
"s_add_u32 s67, s69, 23189504\nv_mul_f32 v234, v44, v234\nv_mul_f32 v235, v45, v235\nv_mul_f32 v236, v46, v236\nM 12,168,32\nR 166,185,61440\n"
"v_mul_f32 v237, v47, v237\nv_exp_f32 v234, v234\nM 12,172,48\nbuffer_load_dwordx4 v221, s[16:19], s63 offen offset:1024 lds\nG 4\nv_exp_f32 v235, v235\n"
"M 96,176,64\nR 168,186,49152\ns_add_u32 m0, s50, 114688\nv_exp_f32 v236, v236\nM 96,180,80\nbuffer_load_dwordx4 a[140:143], v252, s[20:23], s67 offen sc1\n"
"v_exp_f32 v237, v237\nM 112,176,68\nR 170,186,53248\ns_add_u32 s63, s57, 196608\nv_fma_f32 v234, v234, v216, v216\nM 112,180,84\n"
"buffer_load_dwordx4 a[144:147], v252, s[20:23], s64 offen sc1\nbuffer_load_dwordx4 v220, s[16:19], s63 offen lds\nv_fma_f32 v235, v235, v216, v216\nM 128,176,72\nR 172,186,57344\ns_add_u32 s66, s69, 23081984\n"
"v_fma_f32 v236, v236, v216, v216\nM 128,180,88\ns_add_u32 s65, s68, 23196672\nv_rcp_f32 v234, v234\nv_fma_f32 v237, v237, v216, v216\nM 144,176,76\n"
"R 174,186,61440\nv_mul_f32 v234, v44, v234\nv_rcp_f32 v235, v235\nM 144,180,92\nbuffer_load_dwordx4 v221, s[16:19], s63 offen offset:1024 lds\nG 4\n"
"v_rcp_f32 v236, v236\nM 148,160,76\nR 176,187,49152\ns_add_u32 m0, s50, 0\nv_rcp_f32 v237, v237\nM 148,164,92\n"
"buffer_load_dwordx4 a[148:151], v252, s[20:23], s65 offen sc1\nv_mul_f32 v235, v45, v235\nM 132,160,72\nR 178,187,53248\ns_add_u32 s63, s57, 720896\nv_mul_f32 v236, v46, v236\n"
"M 132,164,88\nbuffer_load_dwordx4 a[152:155], v252, s[20:23], s66 offen sc1\nbuffer_load_dwordx4 v220, s[16:19], s63 offen lds\nv_mul_f32 v237, v47, v237\nM 116,160,68\nR 180,187,57344\n"
"v_cvt_pk_bf16_f32 v42, v234, v235\nv_cvt_pk_bf16_f32 v43, v236, v237\nM 116,164,84\ns_add_u32 s67, s69, 23196672\nv_mul_f32 v230, v48, v48\nv_mul_f32 v231, v49, v49\n"
"v_mul_f32 v232, v50, v50\nM 100,160,64\nR 182,187,61440\nv_mul_f32 v233, v51, v51\nv_fma_f32 v230, v230, v218, v217\nM 100,164,80\n"
"buffer_load_dwordx4 v221, s[16:19], s63 offen offset:1024 lds\nG 4\nv_fma_f32 v231, v231, v218, v217\nM 104,168,64\nR 160,188,0\ns_waitcnt vmcnt(18)\n"
"s_barrier\nv_fma_f32 v232, v232, v218, v217\nM 104,172,80\nbuffer_load_dwordx4 a[156:159], v252, s[20:23], s67 offen sc1\nv_fma_f32 v233, v233, v218, v217\nM 120,168,68\n"
"R 162,188,4096\nv_mul_f32 v230, v48, v230\nv_mul_f32 v231, v49, v231\nM 120,172,84\nv_exp_f32 v230, v230\nv_mul_f32 v232, v50, v232\n"
"M 136,168,72\nR 164,188,8192\nv_mul_f32 v233, v51, v233\nv_exp_f32 v231, v231\nM 136,172,88\nbuffer_store_dwordx4 v[32:35], v252, s[20:23], s84 offen offset:0 sc1\n"
"v_exp_f32 v232, v232\nM 152,168,76\nR 166,188,12288\nv_fma_f32 v230, v230, v219, v219\nv_exp_f32 v233, v233\nM 152,172,92\n"
"G 4\nv_rcp_f32 v230, v230\nv_fma_f32 v231, v231, v219, v219\nM 156,176,76\nR 168,189,0\nv_fma_f32 v232, v232, v219, v219\n"
"v_fma_f32 v233, v233, v219, v219\nM 156,180,92\ns_add_u32 m0, s50, 8192\nv_rcp_f32 v231, v231\nv_rcp_f32 v232, v232\nM 140,176,72\n"
"R 170,189,4096\nv_mul_f32 v230, v48, v230\nv_rcp_f32 v233, v233\nM 140,180,88\ns_add_u32 s63, s57, 393216\nv_mul_f32 v231, v49, v231\n"
"v_mul_f32 v232, v50, v232\nv_mul_f32 v233, v51, v233\nM 124,176,68\nR 172,189,8192\nv_cvt_pk_bf16_f32 v48, v230, v231\nv_cvt_pk_bf16_f32 v49, v232, v233\n"
"M 124,180,84\nbuffer_load_dwordx4 v220, s[16:19], s63 offen lds\nv_mul_f32 v234, v52, v52\nM 108,176,64\nR 174,189,12288\nv_mul_f32 v235, v53, v53\n"
"v_mul_f32 v236, v54, v54\nM 108,180,80\nG 4\nv_mul_f32 v237, v55, v55\nv_fma_f32 v234, v234, v218, v217\nv_fma_f32 v235, v235, v218, v217\n"
"M 96,160,96\nR 176,190,0\nv_fma_f32 v236, v236, v218, v217\nv_fma_f32 v237, v237, v218, v217\nM 96,164,112\nbuffer_load_dwordx4 v221, s[16:19], s63 offen offset:1024 lds\n"
"v_mul_f32 v234, v52, v234\nM 112,160,100\nR 178,190,4096\ns_add_u32 s63, s57, 458752\nv_mul_f32 v235, v53, v235\ns_waitcnt vmcnt(21)\n"
"M 112,164,116\ns_add_u32 m0, s50, 16384\nv_exp_f32 v234, v234\nv_mul_f32 v236, v54, v236\nM 128,160,104\nR 180,190,8192\n"
"v_mul_f32 v237, v55, v237\nv_exp_f32 v235, v235\ns_waitcnt vmcnt(20)\nM 128,164,120\nbuffer_load_dwordx4 v220, s[16:19], s63 offen lds\nv_exp_f32 v236, v236\n"
"M 144,160,108\nR 182,190,12288\nv_fma_f32 v234, v234, v219, v219\nv_exp_f32 v237, v237\ns_waitcnt vmcnt(20)\nM 144,164,124\n"
"G 4\nv_rcp_f32 v234, v234\nv_fma_f32 v235, v235, v219, v219\nM 148,168,108\nR 160,191,0\nv_fma_f32 v236, v236, v219, v219\n"
"v_fma_f32 v237, v237, v219, v219\nM 148,172,124\nv_rcp_f32 v235, v235\nv_rcp_f32 v236, v236\nM 132,168,104\nR 162,191,4096\n"
"v_mul_f32 v234, v52, v234\nv_rcp_f32 v237, v237\nM 132,172,120\nbuffer_load_dwordx4 v221, s[16:19], s63 offen offset:1024 lds\nM 116,168,100\nR 164,191,8192\n"
"s_add_u32 s63, s58, 0\nM 116,172,116\ns_add_u32 m0, s50, 24576\nv_mul_f32 v235, v53, v235\nv_mul_f32 v236, v54, v236\nM 100,168,96\n"
"R 166,191,12288\nv_mul_f32 v237, v55, v237\nM 100,172,112\nG 4\nv_cvt_pk_bf16_f32 v50, v234, v235\nv_cvt_pk_bf16_f32 v51, v236, v237\n"
"M 104,176,96\nR 168,188,16384\nv_mul_f32 v230, v56, v56\nM 104,180,112\nbuffer_load_dwordx4 v220, s[16:19], s63 offen lds\nM 120,176,100\n"
"R 170,188,20480\nv_mul_f32 v231, v57, v57\nM 120,180,116\nv_mul_f32 v232, v58, v58\nv_mul_f32 v233, v59, v59\nM 136,176,104\n"
"R 172,188,24576\nv_fma_f32 v230, v230, v218, v217\nM 136,180,120\nbuffer_load_dwordx4 v221, s[16:19], s63 offen offset:1024 lds\nM 152,176,108\nR 174,188,28672\n"
"s_add_u32 m0, s50, 32768\nM 152,180,124\nG 4\nv_fma_f32 v231, v231, v218, v217\nv_fma_f32 v232, v232, v218, v217\nM 156,160,108\n"
"R 176,189,16384\ns_add_u32 s63, s58, 524288\nM 156,164,124\nbuffer_load_dwordx4 v220, s[16:19], s63 offen lds\nM 140,160,104\nR 178,189,20480\n"
"v_fma_f32 v233, v233, v218, v217\nM 140,164,120\nv_mul_f32 v230, v56, v230\nv_mul_f32 v231, v57, v231\nM 124,160,100\nR 180,189,24576\n"
"v_mul_f32 v232, v58, v232\nM 124,164,116\nbuffer_load_dwordx4 v221, s[16:19], s63 offen offset:1024 lds\nM 108,160,96\nR 182,189,28672\ns_add_u32 s63, s58, 65536\n"
"M 108,164,112\ns_waitcnt vmcnt(10)\ns_barrier\nG 4\nM 96,168,128\nR 160,190,16384\n"
"v_mul_f32 v233, v59, v233\nM 96,172,144\ns_add_u32 m0, s50, 40960\nv_exp_f32 v230, v230\nM 112,168,132\nR 162,190,20480\n"
"v_fma_f32 v230, v230, v219, v219\nM 112,172,148\nbuffer_load_dwordx4 v220, s[16:19], s63 offen lds\nM 128,168,136\nR 164,190,24576\nv_mul_f32 v234, v60, v60\n"
"M 128,172,152\nv_exp_f32 v231, v231\nM 144,168,140\nR 166,190,28672\nv_fma_f32 v231, v231, v219, v219\ns_waitcnt vmcnt(10)\n"
"M 144,172,156\nG 4\nv_exp_f32 v232, v232\nM 148,176,140\nR 168,191,16384\nv_fma_f32 v232, v232, v219, v219\n"
"M 148,180,156\nbuffer_load_dwordx4 v221, s[16:19], s63 offen offset:1024 lds\nM 132,176,136\nR 170,191,20480\nv_mul_f32 v235, v61, v61\nM 132,180,152\n"
"s_add_u32 m0, s50, 49152\nv_exp_f32 v233, v233\nM 116,176,132\nR 172,191,24576\nv_fma_f32 v233, v233, v219, v219\nM 116,180,148\n"
"s_add_u32 s63, s58, 589824\nv_rcp_f32 v230, v230\nM 100,176,128\nR 174,191,28672\nv_mul_f32 v230, v56, v230\nM 100,180,144\n"
"G 4\nv_rcp_f32 v231, v231\nM 104,160,128\nR 176,188,32768\nv_mul_f32 v231, v57, v231\nM 104,164,144\n"
"buffer_load_dwordx4 v220, s[16:19], s63 offen lds\nM 120,160,132\nR 178,188,36864\nv_cvt_pk_bf16_f32 v56, v230, v231\nM 120,164,148\nv_rcp_f32 v232, v232\n"
"M 136,160,136\nR 180,188,40960\nv_mul_f32 v232, v58, v232\nM 136,164,152\nbuffer_store_dwordx4 v[40:43], v252, s[20:23], s84 offen offset:1024 sc1\nM 152,160,140\n"
"R 182,188,45056\nv_mul_f32 v236, v62, v62\nM 152,164,156\nG 4\nv_rcp_f32 v233, v233\nM 156,168,140\n"
"R 160,189,32768\nv_mul_f32 v233, v59, v233\nM 156,172,156\nbuffer_load_dwordx4 v221, s[16:19], s63 offen offset:1024 lds\nM 140,168,136\nR 162,189,36864\n"
"v_cvt_pk_bf16_f32 v57, v232, v233\nM 140,172,152\ns_add_u32 m0, s50, 57344\nv_mul_f32 v237, v63, v63\nv_fma_f32 v234, v234, v218, v217\nM 124,168,132\n"
"R 164,189,40960\nv_fma_f32 v235, v235, v218, v217\nM 124,172,148\ns_add_u32 s63, s58, 131072\nv_fma_f32 v236, v236, v218, v217\nv_fma_f32 v237, v237, v218, v217\n"
"M 108,168,128\nR 166,189,45056\nv_mul_f32 v234, v60, v234\nM 108,172,144\nG 4\nv_exp_f32 v234, v234\n"
"M 0,176,64\nR 168,190,32768\nv_mul_f32 v235, v61, v235\nM 0,180,80\nbuffer_load_dwordx4 v220, s[16:19], s63 offen lds\nM 16,176,68\n"
"R 170,190,36864\nv_mul_f32 v236, v62, v236\nM 16,180,84\nv_exp_f32 v235, v235\nv_mfma_f32_16x16x32_bf16 a[224:227], v[176:179], a[72:75], a[224:227]\nR 172,190,40960\n"
"v_mul_f32 v237, v63, v237\nv_mfma_f32_16x16x32_bf16 a[224:227], v[180:183], a[88:91], a[224:227]\nbuffer_store_dwordx4 v[48:51], v252, s[20:23], s85 offen offset:0 sc1\nv_mfma_f32_16x16x32_bf16 a[240:243], v[176:179], a[76:79], a[240:243]\nR 174,190,45056\nv_fma_f32 v234, v234, v219, v219\n"
"v_mfma_f32_16x16x32_bf16 a[240:243], v[180:183], a[92:95], a[240:243]\nG 4\nv_exp_f32 v236, v236\nv_mfma_f32_16x16x32_bf16 a[244:247], v[160:163], a[76:79], a[244:247]\nR 176,191,32768\nv_fma_f32 v235, v235, v219, v219\n"
"v_mfma_f32_16x16x32_bf16 a[244:247], v[164:167], a[92:95], a[244:247]\nbuffer_load_dwordx4 v221, s[16:19], s63 offen offset:1024 lds\nv_mfma_f32_16x16x32_bf16 a[228:231], v[160:163], a[72:75], a[228:231]\nR 178,191,36864\nv_fma_f32 v236, v236, v219, v219\nv_mfma_f32_16x16x32_bf16 a[228:231], v[164:167], a[88:91], a[228:231]\n"
"s_add_u32 m0, s50, 65536\nv_exp_f32 v237, v237\nM 20,160,68\nR 180,191,40960\nv_fma_f32 v237, v237, v219, v219\nM 20,164,84\n"
"s_add_u32 s63, s58, 655360\nv_rcp_f32 v234, v234\nM 4,160,64\nR 182,191,45056\nv_mul_f32 v234, v60, v234\nM 4,164,80\n"
"G 4\nv_rcp_f32 v235, v235\nM 8,168,64\nR 160,188,49152\nv_mul_f32 v235, v61, v235\nM 8,172,80\n"
"buffer_load_dwordx4 v220, s[16:19], s63 offen lds\nM 24,168,68\nR 162,188,53248\nv_cvt_pk_bf16_f32 v58, v234, v235\nM 24,172,84\nv_rcp_f32 v236, v236\n"
"v_mfma_f32_16x16x32_bf16 a[232:235], v[168:171], a[72:75], a[232:235]\nR 164,184,0\nv_mul_f32 v236, v62, v236\nv_mfma_f32_16x16x32_bf16 a[232:235], v[172:175], a[88:91], a[232:235]\nbuffer_load_dwordx4 v221, s[16:19], s63 offen offset:1024 lds\nv_mfma_f32_16x16x32_bf16 a[248:251], v[168:171], a[76:79], a[248:251]\n"
"R 166,184,4096\ns_add_u32 m0, s50, 73728\nv_mfma_f32_16x16x32_bf16 a[248:251], v[172:175], a[92:95], a[248:251]\nG 4\nv_rcp_f32 v237, v237\nv_mfma_f32_16x16x32_bf16 a[252:255], v[176:179], a[76:79], a[252:255]\n"
"R 168,189,49152\ns_add_u32 s63, s58, 196608\nv_mfma_f32_16x16x32_bf16 a[252:255], v[180:183], a[92:95], a[252:255]\nbuffer_load_dwordx4 v220, s[16:19], s63 offen lds\nv_mfma_f32_16x16x32_bf16 a[236:239], v[176:179], a[72:75], a[236:239]\nR 170,189,53248\n"
"v_mul_f32 v237, v63, v237\nv_mfma_f32_16x16x32_bf16 a[236:239], v[180:183], a[88:91], a[236:239]\nv_cvt_pk_bf16_f32 v59, v236, v237\nbuffer_store_dwordx4 v[56:59], v252, s[20:23], s85 offen offset:1024 sc1\nM 28,176,68\nR 172,185,0\n"
"M 28,180,84\nbuffer_load_dwordx4 v221, s[16:19], s63 offen offset:1024 lds\nM 12,176,64\nR 174,185,4096\nM 12,180,80\ns_waitcnt vmcnt(13)\n"
"s_barrier\nG 4\nM 0,160,96\nR 176,190,49152\nM 0,164,112\nM 16,160,100\n"
"R 178,190,53248\nM 16,164,116\nv_mfma_f32_16x16x32_bf16 a[224:227], v[160:163], a[104:107], a[224:227]\nR 180,186,0\nv_mfma_f32_16x16x32_bf16 a[224:227], v[164:167], a[120:123], a[224:227]\nv_mfma_f32_16x16x32_bf16 a[240:243], v[160:163], a[108:111], a[240:243]\n"
"R 182,186,4096\nv_mfma_f32_16x16x32_bf16 a[240:243], v[164:167], a[124:127], a[240:243]\nG 4\nv_mfma_f32_16x16x32_bf16 a[244:247], v[168:171], a[108:111], a[244:247]\nR 160,191,49152\nv_mfma_f32_16x16x32_bf16 a[244:247], v[172:175], a[124:127], a[244:247]\n"
"v_mfma_f32_16x16x32_bf16 a[228:231], v[168:171], a[104:107], a[228:231]\nR 162,191,53248\nv_mfma_f32_16x16x32_bf16 a[228:231], v[172:175], a[120:123], a[228:231]\nM 20,168,100\nR 164,187,0\nM 20,172,116\n"
"M 4,168,96\nR 166,187,4096\nM 4,172,112\nG 4\nM 8,176,96\nR 168,184,8192\n"
"M 8,180,112\nM 24,176,100\nR 170,184,12288\nM 24,180,116\nv_mfma_f32_16x16x32_bf16 a[232:235], v[176:179], a[104:107], a[232:235]\nR 172,184,16384\n"
"v_mfma_f32_16x16x32_bf16 a[232:235], v[180:183], a[120:123], a[232:235]\nv_mfma_f32_16x16x32_bf16 a[248:251], v[176:179], a[108:111], a[248:251]\nR 174,184,20480\nv_mfma_f32_16x16x32_bf16 a[248:251], v[180:183], a[124:127], a[248:251]\nG 4\nv_mfma_f32_16x16x32_bf16 a[252:255], v[160:163], a[108:111], a[252:255]\n"
"R 176,185,8192\nv_mfma_f32_16x16x32_bf16 a[252:255], v[164:167], a[124:127], a[252:255]\nv_mfma_f32_16x16x32_bf16 a[236:239], v[160:163], a[104:107], a[236:239]\nR 178,185,12288\nv_mfma_f32_16x16x32_bf16 a[236:239], v[164:167], a[120:123], a[236:239]\nM 28,160,100\n"
"R 180,185,16384\nM 28,164,116\nM 12,160,96\nR 182,185,20480\nM 12,164,112\nG 4\n"
"M 0,168,128\nR 160,186,8192\nM 0,172,144\nM 16,168,132\nR 162,186,12288\nM 16,172,148\n"
"v_mfma_f32_16x16x32_bf16 a[224:227], v[168:171], a[136:139], a[224:227]\nR 164,186,16384\nv_mfma_f32_16x16x32_bf16 a[224:227], v[172:175], a[152:155], a[224:227]\nv_mfma_f32_16x16x32_bf16 a[240:243], v[168:171], a[140:143], a[240:243]\nR 166,186,20480\nv_mfma_f32_16x16x32_bf16 a[240:243], v[172:175], a[156:159], a[240:243]\n"
"G 4\nv_mfma_f32_16x16x32_bf16 a[244:247], v[176:179], a[140:143], a[244:247]\nR 168,187,8192\nv_mfma_f32_16x16x32_bf16 a[244:247], v[180:183], a[156:159], a[244:247]\nv_mfma_f32_16x16x32_bf16 a[228:231], v[176:179], a[136:139], a[228:231]\nR 170,187,12288\n"
"v_mov_b32 v226, s46\nv_mfma_f32_16x16x32_bf16 a[228:231], v[180:183], a[152:155], a[228:231]\ns_add_u32 m0, s50, 81920\nv_mov_b32 v227, s47\nM 20,176,132\nR 172,187,16384\n"
"s_add_u32 s61, s81, 385040\ns_lshl_b32 s62, s44, 5\ns_add_u32 s61, s61, s62\ns_waitcnt vmcnt(0)\ns_mov_b64 exec, 1\nbuffer_atomic_swap_x2 v[226:227], off, s[20:23], s61 sc1\n"
"s_mov_b64 exec, -1\nM 20,180,148\ns_add_u32 s63, s58, 720896\nM 4,176,128\nR 174,187,20480\nM 4,180,144\n"
"G 4\nM 8,160,128\nR 176,184,24576\nM 8,164,144\nbuffer_load_dwordx4 v220, s[16:19], s63 offen lds\nM 24,160,132\n"
"R 178,184,28672\nM 24,164,148\nv_mfma_f32_16x16x32_bf16 a[232:235], v[160:163], a[136:139], a[232:235]\nR 180,184,32768\nv_mfma_f32_16x16x32_bf16 a[232:235], v[164:167], a[152:155], a[232:235]\nbuffer_load_dwordx4 v221, s[16:19], s63 offen offset:1024 lds\n"
"v_mfma_f32_16x16x32_bf16 a[248:251], v[160:163], a[140:143], a[248:251]\nR 182,184,36864\ns_add_u32 m0, s50, 90112\nv_mfma_f32_16x16x32_bf16 a[248:251], v[164:167], a[156:159], a[248:251]\nG 4\nv_mfma_f32_16x16x32_bf16 a[252:255], v[168:171], a[140:143], a[252:255]\n"
"R 160,185,24576\ns_add_u32 s63, s58, 393216\nv_mfma_f32_16x16x32_bf16 a[252:255], v[172:175], a[156:159], a[252:255]\nbuffer_load_dwordx4 v220, s[16:19], s63 offen lds\nv_mfma_f32_16x16x32_bf16 a[236:239], v[168:171], a[136:139], a[236:239]\nR 162,185,28672\n"
"v_mfma_f32_16x16x32_bf16 a[236:239], v[172:175], a[152:155], a[236:239]\nM 28,168,132\nR 164,185,32768\nM 28,172,148\nbuffer_load_dwordx4 v221, s[16:19], s63 offen offset:1024 lds\nM 12,168,128\n"
"R 166,185,36864\ns_add_u32 s63, s58, 458752\nM 12,172,144\ns_barrier\nG 4\nv_mfma_f32_16x16x32_bf16 v[32:35], v[176:179], a[0:3], 0\n"
"R 168,186,24576\nM 32,180,16\ns_add_u32 m0, s50, 98304\nv_mfma_f32_16x16x32_bf16 v[48:51], v[176:179], a[4:7], 0\nR 170,186,28672\nM 48,180,20\n"
"buffer_load_dwordx4 v220, s[16:19], s63 offen lds\nv_mfma_f32_16x16x32_bf16 v[64:67], v[176:179], a[8:11], 0\nR 172,186,32768\nM 64,180,24\nv_mfma_f32_16x16x32_bf16 v[80:83], v[176:179], a[12:15], 0\nR 174,186,36864\n"
"M 80,180,28\nG 4\nv_mfma_f32_16x16x32_bf16 v[84:87], v[160:163], a[12:15], 0\nR 176,187,24576\nM 84,164,28\nbuffer_load_dwordx4 v221, s[16:19], s63 offen offset:1024 lds\n"
"v_mfma_f32_16x16x32_bf16 v[68:71], v[160:163], a[8:11], 0\nR 178,187,28672\nM 68,164,24\nv_mfma_f32_16x16x32_bf16 v[52:55], v[160:163], a[4:7], 0\nR 180,187,32768\nM 52,164,20\n"
"s_add_u32 m0, s50, 106496\nv_mfma_f32_16x16x32_bf16 v[36:39], v[160:163], a[0:3], 0\nR 182,187,36864\nv_mov_b32 v248, 0\nM 36,164,16\nG 4\n"
"v_mov_b32 v249, 0\nv_mfma_f32_16x16x32_bf16 v[40:43], v[168:171], a[0:3], 0\nR 160,184,40960\ns_add_u32 s61, s82, 385040\ns_mov_b64 exec, 0xf\nbuffer_atomic_or_x2 v[248:249], v250, s[20:23], s61 offen sc0 sc1\n"
"s_mov_b64 exec, -1\nM 40,172,16\ns_add_u32 s63, s56, 262144\nv_mfma_f32_16x16x32_bf16 v[56:59], v[168:171], a[4:7], 0\nR 162,184,45056\nM 56,172,20\n"
"buffer_load_dwordx4 v220, s[16:19], s63 offen lds\nv_mfma_f32_16x16x32_bf16 v[72:75], v[168:171], a[8:11], 0\nR 164,184,49152\nM 72,172,24\nv_mfma_f32_16x16x32_bf16 v[88:91], v[168:171], a[12:15], 0\nR 166,184,53248\n"
"M 88,172,28\nG 4\nv_mfma_f32_16x16x32_bf16 v[92:95], v[176:179], a[12:15], 0\nR 168,185,40960\nM 92,180,28\nbuffer_load_dwordx4 v221, s[16:19], s63 offen offset:1024 lds\n"
"v_mfma_f32_16x16x32_bf16 v[76:79], v[176:179], a[8:11], 0\nR 170,185,45056\ns_add_u32 s63, s56, 786432\nM 76,180,24\ns_add_u32 m0, s50, 114688\nv_mfma_f32_16x16x32_bf16 v[60:63], v[176:179], a[4:7], 0\n"
"R 172,185,49152\nM 60,180,20\nbuffer_load_dwordx4 v220, s[16:19], s63 offen lds\nv_mfma_f32_16x16x32_bf16 v[44:47], v[176:179], a[0:3], 0\nR 174,185,53248\nM 44,180,16\n"
"G 4\nM 32,160,32\nR 176,186,40960\nM 32,164,48\nbuffer_load_dwordx4 v221, s[16:19], s63 offen offset:1024 lds\nM 48,160,36\n"
"R 178,186,45056\ns_add_u32 s63, s56, 327680\nM 48,164,52\ns_add_u32 m0, s50, 0\nM 64,160,40\nR 180,186,49152\n"
"M 64,164,56\nbuffer_load_dwordx4 v220, s[16:19], s63 offen lds\nM 80,160,44\nR 182,186,53248\nM 80,164,60\nG 4\n"
"M 84,168,44\nR 160,187,40960\nM 84,172,60\nbuffer_load_dwordx4 v221, s[16:19], s63 offen offset:1024 lds\nM 68,168,40\nR 162,187,45056\n"
"M 68,172,56\nM 52,168,36\nR 164,187,49152\nM 52,172,52\ns_add_u32 m0, s50, 8192\nM 36,168,32\n"
"R 166,187,53248\ns_add_u32 s63, s56, 851968\nM 36,172,48\nG 4\nM 40,176,32\nR 168,184,57344\n"
"M 40,180,48\ns_add_u32 s64, s68, 23072768\nM 56,176,36\nR 170,184,61440\ns_waitcnt vmcnt(6)\nv_cmp_eq_u64 s[86:87], s[46:47], v[248:249]\n"
"M 56,180,52\nbuffer_load_dwordx4 v220, s[16:19], s63 offen lds\nM 72,176,40\nR 172,188,0\ns_and_b32 s62, s86, 0xf\ns_cmp_eq_u32 s62, 0xf\n"
"s_cbranch_scc0 Lq456\nLq457:\nM 72,180,56\ns_add_u32 s65, s68, 23187456\nM 88,176,44\nR 174,188,4096\n"
"s_barrier\nM 88,180,60\nG 4\nM 92,160,44\nR 176,185,57344\nM 92,164,60\n"
"buffer_load_dwordx4 a[160:163], v252, s[20:23], s64 offen sc1\nM 76,160,40\nR 178,185,61440\nM 76,164,56\ns_add_u32 s66, s69, 23072768\nM 60,160,36\n"
"R 180,189,0\nM 60,164,52\nbuffer_load_dwordx4 a[164:167], v252, s[20:23], s65 offen sc1\nM 44,160,32\nR 182,189,4096\nM 44,164,48\n"
"G 4\nM 32,168,64\nR 160,186,57344\nM 32,172,80\ns_add_u32 s67, s69, 23187456\nM 48,168,68\n"
"R 162,186,61440\nM 48,172,84\nbuffer_load_dwordx4 a[168:171], v252, s[20:23], s66 offen sc1\nM 64,168,72\nR 164,190,0\nM 64,172,88\n"
"M 80,168,76\nR 166,190,4096\nM 80,172,92\nG 4\nM 84,176,76\nR 168,187,57344\n"
"M 84,180,92\nbuffer_load_dwordx4 a[172:175], v252, s[20:23], s67 offen sc1\nM 68,176,72\nR 170,187,61440\nM 68,180,88\ns_add_u32 s64, s68, 23079936\n"
"M 52,176,68\nR 172,191,0\nM 52,180,84\ns_add_u32 s65, s68, 23194624\nM 36,176,64\nR 174,191,4096\n"
"M 36,180,80\nbuffer_load_dwordx4 v221, s[16:19], s63 offen offset:1024 lds\nG 4\nM 40,160,64\nR 176,188,8192\ns_add_u32 m0, s50, 16384\n"
"M 40,164,80\nbuffer_load_dwordx4 a[176:179], v252, s[20:23], s64 offen sc1\nM 56,160,68\nR 178,188,12288\ns_add_u32 s63, s57, 262144\nM 56,164,84\n"
"s_add_u32 s66, s69, 23079936\nbuffer_load_dwordx4 v220, s[16:19], s63 offen lds\nM 72,160,72\nR 180,188,16384\nLq572:\ns_lshr_b32 s60, s45, 3\n"
"s_lshl2_add_u32 s60, s60, s40\ns_lshl_b32 s60, s60, 8\nv_lshl_add_u32 v224, v252, 3, s60\nv_add_u32 v224, Lq572+4096-LqPC, v224\ns_mov_b64 exec, 3\nglobal_load_dword v224, v224, s[98:99]\n"
"s_mov_b64 exec, -1\nM 72,164,88\nbuffer_load_dwordx4 a[180:183], v252, s[20:23], s65 offen sc1\nM 88,160,76\nR 182,188,20480\nM 88,164,92\n"
"buffer_load_dwordx4 v221, s[16:19], s63 offen offset:1024 lds\nG 4\nM 92,168,76\nR 160,189,8192\ns_add_u32 m0, s50, 24576\nM 92,172,92\n"
"s_add_u32 s67, s69, 23194624\nM 76,168,72\nR 162,189,12288\ns_add_u32 s63, s57, 786432\nM 76,172,88\nbuffer_load_dwordx4 a[184:187], v252, s[20:23], s66 offen sc1\n"
"buffer_load_dwordx4 v220, s[16:19], s63 offen lds\nM 60,168,68\nR 164,189,16384\nM 60,172,84\nbuffer_load_dwordx4 v221, s[16:19], s63 offen offset:1024 lds\nM 44,168,64\n"
"R 166,189,20480\ns_add_u32 m0, s50, 32768\nM 44,172,80\nG 4\nM 32,176,96\nR 168,190,8192\n"
"s_add_u32 s63, s57, 327680\nM 32,180,112\nbuffer_load_dwordx4 a[188:191], v252, s[20:23], s67 offen sc1\nM 48,176,100\nR 170,190,12288\nM 48,180,116\n"
"s_add_u32 s64, s68, 23073792\nbuffer_load_dwordx4 v220, s[16:19], s63 offen lds\nM 64,176,104\nR 172,190,16384\nM 64,180,120\nbuffer_load_dwordx4 v221, s[16:19], s63 offen offset:1024 lds\n"
"M 80,176,108\nR 174,190,20480\ns_add_u32 m0, s50, 40960\nM 80,180,124\nG 4\nM 84,160,108\n"
"R 176,191,8192\ns_waitcnt vmcnt(19)\ns_barrier\nM 84,164,124\nbuffer_load_dwordx4 a[192:195], v252, s[20:23], s64 offen sc1\nM 68,160,104\n"
"R 178,191,12288\ns_add_u32 s63, s57, 851968\nM 68,164,120\ns_add_u32 s65, s68, 23188480\nM 52,160,100\nR 180,191,16384\n"
"buffer_load_dwordx4 v220, s[16:19], s63 offen lds\nM 52,164,116\ns_add_u32 s66, s69, 23073792\nM 36,160,96\nR 182,191,20480\nM 36,164,112\n"
"G 4\nM 40,168,96\nR 160,188,24576\nbuffer_load_dwordx4 v221, s[16:19], s63 offen offset:1024 lds\nM 40,172,112\nbuffer_load_dwordx4 a[196:199], v252, s[20:23], s65 offen sc1\n"
"M 56,168,100\nR 162,188,28672\nM 56,172,116\ns_add_u32 s67, s69, 23188480\nM 72,168,104\nR 164,188,32768\n"
"M 72,172,120\nbuffer_load_dwordx4 a[200:203], v252, s[20:23], s66 offen sc1\nM 88,168,108\nR 166,188,36864\nM 88,172,124\nG 4\n"
"M 92,176,108\nR 168,189,24576\nM 92,180,124\ns_add_u32 s64, s68, 23080960\nM 76,176,104\nR 170,189,28672\n"
"M 76,180,120\nbuffer_load_dwordx4 a[204:207], v252, s[20:23], s67 offen sc1\nM 60,176,100\nR 172,189,32768\nM 60,180,116\nM 44,176,96\n"
"R 174,189,36864\nM 44,180,112\nG 4\nM 32,160,128\nR 176,190,24576\nM 32,164,144\n"
"buffer_load_dwordx4 a[208:211], v252, s[20:23], s64 offen sc1\nM 48,160,132\nR 178,190,28672\nM 48,164,148\ns_add_u32 s65, s68, 23195648\nM 64,160,136\n"
"R 180,190,32768\nM 64,164,152\ns_add_u32 s66, s69, 23080960\nM 80,160,140\nR 182,190,36864\nM 80,164,156\n"
"G 4\nM 84,168,140\nR 160,191,24576\nM 84,172,156\nbuffer_load_dwordx4 a[212:215], v252, s[20:23], s65 offen sc1\nM 68,168,136\n"
"R 162,191,28672\nM 68,172,152\ns_add_u32 s67, s69, 23195648\nM 52,168,132\nR 164,191,32768\nM 52,172,148\n"
"buffer_load_dwordx4 a[216:219], v252, s[20:23], s66 offen sc1\nM 36,168,128\nR 166,191,36864\nM 36,172,144\nG 4\nM 40,176,128\n"
"R 168,188,40960\nM 40,180,144\nM 56,176,132\nR 170,188,45056\nM 56,180,148\nbuffer_load_dwordx4 a[220:223], v252, s[20:23], s67 offen sc1\n"
"M 72,176,136\nR 172,188,49152\ns_add_u32 s63, s58, 262144\nM 72,180,152\ns_add_u32 m0, s50, 49152\nM 88,176,140\n"
"R 174,188,53248\nM 88,180,156\nG 4\nM 92,160,140\nR 176,189,40960\nM 92,164,156\n"
"buffer_load_dwordx4 v220, s[16:19], s63 offen lds\nM 76,160,136\nR 178,189,45056\nM 76,164,152\nM 60,160,132\nR 180,189,49152\n"
"M 60,164,148\nbuffer_load_dwordx4 v221, s[16:19], s63 offen offset:1024 lds\nM 44,160,128\nR 182,189,53248\nM 44,164,144\ns_waitcnt vmcnt(9)\n"
"s_barrier\nG 4\nM 96,168,160\nR 160,190,40960\nM 96,172,176\ns_add_u32 m0, s50, 57344\n"
"M 112,168,164\nR 162,190,45056\nM 112,172,180\ns_add_u32 s63, s58, 786432\nM 128,168,168\nR 164,190,49152\n"
"v_lshrrev_b32 v230, 8, v252\nM 128,172,184\nbuffer_load_dwordx4 v220, s[16:19], s63 offen lds\nM 144,168,172\nR 166,190,53248\nv_lshlrev_b32 v230, 3, v230\n"
"M 144,172,188\nG 4\nM 148,176,172\nR 168,191,40960\nM 148,180,188\nbuffer_load_dwordx2 v[232:233], v230, s[36:39], s53 offen offset:0\n"
"M 132,176,168\nR 170,191,45056\nM 132,180,184\nbuffer_load_dwordx2 v[234:235], v230, s[36:39], s53 offen offset:32\nM 116,176,164\nR 172,191,49152\n"
"M 116,180,180\nbuffer_load_dwordx4 v221, s[16:19], s63 offen offset:1024 lds\nM 100,176,160\nR 174,191,53248\nM 100,180,176\nG 4\n"
"M 104,160,160\nR 176,184,0\nM 104,164,176\ns_add_u32 m0, s50, 65536\nM 120,160,164\nR 178,184,4096\n"
"M 120,164,180\ns_add_u32 s63, s58, 327680\nM 136,160,168\nR 180,184,8192\nbuffer_load_dwordx2 v[236:237], v230, s[36:39], s53 offen offset:64\nM 136,164,184\n"
"buffer_load_dwordx4 v220, s[16:19], s63 offen lds\nM 152,160,172\nR 182,184,12288\nM 152,164,188\nbuffer_load_dwordx2 v[238:239], v230, s[36:39], s53 offen offset:96\nG 4\n"
"M 156,168,172\nR 160,185,0\nv_bfe_u32 v242, v252, 4, 4\nM 156,172,188\nv_lshrrev_b32 v243, 8, v252\nv_lshlrev_b32 v243, 2, v243\n"
"M 140,168,168\nR 162,185,4096\nv_sub_u32 v242, v242, v243\nM 140,172,184\nv_lshlrev_b32 v243, 4, v242\ns_mov_b64 s[60:61], 0x3f80\n"
"v_lshlrev_b64 v[208:209], v243, s[60:61]\nM 124,168,164\nR 164,185,8192\nM 124,172,180\nbuffer_load_dwordx4 v221, s[16:19], s63 offen offset:1024 lds\nM 108,168,160\n"
"R 166,185,12288\nM 108,172,176\nG 4\nM 96,176,192\nR 168,186,0\ns_waitcnt vmcnt(13)\n"
"M 96,180,208\ns_add_u32 m0, s50, 73728\nM 112,176,196\nR 170,186,4096\ns_waitcnt vmcnt(12)\nM 112,180,212\n"
"s_add_u32 s63, s58, 851968\nM 128,176,200\nR 172,186,8192\ns_waitcnt vmcnt(11)\nM 128,180,216\nbuffer_load_dwordx4 v220, s[16:19], s63 offen lds\n"
"M 144,176,204\nR 174,186,12288\ns_waitcnt vmcnt(11)\nM 144,180,220\nG 4\nM 148,160,204\n"
"R 176,187,0\nM 148,164,220\nv_cmp_gt_u32 s[60:61], 4, v242\ns_nop 1\nv_cndmask_b32 v212, 0, v208, s[60:61]\nv_cndmask_b32 v208, 0, v208, s[60:61]\n"
"v_cndmask_b32 v213, 0, v209, s[60:61]\nv_cndmask_b32 v209, 0, v209, s[60:61]\nM 132,160,200\nR 178,187,4096\nM 132,164,216\nM 116,160,196\n"
"R 180,187,8192\nM 116,164,212\nbuffer_load_dwordx4 v221, s[16:19], s63 offen offset:1024 lds\nM 100,160,192\nR 182,187,12288\nM 100,164,208\n"
"G 4\nM 104,168,192\nR 160,184,16384\nM 104,172,208\ns_add_u32 m0, s50, 81920\nM 120,168,196\n"
"R 162,184,20480\nM 120,172,212\ns_add_u32 s63, s59, 0\nM 136,168,200\nR 164,184,24576\nM 136,172,216\n"
"buffer_load_dwordx4 v220, s[16:19], s63 offen lds\nM 152,168,204\nR 166,184,28672\nM 152,172,220\nG 4\nM 156,176,204\n"
"R 168,185,16384\nM 156,180,220\nM 140,176,200\nR 170,185,20480\nM 140,180,216\nbuffer_load_dwordx4 v221, s[16:19], s63 offen offset:1024 lds\n"
"M 124,176,196\nR 172,185,24576\nM 124,180,212\nM 108,176,192\nR 174,185,28672\nM 108,180,208\n"
"G 4\nM 0,160,160\nR 176,186,16384\nM 0,164,176\ns_mov_b32 m0, s51\nM 16,160,164\n"
"R 178,186,20480\nv_cvt_pk_bf16_f32 v128, v128, v129\nv_cvt_pk_bf16_f32 v129, v130, v131\nM 16,164,180\nds_write_addtid_b32 v128 offset:0\nv_cvt_pk_bf16_f32 v130, v132, v133\n"
"v_cvt_pk_bf16_f32 v131, v134, v135\nv_mfma_f32_16x16x32_bf16 a[224:227], v[160:163], a[168:171], a[224:227]\nR 180,186,24576\nv_cvt_pk_bf16_f32 v132, v136, v137\nv_mfma_f32_16x16x32_bf16 a[224:227], v[164:167], a[184:187], a[224:227]\nds_write_addtid_b32 v129 offset:256\n"
"v_cvt_pk_bf16_f32 v133, v138, v139\nv_mfma_f32_16x16x32_bf16 a[240:243], v[160:163], a[172:175], a[240:243]\nR 182,186,28672\nv_cvt_pk_bf16_f32 v134, v140, v141\nv_mfma_f32_16x16x32_bf16 a[240:243], v[164:167], a[188:191], a[240:243]\nds_write_addtid_b32 v130 offset:512\n"
"G 7\nv_cvt_pk_bf16_f32 v135, v142, v143\nv_mfma_f32_16x16x32_bf16 a[244:247], v[168:171], a[172:175], a[244:247]\nR 160,187,16384\nv_cvt_pk_bf16_f32 v136, v144, v145\nv_mfma_f32_16x16x32_bf16 a[244:247], v[172:175], a[188:191], a[244:247]\n"
"ds_write_addtid_b32 v131 offset:768\nv_cvt_pk_bf16_f32 v137, v146, v147\nv_mfma_f32_16x16x32_bf16 a[228:231], v[168:171], a[168:171], a[228:231]\nR 162,187,20480\nv_cvt_pk_bf16_f32 v138, v148, v149\nv_mfma_f32_16x16x32_bf16 a[228:231], v[172:175], a[184:187], a[228:231]\n"
"ds_write_addtid_b32 v132 offset:1024\nv_cvt_pk_bf16_f32 v139, v150, v151\nM 20,168,164\nR 164,187,24576\nv_cvt_pk_bf16_f32 v140, v152, v153\nM 20,172,180\n"
"ds_write_addtid_b32 v133 offset:1280\nv_cvt_pk_bf16_f32 v141, v154, v155\nM 4,168,160\nR 166,187,28672\nv_cvt_pk_bf16_f32 v142, v156, v157\nM 4,172,176\n"
"ds_write_addtid_b32 v134 offset:1536\nG 9\nv_cvt_pk_bf16_f32 v143, v158, v159\nM 8,176,160\nR 168,184,32768\nds_write_addtid_b32 v135 offset:1792\n"
"M 8,180,176\nds_write_addtid_b32 v136 offset:2048\nds_write_addtid_b32 v137 offset:2304\nM 24,176,164\nR 170,184,36864\nds_write_addtid_b32 v138 offset:2560\n"
"M 24,180,180\nds_write_addtid_b32 v139 offset:2816\nds_write_addtid_b32 v140 offset:3072\nv_mfma_f32_16x16x32_bf16 a[232:235], v[176:179], a[168:171], a[232:235]\nR 172,184,40960\nds_write_addtid_b32 v141 offset:3328\n"
"v_mfma_f32_16x16x32_bf16 a[232:235], v[180:183], a[184:187], a[232:235]\nds_write_addtid_b32 v142 offset:3584\nds_write_addtid_b32 v143 offset:3840\nv_mfma_f32_16x16x32_bf16 a[248:251], v[176:179], a[172:175], a[248:251]\nR 174,184,45056\ns_add_u32 m0, s50, 90112\n"
"v_mfma_f32_16x16x32_bf16 a[248:251], v[180:183], a[188:191], a[248:251]\ns_add_u32 s63, s59, 524288\nG 14\nv_mfma_f32_16x16x32_bf16 a[252:255], v[160:163], a[172:175], a[252:255]\nR 176,185,32768\ns_waitcnt vmcnt(8) lgkmcnt(2)\n"
"s_barrier\nv_mfma_f32_16x16x32_bf16 a[252:255], v[164:167], a[188:191], a[252:255]\nbuffer_load_dwordx4 v220, s[16:19], s63 offen lds\nv_mfma_f32_16x16x32_bf16 a[236:239], v[160:163], a[168:171], a[236:239]\nR 178,185,36864\nv_mfma_f32_16x16x32_bf16 a[236:239], v[164:167], a[184:187], a[236:239]\n"
"ds_read2st64_b32 v[128:129], v225 offset0:0 offset1:1\nM 28,160,164\nR 180,185,40960\nM 28,164,180\nbuffer_load_dwordx4 v221, s[16:19], s63 offen offset:1024 lds\nM 12,160,160\n"
"R 182,185,45056\ns_add_u32 m0, s50, 98304\nM 12,164,176\nds_read2st64_b32 v[130:131], v225 offset0:2 offset1:3\nG 6\nM 0,168,192\n"
"R 160,186,32768\ns_add_u32 s63, s59, 65536\nM 0,172,208\nbuffer_load_dwordx4 v220, s[16:19], s63 offen lds\nM 16,168,196\nR 162,186,36864\n"
"M 16,172,212\nds_read2st64_b32 v[132:133], v225 offset0:4 offset1:5\nv_mfma_f32_16x16x32_bf16 a[224:227], v[168:171], a[200:203], a[224:227]\nR 164,186,40960\nv_mfma_f32_16x16x32_bf16 a[224:227], v[172:175], a[216:219], a[224:227]\nbuffer_load_dwordx4 v221, s[16:19], s63 offen offset:1024 lds\n"
"G 4\nv_mfma_f32_16x16x32_bf16 v[96:99], v[208:211], v[128:131], v[96:99]\nv_mfma_f32_16x16x32_bf16 v[100:103], v[210:213], v[128:131], v[100:103]\nv_mfma_f32_16x16x32_bf16 a[240:243], v[168:171], a[204:207], a[240:243]\nR 166,186,45056\ns_add_u32 m0, s50, 106496\n"
"v_mfma_f32_16x16x32_bf16 a[240:243], v[172:175], a[220:223], a[240:243]\nds_read2st64_b32 v[134:135], v225 offset0:6 offset1:7\nv_mfma_f32_16x16x32_bf16 a[244:247], v[176:179], a[204:207], a[244:247]\nR 168,187,32768\ns_add_u32 s63, s59, 589824\nv_mfma_f32_16x16x32_bf16 a[244:247], v[180:183], a[220:223], a[244:247]\n"
"buffer_load_dwordx4 v220, s[16:19], s63 offen lds\nv_mfma_f32_16x16x32_bf16 a[228:231], v[176:179], a[200:203], a[228:231]\nR 170,187,36864\nv_mfma_f32_16x16x32_bf16 a[228:231], v[180:183], a[216:219], a[228:231]\nds_read2st64_b32 v[136:137], v225 offset0:8 offset1:9\nM 20,176,196\n"
"R 172,187,40960\nM 20,180,212\nds_read2st64_b32 v[138:139], v225 offset0:10 offset1:11\nG 5\nv_mfma_f32_16x16x32_bf16 v[104:107], v[208:211], v[132:135], v[104:107]\nv_mfma_f32_16x16x32_bf16 v[108:111], v[210:213], v[132:135], v[108:111]\n"
"M 4,176,192\nR 174,187,45056\nM 4,180,208\nds_read2st64_b32 v[140:141], v225 offset0:12 offset1:13\nM 8,160,192\nR 176,184,49152\n"
"ds_read2st64_b32 v[142:143], v225 offset0:14 offset1:15\nG 4\nv_mfma_f32_16x16x32_bf16 v[112:115], v[208:211], v[136:139], v[112:115]\nv_mfma_f32_16x16x32_bf16 v[116:119], v[210:213], v[136:139], v[116:119]\nG 0\nv_mfma_f32_16x16x32_bf16 v[120:123], v[208:211], v[140:143], v[120:123]\n"
"v_mfma_f32_16x16x32_bf16 v[124:127], v[210:213], v[140:143], v[124:127]\nM 8,164,208\nbuffer_load_dwordx4 v221, s[16:19], s63 offen offset:1024 lds\nM 24,160,196\nR 178,184,53248\ns_add_u32 s63, s59, 131072\n"
"M 24,164,212\ns_add_u32 m0, s50, 114688\nv_mfma_f32_16x16x32_bf16 a[232:235], v[160:163], a[200:203], a[232:235]\nR 180,184,57344\nv_cvt_pk_bf16_f32 v96, v96, v97\nv_cvt_pk_bf16_f32 v97, v98, v99\n"
"v_mfma_f32_16x16x32_bf16 a[232:235], v[164:167], a[216:219], a[232:235]\nbuffer_load_dwordx4 v220, s[16:19], s63 offen lds\nv_cvt_pk_bf16_f32 v98, v100, v101\nv_mfma_f32_16x16x32_bf16 a[248:251], v[160:163], a[204:207], a[248:251]\nR 182,184,61440\nv_cvt_pk_bf16_f32 v99, v102, v103\n"
"v_cvt_pk_bf16_f32 v104, v104, v105\nv_mfma_f32_16x16x32_bf16 a[248:251], v[164:167], a[220:223], a[248:251]\nv_cvt_pk_bf16_f32 v105, v106, v107\nv_cvt_pk_bf16_f32 v106, v108, v109\nv_cvt_pk_bf16_f32 v107, v110, v111\nv_mfma_f32_16x16x32_bf16 a[252:255], v[168:171], a[204:207], a[252:255]\n"
"R 160,185,49152\nv_cvt_pk_bf16_f32 v112, v112, v113\nv_cvt_pk_bf16_f32 v113, v114, v115\nv_mfma_f32_16x16x32_bf16 a[252:255], v[172:175], a[220:223], a[252:255]\nbuffer_load_dwordx4 v221, s[16:19], s63 offen offset:1024 lds\nv_cvt_pk_bf16_f32 v114, v116, v117\n"
"v_mfma_f32_16x16x32_bf16 a[236:239], v[168:171], a[200:203], a[236:239]\nR 162,185,53248\ns_add_u32 s63, s59, 655360\nv_cvt_pk_bf16_f32 v115, v118, v119\nv_mfma_f32_16x16x32_bf16 a[236:239], v[172:175], a[216:219], a[236:239]\ns_add_u32 m0, s50, 0\n"
"v_cvt_pk_bf16_f32 v120, v120, v121\nv_cvt_pk_bf16_f32 v121, v122, v123\nv_cvt_pk_bf16_f32 v122, v124, v125\nM 28,168,196\nR 164,185,57344\ns_bfe_u32 s100, s56, 0x20007\n"
"s_mul_i32 s100, s100, 49152\ns_add_u32 s100, s100, s72\ns_add_u32 s100, s100, 52428800\nv_cvt_pk_bf16_f32 v123, v126, v127\nM 28,172,212\nbuffer_load_dwordx4 v220, s[16:19], s63 offen lds\n"
"buffer_store_dwordx4 v[96:99], v252, s[20:23], s100 offen offset:0 sc1\nM 12,168,192\nR 166,185,61440\nbuffer_store_dwordx4 v[104:107], v252, s[20:23], s100 offen offset:1024 sc1\nM 12,172,208\ns_waitcnt vmcnt(13)\n"
"s_barrier\nbuffer_store_dwordx4 v[112:115], v252, s[20:23], s100 offen offset:2048 sc1\nG 4\nM 32,176,160\nR 168,186,49152\nbuffer_store_dwordx4 v[120:123], v252, s[20:23], s100 offen offset:3072 sc1\n"
"M 32,180,176\nbuffer_load_dwordx4 v221, s[16:19], s63 offen offset:1024 lds\nv_xor_b32 v240, 0x2000, v225\nM 48,176,164\nR 170,186,53248\nds_write2st64_b32 v240, a224, a225 offset0:0 offset1:1\n"
"M 48,180,180\ns_add_u32 m0, s50, 8192\nds_write2st64_b32 v240, a226, a227 offset0:2 offset1:3\nM 64,176,168\nR 172,186,57344\nds_write2st64_b32 v240, a228, a229 offset0:4 offset1:5\n"
"M 64,180,184\ns_add_u32 s63, s59, 196608\nds_write2st64_b32 v240, a230, a231 offset0:6 offset1:7\nM 80,176,172\nR 174,186,61440\nv_mfma_f32_16x16x32_bf16 v[96:99], v[208:211], v[192:195], 0\n"
"ds_write2st64_b32 v240, a232, a233 offset0:8 offset1:9\nM 80,180,188\nv_mfma_f32_16x16x32_bf16 v[100:103], v[210:213], v[192:195], 0\nds_write2st64_b32 v240, a234, a235 offset0:10 offset1:11\nG 10\nM 84,160,172\n"
"R 176,187,49152\nv_mfma_f32_16x16x32_bf16 v[104:107], v[208:211], v[196:199], 0\nds_write2st64_b32 v240, a236, a237 offset0:12 offset1:13\nM 84,164,188\nv_mfma_f32_16x16x32_bf16 v[108:111], v[210:213], v[196:199], 0\nbuffer_load_dwordx4 v220, s[16:19], s63 offen lds\n"
"ds_write2st64_b32 v240, a238, a239 offset0:14 offset1:15\nM 68,160,168\nR 178,187,53248\nv_mfma_f32_16x16x32_bf16 v[112:115], v[208:211], v[200:203], 0\nds_write2st64_b32 v240, a240, a241 offset0:16 offset1:17\nM 68,164,184\n"
"v_mfma_f32_16x16x32_bf16 v[116:119], v[210:213], v[200:203], 0\nbuffer_load_dwordx4 v221, s[16:19], s63 offen offset:1024 lds\nds_write2st64_b32 v240, a242, a243 offset0:18 offset1:19\nM 52,160,164\nR 180,187,57344\nv_mfma_f32_16x16x32_bf16 v[120:123], v[208:211], v[204:207], 0\n"
"s_add_u32 m0, s50, 16384\ns_add_u32 s63, s59, 720896\nds_write2st64_b32 v240, a244, a245 offset0:20 offset1:21\nM 52,164,180\nv_mfma_f32_16x16x32_bf16 v[124:127], v[210:213], v[204:207], 0\nbuffer_load_dwordx4 v220, s[16:19], s63 offen lds\n"
"ds_write2st64_b32 v240, a246, a247 offset0:22 offset1:23\nM 36,160,160\nR 182,187,61440\nv_mfma_f32_16x16x32_bf16 v[96:99], v[208:211], v[232:235], v[96:99]\nds_write2st64_b32 v240, a248, a249 offset0:24 offset1:25\nM 36,164,176\n"
"v_mfma_f32_16x16x32_bf16 v[100:103], v[210:213], v[232:235], v[100:103]\nds_write2st64_b32 v240, a250, a251 offset0:26 offset1:27\nG 14\nM 40,168,160\nR 160,188,0\nv_mfma_f32_16x16x32_bf16 v[104:107], v[208:211], v[236:239], v[104:107]\n"
"ds_write2st64_b32 v240, a252, a253 offset0:28 offset1:29\nM 40,172,176\nv_mfma_f32_16x16x32_bf16 v[108:111], v[210:213], v[236:239], v[108:111]\nbuffer_load_dwordx4 v221, s[16:19], s63 offen offset:1024 lds\nds_write2st64_b32 v240, a254, a255 offset0:30 offset1:31\nM 56,168,164\n"
"R 162,188,4096\nv_mfma_f32_16x16x32_bf16 v[112:115], v[208:211], v[232:235], v[112:115]\ns_add_u32 m0, s50, 24576\ns_add_u32 s63, s59, 393216\nM 56,172,180\nv_mfma_f32_16x16x32_bf16 v[116:119], v[210:213], v[232:235], v[116:119]\n"
"buffer_load_dwordx4 v220, s[16:19], s63 offen lds\nM 72,168,168\nR 164,188,8192\nv_mfma_f32_16x16x32_bf16 v[120:123], v[208:211], v[236:239], v[120:123]\nM 72,172,184\nv_mfma_f32_16x16x32_bf16 v[124:127], v[210:213], v[236:239], v[124:127]\n"
"buffer_load_dwordx4 v221, s[16:19], s63 offen offset:1024 lds\nM 88,168,172\nR 166,188,12288\ns_add_u32 s63, s59, 458752\nM 88,172,188\nG 8\n"
"M 92,176,172\nR 168,189,0\ns_waitcnt vmcnt(18) lgkmcnt(4)\ns_barrier\nM 92,180,188\ns_add_u32 m0, s50, 32768\n"
"ds_read2st64_b32 v[128:129], v225 offset0:0 offset1:1\nds_read2st64_b32 v[130:131], v225 offset0:2 offset1:3\nM 76,176,168\nR 170,189,4096\nds_read2st64_b32 v[132:133], v225 offset0:4 offset1:5\nM 76,180,184\n"
"buffer_load_dwordx4 v220, s[16:19], s63 offen lds\nds_read2st64_b32 v[134:135], v225 offset0:6 offset1:7\nM 60,176,164\nR 172,189,8192\nds_read2st64_b32 v[136:137], v225 offset0:8 offset1:9\nM 60,180,180\n"
"ds_read2st64_b32 v[138:139], v225 offset0:10 offset1:11\nds_read2st64_b32 v[140:141], v225 offset0:12 offset1:13\nM 44,176,160\nR 174,189,12288\nds_read2st64_b32 v[142:143], v225 offset0:14 offset1:15\nM 44,180,176\n"
"ds_read2st64_b32 v[144:145], v225 offset0:16 offset1:17\nG 13\nG 11\nv_add_f32 v0, v0, v128\nM 32,160,192\nR 176,190,0\n"
"v_add_f32 v1, v1, v129\nG 11\nv_add_f32 v2, v2, v130\nM 32,164,208\nbuffer_load_dwordx4 v221, s[16:19], s63 offen offset:1024 lds\nv_add_f32 v3, v3, v131\n"
"M 48,160,196\nR 178,190,4096\nG 10\nv_add_f32 v4, v4, v132\nv_add_f32 v5, v5, v133\nM 48,164,212\n"
"s_add_u32 m0, s50, 40960\nds_read2st64_b32 v[146:147], v225 offset0:18 offset1:19\nG 10\nv_add_f32 v6, v6, v134\nM 64,160,200\nR 180,190,8192\n"
"v_add_f32 v7, v7, v135\nG 9\nv_add_f32 v8, v8, v136\nM 64,164,216\ns_add_u32 s63, s59, 262144\nds_read2st64_b32 v[148:149], v225 offset0:20 offset1:21\n"
"v_add_f32 v9, v9, v137\nM 80,160,204\nR 182,190,12288\nG 10\nv_add_f32 v10, v10, v138\nv_add_f32 v11, v11, v139\n"
"M 80,164,220\nds_read2st64_b32 v[150:151], v225 offset0:22 offset1:23\nG 9\nv_add_f32 v12, v12, v140\nM 84,168,204\nR 160,191,0\n"
"v_add_f32 v13, v13, v141\nG 9\nv_add_f32 v14, v14, v142\nM 84,172,220\nbuffer_load_dwordx4 v220, s[16:19], s63 offen lds\nv_add_f32 v15, v15, v143\n"
"M 68,168,200\nR 162,191,4096\nG 9\nv_add_f32 v16, v16, v144\nv_add_f32 v17, v17, v145\nM 68,172,216\n"
"ds_read2st64_b32 v[152:153], v225 offset0:24 offset1:25\nG 7\nv_add_f32 v18, v18, v146\nv_add_f32 v19, v19, v147\nM 52,168,196\nR 164,191,8192\n"
"G 6\nv_add_f32 v20, v20, v148\nv_add_f32 v21, v21, v149\nM 52,172,212\nds_read2st64_b32 v[154:155], v225 offset0:26 offset1:27\nG 5\n"
"v_add_f32 v22, v22, v150\nv_add_f32 v23, v23, v151\nM 36,168,192\nR 166,191,12288\nds_read2st64_b32 v[156:157], v225 offset0:28 offset1:29\nM 36,172,208\n"
"ds_read2st64_b32 v[158:159], v225 offset0:30 offset1:31\nM 40,176,192\nR 168,188,16384\nM 40,180,208\nbuffer_load_dwordx4 v221, s[16:19], s63 offen offset:1024 lds\nG 6\n"
"v_add_f32 v24, v24, v152\nM 56,176,196\nR 170,188,20480\nv_add_f32 v25, v25, v153\nM 56,180,212\ns_add_u32 m0, s50, 49152\n"
"G 5\nv_add_f32 v26, v26, v154\nv_add_f32 v27, v27, v155\nG 3\nv_add_f32 v28, v28, v156\nv_add_f32 v29, v29, v157\n"
"G 2\nv_add_f32 v30, v30, v158\nv_add_f32 v31, v31, v159\nM 72,176,200\nR 172,188,24576\nv_cvt_pk_bf16_f32 v0, v0, v1\n"
"v_cvt_pk_bf16_f32 v1, v2, v3\nM 72,180,216\ns_add_u32 s63, s59, 786432\nv_cvt_pk_bf16_f32 v2, v4, v5\nv_cvt_pk_bf16_f32 v3, v6, v7\nv_cvt_pk_bf16_f32 v8, v8, v9\n"
"M 88,176,204\nR 174,188,28672\nv_cvt_pk_bf16_f32 v9, v10, v11\nv_cvt_pk_bf16_f32 v10, v12, v13\nM 88,180,220\nv_cvt_pk_bf16_f32 v11, v14, v15\n"
"v_cvt_pk_bf16_f32 v16, v16, v17\nv_cvt_pk_bf16_f32 v17, v18, v19\nM 92,160,204\nR 176,189,16384\nv_cvt_pk_bf16_f32 v18, v20, v21\nv_cvt_pk_bf16_f32 v19, v22, v23\n"
"M 92,164,220\nbuffer_load_dwordx4 v220, s[16:19], s63 offen lds\nv_cvt_pk_bf16_f32 v24, v24, v25\nM 76,160,200\nR 178,189,20480\nv_cvt_pk_bf16_f32 v25, v26, v27\n"
"v_cvt_pk_bf16_f32 v26, v28, v29\nM 76,164,216\ns_bfe_u32 s100, s57, 0x20007\ns_mul_i32 s100, s100, 49152\ns_add_u32 s100, s100, s72\ns_add_u32 s100, s100, 52445184\n"
"v_cvt_pk_bf16_f32 v27, v30, v31\nM 60,160,196\nR 180,189,24576\nbuffer_store_dwordx4 v[0:3], v252, s[20:23], s100 offen offset:0 sc1\nM 60,164,212\nbuffer_load_dwordx4 v221, s[16:19], s63 offen offset:1024 lds\n"
"buffer_store_dwordx4 v[8:11], v252, s[20:23], s100 offen offset:1024 sc1\nM 44,160,192\nR 182,189,28672\nbuffer_store_dwordx4 v[16:19], v252, s[20:23], s100 offen offset:2048 sc1\nM 44,164,208\ns_waitcnt vmcnt(23)\n"
"s_barrier\nG 4\nM 96,168,0\nR 160,190,16384\nbuffer_store_dwordx4 v[24:27], v252, s[20:23], s100 offen offset:3072 sc1\nM 96,172,16\n"
"s_mov_b32 m0, s51\nM 112,168,4\nR 162,190,20480\nv_cvt_pk_bf16_f32 v64, v64, v65\nv_cvt_pk_bf16_f32 v65, v66, v67\nM 112,172,20\n"
"ds_write_addtid_b32 v64 offset:0\nv_cvt_pk_bf16_f32 v66, v68, v69\nv_cvt_pk_bf16_f32 v67, v70, v71\nv_mfma_f32_16x16x32_bf16 v[128:131], v[168:171], a[8:11], 0\nR 164,190,24576\nv_cvt_pk_bf16_f32 v68, v72, v73\n"
"M 128,172,24\nds_write_addtid_b32 v65 offset:256\nv_cvt_pk_bf16_f32 v69, v74, v75\nv_mfma_f32_16x16x32_bf16 v[144:147], v[168:171], a[12:15], 0\nR 166,190,28672\nv_cvt_pk_bf16_f32 v70, v76, v77\n"
"M 144,172,28\nds_write_addtid_b32 v66 offset:512\nG 7\nv_cvt_pk_bf16_f32 v71, v78, v79\nv_mfma_f32_16x16x32_bf16 v[148:151], v[176:179], a[12:15], 0\nR 168,191,16384\n"
"v_cvt_pk_bf16_f32 v72, v80, v81\nM 148,180,28\nds_write_addtid_b32 v67 offset:768\nv_cvt_pk_bf16_f32 v73, v82, v83\nv_mfma_f32_16x16x32_bf16 v[132:135], v[176:179], a[8:11], 0\nR 170,191,20480\n"
"v_cvt_pk_bf16_f32 v74, v84, v85\nM 132,180,24\nds_write_addtid_b32 v68 offset:1024\nv_cvt_pk_bf16_f32 v75, v86, v87\nM 116,176,4\nR 172,191,24576\n"
"v_cvt_pk_bf16_f32 v76, v88, v89\nM 116,180,20\nds_write_addtid_b32 v69 offset:1280\nv_cvt_pk_bf16_f32 v77, v90, v91\nM 100,176,0\nR 174,191,28672\n"
"v_cvt_pk_bf16_f32 v78, v92, v93\nM 100,180,16\nds_write_addtid_b32 v70 offset:1536\nG 9\nv_cvt_pk_bf16_f32 v79, v94, v95\nM 104,160,0\n"
"R 176,188,32768\nds_write_addtid_b32 v71 offset:1792\nM 104,164,16\nds_write_addtid_b32 v72 offset:2048\nds_write_addtid_b32 v73 offset:2304\nM 120,160,4\n"
"R 178,188,36864\nds_write_addtid_b32 v74 offset:2560\nM 120,164,20\nds_write_addtid_b32 v75 offset:2816\nds_write_addtid_b32 v76 offset:3072\nv_mfma_f32_16x16x32_bf16 v[136:139], v[160:163], a[8:11], 0\n"
"R 180,188,40960\nds_write_addtid_b32 v77 offset:3328\nM 136,164,24\nds_write_addtid_b32 v78 offset:3584\nds_write_addtid_b32 v79 offset:3840\nv_mfma_f32_16x16x32_bf16 v[152:155], v[160:163], a[12:15], 0\n"
"R 182,188,45056\nM 152,164,28\nG 14\nv_mfma_f32_16x16x32_bf16 v[156:159], v[168:171], a[12:15], 0\nR 160,189,32768\ns_waitcnt vmcnt(16) lgkmcnt(2)\n"
"s_barrier\nM 156,172,28\nds_read2st64_b32 v[64:65], v225 offset0:0 offset1:1\nv_mfma_f32_16x16x32_bf16 v[140:143], v[168:171], a[8:11], 0\nR 162,189,36864\nM 140,172,24\n"
"ds_read2st64_b32 v[66:67], v225 offset0:2 offset1:3\nM 124,168,4\nR 164,189,40960\nM 124,172,20\nds_read2st64_b32 v[68:69], v225 offset0:4 offset1:5\nM 108,168,0\n"
"R 166,189,45056\nM 108,172,16\nds_read2st64_b32 v[70:71], v225 offset0:6 offset1:7\nG 8\nM 96,176,32\nR 168,190,32768\n"
"M 96,180,48\nds_read2st64_b32 v[72:73], v225 offset0:8 offset1:9\nG 6\nv_mfma_f32_16x16x32_bf16 v[32:35], v[208:211], v[64:67], v[32:35]\nv_mfma_f32_16x16x32_bf16 v[36:39], v[210:213], v[64:67], v[36:39]\nM 112,176,36\n"
"R 170,190,36864\nM 112,180,52\nds_read2st64_b32 v[74:75], v225 offset0:10 offset1:11\nM 128,176,40\nR 172,190,40960\nM 128,180,56\n"
"ds_read2st64_b32 v[76:77], v225 offset0:12 offset1:13\nG 6\nv_mfma_f32_16x16x32_bf16 v[40:43], v[208:211], v[68:71], v[40:43]\nv_mfma_f32_16x16x32_bf16 v[44:47], v[210:213], v[68:71], v[44:47]\nM 144,176,44\nR 174,190,45056\n"
"M 144,180,60\nds_read2st64_b32 v[78:79], v225 offset0:14 offset1:15\nM 148,160,44\nR 176,191,32768\nM 148,164,60\nG 5\n"
"v_mfma_f32_16x16x32_bf16 v[48:51], v[208:211], v[72:75], v[48:51]\nv_mfma_f32_16x16x32_bf16 v[52:55], v[210:213], v[72:75], v[52:55]\nM 132,160,40\nR 178,191,36864\nM 132,164,56\nM 116,160,36\n"
"R 180,191,40960\nM 116,164,52\nG 3\nv_mfma_f32_16x16x32_bf16 v[56:59], v[208:211], v[76:79], v[56:59]\nv_mfma_f32_16x16x32_bf16 v[60:63], v[210:213], v[76:79], v[60:63]\nM 100,160,32\n"
"R 182,191,45056\nv_mov_b32 v226, s46\nM 100,164,48\nv_mov_b32 v227, s47\nM 104,168,32\nR 160,188,49152\n"
"s_bfe_u32 s62, s56, 0x20007\ns_lshl_b32 s62, s62, 5\ns_add_u32 s61, s74, 548864\ns_add_u32 s61, s61, s62\ns_waitcnt vmcnt(0)\ns_mov_b64 exec, 1\n"
"buffer_atomic_swap_x2 v[226:227], off, s[20:23], s61 sc1\ns_mov_b64 exec, -1\nM 104,172,48\ns_bfe_u32 s62, s57, 0x20007\ns_lshl_b32 s62, s62, 5\ns_add_u32 s61, s74, 548872\n"
"s_add_u32 s61, s61, s62\ns_mov_b64 exec, 1\nbuffer_atomic_swap_x2 v[226:227], off, s[20:23], s61 sc1\ns_mov_b64 exec, -1\nM 120,168,36\nR 162,188,53248\n"
"M 120,172,52\ns_add_u32 m0, s50, 57344\nM 136,168,40\nR 164,184,0\nv_cvt_pk_bf16_f32 v32, v32, v33\nv_cvt_pk_bf16_f32 v33, v34, v35\n"
"M 136,172,56\ns_add_u32 s63, s59, 327680\nv_cvt_pk_bf16_f32 v34, v36, v37\nv_cvt_pk_bf16_f32 v35, v38, v39\nv_cvt_pk_bf16_f32 v40, v40, v41\nM 152,168,44\n"
"R 166,184,4096\nv_cvt_pk_bf16_f32 v41, v42, v43\nv_cvt_pk_bf16_f32 v42, v44, v45\nM 152,172,60\nG 4\nv_cvt_pk_bf16_f32 v43, v46, v47\n"
"v_cvt_pk_bf16_f32 v48, v48, v49\nv_cvt_pk_bf16_f32 v49, v50, v51\nM 156,176,44\nR 168,189,49152\nv_cvt_pk_bf16_f32 v50, v52, v53\nv_cvt_pk_bf16_f32 v51, v54, v55\n"
"M 156,180,60\nbuffer_load_dwordx4 v220, s[16:19], s63 offen lds\nv_cvt_pk_bf16_f32 v56, v56, v57\nM 140,176,40\nR 170,189,53248\nv_cvt_pk_bf16_f32 v57, v58, v59\n"
"v_cvt_pk_bf16_f32 v58, v60, v61\nM 140,180,56\nbuffer_load_dwordx4 v221, s[16:19], s63 offen offset:1024 lds\nv_cvt_pk_bf16_f32 v59, v62, v63\nM 124,176,36\nR 172,185,0\n"
"s_add_u32 s63, s59, 851968\ns_bfe_u32 s100, s58, 0x20007\ns_mul_i32 s100, s100, 49152\ns_add_u32 s100, s100, s72\ns_add_u32 s100, s100, 52461568\nM 124,180,52\n"
"s_add_u32 m0, s50, 65536\nbuffer_store_dwordx4 v[32:35], v252, s[20:23], s100 offen offset:0 sc1\nM 108,176,32\nR 174,185,4096\nbuffer_store_dwordx4 v[40:43], v252, s[20:23], s100 offen offset:1024 sc1\nM 108,180,48\n"
"s_barrier\nbuffer_store_dwordx4 v[48:51], v252, s[20:23], s100 offen offset:2048 sc1\nG 4\nM 96,160,64\nR 176,190,49152\nbuffer_store_dwordx4 v[56:59], v252, s[20:23], s100 offen offset:3072 sc1\n"
"M 96,164,80\nbuffer_load_dwordx4 v220, s[16:19], s63 offen lds\nM 112,160,68\nR 178,190,53248\nM 112,164,84\nbuffer_load_dwordx4 v221, s[16:19], s63 offen offset:1024 lds\n"
"M 128,160,72\nR 180,186,0\nM 128,164,88\nM 144,160,76\nR 182,186,4096\nM 144,164,92\n"
"G 4\nM 148,168,76\nR 160,191,49152\nLq574:\ns_lshr_b32 s60, s45, 3\ns_lshl2_add_u32 s60, s60, s40\n"
"s_lshl_b32 s60, s60, 8\nv_lshl_add_u32 v224, v252, 3, s60\nv_add_u32 v224, Lq574+4096-LqPC, v224\ns_mov_b64 exec, 3\nglobal_load_dword v224, v224, s[98:99]\ns_mov_b64 exec, -1\n"
"M 148,172,92\nv_bfe_u32 v250, v252, 4, 2\nv_lshlrev_b32 v250, 3, v250\nM 132,168,72\nR 162,191,53248\nM 132,172,88\n"
"s_lshl_b32 s60, s43, 5\nv_add_u32 v250, s60, v250\nM 116,168,68\nR 164,187,0\nM 116,172,84\nM 100,168,64\n"
"R 166,187,4096\nM 100,172,80\nG 4\nM 104,176,64\nR 168,184,8192\nM 104,180,80\n"
"M 120,176,68\nR 170,184,12288\nM 120,180,84\nM 136,176,72\nR 172,184,16384\nM 136,180,88\n"
"M 152,176,76\nR 174,184,20480\nM 152,180,92\nG 4\nM 156,160,76\nR 176,185,8192\n"
"M 156,164,92\nM 140,160,72\nR 178,185,12288\nM 140,164,88\nM 124,160,68\nR 180,185,16384\n"
"M 124,164,84\nM 108,160,64\nR 182,185,20480\nM 108,164,80\nG 4\nM 96,168,96\n"
"R 160,186,8192\nM 96,172,112\nM 112,168,100\nR 162,186,12288\nM 112,172,116\nM 128,168,104\n"
"R 164,186,16384\nM 128,172,120\nM 144,168,108\nR 166,186,20480\nM 144,172,124\nG 4\n"
"M 148,176,108\nR 168,187,8192\nM 148,180,124\nM 132,176,104\nR 170,187,12288\nM 132,180,120\n"
"M 116,176,100\nR 172,187,16384\nM 116,180,116\nM 100,176,96\nR 174,187,20480\nv_mov_b32 v226, s46\n"
"M 100,180,112\nG 4\nv_mov_b32 v227, s47\nM 104,160,96\nR 176,184,24576\ns_bfe_u32 s62, s58, 0x20007\n"
"s_lshl_b32 s62, s62, 5\ns_add_u32 s61, s74, 548880\ns_add_u32 s61, s61, s62\ns_waitcnt vmcnt(0)\ns_mov_b64 exec, 1\nbuffer_atomic_swap_x2 v[226:227], off, s[20:23], s61 sc1\n"
"s_mov_b64 exec, -1\ns_barrier\nM 104,164,112\nM 120,160,100\nR 178,184,28672\nM 120,164,116\n"
"s_movk_i32 s63, 6144\nv_mad_u32_u24 v220, v228, s63, v229\nv_add_u32 v221, 3144704, v220\ns_add_u32 m0, s50, 73728\ns_add_u32 s63, s52, 2048\nbuffer_load_dwordx4 v220, s[4:7], s63 offen lds\n"
"M 136,160,104\nR 180,184,32768\nv_mov_b32 v248, 0\nM 136,164,120\nv_mov_b32 v249, 0\nM 152,160,108\n"
"R 182,184,36864\ns_add_u32 s61, s74, 548864\ns_mov_b64 exec, 3\nbuffer_atomic_or_x2 v[248:249], v250, s[20:23], s61 offen sc0 sc1\ns_mov_b64 exec, -1\nM 152,164,124\n"
"G 4\nM 156,168,108\nR 160,185,24576\nM 156,172,124\nM 140,168,104\nR 162,185,28672\n"
"M 140,172,120\nM 124,168,100\nR 164,185,32768\nM 124,172,116\nbuffer_load_dwordx4 v221, s[4:7], s63 offen offset:1024 lds\nM 108,168,96\n"
"R 166,185,36864\ns_add_u32 m0, s50, 81920\nM 108,172,112\nG 4\nM 96,176,128\nR 168,186,24576\n"
"s_add_u32 s63, s52, 198656\nM 96,180,144\nbuffer_load_dwordx4 v220, s[4:7], s63 offen lds\nM 112,176,132\nR 170,186,28672\nM 112,180,148\n"
"M 128,176,136\nR 172,186,32768\ns_lshr_b32 s60, s45, 3\ns_lshl2_add_u32 s60, s60, s40\ns_lshl_b32 s60, s60, 8\nv_lshl_add_u32 v224, v252, 3, s60\n"
"v_add_u32 v224, LqM-LqPC, v224\ns_mov_b64 exec, 3\nglobal_load_dword v224, v224, s[98:99]\ns_mov_b64 exec, -1\nM 128,180,152\nM 144,176,140\n"
"R 174,186,36864\nM 144,180,156\nG 4\nM 148,160,140\nR 176,187,24576\nM 148,164,156\n"
"buffer_load_dwordx4 v221, s[4:7], s63 offen offset:1024 lds\nM 132,160,136\nR 178,187,28672\ns_add_u32 s63, s52, 4096\nM 132,164,152\ns_add_u32 m0, s50, 90112\n"
"M 116,160,132\nR 180,187,32768\nM 116,164,148\nbuffer_load_dwordx4 v220, s[4:7], s63 offen lds\nM 100,160,128\nR 182,187,36864\n"
"M 100,164,144\nG 4\nM 104,168,128\nR 160,184,40960\nM 104,172,144\nM 120,168,132\n"
"R 162,184,45056\nM 120,172,148\nbuffer_load_dwordx4 v221, s[4:7], s63 offen offset:1024 lds\nM 136,168,136\nR 164,184,49152\nM 136,172,152\n"
"s_mul_i32 s64, s43, 49152\nM 152,168,140\nR 166,184,53248\nM 152,172,156\nG 4\nM 156,176,140\n"
"R 168,185,40960\ns_barrier\ns_waitcnt vmcnt(6)\nv_cmp_eq_u64 s[86:87], s[46:47], v[248:249]\nM 156,180,156\ns_add_u32 s64, s64, s72\n"
"M 140,176,136\nR 170,185,45056\ns_and_b32 s62, s86, 3\ns_cmp_eq_u32 s62, 3\ns_cbranch_scc0 Lq499\nLq500:\n"
"v_mov_b32 v248, 0\nM 140,180,152\ns_add_u32 s64, s64, 52428800\nv_mov_b32 v249, 0\nM 124,176,132\nR 172,185,49152\n"
"s_add_u32 s61, s74, 548864\ns_mov_b64 exec, 4\nbuffer_atomic_or_x2 v[248:249], v250, s[20:23], s61 offen sc0 sc1\ns_mov_b64 exec, -1\nM 124,180,148\nbuffer_load_dwordx4 v[0:3], v252, s[20:23], s64 offen offset:0 sc1\n"
"M 108,176,128\nR 174,185,53248\nM 108,180,144\nbuffer_load_dwordx4 v[4:7], v252, s[20:23], s64 offen offset:1024 sc1\nG 4\nM 128,160,168\n"
"R 176,186,40960\ns_mul_i32 s65, s43, 49152\nM 128,164,184\nR 178,186,45056\ns_add_u32 s65, s65, s72\nM 144,160,172\n"
"R 180,186,49152\nbuffer_load_dwordx4 v[8:11], v252, s[20:23], s64 offen offset:2048 sc1\nM 144,164,188\nR 182,186,53248\nG 4\nM 148,168,172\n"
"R 160,187,40960\ns_add_u32 s65, s65, 52445184\nM 148,172,188\nR 162,187,45056\nbuffer_load_dwordx4 v[12:15], v252, s[20:23], s64 offen offset:3072 sc1\nM 132,168,168\n"
"R 164,187,49152\nM 132,172,184\nR 166,187,53248\nG 4\nM 136,176,168\nR 168,184,57344\n"
"buffer_load_dwordx4 v[16:19], v252, s[20:23], s65 offen offset:0 sc1\nM 136,180,184\nR 170,184,61440\nM 152,176,172\nR 172,188,0\nM 152,180,188\n"
"R 174,188,4096\nG 4\nM 156,160,172\nR 176,185,57344\nbuffer_load_dwordx4 v[20:23], v252, s[20:23], s65 offen offset:1024 sc1\nM 156,164,188\n"
"R 178,185,61440\nM 140,160,168\nR 180,189,0\nbuffer_load_dwordx4 v[24:27], v252, s[20:23], s65 offen offset:2048 sc1\nM 140,164,184\nR 182,189,4096\n"
"G 4\nM 128,168,200\nR 160,186,57344\nM 128,172,216\nR 162,186,61440\nbuffer_load_dwordx4 v[28:31], v252, s[20:23], s65 offen offset:3072 sc1\n"
"M 144,168,204\nR 164,190,0\nM 144,172,220\nR 166,190,4096\nG 4\nM 148,176,204\n"
"R 168,187,57344\nM 148,180,220\nR 170,187,61440\ns_add_u32 m0, s50, 98304\nM 132,176,200\nR 172,191,0\n"
"s_add_u32 s63, s52, 200704\nM 132,180,216\nR 174,191,4096\nG 4\nM 136,160,200\nR 176,184,40960\n"
"M 136,164,216\nR 178,184,45056\nM 152,160,204\nR 180,184,49152\ns_mul_i32 s66, s43, 49152\nM 152,164,220\n"
"R 182,184,53248\nbuffer_load_dwordx4 v220, s[4:7], s63 offen lds\nG 4\nM 156,168,204\nR 160,185,40960\ns_add_u32 s66, s66, s72\n"
"M 156,172,220\nR 162,185,45056\ns_add_u32 s66, s66, 52461568\nM 140,168,200\nR 164,185,49152\ns_waitcnt vmcnt(9)\n"
"v_cmp_eq_u64 s[86:87], s[46:47], v[248:249]\nM 140,172,216\nR 166,185,53248\nbuffer_load_dwordx4 v221, s[4:7], s63 offen offset:1024 lds\nG 4\nM 96,176,160\n"
"R 168,186,40960\ns_and_b32 s62, s86, 4\ns_cmp_eq_u32 s62, 4\ns_cbranch_scc0 Lq515\nLq516:\nM 96,180,176\n"
"R 170,186,45056\ns_mov_b32 m0, s51\nM 112,176,164\nR 172,186,49152\nbuffer_load_dwordx4 v[64:67], v252, s[20:23], s66 offen offset:0 sc1\nv_cvt_pk_bf16_f32 v128, v128, v129\n"
"M 112,180,180\nR 174,186,53248\nG 4\nv_cvt_pk_bf16_f32 v129, v130, v131\nM 116,160,164\nR 176,187,40960\n"
"buffer_load_dwordx4 v[68:71], v252, s[20:23], s66 offen offset:1024 sc1\nv_cvt_pk_bf16_f32 v130, v132, v133\nM 116,164,180\nR 178,187,45056\nv_cvt_pk_bf16_f32 v131, v134, v135\nv_cvt_pk_bf16_f32 v132, v136, v137\n"
"M 100,160,160\nR 180,187,49152\nv_cvt_pk_bf16_f32 v133, v138, v139\nv_cvt_pk_bf16_f32 v134, v140, v141\nM 100,164,176\nR 182,187,53248\n"
"G 4\nv_cvt_pk_bf16_f32 v135, v142, v143\nM 104,168,160\nR 160,184,57344\nbuffer_load_dwordx4 v[72:75], v252, s[20:23], s66 offen offset:2048 sc1\nv_cvt_pk_bf16_f32 v136, v144, v145\n"
"M 104,172,176\nR 162,184,61440\nbuffer_load_dwordx4 v[76:79], v252, s[20:23], s66 offen offset:3072 sc1\nv_cvt_pk_bf16_f32 v137, v146, v147\nM 120,168,164\nR 164,188,0\n"
"v_cvt_pk_bf16_f32 v138, v148, v149\nv_cvt_pk_bf16_f32 v139, v150, v151\nM 120,172,180\nR 166,188,4096\nG 4\nv_cvt_pk_bf16_f32 v140, v152, v153\n"
"M 124,176,164\nR 168,185,57344\nv_cvt_pk_bf16_f32 v141, v154, v155\nv_cvt_pk_bf16_f32 v142, v156, v157\nM 124,180,180\nR 170,185,61440\n"
"ds_write_addtid_b32 v128 offset:0\nv_cvt_pk_bf16_f32 v143, v158, v159\nM 108,176,160\nR 172,189,0\nds_write_addtid_b32 v129 offset:256\nds_write_addtid_b32 v130 offset:512\n"
"ds_write_addtid_b32 v131 offset:768\nds_write_addtid_b32 v132 offset:1024\nds_write_addtid_b32 v133 offset:1280\nds_write_addtid_b32 v134 offset:1536\nds_write_addtid_b32 v135 offset:1792\nds_write_addtid_b32 v136 offset:2048\n"
"ds_write_addtid_b32 v137 offset:2304\nds_write_addtid_b32 v138 offset:2560\nds_write_addtid_b32 v139 offset:2816\nds_write_addtid_b32 v140 offset:3072\nds_write_addtid_b32 v141 offset:3328\nds_write_addtid_b32 v142 offset:3584\n"
"ds_write_addtid_b32 v143 offset:3840\nM 108,180,176\nR 174,189,4096\ns_add_u32 m0, s50, 106496\ns_add_u32 s63, s52, 395264\nG 15\n"
"M 96,160,192\nR 176,186,57344\nbuffer_load_dwordx4 v220, s[4:7], s63 offen lds\nM 96,164,208\nR 178,186,61440\nG 3\n"
"s_barrier\nM 112,160,196\nR 180,190,0\nM 112,164,212\nR 182,190,4096\nG 4\n"
"M 116,168,196\nR 160,187,57344\nM 116,172,212\nR 162,187,61440\nbuffer_load_dwordx4 v221, s[4:7], s63 offen offset:1024 lds\nM 100,168,192\n"
"R 164,191,0\ns_add_u32 m0, s50, 114688\ns_add_u32 s63, s52, 591872\nM 100,172,208\nR 166,191,4096\nG 4\n"
"M 104,176,192\nbuffer_load_dwordx4 v220, s[4:7], s63 offen lds\nM 104,180,208\nds_read2st64_b32 v[128:129], v225 offset0:0 offset1:1\nM 120,176,196\nbuffer_load_dwordx4 v221, s[4:7], s63 offen offset:1024 lds\n"
"M 120,180,212\nds_read2st64_b32 v[130:131], v225 offset0:2 offset1:3\nG 2\nM 124,160,196\nds_read2st64_b32 v[132:133], v225 offset0:4 offset1:5\nM 124,164,212\n"
"ds_read2st64_b32 v[134:135], v225 offset0:6 offset1:7\nM 108,160,192\nds_read2st64_b32 v[136:137], v225 offset0:8 offset1:9\nds_read2st64_b32 v[138:139], v225 offset0:10 offset1:11\nM 108,164,208\nds_read2st64_b32 v[140:141], v225 offset0:12 offset1:13\n"
"ds_read2st64_b32 v[142:143], v225 offset0:14 offset1:15\nG 0\ns_barrier\nv_mfma_f32_16x16x32_bf16 v[96:99], v[208:211], v[128:131], v[96:99]\nv_mfma_f32_16x16x32_bf16 v[100:103], v[210:213], v[128:131], v[100:103]\nv_mfma_f32_16x16x32_bf16 v[104:107], v[208:211], v[132:135], v[104:107]\n"
"v_mfma_f32_16x16x32_bf16 v[108:111], v[210:213], v[132:135], v[108:111]\nv_mfma_f32_16x16x32_bf16 v[112:115], v[208:211], v[136:139], v[112:115]\nv_mfma_f32_16x16x32_bf16 v[116:119], v[210:213], v[136:139], v[116:119]\nv_mfma_f32_16x16x32_bf16 v[120:123], v[208:211], v[140:143], v[120:123]\nv_mfma_f32_16x16x32_bf16 v[124:127], v[210:213], v[140:143], v[124:127]\ns_lshl_b32 s84, s75, 11\n"
"s_add_u32 s84, s84, s49\ns_add_u32 s84, s84, 18874368\ns_add_u32 s85, s84, 32768\nv_mov_b32 v238, s46\nv_mov_b32 v239, s47\ns_waitcnt vmcnt(17)\n"
"v_mfma_f32_16x16x32_bf16 v[96:99], v[208:211], v[0:3], v[96:99]\nv_mfma_f32_16x16x32_bf16 v[100:103], v[210:213], v[0:3], v[100:103]\ns_waitcnt vmcnt(16)\nv_mfma_f32_16x16x32_bf16 v[104:107], v[208:211], v[4:7], v[104:107]\nv_mfma_f32_16x16x32_bf16 v[108:111], v[210:213], v[4:7], v[108:111]\ns_waitcnt vmcnt(15)\n"
"v_mfma_f32_16x16x32_bf16 v[112:115], v[208:211], v[8:11], v[112:115]\nv_mfma_f32_16x16x32_bf16 v[116:119], v[210:213], v[8:11], v[116:119]\ns_waitcnt vmcnt(14)\nv_mfma_f32_16x16x32_bf16 v[120:123], v[208:211], v[12:15], v[120:123]\nv_mfma_f32_16x16x32_bf16 v[124:127], v[210:213], v[12:15], v[124:127]\ns_waitcnt vmcnt(13)\n"
"v_mfma_f32_16x16x32_bf16 v[96:99], v[208:211], v[16:19], v[96:99]\nv_mfma_f32_16x16x32_bf16 v[100:103], v[210:213], v[16:19], v[100:103]\ns_waitcnt vmcnt(12)\nv_mfma_f32_16x16x32_bf16 v[104:107], v[208:211], v[20:23], v[104:107]\nv_mfma_f32_16x16x32_bf16 v[108:111], v[210:213], v[20:23], v[108:111]\ns_waitcnt vmcnt(11)\n"
"v_mfma_f32_16x16x32_bf16 v[112:115], v[208:211], v[24:27], v[112:115]\nv_mfma_f32_16x16x32_bf16 v[116:119], v[210:213], v[24:27], v[116:119]\ns_waitcnt vmcnt(10)\nv_mfma_f32_16x16x32_bf16 v[120:123], v[208:211], v[28:31], v[120:123]\nv_mfma_f32_16x16x32_bf16 v[124:127], v[210:213], v[28:31], v[124:127]\ns_waitcnt vmcnt(7)\n"
"v_mfma_f32_16x16x32_bf16 v[96:99], v[208:211], v[64:67], v[96:99]\nv_mfma_f32_16x16x32_bf16 v[100:103], v[210:213], v[64:67], v[100:103]\ns_waitcnt vmcnt(6)\nv_mfma_f32_16x16x32_bf16 v[104:107], v[208:211], v[68:71], v[104:107]\nv_mfma_f32_16x16x32_bf16 v[108:111], v[210:213], v[68:71], v[108:111]\ns_waitcnt vmcnt(5)\n"
"v_mfma_f32_16x16x32_bf16 v[112:115], v[208:211], v[72:75], v[112:115]\nv_mfma_f32_16x16x32_bf16 v[116:119], v[210:213], v[72:75], v[116:119]\ns_waitcnt vmcnt(4)\nv_mfma_f32_16x16x32_bf16 v[120:123], v[208:211], v[76:79], v[120:123]\nv_mfma_f32_16x16x32_bf16 v[124:127], v[210:213], v[76:79], v[124:127]\nv_cvt_pk_bf16_f32 v192, v96, v97\n"
"v_cvt_pk_bf16_f32 v193, v98, v99\nv_cvt_pk_bf16_f32 v194, v100, v101\nv_cvt_pk_bf16_f32 v195, v102, v103\nbuffer_store_dwordx4 v[192:195], v252, s[20:23], s84 offen offset:0 sc1\nv_cvt_pk_bf16_f32 v196, v104, v105\nv_cvt_pk_bf16_f32 v197, v106, v107\n"
"v_cvt_pk_bf16_f32 v198, v108, v109\nv_cvt_pk_bf16_f32 v199, v110, v111\nbuffer_store_dwordx4 v[196:199], v252, s[20:23], s84 offen offset:1024 sc1\nv_cvt_pk_bf16_f32 v200, v112, v113\nv_cvt_pk_bf16_f32 v201, v114, v115\nv_cvt_pk_bf16_f32 v202, v116, v117\n"
"v_cvt_pk_bf16_f32 v203, v118, v119\nbuffer_store_dwordx4 v[200:203], v252, s[20:23], s85 offen offset:0 sc1\nv_cvt_pk_bf16_f32 v204, v120, v121\nv_cvt_pk_bf16_f32 v205, v122, v123\nv_cvt_pk_bf16_f32 v206, v124, v125\nv_cvt_pk_bf16_f32 v207, v126, v127\n"
"buffer_store_dwordx4 v[204:207], v252, s[20:23], s85 offen offset:1024 sc1\ns_add_u32 s61, s79, 88358912\ns_lshl_b32 s62, s75, 3\ns_add_u32 s61, s61, s62\ns_waitcnt vmcnt(0)\ns_mov_b64 exec, 1\n"
"buffer_atomic_swap_x2 v[238:239], off, s[20:23], s61 sc1\ns_mov_b64 exec, -1\ns_branch LqM\nLq116:\ns_lshl_b32 s61, s42, 13\ns_add_u32 s61, s61, 84017152\n"
"buffer_load_dwordx4 v[128:131], v230, s[20:23], s61 offen sc1\ns_lshl_b32 s61, s42, 13\ns_add_u32 s61, s61, 84021248\nbuffer_load_dwordx4 v[132:135], v230, s[20:23], s61 offen sc1\ns_waitcnt vmcnt(0)\nv_min3_u32 v232, v129, v131, v133\n"
"v_min3_u32 v232, v232, v135, v135\nv_max3_u32 v233, v129, v131, v133\nv_max3_u32 v233, v233, v135, v135\nv_cmp_ne_u32 s[86:87], s46, v232\nv_cmp_ne_u32 s[88:89], s46, v233\ns_nop 0\n"
"s_or_b64 s[86:87], s[86:87], s[88:89]\ns_cbranch_scc0 Lq117\ns_sub_u32 s48, s48, 1\ns_cmp_gt_i32 s48, 0\ns_cbranch_scc1 Lq116\ns_branch Lq117\n"
"Lq124:\ns_lshl_b32 s61, s42, 13\ns_add_u32 s61, s61, 84148224\nbuffer_load_dwordx4 v[144:147], v234, s[20:23], s61 offen sc1\ns_lshl_b32 s61, s42, 13\ns_add_u32 s61, s61, 84152320\n"
"buffer_load_dwordx4 v[148:151], v234, s[20:23], s61 offen sc1\ns_waitcnt vmcnt(0)\nv_min3_u32 v236, v145, v147, v149\nv_min3_u32 v236, v236, v151, v151\nv_max3_u32 v237, v145, v147, v149\nv_max3_u32 v237, v237, v151, v151\n"
"v_cmp_ne_u32 s[86:87], s46, v236\nv_cmp_ne_u32 s[88:89], s46, v237\ns_nop 0\ns_or_b64 s[86:87], s[86:87], s[88:89]\ns_cbranch_scc0 Lq125\ns_sub_u32 s48, s48, 1\n"
"s_cmp_gt_i32 s48, 0\ns_cbranch_scc1 Lq124\ns_branch Lq125\nLq132:\ns_lshl_b32 s61, s42, 13\ns_add_u32 s61, s61, 83886080\n"
"buffer_load_dwordx4 v[128:131], v230, s[20:23], s61 offen sc1\ns_lshl_b32 s61, s42, 13\ns_add_u32 s61, s61, 83890176\nbuffer_load_dwordx4 v[132:135], v230, s[20:23], s61 offen sc1\ns_waitcnt vmcnt(0)\nv_min3_u32 v232, v129, v131, v133\n"
"v_min3_u32 v232, v232, v135, v135\nv_max3_u32 v233, v129, v131, v133\nv_max3_u32 v233, v233, v135, v135\nv_cmp_ne_u32 s[86:87], s46, v232\nv_cmp_ne_u32 s[88:89], s46, v233\ns_nop 0\n"
"s_or_b64 s[86:87], s[86:87], s[88:89]\ns_cbranch_scc0 Lq133\ns_sub_u32 s48, s48, 1\ns_cmp_gt_i32 s48, 0\ns_cbranch_scc1 Lq132\ns_branch Lq133\n"
"Lq140:\ns_lshr_b32 s84, s49, 3\ns_add_u32 s84, s84, 81788928\nbuffer_load_dwordx4 v[128:131], v252, s[20:23], s84 offen offset:0 sc1\nbuffer_load_dwordx4 v[132:135], v252, s[20:23], s84 offen offset:1024 sc1\nbuffer_load_dwordx4 v[136:139], v252, s[20:23], s84 offen offset:2048 sc1\n"
"buffer_load_dwordx4 v[140:143], v252, s[20:23], s84 offen offset:3072 sc1\ns_lshr_b32 s85, s49, 3\ns_add_u32 s85, s85, 81793024\nbuffer_load_dwordx4 v[144:147], v252, s[20:23], s85 offen offset:0 sc1\nbuffer_load_dwordx4 v[148:151], v252, s[20:23], s85 offen offset:1024 sc1\nbuffer_load_dwordx4 v[152:155], v252, s[20:23], s85 offen offset:2048 sc1\n"
"buffer_load_dwordx4 v[156:159], v252, s[20:23], s85 offen offset:3072 sc1\ns_waitcnt vmcnt(6)\nv_min3_u32 v230, v129, v131, v133\ns_waitcnt vmcnt(5)\nv_min3_u32 v230, v230, v135, v137\ns_waitcnt vmcnt(4)\n"
"v_min3_u32 v230, v230, v139, v141\ns_waitcnt vmcnt(3)\nv_min3_u32 v230, v230, v143, v145\ns_waitcnt vmcnt(2)\nv_min3_u32 v230, v230, v147, v149\ns_waitcnt vmcnt(1)\n"
"v_min3_u32 v230, v230, v151, v153\ns_waitcnt vmcnt(0)\nv_min3_u32 v230, v230, v155, v157\nv_min3_u32 v230, v230, v159, v159\nv_max3_u32 v231, v129, v131, v133\nv_max3_u32 v231, v231, v135, v137\n"
"v_max3_u32 v231, v231, v139, v141\nv_max3_u32 v231, v231, v143, v145\nv_max3_u32 v231, v231, v147, v149\nv_max3_u32 v231, v231, v151, v153\nv_max3_u32 v231, v231, v155, v157\nv_max3_u32 v231, v231, v159, v159\n"
"v_cmp_ne_u32 s[86:87], s46, v230\nv_cmp_ne_u32 s[88:89], s46, v231\ns_nop 0\ns_or_b64 s[86:87], s[86:87], s[88:89]\ns_cbranch_scc0 Lq141\ns_sub_u32 s48, s48, 1\n"
"s_cmp_gt_i32 s48, 0\ns_cbranch_scc1 Lq140\ns_branch Lq141\nLq190:\ns_mov_b64 exec, s[66:67]\nbuffer_load_dwordx4 v[230:233], v252, s[20:23], s65 offen offset:0 sc1\n"
"buffer_load_dwordx2 v[64:65], v227, s[20:23], s65 offen offset:2048 sc1\nbuffer_load_dwordx2 v[70:71], v227, s[20:23], s65 offen offset:2560 sc1\ns_mov_b64 exec, -1\ns_waitcnt vmcnt(0)\nv_max3_u32 v248, v233, v65, v71\nv_cmp_eq_u32 vcc, -1, v248\n"
"s_nop 1\ns_cbranch_vccz Lq191\ns_sub_u32 s48, s48, 1\ns_cmp_gt_i32 s48, 0\ns_cbranch_scc1 Lq190\ns_branch Lq191\n"
"Lq192:\ns_mov_b64 exec, s[66:67]\nbuffer_load_dwordx4 v[234:237], v252, s[20:23], s65 offen offset:1024 sc1\nbuffer_load_dwordx2 v[76:77], v227, s[20:23], s65 offen offset:3072 sc1\nbuffer_load_dwordx2 v[82:83], v227, s[20:23], s65 offen offset:3584 sc1\ns_mov_b64 exec, -1\n"
"s_waitcnt vmcnt(0)\nv_max3_u32 v248, v237, v77, v83\nv_cmp_eq_u32 vcc, -1, v248\ns_nop 1\ns_cbranch_vccz Lq193\ns_sub_u32 s48, s48, 1\n"
"s_cmp_gt_i32 s48, 0\ns_cbranch_scc1 Lq192\ns_branch Lq193\nLq195:\nv_mov_b32 v248, 0\nv_mov_b32 v249, 0\n"
"s_add_u32 s61, s80, 88080384\ns_mov_b64 exec, 0xffff\nbuffer_atomic_or_x2 v[248:249], v250, s[20:23], s61 offen sc0 sc1\ns_mov_b64 exec, -1\ns_waitcnt vmcnt(0)\nv_cmp_eq_u64 s[86:87], s[46:47], v[248:249]\n"
"s_nop 1\ns_and_b32 s62, s86, 0xffff\ns_cmp_eq_u32 s62, 0xffff\ns_cbranch_scc1 Lq196\ns_sleep 1\ns_sub_u32 s48, s48, 1\n"
"s_cmp_gt_i32 s48, 0\ns_cbranch_scc1 Lq195\ns_branch Lq196\nLq249:\nv_mov_b32 v248, 0\nv_mov_b32 v249, 0\n"
"s_add_u32 s61, s80, 88219648\ns_mov_b64 exec, 0xffff\nbuffer_atomic_or_x2 v[248:249], v250, s[20:23], s61 offen sc0 sc1\ns_mov_b64 exec, -1\ns_waitcnt vmcnt(0)\nv_cmp_eq_u64 s[86:87], s[46:47], v[248:249]\n"
"s_nop 1\ns_and_b32 s62, s86, 0xffff\ns_cmp_eq_u32 s62, 0xffff\ns_cbranch_scc1 Lq250\ns_sleep 1\ns_sub_u32 s48, s48, 1\n"
"s_cmp_gt_i32 s48, 0\ns_cbranch_scc1 Lq249\ns_branch Lq250\nLq301:\ns_lshr_b32 s84, s49, 3\ns_add_u32 s84, s84, 82313216\n"
"buffer_load_dwordx4 v[0:3], v252, s[20:23], s84 offen offset:0 sc1\nbuffer_load_dwordx4 v[4:7], v252, s[20:23], s84 offen offset:1024 sc1\nbuffer_load_dwordx4 v[8:11], v252, s[20:23], s84 offen offset:2048 sc1\nbuffer_load_dwordx4 v[12:15], v252, s[20:23], s84 offen offset:3072 sc1\ns_lshr_b32 s85, s49, 3\ns_add_u32 s85, s85, 82317312\n"
"buffer_load_dwordx4 v[16:19], v252, s[20:23], s85 offen offset:0 sc1\nbuffer_load_dwordx4 v[20:23], v252, s[20:23], s85 offen offset:1024 sc1\nbuffer_load_dwordx4 v[24:27], v252, s[20:23], s85 offen offset:2048 sc1\nbuffer_load_dwordx4 v[28:31], v252, s[20:23], s85 offen offset:3072 sc1\ns_waitcnt vmcnt(6)\nv_min3_u32 v230, v1, v3, v5\n"
"s_waitcnt vmcnt(5)\nv_min3_u32 v230, v230, v7, v9\ns_waitcnt vmcnt(4)\nv_min3_u32 v230, v230, v11, v13\ns_waitcnt vmcnt(3)\nv_min3_u32 v230, v230, v15, v17\n"
"s_waitcnt vmcnt(2)\nv_min3_u32 v230, v230, v19, v21\ns_waitcnt vmcnt(1)\nv_min3_u32 v230, v230, v23, v25\ns_waitcnt vmcnt(0)\nv_min3_u32 v230, v230, v27, v29\n"
"v_min3_u32 v230, v230, v31, v31\nv_max3_u32 v231, v1, v3, v5\nv_max3_u32 v231, v231, v7, v9\nv_max3_u32 v231, v231, v11, v13\nv_max3_u32 v231, v231, v15, v17\nv_max3_u32 v231, v231, v19, v21\n"
"v_max3_u32 v231, v231, v23, v25\nv_max3_u32 v231, v231, v27, v29\nv_max3_u32 v231, v231, v31, v31\nv_cmp_ne_u32 s[86:87], s46, v230\nv_cmp_ne_u32 s[88:89], s46, v231\ns_nop 0\n"
"s_or_b64 s[86:87], s[86:87], s[88:89]\ns_cbranch_scc0 Lq302\ns_sub_u32 s48, s48, 1\ns_cmp_gt_i32 s48, 0\ns_cbranch_scc1 Lq301\ns_branch Lq302\n"
"Lq303:\ns_lshl_b32 s61, s42, 13\ns_add_u32 s61, s61, 84279296\nbuffer_load_dwordx4 v[0:3], v238, s[20:23], s61 offen sc1\ns_lshl_b32 s61, s42, 13\ns_add_u32 s61, s61, 84283392\n"
"buffer_load_dwordx4 v[4:7], v238, s[20:23], s61 offen sc1\ns_waitcnt vmcnt(0)\nv_min3_u32 v240, v1, v3, v5\nv_min3_u32 v240, v240, v7, v7\nv_max3_u32 v241, v1, v3, v5\nv_max3_u32 v241, v241, v7, v7\n"
"v_cmp_ne_u32 s[86:87], s46, v240\nv_cmp_ne_u32 s[88:89], s46, v241\ns_nop 0\ns_or_b64 s[86:87], s[86:87], s[88:89]\ns_cbranch_scc0 Lq304\ns_sub_u32 s48, s48, 1\n"
"s_cmp_gt_i32 s48, 0\ns_cbranch_scc1 Lq303\ns_branch Lq304\nLq313:\ns_lshl_b32 s61, s42, 13\ns_add_u32 s61, s61, 84410368\n"
"buffer_load_dwordx4 v[16:19], v242, s[20:23], s61 offen sc1\ns_lshl_b32 s61, s42, 13\ns_add_u32 s61, s61, 84414464\nbuffer_load_dwordx4 v[20:23], v242, s[20:23], s61 offen sc1\ns_waitcnt vmcnt(0)\nv_min3_u32 v244, v17, v19, v21\n"
"v_min3_u32 v244, v244, v23, v23\nv_max3_u32 v245, v17, v19, v21\nv_max3_u32 v245, v245, v23, v23\nv_cmp_ne_u32 s[86:87], s46, v244\nv_cmp_ne_u32 s[88:89], s46, v245\ns_nop 0\n"
"s_or_b64 s[86:87], s[86:87], s[88:89]\ns_cbranch_scc0 Lq314\ns_sub_u32 s48, s48, 1\ns_cmp_gt_i32 s48, 0\ns_cbranch_scc1 Lq313\ns_branch Lq314\n"
"Lq323:\ns_lshl_b32 s61, s42, 13\ns_add_u32 s61, s61, 84541440\nbuffer_load_dwordx4 v[0:3], v238, s[20:23], s61 offen sc1\ns_lshl_b32 s61, s42, 13\ns_add_u32 s61, s61, 84545536\n"
"buffer_load_dwordx4 v[4:7], v238, s[20:23], s61 offen sc1\ns_waitcnt vmcnt(0)\nv_min3_u32 v240, v1, v3, v5\nv_min3_u32 v240, v240, v7, v7\nv_max3_u32 v241, v1, v3, v5\nv_max3_u32 v241, v241, v7, v7\n"
"v_cmp_ne_u32 s[86:87], s46, v240\nv_cmp_ne_u32 s[88:89], s46, v241\ns_nop 0\ns_or_b64 s[86:87], s[86:87], s[88:89]\ns_cbranch_scc0 Lq324\ns_sub_u32 s48, s48, 1\n"
"s_cmp_gt_i32 s48, 0\ns_cbranch_scc1 Lq323\ns_branch Lq324\nLq333:\ns_lshl_b32 s61, s42, 13\ns_add_u32 s61, s61, 84672512\n"
"buffer_load_dwordx4 v[16:19], v242, s[20:23], s61 offen sc1\ns_lshl_b32 s61, s42, 13\ns_add_u32 s61, s61, 84676608\nbuffer_load_dwordx4 v[20:23], v242, s[20:23], s61 offen sc1\ns_waitcnt vmcnt(0)\nv_min3_u32 v244, v17, v19, v21\n"
"v_min3_u32 v244, v244, v23, v23\nv_max3_u32 v245, v17, v19, v21\nv_max3_u32 v245, v245, v23, v23\nv_cmp_ne_u32 s[86:87], s46, v244\nv_cmp_ne_u32 s[88:89], s46, v245\ns_nop 0\n"
"s_or_b64 s[86:87], s[86:87], s[88:89]\ns_cbranch_scc0 Lq334\ns_sub_u32 s48, s48, 1\ns_cmp_gt_i32 s48, 0\ns_cbranch_scc1 Lq333\ns_branch Lq334\n"
"Lq367:\nv_mov_b32 v248, 0\nv_mov_b32 v249, 0\ns_add_u32 s61, s82, 385024\ns_mov_b64 exec, 0xf\nbuffer_atomic_or_x2 v[248:249], v250, s[20:23], s61 offen sc0 sc1\n"
"s_mov_b64 exec, -1\ns_waitcnt vmcnt(0)\nv_cmp_eq_u64 s[86:87], s[46:47], v[248:249]\ns_nop 1\ns_and_b32 s62, s86, 0xf\ns_cmp_eq_u32 s62, 0xf\n"
"s_cbranch_scc1 Lq368\ns_sleep 1\ns_sub_u32 s48, s48, 1\ns_cmp_gt_i32 s48, 0\ns_cbranch_scc1 Lq367\ns_branch Lq368\n"
"Lq402:\nv_mov_b32 v248, 0\nv_mov_b32 v249, 0\ns_add_u32 s61, s82, 385032\ns_mov_b64 exec, 0xf\nbuffer_atomic_or_x2 v[248:249], v250, s[20:23], s61 offen sc0 sc1\n"
"s_mov_b64 exec, -1\ns_waitcnt vmcnt(0)\nv_cmp_eq_u64 s[86:87], s[46:47], v[248:249]\ns_nop 1\ns_and_b32 s62, s86, 0xf\ns_cmp_eq_u32 s62, 0xf\n"
"s_cbranch_scc1 Lq403\ns_sleep 1\ns_sub_u32 s48, s48, 1\ns_cmp_gt_i32 s48, 0\ns_cbranch_scc1 Lq402\ns_branch Lq403\n"
"Lq437:\nv_mov_b32 v248, 0\nv_mov_b32 v249, 0\ns_add_u32 s61, s82, 385048\ns_mov_b64 exec, 0xf\nbuffer_atomic_or_x2 v[248:249], v250, s[20:23], s61 offen sc0 sc1\n"
"s_mov_b64 exec, -1\ns_waitcnt vmcnt(0)\nv_cmp_eq_u64 s[86:87], s[46:47], v[248:249]\ns_nop 1\ns_and_b32 s62, s86, 0xf\ns_cmp_eq_u32 s62, 0xf\n"
"s_cbranch_scc1 Lq438\ns_sleep 1\ns_sub_u32 s48, s48, 1\ns_cmp_gt_i32 s48, 0\ns_cbranch_scc1 Lq437\ns_branch Lq438\n"
"Lq456:\nv_mov_b32 v248, 0\nv_mov_b32 v249, 0\ns_add_u32 s61, s82, 385040\ns_mov_b64 exec, 0xf\nbuffer_atomic_or_x2 v[248:249], v250, s[20:23], s61 offen sc0 sc1\n"
"s_mov_b64 exec, -1\ns_waitcnt vmcnt(0)\nv_cmp_eq_u64 s[86:87], s[46:47], v[248:249]\ns_nop 1\ns_and_b32 s62, s86, 0xf\ns_cmp_eq_u32 s62, 0xf\n"
"s_cbranch_scc1 Lq457\ns_sleep 1\ns_sub_u32 s48, s48, 1\ns_cmp_gt_i32 s48, 0\ns_cbranch_scc1 Lq456\ns_branch Lq457\n"
"Lq499:\nv_mov_b32 v248, 0\nv_mov_b32 v249, 0\ns_add_u32 s61, s74, 548864\ns_mov_b64 exec, 3\nbuffer_atomic_or_x2 v[248:249], v250, s[20:23], s61 offen sc0 sc1\n"
"s_mov_b64 exec, -1\ns_waitcnt vmcnt(0)\nv_cmp_eq_u64 s[86:87], s[46:47], v[248:249]\ns_nop 1\ns_and_b32 s62, s86, 3\ns_cmp_eq_u32 s62, 3\n"
"s_cbranch_scc1 Lq500\ns_sleep 1\ns_sub_u32 s48, s48, 1\ns_cmp_gt_i32 s48, 0\ns_cbranch_scc1 Lq499\ns_branch Lq500\n"
"Lq515:\nv_mov_b32 v248, 0\nv_mov_b32 v249, 0\ns_add_u32 s61, s74, 548864\ns_mov_b64 exec, 4\nbuffer_atomic_or_x2 v[248:249], v250, s[20:23], s61 offen sc0 sc1\n"
"s_mov_b64 exec, -1\ns_waitcnt vmcnt(0)\nv_cmp_eq_u64 s[86:87], s[46:47], v[248:249]\ns_nop 1\ns_and_b32 s62, s86, 4\ns_cmp_eq_u32 s62, 4\n"
"s_cbranch_scc1 Lq516\ns_sleep 1\ns_sub_u32 s48, s48, 1\ns_cmp_gt_i32 s48, 0\ns_cbranch_scc1 Lq515\ns_branch Lq516\n"
".p2align 6\nLqM:\nLqq1:\nv_bfe_u32 v250, v252, 7, 1\nv_mul_u32_u24 v250, 2176, v250\nv_bfe_u32 v251, v252, 4, 3\n"
"v_lshl_add_u32 v250, v251, 3, v250\ns_lshl_b32 s60, s90, 6\nv_add_u32 v250, s60, v250\ns_sleep 5\nv_mov_b32 v248, 0\nv_mov_b32 v249, 0\n"
"s_add_u32 s61, s80, 88358912\ns_mov_b64 exec, 0xffff\nbuffer_atomic_or_x2 v[248:249], v250, s[20:23], s61 offen sc0 sc1\ns_mov_b64 exec, -1\ns_add_u32 m0, s50, 0\ns_add_u32 s63, s52, 397312\n"
"buffer_load_dwordx4 v220, s[4:7], s63 offen lds\nbuffer_load_dwordx4 v221, s[4:7], s63 offen offset:1024 lds\ns_add_u32 m0, s50, 8192\ns_add_u32 s63, s52, 593920\nbuffer_load_dwordx4 v220, s[4:7], s63 offen lds\nbuffer_load_dwordx4 v221, s[4:7], s63 offen offset:1024 lds\n"
"s_add_u32 m0, s50, 16384\ns_add_u32 s63, s52, 788480\nbuffer_load_dwordx4 v220, s[4:7], s63 offen lds\nbuffer_load_dwordx4 v221, s[4:7], s63 offen offset:1024 lds\ns_add_u32 m0, s50, 24576\ns_add_u32 s63, s52, 985088\n"
"buffer_load_dwordx4 v220, s[4:7], s63 offen lds\nbuffer_load_dwordx4 v221, s[4:7], s63 offen offset:1024 lds\ns_add_u32 m0, s50, 32768\ns_add_u32 s63, s52, 790528\nbuffer_load_dwordx4 v220, s[4:7], s63 offen lds\nbuffer_load_dwordx4 v221, s[4:7], s63 offen offset:1024 lds\n"
"s_add_u32 m0, s50, 40960\ns_add_u32 s63, s52, 987136\nbuffer_load_dwordx4 v220, s[4:7], s63 offen lds\nbuffer_load_dwordx4 v221, s[4:7], s63 offen offset:1024 lds\nv_mov_b32 v230, 0\nv_mov_b32 v231, 0\n"
"v_mov_b32 v232, 0\nv_mov_b32 v233, 0\nv_dot2c_f32_bf16 v230, s76, v192\nv_dot2c_f32_bf16 v231, v192, v192\nv_dot2c_f32_bf16 v232, s76, v200\nv_dot2c_f32_bf16 v233, v200, v200\n"
"v_dot2c_f32_bf16 v230, s76, v193\nv_dot2c_f32_bf16 v231, v193, v193\nv_dot2c_f32_bf16 v232, s76, v201\nv_dot2c_f32_bf16 v233, v201, v201\nv_dot2c_f32_bf16 v230, s76, v194\nv_dot2c_f32_bf16 v231, v194, v194\n"
"v_dot2c_f32_bf16 v232, s76, v202\nv_dot2c_f32_bf16 v233, v202, v202\nv_dot2c_f32_bf16 v230, s76, v195\nv_dot2c_f32_bf16 v231, v195, v195\nv_dot2c_f32_bf16 v232, s76, v203\nv_dot2c_f32_bf16 v233, v203, v203\n"
"v_dot2c_f32_bf16 v230, s76, v196\nv_dot2c_f32_bf16 v231, v196, v196\nv_dot2c_f32_bf16 v232, s76, v204\nv_dot2c_f32_bf16 v233, v204, v204\nv_dot2c_f32_bf16 v230, s76, v197\nv_dot2c_f32_bf16 v231, v197, v197\n"
"v_dot2c_f32_bf16 v232, s76, v205\nv_dot2c_f32_bf16 v233, v205, v205\nv_dot2c_f32_bf16 v230, s76, v198\nv_dot2c_f32_bf16 v231, v198, v198\nv_dot2c_f32_bf16 v232, s76, v206\nv_dot2c_f32_bf16 v233, v206, v206\n"
"v_dot2c_f32_bf16 v230, s76, v199\nv_dot2c_f32_bf16 v231, v199, v199\nv_dot2c_f32_bf16 v232, s76, v207\nv_dot2c_f32_bf16 v233, v207, v207\nv_lshrrev_b32 v234, 1, v252\nv_permlane32_swap_b32 v230, v231\n"
"s_nop 0\nv_permlane32_swap_b32 v232, v233\nv_add_f32 v230, v230, v231\nv_add_f32 v232, v232, v233\nv_mov_b32 v231, s46\ns_nop 0\n"
"v_permlane16_swap_b32 v230, v232\nv_add_f32 v230, v230, v232\ns_lshr_b32 s61, s49, 3\ns_lshl2_add_u32 s61, s53, s61\ns_add_u32 s61, s61, 82837504\nbuffer_store_dwordx2 v[230:231], v234, s[20:23], s61 offen sc1\n"
"s_waitcnt vmcnt(13)\nv_cmp_eq_u64 s[86:87], s[46:47], v[248:249]\ns_nop 0\ns_and_b32 s62, s86, 0xffff\ns_cmp_eq_u32 s62, 0xffff\ns_cbranch_scc0 Lq579\n"
"Lq580:\ns_add_u32 s64, s70, 18874368\nbuffer_load_dwordx4 a[0:3], v252, s[20:23], s64 offen offset:0 sc1\ns_add_u32 s65, s70, 18907136\nbuffer_load_dwordx4 a[4:7], v252, s[20:23], s65 offen offset:0 sc1\ns_add_u32 s66, s71, 18874368\n"
"buffer_load_dwordx4 a[8:11], v252, s[20:23], s66 offen offset:0 sc1\ns_add_u32 s67, s71, 18907136\nbuffer_load_dwordx4 a[12:15], v252, s[20:23], s67 offen offset:0 sc1\nbuffer_load_dwordx4 a[16:19], v252, s[20:23], s64 offen offset:1024 sc1\nbuffer_load_dwordx4 a[20:23], v252, s[20:23], s65 offen offset:1024 sc1\nbuffer_load_dwordx4 a[24:27], v252, s[20:23], s66 offen offset:1024 sc1\n"
"buffer_load_dwordx4 a[28:31], v252, s[20:23], s67 offen offset:1024 sc1\ns_barrier\nR 160,188,8192\nR 162,188,12288\nR 164,188,16384\nR 166,188,20480\n"
"R 168,189,8192\nR 170,189,12288\nR 172,189,16384\nR 174,189,20480\nR 176,190,8192\nR 178,190,12288\n"
"R 180,190,16384\nR 182,190,20480\nbuffer_load_dwordx4 a[32:35], v252, s[20:23], s64 offen offset:2048 sc1\nbuffer_load_dwordx4 a[36:39], v252, s[20:23], s65 offen offset:2048 sc1\nbuffer_load_dwordx4 a[40:43], v252, s[20:23], s66 offen offset:2048 sc1\nbuffer_load_dwordx4 a[44:47], v252, s[20:23], s67 offen offset:2048 sc1\n"
"buffer_load_dwordx4 a[48:51], v252, s[20:23], s64 offen offset:3072 sc1\nbuffer_load_dwordx4 a[52:55], v252, s[20:23], s65 offen offset:3072 sc1\nbuffer_load_dwordx4 a[56:59], v252, s[20:23], s66 offen offset:3072 sc1\nbuffer_load_dwordx4 a[60:63], v252, s[20:23], s67 offen offset:3072 sc1\ns_waitcnt vmcnt(15) lgkmcnt(10)\nv_mfma_f32_16x16x32_bf16 v[0:3], v[160:163], a[0:3], 0\n"
"s_add_u32 s64, s70, 18878464\nbuffer_load_dwordx4 a[64:67], v252, s[20:23], s64 offen offset:0 sc1\ns_waitcnt vmcnt(12) lgkmcnt(8)\nM 0,164,16\nv_mfma_f32_16x16x32_bf16 v[16:19], v[160:163], a[4:7], 0\ns_waitcnt vmcnt(11)\n"
"M 16,164,20\nv_mfma_f32_16x16x32_bf16 v[32:35], v[160:163], a[8:11], 0\ns_waitcnt vmcnt(10)\nM 32,164,24\nv_mfma_f32_16x16x32_bf16 v[48:51], v[160:163], a[12:15], 0\ns_add_u32 s65, s70, 18911232\n"
"buffer_load_dwordx4 a[68:71], v252, s[20:23], s65 offen offset:0 sc1\ns_waitcnt vmcnt(10)\nM 48,164,28\nG 4\nv_mfma_f32_16x16x32_bf16 v[52:55], v[168:171], a[12:15], 0\nR 160,191,8192\n"
"M 52,172,28\nv_mfma_f32_16x16x32_bf16 v[36:39], v[168:171], a[8:11], 0\nR 162,191,12288\nM 36,172,24\nv_mfma_f32_16x16x32_bf16 v[20:23], v[168:171], a[4:7], 0\nR 164,191,16384\n"
"M 20,172,20\ns_add_u32 s66, s71, 18878464\nbuffer_load_dwordx4 a[72:75], v252, s[20:23], s66 offen offset:0 sc1\nv_mfma_f32_16x16x32_bf16 v[4:7], v[168:171], a[0:3], 0\nR 166,191,20480\nM 4,172,16\n"
"G 4\nv_mfma_f32_16x16x32_bf16 v[8:11], v[176:179], a[0:3], 0\nR 168,188,24576\nM 8,180,16\nv_mfma_f32_16x16x32_bf16 v[24:27], v[176:179], a[4:7], 0\nR 170,188,28672\n"
"M 24,180,20\ns_add_u32 m0, s50, 49152\nv_mfma_f32_16x16x32_bf16 v[40:43], v[176:179], a[8:11], 0\nR 172,188,32768\ns_add_u32 s63, s52, 1181696\nM 40,180,24\n"
"s_add_u32 s67, s71, 18911232\nbuffer_load_dwordx4 a[76:79], v252, s[20:23], s67 offen offset:0 sc1\nv_mfma_f32_16x16x32_bf16 v[56:59], v[176:179], a[12:15], 0\nR 174,188,36864\nM 56,180,28\nG 4\n"
"v_mfma_f32_16x16x32_bf16 v[60:63], v[160:163], a[12:15], 0\nR 176,189,24576\nM 60,164,28\nbuffer_load_dwordx4 v220, s[4:7], s63 offen lds\nv_mfma_f32_16x16x32_bf16 v[44:47], v[160:163], a[8:11], 0\nR 178,189,28672\n"
"M 44,164,24\nbuffer_load_dwordx4 a[80:83], v252, s[20:23], s64 offen offset:1024 sc1\nv_mfma_f32_16x16x32_bf16 v[28:31], v[160:163], a[4:7], 0\nR 180,189,32768\nM 28,164,20\nv_mfma_f32_16x16x32_bf16 v[12:15], v[160:163], a[0:3], 0\n"
"R 182,189,36864\nM 12,164,16\nG 4\nv_mfma_f32_16x16x32_bf16 v[64:67], a[0:3], v[168:171], 0\nR 160,190,24576\nN 64,16,172\n"
"buffer_load_dwordx4 a[84:87], v252, s[20:23], s65 offen offset:1024 sc1\nv_mfma_f32_16x16x32_bf16 v[68:71], a[4:7], v[168:171], 0\nR 162,190,28672\nN 68,20,172\nv_mfma_f32_16x16x32_bf16 v[96:99], a[8:11], v[168:171], 0\nR 164,190,32768\n"
"N 96,24,172\nv_mfma_f32_16x16x32_bf16 v[100:103], a[12:15], v[168:171], 0\nR 166,190,36864\nN 100,28,172\ns_barrier\nG 4\n"
"v_mfma_f32_16x16x32_bf16 v[108:111], a[12:15], v[176:179], 0\nR 168,191,24576\nN 108,28,180\nbuffer_load_dwordx4 a[88:91], v252, s[20:23], s66 offen offset:1024 sc1\nv_mfma_f32_16x16x32_bf16 v[104:107], a[8:11], v[176:179], 0\nR 170,191,28672\n"
"N 104,24,180\nv_mfma_f32_16x16x32_bf16 v[76:79], a[4:7], v[176:179], 0\nR 172,191,32768\nN 76,20,180\nbuffer_load_dwordx4 v221, s[4:7], s63 offen offset:1024 lds\nv_mfma_f32_16x16x32_bf16 v[72:75], a[0:3], v[176:179], 0\n"
"R 174,191,36864\nN 72,16,180\nG 4\nv_mfma_f32_16x16x32_bf16 v[80:83], a[0:3], v[160:163], 0\nR 176,188,40960\ns_add_u32 s63, s52, 1378304\n"
"N 80,16,164\nbuffer_load_dwordx4 a[92:95], v252, s[20:23], s67 offen offset:1024 sc1\nv_mfma_f32_16x16x32_bf16 v[84:87], a[4:7], v[160:163], 0\nR 178,188,45056\nN 84,20,164\ns_add_u32 m0, s50, 57344\n"
"v_mfma_f32_16x16x32_bf16 v[112:115], a[8:11], v[160:163], 0\nR 180,188,49152\nN 112,24,164\nbuffer_load_dwordx4 v220, s[4:7], s63 offen lds\nv_mfma_f32_16x16x32_bf16 v[116:119], a[12:15], v[160:163], 0\nR 182,188,53248\n"
"N 116,28,164\nG 4\nv_mfma_f32_16x16x32_bf16 v[124:127], a[12:15], v[168:171], 0\nR 160,189,40960\nN 124,28,172\nbuffer_load_dwordx4 a[96:99], v252, s[20:23], s64 offen offset:2048 sc1\n"
"v_mfma_f32_16x16x32_bf16 v[120:123], a[8:11], v[168:171], 0\nR 162,189,45056\nN 120,24,172\nv_mfma_f32_16x16x32_bf16 v[92:95], a[4:7], v[168:171], 0\nR 164,189,49152\nN 92,20,172\n"
"buffer_load_dwordx4 a[100:103], v252, s[20:23], s65 offen offset:2048 sc1\nv_mfma_f32_16x16x32_bf16 v[88:91], a[0:3], v[168:171], 0\nR 166,189,53248\nN 88,16,172\nG 4\ns_waitcnt vmcnt(20)\n"
"M 0,176,32\nR 168,190,40960\ns_waitcnt vmcnt(16)\nM 0,180,48\nM 16,176,36\nR 170,190,45056\n"
"s_waitcnt vmcnt(15)\nM 16,180,52\nbuffer_load_dwordx4 a[104:107], v252, s[20:23], s66 offen offset:2048 sc1\nM 32,176,40\nR 172,190,49152\ns_waitcnt vmcnt(15)\n"
"M 32,180,56\nM 48,176,44\nR 174,190,53248\ns_waitcnt vmcnt(14)\nM 48,180,60\ns_barrier\n"
"G 4\nM 52,160,44\nR 176,191,40960\nM 52,164,60\nbuffer_load_dwordx4 v221, s[4:7], s63 offen offset:1024 lds\nM 36,160,40\n"
"R 178,191,45056\nM 36,164,56\nbuffer_load_dwordx4 a[108:111], v252, s[20:23], s67 offen offset:2048 sc1\nM 20,160,36\nR 180,191,49152\nM 20,164,52\n"
"M 4,160,32\nR 182,191,53248\nM 4,164,48\nG 4\nM 8,168,32\nR 160,184,0\n"
"s_add_u32 s63, s52, 1183744\nM 8,172,48\nbuffer_load_dwordx4 a[112:115], v252, s[20:23], s64 offen offset:3072 sc1\nM 24,168,36\nR 162,184,4096\nM 24,172,52\n"
"s_add_u32 m0, s50, 65536\nM 40,168,40\nR 164,184,8192\nM 40,172,56\nbuffer_load_dwordx4 v220, s[4:7], s63 offen lds\nM 56,168,44\n"
"R 166,184,12288\nM 56,172,60\nG 4\nM 60,176,44\nR 168,185,0\nM 60,180,60\n"
"buffer_load_dwordx4 a[116:119], v252, s[20:23], s65 offen offset:3072 sc1\nM 44,176,40\nR 170,185,4096\nM 44,180,56\nM 28,176,36\nR 172,185,8192\n"
"M 28,180,52\nM 12,176,32\nR 174,185,12288\nM 12,180,48\nG 4\nN 64,32,160\n"
"R 176,186,0\nN 64,48,164\nbuffer_load_dwordx4 a[120:123], v252, s[20:23], s66 offen offset:3072 sc1\nN 68,36,160\nR 178,186,4096\nN 68,52,164\n"
"N 96,40,160\nR 180,186,8192\nN 96,56,164\nbuffer_load_dwordx4 a[124:127], v252, s[20:23], s67 offen offset:3072 sc1\nN 100,44,160\nR 182,186,12288\n"
"N 100,60,164\ns_barrier\nG 4\nN 108,44,168\nR 160,187,0\nN 108,60,172\n"
"buffer_load_dwordx4 v221, s[4:7], s63 offen offset:1024 lds\nN 104,40,168\nR 162,187,4096\nN 104,56,172\ns_add_u32 m0, s50, 73728\nN 76,36,168\n"
"R 164,187,8192\ns_add_u32 s63, s52, 1380352\nN 76,52,172\ns_add_u32 s64, s70, 18882560\nbuffer_load_dwordx4 a[128:131], v252, s[20:23], s64 offen offset:0 sc1\nN 72,32,168\n"
"R 166,187,12288\nN 72,48,172\nG 4\nN 80,32,176\nR 168,184,16384\nN 80,48,180\n"
"buffer_load_dwordx4 v220, s[4:7], s63 offen lds\nN 84,36,176\nR 170,184,20480\nN 84,52,180\ns_add_u32 s65, s70, 18915328\nbuffer_load_dwordx4 a[132:135], v252, s[20:23], s65 offen offset:0 sc1\n"
"N 112,40,176\nR 172,184,24576\nN 112,56,180\nN 116,44,176\nR 174,184,28672\nN 116,60,180\n"
"G 4\nN 124,44,160\nR 176,185,16384\nN 124,60,164\ns_add_u32 s66, s71, 18882560\nbuffer_load_dwordx4 a[136:139], v252, s[20:23], s66 offen offset:0 sc1\n"
"N 120,40,160\nR 178,185,20480\nN 120,56,164\nN 92,36,160\nR 180,185,24576\nN 92,52,164\n"
"N 88,32,160\nR 182,185,28672\nN 88,48,164\nG 4\ns_waitcnt vmcnt(25)\nM 0,168,64\n"
"R 160,186,16384\ns_waitcnt vmcnt(20)\nM 0,172,80\ns_add_u32 s67, s71, 18915328\nbuffer_load_dwordx4 a[140:143], v252, s[20:23], s67 offen offset:0 sc1\nM 16,168,68\n"
"R 162,186,20480\ns_waitcnt vmcnt(20)\nM 16,172,84\nM 32,168,72\nR 164,186,24576\ns_waitcnt vmcnt(19)\n"
"M 32,172,88\nbuffer_load_dwordx4 v221, s[4:7], s63 offen offset:1024 lds\nM 48,168,76\nR 166,186,28672\ns_waitcnt vmcnt(18)\nM 48,172,92\n"
"s_barrier\nG 4\nM 52,176,76\nR 168,187,16384\nM 52,180,92\nbuffer_load_dwordx4 a[144:147], v252, s[20:23], s64 offen offset:1024 sc1\n"
"M 36,176,72\nR 170,187,20480\nM 36,180,88\ns_add_u32 m0, s50, 81920\nM 20,176,68\nR 172,187,24576\n"
"s_add_u32 s63, s52, 1574912\nM 20,180,84\nbuffer_load_dwordx4 a[148:151], v252, s[20:23], s65 offen offset:1024 sc1\nM 4,176,64\nR 174,187,28672\nM 4,180,80\n"
"G 4\nM 8,160,64\nR 176,184,32768\nM 8,164,80\nbuffer_load_dwordx4 v220, s[4:7], s63 offen lds\nM 24,160,68\n"
"R 178,184,36864\nM 24,164,84\nM 40,160,72\nR 180,184,40960\nM 40,164,88\nbuffer_load_dwordx4 a[152:155], v252, s[20:23], s66 offen offset:1024 sc1\n"
"M 56,160,76\nR 182,184,45056\nM 56,164,92\nG 4\nM 60,168,76\nR 160,185,32768\n"
"M 60,172,92\nM 44,168,72\nR 162,185,36864\nM 44,172,88\nbuffer_load_dwordx4 a[156:159], v252, s[20:23], s67 offen offset:1024 sc1\nM 28,168,68\n"
"R 164,185,40960\nM 28,172,84\nM 12,168,64\nR 166,185,45056\nM 12,172,80\nG 4\n"
"N 64,64,176\nR 168,186,32768\nN 64,80,180\nbuffer_load_dwordx4 v221, s[4:7], s63 offen offset:1024 lds\nN 68,68,176\nR 170,186,36864\n"
"N 68,84,180\nbuffer_load_dwordx4 a[160:163], v252, s[20:23], s64 offen offset:2048 sc1\nN 96,72,176\nR 172,186,40960\nN 96,88,180\nN 100,76,176\n"
"R 174,186,45056\nN 100,92,180\ns_waitcnt vmcnt(20)\ns_barrier\nG 4\nN 108,76,160\n"
"R 176,187,32768\ns_add_u32 s63, s52, 1771520\nN 108,92,164\nbuffer_load_dwordx4 a[164:167], v252, s[20:23], s65 offen offset:2048 sc1\nN 104,72,160\nR 178,187,36864\n"
"N 104,88,164\ns_add_u32 m0, s50, 90112\nN 76,68,160\nR 180,187,40960\nN 76,84,164\nbuffer_load_dwordx4 v220, s[4:7], s63 offen lds\n"
"N 72,64,160\nR 182,187,45056\nN 72,80,164\nG 4\nN 80,64,168\nR 160,184,49152\n"
"N 80,80,172\nbuffer_load_dwordx4 a[168:171], v252, s[20:23], s66 offen offset:2048 sc1\nN 84,68,168\nR 162,184,53248\nN 84,84,172\nN 112,72,168\n"
"R 164,184,57344\nN 112,88,172\nbuffer_load_dwordx4 v221, s[4:7], s63 offen offset:1024 lds\nN 116,76,168\nR 166,184,61440\nN 116,92,172\n"
"G 4\nN 124,76,176\nR 168,185,49152\nN 124,92,180\nbuffer_load_dwordx4 a[172:175], v252, s[20:23], s67 offen offset:2048 sc1\nN 120,72,176\n"
"R 170,185,53248\nN 120,88,180\nN 92,68,176\nR 172,185,57344\nN 92,84,180\nbuffer_load_dwordx4 a[176:179], v252, s[20:23], s64 offen offset:3072 sc1\n"
"N 88,64,176\nR 174,185,61440\nN 88,80,180\nG 4\nM 0,160,96\nR 176,186,49152\n"
"s_waitcnt vmcnt(24)\nM 0,164,112\nM 16,160,100\nR 178,186,53248\ns_waitcnt vmcnt(22)\nM 16,164,116\n"
"buffer_load_dwordx4 a[180:183], v252, s[20:23], s65 offen offset:3072 sc1\nM 32,160,104\nR 180,186,57344\ns_add_u32 s63, s52, 1576960\ns_waitcnt vmcnt(22)\nM 32,164,120\n"
"s_add_u32 m0, s50, 98304\nM 48,160,108\nR 182,186,61440\ns_waitcnt vmcnt(21)\nM 48,164,124\ns_waitcnt vmcnt(14)\n"
"s_barrier\nG 4\nM 52,168,108\nR 160,187,49152\nM 52,172,124\nbuffer_load_dwordx4 v220, s[4:7], s63 offen lds\n"
"M 36,168,104\nR 162,187,53248\nM 36,172,120\nbuffer_load_dwordx4 a[184:187], v252, s[20:23], s66 offen offset:3072 sc1\nM 20,168,100\nR 164,187,57344\n"
"M 20,172,116\nbuffer_load_dwordx4 v221, s[4:7], s63 offen offset:1024 lds\nM 4,168,96\nR 166,187,61440\ns_add_u32 m0, s50, 106496\nM 4,172,112\n"
"G 4\nM 8,176,96\nR 168,188,0\ns_add_u32 s63, s52, 1773568\nM 8,180,112\nbuffer_load_dwordx4 a[188:191], v252, s[20:23], s67 offen offset:3072 sc1\n"
"M 24,176,100\nR 170,188,4096\nM 24,180,116\nbuffer_load_dwordx4 v220, s[4:7], s63 offen lds\nM 40,176,104\nR 172,188,8192\n"
"M 40,180,120\nbuffer_load_dwordx4 v221, s[4:7], s63 offen offset:1024 lds\nM 56,176,108\nR 174,188,12288\nM 56,180,124\nG 4\n"
"M 60,160,108\nR 176,189,0\nM 60,164,124\ns_add_u32 s64, s70, 18886656\nbuffer_load_dwordx4 a[192:195], v252, s[20:23], s64 offen offset:0 sc1\nM 44,160,104\n"
"R 178,189,4096\nM 44,164,120\nM 28,160,100\nR 180,189,8192\nM 28,164,116\nM 12,160,96\n"
"R 182,189,12288\nM 12,164,112\nG 4\nN 64,96,168\nR 160,190,0\nN 64,112,172\n"
"s_add_u32 s65, s70, 18919424\nbuffer_load_dwordx4 a[196:199], v252, s[20:23], s65 offen offset:0 sc1\nN 68,100,168\nR 162,190,4096\nN 68,116,172\ns_add_u32 m0, s50, 114688\n"
"N 96,104,168\nR 164,190,8192\ns_add_u32 s63, s52, 1968128\nN 96,120,172\ns_add_u32 s66, s71, 18886656\nbuffer_load_dwordx4 a[200:203], v252, s[20:23], s66 offen offset:0 sc1\n"
"N 100,108,168\nR 166,190,12288\nN 100,124,172\ns_waitcnt vmcnt(12)\ns_barrier\nG 4\n"
"N 108,108,176\nR 168,191,0\nN 108,124,180\nbuffer_load_dwordx4 v220, s[4:7], s63 offen lds\nN 104,104,176\nR 170,191,4096\n"
"N 104,120,180\nN 76,100,176\nR 172,191,8192\nN 76,116,180\ns_add_u32 s67, s71, 18919424\nbuffer_load_dwordx4 a[204:207], v252, s[20:23], s67 offen offset:0 sc1\n"
"N 72,96,176\nR 174,191,12288\nN 72,112,180\nG 4\nN 80,96,160\nR 176,188,16384\n"
"buffer_load_dwordx4 v221, s[4:7], s63 offen offset:1024 lds\nN 80,112,164\ns_add_u32 m0, s50, 0\ns_add_u32 s63, s52, 2164736\nbuffer_load_dwordx4 v220, s[4:7], s63 offen lds\nN 84,100,160\n"
"R 178,188,20480\nN 84,116,164\nbuffer_load_dwordx4 a[208:211], v252, s[20:23], s64 offen offset:1024 sc1\nN 112,104,160\nR 180,188,24576\nN 112,120,164\n"
"buffer_load_dwordx4 v221, s[4:7], s63 offen offset:1024 lds\nN 116,108,160\nR 182,188,28672\nN 116,124,164\nG 4\nN 124,108,168\n"
"R 160,189,16384\nN 124,124,172\nbuffer_load_dwordx4 a[212:215], v252, s[20:23], s65 offen offset:1024 sc1\nN 120,104,168\nR 162,189,20480\nN 120,120,172\n"
"N 92,100,168\nR 164,189,24576\nN 92,116,172\nN 88,96,168\nR 166,189,28672\nN 88,112,172\n"
"G 4\nM 0,176,128\nR 168,190,16384\nM 0,180,144\nbuffer_load_dwordx4 a[216:219], v252, s[20:23], s66 offen offset:1024 sc1\nM 16,176,132\n"
"R 170,190,20480\nM 16,180,148\nM 32,176,136\nR 172,190,24576\nM 32,180,152\ns_add_u32 m0, s50, 8192\n"
"M 48,176,140\nR 174,190,28672\ns_add_u32 s63, s52, 1970176\nM 48,180,156\ns_waitcnt vmcnt(11)\ns_barrier\n"
"G 4\nM 52,160,140\nR 176,191,16384\nM 52,164,156\nbuffer_load_dwordx4 a[220:223], v252, s[20:23], s67 offen offset:1024 sc1\nM 36,160,136\n"
"R 178,191,20480\nM 36,164,152\nbuffer_load_dwordx4 v220, s[4:7], s63 offen lds\nM 20,160,132\nR 180,191,24576\nM 20,164,148\n"
"buffer_load_dwordx4 v221, s[4:7], s63 offen offset:1024 lds\nM 4,160,128\nR 182,191,28672\ns_add_u32 m0, s50, 16384\nM 4,164,144\nG 4\n"
"M 8,168,128\nR 160,188,32768\ns_add_u32 s63, s52, 2166784\nM 8,172,144\nbuffer_load_dwordx4 a[224:227], v252, s[20:23], s64 offen offset:2048 sc1\nM 24,168,132\n"
"R 162,188,36864\nM 24,172,148\nbuffer_load_dwordx4 v220, s[4:7], s63 offen lds\nM 40,168,136\nR 164,188,40960\nM 40,172,152\n"
"buffer_load_dwordx4 a[228:231], v252, s[20:23], s65 offen offset:2048 sc1\nM 56,168,140\nR 166,188,45056\nM 56,172,156\nG 4\nM 60,176,140\n"
"R 168,189,32768\nbuffer_load_dwordx4 v221, s[4:7], s63 offen offset:1024 lds\nM 60,180,156\nM 44,176,136\nR 170,189,36864\nM 44,180,152\n"
"buffer_load_dwordx4 a[232:235], v252, s[20:23], s66 offen offset:2048 sc1\nM 28,176,132\nR 172,189,40960\nM 28,180,148\nM 12,176,128\nR 174,189,45056\n"
"M 12,180,144\nG 4\nN 64,128,160\nR 176,190,32768\nN 64,144,164\nN 68,132,160\n"
"R 178,190,36864\nN 68,148,164\nbuffer_load_dwordx4 a[236:239], v252, s[20:23], s67 offen offset:2048 sc1\nN 96,136,160\nR 180,190,40960\nN 96,152,164\n"
"s_add_u32 m0, s50, 24576\nN 100,140,160\nR 182,190,45056\ns_add_u32 s63, s52, 2361344\nN 100,156,164\ns_waitcnt vmcnt(11)\n"
"s_barrier\nG 4\nN 108,140,168\nR 160,191,32768\nN 108,156,172\nbuffer_load_dwordx4 a[240:243], v252, s[20:23], s64 offen offset:3072 sc1\n"
"N 104,136,168\nR 162,191,36864\nN 104,152,172\nbuffer_load_dwordx4 v220, s[4:7], s63 offen lds\nN 76,132,168\nR 164,191,40960\n"
"Lq675:\ns_lshr_b32 s60, s45, 3\ns_lshl2_add_u32 s60, s60, s40\ns_lshl_b32 s60, s60, 8\nv_lshl_add_u32 v224, v252, 3, s60\nv_add_u32 v224, Lq675+4096-LqPC, v224\n"
"s_mov_b64 exec, 3\nglobal_load_dword v224, v224, s[98:99]\ns_mov_b64 exec, -1\nN 76,148,172\nbuffer_load_dwordx4 v221, s[4:7], s63 offen offset:1024 lds\nN 72,128,168\n"
"R 166,191,45056\ns_add_u32 m0, s50, 32768\nN 72,144,172\nG 4\nN 80,128,176\nR 168,188,49152\n"
"s_add_u32 s63, s52, 2557952\nN 80,144,180\nbuffer_load_dwordx4 a[244:247], v252, s[20:23], s65 offen offset:3072 sc1\nN 84,132,176\nR 170,188,53248\nN 84,148,180\n"
"buffer_load_dwordx4 v220, s[4:7], s63 offen lds\nN 112,136,176\nR 172,184,0\nN 112,152,180\nbuffer_load_dwordx4 v221, s[4:7], s63 offen offset:1024 lds\nN 116,140,176\n"
"R 174,184,4096\nN 116,156,180\nG 4\nN 124,140,160\nR 176,189,49152\nN 124,156,164\n"
"buffer_load_dwordx4 a[248:251], v252, s[20:23], s66 offen offset:3072 sc1\nN 120,136,160\nR 178,189,53248\nN 120,152,164\nN 92,132,160\nR 180,185,0\n"
"N 92,148,164\nbuffer_load_dwordx4 a[252:255], v252, s[20:23], s67 offen offset:3072 sc1\nN 88,128,160\nR 182,185,4096\nN 88,144,164\nG 4\n"
"M 0,168,160\nR 160,190,49152\nM 0,172,176\nM 16,168,164\nR 162,190,53248\nM 16,172,180\n"
"s_lshr_b32 s84, s49, 3\nM 32,168,168\nR 164,186,0\ns_add_u32 m0, s50, 40960\nM 32,172,184\ns_add_u32 s84, s84, 82837504\n"
"M 48,168,172\nR 166,186,4096\ns_add_u32 s63, s52, 2754560\nM 48,172,188\ns_waitcnt vmcnt(11)\ns_barrier\n"
"G 4\nM 52,176,172\nR 168,191,49152\nM 52,180,188\nbuffer_load_dwordx4 v220, s[4:7], s63 offen lds\nM 36,176,168\n"
"R 170,191,53248\nM 36,180,184\nbuffer_load_dwordx4 v[128:131], v252, s[20:23], s84 offen offset:0 sc1\nM 20,176,164\nR 172,187,0\nM 20,180,180\n"
"buffer_load_dwordx4 v[132:135], v252, s[20:23], s84 offen offset:1024 sc1\nM 4,176,160\nR 174,187,4096\nM 4,180,176\nG 4\nM 8,160,160\n"
"R 176,184,8192\nbuffer_load_dwordx4 v221, s[4:7], s63 offen offset:1024 lds\nM 8,164,176\nbuffer_load_dwordx4 v[136:139], v252, s[20:23], s84 offen offset:2048 sc1\ns_add_u32 s63, s52, 2951168\nM 24,160,164\n"
"R 178,184,12288\ns_add_u32 m0, s50, 49152\nM 24,164,180\nbuffer_load_dwordx4 v[140:143], v252, s[20:23], s84 offen offset:3072 sc1\nM 40,160,168\nR 180,184,16384\n"
"buffer_load_dwordx4 v220, s[4:7], s63 offen lds\nM 40,164,184\ns_lshr_b32 s85, s49, 3\nM 56,160,172\nR 182,184,20480\ns_add_u32 s85, s85, 82841600\n"
"M 56,164,188\nbuffer_load_dwordx4 v[144:147], v252, s[20:23], s85 offen offset:0 sc1\nG 4\nM 60,168,172\nR 160,185,8192\nbuffer_load_dwordx4 v221, s[4:7], s63 offen offset:1024 lds\n"
"M 60,172,188\nbuffer_load_dwordx4 v[148:151], v252, s[20:23], s85 offen offset:1024 sc1\nM 44,168,168\nR 162,185,12288\nM 44,172,184\nM 28,168,164\n"
"R 164,185,16384\nM 28,172,180\nbuffer_load_dwordx4 v[152:155], v252, s[20:23], s85 offen offset:2048 sc1\nM 12,168,160\nR 166,185,20480\nM 12,172,176\n"
"G 4\nN 64,160,176\nR 168,186,8192\nN 64,176,180\nbuffer_load_dwordx4 v[156:159], v252, s[20:23], s85 offen offset:3072 sc1\nN 68,164,176\n"
"R 170,186,12288\nN 68,180,180\ns_add_u32 m0, s50, 57344\nN 96,168,176\nR 172,186,16384\nN 96,184,180\n"
"s_add_u32 s63, s52, 2363392\nN 100,172,176\nR 174,186,20480\nN 100,188,180\ns_waitcnt vmcnt(14)\ns_barrier\n"
"G 4\nN 108,172,160\nR 176,187,8192\nN 108,188,164\nbuffer_load_dwordx4 v220, s[4:7], s63 offen lds\nN 104,168,160\n"
"R 178,187,12288\nN 104,184,164\nN 76,164,160\nR 180,187,16384\nN 76,180,164\nbuffer_load_dwordx4 v221, s[4:7], s63 offen offset:1024 lds\n"
"N 72,160,160\nR 182,187,20480\ns_add_u32 m0, s50, 65536\nN 72,176,164\nG 4\nN 80,160,168\n"
"R 160,184,24576\ns_add_u32 s63, s52, 2560000\nN 80,176,172\nbuffer_load_dwordx4 v220, s[4:7], s63 offen lds\nN 84,164,168\nR 162,184,28672\n"
"N 84,180,172\nN 112,168,168\nR 164,184,32768\nN 112,184,172\nbuffer_load_dwordx4 v221, s[4:7], s63 offen offset:1024 lds\nN 116,172,168\n"
"R 166,184,36864\nN 116,188,172\nG 4\nN 124,172,176\nR 168,185,24576\nN 124,188,180\n"
"N 120,168,176\nR 170,185,28672\nN 120,184,180\ns_add_u32 m0, s50, 73728\nN 92,164,176\nR 172,185,32768\n"
"N 92,180,180\ns_add_u32 s63, s52, 2756608\nN 88,160,176\nR 174,185,36864\nN 88,176,180\nG 4\n"
"M 0,160,192\nR 176,186,24576\nM 0,164,208\nbuffer_load_dwordx4 v220, s[4:7], s63 offen lds\nM 16,160,196\nR 178,186,28672\n"
"M 16,164,212\nM 32,160,200\nR 180,186,32768\nM 32,164,216\nbuffer_load_dwordx4 v221, s[4:7], s63 offen offset:1024 lds\nM 48,160,204\n"
"R 182,186,36864\ns_add_u32 m0, s50, 81920\nM 48,164,220\ns_waitcnt vmcnt(9)\ns_barrier\nG 4\n"
"M 52,168,204\nR 160,187,24576\ns_add_u32 s63, s52, 2953216\nM 52,172,220\nbuffer_load_dwordx4 v220, s[4:7], s63 offen lds\nM 36,168,200\n"
"R 162,187,28672\nv_min3_u32 v230, v129, v131, v133\nv_min3_u32 v230, v230, v135, v137\nM 36,172,216\nv_min3_u32 v230, v230, v139, v141\nv_min3_u32 v230, v230, v143, v145\n"
"s_waitcnt vmcnt(9)\nv_min3_u32 v230, v230, v147, v149\nM 20,168,196\nR 164,187,32768\ns_waitcnt vmcnt(8)\nv_min3_u32 v230, v230, v151, v153\n"
"s_waitcnt vmcnt(7)\nv_min3_u32 v230, v230, v155, v157\nM 20,172,212\nbuffer_load_dwordx4 v221, s[4:7], s63 offen offset:1024 lds\nv_min3_u32 v230, v230, v159, v159\nM 4,168,192\n"
"R 166,187,36864\ns_add_u32 m0, s50, 90112\nv_max3_u32 v231, v129, v131, v133\nM 4,172,208\nG 4\nv_max3_u32 v231, v231, v135, v137\n"
"v_max3_u32 v231, v231, v139, v141\nv_max3_u32 v231, v231, v143, v145\nM 8,176,192\nR 168,184,40960\ns_add_u32 s63, s52, 0\nv_max3_u32 v231, v231, v147, v149\n"
"M 8,180,208\nbuffer_load_dwordx4 v220, s[4:7], s63 offen lds\nv_max3_u32 v231, v231, v151, v153\nM 24,176,196\nR 170,184,45056\nv_max3_u32 v231, v231, v155, v157\n"
"v_max3_u32 v231, v231, v159, v159\nM 24,180,212\nv_cmp_ne_u32 s[86:87], s46, v230\nv_cmp_ne_u32 s[88:89], s46, v231\nM 40,176,200\nR 172,184,49152\n"
"s_or_b64 s[86:87], s[86:87], s[88:89]\ns_cbranch_scc1 Lq629\nLq630:\nM 40,180,216\nv_add_f32 v128, v128, v144\nv_add_f32 v130, v130, v146\n"
"v_add_f32 v132, v132, v148\nM 56,176,204\nR 174,184,53248\nv_add_f32 v134, v134, v150\nv_add_f32 v136, v136, v152\nM 56,180,220\n"
"G 4\nv_add_f32 v138, v138, v154\nv_add_f32 v140, v140, v156\nv_add_f32 v142, v142, v158\nM 60,160,204\nR 176,185,40960\n"
"v_add_f32 v128, v128, v136\nv_add_f32 v130, v130, v138\nM 60,164,220\nbuffer_load_dwordx4 v221, s[4:7], s63 offen offset:1024 lds\nv_add_f32 v132, v132, v140\nM 44,160,200\n"
"R 178,185,45056\ns_add_u32 s63, s52, 196608\nv_add_f32 v134, v134, v142\nM 44,164,216\ns_add_u32 m0, s50, 98304\nv_add_f32 v128, v128, v132\n"
"v_add_f32 v130, v130, v134\nv_lshrrev_b32 v231, 1, v252\nM 28,160,196\nR 180,185,49152\nv_lshrrev_b32 v230, 2, v252\ns_lshl_b32 s62, s40, 9\n"
"s_add_u32 s62, s62, 159232\nv_add_u32 v231, s62, v231\nM 28,164,212\nbuffer_load_dwordx4 v220, s[4:7], s63 offen lds\nds_write2_b32 v231, v128, v130 offset1:1\nM 12,160,192\n"
"R 182,185,53248\ns_lshl_b32 s62, s40, 9\ns_add_u32 s62, s62, 159232\nv_add_u32 v230, s62, v230\nM 12,164,208\nds_read2st64_b32 v[232:233], v230 offset1:1\n"
"G 6\nM 0,168,224\nR 160,186,40960\nM 0,172,240\nbuffer_load_dwordx4 v221, s[4:7], s63 offen offset:1024 lds\nM 16,168,228\n"
"R 162,186,45056\ns_add_u32 s63, s52, 393216\nM 16,172,244\ns_add_u32 m0, s50, 106496\nM 32,168,232\nR 164,186,49152\n"
"M 32,172,248\nbuffer_load_dwordx4 v220, s[4:7], s63 offen lds\nG 3\nv_add_f32 v232, v232, v233\nM 48,168,236\nR 166,186,53248\n"
"v_mov_b32 v233, v232\ns_nop 1\nv_permlane32_swap_b32 v232, v233\nM 48,172,252\ns_waitcnt vmcnt(9)\ns_barrier\n"
"v_mov_b32 v230, v232\nM 52,176,236\nR 168,187,40960\nv_mov_b32 v231, v233\nv_permlane16_swap_b32 v230, v232\nM 52,180,252\n"
"buffer_load_dwordx4 v221, s[4:7], s63 offen offset:1024 lds\nv_permlane16_swap_b32 v231, v233\nM 36,176,232\nR 170,187,45056\nv_mul_f32 v230, 0xba800000, v230\nv_mul_f32 v214, v230, v230\n"
"M 36,180,248\ns_add_u32 m0, s50, 114688\nv_mul_f32 v231, 0x3a800000, v231\nv_sub_f32 v231, v231, v214\nv_max_f32 v231, 0, v231\nM 20,176,228\n"
"R 172,187,49152\nv_add_f32 v231, 0x3727c5ac, v231\nv_rsq_f32 v234, v231\nM 20,180,244\ns_add_u32 s63, s52, 589824\nv_and_b32 v208, s77, v230\n"
"v_mul_f32 v216, v231, v234\nv_sub_f32 v230, v230, v208\nM 4,176,224\nR 174,187,53248\nv_cvt_pk_bf16_f32 v209, v230, v216\nv_cvt_pk_bf16_f32 v208, v208, v208\n"
"M 4,180,240\ns_mov_b64 s[60:61], 0xffff\nv_cndmask_b32 v208, 0, v208, s[60:61]\nG 4\nv_mov_b32 v214, v234\nM 8,160,224\n"
"R 176,184,57344\nv_mul_f32 v215, 0x3e8293ee, v234\nv_mul_f32 v232, 0xba800000, v232\nM 8,164,240\nbuffer_load_dwordx4 v220, s[4:7], s63 offen lds\nv_mul_f32 v217, v232, v232\n"
"M 24,160,228\nR 178,184,61440\nv_mul_f32 v233, 0x3a800000, v233\nv_sub_f32 v233, v233, v217\nM 24,164,244\ns_mov_b64 s[60:61], 0xffff\n"
"v_cndmask_b32 v209, 0, v209, s[60:61]\nv_max_f32 v233, 0, v233\nv_add_f32 v233, 0x3727c5ac, v233\nM 40,160,232\nR 180,188,0\nv_and_b32 v212, s77, v232\n"
"v_rsq_f32 v235, v233\nM 40,164,248\nbuffer_load_dwordx4 v221, s[4:7], s63 offen offset:1024 lds\nv_mul_f32 v219, v233, v235\nM 56,160,236\nR 182,188,4096\n"
"s_add_u32 m0, s50, 0\nv_sub_f32 v232, v232, v212\nM 56,164,252\nG 4\nv_cvt_pk_bf16_f32 v213, v232, v219\nv_cvt_pk_bf16_f32 v212, v212, v212\n"
"v_mov_b32 v217, v235\nM 60,168,236\nR 160,185,57344\ns_add_u32 s63, s52, 786432\nv_mul_f32 v218, 0x3e8293ee, v235\nM 60,172,252\n"
"buffer_load_dwordx4 v220, s[4:7], s63 offen lds\nv_bfe_u32 v230, v252, 2, 6\nM 44,168,232\nR 162,185,61440\ns_mov_b64 s[60:61], 0xffff\nv_cndmask_b32 v212, 0, v212, s[60:61]\n"
"M 44,172,248\ns_mov_b64 s[60:61], 0xffff\nv_cndmask_b32 v213, 0, v213, s[60:61]\ns_lshl_b32 s60, s40, 7\ns_add_u32 s60, s60, 161280\nv_add_u32 v230, s60, v230\n"
"M 28,168,228\nR 164,189,0\nds_write_b32 v230, v214 offset:0\nM 28,172,244\nbuffer_load_dwordx4 v221, s[4:7], s63 offen offset:1024 lds\nds_write_b32 v230, v217 offset:64\n"
"M 12,168,224\nR 166,189,4096\ns_mov_b32 s60, 156160\nv_bfe_u32 v246, v252, 4, 4\nv_lshl_add_u32 v246, v246, 3, s60\nM 12,172,240\n"
"ds_read_b64 v[238:239], v246 offset:0\ns_waitcnt vmcnt(10) lgkmcnt(0)\ns_barrier\nN 64,192,176\nR 168,186,57344\nds_read_b64 v[240:241], v246 offset:128\n"
"N 64,208,180\ns_mov_b32 m0, s51\nds_read_b64 v[242:243], v246 offset:256\nN 68,196,176\nR 170,186,61440\nds_read_b64 v[244:245], v246 offset:384\n"
"ds_write_addtid_b32 v32 offset:0\nN 68,212,180\nds_read_b64 v[246:247], v246 offset:0\nds_write_addtid_b32 v33 offset:256\nN 96,200,176\nR 172,190,0\n"
"ds_write_addtid_b32 v34 offset:512\nN 96,216,180\nds_write_addtid_b32 v35 offset:768\nds_write_addtid_b32 v36 offset:1024\nN 100,204,176\nR 174,190,4096\n"
"ds_write_addtid_b32 v37 offset:1280\nN 100,220,180\nds_write_addtid_b32 v38 offset:1536\nds_write_addtid_b32 v39 offset:1792\nN 108,204,160\nR 176,187,57344\n"
"ds_write_addtid_b32 v40 offset:2048\nN 108,220,164\nds_write_addtid_b32 v41 offset:2304\nds_write_addtid_b32 v42 offset:2560\nN 104,200,160\nR 178,187,61440\n"
"ds_write_addtid_b32 v43 offset:2816\nN 104,216,164\nds_write_addtid_b32 v44 offset:3072\nds_write_addtid_b32 v45 offset:3328\nN 76,196,160\nR 180,191,0\n"
"G 15\nv_mfma_f32_16x16x32_bf16 v[0:3], v[238:241], v[208:211], v[0:3]\nds_write_addtid_b32 v46 offset:3584\nN 76,212,164\nv_mfma_f32_16x16x32_bf16 v[4:7], v[240:243], v[208:211], v[4:7]\nds_write_addtid_b32 v47 offset:3840\n"
"ds_write_addtid_b32 v48 offset:4096\nN 72,192,160\nR 182,191,4096\nv_mfma_f32_16x16x32_bf16 v[8:11], v[242:245], v[208:211], v[8:11]\nds_write_addtid_b32 v49 offset:4352\nN 72,208,164\n"
"v_mfma_f32_16x16x32_bf16 v[12:15], v[244:247], v[208:211], v[12:15]\nds_write_addtid_b32 v50 offset:4608\nds_write_addtid_b32 v51 offset:4864\nG 15\nN 80,192,168\nR 160,188,8192\n"
"v_mfma_f32_16x16x32_bf16 v[16:19], v[244:247], v[210:213], v[16:19]\nds_write_addtid_b32 v52 offset:5120\nN 80,208,172\nv_mfma_f32_16x16x32_bf16 v[20:23], v[238:241], v[210:213], v[20:23]\nds_write_addtid_b32 v53 offset:5376\nds_write_addtid_b32 v54 offset:5632\n"
"N 84,196,168\nR 162,188,12288\nv_mfma_f32_16x16x32_bf16 v[24:27], v[240:243], v[210:213], v[24:27]\nds_write_addtid_b32 v55 offset:5888\nN 84,212,172\nv_mfma_f32_16x16x32_bf16 v[28:31], v[242:245], v[210:213], v[28:31]\n"
"ds_write_addtid_b32 v56 offset:6144\nds_write_addtid_b32 v57 offset:6400\nN 112,200,168\nR 164,188,16384\nds_write_addtid_b32 v58 offset:6656\nN 112,216,172\n"
"ds_write_addtid_b32 v59 offset:6912\nds_write_addtid_b32 v60 offset:7168\nN 116,204,168\nR 166,188,20480\nds_write_addtid_b32 v61 offset:7424\nN 116,220,172\n"
"ds_write_addtid_b32 v62 offset:7680\nds_write_addtid_b32 v63 offset:7936\nG 15\nN 124,204,176\nR 168,189,8192\ns_add_u32 m0, s50, 8192\n"
"s_add_u32 s63, s52, 983040\nN 124,220,180\nbuffer_load_dwordx4 v220, s[4:7], s63 offen lds\nN 120,200,176\nR 170,189,12288\ns_waitcnt vmcnt(7) lgkmcnt(2)\n"
"s_barrier\nN 120,216,180\nds_read2st64_b32 v[32:33], v225 offset0:0 offset1:1\nds_read2st64_b32 v[34:35], v225 offset0:2 offset1:3\nN 92,196,176\nR 172,189,16384\n"
"ds_read2st64_b32 v[36:37], v225 offset0:4 offset1:5\nN 92,212,180\nbuffer_load_dwordx4 v221, s[4:7], s63 offen offset:1024 lds\nds_read2st64_b32 v[38:39], v225 offset0:6 offset1:7\nN 88,192,176\nR 174,189,20480\n"
"ds_read2st64_b32 v[40:41], v225 offset0:8 offset1:9\nN 88,208,180\nds_read2st64_b32 v[42:43], v225 offset0:10 offset1:11\nds_read2st64_b32 v[44:45], v225 offset0:12 offset1:13\nN 64,224,160\nR 176,190,8192\n"
"s_mov_b32 s60, 156672\nv_bfe_u32 v246, v252, 4, 4\nv_lshl_add_u32 v246, v246, 3, s60\nds_read2st64_b32 v[46:47], v225 offset0:14 offset1:15\nN 64,240,164\nds_read_b64 v[238:239], v246 offset:0\n"
"G 11\nv_add_f32 v0, v0, v32\nv_add_f32 v1, v1, v33\nN 68,228,160\nR 178,190,12288\nds_read_b64 v[240:241], v246 offset:128\n"
"G 12\nv_add_f32 v2, v2, v34\nN 68,244,164\nds_read_b64 v[242:243], v246 offset:256\ns_add_u32 m0, s50, 16384\nv_add_f32 v3, v3, v35\n"
"N 96,232,160\nR 180,190,16384\nds_read_b64 v[244:245], v246 offset:384\nG 13\nv_add_f32 v4, v4, v36\nN 96,248,164\n"
"ds_read_b64 v[246:247], v246 offset:0\ns_add_u32 s63, s52, 1179648\nv_add_f32 v5, v5, v37\nN 100,236,160\nR 182,190,20480\nG 14\n"
"v_add_f32 v6, v6, v38\nv_add_f32 v7, v7, v39\nN 100,252,164\nds_read2st64_b32 v[48:49], v225 offset0:16 offset1:17\nG 14\nG 13\n"
"v_add_f32 v8, v8, v40\nN 108,236,168\nR 160,191,8192\nv_add_f32 v9, v9, v41\nG 13\nv_add_f32 v10, v10, v42\n"
"N 108,252,172\nbuffer_load_dwordx4 v220, s[4:7], s63 offen lds\nv_add_f32 v11, v11, v43\nN 104,232,168\nR 162,191,12288\nG 13\n"
"v_add_f32 v12, v12, v44\nv_add_f32 v13, v13, v45\nN 104,248,172\nds_read2st64_b32 v[50:51], v225 offset0:18 offset1:19\nG 12\nv_add_f32 v14, v14, v46\n"
"v_add_f32 v15, v15, v47\nN 76,228,168\nR 164,191,16384\nds_read2st64_b32 v[52:53], v225 offset0:20 offset1:21\nN 76,244,172\nG 11\n"
"v_mfma_f32_16x16x32_bf16 v[64:67], v[208:211], v[238:241], v[64:67]\nbuffer_load_dwordx4 v221, s[4:7], s63 offen offset:1024 lds\nG 5\nv_add_f32 v16, v16, v48\nN 72,224,168\nR 166,191,20480\n"
"s_add_u32 m0, s50, 24576\nv_add_f32 v17, v17, v49\nN 72,240,172\nv_mfma_f32_16x16x32_bf16 v[72:75], v[208:211], v[240:243], v[72:75]\nds_read2st64_b32 v[54:55], v225 offset0:22 offset1:23\nds_read2st64_b32 v[56:57], v225 offset0:24 offset1:25\n"
"N 80,224,176\nR 168,188,24576\ns_add_u32 s63, s52, 1376256\nds_read2st64_b32 v[58:59], v225 offset0:26 offset1:27\nN 80,240,180\nv_mfma_f32_16x16x32_bf16 v[80:83], v[208:211], v[242:245], v[80:83]\n"
"buffer_load_dwordx4 v220, s[4:7], s63 offen lds\nG 7\nv_add_f32 v18, v18, v50\nN 84,228,176\nR 170,188,28672\nv_add_f32 v19, v19, v51\n"
"G 6\nv_add_f32 v20, v20, v52\nN 84,244,180\nv_mfma_f32_16x16x32_bf16 v[88:91], v[208:211], v[244:247], v[88:91]\nbuffer_load_dwordx4 v221, s[4:7], s63 offen offset:1024 lds\nv_add_f32 v21, v21, v53\n"
"N 112,232,176\nR 172,188,32768\ns_add_u32 s63, s52, 1572864\nds_read2st64_b32 v[60:61], v225 offset0:28 offset1:29\nN 112,248,180\nv_mfma_f32_16x16x32_bf16 v[68:71], v[210:213], v[244:247], v[68:71]\n"
"s_add_u32 m0, s50, 49152\nds_read2st64_b32 v[62:63], v225 offset0:30 offset1:31\nG 7\nv_add_f32 v22, v22, v54\nN 116,236,176\nR 174,188,36864\n"
"v_add_f32 v23, v23, v55\nG 7\nv_add_f32 v24, v24, v56\nN 116,252,180\nv_mfma_f32_16x16x32_bf16 v[76:79], v[210:213], v[238:241], v[76:79]\nv_add_f32 v25, v25, v57\n"
"G 5\nv_add_f32 v26, v26, v58\nv_add_f32 v27, v27, v59\nN 124,236,160\nR 176,189,24576\nN 124,252,164\n"
"v_mfma_f32_16x16x32_bf16 v[84:87], v[210:213], v[240:243], v[84:87]\nbuffer_load_dwordx4 v220, s[4:7], s63 offen lds\nG 3\nv_add_f32 v28, v28, v60\nv_add_f32 v29, v29, v61\nG 2\n"
"v_add_f32 v30, v30, v62\nv_add_f32 v31, v31, v63\nN 120,232,160\nR 178,189,28672\ns_lshl4_add_u32 s64, s41, s42\nv_lshrrev_b32 v227, 1, v252\n"
"N 120,248,164\nv_mfma_f32_16x16x32_bf16 v[92:95], v[210:213], v[242:245], v[92:95]\nbuffer_load_dwordx4 v221, s[4:7], s63 offen offset:1024 lds\nv_mul_f32 v16, v16, v217\nN 92,228,160\nR 180,189,32768\n"
"s_add_u32 m0, s50, 57344\ns_add_u32 s63, s52, 1769472\nv_mul_f32 v17, v17, v217\nN 92,244,164\nbuffer_load_dwordx4 v220, s[4:7], s63 offen lds\nv_mul_f32 v18, v18, v217\n"
"N 88,224,160\nR 182,189,36864\ns_lshl_b32 s64, s64, 12\nv_mul_f32 v19, v19, v217\nN 88,240,164\ns_waitcnt vmcnt(11)\n"
"s_barrier\nG 4\nv_mul_f32 v20, v20, v217\nv_mfma_f32_16x16x32_bf16 v[128:131], v[168:171], a[0:3], 0\nR 160,190,24576\ns_add_u32 s64, s64, 78643200\n"
"v_mul_f32 v21, v21, v217\nM 128,172,16\ns_mov_b32 m0, s51\ns_cmp_eq_u32 s40, 3\ns_cselect_b64 s[66:67], -1, 0\nv_mul_f32 v22, v22, v217\n"
"v_mfma_f32_16x16x32_bf16 v[144:147], v[168:171], a[4:7], 0\nR 162,190,28672\nv_mul_f32 v23, v23, v217\nv_mul_f32 v24, v24, v217\nM 144,172,20\nv_mul_f32 v25, v25, v217\n"
"v_mul_f32 v26, v26, v217\nv_mul_f32 v27, v27, v217\nv_mfma_f32_16x16x32_bf16 v[32:35], v[168:171], a[8:11], 0\nR 164,190,32768\nv_mul_f32 v28, v28, v217\nv_mul_f32 v29, v29, v217\n"
"M 32,172,24\nv_mul_f32 v30, v30, v217\nv_mul_f32 v31, v31, v217\nv_cvt_pk_bf16_f32 v16, v16, v17\nv_mfma_f32_16x16x32_bf16 v[48:51], v[168:171], a[12:15], 0\nR 166,190,36864\n"
"v_cvt_pk_bf16_f32 v17, v18, v19\nv_cvt_pk_bf16_f32 v18, v20, v21\nM 48,172,28\nds_write_addtid_b32 v96 offset:0\nG 5\nv_cvt_pk_bf16_f32 v19, v22, v23\n"
"v_cvt_pk_bf16_f32 v20, v24, v25\nv_cvt_pk_bf16_f32 v21, v26, v27\nv_mfma_f32_16x16x32_bf16 v[52:55], v[176:179], a[12:15], 0\nR 168,191,24576\nds_write_addtid_b32 v97 offset:256\nv_cvt_pk_bf16_f32 v22, v28, v29\n"
"v_cvt_pk_bf16_f32 v23, v30, v31\nM 52,180,28\nds_write_addtid_b32 v98 offset:512\nv_mul_f32 v0, v0, v214\nv_mul_f32 v1, v1, v214\nv_mul_f32 v2, v2, v214\n"
"v_mfma_f32_16x16x32_bf16 v[36:39], v[176:179], a[8:11], 0\nR 170,191,28672\nds_write_addtid_b32 v99 offset:768\nv_mul_f32 v3, v3, v214\nv_mul_f32 v4, v4, v214\nM 36,180,24\n"
"ds_write_addtid_b32 v100 offset:1024\nv_mul_f32 v5, v5, v214\nv_mul_f32 v6, v6, v214\nv_mul_f32 v7, v7, v214\nv_mfma_f32_16x16x32_bf16 v[148:151], v[176:179], a[4:7], 0\nR 172,191,32768\n"
"ds_write_addtid_b32 v101 offset:1280\nv_mul_f32 v8, v8, v214\nv_mul_f32 v9, v9, v214\nM 148,180,20\nds_write_addtid_b32 v102 offset:1536\nv_mul_f32 v10, v10, v214\n"
"v_mul_f32 v11, v11, v214\nv_mul_f32 v12, v12, v214\nv_mfma_f32_16x16x32_bf16 v[132:135], v[176:179], a[0:3], 0\nR 174,191,36864\nds_write_addtid_b32 v103 offset:1792\nv_mul_f32 v13, v13, v214\n"
"v_mul_f32 v14, v14, v214\nM 132,180,16\nds_write_addtid_b32 v104 offset:2048\nG 13\nv_mul_f32 v15, v15, v214\nv_cvt_pk_bf16_f32 v0, v0, v1\n"
"v_cvt_pk_bf16_f32 v1, v2, v3\nv_mfma_f32_16x16x32_bf16 v[136:139], v[160:163], a[0:3], 0\nR 176,188,40960\nds_write_addtid_b32 v105 offset:2304\nv_cvt_pk_bf16_f32 v2, v4, v5\nv_cvt_pk_bf16_f32 v3, v6, v7\n"
"M 136,164,16\nds_write_addtid_b32 v106 offset:2560\nv_cvt_pk_bf16_f32 v4, v8, v9\nv_cvt_pk_bf16_f32 v5, v10, v11\nv_cvt_pk_bf16_f32 v6, v12, v13\nv_mfma_f32_16x16x32_bf16 v[152:155], v[160:163], a[4:7], 0\n"
"R 178,188,45056\nds_write_addtid_b32 v107 offset:2816\ns_mov_b64 exec, s[66:67]\nbuffer_store_dwordx4 v[16:19], v252, s[20:23], s64 offen offset:0 sc1\nbuffer_store_dwordx4 v[20:23], v252, s[20:23], s64 offen offset:1024 sc1\ns_mov_b64 exec, -1\n"
"v_cvt_pk_bf16_f32 v7, v14, v15\nM 152,164,20\nds_write_addtid_b32 v108 offset:3072\nds_write_addtid_b32 v109 offset:3328\nv_mfma_f32_16x16x32_bf16 v[40:43], v[160:163], a[8:11], 0\nR 180,188,49152\n"
"ds_write_addtid_b32 v110 offset:3584\nM 40,164,24\nds_write_addtid_b32 v111 offset:3840\nds_write_addtid_b32 v112 offset:4096\nv_mfma_f32_16x16x32_bf16 v[56:59], v[160:163], a[12:15], 0\nR 182,188,53248\n"
"ds_write_addtid_b32 v113 offset:4352\nM 56,164,28\nds_write_addtid_b32 v114 offset:4608\nds_write_addtid_b32 v115 offset:4864\nG 15\nv_mfma_f32_16x16x32_bf16 v[60:63], v[168:171], a[12:15], 0\n"
"R 160,189,40960\nds_write_addtid_b32 v116 offset:5120\nM 60,172,28\nds_write_addtid_b32 v117 offset:5376\nds_write_addtid_b32 v118 offset:5632\nv_mfma_f32_16x16x32_bf16 v[44:47], v[168:171], a[8:11], 0\n"
"R 162,189,45056\nds_write_addtid_b32 v119 offset:5888\nM 44,172,24\nds_write_addtid_b32 v120 offset:6144\nds_write_addtid_b32 v121 offset:6400\nds_write_addtid_b32 v122 offset:6656\n"
"ds_write_addtid_b32 v123 offset:6912\nds_write_addtid_b32 v124 offset:7168\nds_write_addtid_b32 v125 offset:7424\nds_write_addtid_b32 v126 offset:7680\nds_write_addtid_b32 v127 offset:7936\nv_mfma_f32_16x16x32_bf16 v[156:159], v[168:171], a[4:7], 0\n"
"R 164,189,49152\ns_add_u32 m0, s50, 57344\nM 156,172,20\nbuffer_load_dwordx4 v221, s[4:7], s63 offen offset:1024 lds\nv_mfma_f32_16x16x32_bf16 v[140:143], v[168:171], a[0:3], 0\nR 166,189,53248\n"
"s_waitcnt vmcnt(10) lgkmcnt(2)\ns_barrier\nM 140,172,16\nds_read2st64_b32 v[96:97], v225 offset0:0 offset1:1\nds_read2st64_b32 v[98:99], v225 offset0:2 offset1:3\nv_lshrrev_b32 v226, 8, v252\n"
"M 128,176,32\nR 168,190,40960\ns_add_u32 s63, s52, 1966080\nds_read2st64_b32 v[100:101], v225 offset0:4 offset1:5\ns_lshl_b32 s60, s40, 7\ns_add_u32 s60, s60, 161280\n"
"v_lshl_add_u32 v226, v226, 4, s60\nM 128,180,48\ns_add_u32 m0, s50, 65536\nds_read2st64_b32 v[102:103], v225 offset0:6 offset1:7\nds_read2st64_b32 v[104:105], v225 offset0:8 offset1:9\nds_read_b128 v[230:233], v226 offset:0\n"
"M 144,176,36\nR 170,190,45056\nds_read2st64_b32 v[106:107], v225 offset0:10 offset1:11\nds_read_b128 v[234:237], v226 offset:64\nM 144,180,52\nbuffer_load_dwordx4 v220, s[4:7], s63 offen lds\n"
"ds_read2st64_b32 v[108:109], v225 offset0:12 offset1:13\nM 32,176,40\nR 172,190,49152\nds_read2st64_b32 v[110:111], v225 offset0:14 offset1:15\nM 32,180,56\nds_read2st64_b32 v[112:113], v225 offset0:16 offset1:17\n"
"G 13\nv_add_f32 v64, v64, v96\nv_add_f32 v65, v65, v97\nM 48,176,44\nR 174,190,53248\nG 13\n"
"v_add_f32 v66, v66, v98\nv_add_f32 v67, v67, v99\nM 48,180,60\nds_read2st64_b32 v[114:115], v225 offset0:18 offset1:19\nG 12\nv_add_f32 v68, v68, v100\n"
"M 52,160,44\nR 176,191,40960\nv_add_f32 v69, v69, v101\nG 12\nv_add_f32 v70, v70, v102\nM 52,164,60\n"
"buffer_load_dwordx4 v221, s[4:7], s63 offen offset:1024 lds\nv_add_f32 v71, v71, v103\nM 36,160,40\nR 178,191,45056\nG 12\nv_add_f32 v72, v72, v104\n"
"v_add_f32 v73, v73, v105\nM 36,164,56\ns_add_u32 m0, s50, 73728\nds_read2st64_b32 v[116:117], v225 offset0:20 offset1:21\nG 10\nv_add_f32 v74, v74, v106\n"
"M 148,160,36\nR 180,191,49152\nv_add_f32 v75, v75, v107\nG 9\nv_add_f32 v76, v76, v108\nM 148,164,52\n"
"s_add_u32 s63, s52, 2162688\nds_read2st64_b32 v[118:119], v225 offset0:22 offset1:23\nv_add_f32 v77, v77, v109\nM 132,160,32\nR 182,191,53248\nG 9\n"
"v_add_f32 v78, v78, v110\nv_add_f32 v79, v79, v111\nM 132,164,48\nds_read2st64_b32 v[120:121], v225 offset0:24 offset1:25\nG 8\nv_add_f32 v80, v80, v112\n"
"M 136,168,32\nR 160,184,0\nv_add_f32 v81, v81, v113\nG 8\nv_add_f32 v82, v82, v114\nM 136,172,48\n"
"buffer_load_dwordx4 v220, s[4:7], s63 offen lds\nv_add_f32 v83, v83, v115\nM 152,168,36\nR 162,184,4096\nG 6\nv_add_f32 v84, v84, v116\n"
"v_add_f32 v85, v85, v117\nM 152,172,52\nds_read2st64_b32 v[122:123], v225 offset0:26 offset1:27\nG 5\nv_add_f32 v86, v86, v118\nv_add_f32 v87, v87, v119\n"
"M 40,168,40\nR 164,184,8192\nds_read2st64_b32 v[124:125], v225 offset0:28 offset1:29\nM 40,172,56\nbuffer_load_dwordx4 v221, s[4:7], s63 offen offset:1024 lds\nG 5\n"
"v_add_f32 v88, v88, v120\nM 56,168,44\nR 166,184,12288\ns_add_u32 m0, s50, 81920\nM 56,172,60\nds_read2st64_b32 v[126:127], v225 offset0:30 offset1:31\n"
"v_add_f32 v89, v89, v121\nM 60,176,44\nR 168,185,0\ns_add_u32 s63, s52, 2359296\nM 60,180,60\nbuffer_load_dwordx4 v220, s[4:7], s63 offen lds\n"
"M 44,176,40\nR 170,185,4096\nG 6\nv_add_f32 v90, v90, v122\nM 44,180,56\nv_add_f32 v91, v91, v123\n"
"G 4\nv_add_f32 v92, v92, v124\nM 156,176,36\nR 172,185,8192\nv_add_f32 v93, v93, v125\nM 156,180,52\n"
"buffer_load_dwordx4 v221, s[4:7], s63 offen offset:1024 lds\nM 140,176,32\nR 174,185,12288\ns_add_u32 m0, s50, 90112\nM 140,180,48\nG 5\n"
"G 4\nv_add_f32 v94, v94, v126\nv_add_f32 v95, v95, v127\nM 128,160,64\nR 176,186,0\ns_add_u32 s63, s52, 2555904\n"
"v_mul_f32 v64, v64, v230\nM 128,164,80\nbuffer_load_dwordx4 v220, s[4:7], s63 offen lds\nv_mul_f32 v65, v65, v231\nM 144,160,68\nR 178,186,4096\n"
"v_mul_f32 v66, v66, v232\nv_mul_f32 v67, v67, v233\nM 144,164,84\nv_mul_f32 v68, v68, v234\nv_mul_f32 v69, v69, v235\nv_mul_f32 v70, v70, v236\n"
"M 32,160,72\nR 180,186,8192\nv_mul_f32 v71, v71, v237\nv_cvt_pk_bf16_f32 v67, v66, v67\nM 32,164,88\nv_cvt_pk_bf16_f32 v66, v64, v65\n"
"v_cvt_pk_bf16_f32 v68, v68, v69\nv_cvt_pk_bf16_f32 v69, v70, v71\nM 48,160,76\nR 182,186,12288\nv_mul_f32 v72, v72, v230\nv_mul_f32 v73, v73, v231\n"
"M 48,164,92\nG 4\nv_mul_f32 v74, v74, v232\nv_mul_f32 v75, v75, v233\nv_mul_f32 v76, v76, v234\nM 52,168,76\n"
"R 160,187,0\nv_mul_f32 v77, v77, v235\nv_mul_f32 v78, v78, v236\nM 52,172,92\nbuffer_load_dwordx4 v221, s[4:7], s63 offen offset:1024 lds\nv_mul_f32 v79, v79, v237\n"
"M 36,168,72\nR 162,187,4096\ns_add_u32 s63, s52, 2752512\nv_cvt_pk_bf16_f32 v72, v72, v73\nM 36,172,88\ns_add_u32 m0, s50, 98304\n"
"v_cvt_pk_bf16_f32 v73, v74, v75\nv_cvt_pk_bf16_f32 v74, v76, v77\nv_cvt_pk_bf16_f32 v75, v78, v79\nM 148,168,68\nR 164,187,8192\nv_mul_f32 v80, v80, v230\n"
"v_mul_f32 v81, v81, v231\nM 148,172,84\nbuffer_load_dwordx4 v220, s[4:7], s63 offen lds\nv_mul_f32 v82, v82, v232\nM 132,168,64\nR 166,187,12288\n"
"s_waitcnt vmcnt(9)\ns_barrier\nv_mul_f32 v83, v83, v233\nM 132,172,80\nG 4\nv_mul_f32 v84, v84, v234\n"
"v_mul_f32 v85, v85, v235\nv_mul_f32 v86, v86, v236\nM 136,176,64\nR 168,184,16384\nv_mul_f32 v87, v87, v237\nv_cvt_pk_bf16_f32 v78, v80, v81\n"
"M 136,180,80\ns_mov_b64 exec, s[66:67]\nbuffer_store_dwordx2 v[68:69], v227, s[20:23], s64 offen offset:2048 sc1\nbuffer_store_dwordx2 v[74:75], v227, s[20:23], s64 offen offset:2560 sc1\ns_mov_b64 exec, -1\nv_cvt_pk_bf16_f32 v79, v82, v83\n"
"v_cvt_pk_bf16_f32 v80, v84, v85\nM 152,176,68\nR 170,184,20480\nv_cvt_pk_bf16_f32 v81, v86, v87\nv_mul_f32 v88, v88, v230\nM 152,180,84\n"
"v_mul_f32 v89, v89, v231\nv_mul_f32 v90, v90, v232\nv_mul_f32 v91, v91, v233\nM 40,176,72\nR 172,184,24576\ns_mov_b32 s60, 155648\n"
"v_bfe_u32 v246, v252, 4, 4\nv_lshl_add_u32 v246, v246, 3, s60\nv_mul_f32 v92, v92, v234\nM 40,180,88\nbuffer_load_dwordx4 v221, s[4:7], s63 offen offset:1024 lds\nv_mul_f32 v93, v93, v235\n"
"M 56,176,76\nR 174,184,28672\nds_read_b64 v[238:239], v246 offset:0\nv_mul_f32 v94, v94, v236\nM 56,180,92\nG 5\n"
"v_mul_f32 v95, v95, v237\nv_cvt_pk_bf16_f32 v84, v88, v89\nv_cvt_pk_bf16_f32 v85, v90, v91\nM 60,160,76\nR 176,185,16384\nds_read_b64 v[240:241], v246 offset:128\n"
"v_cvt_pk_bf16_f32 v86, v92, v93\nM 60,164,92\ns_add_u32 m0, s50, 106496\nv_cvt_pk_bf16_f32 v87, v94, v95\ns_mov_b64 exec, s[66:67]\nbuffer_store_dwordx2 v[80:81], v227, s[20:23], s64 offen offset:3072 sc1\n"
"buffer_store_dwordx2 v[86:87], v227, s[20:23], s64 offen offset:3584 sc1\ns_mov_b64 exec, -1\nv_add_u32 v108, 161792, v252\nM 44,160,72\nR 178,185,20480\nds_read_b64 v[242:243], v246 offset:256\n"
"M 44,164,88\ns_add_u32 s63, s52, 2949120\nds_read_b128 v[112:115], v108\nM 156,160,68\nR 180,185,24576\nds_read_b64 v[244:245], v246 offset:384\n"
"M 156,164,84\nbuffer_load_dwordx4 v220, s[4:7], s63 offen lds\nM 140,160,64\nR 182,185,28672\nds_read_b64 v[246:247], v246 offset:0\nM 140,164,80\n"
"ds_read_b128 v[104:107], v108 offset:1024\nG 11\nM 128,168,96\nR 160,186,16384\nM 128,172,112\ns_lshl_b32 s60, s40, 12\n"
"s_add_u32 s60, s60, 69632\nv_add_u32 v230, s60, v252\nv_add_u32 v231, s60, v227\nM 144,168,100\nR 162,186,20480\nM 144,172,116\n"
"s_not_b64 exec, s[66:67]\nds_write_b128 v230, v[16:19] offset:0\nds_write_b128 v230, v[20:23] offset:1024\ns_mov_b64 exec, -1\nM 32,168,104\nR 164,186,24576\n"
"G 13\nv_mfma_f32_16x16x32_bf16 v[128:131], v[238:241], v[208:211], v[128:131]\nM 32,172,120\nbuffer_load_dwordx4 v221, s[4:7], s63 offen offset:1024 lds\nM 48,168,108\nR 166,186,28672\n"
"G 12\nv_mfma_f32_16x16x32_bf16 v[132:135], v[240:243], v[208:211], v[132:135]\nG 6\nv_lshlrev_b32 v100, 16, v104\nM 48,172,124\nv_and_b32 v101, s77, v104\n"
"v_lshlrev_b32 v102, 16, v105\nM 52,176,108\nR 168,187,16384\nv_mfma_f32_16x16x32_bf16 v[144:147], v[244:247], v[210:213], v[144:147]\nv_and_b32 v103, s77, v105\nM 52,180,124\n"
"v_lshlrev_b32 v96, 16, v106\nv_and_b32 v97, s77, v106\nM 36,176,104\nR 170,187,20480\nv_mfma_f32_16x16x32_bf16 v[148:151], v[238:241], v[210:213], v[148:151]\nv_lshlrev_b32 v98, 16, v107\n"
"M 36,180,120\ns_movk_i32 s63, 2048\nv_mad_u32_u24 v220, v228, s63, v229\nv_add_u32 v221, 1047552, v220\ns_add_u32 m0, s50, 114688\ns_add_u32 s63, s53, 0\n"
"buffer_load_dwordx4 v220, s[8:11], s63 offen lds\nM 148,176,100\nR 172,187,24576\nv_and_b32 v99, s77, v107\nM 148,180,116\nv_add_f32 v104, s78, v100\n"
"v_add_f32 v105, s78, v101\nM 132,176,96\nR 174,187,28672\nv_add_f32 v106, s78, v102\nM 132,180,112\nG 4\n"
"v_add_f32 v107, s78, v103\nM 136,160,96\nR 176,184,49152\nM 136,164,112\ns_not_b64 exec, s[66:67]\nds_write_b64 v231, v[68:69] offset:2048\n"
"ds_write_b64 v231, v[74:75] offset:2560\ns_mov_b64 exec, -1\nM 152,160,100\nR 178,184,53248\nM 152,164,116\nbuffer_load_dwordx4 v221, s[8:11], s63 offen offset:1024 lds\n"
"M 40,160,104\nR 180,184,57344\nM 40,164,120\nM 56,160,108\nR 182,184,61440\nM 56,164,124\n"
"G 6\nM 60,168,108\nR 160,185,49152\ns_not_b64 exec, s[66:67]\nds_write_b64 v231, v[80:81] offset:3072\nds_write_b64 v231, v[86:87] offset:3584\n"
"s_mov_b64 exec, -1\nM 60,172,124\nM 44,168,104\nR 162,185,53248\nM 44,172,120\ns_add_u32 m0, s50, 0\n"
"M 156,168,100\nR 164,185,57344\nM 156,172,116\ns_add_u32 s63, s53, 65536\nM 140,168,96\nR 166,185,61440\n"
"M 140,172,112\nG 6\nM 128,176,128\nR 168,186,49152\ns_waitcnt vmcnt(12) lgkmcnt(4)\ns_barrier\n"
"M 128,180,144\nbuffer_load_dwordx4 v220, s[8:11], s63 offen lds\nM 144,176,132\nR 170,186,53248\nM 144,180,148\ns_cmp_eq_u32 s78, 0\n"
"s_cbranch_scc1 Lq646\nv_mov_b32 v230, 0\nv_mov_b32 v231, 0\nv_mov_b32 v232, 0\nv_mov_b32 v233, 0\nv_mov_b32 v64, 0\n"
"v_mov_b32 v65, 0\nv_mov_b32 v70, 0\nv_mov_b32 v71, 0\nv_mov_b32 v234, 0\nv_mov_b32 v235, 0\nv_mov_b32 v236, 0\n"
"v_mov_b32 v237, 0\nv_mov_b32 v76, 0\nv_mov_b32 v77, 0\nv_mov_b32 v82, 0\nv_mov_b32 v83, 0\nLq646:\n"
"s_lshl4_add_u32 s65, s41, s42\nM 32,176,136\nR 172,186,57344\ns_lshl_b32 s65, s65, 12\nM 32,180,152\ns_add_u32 s65, s65, 78577664\n"
"M 48,176,140\nR 174,186,61440\nM 48,180,156\nG 4\nM 52,160,140\nR 176,187,49152\n"
"M 52,164,156\nbuffer_load_dwordx4 v221, s[8:11], s63 offen offset:1024 lds\nM 36,160,136\nR 178,187,53248\nM 36,164,152\ns_add_u32 m0, s50, 8192\n"
"M 148,160,132\nR 180,187,57344\nM 148,164,148\ns_add_u32 s63, s53, 131072\nM 132,160,128\nR 182,187,61440\n"
"M 132,164,144\nG 4\nM 136,168,128\nR 160,188,0\nM 136,172,144\nbuffer_load_dwordx4 v220, s[8:11], s63 offen lds\n"
"M 152,168,132\nR 162,188,4096\nM 152,172,148\nM 40,168,136\nR 164,188,8192\nM 40,172,152\n"
"M 56,168,140\nR 166,188,12288\nv_mfma_f32_16x16x32_bf16 v[136:139], v[242:245], v[208:211], v[136:139]\nM 56,172,156\nG 4\nM 60,176,140\n"
"R 168,189,0\nv_mfma_f32_16x16x32_bf16 v[140:143], v[244:247], v[208:211], v[140:143]\nM 60,180,156\nbuffer_load_dwordx4 v221, s[8:11], s63 offen offset:1024 lds\nM 44,176,136\nR 170,189,4096\n"
"v_mfma_f32_16x16x32_bf16 v[152:155], v[240:243], v[210:213], v[152:155]\ns_add_u32 s63, s53, 196608\nM 44,180,152\ns_add_u32 m0, s50, 16384\nM 156,176,132\nR 172,189,8192\n"
"v_mfma_f32_16x16x32_bf16 v[156:159], v[242:245], v[210:213], v[156:159]\nM 156,180,148\nbuffer_load_dwordx4 v220, s[8:11], s63 offen lds\nM 140,176,128\nR 174,189,12288\nM 140,180,144\n"
"G 4\nM 128,160,160\nR 176,190,0\nM 128,164,176\nM 144,160,164\nR 178,190,4096\n"
"M 144,164,180\nbuffer_load_dwordx4 v221, s[8:11], s63 offen offset:1024 lds\nM 32,160,168\nR 180,190,8192\ns_add_u32 s63, s53, 262144\nM 32,164,184\n"
"s_add_u32 m0, s50, 24576\nM 48,160,172\nR 182,190,12288\nM 48,164,188\nG 4\nM 52,168,172\n"
"R 160,191,0\nM 52,172,188\nbuffer_load_dwordx4 v220, s[8:11], s63 offen lds\nM 36,168,168\nR 162,191,4096\nM 36,172,184\n"
"M 148,168,164\nR 164,191,8192\ns_lshl_b32 s60, s40, 12\ns_add_u32 s60, s60, 65536\nv_add_u32 v226, s60, v252\nv_add_u32 v111, s60, v227\n"
"M 148,172,180\nM 132,168,160\nR 166,191,12288\ns_cmp_eq_u32 s40, 0\ns_cselect_b64 s[88:89], 0, -1\ns_cselect_b32 s60, s41, 0\n"
"s_cmp_lg_u32 s60, 0\ns_cselect_b64 s[66:67], -1, 0\nM 132,172,176\nG 4\nM 136,176,160\nR 168,188,16384\n"
"M 136,180,176\ns_mov_b64 exec, s[88:89]\nds_read_b128 v[230:233], v226 offset:0\nds_read_b64 v[64:65], v111 offset:2048\ns_mov_b64 exec, -1\nM 152,176,164\n"
"R 170,188,20480\ns_waitcnt vmcnt(9) lgkmcnt(1)\ns_barrier\nM 152,180,180\nbuffer_load_dwordx4 v221, s[8:11], s63 offen offset:1024 lds\nM 40,176,168\n"
"R 172,188,24576\nM 40,180,184\ns_mov_b64 exec, s[88:89]\nds_read_b64 v[70:71], v111 offset:2560\nds_read_b128 v[234:237], v226 offset:1024\ns_mov_b64 exec, -1\n"
"M 56,176,172\nR 174,188,28672\nM 56,180,188\nM 60,160,172\nR 176,189,16384\nM 60,164,188\n"
"s_mov_b64 exec, s[88:89]\nds_read_b64 v[76:77], v111 offset:3072\nds_read_b64 v[82:83], v111 offset:3584\ns_mov_b64 exec, -1\nM 44,160,168\nR 178,189,20480\n"
"M 44,164,184\nM 156,160,164\nR 180,189,24576\nM 156,164,180\nM 140,160,160\nR 182,189,28672\n"
"M 140,164,176\nG 6\nM 32,168,200\nR 160,190,16384\ns_mov_b64 exec, s[66:67]\nbuffer_load_dwordx4 v[230:233], v252, s[20:23], s65 offen offset:0 sc1\n"
"buffer_load_dwordx2 v[64:65], v227, s[20:23], s65 offen offset:2048 sc1\ns_mov_b64 exec, -1\nM 32,172,216\nR 162,190,20480\nM 48,168,204\nR 164,190,24576\n"
"M 48,172,220\nR 166,190,28672\nG 4\nM 52,176,204\nR 168,191,16384\nM 52,180,220\n"
"R 170,191,20480\ns_mov_b64 exec, s[66:67]\nbuffer_load_dwordx2 v[70:71], v227, s[20:23], s65 offen offset:2560 sc1\nbuffer_load_dwordx4 v[234:237], v252, s[20:23], s65 offen offset:1024 sc1\ns_mov_b64 exec, -1\nM 36,176,200\n"
"R 172,191,24576\nM 36,180,216\nR 174,191,28672\nG 4\nM 40,160,200\nR 176,188,32768\n"
"M 40,164,216\nR 178,188,36864\nM 56,160,204\nR 180,188,40960\ns_mov_b64 exec, s[66:67]\nbuffer_load_dwordx2 v[76:77], v227, s[20:23], s65 offen offset:3072 sc1\n"
"buffer_load_dwordx2 v[82:83], v227, s[20:23], s65 offen offset:3584 sc1\ns_mov_b64 exec, -1\nM 56,164,220\nR 182,188,45056\nG 4\nM 60,168,204\n"
"R 160,189,32768\nM 60,172,220\nR 162,189,36864\nM 44,168,200\nR 164,189,40960\nM 44,172,216\n"
"R 166,189,45056\nG 4\nM 32,176,232\nR 168,190,32768\nLq677:\ns_lshr_b32 s60, s45, 3\n"
"s_lshl2_add_u32 s60, s60, s40\ns_lshl_b32 s60, s60, 8\nv_lshl_add_u32 v224, v252, 3, s60\nv_add_u32 v224, Lq677+4096-LqPC, v224\ns_mov_b64 exec, 3\nglobal_load_dword v224, v224, s[98:99]\n"
"s_mov_b64 exec, -1\nM 32,180,248\nR 170,190,36864\nM 48,176,236\nR 172,190,40960\nM 48,180,252\n"
"R 174,190,45056\nG 4\nM 52,160,236\nR 176,191,32768\nM 52,164,252\nR 178,191,36864\n"
"M 36,160,232\nR 180,191,40960\nM 36,164,248\nR 182,191,45056\nG 4\nM 40,168,232\n"
"R 160,188,16384\nM 40,172,248\nR 162,188,20480\nM 56,168,236\nR 164,188,24576\nM 56,172,252\n"
"R 166,188,28672\nG 4\nM 60,176,236\nR 168,189,16384\nM 60,180,252\nR 170,189,20480\n"
"M 44,176,232\nR 172,189,24576\nM 44,180,248\nR 174,189,28672\nG 4\nM 128,160,192\n"
"R 176,190,16384\nM 128,164,208\nR 178,190,20480\ns_mov_b32 m0, s51\nM 144,160,196\nR 180,190,24576\n"
"ds_write_addtid_b32 v32 offset:0\nM 144,164,212\nR 182,190,28672\nds_write_addtid_b32 v33 offset:256\nG 6\nM 148,168,196\n"
"R 160,191,16384\nds_write_addtid_b32 v34 offset:512\nM 148,172,212\nR 162,191,20480\nds_write_addtid_b32 v35 offset:768\nM 132,168,192\n"
"R 164,191,24576\nds_write_addtid_b32 v36 offset:1024\nM 132,172,208\nR 166,191,28672\nds_write_addtid_b32 v37 offset:1280\nG 9\n"
"M 136,176,192\nR 168,188,32768\nds_write_addtid_b32 v38 offset:1536\nM 136,180,208\nR 170,188,36864\nds_write_addtid_b32 v39 offset:1792\n"
"M 152,176,196\nR 172,188,40960\nds_write_addtid_b32 v40 offset:2048\nM 152,180,212\nR 174,188,45056\nds_write_addtid_b32 v41 offset:2304\n"
"G 9\nM 156,160,196\nR 176,189,32768\nds_write_addtid_b32 v42 offset:2560\nM 156,164,212\nR 178,189,36864\n"
"ds_write_addtid_b32 v43 offset:2816\nM 140,160,192\nR 180,189,40960\nds_write_addtid_b32 v44 offset:3072\nds_write_addtid_b32 v45 offset:3328\nds_write_addtid_b32 v46 offset:3584\n"
"ds_write_addtid_b32 v47 offset:3840\nds_write_addtid_b32 v48 offset:4096\nds_write_addtid_b32 v49 offset:4352\nds_write_addtid_b32 v50 offset:4608\nds_write_addtid_b32 v51 offset:4864\nds_write_addtid_b32 v52 offset:5120\n"
"ds_write_addtid_b32 v53 offset:5376\nds_write_addtid_b32 v54 offset:5632\nds_write_addtid_b32 v55 offset:5888\nds_write_addtid_b32 v56 offset:6144\nds_write_addtid_b32 v57 offset:6400\nds_write_addtid_b32 v58 offset:6656\n"
"ds_write_addtid_b32 v59 offset:6912\nds_write_addtid_b32 v60 offset:7168\nds_write_addtid_b32 v61 offset:7424\nds_write_addtid_b32 v62 offset:7680\nds_write_addtid_b32 v63 offset:7936\nM 140,164,208\n"
"R 182,189,45056\nG 15\nM 128,168,224\nR 160,190,32768\ns_add_u32 m0, s50, 32768\nM 128,172,240\n"
"R 162,190,36864\nG 3\ns_barrier\ns_add_u32 s63, s53, 327680\nM 144,168,228\nR 164,190,40960\n"
"buffer_load_dwordx4 v220, s[8:11], s63 offen lds\nds_read2st64_b32 v[32:33], v225 offset0:0 offset1:1\nM 144,172,244\nR 166,190,45056\nbuffer_load_dwordx4 v221, s[8:11], s63 offen offset:1024 lds\nds_read2st64_b32 v[34:35], v225 offset0:2 offset1:3\n"
"G 6\nM 148,176,228\nR 168,191,32768\ns_add_u32 m0, s50, 40960\nds_read2st64_b32 v[36:37], v225 offset0:4 offset1:5\nM 148,180,244\n"
"R 170,191,36864\ns_add_u32 s63, s53, 393216\nM 132,176,224\nR 172,191,40960\nds_read2st64_b32 v[38:39], v225 offset0:6 offset1:7\nM 132,180,240\n"
"R 174,191,45056\nG 7\nM 136,160,224\nbuffer_load_dwordx4 v220, s[8:11], s63 offen lds\nM 136,164,240\nds_read2st64_b32 v[40:41], v225 offset0:8 offset1:9\n"
"ds_read2st64_b32 v[42:43], v225 offset0:10 offset1:11\nM 152,160,228\nbuffer_load_dwordx4 v221, s[8:11], s63 offen offset:1024 lds\nM 152,164,244\nds_read2st64_b32 v[44:45], v225 offset0:12 offset1:13\nds_read2st64_b32 v[46:47], v225 offset0:14 offset1:15\n"
"G 4\nM 156,168,228\nds_read2st64_b32 v[48:49], v225 offset0:16 offset1:17\nds_read2st64_b32 v[50:51], v225 offset0:18 offset1:19\nM 156,172,244\nds_read2st64_b32 v[52:53], v225 offset0:20 offset1:21\n"
"ds_read2st64_b32 v[54:55], v225 offset0:22 offset1:23\nM 140,168,224\nds_read2st64_b32 v[56:57], v225 offset0:24 offset1:25\nds_read2st64_b32 v[58:59], v225 offset0:26 offset1:27\nM 140,172,240\nds_read2st64_b32 v[60:61], v225 offset0:28 offset1:29\n"
"ds_read2st64_b32 v[62:63], v225 offset0:30 offset1:31\nG 0\ns_barrier\nv_pk_add_f32 v[144:145], v[144:145], v[48:49]\nv_pk_add_f32 v[146:147], v[146:147], v[50:51]\nv_pk_add_f32 v[148:149], v[148:149], v[52:53]\n"
"v_pk_add_f32 v[150:151], v[150:151], v[54:55]\nv_pk_add_f32 v[152:153], v[152:153], v[56:57]\nv_pk_add_f32 v[154:155], v[154:155], v[58:59]\nv_pk_add_f32 v[156:157], v[156:157], v[60:61]\nv_pk_add_f32 v[158:159], v[158:159], v[62:63]\nv_pk_add_f32 v[128:129], v[128:129], v[32:33]\n"
"v_pk_add_f32 v[130:131], v[130:131], v[34:35]\nv_pk_add_f32 v[132:133], v[132:133], v[36:37]\nv_pk_add_f32 v[134:135], v[134:135], v[38:39]\nv_pk_add_f32 v[136:137], v[136:137], v[40:41]\nv_pk_add_f32 v[138:139], v[138:139], v[42:43]\nv_pk_add_f32 v[140:141], v[140:141], v[44:45]\n"
"v_pk_add_f32 v[142:143], v[142:143], v[46:47]\nv_cvt_pk_bf16_f32 v144, v144, v145\nv_cvt_pk_bf16_f32 v145, v146, v147\nv_cvt_pk_bf16_f32 v146, v148, v149\nv_cvt_pk_bf16_f32 v147, v150, v151\nv_cvt_pk_bf16_f32 v152, v152, v153\n"
"v_cvt_pk_bf16_f32 v153, v154, v155\nv_cvt_pk_bf16_f32 v154, v156, v157\nv_cvt_pk_bf16_f32 v155, v158, v159\nv_cvt_pk_bf16_f32 v128, v128, v129\nv_cvt_pk_bf16_f32 v129, v130, v131\nv_cvt_pk_bf16_f32 v130, v132, v133\n"
"v_cvt_pk_bf16_f32 v131, v134, v135\nv_cvt_pk_bf16_f32 v136, v136, v137\nv_cvt_pk_bf16_f32 v137, v138, v139\nv_cvt_pk_bf16_f32 v138, v140, v141\nv_cvt_pk_bf16_f32 v139, v142, v143\nv_mfma_f32_16x16x32_bf16 v[52:55], v[16:19], v[144:147], v[96:99]\n"
"v_mfma_f32_16x16x32_bf16 v[48:51], v[0:3], v[144:147], v[100:103]\nv_mfma_f32_16x16x32_bf16 v[60:63], v[20:23], v[152:155], v[96:99]\nv_mfma_f32_16x16x32_bf16 v[56:59], v[4:7], v[152:155], v[100:103]\ns_waitcnt vmcnt(8)\nv_max3_u32 v248, v233, v65, v71\nv_cmp_eq_u32 vcc, -1, v248\n"
"s_nop 0\ns_cbranch_vccnz Lq679\nLq680:\ns_waitcnt vmcnt(5)\nv_max3_u32 v248, v237, v77, v83\nv_cmp_eq_u32 vcc, -1, v248\n"
"s_nop 0\ns_cbranch_vccnz Lq681\nLq682:\nv_mfma_f32_16x16x32_bf16 v[36:39], v[0:3], v[128:131], v[96:99]\nv_mfma_f32_16x16x32_bf16 v[32:35], v[230:233], v[128:131], v[104:107]\nv_mfma_f32_16x16x32_bf16 v[44:47], v[4:7], v[136:139], v[96:99]\n"
"v_mfma_f32_16x16x32_bf16 v[40:43], v[234:237], v[136:139], v[104:107]\ns_lshl_b32 s84, s42, 11\ns_add_u32 s84, s84, s49\ns_add_u32 s84, s84, 6291456\ns_add_u32 s85, s84, 32768\nv_mov_b32 v238, s46\n"
"v_mov_b32 v239, s47\nv_max_f32 v52, v52, v53\nv_max_f32 v54, v54, v55\nv_max3_f32 v52, v52, v48, v49\nv_max3_f32 v54, v54, v50, v51\nv_max_f32 v118, v52, v54\n"
"v_max_f32 v60, v60, v61\nv_max_f32 v62, v62, v63\nv_max3_f32 v60, v60, v56, v57\nv_max3_f32 v62, v62, v58, v59\nv_max_f32 v119, v60, v62\nv_max_f32 v36, v36, v37\n"
"v_max_f32 v38, v38, v39\nv_max3_f32 v36, v36, v32, v33\nv_max3_f32 v38, v38, v34, v35\nv_max_f32 v116, v36, v38\nv_max_f32 v44, v44, v45\nv_max_f32 v46, v46, v47\n"
"v_max3_f32 v44, v44, v40, v41\nv_max3_f32 v46, v46, v42, v43\nv_max_f32 v117, v44, v46\nv_permlane32_swap_b32 v116, v118\ns_nop 0\nv_permlane32_swap_b32 v117, v119\n"
"v_max_f32 v116, v116, v118\nv_max_f32 v117, v117, v119\ns_nop 1\nv_permlane16_swap_b32 v116, v117\nv_max_f32 v116, v116, v117\nv_mov_b32 v117, v116\n"
"s_nop 1\nv_permlane16_swap_b32 v116, v117\nv_mov_b32 v118, v116\nv_mov_b32 v119, v117\ns_nop 0\nv_permlane32_swap_b32 v116, v118\n"
"v_permlane32_swap_b32 v117, v119\nv_mul_f32 v116, v116, v215\nv_mul_f32 v118, v118, v218\nv_mul_f32 v117, v117, v215\nv_mul_f32 v119, v119, v218\nv_fma_f32 v52, v52, v218, -v118\n"
"v_fma_f32 v54, v54, v218, -v118\nv_fma_f32 v60, v60, v218, -v119\nv_fma_f32 v62, v62, v218, -v119\nv_fma_f32 v36, v36, v215, -v116\nv_fma_f32 v38, v38, v215, -v116\nv_fma_f32 v44, v44, v215, -v117\n"
"v_fma_f32 v46, v46, v215, -v117\nv_exp_f32 v52, v52\nv_exp_f32 v54, v54\nv_exp_f32 v60, v60\nv_exp_f32 v62, v62\nv_exp_f32 v36, v36\n"
"v_exp_f32 v38, v38\nv_exp_f32 v44, v44\nv_exp_f32 v46, v46\nv_add_f32 v118, v52, v54\nv_add_f32 v119, v60, v62\nv_add_f32 v116, v36, v38\n"
"v_add_f32 v117, v44, v46\ns_nop 0\nv_permlane32_swap_b32 v116, v118\nv_permlane32_swap_b32 v117, v119\nv_add_f32 v116, v116, v118\nv_add_f32 v117, v117, v119\n"
"s_nop 1\nv_permlane16_swap_b32 v116, v117\nv_add_f32 v116, v116, v117\nv_rcp_f32 v116, v116\ns_nop 0\nv_mov_b32 v117, v116\n"
"s_nop 1\nv_permlane16_swap_b32 v116, v117\nv_mov_b32 v118, v116\nv_mov_b32 v119, v117\ns_nop 0\nv_permlane32_swap_b32 v116, v118\n"
"v_permlane32_swap_b32 v117, v119\nv_mul_f32 v52, v52, v118\nv_mul_f32 v54, v54, v118\nv_mul_f32 v60, v60, v119\nv_mul_f32 v62, v62, v119\nv_mul_f32 v36, v36, v116\n"
"v_mul_f32 v38, v38, v116\nv_mul_f32 v44, v44, v117\nv_mul_f32 v46, v46, v117\nv_cvt_pk_bf16_f32 v52, v52, v52\nv_cvt_pk_bf16_f32 v54, v54, v54\nv_cvt_pk_bf16_f32 v60, v60, v60\n"
"v_cvt_pk_bf16_f32 v62, v62, v62\nv_cvt_pk_bf16_f32 v36, v36, v36\nv_cvt_pk_bf16_f32 v38, v38, v38\nv_cvt_pk_bf16_f32 v44, v44, v44\nv_cvt_pk_bf16_f32 v46, v46, v46\nv_and_b32 v48, v52, v112\n"
"v_and_b32 v49, v54, v113\nv_and_b32 v50, v52, v114\nv_and_b32 v51, v54, v115\nv_and_b32 v56, v60, v112\nv_and_b32 v57, v62, v113\nv_and_b32 v58, v60, v114\n"
"v_and_b32 v59, v62, v115\nv_mfma_f32_16x16x32_bf16 v[144:147], v[66:69], v[48:51], 0\nv_mfma_f32_16x16x32_bf16 v[148:151], v[72:75], v[48:51], 0\nv_and_b32 v32, v36, v112\nv_and_b32 v33, v38, v113\nv_and_b32 v34, v36, v114\n"
"v_and_b32 v35, v38, v115\nv_mfma_f32_16x16x32_bf16 v[152:155], v[78:81], v[56:59], 0\nv_mfma_f32_16x16x32_bf16 v[156:159], v[84:87], v[56:59], 0\nv_and_b32 v40, v44, v112\nv_and_b32 v41, v46, v113\nv_and_b32 v42, v44, v114\n"
"v_and_b32 v43, v46, v115\nv_mfma_f32_16x16x32_bf16 v[128:131], v[64:67], v[32:35], 0\nv_mfma_f32_16x16x32_bf16 v[132:135], v[70:73], v[32:35], 0\nv_mfma_f32_16x16x32_bf16 v[136:139], v[76:79], v[40:43], 0\nv_mfma_f32_16x16x32_bf16 v[140:143], v[82:85], v[40:43], 0\nv_cvt_pk_bf16_f32 v144, v144, v145\n"
"v_cvt_pk_bf16_f32 v145, v146, v147\nv_cvt_pk_bf16_f32 v146, v148, v149\nv_cvt_pk_bf16_f32 v147, v150, v151\nbuffer_store_dwordx4 v[144:147], v252, s[20:23], s85 offen offset:0 sc1\nv_cvt_pk_bf16_f32 v152, v152, v153\nv_cvt_pk_bf16_f32 v153, v154, v155\n"
"v_cvt_pk_bf16_f32 v154, v156, v157\nv_cvt_pk_bf16_f32 v155, v158, v159\nbuffer_store_dwordx4 v[152:155], v252, s[20:23], s85 offen offset:1024 sc1\nv_cvt_pk_bf16_f32 v128, v128, v129\nv_cvt_pk_bf16_f32 v129, v130, v131\nv_cvt_pk_bf16_f32 v130, v132, v133\n"
"v_cvt_pk_bf16_f32 v131, v134, v135\nbuffer_store_dwordx4 v[128:131], v252, s[20:23], s84 offen offset:0 sc1\nv_cvt_pk_bf16_f32 v136, v136, v137\nv_cvt_pk_bf16_f32 v137, v138, v139\nv_cvt_pk_bf16_f32 v138, v140, v141\nv_cvt_pk_bf16_f32 v139, v142, v143\n"
"buffer_store_dwordx4 v[136:139], v252, s[20:23], s84 offen offset:1024 sc1\ns_add_u32 s61, s79, 88498176\ns_lshl_b32 s62, s42, 3\ns_add_u32 s61, s61, s62\ns_waitcnt vmcnt(0)\ns_mov_b64 exec, 1\n"
"buffer_atomic_swap_x2 v[238:239], off, s[20:23], s61 sc1\ns_mov_b64 exec, -1\nv_bfe_u32 v250, v252, 7, 1\nv_mul_u32_u24 v250, 2176, v250\nv_bfe_u32 v251, v252, 4, 3\nv_lshl_add_u32 v250, v251, 3, v250\n"
"s_lshl_b32 s60, s90, 6\nv_add_u32 v250, s60, v250\ns_sleep 5\nv_mov_b32 v248, 0\nv_mov_b32 v249, 0\ns_add_u32 s61, s80, 88498176\n"
"s_mov_b64 exec, 0xffff\nbuffer_atomic_or_x2 v[248:249], v250, s[20:23], s61 offen sc0 sc1\ns_mov_b64 exec, -1\ns_add_u32 m0, s50, 49152\ns_add_u32 s63, s53, 458752\nbuffer_load_dwordx4 v220, s[8:11], s63 offen lds\n"
"buffer_load_dwordx4 v221, s[8:11], s63 offen offset:1024 lds\ns_add_u32 m0, s50, 57344\ns_add_u32 s63, s53, 524288\nbuffer_load_dwordx4 v220, s[8:11], s63 offen lds\nbuffer_load_dwordx4 v221, s[8:11], s63 offen offset:1024 lds\ns_add_u32 m0, s50, 65536\n"
"s_add_u32 s63, s53, 589824\nbuffer_load_dwordx4 v220, s[8:11], s63 offen lds\nbuffer_load_dwordx4 v221, s[8:11], s63 offen offset:1024 lds\ns_add_u32 m0, s50, 73728\ns_add_u32 s63, s53, 655360\nbuffer_load_dwordx4 v220, s[8:11], s63 offen lds\n"
"buffer_load_dwordx4 v221, s[8:11], s63 offen offset:1024 lds\ns_add_u32 m0, s50, 81920\ns_add_u32 s63, s53, 720896\nbuffer_load_dwordx4 v220, s[8:11], s63 offen lds\nbuffer_load_dwordx4 v221, s[8:11], s63 offen offset:1024 lds\ns_add_u32 m0, s50, 90112\n"
"s_add_u32 s63, s53, 786432\nbuffer_load_dwordx4 v220, s[8:11], s63 offen lds\nbuffer_load_dwordx4 v221, s[8:11], s63 offen offset:1024 lds\nv_bfe_u32 v234, v252, 4, 4\nv_lshrrev_b32 v235, 8, v252\nv_lshlrev_b32 v235, 2, v235\n"
"v_sub_u32 v234, v234, v235\nv_lshlrev_b32 v235, 4, v234\ns_mov_b64 s[60:61], 0x3f80\nv_lshlrev_b64 v[208:209], v235, s[60:61]\nv_cmp_gt_u32 s[60:61], 4, v234\ns_nop 1\n"
"v_cndmask_b32 v212, 0, v208, s[60:61]\nv_cndmask_b32 v208, 0, v208, s[60:61]\nv_cndmask_b32 v213, 0, v209, s[60:61]\nv_cndmask_b32 v209, 0, v209, s[60:61]\ns_nop 1\nv_mfma_f32_16x16x32_bf16 v[0:3], v[208:211], v[192:195], 0\n"
"v_mfma_f32_16x16x32_bf16 v[4:7], v[210:213], v[192:195], 0\nv_mfma_f32_16x16x32_bf16 v[8:11], v[208:211], v[196:199], 0\nv_mfma_f32_16x16x32_bf16 v[12:15], v[210:213], v[196:199], 0\nv_mfma_f32_16x16x32_bf16 v[16:19], v[208:211], v[200:203], 0\nv_mfma_f32_16x16x32_bf16 v[20:23], v[210:213], v[200:203], 0\nv_mfma_f32_16x16x32_bf16 v[24:27], v[208:211], v[204:207], 0\n"
"v_mfma_f32_16x16x32_bf16 v[28:31], v[210:213], v[204:207], 0\ns_waitcnt vmcnt(12)\nv_cmp_eq_u64 s[86:87], s[46:47], v[248:249]\ns_nop 0\ns_and_b32 s62, s86, 0xffff\ns_cmp_eq_u32 s62, 0xffff\n"
"s_cbranch_scc0 Lq684\nLq685:\ns_add_u32 s64, s70, 6291456\nbuffer_load_dwordx4 a[0:3], v252, s[20:23], s64 offen offset:0 sc1\ns_add_u32 s65, s70, 6324224\nbuffer_load_dwordx4 a[4:7], v252, s[20:23], s65 offen offset:0 sc1\n"
"s_add_u32 s66, s71, 6291456\nbuffer_load_dwordx4 a[8:11], v252, s[20:23], s66 offen offset:0 sc1\ns_add_u32 s67, s71, 6324224\nbuffer_load_dwordx4 a[12:15], v252, s[20:23], s67 offen offset:0 sc1\nbuffer_load_dwordx4 a[16:19], v252, s[20:23], s64 offen offset:1024 sc1\nbuffer_load_dwordx4 a[20:23], v252, s[20:23], s65 offen offset:1024 sc1\n"
"buffer_load_dwordx4 a[24:27], v252, s[20:23], s66 offen offset:1024 sc1\nbuffer_load_dwordx4 a[28:31], v252, s[20:23], s67 offen offset:1024 sc1\ns_barrier\nR 160,188,49152\nR 162,188,53248\nR 164,184,0\n"
"R 166,184,4096\nR 168,189,49152\nR 170,189,53248\nR 172,185,0\nR 174,185,4096\nR 176,190,49152\n"
"R 178,190,53248\nR 180,186,0\nR 182,186,4096\nbuffer_load_dwordx4 a[32:35], v252, s[20:23], s64 offen offset:2048 sc1\nbuffer_load_dwordx4 a[36:39], v252, s[20:23], s65 offen offset:2048 sc1\nbuffer_load_dwordx4 a[40:43], v252, s[20:23], s66 offen offset:2048 sc1\n"
"buffer_load_dwordx4 a[44:47], v252, s[20:23], s67 offen offset:2048 sc1\nbuffer_load_dwordx4 a[48:51], v252, s[20:23], s64 offen offset:3072 sc1\nbuffer_load_dwordx4 a[52:55], v252, s[20:23], s65 offen offset:3072 sc1\nbuffer_load_dwordx4 a[56:59], v252, s[20:23], s66 offen offset:3072 sc1\nbuffer_load_dwordx4 a[60:63], v252, s[20:23], s67 offen offset:3072 sc1\ns_add_u32 s64, s70, 6295552\n"
"buffer_load_dwordx4 a[64:67], v252, s[20:23], s64 offen offset:0 sc1\ns_add_u32 s65, s70, 6328320\nbuffer_load_dwordx4 a[68:71], v252, s[20:23], s65 offen offset:0 sc1\ns_add_u32 s66, s71, 6295552\nbuffer_load_dwordx4 a[72:75], v252, s[20:23], s66 offen offset:0 sc1\ns_add_u32 s67, s71, 6328320\n"
"buffer_load_dwordx4 a[76:79], v252, s[20:23], s67 offen offset:0 sc1\ns_waitcnt vmcnt(19) lgkmcnt(10)\nM 0,160,0\nbuffer_load_dwordx4 a[80:83], v252, s[20:23], s64 offen offset:1024 sc1\ns_waitcnt vmcnt(16) lgkmcnt(8)\nM 0,164,16\n"
"M 16,160,4\ns_waitcnt vmcnt(15)\nM 16,164,20\nv_mfma_f32_16x16x32_bf16 v[32:35], v[160:163], a[8:11], 0\nbuffer_load_dwordx4 a[84:87], v252, s[20:23], s65 offen offset:1024 sc1\ns_waitcnt vmcnt(15)\n"
"M 32,164,24\nv_mfma_f32_16x16x32_bf16 v[48:51], v[160:163], a[12:15], 0\ns_waitcnt vmcnt(14)\nM 48,164,28\nG 4\nv_mfma_f32_16x16x32_bf16 v[52:55], v[168:171], a[12:15], 0\n"
"R 160,191,49152\nM 52,172,28\nbuffer_load_dwordx4 a[88:91], v252, s[20:23], s66 offen offset:1024 sc1\nv_mfma_f32_16x16x32_bf16 v[36:39], v[168:171], a[8:11], 0\nR 162,191,53248\nM 36,172,24\n"
"M 20,168,4\nR 164,187,0\nM 20,172,20\nbuffer_load_dwordx4 a[92:95], v252, s[20:23], s67 offen offset:1024 sc1\nM 4,168,0\nR 166,187,4096\n"
"M 4,172,16\nG 4\nM 8,176,0\nR 168,184,8192\nM 8,180,16\nbuffer_load_dwordx4 a[96:99], v252, s[20:23], s64 offen offset:2048 sc1\n"
"M 24,176,4\nR 170,184,12288\nM 24,180,20\nv_mfma_f32_16x16x32_bf16 v[40:43], v[176:179], a[8:11], 0\nR 172,184,16384\nM 40,180,24\n"
"buffer_load_dwordx4 a[100:103], v252, s[20:23], s65 offen offset:2048 sc1\nv_mfma_f32_16x16x32_bf16 v[56:59], v[176:179], a[12:15], 0\nR 174,184,20480\nM 56,180,28\nG 4\nv_mfma_f32_16x16x32_bf16 v[60:63], v[160:163], a[12:15], 0\n"
"R 176,185,8192\nM 60,164,28\nbuffer_load_dwordx4 a[104:107], v252, s[20:23], s66 offen offset:2048 sc1\nv_mfma_f32_16x16x32_bf16 v[44:47], v[160:163], a[8:11], 0\nR 178,185,12288\nM 44,164,24\n"
"M 28,160,4\nR 180,185,16384\nM 28,164,20\nbuffer_load_dwordx4 a[108:111], v252, s[20:23], s67 offen offset:2048 sc1\nM 12,160,0\nR 182,185,20480\n"
"M 12,164,16\nG 4\ns_waitcnt vmcnt(19)\nM 0,168,32\nR 160,186,8192\ns_waitcnt vmcnt(15)\n"
"M 0,172,48\nbuffer_load_dwordx4 a[112:115], v252, s[20:23], s64 offen offset:3072 sc1\nM 16,168,36\nR 162,186,12288\ns_waitcnt vmcnt(15)\nM 16,172,52\n"
"M 32,168,40\nR 164,186,16384\ns_waitcnt vmcnt(14)\nM 32,172,56\nbuffer_load_dwordx4 a[116:119], v252, s[20:23], s65 offen offset:3072 sc1\nM 48,168,44\n"
"R 166,186,20480\ns_waitcnt vmcnt(14)\nM 48,172,60\nG 4\nM 52,176,44\nR 168,187,8192\n"
"M 52,180,60\nbuffer_load_dwordx4 a[120:123], v252, s[20:23], s66 offen offset:3072 sc1\nM 36,176,40\nR 170,187,12288\nM 36,180,56\nM 20,176,36\n"
"R 172,187,16384\nM 20,180,52\nbuffer_load_dwordx4 a[124:127], v252, s[20:23], s67 offen offset:3072 sc1\nM 4,176,32\nR 174,187,20480\nM 4,180,48\n"
"G 4\nM 8,160,32\nR 176,184,24576\nM 8,164,48\ns_add_u32 s64, s70, 6299648\nbuffer_load_dwordx4 a[128:131], v252, s[20:23], s64 offen offset:0 sc1\n"
"M 24,160,36\nR 178,184,28672\nM 24,164,52\nM 40,160,40\nR 180,184,32768\nM 40,164,56\n"
"s_add_u32 s65, s70, 6332416\nbuffer_load_dwordx4 a[132:135], v252, s[20:23], s65 offen offset:0 sc1\nM 56,160,44\nR 182,184,36864\nM 56,164,60\nG 4\n"
"M 60,168,44\nR 160,185,24576\nM 60,172,60\ns_add_u32 s66, s71, 6299648\nbuffer_load_dwordx4 a[136:139], v252, s[20:23], s66 offen offset:0 sc1\nM 44,168,40\n"
"R 162,185,28672\nM 44,172,56\nM 28,168,36\nR 164,185,32768\nM 28,172,52\ns_add_u32 s67, s71, 6332416\n"
"buffer_load_dwordx4 a[140:143], v252, s[20:23], s67 offen offset:0 sc1\nM 12,168,32\nR 166,185,36864\nM 12,172,48\ns_barrier\nG 4\n"
"s_waitcnt vmcnt(19)\nM 0,176,64\nR 168,186,24576\ns_waitcnt vmcnt(15)\nM 0,180,80\nbuffer_load_dwordx4 a[144:147], v252, s[20:23], s64 offen offset:1024 sc1\n"
"M 16,176,68\nR 170,186,28672\ns_waitcnt vmcnt(15)\nM 16,180,84\nM 32,176,72\nR 172,186,32768\n"
"s_waitcnt vmcnt(14)\nM 32,180,88\nbuffer_load_dwordx4 a[148:151], v252, s[20:23], s65 offen offset:1024 sc1\nM 48,176,76\nR 174,186,36864\ns_waitcnt vmcnt(14)\n"
"M 48,180,92\nG 4\nM 52,160,76\nR 176,187,24576\nM 52,164,92\nbuffer_load_dwordx4 a[152:155], v252, s[20:23], s66 offen offset:1024 sc1\n"
"M 36,160,72\nR 178,187,28672\nM 36,164,88\nM 20,160,68\nR 180,187,32768\nM 20,164,84\n"
"buffer_load_dwordx4 a[156:159], v252, s[20:23], s67 offen offset:1024 sc1\nM 4,160,64\nR 182,187,36864\nM 4,164,80\nG 4\nM 8,168,64\n"
"R 160,184,40960\nM 8,172,80\nbuffer_load_dwordx4 a[160:163], v252, s[20:23], s64 offen offset:2048 sc1\nM 24,168,68\nR 162,184,45056\nM 24,172,84\n"
"M 40,168,72\nR 164,184,49152\nM 40,172,88\nbuffer_load_dwordx4 a[164:167], v252, s[20:23], s65 offen offset:2048 sc1\nM 56,168,76\nR 166,184,53248\n"
"M 56,172,92\nG 4\nM 60,176,76\nR 168,185,40960\nM 60,180,92\nbuffer_load_dwordx4 a[168:171], v252, s[20:23], s66 offen offset:2048 sc1\n"
"M 44,176,72\nR 170,185,45056\nM 44,180,88\nM 28,176,68\nR 172,185,49152\nM 28,180,84\n"
"buffer_load_dwordx4 a[172:175], v252, s[20:23], s67 offen offset:2048 sc1\nM 12,176,64\nR 174,185,53248\nM 12,180,80\nG 4\ns_waitcnt vmcnt(19)\n"
"M 0,160,96\nR 176,186,40960\ns_waitcnt vmcnt(15)\nM 0,164,112\nbuffer_load_dwordx4 a[176:179], v252, s[20:23], s64 offen offset:3072 sc1\nM 16,160,100\n"
"R 178,186,45056\ns_add_u32 s63, s53, 851968\ns_waitcnt vmcnt(15)\nM 16,164,116\ns_add_u32 m0, s50, 98304\nM 32,160,104\n"
"R 180,186,49152\ns_waitcnt vmcnt(14)\nM 32,164,120\nbuffer_load_dwordx4 a[180:183], v252, s[20:23], s65 offen offset:3072 sc1\nM 48,160,108\nR 182,186,53248\n"
"s_waitcnt vmcnt(14)\nM 48,164,124\nbuffer_load_dwordx4 v220, s[8:11], s63 offen lds\nG 4\nM 52,168,108\nR 160,187,40960\n"
"M 52,172,124\nbuffer_load_dwordx4 a[184:187], v252, s[20:23], s66 offen offset:3072 sc1\nM 36,168,104\nR 162,187,45056\nM 36,172,120\nbuffer_load_dwordx4 v221, s[8:11], s63 offen offset:1024 lds\n"
"M 20,168,100\nR 164,187,49152\ns_add_u32 m0, s50, 106496\nM 20,172,116\nbuffer_load_dwordx4 a[188:191], v252, s[20:23], s67 offen offset:3072 sc1\nM 4,168,96\n"
"R 166,187,53248\ns_add_u32 s63, s53, 917504\nM 4,172,112\nbuffer_load_dwordx4 v220, s[8:11], s63 offen lds\nG 4\nM 8,176,96\n"
"R 168,184,57344\nM 8,180,112\ns_add_u32 s64, s70, 6303744\nbuffer_load_dwordx4 a[192:195], v252, s[20:23], s64 offen offset:0 sc1\nM 24,176,100\nR 170,184,61440\n"
"M 24,180,116\nbuffer_load_dwordx4 v221, s[8:11], s63 offen offset:1024 lds\nM 40,176,104\nR 172,188,0\ns_add_u32 m0, s50, 114688\nM 40,180,120\n"
"s_add_u32 s65, s70, 6336512\nbuffer_load_dwordx4 a[196:199], v252, s[20:23], s65 offen offset:0 sc1\nM 56,176,108\nR 174,188,4096\ns_add_u32 s63, s53, 983040\nM 56,180,124\n"
"buffer_load_dwordx4 v220, s[8:11], s63 offen lds\nG 4\nM 60,160,108\nR 176,185,57344\nM 60,164,124\ns_add_u32 s66, s71, 6303744\n"
"buffer_load_dwordx4 a[200:203], v252, s[20:23], s66 offen offset:0 sc1\nM 44,160,104\nR 178,185,61440\nM 44,164,120\nbuffer_load_dwordx4 v221, s[8:11], s63 offen offset:1024 lds\nM 28,160,100\n"
"R 180,189,0\nM 28,164,116\ns_add_u32 s67, s71, 6336512\nbuffer_load_dwordx4 a[204:207], v252, s[20:23], s67 offen offset:0 sc1\nM 12,160,96\nR 182,189,4096\n"
"M 12,164,112\ns_barrier\nG 4\ns_waitcnt vmcnt(25)\nM 0,168,128\nR 160,186,57344\n"
"s_waitcnt vmcnt(21)\nM 0,172,144\nbuffer_load_dwordx4 a[208:211], v252, s[20:23], s64 offen offset:1024 sc1\nM 16,168,132\nR 162,186,61440\ns_waitcnt vmcnt(21)\n"
"M 16,172,148\nM 32,168,136\nR 164,190,0\ns_waitcnt vmcnt(20)\nM 32,172,152\nbuffer_load_dwordx4 a[212:215], v252, s[20:23], s65 offen offset:1024 sc1\n"
"M 48,168,140\nR 166,190,4096\ns_waitcnt vmcnt(20)\nM 48,172,156\nG 4\nM 52,176,140\n"
"R 168,187,57344\nM 52,180,156\nbuffer_load_dwordx4 a[216:219], v252, s[20:23], s66 offen offset:1024 sc1\nM 36,176,136\nR 170,187,61440\nM 36,180,152\n"
"M 20,176,132\nR 172,191,0\nM 20,180,148\nbuffer_load_dwordx4 a[220:223], v252, s[20:23], s67 offen offset:1024 sc1\nM 4,176,128\nR 174,191,4096\n"
"M 4,180,144\nG 4\nM 8,160,128\nR 176,188,8192\nM 8,164,144\nbuffer_load_dwordx4 a[224:227], v252, s[20:23], s64 offen offset:2048 sc1\n"
"M 24,160,132\nR 178,188,12288\nM 24,164,148\nM 40,160,136\nR 180,188,16384\nM 40,164,152\n"
"buffer_load_dwordx4 a[228:231], v252, s[20:23], s65 offen offset:2048 sc1\nM 56,160,140\nR 182,188,20480\nM 56,164,156\nG 4\nM 60,168,140\n"
"R 160,189,8192\nM 60,172,156\nbuffer_load_dwordx4 a[232:235], v252, s[20:23], s66 offen offset:2048 sc1\nM 44,168,136\nR 162,189,12288\nM 44,172,152\n"
"M 28,168,132\nR 164,189,16384\nM 28,172,148\nbuffer_load_dwordx4 a[236:239], v252, s[20:23], s67 offen offset:2048 sc1\nM 12,168,128\nR 166,189,20480\n"
"M 12,172,144\nG 4\ns_waitcnt vmcnt(25)\nM 0,176,160\nR 168,190,8192\ns_waitcnt vmcnt(21)\n"
"M 0,180,176\nbuffer_load_dwordx4 a[240:243], v252, s[20:23], s64 offen offset:3072 sc1\nM 16,176,164\nR 170,190,12288\ns_waitcnt vmcnt(21)\nM 16,180,180\n"
"M 32,176,168\nR 172,190,16384\ns_waitcnt vmcnt(19)\nM 32,180,184\nbuffer_load_dwordx4 a[244:247], v252, s[20:23], s65 offen offset:3072 sc1\nM 48,176,172\n"
"R 174,190,20480\ns_waitcnt vmcnt(18)\nM 48,180,188\nG 4\nM 52,160,172\nR 176,191,8192\n"
"M 52,164,188\nbuffer_load_dwordx4 a[248:251], v252, s[20:23], s66 offen offset:3072 sc1\nM 36,160,168\nR 178,191,12288\ns_waitcnt vmcnt(12)\ns_barrier\n"
"M 36,164,184\nM 20,160,164\nR 180,191,16384\nM 20,164,180\nbuffer_load_dwordx4 a[252:255], v252, s[20:23], s67 offen offset:3072 sc1\nM 4,160,160\n"
"R 182,191,20480\nM 4,164,176\nG 4\nM 8,168,160\nR 160,188,24576\nM 8,172,176\n"
"M 24,168,164\nR 162,188,28672\nM 24,172,180\nM 40,168,168\nR 164,188,32768\nM 40,172,184\n"
"M 56,168,172\nR 166,188,36864\nM 56,172,188\nG 4\nM 60,176,172\nR 168,189,24576\n"
"M 60,180,188\ns_movk_i32 s63, 7168\nv_mad_u32_u24 v220, v228, s63, v229\nv_add_u32 v221, 3668992, v220\ns_add_u32 m0, s50, 0\ns_add_u32 s63, s54, 0\n"
"buffer_load_dwordx4 v220, s[12:15], s63 offen lds\nM 44,176,168\nR 170,189,28672\nM 44,180,184\nM 28,176,164\nR 172,189,32768\n"
"M 28,180,180\nbuffer_load_dwordx4 v221, s[12:15], s63 offen offset:1024 lds\nM 12,176,160\nR 174,189,36864\ns_add_u32 m0, s50, 8192\nM 12,180,176\n"
"G 4\nM 32,160,200\nR 176,190,24576\ns_add_u32 s63, s54, 229376\ns_waitcnt vmcnt(11)\nM 32,164,216\n"
"R 178,190,28672\nM 48,160,204\nR 180,190,32768\ns_waitcnt vmcnt(10)\nM 48,164,220\nR 182,190,36864\n"
"G 4\nM 52,168,204\nR 160,191,24576\nbuffer_load_dwordx4 v220, s[12:15], s63 offen lds\nM 52,172,220\nR 162,191,28672\n"
"M 36,168,200\nR 164,191,32768\nM 36,172,216\nR 166,191,36864\nG 4\nM 40,176,200\n"
"R 168,188,40960\nbuffer_load_dwordx4 v221, s[12:15], s63 offen offset:1024 lds\nM 40,180,216\nR 170,188,45056\ns_add_u32 m0, s50, 16384\nM 56,176,204\n"
"R 172,188,49152\ns_add_u32 s63, s54, 128\nM 56,180,220\nR 174,188,53248\nG 4\nM 60,160,204\n"
"R 176,189,40960\nbuffer_load_dwordx4 v220, s[12:15], s63 offen lds\nM 60,164,220\nR 178,189,45056\nM 44,160,200\nR 180,189,49152\n"
"M 44,164,216\nR 182,189,53248\nG 4\ns_waitcnt vmcnt(10)\nM 32,168,232\nR 160,190,40960\n"
"buffer_load_dwordx4 v221, s[12:15], s63 offen offset:1024 lds\ns_waitcnt vmcnt(7)\nM 32,172,248\nR 162,190,45056\ns_add_u32 m0, s50, 24576\nM 48,168,236\n"
"R 164,190,49152\ns_add_u32 s63, s54, 229504\ns_waitcnt vmcnt(6)\nM 48,172,252\nR 166,190,53248\nG 4\n"
"M 52,176,236\nR 168,191,40960\nbuffer_load_dwordx4 v220, s[12:15], s63 offen lds\nM 52,180,252\nR 170,191,45056\nM 36,176,232\n"
"R 172,191,49152\nM 36,180,248\nR 174,191,53248\nG 4\nM 40,160,232\nR 176,188,24576\n"
"buffer_load_dwordx4 v221, s[12:15], s63 offen offset:1024 lds\nM 40,164,248\nR 178,188,28672\nM 56,160,236\nR 180,188,32768\nM 56,164,252\n"
"R 182,188,36864\nG 4\nM 60,168,236\nR 160,189,24576\nM 60,172,252\nR 162,189,28672\n"
"M 44,168,232\nR 164,189,32768\nM 44,172,248\nR 166,189,36864\nG 4\nM 0,176,192\n"
"R 168,190,24576\nM 0,180,208\nR 170,190,28672\ns_mov_b32 m0, s51\nM 16,176,196\nR 172,190,32768\n"
"v_cvt_pk_bf16_f32 v32, v32, v33\nv_cvt_pk_bf16_f32 v33, v34, v35\nM 16,180,212\nR 174,190,36864\nG 4\nv_cvt_pk_bf16_f32 v34, v36, v37\n"
"M 20,160,196\nR 176,191,24576\nv_cvt_pk_bf16_f32 v35, v38, v39\nv_cvt_pk_bf16_f32 v36, v40, v41\nM 20,164,212\nR 178,191,28672\n"
"v_cvt_pk_bf16_f32 v37, v42, v43\nv_cvt_pk_bf16_f32 v38, v44, v45\nM 4,160,192\nR 180,191,32768\nv_cvt_pk_bf16_f32 v39, v46, v47\nv_cvt_pk_bf16_f32 v40, v48, v49\n"
"M 4,164,208\nR 182,191,36864\nG 4\nv_cvt_pk_bf16_f32 v41, v50, v51\nM 8,168,192\nR 160,188,40960\n"
"v_cvt_pk_bf16_f32 v42, v52, v53\nv_cvt_pk_bf16_f32 v43, v54, v55\nM 8,172,208\nR 162,188,45056\nv_cvt_pk_bf16_f32 v44, v56, v57\nv_cvt_pk_bf16_f32 v45, v58, v59\n"
"M 24,168,196\nR 164,188,49152\nv_cvt_pk_bf16_f32 v46, v60, v61\nv_cvt_pk_bf16_f32 v47, v62, v63\nM 24,172,212\nR 166,188,53248\n"
"ds_write_addtid_b32 v32 offset:0\nG 5\nM 28,176,196\nR 168,189,40960\nds_write_addtid_b32 v33 offset:256\nM 28,180,212\n"
"R 170,189,45056\nds_write_addtid_b32 v34 offset:512\nM 12,176,192\nR 172,189,49152\nds_write_addtid_b32 v35 offset:768\nds_write_addtid_b32 v36 offset:1024\n"
"ds_write_addtid_b32 v37 offset:1280\nds_write_addtid_b32 v38 offset:1536\nds_write_addtid_b32 v39 offset:1792\nds_write_addtid_b32 v40 offset:2048\nds_write_addtid_b32 v41 offset:2304\nds_write_addtid_b32 v42 offset:2560\n"
"ds_write_addtid_b32 v43 offset:2816\nds_write_addtid_b32 v44 offset:3072\nds_write_addtid_b32 v45 offset:3328\nds_write_addtid_b32 v46 offset:3584\nds_write_addtid_b32 v47 offset:3840\nM 12,180,208\n"
"R 174,189,53248\nG 15\nM 0,160,224\nR 176,190,40960\ns_add_u32 m0, s50, 32768\nM 0,164,240\n"
"R 178,190,45056\nG 3\ns_barrier\ns_add_u32 s63, s54, 458752\nM 16,160,228\nR 180,190,49152\n"
"buffer_load_dwordx4 v220, s[12:15], s63 offen lds\nM 16,164,244\nR 182,190,53248\nG 4\nM 20,168,228\nR 160,191,40960\n"
"M 20,172,244\nR 162,191,45056\nM 4,168,224\nR 164,191,49152\nbuffer_load_dwordx4 v221, s[12:15], s63 offen offset:1024 lds\nM 4,172,240\n"
"R 166,191,53248\ns_add_u32 m0, s50, 40960\ns_add_u32 s63, s54, 688128\nG 4\nM 8,176,224\nbuffer_load_dwordx4 v220, s[12:15], s63 offen lds\n"
"M 8,180,240\nds_read2st64_b32 v[32:33], v225 offset0:0 offset1:1\nM 24,176,228\nds_read2st64_b32 v[34:35], v225 offset0:2 offset1:3\nM 24,180,244\nds_read2st64_b32 v[36:37], v225 offset0:4 offset1:5\n"
"G 3\nM 28,160,228\nbuffer_load_dwordx4 v221, s[12:15], s63 offen offset:1024 lds\nM 28,164,244\nds_read2st64_b32 v[38:39], v225 offset0:6 offset1:7\nM 12,160,224\n"
"ds_read2st64_b32 v[40:41], v225 offset0:8 offset1:9\nds_read2st64_b32 v[42:43], v225 offset0:10 offset1:11\nM 12,164,240\nds_read2st64_b32 v[44:45], v225 offset0:12 offset1:13\nds_read2st64_b32 v[46:47], v225 offset0:14 offset1:15\nG 0\n"
"s_barrier\nv_mfma_f32_16x16x32_bf16 v[0:3], v[208:211], v[32:35], v[0:3]\nv_mfma_f32_16x16x32_bf16 v[4:7], v[210:213], v[32:35], v[4:7]\nv_mfma_f32_16x16x32_bf16 v[8:11], v[208:211], v[36:39], v[8:11]\nv_mfma_f32_16x16x32_bf16 v[12:15], v[210:213], v[36:39], v[12:15]\nv_mfma_f32_16x16x32_bf16 v[16:19], v[208:211], v[40:43], v[16:19]\n"
"v_mfma_f32_16x16x32_bf16 v[20:23], v[210:213], v[40:43], v[20:23]\nv_mfma_f32_16x16x32_bf16 v[24:27], v[208:211], v[44:47], v[24:27]\nv_mfma_f32_16x16x32_bf16 v[28:31], v[210:213], v[44:47], v[28:31]\ns_lshl_b32 s84, s75, 11\ns_add_u32 s84, s84, s49\ns_add_u32 s84, s84, 14680064\n"
"s_add_u32 s85, s84, 32768\nv_mov_b32 v238, s46\nv_mov_b32 v239, s47\nv_cvt_pk_bf16_f32 v192, v0, v1\nv_cvt_pk_bf16_f32 v193, v2, v3\nv_cvt_pk_bf16_f32 v194, v4, v5\n"
"v_cvt_pk_bf16_f32 v195, v6, v7\nbuffer_store_dwordx4 v[192:195], v252, s[20:23], s84 offen offset:0 sc1\nv_cvt_pk_bf16_f32 v196, v8, v9\nv_cvt_pk_bf16_f32 v197, v10, v11\nv_cvt_pk_bf16_f32 v198, v12, v13\nv_cvt_pk_bf16_f32 v199, v14, v15\n"
"buffer_store_dwordx4 v[196:199], v252, s[20:23], s84 offen offset:1024 sc1\nv_cvt_pk_bf16_f32 v200, v16, v17\nv_cvt_pk_bf16_f32 v201, v18, v19\nv_cvt_pk_bf16_f32 v202, v20, v21\nv_cvt_pk_bf16_f32 v203, v22, v23\nbuffer_store_dwordx4 v[200:203], v252, s[20:23], s85 offen offset:0 sc1\n"
"v_cvt_pk_bf16_f32 v204, v24, v25\nv_cvt_pk_bf16_f32 v205, v26, v27\nv_cvt_pk_bf16_f32 v206, v28, v29\nv_cvt_pk_bf16_f32 v207, v30, v31\nbuffer_store_dwordx4 v[204:207], v252, s[20:23], s85 offen offset:1024 sc1\ns_add_u32 s61, s79, 88637440\n"
"s_lshl_b32 s62, s75, 3\ns_add_u32 s61, s61, s62\ns_waitcnt vmcnt(0)\ns_mov_b64 exec, 1\nbuffer_atomic_swap_x2 v[238:239], off, s[20:23], s61 sc1\ns_mov_b64 exec, -1\n"
"Lqw1:\nv_bfe_u32 v250, v252, 7, 1\nv_mul_u32_u24 v250, 2176, v250\nv_bfe_u32 v251, v252, 4, 3\nv_lshl_add_u32 v250, v251, 3, v250\ns_lshl_b32 s60, s90, 6\n"
"v_add_u32 v250, s60, v250\ns_sleep 5\nv_mov_b32 v248, 0\nv_mov_b32 v249, 0\ns_add_u32 s61, s80, 88637440\ns_mov_b64 exec, 0xffff\n"
"buffer_atomic_or_x2 v[248:249], v250, s[20:23], s61 offen sc0 sc1\ns_mov_b64 exec, -1\ns_add_u32 m0, s50, 49152\ns_add_u32 s63, s54, 458880\nbuffer_load_dwordx4 v220, s[12:15], s63 offen lds\nbuffer_load_dwordx4 v221, s[12:15], s63 offen offset:1024 lds\n"
"s_add_u32 m0, s50, 57344\ns_add_u32 s63, s54, 688256\nbuffer_load_dwordx4 v220, s[12:15], s63 offen lds\nbuffer_load_dwordx4 v221, s[12:15], s63 offen offset:1024 lds\ns_add_u32 m0, s50, 65536\ns_add_u32 s63, s54, 917504\n"
"buffer_load_dwordx4 v220, s[12:15], s63 offen lds\nbuffer_load_dwordx4 v221, s[12:15], s63 offen offset:1024 lds\ns_add_u32 m0, s50, 73728\ns_add_u32 s63, s54, 1146880\nbuffer_load_dwordx4 v220, s[12:15], s63 offen lds\nbuffer_load_dwordx4 v221, s[12:15], s63 offen offset:1024 lds\n"
"s_add_u32 m0, s50, 81920\ns_add_u32 s63, s54, 917632\nbuffer_load_dwordx4 v220, s[12:15], s63 offen lds\nbuffer_load_dwordx4 v221, s[12:15], s63 offen offset:1024 lds\ns_add_u32 m0, s50, 90112\ns_add_u32 s63, s54, 1147008\n"
"buffer_load_dwordx4 v220, s[12:15], s63 offen lds\nbuffer_load_dwordx4 v221, s[12:15], s63 offen offset:1024 lds\nv_mov_b32 v230, 0\nv_mov_b32 v231, 0\nv_mov_b32 v232, 0\nv_mov_b32 v233, 0\n"
"v_dot2c_f32_bf16 v230, s76, v192\nv_dot2c_f32_bf16 v231, v192, v192\nv_dot2c_f32_bf16 v232, s76, v200\nv_dot2c_f32_bf16 v233, v200, v200\nv_dot2c_f32_bf16 v230, s76, v193\nv_dot2c_f32_bf16 v231, v193, v193\n"
"v_dot2c_f32_bf16 v232, s76, v201\nv_dot2c_f32_bf16 v233, v201, v201\nv_dot2c_f32_bf16 v230, s76, v194\nv_dot2c_f32_bf16 v231, v194, v194\nv_dot2c_f32_bf16 v232, s76, v202\nv_dot2c_f32_bf16 v233, v202, v202\n"
"v_dot2c_f32_bf16 v230, s76, v195\nv_dot2c_f32_bf16 v231, v195, v195\nv_dot2c_f32_bf16 v232, s76, v203\nv_dot2c_f32_bf16 v233, v203, v203\nv_dot2c_f32_bf16 v230, s76, v196\nv_dot2c_f32_bf16 v231, v196, v196\n"
"v_dot2c_f32_bf16 v232, s76, v204\nv_dot2c_f32_bf16 v233, v204, v204\nv_dot2c_f32_bf16 v230, s76, v197\nv_dot2c_f32_bf16 v231, v197, v197\nv_dot2c_f32_bf16 v232, s76, v205\nv_dot2c_f32_bf16 v233, v205, v205\n"
"v_dot2c_f32_bf16 v230, s76, v198\nv_dot2c_f32_bf16 v231, v198, v198\nv_dot2c_f32_bf16 v232, s76, v206\nv_dot2c_f32_bf16 v233, v206, v206\nv_dot2c_f32_bf16 v230, s76, v199\nv_dot2c_f32_bf16 v231, v199, v199\n"
"v_dot2c_f32_bf16 v232, s76, v207\nv_dot2c_f32_bf16 v233, v207, v207\nv_lshrrev_b32 v234, 1, v252\nv_permlane32_swap_b32 v230, v231\ns_nop 0\nv_permlane32_swap_b32 v232, v233\n"
"v_add_f32 v230, v230, v231\nv_add_f32 v232, v232, v233\nv_mov_b32 v231, s46\ns_nop 0\nv_permlane16_swap_b32 v230, v232\nv_add_f32 v230, v230, v232\n"
"s_lshr_b32 s61, s49, 3\ns_lshl2_add_u32 s61, s53, s61\ns_add_u32 s61, s61, 83361792\nbuffer_store_dwordx2 v[230:231], v234, s[20:23], s61 offen sc1\ns_waitcnt vmcnt(13)\nv_cmp_eq_u64 s[86:87], s[46:47], v[248:249]\n"
"s_nop 0\ns_and_b32 s62, s86, 0xffff\ns_cmp_eq_u32 s62, 0xffff\ns_cbranch_scc0 Lq736\nLq737:\ns_add_u32 s64, s70, 14680064\n"
"buffer_load_dwordx4 a[0:3], v252, s[20:23], s64 offen offset:0 sc1\ns_add_u32 s65, s70, 14712832\nbuffer_load_dwordx4 a[4:7], v252, s[20:23], s65 offen offset:0 sc1\ns_add_u32 s66, s71, 14680064\nbuffer_load_dwordx4 a[8:11], v252, s[20:23], s66 offen offset:0 sc1\ns_add_u32 s67, s71, 14712832\n"
"buffer_load_dwordx4 a[12:15], v252, s[20:23], s67 offen offset:0 sc1\nbuffer_load_dwordx4 a[16:19], v252, s[20:23], s64 offen offset:1024 sc1\nbuffer_load_dwordx4 a[20:23], v252, s[20:23], s65 offen offset:1024 sc1\nbuffer_load_dwordx4 a[24:27], v252, s[20:23], s66 offen offset:1024 sc1\nbuffer_load_dwordx4 a[28:31], v252, s[20:23], s67 offen offset:1024 sc1\ns_barrier\n"
"R 160,184,0\nR 162,184,4096\nR 164,184,8192\nR 166,184,12288\nR 168,185,0\nR 170,185,4096\n"
"R 172,185,8192\nR 174,185,12288\nR 176,186,0\nR 178,186,4096\nR 180,186,8192\nR 182,186,12288\n"
"buffer_load_dwordx4 a[32:35], v252, s[20:23], s64 offen offset:2048 sc1\nbuffer_load_dwordx4 a[36:39], v252, s[20:23], s65 offen offset:2048 sc1\nbuffer_load_dwordx4 a[40:43], v252, s[20:23], s66 offen offset:2048 sc1\nbuffer_load_dwordx4 a[44:47], v252, s[20:23], s67 offen offset:2048 sc1\nbuffer_load_dwordx4 a[48:51], v252, s[20:23], s64 offen offset:3072 sc1\nbuffer_load_dwordx4 a[52:55], v252, s[20:23], s65 offen offset:3072 sc1\n"
"buffer_load_dwordx4 a[56:59], v252, s[20:23], s66 offen offset:3072 sc1\nbuffer_load_dwordx4 a[60:63], v252, s[20:23], s67 offen offset:3072 sc1\ns_waitcnt vmcnt(15) lgkmcnt(10)\nv_mfma_f32_16x16x32_bf16 v[32:35], v[160:163], a[0:3], 0\ns_add_u32 s64, s70, 14684160\nbuffer_load_dwordx4 a[64:67], v252, s[20:23], s64 offen offset:0 sc1\n"
"s_waitcnt vmcnt(12) lgkmcnt(8)\nM 32,164,16\nv_mfma_f32_16x16x32_bf16 v[48:51], v[160:163], a[4:7], 0\ns_waitcnt vmcnt(11)\nM 48,164,20\nv_mfma_f32_16x16x32_bf16 v[64:67], v[160:163], a[8:11], 0\n"
"s_waitcnt vmcnt(10)\nM 64,164,24\ns_add_u32 s65, s70, 14716928\nbuffer_load_dwordx4 a[68:71], v252, s[20:23], s65 offen offset:0 sc1\nv_mfma_f32_16x16x32_bf16 v[80:83], v[160:163], a[12:15], 0\ns_waitcnt vmcnt(10)\n"
"M 80,164,28\nG 4\nv_mfma_f32_16x16x32_bf16 v[84:87], v[168:171], a[12:15], 0\nR 160,187,0\nM 84,172,28\nv_mfma_f32_16x16x32_bf16 v[68:71], v[168:171], a[8:11], 0\n"
"R 162,187,4096\nM 68,172,24\ns_add_u32 s66, s71, 14684160\nbuffer_load_dwordx4 a[72:75], v252, s[20:23], s66 offen offset:0 sc1\nv_mfma_f32_16x16x32_bf16 v[52:55], v[168:171], a[4:7], 0\nR 164,187,8192\n"
"M 52,172,20\nv_mfma_f32_16x16x32_bf16 v[36:39], v[168:171], a[0:3], 0\nR 166,187,12288\nM 36,172,16\nG 4\nv_mfma_f32_16x16x32_bf16 v[40:43], v[176:179], a[0:3], 0\n"
"R 168,184,16384\nM 40,180,16\ns_add_u32 s67, s71, 14716928\nbuffer_load_dwordx4 a[76:79], v252, s[20:23], s67 offen offset:0 sc1\nv_mfma_f32_16x16x32_bf16 v[56:59], v[176:179], a[4:7], 0\nR 170,184,20480\n"
"M 56,180,20\nv_mfma_f32_16x16x32_bf16 v[72:75], v[176:179], a[8:11], 0\nR 172,184,24576\nM 72,180,24\nv_mfma_f32_16x16x32_bf16 v[88:91], v[176:179], a[12:15], 0\nR 174,184,28672\n"
"M 88,180,28\nG 4\nv_mfma_f32_16x16x32_bf16 v[92:95], v[160:163], a[12:15], 0\nR 176,185,16384\nM 92,164,28\nbuffer_load_dwordx4 a[80:83], v252, s[20:23], s64 offen offset:1024 sc1\n"
"v_mfma_f32_16x16x32_bf16 v[76:79], v[160:163], a[8:11], 0\nR 178,185,20480\nM 76,164,24\nv_mfma_f32_16x16x32_bf16 v[60:63], v[160:163], a[4:7], 0\nR 180,185,24576\nM 60,164,20\n"
"buffer_load_dwordx4 a[84:87], v252, s[20:23], s65 offen offset:1024 sc1\nv_mfma_f32_16x16x32_bf16 v[44:47], v[160:163], a[0:3], 0\nR 182,185,28672\nM 44,164,16\nG 4\nv_mfma_f32_16x16x32_bf16 v[96:99], v[168:171], a[0:3], 0\n"
"R 160,186,16384\nM 96,172,16\nv_mfma_f32_16x16x32_bf16 v[112:115], v[168:171], a[4:7], 0\nR 162,186,20480\nM 112,172,20\nbuffer_load_dwordx4 a[88:91], v252, s[20:23], s66 offen offset:1024 sc1\n"
"v_mfma_f32_16x16x32_bf16 v[128:131], v[168:171], a[8:11], 0\nR 164,186,24576\nM 128,172,24\nv_mfma_f32_16x16x32_bf16 v[144:147], v[168:171], a[12:15], 0\nR 166,186,28672\nM 144,172,28\n"
"s_barrier\nG 4\nv_mfma_f32_16x16x32_bf16 v[148:151], v[176:179], a[12:15], 0\nR 168,187,16384\ns_add_u32 s63, s54, 1376256\nM 148,180,28\n"
"buffer_load_dwordx4 a[92:95], v252, s[20:23], s67 offen offset:1024 sc1\nv_mfma_f32_16x16x32_bf16 v[132:135], v[176:179], a[8:11], 0\nR 170,187,20480\nM 132,180,24\ns_add_u32 m0, s50, 98304\nv_mfma_f32_16x16x32_bf16 v[116:119], v[176:179], a[4:7], 0\n"
"R 172,187,24576\nM 116,180,20\nbuffer_load_dwordx4 v220, s[12:15], s63 offen lds\nv_mfma_f32_16x16x32_bf16 v[100:103], v[176:179], a[0:3], 0\nR 174,187,28672\nM 100,180,16\n"
"G 4\nv_mfma_f32_16x16x32_bf16 v[104:107], v[160:163], a[0:3], 0\nR 176,184,32768\nM 104,164,16\nbuffer_load_dwordx4 a[96:99], v252, s[20:23], s64 offen offset:2048 sc1\nv_mfma_f32_16x16x32_bf16 v[120:123], v[160:163], a[4:7], 0\n"
"R 178,184,36864\nM 120,164,20\nbuffer_load_dwordx4 a[100:103], v252, s[20:23], s65 offen offset:2048 sc1\nv_mfma_f32_16x16x32_bf16 v[136:139], v[160:163], a[8:11], 0\nR 180,184,40960\nM 136,164,24\n"
"v_mfma_f32_16x16x32_bf16 v[152:155], v[160:163], a[12:15], 0\nR 182,184,45056\nM 152,164,28\nG 4\nv_mfma_f32_16x16x32_bf16 v[156:159], v[168:171], a[12:15], 0\nR 160,185,32768\n"
"M 156,172,28\nbuffer_load_dwordx4 a[104:107], v252, s[20:23], s66 offen offset:2048 sc1\nv_mfma_f32_16x16x32_bf16 v[140:143], v[168:171], a[8:11], 0\nR 162,185,36864\nM 140,172,24\nv_mfma_f32_16x16x32_bf16 v[124:127], v[168:171], a[4:7], 0\n"
"R 164,185,40960\nM 124,172,20\nbuffer_load_dwordx4 v221, s[12:15], s63 offen offset:1024 lds\nv_mfma_f32_16x16x32_bf16 v[108:111], v[168:171], a[0:3], 0\nR 166,185,45056\nM 108,172,16\n"
"G 4\ns_waitcnt vmcnt(20)\nM 32,176,32\nR 168,186,32768\ns_waitcnt vmcnt(16)\nM 32,180,48\n"
"buffer_load_dwordx4 a[108:111], v252, s[20:23], s67 offen offset:2048 sc1\nM 48,176,36\nR 170,186,36864\ns_waitcnt vmcnt(16)\nM 48,180,52\nM 64,176,40\n"
"R 172,186,40960\ns_waitcnt vmcnt(15)\nM 64,180,56\nbuffer_load_dwordx4 a[112:115], v252, s[20:23], s64 offen offset:3072 sc1\nM 80,176,44\nR 174,186,45056\n"
"s_waitcnt vmcnt(15)\nM 80,180,60\ns_barrier\nG 4\nM 84,160,44\nR 176,187,32768\n"
"M 84,164,60\nM 68,160,40\nR 178,187,36864\nM 68,164,56\nbuffer_load_dwordx4 a[116:119], v252, s[20:23], s65 offen offset:3072 sc1\nM 52,160,36\n"
"R 180,187,40960\nM 52,164,52\nM 36,160,32\nR 182,187,45056\nM 36,164,48\nG 4\n"
"M 40,168,32\nR 160,184,49152\ns_add_u32 s63, s54, 1605632\nM 40,172,48\nbuffer_load_dwordx4 a[120:123], v252, s[20:23], s66 offen offset:3072 sc1\nM 56,168,36\n"
"R 162,184,53248\nM 56,172,52\ns_add_u32 m0, s50, 106496\nM 72,168,40\nR 164,184,57344\nM 72,172,56\n"
"buffer_load_dwordx4 v220, s[12:15], s63 offen lds\nM 88,168,44\nR 166,184,61440\nM 88,172,60\nG 4\nM 92,176,44\n"
"R 168,185,49152\nM 92,180,60\nbuffer_load_dwordx4 a[124:127], v252, s[20:23], s67 offen offset:3072 sc1\nM 76,176,40\nR 170,185,53248\nM 76,180,56\n"
"M 60,176,36\nR 172,185,57344\nM 60,180,52\ns_add_u32 s64, s70, 14688256\nbuffer_load_dwordx4 a[128:131], v252, s[20:23], s64 offen offset:0 sc1\nM 44,176,32\n"
"R 174,185,61440\nM 44,180,48\nG 4\nM 96,160,32\nR 176,186,49152\nM 96,164,48\n"
"s_add_u32 s65, s70, 14721024\nbuffer_load_dwordx4 a[132:135], v252, s[20:23], s65 offen offset:0 sc1\nM 112,160,36\nR 178,186,53248\nM 112,164,52\nM 128,160,40\n"
"R 180,186,57344\nM 128,164,56\nbuffer_load_dwordx4 v221, s[12:15], s63 offen offset:1024 lds\nM 144,160,44\nR 182,186,61440\nM 144,164,60\n"
"s_barrier\nG 4\nM 148,168,44\nR 160,187,49152\nM 148,172,60\ns_add_u32 s66, s71, 14688256\n"
"buffer_load_dwordx4 a[136:139], v252, s[20:23], s66 offen offset:0 sc1\nM 132,168,40\nR 162,187,53248\nM 132,172,56\nM 116,168,36\nR 164,187,57344\n"
"M 116,172,52\ns_add_u32 s67, s71, 14721024\nbuffer_load_dwordx4 a[140:143], v252, s[20:23], s67 offen offset:0 sc1\nM 100,168,32\nR 166,187,61440\nM 100,172,48\n"
"G 4\nM 104,176,32\nR 168,188,0\nM 104,180,48\nM 120,176,36\nR 170,188,4096\n"
"M 120,180,52\nbuffer_load_dwordx4 a[144:147], v252, s[20:23], s64 offen offset:1024 sc1\nM 136,176,40\nR 172,188,8192\nM 136,180,56\nM 152,176,44\n"
"R 174,188,12288\nM 152,180,60\nG 4\nM 156,160,44\nR 176,189,0\ns_add_u32 s63, s54, 1376384\n"
"M 156,164,60\nbuffer_load_dwordx4 a[148:151], v252, s[20:23], s65 offen offset:1024 sc1\nM 140,160,40\nR 178,189,4096\nM 140,164,56\ns_add_u32 m0, s50, 114688\n"
"M 124,160,36\nR 180,189,8192\nM 124,164,52\nbuffer_load_dwordx4 v220, s[12:15], s63 offen lds\nM 108,160,32\nR 182,189,12288\n"
"M 108,164,48\nG 4\ns_waitcnt vmcnt(26)\nM 32,168,64\nR 160,190,0\ns_waitcnt vmcnt(22)\n"
"M 32,172,80\nbuffer_load_dwordx4 a[152:155], v252, s[20:23], s66 offen offset:1024 sc1\nM 48,168,68\nR 162,190,4096\ns_waitcnt vmcnt(22)\nM 48,172,84\n"
"M 64,168,72\nR 164,190,8192\ns_waitcnt vmcnt(21)\nM 64,172,88\nbuffer_load_dwordx4 a[156:159], v252, s[20:23], s67 offen offset:1024 sc1\nM 80,168,76\n"
"R 166,190,12288\ns_waitcnt vmcnt(21)\nM 80,172,92\ns_barrier\nG 4\nM 84,176,76\n"
"R 168,191,0\nM 84,180,92\nbuffer_load_dwordx4 v221, s[12:15], s63 offen offset:1024 lds\nM 68,176,72\nR 170,191,4096\nM 68,180,88\n"
"buffer_load_dwordx4 a[160:163], v252, s[20:23], s64 offen offset:2048 sc1\nM 52,176,68\nR 172,191,8192\nM 52,180,84\ns_add_u32 m0, s50, 0\nM 36,176,64\n"
"R 174,191,12288\ns_add_u32 s63, s54, 1605760\nM 36,180,80\nG 4\nM 40,160,64\nR 176,188,16384\n"
"M 40,164,80\nbuffer_load_dwordx4 a[164:167], v252, s[20:23], s65 offen offset:2048 sc1\nM 56,160,68\nR 178,188,20480\nM 56,164,84\nbuffer_load_dwordx4 v220, s[12:15], s63 offen lds\n"
"M 72,160,72\nR 180,188,24576\nM 72,164,88\nbuffer_load_dwordx4 a[168:171], v252, s[20:23], s66 offen offset:2048 sc1\nM 88,160,76\nR 182,188,28672\n"
"M 88,164,92\nG 4\nM 92,168,76\nR 160,189,16384\nbuffer_load_dwordx4 v221, s[12:15], s63 offen offset:1024 lds\nM 92,172,92\n"
"M 76,168,72\nR 162,189,20480\nM 76,172,88\nbuffer_load_dwordx4 a[172:175], v252, s[20:23], s67 offen offset:2048 sc1\nM 60,168,68\nR 164,189,24576\n"
"M 60,172,84\nM 44,168,64\nR 166,189,28672\nM 44,172,80\nG 4\nM 96,176,64\n"
"R 168,190,16384\nM 96,180,80\nbuffer_load_dwordx4 a[176:179], v252, s[20:23], s64 offen offset:3072 sc1\nM 112,176,68\nR 170,190,20480\nM 112,180,84\n"
"M 128,176,72\nR 172,190,24576\nM 128,180,88\ns_add_u32 m0, s50, 8192\nM 144,176,76\nR 174,190,28672\n"
"s_add_u32 s63, s54, 1835008\nM 144,180,92\ns_waitcnt vmcnt(15)\ns_barrier\nG 4\nM 148,160,76\n"
"R 176,191,16384\nM 148,164,92\nbuffer_load_dwordx4 a[180:183], v252, s[20:23], s65 offen offset:3072 sc1\nM 132,160,72\nR 178,191,20480\nM 132,164,88\n"
"buffer_load_dwordx4 v220, s[12:15], s63 offen lds\nM 116,160,68\nR 180,191,24576\nM 116,164,84\nbuffer_load_dwordx4 a[184:187], v252, s[20:23], s66 offen offset:3072 sc1\nM 100,160,64\n"
"R 182,191,28672\nM 100,164,80\nG 4\nM 104,168,64\nR 160,188,32768\nbuffer_load_dwordx4 v221, s[12:15], s63 offen offset:1024 lds\n"
"M 104,172,80\ns_add_u32 m0, s50, 16384\ns_add_u32 s63, s54, 2064384\nbuffer_load_dwordx4 v220, s[12:15], s63 offen lds\nM 120,168,68\nR 162,188,36864\n"
"M 120,172,84\nbuffer_load_dwordx4 a[188:191], v252, s[20:23], s67 offen offset:3072 sc1\nM 136,168,72\nR 164,188,40960\nM 136,172,88\nbuffer_load_dwordx4 v221, s[12:15], s63 offen offset:1024 lds\n"
"M 152,168,76\nR 166,188,45056\nM 152,172,92\nG 4\nM 156,176,76\nR 168,189,32768\n"
"M 156,180,92\ns_add_u32 s64, s70, 14692352\nbuffer_load_dwordx4 a[192:195], v252, s[20:23], s64 offen offset:0 sc1\nM 140,176,72\nR 170,189,36864\nM 140,180,88\n"
"M 124,176,68\nR 172,189,40960\nM 124,180,84\ns_add_u32 s65, s70, 14725120\nbuffer_load_dwordx4 a[196:199], v252, s[20:23], s65 offen offset:0 sc1\nM 108,176,64\n"
"R 174,189,45056\nM 108,180,80\nG 4\nM 32,160,96\nR 176,190,32768\nM 32,164,112\n"
"M 48,160,100\nR 178,190,36864\nM 48,164,116\ns_add_u32 s66, s71, 14692352\nbuffer_load_dwordx4 a[200:203], v252, s[20:23], s66 offen offset:0 sc1\nM 64,160,104\n"
"R 180,190,40960\nM 64,164,120\ns_add_u32 m0, s50, 24576\nM 80,160,108\nR 182,190,45056\ns_add_u32 s63, s54, 2293760\n"
"M 80,164,124\ns_waitcnt vmcnt(12)\ns_barrier\nG 4\nM 84,168,108\nR 160,191,32768\n"
"M 84,172,124\ns_add_u32 s67, s71, 14725120\nbuffer_load_dwordx4 a[204:207], v252, s[20:23], s67 offen offset:0 sc1\nM 68,168,104\nR 162,191,36864\nM 68,172,120\n"
"buffer_load_dwordx4 v220, s[12:15], s63 offen lds\nM 52,168,100\nR 164,191,40960\nM 52,172,116\nbuffer_load_dwordx4 v221, s[12:15], s63 offen offset:1024 lds\nM 36,168,96\n"
"R 166,191,45056\ns_add_u32 m0, s50, 32768\nM 36,172,112\nG 4\nM 40,176,96\nR 168,188,49152\n"
"s_add_u32 s63, s54, 2523136\nM 40,180,112\nbuffer_load_dwordx4 a[208:211], v252, s[20:23], s64 offen offset:1024 sc1\nM 56,176,100\nR 170,188,53248\nM 56,180,116\n"
"buffer_load_dwordx4 v220, s[12:15], s63 offen lds\nM 72,176,104\nR 172,184,0\nM 72,180,120\nbuffer_load_dwordx4 a[212:215], v252, s[20:23], s65 offen offset:1024 sc1\nM 88,176,108\n"
"R 174,184,4096\nM 88,180,124\nG 4\nM 92,160,108\nR 176,189,49152\nbuffer_load_dwordx4 v221, s[12:15], s63 offen offset:1024 lds\n"
"M 92,164,124\nM 76,160,104\nR 178,189,53248\nM 76,164,120\nbuffer_load_dwordx4 a[216:219], v252, s[20:23], s66 offen offset:1024 sc1\nM 60,160,100\n"
"R 180,185,0\nLq1011:\ns_lshr_b32 s60, s45, 3\ns_lshl2_add_u32 s60, s60, s40\ns_lshl_b32 s60, s60, 8\nv_lshl_add_u32 v224, v252, 3, s60\n"
"v_add_u32 v224, Lq1011+4096-LqPC, v224\ns_mov_b64 exec, 3\nglobal_load_dword v224, v224, s[98:99]\ns_mov_b64 exec, -1\nM 60,164,116\nM 44,160,96\n"
"R 182,185,4096\nM 44,164,112\nG 4\nM 96,168,96\nR 160,190,49152\nM 96,172,112\n"
"buffer_load_dwordx4 a[220:223], v252, s[20:23], s67 offen offset:1024 sc1\nM 112,168,100\nR 162,190,53248\nM 112,172,116\nM 128,168,104\nR 164,186,0\n"
"M 128,172,120\nM 144,168,108\nR 166,186,4096\ns_add_u32 m0, s50, 40960\nM 144,172,124\ns_waitcnt vmcnt(13)\n"
"s_barrier\nG 4\nM 148,176,108\nR 168,191,49152\ns_add_u32 s63, s54, 2752512\nM 148,180,124\n"
"buffer_load_dwordx4 a[224:227], v252, s[20:23], s64 offen offset:2048 sc1\nM 132,176,104\nR 170,191,53248\nM 132,180,120\nbuffer_load_dwordx4 a[228:231], v252, s[20:23], s65 offen offset:2048 sc1\nM 116,176,100\n"
"R 172,187,0\nbuffer_load_dwordx4 v220, s[12:15], s63 offen lds\nM 116,180,116\nbuffer_load_dwordx4 v221, s[12:15], s63 offen offset:1024 lds\nM 100,176,96\nR 174,187,4096\n"
"s_add_u32 m0, s50, 49152\nM 100,180,112\nG 4\nM 104,160,96\nR 176,184,8192\ns_add_u32 s63, s54, 2981888\n"
"M 104,164,112\nbuffer_load_dwordx4 a[232:235], v252, s[20:23], s66 offen offset:2048 sc1\nM 120,160,100\nR 178,184,12288\nM 120,164,116\nbuffer_load_dwordx4 v220, s[12:15], s63 offen lds\n"
"M 136,160,104\nR 180,184,16384\nM 136,164,120\nbuffer_load_dwordx4 v221, s[12:15], s63 offen offset:1024 lds\nM 152,160,108\nR 182,184,20480\n"
"M 152,164,124\nG 4\nM 156,168,108\nR 160,185,8192\nM 156,172,124\nbuffer_load_dwordx4 a[236:239], v252, s[20:23], s67 offen offset:2048 sc1\n"
"M 140,168,104\nR 162,185,12288\nM 140,172,120\nM 124,168,100\nR 164,185,16384\nM 124,172,116\n"
"buffer_load_dwordx4 a[240:243], v252, s[20:23], s64 offen offset:3072 sc1\nM 108,168,96\nR 166,185,20480\nM 108,172,112\nG 4\nM 32,176,128\n"
"R 168,186,8192\nM 32,180,144\nM 48,176,132\nR 170,186,12288\nM 48,180,148\nbuffer_load_dwordx4 a[244:247], v252, s[20:23], s65 offen offset:3072 sc1\n"
"M 64,176,136\nR 172,186,16384\nM 64,180,152\nM 80,176,140\nR 174,186,20480\ns_add_u32 m0, s50, 57344\n"
"M 80,180,156\ns_waitcnt vmcnt(13)\ns_barrier\nG 4\nM 84,160,140\nR 176,187,8192\n"
"s_add_u32 s63, s54, 3211264\nM 84,164,156\nbuffer_load_dwordx4 a[248:251], v252, s[20:23], s66 offen offset:3072 sc1\nM 68,160,136\nR 178,187,12288\nM 68,164,152\n"
"s_lshr_b32 s84, s49, 3\nM 52,160,132\nR 180,187,16384\nbuffer_load_dwordx4 v220, s[12:15], s63 offen lds\nM 52,164,148\ns_add_u32 s84, s84, 83361792\n"
"M 36,160,128\nR 182,187,20480\nM 36,164,144\nbuffer_load_dwordx4 v[0:3], v252, s[20:23], s84 offen offset:0 sc1\nG 4\nM 40,168,128\n"
"R 160,184,24576\nbuffer_load_dwordx4 v221, s[12:15], s63 offen offset:1024 lds\nM 40,172,144\nbuffer_load_dwordx4 a[252:255], v252, s[20:23], s67 offen offset:3072 sc1\ns_add_u32 s63, s54, 3440640\nM 56,168,132\n"
"R 162,184,28672\ns_add_u32 m0, s50, 65536\nM 56,172,148\nbuffer_load_dwordx4 v[4:7], v252, s[20:23], s84 offen offset:1024 sc1\nM 72,168,136\nR 164,184,32768\n"
"s_lshr_b32 s85, s49, 3\nbuffer_load_dwordx4 v220, s[12:15], s63 offen lds\nM 72,172,152\nbuffer_load_dwordx4 v[8:11], v252, s[20:23], s84 offen offset:2048 sc1\nM 88,168,140\nR 166,184,36864\n"
"s_add_u32 s85, s85, 83365888\nM 88,172,156\nG 4\nM 92,176,140\nR 168,185,24576\nbuffer_load_dwordx4 v221, s[12:15], s63 offen offset:1024 lds\n"
"M 92,180,156\nbuffer_load_dwordx4 v[12:15], v252, s[20:23], s84 offen offset:3072 sc1\nM 76,176,136\nR 170,185,28672\nM 76,180,152\nbuffer_load_dwordx4 v[16:19], v252, s[20:23], s85 offen offset:0 sc1\n"
"M 60,176,132\nR 172,185,32768\nM 60,180,148\nbuffer_load_dwordx4 v[20:23], v252, s[20:23], s85 offen offset:1024 sc1\nM 44,176,128\nR 174,185,36864\n"
"M 44,180,144\nG 4\nM 32,160,160\nR 176,186,24576\nM 32,164,176\nbuffer_load_dwordx4 v[24:27], v252, s[20:23], s85 offen offset:2048 sc1\n"
"M 48,160,164\nR 178,186,28672\nM 48,164,180\nbuffer_load_dwordx4 v[28:31], v252, s[20:23], s85 offen offset:3072 sc1\nM 64,160,168\nR 180,186,32768\n"
"s_add_u32 s63, s54, 1835136\nM 64,164,184\ns_add_u32 m0, s50, 73728\nM 80,160,172\nR 182,186,36864\nM 80,164,188\n"
"s_waitcnt vmcnt(17)\ns_barrier\nG 4\nM 84,168,172\nR 160,187,24576\nM 84,172,188\n"
"buffer_load_dwordx4 v220, s[12:15], s63 offen lds\nM 68,168,168\nR 162,187,28672\nM 68,172,184\nM 52,168,164\nR 164,187,32768\n"
"v_bfe_u32 v250, v252, 5, 1\nv_mul_u32_u24 v250, 2560, v250\nM 52,172,180\nbuffer_load_dwordx4 v221, s[12:15], s63 offen offset:1024 lds\nv_and_b32 v251, 16, v252\nM 36,168,160\n"
"R 166,187,36864\ns_add_u32 m0, s50, 81920\nv_lshl_add_u32 v250, v251, 1, v250\nM 36,172,176\ns_lshl_b32 s60, s90, 6\nv_add_u32 v250, s60, v250\n"
"G 4\nM 40,176,160\nR 168,184,40960\ns_add_u32 s63, s54, 2064512\nM 40,180,176\nbuffer_load_dwordx4 v220, s[12:15], s63 offen lds\n"
"M 56,176,164\nR 170,184,45056\nM 56,180,180\nM 72,176,168\nR 172,184,49152\nM 72,180,184\n"
"buffer_load_dwordx4 v221, s[12:15], s63 offen offset:1024 lds\nM 88,176,172\nR 174,184,53248\nM 88,180,188\nG 4\nM 92,160,172\n"
"R 176,185,40960\nM 92,164,188\nM 76,160,168\nR 178,185,45056\nM 76,164,184\ns_add_u32 m0, s50, 90112\n"
"M 60,160,164\nR 180,185,49152\nM 60,164,180\ns_add_u32 s63, s54, 2293888\nM 44,160,160\nR 182,185,53248\n"
"M 44,164,176\nG 4\nM 32,168,192\nR 160,186,40960\nM 32,172,208\nbuffer_load_dwordx4 v220, s[12:15], s63 offen lds\n"
"M 48,168,196\nR 162,186,45056\nM 48,172,212\nM 64,168,200\nR 164,186,49152\nM 64,172,216\n"
"buffer_load_dwordx4 v221, s[12:15], s63 offen offset:1024 lds\nM 80,168,204\nR 166,186,53248\ns_add_u32 m0, s50, 98304\nM 80,172,220\ns_waitcnt vmcnt(11)\n"
"s_barrier\nG 4\nM 84,176,204\nR 168,187,40960\ns_add_u32 s63, s54, 2523264\nM 84,180,220\n"
"buffer_load_dwordx4 v220, s[12:15], s63 offen lds\nM 68,176,200\nR 170,187,45056\nv_min3_u32 v230, v1, v3, v5\nv_min3_u32 v230, v230, v7, v9\nM 68,180,216\n"
"s_waitcnt vmcnt(11)\nv_min3_u32 v230, v230, v11, v13\ns_waitcnt vmcnt(10)\nv_min3_u32 v230, v230, v15, v17\ns_waitcnt vmcnt(9)\nv_min3_u32 v230, v230, v19, v21\n"
"M 52,176,196\nR 172,187,49152\ns_waitcnt vmcnt(8)\nv_min3_u32 v230, v230, v23, v25\ns_waitcnt vmcnt(7)\nv_min3_u32 v230, v230, v27, v29\n"
"M 52,180,212\nbuffer_load_dwordx4 v221, s[12:15], s63 offen offset:1024 lds\nv_min3_u32 v230, v230, v31, v31\nM 36,176,192\nR 174,187,53248\ns_add_u32 s63, s54, 2752640\n"
"v_max3_u32 v231, v1, v3, v5\nM 36,180,208\nG 4\nv_max3_u32 v231, v231, v7, v9\nv_max3_u32 v231, v231, v11, v13\nv_max3_u32 v231, v231, v15, v17\n"
"M 40,160,192\nR 176,184,57344\nv_max3_u32 v231, v231, v19, v21\nv_max3_u32 v231, v231, v23, v25\nM 40,164,208\ns_add_u32 m0, s50, 106496\n"
"v_max3_u32 v231, v231, v27, v29\nv_max3_u32 v231, v231, v31, v31\nv_cmp_ne_u32 s[86:87], s46, v230\nM 56,160,196\nR 178,184,61440\nv_cmp_ne_u32 s[88:89], s46, v231\n"
"M 56,164,212\nbuffer_load_dwordx4 v220, s[12:15], s63 offen lds\ns_or_b64 s[86:87], s[86:87], s[88:89]\ns_cbranch_scc1 Lq786\nLq787:\nM 72,160,200\n"
"R 180,188,0\nv_add_f32 v0, v0, v16\nv_add_f32 v2, v2, v18\nM 72,164,216\nv_add_f32 v4, v4, v20\nv_add_f32 v6, v6, v22\n"
"v_add_f32 v8, v8, v24\nM 88,160,204\nR 182,188,4096\nv_add_f32 v10, v10, v26\nv_add_f32 v12, v12, v28\nM 88,164,220\n"
"G 4\nv_add_f32 v14, v14, v30\nv_add_f32 v0, v0, v8\nv_add_f32 v2, v2, v10\nM 92,168,204\nR 160,185,57344\n"
"v_add_f32 v4, v4, v12\nv_add_f32 v6, v6, v14\nM 92,172,220\nbuffer_load_dwordx4 v221, s[12:15], s63 offen offset:1024 lds\nv_add_f32 v0, v0, v4\nM 76,168,200\n"
"R 162,185,61440\ns_add_u32 s63, s54, 2982016\nv_add_f32 v2, v2, v6\nM 76,172,216\ns_add_u32 m0, s50, 114688\nv_lshrrev_b32 v231, 1, v252\n"
"v_lshrrev_b32 v230, 2, v252\ns_lshl_b32 s62, s40, 9\ns_add_u32 s62, s62, 159232\nv_add_u32 v231, s62, v231\nM 60,168,196\nR 164,189,0\n"
"ds_write2_b32 v231, v0, v2 offset1:1\nM 60,172,212\nbuffer_load_dwordx4 v220, s[12:15], s63 offen lds\ns_lshl_b32 s62, s40, 9\ns_add_u32 s62, s62, 159232\nv_add_u32 v230, s62, v230\n"
"M 44,168,192\nR 166,189,4096\nds_read2st64_b32 v[232:233], v230 offset1:1\nM 44,172,208\nG 6\nM 32,176,224\n"
"R 168,186,57344\nM 32,180,240\nbuffer_load_dwordx4 v221, s[12:15], s63 offen offset:1024 lds\nM 48,176,228\nR 170,186,61440\ns_add_u32 s63, s54, 3211392\n"
"M 48,180,244\ns_add_u32 m0, s50, 0\nM 64,176,232\nR 172,190,0\nG 3\nv_add_f32 v232, v232, v233\n"
"v_mov_b32 v233, v232\nM 64,180,248\nbuffer_load_dwordx4 v220, s[12:15], s63 offen lds\nv_permlane32_swap_b32 v232, v233\nM 80,176,236\nR 174,190,4096\n"
"v_mov_b32 v230, v232\nv_mov_b32 v231, v233\nM 80,180,252\ns_waitcnt vmcnt(9)\ns_barrier\nv_permlane16_swap_b32 v230, v232\n"
"M 84,160,236\nR 176,187,57344\nv_permlane16_swap_b32 v231, v233\nv_mul_f32 v230, 0xba800000, v230\nM 84,164,252\nbuffer_load_dwordx4 v221, s[12:15], s63 offen offset:1024 lds\n"
"v_mul_f32 v214, v230, v230\nM 68,160,232\nR 178,187,61440\ns_add_u32 s63, s54, 3440768\nv_mul_f32 v231, 0x3a800000, v231\nM 68,164,248\n"
"s_add_u32 m0, s50, 8192\nv_sub_f32 v231, v231, v214\nv_max_f32 v231, 0, v231\nv_add_f32 v231, 0x3727c5ac, v231\nM 52,160,228\nR 180,191,0\n"
"v_and_b32 v208, s77, v230\nv_rsq_f32 v234, v231\nM 52,164,244\nbuffer_load_dwordx4 v220, s[12:15], s63 offen lds\nv_mul_f32 v216, v231, v234\nM 36,160,224\n"
"R 182,191,4096\nv_sub_f32 v230, v230, v208\nv_cvt_pk_bf16_f32 v209, v230, v216\nM 36,164,240\nG 4\nv_cvt_pk_bf16_f32 v208, v208, v208\n"
"v_mul_f32 v214, 0xc0135761, v234\nv_mul_f32 v215, v234, v234\nM 40,168,224\nR 160,188,8192\nv_mul_f32 v215, v215, v214\nv_mul_f32 v215, 0x3d372713, v215\n"
"M 40,172,240\ns_mov_b64 s[60:61], 0xffff\nv_cndmask_b32 v208, 0, v208, s[60:61]\nv_mul_f32 v232, 0xba800000, v232\nv_mul_f32 v217, v232, v232\nM 56,168,228\n"
"R 162,188,12288\nv_mul_f32 v233, 0x3a800000, v233\nv_sub_f32 v233, v233, v217\nM 56,172,244\nbuffer_load_dwordx4 v221, s[12:15], s63 offen offset:1024 lds\nv_max_f32 v233, 0, v233\n"
"M 72,168,232\nR 164,188,16384\ns_add_u32 s63, s55, 0\nv_add_f32 v233, 0x3727c5ac, v233\nM 72,172,248\ns_add_u32 m0, s50, 16384\n"
"v_rsq_f32 v235, v233\nv_and_b32 v212, s77, v232\nM 88,168,236\nR 166,188,20480\nv_mul_f32 v219, v233, v235\nv_sub_f32 v232, v232, v212\n"
"M 88,172,252\ns_mov_b64 s[60:61], 0xffff\nv_cndmask_b32 v209, 0, v209, s[60:61]\nG 4\nv_cvt_pk_bf16_f32 v213, v232, v219\nM 92,176,236\n"
"R 168,189,8192\nv_cvt_pk_bf16_f32 v212, v212, v212\nv_mul_f32 v217, 0xc0135761, v235\nM 92,180,252\nbuffer_load_dwordx4 v222, s[12:15], s63 offen lds\nv_mul_f32 v218, v235, v235\n"
"M 76,176,232\nR 170,189,12288\nv_mul_f32 v218, v218, v217\nv_mul_f32 v218, 0x3d372713, v218\nM 76,180,248\ns_mov_b64 s[60:61], 0xffff\n"
"v_cndmask_b32 v212, 0, v212, s[60:61]\ns_mov_b64 s[60:61], 0xffff\nv_cndmask_b32 v213, 0, v213, s[60:61]\nM 60,176,228\nR 172,189,16384\nM 60,180,244\n"
"buffer_load_dwordx4 v223, s[12:15], s63 offen offset:1024 lds\nM 44,176,224\nR 174,189,20480\nM 44,180,240\ns_waitcnt vmcnt(10)\ns_barrier\n"
"G 4\nM 96,160,128\nR 176,190,8192\nM 96,164,144\ns_mov_b32 m0, s51\nM 112,160,132\n"
"R 178,190,12288\nM 112,164,148\nds_write_addtid_b32 v64 offset:0\nM 128,160,136\nR 180,190,16384\nM 128,164,152\n"
"ds_write_addtid_b32 v65 offset:256\nM 144,160,140\nR 182,190,20480\nds_write_addtid_b32 v66 offset:512\nM 144,164,156\nds_write_addtid_b32 v67 offset:768\n"
"ds_write_addtid_b32 v68 offset:1024\nG 9\nM 148,168,140\nR 160,191,8192\nds_write_addtid_b32 v69 offset:1280\nM 148,172,156\n"
"ds_write_addtid_b32 v70 offset:1536\nds_write_addtid_b32 v71 offset:1792\nM 132,168,136\nR 162,191,12288\nds_write_addtid_b32 v72 offset:2048\nM 132,172,152\n"
"ds_write_addtid_b32 v73 offset:2304\nds_write_addtid_b32 v74 offset:2560\nM 116,168,132\nR 164,191,16384\nds_write_addtid_b32 v75 offset:2816\nM 116,172,148\n"
"ds_write_addtid_b32 v76 offset:3072\nds_write_addtid_b32 v77 offset:3328\nM 100,168,128\nR 166,191,20480\nds_write_addtid_b32 v78 offset:3584\nM 100,172,144\n"
"ds_write_addtid_b32 v79 offset:3840\nds_write_addtid_b32 v80 offset:4096\nG 15\nM 104,176,128\nR 168,188,24576\nds_write_addtid_b32 v81 offset:4352\n"
"M 104,180,144\nds_write_addtid_b32 v82 offset:4608\nds_write_addtid_b32 v83 offset:4864\nM 120,176,132\nR 170,188,28672\nds_write_addtid_b32 v84 offset:5120\n"
"M 120,180,148\nds_write_addtid_b32 v85 offset:5376\nds_write_addtid_b32 v86 offset:5632\nM 136,176,136\nR 172,188,32768\nds_write_addtid_b32 v87 offset:5888\n"
"M 136,180,152\nds_write_addtid_b32 v88 offset:6144\nds_write_addtid_b32 v89 offset:6400\nM 152,176,140\nR 174,188,36864\nds_write_addtid_b32 v90 offset:6656\n"
"M 152,180,156\nds_write_addtid_b32 v91 offset:6912\nds_write_addtid_b32 v92 offset:7168\nG 15\nM 156,160,140\nR 176,189,24576\n"
"ds_write_addtid_b32 v93 offset:7424\nM 156,164,156\nds_write_addtid_b32 v94 offset:7680\nds_write_addtid_b32 v95 offset:7936\nM 140,160,136\nR 178,189,28672\n"
"s_add_u32 m0, s50, 24576\ns_add_u32 s63, s55, 458752\nM 140,164,152\nbuffer_load_dwordx4 v222, s[12:15], s63 offen lds\nM 124,160,132\nR 180,189,32768\n"
"s_waitcnt vmcnt(7) lgkmcnt(2)\ns_barrier\nM 124,164,148\nds_read2st64_b32 v[64:65], v225 offset0:0 offset1:1\nds_read2st64_b32 v[66:67], v225 offset0:2 offset1:3\nM 108,160,128\n"
"R 182,189,36864\nds_read2st64_b32 v[68:69], v225 offset0:4 offset1:5\nM 108,164,144\nds_read2st64_b32 v[70:71], v225 offset0:6 offset1:7\nds_read2st64_b32 v[72:73], v225 offset0:8 offset1:9\nM 96,168,160\n"
"R 160,190,24576\nds_read2st64_b32 v[74:75], v225 offset0:10 offset1:11\nM 96,172,176\nbuffer_load_dwordx4 v223, s[12:15], s63 offen offset:1024 lds\nds_read2st64_b32 v[76:77], v225 offset0:12 offset1:13\nM 112,168,164\n"
"R 162,190,28672\nds_read2st64_b32 v[78:79], v225 offset0:14 offset1:15\nM 112,172,180\ns_add_u32 m0, s50, 32768\nds_read2st64_b32 v[80:81], v225 offset0:16 offset1:17\nG 11\n"
"v_add_f32 v32, v32, v64\nM 128,168,168\nR 164,190,32768\nv_add_f32 v33, v33, v65\nG 11\nv_add_f32 v34, v34, v66\n"
"M 128,172,184\ns_add_u32 s63, s55, 917504\nds_read2st64_b32 v[82:83], v225 offset0:18 offset1:19\nv_add_f32 v35, v35, v67\nM 144,168,172\nR 166,190,36864\n"
"G 11\nv_add_f32 v36, v36, v68\nv_add_f32 v37, v37, v69\nM 144,172,188\nds_read2st64_b32 v[84:85], v225 offset0:20 offset1:21\nG 11\n"
"v_add_f32 v38, v38, v70\nM 148,176,172\nR 168,191,24576\nv_add_f32 v39, v39, v71\nG 11\nv_add_f32 v40, v40, v72\n"
"M 148,180,188\nbuffer_load_dwordx4 v222, s[12:15], s63 offen lds\nv_add_f32 v41, v41, v73\nM 132,176,168\nR 170,191,28672\nG 10\n"
"v_add_f32 v42, v42, v74\nv_add_f32 v43, v43, v75\nM 132,180,184\nds_read2st64_b32 v[86:87], v225 offset0:22 offset1:23\nG 10\nv_add_f32 v44, v44, v76\n"
"v_add_f32 v45, v45, v77\nM 116,176,164\nR 172,191,32768\nG 9\nv_add_f32 v46, v46, v78\nv_add_f32 v47, v47, v79\n"
"M 116,180,180\nds_read2st64_b32 v[88:89], v225 offset0:24 offset1:25\nG 9\nv_add_f32 v48, v48, v80\nv_add_f32 v49, v49, v81\nM 100,176,160\n"
"R 174,191,36864\nG 8\nv_add_f32 v50, v50, v82\nv_add_f32 v51, v51, v83\nM 100,180,176\nds_read2st64_b32 v[90:91], v225 offset0:26 offset1:27\n"
"G 8\nG 7\nv_add_f32 v52, v52, v84\nM 104,160,160\nR 176,188,40960\ns_mov_b32 s60, 157184\n"
"v_bfe_u32 v246, v252, 4, 4\nv_lshl_add_u32 v246, v246, 3, s60\nM 104,164,176\nds_read_b64 v[238:239], v246 offset:0\nv_add_f32 v53, v53, v85\nM 120,160,164\n"
"R 178,188,45056\nds_read_b64 v[240:241], v246 offset:128\nM 120,164,180\nds_read_b64 v[242:243], v246 offset:256\nG 9\nv_add_f32 v54, v54, v86\n"
"M 136,160,168\nR 180,188,49152\nds_read_b64 v[244:245], v246 offset:384\nM 136,164,184\nds_read_b64 v[246:247], v246 offset:0\nv_add_f32 v55, v55, v87\n"
"M 152,160,172\nR 182,188,53248\nG 11\nv_add_f32 v56, v56, v88\nM 152,164,188\nds_read2st64_b32 v[92:93], v225 offset0:28 offset1:29\n"
"G 11\nv_add_f32 v57, v57, v89\nM 156,168,172\nR 160,189,40960\nG 11\nv_add_f32 v58, v58, v90\n"
"M 156,172,188\nbuffer_load_dwordx4 v223, s[12:15], s63 offen offset:1024 lds\nM 140,168,168\nR 162,189,45056\nv_add_f32 v59, v59, v91\nM 140,172,184\n"
"s_add_u32 m0, s50, 40960\nds_read2st64_b32 v[94:95], v225 offset0:30 offset1:31\nM 124,168,164\nR 164,189,49152\nM 124,172,180\ns_add_u32 s63, s55, 1376256\n"
"G 4\nv_add_f32 v60, v60, v92\nv_add_f32 v61, v61, v93\nG 1\nv_add_f32 v62, v62, v94\nv_add_f32 v63, v63, v95\n"
"M 108,168,160\nR 166,189,53248\nv_mfma_f32_16x16x32_bf16 v[32:35], v[238:241], v[208:211], v[32:35]\nM 108,172,176\nv_mfma_f32_16x16x32_bf16 v[36:39], v[240:243], v[208:211], v[36:39]\nM 96,176,192\n"
"R 168,190,40960\nv_mfma_f32_16x16x32_bf16 v[40:43], v[242:245], v[208:211], v[40:43]\nM 96,180,208\nv_mfma_f32_16x16x32_bf16 v[44:47], v[244:247], v[208:211], v[44:47]\nbuffer_load_dwordx4 v222, s[12:15], s63 offen lds\nM 112,176,196\n"
"R 170,190,45056\nv_mfma_f32_16x16x32_bf16 v[48:51], v[244:247], v[210:213], v[48:51]\ns_waitcnt vmcnt(5)\ns_barrier\nM 112,180,212\nv_mfma_f32_16x16x32_bf16 v[52:55], v[238:241], v[210:213], v[52:55]\n"
"M 128,176,200\nR 172,190,49152\nv_mfma_f32_16x16x32_bf16 v[56:59], v[240:243], v[210:213], v[56:59]\nM 128,180,216\nv_mfma_f32_16x16x32_bf16 v[60:63], v[242:245], v[210:213], v[60:63]\nbuffer_load_dwordx4 v223, s[12:15], s63 offen offset:1024 lds\n"
"M 144,176,204\nR 174,190,53248\ns_add_u32 s63, s55, 1835008\nM 144,180,220\nG 4\nM 148,160,204\n"
"R 176,191,40960\nM 148,164,220\ns_add_u32 m0, s50, 49152\nM 132,160,200\nR 178,191,45056\nM 132,164,216\n"
"buffer_load_dwordx4 v222, s[12:15], s63 offen lds\nM 116,160,196\nR 180,191,49152\nv_mul_f32 v230, v32, v32\nv_mul_f32 v231, v33, v33\nM 116,164,212\n"
"s_add_u32 s84, s73, 37748736\ns_add_u32 s85, s73, 37863424\nv_mul_f32 v232, v34, v34\nv_mul_f32 v233, v35, v35\nM 100,160,192\nR 182,191,53248\n"
"v_fma_f32 v230, v230, v215, v214\nv_fma_f32 v231, v231, v215, v214\nM 100,164,208\nG 4\nv_fma_f32 v232, v232, v215, v214\nv_fma_f32 v233, v233, v215, v214\n"
"v_mul_f32 v230, v32, v230\nM 104,168,192\nR 160,184,0\nv_mul_f32 v231, v33, v231\nv_mul_f32 v232, v34, v232\nM 104,172,208\n"
"v_exp_f32 v230, v230\nv_mul_f32 v233, v35, v233\nM 120,168,196\nR 162,184,4096\nv_fma_f32 v230, v230, v216, v216\nv_exp_f32 v231, v231\n"
"M 120,172,212\nbuffer_load_dwordx4 v223, s[12:15], s63 offen offset:1024 lds\nv_exp_f32 v232, v232\nM 136,168,200\nR 164,184,8192\nv_fma_f32 v231, v231, v216, v216\n"
"v_exp_f32 v233, v233\nM 136,172,216\ns_add_u32 m0, s50, 57344\nv_rcp_f32 v230, v230\nv_fma_f32 v232, v232, v216, v216\nM 152,168,204\n"
"R 166,184,12288\nv_fma_f32 v233, v233, v216, v216\nv_rcp_f32 v231, v231\nM 152,172,220\nG 4\nv_rcp_f32 v232, v232\n"
"v_rcp_f32 v233, v233\nM 156,176,204\nR 168,185,0\nv_mul_f32 v230, v32, v230\nv_mul_f32 v231, v33, v231\nM 156,180,220\n"
"s_add_u32 s63, s55, 2293760\nv_mul_f32 v232, v34, v232\nv_mul_f32 v233, v35, v233\nv_cvt_pk_bf16_f32 v32, v230, v231\nM 140,176,200\nR 170,185,4096\n"
"v_cvt_pk_bf16_f32 v33, v232, v233\nv_mul_f32 v234, v36, v36\nM 140,180,216\nbuffer_load_dwordx4 v222, s[12:15], s63 offen lds\nv_mul_f32 v235, v37, v37\nM 124,176,196\n"
"R 172,185,8192\nv_mul_f32 v236, v38, v38\nv_mul_f32 v237, v39, v39\nM 124,180,212\nv_fma_f32 v234, v234, v215, v214\nv_fma_f32 v235, v235, v215, v214\n"
"v_fma_f32 v236, v236, v215, v214\nM 108,176,192\nR 174,185,12288\ns_mov_b32 s60, 157696\nv_bfe_u32 v246, v252, 4, 4\nv_lshl_add_u32 v246, v246, 3, s60\n"
"v_fma_f32 v237, v237, v215, v214\nM 108,180,208\nds_read_b64 v[238:239], v246 offset:0\nG 5\nv_mul_f32 v234, v36, v234\nM 96,160,224\n"
"R 176,186,0\nds_read_b64 v[240:241], v246 offset:128\nv_mul_f32 v235, v37, v235\nM 96,164,240\nds_read_b64 v[242:243], v246 offset:256\nv_mul_f32 v236, v38, v236\n"
"v_mul_f32 v237, v39, v237\nM 112,160,228\nR 178,186,4096\nds_read_b64 v[244:245], v246 offset:384\nv_exp_f32 v234, v234\nM 112,164,244\n"
"ds_read_b64 v[246:247], v246 offset:0\nv_fma_f32 v234, v234, v216, v216\nv_exp_f32 v235, v235\nM 128,160,232\nR 180,186,8192\nv_fma_f32 v235, v235, v216, v216\n"
"v_exp_f32 v236, v236\nM 128,164,248\nbuffer_load_dwordx4 v223, s[12:15], s63 offen offset:1024 lds\nv_exp_f32 v237, v237\nM 144,160,236\nR 182,186,12288\n"
"s_add_u32 s63, s55, 2752512\nv_fma_f32 v236, v236, v216, v216\nM 144,164,252\nG 9\nv_rcp_f32 v234, v234\nv_fma_f32 v237, v237, v216, v216\n"
"M 148,168,236\nR 160,187,0\nv_mul_f32 v234, v36, v234\nv_rcp_f32 v235, v235\nM 148,172,252\ns_add_u32 m0, s50, 65536\n"
"v_rcp_f32 v236, v236\nv_rcp_f32 v237, v237\nM 132,168,232\nR 162,187,4096\nv_mul_f32 v235, v37, v235\nv_mul_f32 v236, v38, v236\n"
"M 132,172,248\nbuffer_load_dwordx4 v222, s[12:15], s63 offen lds\nv_mul_f32 v237, v39, v237\nM 116,168,228\nR 164,187,8192\nG 9\n"
"v_mfma_f32_16x16x32_bf16 v[96:99], v[238:241], v[208:211], v[96:99]\nv_cvt_pk_bf16_f32 v34, v234, v235\nv_cvt_pk_bf16_f32 v35, v236, v237\nM 116,172,244\nv_mul_f32 v230, v40, v40\nv_mul_f32 v231, v41, v41\n"
"v_mul_f32 v232, v42, v42\nM 100,168,224\nR 166,187,12288\nG 9\nv_mfma_f32_16x16x32_bf16 v[100:103], v[240:243], v[208:211], v[100:103]\nv_mul_f32 v233, v43, v43\n"
"v_fma_f32 v230, v230, v215, v214\nM 100,172,240\nG 4\nv_fma_f32 v231, v231, v215, v214\nv_fma_f32 v232, v232, v215, v214\nv_fma_f32 v233, v233, v215, v214\n"
"M 104,176,224\nR 168,184,16384\nv_mfma_f32_16x16x32_bf16 v[104:107], v[242:245], v[208:211], v[104:107]\nv_mul_f32 v230, v40, v230\nv_mul_f32 v231, v41, v231\nM 104,180,240\n"
"buffer_load_dwordx4 v223, s[12:15], s63 offen offset:1024 lds\nv_mul_f32 v232, v42, v232\nM 120,176,228\nR 170,184,20480\nv_mfma_f32_16x16x32_bf16 v[108:111], v[244:247], v[208:211], v[108:111]\nv_mul_f32 v233, v43, v233\n"
"v_exp_f32 v230, v230\nM 120,180,244\ns_add_u32 m0, s50, 73728\nv_exp_f32 v231, v231\nv_exp_f32 v232, v232\nM 136,176,232\n"
"R 172,186,16384\nv_mfma_f32_16x16x32_bf16 v[112:115], v[244:247], v[210:213], v[112:115]\nv_fma_f32 v230, v230, v216, v216\nv_exp_f32 v233, v233\nM 136,180,248\ns_add_u32 s63, s55, 3211264\n"
"v_rcp_f32 v230, v230\nv_fma_f32 v231, v231, v216, v216\nM 152,176,236\nR 174,186,20480\nv_mfma_f32_16x16x32_bf16 v[116:119], v[238:241], v[210:213], v[116:119]\nv_fma_f32 v232, v232, v216, v216\n"
"v_fma_f32 v233, v233, v216, v216\nM 152,180,252\nG 4\nv_rcp_f32 v231, v231\nv_rcp_f32 v232, v232\nM 156,160,236\n"
"R 176,185,16384\nv_mfma_f32_16x16x32_bf16 v[120:123], v[240:243], v[210:213], v[120:123]\nv_mul_f32 v230, v40, v230\nv_rcp_f32 v233, v233\nM 156,164,252\nbuffer_load_dwordx4 v222, s[12:15], s63 offen lds\n"
"v_mul_f32 v231, v41, v231\nM 140,160,232\nR 178,185,20480\nv_mfma_f32_16x16x32_bf16 v[124:127], v[242:245], v[210:213], v[124:127]\nv_mul_f32 v232, v42, v232\nv_mul_f32 v233, v43, v233\n"
"M 140,164,248\nv_cvt_pk_bf16_f32 v40, v230, v231\nv_cvt_pk_bf16_f32 v41, v232, v233\nv_mul_f32 v234, v44, v44\nM 124,160,228\nR 180,187,16384\n"
"v_mul_f32 v235, v45, v45\nv_mul_f32 v236, v46, v46\nM 124,164,244\nbuffer_load_dwordx4 v223, s[12:15], s63 offen offset:1024 lds\nv_mul_f32 v237, v47, v47\nM 108,160,224\n"
"R 182,187,20480\nv_fma_f32 v234, v234, v215, v214\nv_fma_f32 v235, v235, v215, v214\nM 108,164,240\ns_waitcnt vmcnt(10)\ns_barrier\n"
"G 4\nv_fma_f32 v236, v236, v215, v214\nv_mfma_f32_16x16x32_bf16 v[0:3], v[168:171], a[0:3], 0\nR 160,184,24576\nv_fma_f32 v237, v237, v215, v214\nv_mul_f32 v234, v44, v234\n"
"M 0,172,16\ns_mov_b32 m0, s51\nv_exp_f32 v234, v234\nv_mul_f32 v235, v45, v235\nv_mfma_f32_16x16x32_bf16 v[16:19], v[168:171], a[4:7], 0\nR 162,184,28672\n"
"v_mul_f32 v236, v46, v236\nv_mul_f32 v237, v47, v237\nM 16,172,20\nbuffer_store_dwordx4 v[32:35], v252, s[20:23], s84 offen offset:0 sc1\nv_exp_f32 v235, v235\nv_mfma_f32_16x16x32_bf16 v[8:11], v[168:171], a[8:11], 0\n"
"R 164,186,24576\nv_fma_f32 v234, v234, v216, v216\nv_exp_f32 v236, v236\nM 8,172,24\nds_write_addtid_b32 v128 offset:0\nv_exp_f32 v237, v237\n"
"v_fma_f32 v235, v235, v216, v216\nv_mfma_f32_16x16x32_bf16 v[24:27], v[168:171], a[12:15], 0\nR 166,186,28672\nds_write_addtid_b32 v129 offset:256\nv_fma_f32 v236, v236, v216, v216\nv_fma_f32 v237, v237, v216, v216\n"
"M 24,172,28\nds_write_addtid_b32 v130 offset:512\nG 7\nv_rcp_f32 v234, v234\nv_rcp_f32 v235, v235\nv_mfma_f32_16x16x32_bf16 v[28:31], v[176:179], a[12:15], 0\n"
"R 168,185,24576\nds_write_addtid_b32 v131 offset:768\nv_mul_f32 v234, v44, v234\nv_rcp_f32 v236, v236\nM 28,180,28\nds_write_addtid_b32 v132 offset:1024\n"
"v_rcp_f32 v237, v237\nv_mul_f32 v235, v45, v235\nv_mfma_f32_16x16x32_bf16 v[12:15], v[176:179], a[8:11], 0\nR 170,185,28672\nds_write_addtid_b32 v133 offset:1280\nv_mul_f32 v236, v46, v236\n"
"v_mul_f32 v237, v47, v237\nM 12,180,24\nds_write_addtid_b32 v134 offset:1536\nv_cvt_pk_bf16_f32 v42, v234, v235\nv_cvt_pk_bf16_f32 v43, v236, v237\nv_mul_f32 v230, v48, v48\n"
"v_mfma_f32_16x16x32_bf16 v[20:23], v[176:179], a[4:7], 0\nR 172,187,24576\nds_write_addtid_b32 v135 offset:1792\nv_mul_f32 v231, v49, v49\nv_mul_f32 v232, v50, v50\nM 20,180,20\n"
"buffer_store_dwordx4 v[40:43], v252, s[20:23], s84 offen offset:1024 sc1\nds_write_addtid_b32 v136 offset:2048\nv_mul_f32 v233, v51, v51\nv_mfma_f32_16x16x32_bf16 v[4:7], v[176:179], a[0:3], 0\nR 174,187,28672\nds_write_addtid_b32 v137 offset:2304\n"
"v_fma_f32 v230, v230, v218, v217\nv_fma_f32 v231, v231, v218, v217\nM 4,180,16\nds_write_addtid_b32 v138 offset:2560\nG 14\nv_fma_f32 v232, v232, v218, v217\n"
"v_fma_f32 v233, v233, v218, v217\nv_mul_f32 v230, v48, v230\nM 0,160,32\nR 176,184,32768\nds_write_addtid_b32 v139 offset:2816\nv_mul_f32 v231, v49, v231\n"
"v_mul_f32 v232, v50, v232\nM 0,164,48\nds_write_addtid_b32 v140 offset:3072\nv_exp_f32 v230, v230\nv_mul_f32 v233, v51, v233\nM 16,160,36\n"
"R 178,184,36864\nds_write_addtid_b32 v141 offset:3328\nv_fma_f32 v230, v230, v219, v219\nv_exp_f32 v231, v231\nM 16,164,52\nds_write_addtid_b32 v142 offset:3584\n"
"v_exp_f32 v232, v232\nv_exp_f32 v233, v233\nM 8,160,40\nR 180,186,32768\nds_write_addtid_b32 v143 offset:3840\nv_fma_f32 v231, v231, v219, v219\n"
"v_fma_f32 v232, v232, v219, v219\nM 8,164,56\nds_write_addtid_b32 v144 offset:4096\nv_rcp_f32 v230, v230\nv_fma_f32 v233, v233, v219, v219\nM 24,160,44\n"
"R 182,186,36864\nds_write_addtid_b32 v145 offset:4352\nv_mul_f32 v230, v48, v230\nv_rcp_f32 v231, v231\nM 24,164,60\nds_write_addtid_b32 v146 offset:4608\n"
"G 14\nv_rcp_f32 v232, v232\nv_rcp_f32 v233, v233\nM 28,168,44\nR 160,185,32768\nds_write_addtid_b32 v147 offset:4864\n"
"v_mul_f32 v231, v49, v231\nv_mul_f32 v232, v50, v232\nM 28,172,60\nds_write_addtid_b32 v148 offset:5120\nds_write_addtid_b32 v149 offset:5376\nds_write_addtid_b32 v150 offset:5632\n"
"ds_write_addtid_b32 v151 offset:5888\nds_write_addtid_b32 v152 offset:6144\nds_write_addtid_b32 v153 offset:6400\nds_write_addtid_b32 v154 offset:6656\nds_write_addtid_b32 v155 offset:6912\nds_write_addtid_b32 v156 offset:7168\n"
"ds_write_addtid_b32 v157 offset:7424\nds_write_addtid_b32 v158 offset:7680\nds_write_addtid_b32 v159 offset:7936\nv_mul_f32 v233, v51, v233\nv_cvt_pk_bf16_f32 v48, v230, v231\nv_cvt_pk_bf16_f32 v49, v232, v233\n"
"M 12,168,40\nR 162,185,36864\ns_add_u32 m0, s50, 81920\ns_add_u32 s63, s54, 256\nv_mul_f32 v234, v52, v52\nM 12,172,56\n"
"buffer_load_dwordx4 v220, s[12:15], s63 offen lds\nv_mul_f32 v235, v53, v53\nM 20,168,36\nR 164,187,32768\ns_waitcnt vmcnt(5) lgkmcnt(2)\ns_barrier\n"
"v_mul_f32 v236, v54, v54\nM 20,172,52\nds_read2st64_b32 v[128:129], v225 offset0:0 offset1:1\nv_mul_f32 v237, v55, v55\nv_fma_f32 v234, v234, v218, v217\nv_fma_f32 v235, v235, v218, v217\n"
"M 4,168,32\nR 166,187,36864\nds_read2st64_b32 v[130:131], v225 offset0:2 offset1:3\nv_fma_f32 v236, v236, v218, v217\nv_fma_f32 v237, v237, v218, v217\nM 4,172,48\n"
"ds_read2st64_b32 v[132:133], v225 offset0:4 offset1:5\nv_mul_f32 v234, v52, v234\nv_mul_f32 v235, v53, v235\nv_mul_f32 v236, v54, v236\nM 0,176,64\nR 168,184,40960\n"
"ds_read2st64_b32 v[134:135], v225 offset0:6 offset1:7\nv_mul_f32 v237, v55, v237\nv_exp_f32 v234, v234\nM 0,180,80\nbuffer_load_dwordx4 v221, s[12:15], s63 offen offset:1024 lds\nds_read2st64_b32 v[136:137], v225 offset0:8 offset1:9\n"
"v_exp_f32 v235, v235\nM 16,176,68\nR 170,184,45056\ns_add_u32 m0, s50, 90112\ns_add_u32 s63, s54, 229632\nds_read2st64_b32 v[138:139], v225 offset0:10 offset1:11\n"
"v_exp_f32 v236, v236\nM 16,180,84\nbuffer_load_dwordx4 v220, s[12:15], s63 offen lds\nG 8\nv_add_f32 v96, v96, v128\nv_exp_f32 v237, v237\n"
"M 8,176,72\nR 172,186,40960\nv_fma_f32 v234, v234, v219, v219\nv_add_f32 v97, v97, v129\nv_fma_f32 v235, v235, v219, v219\nM 8,180,88\n"
"ds_read2st64_b32 v[140:141], v225 offset0:12 offset1:13\nv_rcp_f32 v234, v234\nv_fma_f32 v236, v236, v219, v219\nM 24,176,76\nR 174,186,45056\nv_fma_f32 v237, v237, v219, v219\n"
"G 9\nv_add_f32 v98, v98, v130\nv_rcp_f32 v235, v235\nM 24,180,92\nv_rcp_f32 v236, v236\nv_add_f32 v99, v99, v131\n"
"v_rcp_f32 v237, v237\nM 28,160,76\nR 176,185,40960\nv_mul_f32 v234, v52, v234\nG 9\nv_add_f32 v100, v100, v132\n"
"v_mul_f32 v235, v53, v235\nM 28,164,92\nbuffer_load_dwordx4 v221, s[12:15], s63 offen offset:1024 lds\nv_add_f32 v101, v101, v133\nv_mul_f32 v236, v54, v236\nM 12,160,72\n"
"R 178,185,45056\ns_add_u32 s63, s54, 459008\nG 8\nv_add_f32 v102, v102, v134\nv_mul_f32 v237, v55, v237\nM 12,164,88\n"
"s_add_u32 m0, s50, 98304\nv_cvt_pk_bf16_f32 v50, v234, v235\nv_cvt_pk_bf16_f32 v51, v236, v237\nv_add_f32 v103, v103, v135\nv_mul_f32 v230, v56, v56\nM 20,160,68\n"
"R 180,187,40960\nv_mul_f32 v231, v57, v57\nG 8\nv_add_f32 v104, v104, v136\nv_mul_f32 v232, v58, v58\nM 20,164,84\n"
"buffer_load_dwordx4 v220, s[12:15], s63 offen lds\nv_add_f32 v105, v105, v137\nv_mul_f32 v233, v59, v59\nM 4,160,64\nR 182,187,45056\nv_fma_f32 v230, v230, v218, v217\n"
"G 7\nv_add_f32 v106, v106, v138\nv_fma_f32 v231, v231, v218, v217\nM 4,164,80\nG 4\nv_fma_f32 v232, v232, v218, v217\n"
"v_fma_f32 v233, v233, v218, v217\nv_add_f32 v107, v107, v139\nv_mul_f32 v230, v56, v230\nM 0,168,96\nR 160,184,49152\nv_mul_f32 v231, v57, v231\n"
"v_add_f32 v108, v108, v140\nv_mul_f32 v232, v58, v232\nM 0,172,112\nbuffer_load_dwordx4 v221, s[12:15], s63 offen offset:1024 lds\nv_add_f32 v109, v109, v141\nv_mul_f32 v233, v59, v233\n"
"M 16,168,100\nR 162,184,53248\ns_add_u32 s63, s54, 688384\nds_read2st64_b32 v[142:143], v225 offset0:14 offset1:15\nv_exp_f32 v230, v230\nM 16,172,116\n"
"s_add_u32 m0, s50, 106496\nds_read2st64_b32 v[144:145], v225 offset0:16 offset1:17\nv_exp_f32 v231, v231\nv_exp_f32 v232, v232\nM 8,168,104\nR 164,186,49152\n"
"ds_read2st64_b32 v[146:147], v225 offset0:18 offset1:19\nv_fma_f32 v230, v230, v219, v219\nv_exp_f32 v233, v233\nM 8,172,120\nbuffer_load_dwordx4 v220, s[12:15], s63 offen lds\nds_read2st64_b32 v[148:149], v225 offset0:20 offset1:21\n"
"v_fma_f32 v231, v231, v219, v219\nM 24,168,108\nR 166,186,53248\nds_read2st64_b32 v[150:151], v225 offset0:22 offset1:23\nv_fma_f32 v232, v232, v219, v219\nv_fma_f32 v233, v233, v219, v219\n"
"M 24,172,124\nds_read2st64_b32 v[152:153], v225 offset0:24 offset1:25\nG 10\nv_rcp_f32 v230, v230\nv_rcp_f32 v231, v231\nM 28,176,108\n"
"R 168,185,49152\nv_mul_f32 v230, v56, v230\nG 8\nv_add_f32 v110, v110, v142\nv_rcp_f32 v232, v232\nM 28,180,124\n"
"ds_read2st64_b32 v[154:155], v225 offset0:26 offset1:27\nv_rcp_f32 v233, v233\nv_mul_f32 v231, v57, v231\nM 12,176,104\nR 170,185,53248\nv_mul_f32 v232, v58, v232\n"
"v_add_f32 v111, v111, v143\nv_mul_f32 v233, v59, v233\nM 12,180,120\nbuffer_load_dwordx4 v221, s[12:15], s63 offen offset:1024 lds\nG 9\nv_add_f32 v112, v112, v144\n"
"v_cvt_pk_bf16_f32 v56, v230, v231\nM 20,176,100\nR 172,187,49152\ns_add_u32 s63, s54, 917760\nv_add_f32 v113, v113, v145\nv_cvt_pk_bf16_f32 v57, v232, v233\n"
"M 20,180,116\ns_add_u32 m0, s50, 114688\nds_read2st64_b32 v[156:157], v225 offset0:28 offset1:29\nds_read2st64_b32 v[158:159], v225 offset0:30 offset1:31\nv_mul_f32 v234, v60, v60\nv_mul_f32 v235, v61, v61\n"
"G 10\nv_add_f32 v114, v114, v146\nv_add_f32 v115, v115, v147\nG 9\nv_add_f32 v116, v116, v148\nv_add_f32 v117, v117, v149\n"
"G 7\nv_add_f32 v118, v118, v150\nv_add_f32 v119, v119, v151\nG 6\nv_add_f32 v120, v120, v152\nv_add_f32 v121, v121, v153\n"
"G 4\nv_add_f32 v122, v122, v154\nv_add_f32 v123, v123, v155\nG 1\nv_add_f32 v124, v124, v156\nv_add_f32 v125, v125, v157\n"
"G 0\nv_add_f32 v126, v126, v158\nv_add_f32 v127, v127, v159\nv_mul_f32 v236, v62, v62\nM 4,176,96\nR 174,187,53248\n"
"v_mul_f32 v237, v63, v63\nv_fma_f32 v234, v234, v218, v217\nM 4,180,112\nv_fma_f32 v235, v235, v218, v217\nv_fma_f32 v236, v236, v218, v217\nv_fma_f32 v237, v237, v218, v217\n"
"M 0,160,128\nR 176,184,57344\nv_mul_f32 v234, v60, v234\nv_mul_f32 v235, v61, v235\nM 0,164,144\nbuffer_load_dwordx4 v220, s[12:15], s63 offen lds\n"
"v_mul_f32 v236, v62, v236\nM 16,160,132\nR 178,184,61440\nv_mul_f32 v237, v63, v237\nv_exp_f32 v234, v234\nM 16,164,148\n"
"v_exp_f32 v235, v235\nv_exp_f32 v236, v236\nM 8,160,136\nR 180,186,57344\nLq1013:\ns_lshr_b32 s60, s45, 3\n"
"s_lshl2_add_u32 s60, s60, s40\ns_lshl_b32 s60, s60, 8\nv_lshl_add_u32 v224, v252, 3, s60\nv_add_u32 v224, Lq1013+4096-LqPC, v224\ns_mov_b64 exec, 3\nglobal_load_dword v224, v224, s[98:99]\n"
"s_mov_b64 exec, -1\nv_exp_f32 v237, v237\nM 8,164,152\nv_fma_f32 v234, v234, v219, v219\nv_fma_f32 v235, v235, v219, v219\nv_fma_f32 v236, v236, v219, v219\n"
"M 24,160,140\nR 182,186,61440\nv_fma_f32 v237, v237, v219, v219\nv_rcp_f32 v234, v234\nM 24,164,156\nG 4\n"
"v_rcp_f32 v235, v235\nM 28,168,140\nR 160,185,57344\nv_mul_f32 v234, v60, v234\nM 28,172,156\nbuffer_load_dwordx4 v221, s[12:15], s63 offen offset:1024 lds\n"
"M 12,168,136\nR 162,185,61440\ns_add_u32 m0, s50, 0\ns_add_u32 s63, s54, 1147136\nM 12,172,152\nbuffer_load_dwordx4 v220, s[12:15], s63 offen lds\n"
"M 20,168,132\nR 164,187,57344\nv_mul_f32 v235, v61, v235\nM 20,172,148\nv_rcp_f32 v236, v236\nM 4,168,128\n"
"R 166,187,61440\nv_mul_f32 v236, v62, v236\nM 4,172,144\nG 4\nv_rcp_f32 v237, v237\nM 0,176,160\n"
"R 168,188,0\nv_mul_f32 v237, v63, v237\nM 0,180,176\nbuffer_load_dwordx4 v221, s[12:15], s63 offen offset:1024 lds\nM 16,176,164\nR 170,188,4096\n"
"s_add_u32 m0, s50, 8192\ns_add_u32 s63, s54, 1376512\nM 16,180,180\nbuffer_load_dwordx4 v220, s[12:15], s63 offen lds\nM 8,176,168\nR 172,190,0\n"
"v_cvt_pk_bf16_f32 v58, v234, v235\nM 8,180,184\nbuffer_store_dwordx4 v[48:51], v252, s[20:23], s85 offen offset:0 sc1\nM 24,176,172\nR 174,190,4096\nv_cvt_pk_bf16_f32 v59, v236, v237\n"
"M 24,180,188\nbuffer_store_dwordx4 v[56:59], v252, s[20:23], s85 offen offset:1024 sc1\nG 4\nM 28,160,172\nR 176,189,0\ns_waitcnt vmcnt(12)\n"
"s_barrier\nv_mul_f32 v230, v96, v96\nM 28,164,188\nbuffer_load_dwordx4 v221, s[12:15], s63 offen offset:1024 lds\nv_mul_f32 v231, v97, v97\nM 12,160,168\n"
"R 178,189,4096\ns_add_u32 m0, s50, 16384\ns_add_u32 s63, s54, 1605888\nv_mul_f32 v232, v98, v98\nM 12,164,184\nbuffer_load_dwordx4 v220, s[12:15], s63 offen lds\n"
"v_mul_f32 v233, v99, v99\nM 20,160,164\nR 180,191,0\nv_fma_f32 v230, v230, v215, v214\nv_fma_f32 v231, v231, v215, v214\nM 20,164,180\n"
"s_add_u32 s84, s73, 37750784\ns_add_u32 s85, s73, 37865472\nv_fma_f32 v232, v232, v215, v214\nv_fma_f32 v233, v233, v215, v214\nM 4,160,160\nR 182,191,4096\n"
"s_mov_b32 s60, 158720\nv_bfe_u32 v242, v252, 4, 4\nv_lshl_add_u32 v242, v242, 3, s60\nv_mul_f32 v230, v96, v230\nM 4,164,176\nds_read_b64 v[238:239], v242 offset:0\n"
"G 5\nv_mul_f32 v231, v97, v231\nM 0,168,192\nR 160,188,8192\nds_read_b64 v[240:241], v242 offset:128\nv_mul_f32 v232, v98, v232\n"
"M 0,172,208\nds_read_b64 v[242:243], v242 offset:0\nv_mul_f32 v233, v99, v233\nv_exp_f32 v230, v230\nM 16,168,196\nR 162,188,12288\n"
"v_fma_f32 v230, v230, v216, v216\nv_exp_f32 v231, v231\nM 16,172,212\nv_exp_f32 v232, v232\nv_exp_f32 v233, v233\nM 8,168,200\n"
"R 164,190,8192\nv_fma_f32 v231, v231, v216, v216\nv_fma_f32 v232, v232, v216, v216\nM 8,172,216\nv_rcp_f32 v230, v230\nv_fma_f32 v233, v233, v216, v216\n"
"M 24,168,204\nR 166,190,12288\nv_mul_f32 v230, v96, v230\nv_rcp_f32 v231, v231\nM 24,172,220\nG 7\n"
"v_rcp_f32 v232, v232\nv_rcp_f32 v233, v233\nM 28,176,204\nR 168,189,8192\nv_mul_f32 v231, v97, v231\nv_mul_f32 v232, v98, v232\n"
"M 28,180,220\nv_mul_f32 v233, v99, v233\nv_cvt_pk_bf16_f32 v96, v230, v231\nv_cvt_pk_bf16_f32 v97, v232, v233\nM 12,176,200\nR 170,189,12288\n"
"v_mul_f32 v234, v100, v100\nv_mul_f32 v235, v101, v101\nM 12,180,216\nv_mul_f32 v236, v102, v102\nv_mul_f32 v237, v103, v103\nv_fma_f32 v234, v234, v215, v214\n"
"M 20,176,196\nR 172,191,8192\nG 7\nv_mfma_f32_16x16x32_bf16 v[0:3], v[238:241], v[208:211], v[0:3]\nv_fma_f32 v235, v235, v215, v214\nv_fma_f32 v236, v236, v215, v214\n"
"M 20,180,212\nv_fma_f32 v237, v237, v215, v214\nv_mul_f32 v234, v100, v234\nv_mul_f32 v235, v101, v235\nM 4,176,192\nR 174,191,12288\n"
"G 7\nv_mfma_f32_16x16x32_bf16 v[4:7], v[240:243], v[208:211], v[4:7]\nv_mul_f32 v236, v102, v236\nv_mul_f32 v237, v103, v237\nM 4,180,208\nG 4\n"
"v_exp_f32 v234, v234\nv_exp_f32 v235, v235\nM 0,160,224\nR 176,188,16384\nv_mfma_f32_16x16x32_bf16 v[16:19], v[240:243], v[210:213], v[16:19]\nv_fma_f32 v234, v234, v216, v216\n"
"v_exp_f32 v236, v236\nM 0,164,240\nv_exp_f32 v237, v237\nv_fma_f32 v235, v235, v216, v216\nM 16,160,228\nR 178,188,20480\n"
"v_mfma_f32_16x16x32_bf16 v[20:23], v[238:241], v[210:213], v[20:23]\nv_fma_f32 v236, v236, v216, v216\nv_fma_f32 v237, v237, v216, v216\nM 16,164,244\nv_rcp_f32 v234, v234\nv_rcp_f32 v235, v235\n"
"M 8,160,232\nR 180,188,24576\nv_mul_f32 v234, v100, v234\nv_rcp_f32 v236, v236\nM 8,164,248\nv_rcp_f32 v237, v237\n"
"v_mul_f32 v235, v101, v235\nM 24,160,236\nR 182,188,28672\nv_mul_f32 v236, v102, v236\nv_mul_f32 v237, v103, v237\nM 24,164,252\n"
"G 4\nv_cvt_pk_bf16_f32 v98, v234, v235\nv_cvt_pk_bf16_f32 v99, v236, v237\nv_mul_f32 v230, v104, v104\nM 28,168,236\nR 160,189,16384\n"
"v_mul_f32 v231, v105, v105\nv_mul_f32 v232, v106, v106\nM 28,172,252\nv_mul_f32 v233, v107, v107\nv_fma_f32 v230, v230, v215, v214\nv_fma_f32 v231, v231, v215, v214\n"
"M 12,168,232\nR 162,189,20480\nv_fma_f32 v232, v232, v215, v214\nv_fma_f32 v233, v233, v215, v214\nM 12,172,248\nv_mul_f32 v230, v104, v230\n"
"v_mul_f32 v231, v105, v231\nv_mul_f32 v232, v106, v232\nM 20,168,228\nR 164,189,24576\nv_mul_f32 v233, v107, v233\nv_exp_f32 v230, v230\n"
"M 20,172,244\nv_exp_f32 v231, v231\nv_exp_f32 v232, v232\nM 4,168,224\nR 166,189,28672\nv_fma_f32 v230, v230, v216, v216\n"
"v_exp_f32 v233, v233\nM 4,172,240\ns_waitcnt vmcnt(12)\ns_barrier\nG 4\nv_fma_f32 v231, v231, v216, v216\n"
"v_mfma_f32_16x16x32_bf16 v[32:35], v[176:179], a[0:3], 0\nR 168,190,16384\nv_fma_f32 v232, v232, v216, v216\nv_fma_f32 v233, v233, v216, v216\nM 32,180,16\ns_mov_b32 m0, s51\n"
"v_rcp_f32 v230, v230\nv_rcp_f32 v231, v231\nv_mfma_f32_16x16x32_bf16 v[48:51], v[176:179], a[4:7], 0\nR 170,190,20480\nds_write_addtid_b32 v8 offset:0\nv_mul_f32 v230, v104, v230\n"
"v_rcp_f32 v232, v232\nM 48,180,20\nds_write_addtid_b32 v9 offset:256\nv_rcp_f32 v233, v233\nv_mul_f32 v231, v105, v231\nv_mfma_f32_16x16x32_bf16 v[64:67], v[176:179], a[8:11], 0\n"
"R 172,190,24576\nds_write_addtid_b32 v10 offset:512\nv_mov_b32 v226, s46\nv_mul_f32 v232, v106, v232\nM 64,180,24\nds_write_addtid_b32 v11 offset:768\n"
"v_mov_b32 v227, s47\nv_mul_f32 v233, v107, v233\nv_cvt_pk_bf16_f32 v104, v230, v231\nv_mfma_f32_16x16x32_bf16 v[80:83], v[176:179], a[12:15], 0\nR 174,190,28672\ns_add_u32 s61, s81, 845824\n"
"s_lshl_b32 s62, s44, 5\ns_add_u32 s61, s61, s62\ns_waitcnt vmcnt(0)\ns_mov_b64 exec, 1\nbuffer_atomic_swap_x2 v[226:227], off, s[20:23], s61 sc1\ns_mov_b64 exec, -1\n"
"ds_write_addtid_b32 v12 offset:1024\nv_cvt_pk_bf16_f32 v105, v232, v233\nM 80,180,28\nds_write_addtid_b32 v13 offset:1280\nG 10\nv_mul_f32 v234, v108, v108\n"
"v_mul_f32 v235, v109, v109\nv_mul_f32 v236, v110, v110\nv_mfma_f32_16x16x32_bf16 v[84:87], v[160:163], a[12:15], 0\nR 176,191,16384\nds_write_addtid_b32 v14 offset:1536\nv_mul_f32 v237, v111, v111\n"
"v_fma_f32 v234, v234, v215, v214\nM 84,164,28\nds_write_addtid_b32 v15 offset:1792\nv_fma_f32 v235, v235, v215, v214\nv_fma_f32 v236, v236, v215, v214\nv_fma_f32 v237, v237, v215, v214\n"
"v_mfma_f32_16x16x32_bf16 v[68:71], v[160:163], a[8:11], 0\nR 178,191,20480\nds_write_addtid_b32 v24 offset:4096\nv_mul_f32 v234, v108, v234\nv_mul_f32 v235, v109, v235\nM 68,164,24\n"
"buffer_store_dwordx4 v[96:99], v252, s[20:23], s84 offen offset:0 sc1\nds_write_addtid_b32 v25 offset:4352\nv_mul_f32 v236, v110, v236\nv_mfma_f32_16x16x32_bf16 v[52:55], v[160:163], a[4:7], 0\nR 180,191,24576\nds_write_addtid_b32 v26 offset:4608\n"
"v_mul_f32 v237, v111, v237\nv_exp_f32 v234, v234\nM 52,164,20\nds_write_addtid_b32 v27 offset:4864\nds_write_addtid_b32 v28 offset:5120\nds_write_addtid_b32 v29 offset:5376\n"
"ds_write_addtid_b32 v30 offset:5632\nds_write_addtid_b32 v31 offset:5888\nv_exp_f32 v235, v235\nv_exp_f32 v236, v236\nv_mfma_f32_16x16x32_bf16 v[36:39], v[160:163], a[0:3], 0\nR 182,191,28672\n"
"s_add_u32 m0, s50, 16384\nv_exp_f32 v237, v237\nM 36,164,16\nG 15\nv_fma_f32 v234, v234, v216, v216\nv_fma_f32 v235, v235, v216, v216\n"
"v_fma_f32 v236, v236, v216, v216\nv_mfma_f32_16x16x32_bf16 v[40:43], v[168:171], a[0:3], 0\nR 160,188,32768\nG 2\ns_barrier\nv_fma_f32 v237, v237, v216, v216\n"
"M 40,172,16\nbuffer_load_dwordx4 v221, s[12:15], s63 offen offset:1024 lds\nds_read2st64_b32 v[8:9], v225 offset0:0 offset1:1\nv_rcp_f32 v234, v234\nv_mfma_f32_16x16x32_bf16 v[56:59], v[168:171], a[4:7], 0\nR 162,188,36864\n"
"s_add_u32 m0, s50, 24576\ns_add_u32 s63, s54, 1835264\nds_read2st64_b32 v[10:11], v225 offset0:2 offset1:3\nv_rcp_f32 v235, v235\nM 56,172,20\nbuffer_load_dwordx4 v220, s[12:15], s63 offen lds\n"
"ds_read2st64_b32 v[12:13], v225 offset0:4 offset1:5\nv_rcp_f32 v236, v236\nv_mfma_f32_16x16x32_bf16 v[72:75], v[168:171], a[8:11], 0\nR 164,188,40960\nds_read2st64_b32 v[14:15], v225 offset0:6 offset1:7\nv_mov_b32 v248, 0\n"
"v_rcp_f32 v237, v237\nM 72,172,24\nds_read2st64_b32 v[24:25], v225 offset0:16 offset1:17\nv_mov_b32 v249, 0\nv_mul_f32 v234, v108, v234\nv_mul_f32 v235, v109, v235\n"
"v_mfma_f32_16x16x32_bf16 v[88:91], v[168:171], a[12:15], 0\nR 166,188,45056\ns_add_u32 s61, s82, 845824\ns_mov_b64 exec, 0xf\nbuffer_atomic_or_x2 v[248:249], v250, s[20:23], s61 offen sc0 sc1\ns_mov_b64 exec, -1\n"
"ds_read2st64_b32 v[26:27], v225 offset0:18 offset1:19\nv_mul_f32 v236, v110, v236\nM 88,172,28\nG 10\nv_mul_f32 v237, v111, v237\nv_cvt_pk_bf16_f32 v106, v234, v235\n"
"G 8\nv_add_f32 v0, v0, v8\nv_cvt_pk_bf16_f32 v107, v236, v237\nv_mfma_f32_16x16x32_bf16 v[92:95], v[176:179], a[12:15], 0\nR 168,189,32768\nv_mul_f32 v230, v112, v112\n"
"v_add_f32 v1, v1, v9\nv_mul_f32 v231, v113, v113\nM 92,180,28\nbuffer_load_dwordx4 v221, s[12:15], s63 offen offset:1024 lds\nG 7\nv_add_f32 v2, v2, v10\n"
"v_mul_f32 v232, v114, v114\nv_mfma_f32_16x16x32_bf16 v[76:79], v[176:179], a[8:11], 0\nR 170,189,36864\ns_add_u32 m0, s50, 32768\ns_add_u32 s63, s54, 2064640\nv_add_f32 v3, v3, v11\n"
"v_mul_f32 v233, v115, v115\nM 76,180,24\nbuffer_load_dwordx4 v220, s[12:15], s63 offen lds\nG 7\nv_add_f32 v4, v4, v12\nv_fma_f32 v230, v230, v218, v217\n"
"v_mfma_f32_16x16x32_bf16 v[60:63], v[176:179], a[4:7], 0\nR 172,189,40960\nv_fma_f32 v231, v231, v218, v217\nv_add_f32 v5, v5, v13\nv_fma_f32 v232, v232, v218, v217\nM 60,180,20\n"
"ds_read2st64_b32 v[28:29], v225 offset0:20 offset1:21\nv_fma_f32 v233, v233, v218, v217\nv_mul_f32 v230, v112, v230\nv_mul_f32 v231, v113, v231\nv_mfma_f32_16x16x32_bf16 v[44:47], v[176:179], a[0:3], 0\nR 174,189,45056\n"
"v_mul_f32 v232, v114, v232\nG 8\nv_add_f32 v6, v6, v14\nv_mul_f32 v233, v115, v233\nM 44,180,16\nG 6\n"
"v_exp_f32 v230, v230\nv_add_f32 v7, v7, v15\nv_exp_f32 v231, v231\nM 32,160,32\nR 176,190,32768\nv_fma_f32 v230, v230, v219, v219\n"
"v_add_f32 v16, v16, v24\nv_exp_f32 v232, v232\nM 32,164,48\nbuffer_load_dwordx4 v221, s[12:15], s63 offen offset:1024 lds\nv_add_f32 v17, v17, v25\nv_exp_f32 v233, v233\n"
"M 48,160,36\nR 178,190,36864\nv_fma_f32 v231, v231, v219, v219\nG 7\nv_add_f32 v18, v18, v26\nv_fma_f32 v232, v232, v219, v219\n"
"M 48,164,52\nds_read2st64_b32 v[30:31], v225 offset0:22 offset1:23\nv_rcp_f32 v230, v230\nv_fma_f32 v233, v233, v219, v219\nM 64,160,40\nR 180,190,40960\n"
"v_mul_f32 v230, v112, v230\nv_add_f32 v19, v19, v27\nG 5\nv_add_f32 v20, v20, v28\nv_add_f32 v21, v21, v29\nG 1\n"
"v_add_f32 v22, v22, v30\nv_add_f32 v23, v23, v31\nv_rcp_f32 v231, v231\nM 64,164,56\ns_add_u32 m0, s50, 40960\nv_rcp_f32 v232, v232\n"
"v_rcp_f32 v233, v233\nM 80,160,44\nR 182,190,45056\nv_mul_f32 v231, v113, v231\nv_mul_f32 v232, v114, v232\nM 80,164,60\n"
"v_mul_f32 v233, v115, v233\nv_cvt_pk_bf16_f32 v112, v230, v231\nv_cvt_pk_bf16_f32 v113, v232, v233\nM 84,168,44\nR 160,191,32768\nv_mul_f32 v234, v116, v116\n"
"v_mul_f32 v235, v117, v117\nM 84,172,60\ns_add_u32 s63, s54, 2294016\nv_mul_f32 v236, v118, v118\nv_mul_f32 v237, v119, v119\nv_fma_f32 v234, v234, v218, v217\n"
"M 68,168,40\nR 162,191,36864\nv_fma_f32 v235, v235, v218, v217\nM 68,172,56\nbuffer_load_dwordx4 v220, s[12:15], s63 offen lds\nM 52,168,36\n"
"R 164,191,40960\nv_fma_f32 v236, v236, v218, v217\nM 52,172,52\nv_fma_f32 v237, v237, v218, v217\nv_mul_f32 v234, v116, v234\nM 36,168,32\n"
"R 166,191,45056\nv_mul_f32 v235, v117, v235\nM 36,172,48\nG 4\nv_exp_f32 v234, v234\nM 40,176,32\n"
"R 168,188,49152\nv_mul_f32 v236, v118, v236\nM 40,180,48\nbuffer_load_dwordx4 v221, s[12:15], s63 offen offset:1024 lds\nM 56,176,36\nR 170,188,53248\n"
"v_mul_f32 v237, v119, v237\nM 56,180,52\ns_add_u32 m0, s50, 49152\nv_exp_f32 v235, v235\nM 72,176,40\nR 172,184,0\n"
"s_add_u32 s63, s54, 2523392\nM 72,180,56\ns_add_u32 s64, s68, 37748736\nv_exp_f32 v236, v236\nM 88,176,44\nR 174,184,4096\n"
"v_fma_f32 v234, v234, v219, v219\nM 88,180,60\nG 4\nv_exp_f32 v237, v237\nM 92,160,44\nR 176,189,49152\n"
"s_waitcnt vmcnt(5)\nv_cmp_eq_u64 s[86:87], s[46:47], v[248:249]\nM 92,164,60\nbuffer_load_dwordx4 v220, s[12:15], s63 offen lds\nM 76,160,40\nR 178,189,53248\n"
"s_and_b32 s62, s86, 0xf\ns_cmp_eq_u32 s62, 0xf\ns_cbranch_scc0 Lq812\nLq813:\nM 76,164,56\ns_add_u32 s65, s68, 37863424\n"
"v_rcp_f32 v234, v234\nM 60,160,36\nR 180,185,0\nv_fma_f32 v235, v235, v219, v219\nM 60,164,52\nbuffer_load_dwordx4 a[0:3], v252, s[20:23], s64 offen sc1\n"
"M 44,160,32\nR 182,185,4096\nv_fma_f32 v236, v236, v219, v219\nM 44,164,48\nG 4\nv_rcp_f32 v235, v235\n"
"M 32,168,64\nR 160,190,49152\nv_fma_f32 v237, v237, v219, v219\nM 32,172,80\ns_add_u32 s66, s69, 37748736\nv_rcp_f32 v236, v236\n"
"M 48,168,68\nR 162,190,53248\nv_mul_f32 v234, v116, v234\nM 48,172,84\nbuffer_load_dwordx4 a[4:7], v252, s[20:23], s65 offen sc1\nM 64,168,72\n"
"R 164,186,0\nv_mul_f32 v235, v117, v235\nM 64,172,88\nv_rcp_f32 v237, v237\nM 80,168,76\nR 166,186,4096\n"
"v_mul_f32 v236, v118, v236\nM 80,172,92\nG 4\nv_mul_f32 v237, v119, v237\nv_cvt_pk_bf16_f32 v114, v234, v235\nM 84,176,76\n"
"R 168,191,49152\nv_cvt_pk_bf16_f32 v115, v236, v237\nM 84,180,92\nbuffer_load_dwordx4 a[8:11], v252, s[20:23], s66 offen sc1\nM 68,176,72\nR 170,191,53248\n"
"v_mul_f32 v230, v120, v120\nM 68,180,88\ns_add_u32 s67, s69, 37863424\nv_mul_f32 v231, v121, v121\nv_mul_f32 v232, v122, v122\nM 52,176,68\n"
"R 172,187,0\nv_mul_f32 v233, v123, v123\nM 52,180,84\ns_add_u32 s64, s68, 37755904\nbuffer_load_dwordx4 v221, s[12:15], s63 offen offset:1024 lds\nM 36,176,64\n"
"R 174,187,4096\ns_add_u32 m0, s50, 57344\nM 36,180,80\ns_add_u32 s63, s54, 2752768\nG 4\nv_fma_f32 v230, v230, v218, v217\n"
"M 40,160,64\nR 176,184,8192\ns_waitcnt vmcnt(7)\ns_barrier\nM 40,164,80\nbuffer_load_dwordx4 a[12:15], v252, s[20:23], s67 offen sc1\n"
"buffer_load_dwordx4 v220, s[12:15], s63 offen lds\nM 56,160,68\nR 178,184,12288\nv_fma_f32 v231, v231, v218, v217\nM 56,164,84\ns_add_u32 s65, s68, 37870592\n"
"v_fma_f32 v232, v232, v218, v217\nv_fma_f32 v233, v233, v218, v217\nM 72,160,72\nR 180,184,16384\nv_mul_f32 v230, v120, v230\nM 72,164,88\n"
"buffer_load_dwordx4 a[16:19], v252, s[20:23], s64 offen sc1\nbuffer_load_dwordx4 v221, s[12:15], s63 offen offset:1024 lds\nM 88,160,76\nR 182,184,20480\ns_add_u32 m0, s50, 65536\nM 88,164,92\n"
"G 4\nv_exp_f32 v230, v230\nM 92,168,76\nR 160,185,8192\ns_add_u32 s63, s54, 2982144\nM 92,172,92\n"
"s_add_u32 s66, s69, 37755904\nbuffer_load_dwordx4 v220, s[12:15], s63 offen lds\nM 76,168,72\nR 162,185,12288\nv_mul_f32 v231, v121, v231\nM 76,172,88\n"
"buffer_load_dwordx4 a[20:23], v252, s[20:23], s65 offen sc1\nM 60,168,68\nR 164,185,16384\nv_mul_f32 v232, v122, v232\nM 60,172,84\nbuffer_load_dwordx4 v221, s[12:15], s63 offen offset:1024 lds\n"
"M 44,168,64\nR 166,185,20480\nv_mul_f32 v233, v123, v233\nM 44,172,80\nG 4\nv_exp_f32 v231, v231\n"
"M 32,176,96\nR 168,186,8192\nv_fma_f32 v230, v230, v219, v219\nM 32,180,112\nbuffer_load_dwordx4 a[24:27], v252, s[20:23], s66 offen sc1\nM 48,176,100\n"
"R 170,186,12288\nv_fma_f32 v231, v231, v219, v219\nM 48,180,116\ns_add_u32 s67, s69, 37870592\nv_exp_f32 v232, v232\nM 64,176,104\n"
"R 172,186,16384\nv_fma_f32 v232, v232, v219, v219\nM 64,180,120\nbuffer_store_dwordx4 v[104:107], v252, s[20:23], s84 offen offset:1024 sc1\nM 80,176,108\nR 174,186,20480\n"
"v_mul_f32 v234, v124, v124\nM 80,180,124\nG 4\nv_exp_f32 v233, v233\nM 84,160,108\nR 176,187,8192\n"
"v_fma_f32 v233, v233, v219, v219\nM 84,164,124\nbuffer_load_dwordx4 a[28:31], v252, s[20:23], s67 offen sc1\nM 68,160,104\nR 178,187,12288\nv_mul_f32 v235, v125, v125\n"
"M 68,164,120\ns_add_u32 s64, s68, 37749760\nv_rcp_f32 v230, v230\nM 52,160,100\nR 180,187,16384\nv_mul_f32 v230, v120, v230\n"
"M 52,164,116\ns_add_u32 s65, s68, 37864448\nv_rcp_f32 v231, v231\nM 36,160,96\nR 182,187,20480\nv_mul_f32 v231, v121, v231\n"
"M 36,164,112\nG 4\nv_rcp_f32 v232, v232\nM 40,168,96\nR 160,184,24576\nv_mul_f32 v232, v122, v232\n"
"M 40,172,112\nbuffer_load_dwordx4 a[32:35], v252, s[20:23], s64 offen sc1\nM 56,168,100\nR 162,184,28672\nv_cvt_pk_bf16_f32 v120, v230, v231\nM 56,172,116\n"
"s_add_u32 s66, s69, 37749760\nv_rcp_f32 v233, v233\nM 72,168,104\nR 164,184,32768\nv_mul_f32 v233, v123, v233\nM 72,172,120\n"
"buffer_load_dwordx4 a[36:39], v252, s[20:23], s65 offen sc1\nM 88,168,108\nR 166,184,36864\nv_cvt_pk_bf16_f32 v121, v232, v233\nM 88,172,124\nG 4\n"
"v_mul_f32 v236, v126, v126\nv_mul_f32 v237, v127, v127\nM 92,176,108\nR 168,185,24576\ns_add_u32 m0, s50, 73728\nM 92,180,124\n"
"s_add_u32 s67, s69, 37864448\nv_fma_f32 v234, v234, v218, v217\nv_fma_f32 v235, v235, v218, v217\nM 76,176,104\nR 170,185,28672\ns_add_u32 s63, s54, 3211520\n"
"M 76,180,120\nbuffer_load_dwordx4 a[40:43], v252, s[20:23], s66 offen sc1\nM 60,176,100\nR 172,185,32768\nv_fma_f32 v236, v236, v218, v217\nM 60,180,116\n"
"buffer_load_dwordx4 v220, s[12:15], s63 offen lds\nM 44,176,96\nR 174,185,36864\nv_fma_f32 v237, v237, v218, v217\nM 44,180,112\nG 4\n"
"v_mul_f32 v234, v124, v234\nv_mul_f32 v235, v125, v235\nM 32,160,128\nR 176,186,24576\nv_mul_f32 v236, v126, v236\nM 32,164,144\n"
"buffer_load_dwordx4 a[44:47], v252, s[20:23], s67 offen sc1\nbuffer_load_dwordx4 v221, s[12:15], s63 offen offset:1024 lds\nM 48,160,132\nR 178,186,28672\ns_add_u32 m0, s50, 81920\nM 48,164,148\n"
"s_add_u32 s64, s68, 37756928\nv_exp_f32 v234, v234\nM 64,160,136\nR 180,186,32768\ns_add_u32 s63, s54, 3440896\nM 64,164,152\n"
"s_add_u32 s65, s68, 37871616\nbuffer_load_dwordx4 v220, s[12:15], s63 offen lds\nM 80,160,140\nR 182,186,36864\nv_mul_f32 v237, v127, v237\nM 80,164,156\n"
"G 4\nv_exp_f32 v235, v235\nM 84,168,140\nR 160,187,24576\nv_fma_f32 v234, v234, v219, v219\nM 84,172,156\n"
"buffer_load_dwordx4 a[48:51], v252, s[20:23], s64 offen sc1\nbuffer_load_dwordx4 v221, s[12:15], s63 offen offset:1024 lds\nM 68,168,136\nR 162,187,28672\nv_fma_f32 v235, v235, v219, v219\nM 68,172,152\n"
"s_add_u32 s66, s69, 37756928\nv_exp_f32 v236, v236\nM 52,168,132\nR 164,187,32768\ns_waitcnt vmcnt(12)\ns_barrier\n"
"M 52,172,148\nbuffer_load_dwordx4 a[52:55], v252, s[20:23], s65 offen sc1\ns_movk_i32 s63, 2048\nv_mad_u32_u24 v220, v228, s63, v229\nv_add_u32 v221, 916480, v220\ns_add_u32 m0, s50, 90112\n"
"s_add_u32 s63, s56, 0\nbuffer_load_dwordx4 v220, s[16:19], s63 offen lds\nM 36,168,128\nR 166,187,36864\nv_fma_f32 v236, v236, v219, v219\nM 36,172,144\n"
"G 4\nv_exp_f32 v237, v237\nM 40,176,128\nR 168,184,40960\nv_fma_f32 v237, v237, v219, v219\nM 40,180,144\n"
"s_add_u32 s67, s69, 37871616\nbuffer_load_dwordx4 v221, s[16:19], s63 offen offset:1024 lds\nM 56,176,132\nR 170,184,45056\ns_add_u32 m0, s50, 98304\nM 56,180,148\n"
"buffer_load_dwordx4 a[56:59], v252, s[20:23], s66 offen sc1\nM 72,176,136\nR 172,184,49152\ns_add_u32 s63, s56, 524288\nM 72,180,152\nbuffer_load_dwordx4 v220, s[16:19], s63 offen lds\n"
"M 88,176,140\nR 174,184,53248\nv_rcp_f32 v234, v234\nM 88,180,156\nG 4\nv_rcp_f32 v235, v235\n"
"M 92,160,140\nR 176,185,40960\nv_mul_f32 v234, v124, v234\nM 92,164,156\nbuffer_load_dwordx4 a[60:63], v252, s[20:23], s67 offen sc1\nbuffer_load_dwordx4 v221, s[16:19], s63 offen offset:1024 lds\n"
"M 76,160,136\nR 178,185,45056\ns_add_u32 s63, s56, 65536\nM 76,164,152\ns_add_u32 m0, s50, 106496\nv_rcp_f32 v236, v236\n"
"M 60,160,132\nR 180,185,49152\nv_mul_f32 v235, v125, v235\nM 60,164,148\nbuffer_load_dwordx4 v220, s[16:19], s63 offen lds\nM 44,160,128\n"
"R 182,185,53248\nv_mul_f32 v236, v126, v236\nM 44,164,144\nG 4\nv_rcp_f32 v237, v237\nM 32,168,160\n"
"R 160,186,40960\nv_mul_f32 v237, v127, v237\nM 32,172,176\nbuffer_load_dwordx4 v221, s[16:19], s63 offen offset:1024 lds\nM 48,168,164\nR 162,186,45056\n"
"s_add_u32 s63, s56, 589824\nM 48,172,180\ns_add_u32 m0, s50, 114688\nv_cvt_pk_bf16_f32 v122, v234, v235\nv_cvt_pk_bf16_f32 v123, v236, v237\nM 64,168,168\n"
"R 164,186,49152\nM 64,172,184\nbuffer_load_dwordx4 v220, s[16:19], s63 offen lds\nM 80,168,172\nR 166,186,53248\nbuffer_store_dwordx4 v[112:115], v252, s[20:23], s85 offen offset:0 sc1\n"
"M 80,172,188\nbuffer_store_dwordx4 v[120:123], v252, s[20:23], s85 offen offset:1024 sc1\nG 4\nM 84,176,172\nR 168,187,40960\nM 84,180,188\n"
"buffer_load_dwordx4 v221, s[16:19], s63 offen offset:1024 lds\nM 68,176,168\nR 170,187,45056\ns_add_u32 s63, s57, 0\nv_mul_f32 v230, v0, v0\nM 68,180,184\n"
"s_add_u32 m0, s50, 0\ns_add_u32 s84, s73, 37754880\ns_add_u32 s85, s73, 37869568\nv_mul_f32 v231, v1, v1\nM 52,176,164\nR 172,187,49152\n"
"v_mul_f32 v232, v2, v2\nv_mul_f32 v233, v3, v3\nM 52,180,180\nbuffer_load_dwordx4 v220, s[16:19], s63 offen lds\nv_fma_f32 v230, v230, v215, v214\nM 36,176,160\n"
"R 174,187,53248\nv_fma_f32 v231, v231, v215, v214\nv_fma_f32 v232, v232, v215, v214\nM 36,180,176\nG 4\nv_fma_f32 v233, v233, v215, v214\n"
"v_mul_f32 v230, v0, v230\nv_mul_f32 v231, v1, v231\nM 40,160,160\nR 176,184,57344\nv_mul_f32 v232, v2, v232\nv_mul_f32 v233, v3, v233\n"
"M 40,164,176\nv_exp_f32 v230, v230\nv_exp_f32 v231, v231\nM 56,160,164\nR 178,184,61440\nv_fma_f32 v230, v230, v216, v216\n"
"v_exp_f32 v232, v232\nM 56,164,180\nv_exp_f32 v233, v233\nv_fma_f32 v231, v231, v216, v216\nM 72,160,168\nR 180,188,0\n"
"v_fma_f32 v232, v232, v216, v216\nv_fma_f32 v233, v233, v216, v216\nM 72,164,184\nv_rcp_f32 v230, v230\nv_rcp_f32 v231, v231\nM 88,160,172\n"
"R 182,188,4096\nv_mul_f32 v230, v0, v230\nv_rcp_f32 v232, v232\nM 88,164,188\nG 4\nv_rcp_f32 v233, v233\n"
"v_mul_f32 v231, v1, v231\nM 92,168,172\nR 160,185,57344\nv_mul_f32 v232, v2, v232\nv_mul_f32 v233, v3, v233\nM 92,172,188\n"
"v_cvt_pk_bf16_f32 v0, v230, v231\nv_cvt_pk_bf16_f32 v1, v232, v233\nv_mul_f32 v234, v4, v4\nM 76,168,168\nR 162,185,61440\nv_mul_f32 v235, v5, v5\n"
"v_mul_f32 v236, v6, v6\nM 76,172,184\nv_mul_f32 v237, v7, v7\nv_fma_f32 v234, v234, v215, v214\nv_fma_f32 v235, v235, v215, v214\nM 60,168,164\n"
"R 164,189,0\nv_fma_f32 v236, v236, v215, v214\nv_fma_f32 v237, v237, v215, v214\nM 60,172,180\nv_mul_f32 v234, v4, v234\nv_mul_f32 v235, v5, v235\n"
"v_mul_f32 v236, v6, v236\nM 44,168,160\nR 166,189,4096\nv_mul_f32 v237, v7, v237\nv_exp_f32 v234, v234\nM 44,172,176\n"
"G 4\nv_exp_f32 v235, v235\nv_exp_f32 v236, v236\nM 32,176,192\nR 168,186,57344\nv_fma_f32 v234, v234, v216, v216\n"
"v_exp_f32 v237, v237\nM 32,180,208\nv_rcp_f32 v234, v234\nv_fma_f32 v235, v235, v216, v216\nM 48,176,196\nR 170,186,61440\n"
"v_fma_f32 v236, v236, v216, v216\nv_fma_f32 v237, v237, v216, v216\nM 48,180,212\nv_rcp_f32 v235, v235\nv_rcp_f32 v236, v236\nM 64,176,200\n"
"R 172,190,0\nv_mul_f32 v234, v4, v234\nv_rcp_f32 v237, v237\nM 64,180,216\nv_mul_f32 v235, v5, v235\nv_mul_f32 v236, v6, v236\n"
"v_mul_f32 v237, v7, v237\nM 80,176,204\nR 174,190,4096\nv_cvt_pk_bf16_f32 v2, v234, v235\nv_cvt_pk_bf16_f32 v3, v236, v237\nM 80,180,220\n"
"G 4\nv_mul_f32 v230, v16, v16\nv_mul_f32 v231, v17, v17\nv_mul_f32 v232, v18, v18\nM 84,160,204\nR 176,187,57344\n"
"s_waitcnt vmcnt(7)\ns_barrier\nv_mul_f32 v233, v19, v19\nM 84,164,220\nv_fma_f32 v230, v230, v218, v217\nv_fma_f32 v231, v231, v218, v217\n"
"v_fma_f32 v232, v232, v218, v217\nM 68,160,200\nR 178,187,61440\nv_fma_f32 v233, v233, v218, v217\nv_mul_f32 v230, v16, v230\nM 68,164,216\n"
"v_exp_f32 v230, v230\nv_mul_f32 v231, v17, v231\nM 52,160,196\nR 180,191,0\nv_mul_f32 v232, v18, v232\nv_mul_f32 v233, v19, v233\n"
"M 52,164,212\nv_exp_f32 v231, v231\nv_exp_f32 v232, v232\nM 36,160,192\nR 182,191,4096\nv_fma_f32 v230, v230, v219, v219\n"
"v_exp_f32 v233, v233\nM 36,164,208\nG 4\nv_rcp_f32 v230, v230\nv_fma_f32 v231, v231, v219, v219\nM 40,168,192\n"
"R 160,188,8192\nv_fma_f32 v232, v232, v219, v219\nv_fma_f32 v233, v233, v219, v219\nM 40,172,208\nv_rcp_f32 v231, v231\nv_rcp_f32 v232, v232\n"
"M 56,168,196\nR 162,188,12288\nv_mul_f32 v230, v16, v230\nv_rcp_f32 v233, v233\nM 56,172,212\nv_mul_f32 v231, v17, v231\n"
"v_mul_f32 v232, v18, v232\nv_mul_f32 v233, v19, v233\nM 72,168,200\nR 164,188,16384\nv_cvt_pk_bf16_f32 v16, v230, v231\nv_cvt_pk_bf16_f32 v17, v232, v233\n"
"M 72,172,216\nv_mul_f32 v234, v20, v20\nv_mul_f32 v235, v21, v21\nv_mul_f32 v236, v22, v22\nM 88,168,204\nR 166,188,20480\n"
"v_mov_b32 v226, s46\nv_mul_f32 v237, v23, v23\nM 88,172,220\nG 4\nv_mov_b32 v227, s47\nv_fma_f32 v234, v234, v218, v217\n"
"v_fma_f32 v235, v235, v218, v217\nM 92,176,204\nR 168,189,8192\ns_add_u32 s61, s81, 845832\ns_lshl_b32 s62, s44, 5\ns_add_u32 s61, s61, s62\n"
"s_waitcnt vmcnt(0)\ns_mov_b64 exec, 1\nbuffer_atomic_swap_x2 v[226:227], off, s[20:23], s61 sc1\ns_mov_b64 exec, -1\nv_fma_f32 v236, v236, v218, v217\nM 92,180,220\n"
"buffer_load_dwordx4 v221, s[16:19], s63 offen offset:1024 lds\nv_fma_f32 v237, v237, v218, v217\nM 76,176,200\nR 170,189,12288\ns_add_u32 s63, s57, 524288\nv_mul_f32 v234, v20, v234\n"
"M 76,180,216\ns_add_u32 m0, s50, 8192\nv_exp_f32 v234, v234\nv_mul_f32 v235, v21, v235\nM 60,176,196\nR 172,189,16384\n"
"v_mul_f32 v236, v22, v236\nv_mul_f32 v237, v23, v237\nM 60,180,212\nbuffer_load_dwordx4 v220, s[16:19], s63 offen lds\nv_exp_f32 v235, v235\nM 44,176,192\n"
"R 174,189,20480\ns_mov_b32 s60, 158208\nv_bfe_u32 v246, v252, 4, 4\nv_lshl_add_u32 v246, v246, 3, s60\nv_exp_f32 v236, v236\nM 44,180,208\n"
"ds_read_b64 v[238:239], v246 offset:0\nG 5\nv_exp_f32 v237, v237\nM 32,160,224\nR 176,190,8192\nds_read_b64 v[240:241], v246 offset:128\n"
"v_fma_f32 v234, v234, v219, v219\nM 32,164,240\nds_read_b64 v[242:243], v246 offset:256\nv_fma_f32 v235, v235, v219, v219\nv_fma_f32 v236, v236, v219, v219\nM 48,160,228\n"
"R 178,190,12288\nds_read_b64 v[244:245], v246 offset:384\nv_fma_f32 v237, v237, v219, v219\nM 48,164,244\nds_read_b64 v[246:247], v246 offset:0\nbuffer_load_dwordx4 v221, s[16:19], s63 offen offset:1024 lds\n"
"v_rcp_f32 v234, v234\nM 64,160,232\nR 180,190,16384\ns_add_u32 m0, s50, 16384\ns_add_u32 s63, s57, 65536\nv_rcp_f32 v235, v235\n"
"M 64,164,248\nbuffer_load_dwordx4 v220, s[16:19], s63 offen lds\nM 80,160,236\nR 182,190,20480\nv_mul_f32 v234, v20, v234\nM 80,164,252\n"
"G 9\nv_rcp_f32 v236, v236\nM 84,168,236\nR 160,191,8192\nv_mul_f32 v235, v21, v235\nM 84,172,252\n"
"buffer_load_dwordx4 v221, s[16:19], s63 offen offset:1024 lds\nM 68,168,232\nR 162,191,12288\ns_add_u32 s63, s57, 589824\nM 68,172,248\ns_add_u32 m0, s50, 24576\n"
"v_rcp_f32 v237, v237\nM 52,168,228\nR 164,191,16384\nG 9\nv_mfma_f32_16x16x32_bf16 v[32:35], v[238:241], v[208:211], v[32:35]\nv_mul_f32 v236, v22, v236\n"
"M 52,172,244\nbuffer_load_dwordx4 v220, s[16:19], s63 offen lds\nM 36,168,224\nR 166,191,20480\nG 9\nv_mfma_f32_16x16x32_bf16 v[36:39], v[240:243], v[208:211], v[36:39]\n"
"v_mul_f32 v237, v23, v237\nM 36,172,240\nG 4\nv_cvt_pk_bf16_f32 v18, v234, v235\nv_cvt_pk_bf16_f32 v19, v236, v237\nM 40,176,224\n"
"R 168,188,24576\nv_mfma_f32_16x16x32_bf16 v[40:43], v[242:245], v[208:211], v[40:43]\nbuffer_store_dwordx4 v[0:3], v252, s[20:23], s84 offen offset:0 sc1\nM 40,180,240\nbuffer_load_dwordx4 v221, s[16:19], s63 offen offset:1024 lds\nM 56,176,228\n"
"R 170,188,28672\nv_mfma_f32_16x16x32_bf16 v[44:47], v[244:247], v[208:211], v[44:47]\ns_add_u32 s63, s56, 131072\nM 56,180,244\ns_add_u32 m0, s50, 32768\nbuffer_store_dwordx4 v[16:19], v252, s[20:23], s85 offen offset:0 sc1\n"
"M 72,176,232\nR 172,188,32768\nv_mfma_f32_16x16x32_bf16 v[48:51], v[244:247], v[210:213], v[48:51]\nM 72,180,248\nbuffer_load_dwordx4 v220, s[16:19], s63 offen lds\nM 88,176,236\n"
"R 174,188,36864\nv_mfma_f32_16x16x32_bf16 v[52:55], v[238:241], v[210:213], v[52:55]\nM 88,180,252\nG 4\nM 92,160,236\nR 176,189,24576\n"
"v_mfma_f32_16x16x32_bf16 v[56:59], v[240:243], v[210:213], v[56:59]\nM 92,164,252\nbuffer_load_dwordx4 v221, s[16:19], s63 offen offset:1024 lds\nM 76,160,232\nR 178,189,28672\nv_mfma_f32_16x16x32_bf16 v[60:63], v[242:245], v[210:213], v[60:63]\n"
"v_mov_b32 v248, 0\nM 76,164,248\nv_mov_b32 v249, 0\nM 60,160,228\nR 180,189,32768\ns_add_u32 s61, s82, 845832\n"
"s_mov_b64 exec, 0xf\nbuffer_atomic_or_x2 v[248:249], v250, s[20:23], s61 offen sc0 sc1\ns_mov_b64 exec, -1\nM 60,164,244\nM 44,160,224\nR 182,189,36864\n"
"M 44,164,240\ns_barrier\nG 4\nv_mfma_f32_16x16x32_bf16 v[96:99], v[168:171], a[0:3], 0\nR 160,190,24576\nM 96,172,16\n"
"s_mov_b32 m0, s51\nv_mfma_f32_16x16x32_bf16 v[112:115], v[168:171], a[4:7], 0\nR 162,190,28672\nM 112,172,20\nds_write_addtid_b32 v64 offset:0\nv_mfma_f32_16x16x32_bf16 v[128:131], v[168:171], a[8:11], 0\n"
"R 164,190,32768\nM 128,172,24\nds_write_addtid_b32 v65 offset:256\nv_mfma_f32_16x16x32_bf16 v[144:147], v[168:171], a[12:15], 0\nR 166,190,36864\nds_write_addtid_b32 v66 offset:512\n"
"M 144,172,28\nds_write_addtid_b32 v67 offset:768\nds_write_addtid_b32 v68 offset:1024\nG 9\nv_mfma_f32_16x16x32_bf16 v[148:151], v[176:179], a[12:15], 0\nR 168,191,24576\n"
"ds_write_addtid_b32 v69 offset:1280\nM 148,180,28\nds_write_addtid_b32 v70 offset:1536\nds_write_addtid_b32 v71 offset:1792\nv_mfma_f32_16x16x32_bf16 v[132:135], v[176:179], a[8:11], 0\nR 170,191,28672\n"
"ds_write_addtid_b32 v72 offset:2048\nM 132,180,24\nds_write_addtid_b32 v73 offset:2304\nds_write_addtid_b32 v74 offset:2560\nv_mfma_f32_16x16x32_bf16 v[116:119], v[176:179], a[4:7], 0\nR 172,191,32768\n"
"ds_write_addtid_b32 v75 offset:2816\nM 116,180,20\nds_write_addtid_b32 v76 offset:3072\nds_write_addtid_b32 v77 offset:3328\nv_mfma_f32_16x16x32_bf16 v[100:103], v[176:179], a[0:3], 0\nR 174,191,36864\n"
"ds_write_addtid_b32 v78 offset:3584\nM 100,180,16\nds_write_addtid_b32 v79 offset:3840\nds_write_addtid_b32 v80 offset:4096\nG 15\nv_mfma_f32_16x16x32_bf16 v[104:107], v[160:163], a[0:3], 0\n"
"R 176,188,40960\nds_write_addtid_b32 v81 offset:4352\nM 104,164,16\nds_write_addtid_b32 v82 offset:4608\nds_write_addtid_b32 v83 offset:4864\nv_mfma_f32_16x16x32_bf16 v[120:123], v[160:163], a[4:7], 0\n"
"R 178,188,45056\nds_write_addtid_b32 v84 offset:5120\nM 120,164,20\nds_write_addtid_b32 v85 offset:5376\nds_write_addtid_b32 v86 offset:5632\nv_mfma_f32_16x16x32_bf16 v[136:139], v[160:163], a[8:11], 0\n"
"R 180,188,49152\nds_write_addtid_b32 v87 offset:5888\nM 136,164,24\nds_write_addtid_b32 v88 offset:6144\nds_write_addtid_b32 v89 offset:6400\nv_mfma_f32_16x16x32_bf16 v[152:155], v[160:163], a[12:15], 0\n"
"R 182,188,53248\nds_write_addtid_b32 v90 offset:6656\nM 152,164,28\nds_write_addtid_b32 v91 offset:6912\nds_write_addtid_b32 v92 offset:7168\nG 15\n"
"v_mfma_f32_16x16x32_bf16 v[156:159], v[168:171], a[12:15], 0\nR 160,189,40960\nds_write_addtid_b32 v93 offset:7424\nM 156,172,28\nds_write_addtid_b32 v94 offset:7680\nds_write_addtid_b32 v95 offset:7936\n"
"v_mfma_f32_16x16x32_bf16 v[140:143], v[168:171], a[8:11], 0\nR 162,189,45056\nM 140,172,24\nv_mfma_f32_16x16x32_bf16 v[124:127], v[168:171], a[4:7], 0\nR 164,189,49152\ns_waitcnt vmcnt(4) lgkmcnt(2)\n"
"s_barrier\nM 124,172,20\ns_add_u32 s64, s68, 37750784\nds_read2st64_b32 v[64:65], v225 offset0:0 offset1:1\nds_read2st64_b32 v[66:67], v225 offset0:2 offset1:3\nv_mfma_f32_16x16x32_bf16 v[108:111], v[168:171], a[0:3], 0\n"
"R 166,189,53248\nds_read2st64_b32 v[68:69], v225 offset0:4 offset1:5\ns_waitcnt vmcnt(0)\nv_cmp_eq_u64 s[86:87], s[46:47], v[248:249]\nM 108,172,16\ns_add_u32 m0, s50, 40960\n"
"s_add_u32 s63, s56, 655360\nds_read2st64_b32 v[70:71], v225 offset0:6 offset1:7\nM 96,176,32\nR 168,190,40960\ns_and_b32 s62, s86, 0xf\ns_cmp_eq_u32 s62, 0xf\n"
"s_cbranch_scc0 Lq847\nLq848:\nds_read2st64_b32 v[72:73], v225 offset0:8 offset1:9\nM 96,180,48\ns_add_u32 s65, s68, 37865472\nds_read2st64_b32 v[74:75], v225 offset0:10 offset1:11\n"
"v_mov_b32 v226, s46\nM 112,176,36\nR 170,190,45056\nds_read2st64_b32 v[76:77], v225 offset0:12 offset1:13\nv_mov_b32 v227, s47\nM 112,180,52\n"
"s_add_u32 s61, s81, 845848\ns_lshl_b32 s62, s44, 5\ns_add_u32 s61, s61, s62\ns_waitcnt vmcnt(0)\ns_mov_b64 exec, 1\nbuffer_atomic_swap_x2 v[226:227], off, s[20:23], s61 sc1\n"
"s_mov_b64 exec, -1\nG 9\nv_add_f32 v32, v32, v64\nM 128,176,40\nR 172,190,49152\nbuffer_load_dwordx4 v220, s[16:19], s63 offen lds\n"
"v_add_f32 v33, v33, v65\nM 128,180,56\nbuffer_load_dwordx4 a[64:67], v252, s[20:23], s64 offen sc1\nG 9\nv_add_f32 v34, v34, v66\nM 144,176,44\n"
"R 174,190,53248\nv_add_f32 v35, v35, v67\nG 8\nv_add_f32 v36, v36, v68\nM 144,180,60\nds_read2st64_b32 v[78:79], v225 offset0:14 offset1:15\n"
"v_add_f32 v37, v37, v69\nM 148,160,44\nR 176,191,40960\nbuffer_load_dwordx4 v221, s[16:19], s63 offen offset:1024 lds\nG 9\nv_add_f32 v38, v38, v70\n"
"M 148,164,60\nbuffer_load_dwordx4 a[68:71], v252, s[20:23], s65 offen sc1\nv_add_f32 v39, v39, v71\nM 132,160,40\nR 178,191,45056\ns_add_u32 s67, s69, 37865472\n"
"G 8\nv_add_f32 v40, v40, v72\nM 132,164,56\ns_add_u32 s66, s69, 37750784\ns_add_u32 m0, s50, 49152\ns_add_u32 s63, s56, 196608\n"
"v_add_f32 v41, v41, v73\nM 116,160,36\nR 180,191,49152\nbuffer_load_dwordx4 v220, s[16:19], s63 offen lds\nG 8\nv_add_f32 v42, v42, v74\n"
"M 116,164,52\nbuffer_load_dwordx4 a[72:75], v252, s[20:23], s66 offen sc1\nv_add_f32 v43, v43, v75\nM 100,160,32\nR 182,191,53248\nG 7\n"
"v_add_f32 v44, v44, v76\nv_add_f32 v45, v45, v77\nM 100,164,48\nds_read2st64_b32 v[80:81], v225 offset0:16 offset1:17\nG 6\nG 5\n"
"v_add_f32 v46, v46, v78\nM 104,168,32\nR 160,184,0\nbuffer_load_dwordx4 v221, s[16:19], s63 offen offset:1024 lds\nv_add_f32 v47, v47, v79\nM 104,172,48\n"
"buffer_load_dwordx4 a[76:79], v252, s[20:23], s67 offen sc1\nds_read2st64_b32 v[82:83], v225 offset0:18 offset1:19\nM 120,168,36\nR 162,184,4096\nds_read2st64_b32 v[84:85], v225 offset0:20 offset1:21\nM 120,172,52\n"
"s_add_u32 s64, s68, 37757952\nds_read2st64_b32 v[86:87], v225 offset0:22 offset1:23\nds_read2st64_b32 v[88:89], v225 offset0:24 offset1:25\nM 136,168,40\nR 164,184,8192\nds_read2st64_b32 v[90:91], v225 offset0:26 offset1:27\n"
"M 136,172,56\ns_add_u32 s65, s68, 37872640\nds_read2st64_b32 v[92:93], v225 offset0:28 offset1:29\nG 9\nv_add_f32 v48, v48, v80\nM 152,168,44\n"
"R 166,184,12288\nds_read2st64_b32 v[94:95], v225 offset0:30 offset1:31\nv_add_f32 v49, v49, v81\nM 152,172,60\nG 9\nv_add_f32 v50, v50, v82\n"
"v_add_f32 v51, v51, v83\nM 156,176,44\nR 168,185,0\nG 8\nv_add_f32 v52, v52, v84\nv_add_f32 v53, v53, v85\n"
"M 156,180,60\nbuffer_load_dwordx4 a[80:83], v252, s[20:23], s64 offen sc1\nG 7\nv_add_f32 v54, v54, v86\nM 140,176,40\nR 170,185,4096\n"
"v_add_f32 v55, v55, v87\nG 7\nv_add_f32 v56, v56, v88\nM 140,180,56\nbuffer_load_dwordx4 a[84:87], v252, s[20:23], s65 offen sc1\nv_add_f32 v57, v57, v89\n"
"M 124,176,36\nR 172,185,8192\nG 6\nv_add_f32 v58, v58, v90\nv_add_f32 v59, v59, v91\nM 124,180,52\n"
"s_add_u32 s66, s69, 37757952\nG 5\nv_add_f32 v60, v60, v92\nv_add_f32 v61, v61, v93\nG 3\nv_add_f32 v62, v62, v94\n"
"v_add_f32 v63, v63, v95\nM 108,176,32\nR 174,185,12288\nv_mul_f32 v230, v32, v32\nv_mul_f32 v231, v33, v33\nM 108,180,48\n"
"s_add_u32 s84, s73, 37752832\ns_add_u32 s85, s73, 37867520\nv_mul_f32 v232, v34, v34\nv_mfma_f32_16x16x32_bf16 v[0:3], v[160:163], a[0:3], 0\nR 176,186,0\ns_lshr_b32 s60, s45, 3\n"
"s_lshl2_add_u32 s60, s60, s40\ns_lshl_b32 s60, s60, 8\nv_lshl_add_u32 v224, v252, 3, s60\nv_add_u32 v224, LqF-32768-LqPC, v224\ns_mov_b64 exec, 3\nglobal_load_dword v224, v224, s[98:99]\n"
"s_mov_b64 exec, -1\nv_mul_f32 v233, v35, v35\nM 0,164,16\nbuffer_load_dwordx4 a[88:91], v252, s[20:23], s66 offen sc1\nv_fma_f32 v230, v230, v215, v214\nv_mfma_f32_16x16x32_bf16 v[16:19], v[160:163], a[4:7], 0\n"
"R 178,186,4096\nv_fma_f32 v231, v231, v215, v214\nv_fma_f32 v232, v232, v215, v214\nM 16,164,20\ns_add_u32 s67, s69, 37872640\nv_fma_f32 v233, v233, v215, v214\n"
"v_mul_f32 v230, v32, v230\nv_mul_f32 v231, v33, v231\nv_mfma_f32_16x16x32_bf16 a[224:227], v[160:163], a[8:11], 0\nR 180,186,8192\nv_mul_f32 v232, v34, v232\nv_mul_f32 v233, v35, v233\n"
"v_mfma_f32_16x16x32_bf16 a[224:227], v[164:167], a[24:27], a[224:227]\nbuffer_load_dwordx4 a[92:95], v252, s[20:23], s67 offen sc1\nv_exp_f32 v230, v230\nv_mfma_f32_16x16x32_bf16 a[240:243], v[160:163], a[12:15], 0\nR 182,186,12288\nv_fma_f32 v230, v230, v216, v216\n"
"v_exp_f32 v231, v231\nv_mfma_f32_16x16x32_bf16 a[240:243], v[164:167], a[28:31], a[240:243]\ns_add_u32 s63, s56, 720896\nG 4\nv_fma_f32 v231, v231, v216, v216\nv_exp_f32 v232, v232\n"
"v_mfma_f32_16x16x32_bf16 a[244:247], v[168:171], a[12:15], 0\nR 160,187,0\ns_add_u32 m0, s50, 57344\nv_exp_f32 v233, v233\nv_mfma_f32_16x16x32_bf16 a[244:247], v[172:175], a[28:31], a[244:247]\ns_add_u32 s64, s68, 37751808\n"
"v_rcp_f32 v230, v230\nv_fma_f32 v232, v232, v216, v216\nv_mfma_f32_16x16x32_bf16 a[228:231], v[168:171], a[8:11], 0\nR 162,187,4096\nv_mov_b32 v248, 0\nv_fma_f32 v233, v233, v216, v216\n"
"v_mfma_f32_16x16x32_bf16 a[228:231], v[172:175], a[24:27], a[228:231]\nbuffer_load_dwordx4 a[96:99], v252, s[20:23], s64 offen sc1\nv_mov_b32 v249, 0\nv_rcp_f32 v231, v231\nv_mfma_f32_16x16x32_bf16 v[20:23], v[168:171], a[4:7], 0\nR 164,187,8192\n"
"s_add_u32 s61, s82, 845848\ns_mov_b64 exec, 0xf\nbuffer_atomic_or_x2 v[248:249], v250, s[20:23], s61 offen sc0 sc1\ns_mov_b64 exec, -1\nv_rcp_f32 v232, v232\nM 20,172,20\n"
"s_add_u32 s65, s68, 37866496\nbuffer_load_dwordx4 v220, s[16:19], s63 offen lds\nv_rcp_f32 v233, v233\nv_mfma_f32_16x16x32_bf16 v[4:7], v[168:171], a[0:3], 0\nR 166,187,12288\nv_mul_f32 v230, v32, v230\n"
"v_mul_f32 v231, v33, v231\nM 4,172,16\nG 4\nv_mul_f32 v232, v34, v232\nv_mul_f32 v233, v35, v233\nv_cvt_pk_bf16_f32 v32, v230, v231\n"
"v_mfma_f32_16x16x32_bf16 v[8:11], v[176:179], a[0:3], 0\nR 168,184,16384\nv_cvt_pk_bf16_f32 v33, v232, v233\nv_mul_f32 v234, v36, v36\nM 8,180,16\nbuffer_load_dwordx4 a[100:103], v252, s[20:23], s65 offen sc1\n"
"buffer_load_dwordx4 v221, s[16:19], s63 offen offset:1024 lds\nv_mul_f32 v235, v37, v37\nv_mfma_f32_16x16x32_bf16 v[24:27], v[176:179], a[4:7], 0\nR 170,184,20480\ns_add_u32 s67, s69, 37866496\nv_mul_f32 v236, v38, v38\n"
"M 24,180,20\ns_add_u32 s66, s69, 37751808\ns_add_u32 s63, s56, 393216\nv_mul_f32 v237, v39, v39\nv_fma_f32 v234, v234, v215, v214\nv_mfma_f32_16x16x32_bf16 a[232:235], v[176:179], a[8:11], 0\n"
"R 172,184,24576\ns_add_u32 m0, s50, 65536\nv_fma_f32 v235, v235, v215, v214\nv_mfma_f32_16x16x32_bf16 a[232:235], v[180:183], a[24:27], a[232:235]\nbuffer_load_dwordx4 a[104:107], v252, s[20:23], s66 offen sc1\nbuffer_load_dwordx4 v220, s[16:19], s63 offen lds\n"
"v_fma_f32 v236, v236, v215, v214\nv_mfma_f32_16x16x32_bf16 a[248:251], v[176:179], a[12:15], 0\nR 174,184,28672\nv_fma_f32 v237, v237, v215, v214\nv_mul_f32 v234, v36, v234\nv_mfma_f32_16x16x32_bf16 a[248:251], v[180:183], a[28:31], a[248:251]\n"
"G 4\nv_exp_f32 v234, v234\nv_mul_f32 v235, v37, v235\nv_mfma_f32_16x16x32_bf16 a[252:255], v[160:163], a[12:15], 0\nR 176,185,16384\nv_mul_f32 v236, v38, v236\n"
"v_mul_f32 v237, v39, v237\nv_mfma_f32_16x16x32_bf16 a[252:255], v[164:167], a[28:31], a[252:255]\nbuffer_load_dwordx4 a[108:111], v252, s[20:23], s67 offen sc1\nbuffer_load_dwordx4 v221, s[16:19], s63 offen offset:1024 lds\nv_exp_f32 v235, v235\nv_mfma_f32_16x16x32_bf16 a[236:239], v[160:163], a[8:11], 0\n"
"R 178,185,20480\ns_add_u32 m0, s50, 73728\nv_exp_f32 v236, v236\nv_mfma_f32_16x16x32_bf16 a[236:239], v[164:167], a[24:27], a[236:239]\ns_add_u32 s64, s68, 37758976\nv_exp_f32 v237, v237\n"
"v_fma_f32 v234, v234, v216, v216\nv_mfma_f32_16x16x32_bf16 v[28:31], v[160:163], a[4:7], 0\nR 180,185,24576\ns_add_u32 s63, s56, 458752\nv_fma_f32 v235, v235, v216, v216\nM 28,164,20\n"
"s_add_u32 s65, s68, 37873664\nbuffer_load_dwordx4 v220, s[16:19], s63 offen lds\nv_fma_f32 v236, v236, v216, v216\nv_mfma_f32_16x16x32_bf16 v[12:15], v[160:163], a[0:3], 0\nR 182,185,28672\nv_fma_f32 v237, v237, v216, v216\n"
"v_rcp_f32 v234, v234\nM 12,164,16\ns_waitcnt vmcnt(16)\ns_barrier\nG 4\nv_rcp_f32 v235, v235\n"
"M 0,168,32\nR 160,186,16384\nv_mul_f32 v234, v36, v234\nv_rcp_f32 v236, v236\nM 0,172,48\nbuffer_load_dwordx4 a[112:115], v252, s[20:23], s64 offen sc1\n"
"buffer_load_dwordx4 v221, s[16:19], s63 offen offset:1024 lds\nv_rcp_f32 v237, v237\nM 16,168,36\nR 162,186,20480\nv_mul_f32 v235, v37, v235\nv_mul_f32 v236, v38, v236\n"
"M 16,172,52\nbuffer_load_dwordx4 a[116:119], v252, s[20:23], s65 offen sc1\nv_mul_f32 v237, v39, v237\nv_mfma_f32_16x16x32_bf16 a[224:227], v[168:171], a[40:43], a[224:227]\nR 164,186,24576\nv_cvt_pk_bf16_f32 v34, v234, v235\n"
"v_cvt_pk_bf16_f32 v35, v236, v237\nv_mfma_f32_16x16x32_bf16 a[224:227], v[172:175], a[56:59], a[224:227]\ns_add_u32 s66, s69, 37758976\nv_mul_f32 v230, v40, v40\nv_mul_f32 v231, v41, v41\nv_mul_f32 v232, v42, v42\n"
"v_mfma_f32_16x16x32_bf16 a[240:243], v[168:171], a[44:47], a[240:243]\nR 166,186,28672\nv_mul_f32 v233, v43, v43\nv_fma_f32 v230, v230, v215, v214\nv_mfma_f32_16x16x32_bf16 a[240:243], v[172:175], a[60:63], a[240:243]\nG 4\n"
"v_fma_f32 v231, v231, v215, v214\nv_fma_f32 v232, v232, v215, v214\nv_fma_f32 v233, v233, v215, v214\nv_mfma_f32_16x16x32_bf16 a[244:247], v[176:179], a[44:47], a[244:247]\nR 168,187,16384\nv_mul_f32 v230, v40, v230\n"
"v_mul_f32 v231, v41, v231\nv_mfma_f32_16x16x32_bf16 a[244:247], v[180:183], a[60:63], a[244:247]\nbuffer_load_dwordx4 a[120:123], v252, s[20:23], s66 offen sc1\nv_mul_f32 v232, v42, v232\nv_mfma_f32_16x16x32_bf16 a[228:231], v[176:179], a[40:43], a[228:231]\nR 170,187,20480\n"
"v_mul_f32 v233, v43, v233\nv_exp_f32 v230, v230\nv_mfma_f32_16x16x32_bf16 a[228:231], v[180:183], a[56:59], a[228:231]\ns_add_u32 s67, s69, 37873664\nv_exp_f32 v231, v231\nv_exp_f32 v232, v232\n"
"M 20,176,36\nR 172,187,24576\nv_fma_f32 v230, v230, v216, v216\nv_exp_f32 v233, v233\nM 20,180,52\nbuffer_load_dwordx4 a[124:127], v252, s[20:23], s67 offen sc1\n"
"v_fma_f32 v231, v231, v216, v216\nM 4,176,32\nR 174,187,28672\ns_waitcnt vmcnt(13)\nv_cmp_eq_u64 s[86:87], s[46:47], v[248:249]\nv_fma_f32 v232, v232, v216, v216\n"
"M 4,180,48\ns_add_u32 s63, s57, 131072\nG 4\nv_fma_f32 v233, v233, v216, v216\nv_rcp_f32 v230, v230\nM 8,160,32\n"
"R 176,184,32768\ns_and_b32 s62, s86, 0xf\ns_cmp_eq_u32 s62, 0xf\ns_cbranch_scc0 Lq882\nLq883:\nv_rcp_f32 v231, v231\n"
"M 8,164,48\ns_add_u32 s64, s68, 37754880\nv_rcp_f32 v232, v232\nv_rcp_f32 v233, v233\nM 24,160,36\nR 178,184,36864\n"
"s_add_u32 m0, s50, 81920\nv_mul_f32 v230, v40, v230\nM 24,164,52\nbuffer_load_dwordx4 a[128:131], v252, s[20:23], s64 offen sc1\nbuffer_load_dwordx4 v220, s[16:19], s63 offen lds\nv_mul_f32 v231, v41, v231\n"
"v_mfma_f32_16x16x32_bf16 a[232:235], v[160:163], a[40:43], a[232:235]\nR 180,184,40960\ns_add_u32 s66, s69, 37754880\nv_mul_f32 v232, v42, v232\nv_mfma_f32_16x16x32_bf16 a[232:235], v[164:167], a[56:59], a[232:235]\ns_add_u32 s65, s68, 37869568\n"
"v_mul_f32 v233, v43, v233\nv_cvt_pk_bf16_f32 v40, v230, v231\nv_cvt_pk_bf16_f32 v41, v232, v233\nv_mfma_f32_16x16x32_bf16 a[248:251], v[160:163], a[44:47], a[248:251]\nR 182,184,45056\nv_mul_f32 v234, v44, v44\n"
"v_mul_f32 v235, v45, v45\nv_mfma_f32_16x16x32_bf16 a[248:251], v[164:167], a[60:63], a[248:251]\nbuffer_load_dwordx4 v221, s[16:19], s63 offen offset:1024 lds\nG 4\nv_mul_f32 v236, v46, v46\nv_mfma_f32_16x16x32_bf16 a[252:255], v[168:171], a[44:47], a[252:255]\n"
"R 160,185,32768\ns_add_u32 m0, s50, 90112\nv_mul_f32 v237, v47, v47\nv_mfma_f32_16x16x32_bf16 a[252:255], v[172:175], a[60:63], a[252:255]\nbuffer_load_dwordx4 a[132:135], v252, s[20:23], s65 offen sc1\nv_fma_f32 v234, v234, v215, v214\n"
"v_mfma_f32_16x16x32_bf16 a[236:239], v[168:171], a[40:43], a[236:239]\nR 162,185,36864\ns_add_u32 s63, s57, 655360\nv_fma_f32 v235, v235, v215, v214\nv_mfma_f32_16x16x32_bf16 a[236:239], v[172:175], a[56:59], a[236:239]\nbuffer_load_dwordx4 a[136:139], v252, s[20:23], s66 offen sc1\n"
"buffer_load_dwordx4 v220, s[16:19], s63 offen lds\nv_fma_f32 v236, v236, v215, v214\nM 28,168,36\nR 164,185,40960\ns_add_u32 s64, s68, 37762048\nv_fma_f32 v237, v237, v215, v214\n"
"M 28,172,52\ns_add_u32 s67, s69, 37869568\nv_mul_f32 v234, v44, v234\nv_mul_f32 v235, v45, v235\nv_mul_f32 v236, v46, v236\nM 12,168,32\n"
"R 166,185,45056\nv_mul_f32 v237, v47, v237\nv_exp_f32 v234, v234\nM 12,172,48\nbuffer_load_dwordx4 v221, s[16:19], s63 offen offset:1024 lds\nG 4\n"
"v_exp_f32 v235, v235\nM 96,176,64\nR 168,186,32768\ns_add_u32 m0, s50, 98304\nv_exp_f32 v236, v236\nM 96,180,80\n"
"buffer_load_dwordx4 a[140:143], v252, s[20:23], s67 offen sc1\nv_exp_f32 v237, v237\nM 112,176,68\nR 170,186,36864\ns_add_u32 s63, s57, 196608\nv_fma_f32 v234, v234, v216, v216\n"
"M 112,180,84\nbuffer_load_dwordx4 a[144:147], v252, s[20:23], s64 offen sc1\nbuffer_load_dwordx4 v220, s[16:19], s63 offen lds\nv_fma_f32 v235, v235, v216, v216\nM 128,176,72\nR 172,186,40960\n"
"s_add_u32 s66, s69, 37762048\nv_fma_f32 v236, v236, v216, v216\nM 128,180,88\ns_add_u32 s65, s68, 37876736\nv_rcp_f32 v234, v234\nv_fma_f32 v237, v237, v216, v216\n"
"M 144,176,76\nR 174,186,45056\nv_mul_f32 v234, v44, v234\nv_rcp_f32 v235, v235\nM 144,180,92\nbuffer_load_dwordx4 v221, s[16:19], s63 offen offset:1024 lds\n"
"G 4\nv_rcp_f32 v236, v236\nM 148,160,76\nR 176,187,32768\ns_add_u32 m0, s50, 106496\nv_rcp_f32 v237, v237\n"
"M 148,164,92\nbuffer_load_dwordx4 a[148:151], v252, s[20:23], s65 offen sc1\nv_mul_f32 v235, v45, v235\nM 132,160,72\nR 178,187,36864\ns_add_u32 s63, s57, 720896\n"
"v_mul_f32 v236, v46, v236\nM 132,164,88\nbuffer_load_dwordx4 a[152:155], v252, s[20:23], s66 offen sc1\nbuffer_load_dwordx4 v220, s[16:19], s63 offen lds\nv_mul_f32 v237, v47, v237\nM 116,160,68\n"
"R 180,187,40960\nv_cvt_pk_bf16_f32 v42, v234, v235\nv_cvt_pk_bf16_f32 v43, v236, v237\nM 116,164,84\ns_add_u32 s67, s69, 37876736\nv_mul_f32 v230, v48, v48\n"
"v_mul_f32 v231, v49, v49\nv_mul_f32 v232, v50, v50\nM 100,160,64\nR 182,187,45056\nv_mul_f32 v233, v51, v51\nv_fma_f32 v230, v230, v218, v217\n"
"M 100,164,80\nbuffer_load_dwordx4 v221, s[16:19], s63 offen offset:1024 lds\nG 4\nv_fma_f32 v231, v231, v218, v217\nM 104,168,64\nR 160,184,49152\n"
"s_waitcnt vmcnt(18)\ns_barrier\nv_fma_f32 v232, v232, v218, v217\nM 104,172,80\nbuffer_load_dwordx4 a[156:159], v252, s[20:23], s67 offen sc1\nv_fma_f32 v233, v233, v218, v217\n"
"M 120,168,68\nR 162,184,53248\nv_mul_f32 v230, v48, v230\nv_mul_f32 v231, v49, v231\nM 120,172,84\nv_exp_f32 v230, v230\n"
"v_mul_f32 v232, v50, v232\nM 136,168,72\nR 164,184,57344\nv_mul_f32 v233, v51, v233\nv_exp_f32 v231, v231\nM 136,172,88\n"
"buffer_store_dwordx4 v[32:35], v252, s[20:23], s84 offen offset:0 sc1\nv_exp_f32 v232, v232\nM 152,168,76\nR 166,184,61440\nv_fma_f32 v230, v230, v219, v219\nv_exp_f32 v233, v233\n"
"M 152,172,92\nG 4\nv_rcp_f32 v230, v230\nv_fma_f32 v231, v231, v219, v219\nM 156,176,76\nR 168,185,49152\n"
"v_fma_f32 v232, v232, v219, v219\nv_fma_f32 v233, v233, v219, v219\nM 156,180,92\ns_add_u32 m0, s50, 114688\nv_rcp_f32 v231, v231\nv_rcp_f32 v232, v232\n"
"M 140,176,72\nR 170,185,53248\nv_mul_f32 v230, v48, v230\nv_rcp_f32 v233, v233\nM 140,180,88\ns_add_u32 s63, s57, 393216\n"
"v_mul_f32 v231, v49, v231\nv_mul_f32 v232, v50, v232\nv_mul_f32 v233, v51, v233\nM 124,176,68\nR 172,185,57344\nv_cvt_pk_bf16_f32 v48, v230, v231\n"
"v_cvt_pk_bf16_f32 v49, v232, v233\nM 124,180,84\nbuffer_load_dwordx4 v220, s[16:19], s63 offen lds\nv_mul_f32 v234, v52, v52\nM 108,176,64\nR 174,185,61440\n"
"v_mul_f32 v235, v53, v53\nv_mul_f32 v236, v54, v54\nM 108,180,80\nG 4\nv_mul_f32 v237, v55, v55\nv_fma_f32 v234, v234, v218, v217\n"
"v_fma_f32 v235, v235, v218, v217\nM 96,160,96\nR 176,186,49152\nv_fma_f32 v236, v236, v218, v217\nv_fma_f32 v237, v237, v218, v217\nM 96,164,112\n"
"buffer_load_dwordx4 v221, s[16:19], s63 offen offset:1024 lds\nv_mul_f32 v234, v52, v234\nM 112,160,100\nR 178,186,53248\ns_add_u32 s63, s57, 458752\nv_mul_f32 v235, v53, v235\n"
"s_waitcnt vmcnt(21)\nM 112,164,116\ns_add_u32 m0, s50, 0\nv_exp_f32 v234, v234\nv_mul_f32 v236, v54, v236\nM 128,160,104\n"
"R 180,186,57344\nv_mul_f32 v237, v55, v237\nv_exp_f32 v235, v235\ns_waitcnt vmcnt(20)\nM 128,164,120\nbuffer_load_dwordx4 v220, s[16:19], s63 offen lds\n"
"v_exp_f32 v236, v236\nM 144,160,108\nR 182,186,61440\nv_fma_f32 v234, v234, v219, v219\nv_exp_f32 v237, v237\ns_waitcnt vmcnt(20)\n"
"M 144,164,124\nG 4\nv_rcp_f32 v234, v234\nv_fma_f32 v235, v235, v219, v219\nM 148,168,108\nR 160,187,49152\n"
"v_fma_f32 v236, v236, v219, v219\nv_fma_f32 v237, v237, v219, v219\nM 148,172,124\nv_rcp_f32 v235, v235\nv_rcp_f32 v236, v236\nM 132,168,104\n"
"R 162,187,53248\nv_mul_f32 v234, v52, v234\nv_rcp_f32 v237, v237\nM 132,172,120\nbuffer_load_dwordx4 v221, s[16:19], s63 offen offset:1024 lds\nM 116,168,100\n"
"R 164,187,57344\ns_add_u32 s63, s58, 0\nM 116,172,116\ns_add_u32 m0, s50, 8192\nv_mul_f32 v235, v53, v235\nv_mul_f32 v236, v54, v236\n"
"M 100,168,96\nR 166,187,61440\nv_mul_f32 v237, v55, v237\nM 100,172,112\nG 4\nv_cvt_pk_bf16_f32 v50, v234, v235\n"
"v_cvt_pk_bf16_f32 v51, v236, v237\nM 104,176,96\nR 168,188,0\nv_mul_f32 v230, v56, v56\nM 104,180,112\nbuffer_load_dwordx4 v220, s[16:19], s63 offen lds\n"
"M 120,176,100\nR 170,188,4096\nv_mul_f32 v231, v57, v57\nM 120,180,116\nv_mul_f32 v232, v58, v58\nv_mul_f32 v233, v59, v59\n"
"M 136,176,104\nR 172,188,8192\nv_fma_f32 v230, v230, v218, v217\nM 136,180,120\nbuffer_load_dwordx4 v221, s[16:19], s63 offen offset:1024 lds\nM 152,176,108\n"
"R 174,188,12288\ns_add_u32 m0, s50, 16384\nM 152,180,124\nG 4\nv_fma_f32 v231, v231, v218, v217\nv_fma_f32 v232, v232, v218, v217\n"
"M 156,160,108\nR 176,189,0\ns_add_u32 s63, s58, 524288\nM 156,164,124\nbuffer_load_dwordx4 v220, s[16:19], s63 offen lds\nM 140,160,104\n"
"R 178,189,4096\nv_fma_f32 v233, v233, v218, v217\nM 140,164,120\nv_mul_f32 v230, v56, v230\nv_mul_f32 v231, v57, v231\nM 124,160,100\n"
"R 180,189,8192\nv_mul_f32 v232, v58, v232\nM 124,164,116\nbuffer_load_dwordx4 v221, s[16:19], s63 offen offset:1024 lds\nM 108,160,96\nR 182,189,12288\n"
"s_add_u32 s63, s58, 65536\nM 108,164,112\ns_waitcnt vmcnt(10)\ns_barrier\nG 4\nM 96,168,128\n"
"R 160,190,0\nv_mul_f32 v233, v59, v233\nM 96,172,144\ns_add_u32 m0, s50, 24576\nv_exp_f32 v230, v230\nM 112,168,132\n"
"R 162,190,4096\nv_fma_f32 v230, v230, v219, v219\nM 112,172,148\nbuffer_load_dwordx4 v220, s[16:19], s63 offen lds\nM 128,168,136\nR 164,190,8192\n"
"v_mul_f32 v234, v60, v60\nM 128,172,152\nv_exp_f32 v231, v231\nM 144,168,140\nR 166,190,12288\nv_fma_f32 v231, v231, v219, v219\n"
"s_waitcnt vmcnt(10)\nM 144,172,156\nG 4\nv_exp_f32 v232, v232\nM 148,176,140\nR 168,191,0\n"
"v_fma_f32 v232, v232, v219, v219\nM 148,180,156\nbuffer_load_dwordx4 v221, s[16:19], s63 offen offset:1024 lds\nM 132,176,136\nR 170,191,4096\nv_mul_f32 v235, v61, v61\n"
"M 132,180,152\ns_add_u32 m0, s50, 32768\nv_exp_f32 v233, v233\nM 116,176,132\nR 172,191,8192\nv_fma_f32 v233, v233, v219, v219\n"
"M 116,180,148\ns_add_u32 s63, s58, 589824\nv_rcp_f32 v230, v230\nM 100,176,128\nR 174,191,12288\nv_mul_f32 v230, v56, v230\n"
"M 100,180,144\nG 4\nv_rcp_f32 v231, v231\nM 104,160,128\nR 176,188,16384\nv_mul_f32 v231, v57, v231\n"
"M 104,164,144\nbuffer_load_dwordx4 v220, s[16:19], s63 offen lds\nM 120,160,132\nR 178,188,20480\nv_cvt_pk_bf16_f32 v56, v230, v231\nM 120,164,148\n"
"v_rcp_f32 v232, v232\nM 136,160,136\nR 180,188,24576\nv_mul_f32 v232, v58, v232\nM 136,164,152\nbuffer_store_dwordx4 v[40:43], v252, s[20:23], s84 offen offset:1024 sc1\n"
"M 152,160,140\nR 182,188,28672\nv_mul_f32 v236, v62, v62\nM 152,164,156\nG 4\nv_rcp_f32 v233, v233\n"
"M 156,168,140\nR 160,189,16384\nv_mul_f32 v233, v59, v233\nM 156,172,156\nbuffer_load_dwordx4 v221, s[16:19], s63 offen offset:1024 lds\nM 140,168,136\n"
"R 162,189,20480\nv_cvt_pk_bf16_f32 v57, v232, v233\nM 140,172,152\ns_add_u32 m0, s50, 40960\nv_mul_f32 v237, v63, v63\nv_fma_f32 v234, v234, v218, v217\n"
"M 124,168,132\nR 164,189,24576\nv_fma_f32 v235, v235, v218, v217\nM 124,172,148\ns_add_u32 s63, s58, 131072\nv_fma_f32 v236, v236, v218, v217\n"
"v_fma_f32 v237, v237, v218, v217\nM 108,168,128\nR 166,189,28672\nv_mul_f32 v234, v60, v234\nM 108,172,144\nG 4\n"
"v_exp_f32 v234, v234\nM 0,176,64\nR 168,190,16384\nv_mul_f32 v235, v61, v235\nM 0,180,80\nbuffer_load_dwordx4 v220, s[16:19], s63 offen lds\n"
"M 16,176,68\nR 170,190,20480\nv_mul_f32 v236, v62, v236\nM 16,180,84\nv_exp_f32 v235, v235\nv_mfma_f32_16x16x32_bf16 a[224:227], v[176:179], a[72:75], a[224:227]\n"
"R 172,190,24576\nv_mul_f32 v237, v63, v237\nv_mfma_f32_16x16x32_bf16 a[224:227], v[180:183], a[88:91], a[224:227]\nbuffer_store_dwordx4 v[48:51], v252, s[20:23], s85 offen offset:0 sc1\nv_mfma_f32_16x16x32_bf16 a[240:243], v[176:179], a[76:79], a[240:243]\nR 174,190,28672\n"
"v_fma_f32 v234, v234, v219, v219\nv_mfma_f32_16x16x32_bf16 a[240:243], v[180:183], a[92:95], a[240:243]\nG 4\nv_exp_f32 v236, v236\nv_mfma_f32_16x16x32_bf16 a[244:247], v[160:163], a[76:79], a[244:247]\nR 176,191,16384\n"
"v_fma_f32 v235, v235, v219, v219\nv_mfma_f32_16x16x32_bf16 a[244:247], v[164:167], a[92:95], a[244:247]\nbuffer_load_dwordx4 v221, s[16:19], s63 offen offset:1024 lds\nv_mfma_f32_16x16x32_bf16 a[228:231], v[160:163], a[72:75], a[228:231]\nR 178,191,20480\nv_fma_f32 v236, v236, v219, v219\n"
"v_mfma_f32_16x16x32_bf16 a[228:231], v[164:167], a[88:91], a[228:231]\ns_add_u32 m0, s50, 49152\nv_exp_f32 v237, v237\nM 20,160,68\nR 180,191,24576\nv_fma_f32 v237, v237, v219, v219\n"
"M 20,164,84\ns_add_u32 s63, s58, 655360\nv_rcp_f32 v234, v234\nM 4,160,64\nR 182,191,28672\nv_mul_f32 v234, v60, v234\n"
"M 4,164,80\nG 4\nv_rcp_f32 v235, v235\nM 8,168,64\nR 160,188,32768\nv_mul_f32 v235, v61, v235\n"
"M 8,172,80\nbuffer_load_dwordx4 v220, s[16:19], s63 offen lds\nM 24,168,68\nR 162,188,36864\nv_cvt_pk_bf16_f32 v58, v234, v235\nM 24,172,84\n"
"v_rcp_f32 v236, v236\nv_mfma_f32_16x16x32_bf16 a[232:235], v[168:171], a[72:75], a[232:235]\nR 164,188,40960\nv_mul_f32 v236, v62, v236\nv_mfma_f32_16x16x32_bf16 a[232:235], v[172:175], a[88:91], a[232:235]\nbuffer_load_dwordx4 v221, s[16:19], s63 offen offset:1024 lds\n"
"v_mfma_f32_16x16x32_bf16 a[248:251], v[168:171], a[76:79], a[248:251]\nR 166,188,45056\ns_add_u32 m0, s50, 57344\nv_mfma_f32_16x16x32_bf16 a[248:251], v[172:175], a[92:95], a[248:251]\nG 4\nv_rcp_f32 v237, v237\n"
"v_mfma_f32_16x16x32_bf16 a[252:255], v[176:179], a[76:79], a[252:255]\nR 168,189,32768\ns_add_u32 s63, s58, 196608\nv_mfma_f32_16x16x32_bf16 a[252:255], v[180:183], a[92:95], a[252:255]\nbuffer_load_dwordx4 v220, s[16:19], s63 offen lds\nv_mfma_f32_16x16x32_bf16 a[236:239], v[176:179], a[72:75], a[236:239]\n"
"R 170,189,36864\nv_mul_f32 v237, v63, v237\nv_mfma_f32_16x16x32_bf16 a[236:239], v[180:183], a[88:91], a[236:239]\nv_cvt_pk_bf16_f32 v59, v236, v237\nbuffer_store_dwordx4 v[56:59], v252, s[20:23], s85 offen offset:1024 sc1\nM 28,176,68\n"
"R 172,189,40960\nM 28,180,84\nbuffer_load_dwordx4 v221, s[16:19], s63 offen offset:1024 lds\nM 12,176,64\nR 174,189,45056\nM 12,180,80\n"
"s_waitcnt vmcnt(13)\ns_barrier\nG 4\nM 0,160,96\nR 176,190,32768\nM 0,164,112\n"
"M 16,160,100\nR 178,190,36864\nM 16,164,116\nv_mfma_f32_16x16x32_bf16 a[224:227], v[160:163], a[104:107], a[224:227]\nR 180,190,40960\nv_mfma_f32_16x16x32_bf16 a[224:227], v[164:167], a[120:123], a[224:227]\n"
"v_mfma_f32_16x16x32_bf16 a[240:243], v[160:163], a[108:111], a[240:243]\nR 182,190,45056\nv_mfma_f32_16x16x32_bf16 a[240:243], v[164:167], a[124:127], a[240:243]\nG 4\nv_mfma_f32_16x16x32_bf16 a[244:247], v[168:171], a[108:111], a[244:247]\nR 160,191,32768\n"
"v_mfma_f32_16x16x32_bf16 a[244:247], v[172:175], a[124:127], a[244:247]\nv_mfma_f32_16x16x32_bf16 a[228:231], v[168:171], a[104:107], a[228:231]\nR 162,191,36864\nv_mfma_f32_16x16x32_bf16 a[228:231], v[172:175], a[120:123], a[228:231]\nM 20,168,100\nR 164,191,40960\n"
"M 20,172,116\nM 4,168,96\nR 166,191,45056\nM 4,172,112\nG 4\nM 8,176,96\n"
"R 168,188,49152\nM 8,180,112\nM 24,176,100\nR 170,188,53248\nM 24,180,116\nv_mfma_f32_16x16x32_bf16 a[232:235], v[176:179], a[104:107], a[232:235]\n"
"R 172,184,0\nv_mfma_f32_16x16x32_bf16 a[232:235], v[180:183], a[120:123], a[232:235]\nv_mfma_f32_16x16x32_bf16 a[248:251], v[176:179], a[108:111], a[248:251]\nR 174,184,4096\nv_mfma_f32_16x16x32_bf16 a[248:251], v[180:183], a[124:127], a[248:251]\nG 4\n"
"v_mfma_f32_16x16x32_bf16 a[252:255], v[160:163], a[108:111], a[252:255]\nR 176,189,49152\nv_mfma_f32_16x16x32_bf16 a[252:255], v[164:167], a[124:127], a[252:255]\nv_mfma_f32_16x16x32_bf16 a[236:239], v[160:163], a[104:107], a[236:239]\nR 178,189,53248\nv_mfma_f32_16x16x32_bf16 a[236:239], v[164:167], a[120:123], a[236:239]\n"
"M 28,160,100\nR 180,185,0\nM 28,164,116\nM 12,160,96\nR 182,185,4096\nM 12,164,112\n"
"G 4\nM 0,168,128\nR 160,190,49152\nM 0,172,144\nM 16,168,132\nR 162,190,53248\n"
"M 16,172,148\nv_mfma_f32_16x16x32_bf16 a[224:227], v[168:171], a[136:139], a[224:227]\nR 164,186,0\nv_mfma_f32_16x16x32_bf16 a[224:227], v[172:175], a[152:155], a[224:227]\nv_mfma_f32_16x16x32_bf16 a[240:243], v[168:171], a[140:143], a[240:243]\nR 166,186,4096\n"
"v_mfma_f32_16x16x32_bf16 a[240:243], v[172:175], a[156:159], a[240:243]\nG 4\nv_mfma_f32_16x16x32_bf16 a[244:247], v[176:179], a[140:143], a[244:247]\nR 168,191,49152\nv_mfma_f32_16x16x32_bf16 a[244:247], v[180:183], a[156:159], a[244:247]\nv_mfma_f32_16x16x32_bf16 a[228:231], v[176:179], a[136:139], a[228:231]\n"
"R 170,191,53248\nv_mov_b32 v226, s46\nv_mfma_f32_16x16x32_bf16 a[228:231], v[180:183], a[152:155], a[228:231]\ns_add_u32 m0, s50, 65536\nv_mov_b32 v227, s47\nM 20,176,132\n"
"R 172,187,0\ns_add_u32 s61, s81, 845840\ns_lshl_b32 s62, s44, 5\ns_add_u32 s61, s61, s62\ns_waitcnt vmcnt(0)\ns_mov_b64 exec, 1\n"
"buffer_atomic_swap_x2 v[226:227], off, s[20:23], s61 sc1\ns_mov_b64 exec, -1\nM 20,180,148\ns_add_u32 s63, s58, 720896\nM 4,176,128\nR 174,187,4096\n"
"M 4,180,144\nG 4\nM 8,160,128\nR 176,184,8192\nM 8,164,144\nbuffer_load_dwordx4 v220, s[16:19], s63 offen lds\n"
"M 24,160,132\nR 178,184,12288\nM 24,164,148\nv_mfma_f32_16x16x32_bf16 a[232:235], v[160:163], a[136:139], a[232:235]\nR 180,184,16384\nv_mfma_f32_16x16x32_bf16 a[232:235], v[164:167], a[152:155], a[232:235]\n"
"buffer_load_dwordx4 v221, s[16:19], s63 offen offset:1024 lds\nv_mfma_f32_16x16x32_bf16 a[248:251], v[160:163], a[140:143], a[248:251]\nR 182,184,20480\ns_add_u32 m0, s50, 73728\nv_mfma_f32_16x16x32_bf16 a[248:251], v[164:167], a[156:159], a[248:251]\nG 4\n"
"v_mfma_f32_16x16x32_bf16 a[252:255], v[168:171], a[140:143], a[252:255]\nR 160,185,8192\ns_add_u32 s63, s58, 393216\nv_mfma_f32_16x16x32_bf16 a[252:255], v[172:175], a[156:159], a[252:255]\nbuffer_load_dwordx4 v220, s[16:19], s63 offen lds\nv_mfma_f32_16x16x32_bf16 a[236:239], v[168:171], a[136:139], a[236:239]\n"
"R 162,185,12288\nv_mfma_f32_16x16x32_bf16 a[236:239], v[172:175], a[152:155], a[236:239]\nM 28,168,132\nR 164,185,16384\nM 28,172,148\nbuffer_load_dwordx4 v221, s[16:19], s63 offen offset:1024 lds\n"
"M 12,168,128\nR 166,185,20480\ns_add_u32 s63, s58, 458752\nM 12,172,144\ns_barrier\nG 4\n"
"v_mfma_f32_16x16x32_bf16 v[32:35], v[176:179], a[0:3], 0\nR 168,186,8192\nM 32,180,16\ns_add_u32 m0, s50, 81920\nv_mfma_f32_16x16x32_bf16 v[48:51], v[176:179], a[4:7], 0\nR 170,186,12288\n"
"M 48,180,20\nbuffer_load_dwordx4 v220, s[16:19], s63 offen lds\nv_mfma_f32_16x16x32_bf16 v[64:67], v[176:179], a[8:11], 0\nR 172,186,16384\nM 64,180,24\nv_mfma_f32_16x16x32_bf16 v[80:83], v[176:179], a[12:15], 0\n"
"R 174,186,20480\nM 80,180,28\nG 4\nv_mfma_f32_16x16x32_bf16 v[84:87], v[160:163], a[12:15], 0\nR 176,187,8192\nM 84,164,28\n"
"buffer_load_dwordx4 v221, s[16:19], s63 offen offset:1024 lds\nv_mfma_f32_16x16x32_bf16 v[68:71], v[160:163], a[8:11], 0\nR 178,187,12288\nM 68,164,24\nv_mfma_f32_16x16x32_bf16 v[52:55], v[160:163], a[4:7], 0\nR 180,187,16384\n"
"M 52,164,20\ns_add_u32 m0, s50, 90112\nv_mfma_f32_16x16x32_bf16 v[36:39], v[160:163], a[0:3], 0\nR 182,187,20480\nv_mov_b32 v248, 0\nM 36,164,16\n"
"G 4\nv_mov_b32 v249, 0\nv_mfma_f32_16x16x32_bf16 v[40:43], v[168:171], a[0:3], 0\nR 160,184,24576\ns_add_u32 s61, s82, 845840\ns_mov_b64 exec, 0xf\n"
"buffer_atomic_or_x2 v[248:249], v250, s[20:23], s61 offen sc0 sc1\ns_mov_b64 exec, -1\nM 40,172,16\ns_add_u32 s63, s56, 262144\nv_mfma_f32_16x16x32_bf16 v[56:59], v[168:171], a[4:7], 0\nR 162,184,28672\n"
"M 56,172,20\nbuffer_load_dwordx4 v220, s[16:19], s63 offen lds\nv_mfma_f32_16x16x32_bf16 v[72:75], v[168:171], a[8:11], 0\nR 164,184,32768\nM 72,172,24\nv_mfma_f32_16x16x32_bf16 v[88:91], v[168:171], a[12:15], 0\n"
"R 166,184,36864\nM 88,172,28\nG 4\nv_mfma_f32_16x16x32_bf16 v[92:95], v[176:179], a[12:15], 0\nR 168,185,24576\nM 92,180,28\n"
"buffer_load_dwordx4 v221, s[16:19], s63 offen offset:1024 lds\nv_mfma_f32_16x16x32_bf16 v[76:79], v[176:179], a[8:11], 0\nR 170,185,28672\ns_add_u32 s63, s56, 786432\nM 76,180,24\ns_add_u32 m0, s50, 98304\n"
"v_mfma_f32_16x16x32_bf16 v[60:63], v[176:179], a[4:7], 0\nR 172,185,32768\nM 60,180,20\nbuffer_load_dwordx4 v220, s[16:19], s63 offen lds\nv_mfma_f32_16x16x32_bf16 v[44:47], v[176:179], a[0:3], 0\nR 174,185,36864\n"
"M 44,180,16\nG 4\nM 32,160,32\nR 176,186,24576\nM 32,164,48\nbuffer_load_dwordx4 v221, s[16:19], s63 offen offset:1024 lds\n"
"M 48,160,36\nR 178,186,28672\ns_add_u32 s63, s56, 327680\nM 48,164,52\ns_add_u32 m0, s50, 106496\nM 64,160,40\n"
"R 180,186,32768\nM 64,164,56\nbuffer_load_dwordx4 v220, s[16:19], s63 offen lds\nM 80,160,44\nR 182,186,36864\nM 80,164,60\n"
"G 4\nM 84,168,44\nR 160,187,24576\nM 84,172,60\nbuffer_load_dwordx4 v221, s[16:19], s63 offen offset:1024 lds\nM 68,168,40\n"
"R 162,187,28672\ns_add_u32 s63, s56, 851968\nM 68,172,56\ns_add_u32 m0, s50, 114688\nM 52,168,36\nR 164,187,32768\n"
"M 52,172,52\nbuffer_load_dwordx4 v220, s[16:19], s63 offen lds\nM 36,168,32\nR 166,187,36864\nM 36,172,48\nG 4\n"
"M 40,176,32\nR 168,184,40960\nM 40,180,48\ns_add_u32 s64, s68, 37752832\nM 56,176,36\nR 170,184,45056\n"
"s_waitcnt vmcnt(7)\nv_cmp_eq_u64 s[86:87], s[46:47], v[248:249]\nM 56,180,52\nbuffer_load_dwordx4 v221, s[16:19], s63 offen offset:1024 lds\nM 72,176,40\nR 172,184,49152\n"
"s_and_b32 s62, s86, 0xf\ns_cmp_eq_u32 s62, 0xf\ns_cbranch_scc0 Lq901\nLq902:\nM 72,180,56\ns_add_u32 s65, s68, 37867520\n"
"M 88,176,44\nR 174,184,53248\ns_barrier\nM 88,180,60\nG 4\nM 92,160,44\n"
"R 176,185,40960\nM 92,164,60\nbuffer_load_dwordx4 a[160:163], v252, s[20:23], s64 offen sc1\nM 76,160,40\nR 178,185,45056\nM 76,164,56\n"
"s_add_u32 s66, s69, 37752832\nM 60,160,36\nR 180,185,49152\nM 60,164,52\nbuffer_load_dwordx4 a[164:167], v252, s[20:23], s65 offen sc1\nM 44,160,32\n"
"R 182,185,53248\nM 44,164,48\nG 4\nM 32,168,64\nR 160,186,40960\nM 32,172,80\n"
"s_add_u32 s67, s69, 37867520\nM 48,168,68\nR 162,186,45056\nM 48,172,84\nbuffer_load_dwordx4 a[168:171], v252, s[20:23], s66 offen sc1\nM 64,168,72\n"
"R 164,186,49152\nM 64,172,88\nM 80,168,76\nR 166,186,53248\nM 80,172,92\nG 4\n"
"M 84,176,76\nR 168,187,40960\nM 84,180,92\nbuffer_load_dwordx4 a[172:175], v252, s[20:23], s67 offen sc1\nM 68,176,72\nR 170,187,45056\n"
"M 68,180,88\ns_add_u32 s64, s68, 37760000\nM 52,176,68\nR 172,187,49152\nM 52,180,84\ns_add_u32 s65, s68, 37874688\n"
"M 36,176,64\nR 174,187,53248\ns_add_u32 m0, s50, 0\nM 36,180,80\nG 4\nM 40,160,64\n"
"R 176,184,57344\ns_add_u32 s63, s57, 262144\nM 40,164,80\nbuffer_load_dwordx4 a[176:179], v252, s[20:23], s64 offen sc1\nM 56,160,68\nR 178,184,61440\n"
"M 56,164,84\ns_add_u32 s66, s69, 37760000\nbuffer_load_dwordx4 v220, s[16:19], s63 offen lds\nM 72,160,72\nR 180,188,0\nM 72,164,88\n"
"buffer_load_dwordx4 a[180:183], v252, s[20:23], s65 offen sc1\nM 88,160,76\nR 182,188,4096\nM 88,164,92\nbuffer_load_dwordx4 v221, s[16:19], s63 offen offset:1024 lds\nG 4\n"
"M 92,168,76\nR 160,185,57344\ns_add_u32 m0, s50, 8192\nM 92,172,92\ns_add_u32 s67, s69, 37874688\nM 76,168,72\n"
"R 162,185,61440\ns_add_u32 s63, s57, 786432\nM 76,172,88\nbuffer_load_dwordx4 a[184:187], v252, s[20:23], s66 offen sc1\nbuffer_load_dwordx4 v220, s[16:19], s63 offen lds\nM 60,168,68\n"
"R 164,189,0\nM 60,172,84\nbuffer_load_dwordx4 v221, s[16:19], s63 offen offset:1024 lds\nM 44,168,64\nR 166,189,4096\ns_add_u32 m0, s50, 16384\n"
"M 44,172,80\nG 4\nM 32,176,96\nR 168,186,57344\ns_add_u32 s63, s57, 327680\nM 32,180,112\n"
"buffer_load_dwordx4 a[188:191], v252, s[20:23], s67 offen sc1\nM 48,176,100\nR 170,186,61440\nM 48,180,116\ns_add_u32 s64, s68, 37753856\nbuffer_load_dwordx4 v220, s[16:19], s63 offen lds\n"
"M 64,176,104\nR 172,190,0\nM 64,180,120\nbuffer_load_dwordx4 v221, s[16:19], s63 offen offset:1024 lds\nM 80,176,108\nR 174,190,4096\n"
"s_add_u32 m0, s50, 24576\nM 80,180,124\nG 4\nM 84,160,108\nR 176,187,57344\ns_waitcnt vmcnt(18)\n"
"s_barrier\nM 84,164,124\nbuffer_load_dwordx4 a[192:195], v252, s[20:23], s64 offen sc1\nM 68,160,104\nR 178,187,61440\ns_add_u32 s63, s57, 851968\n"
"M 68,164,120\ns_add_u32 s65, s68, 37868544\nM 52,160,100\nR 180,191,0\nbuffer_load_dwordx4 v220, s[16:19], s63 offen lds\nM 52,164,116\n"
"s_add_u32 s66, s69, 37753856\nM 36,160,96\nR 182,191,4096\nM 36,164,112\nG 4\nM 40,168,96\n"
"R 160,188,8192\nbuffer_load_dwordx4 v221, s[16:19], s63 offen offset:1024 lds\nM 40,172,112\nbuffer_load_dwordx4 a[196:199], v252, s[20:23], s65 offen sc1\nM 56,168,100\nR 162,188,12288\n"
"M 56,172,116\ns_add_u32 s67, s69, 37868544\nM 72,168,104\nR 164,188,16384\nM 72,172,120\nbuffer_load_dwordx4 a[200:203], v252, s[20:23], s66 offen sc1\n"
"M 88,168,108\nR 166,188,20480\nM 88,172,124\nG 4\nM 92,176,108\nR 168,189,8192\n"
"M 92,180,124\ns_add_u32 s64, s68, 37761024\nM 76,176,104\nR 170,189,12288\nM 76,180,120\nbuffer_load_dwordx4 a[204:207], v252, s[20:23], s67 offen sc1\n"
"M 60,176,100\nR 172,189,16384\nM 60,180,116\nM 44,176,96\nR 174,189,20480\nM 44,180,112\n"
"G 4\nM 32,160,128\nR 176,190,8192\nM 32,164,144\nbuffer_load_dwordx4 a[208:211], v252, s[20:23], s64 offen sc1\nM 48,160,132\n"
"R 178,190,12288\nM 48,164,148\ns_add_u32 s65, s68, 37875712\nM 64,160,136\nR 180,190,16384\nM 64,164,152\n"
"s_add_u32 s66, s69, 37761024\nM 80,160,140\nR 182,190,20480\nM 80,164,156\nG 4\nM 84,168,140\n"
"R 160,191,8192\nM 84,172,156\nbuffer_load_dwordx4 a[212:215], v252, s[20:23], s65 offen sc1\nM 68,168,136\nR 162,191,12288\nM 68,172,152\n"
"s_add_u32 s67, s69, 37875712\nM 52,168,132\nR 164,191,16384\nM 52,172,148\nbuffer_load_dwordx4 a[216:219], v252, s[20:23], s66 offen sc1\nM 36,168,128\n"
"R 166,191,20480\nM 36,172,144\nG 4\nM 40,176,128\nR 168,188,24576\nM 40,180,144\n"
"M 56,176,132\nR 170,188,28672\nM 56,180,148\nbuffer_load_dwordx4 a[220:223], v252, s[20:23], s67 offen sc1\nM 72,176,136\nR 172,188,32768\n"
"s_add_u32 s63, s58, 262144\nM 72,180,152\ns_add_u32 m0, s50, 32768\nM 88,176,140\nR 174,188,36864\nM 88,180,156\n"
"G 4\nM 92,160,140\nR 176,189,24576\nM 92,164,156\nbuffer_load_dwordx4 v220, s[16:19], s63 offen lds\nM 76,160,136\n"
"R 178,189,28672\nM 76,164,152\nM 60,160,132\nR 180,189,32768\nM 60,164,148\nbuffer_load_dwordx4 v221, s[16:19], s63 offen offset:1024 lds\n"
"M 44,160,128\nR 182,189,36864\nM 44,164,144\ns_waitcnt vmcnt(9)\ns_barrier\nG 4\n"
"M 96,168,160\nR 160,190,24576\nM 96,172,176\ns_add_u32 m0, s50, 40960\nM 112,168,164\nR 162,190,28672\n"
"M 112,172,180\ns_add_u32 s63, s58, 786432\nM 128,168,168\nR 164,190,32768\nv_lshrrev_b32 v230, 8, v252\nM 128,172,184\n"
"buffer_load_dwordx4 v220, s[16:19], s63 offen lds\nM 144,168,172\nR 166,190,36864\nv_lshlrev_b32 v230, 3, v230\nM 144,172,188\nG 4\n"
"M 148,176,172\nR 168,191,24576\nM 148,180,188\nbuffer_load_dwordx2 v[232:233], v230, s[36:39], s53 offen offset:0\nM 132,176,168\nR 170,191,28672\n"
"M 132,180,184\nbuffer_load_dwordx2 v[234:235], v230, s[36:39], s53 offen offset:32\nM 116,176,164\nR 172,191,32768\nM 116,180,180\nbuffer_load_dwordx4 v221, s[16:19], s63 offen offset:1024 lds\n"
"M 100,176,160\nR 174,191,36864\nM 100,180,176\nG 4\nM 104,160,160\nR 176,188,40960\n"
"M 104,164,176\ns_add_u32 m0, s50, 49152\nM 120,160,164\nR 178,188,45056\nM 120,164,180\ns_add_u32 s63, s58, 327680\n"
"M 136,160,168\nR 180,188,49152\nbuffer_load_dwordx2 v[236:237], v230, s[36:39], s53 offen offset:64\nM 136,164,184\nbuffer_load_dwordx4 v220, s[16:19], s63 offen lds\nM 152,160,172\n"
"R 182,188,53248\nM 152,164,188\nbuffer_load_dwordx2 v[238:239], v230, s[36:39], s53 offen offset:96\nG 4\nM 156,168,172\nR 160,189,40960\n"
"v_bfe_u32 v242, v252, 4, 4\nM 156,172,188\nv_lshrrev_b32 v243, 8, v252\nv_lshlrev_b32 v243, 2, v243\nM 140,168,168\nR 162,189,45056\n"
"v_sub_u32 v242, v242, v243\nM 140,172,184\nv_lshlrev_b32 v243, 4, v242\ns_mov_b64 s[60:61], 0x3f80\nv_lshlrev_b64 v[208:209], v243, s[60:61]\nM 124,168,164\n"
"R 164,189,49152\nM 124,172,180\nbuffer_load_dwordx4 v221, s[16:19], s63 offen offset:1024 lds\nM 108,168,160\nR 166,189,53248\nM 108,172,176\n"
"G 4\nM 96,176,192\nR 168,190,40960\ns_waitcnt vmcnt(13)\nM 96,180,208\ns_add_u32 m0, s50, 57344\n"
"M 112,176,196\nR 170,190,45056\ns_waitcnt vmcnt(12)\nM 112,180,212\ns_add_u32 s63, s58, 851968\nM 128,176,200\n"
"R 172,190,49152\ns_waitcnt vmcnt(11)\nM 128,180,216\nbuffer_load_dwordx4 v220, s[16:19], s63 offen lds\nM 144,176,204\nR 174,190,53248\n"
"s_waitcnt vmcnt(11)\nM 144,180,220\nG 4\nM 148,160,204\nR 176,191,40960\nM 148,164,220\n"
"v_cmp_gt_u32 s[60:61], 4, v242\ns_nop 1\nv_cndmask_b32 v212, 0, v208, s[60:61]\nv_cndmask_b32 v208, 0, v208, s[60:61]\nv_cndmask_b32 v213, 0, v209, s[60:61]\nv_cndmask_b32 v209, 0, v209, s[60:61]\n"
"M 132,160,200\nR 178,191,45056\nM 132,164,216\nM 116,160,196\nR 180,191,49152\nM 116,164,212\n"
"buffer_load_dwordx4 v221, s[16:19], s63 offen offset:1024 lds\nM 100,160,192\nR 182,191,53248\nM 100,164,208\nG 4\nM 104,168,192\n"
"R 160,184,0\nM 104,172,208\ns_add_u32 m0, s50, 65536\nM 120,168,196\nR 162,184,4096\nM 120,172,212\n"
"s_add_u32 s63, s59, 0\nM 136,168,200\nR 164,184,8192\nM 136,172,216\nbuffer_load_dwordx4 v220, s[16:19], s63 offen lds\nM 152,168,204\n"
"R 166,184,12288\nM 152,172,220\nG 4\nM 156,176,204\nR 168,185,0\nM 156,180,220\n"
"M 140,176,200\nR 170,185,4096\nM 140,180,216\nbuffer_load_dwordx4 v221, s[16:19], s63 offen offset:1024 lds\nM 124,176,196\nR 172,185,8192\n"
"M 124,180,212\nM 108,176,192\nR 174,185,12288\nM 108,180,208\nG 4\nM 0,160,160\n"
"R 176,186,0\nM 0,164,176\ns_mov_b32 m0, s51\nM 16,160,164\nR 178,186,4096\nv_cvt_pk_bf16_f32 v128, v128, v129\n"
"v_cvt_pk_bf16_f32 v129, v130, v131\nM 16,164,180\nds_write_addtid_b32 v128 offset:0\nv_cvt_pk_bf16_f32 v130, v132, v133\nv_cvt_pk_bf16_f32 v131, v134, v135\nv_mfma_f32_16x16x32_bf16 a[224:227], v[160:163], a[168:171], a[224:227]\n"
"R 180,186,8192\nv_cvt_pk_bf16_f32 v132, v136, v137\nv_mfma_f32_16x16x32_bf16 a[224:227], v[164:167], a[184:187], a[224:227]\nds_write_addtid_b32 v129 offset:256\nv_cvt_pk_bf16_f32 v133, v138, v139\nv_mfma_f32_16x16x32_bf16 a[240:243], v[160:163], a[172:175], a[240:243]\n"
"R 182,186,12288\nv_cvt_pk_bf16_f32 v134, v140, v141\nv_mfma_f32_16x16x32_bf16 a[240:243], v[164:167], a[188:191], a[240:243]\nds_write_addtid_b32 v130 offset:512\nG 7\nv_cvt_pk_bf16_f32 v135, v142, v143\n"
"v_mfma_f32_16x16x32_bf16 a[244:247], v[168:171], a[172:175], a[244:247]\nR 160,187,0\nv_cvt_pk_bf16_f32 v136, v144, v145\nv_mfma_f32_16x16x32_bf16 a[244:247], v[172:175], a[188:191], a[244:247]\nds_write_addtid_b32 v131 offset:768\nv_cvt_pk_bf16_f32 v137, v146, v147\n"
"v_mfma_f32_16x16x32_bf16 a[228:231], v[168:171], a[168:171], a[228:231]\nR 162,187,4096\nv_cvt_pk_bf16_f32 v138, v148, v149\nv_mfma_f32_16x16x32_bf16 a[228:231], v[172:175], a[184:187], a[228:231]\nds_write_addtid_b32 v132 offset:1024\nv_cvt_pk_bf16_f32 v139, v150, v151\n"
"M 20,168,164\nR 164,187,8192\nv_cvt_pk_bf16_f32 v140, v152, v153\nM 20,172,180\nds_write_addtid_b32 v133 offset:1280\nv_cvt_pk_bf16_f32 v141, v154, v155\n"
"M 4,168,160\nR 166,187,12288\nv_cvt_pk_bf16_f32 v142, v156, v157\nM 4,172,176\nds_write_addtid_b32 v134 offset:1536\nG 9\n"
"v_cvt_pk_bf16_f32 v143, v158, v159\nM 8,176,160\nR 168,184,16384\nds_write_addtid_b32 v135 offset:1792\nM 8,180,176\nds_write_addtid_b32 v136 offset:2048\n"
"ds_write_addtid_b32 v137 offset:2304\nM 24,176,164\nR 170,184,20480\nds_write_addtid_b32 v138 offset:2560\nM 24,180,180\nds_write_addtid_b32 v139 offset:2816\n"
"ds_write_addtid_b32 v140 offset:3072\nv_mfma_f32_16x16x32_bf16 a[232:235], v[176:179], a[168:171], a[232:235]\nR 172,184,24576\nds_write_addtid_b32 v141 offset:3328\nv_mfma_f32_16x16x32_bf16 a[232:235], v[180:183], a[184:187], a[232:235]\nds_write_addtid_b32 v142 offset:3584\n"
"ds_write_addtid_b32 v143 offset:3840\nv_mfma_f32_16x16x32_bf16 a[248:251], v[176:179], a[172:175], a[248:251]\nR 174,184,28672\ns_add_u32 m0, s50, 73728\nv_mfma_f32_16x16x32_bf16 a[248:251], v[180:183], a[188:191], a[248:251]\ns_add_u32 s63, s59, 524288\n"
"G 14\nv_mfma_f32_16x16x32_bf16 a[252:255], v[160:163], a[172:175], a[252:255]\nR 176,185,16384\ns_waitcnt vmcnt(8) lgkmcnt(2)\ns_barrier\nv_mfma_f32_16x16x32_bf16 a[252:255], v[164:167], a[188:191], a[252:255]\n"
"buffer_load_dwordx4 v220, s[16:19], s63 offen lds\nv_mfma_f32_16x16x32_bf16 a[236:239], v[160:163], a[168:171], a[236:239]\nR 178,185,20480\nv_mfma_f32_16x16x32_bf16 a[236:239], v[164:167], a[184:187], a[236:239]\nds_read2st64_b32 v[128:129], v225 offset0:0 offset1:1\nM 28,160,164\n"
"R 180,185,24576\nM 28,164,180\nbuffer_load_dwordx4 v221, s[16:19], s63 offen offset:1024 lds\nM 12,160,160\nR 182,185,28672\ns_add_u32 m0, s50, 81920\n"
"M 12,164,176\nds_read2st64_b32 v[130:131], v225 offset0:2 offset1:3\nG 6\nM 0,168,192\nR 160,186,16384\ns_add_u32 s63, s59, 65536\n"
"M 0,172,208\nbuffer_load_dwordx4 v220, s[16:19], s63 offen lds\nM 16,168,196\nR 162,186,20480\nM 16,172,212\nds_read2st64_b32 v[132:133], v225 offset0:4 offset1:5\n"
"v_mfma_f32_16x16x32_bf16 a[224:227], v[168:171], a[200:203], a[224:227]\nR 164,186,24576\nv_mfma_f32_16x16x32_bf16 a[224:227], v[172:175], a[216:219], a[224:227]\nbuffer_load_dwordx4 v221, s[16:19], s63 offen offset:1024 lds\nG 4\nv_mfma_f32_16x16x32_bf16 v[96:99], v[208:211], v[128:131], v[96:99]\n"
"v_mfma_f32_16x16x32_bf16 v[100:103], v[210:213], v[128:131], v[100:103]\nv_mfma_f32_16x16x32_bf16 a[240:243], v[168:171], a[204:207], a[240:243]\nR 166,186,28672\ns_add_u32 m0, s50, 90112\nv_mfma_f32_16x16x32_bf16 a[240:243], v[172:175], a[220:223], a[240:243]\nds_read2st64_b32 v[134:135], v225 offset0:6 offset1:7\n"
"v_mfma_f32_16x16x32_bf16 a[244:247], v[176:179], a[204:207], a[244:247]\nR 168,187,16384\ns_add_u32 s63, s59, 589824\nv_mfma_f32_16x16x32_bf16 a[244:247], v[180:183], a[220:223], a[244:247]\nbuffer_load_dwordx4 v220, s[16:19], s63 offen lds\nv_mfma_f32_16x16x32_bf16 a[228:231], v[176:179], a[200:203], a[228:231]\n"
"R 170,187,20480\nv_mfma_f32_16x16x32_bf16 a[228:231], v[180:183], a[216:219], a[228:231]\nds_read2st64_b32 v[136:137], v225 offset0:8 offset1:9\nM 20,176,196\nR 172,187,24576\nM 20,180,212\n"
"buffer_load_dwordx4 v221, s[16:19], s63 offen offset:1024 lds\nG 4\nv_mfma_f32_16x16x32_bf16 v[104:107], v[208:211], v[132:135], v[104:107]\nv_mfma_f32_16x16x32_bf16 v[108:111], v[210:213], v[132:135], v[108:111]\nM 4,176,192\nR 174,187,28672\n"
"s_add_u32 s63, s59, 131072\nM 4,180,208\nds_read2st64_b32 v[138:139], v225 offset0:10 offset1:11\nds_read2st64_b32 v[140:141], v225 offset0:12 offset1:13\nM 8,160,192\nR 176,184,32768\n"
"ds_read2st64_b32 v[142:143], v225 offset0:14 offset1:15\nG 3\nv_mfma_f32_16x16x32_bf16 v[112:115], v[208:211], v[136:139], v[112:115]\nv_mfma_f32_16x16x32_bf16 v[116:119], v[210:213], v[136:139], v[116:119]\nG 0\nv_mfma_f32_16x16x32_bf16 v[120:123], v[208:211], v[140:143], v[120:123]\n"
"v_mfma_f32_16x16x32_bf16 v[124:127], v[210:213], v[140:143], v[124:127]\nM 8,164,208\ns_add_u32 m0, s50, 98304\nM 24,160,196\nR 178,184,36864\nM 24,164,212\n"
"buffer_load_dwordx4 v220, s[16:19], s63 offen lds\nv_mfma_f32_16x16x32_bf16 a[232:235], v[160:163], a[200:203], a[232:235]\nR 180,184,40960\nv_cvt_pk_bf16_f32 v96, v96, v97\nv_cvt_pk_bf16_f32 v97, v98, v99\nv_mfma_f32_16x16x32_bf16 a[232:235], v[164:167], a[216:219], a[232:235]\n"
"v_cvt_pk_bf16_f32 v98, v100, v101\nv_cvt_pk_bf16_f32 v99, v102, v103\nv_cvt_pk_bf16_f32 v104, v104, v105\nv_mfma_f32_16x16x32_bf16 a[248:251], v[160:163], a[204:207], a[248:251]\nR 182,184,45056\nv_cvt_pk_bf16_f32 v105, v106, v107\n"
"v_cvt_pk_bf16_f32 v106, v108, v109\nv_mfma_f32_16x16x32_bf16 a[248:251], v[164:167], a[220:223], a[248:251]\nv_cvt_pk_bf16_f32 v107, v110, v111\nv_cvt_pk_bf16_f32 v112, v112, v113\nv_cvt_pk_bf16_f32 v113, v114, v115\nv_mfma_f32_16x16x32_bf16 a[252:255], v[168:171], a[204:207], a[252:255]\n"
"R 160,185,32768\nv_cvt_pk_bf16_f32 v114, v116, v117\nv_cvt_pk_bf16_f32 v115, v118, v119\nv_mfma_f32_16x16x32_bf16 a[252:255], v[172:175], a[220:223], a[252:255]\nbuffer_load_dwordx4 v221, s[16:19], s63 offen offset:1024 lds\nv_cvt_pk_bf16_f32 v120, v120, v121\n"
"v_mfma_f32_16x16x32_bf16 a[236:239], v[168:171], a[200:203], a[236:239]\nR 162,185,36864\ns_add_u32 s63, s59, 655360\nv_cvt_pk_bf16_f32 v121, v122, v123\nv_mfma_f32_16x16x32_bf16 a[236:239], v[172:175], a[216:219], a[236:239]\ns_add_u32 m0, s50, 106496\n"
"s_bfe_u32 s100, s56, 0x20007\ns_mul_i32 s100, s100, 49152\ns_add_u32 s100, s100, s72\ns_add_u32 s100, s100, 65011712\nv_cvt_pk_bf16_f32 v122, v124, v125\nv_cvt_pk_bf16_f32 v123, v126, v127\n"
"M 28,168,196\nR 164,185,40960\nbuffer_store_dwordx4 v[96:99], v252, s[20:23], s100 offen offset:0 sc1\nM 28,172,212\nbuffer_load_dwordx4 v220, s[16:19], s63 offen lds\nbuffer_store_dwordx4 v[104:107], v252, s[20:23], s100 offen offset:1024 sc1\n"
"M 12,168,192\nR 166,185,45056\nbuffer_store_dwordx4 v[112:115], v252, s[20:23], s100 offen offset:2048 sc1\nM 12,172,208\ns_waitcnt vmcnt(14)\ns_barrier\n"
"buffer_store_dwordx4 v[120:123], v252, s[20:23], s100 offen offset:3072 sc1\nG 4\nM 32,176,160\nR 168,186,32768\nM 32,180,176\nbuffer_load_dwordx4 v221, s[16:19], s63 offen offset:1024 lds\n"
"v_xor_b32 v240, 0x2000, v225\nM 48,176,164\nR 170,186,36864\ns_add_u32 s63, s59, 196608\nds_write2st64_b32 v240, a224, a225 offset0:0 offset1:1\nM 48,180,180\n"
"s_add_u32 m0, s50, 114688\nds_write2st64_b32 v240, a226, a227 offset0:2 offset1:3\nM 64,176,168\nR 172,186,40960\nds_write2st64_b32 v240, a228, a229 offset0:4 offset1:5\nM 64,180,184\n"
"buffer_load_dwordx4 v220, s[16:19], s63 offen lds\nds_write2st64_b32 v240, a230, a231 offset0:6 offset1:7\nM 80,176,172\nR 174,186,45056\nv_mfma_f32_16x16x32_bf16 v[96:99], v[208:211], v[192:195], 0\nds_write2st64_b32 v240, a232, a233 offset0:8 offset1:9\n"
"M 80,180,188\nv_mfma_f32_16x16x32_bf16 v[100:103], v[210:213], v[192:195], 0\nds_write2st64_b32 v240, a234, a235 offset0:10 offset1:11\nG 10\nM 84,160,172\nR 176,187,32768\n"
"v_mfma_f32_16x16x32_bf16 v[104:107], v[208:211], v[196:199], 0\nds_write2st64_b32 v240, a236, a237 offset0:12 offset1:13\nM 84,164,188\nv_mfma_f32_16x16x32_bf16 v[108:111], v[210:213], v[196:199], 0\nbuffer_load_dwordx4 v221, s[16:19], s63 offen offset:1024 lds\nds_write2st64_b32 v240, a238, a239 offset0:14 offset1:15\n"
"M 68,160,168\nR 178,187,36864\nv_mfma_f32_16x16x32_bf16 v[112:115], v[208:211], v[200:203], 0\ns_add_u32 m0, s50, 0\ns_add_u32 s63, s59, 720896\nds_write2st64_b32 v240, a240, a241 offset0:16 offset1:17\n"
"M 68,164,184\nv_mfma_f32_16x16x32_bf16 v[116:119], v[210:213], v[200:203], 0\nbuffer_load_dwordx4 v220, s[16:19], s63 offen lds\nds_write2st64_b32 v240, a242, a243 offset0:18 offset1:19\nM 52,160,164\nR 180,187,40960\n"
"v_mfma_f32_16x16x32_bf16 v[120:123], v[208:211], v[204:207], 0\nds_write2st64_b32 v240, a244, a245 offset0:20 offset1:21\nM 52,164,180\nv_mfma_f32_16x16x32_bf16 v[124:127], v[210:213], v[204:207], 0\nbuffer_load_dwordx4 v221, s[16:19], s63 offen offset:1024 lds\nds_write2st64_b32 v240, a246, a247 offset0:22 offset1:23\n"
"M 36,160,160\nR 182,187,45056\nv_mfma_f32_16x16x32_bf16 v[96:99], v[208:211], v[232:235], v[96:99]\ns_add_u32 m0, s50, 8192\nds_write2st64_b32 v240, a248, a249 offset0:24 offset1:25\nM 36,164,176\n"
"v_mfma_f32_16x16x32_bf16 v[100:103], v[210:213], v[232:235], v[100:103]\nds_write2st64_b32 v240, a250, a251 offset0:26 offset1:27\nG 14\nM 40,168,160\nR 160,184,49152\nv_mfma_f32_16x16x32_bf16 v[104:107], v[208:211], v[236:239], v[104:107]\n"
"s_add_u32 s63, s59, 393216\nds_write2st64_b32 v240, a252, a253 offset0:28 offset1:29\nM 40,172,176\nv_mfma_f32_16x16x32_bf16 v[108:111], v[210:213], v[236:239], v[108:111]\nbuffer_load_dwordx4 v220, s[16:19], s63 offen lds\nds_write2st64_b32 v240, a254, a255 offset0:30 offset1:31\n"
"M 56,168,164\nR 162,184,53248\nv_mfma_f32_16x16x32_bf16 v[112:115], v[208:211], v[232:235], v[112:115]\nM 56,172,180\nv_mfma_f32_16x16x32_bf16 v[116:119], v[210:213], v[232:235], v[116:119]\nbuffer_load_dwordx4 v221, s[16:19], s63 offen offset:1024 lds\n"
"M 72,168,168\nR 164,184,57344\nv_mfma_f32_16x16x32_bf16 v[120:123], v[208:211], v[236:239], v[120:123]\ns_add_u32 m0, s50, 16384\ns_add_u32 s63, s59, 458752\nM 72,172,184\n"
"v_mfma_f32_16x16x32_bf16 v[124:127], v[210:213], v[236:239], v[124:127]\nbuffer_load_dwordx4 v220, s[16:19], s63 offen lds\nM 88,168,172\nR 166,184,61440\nM 88,172,188\nG 8\n"
"M 92,176,172\nR 168,185,49152\ns_waitcnt vmcnt(19) lgkmcnt(4)\ns_barrier\nM 92,180,188\nds_read2st64_b32 v[128:129], v225 offset0:0 offset1:1\n"
"ds_read2st64_b32 v[130:131], v225 offset0:2 offset1:3\nM 76,176,168\nR 170,185,53248\nds_read2st64_b32 v[132:133], v225 offset0:4 offset1:5\nM 76,180,184\nds_read2st64_b32 v[134:135], v225 offset0:6 offset1:7\n"
"ds_read2st64_b32 v[136:137], v225 offset0:8 offset1:9\nM 60,176,164\nR 172,185,57344\nds_read2st64_b32 v[138:139], v225 offset0:10 offset1:11\nM 60,180,180\nbuffer_load_dwordx4 v221, s[16:19], s63 offen offset:1024 lds\n"
"ds_read2st64_b32 v[140:141], v225 offset0:12 offset1:13\nM 44,176,160\nR 174,185,61440\nds_read2st64_b32 v[142:143], v225 offset0:14 offset1:15\nM 44,180,176\nds_read2st64_b32 v[144:145], v225 offset0:16 offset1:17\n"
"G 13\nG 11\nv_add_f32 v0, v0, v128\nM 32,160,192\nR 176,186,49152\nv_add_f32 v1, v1, v129\n"
"G 11\nv_add_f32 v2, v2, v130\nM 32,164,208\ns_add_u32 m0, s50, 24576\nds_read2st64_b32 v[146:147], v225 offset0:18 offset1:19\nv_add_f32 v3, v3, v131\n"
"M 48,160,196\nR 178,186,53248\nG 11\nv_add_f32 v4, v4, v132\nv_add_f32 v5, v5, v133\nM 48,164,212\n"
"s_add_u32 s63, s59, 262144\nds_read2st64_b32 v[148:149], v225 offset0:20 offset1:21\nG 11\nv_add_f32 v6, v6, v134\nM 64,160,200\nR 180,186,57344\n"
"v_add_f32 v7, v7, v135\nG 11\nv_add_f32 v8, v8, v136\nM 64,164,216\nbuffer_load_dwordx4 v220, s[16:19], s63 offen lds\nv_add_f32 v9, v9, v137\n"
"M 80,160,204\nR 182,186,61440\nG 10\nv_add_f32 v10, v10, v138\nv_add_f32 v11, v11, v139\nM 80,164,220\n"
"ds_read2st64_b32 v[150:151], v225 offset0:22 offset1:23\nG 9\nv_add_f32 v12, v12, v140\nM 84,168,204\nR 160,187,49152\nv_add_f32 v13, v13, v141\n"
"G 9\nv_add_f32 v14, v14, v142\nM 84,172,220\nds_read2st64_b32 v[152:153], v225 offset0:24 offset1:25\nv_add_f32 v15, v15, v143\nG 9\n"
"v_add_f32 v16, v16, v144\nM 68,168,200\nR 162,187,53248\nv_add_f32 v17, v17, v145\nG 8\nv_add_f32 v18, v18, v146\n"
"M 68,172,216\nds_read2st64_b32 v[154:155], v225 offset0:26 offset1:27\nv_add_f32 v19, v19, v147\nG 7\nv_add_f32 v20, v20, v148\nM 52,168,196\n"
"R 164,187,57344\nds_read2st64_b32 v[156:157], v225 offset0:28 offset1:29\nv_add_f32 v21, v21, v149\nM 52,172,212\nbuffer_load_dwordx4 v221, s[16:19], s63 offen offset:1024 lds\nG 6\n"
"v_add_f32 v22, v22, v150\nM 36,168,192\nR 166,187,61440\ns_add_u32 s63, s59, 786432\nv_add_f32 v23, v23, v151\nM 36,172,208\n"
"ds_read2st64_b32 v[158:159], v225 offset0:30 offset1:31\nG 6\nv_add_f32 v24, v24, v152\nM 40,176,192\nR 168,188,0\nv_add_f32 v25, v25, v153\n"
"M 40,180,208\ns_add_u32 m0, s50, 32768\nG 5\nv_add_f32 v26, v26, v154\nv_add_f32 v27, v27, v155\nM 56,176,196\n"
"R 170,188,4096\nG 4\nv_add_f32 v28, v28, v156\nv_add_f32 v29, v29, v157\nM 56,180,212\nbuffer_load_dwordx4 v220, s[16:19], s63 offen lds\n"
"G 2\nv_add_f32 v30, v30, v158\nv_add_f32 v31, v31, v159\nM 72,176,200\nR 172,188,8192\nv_cvt_pk_bf16_f32 v0, v0, v1\n"
"v_cvt_pk_bf16_f32 v1, v2, v3\nM 72,180,216\nv_cvt_pk_bf16_f32 v2, v4, v5\nv_cvt_pk_bf16_f32 v3, v6, v7\nv_cvt_pk_bf16_f32 v8, v8, v9\nM 88,176,204\n"
"R 174,188,12288\nv_cvt_pk_bf16_f32 v9, v10, v11\nv_cvt_pk_bf16_f32 v10, v12, v13\nM 88,180,220\nv_cvt_pk_bf16_f32 v11, v14, v15\nv_cvt_pk_bf16_f32 v16, v16, v17\n"
"v_cvt_pk_bf16_f32 v17, v18, v19\nM 92,160,204\nR 176,189,0\nv_cvt_pk_bf16_f32 v18, v20, v21\nv_cvt_pk_bf16_f32 v19, v22, v23\nM 92,164,220\n"
"buffer_load_dwordx4 v221, s[16:19], s63 offen offset:1024 lds\nv_cvt_pk_bf16_f32 v24, v24, v25\nM 76,160,200\nR 178,189,4096\nv_cvt_pk_bf16_f32 v25, v26, v27\nv_cvt_pk_bf16_f32 v26, v28, v29\n"
"M 76,164,216\ns_bfe_u32 s100, s57, 0x20007\ns_mul_i32 s100, s100, 49152\ns_add_u32 s100, s100, s72\ns_add_u32 s100, s100, 65028096\nv_cvt_pk_bf16_f32 v27, v30, v31\n"
"M 60,160,196\nR 180,189,8192\nbuffer_store_dwordx4 v[0:3], v252, s[20:23], s100 offen offset:0 sc1\nM 60,164,212\nbuffer_store_dwordx4 v[8:11], v252, s[20:23], s100 offen offset:1024 sc1\nbuffer_store_dwordx4 v[16:19], v252, s[20:23], s100 offen offset:2048 sc1\n"
"M 44,160,192\nR 182,189,12288\nM 44,164,208\ns_waitcnt vmcnt(23)\ns_barrier\nG 4\n"
"M 96,168,0\nR 160,190,0\nbuffer_store_dwordx4 v[24:27], v252, s[20:23], s100 offen offset:3072 sc1\nM 96,172,16\ns_mov_b32 m0, s51\nM 112,168,4\n"
"R 162,190,4096\nv_cvt_pk_bf16_f32 v64, v64, v65\nv_cvt_pk_bf16_f32 v65, v66, v67\nM 112,172,20\nds_write_addtid_b32 v64 offset:0\nv_cvt_pk_bf16_f32 v66, v68, v69\n"
"v_cvt_pk_bf16_f32 v67, v70, v71\nv_mfma_f32_16x16x32_bf16 v[128:131], v[168:171], a[8:11], 0\nR 164,190,8192\nv_cvt_pk_bf16_f32 v68, v72, v73\nM 128,172,24\nds_write_addtid_b32 v65 offset:256\n"
"v_cvt_pk_bf16_f32 v69, v74, v75\nv_mfma_f32_16x16x32_bf16 v[144:147], v[168:171], a[12:15], 0\nR 166,190,12288\nv_cvt_pk_bf16_f32 v70, v76, v77\nM 144,172,28\nds_write_addtid_b32 v66 offset:512\n"
"G 7\nv_cvt_pk_bf16_f32 v71, v78, v79\nv_mfma_f32_16x16x32_bf16 v[148:151], v[176:179], a[12:15], 0\nR 168,191,0\nv_cvt_pk_bf16_f32 v72, v80, v81\nM 148,180,28\n"
"ds_write_addtid_b32 v67 offset:768\nv_cvt_pk_bf16_f32 v73, v82, v83\nv_mfma_f32_16x16x32_bf16 v[132:135], v[176:179], a[8:11], 0\nR 170,191,4096\nv_cvt_pk_bf16_f32 v74, v84, v85\nM 132,180,24\n"
"ds_write_addtid_b32 v68 offset:1024\nv_cvt_pk_bf16_f32 v75, v86, v87\nM 116,176,4\nR 172,191,8192\nv_cvt_pk_bf16_f32 v76, v88, v89\nM 116,180,20\n"
"ds_write_addtid_b32 v69 offset:1280\nv_cvt_pk_bf16_f32 v77, v90, v91\nM 100,176,0\nR 174,191,12288\nv_cvt_pk_bf16_f32 v78, v92, v93\nM 100,180,16\n"
"ds_write_addtid_b32 v70 offset:1536\nG 9\nv_cvt_pk_bf16_f32 v79, v94, v95\nM 104,160,0\nR 176,188,16384\nds_write_addtid_b32 v71 offset:1792\n"
"M 104,164,16\nds_write_addtid_b32 v72 offset:2048\nds_write_addtid_b32 v73 offset:2304\nM 120,160,4\nR 178,188,20480\nds_write_addtid_b32 v74 offset:2560\n"
"M 120,164,20\nds_write_addtid_b32 v75 offset:2816\nds_write_addtid_b32 v76 offset:3072\nv_mfma_f32_16x16x32_bf16 v[136:139], v[160:163], a[8:11], 0\nR 180,188,24576\nds_write_addtid_b32 v77 offset:3328\n"
"M 136,164,24\nds_write_addtid_b32 v78 offset:3584\nds_write_addtid_b32 v79 offset:3840\nv_mfma_f32_16x16x32_bf16 v[152:155], v[160:163], a[12:15], 0\nR 182,188,28672\nM 152,164,28\n"
"G 14\nv_mfma_f32_16x16x32_bf16 v[156:159], v[168:171], a[12:15], 0\nR 160,189,16384\ns_waitcnt vmcnt(16) lgkmcnt(2)\ns_barrier\nM 156,172,28\n"
"ds_read2st64_b32 v[64:65], v225 offset0:0 offset1:1\nv_mfma_f32_16x16x32_bf16 v[140:143], v[168:171], a[8:11], 0\nR 162,189,20480\nM 140,172,24\nds_read2st64_b32 v[66:67], v225 offset0:2 offset1:3\nM 124,168,4\n"
"R 164,189,24576\nM 124,172,20\nds_read2st64_b32 v[68:69], v225 offset0:4 offset1:5\nM 108,168,0\nR 166,189,28672\nM 108,172,16\n"
"ds_read2st64_b32 v[70:71], v225 offset0:6 offset1:7\nG 8\nM 96,176,32\nR 168,190,16384\nM 96,180,48\nds_read2st64_b32 v[72:73], v225 offset0:8 offset1:9\n"
"G 6\nv_mfma_f32_16x16x32_bf16 v[32:35], v[208:211], v[64:67], v[32:35]\nv_mfma_f32_16x16x32_bf16 v[36:39], v[210:213], v[64:67], v[36:39]\nM 112,176,36\nR 170,190,20480\nM 112,180,52\n"
"ds_read2st64_b32 v[74:75], v225 offset0:10 offset1:11\nM 128,176,40\nR 172,190,24576\nM 128,180,56\nds_read2st64_b32 v[76:77], v225 offset0:12 offset1:13\nG 6\n"
"v_mfma_f32_16x16x32_bf16 v[40:43], v[208:211], v[68:71], v[40:43]\nv_mfma_f32_16x16x32_bf16 v[44:47], v[210:213], v[68:71], v[44:47]\nM 144,176,44\nR 174,190,28672\nM 144,180,60\nds_read2st64_b32 v[78:79], v225 offset0:14 offset1:15\n"
"M 148,160,44\nR 176,191,16384\nM 148,164,60\nG 5\nv_mfma_f32_16x16x32_bf16 v[48:51], v[208:211], v[72:75], v[48:51]\nv_mfma_f32_16x16x32_bf16 v[52:55], v[210:213], v[72:75], v[52:55]\n"
"M 132,160,40\nR 178,191,20480\nM 132,164,56\nM 116,160,36\nR 180,191,24576\nM 116,164,52\n"
"G 3\nv_mfma_f32_16x16x32_bf16 v[56:59], v[208:211], v[76:79], v[56:59]\nv_mfma_f32_16x16x32_bf16 v[60:63], v[210:213], v[76:79], v[60:63]\nM 100,160,32\nR 182,191,28672\nv_mov_b32 v226, s46\n"
"M 100,164,48\nv_mov_b32 v227, s47\nM 104,168,32\nR 160,188,32768\ns_bfe_u32 s62, s56, 0x20007\ns_lshl_b32 s62, s62, 5\n"
"s_add_u32 s61, s74, 1009664\ns_add_u32 s61, s61, s62\ns_waitcnt vmcnt(0)\ns_mov_b64 exec, 1\nbuffer_atomic_swap_x2 v[226:227], off, s[20:23], s61 sc1\ns_mov_b64 exec, -1\n"
"M 104,172,48\ns_bfe_u32 s62, s57, 0x20007\ns_lshl_b32 s62, s62, 5\ns_add_u32 s61, s74, 1009672\ns_add_u32 s61, s61, s62\ns_mov_b64 exec, 1\n"
"buffer_atomic_swap_x2 v[226:227], off, s[20:23], s61 sc1\ns_mov_b64 exec, -1\nM 120,168,36\nR 162,188,36864\nM 120,172,52\ns_add_u32 m0, s50, 40960\n"
"M 136,168,40\nR 164,188,40960\nv_cvt_pk_bf16_f32 v32, v32, v33\nv_cvt_pk_bf16_f32 v33, v34, v35\nM 136,172,56\ns_add_u32 s63, s59, 327680\n"
"v_cvt_pk_bf16_f32 v34, v36, v37\nv_cvt_pk_bf16_f32 v35, v38, v39\nv_cvt_pk_bf16_f32 v40, v40, v41\nM 152,168,44\nR 166,188,45056\nv_cvt_pk_bf16_f32 v41, v42, v43\n"
"v_cvt_pk_bf16_f32 v42, v44, v45\nM 152,172,60\nG 4\nv_cvt_pk_bf16_f32 v43, v46, v47\nv_cvt_pk_bf16_f32 v48, v48, v49\nv_cvt_pk_bf16_f32 v49, v50, v51\n"
"M 156,176,44\nR 168,189,32768\nv_cvt_pk_bf16_f32 v50, v52, v53\nv_cvt_pk_bf16_f32 v51, v54, v55\nM 156,180,60\nbuffer_load_dwordx4 v220, s[16:19], s63 offen lds\n"
"v_cvt_pk_bf16_f32 v56, v56, v57\nM 140,176,40\nR 170,189,36864\nv_cvt_pk_bf16_f32 v57, v58, v59\nv_cvt_pk_bf16_f32 v58, v60, v61\nM 140,180,56\n"
"buffer_load_dwordx4 v221, s[16:19], s63 offen offset:1024 lds\nv_cvt_pk_bf16_f32 v59, v62, v63\nM 124,176,36\nR 172,189,40960\ns_add_u32 s63, s59, 851968\ns_bfe_u32 s100, s58, 0x20007\n"
"s_mul_i32 s100, s100, 49152\ns_add_u32 s100, s100, s72\ns_add_u32 s100, s100, 65044480\nM 124,180,52\ns_add_u32 m0, s50, 49152\nbuffer_store_dwordx4 v[32:35], v252, s[20:23], s100 offen offset:0 sc1\n"
"M 108,176,32\nR 174,189,45056\nbuffer_store_dwordx4 v[40:43], v252, s[20:23], s100 offen offset:1024 sc1\nM 108,180,48\ns_barrier\nbuffer_store_dwordx4 v[48:51], v252, s[20:23], s100 offen offset:2048 sc1\n"
"G 4\nM 96,160,64\nR 176,190,32768\nbuffer_store_dwordx4 v[56:59], v252, s[20:23], s100 offen offset:3072 sc1\nM 96,164,80\nbuffer_load_dwordx4 v220, s[16:19], s63 offen lds\n"
"M 112,160,68\nR 178,190,36864\nM 112,164,84\nbuffer_load_dwordx4 v221, s[16:19], s63 offen offset:1024 lds\nM 128,160,72\nR 180,190,40960\n"
"M 128,164,88\nM 144,160,76\nR 182,190,45056\nM 144,164,92\nG 4\nM 148,168,76\n"
"R 160,191,32768\nv_bfe_u32 v250, v252, 4, 2\nM 148,172,92\nv_lshlrev_b32 v250, 3, v250\ns_lshl_b32 s60, s43, 5\nv_add_u32 v250, s60, v250\n"
"M 132,168,72\nR 162,191,36864\nM 132,172,88\nM 116,168,68\nR 164,191,40960\nM 116,172,84\n"
"M 100,168,64\nR 166,191,45056\nM 100,172,80\nG 4\nM 104,176,64\nR 168,188,49152\n"
"M 104,180,80\nM 120,176,68\nR 170,188,53248\nM 120,180,84\nM 136,176,72\nR 172,184,0\n"
"M 136,180,88\nM 152,176,76\nR 174,184,4096\nM 152,180,92\nG 4\nM 156,160,76\n"
"R 176,189,49152\nM 156,164,92\nM 140,160,72\nR 178,189,53248\nM 140,164,88\nM 124,160,68\n"
"R 180,185,0\nM 124,164,84\nM 108,160,64\nR 182,185,4096\nM 108,164,80\nG 4\n"
"M 96,168,96\nR 160,190,49152\nM 96,172,112\nM 112,168,100\nR 162,190,53248\nM 112,172,116\n"
"M 128,168,104\nR 164,186,0\nM 128,172,120\nM 144,168,108\nR 166,186,4096\nM 144,172,124\n"
"G 4\nM 148,176,108\nR 168,191,49152\nM 148,180,124\nM 132,176,104\nR 170,191,53248\n"
"M 132,180,120\nM 116,176,100\nR 172,187,0\nM 116,180,116\nM 100,176,96\nR 174,187,4096\n"
"v_mov_b32 v226, s46\nM 100,180,112\nG 4\nv_mov_b32 v227, s47\nM 104,160,96\nR 176,184,8192\n"
"s_bfe_u32 s62, s58, 0x20007\ns_lshl_b32 s62, s62, 5\ns_add_u32 s61, s74, 1009680\ns_add_u32 s61, s61, s62\ns_waitcnt vmcnt(0)\ns_mov_b64 exec, 1\n"
"buffer_atomic_swap_x2 v[226:227], off, s[20:23], s61 sc1\ns_mov_b64 exec, -1\ns_barrier\nM 104,164,112\nM 120,160,100\nR 178,184,12288\n"
"M 120,164,116\nM 136,160,104\nR 180,184,16384\nv_mov_b32 v248, 0\nM 136,164,120\nv_mov_b32 v249, 0\n"
"M 152,160,108\nR 182,184,20480\ns_add_u32 s61, s74, 1009664\ns_mov_b64 exec, 3\nbuffer_atomic_or_x2 v[248:249], v250, s[20:23], s61 offen sc0 sc1\ns_mov_b64 exec, -1\n"
"M 152,164,124\nG 4\nM 156,168,108\nR 160,185,8192\nM 156,172,124\nM 140,168,104\n"
"R 162,185,12288\nM 140,172,120\nM 124,168,100\nR 164,185,16384\nM 124,172,116\nM 108,168,96\n"
"R 166,185,20480\nM 108,172,112\nG 4\nM 96,176,128\nR 168,186,8192\nM 96,180,144\n"
"M 112,176,132\nR 170,186,12288\nM 112,180,148\nM 128,176,136\nR 172,186,16384\nM 128,180,152\n"
"M 144,176,140\nR 174,186,20480\nM 144,180,156\nG 4\nM 148,160,140\nR 176,187,8192\n"
"M 148,164,156\nM 132,160,136\nR 178,187,12288\nM 132,164,152\nM 116,160,132\nR 180,187,16384\n"
"M 116,164,148\nM 100,160,128\nR 182,187,20480\nM 100,164,144\nG 4\nM 104,168,128\n"
"R 160,184,24576\nM 104,172,144\nM 120,168,132\nR 162,184,28672\nM 120,172,148\nM 136,168,136\n"
"R 164,184,32768\nM 136,172,152\ns_mul_i32 s64, s43, 49152\nM 152,168,140\nR 166,184,36864\nM 152,172,156\n"
"G 4\nM 156,176,140\nR 168,185,24576\ns_barrier\ns_waitcnt vmcnt(0)\nv_cmp_eq_u64 s[86:87], s[46:47], v[248:249]\n"
"M 156,180,156\ns_add_u32 s64, s64, s72\nM 140,176,136\nR 170,185,28672\ns_and_b32 s62, s86, 3\ns_cmp_eq_u32 s62, 3\n"
"s_cbranch_scc0 Lq944\nLq945:\nv_mov_b32 v248, 0\nM 140,180,152\ns_add_u32 s64, s64, 65011712\nv_mov_b32 v249, 0\n"
"M 124,176,132\nR 172,185,32768\ns_add_u32 s61, s74, 1009664\ns_mov_b64 exec, 4\nbuffer_atomic_or_x2 v[248:249], v250, s[20:23], s61 offen sc0 sc1\ns_mov_b64 exec, -1\n"
"M 124,180,148\nbuffer_load_dwordx4 v[0:3], v252, s[20:23], s64 offen offset:0 sc1\nM 108,176,128\nR 174,185,36864\nM 108,180,144\nbuffer_load_dwordx4 v[4:7], v252, s[20:23], s64 offen offset:1024 sc1\n"
"G 4\nM 128,160,168\nR 176,186,24576\ns_mul_i32 s65, s43, 49152\nM 128,164,184\nR 178,186,28672\n"
"s_add_u32 s65, s65, s72\nM 144,160,172\nR 180,186,32768\nbuffer_load_dwordx4 v[8:11], v252, s[20:23], s64 offen offset:2048 sc1\nM 144,164,188\nR 182,186,36864\n"
"G 4\nM 148,168,172\nR 160,187,24576\ns_add_u32 s65, s65, 65028096\nM 148,172,188\nR 162,187,28672\n"
"buffer_load_dwordx4 v[12:15], v252, s[20:23], s64 offen offset:3072 sc1\nM 132,168,168\nR 164,187,32768\nM 132,172,184\nR 166,187,36864\nG 4\n"
"M 136,176,168\nR 168,184,40960\nbuffer_load_dwordx4 v[16:19], v252, s[20:23], s65 offen offset:0 sc1\nM 136,180,184\nR 170,184,45056\nM 152,176,172\n"
"R 172,184,49152\nM 152,180,188\nR 174,184,53248\nG 4\nM 156,160,172\nR 176,185,40960\n"
"buffer_load_dwordx4 v[20:23], v252, s[20:23], s65 offen offset:1024 sc1\nM 156,164,188\nR 178,185,45056\nM 140,160,168\nR 180,185,49152\nbuffer_load_dwordx4 v[24:27], v252, s[20:23], s65 offen offset:2048 sc1\n"
"M 140,164,184\nR 182,185,53248\nG 4\nM 128,168,200\nR 160,186,40960\nM 128,172,216\n"
"R 162,186,45056\nbuffer_load_dwordx4 v[28:31], v252, s[20:23], s65 offen offset:3072 sc1\nM 144,168,204\nR 164,186,49152\nM 144,172,220\nR 166,186,53248\n"
"G 4\nM 148,176,204\nR 168,187,40960\nM 148,180,220\nR 170,187,45056\nM 132,176,200\n"
"R 172,187,49152\nM 132,180,216\nR 174,187,53248\nG 4\nM 136,160,200\nR 176,184,24576\n"
"M 136,164,216\nR 178,184,28672\nM 152,160,204\nR 180,184,32768\ns_mul_i32 s66, s43, 49152\nM 152,164,220\n"
"R 182,184,36864\nG 4\nM 156,168,204\nR 160,185,24576\ns_add_u32 s66, s66, s72\nM 156,172,220\n"
"R 162,185,28672\ns_add_u32 s66, s66, 65044480\nM 140,168,200\nR 164,185,32768\ns_waitcnt vmcnt(8)\nv_cmp_eq_u64 s[86:87], s[46:47], v[248:249]\n"
"M 140,172,216\nR 166,185,36864\nG 4\nM 96,176,160\nR 168,186,24576\ns_and_b32 s62, s86, 4\n"
"s_cmp_eq_u32 s62, 4\ns_cbranch_scc0 Lq960\nLq961:\nM 96,180,176\nR 170,186,28672\ns_mov_b32 m0, s51\n"
"M 112,176,164\nR 172,186,32768\nbuffer_load_dwordx4 v[64:67], v252, s[20:23], s66 offen offset:0 sc1\nv_cvt_pk_bf16_f32 v128, v128, v129\nM 112,180,180\nR 174,186,36864\n"
"G 4\nv_cvt_pk_bf16_f32 v129, v130, v131\nM 116,160,164\nR 176,187,24576\nbuffer_load_dwordx4 v[68:71], v252, s[20:23], s66 offen offset:1024 sc1\nv_cvt_pk_bf16_f32 v130, v132, v133\n"
"M 116,164,180\nR 178,187,28672\nv_cvt_pk_bf16_f32 v131, v134, v135\nv_cvt_pk_bf16_f32 v132, v136, v137\nM 100,160,160\nR 180,187,32768\n"
"v_cvt_pk_bf16_f32 v133, v138, v139\nv_cvt_pk_bf16_f32 v134, v140, v141\nM 100,164,176\nR 182,187,36864\nG 4\nv_cvt_pk_bf16_f32 v135, v142, v143\n"
"M 104,168,160\nR 160,184,40960\nbuffer_load_dwordx4 v[72:75], v252, s[20:23], s66 offen offset:2048 sc1\nv_cvt_pk_bf16_f32 v136, v144, v145\nM 104,172,176\nR 162,184,45056\n"
"buffer_load_dwordx4 v[76:79], v252, s[20:23], s66 offen offset:3072 sc1\nv_cvt_pk_bf16_f32 v137, v146, v147\nM 120,168,164\nR 164,184,49152\nv_cvt_pk_bf16_f32 v138, v148, v149\nv_cvt_pk_bf16_f32 v139, v150, v151\n"
"M 120,172,180\nR 166,184,53248\nG 4\nv_cvt_pk_bf16_f32 v140, v152, v153\nM 124,176,164\nR 168,185,40960\n"
"v_cvt_pk_bf16_f32 v141, v154, v155\nv_cvt_pk_bf16_f32 v142, v156, v157\nM 124,180,180\nR 170,185,45056\nds_write_addtid_b32 v128 offset:0\nv_cvt_pk_bf16_f32 v143, v158, v159\n"
"M 108,176,160\nR 172,185,49152\nds_write_addtid_b32 v129 offset:256\nds_write_addtid_b32 v130 offset:512\nds_write_addtid_b32 v131 offset:768\nds_write_addtid_b32 v132 offset:1024\n"
"ds_write_addtid_b32 v133 offset:1280\nds_write_addtid_b32 v134 offset:1536\nds_write_addtid_b32 v135 offset:1792\nds_write_addtid_b32 v136 offset:2048\nds_write_addtid_b32 v137 offset:2304\nds_write_addtid_b32 v138 offset:2560\n"
"ds_write_addtid_b32 v139 offset:2816\nds_write_addtid_b32 v140 offset:3072\nds_write_addtid_b32 v141 offset:3328\nds_write_addtid_b32 v142 offset:3584\nds_write_addtid_b32 v143 offset:3840\nM 108,180,176\n"
"R 174,185,53248\nG 15\nM 96,160,192\nR 176,186,40960\nM 96,164,208\nR 178,186,45056\n"
"G 3\ns_barrier\nM 112,160,196\nR 180,186,49152\nM 112,164,212\nR 182,186,53248\n"
"G 4\nM 116,168,196\nR 160,187,40960\nM 116,172,212\nR 162,187,45056\nM 100,168,192\n"
"R 164,187,49152\nM 100,172,208\nR 166,187,53248\nG 4\nM 104,176,192\nds_read2st64_b32 v[128:129], v225 offset0:0 offset1:1\n"
"M 104,180,208\nds_read2st64_b32 v[130:131], v225 offset0:2 offset1:3\nM 120,176,196\nds_read2st64_b32 v[132:133], v225 offset0:4 offset1:5\nM 120,180,212\nds_read2st64_b32 v[134:135], v225 offset0:6 offset1:7\n"
"G 4\nM 124,160,196\nds_read2st64_b32 v[136:137], v225 offset0:8 offset1:9\nM 124,164,212\nds_read2st64_b32 v[138:139], v225 offset0:10 offset1:11\nM 108,160,192\n"
"ds_read2st64_b32 v[140:141], v225 offset0:12 offset1:13\nM 108,164,208\nds_read2st64_b32 v[142:143], v225 offset0:14 offset1:15\nG 6\nv_mfma_f32_16x16x32_bf16 v[96:99], v[208:211], v[128:131], v[96:99]\nv_mfma_f32_16x16x32_bf16 v[100:103], v[210:213], v[128:131], v[100:103]\n"
"G 4\nv_mfma_f32_16x16x32_bf16 v[104:107], v[208:211], v[132:135], v[104:107]\nv_mfma_f32_16x16x32_bf16 v[108:111], v[210:213], v[132:135], v[108:111]\nG 2\nv_mfma_f32_16x16x32_bf16 v[112:115], v[208:211], v[136:139], v[112:115]\nv_mfma_f32_16x16x32_bf16 v[116:119], v[210:213], v[136:139], v[116:119]\n"
"G 0\nv_mfma_f32_16x16x32_bf16 v[120:123], v[208:211], v[140:143], v[120:123]\nv_mfma_f32_16x16x32_bf16 v[124:127], v[210:213], v[140:143], v[124:127]\ns_waitcnt vmcnt(11)\nv_mfma_f32_16x16x32_bf16 v[96:99], v[208:211], v[0:3], v[96:99]\nv_mfma_f32_16x16x32_bf16 v[100:103], v[210:213], v[0:3], v[100:103]\n"
"s_waitcnt vmcnt(10)\nv_mfma_f32_16x16x32_bf16 v[104:107], v[208:211], v[4:7], v[104:107]\nv_mfma_f32_16x16x32_bf16 v[108:111], v[210:213], v[4:7], v[108:111]\ns_waitcnt vmcnt(9)\nv_mfma_f32_16x16x32_bf16 v[112:115], v[208:211], v[8:11], v[112:115]\nv_mfma_f32_16x16x32_bf16 v[116:119], v[210:213], v[8:11], v[116:119]\n"
"s_waitcnt vmcnt(8)\nv_mfma_f32_16x16x32_bf16 v[120:123], v[208:211], v[12:15], v[120:123]\nv_mfma_f32_16x16x32_bf16 v[124:127], v[210:213], v[12:15], v[124:127]\ns_waitcnt vmcnt(7)\nv_mfma_f32_16x16x32_bf16 v[96:99], v[208:211], v[16:19], v[96:99]\nv_mfma_f32_16x16x32_bf16 v[100:103], v[210:213], v[16:19], v[100:103]\n"
"s_waitcnt vmcnt(6)\nv_mfma_f32_16x16x32_bf16 v[104:107], v[208:211], v[20:23], v[104:107]\nv_mfma_f32_16x16x32_bf16 v[108:111], v[210:213], v[20:23], v[108:111]\ns_waitcnt vmcnt(5)\nv_mfma_f32_16x16x32_bf16 v[112:115], v[208:211], v[24:27], v[112:115]\nv_mfma_f32_16x16x32_bf16 v[116:119], v[210:213], v[24:27], v[116:119]\n"
"s_waitcnt vmcnt(4)\nv_mfma_f32_16x16x32_bf16 v[120:123], v[208:211], v[28:31], v[120:123]\nv_mfma_f32_16x16x32_bf16 v[124:127], v[210:213], v[28:31], v[124:127]\ns_waitcnt vmcnt(3)\nv_mfma_f32_16x16x32_bf16 v[96:99], v[208:211], v[64:67], v[96:99]\nv_mfma_f32_16x16x32_bf16 v[100:103], v[210:213], v[64:67], v[100:103]\n"
"s_waitcnt vmcnt(2)\nv_mfma_f32_16x16x32_bf16 v[104:107], v[208:211], v[68:71], v[104:107]\nv_mfma_f32_16x16x32_bf16 v[108:111], v[210:213], v[68:71], v[108:111]\ns_waitcnt vmcnt(1)\nv_mfma_f32_16x16x32_bf16 v[112:115], v[208:211], v[72:75], v[112:115]\nv_mfma_f32_16x16x32_bf16 v[116:119], v[210:213], v[72:75], v[116:119]\n"
"s_waitcnt vmcnt(0)\nv_mfma_f32_16x16x32_bf16 v[120:123], v[208:211], v[76:79], v[120:123]\nv_mfma_f32_16x16x32_bf16 v[124:127], v[210:213], v[76:79], v[124:127]\nv_cvt_pk_bf16_f32 v192, v96, v97\nv_cvt_pk_bf16_f32 v193, v98, v99\nv_cvt_pk_bf16_f32 v194, v100, v101\n"
"v_cvt_pk_bf16_f32 v195, v102, v103\nv_cvt_pk_bf16_f32 v196, v104, v105\nv_cvt_pk_bf16_f32 v197, v106, v107\nv_cvt_pk_bf16_f32 v198, v108, v109\nv_cvt_pk_bf16_f32 v199, v110, v111\nv_cvt_pk_bf16_f32 v200, v112, v113\n"
"v_cvt_pk_bf16_f32 v201, v114, v115\nv_cvt_pk_bf16_f32 v202, v116, v117\nv_cvt_pk_bf16_f32 v203, v118, v119\nv_cvt_pk_bf16_f32 v204, v120, v121\nv_cvt_pk_bf16_f32 v205, v122, v123\nv_cvt_pk_bf16_f32 v206, v124, v125\n"
"v_cvt_pk_bf16_f32 v207, v126, v127\nv_and_b32 v230, 0xf0, v252\nv_lshlrev_b32 v230, 7, v230\nv_bfe_u32 v231, v252, 8, 1\nv_lshl_add_u32 v230, v231, 5, v230\nv_lshrrev_b32 v231, 9, v252\n"
"v_lshl_add_u32 v230, v231, 4, v230\ns_add_u32 s61, s49, s53\ns_add_u32 s62, s61, 32768\nv_permlane16_swap_b32 v192, v194\nv_permlane16_swap_b32 v193, v195\nbuffer_store_dwordx4 v[192:195], v230, s[28:31], s61 offen offset:0\n"
"v_permlane16_swap_b32 v196, v198\nv_permlane16_swap_b32 v197, v199\nbuffer_store_dwordx4 v[196:199], v230, s[28:31], s61 offen offset:64\nv_permlane16_swap_b32 v200, v202\nv_permlane16_swap_b32 v201, v203\nbuffer_store_dwordx4 v[200:203], v230, s[28:31], s62 offen offset:0\n"
"v_permlane16_swap_b32 v204, v206\nv_permlane16_swap_b32 v205, v207\nbuffer_store_dwordx4 v[204:207], v230, s[28:31], s62 offen offset:64\ns_add_u32 s60, s83, 1\nv_mov_b32 v234, s60\ns_or_b32 s60, s45, s40\n"
"s_cmp_eq_u32 s60, 0\ns_cselect_b64 exec, 1, 0\nbuffer_store_dword v234, off, s[20:23], 0 sc1\ns_mov_b64 exec, -1\ns_endpgm\nLqF:\n"
"Lq579:\nv_mov_b32 v248, 0\nv_mov_b32 v249, 0\ns_add_u32 s61, s80, 88358912\ns_mov_b64 exec, 0xffff\nbuffer_atomic_or_x2 v[248:249], v250, s[20:23], s61 offen sc0 sc1\n"
"s_mov_b64 exec, -1\ns_waitcnt vmcnt(0)\nv_cmp_eq_u64 s[86:87], s[46:47], v[248:249]\ns_nop 1\ns_and_b32 s62, s86, 0xffff\ns_cmp_eq_u32 s62, 0xffff\n"
"s_cbranch_scc1 Lq580\ns_sleep 1\ns_sub_u32 s48, s48, 1\ns_cmp_gt_i32 s48, 0\ns_cbranch_scc1 Lq579\ns_branch Lq580\n"
"Lq629:\ns_lshr_b32 s84, s49, 3\ns_add_u32 s84, s84, 82837504\nbuffer_load_dwordx4 v[128:131], v252, s[20:23], s84 offen offset:0 sc1\nbuffer_load_dwordx4 v[132:135], v252, s[20:23], s84 offen offset:1024 sc1\nbuffer_load_dwordx4 v[136:139], v252, s[20:23], s84 offen offset:2048 sc1\n"
"buffer_load_dwordx4 v[140:143], v252, s[20:23], s84 offen offset:3072 sc1\ns_lshr_b32 s85, s49, 3\ns_add_u32 s85, s85, 82841600\nbuffer_load_dwordx4 v[144:147], v252, s[20:23], s85 offen offset:0 sc1\nbuffer_load_dwordx4 v[148:151], v252, s[20:23], s85 offen offset:1024 sc1\nbuffer_load_dwordx4 v[152:155], v252, s[20:23], s85 offen offset:2048 sc1\n"
"buffer_load_dwordx4 v[156:159], v252, s[20:23], s85 offen offset:3072 sc1\ns_waitcnt vmcnt(6)\nv_min3_u32 v230, v129, v131, v133\ns_waitcnt vmcnt(5)\nv_min3_u32 v230, v230, v135, v137\ns_waitcnt vmcnt(4)\n"
"v_min3_u32 v230, v230, v139, v141\ns_waitcnt vmcnt(3)\nv_min3_u32 v230, v230, v143, v145\ns_waitcnt vmcnt(2)\nv_min3_u32 v230, v230, v147, v149\ns_waitcnt vmcnt(1)\n"
"v_min3_u32 v230, v230, v151, v153\ns_waitcnt vmcnt(0)\nv_min3_u32 v230, v230, v155, v157\nv_min3_u32 v230, v230, v159, v159\nv_max3_u32 v231, v129, v131, v133\nv_max3_u32 v231, v231, v135, v137\n"
"v_max3_u32 v231, v231, v139, v141\nv_max3_u32 v231, v231, v143, v145\nv_max3_u32 v231, v231, v147, v149\nv_max3_u32 v231, v231, v151, v153\nv_max3_u32 v231, v231, v155, v157\nv_max3_u32 v231, v231, v159, v159\n"
"v_cmp_ne_u32 s[86:87], s46, v230\nv_cmp_ne_u32 s[88:89], s46, v231\ns_nop 0\ns_or_b64 s[86:87], s[86:87], s[88:89]\ns_cbranch_scc0 Lq630\ns_sub_u32 s48, s48, 1\n"
"s_cmp_gt_i32 s48, 0\ns_cbranch_scc1 Lq629\ns_branch Lq630\nLq679:\ns_mov_b64 exec, s[66:67]\nbuffer_load_dwordx4 v[230:233], v252, s[20:23], s65 offen offset:0 sc1\n"
"buffer_load_dwordx2 v[64:65], v227, s[20:23], s65 offen offset:2048 sc1\nbuffer_load_dwordx2 v[70:71], v227, s[20:23], s65 offen offset:2560 sc1\ns_mov_b64 exec, -1\ns_waitcnt vmcnt(0)\nv_max3_u32 v248, v233, v65, v71\nv_cmp_eq_u32 vcc, -1, v248\n"
"s_nop 1\ns_cbranch_vccz Lq680\ns_sub_u32 s48, s48, 1\ns_cmp_gt_i32 s48, 0\ns_cbranch_scc1 Lq679\ns_branch Lq680\n"
"Lq681:\ns_mov_b64 exec, s[66:67]\nbuffer_load_dwordx4 v[234:237], v252, s[20:23], s65 offen offset:1024 sc1\nbuffer_load_dwordx2 v[76:77], v227, s[20:23], s65 offen offset:3072 sc1\nbuffer_load_dwordx2 v[82:83], v227, s[20:23], s65 offen offset:3584 sc1\ns_mov_b64 exec, -1\n"
"s_waitcnt vmcnt(0)\nv_max3_u32 v248, v237, v77, v83\nv_cmp_eq_u32 vcc, -1, v248\ns_nop 1\ns_cbranch_vccz Lq682\ns_sub_u32 s48, s48, 1\n"
"s_cmp_gt_i32 s48, 0\ns_cbranch_scc1 Lq681\ns_branch Lq682\nLq684:\nv_mov_b32 v248, 0\nv_mov_b32 v249, 0\n"
"s_add_u32 s61, s80, 88498176\ns_mov_b64 exec, 0xffff\nbuffer_atomic_or_x2 v[248:249], v250, s[20:23], s61 offen sc0 sc1\ns_mov_b64 exec, -1\ns_waitcnt vmcnt(0)\nv_cmp_eq_u64 s[86:87], s[46:47], v[248:249]\n"
"s_nop 1\ns_and_b32 s62, s86, 0xffff\ns_cmp_eq_u32 s62, 0xffff\ns_cbranch_scc1 Lq685\ns_sleep 1\ns_sub_u32 s48, s48, 1\n"
"s_cmp_gt_i32 s48, 0\ns_cbranch_scc1 Lq684\ns_branch Lq685\nLq736:\nv_mov_b32 v248, 0\nv_mov_b32 v249, 0\n"
"s_add_u32 s61, s80, 88637440\ns_mov_b64 exec, 0xffff\nbuffer_atomic_or_x2 v[248:249], v250, s[20:23], s61 offen sc0 sc1\ns_mov_b64 exec, -1\ns_waitcnt vmcnt(0)\nv_cmp_eq_u64 s[86:87], s[46:47], v[248:249]\n"
"s_nop 1\ns_and_b32 s62, s86, 0xffff\ns_cmp_eq_u32 s62, 0xffff\ns_cbranch_scc1 Lq737\ns_sleep 1\ns_sub_u32 s48, s48, 1\n"
"s_cmp_gt_i32 s48, 0\ns_cbranch_scc1 Lq736\ns_branch Lq737\nLq786:\ns_lshr_b32 s84, s49, 3\ns_add_u32 s84, s84, 83361792\n"
"buffer_load_dwordx4 v[0:3], v252, s[20:23], s84 offen offset:0 sc1\nbuffer_load_dwordx4 v[4:7], v252, s[20:23], s84 offen offset:1024 sc1\nbuffer_load_dwordx4 v[8:11], v252, s[20:23], s84 offen offset:2048 sc1\nbuffer_load_dwordx4 v[12:15], v252, s[20:23], s84 offen offset:3072 sc1\ns_lshr_b32 s85, s49, 3\ns_add_u32 s85, s85, 83365888\n"
"buffer_load_dwordx4 v[16:19], v252, s[20:23], s85 offen offset:0 sc1\nbuffer_load_dwordx4 v[20:23], v252, s[20:23], s85 offen offset:1024 sc1\nbuffer_load_dwordx4 v[24:27], v252, s[20:23], s85 offen offset:2048 sc1\nbuffer_load_dwordx4 v[28:31], v252, s[20:23], s85 offen offset:3072 sc1\ns_waitcnt vmcnt(6)\nv_min3_u32 v230, v1, v3, v5\n"
"s_waitcnt vmcnt(5)\nv_min3_u32 v230, v230, v7, v9\ns_waitcnt vmcnt(4)\nv_min3_u32 v230, v230, v11, v13\ns_waitcnt vmcnt(3)\nv_min3_u32 v230, v230, v15, v17\n"
"s_waitcnt vmcnt(2)\nv_min3_u32 v230, v230, v19, v21\ns_waitcnt vmcnt(1)\nv_min3_u32 v230, v230, v23, v25\ns_waitcnt vmcnt(0)\nv_min3_u32 v230, v230, v27, v29\n"
"v_min3_u32 v230, v230, v31, v31\nv_max3_u32 v231, v1, v3, v5\nv_max3_u32 v231, v231, v7, v9\nv_max3_u32 v231, v231, v11, v13\nv_max3_u32 v231, v231, v15, v17\nv_max3_u32 v231, v231, v19, v21\n"
"v_max3_u32 v231, v231, v23, v25\nv_max3_u32 v231, v231, v27, v29\nv_max3_u32 v231, v231, v31, v31\nv_cmp_ne_u32 s[86:87], s46, v230\nv_cmp_ne_u32 s[88:89], s46, v231\ns_nop 0\n"
"s_or_b64 s[86:87], s[86:87], s[88:89]\ns_cbranch_scc0 Lq787\ns_sub_u32 s48, s48, 1\ns_cmp_gt_i32 s48, 0\ns_cbranch_scc1 Lq786\ns_branch Lq787\n"
"Lq812:\nv_mov_b32 v248, 0\nv_mov_b32 v249, 0\ns_add_u32 s61, s82, 845824\ns_mov_b64 exec, 0xf\nbuffer_atomic_or_x2 v[248:249], v250, s[20:23], s61 offen sc0 sc1\n"
"s_mov_b64 exec, -1\ns_waitcnt vmcnt(0)\nv_cmp_eq_u64 s[86:87], s[46:47], v[248:249]\ns_nop 1\ns_and_b32 s62, s86, 0xf\ns_cmp_eq_u32 s62, 0xf\n"
"s_cbranch_scc1 Lq813\ns_sleep 1\ns_sub_u32 s48, s48, 1\ns_cmp_gt_i32 s48, 0\ns_cbranch_scc1 Lq812\ns_branch Lq813\n"
"Lq847:\nv_mov_b32 v248, 0\nv_mov_b32 v249, 0\ns_add_u32 s61, s82, 845832\ns_mov_b64 exec, 0xf\nbuffer_atomic_or_x2 v[248:249], v250, s[20:23], s61 offen sc0 sc1\n"
"s_mov_b64 exec, -1\ns_waitcnt vmcnt(0)\nv_cmp_eq_u64 s[86:87], s[46:47], v[248:249]\ns_nop 1\ns_and_b32 s62, s86, 0xf\ns_cmp_eq_u32 s62, 0xf\n"
"s_cbranch_scc1 Lq848\ns_sleep 1\ns_sub_u32 s48, s48, 1\ns_cmp_gt_i32 s48, 0\ns_cbranch_scc1 Lq847\ns_branch Lq848\n"
"Lq882:\nv_mov_b32 v248, 0\nv_mov_b32 v249, 0\ns_add_u32 s61, s82, 845848\ns_mov_b64 exec, 0xf\nbuffer_atomic_or_x2 v[248:249], v250, s[20:23], s61 offen sc0 sc1\n"
"s_mov_b64 exec, -1\ns_waitcnt vmcnt(0)\nv_cmp_eq_u64 s[86:87], s[46:47], v[248:249]\ns_nop 1\ns_and_b32 s62, s86, 0xf\ns_cmp_eq_u32 s62, 0xf\n"
"s_cbranch_scc1 Lq883\ns_sleep 1\ns_sub_u32 s48, s48, 1\ns_cmp_gt_i32 s48, 0\ns_cbranch_scc1 Lq882\ns_branch Lq883\n"
"Lq901:\nv_mov_b32 v248, 0\nv_mov_b32 v249, 0\ns_add_u32 s61, s82, 845840\ns_mov_b64 exec, 0xf\nbuffer_atomic_or_x2 v[248:249], v250, s[20:23], s61 offen sc0 sc1\n"
"s_mov_b64 exec, -1\ns_waitcnt vmcnt(0)\nv_cmp_eq_u64 s[86:87], s[46:47], v[248:249]\ns_nop 1\ns_and_b32 s62, s86, 0xf\ns_cmp_eq_u32 s62, 0xf\n"
"s_cbranch_scc1 Lq902\ns_sleep 1\ns_sub_u32 s48, s48, 1\ns_cmp_gt_i32 s48, 0\ns_cbranch_scc1 Lq901\ns_branch Lq902\n"
"Lq944:\nv_mov_b32 v248, 0\nv_mov_b32 v249, 0\ns_add_u32 s61, s74, 1009664\ns_mov_b64 exec, 3\nbuffer_atomic_or_x2 v[248:249], v250, s[20:23], s61 offen sc0 sc1\n"
"s_mov_b64 exec, -1\ns_waitcnt vmcnt(0)\nv_cmp_eq_u64 s[86:87], s[46:47], v[248:249]\ns_nop 1\ns_and_b32 s62, s86, 3\ns_cmp_eq_u32 s62, 3\n"
"s_cbranch_scc1 Lq945\ns_sleep 1\ns_sub_u32 s48, s48, 1\ns_cmp_gt_i32 s48, 0\ns_cbranch_scc1 Lq944\ns_branch Lq945\n"
"Lq960:\nv_mov_b32 v248, 0\nv_mov_b32 v249, 0\ns_add_u32 s61, s74, 1009664\ns_mov_b64 exec, 4\nbuffer_atomic_or_x2 v[248:249], v250, s[20:23], s61 offen sc0 sc1\n"
"s_mov_b64 exec, -1\ns_waitcnt vmcnt(0)\nv_cmp_eq_u64 s[86:87], s[46:47], v[248:249]\ns_nop 1\ns_and_b32 s62, s86, 4\ns_cmp_eq_u32 s62, 4\n"
"s_cbranch_scc1 Lq961\ns_sleep 1\ns_sub_u32 s48, s48, 1\ns_cmp_gt_i32 s48, 0\ns_cbranch_scc1 Lq960\ns_branch Lq961\n"
"LqE:\n"
  : "+{v0}"(tid), "+{s[92:93]}"(ka), "+{s45}"(bid) :
  : "a0", "a1", "a2", "a3", "a4", "a5", "a6", "a7", "a8", "a9", "a10", "a11", "a12", "a13", "a14", "a15", "a16", "a17", "a18", "a19", "a20", "a21", "a22", "a23", "a24", "a25", "a26", "a27", "a28", "a29", "a30", "a31", "a32", "a33", "a34", "a35", "a36", "a37", "a38", "a39", "a40", "a41", "a42", "a43", "a44", "a45", "a46", "a47", "a48", "a49", "a50", "a51", "a52", "a53", "a54", "a55", "a56", "a57", "a58", "a59", "a60", "a61", "a62", "a63", "a64", "a65", "a66", "a67", "a68", "a69", "a70", "a71", "a72", "a73", "a74", "a75", "a76", "a77", "a78", "a79", "a80", "a81", "a82", "a83", "a84", "a85", "a86", "a87", "a88", "a89", "a90", "a91", "a92", "a93", "a94", "a95", "a96", "a97", "a98", "a99", "a100", "a101", "a102", "a103", "a104", "a105", "a106", "a107", "a108", "a109", "a110", "a111", "a112", "a113", "a114", "a115", "a116", "a117", "a118", "a119", "a120", "a121", "a122", "a123", "a124", "a125", "a126", "a127", "a128", "a129", "a130", "a131", "a132", "a133", "a134", "a135", "a136", "a137", "a138", "a139", "a140", "a141", "a142", "a143", "a144", "a145", "a146", "a147", "a148", "a149", "a150", "a151", "a152", "a153", "a154", "a155", "a156", "a157", "a158", "a159", "a160", "a161", "a162", "a163", "a164", "a165", "a166", "a167", "a168", "a169", "a170", "a171", "a172", "a173", "a174", "a175", "a176", "a177", "a178", "a179", "a180", "a181", "a182", "a183", "a184", "a185", "a186", "a187", "a188", "a189", "a190", "a191", "a192", "a193", "a194", "a195", "a196", "a197", "a198", "a199", "a200", "a201", "a202", "a203", "a204", "a205", "a206", "a207", "a208", "a209", "a210", "a211", "a212", "a213", "a214", "a215", "a216", "a217", "a218", "a219", "a220", "a221", "a222", "a223", "a224", "a225", "a226", "a227", "a228", "a229", "a230", "a231", "a232", "a233", "a234", "a235", "a236", "a237", "a238", "a239", "a240", "a241", "a242", "a243", "a244", "a245", "a246", "a247", "a248", "a249", "a250", "a251", "a252", "a253", "a254", "a255", "v1", "v2", "v3", "v4", "v5", "v6", "v7", "v8", "v9", "v10", "v11", "v12", "v13", "v14", "v15", "v16", "v17", "v18", "v19", "v20", "v21", "v22", "v23", "v24", "v25", "v26", "v27", "v28", "v29", "v30", "v31", "v32", "v33", "v34", "v35", "v36", "v37", "v38", "v39", "v40", "v41", "v42", "v43", "v44", "v45", "v46", "v47", "v48", "v49", "v50", "v51", "v52", "v53", "v54", "v55", "v56", "v57", "v58", "v59", "v60", "v61", "v62", "v63", "v64", "v65", "v66", "v67", "v68", "v69", "v70", "v71", "v72", "v73", "v74", "v75", "v76", "v77", "v78", "v79", "v80", "v81", "v82", "v83", "v84", "v85", "v86", "v87", "v88", "v89", "v90", "v91", "v92", "v93", "v94", "v95", "v96", "v97", "v98", "v99", "v100", "v101", "v102", "v103", "v104", "v105", "v106", "v107", "v108", "v109", "v110", "v111", "v112", "v113", "v114", "v115", "v116", "v117", "v118", "v119", "v120", "v121", "v122", "v123", "v124", "v125", "v126", "v127", "v128", "v129", "v130", "v131", "v132", "v133", "v134", "v135", "v136", "v137", "v138", "v139", "v140", "v141", "v142", "v143", "v144", "v145", "v146", "v147", "v148", "v149", "v150", "v151", "v152", "v153", "v154", "v155", "v156", "v157", "v158", "v159", "v160", "v161", "v162", "v163", "v164", "v165", "v166", "v167", "v168", "v169", "v170", "v171", "v172", "v173", "v174", "v175", "v176", "v177", "v178", "v179", "v180", "v181", "v182", "v183", "v184", "v185", "v186", "v187", "v188", "v189", "v190", "v191", "v192", "v193", "v194", "v195", "v196", "v197", "v198", "v199", "v200", "v201", "v202", "v203", "v204", "v205", "v206", "v207", "v208", "v209", "v210", "v211", "v212", "v213", "v214", "v215", "v216", "v217", "v218", "v219", "v220", "v221", "v222", "v223", "v224", "v225", "v226", "v227", "v228", "v229", "v230", "v231", "v232", "v233", "v234", "v235", "v236", "v237", "v238", "v239", "v240", "v241", "v242", "v243", "v244", "v245", "v246", "v247", "v248", "v249", "v250", "v251", "v252", "v253", "v254", "v255", "s4", "s5", "s6", "s7", "s8", "s9", "s10", "s11", "s12", "s13", "s14", "s15", "s16", "s17", "s18", "s19", "s20", "s21", "s22", "s23", "s24", "s25", "s26", "s27", "s28", "s29", "s30", "s31", "s32", "s33", "s34", "s35", "s36", "s37", "s38", "s39", "s40", "s41", "s42", "s43", "s44", "s46", "s47", "s48", "s49", "s50", "s51", "s52", "s53", "s54", "s55", "s56", "s57", "s58", "s59", "s60", "s61", "s62", "s63", "s64", "s65", "s66", "s67", "s68", "s69", "s70", "s71", "s72", "s73", "s74", "s75", "s76", "s77", "s78", "s79", "s80", "s81", "s82", "s83", "s84", "s85", "s86", "s87", "s88", "s89", "s90", "s91", "s94", "s95", "s96", "s97", "s98", "s99", "s100", "s101", "vcc", "m0", "scc", "memory");
#endif
}
extern "C" void kernel_launch(void* const* d_in, const int* in_sizes, int n_in, void* d_out, int out_size, void* d_ws, size_t ws_size, hipStream_t stream) {
  (void)hipFuncSetAttribute((const void*)layers, hipFuncAttributeMaxDynamicSharedMemorySize, 160 * 1024);
  hipLaunchKernelGGL(layers, dim3(256), dim3(256), 160 * 1024, stream, d_in[0], d_in[1], d_in[2], d_in[4], d_in[5], d_in[6], d_in[7], d_out, d_ws);
}
```

```cpp
#include <hip/hip_runtime.h>
__global__ __launch_bounds__(256, 1) __attribute__((aligned(4096))) void layers(const void* x, const void* wqkv, const void* wo, const void* w1, const void* b1, const void* w2, const void* b2, void* out, void* ws) {
#if defined(__HIP_DEVICE_COMPILE__)
  int tid = threadIdx.x, bid = blockIdx.x;
  unsigned long long ka = (unsigned long long)__builtin_amdgcn_kernarg_segment_ptr();
  asm volatile(
".macro M d,w,a\nv_mfma_f32_16x16x32_bf16 v[\\d:\\d+3], v[\\w:\\w+3], a[\\a:\\a+3], v[\\d:\\d+3]\n.endm\n.macro N d,a,w\nv_mfma_f32_16x16x32_bf16 v[\\d:\\d+3], a[\\a:\\a+3], v[\\w:\\w+3], v[\\d:\\d+3]\n.endm\n"
".macro R d,b,o\nds_read_b64_tr_b16 v[\\d:\\d+1], v\\b offset:\\o\n.endm\n.macro G n\ns_waitcnt lgkmcnt(\\n)\n.endm\n"
"s_load_dwordx2 s[24:25], s[92:93], 0\ns_load_dwordx2 s[4:5], s[92:93], 8\ns_load_dwordx2 s[8:9], s[92:93], 16\ns_load_dwordx2 s[12:13], s[92:93], 24\ns_load_dwordx2 s[32:33], s[92:93], 32\ns_load_dwordx2 s[16:17], s[92:93], 40\n"
"s_load_dwordx2 s[36:37], s[92:93], 48\ns_load_dwordx2 s[28:29], s[92:93], 56\ns_load_dwordx2 s[20:21], s[92:93], 64\nv_lshrrev_b32 v1, 6, v0\nv_and_b32 v0, 63, v0\nv_readfirstlane_b32 s40, v1\n"
"v_lshlrev_b32 v252, 4, v0\ns_getpc_b64 s[98:99]\nLqPC:\ns_lshr_b32 s60, s45, 3\ns_lshl2_add_u32 s60, s60, s40\ns_lshl_b32 s60, s60, 8\n"
"v_lshl_add_u32 v224, v252, 3, s60\nv_add_u32 v224, LqPC-LqPC, v224\nv_min_u32 v224, LqE-LqPC-4, v224\ns_mov_b64 exec, 3\nglobal_load_dword v224, v224, s[98:99]\ns_mov_b64 exec, -1\n"
"s_mov_b32 s6, 6291456\ns_mov_b32 s7, 0x20000\ns_mov_b32 s10, 2097152\ns_mov_b32 s11, 0x20000\ns_mov_b32 s14, 7340032\ns_mov_b32 s15, 0x20000\n"
"s_mov_b32 s18, 7340032\ns_mov_b32 s19, 0x20000\ns_mov_b32 s22, 89128960\ns_mov_b32 s23, 0x20000\ns_mov_b32 s26, 4194304\ns_mov_b32 s27, 0x20000\n"
"s_mov_b32 s30, 4194304\ns_mov_b32 s31, 0x20000\ns_mov_b32 s34, 7168\ns_mov_b32 s35, 0x20000\ns_mov_b32 s38, 2048\ns_mov_b32 s39, 0x20000\n"
"s_bfe_u32 s41, s45, 0x30003\ns_and_b32 s60, s45, 1\ns_lshl3_add_u32 s41, s60, s41\ns_bfe_u32 s43, s45, 0x20001\ns_lshr_b32 s44, s45, 6\ns_lshl2_add_u32 s42, s43, s44\n"
"s_lshl2_add_u32 s75, s44, s43\ns_and_b32 s90, s40, 1\ns_lshl2_add_u32 s60, s41, s40\ns_lshl_b32 s49, s60, 16\ns_lshl_b32 s50, s40, 11\ns_add_u32 s50, s50, 32768\n"
"s_lshl_b32 s52, s42, 7\ns_lshl_b32 s51, s40, 13\ns_lshl_b32 s60, s90, 10\ns_add_u32 s70, s49, s60\ns_xor_b32 s71, s70, 0x10000\ns_mul_i32 s101, s41, 196608\n"
"s_add_u32 s101, s101, s52\ns_lshl_b32 s53, s75, 7\nLqq0:\nv_lshrrev_b32 v96, 7, v252\nv_lshlrev_b32 v96, 11, v96\nv_lshrrev_b32 v97, 8, v252\n"
"v_lshlrev_b32 v97, 4, v97\nv_and_b32 v98, 0x70, v252\nv_xor_b32 v98, v98, v97\nv_add_u32 v226, v96, v98\nv_xor_b32 v227, 64, v226\nG 0\n"
"s_add_u32 m0, s51, 0\ns_add_u32 s60, s70, 0\nbuffer_load_dwordx4 v226, s[24:27], s60 offen lds\ns_add_u32 m0, s51, 1024\ns_add_u32 s60, s70, 16384\nbuffer_load_dwordx4 v227, s[24:27], s60 offen lds\n"
"s_add_u32 m0, s51, 2048\ns_add_u32 s60, s70, 32768\nbuffer_load_dwordx4 v226, s[24:27], s60 offen lds\ns_add_u32 m0, s51, 3072\ns_add_u32 s60, s70, 49152\nbuffer_load_dwordx4 v227, s[24:27], s60 offen lds\n"
"s_add_u32 m0, s51, 4096\ns_add_u32 s60, s71, 0\nbuffer_load_dwordx4 v226, s[24:27], s60 offen lds\ns_add_u32 m0, s51, 5120\ns_add_u32 s60, s71, 16384\nbuffer_load_dwordx4 v227, s[24:27], s60 offen lds\n"
"s_add_u32 m0, s51, 6144\ns_add_u32 s60, s71, 32768\nbuffer_load_dwordx4 v226, s[24:27], s60 offen lds\ns_add_u32 m0, s51, 7168\ns_add_u32 s60, s71, 49152\nbuffer_load_dwordx4 v227, s[24:27], s60 offen lds\n"
"buffer_load_dwordx2 v[230:231], off, s[20:23], 0 sc1\nv_bfe_u32 v96, v252, 5, 2\nv_bfe_u32 v97, v252, 8, 2\nv_xor_b32 v96, v96, v97\ns_and_b32 s60, s40, 1\ns_lshl_b32 s60, s60, 4\n"
"s_and_b32 s61, s40, 2\ns_lshl1_add_u32 s60, s61, s60\nv_bfe_u32 v97, v252, 7, 2\nv_lshrrev_b32 v98, 9, v252\nv_lshl_add_u32 v97, v98, 3, v97\nv_add_u32 v97, s60, v97\n"
"v_mul_u32_u24 v97, 6144, v97\nv_lshl_add_u32 v97, v96, 5, v97\nv_and_b32 v96, 16, v252\nv_add_u32 v220, v96, v97\nv_add_u32 v221, 3144704, v220\ns_add_u32 m0, s50, 0\n"
"s_add_u32 s63, s52, 2048\nbuffer_load_dwordx4 v220, s[4:7], s63 offen lds\nbuffer_load_dwordx4 v221, s[4:7], s63 offen offset:1024 lds\ns_add_u32 m0, s50, 8192\ns_add_u32 s63, s52, 198656\nbuffer_load_dwordx4 v220, s[4:7], s63 offen lds\n"
"buffer_load_dwordx4 v221, s[4:7], s63 offen offset:1024 lds\ns_lshl2_add_u32 s60, s41, s40\ns_mul_i32 s79, s60, 2176\ns_and_b32 s61, s60, -2\ns_mul_i32 s80, s61, 2176\ns_lshl2_add_u32 s61, s61, s43\n"
"s_mul_i32 s82, s61, 640\ns_mul_i32 s61, s43, 28672\ns_mul_i32 s73, s60, 229376\ns_add_u32 s73, s73, s61\ns_mul_i32 s62, s90, 14336\ns_add_u32 s68, s73, s62\n"
"s_add_u32 s61, s61, s62\ns_xor_b32 s62, s60, 1\ns_mul_i32 s69, s62, 229376\ns_add_u32 s69, s69, s61\ns_mul_i32 s61, s44, 7168\ns_add_u32 s73, s73, s61\n"
"s_lshl2_add_u32 s61, s60, s43\ns_mul_i32 s81, s61, 640\ns_lshl2_add_u32 s61, s60, s44\ns_mul_i32 s74, s61, 640\ns_lshl2_add_u32 s61, s41, s44\ns_mul_i32 s61, s61, 48\n"
"s_add_u32 s61, s61, s40\ns_lshl_b32 s72, s61, 12\ns_mul_i32 s55, s42, 448\ns_add_u32 s54, s55, 64\ns_and_b32 s54, s54, 0xffffff80\ns_bitcmp0_b32 s42, 0\n"
"s_cselect_b32 s61, 384, 0\ns_add_u32 s55, s55, s61\ns_lshl_b32 s61, s41, 9\ns_lshl_b32 s91, s42, 13\ns_add_u32 s91, s91, s61\ns_add_u32 s91, s91, 83886080\n"
"s_mul_i32 s61, s43, 1835008\ns_lshl_b32 s62, s44, 9\ns_add_u32 s61, s61, s62\ns_add_u32 s62, s43, 1\ns_and_b32 s62, s62, 3\ns_lshl_b32 s62, s62, 7\n"
"s_add_u32 s56, s61, s62\ns_add_u32 s62, s43, 2\ns_and_b32 s62, s62, 3\ns_lshl_b32 s62, s62, 7\ns_add_u32 s57, s61, s62\ns_add_u32 s62, s43, 3\n"
"s_and_b32 s62, s62, 3\ns_lshl_b32 s62, s62, 7\ns_add_u32 s58, s61, s62\ns_add_u32 s62, s43, 4\ns_and_b32 s62, s62, 3\ns_lshl_b32 s62, s62, 7\n"
"s_add_u32 s59, s61, s62\ns_mov_b32 s76, 0x3f803f80\ns_mov_b32 s77, 0xffff0000\ns_mov_b32 s48, 131072\ns_cmp_eq_u32 s60, 0\ns_cselect_b32 s78, 0xf149f2ca, 0\n"
"v_bfe_u32 v96, v252, 6, 3\nv_lshlrev_b32 v96, 7, v96\nv_lshrrev_b32 v97, 9, v252\nv_lshl_add_u32 v96, v97, 11, v96\nv_bfe_u32 v97, v252, 4, 2\nv_lshl_add_u32 v96, v97, 3, v96\n"
"s_lshl_b32 s60, s90, 10\ns_add_u32 s60, s60, 32768\nv_add_u32 v96, s60, v96\nv_bfe_u32 v97, v252, 7, 2\nv_xor_b32 v98, 0, v97\nv_lshl_add_u32 v184, v98, 5, v96\n"
"v_add_u32 v188, 0x10000, v184\nv_xor_b32 v98, 1, v97\nv_lshl_add_u32 v185, v98, 5, v96\nv_add_u32 v189, 0x10000, v185\nv_xor_b32 v98, 2, v97\nv_lshl_add_u32 v186, v98, 5, v96\n"
"v_add_u32 v190, 0x10000, v186\nv_xor_b32 v98, 3, v97\nv_lshl_add_u32 v187, v98, 5, v96\nv_add_u32 v191, 0x10000, v187\nv_lshrrev_b32 v96, 2, v252\ns_xor_b32 s60, s51, 0x2000\n"
"v_add_u32 v225, s60, v96\nv_mov_b32 v210, 0\nv_mov_b32 v211, 0\nv_bfe_u32 v96, v252, 5, 2\nv_bfe_u32 v97, v252, 8, 2\nv_xor_b32 v96, v96, v97\n"
"v_lshrrev_b32 v97, 7, v252\ns_lshl_b32 s60, s40, 3\nv_add_u32 v97, s60, v97\nv_mov_b32 v228, v97\nv_and_b32 v97, 16, v252\nv_lshl_add_u32 v229, v96, 5, v97\n"
"v_bfe_u32 v96, v252, 5, 2\nv_bfe_u32 v97, v252, 8, 2\nv_xor_b32 v96, v96, v97\nv_lshrrev_b32 v97, 7, v252\ns_lshl_b32 s60, s40, 3\nv_add_u32 v97, s60, v97\n"
"v_lshrrev_b32 v98, 1, v96\nv_lshl_add_u32 v97, v98, 5, v97\nv_and_b32 v96, 1, v96\nv_mul_u32_u24 v97, 7168, v97\nv_lshl_add_u32 v97, v96, 5, v97\nv_and_b32 v96, 16, v252\n"
"v_add_u32 v222, v96, v97\nv_add_u32 v223, 3668992, v222\nv_bfe_u32 v96, v252, 4, 4\nv_lshrrev_b32 v97, 8, v252\nv_lshrrev_b32 v98, 1, v96\nv_xor_b32 v97, v97, v98\n"
"v_lshlrev_b32 v96, 7, v96\nv_lshl_add_u32 v96, v97, 4, v96\nv_add_u32 v248, s51, v96\nv_xor_b32 v249, 64, v248\nv_add_u32 v250, 122880, v248\nv_add_u32 v251, 122880, v249\n"
"s_add_u32 m0, s51, 122880\ns_add_u32 s60, s70, 128\nbuffer_load_dwordx4 v226, s[24:27], s60 offen lds\ns_add_u32 m0, s51, 123904\ns_add_u32 s60, s70, 16512\nbuffer_load_dwordx4 v227, s[24:27], s60 offen lds\n"
"s_add_u32 m0, s51, 124928\ns_add_u32 s60, s70, 32896\nbuffer_load_dwordx4 v226, s[24:27], s60 offen lds\ns_add_u32 m0, s51, 125952\ns_add_u32 s60, s70, 49280\nbuffer_load_dwordx4 v227, s[24:27], s60 offen lds\n"
"s_add_u32 m0, s51, 126976\ns_add_u32 s60, s71, 128\nbuffer_load_dwordx4 v226, s[24:27], s60 offen lds\ns_add_u32 m0, s51, 128000\ns_add_u32 s60, s71, 16512\nbuffer_load_dwordx4 v227, s[24:27], s60 offen lds\n"
"s_add_u32 m0, s51, 129024\ns_add_u32 s60, s71, 32896\nbuffer_load_dwordx4 v226, s[24:27], s60 offen lds\ns_add_u32 m0, s51, 130048\ns_add_u32 s60, s71, 49280\nbuffer_load_dwordx4 v227, s[24:27], s60 offen lds\n"
"s_add_u32 m0, s50, 16384\ns_add_u32 s63, s52, 4096\nbuffer_load_dwordx4 v220, s[4:7], s63 offen lds\nbuffer_load_dwordx4 v221, s[4:7], s63 offen offset:1024 lds\ns_add_u32 m0, s50, 24576\ns_add_u32 s63, s52, 200704\n"
"buffer_load_dwordx4 v220, s[4:7], s63 offen lds\nbuffer_load_dwordx4 v221, s[4:7], s63 offen offset:1024 lds\ns_add_u32 m0, s50, 32768\ns_add_u32 s63, s101, 2048\nbuffer_load_dwordx4 v220, s[4:7], s63 offen lds\nbuffer_load_dwordx4 v221, s[4:7], s63 offen offset:1024 lds\n"
"s_add_u32 m0, s50, 40960\ns_add_u32 s63, s101, 4096\nbuffer_load_dwordx4 v220, s[4:7], s63 offen lds\nbuffer_load_dwordx4 v221, s[4:7], s63 offen offset:1024 lds\nv_bfe_u32 v246, v252, 4, 4\nv_lshlrev_b32 v246, 11, v246\n"
"v_lshrrev_b32 v247, 8, v252\nv_lshl_add_u32 v246, v247, 3, v246\ns_add_u32 s84, s49, s53\ns_add_u32 s85, s84, 32768\nbuffer_load_dwordx2 v[192:193], v246, s[24:27], s84 offen offset:0\nbuffer_load_dwordx2 v[194:195], v246, s[24:27], s84 offen offset:32\n"
"buffer_load_dwordx2 v[196:197], v246, s[24:27], s84 offen offset:64\nbuffer_load_dwordx2 v[198:199], v246, s[24:27], s84 offen offset:96\nbuffer_load_dwordx2 v[200:201], v246, s[24:27], s85 offen offset:0\nbuffer_load_dwordx2 v[202:203], v246, s[24:27], s85 offen offset:32\nbuffer_load_dwordx2 v[204:205], v246, s[24:27], s85 offen offset:64\nbuffer_load_dwordx2 v[206:207], v246, s[24:27], s85 offen offset:96\n"
"v_mov_b32 v106, 0xf149f149\nv_mov_b32 v104, 0xffff\nv_mov_b32 v105, 0xffff0000\ns_mov_b32 s60, 0x50000\ns_mov_b32 s61, 0x5550055\nv_cndmask_b32 v96, 0, v104, s[60:61]\n"
"s_mov_b32 s60, 0xa0000\ns_mov_b32 s61, 0xaaa00aa\nv_cndmask_b32 v96, v96, v105, s[60:61]\nv_bfi_b32 v100, v96, 0, v106\ns_mov_b32 s60, 0x150001\ns_mov_b32 s61, 0x15550155\n"
"v_cndmask_b32 v97, 0, v104, s[60:61]\ns_mov_b32 s60, 0x2a0002\ns_mov_b32 s61, 0x2aaa02aa\nv_cndmask_b32 v97, v97, v105, s[60:61]\nv_bfi_b32 v101, v97, 0, v106\ns_mov_b32 s60, 0x55505555\n"
"s_mov_b32 s61, 0x50005500\nv_cndmask_b32 v98, 0, v104, s[60:61]\ns_mov_b32 s60, 0xaaa0aaaa\ns_mov_b32 s61, 0xa000aa00\nv_cndmask_b32 v98, v98, v105, s[60:61]\nv_bfi_b32 v102, v98, 0, v106\n"
"s_mov_b32 s60, 0x55405554\ns_mov_b32 s61, 0x40005400\nv_cndmask_b32 v99, 0, v104, s[60:61]\ns_mov_b32 s60, 0xaa80aaa8\ns_mov_b32 s61, 0x8000a800\nv_cndmask_b32 v99, v99, v105, s[60:61]\n"
"v_bfi_b32 v103, v99, 0, v106\nv_add_u32 v104, 161792, v252\nds_write_b128 v104, v[96:99] offset:0\nds_write_b128 v104, v[100:103] offset:1024\ns_waitcnt vmcnt(28)\nv_readfirstlane_b32 s83, v230\n"
"v_readfirstlane_b32 s60, v231\ns_mul_i32 s46, s83, 0x9e3779b1\ns_add_u32 s46, s46, 0x7f4a7c15\ns_xor_b32 s47, s83, s60\ns_mul_i32 s47, s47, 0x85ebca6b\ns_add_u32 s47, s47, 0xc2b2ae35\n"
"v_mov_b32 v144, -1\nv_mov_b32 v145, -1\nv_mov_b32 v146, -1\nv_mov_b32 v147, -1\ns_mov_b64 exec, 0\ns_cmp_eq_u32 s40, 1\n"
"s_cbranch_scc1 Lpg2\ns_cmp_eq_u32 s40, 2\ns_cbranch_scc1 Lpg3\ns_cmp_eq_u32 s40, 3\ns_cbranch_scc1 Lpg4\ns_branch Lpg1\n"
"LretQ0:\ns_mov_b64 exec, -1\nds_read_b128 a[0:3], v248 offset:0\nds_read_b128 a[4:7], v248 offset:2048\nds_read_b128 a[8:11], v248 offset:4096\nds_read_b128 a[12:15], v248 offset:6144\n"
"ds_read_b128 a[16:19], v249 offset:0\nds_read_b128 a[20:23], v249 offset:2048\nds_read_b128 a[24:27], v249 offset:4096\nds_read_b128 a[28:31], v249 offset:6144\ns_waitcnt vmcnt(12) lgkmcnt(8)\ns_barrier\n"
"R 160,184,0\nR 162,184,4096\nR 164,184,8192\nR 166,184,12288\nR 168,185,0\nR 170,185,4096\n"
"R 172,185,8192\nR 174,185,12288\nR 176,186,0\nR 178,186,4096\nR 180,186,8192\nR 182,186,12288\n"
"G 10\nv_mfma_f32_16x16x32_bf16 v[0:3], v[160:163], a[0:3], 0\ns_add_u32 m0, s50, 49152\ns_add_u32 s63, s52, 395264\nbuffer_load_dwordx4 v220, s[4:7], s63 offen lds\nG 8\n"
"M 0,164,16\nbuffer_load_dwordx4 v221, s[4:7], s63 offen offset:1024 lds\nv_mfma_f32_16x16x32_bf16 v[16:19], v[160:163], a[4:7], 0\ns_add_u32 m0, s51, 0\ns_add_u32 s60, s70, 256\nbuffer_load_dwordx4 v226, s[24:27], s60 offen lds\n"
"M 16,164,20\ns_lshl4_add_u32 s62, s41, s42\ns_lshl_b32 s62, s62, 12\ns_add_u32 s62, s62, 77594624\ns_lshl_b32 s60, s40, 10\ns_add_u32 s62, s62, s60\n"
"buffer_store_dwordx4 v[144:147], v252, s[20:23], s62 offen sc1\ns_add_u32 m0, s50, 57344\ns_add_u32 s63, s52, 591872\nv_mfma_f32_16x16x32_bf16 v[32:35], v[160:163], a[8:11], 0\nbuffer_load_dwordx4 v220, s[4:7], s63 offen lds\nM 32,164,24\n"
"buffer_load_dwordx4 v221, s[4:7], s63 offen offset:1024 lds\nv_mfma_f32_16x16x32_bf16 v[48:51], v[160:163], a[12:15], 0\ns_add_u32 m0, s51, 1024\ns_add_u32 s60, s70, 16640\nbuffer_load_dwordx4 v227, s[24:27], s60 offen lds\nM 48,164,28\n"
"s_add_u32 m0, s50, 65536\ns_add_u32 s63, s52, 397312\nbuffer_load_dwordx4 v220, s[4:7], s63 offen lds\nG 4\nv_mfma_f32_16x16x32_bf16 v[52:55], v[168:171], a[12:15], 0\nR 160,187,0\n"
"M 52,172,28\nbuffer_load_dwordx4 v221, s[4:7], s63 offen offset:1024 lds\nv_mfma_f32_16x16x32_bf16 v[36:39], v[168:171], a[8:11], 0\nR 162,187,4096\nM 36,172,24\ns_add_u32 m0, s51, 2048\n"
"s_add_u32 s60, s70, 33024\nbuffer_load_dwordx4 v226, s[24:27], s60 offen lds\nv_mfma_f32_16x16x32_bf16 v[20:23], v[168:171], a[4:7], 0\nR 164,187,8192\ns_add_u32 m0, s50, 73728\nM 20,172,20\n"
"s_lshl4_add_u32 s62, s41, s42\ns_lshl_b32 s62, s62, 12\ns_add_u32 s62, s62, 78643200\ns_lshl_b32 s60, s40, 10\ns_add_u32 s62, s62, s60\nbuffer_store_dwordx4 v[144:147], v252, s[20:23], s62 offen sc1\n"
"s_add_u32 s63, s52, 593920\nv_mfma_f32_16x16x32_bf16 v[4:7], v[168:171], a[0:3], 0\nR 166,187,12288\nbuffer_load_dwordx4 v220, s[4:7], s63 offen lds\nM 4,172,16\nG 4\n"
"v_mfma_f32_16x16x32_bf16 v[8:11], v[176:179], a[0:3], 0\nR 168,184,16384\nM 8,180,16\ns_add_u32 m0, s51, 3072\ns_add_u32 s60, s70, 49408\nbuffer_load_dwordx4 v227, s[24:27], s60 offen lds\n"
"v_mfma_f32_16x16x32_bf16 v[24:27], v[176:179], a[4:7], 0\nR 170,184,20480\ns_add_u32 m0, s50, 73728\ns_nop 0\nbuffer_load_dwordx4 v221, s[4:7], s63 offen offset:1024 lds\nM 24,180,20\n"
"s_add_u32 m0, s51, 4096\ns_add_u32 s60, s71, 256\nbuffer_load_dwordx4 v226, s[24:27], s60 offen lds\nv_mfma_f32_16x16x32_bf16 v[40:43], v[176:179], a[8:11], 0\nR 172,184,24576\nM 40,180,24\n"
"v_mfma_f32_16x16x32_bf16 v[56:59], v[176:179], a[12:15], 0\nR 174,184,28672\nM 56,180,28\nG 4\nv_mfma_f32_16x16x32_bf16 v[60:63], v[160:163], a[12:15], 0\nR 176,185,16384\n"
"M 60,164,28\ns_add_u32 m0, s51, 5120\ns_add_u32 s60, s71, 16640\nbuffer_load_dwordx4 v227, s[24:27], s60 offen lds\nv_mfma_f32_16x16x32_bf16 v[44:47], v[160:163], a[8:11], 0\nR 178,185,20480\n"
"M 44,164,24\ns_add_u32 m0, s51, 6144\ns_add_u32 s60, s71, 33024\nbuffer_load_dwordx4 v226, s[24:27], s60 offen lds\nv_mfma_f32_16x16x32_bf16 v[28:31], v[160:163], a[4:7], 0\nR 180,185,24576\n"
"s_add_u32 m0, s50, 81920\ns_add_u32 s63, s101, 0\nM 28,164,20\nbuffer_load_dwordx4 v220, s[4:7], s63 offen lds\nv_mfma_f32_16x16x32_bf16 v[12:15], v[160:163], a[0:3], 0\nR 182,185,28672\n"
"M 12,164,16\ns_waitcnt vmcnt(12)\ns_barrier\nG 4\nv_mfma_f32_16x16x32_bf16 v[64:67], a[0:3], v[168:171], 0\nR 160,186,16384\n"
"N 64,16,172\ns_add_u32 m0, s51, 7168\ns_add_u32 s60, s71, 49408\nbuffer_load_dwordx4 v227, s[24:27], s60 offen lds\nv_mfma_f32_16x16x32_bf16 v[68:71], a[4:7], v[168:171], 0\nR 162,186,20480\n"
"s_add_u32 m0, s50, 81920\nN 68,20,172\nbuffer_load_dwordx4 v221, s[4:7], s63 offen offset:1024 lds\nv_mfma_f32_16x16x32_bf16 v[96:99], a[8:11], v[168:171], 0\nR 164,186,24576\ns_add_u32 m0, s50, 0\n"
"s_add_u32 s63, s52, 788480\nN 96,24,172\nbuffer_load_dwordx4 v220, s[4:7], s63 offen lds\nv_mfma_f32_16x16x32_bf16 v[100:103], a[12:15], v[168:171], 0\nR 166,186,28672\nN 100,28,172\n"
"G 4\nv_mfma_f32_16x16x32_bf16 v[108:111], a[12:15], v[176:179], 0\nR 168,187,16384\nv_mov_b32 v242, s76\nv_mov_b32 v243, s76\nN 108,28,180\n"
"buffer_load_dwordx4 v221, s[4:7], s63 offen offset:1024 lds\nv_mov_b32 v244, s76\nv_mfma_f32_16x16x32_bf16 v[104:107], a[8:11], v[176:179], 0\nR 170,187,20480\ns_add_u32 s63, s52, 985088\nN 104,24,180\n"
"s_add_u32 m0, s50, 8192\ns_lshl_b32 s60, s40, 5\nv_xor_b32 v238, s60, v184\nv_mfma_f32_16x16x32_bf16 v[76:79], a[4:7], v[176:179], 0\nR 172,187,24576\nv_and_b32 v238, 0xfffffbff, v238\n"
"N 76,20,180\nbuffer_load_dwordx4 v220, s[4:7], s63 offen lds\nv_mfma_f32_16x16x32_bf16 v[72:75], a[0:3], v[176:179], 0\nR 174,187,28672\nv_mov_b32 v245, s76\nN 72,16,180\n"
"G 4\nv_mfma_f32_16x16x32_bf16 v[80:83], a[0:3], v[160:163], 0\nR 176,184,49152\nds_read_b128 a[32:35], v250 offset:0\nR 128,238,32768\nR 130,238,36864\n"
"N 80,16,164\nds_read_b128 a[36:39], v250 offset:2048\nv_mfma_f32_16x16x32_bf16 v[84:87], a[4:7], v[160:163], 0\nR 178,184,53248\nds_read_b128 a[40:43], v250 offset:4096\nR 132,238,33792\n"
"R 134,238,37888\nN 84,20,164\nds_read_b128 a[44:47], v250 offset:6144\nv_mfma_f32_16x16x32_bf16 v[112:115], a[8:11], v[160:163], 0\nR 180,184,57344\nbuffer_load_dwordx4 v221, s[4:7], s63 offen offset:1024 lds\n"
"N 112,24,164\nds_read_b128 a[48:51], v251 offset:0\nv_mfma_f32_16x16x32_bf16 v[116:119], a[12:15], v[160:163], 0\nR 182,184,61440\nds_read_b128 a[52:55], v251 offset:2048\nN 116,28,164\n"
"ds_read_b128 a[56:59], v251 offset:4096\nG 15\nv_mfma_f32_16x16x32_bf16 v[124:127], a[12:15], v[168:171], 0\nR 160,185,49152\nG 12\nv_mfma_f32_16x16x32_bf16 v[136:139], v[242:245], v[128:131], 0\n"
"N 124,28,172\nds_read_b128 a[60:63], v251 offset:6144\nG 8\nv_mfma_f32_16x16x32_bf16 v[136:139], v[242:245], v[132:135], v[136:139]\nv_mfma_f32_16x16x32_bf16 v[120:123], a[8:11], v[168:171], 0\nR 162,185,53248\n"
"N 120,24,172\nv_mfma_f32_16x16x32_bf16 v[92:95], a[4:7], v[168:171], 0\nR 164,185,57344\nN 92,20,172\nG 2\ns_add_u32 m0, s51, 122880\n"
"s_add_u32 s60, s70, 384\nbuffer_load_dwordx4 v226, s[24:27], s60 offen lds\nv_mfma_f32_16x16x32_bf16 v[88:91], a[0:3], v[168:171], 0\nR 166,185,61440\nv_lshrrev_b32 v128, 1, v252\nv_mov_b32 v137, s46\n"
"N 88,16,172\ns_waitcnt vmcnt(11)\ns_barrier\nM 0,176,32\nR 168,186,49152\ns_lshl_b32 s60, s40, 5\n"
"v_xor_b32 v238, s60, v184\nM 0,180,48\ns_add_u32 m0, s51, 123904\ns_add_u32 s60, s70, 16768\nbuffer_load_dwordx4 v227, s[24:27], s60 offen lds\ns_and_b32 s61, s40, 3\n"
"s_lshl_b32 s61, s61, 7\ns_add_u32 s61, s61, s91\ns_add_u32 s61, s61, 131072\ns_mov_b64 exec, 0xffff\nbuffer_store_dwordx2 v[136:137], v128, s[20:23], s61 offen sc1\ns_mov_b64 exec, -1\n"
"M 16,176,36\nR 170,186,53248\ns_add_u32 m0, s50, 16384\ns_add_u32 s63, s52, 790528\nM 16,180,52\nbuffer_load_dwordx4 v220, s[4:7], s63 offen lds\n"
"M 32,176,40\nR 172,186,57344\nv_and_b32 v238, 0xfffffbff, v238\nM 32,180,56\ns_add_u32 m0, s51, 124928\ns_add_u32 s60, s70, 33152\n"
"buffer_load_dwordx4 v226, s[24:27], s60 offen lds\nM 48,176,44\nR 174,186,61440\ns_add_u32 m0, s50, 16384\nv_mov_b32 v230, 0\nM 48,180,60\n"
"Lpg1:\ns_cbranch_execz Lpg5\nbuffer_load_dwordx4 v221, s[4:7], s63 offen offset:1024 lds\nG 4\nv_mov_b32 v231, 0\nM 52,160,44\n"
"R 176,187,49152\nR 128,238,40960\nR 130,238,45056\nv_mov_b32 v232, 0\nM 52,164,60\ns_add_u32 m0, s51, 125952\n"
"s_add_u32 s60, s70, 49536\nbuffer_load_dwordx4 v227, s[24:27], s60 offen lds\nv_mov_b32 v233, 0\nM 36,160,40\nR 178,187,53248\nR 132,238,41984\n"
"R 134,238,46080\ns_add_u32 m0, s50, 24576\ns_add_u32 s63, s52, 987136\nv_dot2c_f32_bf16 v230, s76, v192\nM 36,164,56\nbuffer_load_dwordx4 v220, s[4:7], s63 offen lds\n"
"v_dot2c_f32_bf16 v231, v192, v192\nM 20,160,36\nR 180,187,57344\nv_lshrrev_b32 v234, 1, v252\nv_dot2c_f32_bf16 v232, s76, v200\nM 20,164,52\n"
"s_add_u32 m0, s51, 126976\ns_add_u32 s60, s71, 384\nbuffer_load_dwordx4 v226, s[24:27], s60 offen lds\nv_dot2c_f32_bf16 v233, v200, v200\nM 4,160,32\nR 182,187,61440\n"
"s_add_u32 m0, s50, 24576\nv_dot2c_f32_bf16 v230, s76, v193\nM 4,164,48\nG 8\nv_dot2c_f32_bf16 v231, v193, v193\nM 8,168,32\n"
"R 160,188,0\nG 6\nv_mfma_f32_16x16x32_bf16 v[136:139], v[242:245], v[128:131], 0\nbuffer_load_dwordx4 v221, s[4:7], s63 offen offset:1024 lds\nv_dot2c_f32_bf16 v232, s76, v201\nM 8,172,48\n"
"s_add_u32 m0, s51, 128000\ns_add_u32 s60, s71, 16768\nbuffer_load_dwordx4 v227, s[24:27], s60 offen lds\nG 3\nv_mfma_f32_16x16x32_bf16 v[136:139], v[242:245], v[132:135], v[136:139]\nv_dot2c_f32_bf16 v233, v201, v201\n"
"M 24,168,36\nR 162,188,4096\nv_dot2c_f32_bf16 v230, s76, v194\nM 24,172,52\nv_dot2c_f32_bf16 v231, v194, v194\nM 40,168,40\n"
"R 164,188,8192\nv_dot2c_f32_bf16 v232, s76, v202\nM 40,172,56\ns_add_u32 m0, s51, 129024\ns_add_u32 s60, s71, 33152\nbuffer_load_dwordx4 v226, s[24:27], s60 offen lds\n"
"v_dot2c_f32_bf16 v233, v202, v202\nM 56,168,44\nR 166,188,12288\nv_lshrrev_b32 v128, 1, v252\nv_dot2c_f32_bf16 v230, s76, v195\nM 56,172,60\n"
"G 4\nv_mov_b32 v137, s46\nv_dot2c_f32_bf16 v231, v195, v195\nM 60,176,44\nR 168,189,0\nv_dot2c_f32_bf16 v232, s76, v203\n"
"M 60,180,60\ns_add_u32 m0, s51, 130048\ns_add_u32 s60, s71, 49536\nbuffer_load_dwordx4 v227, s[24:27], s60 offen lds\ns_and_b32 s61, s40, 3\ns_lshl_b32 s61, s61, 7\n"
"s_add_u32 s61, s61, s91\ns_add_u32 s61, s61, 262144\ns_mov_b64 exec, 0xffff\nbuffer_store_dwordx2 v[136:137], v128, s[20:23], s61 offen sc1\ns_mov_b64 exec, -1\nv_dot2c_f32_bf16 v233, v203, v203\n"
"M 44,176,40\nR 170,189,4096\nv_dot2c_f32_bf16 v230, s76, v196\nM 44,180,56\nv_dot2c_f32_bf16 v231, v196, v196\nM 28,176,36\n"
"R 172,189,8192\nv_dot2c_f32_bf16 v232, s76, v204\nM 28,180,52\ns_add_u32 m0, s50, 32768\nv_dot2c_f32_bf16 v233, v204, v204\nM 12,176,32\n"
"R 174,189,12288\nv_dot2c_f32_bf16 v230, s76, v197\nM 12,180,48\ns_waitcnt vmcnt(14)\ns_barrier\nG 4\n"
"v_dot2c_f32_bf16 v231, v197, v197\nN 64,32,160\nR 176,190,0\nv_dot2c_f32_bf16 v232, s76, v205\nN 64,48,164\ns_add_u32 s63, s52, 1181696\n"
"v_dot2c_f32_bf16 v233, v205, v205\nN 68,36,160\nR 178,190,4096\nv_dot2c_f32_bf16 v230, s76, v198\nN 68,52,164\nbuffer_load_dwordx4 v220, s[4:7], s63 offen lds\n"
"v_dot2c_f32_bf16 v231, v198, v198\nN 96,40,160\nR 180,190,8192\nds_read_b128 a[64:67], v248 offset:0\nv_dot2c_f32_bf16 v232, s76, v206\nN 96,56,164\n"
"ds_read_b128 a[68:71], v248 offset:2048\nv_dot2c_f32_bf16 v233, v206, v206\nN 100,44,160\nR 182,190,12288\nds_read_b128 a[72:75], v248 offset:4096\nv_dot2c_f32_bf16 v230, s76, v199\n"
"N 100,60,164\nds_read_b128 a[76:79], v248 offset:6144\nG 8\nv_dot2c_f32_bf16 v231, v199, v199\nN 108,44,168\nR 160,191,0\n"
"s_nop 0\nv_permlane32_swap_b32 v230, v231\nv_dot2c_f32_bf16 v232, s76, v207\nN 108,60,172\nds_read_b128 a[80:83], v249 offset:0\nbuffer_load_dwordx4 v221, s[4:7], s63 offen offset:1024 lds\n"
"v_dot2c_f32_bf16 v233, v207, v207\nN 104,40,168\nR 162,191,4096\nds_read_b128 a[84:87], v249 offset:2048\nN 104,56,172\nds_read_b128 a[88:91], v249 offset:4096\n"
"s_add_u32 m0, s50, 40960\nN 76,36,168\nR 164,191,8192\ns_add_u32 s63, s52, 1378304\nN 76,52,172\nds_read_b128 a[92:95], v249 offset:6144\n"
"v_permlane32_swap_b32 v232, v233\nN 72,32,168\nR 166,191,12288\nbuffer_load_dwordx4 v220, s[4:7], s63 offen lds\nN 72,48,172\nG 10\n"
"v_add_f32 v230, v230, v231\nv_add_f32 v232, v232, v233\nN 80,32,176\nR 168,184,0\nv_mov_b32 v231, s46\nN 80,48,180\n"
"G 2\ns_add_u32 m0, s51, 0\ns_add_u32 s60, s70, 512\nbuffer_load_dwordx4 v226, s[24:27], s60 offen lds\nN 84,36,176\nR 170,184,4096\n"
"s_add_u32 m0, s50, 40960\ns_nop 0\nbuffer_load_dwordx4 v221, s[4:7], s63 offen offset:1024 lds\nN 84,52,180\nv_permlane16_swap_b32 v230, v232\nv_add_f32 v230, v230, v232\n"
"N 112,40,176\nR 172,184,8192\nN 112,56,180\ns_add_u32 m0, s51, 1024\ns_add_u32 s60, s70, 16896\nbuffer_load_dwordx4 v227, s[24:27], s60 offen lds\n"
"N 116,44,176\nR 174,184,12288\nN 116,60,180\nG 4\nN 124,44,160\nR 176,185,0\n"
"N 124,60,164\ns_add_u32 m0, s51, 2048\ns_add_u32 s60, s70, 33280\nbuffer_load_dwordx4 v226, s[24:27], s60 offen lds\nN 120,40,160\nR 178,185,4096\n"
"N 120,56,164\ns_lshl_b32 s60, s40, 5\nv_xor_b32 v238, s60, v188\ns_lshr_b32 s61, s49, 3\ns_lshl2_add_u32 s61, s53, s61\ns_add_u32 s61, s61, 81788928\n"
"buffer_store_dwordx2 v[230:231], v234, s[20:23], s61 offen sc1\nv_and_b32 v238, 0xfffffbff, v238\nN 92,36,160\nR 180,185,8192\nN 92,52,164\ns_add_u32 m0, s51, 3072\n"
"s_add_u32 s60, s70, 49664\nbuffer_load_dwordx4 v227, s[24:27], s60 offen lds\nN 88,32,160\nR 182,185,12288\nN 88,48,164\ns_waitcnt vmcnt(13)\n"
"s_barrier\nG 4\nM 0,168,64\nR 160,186,0\nM 0,172,80\ns_add_u32 m0, s51, 4096\n"
"s_add_u32 s60, s71, 512\nbuffer_load_dwordx4 v226, s[24:27], s60 offen lds\nM 16,168,68\nR 162,186,4096\nR 128,238,16384\nR 130,238,20480\n"
"s_add_u32 m0, s50, 49152\ns_add_u32 s63, s52, 1183744\nM 16,172,84\nbuffer_load_dwordx4 v220, s[4:7], s63 offen lds\nM 32,168,72\nR 164,186,8192\n"
"R 132,238,17408\nR 134,238,21504\nM 32,172,88\ns_add_u32 m0, s51, 5120\ns_add_u32 s60, s71, 16896\nbuffer_load_dwordx4 v227, s[24:27], s60 offen lds\n"
"M 48,168,76\nR 166,186,12288\ns_add_u32 m0, s50, 49152\nM 48,172,92\nG 8\nM 52,176,76\n"
"R 168,187,0\nbuffer_load_dwordx4 v221, s[4:7], s63 offen offset:1024 lds\nM 52,180,92\ns_add_u32 m0, s51, 6144\ns_add_u32 s60, s71, 33280\nbuffer_load_dwordx4 v226, s[24:27], s60 offen lds\n"
"M 36,176,72\nR 170,187,4096\nG 6\nv_mfma_f32_16x16x32_bf16 v[136:139], v[242:245], v[128:131], 0\ns_add_u32 m0, s50, 57344\ns_add_u32 s63, s52, 1380352\n"
"M 36,180,88\nG 3\nv_mfma_f32_16x16x32_bf16 v[136:139], v[242:245], v[132:135], v[136:139]\nbuffer_load_dwordx4 v220, s[4:7], s63 offen lds\nM 20,176,68\nR 172,187,8192\n"
"M 20,180,84\ns_add_u32 m0, s51, 7168\ns_add_u32 s60, s71, 49664\nbuffer_load_dwordx4 v227, s[24:27], s60 offen lds\nM 4,176,64\nR 174,187,12288\n"
"s_add_u32 m0, s50, 57344\nM 4,180,80\nG 4\nM 8,160,64\nR 176,184,16384\nbuffer_load_dwordx4 v221, s[4:7], s63 offen offset:1024 lds\n"
"M 8,164,80\nv_lshrrev_b32 v128, 1, v252\nv_mov_b32 v137, s46\nM 24,160,68\nR 178,184,20480\nM 24,164,84\n"
"s_and_b32 s61, s40, 3\ns_lshl_b32 s61, s61, 7\ns_add_u32 s61, s61, s91\ns_add_u32 s61, s61, 0\ns_mov_b64 exec, 0xffff\nbuffer_store_dwordx2 v[136:137], v128, s[20:23], s61 offen sc1\n"
"s_mov_b64 exec, -1\nM 40,160,72\nR 180,184,24576\nM 40,164,88\nM 56,160,76\nR 182,184,28672\n"
"M 56,164,92\nG 4\nM 60,168,76\nR 160,185,16384\nM 60,172,92\ns_add_u32 m0, s50, 65536\n"
"M 44,168,72\nR 162,185,20480\nM 44,172,88\ns_add_u32 s63, s52, 1574912\nM 28,168,68\nR 164,185,24576\n"
"M 28,172,84\nbuffer_load_dwordx4 v220, s[4:7], s63 offen lds\nM 12,168,64\nR 166,185,28672\nM 12,172,80\ns_waitcnt vmcnt(14)\n"
"s_barrier\nG 4\nN 64,64,176\nR 168,186,16384\nN 64,80,180\nv_lshrrev_b32 v230, 7, v252\n"
"N 68,68,176\nR 170,186,20480\nN 68,84,180\nbuffer_load_dwordx4 v221, s[4:7], s63 offen offset:1024 lds\nv_lshlrev_b32 v230, 9, v230\nN 96,72,176\n"
"R 172,186,24576\ns_add_u32 m0, s50, 73728\nN 96,88,180\nds_read_b128 a[96:99], v250 offset:0\nN 100,76,176\nR 174,186,28672\n"
"ds_read_b128 a[100:103], v250 offset:2048\ns_add_u32 s63, s52, 1771520\nN 100,92,180\nds_read_b128 a[104:107], v250 offset:4096\nG 7\nN 108,76,160\n"
"R 176,187,16384\nv_and_b32 v231, 0x70, v252\nN 108,92,164\nds_read_b128 a[108:111], v250 offset:6144\nbuffer_load_dwordx4 v220, s[4:7], s63 offen lds\nN 104,72,160\n"
"R 178,187,20480\nds_read_b128 a[112:115], v251 offset:0\nN 104,88,164\nds_read_b128 a[116:119], v251 offset:2048\ns_and_b32 s60, s40, 3\ns_lshl_b32 s60, s60, 7\n"
"v_add3_u32 v230, v230, v231, s60\nN 76,68,160\nR 180,187,24576\nds_read_b128 a[120:123], v251 offset:4096\nN 76,84,164\nds_read_b128 a[124:127], v251 offset:6144\n"
"N 72,64,160\nR 182,187,28672\nN 72,80,164\ns_lshl_b32 s61, s42, 13\ns_add_u32 s61, s61, 84017152\nbuffer_load_dwordx4 v[128:131], v230, s[20:23], s61 offen sc1\n"
"G 11\nN 80,64,168\nR 160,184,32768\nbuffer_load_dwordx4 v221, s[4:7], s63 offen offset:1024 lds\nN 80,80,172\ns_lshl_b32 s61, s42, 13\n"
"s_add_u32 s61, s61, 84021248\nbuffer_load_dwordx4 v[132:135], v230, s[20:23], s61 offen sc1\nN 84,68,168\nR 162,184,36864\nN 84,84,172\nN 112,72,168\n"
"R 164,184,40960\ns_waitcnt vmcnt(8)\nds_read_b128 a[128:131], v248 offset:0\nN 112,88,172\nds_read_b128 a[132:135], v248 offset:2048\nN 116,76,168\n"
"R 166,184,45056\nds_read_b128 a[136:139], v248 offset:4096\nN 116,92,172\nds_read_b128 a[140:143], v248 offset:6144\nG 8\nN 124,76,176\n"
"R 168,185,32768\nN 124,92,180\nds_read_b128 a[144:147], v249 offset:0\nN 120,72,176\nR 170,185,36864\nds_read_b128 a[148:151], v249 offset:2048\n"
"N 120,88,180\nds_read_b128 a[152:155], v249 offset:4096\nN 92,68,176\nR 172,185,40960\nN 92,84,180\nds_read_b128 a[156:159], v249 offset:6144\n"
"N 88,64,176\nR 174,185,45056\nN 88,80,180\ns_waitcnt vmcnt(7)\ns_barrier\nG 10\n"
"M 0,160,96\nR 176,186,32768\nM 0,164,112\nG 2\ns_add_u32 m0, s51, 0\ns_add_u32 s60, s70, 640\n"
"buffer_load_dwordx4 v226, s[24:27], s60 offen lds\nM 16,160,100\nR 178,186,36864\nM 16,164,116\ns_add_u32 m0, s51, 1024\ns_add_u32 s60, s70, 17024\n"
"buffer_load_dwordx4 v227, s[24:27], s60 offen lds\nM 32,160,104\nR 180,186,40960\ns_add_u32 m0, s50, 81920\ns_add_u32 s63, s52, 1576960\nbuffer_load_dwordx4 v220, s[4:7], s63 offen lds\n"
"v_lshrrev_b32 v234, 7, v252\nM 32,164,120\ns_add_u32 m0, s51, 2048\ns_add_u32 s60, s70, 33408\nbuffer_load_dwordx4 v226, s[24:27], s60 offen lds\nM 48,160,108\n"
"R 182,186,45056\nM 48,164,124\ns_add_u32 m0, s51, 3072\ns_add_u32 s60, s70, 49792\nbuffer_load_dwordx4 v227, s[24:27], s60 offen lds\nG 4\n"
"M 52,168,108\nR 160,187,32768\ns_add_u32 m0, s50, 81920\ns_nop 0\nbuffer_load_dwordx4 v221, s[4:7], s63 offen offset:1024 lds\nv_lshlrev_b32 v234, 9, v234\n"
"M 52,172,124\ns_add_u32 m0, s51, 4096\ns_add_u32 s60, s71, 640\nbuffer_load_dwordx4 v226, s[24:27], s60 offen lds\nM 36,168,104\nR 162,187,36864\n"
"v_and_b32 v235, 0x70, v252\nM 36,172,120\ns_add_u32 m0, s51, 5120\ns_add_u32 s60, s71, 17024\nbuffer_load_dwordx4 v227, s[24:27], s60 offen lds\nM 20,168,100\n"
"R 164,187,40960\ns_add_u32 m0, s50, 90112\ns_add_u32 s63, s52, 1773568\nbuffer_load_dwordx4 v220, s[4:7], s63 offen lds\nM 20,172,116\ns_add_u32 m0, s51, 6144\n"
"s_add_u32 s60, s71, 33408\nbuffer_load_dwordx4 v226, s[24:27], s60 offen lds\nM 4,168,96\nR 166,187,45056\ns_and_b32 s60, s40, 3\ns_lshl_b32 s60, s60, 7\n"
"v_add3_u32 v234, v234, v235, s60\nM 4,172,112\ns_add_u32 m0, s51, 7168\ns_add_u32 s60, s71, 49792\nbuffer_load_dwordx4 v227, s[24:27], s60 offen lds\nG 4\n"
"M 8,176,96\nR 168,184,49152\ns_add_u32 m0, s50, 90112\ns_nop 0\nbuffer_load_dwordx4 v221, s[4:7], s63 offen offset:1024 lds\nM 8,180,112\n"
"s_lshl_b32 s61, s42, 13\ns_add_u32 s61, s61, 84148224\nbuffer_load_dwordx4 v[144:147], v234, s[20:23], s61 offen sc1\nM 24,176,100\nR 170,184,53248\nM 24,180,116\n"
"M 40,176,104\nR 172,184,57344\nM 40,180,120\ns_lshl_b32 s61, s42, 13\ns_add_u32 s61, s61, 84152320\nbuffer_load_dwordx4 v[148:151], v234, s[20:23], s61 offen sc1\n"
"M 56,176,108\nR 174,184,61440\nM 56,180,124\nG 4\nM 60,160,108\nR 176,185,49152\n"
"M 60,164,124\nM 44,160,104\nR 178,185,53248\nM 44,164,120\ns_add_u32 m0, s50, 98304\nM 28,160,100\n"
"R 180,185,57344\nM 28,164,116\ns_add_u32 s63, s52, 1968128\nM 12,160,96\nR 182,185,61440\nM 12,164,112\n"
"s_waitcnt vmcnt(15)\ns_barrier\nG 4\nN 64,96,168\nR 160,186,49152\ns_waitcnt vmcnt(14)\n"
"v_min3_u32 v232, v129, v131, v133\nv_min3_u32 v232, v232, v135, v135\nN 64,112,172\nbuffer_load_dwordx4 v220, s[4:7], s63 offen lds\nv_max3_u32 v233, v129, v131, v133\nN 68,100,168\n"
"R 162,186,53248\nv_max3_u32 v233, v233, v135, v135\nv_cmp_ne_u32 s[86:87], s46, v232\nN 68,116,172\nv_cmp_ne_u32 s[88:89], s46, v233\nN 96,104,168\n"
"R 164,186,57344\ns_or_b64 s[86:87], s[86:87], s[88:89]\ns_cbranch_scc1 Lq116\nLq117:\nN 96,120,172\nbuffer_load_dwordx4 v221, s[4:7], s63 offen offset:1024 lds\n"
"v_add_f32 v128, v128, v132\nN 100,108,168\nR 166,186,61440\ns_add_u32 m0, s50, 106496\nv_add_f32 v130, v130, v134\nN 100,124,172\n"
"G 4\nv_add_f32_dpp v128, v128, v128 row_ror:8 row_mask:0xf bank_mask:0xf\nv_add_f32_dpp v130, v130, v130 row_ror:8 row_mask:0xf bank_mask:0xf\nv_mov_b32 v132, v128\nN 108,108,176\nR 168,187,49152\n"
"s_add_u32 s63, s52, 2164736\nv_mov_b32 v133, v130\nN 108,124,180\nbuffer_load_dwordx4 v220, s[4:7], s63 offen lds\nv_permlane32_swap_b32 v132, v128\nN 104,104,176\n"
"R 170,187,53248\nv_permlane32_swap_b32 v133, v130\nv_add_f32 v128, v128, v132\nN 104,120,180\nv_add_f32 v130, v130, v133\nv_mov_b32 v132, v128\n"
"v_mov_b32 v133, v130\nN 76,100,176\nR 172,187,57344\nv_permlane16_swap_b32 v132, v128\nv_permlane16_swap_b32 v133, v130\nN 76,116,180\n"
"buffer_load_dwordx4 v221, s[4:7], s63 offen offset:1024 lds\nv_add_f32 v128, v128, v132\nN 72,96,176\nR 174,187,61440\ns_add_u32 s63, s52, 1970176\nN 72,112,180\n"
"G 4\nv_add_f32 v130, v130, v133\nv_and_b32 v136, s77, v128\nN 80,96,160\nR 176,188,0\nv_sub_f32 v137, v128, v136\n"
"N 80,112,164\ns_add_u32 m0, s50, 114688\nv_cvt_pk_bf16_f32 v132, v136, v137\nv_cvt_pk_bf16_f32 v133, v136, 0\nN 84,100,160\nR 178,188,4096\n"
"v_and_b32 v136, s77, v130\nN 84,116,164\nbuffer_load_dwordx4 v220, s[4:7], s63 offen lds\nN 112,104,160\nR 180,188,8192\nv_sub_f32 v137, v130, v136\n"
"N 112,120,164\nLpg2:\ns_cbranch_execz LretQ0\nv_cvt_pk_bf16_f32 v134, v136, v137\nv_cvt_pk_bf16_f32 v135, v136, 0\nN 116,108,160\n"
"R 182,188,12288\nv_and_b32 v230, 0x1ff, v230\nN 116,124,164\nv_add_u32 v230, 156160, v230\nds_write_b128 v230, v[132:135]\nG 5\n"
"N 124,108,168\nR 160,189,0\nN 124,124,172\nbuffer_load_dwordx4 v221, s[4:7], s63 offen offset:1024 lds\nN 120,104,168\nR 162,189,4096\n"
"s_add_u32 s63, s52, 2166784\nN 120,120,172\ns_add_u32 m0, s50, 0\nN 92,100,168\nR 164,189,8192\nN 92,116,172\n"
"buffer_load_dwordx4 v220, s[4:7], s63 offen lds\nN 88,96,168\nR 166,189,12288\nN 88,112,172\ns_waitcnt vmcnt(10)\nds_read_b128 a[160:163], v248 offset:0\n"
"s_waitcnt vmcnt(9) lgkmcnt(5)\ns_barrier\nM 0,176,128\nR 168,190,0\nds_read_b128 a[164:167], v248 offset:2048\nM 0,180,144\n"
"ds_read_b128 a[168:171], v248 offset:4096\nM 16,176,132\nR 170,190,4096\ns_waitcnt vmcnt(7)\nv_min3_u32 v236, v145, v147, v149\nv_min3_u32 v236, v236, v151, v151\n"
"M 16,180,148\nds_read_b128 a[172:175], v248 offset:6144\nv_max3_u32 v237, v145, v147, v149\nv_max3_u32 v237, v237, v151, v151\nM 32,176,136\nR 172,190,8192\n"
"ds_read_b128 a[176:179], v249 offset:0\nv_cmp_ne_u32 s[86:87], s46, v236\nM 32,180,152\nds_read_b128 a[180:183], v249 offset:2048\nv_lshrrev_b32 v230, 7, v252\nv_cmp_ne_u32 s[88:89], s46, v237\n"
"M 48,176,140\nR 174,190,12288\nds_read_b128 a[184:187], v249 offset:4096\ns_or_b64 s[86:87], s[86:87], s[88:89]\ns_cbranch_scc1 Lq124\nLq125:\n"
"M 48,180,156\nds_read_b128 a[188:191], v249 offset:6144\nG 12\nv_add_f32 v144, v144, v148\nM 52,160,140\nR 176,191,0\n"
"v_add_f32 v146, v146, v150\nv_add_f32_dpp v144, v144, v144 row_ror:8 row_mask:0xf bank_mask:0xf\nM 52,164,156\nbuffer_load_dwordx4 v221, s[4:7], s63 offen offset:1024 lds\nv_lshlrev_b32 v230, 9, v230\nv_add_f32_dpp v146, v146, v146 row_ror:8 row_mask:0xf bank_mask:0xf\n"
"M 36,160,136\nR 178,191,4096\nv_and_b32 v231, 0x70, v252\nv_mov_b32 v148, v144\nM 36,164,152\nG 2\n"
"s_add_u32 m0, s51, 0\ns_add_u32 s60, s70, 768\nbuffer_load_dwordx4 v226, s[24:27], s60 offen lds\nv_mov_b32 v149, v146\nM 20,160,132\nR 180,191,8192\n"
"v_permlane32_swap_b32 v148, v144\nv_permlane32_swap_b32 v149, v146\nM 20,164,148\ns_add_u32 m0, s51, 1024\ns_add_u32 s60, s70, 17152\nbuffer_load_dwordx4 v227, s[24:27], s60 offen lds\n"
"v_add_f32 v144, v144, v148\nM 4,160,128\nR 182,191,12288\ns_and_b32 s60, s40, 3\ns_lshl_b32 s60, s60, 7\nv_add3_u32 v230, v230, v231, s60\n"
"v_add_f32 v146, v146, v149\nM 4,164,144\ns_add_u32 m0, s51, 2048\ns_add_u32 s60, s70, 33536\nbuffer_load_dwordx4 v226, s[24:27], s60 offen lds\nv_mov_b32 v148, v144\n"
"M 8,168,128\nR 160,188,16384\nv_mov_b32 v149, v146\nv_permlane16_swap_b32 v148, v144\nM 8,172,144\ns_add_u32 m0, s51, 3072\n"
"s_add_u32 s60, s70, 49920\nbuffer_load_dwordx4 v227, s[24:27], s60 offen lds\nv_permlane16_swap_b32 v149, v146\nM 24,168,132\nR 162,188,20480\ns_lshl_b32 s61, s42, 13\n"
"s_add_u32 s61, s61, 83886080\nbuffer_load_dwordx4 v[128:131], v230, s[20:23], s61 offen sc1\nv_add_f32 v144, v144, v148\nM 24,172,148\ns_add_u32 m0, s51, 4096\ns_add_u32 s60, s71, 768\n"
"buffer_load_dwordx4 v226, s[24:27], s60 offen lds\nv_add_f32 v146, v146, v149\nM 40,168,136\nR 164,188,24576\ns_lshl_b32 s61, s42, 13\ns_add_u32 s61, s61, 83890176\n"
"buffer_load_dwordx4 v[132:135], v230, s[20:23], s61 offen sc1\nv_and_b32 v152, s77, v144\nM 40,172,152\ns_add_u32 m0, s51, 5120\ns_add_u32 s60, s71, 17152\nbuffer_load_dwordx4 v227, s[24:27], s60 offen lds\n"
"M 56,168,140\nR 166,188,28672\nv_sub_f32 v153, v144, v152\nM 56,172,156\ns_add_u32 m0, s51, 6144\ns_add_u32 s60, s71, 33536\n"
"buffer_load_dwordx4 v226, s[24:27], s60 offen lds\nG 4\nM 60,176,140\nR 168,189,16384\nv_cvt_pk_bf16_f32 v148, v152, v153\nM 60,180,156\n"
"s_add_u32 m0, s51, 7168\ns_add_u32 s60, s71, 49920\nbuffer_load_dwordx4 v227, s[24:27], s60 offen lds\nM 44,176,136\nR 170,189,20480\nv_cvt_pk_bf16_f32 v149, v152, 0\n"
"M 44,180,152\ns_add_u32 m0, s50, 8192\nv_and_b32 v152, s77, v146\nv_sub_f32 v153, v146, v152\nM 28,176,132\nR 172,189,24576\n"
"v_cvt_pk_bf16_f32 v150, v152, v153\nM 28,180,148\ns_add_u32 s63, s52, 2361344\nv_cvt_pk_bf16_f32 v151, v152, 0\nv_and_b32 v234, 0x1ff, v234\nM 12,176,128\n"
"R 174,189,28672\nv_add_u32 v234, 156672, v234\nM 12,180,144\ns_waitcnt vmcnt(14)\ns_barrier\nG 4\n"
"N 64,128,160\nR 176,190,16384\nds_write_b128 v234, v[148:151]\nN 64,144,164\nbuffer_load_dwordx4 v220, s[4:7], s63 offen lds\nN 68,132,160\n"
"R 178,190,20480\nN 68,148,164\nN 96,136,160\nR 180,190,24576\nN 96,152,164\nbuffer_load_dwordx4 v221, s[4:7], s63 offen offset:1024 lds\n"
"N 100,140,160\nR 182,190,28672\ns_add_u32 m0, s50, 16384\nN 100,156,164\nG 5\nN 108,140,168\n"
"R 160,191,16384\ns_add_u32 s63, s52, 2557952\nN 108,156,172\nbuffer_load_dwordx4 v220, s[4:7], s63 offen lds\nN 104,136,168\nR 162,191,20480\n"
"N 104,152,172\nN 76,132,168\nR 164,191,24576\nLq189:\ns_lshr_b32 s60, s45, 3\ns_lshl2_add_u32 s60, s60, s40\n"
"s_lshl_b32 s60, s60, 8\nv_lshl_add_u32 v224, v252, 3, s60\nv_add_u32 v224, Lq189+4096-LqPC, v224\nv_min_u32 v224, LqE-LqPC-4, v224\ns_mov_b64 exec, 3\nglobal_load_dword v224, v224, s[98:99]\n"
"s_mov_b64 exec, -1\nN 76,148,172\nbuffer_load_dwordx4 v221, s[4:7], s63 offen offset:1024 lds\nN 72,128,168\nR 166,191,28672\nN 72,144,172\n"
"G 4\nN 80,128,176\nR 168,188,32768\nN 80,144,180\nN 84,132,176\nR 170,188,36864\n"
"N 84,148,180\ns_add_u32 m0, s50, 24576\nN 112,136,176\nR 172,188,40960\nN 112,152,180\ns_add_u32 s63, s52, 2754560\n"
"N 116,140,176\nR 174,188,45056\nN 116,156,180\nG 4\nN 124,140,160\nR 176,189,32768\n"
"N 124,156,164\nbuffer_load_dwordx4 v220, s[4:7], s63 offen lds\nN 120,136,160\nR 178,189,36864\nN 120,152,164\nN 92,132,160\n"
"R 180,189,40960\ns_waitcnt vmcnt(9)\nv_min3_u32 v232, v129, v131, v133\nv_min3_u32 v232, v232, v135, v135\nN 92,148,164\nbuffer_load_dwordx4 v221, s[4:7], s63 offen offset:1024 lds\n"
"v_max3_u32 v233, v129, v131, v133\nN 88,128,160\nR 182,189,45056\ns_add_u32 m0, s50, 32768\nv_max3_u32 v233, v233, v135, v135\nN 88,144,164\n"
"s_barrier\nG 4\nv_cmp_ne_u32 s[86:87], s46, v232\nM 0,168,160\nR 160,190,32768\ns_add_u32 s63, s52, 2951168\n"
"v_cmp_ne_u32 s[88:89], s46, v233\nM 0,172,176\nbuffer_load_dwordx4 v220, s[4:7], s63 offen lds\ns_or_b64 s[86:87], s[86:87], s[88:89]\ns_cbranch_scc1 Lq132\nLq133:\n"
"M 16,168,164\nR 162,190,36864\nv_add_f32 v128, v128, v132\nv_add_f32 v130, v130, v134\nM 16,172,180\nv_add_f32_dpp v128, v128, v128 row_ror:8 row_mask:0xf bank_mask:0xf\n"
"v_add_f32_dpp v130, v130, v130 row_ror:8 row_mask:0xf bank_mask:0xf\nv_mov_b32 v132, v128\nM 32,168,168\nR 164,190,40960\nv_mov_b32 v133, v130\nv_permlane32_swap_b32 v132, v128\n"
"M 32,172,184\nv_permlane32_swap_b32 v133, v130\nv_add_f32 v128, v128, v132\nv_add_f32 v130, v130, v133\nM 48,168,172\nR 166,190,45056\n"
"v_mov_b32 v132, v128\nv_mov_b32 v133, v130\nM 48,172,188\nG 4\nv_permlane16_swap_b32 v132, v128\nv_permlane16_swap_b32 v133, v130\n"
"v_add_f32 v128, v128, v132\nM 52,176,172\nR 168,191,32768\nv_add_f32 v130, v130, v133\nv_and_b32 v136, s77, v128\nM 52,180,188\n"
"buffer_load_dwordx4 v221, s[4:7], s63 offen offset:1024 lds\nv_sub_f32 v137, v128, v136\nM 36,176,168\nR 170,191,36864\ns_waitcnt vmcnt(9)\nds_read_b128 a[192:195], v248 offset:0\n"
"v_cvt_pk_bf16_f32 v132, v136, v137\nM 36,180,184\nds_read_b128 a[196:199], v248 offset:2048\nv_cvt_pk_bf16_f32 v133, v136, 0\nv_and_b32 v136, s77, v130\nM 20,176,164\n"
"R 172,191,40960\nds_read_b128 a[200:203], v248 offset:4096\nv_sub_f32 v137, v130, v136\nM 20,180,180\nds_read_b128 a[204:207], v248 offset:6144\nv_cvt_pk_bf16_f32 v134, v136, v137\n"
"v_cvt_pk_bf16_f32 v135, v136, 0\nM 4,176,160\nR 174,191,45056\nv_and_b32 v230, 0x1ff, v230\nv_add_u32 v230, 155648, v230\nM 4,180,176\n"
"ds_read_b128 a[208:211], v249 offset:0\nds_write_b128 v230, v[132:135]\nG 10\nM 8,160,160\nR 176,188,49152\nds_read_b128 a[212:215], v249 offset:2048\n"
"M 8,164,176\nds_read_b128 a[216:219], v249 offset:4096\nM 24,160,164\nR 178,188,53248\nM 24,164,180\nds_read_b128 a[220:223], v249 offset:6144\n"
"M 40,160,168\nR 180,184,0\nM 40,164,184\ns_lshr_b32 s84, s49, 3\nM 56,160,172\nR 182,184,4096\n"
"s_add_u32 s84, s84, 81788928\nM 56,164,188\nG 2\ns_add_u32 m0, s51, 0\ns_add_u32 s60, s70, 896\nbuffer_load_dwordx4 v226, s[24:27], s60 offen lds\n"
"M 60,168,172\nR 160,189,49152\nbuffer_load_dwordx4 v[128:131], v252, s[20:23], s84 offen offset:0 sc1\nM 60,172,188\ns_add_u32 m0, s51, 1024\ns_add_u32 s60, s70, 17280\n"
"buffer_load_dwordx4 v227, s[24:27], s60 offen lds\nM 44,168,168\nR 162,189,53248\nM 44,172,184\ns_add_u32 m0, s51, 2048\ns_add_u32 s60, s70, 33664\n"
"buffer_load_dwordx4 v226, s[24:27], s60 offen lds\nM 28,168,164\nR 164,185,0\nbuffer_load_dwordx4 v[132:135], v252, s[20:23], s84 offen offset:1024 sc1\nM 28,172,180\ns_add_u32 m0, s51, 3072\n"
"s_add_u32 s60, s70, 50048\nbuffer_load_dwordx4 v227, s[24:27], s60 offen lds\nM 12,168,160\nR 166,185,4096\nbuffer_load_dwordx4 v[136:139], v252, s[20:23], s84 offen offset:2048 sc1\nM 12,172,176\n"
"s_add_u32 m0, s51, 4096\ns_add_u32 s60, s71, 896\nbuffer_load_dwordx4 v226, s[24:27], s60 offen lds\ns_waitcnt vmcnt(12)\ns_barrier\nG 4\n"
"N 64,160,176\nR 168,190,49152\ns_lshr_b32 s85, s49, 3\ns_add_u32 m0, s50, 40960\ns_add_u32 s63, s52, 2363392\nbuffer_load_dwordx4 v220, s[4:7], s63 offen lds\n"
"N 64,176,180\ns_add_u32 m0, s51, 5120\ns_add_u32 s60, s71, 17280\nbuffer_load_dwordx4 v227, s[24:27], s60 offen lds\ns_add_u32 s85, s85, 81793024\nN 68,164,176\n"
"R 170,190,53248\nbuffer_load_dwordx4 v[140:143], v252, s[20:23], s84 offen offset:3072 sc1\nN 68,180,180\ns_add_u32 m0, s51, 6144\ns_add_u32 s60, s71, 33664\nbuffer_load_dwordx4 v226, s[24:27], s60 offen lds\n"
"N 96,168,176\nR 172,186,0\nbuffer_load_dwordx4 v[144:147], v252, s[20:23], s85 offen offset:0 sc1\nN 96,184,180\ns_add_u32 m0, s51, 7168\ns_add_u32 s60, s71, 50048\n"
"buffer_load_dwordx4 v227, s[24:27], s60 offen lds\nN 100,172,176\nR 174,186,4096\ns_add_u32 m0, s50, 40960\nN 100,188,180\nG 4\n"
"N 108,172,160\nR 176,191,49152\nbuffer_load_dwordx4 v221, s[4:7], s63 offen offset:1024 lds\nN 108,188,164\nbuffer_load_dwordx4 v[148:151], v252, s[20:23], s85 offen offset:1024 sc1\ns_add_u32 s63, s52, 2560000\n"
"N 104,168,160\nR 178,191,53248\ns_add_u32 m0, s50, 49152\nN 104,184,164\nbuffer_load_dwordx4 v[152:155], v252, s[20:23], s85 offen offset:2048 sc1\nN 76,164,160\n"
"R 180,187,0\nbuffer_load_dwordx4 v220, s[4:7], s63 offen lds\nN 76,180,164\nbuffer_load_dwordx4 v[156:159], v252, s[20:23], s85 offen offset:3072 sc1\nN 72,160,160\nR 182,187,4096\n"
"N 72,176,164\nG 4\nN 80,160,168\nR 160,184,8192\nbuffer_load_dwordx4 v221, s[4:7], s63 offen offset:1024 lds\nN 80,176,172\n"
"N 84,164,168\nR 162,184,12288\nN 84,180,172\nN 112,168,168\nR 164,184,16384\nN 112,184,172\n"
"N 116,172,168\nR 166,184,20480\nN 116,188,172\nG 4\nN 124,172,176\nR 168,185,8192\n"
"N 124,188,180\ns_add_u32 m0, s50, 57344\nN 120,168,176\nR 170,185,12288\nN 120,184,180\ns_add_u32 s63, s52, 2756608\n"
"N 92,164,176\nR 172,185,16384\nN 92,180,180\nbuffer_load_dwordx4 v220, s[4:7], s63 offen lds\nN 88,160,176\nR 174,185,20480\n"
"N 88,176,180\ns_waitcnt vmcnt(21)\ns_barrier\nG 4\nM 0,160,192\nR 176,186,8192\n"
"M 0,164,208\nbuffer_load_dwordx4 v221, s[4:7], s63 offen offset:1024 lds\nM 16,160,196\nR 178,186,12288\nM 16,164,212\ns_add_u32 m0, s50, 65536\n"
"M 32,160,200\nR 180,186,16384\nM 32,164,216\ns_add_u32 s63, s52, 2953216\nM 48,160,204\nR 182,186,20480\n"
"s_lshl2_add_u32 s60, s45, s40\nv_lshlrev_b32 v224, 3, v252\ns_mul_i32 s60, s60, 2048\ns_bfm_b64 exec, 16, 0\nbuffer_load_dword v224, v224, s[8:11], s60 offen\ns_mov_b64 exec, -1\n"
"M 48,164,220\nG 4\nM 52,168,204\nR 160,187,8192\nM 52,172,220\nbuffer_load_dwordx4 v220, s[4:7], s63 offen lds\n"
"M 36,168,200\nR 162,187,12288\nM 36,172,216\nM 20,168,196\nR 164,187,16384\nM 20,172,212\n"
"buffer_load_dwordx4 v221, s[4:7], s63 offen offset:1024 lds\nM 4,168,192\nR 166,187,20480\nM 4,172,208\nG 4\nM 8,176,192\n"
"R 168,184,24576\nM 8,180,208\nM 24,176,196\nR 170,184,28672\ns_waitcnt vmcnt(11)\nds_read_b128 a[224:227], v248 offset:0\n"
"M 24,180,212\nds_read_b128 a[228:231], v248 offset:2048\nM 40,176,200\nR 172,184,32768\nds_read_b128 a[232:235], v248 offset:4096\nM 40,180,216\n"
"ds_read_b128 a[236:239], v248 offset:6144\nM 56,176,204\nR 174,184,36864\nM 56,180,220\nds_read_b128 a[240:243], v249 offset:0\nG 9\n"
"M 60,160,204\nR 176,185,24576\nds_read_b128 a[244:247], v249 offset:2048\nv_min3_u32 v230, v129, v131, v133\nM 60,164,220\nds_read_b128 a[248:251], v249 offset:4096\n"
"s_add_u32 m0, s50, 73728\nv_min3_u32 v230, v230, v135, v137\nM 44,160,200\nR 178,185,28672\ns_add_u32 s63, s52, 0\nv_min3_u32 v230, v230, v139, v141\n"
"M 44,164,216\nds_read_b128 a[252:255], v249 offset:6144\nv_min3_u32 v230, v230, v143, v145\ns_waitcnt vmcnt(9)\nv_min3_u32 v230, v230, v147, v149\nM 28,160,196\n"
"R 180,185,32768\ns_waitcnt vmcnt(8)\nv_min3_u32 v230, v230, v151, v153\ns_waitcnt vmcnt(6)\nv_min3_u32 v230, v230, v155, v157\nM 28,164,212\n"
"buffer_load_dwordx4 v220, s[4:7], s63 offen lds\nv_min3_u32 v230, v230, v159, v159\nM 12,160,192\nR 182,185,36864\nv_max3_u32 v231, v129, v131, v133\nv_max3_u32 v231, v231, v135, v137\n"
"M 12,164,208\ns_waitcnt vmcnt(6)\ns_barrier\nG 8\nv_max3_u32 v231, v231, v139, v141\nM 0,168,224\n"
"R 160,186,24576\nv_max3_u32 v231, v231, v143, v145\nv_max3_u32 v231, v231, v147, v149\nG 8\nM 0,172,240\nbuffer_load_dwordx4 v221, s[4:7], s63 offen offset:1024 lds\n"
"v_max3_u32 v231, v231, v151, v153\nM 16,168,228\nR 162,186,28672\ns_add_u32 s63, s52, 196608\nv_max3_u32 v231, v231, v155, v157\nG 7\n"
"M 16,172,244\ns_add_u32 m0, s50, 81920\nv_max3_u32 v231, v231, v159, v159\nv_cmp_ne_u32 s[86:87], s46, v230\nv_cmp_ne_u32 s[88:89], s46, v231\nM 32,168,232\n"
"R 164,186,32768\ns_or_b64 s[86:87], s[86:87], s[88:89]\ns_cbranch_scc1 Lq140\nLq141:\nG 7\nM 32,172,248\n"
"buffer_load_dwordx4 v220, s[4:7], s63 offen lds\nv_add_f32 v128, v128, v144\nM 48,168,236\nR 166,186,36864\ns_lshl_b32 s60, s90, 14\ns_add_u32 s70, s49, s60\n"
"s_xor_b32 s71, s70, 0x10000\nv_add_f32 v130, v130, v146\nG 6\nM 48,172,252\nLpg3:\ns_cbranch_execz LretQ0\n"
"G 4\nv_add_f32 v132, v132, v148\nv_add_f32 v134, v134, v150\nv_add_f32 v136, v136, v152\nM 52,176,236\nR 168,187,24576\n"
"v_add_f32 v138, v138, v154\nv_add_f32 v140, v140, v156\nM 52,180,252\nbuffer_load_dwordx4 v221, s[4:7], s63 offen offset:1024 lds\nv_add_f32 v142, v142, v158\nM 36,176,232\n"
"R 170,187,28672\ns_add_u32 s63, s52, 393216\nv_add_f32 v128, v128, v136\nM 36,180,248\ns_add_u32 m0, s50, 90112\nv_add_f32 v130, v130, v138\n"
"v_add_f32 v132, v132, v140\nv_add_f32 v134, v134, v142\nM 20,176,228\nR 172,187,32768\nv_add_f32 v128, v128, v132\nv_add_f32 v130, v130, v134\n"
"M 20,180,244\nbuffer_load_dwordx4 v220, s[4:7], s63 offen lds\nv_lshrrev_b32 v231, 1, v252\nM 4,176,224\nR 174,187,36864\nv_lshrrev_b32 v230, 2, v252\n"
"s_lshl_b32 s62, s40, 9\ns_add_u32 s62, s62, 159232\nv_add_u32 v231, s62, v231\nM 4,180,240\nds_write2_b32 v231, v128, v130 offset1:1\nG 5\n"
"M 8,160,224\nR 176,184,40960\ns_lshl_b32 s62, s40, 9\ns_add_u32 s62, s62, 159232\nv_add_u32 v230, s62, v230\nM 8,164,240\n"
"buffer_load_dwordx4 v221, s[4:7], s63 offen offset:1024 lds\nds_read2st64_b32 v[232:233], v230 offset1:1\nM 24,160,228\nR 178,184,45056\nM 24,164,244\ns_add_u32 m0, s50, 98304\n"
"M 40,160,232\nR 180,184,49152\nM 40,164,248\ns_add_u32 s63, s52, 589824\nM 56,160,236\nR 182,184,53248\n"
"M 56,164,252\nG 6\nG 3\nv_add_f32 v232, v232, v233\nv_mov_b32 v233, v232\ns_nop 1\n"
"v_permlane32_swap_b32 v232, v233\nM 60,168,236\nR 160,185,40960\nv_mov_b32 v230, v232\nv_mov_b32 v231, v233\nM 60,172,252\n"
"buffer_load_dwordx4 v220, s[4:7], s63 offen lds\nv_permlane16_swap_b32 v230, v232\nM 44,168,232\nR 162,185,45056\nv_permlane16_swap_b32 v231, v233\nv_mul_f32 v230, 0xba800000, v230\n"
"M 44,172,248\nv_mul_f32 v214, v230, v230\nv_mul_f32 v231, 0x3a800000, v231\nv_sub_f32 v231, v231, v214\nM 28,168,228\nR 164,185,49152\n"
"v_max_f32 v231, 0, v231\nv_add_f32 v231, 0x3727c5ac, v231\nM 28,172,244\nbuffer_load_dwordx4 v221, s[4:7], s63 offen offset:1024 lds\nv_rsq_f32 v234, v231\nM 12,168,224\n"
"R 166,185,53248\ns_mov_b32 s60, 156160\nv_bfe_u32 v246, v252, 4, 4\nv_lshl_add_u32 v246, v246, 3, s60\nv_and_b32 v208, s77, v230\nM 12,172,240\n"
"ds_read_b64 v[238:239], v246 offset:0\ns_waitcnt vmcnt(8) lgkmcnt(0)\ns_barrier\nv_mul_f32 v216, v231, v234\nN 64,192,176\nR 168,186,40960\n"
"ds_read_b64 v[240:241], v246 offset:128\nv_sub_f32 v230, v230, v208\nN 64,208,180\ns_mov_b32 m0, s51\nds_read_b64 v[242:243], v246 offset:256\nv_cvt_pk_bf16_f32 v209, v230, v216\n"
"N 68,196,176\nR 170,186,45056\ns_lshl2_add_u32 s60, s45, s40\nv_lshlrev_b32 v224, 3, v252\ns_mul_i32 s60, s60, 7168\ns_bfm_b64 exec, 14, 0\n"
"buffer_load_dword v224, v224, s[12:15], s60 offen\ns_mov_b64 exec, -1\nds_read_b64 v[244:245], v246 offset:384\nds_write_addtid_b32 v32 offset:0\nv_cvt_pk_bf16_f32 v208, v208, v208\nN 68,212,180\n"
"ds_read_b64 v[246:247], v246 offset:0\nds_write_addtid_b32 v33 offset:256\nv_mov_b32 v214, v234\nv_mul_f32 v215, 0x3e8293ee, v234\nN 96,200,176\nR 172,186,49152\n"
"ds_write_addtid_b32 v34 offset:512\nv_mul_f32 v232, 0xba800000, v232\nv_mul_f32 v217, v232, v232\nN 96,216,180\nds_write_addtid_b32 v35 offset:768\nds_write_addtid_b32 v36 offset:1024\n"
"v_mul_f32 v233, 0x3a800000, v233\nv_sub_f32 v233, v233, v217\nN 100,204,176\nR 174,186,53248\nds_write_addtid_b32 v37 offset:1280\nv_max_f32 v233, 0, v233\n"
"v_add_f32 v233, 0x3727c5ac, v233\nN 100,220,180\nds_write_addtid_b32 v38 offset:1536\nv_rsq_f32 v235, v233\nv_and_b32 v212, s77, v232\nv_mul_f32 v219, v233, v235\n"
"v_sub_f32 v232, v232, v212\nv_cvt_pk_bf16_f32 v213, v232, v219\nv_cvt_pk_bf16_f32 v212, v212, v212\nv_mov_b32 v217, v235\nv_mul_f32 v218, 0x3e8293ee, v235\ns_mov_b64 s[60:61], 0xffff\n"
"v_cndmask_b32 v208, 0, v208, s[60:61]\ns_mov_b64 s[60:61], 0xffff\nv_cndmask_b32 v209, 0, v209, s[60:61]\ns_mov_b64 s[60:61], 0xffff\nv_cndmask_b32 v212, 0, v212, s[60:61]\ns_mov_b64 s[60:61], 0xffff\n"
"v_cndmask_b32 v213, 0, v213, s[60:61]\nv_bfe_u32 v230, v252, 2, 6\ns_lshl_b32 s60, s40, 7\ns_add_u32 s60, s60, 161280\nv_add_u32 v230, s60, v230\nds_write_b32 v230, v214 offset:0\n"
"ds_write_b32 v230, v217 offset:64\nds_write_addtid_b32 v39 offset:1792\nN 108,204,160\nR 176,187,40960\nds_write_addtid_b32 v40 offset:2048\nN 108,220,164\n"
"ds_write_addtid_b32 v41 offset:2304\nds_write_addtid_b32 v42 offset:2560\nN 104,200,160\nR 178,187,45056\nds_write_addtid_b32 v43 offset:2816\nN 104,216,164\n"
"ds_write_addtid_b32 v44 offset:3072\nds_write_addtid_b32 v45 offset:3328\nN 76,196,160\nR 180,187,49152\nG 15\nv_mfma_f32_16x16x32_bf16 v[0:3], v[238:241], v[208:211], v[0:3]\n"
"ds_write_addtid_b32 v46 offset:3584\nN 76,212,164\nv_mfma_f32_16x16x32_bf16 v[4:7], v[240:243], v[208:211], v[4:7]\nds_write_addtid_b32 v47 offset:3840\nds_write_addtid_b32 v48 offset:4096\nN 72,192,160\n"
"R 182,187,53248\nv_mfma_f32_16x16x32_bf16 v[8:11], v[242:245], v[208:211], v[8:11]\nds_write_addtid_b32 v49 offset:4352\nN 72,208,164\nv_mfma_f32_16x16x32_bf16 v[12:15], v[244:247], v[208:211], v[12:15]\nds_write_addtid_b32 v50 offset:4608\n"
"ds_write_addtid_b32 v51 offset:4864\nG 15\nN 80,192,168\nR 160,184,57344\nv_mfma_f32_16x16x32_bf16 v[16:19], v[244:247], v[210:213], v[16:19]\nds_write_addtid_b32 v52 offset:5120\n"
"N 80,208,172\nv_mfma_f32_16x16x32_bf16 v[20:23], v[238:241], v[210:213], v[20:23]\nds_write_addtid_b32 v53 offset:5376\nds_write_addtid_b32 v54 offset:5632\nN 84,196,168\nR 162,184,61440\n"
"v_mfma_f32_16x16x32_bf16 v[24:27], v[240:243], v[210:213], v[24:27]\nds_write_addtid_b32 v55 offset:5888\nN 84,212,172\nv_mfma_f32_16x16x32_bf16 v[28:31], v[242:245], v[210:213], v[28:31]\nds_write_addtid_b32 v56 offset:6144\nds_write_addtid_b32 v57 offset:6400\n"
"N 112,200,168\nR 164,188,0\nds_write_addtid_b32 v58 offset:6656\nN 112,216,172\nds_write_addtid_b32 v59 offset:6912\nds_write_addtid_b32 v60 offset:7168\n"
"N 116,204,168\nR 166,188,4096\nds_write_addtid_b32 v61 offset:7424\nN 116,220,172\nds_write_addtid_b32 v62 offset:7680\nds_write_addtid_b32 v63 offset:7936\n"
"G 15\nN 124,204,176\nR 168,185,57344\ns_add_u32 m0, s50, 106496\ns_add_u32 s63, s52, 786432\nN 124,220,180\n"
"buffer_load_dwordx4 v220, s[4:7], s63 offen lds\nN 120,200,176\nR 170,185,61440\ns_waitcnt vmcnt(6) lgkmcnt(2)\ns_barrier\nN 120,216,180\n"
"ds_read2st64_b32 v[32:33], v225 offset0:0 offset1:1\nds_read2st64_b32 v[34:35], v225 offset0:2 offset1:3\nN 92,196,176\nR 172,189,0\nds_read2st64_b32 v[36:37], v225 offset0:4 offset1:5\nN 92,212,180\n"
"buffer_load_dwordx4 v221, s[4:7], s63 offen offset:1024 lds\nds_read2st64_b32 v[38:39], v225 offset0:6 offset1:7\nN 88,192,176\nR 174,189,4096\nds_read2st64_b32 v[40:41], v225 offset0:8 offset1:9\nN 88,208,180\n"
"ds_read2st64_b32 v[42:43], v225 offset0:10 offset1:11\nds_read2st64_b32 v[44:45], v225 offset0:12 offset1:13\nN 64,224,160\nR 176,186,57344\ns_mov_b32 s60, 156672\nv_bfe_u32 v246, v252, 4, 4\n"
"v_lshl_add_u32 v246, v246, 3, s60\nds_read2st64_b32 v[46:47], v225 offset0:14 offset1:15\nN 64,240,164\nds_read_b64 v[238:239], v246 offset:0\ns_add_u32 m0, s50, 114688\nds_read2st64_b32 v[48:49], v225 offset0:16 offset1:17\n"
"N 68,228,160\nR 178,186,61440\nds_read_b64 v[240:241], v246 offset:128\nds_read2st64_b32 v[50:51], v225 offset0:18 offset1:19\nN 68,244,164\nds_read_b64 v[242:243], v246 offset:256\n"
"s_add_u32 s63, s52, 983040\nds_read2st64_b32 v[52:53], v225 offset0:20 offset1:21\nN 96,232,160\nR 180,190,0\nds_read_b64 v[244:245], v246 offset:384\nds_read2st64_b32 v[54:55], v225 offset0:22 offset1:23\n"
"N 96,248,164\nds_read_b64 v[246:247], v246 offset:0\nds_read2st64_b32 v[56:57], v225 offset0:24 offset1:25\nG 15\nv_add_f32 v0, v0, v32\nN 100,236,160\n"
"R 182,190,4096\nbuffer_load_dwordx4 v220, s[4:7], s63 offen lds\nds_read2st64_b32 v[58:59], v225 offset0:26 offset1:27\nN 100,252,164\nds_read2st64_b32 v[60:61], v225 offset0:28 offset1:29\nds_read2st64_b32 v[62:63], v225 offset0:30 offset1:31\n"
"N 108,236,168\nR 160,187,57344\nv_add_f32 v1, v1, v33\nv_add_f32 v2, v2, v34\nN 108,252,172\nbuffer_load_dwordx4 v221, s[4:7], s63 offen offset:1024 lds\n"
"v_add_f32 v3, v3, v35\nN 104,232,168\nR 162,187,61440\ns_add_u32 m0, s50, 0\ns_add_u32 s63, s52, 1179648\nv_add_f32 v4, v4, v36\n"
"N 104,248,172\nbuffer_load_dwordx4 v220, s[4:7], s63 offen lds\nv_add_f32 v5, v5, v37\nN 76,228,168\nR 164,191,0\nv_add_f32 v6, v6, v38\n"
"v_add_f32 v7, v7, v39\nN 76,244,172\nG 15\nv_mfma_f32_16x16x32_bf16 v[64:67], v[208:211], v[238:241], v[64:67]\nv_add_f32 v8, v8, v40\nv_add_f32 v9, v9, v41\n"
"v_add_f32 v10, v10, v42\nN 72,224,168\nR 166,191,4096\nv_add_f32 v11, v11, v43\nv_add_f32 v12, v12, v44\nN 72,240,172\n"
"G 14\nv_mfma_f32_16x16x32_bf16 v[72:75], v[208:211], v[240:243], v[72:75]\nG 7\nv_add_f32 v13, v13, v45\nv_add_f32 v14, v14, v46\nv_add_f32 v15, v15, v47\n"
"N 80,224,176\nR 168,188,8192\nbuffer_load_dwordx4 v221, s[4:7], s63 offen offset:1024 lds\nv_add_f32 v16, v16, v48\nN 80,240,180\nv_mfma_f32_16x16x32_bf16 v[80:83], v[208:211], v[242:245], v[80:83]\n"
"s_add_u32 m0, s50, 8192\ns_add_u32 s63, s52, 1376256\nbuffer_load_dwordx4 v220, s[4:7], s63 offen lds\nv_add_f32 v17, v17, v49\nN 84,228,176\nR 170,188,12288\n"
"v_add_f32 v18, v18, v50\nv_add_f32 v19, v19, v51\nN 84,244,180\nv_mfma_f32_16x16x32_bf16 v[88:91], v[208:211], v[244:247], v[88:91]\nv_add_f32 v20, v20, v52\nv_add_f32 v21, v21, v53\n"
"v_add_f32 v22, v22, v54\nN 112,232,176\nR 172,188,16384\nv_add_f32 v23, v23, v55\nv_add_f32 v24, v24, v56\nN 112,248,180\n"
"v_mfma_f32_16x16x32_bf16 v[68:71], v[210:213], v[244:247], v[68:71]\nbuffer_load_dwordx4 v221, s[4:7], s63 offen offset:1024 lds\nv_add_f32 v25, v25, v57\nN 116,236,176\nR 174,188,20480\ns_add_u32 m0, s50, 32768\n"
"G 10\nv_add_f32 v26, v26, v58\nN 116,252,180\nv_mfma_f32_16x16x32_bf16 v[76:79], v[210:213], v[238:241], v[76:79]\nG 4\nv_add_f32 v27, v27, v59\n"
"v_add_f32 v28, v28, v60\nv_add_f32 v29, v29, v61\nN 124,236,160\nR 176,189,8192\ns_add_u32 s63, s52, 1572864\nv_add_f32 v30, v30, v62\n"
"N 124,252,164\nv_mfma_f32_16x16x32_bf16 v[84:87], v[210:213], v[240:243], v[84:87]\nbuffer_load_dwordx4 v220, s[4:7], s63 offen lds\nv_add_f32 v31, v31, v63\nN 120,232,160\nR 178,189,12288\n"
"s_lshl4_add_u32 s64, s41, s42\nv_lshrrev_b32 v227, 1, v252\nN 120,248,164\nv_mfma_f32_16x16x32_bf16 v[92:95], v[210:213], v[242:245], v[92:95]\nbuffer_load_dwordx4 v221, s[4:7], s63 offen offset:1024 lds\nv_mul_f32 v16, v16, v217\n"
"N 92,228,160\nR 180,189,16384\ns_add_u32 m0, s50, 40960\ns_add_u32 s63, s52, 1769472\nv_mul_f32 v17, v17, v217\nN 92,244,164\n"
"buffer_load_dwordx4 v220, s[4:7], s63 offen lds\nv_mul_f32 v18, v18, v217\nN 88,224,160\nR 182,189,20480\ns_lshl_b32 s64, s64, 12\nv_mul_f32 v19, v19, v217\n"
"N 88,240,164\ns_waitcnt vmcnt(12)\ns_barrier\nG 4\nv_mul_f32 v20, v20, v217\nv_mfma_f32_16x16x32_bf16 v[128:131], v[168:171], a[0:3], 0\n"
"R 160,190,8192\ns_add_u32 s64, s64, 77594624\nv_mul_f32 v21, v21, v217\nM 128,172,16\ns_mov_b32 m0, s51\ns_cmp_eq_u32 s40, 3\n"
"s_cselect_b64 s[66:67], -1, 0\nv_mul_f32 v22, v22, v217\nv_mfma_f32_16x16x32_bf16 v[144:147], v[168:171], a[4:7], 0\nR 162,190,12288\ns_lshl2_add_u32 s60, s45, s40\ns_waitcnt vmcnt(11)\n"
"v_lshlrev_b32 v224, 3, v252\ns_mul_i32 s60, s60, 7168\ns_bfm_b64 exec, 14, 14\nbuffer_load_dword v224, v224, s[12:15], s60 offen\ns_mov_b64 exec, -1\nv_mul_f32 v23, v23, v217\n"
"M 144,172,20\nv_mul_f32 v24, v24, v217\nv_mul_f32 v25, v25, v217\nv_mul_f32 v26, v26, v217\nv_mfma_f32_16x16x32_bf16 v[32:35], v[168:171], a[8:11], 0\nR 164,190,16384\n"
"v_mul_f32 v27, v27, v217\nv_mul_f32 v28, v28, v217\nM 32,172,24\nv_mul_f32 v29, v29, v217\nv_mul_f32 v30, v30, v217\nv_mul_f32 v31, v31, v217\n"
"v_mfma_f32_16x16x32_bf16 v[48:51], v[168:171], a[12:15], 0\nR 166,190,20480\nds_write_addtid_b32 v96 offset:0\nv_cvt_pk_bf16_f32 v16, v16, v17\nv_cvt_pk_bf16_f32 v17, v18, v19\nM 48,172,28\n"
"ds_write_addtid_b32 v97 offset:256\nG 6\nv_cvt_pk_bf16_f32 v18, v20, v21\nv_cvt_pk_bf16_f32 v19, v22, v23\nv_cvt_pk_bf16_f32 v20, v24, v25\nv_mfma_f32_16x16x32_bf16 v[52:55], v[176:179], a[12:15], 0\n"
"R 168,191,8192\nds_write_addtid_b32 v98 offset:512\ns_lshr_b32 s100, s41, 1\ns_mul_i32 s100, s100, 458752\ns_add_u32 s100, s100, s55\nv_cvt_pk_bf16_f32 v21, v26, v27\n"
"v_cvt_pk_bf16_f32 v22, v28, v29\nM 52,180,28\nds_write_addtid_b32 v99 offset:768\ns_mul_i32 s101, s41, 229376\ns_add_u32 s101, s101, s54\nv_cvt_pk_bf16_f32 v23, v30, v31\n"
"v_mul_f32 v0, v0, v214\nv_mul_f32 v1, v1, v214\nv_mfma_f32_16x16x32_bf16 v[36:39], v[176:179], a[8:11], 0\nR 170,191,12288\nds_write_addtid_b32 v100 offset:1024\nv_mul_f32 v2, v2, v214\n"
"v_mul_f32 v3, v3, v214\nM 36,180,24\nds_write_addtid_b32 v101 offset:1280\nv_mul_f32 v4, v4, v214\nv_mul_f32 v5, v5, v214\nv_mul_f32 v6, v6, v214\n"
"v_mfma_f32_16x16x32_bf16 v[148:151], v[176:179], a[4:7], 0\nR 172,191,16384\nds_write_addtid_b32 v102 offset:1536\nv_mul_f32 v7, v7, v214\nv_mul_f32 v8, v8, v214\nM 148,180,20\n"
"ds_write_addtid_b32 v103 offset:1792\nv_mul_f32 v9, v9, v214\nv_mul_f32 v10, v10, v214\nv_mul_f32 v11, v11, v214\nv_mfma_f32_16x16x32_bf16 v[132:135], v[176:179], a[0:3], 0\nR 174,191,20480\n"
"ds_write_addtid_b32 v104 offset:2048\nv_mul_f32 v12, v12, v214\nv_mul_f32 v13, v13, v214\nM 132,180,16\nds_write_addtid_b32 v105 offset:2304\nG 14\n"
"v_mul_f32 v14, v14, v214\nv_mul_f32 v15, v15, v214\nv_cvt_pk_bf16_f32 v0, v0, v1\nv_mfma_f32_16x16x32_bf16 v[136:139], v[160:163], a[0:3], 0\nR 176,188,24576\nds_write_addtid_b32 v106 offset:2560\n"
"v_cvt_pk_bf16_f32 v1, v2, v3\nv_cvt_pk_bf16_f32 v2, v4, v5\nM 136,164,16\nds_write_addtid_b32 v107 offset:2816\nv_cvt_pk_bf16_f32 v3, v6, v7\nv_cvt_pk_bf16_f32 v4, v8, v9\n"
"v_cvt_pk_bf16_f32 v5, v10, v11\nv_mfma_f32_16x16x32_bf16 v[152:155], v[160:163], a[4:7], 0\nR 178,188,28672\nds_write_addtid_b32 v108 offset:3072\nv_cvt_pk_bf16_f32 v6, v12, v13\nv_cvt_pk_bf16_f32 v7, v14, v15\n"
"M 152,164,20\nds_write_addtid_b32 v109 offset:3328\nds_write_addtid_b32 v110 offset:3584\ns_mov_b64 exec, s[66:67]\nbuffer_store_dwordx4 v[16:19], v252, s[20:23], s64 offen offset:0 sc1\ns_mov_b64 exec, -1\n"
"v_mfma_f32_16x16x32_bf16 v[40:43], v[160:163], a[8:11], 0\nR 180,188,32768\nds_write_addtid_b32 v111 offset:3840\nM 40,164,24\nds_write_addtid_b32 v112 offset:4096\nds_write_addtid_b32 v113 offset:4352\n"
"v_mfma_f32_16x16x32_bf16 v[56:59], v[160:163], a[12:15], 0\nR 182,188,36864\nds_write_addtid_b32 v114 offset:4608\nM 56,164,28\nds_write_addtid_b32 v115 offset:4864\nds_write_addtid_b32 v116 offset:5120\n"
"s_mov_b64 exec, s[66:67]\nbuffer_store_dwordx4 v[20:23], v252, s[20:23], s64 offen offset:1024 sc1\ns_mov_b64 exec, -1\nG 15\nv_mfma_f32_16x16x32_bf16 v[60:63], v[168:171], a[12:15], 0\nR 160,189,24576\n"
"ds_write_addtid_b32 v117 offset:5376\nM 60,172,28\nds_write_addtid_b32 v118 offset:5632\nds_write_addtid_b32 v119 offset:5888\nv_mfma_f32_16x16x32_bf16 v[44:47], v[168:171], a[8:11], 0\nR 162,189,28672\n"
"ds_write_addtid_b32 v120 offset:6144\nM 44,172,24\nds_write_addtid_b32 v121 offset:6400\nds_write_addtid_b32 v122 offset:6656\nds_write_addtid_b32 v123 offset:6912\nds_write_addtid_b32 v124 offset:7168\n"
"ds_write_addtid_b32 v125 offset:7424\nds_write_addtid_b32 v126 offset:7680\nds_write_addtid_b32 v127 offset:7936\nv_mfma_f32_16x16x32_bf16 v[156:159], v[168:171], a[4:7], 0\nR 164,189,32768\ns_add_u32 m0, s50, 40960\n"
"M 156,172,20\nbuffer_load_dwordx4 v221, s[4:7], s63 offen offset:1024 lds\nv_mfma_f32_16x16x32_bf16 v[140:143], v[168:171], a[0:3], 0\nR 166,189,36864\ns_waitcnt vmcnt(11) lgkmcnt(2)\ns_barrier\n"
"M 140,172,16\nds_read2st64_b32 v[96:97], v225 offset0:0 offset1:1\nds_read2st64_b32 v[98:99], v225 offset0:2 offset1:3\nv_lshrrev_b32 v226, 8, v252\nM 128,176,32\nR 168,190,24576\n"
"s_add_u32 s63, s52, 1966080\nds_read2st64_b32 v[100:101], v225 offset0:4 offset1:5\ns_lshl_b32 s60, s40, 7\ns_add_u32 s60, s60, 161280\nv_lshl_add_u32 v226, v226, 4, s60\nM 128,180,48\n"
"s_add_u32 m0, s50, 49152\nds_read2st64_b32 v[102:103], v225 offset0:6 offset1:7\nds_read2st64_b32 v[104:105], v225 offset0:8 offset1:9\nds_read_b128 v[230:233], v226 offset:0\nM 144,176,36\nR 170,190,28672\n"
"ds_read2st64_b32 v[106:107], v225 offset0:10 offset1:11\nds_read_b128 v[234:237], v226 offset:64\nM 144,180,52\nbuffer_load_dwordx4 v220, s[4:7], s63 offen lds\nds_read2st64_b32 v[108:109], v225 offset0:12 offset1:13\nM 32,176,40\n"
"R 172,190,32768\nds_read2st64_b32 v[110:111], v225 offset0:14 offset1:15\nM 32,180,56\nds_read2st64_b32 v[112:113], v225 offset0:16 offset1:17\nds_read2st64_b32 v[114:115], v225 offset0:18 offset1:19\nG 14\n"
"v_add_f32 v64, v64, v96\nM 48,176,44\nR 174,190,36864\nds_read2st64_b32 v[116:117], v225 offset0:20 offset1:21\nv_add_f32 v65, v65, v97\nM 48,180,60\n"
"ds_read2st64_b32 v[118:119], v225 offset0:22 offset1:23\nds_read2st64_b32 v[120:121], v225 offset0:24 offset1:25\nM 52,160,44\nR 176,191,24576\nds_read2st64_b32 v[122:123], v225 offset0:26 offset1:27\nG 15\n"
"v_add_f32 v66, v66, v98\nM 52,164,60\nbuffer_load_dwordx4 v221, s[4:7], s63 offen offset:1024 lds\nds_read2st64_b32 v[124:125], v225 offset0:28 offset1:29\nM 36,160,40\nR 178,191,28672\n"
"ds_read2st64_b32 v[126:127], v225 offset0:30 offset1:31\nv_add_f32 v67, v67, v99\nM 36,164,56\ns_add_u32 m0, s50, 57344\nv_add_f32 v68, v68, v100\nv_add_f32 v69, v69, v101\n"
"v_add_f32 v70, v70, v102\nM 148,160,36\nR 180,191,32768\nv_add_f32 v71, v71, v103\nv_add_f32 v72, v72, v104\nM 148,164,52\n"
"s_add_u32 s63, s52, 2162688\nv_add_f32 v73, v73, v105\nG 15\nv_add_f32 v74, v74, v106\nLpg4:\ns_cbranch_execz LretQ0\n"
"v_add_f32 v75, v75, v107\nM 132,160,32\nR 182,191,36864\nG 15\nv_add_f32 v76, v76, v108\nv_add_f32 v77, v77, v109\n"
"M 132,164,48\nG 10\nv_add_f32 v78, v78, v110\nv_add_f32 v79, v79, v111\nv_add_f32 v80, v80, v112\nM 136,168,32\n"
"R 160,188,40960\nv_add_f32 v81, v81, v113\nv_add_f32 v82, v82, v114\nM 136,172,48\nbuffer_load_dwordx4 v220, s[4:7], s63 offen lds\nv_add_f32 v83, v83, v115\n"
"M 152,168,36\nR 162,188,45056\nG 11\nv_add_f32 v84, v84, v116\nM 152,172,52\nv_add_f32 v85, v85, v117\n"
"G 10\nv_add_f32 v86, v86, v118\nM 40,168,40\nR 164,188,49152\nv_add_f32 v87, v87, v119\nM 40,172,56\n"
"buffer_load_dwordx4 v221, s[4:7], s63 offen offset:1024 lds\nM 56,168,44\nR 166,188,53248\ns_add_u32 m0, s50, 65536\nM 56,172,60\nG 4\n"
"v_add_f32 v88, v88, v120\nv_add_f32 v89, v89, v121\nM 60,176,44\nR 168,189,40960\ns_add_u32 s63, s52, 2359296\nM 60,180,60\n"
"buffer_load_dwordx4 v220, s[4:7], s63 offen lds\nM 44,176,40\nR 170,189,45056\nv_add_f32 v90, v90, v122\nM 44,180,56\nv_add_f32 v91, v91, v123\n"
"v_add_f32 v92, v92, v124\nM 156,176,36\nR 172,189,49152\nv_add_f32 v93, v93, v125\nM 156,180,52\nbuffer_load_dwordx4 v221, s[4:7], s63 offen offset:1024 lds\n"
"M 140,176,32\nR 174,189,53248\ns_add_u32 m0, s50, 73728\nM 140,180,48\nG 4\nv_add_f32 v94, v94, v126\n"
"v_add_f32 v95, v95, v127\nM 128,160,64\nR 176,190,40960\ns_add_u32 s63, s52, 2555904\nv_mul_f32 v64, v64, v230\nM 128,164,80\n"
"buffer_load_dwordx4 v220, s[4:7], s63 offen lds\nv_mul_f32 v65, v65, v231\nM 144,160,68\nR 178,190,45056\nv_mul_f32 v66, v66, v232\nv_mul_f32 v67, v67, v233\n"
"M 144,164,84\nv_mul_f32 v68, v68, v234\nv_mul_f32 v69, v69, v235\nv_mul_f32 v70, v70, v236\nM 32,160,72\nR 180,190,49152\n"
"v_mul_f32 v71, v71, v237\nv_cvt_pk_bf16_f32 v67, v66, v67\nM 32,164,88\nbuffer_load_dwordx4 v221, s[4:7], s63 offen offset:1024 lds\nv_cvt_pk_bf16_f32 v66, v64, v65\nM 48,160,76\n"
"R 182,190,53248\ns_add_u32 s63, s52, 2752512\nv_cvt_pk_bf16_f32 v68, v68, v69\nM 48,164,92\nG 4\nv_cvt_pk_bf16_f32 v69, v70, v71\n"
"v_mul_f32 v72, v72, v230\nv_mul_f32 v73, v73, v231\nM 52,168,76\nR 160,191,40960\nv_mul_f32 v74, v74, v232\nv_mul_f32 v75, v75, v233\n"
"M 52,172,92\ns_add_u32 m0, s50, 81920\nv_mul_f32 v76, v76, v234\nv_mul_f32 v77, v77, v235\nv_mul_f32 v78, v78, v236\nM 36,168,72\n"
"R 162,191,45056\nv_mul_f32 v79, v79, v237\nv_cvt_pk_bf16_f32 v72, v72, v73\nM 36,172,88\nbuffer_load_dwordx4 v220, s[4:7], s63 offen lds\nv_cvt_pk_bf16_f32 v73, v74, v75\n"
"M 148,168,68\nR 164,191,49152\nv_cvt_pk_bf16_f32 v74, v76, v77\nv_cvt_pk_bf16_f32 v75, v78, v79\nM 148,172,84\ns_mov_b64 exec, s[66:67]\n"
"buffer_store_dwordx2 v[68:69], v227, s[20:23], s64 offen offset:2048 sc1\ns_mov_b64 exec, -1\nv_mul_f32 v80, v80, v230\nv_mul_f32 v81, v81, v231\nM 132,168,64\nR 166,191,53248\n"
"s_waitcnt vmcnt(10)\ns_barrier\nv_mul_f32 v82, v82, v232\nM 132,172,80\nG 4\nv_mul_f32 v83, v83, v233\n"
"v_mul_f32 v84, v84, v234\nv_mul_f32 v85, v85, v235\nM 136,176,64\nR 168,184,0\ns_lshl2_add_u32 s60, s45, s40\nv_lshlrev_b32 v224, 3, v252\n"
"s_mul_i32 s60, s60, 7168\ns_bfm_b64 exec, 14, 28\nbuffer_load_dword v224, v224, s[12:15], s60 offen\ns_mov_b64 exec, -1\nv_mul_f32 v86, v86, v236\nM 136,180,80\n"
"v_mul_f32 v87, v87, v237\nv_cvt_pk_bf16_f32 v78, v80, v81\nv_cvt_pk_bf16_f32 v79, v82, v83\nM 152,176,68\nR 170,184,4096\nv_cvt_pk_bf16_f32 v80, v84, v85\n"
"v_cvt_pk_bf16_f32 v81, v86, v87\nM 152,180,84\nbuffer_load_dwordx4 v221, s[4:7], s63 offen offset:1024 lds\nv_mul_f32 v88, v88, v230\nM 40,176,72\nR 172,184,8192\n"
"s_mov_b32 s60, 155648\nv_bfe_u32 v246, v252, 4, 4\nv_lshl_add_u32 v246, v246, 3, s60\nv_mul_f32 v89, v89, v231\nM 40,180,88\ns_add_u32 m0, s50, 90112\n"
"v_mul_f32 v90, v90, v232\nv_mul_f32 v91, v91, v233\nv_mul_f32 v92, v92, v234\nM 56,176,76\nR 174,184,12288\nds_read_b64 v[238:239], v246 offset:0\n"
"v_mul_f32 v93, v93, v235\nM 56,180,92\nG 5\nv_mul_f32 v94, v94, v236\nv_mul_f32 v95, v95, v237\nv_cvt_pk_bf16_f32 v84, v88, v89\n"
"M 60,160,76\nR 176,185,0\nds_read_b64 v[240:241], v246 offset:128\nv_cvt_pk_bf16_f32 v85, v90, v91\nM 60,164,92\ns_add_u32 s63, s52, 2949120\n"
"s_mov_b64 exec, s[66:67]\nbuffer_store_dwordx2 v[74:75], v227, s[20:23], s64 offen offset:2560 sc1\ns_mov_b64 exec, -1\nv_cvt_pk_bf16_f32 v86, v92, v93\nv_cvt_pk_bf16_f32 v87, v94, v95\nM 44,160,72\n"
"R 178,185,4096\nds_read_b64 v[242:243], v246 offset:256\ns_lshl_b32 s60, s40, 12\ns_add_u32 s60, s60, 53248\nv_add_u32 v230, s60, v252\nv_add_u32 v231, s60, v227\n"
"M 44,164,88\nbuffer_load_dwordx4 v220, s[4:7], s63 offen lds\nM 156,160,68\nR 180,185,8192\nds_read_b64 v[244:245], v246 offset:384\nM 156,164,84\n"
"v_add_u32 v108, 161792, v252\nds_read_b128 v[112:115], v108\nM 140,160,64\nR 182,185,12288\nds_read_b64 v[246:247], v246 offset:0\nM 140,164,80\n"
"ds_read_b128 v[104:107], v108 offset:1024\ns_mov_b64 exec, s[66:67]\nbuffer_store_dwordx2 v[80:81], v227, s[20:23], s64 offen offset:3072 sc1\ns_mov_b64 exec, -1\nG 11\nM 128,168,96\n"
"R 160,186,0\nM 128,172,112\nM 144,168,100\nR 162,186,4096\nM 144,172,116\nbuffer_load_dwordx4 v221, s[4:7], s63 offen offset:1024 lds\n"
"M 32,168,104\nR 164,186,8192\nG 11\nv_mfma_f32_16x16x32_bf16 v[128:131], v[238:241], v[208:211], v[128:131]\nM 32,172,120\ns_mov_b64 exec, s[66:67]\n"
"buffer_store_dwordx2 v[86:87], v227, s[20:23], s64 offen offset:3584 sc1\ns_mov_b64 exec, -1\nG 3\nv_lshlrev_b32 v100, 16, v104\nv_and_b32 v101, s77, v104\nM 48,168,108\n"
"R 166,186,12288\nv_mfma_f32_16x16x32_bf16 v[132:135], v[240:243], v[208:211], v[132:135]\ns_not_b64 exec, s[66:67]\nds_write_b128 v230, v[16:19] offset:0\nds_write_b128 v230, v[20:23] offset:1024\ns_mov_b64 exec, -1\n"
"v_lshlrev_b32 v102, 16, v105\nM 48,172,124\nv_and_b32 v103, s77, v105\nv_lshlrev_b32 v96, 16, v106\nM 52,176,108\nR 168,187,0\n"
"v_mfma_f32_16x16x32_bf16 v[144:147], v[244:247], v[210:213], v[144:147]\nv_and_b32 v97, s77, v106\nM 52,180,124\ns_movk_i32 s63, 7168\nv_mad_u32_u24 v220, v228, s63, v229\nv_add_u32 v221, 3668992, v220\n"
"s_add_u32 m0, s50, 98304\ns_add_u32 s63, s101, 0\nbuffer_load_dwordx4 v220, s[12:15], s63 offen lds\nM 36,176,104\nR 170,187,4096\nv_mfma_f32_16x16x32_bf16 v[148:151], v[238:241], v[210:213], v[148:151]\n"
"v_lshlrev_b32 v98, 16, v107\nM 36,180,120\nv_and_b32 v99, s77, v107\nv_add_f32 v104, s78, v100\nM 148,176,100\nR 172,187,8192\n"
"v_add_f32 v105, s78, v101\nM 148,180,116\nv_add_f32 v106, s78, v102\nv_add_f32 v107, s78, v103\nM 132,176,96\nR 174,187,12288\n"
"M 132,180,112\nG 6\nM 136,160,96\nR 176,184,32768\nM 136,164,112\nbuffer_load_dwordx4 v221, s[12:15], s63 offen offset:1024 lds\n"
"M 152,160,100\nR 178,184,36864\ns_not_b64 exec, s[66:67]\nds_write_b64 v231, v[68:69] offset:2048\nds_write_b64 v231, v[74:75] offset:2560\ns_mov_b64 exec, -1\n"
"M 152,164,116\nM 40,160,104\nR 180,184,40960\nM 40,164,120\nM 56,160,108\nR 182,184,45056\n"
"M 56,164,124\nG 6\nM 60,168,108\nR 160,185,32768\ns_not_b64 exec, s[66:67]\nds_write_b64 v231, v[80:81] offset:3072\n"
"ds_write_b64 v231, v[86:87] offset:3584\ns_mov_b64 exec, -1\nM 60,172,124\nM 44,168,104\nR 162,185,36864\nM 44,172,120\n"
"s_add_u32 m0, s50, 106496\nM 156,168,100\nR 164,185,40960\nM 156,172,116\ns_add_u32 s63, s101, 128\nM 140,168,96\n"
"R 166,185,45056\nM 140,172,112\nG 6\nM 128,176,128\nR 168,186,32768\ns_waitcnt vmcnt(13) lgkmcnt(4)\n"
"s_barrier\nM 128,180,144\nbuffer_load_dwordx4 v220, s[12:15], s63 offen lds\nM 144,176,132\nR 170,186,36864\nM 144,180,148\n"
"s_cmp_eq_u32 s78, 0\ns_cbranch_scc1 Lq157\nv_mov_b32 v230, 0\nv_mov_b32 v231, 0\nv_mov_b32 v232, 0\nv_mov_b32 v233, 0\n"
"v_mov_b32 v64, 0\nv_mov_b32 v65, 0\nv_mov_b32 v70, 0\nv_mov_b32 v71, 0\nv_mov_b32 v234, 0\nv_mov_b32 v235, 0\n"
"v_mov_b32 v236, 0\nv_mov_b32 v237, 0\nv_mov_b32 v76, 0\nv_mov_b32 v77, 0\nv_mov_b32 v82, 0\nv_mov_b32 v83, 0\n"
"Lq157:\ns_lshl4_add_u32 s65, s41, s42\nM 32,176,136\nR 172,186,40960\ns_lshl_b32 s65, s65, 12\nM 32,180,152\n"
"s_add_u32 s65, s65, 77529088\nM 48,176,140\nR 174,186,45056\nM 48,180,156\nG 4\nM 52,160,140\n"
"R 176,187,32768\nM 52,164,156\nbuffer_load_dwordx4 v221, s[12:15], s63 offen offset:1024 lds\nM 36,160,136\nR 178,187,36864\nM 36,164,152\n"
"s_add_u32 m0, s50, 114688\nM 148,160,132\nR 180,187,40960\nM 148,164,148\ns_add_u32 s63, s101, 256\nM 132,160,128\n"
"R 182,187,45056\nM 132,164,144\nG 4\nM 136,168,128\nR 160,184,49152\nM 136,172,144\n"
"buffer_load_dwordx4 v220, s[12:15], s63 offen lds\nM 152,168,132\nR 162,184,53248\nM 152,172,148\nM 40,168,136\nR 164,184,57344\n"
"s_lshl2_add_u32 s60, s45, s40\ns_waitcnt vmcnt(11)\nv_lshlrev_b32 v224, 3, v252\ns_mul_i32 s60, s60, 7168\ns_bfm_b64 exec, 14, 42\nbuffer_load_dword v224, v224, s[12:15], s60 offen\n"
"s_mov_b64 exec, -1\nM 40,172,152\nM 56,168,140\nR 166,184,61440\nv_mfma_f32_16x16x32_bf16 v[136:139], v[242:245], v[208:211], v[136:139]\nM 56,172,156\n"
"G 4\nM 60,176,140\nR 168,185,49152\nv_mfma_f32_16x16x32_bf16 v[140:143], v[244:247], v[208:211], v[140:143]\nM 60,180,156\nbuffer_load_dwordx4 v221, s[12:15], s63 offen offset:1024 lds\n"
"M 44,176,136\nR 170,185,53248\nv_mfma_f32_16x16x32_bf16 v[152:155], v[240:243], v[210:213], v[152:155]\ns_add_u32 s63, s100, 0\nM 44,180,152\ns_add_u32 m0, s50, 0\n"
"M 156,176,132\nR 172,185,57344\nv_mfma_f32_16x16x32_bf16 v[156:159], v[242:245], v[210:213], v[156:159]\nM 156,180,148\nbuffer_load_dwordx4 v222, s[12:15], s63 offen lds\nM 140,176,128\n"
"R 174,185,61440\nM 140,180,144\nG 4\nM 128,160,160\nR 176,186,49152\nM 128,164,176\n"
"M 144,160,164\nR 178,186,53248\nM 144,164,180\nbuffer_load_dwordx4 v223, s[12:15], s63 offen offset:1024 lds\nM 32,160,168\nR 180,186,57344\n"
"M 32,164,184\nM 48,160,172\nR 182,186,61440\nM 48,164,188\nG 4\nM 52,168,172\n"
"R 160,187,49152\nM 52,172,188\nM 36,168,168\nR 162,187,53248\nM 36,172,184\ns_movk_i32 s63, 2048\n"
"v_mad_u32_u24 v220, v228, s63, v229\nv_add_u32 v221, 1047552, v220\ns_add_u32 m0, s50, 8192\ns_add_u32 s63, s53, 0\nbuffer_load_dwordx4 v220, s[8:11], s63 offen lds\nM 148,168,164\n"
"R 164,187,57344\ns_lshl_b32 s60, s40, 12\ns_add_u32 s60, s60, 49152\nv_add_u32 v226, s60, v252\nv_add_u32 v111, s60, v227\nM 148,172,180\n"
"M 132,168,160\nR 166,187,61440\ns_cmp_eq_u32 s40, 0\ns_cselect_b64 s[88:89], 0, -1\ns_cselect_b32 s60, s41, 0\ns_cmp_lg_u32 s60, 0\n"
"s_cselect_b64 s[66:67], -1, 0\nM 132,172,176\nG 4\nM 136,176,160\nR 168,188,0\nM 136,180,176\n"
"s_mov_b64 exec, s[88:89]\nds_read_b128 v[230:233], v226 offset:0\nds_read_b64 v[64:65], v111 offset:2048\ns_mov_b64 exec, -1\nM 152,176,164\nR 170,188,4096\n"
"s_waitcnt vmcnt(11) lgkmcnt(1)\ns_barrier\nM 152,180,180\nbuffer_load_dwordx4 v221, s[8:11], s63 offen offset:1024 lds\nM 40,176,168\nR 172,188,8192\n"
"M 40,180,184\ns_mov_b64 exec, s[88:89]\nds_read_b64 v[70:71], v111 offset:2560\nds_read_b128 v[234:237], v226 offset:1024\ns_mov_b64 exec, -1\nM 56,176,172\n"
"R 174,188,12288\nM 56,180,188\nM 60,160,172\nR 176,189,0\nM 60,164,188\ns_mov_b64 exec, s[88:89]\n"
"ds_read_b64 v[76:77], v111 offset:3072\nds_read_b64 v[82:83], v111 offset:3584\ns_mov_b64 exec, -1\nM 44,160,168\nR 178,189,4096\nM 44,164,184\n"
"M 156,160,164\nR 180,189,8192\nM 156,164,180\ns_mov_b64 exec, s[66:67]\nbuffer_load_dwordx4 v[230:233], v252, s[20:23], s65 offen offset:0 sc1\ns_mov_b64 exec, -1\n"
"M 140,160,160\nR 182,189,12288\nM 140,164,176\nG 6\nM 32,168,200\nR 160,190,0\n"
"s_mov_b64 exec, s[66:67]\nbuffer_load_dwordx2 v[64:65], v227, s[20:23], s65 offen offset:2048 sc1\ns_mov_b64 exec, -1\nM 32,172,216\nR 162,190,4096\nM 48,168,204\n"
"R 164,190,8192\nM 48,172,220\nR 166,190,12288\nG 4\nM 52,176,204\nR 168,191,0\n"
"s_mov_b64 exec, s[66:67]\nbuffer_load_dwordx2 v[70:71], v227, s[20:23], s65 offen offset:2560 sc1\ns_mov_b64 exec, -1\nM 52,180,220\nR 170,191,4096\nM 36,176,200\n"
"R 172,191,8192\nM 36,180,216\nR 174,191,12288\nG 4\nM 40,160,200\nR 176,188,16384\n"
"s_mov_b64 exec, s[66:67]\nbuffer_load_dwordx4 v[234:237], v252, s[20:23], s65 offen offset:1024 sc1\ns_mov_b64 exec, -1\nM 40,164,216\nR 178,188,20480\nM 56,160,204\n"
"R 180,188,24576\nM 56,164,220\nR 182,188,28672\nG 4\nM 60,168,204\nR 160,189,16384\n"
"s_mov_b64 exec, s[66:67]\nbuffer_load_dwordx2 v[76:77], v227, s[20:23], s65 offen offset:3072 sc1\ns_mov_b64 exec, -1\nM 60,172,220\nR 162,189,20480\nM 44,168,200\n"
"R 164,189,24576\nM 44,172,216\nR 166,189,28672\nG 4\nM 32,176,232\nR 168,190,16384\n"
"s_mov_b64 exec, s[66:67]\nbuffer_load_dwordx2 v[82:83], v227, s[20:23], s65 offen offset:3584 sc1\ns_mov_b64 exec, -1\nLq191:\ns_lshr_b32 s60, s45, 3\ns_lshl2_add_u32 s60, s60, s40\n"
"s_lshl_b32 s60, s60, 8\ns_waitcnt vmcnt(11)\nv_lshl_add_u32 v224, v252, 3, s60\nv_add_u32 v224, Lq191+4096-LqPC, v224\nv_min_u32 v224, LqE-LqPC-4, v224\ns_mov_b64 exec, 3\n"
"global_load_dword v224, v224, s[98:99]\ns_mov_b64 exec, -1\nM 32,180,248\nR 170,190,20480\nM 48,176,236\nR 172,190,24576\n"
"M 48,180,252\nR 174,190,28672\nG 4\nM 52,160,236\nR 176,191,16384\nM 52,164,252\n"
"R 178,191,20480\nM 36,160,232\nR 180,191,24576\nM 36,164,248\nR 182,191,28672\nG 4\n"
"M 40,168,232\nR 160,188,0\nM 40,172,248\nR 162,188,4096\nM 56,168,236\nR 164,188,8192\n"
"M 56,172,252\nR 166,188,12288\nG 4\nM 60,176,236\nR 168,189,0\nM 60,180,252\n"
"R 170,189,4096\nM 44,176,232\nR 172,189,8192\nM 44,180,248\nR 174,189,12288\nG 4\n"
"M 128,160,192\nR 176,190,0\nM 128,164,208\nR 178,190,4096\ns_mov_b32 m0, s51\nM 144,160,196\n"
"R 180,190,8192\nds_write_addtid_b32 v32 offset:0\nM 144,164,212\nR 182,190,12288\nds_write_addtid_b32 v33 offset:256\nG 6\n"
"M 148,168,196\nR 160,191,0\nds_write_addtid_b32 v34 offset:512\nM 148,172,212\nR 162,191,4096\nds_write_addtid_b32 v35 offset:768\n"
"M 132,168,192\nR 164,191,8192\nds_write_addtid_b32 v36 offset:1024\nM 132,172,208\nR 166,191,12288\nds_write_addtid_b32 v37 offset:1280\n"
"G 9\nM 136,176,192\nR 168,188,16384\nds_write_addtid_b32 v38 offset:1536\nM 136,180,208\nR 170,188,20480\n"
"ds_write_addtid_b32 v39 offset:1792\nM 152,176,196\nLpg5:\ns_cbranch_execz LretQ0\nR 172,188,24576\nds_write_addtid_b32 v40 offset:2048\n"
"M 152,180,212\nR 174,188,28672\nds_write_addtid_b32 v41 offset:2304\nG 9\nM 156,160,196\nR 176,189,16384\n"
"ds_write_addtid_b32 v42 offset:2560\nM 156,164,212\nR 178,189,20480\nds_write_addtid_b32 v43 offset:2816\nM 140,160,192\nR 180,189,24576\n"
"ds_write_addtid_b32 v44 offset:3072\nds_write_addtid_b32 v45 offset:3328\nds_write_addtid_b32 v46 offset:3584\nds_write_addtid_b32 v47 offset:3840\nds_write_addtid_b32 v48 offset:4096\nds_write_addtid_b32 v49 offset:4352\n"
"ds_write_addtid_b32 v50 offset:4608\nds_write_addtid_b32 v51 offset:4864\nds_write_addtid_b32 v52 offset:5120\nds_write_addtid_b32 v53 offset:5376\nds_write_addtid_b32 v54 offset:5632\nds_write_addtid_b32 v55 offset:5888\n"
"ds_write_addtid_b32 v56 offset:6144\nds_write_addtid_b32 v57 offset:6400\nds_write_addtid_b32 v58 offset:6656\nds_write_addtid_b32 v59 offset:6912\nds_write_addtid_b32 v60 offset:7168\nds_write_addtid_b32 v61 offset:7424\n"
"ds_write_addtid_b32 v62 offset:7680\nds_write_addtid_b32 v63 offset:7936\nM 140,164,208\nR 182,189,28672\nG 15\nM 128,168,224\n"
"R 160,190,16384\ns_add_u32 m0, s50, 16384\nM 128,172,240\nR 162,190,20480\nG 3\ns_barrier\n"
"s_add_u32 s63, s53, 65536\nM 144,168,228\nR 164,190,24576\nbuffer_load_dwordx4 v220, s[8:11], s63 offen lds\nds_read2st64_b32 v[32:33], v225 offset0:0 offset1:1\nM 144,172,244\n"
"R 166,190,28672\nbuffer_load_dwordx4 v221, s[8:11], s63 offen offset:1024 lds\nds_read2st64_b32 v[34:35], v225 offset0:2 offset1:3\nG 6\nM 148,176,228\nR 168,191,16384\n"
"s_add_u32 m0, s50, 24576\ns_add_u32 s63, s53, 131072\nbuffer_load_dwordx4 v220, s[8:11], s63 offen lds\nds_read2st64_b32 v[36:37], v225 offset0:4 offset1:5\nM 148,180,244\nR 170,191,20480\n"
"buffer_load_dwordx4 v221, s[8:11], s63 offen offset:1024 lds\nds_read2st64_b32 v[38:39], v225 offset0:6 offset1:7\nM 132,176,224\nR 172,191,24576\ns_add_u32 m0, s50, 32768\ns_add_u32 s63, s53, 196608\n"
"buffer_load_dwordx4 v220, s[8:11], s63 offen lds\nds_read2st64_b32 v[40:41], v225 offset0:8 offset1:9\nM 132,180,240\nR 174,191,28672\nbuffer_load_dwordx4 v221, s[8:11], s63 offen offset:1024 lds\nds_read2st64_b32 v[42:43], v225 offset0:10 offset1:11\n"
"G 9\nM 136,160,224\ns_add_u32 m0, s50, 40960\ns_add_u32 s63, s53, 262144\nbuffer_load_dwordx4 v220, s[8:11], s63 offen lds\nds_read2st64_b32 v[44:45], v225 offset0:12 offset1:13\n"
"M 136,164,240\nbuffer_load_dwordx4 v221, s[8:11], s63 offen offset:1024 lds\nds_read2st64_b32 v[46:47], v225 offset0:14 offset1:15\nM 152,160,228\ns_add_u32 m0, s50, 49152\ns_add_u32 s63, s53, 327680\n"
"buffer_load_dwordx4 v220, s[8:11], s63 offen lds\nds_read2st64_b32 v[48:49], v225 offset0:16 offset1:17\nM 152,164,244\nds_read2st64_b32 v[50:51], v225 offset0:18 offset1:19\nds_read2st64_b32 v[52:53], v225 offset0:20 offset1:21\nG 6\n"
"M 156,168,228\nds_read2st64_b32 v[54:55], v225 offset0:22 offset1:23\nds_read2st64_b32 v[56:57], v225 offset0:24 offset1:25\nM 156,172,244\nds_read2st64_b32 v[58:59], v225 offset0:26 offset1:27\nds_read2st64_b32 v[60:61], v225 offset0:28 offset1:29\n"
"M 140,168,224\nds_read2st64_b32 v[62:63], v225 offset0:30 offset1:31\nM 140,172,240\nG 0\ns_barrier\nv_pk_add_f32 v[144:145], v[144:145], v[48:49]\n"
"v_pk_add_f32 v[146:147], v[146:147], v[50:51]\nv_pk_add_f32 v[148:149], v[148:149], v[52:53]\nv_pk_add_f32 v[150:151], v[150:151], v[54:55]\nv_pk_add_f32 v[152:153], v[152:153], v[56:57]\nv_pk_add_f32 v[154:155], v[154:155], v[58:59]\nv_pk_add_f32 v[156:157], v[156:157], v[60:61]\n"
"v_pk_add_f32 v[158:159], v[158:159], v[62:63]\nv_pk_add_f32 v[128:129], v[128:129], v[32:33]\nv_pk_add_f32 v[130:131], v[130:131], v[34:35]\nv_pk_add_f32 v[132:133], v[132:133], v[36:37]\nv_pk_add_f32 v[134:135], v[134:135], v[38:39]\nv_pk_add_f32 v[136:137], v[136:137], v[40:41]\n"
"v_pk_add_f32 v[138:139], v[138:139], v[42:43]\nv_pk_add_f32 v[140:141], v[140:141], v[44:45]\nv_pk_add_f32 v[142:143], v[142:143], v[46:47]\nv_cvt_pk_bf16_f32 v144, v144, v145\nv_cvt_pk_bf16_f32 v145, v146, v147\nv_cvt_pk_bf16_f32 v146, v148, v149\n"
"v_cvt_pk_bf16_f32 v147, v150, v151\nv_cvt_pk_bf16_f32 v152, v152, v153\nv_cvt_pk_bf16_f32 v153, v154, v155\nv_cvt_pk_bf16_f32 v154, v156, v157\nv_cvt_pk_bf16_f32 v155, v158, v159\nv_cvt_pk_bf16_f32 v128, v128, v129\n"
"v_cvt_pk_bf16_f32 v129, v130, v131\nv_cvt_pk_bf16_f32 v130, v132, v133\nv_cvt_pk_bf16_f32 v131, v134, v135\nv_cvt_pk_bf16_f32 v136, v136, v137\nv_cvt_pk_bf16_f32 v137, v138, v139\nv_cvt_pk_bf16_f32 v138, v140, v141\n"
"v_cvt_pk_bf16_f32 v139, v142, v143\nv_mfma_f32_16x16x32_bf16 v[52:55], v[16:19], v[144:147], v[96:99]\nv_mfma_f32_16x16x32_bf16 v[48:51], v[0:3], v[144:147], v[100:103]\nv_mfma_f32_16x16x32_bf16 v[60:63], v[20:23], v[152:155], v[96:99]\nv_mfma_f32_16x16x32_bf16 v[56:59], v[4:7], v[152:155], v[100:103]\ns_waitcnt vmcnt(13)\n"
"v_max3_u32 v248, v233, v65, v71\nv_cmp_eq_u32 vcc, -1, v248\ns_nop 0\ns_cbranch_vccnz Lq193\nLq194:\ns_waitcnt vmcnt(10)\n"
"v_max3_u32 v248, v237, v77, v83\nv_cmp_eq_u32 vcc, -1, v248\ns_nop 0\ns_cbranch_vccnz Lq195\nLq196:\nv_mfma_f32_16x16x32_bf16 v[36:39], v[0:3], v[128:131], v[96:99]\n"
"v_mfma_f32_16x16x32_bf16 v[32:35], v[230:233], v[128:131], v[104:107]\nv_mfma_f32_16x16x32_bf16 v[44:47], v[4:7], v[136:139], v[96:99]\nv_mfma_f32_16x16x32_bf16 v[40:43], v[234:237], v[136:139], v[104:107]\ns_lshl_b32 s84, s42, 11\ns_add_u32 s84, s84, s49\ns_add_u32 s84, s84, 2097152\n"
"s_add_u32 s85, s84, 32768\ns_lshl2_add_u32 s60, s45, s40\ns_waitcnt vmcnt(9)\nv_lshlrev_b32 v224, 3, v252\ns_mul_i32 s60, s60, 7168\ns_bfm_b64 exec, 28, 28\n"
"buffer_load_dword v224, v224, s[16:19], s60 offen\ns_mov_b64 exec, -1\nv_mov_b32 v238, s46\nv_mov_b32 v239, s47\nv_max_f32 v52, v52, v53\nv_max_f32 v54, v54, v55\n"
"v_max3_f32 v52, v52, v48, v49\nv_max3_f32 v54, v54, v50, v51\nv_max_f32 v118, v52, v54\nv_max_f32 v60, v60, v61\nv_max_f32 v62, v62, v63\nv_max3_f32 v60, v60, v56, v57\n"
"v_max3_f32 v62, v62, v58, v59\nv_max_f32 v119, v60, v62\nv_max_f32 v36, v36, v37\nv_max_f32 v38, v38, v39\nv_max3_f32 v36, v36, v32, v33\nv_max3_f32 v38, v38, v34, v35\n"
"v_max_f32 v116, v36, v38\nv_max_f32 v44, v44, v45\nv_max_f32 v46, v46, v47\nv_max3_f32 v44, v44, v40, v41\nv_max3_f32 v46, v46, v42, v43\nv_max_f32 v117, v44, v46\n"
"v_permlane32_swap_b32 v116, v118\ns_nop 0\nv_permlane32_swap_b32 v117, v119\nv_max_f32 v116, v116, v118\nv_max_f32 v117, v117, v119\ns_nop 1\n"
"v_permlane16_swap_b32 v116, v117\nv_max_f32 v116, v116, v117\nv_mov_b32 v117, v116\ns_nop 1\nv_permlane16_swap_b32 v116, v117\nv_mov_b32 v118, v116\n"
"v_mov_b32 v119, v117\ns_nop 0\nv_permlane32_swap_b32 v116, v118\nv_permlane32_swap_b32 v117, v119\nv_mul_f32 v116, v116, v215\nv_mul_f32 v118, v118, v218\n"
"v_mul_f32 v117, v117, v215\nv_mul_f32 v119, v119, v218\nv_fma_f32 v52, v52, v218, -v118\nv_fma_f32 v54, v54, v218, -v118\nv_fma_f32 v60, v60, v218, -v119\nv_fma_f32 v62, v62, v218, -v119\n"
"v_fma_f32 v36, v36, v215, -v116\nv_fma_f32 v38, v38, v215, -v116\nv_fma_f32 v44, v44, v215, -v117\nv_fma_f32 v46, v46, v215, -v117\nv_exp_f32 v52, v52\nv_exp_f32 v54, v54\n"
"v_exp_f32 v60, v60\nv_exp_f32 v62, v62\nv_exp_f32 v36, v36\nv_exp_f32 v38, v38\nv_exp_f32 v44, v44\nv_exp_f32 v46, v46\n"
"v_add_f32 v118, v52, v54\nv_add_f32 v119, v60, v62\nv_add_f32 v116, v36, v38\nv_add_f32 v117, v44, v46\ns_nop 0\nv_permlane32_swap_b32 v116, v118\n"
"v_permlane32_swap_b32 v117, v119\nv_add_f32 v116, v116, v118\nv_add_f32 v117, v117, v119\ns_nop 1\nv_permlane16_swap_b32 v116, v117\nv_add_f32 v116, v116, v117\n"
"v_rcp_f32 v116, v116\ns_nop 0\nv_mov_b32 v117, v116\ns_nop 1\nv_permlane16_swap_b32 v116, v117\nv_mov_b32 v118, v116\n"
"v_mov_b32 v119, v117\ns_nop 0\nv_permlane32_swap_b32 v116, v118\nv_permlane32_swap_b32 v117, v119\nv_mul_f32 v52, v52, v118\nv_mul_f32 v54, v54, v118\n"
"v_mul_f32 v60, v60, v119\nv_mul_f32 v62, v62, v119\nv_mul_f32 v36, v36, v116\nv_mul_f32 v38, v38, v116\nv_mul_f32 v44, v44, v117\nv_mul_f32 v46, v46, v117\n"
"v_cvt_pk_bf16_f32 v52, v52, v52\nv_cvt_pk_bf16_f32 v54, v54, v54\nv_cvt_pk_bf16_f32 v60, v60, v60\nv_cvt_pk_bf16_f32 v62, v62, v62\nv_cvt_pk_bf16_f32 v36, v36, v36\nv_cvt_pk_bf16_f32 v38, v38, v38\n"
"v_cvt_pk_bf16_f32 v44, v44, v44\nv_cvt_pk_bf16_f32 v46, v46, v46\nv_and_b32 v48, v52, v112\nv_and_b32 v49, v54, v113\nv_and_b32 v50, v52, v114\nv_and_b32 v51, v54, v115\n"
"v_and_b32 v56, v60, v112\nv_and_b32 v57, v62, v113\nv_and_b32 v58, v60, v114\nv_and_b32 v59, v62, v115\nv_mfma_f32_16x16x32_bf16 v[144:147], v[66:69], v[48:51], 0\nv_mfma_f32_16x16x32_bf16 v[148:151], v[72:75], v[48:51], 0\n"
"v_and_b32 v32, v36, v112\nv_and_b32 v33, v38, v113\nv_and_b32 v34, v36, v114\nv_and_b32 v35, v38, v115\nv_mfma_f32_16x16x32_bf16 v[152:155], v[78:81], v[56:59], 0\nv_mfma_f32_16x16x32_bf16 v[156:159], v[84:87], v[56:59], 0\n"
"v_and_b32 v40, v44, v112\nv_and_b32 v41, v46, v113\nv_and_b32 v42, v44, v114\nv_and_b32 v43, v46, v115\nv_mfma_f32_16x16x32_bf16 v[128:131], v[64:67], v[32:35], 0\nv_mfma_f32_16x16x32_bf16 v[132:135], v[70:73], v[32:35], 0\n"
"v_mfma_f32_16x16x32_bf16 v[136:139], v[76:79], v[40:43], 0\nv_mfma_f32_16x16x32_bf16 v[140:143], v[82:85], v[40:43], 0\nv_cvt_pk_bf16_f32 v144, v144, v145\nv_cvt_pk_bf16_f32 v145, v146, v147\nv_cvt_pk_bf16_f32 v146, v148, v149\nv_cvt_pk_bf16_f32 v147, v150, v151\n"
"buffer_store_dwordx4 v[144:147], v252, s[20:23], s85 offen offset:0 sc1\nv_cvt_pk_bf16_f32 v152, v152, v153\nv_cvt_pk_bf16_f32 v153, v154, v155\nv_cvt_pk_bf16_f32 v154, v156, v157\nv_cvt_pk_bf16_f32 v155, v158, v159\nbuffer_store_dwordx4 v[152:155], v252, s[20:23], s85 offen offset:1024 sc1\n"
"v_cvt_pk_bf16_f32 v128, v128, v129\nv_cvt_pk_bf16_f32 v129, v130, v131\nv_cvt_pk_bf16_f32 v130, v132, v133\nv_cvt_pk_bf16_f32 v131, v134, v135\nbuffer_store_dwordx4 v[128:131], v252, s[20:23], s84 offen offset:0 sc1\nv_cvt_pk_bf16_f32 v136, v136, v137\n"
"v_cvt_pk_bf16_f32 v137, v138, v139\nv_cvt_pk_bf16_f32 v138, v140, v141\nv_cvt_pk_bf16_f32 v139, v142, v143\nbuffer_store_dwordx4 v[136:139], v252, s[20:23], s84 offen offset:1024 sc1\ns_mov_b64 exec, 0\ns_cmp_eq_u32 s40, 1\n"
"s_cbranch_scc1 Lpg7\ns_cmp_eq_u32 s40, 2\ns_cbranch_scc1 LretO0\ns_cmp_eq_u32 s40, 3\ns_cbranch_scc1 LretO0\ns_branch Lpg6\n"
"LretO0:\ns_mov_b64 exec, -1\ns_add_u32 s61, s79, 88080384\ns_lshl_b32 s62, s42, 3\ns_add_u32 s61, s61, s62\ns_waitcnt vmcnt(0)\n"
"s_mov_b64 exec, 1\nbuffer_atomic_swap_x2 v[238:239], off, s[20:23], s61 sc1\ns_mov_b64 exec, -1\nv_bfe_u32 v250, v252, 7, 1\nv_mul_u32_u24 v250, 2176, v250\nv_bfe_u32 v251, v252, 4, 3\n"
"v_lshl_add_u32 v250, v251, 3, v250\ns_lshl_b32 s60, s90, 6\nv_add_u32 v250, s60, v250\ns_sleep 5\nv_mov_b32 v248, 0\nv_mov_b32 v249, 0\n"
"s_add_u32 s61, s80, 88080384\ns_mov_b64 exec, 0xffff\nbuffer_atomic_or_x2 v[248:249], v250, s[20:23], s61 offen sc0 sc1\ns_mov_b64 exec, -1\nbuffer_load_dwordx4 v221, s[8:11], s63 offen offset:1024 lds\ns_add_u32 m0, s50, 57344\n"
"s_add_u32 s63, s53, 393216\nbuffer_load_dwordx4 v220, s[8:11], s63 offen lds\nbuffer_load_dwordx4 v221, s[8:11], s63 offen offset:1024 lds\ns_add_u32 m0, s50, 65536\ns_add_u32 s63, s53, 458752\nbuffer_load_dwordx4 v220, s[8:11], s63 offen lds\n"
"buffer_load_dwordx4 v221, s[8:11], s63 offen offset:1024 lds\ns_add_u32 m0, s50, 73728\ns_add_u32 s63, s53, 524288\nbuffer_load_dwordx4 v220, s[8:11], s63 offen lds\nbuffer_load_dwordx4 v221, s[8:11], s63 offen offset:1024 lds\ns_add_u32 m0, s50, 81920\n"
"s_add_u32 s63, s53, 589824\nbuffer_load_dwordx4 v220, s[8:11], s63 offen lds\nbuffer_load_dwordx4 v221, s[8:11], s63 offen offset:1024 lds\ns_add_u32 m0, s50, 90112\ns_add_u32 s63, s53, 655360\nbuffer_load_dwordx4 v220, s[8:11], s63 offen lds\n"
"buffer_load_dwordx4 v221, s[8:11], s63 offen offset:1024 lds\nv_bfe_u32 v234, v252, 4, 4\nv_lshrrev_b32 v235, 8, v252\nv_lshlrev_b32 v235, 2, v235\nv_sub_u32 v234, v234, v235\nv_lshlrev_b32 v235, 4, v234\n"
"s_mov_b64 s[60:61], 0x3f80\nv_lshlrev_b64 v[208:209], v235, s[60:61]\nv_cmp_gt_u32 s[60:61], 4, v234\ns_nop 1\nv_cndmask_b32 v212, 0, v208, s[60:61]\nv_cndmask_b32 v208, 0, v208, s[60:61]\n"
"v_cndmask_b32 v213, 0, v209, s[60:61]\nv_cndmask_b32 v209, 0, v209, s[60:61]\ns_nop 1\nv_mfma_f32_16x16x32_bf16 v[0:3], v[208:211], v[192:195], 0\nv_mfma_f32_16x16x32_bf16 v[4:7], v[210:213], v[192:195], 0\nv_mfma_f32_16x16x32_bf16 v[8:11], v[208:211], v[196:199], 0\n"
"v_mfma_f32_16x16x32_bf16 v[12:15], v[210:213], v[196:199], 0\nv_mfma_f32_16x16x32_bf16 v[16:19], v[208:211], v[200:203], 0\nv_mfma_f32_16x16x32_bf16 v[20:23], v[210:213], v[200:203], 0\nv_mfma_f32_16x16x32_bf16 v[24:27], v[208:211], v[204:207], 0\nv_mfma_f32_16x16x32_bf16 v[28:31], v[210:213], v[204:207], 0\ns_waitcnt vmcnt(11)\n"
"v_cmp_eq_u64 s[86:87], s[46:47], v[248:249]\ns_nop 0\ns_and_b32 s62, s86, 0xffff\ns_cmp_eq_u32 s62, 0xffff\ns_cbranch_scc0 Lq198\nLq199:\n"
"s_add_u32 s64, s70, 2097152\nbuffer_load_dwordx4 a[0:3], v252, s[20:23], s64 offen offset:0 sc1\ns_add_u32 s65, s70, 2129920\nbuffer_load_dwordx4 a[4:7], v252, s[20:23], s65 offen offset:0 sc1\ns_add_u32 s66, s71, 2097152\nbuffer_load_dwordx4 a[8:11], v252, s[20:23], s66 offen offset:0 sc1\n"
"s_add_u32 s67, s71, 2129920\nbuffer_load_dwordx4 a[12:15], v252, s[20:23], s67 offen offset:0 sc1\nbuffer_load_dwordx4 a[16:19], v252, s[20:23], s64 offen offset:1024 sc1\nbuffer_load_dwordx4 a[20:23], v252, s[20:23], s65 offen offset:1024 sc1\nbuffer_load_dwordx4 a[24:27], v252, s[20:23], s66 offen offset:1024 sc1\nbuffer_load_dwordx4 a[28:31], v252, s[20:23], s67 offen offset:1024 sc1\n"
"s_barrier\nR 160,184,8192\nR 162,184,12288\nR 164,184,16384\nR 166,184,20480\nR 168,185,8192\n"
"R 170,185,12288\nR 172,185,16384\nR 174,185,20480\nR 176,186,8192\nR 178,186,12288\nR 180,186,16384\n"
"R 182,186,20480\nbuffer_load_dwordx4 a[32:35], v252, s[20:23], s64 offen offset:2048 sc1\nbuffer_load_dwordx4 a[36:39], v252, s[20:23], s65 offen offset:2048 sc1\nbuffer_load_dwordx4 a[40:43], v252, s[20:23], s66 offen offset:2048 sc1\nbuffer_load_dwordx4 a[44:47], v252, s[20:23], s67 offen offset:2048 sc1\nbuffer_load_dwordx4 a[48:51], v252, s[20:23], s64 offen offset:3072 sc1\n"
"buffer_load_dwordx4 a[52:55], v252, s[20:23], s65 offen offset:3072 sc1\nbuffer_load_dwordx4 a[56:59], v252, s[20:23], s66 offen offset:3072 sc1\nbuffer_load_dwordx4 a[60:63], v252, s[20:23], s67 offen offset:3072 sc1\ns_add_u32 s64, s70, 2101248\nbuffer_load_dwordx4 a[64:67], v252, s[20:23], s64 offen offset:0 sc1\ns_add_u32 s65, s70, 2134016\n"
"buffer_load_dwordx4 a[68:71], v252, s[20:23], s65 offen offset:0 sc1\ns_add_u32 s66, s71, 2101248\nbuffer_load_dwordx4 a[72:75], v252, s[20:23], s66 offen offset:0 sc1\ns_add_u32 s67, s71, 2134016\nbuffer_load_dwordx4 a[76:79], v252, s[20:23], s67 offen offset:0 sc1\ns_waitcnt vmcnt(19) lgkmcnt(10)\n"
"M 0,160,0\nbuffer_load_dwordx4 a[80:83], v252, s[20:23], s64 offen offset:1024 sc1\ns_waitcnt vmcnt(16) lgkmcnt(8)\nM 0,164,16\nM 16,160,4\ns_lshl_b32 s60, s40, 5\n"
"v_xor_b32 v238, s60, v188\nv_and_b32 v238, 0xfffffbff, v238\ns_waitcnt vmcnt(15)\nM 16,164,20\nv_mov_b32 v242, s76\nv_mov_b32 v243, s76\n"
"v_mfma_f32_16x16x32_bf16 v[32:35], v[160:163], a[8:11], 0\nbuffer_load_dwordx4 a[84:87], v252, s[20:23], s65 offen offset:1024 sc1\ns_waitcnt vmcnt(15)\nM 32,164,24\nv_mov_b32 v244, s76\nv_mov_b32 v245, s76\n"
"v_mfma_f32_16x16x32_bf16 v[48:51], v[160:163], a[12:15], 0\ns_waitcnt vmcnt(14)\nM 48,164,28\nG 4\nv_mfma_f32_16x16x32_bf16 v[52:55], v[168:171], a[12:15], 0\nR 160,187,8192\n"
"M 52,172,28\nbuffer_load_dwordx4 a[88:91], v252, s[20:23], s66 offen offset:1024 sc1\nv_mfma_f32_16x16x32_bf16 v[36:39], v[168:171], a[8:11], 0\nR 162,187,12288\nM 36,172,24\nM 20,168,4\n"
"R 164,187,16384\nR 64,238,32768\nR 66,238,36864\nM 20,172,20\nbuffer_load_dwordx4 a[92:95], v252, s[20:23], s67 offen offset:1024 sc1\nM 4,168,0\n"
"R 166,187,20480\nR 68,238,33792\nR 70,238,37888\nM 4,172,16\nG 8\nM 8,176,0\n"
"R 168,184,24576\nM 8,180,16\nbuffer_load_dwordx4 a[96:99], v252, s[20:23], s64 offen offset:2048 sc1\nM 24,176,4\nR 170,184,28672\nM 24,180,20\n"
"v_mfma_f32_16x16x32_bf16 v[40:43], v[176:179], a[8:11], 0\nR 172,184,32768\nG 6\nv_mfma_f32_16x16x32_bf16 v[72:75], v[242:245], v[64:67], 0\nM 40,180,24\nG 3\n"
"v_mfma_f32_16x16x32_bf16 v[72:75], v[242:245], v[68:71], v[72:75]\nbuffer_load_dwordx4 a[100:103], v252, s[20:23], s65 offen offset:2048 sc1\nv_mfma_f32_16x16x32_bf16 v[56:59], v[176:179], a[12:15], 0\nR 174,184,36864\nM 56,180,28\nv_mfma_f32_16x16x32_bf16 v[60:63], v[160:163], a[12:15], 0\n"
"R 176,185,24576\nM 60,164,28\nbuffer_load_dwordx4 a[104:107], v252, s[20:23], s66 offen offset:2048 sc1\nv_lshrrev_b32 v64, 1, v252\nv_mfma_f32_16x16x32_bf16 v[44:47], v[160:163], a[8:11], 0\nR 178,185,28672\n"
"v_mov_b32 v73, s46\ns_and_b32 s61, s40, 3\ns_lshl_b32 s61, s61, 7\ns_add_u32 s61, s61, s91\ns_add_u32 s61, s61, 393216\ns_mov_b64 exec, 0xffff\n"
"buffer_store_dwordx2 v[72:73], v64, s[20:23], s61 offen sc1\ns_mov_b64 exec, -1\nM 44,164,24\nM 28,160,4\nR 180,185,32768\nM 28,164,20\n"
"buffer_load_dwordx4 a[108:111], v252, s[20:23], s67 offen offset:2048 sc1\nM 12,160,0\nR 182,185,36864\nM 12,164,16\ns_lshl_b32 s60, s40, 5\nv_xor_b32 v238, s60, v188\n"
"G 4\ns_waitcnt vmcnt(20)\nM 0,168,32\nR 160,186,24576\nv_and_b32 v238, 0xfffffbff, v238\ns_waitcnt vmcnt(16)\n"
"M 0,172,48\nbuffer_load_dwordx4 a[112:115], v252, s[20:23], s64 offen offset:3072 sc1\nM 16,168,36\nR 162,186,28672\ns_waitcnt vmcnt(16)\nM 16,172,52\n"
"M 32,168,40\nR 164,186,32768\ns_waitcnt vmcnt(15)\nM 32,172,56\nbuffer_load_dwordx4 a[116:119], v252, s[20:23], s65 offen offset:3072 sc1\ns_barrier\n"
"M 48,168,44\nR 166,186,36864\nR 64,238,40960\nR 66,238,45056\ns_waitcnt vmcnt(15)\nM 48,172,60\n"
"G 6\nM 52,176,44\nR 168,187,24576\nR 68,238,41984\nR 70,238,46080\nM 52,180,60\n"
"buffer_load_dwordx4 a[120:123], v252, s[20:23], s66 offen offset:3072 sc1\nM 36,176,40\nR 170,187,28672\nM 36,180,56\nM 20,176,36\nR 172,187,32768\n"
"M 20,180,52\nbuffer_load_dwordx4 a[124:127], v252, s[20:23], s67 offen offset:3072 sc1\nM 4,176,32\nR 174,187,36864\nG 6\nv_mfma_f32_16x16x32_bf16 v[72:75], v[242:245], v[64:67], 0\n"
"M 4,180,48\nG 3\nv_mfma_f32_16x16x32_bf16 v[72:75], v[242:245], v[68:71], v[72:75]\nM 8,160,32\nR 176,184,40960\nM 8,164,48\n"
"s_add_u32 s64, s70, 2105344\nbuffer_load_dwordx4 a[128:131], v252, s[20:23], s64 offen offset:0 sc1\nM 24,160,36\nR 178,184,45056\nM 24,164,52\nv_lshrrev_b32 v64, 1, v252\n"
"v_mov_b32 v73, s46\ns_and_b32 s61, s40, 3\ns_lshl_b32 s61, s61, 7\ns_add_u32 s61, s61, s91\ns_add_u32 s61, s61, 524288\ns_mov_b64 exec, 0xffff\n"
"buffer_store_dwordx2 v[72:73], v64, s[20:23], s61 offen sc1\ns_mov_b64 exec, -1\nM 40,160,40\nR 180,184,49152\nM 40,164,56\ns_add_u32 s65, s70, 2138112\n"
"buffer_load_dwordx4 a[132:135], v252, s[20:23], s65 offen offset:0 sc1\nM 56,160,44\nR 182,184,53248\nM 56,164,60\ns_lshl_b32 s60, s40, 5\nv_xor_b32 v238, s60, v188\n"
"G 4\nM 60,168,44\nR 160,185,40960\nv_and_b32 v238, 0xfffffbff, v238\nM 60,172,60\ns_add_u32 s66, s71, 2105344\n"
"buffer_load_dwordx4 a[136:139], v252, s[20:23], s66 offen offset:0 sc1\nM 44,168,40\nR 162,185,45056\nM 44,172,56\nM 28,168,36\nR 164,185,49152\n"
"M 28,172,52\ns_add_u32 s67, s71, 2138112\nbuffer_load_dwordx4 a[140:143], v252, s[20:23], s67 offen offset:0 sc1\nM 12,168,32\nR 166,185,53248\nM 12,172,48\n"
"s_barrier\nG 4\ns_waitcnt vmcnt(21)\nM 0,176,64\nR 168,186,40960\nR 64,238,49152\n"
"R 66,238,53248\nLpg6:\ns_cbranch_execz LretO0\ns_waitcnt vmcnt(17)\nM 0,180,80\nbuffer_load_dwordx4 a[144:147], v252, s[20:23], s64 offen offset:1024 sc1\n"
"M 16,176,68\nR 170,186,45056\nR 68,238,50176\nR 70,238,54272\ns_waitcnt vmcnt(17)\nM 16,180,84\n"
"M 32,176,72\nR 172,186,49152\ns_waitcnt vmcnt(16)\nM 32,180,88\nbuffer_load_dwordx4 a[148:151], v252, s[20:23], s65 offen offset:1024 sc1\nM 48,176,76\n"
"R 174,186,53248\ns_add_u32 m0, s50, 98304\ns_waitcnt vmcnt(16)\nM 48,180,92\nG 8\nM 52,160,76\n"
"R 176,187,40960\nG 6\nv_mfma_f32_16x16x32_bf16 v[72:75], v[242:245], v[64:67], 0\ns_add_u32 s63, s53, 720896\nM 52,164,92\nG 3\n"
"v_mfma_f32_16x16x32_bf16 v[72:75], v[242:245], v[68:71], v[72:75]\nbuffer_load_dwordx4 a[152:155], v252, s[20:23], s66 offen offset:1024 sc1\nM 36,160,72\nR 178,187,45056\nM 36,164,88\nbuffer_load_dwordx4 v220, s[8:11], s63 offen lds\n"
"M 20,160,68\nR 180,187,49152\nM 20,164,84\nbuffer_load_dwordx4 a[156:159], v252, s[20:23], s67 offen offset:1024 sc1\nv_lshrrev_b32 v64, 1, v252\nM 4,160,64\n"
"R 182,187,53248\nv_mov_b32 v73, s46\ns_and_b32 s61, s40, 3\ns_lshl_b32 s61, s61, 7\ns_add_u32 s61, s61, s91\ns_add_u32 s61, s61, 655360\n"
"s_mov_b64 exec, 0xffff\nbuffer_store_dwordx2 v[72:73], v64, s[20:23], s61 offen sc1\ns_mov_b64 exec, -1\nM 4,164,80\nG 4\nM 8,168,64\n"
"R 160,184,57344\nM 8,172,80\nbuffer_load_dwordx4 a[160:163], v252, s[20:23], s64 offen offset:2048 sc1\nM 24,168,68\nR 162,184,61440\nbuffer_load_dwordx4 v221, s[8:11], s63 offen offset:1024 lds\n"
"M 24,172,84\nM 40,168,72\nR 164,188,0\nM 40,172,88\nbuffer_load_dwordx4 a[164:167], v252, s[20:23], s65 offen offset:2048 sc1\nM 56,168,76\n"
"R 166,188,4096\nM 56,172,92\ns_and_b32 s60, s41, 1\ns_lshl1_add_u32 s60, s60, s90\ns_lshl_b32 s60, s60, 5\nv_xor_b32 v238, s60, v184\n"
"G 4\nM 60,176,76\nR 168,185,57344\nv_and_b32 v238, 0xfffffbff, v238\nM 60,180,92\nbuffer_load_dwordx4 a[168:171], v252, s[20:23], s66 offen offset:2048 sc1\n"
"M 44,176,72\nR 170,185,61440\nR 64,238,0\nR 66,238,4096\nM 44,180,88\ns_add_u32 m0, s50, 106496\n"
"M 28,176,68\nR 172,189,0\nR 68,238,1024\nR 70,238,5120\nM 28,180,84\nbuffer_load_dwordx4 a[172:175], v252, s[20:23], s67 offen offset:2048 sc1\n"
"M 12,176,64\nR 174,189,4096\ns_add_u32 s63, s53, 786432\nM 12,180,80\ns_barrier\nG 8\n"
"s_waitcnt vmcnt(24)\nM 0,160,96\nR 176,186,57344\ns_waitcnt vmcnt(19)\nM 0,164,112\nbuffer_load_dwordx4 a[176:179], v252, s[20:23], s64 offen offset:3072 sc1\n"
"buffer_load_dwordx4 v220, s[8:11], s63 offen lds\nM 16,160,100\nR 178,186,61440\nG 6\nv_mfma_f32_16x16x32_bf16 v[72:75], v[242:245], v[64:67], 0\ns_waitcnt vmcnt(20)\n"
"M 16,164,116\nG 3\nv_mfma_f32_16x16x32_bf16 v[72:75], v[242:245], v[68:71], v[72:75]\nbuffer_load_dwordx4 v221, s[8:11], s63 offen offset:1024 lds\nM 32,160,104\nR 180,190,0\n"
"s_add_u32 m0, s50, 114688\ns_waitcnt vmcnt(20)\nM 32,164,120\nbuffer_load_dwordx4 a[180:183], v252, s[20:23], s65 offen offset:3072 sc1\nM 48,160,108\nR 182,190,4096\n"
"s_add_u32 s63, s53, 851968\ns_waitcnt vmcnt(20)\nM 48,164,124\nbuffer_load_dwordx4 v220, s[8:11], s63 offen lds\nG 4\nv_lshrrev_b32 v64, 1, v252\n"
"M 52,168,108\nR 160,187,57344\nv_mov_b32 v73, s46\ns_and_b32 s61, s40, 1\ns_lshl_b32 s61, s61, 7\ns_add_u32 s61, s61, s91\n"
"s_add_u32 s61, s61, 786432\ns_mov_b64 exec, 0xffff\nbuffer_store_dwordx2 v[72:73], v64, s[20:23], s61 offen sc1\ns_mov_b64 exec, -1\nM 52,172,124\nbuffer_load_dwordx4 a[184:187], v252, s[20:23], s66 offen offset:3072 sc1\n"
"M 36,168,104\nR 162,187,61440\nM 36,172,120\nbuffer_load_dwordx4 v221, s[8:11], s63 offen offset:1024 lds\nM 20,168,100\nR 164,191,0\n"
"M 20,172,116\nbuffer_load_dwordx4 a[188:191], v252, s[20:23], s67 offen offset:3072 sc1\nM 4,168,96\nR 166,191,4096\nM 4,172,112\nG 4\n"
"M 8,176,96\nR 168,188,8192\nM 8,180,112\ns_add_u32 s64, s70, 2109440\nbuffer_load_dwordx4 a[192:195], v252, s[20:23], s64 offen offset:0 sc1\nM 24,176,100\n"
"R 170,188,12288\nM 24,180,116\nM 40,176,104\nR 172,188,16384\nM 40,180,120\ns_add_u32 s65, s70, 2142208\n"
"buffer_load_dwordx4 a[196:199], v252, s[20:23], s65 offen offset:0 sc1\nM 56,176,108\nR 174,188,20480\nM 56,180,124\nG 4\nM 60,160,108\n"
"R 176,189,8192\nM 60,164,124\ns_add_u32 s66, s71, 2109440\nbuffer_load_dwordx4 a[200:203], v252, s[20:23], s66 offen offset:0 sc1\nM 44,160,104\nR 178,189,12288\n"
"s_add_u32 s63, s53, 917504\nM 44,164,120\ns_add_u32 m0, s50, 0\nM 28,160,100\nR 180,189,16384\nM 28,164,116\n"
"s_add_u32 s67, s71, 2142208\nbuffer_load_dwordx4 a[204:207], v252, s[20:23], s67 offen offset:0 sc1\nM 12,160,96\nR 182,189,20480\nM 12,164,112\ns_waitcnt vmcnt(16)\n"
"s_barrier\nbuffer_load_dwordx4 v220, s[8:11], s63 offen lds\nG 4\nM 0,168,128\nR 160,190,8192\nbuffer_load_dwordx4 v221, s[8:11], s63 offen offset:1024 lds\n"
"M 0,172,144\nbuffer_load_dwordx4 a[208:211], v252, s[20:23], s64 offen offset:1024 sc1\ns_add_u32 m0, s50, 8192\ns_add_u32 s63, s53, 983040\nbuffer_load_dwordx4 v220, s[8:11], s63 offen lds\nM 16,168,132\n"
"R 162,190,12288\nbuffer_load_dwordx4 v221, s[8:11], s63 offen offset:1024 lds\nM 16,172,148\nM 32,168,136\nR 164,190,16384\nM 32,172,152\n"
"buffer_load_dwordx4 a[212:215], v252, s[20:23], s65 offen offset:1024 sc1\nM 48,168,140\nR 166,190,20480\nM 48,172,156\nG 4\nM 52,176,140\n"
"R 168,191,8192\nM 52,180,156\nbuffer_load_dwordx4 a[216:219], v252, s[20:23], s66 offen offset:1024 sc1\nM 36,176,136\nR 170,191,12288\nM 36,180,152\n"
"M 20,176,132\nR 172,191,16384\nM 20,180,148\nbuffer_load_dwordx4 a[220:223], v252, s[20:23], s67 offen offset:1024 sc1\nM 4,176,128\nR 174,191,20480\n"
"M 4,180,144\nG 4\nM 8,160,128\nR 176,188,24576\nM 8,164,144\nbuffer_load_dwordx4 a[224:227], v252, s[20:23], s64 offen offset:2048 sc1\n"
"M 24,160,132\nR 178,188,28672\nM 24,164,148\nM 40,160,136\nR 180,188,32768\nM 40,164,152\n"
"buffer_load_dwordx4 a[228:231], v252, s[20:23], s65 offen offset:2048 sc1\nM 56,160,140\nR 182,188,36864\nM 56,164,156\nG 4\nM 60,168,140\n"
"R 160,189,24576\nM 60,172,156\nbuffer_load_dwordx4 a[232:235], v252, s[20:23], s66 offen offset:2048 sc1\nM 44,168,136\nR 162,189,28672\nM 44,172,152\n"
"M 28,168,132\nR 164,189,32768\nM 28,172,148\nbuffer_load_dwordx4 a[236:239], v252, s[20:23], s67 offen offset:2048 sc1\nM 12,168,128\nR 166,189,36864\n"
"M 12,172,144\nG 4\nM 0,176,160\nR 168,190,24576\ns_waitcnt vmcnt(24)\nM 0,180,176\n"
"buffer_load_dwordx4 a[240:243], v252, s[20:23], s64 offen offset:3072 sc1\nM 16,176,164\nR 170,190,28672\ns_waitcnt vmcnt(22)\nM 16,180,180\nM 32,176,168\n"
"R 172,190,32768\ns_waitcnt vmcnt(19)\nM 32,180,184\nbuffer_load_dwordx4 a[244:247], v252, s[20:23], s65 offen offset:3072 sc1\nM 48,176,172\nR 174,190,36864\n"
"s_waitcnt vmcnt(18)\nM 48,180,188\nG 4\nM 52,160,172\nR 176,191,24576\nM 52,164,188\n"
"buffer_load_dwordx4 a[248:251], v252, s[20:23], s66 offen offset:3072 sc1\nM 36,160,168\nR 178,191,28672\ns_waitcnt vmcnt(10)\ns_barrier\nM 36,164,184\n"
"M 20,160,164\nR 180,191,32768\nM 20,164,180\nbuffer_load_dwordx4 a[252:255], v252, s[20:23], s67 offen offset:3072 sc1\nM 4,160,160\nR 182,191,36864\n"
"M 4,164,176\nG 4\nM 8,168,160\nR 160,188,40960\nM 8,172,176\nM 24,168,164\n"
"R 162,188,45056\nM 24,172,180\nM 40,168,168\nR 164,188,49152\nM 40,172,184\nM 56,168,172\n"
"R 166,188,53248\nM 56,172,188\nG 4\nM 60,176,172\nR 168,189,40960\nM 60,180,188\n"
"s_movk_i32 s63, 7168\nv_mad_u32_u24 v220, v228, s63, v229\nv_add_u32 v221, 3668992, v220\ns_add_u32 m0, s50, 16384\ns_add_u32 s63, s54, 0\nbuffer_load_dwordx4 v220, s[12:15], s63 offen lds\n"
"M 44,176,168\nR 170,189,45056\nM 44,180,184\nM 28,176,164\nR 172,189,49152\nM 28,180,180\n"
"buffer_load_dwordx4 v221, s[12:15], s63 offen offset:1024 lds\nM 12,176,160\nR 174,189,53248\ns_add_u32 m0, s50, 24576\nM 12,180,176\nG 4\n"
"M 32,160,200\nR 176,190,40960\ns_add_u32 s63, s54, 229376\ns_waitcnt vmcnt(11)\nM 32,164,216\nR 178,190,45056\n"
"M 48,160,204\nR 180,190,49152\ns_waitcnt vmcnt(10)\nM 48,164,220\nR 182,190,53248\nG 4\n"
"M 52,168,204\nR 160,191,40960\nbuffer_load_dwordx4 v220, s[12:15], s63 offen lds\nM 52,172,220\nR 162,191,45056\nM 36,168,200\n"
"R 164,191,49152\nM 36,172,216\nR 166,191,53248\nG 4\nM 40,176,200\nR 168,184,0\n"
"buffer_load_dwordx4 v221, s[12:15], s63 offen offset:1024 lds\nM 40,180,216\nR 170,184,4096\ns_add_u32 m0, s50, 32768\nM 56,176,204\nR 172,184,8192\n"
"s_add_u32 s63, s54, 128\nM 56,180,220\nR 174,184,12288\nG 4\nM 60,160,204\nR 176,185,0\n"
"buffer_load_dwordx4 v220, s[12:15], s63 offen lds\nM 60,164,220\nR 178,185,4096\nM 44,160,200\nR 180,185,8192\nM 44,164,216\n"
"R 182,185,12288\nG 4\ns_waitcnt vmcnt(10)\nM 32,168,232\nR 160,186,0\nbuffer_load_dwordx4 v221, s[12:15], s63 offen offset:1024 lds\n"
"s_waitcnt vmcnt(7)\nM 32,172,248\nR 162,186,4096\ns_add_u32 m0, s50, 40960\nM 48,168,236\nR 164,186,8192\n"
"s_add_u32 s63, s54, 229504\ns_waitcnt vmcnt(6)\nM 48,172,252\nR 166,186,12288\nG 4\nM 52,176,236\n"
"R 168,187,0\nbuffer_load_dwordx4 v220, s[12:15], s63 offen lds\nM 52,180,252\nR 170,187,4096\nM 36,176,232\nR 172,187,8192\n"
"M 36,180,248\nR 174,187,12288\nG 4\nM 40,160,232\nR 176,188,40960\nbuffer_load_dwordx4 v221, s[12:15], s63 offen offset:1024 lds\n"
"M 40,164,248\nR 178,188,45056\nM 56,160,236\nR 180,188,49152\nM 56,164,252\nR 182,188,53248\n"
"G 4\nM 60,168,236\nR 160,189,40960\nM 60,172,252\nR 162,189,45056\nM 44,168,232\n"
"R 164,189,49152\nM 44,172,248\nR 166,189,53248\nG 4\nM 0,176,192\nR 168,190,40960\n"
"M 0,180,208\nR 170,190,45056\ns_mov_b32 m0, s51\nM 16,176,196\nR 172,190,49152\nv_cvt_pk_bf16_f32 v32, v32, v33\n"
"v_cvt_pk_bf16_f32 v33, v34, v35\nM 16,180,212\nR 174,190,53248\nG 4\nv_cvt_pk_bf16_f32 v34, v36, v37\nM 20,160,196\n"
"R 176,191,40960\nv_cvt_pk_bf16_f32 v35, v38, v39\nv_cvt_pk_bf16_f32 v36, v40, v41\nM 20,164,212\nR 178,191,45056\nv_cvt_pk_bf16_f32 v37, v42, v43\n"
"v_cvt_pk_bf16_f32 v38, v44, v45\nM 4,160,192\nR 180,191,49152\ns_lshr_b32 s60, s45, 3\ns_lshl2_add_u32 s60, s60, s40\ns_lshl_b32 s60, s60, 8\n"
"v_lshl_add_u32 v224, v252, 3, s60\nv_add_u32 v224, Lqw0+24576-LqPC, v224\nv_min_u32 v224, LqE-LqPC-4, v224\ns_mov_b64 exec, 3\nglobal_load_dword v224, v224, s[98:99]\ns_mov_b64 exec, -1\n"
"v_cvt_pk_bf16_f32 v39, v46, v47\nM 4,164,208\nR 182,191,53248\nG 4\nv_cvt_pk_bf16_f32 v40, v48, v49\nM 8,168,192\n"
"R 160,184,0\nv_cvt_pk_bf16_f32 v41, v50, v51\nv_cvt_pk_bf16_f32 v42, v52, v53\nM 8,172,208\nR 162,184,4096\nv_cvt_pk_bf16_f32 v43, v54, v55\n"
"v_cvt_pk_bf16_f32 v44, v56, v57\nM 24,168,196\nR 164,184,8192\nv_cvt_pk_bf16_f32 v45, v58, v59\nv_cvt_pk_bf16_f32 v46, v60, v61\nM 24,172,212\n"
"R 166,184,12288\nG 4\nv_cvt_pk_bf16_f32 v47, v62, v63\nM 28,176,196\nR 168,185,0\nds_write_addtid_b32 v32 offset:0\n"
"M 28,180,212\nR 170,185,4096\nds_write_addtid_b32 v33 offset:256\nM 12,176,192\nR 172,185,8192\nds_write_addtid_b32 v34 offset:512\n"
"ds_write_addtid_b32 v35 offset:768\nds_write_addtid_b32 v36 offset:1024\nds_write_addtid_b32 v37 offset:1280\nds_write_addtid_b32 v38 offset:1536\nds_write_addtid_b32 v39 offset:1792\nds_write_addtid_b32 v40 offset:2048\n"
"ds_write_addtid_b32 v41 offset:2304\nds_write_addtid_b32 v42 offset:2560\nds_write_addtid_b32 v43 offset:2816\nds_write_addtid_b32 v44 offset:3072\nds_write_addtid_b32 v45 offset:3328\nds_write_addtid_b32 v46 offset:3584\n"
"ds_write_addtid_b32 v47 offset:3840\nM 12,180,208\nR 174,185,12288\nG 15\nM 0,160,224\nR 176,186,0\n"
"s_add_u32 m0, s50, 49152\nM 0,164,240\nR 178,186,4096\nG 3\ns_barrier\ns_add_u32 s63, s54, 458752\n"
"M 16,160,228\nR 180,186,8192\nbuffer_load_dwordx4 v220, s[12:15], s63 offen lds\nds_read2st64_b32 v[32:33], v225 offset0:0 offset1:1\nM 16,164,244\nR 182,186,12288\n"
"ds_read2st64_b32 v[34:35], v225 offset0:2 offset1:3\nG 6\nM 20,168,228\nR 160,187,0\nds_read2st64_b32 v[36:37], v225 offset0:4 offset1:5\nM 20,172,244\n"
"R 162,187,4096\nds_read2st64_b32 v[38:39], v225 offset0:6 offset1:7\nM 4,168,224\nR 164,187,8192\nbuffer_load_dwordx4 v221, s[12:15], s63 offen offset:1024 lds\nds_read2st64_b32 v[40:41], v225 offset0:8 offset1:9\n"
"M 4,172,240\nR 166,187,12288\ns_add_u32 m0, s50, 57344\ns_add_u32 s63, s54, 688128\nds_read2st64_b32 v[42:43], v225 offset0:10 offset1:11\nG 9\n"
"M 8,176,224\nbuffer_load_dwordx4 v220, s[12:15], s63 offen lds\nds_read2st64_b32 v[44:45], v225 offset0:12 offset1:13\nM 8,180,240\nds_read2st64_b32 v[46:47], v225 offset0:14 offset1:15\nM 24,176,228\n"
"M 24,180,244\nG 3\nM 28,160,228\nbuffer_load_dwordx4 v221, s[12:15], s63 offen offset:1024 lds\nM 28,164,244\nM 12,160,224\n"
"M 12,164,240\nG 0\ns_barrier\nv_mfma_f32_16x16x32_bf16 v[0:3], v[208:211], v[32:35], v[0:3]\nv_mfma_f32_16x16x32_bf16 v[4:7], v[210:213], v[32:35], v[4:7]\nv_mfma_f32_16x16x32_bf16 v[8:11], v[208:211], v[36:39], v[8:11]\n"
"v_mfma_f32_16x16x32_bf16 v[12:15], v[210:213], v[36:39], v[12:15]\nv_mfma_f32_16x16x32_bf16 v[16:19], v[208:211], v[40:43], v[16:19]\nv_mfma_f32_16x16x32_bf16 v[20:23], v[210:213], v[40:43], v[20:23]\nv_mfma_f32_16x16x32_bf16 v[24:27], v[208:211], v[44:47], v[24:27]\nv_mfma_f32_16x16x32_bf16 v[28:31], v[210:213], v[44:47], v[28:31]\ns_lshl_b32 s84, s75, 11\n"
"s_add_u32 s84, s84, s49\ns_add_u32 s84, s84, 10485760\ns_add_u32 s85, s84, 32768\nv_mov_b32 v238, s46\nv_mov_b32 v239, s47\nv_cvt_pk_bf16_f32 v192, v0, v1\n"
"v_cvt_pk_bf16_f32 v193, v2, v3\nv_cvt_pk_bf16_f32 v194, v4, v5\nv_cvt_pk_bf16_f32 v195, v6, v7\nbuffer_store_dwordx4 v[192:195], v252, s[20:23], s84 offen offset:0 sc1\nv_cvt_pk_bf16_f32 v196, v8, v9\nv_cvt_pk_bf16_f32 v197, v10, v11\n"
"v_cvt_pk_bf16_f32 v198, v12, v13\nv_cvt_pk_bf16_f32 v199, v14, v15\nLpg7:\ns_cbranch_execz LretO0\nbuffer_store_dwordx4 v[196:199], v252, s[20:23], s84 offen offset:1024 sc1\nv_cvt_pk_bf16_f32 v200, v16, v17\n"
"v_cvt_pk_bf16_f32 v201, v18, v19\nv_cvt_pk_bf16_f32 v202, v20, v21\nv_cvt_pk_bf16_f32 v203, v22, v23\nbuffer_store_dwordx4 v[200:203], v252, s[20:23], s85 offen offset:0 sc1\nv_cvt_pk_bf16_f32 v204, v24, v25\nv_cvt_pk_bf16_f32 v205, v26, v27\n"
"v_cvt_pk_bf16_f32 v206, v28, v29\nv_cvt_pk_bf16_f32 v207, v30, v31\nbuffer_store_dwordx4 v[204:207], v252, s[20:23], s85 offen offset:1024 sc1\ns_mov_b64 exec, 0\ns_cmp_eq_u32 s40, 1\ns_cbranch_scc1 Lpg9\n"
"s_cmp_eq_u32 s40, 2\ns_cbranch_scc1 Lpg10\ns_cmp_eq_u32 s40, 3\ns_cbranch_scc1 Lpg11\ns_branch Lpg8\nLretO0n:\n"
"s_mov_b64 exec, -1\ns_add_u32 s61, s79, 88219648\ns_lshl_b32 s62, s75, 3\ns_add_u32 s61, s61, s62\ns_waitcnt vmcnt(0)\ns_mov_b64 exec, 1\n"
"buffer_atomic_swap_x2 v[238:239], off, s[20:23], s61 sc1\ns_mov_b64 exec, -1\nLqw0:\nv_bfe_u32 v250, v252, 7, 1\nv_mul_u32_u24 v250, 2176, v250\nv_bfe_u32 v251, v252, 4, 3\n"
"v_lshl_add_u32 v250, v251, 3, v250\ns_lshl_b32 s60, s90, 6\nv_add_u32 v250, s60, v250\ns_sleep 5\nv_mov_b32 v248, 0\nv_mov_b32 v249, 0\n"
"s_add_u32 s61, s80, 88219648\ns_mov_b64 exec, 0xffff\nbuffer_atomic_or_x2 v[248:249], v250, s[20:23], s61 offen sc0 sc1\ns_mov_b64 exec, -1\ns_add_u32 m0, s50, 65536\ns_add_u32 s63, s54, 458880\n"
"buffer_load_dwordx4 v220, s[12:15], s63 offen lds\nbuffer_load_dwordx4 v221, s[12:15], s63 offen offset:1024 lds\ns_add_u32 m0, s50, 73728\ns_add_u32 s63, s54, 688256\nbuffer_load_dwordx4 v220, s[12:15], s63 offen lds\nbuffer_load_dwordx4 v221, s[12:15], s63 offen offset:1024 lds\n"
"s_add_u32 m0, s50, 81920\ns_add_u32 s63, s54, 917504\nbuffer_load_dwordx4 v220, s[12:15], s63 offen lds\nbuffer_load_dwordx4 v221, s[12:15], s63 offen offset:1024 lds\ns_add_u32 m0, s50, 90112\ns_add_u32 s63, s54, 1146880\n"
"buffer_load_dwordx4 v220, s[12:15], s63 offen lds\nbuffer_load_dwordx4 v221, s[12:15], s63 offen offset:1024 lds\ns_add_u32 m0, s50, 98304\ns_add_u32 s63, s54, 917632\nbuffer_load_dwordx4 v220, s[12:15], s63 offen lds\nbuffer_load_dwordx4 v221, s[12:15], s63 offen offset:1024 lds\n"
"s_add_u32 m0, s50, 106496\ns_add_u32 s63, s54, 1147008\nbuffer_load_dwordx4 v220, s[12:15], s63 offen lds\nbuffer_load_dwordx4 v221, s[12:15], s63 offen offset:1024 lds\nv_mov_b32 v230, 0\nv_mov_b32 v231, 0\n"
"v_mov_b32 v232, 0\nv_mov_b32 v233, 0\nv_dot2c_f32_bf16 v230, s76, v192\nv_dot2c_f32_bf16 v231, v192, v192\nv_dot2c_f32_bf16 v232, s76, v200\nv_dot2c_f32_bf16 v233, v200, v200\n"
"v_dot2c_f32_bf16 v230, s76, v193\nv_dot2c_f32_bf16 v231, v193, v193\nv_dot2c_f32_bf16 v232, s76, v201\nv_dot2c_f32_bf16 v233, v201, v201\nv_dot2c_f32_bf16 v230, s76, v194\nv_dot2c_f32_bf16 v231, v194, v194\n"
"v_dot2c_f32_bf16 v232, s76, v202\nv_dot2c_f32_bf16 v233, v202, v202\nv_dot2c_f32_bf16 v230, s76, v195\nv_dot2c_f32_bf16 v231, v195, v195\nv_dot2c_f32_bf16 v232, s76, v203\nv_dot2c_f32_bf16 v233, v203, v203\n"
"v_dot2c_f32_bf16 v230, s76, v196\nv_dot2c_f32_bf16 v231, v196, v196\nv_dot2c_f32_bf16 v232, s76, v204\nv_dot2c_f32_bf16 v233, v204, v204\nv_dot2c_f32_bf16 v230, s76, v197\nv_dot2c_f32_bf16 v231, v197, v197\n"
"v_dot2c_f32_bf16 v232, s76, v205\nv_dot2c_f32_bf16 v233, v205, v205\nv_dot2c_f32_bf16 v230, s76, v198\nv_dot2c_f32_bf16 v231, v198, v198\nv_dot2c_f32_bf16 v232, s76, v206\nv_dot2c_f32_bf16 v233, v206, v206\n"
"v_dot2c_f32_bf16 v230, s76, v199\nv_dot2c_f32_bf16 v231, v199, v199\nv_dot2c_f32_bf16 v232, s76, v207\nv_dot2c_f32_bf16 v233, v207, v207\nv_lshrrev_b32 v234, 1, v252\nv_permlane32_swap_b32 v230, v231\n"
"s_nop 0\nv_permlane32_swap_b32 v232, v233\nv_add_f32 v230, v230, v231\nv_add_f32 v232, v232, v233\nv_mov_b32 v231, s46\ns_nop 0\n"
"v_permlane16_swap_b32 v230, v232\nv_add_f32 v230, v230, v232\ns_lshr_b32 s61, s49, 3\ns_lshl2_add_u32 s61, s53, s61\ns_add_u32 s61, s61, 82313216\nbuffer_store_dwordx2 v[230:231], v234, s[20:23], s61 offen sc1\n"
"s_waitcnt vmcnt(13)\nv_cmp_eq_u64 s[86:87], s[46:47], v[248:249]\ns_nop 0\ns_and_b32 s62, s86, 0xffff\ns_cmp_eq_u32 s62, 0xffff\ns_cbranch_scc0 Lq260\n"
"Lq261:\ns_add_u32 s64, s70, 10485760\nbuffer_load_dwordx4 a[0:3], v252, s[20:23], s64 offen offset:0 sc1\ns_add_u32 s65, s70, 10518528\nbuffer_load_dwordx4 a[4:7], v252, s[20:23], s65 offen offset:0 sc1\ns_add_u32 s66, s71, 10485760\n"
"buffer_load_dwordx4 a[8:11], v252, s[20:23], s66 offen offset:0 sc1\ns_add_u32 s67, s71, 10518528\nbuffer_load_dwordx4 a[12:15], v252, s[20:23], s67 offen offset:0 sc1\nbuffer_load_dwordx4 a[16:19], v252, s[20:23], s64 offen offset:1024 sc1\nbuffer_load_dwordx4 a[20:23], v252, s[20:23], s65 offen offset:1024 sc1\nbuffer_load_dwordx4 a[24:27], v252, s[20:23], s66 offen offset:1024 sc1\n"
"buffer_load_dwordx4 a[28:31], v252, s[20:23], s67 offen offset:1024 sc1\ns_barrier\nR 160,184,16384\nR 162,184,20480\nR 164,184,24576\nR 166,184,28672\n"
"R 168,185,16384\nR 170,185,20480\nR 172,185,24576\nR 174,185,28672\nR 176,186,16384\nR 178,186,20480\n"
"R 180,186,24576\nR 182,186,28672\nbuffer_load_dwordx4 a[32:35], v252, s[20:23], s64 offen offset:2048 sc1\nbuffer_load_dwordx4 a[36:39], v252, s[20:23], s65 offen offset:2048 sc1\nbuffer_load_dwordx4 a[40:43], v252, s[20:23], s66 offen offset:2048 sc1\nbuffer_load_dwordx4 a[44:47], v252, s[20:23], s67 offen offset:2048 sc1\n"
"buffer_load_dwordx4 a[48:51], v252, s[20:23], s64 offen offset:3072 sc1\nbuffer_load_dwordx4 a[52:55], v252, s[20:23], s65 offen offset:3072 sc1\nbuffer_load_dwordx4 a[56:59], v252, s[20:23], s66 offen offset:3072 sc1\nbuffer_load_dwordx4 a[60:63], v252, s[20:23], s67 offen offset:3072 sc1\ns_waitcnt vmcnt(15) lgkmcnt(10)\nv_mfma_f32_16x16x32_bf16 v[32:35], v[160:163], a[0:3], 0\n"
"s_add_u32 s64, s70, 10489856\nbuffer_load_dwordx4 a[64:67], v252, s[20:23], s64 offen offset:0 sc1\ns_waitcnt vmcnt(12) lgkmcnt(8)\nM 32,164,16\nv_mfma_f32_16x16x32_bf16 v[48:51], v[160:163], a[4:7], 0\ns_waitcnt vmcnt(11)\n"
"M 48,164,20\nv_mfma_f32_16x16x32_bf16 v[64:67], v[160:163], a[8:11], 0\nv_lshrrev_b32 v238, 7, v252\ns_waitcnt vmcnt(10)\nM 64,164,24\ns_add_u32 s65, s70, 10522624\n"
"buffer_load_dwordx4 a[68:71], v252, s[20:23], s65 offen offset:0 sc1\nv_mfma_f32_16x16x32_bf16 v[80:83], v[160:163], a[12:15], 0\nv_lshlrev_b32 v238, 9, v238\ns_waitcnt vmcnt(10)\nM 80,164,28\nG 4\n"
"v_and_b32 v239, 0x70, v252\nv_mfma_f32_16x16x32_bf16 v[84:87], v[168:171], a[12:15], 0\nR 160,187,16384\nM 84,172,28\ns_and_b32 s60, s40, 3\ns_lshl_b32 s60, s60, 7\n"
"v_add3_u32 v238, v238, v239, s60\nv_mfma_f32_16x16x32_bf16 v[68:71], v[168:171], a[8:11], 0\nR 162,187,20480\ns_lshl_b32 s61, s42, 13\ns_add_u32 s61, s61, 84279296\nbuffer_load_dwordx4 v[0:3], v238, s[20:23], s61 offen sc1\n"
"M 68,172,24\ns_add_u32 s66, s71, 10489856\nbuffer_load_dwordx4 a[72:75], v252, s[20:23], s66 offen offset:0 sc1\nv_mfma_f32_16x16x32_bf16 v[52:55], v[168:171], a[4:7], 0\nR 164,187,24576\ns_lshl_b32 s61, s42, 13\n"
"s_add_u32 s61, s61, 84283392\nbuffer_load_dwordx4 v[4:7], v238, s[20:23], s61 offen sc1\nM 52,172,20\nv_bfe_u32 v239, v238, 2, 7\nv_mfma_f32_16x16x32_bf16 v[36:39], v[168:171], a[0:3], 0\nR 166,187,28672\n"
"M 36,172,16\nbuffer_load_dword v239, v239, s[32:35], s54 offen offset:0\nG 4\nv_mfma_f32_16x16x32_bf16 v[40:43], v[176:179], a[0:3], 0\nR 168,184,32768\nM 40,180,16\n"
"s_add_u32 s67, s71, 10522624\nbuffer_load_dwordx4 a[76:79], v252, s[20:23], s67 offen offset:0 sc1\nv_mfma_f32_16x16x32_bf16 v[56:59], v[176:179], a[4:7], 0\nR 170,184,36864\nM 56,180,20\nv_mfma_f32_16x16x32_bf16 v[72:75], v[176:179], a[8:11], 0\n"
"R 172,184,40960\nM 72,180,24\nv_mfma_f32_16x16x32_bf16 v[88:91], v[176:179], a[12:15], 0\nR 174,184,45056\nM 88,180,28\nG 4\n"
"v_mfma_f32_16x16x32_bf16 v[92:95], v[160:163], a[12:15], 0\nR 176,185,32768\nM 92,164,28\nbuffer_load_dwordx4 a[80:83], v252, s[20:23], s64 offen offset:1024 sc1\nv_mfma_f32_16x16x32_bf16 v[76:79], v[160:163], a[8:11], 0\nR 178,185,36864\n"
"M 76,164,24\nv_mfma_f32_16x16x32_bf16 v[60:63], v[160:163], a[4:7], 0\nR 180,185,40960\nM 60,164,20\nbuffer_load_dwordx4 a[84:87], v252, s[20:23], s65 offen offset:1024 sc1\nv_mfma_f32_16x16x32_bf16 v[44:47], v[160:163], a[0:3], 0\n"
"R 182,185,45056\nM 44,164,16\nG 4\nv_mfma_f32_16x16x32_bf16 v[96:99], v[168:171], a[0:3], 0\nR 160,186,32768\nM 96,172,16\n"
"v_mfma_f32_16x16x32_bf16 v[112:115], v[168:171], a[4:7], 0\nR 162,186,36864\nM 112,172,20\nbuffer_load_dwordx4 a[88:91], v252, s[20:23], s66 offen offset:1024 sc1\nv_mfma_f32_16x16x32_bf16 v[128:131], v[168:171], a[8:11], 0\nR 164,186,40960\n"
"M 128,172,24\nv_mfma_f32_16x16x32_bf16 v[144:147], v[168:171], a[12:15], 0\nR 166,186,45056\nM 144,172,28\ns_barrier\nG 4\n"
"v_mfma_f32_16x16x32_bf16 v[148:151], v[176:179], a[12:15], 0\nR 168,187,32768\nM 148,180,28\nbuffer_load_dwordx4 a[92:95], v252, s[20:23], s67 offen offset:1024 sc1\nv_mfma_f32_16x16x32_bf16 v[132:135], v[176:179], a[8:11], 0\nR 170,187,36864\n"
"M 132,180,24\nv_mfma_f32_16x16x32_bf16 v[116:119], v[176:179], a[4:7], 0\nR 172,187,40960\nM 116,180,20\nv_mfma_f32_16x16x32_bf16 v[100:103], v[176:179], a[0:3], 0\nR 174,187,45056\n"
"M 100,180,16\nG 4\nv_mfma_f32_16x16x32_bf16 v[104:107], v[160:163], a[0:3], 0\nR 176,184,49152\nv_lshrrev_b32 v242, 7, v252\nM 104,164,16\n"
"buffer_load_dwordx4 a[96:99], v252, s[20:23], s64 offen offset:2048 sc1\nv_mfma_f32_16x16x32_bf16 v[120:123], v[160:163], a[4:7], 0\nR 178,184,53248\nv_lshlrev_b32 v242, 9, v242\ns_waitcnt vmcnt(7)\nv_min3_u32 v240, v1, v3, v5\n"
"M 120,164,20\nbuffer_load_dwordx4 a[100:103], v252, s[20:23], s65 offen offset:2048 sc1\nv_and_b32 v243, 0x70, v252\nv_min3_u32 v240, v240, v7, v7\nv_mfma_f32_16x16x32_bf16 v[136:139], v[160:163], a[8:11], 0\nR 180,184,57344\n"
"v_max3_u32 v241, v1, v3, v5\nv_max3_u32 v241, v241, v7, v7\nM 136,164,24\ns_and_b32 s60, s40, 3\ns_lshl_b32 s60, s60, 7\nv_add3_u32 v242, v242, v243, s60\n"
"v_cmp_ne_u32 s[86:87], s46, v240\nv_mfma_f32_16x16x32_bf16 v[152:155], v[160:163], a[12:15], 0\nR 182,184,61440\ns_lshl_b32 s61, s42, 13\ns_add_u32 s61, s61, 84410368\nbuffer_load_dwordx4 v[16:19], v242, s[20:23], s61 offen sc1\n"
"v_cmp_ne_u32 s[88:89], s46, v241\nM 152,164,28\ns_lshl_b32 s61, s42, 13\ns_add_u32 s61, s61, 84414464\nbuffer_load_dwordx4 v[20:23], v242, s[20:23], s61 offen sc1\ns_or_b64 s[86:87], s[86:87], s[88:89]\n"
"s_cbranch_scc1 Lq321\nLq322:\nG 4\nv_mfma_f32_16x16x32_bf16 v[156:159], v[168:171], a[12:15], 0\nR 160,185,49152\nv_add_f32 v0, v0, v4\n"
"M 156,172,28\nbuffer_load_dwordx4 a[104:107], v252, s[20:23], s66 offen offset:2048 sc1\nv_bfe_u32 v243, v242, 2, 7\nv_mfma_f32_16x16x32_bf16 v[140:143], v[168:171], a[8:11], 0\nR 162,185,53248\nv_add_f32 v2, v2, v6\n"
"M 140,172,24\nbuffer_load_dword v243, v243, s[32:35], s54 offen offset:128\nv_mfma_f32_16x16x32_bf16 v[124:127], v[168:171], a[4:7], 0\nR 164,185,57344\nv_add_f32_dpp v0, v0, v0 row_ror:8 row_mask:0xf bank_mask:0xf\nM 124,172,20\n"
"v_add_f32_dpp v2, v2, v2 row_ror:8 row_mask:0xf bank_mask:0xf\nv_mov_b32 v4, v0\nv_mfma_f32_16x16x32_bf16 v[108:111], v[168:171], a[0:3], 0\nR 166,185,61440\nv_mov_b32 v5, v2\nM 108,172,16\n"
"G 4\nv_permlane32_swap_b32 v4, v0\nv_permlane32_swap_b32 v5, v2\nM 32,176,32\nR 168,186,49152\nv_add_f32 v0, v0, v4\n"
"M 32,180,48\nbuffer_load_dwordx4 a[108:111], v252, s[20:23], s67 offen offset:2048 sc1\nM 48,176,36\nR 170,186,53248\nv_add_f32 v2, v2, v5\nM 48,180,52\n"
"v_mov_b32 v4, v0\nv_mov_b32 v5, v2\nM 64,176,40\nR 172,186,57344\nv_permlane16_swap_b32 v4, v0\nM 64,180,56\n"
"buffer_load_dwordx4 a[112:115], v252, s[20:23], s64 offen offset:3072 sc1\nM 80,176,44\nR 174,186,61440\nv_permlane16_swap_b32 v5, v2\nM 80,180,60\ns_barrier\n"
"G 4\nM 84,160,44\nR 176,187,49152\nv_add_f32 v0, v0, v4\nM 84,164,60\nv_add_f32 v2, v2, v5\n"
"v_and_b32 v8, s77, v0\nM 68,160,40\nR 178,187,53248\nv_sub_f32 v9, v0, v8\nM 68,164,56\nbuffer_load_dwordx4 a[116:119], v252, s[20:23], s65 offen offset:3072 sc1\n"
"M 52,160,36\nR 180,187,57344\ns_waitcnt vmcnt(14)\nv_lshlrev_b32 v10, 16, v239\nM 52,164,52\nv_cvt_pk_bf16_f32 v4, v8, v9\n"
"v_cvt_pk_bf16_f32 v5, v8, v10\nM 36,160,32\nR 182,187,61440\nv_and_b32 v8, s77, v2\nM 36,164,48\nG 4\n"
"v_sub_f32 v9, v2, v8\nv_and_b32 v10, s77, v239\nM 40,168,32\nR 160,188,0\nv_cvt_pk_bf16_f32 v6, v8, v9\nM 40,172,48\n"
"buffer_load_dwordx4 a[120:123], v252, s[20:23], s66 offen offset:3072 sc1\nM 56,168,36\nR 162,188,4096\nv_cvt_pk_bf16_f32 v7, v8, v10\nM 56,172,52\nv_and_b32 v238, 0x1ff, v238\n"
"v_add_u32 v238, 157184, v238\nM 72,168,40\nR 164,188,8192\nds_write_b128 v238, v[4:7]\nM 72,172,56\nM 88,168,44\n"
"R 166,188,12288\nM 88,172,60\nG 5\nM 92,176,44\nR 168,189,0\nM 92,180,60\n"
"buffer_load_dwordx4 a[124:127], v252, s[20:23], s67 offen offset:3072 sc1\nM 76,176,40\nR 170,189,4096\nM 76,180,56\nM 60,176,36\nR 172,189,8192\n"
"v_lshrrev_b32 v238, 7, v252\nM 60,180,52\ns_add_u32 s64, s70, 10493952\nbuffer_load_dwordx4 a[128:131], v252, s[20:23], s64 offen offset:0 sc1\nM 44,176,32\nR 174,189,12288\n"
"v_lshlrev_b32 v238, 9, v238\ns_waitcnt vmcnt(8)\nv_min3_u32 v244, v17, v19, v21\nM 44,180,48\nG 4\nv_and_b32 v239, 0x70, v252\n"
"v_min3_u32 v244, v244, v23, v23\nv_max3_u32 v245, v17, v19, v21\nM 96,160,32\nR 176,190,0\ns_and_b32 s60, s40, 3\ns_lshl_b32 s60, s60, 7\n"
"v_add3_u32 v238, v238, v239, s60\nv_max3_u32 v245, v245, v23, v23\nM 96,164,48\ns_add_u32 s65, s70, 10526720\nbuffer_load_dwordx4 a[132:135], v252, s[20:23], s65 offen offset:0 sc1\nv_cmp_ne_u32 s[86:87], s46, v244\n"
"M 112,160,36\nR 178,190,4096\ns_lshl_b32 s61, s42, 13\ns_add_u32 s61, s61, 84541440\nbuffer_load_dwordx4 v[0:3], v238, s[20:23], s61 offen sc1\nv_cmp_ne_u32 s[88:89], s46, v245\n"
"M 112,164,52\ns_lshl_b32 s61, s42, 13\ns_add_u32 s61, s61, 84545536\nbuffer_load_dwordx4 v[4:7], v238, s[20:23], s61 offen sc1\ns_or_b64 s[86:87], s[86:87], s[88:89]\ns_cbranch_scc1 Lq331\n"
"Lq332:\nM 128,160,40\nR 180,190,8192\nv_add_f32 v16, v16, v20\nv_add_f32 v18, v18, v22\nM 128,164,56\n"
"s_add_u32 m0, s50, 114688\nv_bfe_u32 v239, v238, 2, 7\nv_add_f32_dpp v16, v16, v16 row_ror:8 row_mask:0xf bank_mask:0xf\nv_add_f32_dpp v18, v18, v18 row_ror:8 row_mask:0xf bank_mask:0xf\nM 144,160,44\nR 182,190,12288\n"
"s_add_u32 s63, s54, 1376256\nv_mov_b32 v20, v16\nM 144,164,60\nbuffer_load_dword v239, v239, s[32:35], s54 offen offset:256\ns_barrier\nG 4\n"
"v_mov_b32 v21, v18\nM 148,168,44\nR 160,191,0\nv_permlane32_swap_b32 v20, v16\nv_permlane32_swap_b32 v21, v18\nM 148,172,60\n"
"s_add_u32 s66, s71, 10493952\nbuffer_load_dwordx4 a[136:139], v252, s[20:23], s66 offen offset:0 sc1\nv_add_f32 v16, v16, v20\nM 132,168,40\nR 162,191,4096\nv_add_f32 v18, v18, v21\n"
"v_mov_b32 v20, v16\nM 132,172,56\nbuffer_load_dwordx4 v220, s[12:15], s63 offen lds\nv_mov_b32 v21, v18\nM 116,168,36\nR 164,191,8192\n"
"v_permlane16_swap_b32 v20, v16\nM 116,172,52\ns_add_u32 s67, s71, 10526720\nbuffer_load_dwordx4 a[140:143], v252, s[20:23], s67 offen offset:0 sc1\nM 100,168,32\nR 166,191,12288\n"
"v_permlane16_swap_b32 v21, v18\nM 100,172,48\nG 4\nv_add_f32 v16, v16, v20\nv_add_f32 v18, v18, v21\nM 104,176,32\n"
"R 168,188,16384\nbuffer_load_dwordx4 v221, s[12:15], s63 offen offset:1024 lds\nM 104,180,48\ns_add_u32 m0, s50, 0\ns_add_u32 s63, s54, 1605632\nbuffer_load_dwordx4 v220, s[12:15], s63 offen lds\n"
"M 120,176,36\nR 170,188,20480\nv_and_b32 v24, s77, v16\nM 120,180,52\nbuffer_load_dwordx4 a[144:147], v252, s[20:23], s64 offen offset:1024 sc1\nM 136,176,40\n"
"R 172,188,24576\nv_sub_f32 v25, v16, v24\nM 136,180,56\nbuffer_load_dwordx4 v221, s[12:15], s63 offen offset:1024 lds\nM 152,176,44\nR 174,188,28672\n"
"s_waitcnt vmcnt(17)\nv_lshlrev_b32 v26, 16, v243\nM 152,180,60\nG 4\nv_cvt_pk_bf16_f32 v20, v24, v25\nv_cvt_pk_bf16_f32 v21, v24, v26\n"
"M 156,160,44\nR 176,189,16384\nv_and_b32 v24, s77, v18\nM 156,164,60\nbuffer_load_dwordx4 a[148:151], v252, s[20:23], s65 offen offset:1024 sc1\nM 140,160,40\n"
"R 178,189,20480\nv_sub_f32 v25, v18, v24\nM 140,164,56\nv_and_b32 v26, s77, v243\nv_cvt_pk_bf16_f32 v22, v24, v25\nM 124,160,36\n"
"R 180,189,24576\nv_cvt_pk_bf16_f32 v23, v24, v26\nM 124,164,52\nv_and_b32 v242, 0x1ff, v242\nv_add_u32 v242, 157696, v242\nM 108,160,32\n"
"R 182,189,28672\nds_write_b128 v242, v[20:23]\nM 108,164,48\nG 5\nM 32,168,64\nR 160,190,16384\n"
"M 32,172,80\nbuffer_load_dwordx4 a[152:155], v252, s[20:23], s66 offen offset:1024 sc1\nM 48,168,68\nR 162,190,20480\nM 48,172,84\ns_add_u32 m0, s50, 8192\n"
"M 64,168,72\nR 164,190,24576\ns_add_u32 s63, s54, 1376384\nM 64,172,88\nbuffer_load_dwordx4 a[156:159], v252, s[20:23], s67 offen offset:1024 sc1\nM 80,168,76\n"
"R 166,190,28672\nM 80,172,92\nG 4\ns_barrier\nM 84,176,76\nR 168,191,16384\n"
"v_lshrrev_b32 v242, 7, v252\nM 84,180,92\nbuffer_load_dwordx4 v220, s[12:15], s63 offen lds\nv_lshlrev_b32 v242, 9, v242\nM 68,176,72\nR 170,191,20480\n"
"s_waitcnt vmcnt(12)\nv_min3_u32 v240, v1, v3, v5\nv_min3_u32 v240, v240, v7, v7\nM 68,180,88\nbuffer_load_dwordx4 a[160:163], v252, s[20:23], s64 offen offset:2048 sc1\nv_and_b32 v243, 0x70, v252\n"
"v_max3_u32 v241, v1, v3, v5\nM 52,176,68\nR 172,191,24576\nv_max3_u32 v241, v241, v7, v7\nv_cmp_ne_u32 s[86:87], s46, v240\nM 52,180,84\n"
"s_and_b32 s60, s40, 1\ns_lshl_b32 s60, s60, 7\nv_add3_u32 v242, v242, v243, s60\nv_cmp_ne_u32 s[88:89], s46, v241\nM 36,176,64\nR 174,191,28672\n"
"s_lshl_b32 s61, s42, 13\ns_add_u32 s61, s61, 84672512\nbuffer_load_dwordx4 v[16:19], v242, s[20:23], s61 offen sc1\ns_or_b64 s[86:87], s[86:87], s[88:89]\ns_cbranch_scc1 Lq341\nLq342:\n"
"M 36,180,80\ns_lshl_b32 s61, s42, 13\ns_add_u32 s61, s61, 84676608\nbuffer_load_dwordx4 v[20:23], v242, s[20:23], s61 offen sc1\nG 4\nv_add_f32 v0, v0, v4\n"
"M 40,160,64\nR 176,188,32768\nbuffer_load_dwordx4 v221, s[12:15], s63 offen offset:1024 lds\nv_add_f32 v2, v2, v6\nM 40,164,80\nbuffer_load_dwordx4 a[164:167], v252, s[20:23], s65 offen offset:2048 sc1\n"
"s_add_u32 s63, s54, 1605760\nv_bfe_u32 v243, v242, 2, 7\nv_add_f32_dpp v0, v0, v0 row_ror:8 row_mask:0xf bank_mask:0xf\nM 56,160,68\nR 178,188,36864\ns_add_u32 m0, s50, 16384\n"
"v_add_f32_dpp v2, v2, v2 row_ror:8 row_mask:0xf bank_mask:0xf\nM 56,164,84\nbuffer_load_dword v243, v243, s[32:35], s55 offen offset:0\nv_mov_b32 v4, v0\nM 72,160,72\nR 180,188,40960\n"
"buffer_load_dwordx4 v220, s[12:15], s63 offen lds\nv_mov_b32 v5, v2\nM 72,164,88\nbuffer_load_dwordx4 a[168:171], v252, s[20:23], s66 offen offset:2048 sc1\nv_permlane32_swap_b32 v4, v0\nLpg8:\n"
"s_cbranch_execz Lpg12\nM 88,160,76\nR 182,188,45056\nv_permlane32_swap_b32 v5, v2\nv_add_f32 v0, v0, v4\nM 88,164,92\n"
"G 4\nv_add_f32 v2, v2, v5\nv_mov_b32 v4, v0\nv_mov_b32 v5, v2\nM 92,168,76\nR 160,189,32768\n"
"buffer_load_dwordx4 v221, s[12:15], s63 offen offset:1024 lds\nv_permlane16_swap_b32 v4, v0\nM 92,172,92\nv_permlane16_swap_b32 v5, v2\nv_add_f32 v0, v0, v4\nM 76,168,72\n"
"R 162,189,36864\nv_add_f32 v2, v2, v5\nM 76,172,88\nbuffer_load_dwordx4 a[172:175], v252, s[20:23], s67 offen offset:2048 sc1\nM 60,168,68\nR 164,189,40960\n"
"v_and_b32 v8, s77, v0\nM 60,172,84\nv_sub_f32 v9, v0, v8\ns_waitcnt vmcnt(21)\nv_lshlrev_b32 v10, 16, v239\nM 44,168,64\n"
"R 166,189,45056\nv_cvt_pk_bf16_f32 v4, v8, v9\nM 44,172,80\nG 4\nv_cvt_pk_bf16_f32 v5, v8, v10\nv_and_b32 v8, s77, v2\n"
"M 96,176,64\nR 168,190,32768\nv_sub_f32 v9, v2, v8\nM 96,180,80\nbuffer_load_dwordx4 a[176:179], v252, s[20:23], s64 offen offset:3072 sc1\nM 112,176,68\n"
"R 170,190,36864\nv_and_b32 v10, s77, v239\nM 112,180,84\nv_cvt_pk_bf16_f32 v6, v8, v9\nv_cvt_pk_bf16_f32 v7, v8, v10\nM 128,176,72\n"
"R 172,190,40960\nv_and_b32 v238, 0x1ff, v238\nM 128,180,88\ns_add_u32 m0, s50, 24576\nv_add_u32 v238, 158208, v238\nds_write_b128 v238, v[4:7]\n"
"M 144,176,76\nR 174,190,45056\ns_add_u32 s63, s54, 1835008\nM 144,180,92\ns_waitcnt vmcnt(15) lgkmcnt(1)\ns_barrier\n"
"M 148,160,76\nR 176,191,32768\nM 148,164,92\nbuffer_load_dwordx4 a[180:183], v252, s[20:23], s65 offen offset:3072 sc1\nM 132,160,72\nR 178,191,36864\n"
"M 132,164,88\nbuffer_load_dwordx4 v220, s[12:15], s63 offen lds\nM 116,160,68\nR 180,191,40960\nM 116,164,84\nbuffer_load_dwordx4 a[184:187], v252, s[20:23], s66 offen offset:3072 sc1\n"
"M 100,160,64\nR 182,191,45056\nM 100,164,80\nG 4\nM 104,168,64\nR 160,188,49152\n"
"buffer_load_dwordx4 v221, s[12:15], s63 offen offset:1024 lds\nM 104,172,80\ns_add_u32 m0, s50, 32768\ns_add_u32 s63, s54, 2064384\nbuffer_load_dwordx4 v220, s[12:15], s63 offen lds\nM 120,168,68\n"
"R 162,188,53248\nM 120,172,84\nbuffer_load_dwordx4 a[188:191], v252, s[20:23], s67 offen offset:3072 sc1\nM 136,168,72\nR 164,184,0\nM 136,172,88\n"
"buffer_load_dwordx4 v221, s[12:15], s63 offen offset:1024 lds\nM 152,168,76\nR 166,184,4096\ns_waitcnt vmcnt(15)\nv_min3_u32 v244, v17, v19, v21\nv_min3_u32 v244, v244, v23, v23\n"
"M 152,172,92\nG 4\nv_max3_u32 v245, v17, v19, v21\nv_max3_u32 v245, v245, v23, v23\nv_cmp_ne_u32 s[86:87], s46, v244\nM 156,176,76\n"
"R 168,189,49152\nv_cmp_ne_u32 s[88:89], s46, v245\nM 156,180,92\ns_add_u32 s64, s70, 10498048\nbuffer_load_dwordx4 a[192:195], v252, s[20:23], s64 offen offset:0 sc1\ns_or_b64 s[86:87], s[86:87], s[88:89]\n"
"s_cbranch_scc1 Lq351\nLq352:\nM 140,176,72\nR 170,189,53248\nv_add_f32 v16, v16, v20\nv_add_f32 v18, v18, v22\n"
"M 140,180,88\nv_add_f32_dpp v16, v16, v16 row_ror:8 row_mask:0xf bank_mask:0xf\nv_add_f32_dpp v18, v18, v18 row_ror:8 row_mask:0xf bank_mask:0xf\nv_mov_b32 v20, v16\nM 124,176,68\nR 172,185,0\n"
"v_mov_b32 v21, v18\nv_permlane32_swap_b32 v20, v16\nM 124,180,84\ns_add_u32 s65, s70, 10530816\nbuffer_load_dwordx4 a[196:199], v252, s[20:23], s65 offen offset:0 sc1\nv_permlane32_swap_b32 v21, v18\n"
"M 108,176,64\nR 174,185,4096\nv_add_f32 v16, v16, v20\nv_add_f32 v18, v18, v21\nM 108,180,80\nG 4\n"
"v_mov_b32 v20, v16\nv_mov_b32 v21, v18\ns_nop 0\nv_permlane16_swap_b32 v20, v16\nM 32,160,96\nR 176,190,49152\n"
"v_permlane16_swap_b32 v21, v18\nM 32,164,112\nv_add_f32 v16, v16, v20\nv_add_f32 v18, v18, v21\nM 48,160,100\nR 178,190,53248\n"
"v_and_b32 v24, s77, v16\nM 48,164,116\ns_add_u32 s66, s71, 10498048\nbuffer_load_dwordx4 a[200:203], v252, s[20:23], s66 offen offset:0 sc1\nM 64,160,104\nR 180,186,0\n"
"v_sub_f32 v25, v16, v24\nM 64,164,120\ns_add_u32 m0, s50, 40960\ns_waitcnt vmcnt(15)\nv_lshlrev_b32 v26, 16, v243\nv_cvt_pk_bf16_f32 v20, v24, v25\n"
"M 80,160,108\nR 182,186,4096\ns_add_u32 s63, s54, 2293760\nM 80,164,124\ns_waitcnt vmcnt(12)\ns_barrier\n"
"G 4\nM 84,168,108\nR 160,191,49152\nv_cvt_pk_bf16_f32 v21, v24, v26\nM 84,172,124\ns_add_u32 s67, s71, 10530816\n"
"buffer_load_dwordx4 a[204:207], v252, s[20:23], s67 offen offset:0 sc1\nM 68,168,104\nR 162,191,53248\nv_and_b32 v24, s77, v18\nM 68,172,120\nbuffer_load_dwordx4 v220, s[12:15], s63 offen lds\n"
"M 52,168,100\nR 164,187,0\nv_sub_f32 v25, v18, v24\nM 52,172,116\nbuffer_load_dwordx4 v221, s[12:15], s63 offen offset:1024 lds\nM 36,168,96\n"
"R 166,187,4096\ns_add_u32 m0, s50, 49152\nM 36,172,112\nG 4\nv_and_b32 v26, s77, v243\nv_cvt_pk_bf16_f32 v22, v24, v25\n"
"M 40,176,96\nR 168,184,8192\ns_add_u32 s63, s54, 2523136\nM 40,180,112\nbuffer_load_dwordx4 a[208:211], v252, s[20:23], s64 offen offset:1024 sc1\nM 56,176,100\n"
"R 170,184,12288\nv_cvt_pk_bf16_f32 v23, v24, v26\nM 56,180,116\nbuffer_load_dwordx4 v220, s[12:15], s63 offen lds\nM 72,176,104\nR 172,184,16384\n"
"v_and_b32 v242, 0x1ff, v242\nM 72,180,120\nbuffer_load_dwordx4 a[212:215], v252, s[20:23], s65 offen offset:1024 sc1\nM 88,176,108\nR 174,184,20480\nv_add_u32 v242, 158720, v242\n"
"M 88,180,124\nds_write_b128 v242, v[20:23]\nG 5\nM 92,160,108\nR 176,185,8192\nbuffer_load_dwordx4 v221, s[12:15], s63 offen offset:1024 lds\n"
"M 92,164,124\nM 76,160,104\nR 178,185,12288\nM 76,164,120\nbuffer_load_dwordx4 a[216:219], v252, s[20:23], s66 offen offset:1024 sc1\nM 60,160,100\n"
"R 180,185,16384\nLq581:\ns_lshr_b32 s60, s45, 3\ns_lshl2_add_u32 s60, s60, s40\ns_lshl_b32 s60, s60, 8\nv_lshl_add_u32 v224, v252, 3, s60\n"
"v_add_u32 v224, Lq581+4096-LqPC, v224\nv_min_u32 v224, LqE-LqPC-4, v224\ns_mov_b64 exec, 3\nglobal_load_dword v224, v224, s[98:99]\ns_mov_b64 exec, -1\nM 60,164,116\n"
"M 44,160,96\nR 182,185,20480\nM 44,164,112\nG 5\nM 96,168,96\nR 160,186,8192\n"
"M 96,172,112\nbuffer_load_dwordx4 a[220:223], v252, s[20:23], s67 offen offset:1024 sc1\nM 112,168,100\nR 162,186,12288\nM 112,172,116\nM 128,168,104\n"
"R 164,186,16384\nM 128,172,120\nM 144,168,108\nR 166,186,20480\ns_add_u32 m0, s50, 57344\nM 144,172,124\n"
"s_waitcnt vmcnt(13) lgkmcnt(8)\ns_barrier\nG 4\nM 148,176,108\nR 168,187,8192\ns_add_u32 s63, s54, 2752512\n"
"M 148,180,124\nbuffer_load_dwordx4 a[224:227], v252, s[20:23], s64 offen offset:2048 sc1\nM 132,176,104\nR 170,187,12288\nM 132,180,120\nbuffer_load_dwordx4 a[228:231], v252, s[20:23], s65 offen offset:2048 sc1\n"
"M 116,176,100\nR 172,187,16384\nbuffer_load_dwordx4 v220, s[12:15], s63 offen lds\nM 116,180,116\nbuffer_load_dwordx4 v221, s[12:15], s63 offen offset:1024 lds\nM 100,176,96\n"
"R 174,187,20480\ns_add_u32 m0, s50, 65536\nM 100,180,112\nG 4\nM 104,160,96\nR 176,184,24576\n"
"s_add_u32 s63, s54, 2981888\nM 104,164,112\nbuffer_load_dwordx4 a[232:235], v252, s[20:23], s66 offen offset:2048 sc1\nM 120,160,100\nR 178,184,28672\nM 120,164,116\n"
"buffer_load_dwordx4 v220, s[12:15], s63 offen lds\nM 136,160,104\nR 180,184,32768\nM 136,164,120\nbuffer_load_dwordx4 v221, s[12:15], s63 offen offset:1024 lds\nM 152,160,108\n"
"R 182,184,36864\nM 152,164,124\nG 4\nM 156,168,108\nR 160,185,24576\nM 156,172,124\n"
"buffer_load_dwordx4 a[236:239], v252, s[20:23], s67 offen offset:2048 sc1\nM 140,168,104\nR 162,185,28672\nM 140,172,120\nM 124,168,100\nR 164,185,32768\n"
"M 124,172,116\nbuffer_load_dwordx4 a[240:243], v252, s[20:23], s64 offen offset:3072 sc1\nM 108,168,96\nR 166,185,36864\nM 108,172,112\nG 4\n"
"M 32,176,128\nR 168,186,24576\nM 32,180,144\nM 48,176,132\nR 170,186,28672\nM 48,180,148\n"
"buffer_load_dwordx4 a[244:247], v252, s[20:23], s65 offen offset:3072 sc1\nM 64,176,136\nR 172,186,32768\nM 64,180,152\nM 80,176,140\nR 174,186,36864\n"
"s_add_u32 m0, s50, 73728\nM 80,180,156\ns_waitcnt vmcnt(13)\ns_barrier\nG 4\nM 84,160,140\n"
"R 176,187,24576\ns_add_u32 s63, s54, 3211264\nM 84,164,156\nbuffer_load_dwordx4 a[248:251], v252, s[20:23], s66 offen offset:3072 sc1\nM 68,160,136\nR 178,187,28672\n"
"M 68,164,152\ns_lshr_b32 s84, s49, 3\nM 52,160,132\nR 180,187,32768\nbuffer_load_dwordx4 v220, s[12:15], s63 offen lds\nM 52,164,148\n"
"s_add_u32 s84, s84, 82313216\nM 36,160,128\nR 182,187,36864\nM 36,164,144\nbuffer_load_dwordx4 v[0:3], v252, s[20:23], s84 offen offset:0 sc1\nG 4\n"
"M 40,168,128\nR 160,184,40960\nbuffer_load_dwordx4 v221, s[12:15], s63 offen offset:1024 lds\nM 40,172,144\nbuffer_load_dwordx4 a[252:255], v252, s[20:23], s67 offen offset:3072 sc1\ns_add_u32 s63, s54, 3440640\n"
"M 56,168,132\nR 162,184,45056\ns_add_u32 m0, s50, 81920\nM 56,172,148\nbuffer_load_dwordx4 v[4:7], v252, s[20:23], s84 offen offset:1024 sc1\nM 72,168,136\n"
"R 164,184,49152\ns_lshr_b32 s85, s49, 3\nbuffer_load_dwordx4 v220, s[12:15], s63 offen lds\nM 72,172,152\nbuffer_load_dwordx4 v[8:11], v252, s[20:23], s84 offen offset:2048 sc1\nM 88,168,140\n"
"R 166,184,53248\ns_add_u32 s85, s85, 82317312\nM 88,172,156\nG 4\nM 92,176,140\nR 168,185,40960\n"
"buffer_load_dwordx4 v221, s[12:15], s63 offen offset:1024 lds\nM 92,180,156\nbuffer_load_dwordx4 v[12:15], v252, s[20:23], s84 offen offset:3072 sc1\nM 76,176,136\nR 170,185,45056\nM 76,180,152\n"
"buffer_load_dwordx4 v[16:19], v252, s[20:23], s85 offen offset:0 sc1\nM 60,176,132\nR 172,185,49152\nM 60,180,148\nbuffer_load_dwordx4 v[20:23], v252, s[20:23], s85 offen offset:1024 sc1\nM 44,176,128\n"
"R 174,185,53248\nM 44,180,144\nG 4\nM 32,160,160\nR 176,186,40960\nM 32,164,176\n"
"buffer_load_dwordx4 v[24:27], v252, s[20:23], s85 offen offset:2048 sc1\nM 48,160,164\nR 178,186,45056\nM 48,164,180\nbuffer_load_dwordx4 v[28:31], v252, s[20:23], s85 offen offset:3072 sc1\nM 64,160,168\n"
"R 180,186,49152\ns_add_u32 s63, s54, 1835136\nM 64,164,184\ns_add_u32 m0, s50, 90112\nM 80,160,172\nR 182,186,53248\n"
"M 80,164,188\ns_waitcnt vmcnt(17)\ns_barrier\nG 4\nM 84,168,172\nR 160,187,40960\n"
"M 84,172,188\nbuffer_load_dwordx4 v220, s[12:15], s63 offen lds\nM 68,168,168\nR 162,187,45056\nM 68,172,184\nM 52,168,164\n"
"R 164,187,49152\nv_bfe_u32 v250, v252, 5, 1\nv_mul_u32_u24 v250, 2560, v250\nM 52,172,180\nbuffer_load_dwordx4 v221, s[12:15], s63 offen offset:1024 lds\nv_and_b32 v251, 16, v252\n"
"M 36,168,160\nR 166,187,53248\ns_add_u32 m0, s50, 98304\nv_lshl_add_u32 v250, v251, 1, v250\nM 36,172,176\ns_lshl_b32 s60, s90, 6\n"
"v_add_u32 v250, s60, v250\nG 4\nM 40,176,160\nR 168,184,57344\ns_add_u32 s63, s54, 2064512\nM 40,180,176\n"
"buffer_load_dwordx4 v220, s[12:15], s63 offen lds\nM 56,176,164\nR 170,184,61440\nM 56,180,180\nM 72,176,168\nR 172,188,0\n"
"M 72,180,184\nbuffer_load_dwordx4 v221, s[12:15], s63 offen offset:1024 lds\nM 88,176,172\nR 174,188,4096\nM 88,180,188\nG 4\n"
"M 92,160,172\nR 176,185,57344\nM 92,164,188\nM 76,160,168\nR 178,185,61440\nM 76,164,184\n"
"s_add_u32 m0, s50, 106496\nM 60,160,164\nR 180,189,0\nM 60,164,180\ns_add_u32 s63, s54, 2293888\nM 44,160,160\n"
"R 182,189,4096\nM 44,164,176\nG 4\nM 32,168,192\nR 160,186,57344\nM 32,172,208\n"
"buffer_load_dwordx4 v220, s[12:15], s63 offen lds\nM 48,168,196\nR 162,186,61440\nM 48,172,212\nM 64,168,200\nR 164,190,0\n"
"M 64,172,216\nbuffer_load_dwordx4 v221, s[12:15], s63 offen offset:1024 lds\nM 80,168,204\nR 166,190,4096\ns_add_u32 m0, s50, 114688\nM 80,172,220\n"
"s_waitcnt vmcnt(11)\ns_barrier\nG 4\nM 84,176,204\nR 168,187,57344\ns_add_u32 s63, s54, 2523264\n"
"M 84,180,220\nbuffer_load_dwordx4 v220, s[12:15], s63 offen lds\nM 68,176,200\nR 170,187,61440\nv_min3_u32 v230, v1, v3, v5\nv_min3_u32 v230, v230, v7, v9\n"
"M 68,180,216\ns_waitcnt vmcnt(11)\nv_min3_u32 v230, v230, v11, v13\ns_waitcnt vmcnt(10)\nv_min3_u32 v230, v230, v15, v17\ns_waitcnt vmcnt(9)\n"
"v_min3_u32 v230, v230, v19, v21\nM 52,176,196\nR 172,191,0\ns_waitcnt vmcnt(8)\nv_min3_u32 v230, v230, v23, v25\ns_waitcnt vmcnt(7)\n"
"v_min3_u32 v230, v230, v27, v29\nM 52,180,212\nbuffer_load_dwordx4 v221, s[12:15], s63 offen offset:1024 lds\nv_min3_u32 v230, v230, v31, v31\nM 36,176,192\nR 174,191,4096\n"
"s_add_u32 s63, s54, 2752640\nv_max3_u32 v231, v1, v3, v5\nM 36,180,208\nG 4\nv_max3_u32 v231, v231, v7, v9\nv_max3_u32 v231, v231, v11, v13\n"
"v_max3_u32 v231, v231, v15, v17\nM 40,160,192\nR 176,188,8192\nv_max3_u32 v231, v231, v19, v21\nv_max3_u32 v231, v231, v23, v25\nM 40,164,208\n"
"s_add_u32 m0, s50, 0\nv_max3_u32 v231, v231, v27, v29\nv_max3_u32 v231, v231, v31, v31\nv_cmp_ne_u32 s[86:87], s46, v230\nM 56,160,196\nR 178,188,12288\n"
"v_cmp_ne_u32 s[88:89], s46, v231\nM 56,164,212\nbuffer_load_dwordx4 v220, s[12:15], s63 offen lds\ns_or_b64 s[86:87], s[86:87], s[88:89]\ns_cbranch_scc1 Lq319\nLq320:\n"
"M 72,160,200\nR 180,188,16384\nv_add_f32 v0, v0, v16\nv_add_f32 v2, v2, v18\nM 72,164,216\nv_add_f32 v4, v4, v20\n"
"v_add_f32 v6, v6, v22\nv_add_f32 v8, v8, v24\nM 88,160,204\nR 182,188,20480\nv_add_f32 v10, v10, v26\nv_add_f32 v12, v12, v28\n"
"M 88,164,220\nG 4\nv_add_f32 v14, v14, v30\nv_add_f32 v0, v0, v8\nv_add_f32 v2, v2, v10\nM 92,168,204\n"
"R 160,189,8192\nv_add_f32 v4, v4, v12\nv_add_f32 v6, v6, v14\nM 92,172,220\nbuffer_load_dwordx4 v221, s[12:15], s63 offen offset:1024 lds\nv_add_f32 v0, v0, v4\n"
"M 76,168,200\nR 162,189,12288\ns_add_u32 s63, s54, 2982016\nv_add_f32 v2, v2, v6\nM 76,172,216\ns_add_u32 m0, s50, 8192\n"
"v_lshrrev_b32 v231, 1, v252\nv_lshrrev_b32 v230, 2, v252\ns_lshl_b32 s62, s40, 9\ns_add_u32 s62, s62, 159232\nv_add_u32 v231, s62, v231\nM 60,168,196\n"
"R 164,189,16384\nds_write2_b32 v231, v0, v2 offset1:1\nM 60,172,212\nbuffer_load_dwordx4 v220, s[12:15], s63 offen lds\ns_lshl_b32 s62, s40, 9\ns_add_u32 s62, s62, 159232\n"
"v_add_u32 v230, s62, v230\nM 44,168,192\nR 166,189,20480\nds_read2st64_b32 v[232:233], v230 offset1:1\nM 44,172,208\nG 6\n"
"M 32,176,224\nR 168,190,8192\nM 32,180,240\nbuffer_load_dwordx4 v221, s[12:15], s63 offen offset:1024 lds\nM 48,176,228\nR 170,190,12288\n"
"s_add_u32 s63, s54, 3211392\nM 48,180,244\ns_add_u32 m0, s50, 16384\nM 64,176,232\nLpg9:\ns_cbranch_execz Lpg13\n"
"R 172,190,16384\nG 3\nv_add_f32 v232, v232, v233\nv_mov_b32 v233, v232\nM 64,180,248\nbuffer_load_dwordx4 v220, s[12:15], s63 offen lds\n"
"v_permlane32_swap_b32 v232, v233\nM 80,176,236\nR 174,190,20480\nv_mov_b32 v230, v232\nv_mov_b32 v231, v233\nM 80,180,252\n"
"s_waitcnt vmcnt(9)\ns_barrier\nv_permlane16_swap_b32 v230, v232\nM 84,160,236\nR 176,191,8192\nv_permlane16_swap_b32 v231, v233\n"
"v_mul_f32 v230, 0xba800000, v230\nM 84,164,252\nbuffer_load_dwordx4 v221, s[12:15], s63 offen offset:1024 lds\nv_mul_f32 v214, v230, v230\nM 68,160,232\nR 178,191,12288\n"
"v_mul_f32 v231, 0x3a800000, v231\nv_sub_f32 v231, v231, v214\nM 68,164,248\ns_add_u32 m0, s50, 24576\nv_max_f32 v231, 0, v231\nv_add_f32 v231, 0x3727c5ac, v231\n"
"v_rsq_f32 v234, v231\nM 52,160,228\nR 180,191,16384\nv_and_b32 v208, s77, v230\nv_mul_f32 v216, v231, v234\nM 52,164,244\n"
"s_add_u32 s63, s54, 3440768\nv_sub_f32 v230, v230, v208\nv_cvt_pk_bf16_f32 v209, v230, v216\nv_cvt_pk_bf16_f32 v208, v208, v208\nM 36,160,224\nR 182,191,20480\n"
"v_mul_f32 v214, 0xc0135761, v234\nv_mul_f32 v215, v234, v234\nM 36,164,240\ns_mov_b64 s[60:61], 0xffff\nv_cndmask_b32 v208, 0, v208, s[60:61]\nG 4\n"
"v_mul_f32 v215, v215, v214\nM 40,168,224\nR 160,188,24576\nv_mul_f32 v215, 0x3d372713, v215\nv_mul_f32 v232, 0xba800000, v232\nM 40,172,240\n"
"buffer_load_dwordx4 v220, s[12:15], s63 offen lds\nv_mul_f32 v217, v232, v232\nM 56,168,228\nR 162,188,28672\nv_mul_f32 v233, 0x3a800000, v233\nv_sub_f32 v233, v233, v217\n"
"M 56,172,244\ns_mov_b64 s[60:61], 0xffff\nv_cndmask_b32 v209, 0, v209, s[60:61]\nv_max_f32 v233, 0, v233\nv_add_f32 v233, 0x3727c5ac, v233\nM 72,168,232\n"
"R 164,188,32768\nv_and_b32 v212, s77, v232\nv_rsq_f32 v235, v233\nM 72,172,248\nbuffer_load_dwordx4 v221, s[12:15], s63 offen offset:1024 lds\nv_mul_f32 v219, v233, v235\n"
"M 88,168,236\nR 166,188,36864\ns_add_u32 m0, s50, 32768\nv_sub_f32 v232, v232, v212\nM 88,172,252\nG 4\n"
"v_cvt_pk_bf16_f32 v213, v232, v219\nv_cvt_pk_bf16_f32 v212, v212, v212\nv_mul_f32 v217, 0xc0135761, v235\nM 92,176,236\nR 168,189,24576\ns_add_u32 s63, s55, 0\n"
"v_mul_f32 v218, v235, v235\nM 92,180,252\nbuffer_load_dwordx4 v222, s[12:15], s63 offen lds\nv_mul_f32 v218, v218, v217\nM 76,176,232\nR 170,189,28672\n"
"v_mul_f32 v218, 0x3d372713, v218\ns_mov_b64 s[60:61], 0xffff\nv_cndmask_b32 v212, 0, v212, s[60:61]\nM 76,180,248\ns_mov_b64 s[60:61], 0xffff\nv_cndmask_b32 v213, 0, v213, s[60:61]\n"
"M 60,176,228\nR 172,189,32768\nM 60,180,244\nbuffer_load_dwordx4 v223, s[12:15], s63 offen offset:1024 lds\nM 44,176,224\nR 174,189,36864\n"
"M 44,180,240\ns_waitcnt vmcnt(10)\ns_barrier\nG 4\nM 96,160,128\nR 176,190,24576\n"
"M 96,164,144\ns_mov_b32 m0, s51\nM 112,160,132\nR 178,190,28672\ns_lshl2_add_u32 s60, s45, s40\nv_lshlrev_b32 v224, 3, v252\n"
"s_mul_i32 s60, s60, 7168\ns_bfm_b64 exec, 14, 0\nbuffer_load_dword v224, v224, s[16:19], s60 offen\ns_mov_b64 exec, -1\nM 112,164,148\nds_write_addtid_b32 v64 offset:0\n"
"M 128,160,136\nR 180,190,32768\nM 128,164,152\nds_write_addtid_b32 v65 offset:256\nM 144,160,140\nR 182,190,36864\n"
"ds_write_addtid_b32 v66 offset:512\nM 144,164,156\nds_write_addtid_b32 v67 offset:768\nds_write_addtid_b32 v68 offset:1024\nG 9\nM 148,168,140\n"
"R 160,191,24576\nds_write_addtid_b32 v69 offset:1280\nM 148,172,156\nds_write_addtid_b32 v70 offset:1536\nds_write_addtid_b32 v71 offset:1792\nM 132,168,136\n"
"R 162,191,28672\nds_write_addtid_b32 v72 offset:2048\nM 132,172,152\nds_write_addtid_b32 v73 offset:2304\nds_write_addtid_b32 v74 offset:2560\nM 116,168,132\n"
"R 164,191,32768\nds_write_addtid_b32 v75 offset:2816\nM 116,172,148\nds_write_addtid_b32 v76 offset:3072\nds_write_addtid_b32 v77 offset:3328\nM 100,168,128\n"
"R 166,191,36864\nds_write_addtid_b32 v78 offset:3584\nM 100,172,144\nds_write_addtid_b32 v79 offset:3840\nds_write_addtid_b32 v80 offset:4096\nG 15\n"
"M 104,176,128\nR 168,188,40960\nds_write_addtid_b32 v81 offset:4352\nM 104,180,144\nds_write_addtid_b32 v82 offset:4608\nds_write_addtid_b32 v83 offset:4864\n"
"M 120,176,132\nR 170,188,45056\nds_write_addtid_b32 v84 offset:5120\nM 120,180,148\nds_write_addtid_b32 v85 offset:5376\nds_write_addtid_b32 v86 offset:5632\n"
"M 136,176,136\nR 172,188,49152\nds_write_addtid_b32 v87 offset:5888\nM 136,180,152\nds_write_addtid_b32 v88 offset:6144\nds_write_addtid_b32 v89 offset:6400\n"
"M 152,176,140\nR 174,188,53248\nds_write_addtid_b32 v90 offset:6656\nM 152,180,156\nds_write_addtid_b32 v91 offset:6912\nds_write_addtid_b32 v92 offset:7168\n"
"G 15\nM 156,160,140\nR 176,189,40960\nds_write_addtid_b32 v93 offset:7424\nM 156,164,156\nds_write_addtid_b32 v94 offset:7680\n"
"ds_write_addtid_b32 v95 offset:7936\nM 140,160,136\nR 178,189,45056\ns_add_u32 m0, s50, 40960\ns_add_u32 s63, s55, 458752\nM 140,164,152\n"
"buffer_load_dwordx4 v222, s[12:15], s63 offen lds\nM 124,160,132\nR 180,189,49152\ns_waitcnt vmcnt(8) lgkmcnt(2)\ns_barrier\nM 124,164,148\n"
"ds_read2st64_b32 v[64:65], v225 offset0:0 offset1:1\nds_read2st64_b32 v[66:67], v225 offset0:2 offset1:3\nM 108,160,128\nR 182,189,53248\nds_read2st64_b32 v[68:69], v225 offset0:4 offset1:5\nM 108,164,144\n"
"ds_read2st64_b32 v[70:71], v225 offset0:6 offset1:7\nds_read2st64_b32 v[72:73], v225 offset0:8 offset1:9\nM 96,168,160\nR 160,190,40960\nds_read2st64_b32 v[74:75], v225 offset0:10 offset1:11\nM 96,172,176\n"
"buffer_load_dwordx4 v223, s[12:15], s63 offen offset:1024 lds\nds_read2st64_b32 v[76:77], v225 offset0:12 offset1:13\nM 112,168,164\nR 162,190,45056\nds_read2st64_b32 v[78:79], v225 offset0:14 offset1:15\nM 112,172,180\n"
"s_add_u32 m0, s50, 49152\nds_read2st64_b32 v[80:81], v225 offset0:16 offset1:17\nds_read2st64_b32 v[82:83], v225 offset0:18 offset1:19\nM 128,168,168\nR 164,190,49152\nds_read2st64_b32 v[84:85], v225 offset0:20 offset1:21\n"
"G 14\nv_add_f32 v32, v32, v64\nM 128,172,184\ns_add_u32 s63, s55, 917504\nds_read2st64_b32 v[86:87], v225 offset0:22 offset1:23\nds_read2st64_b32 v[88:89], v225 offset0:24 offset1:25\n"
"M 144,168,172\nR 166,190,53248\nds_read2st64_b32 v[90:91], v225 offset0:26 offset1:27\nv_add_f32 v33, v33, v65\nM 144,172,188\nds_read2st64_b32 v[92:93], v225 offset0:28 offset1:29\n"
"ds_read2st64_b32 v[94:95], v225 offset0:30 offset1:31\nG 15\nM 148,176,172\nR 168,191,40960\nv_add_f32 v34, v34, v66\nv_add_f32 v35, v35, v67\n"
"M 148,180,188\nbuffer_load_dwordx4 v222, s[12:15], s63 offen lds\nv_add_f32 v36, v36, v68\nM 132,176,168\nR 170,191,45056\nv_add_f32 v37, v37, v69\n"
"v_add_f32 v38, v38, v70\nM 132,180,184\nv_add_f32 v39, v39, v71\nv_add_f32 v40, v40, v72\nv_add_f32 v41, v41, v73\nM 116,176,164\n"
"R 172,191,49152\nG 15\nv_add_f32 v42, v42, v74\nv_add_f32 v43, v43, v75\nM 116,180,180\nv_add_f32 v44, v44, v76\n"
"v_add_f32 v45, v45, v77\nG 13\nv_add_f32 v46, v46, v78\nM 100,176,160\nR 174,191,53248\nv_add_f32 v47, v47, v79\n"
"G 13\nv_add_f32 v48, v48, v80\nM 100,180,176\nG 7\nv_add_f32 v49, v49, v81\nv_add_f32 v50, v50, v82\n"
"M 104,160,160\nR 176,184,0\ns_mov_b32 s60, 157184\nv_bfe_u32 v246, v252, 4, 4\nv_lshl_add_u32 v246, v246, 3, s60\nM 104,164,176\n"
"ds_read_b64 v[238:239], v246 offset:0\nv_add_f32 v51, v51, v83\nM 120,160,164\nR 178,184,4096\nds_read_b64 v[240:241], v246 offset:128\nM 120,164,180\n"
"ds_read_b64 v[242:243], v246 offset:256\nv_add_f32 v52, v52, v84\nM 136,160,168\nR 180,184,8192\nds_read_b64 v[244:245], v246 offset:384\nM 136,164,184\n"
"ds_read_b64 v[246:247], v246 offset:0\nv_add_f32 v53, v53, v85\nM 152,160,172\nR 182,184,12288\nv_add_f32 v54, v54, v86\nM 152,164,188\n"
"G 9\nv_add_f32 v55, v55, v87\nv_add_f32 v56, v56, v88\nM 156,168,172\nR 160,185,0\nv_add_f32 v57, v57, v89\n"
"M 156,172,188\nbuffer_load_dwordx4 v223, s[12:15], s63 offen offset:1024 lds\nM 140,168,168\nR 162,185,4096\nv_add_f32 v58, v58, v90\nM 140,172,184\n"
"v_add_f32 v59, v59, v91\nv_add_f32 v60, v60, v92\nM 124,168,164\nR 164,185,8192\nv_add_f32 v61, v61, v93\nM 124,172,180\n"
"s_add_u32 m0, s50, 57344\nv_add_f32 v62, v62, v94\nv_add_f32 v63, v63, v95\nM 108,168,160\nR 166,185,12288\nG 9\n"
"v_mfma_f32_16x16x32_bf16 v[32:35], v[238:241], v[208:211], v[32:35]\nM 108,172,176\nG 8\nv_mfma_f32_16x16x32_bf16 v[36:39], v[240:243], v[208:211], v[36:39]\nG 4\nM 96,176,192\n"
"R 168,186,0\nv_mfma_f32_16x16x32_bf16 v[40:43], v[242:245], v[208:211], v[40:43]\nM 96,180,208\nv_mfma_f32_16x16x32_bf16 v[44:47], v[244:247], v[208:211], v[44:47]\ns_add_u32 s63, s55, 1376256\nM 112,176,196\n"
"R 170,186,4096\ns_lshl2_add_u32 s60, s45, s40\ns_waitcnt vmcnt(4)\nv_lshlrev_b32 v224, 3, v252\ns_mul_i32 s60, s60, 7168\ns_bfm_b64 exec, 14, 14\n"
"buffer_load_dword v224, v224, s[16:19], s60 offen\ns_mov_b64 exec, -1\nv_mfma_f32_16x16x32_bf16 v[48:51], v[244:247], v[210:213], v[48:51]\ns_barrier\nM 112,180,212\nv_mfma_f32_16x16x32_bf16 v[52:55], v[238:241], v[210:213], v[52:55]\n"
"buffer_load_dwordx4 v222, s[12:15], s63 offen lds\nM 128,176,200\nR 172,186,8192\nv_mfma_f32_16x16x32_bf16 v[56:59], v[240:243], v[210:213], v[56:59]\nM 128,180,216\nv_mfma_f32_16x16x32_bf16 v[60:63], v[242:245], v[210:213], v[60:63]\n"
"M 144,176,204\nR 174,186,12288\nM 144,180,220\nG 4\nM 148,160,204\nR 176,187,0\n"
"M 148,164,220\nbuffer_load_dwordx4 v223, s[12:15], s63 offen offset:1024 lds\nM 132,160,200\nR 178,187,4096\nM 132,164,216\ns_add_u32 m0, s50, 65536\n"
"M 116,160,196\nR 180,187,8192\nv_mul_f32 v230, v32, v32\nv_mul_f32 v231, v33, v33\nM 116,164,212\ns_add_u32 s63, s55, 1835008\n"
"s_add_u32 s84, s73, 23068672\ns_add_u32 s85, s73, 23183360\nv_mul_f32 v232, v34, v34\nM 100,160,192\nR 182,187,12288\nv_mul_f32 v233, v35, v35\n"
"v_fma_f32 v230, v230, v215, v214\nM 100,164,208\nG 4\nv_fma_f32 v231, v231, v215, v214\nv_fma_f32 v232, v232, v215, v214\nv_fma_f32 v233, v233, v215, v214\n"
"M 104,168,192\nR 160,184,16384\nv_mul_f32 v230, v32, v230\nv_mul_f32 v231, v33, v231\nM 104,172,208\nbuffer_load_dwordx4 v222, s[12:15], s63 offen lds\n"
"v_mul_f32 v232, v34, v232\nM 120,168,196\nR 162,184,20480\nv_mul_f32 v233, v35, v233\nv_exp_f32 v230, v230\nM 120,172,212\n"
"v_exp_f32 v231, v231\nv_exp_f32 v232, v232\nM 136,168,200\nR 164,184,24576\nv_fma_f32 v230, v230, v216, v216\nv_exp_f32 v233, v233\n"
"M 136,172,216\nv_rcp_f32 v230, v230\nv_fma_f32 v231, v231, v216, v216\nM 152,168,204\nR 166,184,28672\nv_fma_f32 v232, v232, v216, v216\n"
"v_fma_f32 v233, v233, v216, v216\nM 152,172,220\nG 4\nv_rcp_f32 v231, v231\nv_rcp_f32 v232, v232\nM 156,176,204\n"
"R 168,185,16384\nv_mul_f32 v230, v32, v230\nv_rcp_f32 v233, v233\nM 156,180,220\nbuffer_load_dwordx4 v223, s[12:15], s63 offen offset:1024 lds\nv_mul_f32 v231, v33, v231\n"
"M 140,176,200\nR 170,185,20480\nv_mul_f32 v232, v34, v232\nv_mul_f32 v233, v35, v233\nM 140,180,216\nv_cvt_pk_bf16_f32 v32, v230, v231\n"
"v_cvt_pk_bf16_f32 v33, v232, v233\nv_mul_f32 v234, v36, v36\nM 124,176,196\nR 172,185,24576\nv_mul_f32 v235, v37, v37\nv_mul_f32 v236, v38, v38\n"
"M 124,180,212\nv_mul_f32 v237, v39, v39\nv_fma_f32 v234, v234, v215, v214\nv_fma_f32 v235, v235, v215, v214\nM 108,176,192\nR 174,185,28672\n"
"s_mov_b32 s60, 157696\nv_bfe_u32 v246, v252, 4, 4\nv_lshl_add_u32 v246, v246, 3, s60\nv_fma_f32 v236, v236, v215, v214\nM 108,180,208\nds_read_b64 v[238:239], v246 offset:0\n"
"G 5\nv_fma_f32 v237, v237, v215, v214\nM 96,160,224\nR 176,186,16384\nds_read_b64 v[240:241], v246 offset:128\nv_mul_f32 v234, v36, v234\n"
"M 96,164,240\nds_read_b64 v[242:243], v246 offset:256\ns_add_u32 m0, s50, 73728\nv_mul_f32 v235, v37, v235\nM 112,160,228\nR 178,186,20480\n"
"ds_read_b64 v[244:245], v246 offset:384\nv_mul_f32 v236, v38, v236\nM 112,164,244\nds_read_b64 v[246:247], v246 offset:0\ns_add_u32 s63, s55, 2293760\nv_mul_f32 v237, v39, v237\n"
"M 128,160,232\nR 180,186,24576\nv_mul_f32 v230, v40, v40\nv_exp_f32 v234, v234\nM 128,164,248\nbuffer_load_dwordx4 v222, s[12:15], s63 offen lds\n"
"v_exp_f32 v235, v235\nM 144,160,236\nR 182,186,28672\nv_fma_f32 v234, v234, v216, v216\nv_exp_f32 v236, v236\nM 144,164,252\n"
"G 9\nv_exp_f32 v237, v237\nv_fma_f32 v235, v235, v216, v216\nM 148,168,236\nR 160,187,16384\nv_fma_f32 v236, v236, v216, v216\n"
"v_fma_f32 v237, v237, v216, v216\nM 148,172,252\nbuffer_load_dwordx4 v223, s[12:15], s63 offen offset:1024 lds\nv_rcp_f32 v234, v234\nM 132,168,232\nR 162,187,20480\n"
"s_add_u32 s63, s55, 2752512\nv_rcp_f32 v235, v235\nM 132,172,248\ns_add_u32 m0, s50, 81920\nv_rcp_f32 v236, v236\nv_rcp_f32 v237, v237\n"
"M 116,168,228\nR 164,187,24576\nG 9\nv_mfma_f32_16x16x32_bf16 v[96:99], v[238:241], v[208:211], v[96:99]\nv_mul_f32 v234, v36, v234\nv_mul_f32 v235, v37, v235\n"
"M 116,172,244\nbuffer_load_dwordx4 v222, s[12:15], s63 offen lds\nv_mul_f32 v236, v38, v236\nM 100,168,224\nR 166,187,28672\nG 9\n"
"v_mfma_f32_16x16x32_bf16 v[100:103], v[240:243], v[208:211], v[100:103]\nv_mul_f32 v237, v39, v237\nv_cvt_pk_bf16_f32 v34, v234, v235\nM 100,172,240\nG 4\nv_cvt_pk_bf16_f32 v35, v236, v237\n"
"v_mul_f32 v231, v41, v41\nv_mul_f32 v232, v42, v42\nM 104,176,224\nR 168,184,32768\nv_mfma_f32_16x16x32_bf16 v[104:107], v[242:245], v[208:211], v[104:107]\nv_mul_f32 v233, v43, v43\n"
"v_fma_f32 v230, v230, v215, v214\nM 104,180,240\nbuffer_load_dwordx4 v223, s[12:15], s63 offen offset:1024 lds\nv_fma_f32 v231, v231, v215, v214\nM 120,176,228\nR 170,184,36864\n"
"v_mfma_f32_16x16x32_bf16 v[108:111], v[244:247], v[208:211], v[108:111]\nv_fma_f32 v232, v232, v215, v214\nv_fma_f32 v233, v233, v215, v214\nM 120,180,244\ns_add_u32 m0, s50, 90112\nv_mul_f32 v230, v40, v230\n"
"v_mul_f32 v231, v41, v231\nv_mul_f32 v232, v42, v232\nM 136,176,232\nR 172,186,32768\nv_mfma_f32_16x16x32_bf16 v[112:115], v[244:247], v[210:213], v[112:115]\nv_mul_f32 v233, v43, v233\n"
"v_exp_f32 v230, v230\nM 136,180,248\ns_add_u32 s63, s55, 3211264\nv_exp_f32 v231, v231\nv_exp_f32 v232, v232\nM 152,176,236\n"
"R 174,186,36864\nv_mfma_f32_16x16x32_bf16 v[116:119], v[238:241], v[210:213], v[116:119]\nv_fma_f32 v230, v230, v216, v216\nv_exp_f32 v233, v233\nM 152,180,252\nG 4\n"
"v_rcp_f32 v230, v230\nv_fma_f32 v231, v231, v216, v216\nM 156,160,236\nR 176,185,32768\nv_mfma_f32_16x16x32_bf16 v[120:123], v[240:243], v[210:213], v[120:123]\nv_fma_f32 v232, v232, v216, v216\n"
"v_fma_f32 v233, v233, v216, v216\nM 156,164,252\nbuffer_load_dwordx4 v222, s[12:15], s63 offen lds\nv_rcp_f32 v231, v231\nM 140,160,232\nR 178,185,36864\n"
"v_mfma_f32_16x16x32_bf16 v[124:127], v[242:245], v[210:213], v[124:127]\nv_mul_f32 v230, v40, v230\nv_rcp_f32 v232, v232\nM 140,164,248\nbuffer_store_dwordx4 v[32:35], v252, s[20:23], s84 offen offset:0 sc1\nv_rcp_f32 v233, v233\n"
"M 124,160,228\nR 180,187,32768\nv_mul_f32 v231, v41, v231\nv_mul_f32 v232, v42, v232\nM 124,164,244\nbuffer_load_dwordx4 v223, s[12:15], s63 offen offset:1024 lds\n"
"v_mul_f32 v233, v43, v233\nM 108,160,224\nR 182,187,36864\nv_cvt_pk_bf16_f32 v40, v230, v231\nv_cvt_pk_bf16_f32 v41, v232, v233\nM 108,164,240\n"
"s_waitcnt vmcnt(12)\ns_barrier\nG 4\nv_mul_f32 v234, v44, v44\nv_mfma_f32_16x16x32_bf16 v[0:3], v[168:171], a[0:3], 0\nR 160,184,40960\n"
"v_mul_f32 v235, v45, v45\nv_mul_f32 v236, v46, v46\nM 0,172,16\ns_mov_b32 m0, s51\nv_mul_f32 v237, v47, v47\nv_fma_f32 v234, v234, v215, v214\n"
"v_fma_f32 v235, v235, v215, v214\nv_mfma_f32_16x16x32_bf16 v[16:19], v[168:171], a[4:7], 0\nR 162,184,45056\nv_fma_f32 v236, v236, v215, v214\nv_fma_f32 v237, v237, v215, v214\nM 16,172,20\n"
"v_mul_f32 v234, v44, v234\nv_mul_f32 v235, v45, v235\nv_mul_f32 v236, v46, v236\nv_mfma_f32_16x16x32_bf16 v[8:11], v[168:171], a[8:11], 0\nR 164,186,40960\nv_mul_f32 v237, v47, v237\n"
"v_exp_f32 v234, v234\nM 8,172,24\nds_write_addtid_b32 v128 offset:0\nv_exp_f32 v235, v235\nv_exp_f32 v236, v236\nLpg10:\n"
"s_cbranch_execz Lpg14\nv_mfma_f32_16x16x32_bf16 v[24:27], v[168:171], a[12:15], 0\nR 166,186,45056\nds_write_addtid_b32 v129 offset:256\nv_fma_f32 v234, v234, v216, v216\nv_exp_f32 v237, v237\n"
"M 24,172,28\nds_write_addtid_b32 v130 offset:512\nG 7\nv_rcp_f32 v234, v234\nv_fma_f32 v235, v235, v216, v216\nv_mfma_f32_16x16x32_bf16 v[28:31], v[176:179], a[12:15], 0\n"
"R 168,185,40960\nds_write_addtid_b32 v131 offset:768\nv_fma_f32 v236, v236, v216, v216\nv_fma_f32 v237, v237, v216, v216\nM 28,180,28\nds_write_addtid_b32 v132 offset:1024\n"
"v_rcp_f32 v235, v235\nv_rcp_f32 v236, v236\nv_mfma_f32_16x16x32_bf16 v[12:15], v[176:179], a[8:11], 0\nR 170,185,45056\nds_write_addtid_b32 v133 offset:1280\nv_mul_f32 v234, v44, v234\n"
"v_rcp_f32 v237, v237\nM 12,180,24\nds_write_addtid_b32 v134 offset:1536\nv_mul_f32 v235, v45, v235\nv_mul_f32 v236, v46, v236\nv_mul_f32 v237, v47, v237\n"
"v_mfma_f32_16x16x32_bf16 v[20:23], v[176:179], a[4:7], 0\nR 172,187,40960\nds_write_addtid_b32 v135 offset:1792\nv_cvt_pk_bf16_f32 v42, v234, v235\nv_cvt_pk_bf16_f32 v43, v236, v237\nM 20,180,20\n"
"buffer_store_dwordx4 v[40:43], v252, s[20:23], s84 offen offset:1024 sc1\nds_write_addtid_b32 v136 offset:2048\nv_mul_f32 v230, v48, v48\nv_mfma_f32_16x16x32_bf16 v[4:7], v[176:179], a[0:3], 0\nR 174,187,45056\nds_write_addtid_b32 v137 offset:2304\n"
"v_mul_f32 v231, v49, v49\nv_mul_f32 v232, v50, v50\nM 4,180,16\nds_write_addtid_b32 v138 offset:2560\nG 14\nv_mul_f32 v233, v51, v51\n"
"v_fma_f32 v230, v230, v218, v217\nv_fma_f32 v231, v231, v218, v217\nM 0,160,32\nR 176,184,49152\nds_write_addtid_b32 v139 offset:2816\nv_fma_f32 v232, v232, v218, v217\n"
"v_fma_f32 v233, v233, v218, v217\nM 0,164,48\nds_write_addtid_b32 v140 offset:3072\nv_mul_f32 v230, v48, v230\nv_mul_f32 v231, v49, v231\nv_mul_f32 v232, v50, v232\n"
"M 16,160,36\nR 178,184,53248\nds_write_addtid_b32 v141 offset:3328\nv_mul_f32 v233, v51, v233\nv_exp_f32 v230, v230\nM 16,164,52\n"
"ds_write_addtid_b32 v142 offset:3584\nv_exp_f32 v231, v231\nv_exp_f32 v232, v232\nM 8,160,40\nR 180,186,49152\nds_write_addtid_b32 v143 offset:3840\n"
"v_fma_f32 v230, v230, v219, v219\nv_exp_f32 v233, v233\nM 8,164,56\nds_write_addtid_b32 v144 offset:4096\nv_rcp_f32 v230, v230\nv_fma_f32 v231, v231, v219, v219\n"
"M 24,160,44\nR 182,186,53248\nds_write_addtid_b32 v145 offset:4352\nv_fma_f32 v232, v232, v219, v219\nv_fma_f32 v233, v233, v219, v219\nM 24,164,60\n"
"ds_write_addtid_b32 v146 offset:4608\nG 14\nv_rcp_f32 v231, v231\nv_rcp_f32 v232, v232\nM 28,168,44\nR 160,185,49152\n"
"ds_write_addtid_b32 v147 offset:4864\nv_mul_f32 v230, v48, v230\nv_rcp_f32 v233, v233\nM 28,172,60\nds_write_addtid_b32 v148 offset:5120\nds_write_addtid_b32 v149 offset:5376\n"
"ds_write_addtid_b32 v150 offset:5632\nds_write_addtid_b32 v151 offset:5888\nds_write_addtid_b32 v152 offset:6144\nds_write_addtid_b32 v153 offset:6400\nds_write_addtid_b32 v154 offset:6656\nds_write_addtid_b32 v155 offset:6912\n"
"ds_write_addtid_b32 v156 offset:7168\nds_write_addtid_b32 v157 offset:7424\nds_write_addtid_b32 v158 offset:7680\nds_write_addtid_b32 v159 offset:7936\nv_mul_f32 v231, v49, v231\nv_mul_f32 v232, v50, v232\n"
"v_mul_f32 v233, v51, v233\nM 12,168,40\nR 162,185,53248\ns_add_u32 m0, s50, 98304\ns_add_u32 s63, s54, 256\nv_cvt_pk_bf16_f32 v48, v230, v231\n"
"M 12,172,56\nbuffer_load_dwordx4 v220, s[12:15], s63 offen lds\nv_cvt_pk_bf16_f32 v49, v232, v233\nM 20,168,36\nR 164,187,49152\ns_waitcnt vmcnt(5) lgkmcnt(2)\n"
"s_barrier\nv_mul_f32 v234, v52, v52\nM 20,172,52\nds_read2st64_b32 v[128:129], v225 offset0:0 offset1:1\nv_mul_f32 v235, v53, v53\nv_mul_f32 v236, v54, v54\n"
"v_mul_f32 v237, v55, v55\nM 4,168,32\nR 166,187,53248\nds_read2st64_b32 v[130:131], v225 offset0:2 offset1:3\nv_fma_f32 v234, v234, v218, v217\nv_fma_f32 v235, v235, v218, v217\n"
"M 4,172,48\nds_read2st64_b32 v[132:133], v225 offset0:4 offset1:5\nv_fma_f32 v236, v236, v218, v217\nv_fma_f32 v237, v237, v218, v217\nv_mul_f32 v234, v52, v234\nM 0,176,64\n"
"R 168,184,57344\nds_read2st64_b32 v[134:135], v225 offset0:6 offset1:7\nv_mul_f32 v235, v53, v235\nv_mul_f32 v236, v54, v236\nM 0,180,80\nbuffer_load_dwordx4 v221, s[12:15], s63 offen offset:1024 lds\n"
"ds_read2st64_b32 v[136:137], v225 offset0:8 offset1:9\nv_mul_f32 v237, v55, v237\nM 16,176,68\nR 170,184,61440\ns_add_u32 m0, s50, 106496\ns_add_u32 s63, s54, 229632\n"
"ds_read2st64_b32 v[138:139], v225 offset0:10 offset1:11\nv_exp_f32 v234, v234\nM 16,180,84\nbuffer_load_dwordx4 v220, s[12:15], s63 offen lds\nds_read2st64_b32 v[140:141], v225 offset0:12 offset1:13\nv_exp_f32 v235, v235\n"
"M 8,176,72\nR 172,186,57344\nds_read2st64_b32 v[142:143], v225 offset0:14 offset1:15\nv_fma_f32 v234, v234, v219, v219\nv_exp_f32 v236, v236\nM 8,180,88\n"
"ds_read2st64_b32 v[144:145], v225 offset0:16 offset1:17\nv_exp_f32 v237, v237\nv_fma_f32 v235, v235, v219, v219\nM 24,176,76\nR 174,186,61440\nds_read2st64_b32 v[146:147], v225 offset0:18 offset1:19\n"
"v_fma_f32 v236, v236, v219, v219\nv_fma_f32 v237, v237, v219, v219\nM 24,180,92\nds_read2st64_b32 v[148:149], v225 offset0:20 offset1:21\nG 14\nv_rcp_f32 v234, v234\n"
"v_rcp_f32 v235, v235\nM 28,160,76\nR 176,185,57344\nds_read2st64_b32 v[150:151], v225 offset0:22 offset1:23\nv_mul_f32 v234, v52, v234\nv_rcp_f32 v236, v236\n"
"M 28,164,92\nbuffer_load_dwordx4 v221, s[12:15], s63 offen offset:1024 lds\nds_read2st64_b32 v[152:153], v225 offset0:24 offset1:25\nv_rcp_f32 v237, v237\nM 12,160,72\nR 178,185,61440\n"
"s_add_u32 s63, s54, 459008\nds_read2st64_b32 v[154:155], v225 offset0:26 offset1:27\nv_mul_f32 v235, v53, v235\nM 12,164,88\ns_add_u32 m0, s50, 114688\nds_read2st64_b32 v[156:157], v225 offset0:28 offset1:29\n"
"v_mul_f32 v236, v54, v236\nv_mul_f32 v237, v55, v237\nv_cvt_pk_bf16_f32 v50, v234, v235\nM 20,160,68\nR 180,187,57344\nds_read2st64_b32 v[158:159], v225 offset0:30 offset1:31\n"
"v_cvt_pk_bf16_f32 v51, v236, v237\nv_mul_f32 v230, v56, v56\nM 20,164,84\nbuffer_load_dwordx4 v220, s[12:15], s63 offen lds\nv_add_f32 v96, v96, v128\nv_mul_f32 v231, v57, v57\n"
"M 4,160,64\nR 182,187,61440\nv_mul_f32 v232, v58, v58\nv_add_f32 v97, v97, v129\nv_mul_f32 v233, v59, v59\nM 4,164,80\n"
"G 11\nv_fma_f32 v230, v230, v218, v217\nv_fma_f32 v231, v231, v218, v217\nv_add_f32 v98, v98, v130\nv_fma_f32 v232, v232, v218, v217\nM 0,168,96\n"
"R 160,188,0\nv_fma_f32 v233, v233, v218, v217\nv_add_f32 v99, v99, v131\nv_mul_f32 v230, v56, v230\nM 0,172,112\nbuffer_load_dwordx4 v221, s[12:15], s63 offen offset:1024 lds\n"
"v_add_f32 v100, v100, v132\nv_mul_f32 v231, v57, v231\nM 16,168,100\nR 162,188,4096\nv_mul_f32 v232, v58, v232\nv_add_f32 v101, v101, v133\n"
"v_mul_f32 v233, v59, v233\nM 16,172,116\ns_add_u32 m0, s50, 0\nv_exp_f32 v230, v230\nv_add_f32 v102, v102, v134\nv_exp_f32 v231, v231\n"
"M 8,168,104\nR 164,190,0\nv_fma_f32 v230, v230, v219, v219\nv_add_f32 v103, v103, v135\nv_exp_f32 v232, v232\nM 8,172,120\n"
"s_add_u32 s63, s54, 688384\nv_exp_f32 v233, v233\nv_add_f32 v104, v104, v136\nv_fma_f32 v231, v231, v219, v219\nM 24,168,108\nR 166,190,4096\n"
"v_fma_f32 v232, v232, v219, v219\nv_add_f32 v105, v105, v137\nv_fma_f32 v233, v233, v219, v219\nM 24,172,124\nG 4\nv_rcp_f32 v230, v230\n"
"v_add_f32 v106, v106, v138\nv_rcp_f32 v231, v231\nM 28,176,108\nR 168,189,0\nv_mul_f32 v230, v56, v230\nv_add_f32 v107, v107, v139\n"
"v_rcp_f32 v232, v232\nM 28,180,124\nbuffer_load_dwordx4 v220, s[12:15], s63 offen lds\nv_add_f32 v108, v108, v140\nv_rcp_f32 v233, v233\nM 12,176,104\n"
"R 170,189,4096\nv_mul_f32 v231, v57, v231\nv_add_f32 v109, v109, v141\nv_mul_f32 v232, v58, v232\nM 12,180,120\nbuffer_store_dwordx4 v[48:51], v252, s[20:23], s85 offen offset:0 sc1\n"
"v_add_f32 v110, v110, v142\nv_mul_f32 v233, v59, v233\nM 20,176,100\nR 172,191,0\nv_cvt_pk_bf16_f32 v56, v230, v231\nv_add_f32 v111, v111, v143\n"
"v_cvt_pk_bf16_f32 v57, v232, v233\nM 20,180,116\nbuffer_load_dwordx4 v221, s[12:15], s63 offen offset:1024 lds\nv_add_f32 v112, v112, v144\nv_add_f32 v113, v113, v145\nv_add_f32 v114, v114, v146\n"
"v_add_f32 v115, v115, v147\nv_add_f32 v116, v116, v148\nv_add_f32 v117, v117, v149\nv_add_f32 v118, v118, v150\nv_add_f32 v119, v119, v151\nv_add_f32 v120, v120, v152\n"
"v_add_f32 v121, v121, v153\nv_add_f32 v122, v122, v154\nv_add_f32 v123, v123, v155\nv_add_f32 v124, v124, v156\nv_add_f32 v125, v125, v157\nv_add_f32 v126, v126, v158\n"
"v_add_f32 v127, v127, v159\nv_mul_f32 v234, v60, v60\nM 4,176,96\nR 174,191,4096\ns_add_u32 m0, s50, 8192\nv_mul_f32 v235, v61, v61\n"
"M 4,180,112\nG 4\nv_mul_f32 v236, v62, v62\nv_mul_f32 v237, v63, v63\nv_fma_f32 v234, v234, v218, v217\nM 0,160,128\n"
"R 176,188,8192\ns_add_u32 s63, s54, 917760\nv_fma_f32 v235, v235, v218, v217\nM 0,164,144\nbuffer_load_dwordx4 v220, s[12:15], s63 offen lds\nv_fma_f32 v236, v236, v218, v217\n"
"M 16,160,132\nR 178,188,12288\nv_fma_f32 v237, v237, v218, v217\nv_mul_f32 v234, v60, v234\nM 16,164,148\nv_exp_f32 v234, v234\n"
"v_mul_f32 v235, v61, v235\nM 8,160,136\nR 180,190,8192\nLq583:\ns_lshr_b32 s60, s45, 3\ns_lshl2_add_u32 s60, s60, s40\n"
"s_lshl_b32 s60, s60, 8\nv_lshl_add_u32 v224, v252, 3, s60\nv_add_u32 v224, Lq583+4096-LqPC, v224\nv_min_u32 v224, LqE-LqPC-4, v224\ns_mov_b64 exec, 3\nglobal_load_dword v224, v224, s[98:99]\n"
"s_mov_b64 exec, -1\nv_mul_f32 v236, v62, v236\nM 8,164,152\nv_exp_f32 v235, v235\nv_mul_f32 v237, v63, v237\nM 24,160,140\n"
"R 182,190,12288\nv_fma_f32 v234, v234, v219, v219\nv_exp_f32 v236, v236\nM 24,164,156\nG 4\nv_exp_f32 v237, v237\n"
"v_fma_f32 v235, v235, v219, v219\nM 28,168,140\nR 160,189,8192\nv_fma_f32 v236, v236, v219, v219\nv_fma_f32 v237, v237, v219, v219\nM 28,172,156\n"
"buffer_load_dwordx4 v221, s[12:15], s63 offen offset:1024 lds\nv_rcp_f32 v234, v234\nM 12,168,136\nR 162,189,12288\ns_add_u32 m0, s50, 16384\ns_add_u32 s63, s54, 1147136\n"
"v_rcp_f32 v235, v235\nM 12,172,152\nbuffer_load_dwordx4 v220, s[12:15], s63 offen lds\nM 20,168,132\nR 164,191,8192\nv_mul_f32 v234, v60, v234\n"
"M 20,172,148\nv_rcp_f32 v236, v236\nM 4,168,128\nR 166,191,12288\nv_mul_f32 v235, v61, v235\nM 4,172,144\n"
"G 4\nv_rcp_f32 v237, v237\nM 0,176,160\nR 168,188,16384\nv_mul_f32 v236, v62, v236\nM 0,180,176\n"
"buffer_load_dwordx4 v221, s[12:15], s63 offen offset:1024 lds\nM 16,176,164\nR 170,188,20480\ns_add_u32 m0, s50, 24576\ns_add_u32 s63, s54, 1376512\nM 16,180,180\n"
"buffer_load_dwordx4 v220, s[12:15], s63 offen lds\nM 8,176,168\nR 172,190,16384\nv_mul_f32 v237, v63, v237\nM 8,180,184\nv_cvt_pk_bf16_f32 v58, v234, v235\n"
"v_cvt_pk_bf16_f32 v59, v236, v237\nM 24,176,172\nR 174,190,20480\nM 24,180,188\nbuffer_store_dwordx4 v[56:59], v252, s[20:23], s85 offen offset:1024 sc1\nG 4\n"
"M 28,160,172\nR 176,189,16384\ns_waitcnt vmcnt(12)\ns_barrier\nv_mul_f32 v230, v96, v96\nM 28,164,188\n"
"buffer_load_dwordx4 v221, s[12:15], s63 offen offset:1024 lds\nv_mul_f32 v231, v97, v97\nM 12,160,168\nR 178,189,20480\ns_add_u32 m0, s50, 32768\ns_add_u32 s63, s54, 1605888\n"
"v_mul_f32 v232, v98, v98\nM 12,164,184\nbuffer_load_dwordx4 v220, s[12:15], s63 offen lds\nv_mul_f32 v233, v99, v99\nM 20,160,164\nR 180,191,16384\n"
"v_fma_f32 v230, v230, v215, v214\nv_fma_f32 v231, v231, v215, v214\nM 20,164,180\ns_add_u32 s84, s73, 23070720\ns_add_u32 s85, s73, 23185408\nv_fma_f32 v232, v232, v215, v214\n"
"v_fma_f32 v233, v233, v215, v214\nM 4,160,160\nR 182,191,20480\ns_mov_b32 s60, 158720\nv_bfe_u32 v242, v252, 4, 4\nv_lshl_add_u32 v242, v242, 3, s60\n"
"v_mul_f32 v230, v96, v230\nM 4,164,176\nds_read_b64 v[238:239], v242 offset:0\nG 5\nv_mul_f32 v231, v97, v231\nM 0,168,192\n"
"R 160,188,24576\nds_read_b64 v[240:241], v242 offset:128\nv_mul_f32 v232, v98, v232\nM 0,172,208\nds_read_b64 v[242:243], v242 offset:0\nv_mul_f32 v233, v99, v233\n"
"v_exp_f32 v230, v230\nM 16,168,196\nR 162,188,28672\nv_fma_f32 v230, v230, v216, v216\nv_exp_f32 v231, v231\nM 16,172,212\n"
"v_exp_f32 v232, v232\nv_exp_f32 v233, v233\nM 8,168,200\nR 164,190,24576\nv_fma_f32 v231, v231, v216, v216\nv_fma_f32 v232, v232, v216, v216\n"
"M 8,172,216\nv_rcp_f32 v230, v230\nv_fma_f32 v233, v233, v216, v216\nM 24,168,204\nR 166,190,28672\nv_mul_f32 v230, v96, v230\n"
"v_rcp_f32 v231, v231\nM 24,172,220\nG 7\nv_rcp_f32 v232, v232\nv_rcp_f32 v233, v233\nM 28,176,204\n"
"R 168,189,24576\nv_mul_f32 v231, v97, v231\nv_mul_f32 v232, v98, v232\nM 28,180,220\nv_mul_f32 v233, v99, v233\nv_cvt_pk_bf16_f32 v96, v230, v231\n"
"v_cvt_pk_bf16_f32 v97, v232, v233\nM 12,176,200\nR 170,189,28672\nv_mul_f32 v234, v100, v100\nv_mul_f32 v235, v101, v101\nM 12,180,216\n"
"v_mul_f32 v236, v102, v102\nv_mul_f32 v237, v103, v103\nv_fma_f32 v234, v234, v215, v214\nM 20,176,196\nR 172,191,24576\nG 7\n"
"v_mfma_f32_16x16x32_bf16 v[0:3], v[238:241], v[208:211], v[0:3]\nv_fma_f32 v235, v235, v215, v214\nv_fma_f32 v236, v236, v215, v214\nM 20,180,212\nv_fma_f32 v237, v237, v215, v214\nv_mul_f32 v234, v100, v234\n"
"v_mul_f32 v235, v101, v235\nM 4,176,192\nR 174,191,28672\nG 7\nv_mfma_f32_16x16x32_bf16 v[4:7], v[240:243], v[208:211], v[4:7]\nv_mul_f32 v236, v102, v236\n"
"v_mul_f32 v237, v103, v237\nM 4,180,208\nG 4\nv_exp_f32 v234, v234\nv_exp_f32 v235, v235\nM 0,160,224\n"
"R 176,188,32768\nv_mfma_f32_16x16x32_bf16 v[16:19], v[240:243], v[210:213], v[16:19]\nv_fma_f32 v234, v234, v216, v216\nv_exp_f32 v236, v236\nM 0,164,240\nv_exp_f32 v237, v237\n"
"v_fma_f32 v235, v235, v216, v216\nM 16,160,228\nR 178,188,36864\nv_mfma_f32_16x16x32_bf16 v[20:23], v[238:241], v[210:213], v[20:23]\nv_fma_f32 v236, v236, v216, v216\nv_fma_f32 v237, v237, v216, v216\n"
"M 16,164,244\nv_rcp_f32 v234, v234\nv_rcp_f32 v235, v235\nM 8,160,232\nR 180,188,40960\nv_mul_f32 v234, v100, v234\n"
"v_rcp_f32 v236, v236\nM 8,164,248\nv_rcp_f32 v237, v237\nv_mul_f32 v235, v101, v235\nM 24,160,236\nR 182,188,45056\n"
"v_mul_f32 v236, v102, v236\nv_mul_f32 v237, v103, v237\nM 24,164,252\nG 4\nv_cvt_pk_bf16_f32 v98, v234, v235\nv_cvt_pk_bf16_f32 v99, v236, v237\n"
"v_mul_f32 v230, v104, v104\nM 28,168,236\nR 160,189,32768\nv_mul_f32 v231, v105, v105\nv_mul_f32 v232, v106, v106\nM 28,172,252\n"
"v_mul_f32 v233, v107, v107\nv_fma_f32 v230, v230, v215, v214\nv_fma_f32 v231, v231, v215, v214\nM 12,168,232\nR 162,189,36864\nv_fma_f32 v232, v232, v215, v214\n"
"v_fma_f32 v233, v233, v215, v214\nM 12,172,248\nv_mul_f32 v230, v104, v230\nv_mul_f32 v231, v105, v231\nv_mul_f32 v232, v106, v232\nM 20,168,228\n"
"R 164,189,40960\nv_mul_f32 v233, v107, v233\nv_exp_f32 v230, v230\nM 20,172,244\nv_exp_f32 v231, v231\nv_exp_f32 v232, v232\n"
"M 4,168,224\nR 166,189,45056\nv_fma_f32 v230, v230, v216, v216\nv_exp_f32 v233, v233\nM 4,172,240\ns_waitcnt vmcnt(12)\n"
"s_barrier\nG 4\nv_fma_f32 v231, v231, v216, v216\nv_mfma_f32_16x16x32_bf16 v[32:35], v[176:179], a[0:3], 0\nR 168,190,32768\nv_fma_f32 v232, v232, v216, v216\n"
"v_fma_f32 v233, v233, v216, v216\nM 32,180,16\ns_mov_b32 m0, s51\nv_rcp_f32 v230, v230\nv_rcp_f32 v231, v231\nv_mfma_f32_16x16x32_bf16 v[48:51], v[176:179], a[4:7], 0\n"
"R 170,190,36864\nds_write_addtid_b32 v8 offset:0\nv_mul_f32 v230, v104, v230\nv_rcp_f32 v232, v232\nM 48,180,20\nds_write_addtid_b32 v9 offset:256\n"
"v_rcp_f32 v233, v233\nv_mul_f32 v231, v105, v231\nv_mfma_f32_16x16x32_bf16 v[64:67], v[176:179], a[8:11], 0\nR 172,190,40960\nds_write_addtid_b32 v10 offset:512\nv_mov_b32 v226, s46\n"
"v_mul_f32 v232, v106, v232\nM 64,180,24\nds_write_addtid_b32 v11 offset:768\nv_mov_b32 v227, s47\nv_mul_f32 v233, v107, v233\nv_cvt_pk_bf16_f32 v104, v230, v231\n"
"v_mfma_f32_16x16x32_bf16 v[80:83], v[176:179], a[12:15], 0\nR 174,190,45056\ns_add_u32 s61, s81, 385024\ns_lshl_b32 s62, s44, 5\ns_add_u32 s61, s61, s62\ns_waitcnt vmcnt(0)\n"
"s_mov_b64 exec, 1\nbuffer_atomic_swap_x2 v[226:227], off, s[20:23], s61 sc1\ns_mov_b64 exec, -1\nds_write_addtid_b32 v12 offset:1024\nv_cvt_pk_bf16_f32 v105, v232, v233\nM 80,180,28\n"
"ds_write_addtid_b32 v13 offset:1280\nG 10\nv_mul_f32 v234, v108, v108\nv_mul_f32 v235, v109, v109\nv_mul_f32 v236, v110, v110\nv_mfma_f32_16x16x32_bf16 v[84:87], v[160:163], a[12:15], 0\n"
"R 176,191,32768\nds_write_addtid_b32 v14 offset:1536\nv_mul_f32 v237, v111, v111\nv_fma_f32 v234, v234, v215, v214\nM 84,164,28\nds_write_addtid_b32 v15 offset:1792\n"
"v_fma_f32 v235, v235, v215, v214\nv_fma_f32 v236, v236, v215, v214\nv_fma_f32 v237, v237, v215, v214\nv_mfma_f32_16x16x32_bf16 v[68:71], v[160:163], a[8:11], 0\nR 178,191,36864\nds_write_addtid_b32 v24 offset:4096\n"
"v_mul_f32 v234, v108, v234\nv_mul_f32 v235, v109, v235\nM 68,164,24\nbuffer_store_dwordx4 v[96:99], v252, s[20:23], s84 offen offset:0 sc1\nds_write_addtid_b32 v25 offset:4352\nv_mul_f32 v236, v110, v236\n"
"v_mfma_f32_16x16x32_bf16 v[52:55], v[160:163], a[4:7], 0\nR 180,191,40960\nds_write_addtid_b32 v26 offset:4608\nv_mul_f32 v237, v111, v237\nv_exp_f32 v234, v234\nM 52,164,20\n"
"ds_write_addtid_b32 v27 offset:4864\nds_write_addtid_b32 v28 offset:5120\nds_write_addtid_b32 v29 offset:5376\nds_write_addtid_b32 v30 offset:5632\nLpg11:\ns_cbranch_execz Lpg15\n"
"ds_write_addtid_b32 v31 offset:5888\nv_exp_f32 v235, v235\nv_exp_f32 v236, v236\nv_mfma_f32_16x16x32_bf16 v[36:39], v[160:163], a[0:3], 0\nR 182,191,45056\ns_add_u32 m0, s50, 32768\n"
"v_exp_f32 v237, v237\nM 36,164,16\nG 15\nv_fma_f32 v234, v234, v216, v216\nv_fma_f32 v235, v235, v216, v216\nv_fma_f32 v236, v236, v216, v216\n"
"v_mfma_f32_16x16x32_bf16 v[40:43], v[168:171], a[0:3], 0\nR 160,188,49152\nG 2\ns_barrier\nv_fma_f32 v237, v237, v216, v216\nM 40,172,16\n"
"buffer_load_dwordx4 v221, s[12:15], s63 offen offset:1024 lds\nds_read2st64_b32 v[8:9], v225 offset0:0 offset1:1\nv_rcp_f32 v234, v234\nv_mfma_f32_16x16x32_bf16 v[56:59], v[168:171], a[4:7], 0\nR 162,188,53248\ns_add_u32 m0, s50, 40960\n"
"s_add_u32 s63, s54, 1835264\nds_read2st64_b32 v[10:11], v225 offset0:2 offset1:3\nv_rcp_f32 v235, v235\nM 56,172,20\nbuffer_load_dwordx4 v220, s[12:15], s63 offen lds\nds_read2st64_b32 v[12:13], v225 offset0:4 offset1:5\n"
"v_rcp_f32 v236, v236\nv_mfma_f32_16x16x32_bf16 v[72:75], v[168:171], a[8:11], 0\nR 164,184,0\nds_read2st64_b32 v[14:15], v225 offset0:6 offset1:7\nv_mul_f32 v234, v108, v234\nv_rcp_f32 v237, v237\n"
"M 72,172,24\nds_read2st64_b32 v[24:25], v225 offset0:16 offset1:17\nv_mul_f32 v235, v109, v235\nv_mul_f32 v236, v110, v236\nv_mul_f32 v237, v111, v237\nv_mfma_f32_16x16x32_bf16 v[88:91], v[168:171], a[12:15], 0\n"
"R 166,184,4096\nds_read2st64_b32 v[26:27], v225 offset0:18 offset1:19\nv_cvt_pk_bf16_f32 v106, v234, v235\nv_cvt_pk_bf16_f32 v107, v236, v237\nM 88,172,28\nds_read2st64_b32 v[28:29], v225 offset0:20 offset1:21\n"
"G 11\nv_mul_f32 v230, v112, v112\nv_mul_f32 v231, v113, v113\nv_mul_f32 v232, v114, v114\nv_mfma_f32_16x16x32_bf16 v[92:95], v[176:179], a[12:15], 0\nR 168,189,49152\n"
"ds_read2st64_b32 v[30:31], v225 offset0:22 offset1:23\nv_mul_f32 v233, v115, v115\nv_fma_f32 v230, v230, v218, v217\nM 92,180,28\nbuffer_load_dwordx4 v221, s[12:15], s63 offen offset:1024 lds\nG 11\n"
"v_add_f32 v0, v0, v8\nv_fma_f32 v231, v231, v218, v217\nv_mfma_f32_16x16x32_bf16 v[76:79], v[176:179], a[8:11], 0\nR 170,189,53248\ns_add_u32 m0, s50, 49152\ns_add_u32 s63, s54, 2064640\n"
"v_add_f32 v1, v1, v9\nv_fma_f32 v232, v232, v218, v217\nM 76,180,24\nbuffer_load_dwordx4 v220, s[12:15], s63 offen lds\nG 10\nv_add_f32 v2, v2, v10\n"
"v_fma_f32 v233, v233, v218, v217\nv_mfma_f32_16x16x32_bf16 v[60:63], v[176:179], a[4:7], 0\nR 172,185,0\nv_mul_f32 v230, v112, v230\nv_add_f32 v3, v3, v11\nv_mul_f32 v231, v113, v231\n"
"M 60,180,20\nbuffer_store_dwordx4 v[104:107], v252, s[20:23], s84 offen offset:1024 sc1\nG 10\nv_add_f32 v4, v4, v12\nv_mul_f32 v232, v114, v232\nv_mfma_f32_16x16x32_bf16 v[44:47], v[176:179], a[0:3], 0\n"
"R 174,185,4096\nv_mul_f32 v233, v115, v233\nv_add_f32 v5, v5, v13\nv_exp_f32 v230, v230\nM 44,180,16\nG 7\n"
"v_exp_f32 v231, v231\nv_add_f32 v6, v6, v14\nv_exp_f32 v232, v232\nM 32,160,32\nR 176,190,49152\nv_mov_b32 v248, 0\n"
"v_add_f32 v7, v7, v15\nv_exp_f32 v233, v233\nM 32,164,48\nbuffer_load_dwordx4 v221, s[12:15], s63 offen offset:1024 lds\nv_mov_b32 v249, 0\nv_add_f32 v16, v16, v24\n"
"v_fma_f32 v230, v230, v219, v219\nM 48,160,36\nR 178,190,53248\ns_add_u32 s61, s82, 385024\ns_mov_b64 exec, 0xf\nbuffer_atomic_or_x2 v[248:249], v250, s[20:23], s61 offen sc0 sc1\n"
"s_mov_b64 exec, -1\nv_add_f32 v17, v17, v25\nv_fma_f32 v231, v231, v219, v219\nM 48,164,52\nv_rcp_f32 v230, v230\nG 8\n"
"v_add_f32 v18, v18, v26\nv_fma_f32 v232, v232, v219, v219\nM 64,160,40\nR 180,186,0\nv_fma_f32 v233, v233, v219, v219\nv_add_f32 v19, v19, v27\n"
"G 8\nv_add_f32 v20, v20, v28\nv_add_f32 v21, v21, v29\nG 6\nv_add_f32 v22, v22, v30\nv_add_f32 v23, v23, v31\n"
"v_rcp_f32 v231, v231\nM 64,164,56\ns_add_u32 m0, s50, 57344\nv_rcp_f32 v232, v232\nv_rcp_f32 v233, v233\nM 80,160,44\n"
"R 182,186,4096\nv_mul_f32 v230, v112, v230\nv_mul_f32 v231, v113, v231\nM 80,164,60\nG 4\nv_mul_f32 v232, v114, v232\n"
"v_mul_f32 v233, v115, v233\nv_cvt_pk_bf16_f32 v112, v230, v231\nM 84,168,44\nR 160,191,49152\nv_cvt_pk_bf16_f32 v113, v232, v233\nM 84,172,60\n"
"s_add_u32 s63, s54, 2294016\nv_mul_f32 v234, v116, v116\nv_mul_f32 v235, v117, v117\nM 68,168,40\nR 162,191,53248\nv_mul_f32 v236, v118, v118\n"
"M 68,172,56\nbuffer_load_dwordx4 v220, s[12:15], s63 offen lds\nM 52,168,36\nR 164,187,0\nv_mul_f32 v237, v119, v119\nM 52,172,52\n"
"v_fma_f32 v234, v234, v218, v217\nv_fma_f32 v235, v235, v218, v217\nM 36,168,32\nR 166,187,4096\nv_fma_f32 v236, v236, v218, v217\nM 36,172,48\n"
"G 4\nv_fma_f32 v237, v237, v218, v217\nv_mul_f32 v234, v116, v234\nM 40,176,32\nR 168,184,8192\nv_mul_f32 v235, v117, v235\n"
"M 40,180,48\nbuffer_load_dwordx4 v221, s[12:15], s63 offen offset:1024 lds\nM 56,176,36\nR 170,184,12288\ns_add_u32 s63, s54, 2523392\nM 56,180,52\n"
"s_add_u32 m0, s50, 65536\nv_exp_f32 v234, v234\nM 72,176,40\nR 172,184,16384\nv_mul_f32 v236, v118, v236\nM 72,180,56\n"
"buffer_load_dwordx4 v220, s[12:15], s63 offen lds\nM 88,176,44\nR 174,184,20480\nv_mul_f32 v237, v119, v237\nM 88,180,60\nG 4\n"
"v_exp_f32 v235, v235\nM 92,160,44\nR 176,185,8192\nv_fma_f32 v234, v234, v219, v219\nM 92,164,60\nbuffer_load_dwordx4 v221, s[12:15], s63 offen offset:1024 lds\n"
"M 76,160,40\nR 178,185,12288\ns_add_u32 s63, s54, 2752768\nM 76,164,56\ns_add_u32 m0, s50, 73728\nv_exp_f32 v236, v236\n"
"M 60,160,36\nR 180,185,16384\nv_fma_f32 v235, v235, v219, v219\nM 60,164,52\nbuffer_load_dwordx4 v220, s[12:15], s63 offen lds\nM 44,160,32\n"
"R 182,185,20480\nv_fma_f32 v236, v236, v219, v219\nM 44,164,48\nG 4\nv_exp_f32 v237, v237\nM 32,168,64\n"
"R 160,186,8192\nv_fma_f32 v237, v237, v219, v219\nM 32,172,80\nbuffer_load_dwordx4 v221, s[12:15], s63 offen offset:1024 lds\nM 48,168,68\nR 162,186,12288\n"
"v_mul_f32 v230, v120, v120\nM 48,172,84\ns_add_u32 m0, s50, 81920\nv_rcp_f32 v234, v234\nM 64,168,72\nR 164,186,16384\n"
"v_mul_f32 v234, v116, v234\nM 64,172,88\ns_add_u32 s63, s54, 2982144\nv_rcp_f32 v235, v235\nM 80,168,76\nR 166,186,20480\n"
"v_mul_f32 v235, v117, v235\nM 80,172,92\nG 4\nv_rcp_f32 v236, v236\nM 84,176,76\nR 168,187,8192\n"
"v_mul_f32 v236, v118, v236\nM 84,180,92\nbuffer_load_dwordx4 v220, s[12:15], s63 offen lds\nM 68,176,72\nR 170,187,12288\nv_cvt_pk_bf16_f32 v114, v234, v235\n"
"M 68,180,88\nv_rcp_f32 v237, v237\nM 52,176,68\nR 172,187,16384\nv_mul_f32 v237, v119, v237\nM 52,180,84\n"
"s_add_u32 s64, s68, 23068672\nv_cvt_pk_bf16_f32 v115, v236, v237\nv_mul_f32 v231, v121, v121\nM 36,176,64\nR 174,187,20480\nv_mul_f32 v232, v122, v122\n"
"M 36,180,80\nG 4\nv_mul_f32 v233, v123, v123\nv_fma_f32 v230, v230, v218, v217\nM 40,160,64\nR 176,184,24576\n"
"s_waitcnt vmcnt(8)\ns_barrier\ns_waitcnt vmcnt(7)\nv_cmp_eq_u64 s[86:87], s[46:47], v[248:249]\nM 40,164,80\ns_add_u32 s65, s68, 23183360\n"
"v_fma_f32 v231, v231, v218, v217\nv_fma_f32 v232, v232, v218, v217\nM 56,160,68\nR 178,184,28672\ns_and_b32 s62, s86, 0xf\ns_cmp_eq_u32 s62, 0xf\n"
"s_cbranch_scc0 Lq385\nLq386:\nM 56,164,84\nbuffer_load_dwordx4 v221, s[12:15], s63 offen offset:1024 lds\nM 72,160,72\nR 180,184,32768\n"
"v_fma_f32 v233, v233, v218, v217\nM 72,164,88\nbuffer_load_dwordx4 a[0:3], v252, s[20:23], s64 offen sc1\nM 88,160,76\nR 182,184,36864\nv_mul_f32 v230, v120, v230\n"
"M 88,164,92\nG 4\nv_exp_f32 v230, v230\nM 92,168,76\nR 160,185,24576\nv_mul_f32 v231, v121, v231\n"
"M 92,172,92\nbuffer_load_dwordx4 a[4:7], v252, s[20:23], s65 offen sc1\nM 76,168,72\nR 162,185,28672\nv_mul_f32 v232, v122, v232\nM 76,172,88\n"
"s_add_u32 s66, s69, 23068672\nv_exp_f32 v231, v231\nM 60,168,68\nR 164,185,32768\nv_mul_f32 v233, v123, v233\nM 60,172,84\n"
"s_add_u32 s67, s69, 23183360\nv_exp_f32 v232, v232\nM 44,168,64\nR 166,185,36864\nv_fma_f32 v230, v230, v219, v219\nM 44,172,80\n"
"G 4\nv_exp_f32 v233, v233\nM 32,176,96\nR 168,186,24576\nv_fma_f32 v231, v231, v219, v219\nM 32,180,112\n"
"buffer_load_dwordx4 a[8:11], v252, s[20:23], s66 offen sc1\nM 48,176,100\nR 170,186,28672\nv_fma_f32 v232, v232, v219, v219\nM 48,180,116\nbuffer_load_dwordx4 a[12:15], v252, s[20:23], s67 offen sc1\n"
"M 64,176,104\nR 172,186,32768\nv_fma_f32 v233, v233, v219, v219\nM 64,180,120\ns_add_u32 s64, s68, 23075840\nv_rcp_f32 v230, v230\n"
"M 80,176,108\nR 174,186,36864\nv_mul_f32 v230, v120, v230\nM 80,180,124\nG 4\nv_rcp_f32 v231, v231\n"
"M 84,160,108\nR 176,187,24576\nv_mul_f32 v231, v121, v231\nM 84,164,124\nbuffer_load_dwordx4 a[16:19], v252, s[20:23], s64 offen sc1\nM 68,160,104\n"
"R 178,187,28672\nv_cvt_pk_bf16_f32 v120, v230, v231\nM 68,164,120\ns_add_u32 s65, s68, 23190528\nv_rcp_f32 v232, v232\nM 52,160,100\n"
"R 180,187,32768\nv_mul_f32 v232, v122, v232\nM 52,164,116\ns_add_u32 s66, s69, 23075840\nv_rcp_f32 v233, v233\nM 36,160,96\n"
"R 182,187,36864\nv_mul_f32 v233, v123, v233\nM 36,164,112\nG 4\nv_cvt_pk_bf16_f32 v121, v232, v233\nv_mul_f32 v234, v124, v124\n"
"M 40,168,96\nR 160,184,40960\nv_mul_f32 v235, v125, v125\nM 40,172,112\nbuffer_load_dwordx4 a[20:23], v252, s[20:23], s65 offen sc1\nM 56,168,100\n"
"R 162,184,45056\nv_mul_f32 v236, v126, v126\nM 56,172,116\nbuffer_load_dwordx4 a[24:27], v252, s[20:23], s66 offen sc1\nM 72,168,104\nR 164,184,49152\n"
"v_mul_f32 v237, v127, v127\nM 72,172,120\ns_add_u32 s67, s69, 23190528\nv_fma_f32 v234, v234, v218, v217\nv_fma_f32 v235, v235, v218, v217\nM 88,168,108\n"
"R 166,184,53248\nv_fma_f32 v236, v236, v218, v217\nM 88,172,124\nG 4\nv_fma_f32 v237, v237, v218, v217\nv_mul_f32 v234, v124, v234\n"
"M 92,176,108\nR 168,185,40960\ns_add_u32 m0, s50, 90112\nM 92,180,124\nbuffer_load_dwordx4 a[28:31], v252, s[20:23], s67 offen sc1\nM 76,176,104\n"
"R 170,185,45056\ns_add_u32 s63, s54, 3211520\nM 76,180,120\ns_add_u32 s64, s68, 23069696\nv_exp_f32 v234, v234\nM 60,176,100\n"
"R 172,185,49152\nv_mul_f32 v235, v125, v235\nM 60,180,116\ns_add_u32 s65, s68, 23184384\nbuffer_load_dwordx4 v220, s[12:15], s63 offen lds\nM 44,176,96\n"
"R 174,185,53248\nv_mul_f32 v236, v126, v236\nM 44,180,112\nG 4\nv_exp_f32 v235, v235\nM 32,160,128\n"
"R 176,186,40960\nv_mul_f32 v237, v127, v237\nM 32,164,144\nbuffer_load_dwordx4 a[32:35], v252, s[20:23], s64 offen sc1\nbuffer_load_dwordx4 v221, s[12:15], s63 offen offset:1024 lds\nM 48,160,132\n"
"R 178,186,45056\ns_add_u32 m0, s50, 98304\nM 48,164,148\nbuffer_load_dwordx4 a[36:39], v252, s[20:23], s65 offen sc1\nM 64,160,136\nR 180,186,49152\n"
"s_add_u32 s63, s54, 3440896\nM 64,164,152\ns_add_u32 s66, s69, 23069696\nbuffer_load_dwordx4 v220, s[12:15], s63 offen lds\nM 80,160,140\nR 182,186,53248\n"
"v_fma_f32 v234, v234, v219, v219\nM 80,164,156\nG 4\nv_exp_f32 v236, v236\nM 84,168,140\nR 160,187,40960\n"
"v_fma_f32 v235, v235, v219, v219\nM 84,172,156\nbuffer_load_dwordx4 a[40:43], v252, s[20:23], s66 offen sc1\nbuffer_load_dwordx4 v221, s[12:15], s63 offen offset:1024 lds\nM 68,168,136\nR 162,187,45056\n"
"v_fma_f32 v236, v236, v219, v219\nM 68,172,152\ns_add_u32 s67, s69, 23184384\nv_exp_f32 v237, v237\nM 52,168,132\nR 164,187,49152\n"
"s_waitcnt vmcnt(15)\ns_barrier\nM 52,172,148\nbuffer_load_dwordx4 a[44:47], v252, s[20:23], s67 offen sc1\ns_movk_i32 s63, 2048\nv_mad_u32_u24 v220, v228, s63, v229\n"
"v_add_u32 v221, 916480, v220\ns_add_u32 m0, s50, 106496\ns_add_u32 s63, s56, 0\nbuffer_load_dwordx4 v220, s[16:19], s63 offen lds\nM 36,168,128\nR 166,187,53248\n"
"v_fma_f32 v237, v237, v219, v219\nM 36,172,144\nG 4\nv_rcp_f32 v234, v234\nM 40,176,128\nR 168,184,57344\n"
"v_mul_f32 v234, v124, v234\nM 40,180,144\ns_add_u32 s64, s68, 23076864\nbuffer_load_dwordx4 v221, s[16:19], s63 offen offset:1024 lds\nM 56,176,132\nR 170,184,61440\n"
"s_add_u32 m0, s50, 114688\nM 56,180,148\nbuffer_load_dwordx4 a[48:51], v252, s[20:23], s64 offen sc1\nM 72,176,136\nR 172,188,0\ns_add_u32 s63, s56, 524288\n"
"M 72,180,152\ns_add_u32 s65, s68, 23191552\nbuffer_load_dwordx4 v220, s[16:19], s63 offen lds\nM 88,176,140\nR 174,188,4096\nv_rcp_f32 v235, v235\n"
"M 88,180,156\nG 4\nv_rcp_f32 v236, v236\nM 92,160,140\nR 176,185,57344\nv_mul_f32 v235, v125, v235\n"
"M 92,164,156\nbuffer_load_dwordx4 a[52:55], v252, s[20:23], s65 offen sc1\nbuffer_load_dwordx4 v221, s[16:19], s63 offen offset:1024 lds\nM 76,160,136\nR 178,185,61440\ns_add_u32 m0, s50, 0\n"
"M 76,164,152\ns_add_u32 s66, s69, 23076864\nv_rcp_f32 v237, v237\nM 60,160,132\nR 180,189,0\ns_add_u32 s63, s56, 65536\n"
"M 60,164,148\nbuffer_load_dwordx4 a[56:59], v252, s[20:23], s66 offen sc1\nM 44,160,128\nR 182,189,4096\nv_mul_f32 v236, v126, v236\nM 44,164,144\n"
"buffer_load_dwordx4 v220, s[16:19], s63 offen lds\nG 4\nM 32,168,160\nR 160,186,57344\nv_mul_f32 v237, v127, v237\nM 32,172,176\n"
"s_add_u32 s67, s69, 23191552\nv_cvt_pk_bf16_f32 v122, v234, v235\nv_cvt_pk_bf16_f32 v123, v236, v237\nM 48,168,164\nR 162,186,61440\nM 48,172,180\n"
"buffer_load_dwordx4 a[60:63], v252, s[20:23], s67 offen sc1\nbuffer_load_dwordx4 v221, s[16:19], s63 offen offset:1024 lds\nM 64,168,168\nR 164,190,0\ns_add_u32 m0, s50, 8192\ns_add_u32 s63, s56, 589824\n"
"M 64,172,184\nbuffer_load_dwordx4 v220, s[16:19], s63 offen lds\nM 80,168,172\nR 166,190,4096\nbuffer_store_dwordx4 v[112:115], v252, s[20:23], s85 offen offset:0 sc1\nM 80,172,188\n"
"buffer_store_dwordx4 v[120:123], v252, s[20:23], s85 offen offset:1024 sc1\nG 4\nM 84,176,172\nR 168,187,57344\nM 84,180,188\nbuffer_load_dwordx4 v221, s[16:19], s63 offen offset:1024 lds\n"
"M 68,176,168\nR 170,187,61440\ns_add_u32 s63, s57, 0\nv_mul_f32 v230, v0, v0\nM 68,180,184\ns_add_u32 m0, s50, 16384\n"
"s_add_u32 s84, s73, 23074816\ns_add_u32 s85, s73, 23189504\nv_mul_f32 v231, v1, v1\nM 52,176,164\nR 172,191,0\nv_mul_f32 v232, v2, v2\n"
"v_mul_f32 v233, v3, v3\nM 52,180,180\nbuffer_load_dwordx4 v220, s[16:19], s63 offen lds\nv_fma_f32 v230, v230, v215, v214\nM 36,176,160\nR 174,191,4096\n"
"v_fma_f32 v231, v231, v215, v214\nv_fma_f32 v232, v232, v215, v214\nM 36,180,176\nG 4\nv_fma_f32 v233, v233, v215, v214\nv_mul_f32 v230, v0, v230\n"
"v_mul_f32 v231, v1, v231\nM 40,160,160\nR 176,188,8192\nv_mul_f32 v232, v2, v232\nv_mul_f32 v233, v3, v233\nM 40,164,176\n"
"v_exp_f32 v230, v230\nv_exp_f32 v231, v231\nM 56,160,164\nR 178,188,12288\nv_fma_f32 v230, v230, v216, v216\nv_exp_f32 v232, v232\n"
"M 56,164,180\nv_exp_f32 v233, v233\nv_fma_f32 v231, v231, v216, v216\nM 72,160,168\nR 180,188,16384\nv_fma_f32 v232, v232, v216, v216\n"
"v_fma_f32 v233, v233, v216, v216\nM 72,164,184\nv_rcp_f32 v230, v230\nv_rcp_f32 v231, v231\nM 88,160,172\nR 182,188,20480\n"
"v_mul_f32 v230, v0, v230\nv_rcp_f32 v232, v232\nM 88,164,188\nG 4\nv_rcp_f32 v233, v233\nv_mul_f32 v231, v1, v231\n"
"M 92,168,172\nR 160,189,8192\nv_mul_f32 v232, v2, v232\nv_mul_f32 v233, v3, v233\nM 92,172,188\nv_cvt_pk_bf16_f32 v0, v230, v231\n"
"v_cvt_pk_bf16_f32 v1, v232, v233\nv_mul_f32 v234, v4, v4\nM 76,168,168\nR 162,189,12288\nv_mul_f32 v235, v5, v5\nv_mul_f32 v236, v6, v6\n"
"M 76,172,184\nv_mul_f32 v237, v7, v7\nv_fma_f32 v234, v234, v215, v214\nv_fma_f32 v235, v235, v215, v214\nM 60,168,164\nR 164,189,16384\n"
"v_fma_f32 v236, v236, v215, v214\nLpg12:\ns_cbranch_execz Lpg16\nv_fma_f32 v237, v237, v215, v214\nM 60,172,180\nv_mul_f32 v234, v4, v234\n"
"v_mul_f32 v235, v5, v235\nv_mul_f32 v236, v6, v236\nM 44,168,160\nR 166,189,20480\nv_mul_f32 v237, v7, v237\nv_exp_f32 v234, v234\n"
"M 44,172,176\nG 4\nv_exp_f32 v235, v235\nv_exp_f32 v236, v236\nM 32,176,192\nR 168,190,8192\n"
"v_fma_f32 v234, v234, v216, v216\nv_exp_f32 v237, v237\nM 32,180,208\nv_rcp_f32 v234, v234\nv_fma_f32 v235, v235, v216, v216\nM 48,176,196\n"
"R 170,190,12288\nv_fma_f32 v236, v236, v216, v216\nv_fma_f32 v237, v237, v216, v216\nM 48,180,212\nv_rcp_f32 v235, v235\nv_rcp_f32 v236, v236\n"
"M 64,176,200\nR 172,190,16384\nv_mul_f32 v234, v4, v234\nv_rcp_f32 v237, v237\nM 64,180,216\nv_mul_f32 v235, v5, v235\n"
"v_mul_f32 v236, v6, v236\nv_mul_f32 v237, v7, v237\nM 80,176,204\nR 174,190,20480\nv_cvt_pk_bf16_f32 v2, v234, v235\nv_cvt_pk_bf16_f32 v3, v236, v237\n"
"M 80,180,220\nG 4\nv_mul_f32 v230, v16, v16\nv_mul_f32 v231, v17, v17\nv_mul_f32 v232, v18, v18\nM 84,160,204\n"
"R 176,191,8192\ns_waitcnt vmcnt(9)\ns_barrier\nv_mul_f32 v233, v19, v19\nM 84,164,220\nv_fma_f32 v230, v230, v218, v217\n"
"v_fma_f32 v231, v231, v218, v217\nv_fma_f32 v232, v232, v218, v217\nM 68,160,200\nR 178,191,12288\nv_fma_f32 v233, v233, v218, v217\nv_mul_f32 v230, v16, v230\n"
"M 68,164,216\nv_exp_f32 v230, v230\nv_mul_f32 v231, v17, v231\nM 52,160,196\nR 180,191,16384\nv_mul_f32 v232, v18, v232\n"
"v_mul_f32 v233, v19, v233\nM 52,164,212\nv_exp_f32 v231, v231\nv_exp_f32 v232, v232\nM 36,160,192\nR 182,191,20480\n"
"v_fma_f32 v230, v230, v219, v219\nv_exp_f32 v233, v233\nM 36,164,208\nG 4\nv_rcp_f32 v230, v230\nv_fma_f32 v231, v231, v219, v219\n"
"M 40,168,192\nR 160,188,24576\nv_fma_f32 v232, v232, v219, v219\nv_fma_f32 v233, v233, v219, v219\nM 40,172,208\nv_rcp_f32 v231, v231\n"
"v_rcp_f32 v232, v232\nM 56,168,196\nR 162,188,28672\nv_mul_f32 v230, v16, v230\nv_rcp_f32 v233, v233\nM 56,172,212\n"
"v_mul_f32 v231, v17, v231\nv_mul_f32 v232, v18, v232\nv_mul_f32 v233, v19, v233\nM 72,168,200\nR 164,188,32768\nv_cvt_pk_bf16_f32 v16, v230, v231\n"
"v_cvt_pk_bf16_f32 v17, v232, v233\nM 72,172,216\nv_mul_f32 v234, v20, v20\nv_mul_f32 v235, v21, v21\nv_mul_f32 v236, v22, v22\nM 88,168,204\n"
"R 166,188,36864\nv_mov_b32 v226, s46\nv_mul_f32 v237, v23, v23\nM 88,172,220\nG 4\nv_mov_b32 v227, s47\n"
"v_fma_f32 v234, v234, v218, v217\nv_fma_f32 v235, v235, v218, v217\nM 92,176,204\nR 168,189,24576\ns_add_u32 s61, s81, 385032\ns_lshl_b32 s62, s44, 5\n"
"s_add_u32 s61, s61, s62\ns_waitcnt vmcnt(0)\ns_mov_b64 exec, 1\nbuffer_atomic_swap_x2 v[226:227], off, s[20:23], s61 sc1\ns_mov_b64 exec, -1\nv_fma_f32 v236, v236, v218, v217\n"
"M 92,180,220\nbuffer_load_dwordx4 v221, s[16:19], s63 offen offset:1024 lds\nv_fma_f32 v237, v237, v218, v217\nM 76,176,200\nR 170,189,28672\ns_add_u32 m0, s50, 24576\n"
"s_add_u32 s63, s57, 524288\nv_mul_f32 v234, v20, v234\nM 76,180,216\nbuffer_load_dwordx4 v220, s[16:19], s63 offen lds\nv_mul_f32 v235, v21, v235\nM 60,176,196\n"
"R 172,189,32768\nv_mul_f32 v236, v22, v236\nv_mul_f32 v237, v23, v237\nM 60,180,212\nv_exp_f32 v234, v234\nv_exp_f32 v235, v235\n"
"M 44,176,192\nR 174,189,36864\ns_mov_b32 s60, 158208\nv_bfe_u32 v246, v252, 4, 4\nv_lshl_add_u32 v246, v246, 3, s60\nv_exp_f32 v236, v236\n"
"M 44,180,208\nds_read_b64 v[238:239], v246 offset:0\nG 5\nv_exp_f32 v237, v237\nM 32,160,224\nR 176,190,24576\n"
"ds_read_b64 v[240:241], v246 offset:128\nv_fma_f32 v234, v234, v219, v219\nM 32,164,240\nds_read_b64 v[242:243], v246 offset:256\nbuffer_load_dwordx4 v221, s[16:19], s63 offen offset:1024 lds\nv_fma_f32 v235, v235, v219, v219\n"
"M 48,160,228\nR 178,190,28672\nds_read_b64 v[244:245], v246 offset:384\nv_fma_f32 v236, v236, v219, v219\nM 48,164,244\nds_read_b64 v[246:247], v246 offset:0\n"
"s_add_u32 m0, s50, 32768\nv_fma_f32 v237, v237, v219, v219\nM 64,160,232\nR 180,190,32768\ns_add_u32 s63, s57, 65536\nv_rcp_f32 v234, v234\n"
"M 64,164,248\nbuffer_load_dwordx4 v220, s[16:19], s63 offen lds\nv_rcp_f32 v235, v235\nM 80,160,236\nR 182,190,36864\nv_mul_f32 v234, v20, v234\n"
"v_rcp_f32 v236, v236\nM 80,164,252\nG 9\nv_rcp_f32 v237, v237\nv_mul_f32 v235, v21, v235\nM 84,168,236\n"
"R 160,191,24576\nv_mul_f32 v236, v22, v236\nM 84,172,252\nbuffer_load_dwordx4 v221, s[16:19], s63 offen offset:1024 lds\nM 68,168,232\nR 162,191,28672\n"
"s_add_u32 m0, s50, 40960\ns_add_u32 s63, s57, 589824\nM 68,172,248\nbuffer_load_dwordx4 v220, s[16:19], s63 offen lds\nM 52,168,228\nR 164,191,32768\n"
"G 9\nv_mfma_f32_16x16x32_bf16 v[32:35], v[238:241], v[208:211], v[32:35]\nv_mul_f32 v237, v23, v237\nM 52,172,244\nbuffer_store_dwordx4 v[0:3], v252, s[20:23], s84 offen offset:0 sc1\nM 36,168,224\n"
"R 166,191,36864\nG 9\nv_mfma_f32_16x16x32_bf16 v[36:39], v[240:243], v[208:211], v[36:39]\nv_cvt_pk_bf16_f32 v18, v234, v235\nM 36,172,240\nG 4\n"
"v_cvt_pk_bf16_f32 v19, v236, v237\nM 40,176,224\nR 168,188,40960\nv_mfma_f32_16x16x32_bf16 v[40:43], v[242:245], v[208:211], v[40:43]\nbuffer_load_dwordx4 v221, s[16:19], s63 offen offset:1024 lds\nbuffer_store_dwordx4 v[16:19], v252, s[20:23], s85 offen offset:0 sc1\n"
"M 40,180,240\ns_add_u32 m0, s50, 49152\ns_add_u32 s63, s56, 131072\nbuffer_load_dwordx4 v220, s[16:19], s63 offen lds\nM 56,176,228\nR 170,188,45056\n"
"v_mfma_f32_16x16x32_bf16 v[44:47], v[244:247], v[208:211], v[44:47]\nv_mov_b32 v248, 0\nM 56,180,244\nv_mov_b32 v249, 0\nM 72,176,232\nR 172,188,49152\n"
"v_mfma_f32_16x16x32_bf16 v[48:51], v[244:247], v[210:213], v[48:51]\ns_add_u32 s61, s82, 385032\ns_mov_b64 exec, 0xf\nbuffer_atomic_or_x2 v[248:249], v250, s[20:23], s61 offen sc0 sc1\ns_mov_b64 exec, -1\nM 72,180,248\n"
"buffer_load_dwordx4 v221, s[16:19], s63 offen offset:1024 lds\nM 88,176,236\nR 174,188,53248\nv_mfma_f32_16x16x32_bf16 v[52:55], v[238:241], v[210:213], v[52:55]\ns_add_u32 m0, s50, 57344\nM 88,180,252\n"
"G 4\nM 92,160,236\nR 176,189,40960\nv_mfma_f32_16x16x32_bf16 v[56:59], v[240:243], v[210:213], v[56:59]\ns_add_u32 s63, s56, 655360\nM 92,164,252\n"
"buffer_load_dwordx4 v220, s[16:19], s63 offen lds\nM 76,160,232\nR 178,189,45056\nv_mfma_f32_16x16x32_bf16 v[60:63], v[242:245], v[210:213], v[60:63]\nM 76,164,248\nM 60,160,228\n"
"R 180,189,49152\nM 60,164,244\nM 44,160,224\nR 182,189,53248\nM 44,164,240\ns_barrier\n"
"G 4\nv_mfma_f32_16x16x32_bf16 v[96:99], v[168:171], a[0:3], 0\nR 160,190,40960\nM 96,172,16\ns_mov_b32 m0, s51\nv_mfma_f32_16x16x32_bf16 v[112:115], v[168:171], a[4:7], 0\n"
"R 162,190,45056\nM 112,172,20\nds_write_addtid_b32 v64 offset:0\nv_mfma_f32_16x16x32_bf16 v[128:131], v[168:171], a[8:11], 0\nR 164,190,49152\nM 128,172,24\n"
"ds_write_addtid_b32 v65 offset:256\nv_mfma_f32_16x16x32_bf16 v[144:147], v[168:171], a[12:15], 0\nR 166,190,53248\nds_write_addtid_b32 v66 offset:512\nM 144,172,28\nds_write_addtid_b32 v67 offset:768\n"
"ds_write_addtid_b32 v68 offset:1024\nG 9\nv_mfma_f32_16x16x32_bf16 v[148:151], v[176:179], a[12:15], 0\nR 168,191,40960\nds_write_addtid_b32 v69 offset:1280\nM 148,180,28\n"
"ds_write_addtid_b32 v70 offset:1536\nds_write_addtid_b32 v71 offset:1792\nv_mfma_f32_16x16x32_bf16 v[132:135], v[176:179], a[8:11], 0\nR 170,191,45056\nds_write_addtid_b32 v72 offset:2048\nM 132,180,24\n"
"ds_write_addtid_b32 v73 offset:2304\nds_write_addtid_b32 v74 offset:2560\nv_mfma_f32_16x16x32_bf16 v[116:119], v[176:179], a[4:7], 0\nR 172,191,49152\nds_write_addtid_b32 v75 offset:2816\nM 116,180,20\n"
"ds_write_addtid_b32 v76 offset:3072\nds_write_addtid_b32 v77 offset:3328\nv_mfma_f32_16x16x32_bf16 v[100:103], v[176:179], a[0:3], 0\nR 174,191,53248\nds_write_addtid_b32 v78 offset:3584\nM 100,180,16\n"
"ds_write_addtid_b32 v79 offset:3840\nds_write_addtid_b32 v80 offset:4096\nG 15\nv_mfma_f32_16x16x32_bf16 v[104:107], v[160:163], a[0:3], 0\nR 176,184,0\nds_write_addtid_b32 v81 offset:4352\n"
"M 104,164,16\nds_write_addtid_b32 v82 offset:4608\nds_write_addtid_b32 v83 offset:4864\nv_mfma_f32_16x16x32_bf16 v[120:123], v[160:163], a[4:7], 0\nR 178,184,4096\nds_write_addtid_b32 v84 offset:5120\n"
"M 120,164,20\nds_write_addtid_b32 v85 offset:5376\nds_write_addtid_b32 v86 offset:5632\nv_mfma_f32_16x16x32_bf16 v[136:139], v[160:163], a[8:11], 0\nR 180,184,8192\nds_write_addtid_b32 v87 offset:5888\n"
"M 136,164,24\nds_write_addtid_b32 v88 offset:6144\nds_write_addtid_b32 v89 offset:6400\nv_mfma_f32_16x16x32_bf16 v[152:155], v[160:163], a[12:15], 0\nR 182,184,12288\nds_write_addtid_b32 v90 offset:6656\n"
"M 152,164,28\nds_write_addtid_b32 v91 offset:6912\nds_write_addtid_b32 v92 offset:7168\nG 15\nv_mfma_f32_16x16x32_bf16 v[156:159], v[168:171], a[12:15], 0\nR 160,185,0\n"
"ds_write_addtid_b32 v93 offset:7424\nM 156,172,28\nds_write_addtid_b32 v94 offset:7680\nds_write_addtid_b32 v95 offset:7936\nv_mfma_f32_16x16x32_bf16 v[140:143], v[168:171], a[8:11], 0\nR 162,185,4096\n"
"M 140,172,24\nv_mfma_f32_16x16x32_bf16 v[124:127], v[168:171], a[4:7], 0\nR 164,185,8192\ns_waitcnt vmcnt(5) lgkmcnt(2)\ns_barrier\nM 124,172,20\n"
"ds_read2st64_b32 v[64:65], v225 offset0:0 offset1:1\nds_read2st64_b32 v[66:67], v225 offset0:2 offset1:3\nv_mfma_f32_16x16x32_bf16 v[108:111], v[168:171], a[0:3], 0\nR 166,185,12288\nds_read2st64_b32 v[68:69], v225 offset0:4 offset1:5\nM 108,172,16\n"
"ds_read2st64_b32 v[70:71], v225 offset0:6 offset1:7\nds_read2st64_b32 v[72:73], v225 offset0:8 offset1:9\nM 96,176,32\nR 168,186,0\nds_read2st64_b32 v[74:75], v225 offset0:10 offset1:11\nM 96,180,48\n"
"s_add_u32 s64, s68, 23070720\nds_read2st64_b32 v[76:77], v225 offset0:12 offset1:13\nv_mov_b32 v226, s46\nM 112,176,36\nR 170,186,4096\nds_read2st64_b32 v[78:79], v225 offset0:14 offset1:15\n"
"v_mov_b32 v227, s47\ns_waitcnt vmcnt(2)\nv_cmp_eq_u64 s[86:87], s[46:47], v[248:249]\nM 112,180,52\ns_add_u32 s61, s81, 385048\ns_lshl_b32 s62, s44, 5\n"
"s_add_u32 s61, s61, s62\ns_waitcnt vmcnt(0)\ns_mov_b64 exec, 1\nbuffer_atomic_swap_x2 v[226:227], off, s[20:23], s61 sc1\ns_mov_b64 exec, -1\nds_read2st64_b32 v[80:81], v225 offset0:16 offset1:17\n"
"M 128,176,40\nR 172,186,8192\ns_and_b32 s62, s86, 0xf\ns_cmp_eq_u32 s62, 0xf\ns_cbranch_scc0 Lq420\nLq421:\n"
"ds_read2st64_b32 v[82:83], v225 offset0:18 offset1:19\nM 128,180,56\ns_add_u32 s65, s68, 23185408\nds_read2st64_b32 v[84:85], v225 offset0:20 offset1:21\nds_read2st64_b32 v[86:87], v225 offset0:22 offset1:23\nM 144,176,44\n"
"R 174,186,12288\ns_add_u32 m0, s50, 57344\nds_read2st64_b32 v[88:89], v225 offset0:24 offset1:25\nM 144,180,60\nds_read2st64_b32 v[90:91], v225 offset0:26 offset1:27\nds_read2st64_b32 v[92:93], v225 offset0:28 offset1:29\n"
"G 15\nM 148,160,44\nR 176,187,0\nbuffer_load_dwordx4 v221, s[16:19], s63 offen offset:1024 lds\nds_read2st64_b32 v[94:95], v225 offset0:30 offset1:31\nM 148,164,60\n"
"buffer_load_dwordx4 a[64:67], v252, s[20:23], s64 offen sc1\nv_add_f32 v32, v32, v64\nM 132,160,40\nR 178,187,4096\ns_add_u32 m0, s50, 65536\nv_add_f32 v33, v33, v65\n"
"M 132,164,56\ns_add_u32 s66, s69, 23070720\ns_add_u32 s63, s56, 196608\nv_add_f32 v34, v34, v66\nv_add_f32 v35, v35, v67\nM 116,160,36\n"
"R 180,187,8192\nbuffer_load_dwordx4 v220, s[16:19], s63 offen lds\nv_add_f32 v36, v36, v68\nM 116,164,52\nbuffer_load_dwordx4 a[68:71], v252, s[20:23], s65 offen sc1\nv_add_f32 v37, v37, v69\n"
"M 100,160,32\nR 182,187,12288\nv_add_f32 v38, v38, v70\nv_add_f32 v39, v39, v71\nM 100,164,48\nG 8\n"
"v_add_f32 v40, v40, v72\nv_add_f32 v41, v41, v73\nv_add_f32 v42, v42, v74\nM 104,168,32\nR 160,184,16384\nbuffer_load_dwordx4 v221, s[16:19], s63 offen offset:1024 lds\n"
"v_add_f32 v43, v43, v75\nM 104,172,48\ns_add_u32 s67, s69, 23185408\nv_add_f32 v44, v44, v76\nv_add_f32 v45, v45, v77\nv_add_f32 v46, v46, v78\n"
"M 120,168,36\nR 162,184,20480\nv_add_f32 v47, v47, v79\nv_add_f32 v48, v48, v80\nM 120,172,52\nbuffer_load_dwordx4 a[72:75], v252, s[20:23], s66 offen sc1\n"
"v_add_f32 v49, v49, v81\nM 136,168,40\nR 164,184,24576\nv_add_f32 v50, v50, v82\nv_add_f32 v51, v51, v83\nM 136,172,56\n"
"v_add_f32 v52, v52, v84\nv_add_f32 v53, v53, v85\nM 152,168,44\nR 166,184,28672\nv_add_f32 v54, v54, v86\nM 152,172,60\n"
"G 4\nv_add_f32 v55, v55, v87\nv_add_f32 v56, v56, v88\nM 156,176,44\nR 168,185,16384\nv_add_f32 v57, v57, v89\n"
"M 156,180,60\nbuffer_load_dwordx4 a[76:79], v252, s[20:23], s67 offen sc1\nM 140,176,40\nR 170,185,20480\nv_add_f32 v58, v58, v90\nM 140,180,56\n"
"s_add_u32 s64, s68, 23077888\nv_add_f32 v59, v59, v91\nv_add_f32 v60, v60, v92\nM 124,176,36\nR 172,185,24576\nv_add_f32 v61, v61, v93\n"
"M 124,180,52\ns_add_u32 s65, s68, 23192576\nv_add_f32 v62, v62, v94\nv_add_f32 v63, v63, v95\nM 108,176,32\nR 174,185,28672\n"
"v_mul_f32 v230, v32, v32\nv_mul_f32 v231, v33, v33\nM 108,180,48\ns_add_u32 s84, s73, 23072768\ns_add_u32 s85, s73, 23187456\nG 4\n"
"v_mul_f32 v232, v34, v34\nv_mfma_f32_16x16x32_bf16 v[0:3], v[160:163], a[0:3], 0\nR 176,186,16384\nLq585:\ns_lshr_b32 s60, s45, 3\ns_lshl2_add_u32 s60, s60, s40\n"
"s_lshl_b32 s60, s60, 8\nv_lshl_add_u32 v224, v252, 3, s60\nv_add_u32 v224, Lq585+4096-LqPC, v224\nv_min_u32 v224, LqE-LqPC-4, v224\ns_mov_b64 exec, 3\nglobal_load_dword v224, v224, s[98:99]\n"
"s_mov_b64 exec, -1\nv_mul_f32 v233, v35, v35\nM 0,164,16\nbuffer_load_dwordx4 a[80:83], v252, s[20:23], s64 offen sc1\nv_fma_f32 v230, v230, v215, v214\nv_mfma_f32_16x16x32_bf16 v[16:19], v[160:163], a[4:7], 0\n"
"R 178,186,20480\nv_fma_f32 v231, v231, v215, v214\nv_fma_f32 v232, v232, v215, v214\nM 16,164,20\ns_add_u32 s66, s69, 23077888\nv_fma_f32 v233, v233, v215, v214\n"
"v_mul_f32 v230, v32, v230\nv_mul_f32 v231, v33, v231\nv_mfma_f32_16x16x32_bf16 a[224:227], v[160:163], a[8:11], 0\nR 180,186,24576\nv_mul_f32 v232, v34, v232\nv_mul_f32 v233, v35, v233\n"
"v_mfma_f32_16x16x32_bf16 a[224:227], v[164:167], a[24:27], a[224:227]\nbuffer_load_dwordx4 a[84:87], v252, s[20:23], s65 offen sc1\nv_exp_f32 v230, v230\nv_mfma_f32_16x16x32_bf16 a[240:243], v[160:163], a[12:15], 0\nR 182,186,28672\nv_fma_f32 v230, v230, v216, v216\n"
"v_exp_f32 v231, v231\nv_mfma_f32_16x16x32_bf16 a[240:243], v[164:167], a[28:31], a[240:243]\nG 4\nv_exp_f32 v232, v232\nv_exp_f32 v233, v233\nv_mfma_f32_16x16x32_bf16 a[244:247], v[168:171], a[12:15], 0\n"
"R 160,187,16384\ns_add_u32 m0, s50, 73728\nv_fma_f32 v231, v231, v216, v216\nv_mfma_f32_16x16x32_bf16 a[244:247], v[172:175], a[28:31], a[244:247]\ns_add_u32 s67, s69, 23192576\nv_rcp_f32 v230, v230\n"
"v_fma_f32 v232, v232, v216, v216\nv_mfma_f32_16x16x32_bf16 a[228:231], v[168:171], a[8:11], 0\nR 162,187,20480\ns_add_u32 s63, s56, 720896\nv_fma_f32 v233, v233, v216, v216\nv_mfma_f32_16x16x32_bf16 a[228:231], v[172:175], a[24:27], a[228:231]\n"
"buffer_load_dwordx4 a[88:91], v252, s[20:23], s66 offen sc1\nv_rcp_f32 v231, v231\nv_mfma_f32_16x16x32_bf16 v[20:23], v[168:171], a[4:7], 0\nR 164,187,24576\nv_mul_f32 v230, v32, v230\nv_rcp_f32 v232, v232\n"
"M 20,172,20\nbuffer_load_dwordx4 v220, s[16:19], s63 offen lds\nv_rcp_f32 v233, v233\nv_mfma_f32_16x16x32_bf16 v[4:7], v[168:171], a[0:3], 0\nR 166,187,28672\nv_mul_f32 v231, v33, v231\n"
"v_mul_f32 v232, v34, v232\nM 4,172,16\nG 4\nv_mul_f32 v233, v35, v233\nv_cvt_pk_bf16_f32 v32, v230, v231\nv_cvt_pk_bf16_f32 v33, v232, v233\n"
"v_mfma_f32_16x16x32_bf16 v[8:11], v[176:179], a[0:3], 0\nR 168,184,32768\nv_mul_f32 v234, v36, v36\nv_mul_f32 v235, v37, v37\nM 8,180,16\nbuffer_load_dwordx4 a[92:95], v252, s[20:23], s67 offen sc1\n"
"buffer_load_dwordx4 v221, s[16:19], s63 offen offset:1024 lds\nv_mul_f32 v236, v38, v38\nv_mfma_f32_16x16x32_bf16 v[24:27], v[176:179], a[4:7], 0\nR 170,184,36864\ns_add_u32 m0, s50, 81920\nv_mul_f32 v237, v39, v39\n"
"M 24,180,20\ns_add_u32 s64, s68, 23071744\nv_fma_f32 v234, v234, v215, v214\nv_fma_f32 v235, v235, v215, v214\nv_fma_f32 v236, v236, v215, v214\nv_mfma_f32_16x16x32_bf16 a[232:235], v[176:179], a[8:11], 0\n"
"R 172,184,40960\ns_add_u32 s63, s57, 131072\nv_fma_f32 v237, v237, v215, v214\nv_mfma_f32_16x16x32_bf16 a[232:235], v[180:183], a[24:27], a[232:235]\ns_add_u32 s65, s68, 23186432\nbuffer_load_dwordx4 v220, s[16:19], s63 offen lds\n"
"v_mul_f32 v234, v36, v234\nv_mfma_f32_16x16x32_bf16 a[248:251], v[176:179], a[12:15], 0\nR 174,184,45056\nv_mul_f32 v235, v37, v235\nv_mul_f32 v236, v38, v236\nv_mfma_f32_16x16x32_bf16 a[248:251], v[180:183], a[28:31], a[248:251]\n"
"G 4\nv_exp_f32 v234, v234\nv_mul_f32 v237, v39, v237\nv_mfma_f32_16x16x32_bf16 a[252:255], v[160:163], a[12:15], 0\nR 176,185,32768\nv_fma_f32 v234, v234, v216, v216\n"
"v_exp_f32 v235, v235\nv_mfma_f32_16x16x32_bf16 a[252:255], v[164:167], a[28:31], a[252:255]\nbuffer_load_dwordx4 a[96:99], v252, s[20:23], s64 offen sc1\nbuffer_load_dwordx4 v221, s[16:19], s63 offen offset:1024 lds\nv_exp_f32 v236, v236\nv_mfma_f32_16x16x32_bf16 a[236:239], v[160:163], a[8:11], 0\n"
"R 178,185,36864\ns_add_u32 m0, s50, 90112\nv_exp_f32 v237, v237\nv_mfma_f32_16x16x32_bf16 a[236:239], v[164:167], a[24:27], a[236:239]\ns_add_u32 s66, s69, 23071744\nv_rcp_f32 v234, v234\n"
"v_fma_f32 v235, v235, v216, v216\nv_mfma_f32_16x16x32_bf16 v[28:31], v[160:163], a[4:7], 0\nR 180,185,40960\ns_add_u32 s63, s57, 655360\nv_fma_f32 v236, v236, v216, v216\nM 28,164,20\n"
"buffer_load_dwordx4 a[100:103], v252, s[20:23], s65 offen sc1\nbuffer_load_dwordx4 v220, s[16:19], s63 offen lds\nv_fma_f32 v237, v237, v216, v216\nv_mfma_f32_16x16x32_bf16 v[12:15], v[160:163], a[0:3], 0\nLpg13:\ns_cbranch_execz Lpg17\n"
"R 182,185,45056\nv_mul_f32 v234, v36, v234\nv_rcp_f32 v235, v235\nM 12,164,16\ns_waitcnt vmcnt(14)\ns_barrier\n"
"G 4\nv_rcp_f32 v236, v236\nM 0,168,32\nR 160,186,32768\nv_mul_f32 v235, v37, v235\nv_rcp_f32 v237, v237\n"
"M 0,172,48\ns_add_u32 s67, s69, 23186432\nbuffer_load_dwordx4 v221, s[16:19], s63 offen offset:1024 lds\nv_mul_f32 v236, v38, v236\nM 16,168,36\nR 162,186,36864\n"
"v_mul_f32 v237, v39, v237\nv_cvt_pk_bf16_f32 v34, v234, v235\nM 16,172,52\nbuffer_load_dwordx4 a[104:107], v252, s[20:23], s66 offen sc1\nv_cvt_pk_bf16_f32 v35, v236, v237\nv_mfma_f32_16x16x32_bf16 a[224:227], v[168:171], a[40:43], a[224:227]\n"
"R 164,186,40960\nv_mul_f32 v230, v40, v40\nv_mul_f32 v231, v41, v41\nv_mfma_f32_16x16x32_bf16 a[224:227], v[172:175], a[56:59], a[224:227]\nv_mul_f32 v232, v42, v42\nv_mul_f32 v233, v43, v43\n"
"v_fma_f32 v230, v230, v215, v214\nv_mfma_f32_16x16x32_bf16 a[240:243], v[168:171], a[44:47], a[240:243]\nR 166,186,45056\nv_fma_f32 v231, v231, v215, v214\nv_fma_f32 v232, v232, v215, v214\nv_mfma_f32_16x16x32_bf16 a[240:243], v[172:175], a[60:63], a[240:243]\n"
"G 4\nv_fma_f32 v233, v233, v215, v214\nv_mul_f32 v230, v40, v230\nv_mul_f32 v231, v41, v231\nv_mfma_f32_16x16x32_bf16 a[244:247], v[176:179], a[44:47], a[244:247]\nR 168,187,32768\n"
"v_mul_f32 v232, v42, v232\nv_mul_f32 v233, v43, v233\nv_mfma_f32_16x16x32_bf16 a[244:247], v[180:183], a[60:63], a[244:247]\nbuffer_load_dwordx4 a[108:111], v252, s[20:23], s67 offen sc1\nv_exp_f32 v230, v230\nv_mfma_f32_16x16x32_bf16 a[228:231], v[176:179], a[40:43], a[228:231]\n"
"R 170,187,36864\nv_fma_f32 v230, v230, v216, v216\nv_exp_f32 v231, v231\nv_mfma_f32_16x16x32_bf16 a[228:231], v[180:183], a[56:59], a[228:231]\ns_add_u32 s64, s68, 23078912\nv_exp_f32 v232, v232\n"
"v_exp_f32 v233, v233\nM 20,176,36\nR 172,187,40960\nv_fma_f32 v231, v231, v216, v216\nv_fma_f32 v232, v232, v216, v216\nM 20,180,52\n"
"s_add_u32 s65, s68, 23193600\nv_rcp_f32 v230, v230\nv_fma_f32 v233, v233, v216, v216\nM 4,176,32\nR 174,187,45056\ns_add_u32 m0, s50, 98304\n"
"v_rcp_f32 v231, v231\nM 4,180,48\nG 4\nv_rcp_f32 v232, v232\nv_rcp_f32 v233, v233\nM 8,160,32\n"
"R 176,184,49152\ns_add_u32 s63, s57, 196608\nv_mul_f32 v230, v40, v230\nM 8,164,48\nbuffer_load_dwordx4 a[112:115], v252, s[20:23], s64 offen sc1\nv_mul_f32 v231, v41, v231\n"
"M 24,160,36\nR 178,184,53248\nv_mul_f32 v232, v42, v232\nv_mul_f32 v233, v43, v233\nM 24,164,52\ns_add_u32 s66, s69, 23078912\n"
"buffer_load_dwordx4 v220, s[16:19], s63 offen lds\nv_cvt_pk_bf16_f32 v40, v230, v231\nv_mfma_f32_16x16x32_bf16 a[232:235], v[160:163], a[40:43], a[232:235]\nR 180,184,57344\nv_cvt_pk_bf16_f32 v41, v232, v233\nv_mul_f32 v234, v44, v44\n"
"v_mfma_f32_16x16x32_bf16 a[232:235], v[164:167], a[56:59], a[232:235]\nbuffer_load_dwordx4 a[116:119], v252, s[20:23], s65 offen sc1\nv_mul_f32 v235, v45, v45\nv_mfma_f32_16x16x32_bf16 a[248:251], v[160:163], a[44:47], a[248:251]\nR 182,184,61440\nv_mul_f32 v236, v46, v46\n"
"v_mul_f32 v237, v47, v47\nv_mfma_f32_16x16x32_bf16 a[248:251], v[164:167], a[60:63], a[248:251]\nbuffer_load_dwordx4 v221, s[16:19], s63 offen offset:1024 lds\nG 4\nv_fma_f32 v234, v234, v215, v214\nv_mfma_f32_16x16x32_bf16 a[252:255], v[168:171], a[44:47], a[252:255]\n"
"R 160,185,49152\ns_add_u32 m0, s50, 106496\nv_fma_f32 v235, v235, v215, v214\nv_mfma_f32_16x16x32_bf16 a[252:255], v[172:175], a[60:63], a[252:255]\ns_add_u32 s67, s69, 23193600\ns_add_u32 s63, s57, 720896\n"
"v_fma_f32 v236, v236, v215, v214\nv_fma_f32 v237, v237, v215, v214\nv_mfma_f32_16x16x32_bf16 a[236:239], v[168:171], a[40:43], a[236:239]\nR 162,185,53248\nv_mov_b32 v248, 0\nv_mul_f32 v234, v44, v234\n"
"v_mfma_f32_16x16x32_bf16 a[236:239], v[172:175], a[56:59], a[236:239]\nbuffer_load_dwordx4 a[120:123], v252, s[20:23], s66 offen sc1\nbuffer_load_dwordx4 v220, s[16:19], s63 offen lds\nv_mov_b32 v249, 0\nv_mul_f32 v235, v45, v235\nM 28,168,36\n"
"R 164,185,57344\ns_add_u32 s61, s82, 385048\ns_mov_b64 exec, 0xf\nbuffer_atomic_or_x2 v[248:249], v250, s[20:23], s61 offen sc0 sc1\ns_mov_b64 exec, -1\nv_mul_f32 v236, v46, v236\n"
"M 28,172,52\nbuffer_load_dwordx4 v221, s[16:19], s63 offen offset:1024 lds\nv_mul_f32 v237, v47, v237\nM 12,168,32\nR 166,185,61440\ns_add_u32 m0, s50, 114688\n"
"v_exp_f32 v234, v234\nM 12,172,48\nG 4\nv_exp_f32 v235, v235\nv_exp_f32 v236, v236\nM 96,176,64\n"
"R 168,186,49152\ns_add_u32 s63, s58, 0\nv_exp_f32 v237, v237\ns_waitcnt vmcnt(21)\nM 96,180,80\nbuffer_load_dwordx4 a[124:127], v252, s[20:23], s67 offen sc1\n"
"v_fma_f32 v234, v234, v216, v216\nM 112,176,68\nR 170,186,53248\nv_fma_f32 v235, v235, v216, v216\nv_fma_f32 v236, v236, v216, v216\ns_waitcnt vmcnt(21)\n"
"M 112,180,84\nbuffer_load_dwordx4 v220, s[16:19], s63 offen lds\nv_fma_f32 v237, v237, v216, v216\nM 128,176,72\nR 172,186,57344\nv_mul_f32 v230, v48, v48\n"
"v_rcp_f32 v234, v234\ns_waitcnt vmcnt(21)\nM 128,180,88\nbuffer_load_dwordx4 v221, s[16:19], s63 offen offset:1024 lds\nv_rcp_f32 v235, v235\nM 144,176,76\n"
"R 174,186,61440\ns_add_u32 m0, s50, 0\nv_rcp_f32 v236, v236\ns_waitcnt vmcnt(20)\nM 144,180,92\nG 4\n"
"v_rcp_f32 v237, v237\nv_mul_f32 v234, v44, v234\nM 148,160,76\nR 176,187,49152\ns_add_u32 s63, s58, 524288\nv_mul_f32 v235, v45, v235\n"
"M 148,164,92\nbuffer_load_dwordx4 v220, s[16:19], s63 offen lds\nv_mul_f32 v236, v46, v236\nM 132,160,72\nR 178,187,53248\nv_mul_f32 v237, v47, v237\n"
"v_cvt_pk_bf16_f32 v42, v234, v235\nM 132,164,88\nbuffer_store_dwordx4 v[32:35], v252, s[20:23], s84 offen offset:0 sc1\nv_cvt_pk_bf16_f32 v43, v236, v237\nM 116,160,68\nR 180,187,57344\n"
"v_mul_f32 v231, v49, v49\nv_mul_f32 v232, v50, v50\nM 116,164,84\nbuffer_load_dwordx4 v221, s[16:19], s63 offen offset:1024 lds\nv_mul_f32 v233, v51, v51\nM 100,160,64\n"
"R 182,187,61440\nv_fma_f32 v230, v230, v218, v217\nv_fma_f32 v231, v231, v218, v217\nM 100,164,80\nG 4\nv_fma_f32 v232, v232, v218, v217\n"
"v_fma_f32 v233, v233, v218, v217\nv_mul_f32 v230, v48, v230\nM 104,168,64\nR 160,188,0\ns_waitcnt vmcnt(16)\ns_barrier\n"
"v_mul_f32 v231, v49, v231\nM 104,172,80\nbuffer_store_dwordx4 v[40:43], v252, s[20:23], s84 offen offset:1024 sc1\nv_mul_f32 v232, v50, v232\nM 120,168,68\nR 162,188,4096\n"
"v_mul_f32 v233, v51, v233\nv_exp_f32 v230, v230\nM 120,172,84\ns_add_u32 m0, s50, 8192\nv_exp_f32 v231, v231\nv_exp_f32 v232, v232\n"
"M 136,168,72\nR 164,188,8192\nv_fma_f32 v230, v230, v219, v219\nv_exp_f32 v233, v233\nM 136,172,88\ns_add_u32 s63, s58, 65536\n"
"v_rcp_f32 v230, v230\nv_fma_f32 v231, v231, v219, v219\nM 152,168,76\nR 166,188,12288\nv_fma_f32 v232, v232, v219, v219\nv_fma_f32 v233, v233, v219, v219\n"
"M 152,172,92\nG 4\nv_rcp_f32 v231, v231\nv_rcp_f32 v232, v232\nM 156,176,76\nR 168,189,0\n"
"v_mul_f32 v230, v48, v230\nv_rcp_f32 v233, v233\nM 156,180,92\nbuffer_load_dwordx4 v220, s[16:19], s63 offen lds\nv_mul_f32 v231, v49, v231\nM 140,176,72\n"
"R 170,189,4096\nv_mul_f32 v232, v50, v232\nv_mul_f32 v233, v51, v233\nM 140,180,88\nv_cvt_pk_bf16_f32 v48, v230, v231\nv_cvt_pk_bf16_f32 v49, v232, v233\n"
"v_mul_f32 v234, v52, v52\nM 124,176,68\nR 172,189,8192\nv_mul_f32 v235, v53, v53\nv_mul_f32 v236, v54, v54\nM 124,180,84\n"
"buffer_load_dwordx4 v221, s[16:19], s63 offen offset:1024 lds\nv_mul_f32 v237, v55, v55\nM 108,176,64\nR 174,189,12288\ns_add_u32 m0, s50, 16384\nv_fma_f32 v234, v234, v218, v217\n"
"M 108,180,80\nG 4\nv_fma_f32 v235, v235, v218, v217\nv_fma_f32 v236, v236, v218, v217\nv_fma_f32 v237, v237, v218, v217\nM 96,160,96\n"
"R 176,190,0\ns_add_u32 s63, s58, 589824\nv_mul_f32 v234, v52, v234\ns_waitcnt vmcnt(16)\nM 96,164,112\nbuffer_load_dwordx4 v220, s[16:19], s63 offen lds\n"
"v_mul_f32 v235, v53, v235\nM 112,160,100\nR 178,190,4096\nv_mul_f32 v236, v54, v236\nv_mul_f32 v237, v55, v237\ns_waitcnt vmcnt(15)\n"
"M 112,164,116\nv_exp_f32 v234, v234\nv_exp_f32 v235, v235\nM 128,160,104\nR 180,190,8192\nv_fma_f32 v234, v234, v219, v219\n"
"v_exp_f32 v236, v236\ns_waitcnt vmcnt(13)\nM 128,164,120\nv_exp_f32 v237, v237\nv_fma_f32 v235, v235, v219, v219\nM 144,160,108\n"
"R 182,190,12288\nv_fma_f32 v236, v236, v219, v219\nv_fma_f32 v237, v237, v219, v219\ns_waitcnt vmcnt(9)\nM 144,164,124\nG 4\n"
"v_rcp_f32 v234, v234\nv_rcp_f32 v235, v235\nM 148,168,108\nR 160,191,0\nv_mul_f32 v234, v52, v234\nv_rcp_f32 v236, v236\n"
"M 148,172,124\ns_add_u32 s64, s68, 23074816\nv_rcp_f32 v237, v237\nv_mul_f32 v235, v53, v235\nM 132,168,104\nR 162,191,4096\n"
"v_cmp_eq_u64 s[86:87], s[46:47], v[248:249]\nv_mul_f32 v236, v54, v236\nM 132,172,120\nbuffer_load_dwordx4 v221, s[16:19], s63 offen offset:1024 lds\nv_mul_f32 v237, v55, v237\nM 116,168,100\n"
"R 164,191,8192\ns_and_b32 s62, s86, 0xf\ns_cmp_eq_u32 s62, 0xf\ns_cbranch_scc0 Lq455\nLq456:\nv_cvt_pk_bf16_f32 v50, v234, v235\n"
"M 116,172,116\ns_add_u32 s65, s68, 23189504\nv_cvt_pk_bf16_f32 v51, v236, v237\nv_mul_f32 v230, v56, v56\nv_mul_f32 v231, v57, v57\nM 100,168,96\n"
"R 166,191,12288\nv_mul_f32 v232, v58, v58\nv_mul_f32 v233, v59, v59\nM 100,172,112\nG 4\nv_fma_f32 v230, v230, v218, v217\n"
"v_fma_f32 v231, v231, v218, v217\nM 104,176,96\nR 168,188,16384\nv_fma_f32 v232, v232, v218, v217\nM 104,180,112\nbuffer_load_dwordx4 a[128:131], v252, s[20:23], s64 offen sc1\n"
"M 120,176,100\nR 170,188,20480\nv_fma_f32 v233, v233, v218, v217\nM 120,180,116\ns_add_u32 s66, s69, 23074816\nv_mul_f32 v230, v56, v230\n"
"v_mul_f32 v231, v57, v231\nM 136,176,104\nR 172,188,24576\nv_mul_f32 v232, v58, v232\nM 136,180,120\nbuffer_load_dwordx4 a[132:135], v252, s[20:23], s65 offen sc1\n"
"M 152,176,108\nR 174,188,28672\nv_mul_f32 v233, v59, v233\nM 152,180,124\nG 4\nv_exp_f32 v230, v230\n"
"M 156,160,108\nR 176,189,16384\nv_fma_f32 v230, v230, v219, v219\nM 156,164,124\ns_add_u32 s67, s69, 23189504\nv_exp_f32 v231, v231\n"
"M 140,160,104\nR 178,189,20480\nv_fma_f32 v231, v231, v219, v219\nM 140,164,120\nbuffer_load_dwordx4 a[136:139], v252, s[20:23], s66 offen sc1\nM 124,160,100\n"
"R 180,189,24576\nv_mul_f32 v234, v60, v60\nM 124,164,116\nbuffer_store_dwordx4 v[48:51], v252, s[20:23], s85 offen offset:0 sc1\nM 108,160,96\nR 182,189,28672\n"
"s_add_u32 m0, s50, 24576\nM 108,164,112\ns_waitcnt vmcnt(9)\ns_barrier\nG 4\nM 0,168,64\n"
"R 160,190,16384\ns_add_u32 s63, s58, 131072\nM 0,172,80\nbuffer_load_dwordx4 a[140:143], v252, s[20:23], s67 offen sc1\nM 16,168,68\nR 162,190,20480\n"
"v_mul_f32 v235, v61, v61\nM 16,172,84\ns_add_u32 s64, s68, 23081984\nv_exp_f32 v232, v232\nv_mfma_f32_16x16x32_bf16 a[224:227], v[168:171], a[72:75], a[224:227]\nR 164,190,24576\n"
"buffer_load_dwordx4 v220, s[16:19], s63 offen lds\nv_mfma_f32_16x16x32_bf16 a[224:227], v[172:175], a[88:91], a[224:227]\ns_add_u32 s65, s68, 23196672\nv_exp_f32 v233, v233\nv_mfma_f32_16x16x32_bf16 a[240:243], v[168:171], a[76:79], a[240:243]\nR 166,190,28672\n"
"v_fma_f32 v232, v232, v219, v219\nv_mfma_f32_16x16x32_bf16 a[240:243], v[172:175], a[92:95], a[240:243]\nG 4\nv_rcp_f32 v230, v230\nv_mfma_f32_16x16x32_bf16 a[244:247], v[176:179], a[76:79], a[244:247]\nR 168,191,16384\n"
"buffer_load_dwordx4 v221, s[16:19], s63 offen offset:1024 lds\nv_mfma_f32_16x16x32_bf16 a[244:247], v[180:183], a[92:95], a[244:247]\nbuffer_load_dwordx4 a[144:147], v252, s[20:23], s64 offen sc1\nv_mfma_f32_16x16x32_bf16 a[228:231], v[176:179], a[72:75], a[228:231]\nR 170,191,20480\ns_add_u32 m0, s50, 32768\n"
"v_mfma_f32_16x16x32_bf16 a[228:231], v[180:183], a[88:91], a[228:231]\ns_add_u32 s66, s69, 23081984\ns_add_u32 s63, s58, 655360\nv_fma_f32 v233, v233, v219, v219\nM 20,176,68\nR 172,191,24576\n"
"buffer_load_dwordx4 v220, s[16:19], s63 offen lds\nM 20,180,84\nbuffer_load_dwordx4 a[148:151], v252, s[20:23], s65 offen sc1\nM 4,176,64\nR 174,191,28672\nv_mul_f32 v230, v56, v230\n"
"M 4,180,80\nG 4\nv_rcp_f32 v231, v231\nM 8,160,64\nR 176,188,32768\nbuffer_load_dwordx4 v221, s[16:19], s63 offen offset:1024 lds\n"
"M 8,164,80\ns_add_u32 s67, s69, 23196672\nv_rcp_f32 v232, v232\nM 24,160,68\nR 178,188,36864\nv_mul_f32 v231, v57, v231\n"
"M 24,164,84\nbuffer_load_dwordx4 a[152:155], v252, s[20:23], s66 offen sc1\nv_mfma_f32_16x16x32_bf16 a[232:235], v[160:163], a[72:75], a[232:235]\nR 180,188,40960\nv_mul_f32 v232, v58, v232\nv_mfma_f32_16x16x32_bf16 a[232:235], v[164:167], a[88:91], a[232:235]\n"
"v_rcp_f32 v233, v233\nv_mfma_f32_16x16x32_bf16 a[248:251], v[160:163], a[76:79], a[248:251]\nR 182,188,45056\nv_mul_f32 v233, v59, v233\nv_mfma_f32_16x16x32_bf16 a[248:251], v[164:167], a[92:95], a[248:251]\nG 4\n"
"v_cvt_pk_bf16_f32 v56, v230, v231\nv_cvt_pk_bf16_f32 v57, v232, v233\nv_mfma_f32_16x16x32_bf16 a[252:255], v[168:171], a[76:79], a[252:255]\nR 160,189,32768\nv_mul_f32 v236, v62, v62\nv_mfma_f32_16x16x32_bf16 a[252:255], v[172:175], a[92:95], a[252:255]\n"
"buffer_load_dwordx4 a[156:159], v252, s[20:23], s67 offen sc1\nv_mfma_f32_16x16x32_bf16 a[236:239], v[168:171], a[72:75], a[236:239]\nR 162,189,36864\nv_mul_f32 v237, v63, v63\nv_mfma_f32_16x16x32_bf16 a[236:239], v[172:175], a[88:91], a[236:239]\nv_fma_f32 v234, v234, v218, v217\n"
"v_fma_f32 v235, v235, v218, v217\nM 28,168,68\nR 164,189,40960\nv_fma_f32 v236, v236, v218, v217\nM 28,172,84\ns_add_u32 m0, s50, 40960\n"
"v_fma_f32 v237, v237, v218, v217\nv_mul_f32 v234, v60, v234\nM 12,168,64\nR 166,189,45056\nv_mul_f32 v235, v61, v235\nM 12,172,80\n"
"G 4\nv_exp_f32 v234, v234\nM 0,176,96\nR 168,190,32768\nv_mul_f32 v236, v62, v236\nM 0,180,112\n"
"s_add_u32 s63, s58, 196608\nv_exp_f32 v235, v235\nM 16,176,100\nR 170,190,36864\nv_mul_f32 v237, v63, v237\nM 16,180,116\n"
"buffer_load_dwordx4 v220, s[16:19], s63 offen lds\nv_mfma_f32_16x16x32_bf16 a[224:227], v[176:179], a[104:107], a[224:227]\nR 172,190,40960\nv_fma_f32 v234, v234, v219, v219\nv_mfma_f32_16x16x32_bf16 a[224:227], v[180:183], a[120:123], a[224:227]\nv_exp_f32 v236, v236\n"
"v_mfma_f32_16x16x32_bf16 a[240:243], v[176:179], a[108:111], a[240:243]\nR 174,190,45056\nv_fma_f32 v235, v235, v219, v219\nv_mfma_f32_16x16x32_bf16 a[240:243], v[180:183], a[124:127], a[240:243]\nG 4\nv_exp_f32 v237, v237\n"
"v_mfma_f32_16x16x32_bf16 a[244:247], v[160:163], a[108:111], a[244:247]\nR 176,191,32768\nv_fma_f32 v236, v236, v219, v219\nv_mfma_f32_16x16x32_bf16 a[244:247], v[164:167], a[124:127], a[244:247]\nbuffer_load_dwordx4 v221, s[16:19], s63 offen offset:1024 lds\nv_mfma_f32_16x16x32_bf16 a[228:231], v[160:163], a[104:107], a[228:231]\n"
"R 178,191,36864\ns_add_u32 s63, s58, 720896\nv_mfma_f32_16x16x32_bf16 a[228:231], v[164:167], a[120:123], a[228:231]\ns_add_u32 m0, s50, 49152\nv_rcp_f32 v234, v234\nM 20,160,100\n"
"R 180,191,40960\nv_fma_f32 v237, v237, v219, v219\nM 20,164,116\nbuffer_load_dwordx4 v220, s[16:19], s63 offen lds\nM 4,160,96\nR 182,191,45056\n"
"v_mul_f32 v234, v60, v234\nM 4,164,112\nG 4\nv_rcp_f32 v235, v235\nM 8,168,96\nR 160,188,49152\n"
"v_mul_f32 v235, v61, v235\nM 8,172,112\nbuffer_load_dwordx4 v221, s[16:19], s63 offen offset:1024 lds\nM 24,168,100\nR 162,188,53248\ns_add_u32 s63, s58, 393216\n"
"M 24,172,116\ns_add_u32 m0, s50, 57344\nv_rcp_f32 v236, v236\nv_mfma_f32_16x16x32_bf16 a[232:235], v[168:171], a[104:107], a[232:235]\nR 164,184,0\nv_mul_f32 v236, v62, v236\n"
"v_mfma_f32_16x16x32_bf16 a[232:235], v[172:175], a[120:123], a[232:235]\nbuffer_load_dwordx4 v220, s[16:19], s63 offen lds\nv_mfma_f32_16x16x32_bf16 a[248:251], v[168:171], a[108:111], a[248:251]\nR 166,184,4096\nv_cvt_pk_bf16_f32 v58, v234, v235\nv_mfma_f32_16x16x32_bf16 a[248:251], v[172:175], a[124:127], a[248:251]\n"
"G 4\nv_rcp_f32 v237, v237\nv_mfma_f32_16x16x32_bf16 a[252:255], v[176:179], a[108:111], a[252:255]\nR 168,189,49152\nv_mul_f32 v237, v63, v237\nv_mfma_f32_16x16x32_bf16 a[252:255], v[180:183], a[124:127], a[252:255]\n"
"buffer_load_dwordx4 v221, s[16:19], s63 offen offset:1024 lds\nv_mfma_f32_16x16x32_bf16 a[236:239], v[176:179], a[104:107], a[236:239]\nR 170,189,53248\ns_add_u32 s63, s58, 458752\nv_mfma_f32_16x16x32_bf16 a[236:239], v[180:183], a[120:123], a[236:239]\ns_add_u32 m0, s50, 65536\n"
"v_cvt_pk_bf16_f32 v59, v236, v237\nbuffer_store_dwordx4 v[56:59], v252, s[20:23], s85 offen offset:1024 sc1\nM 28,176,100\nR 172,185,0\nM 28,180,116\nbuffer_load_dwordx4 v220, s[16:19], s63 offen lds\n"
"M 12,176,96\nR 174,185,4096\nM 12,180,112\ns_waitcnt vmcnt(10)\ns_barrier\nG 4\n"
"v_mfma_f32_16x16x32_bf16 v[32:35], v[160:163], a[0:3], 0\nR 176,190,49152\nM 32,164,16\nbuffer_load_dwordx4 v221, s[16:19], s63 offen offset:1024 lds\nv_mfma_f32_16x16x32_bf16 v[48:51], v[160:163], a[4:7], 0\nR 178,190,53248\n"
"M 48,164,20\nv_mfma_f32_16x16x32_bf16 v[64:67], v[160:163], a[8:11], 0\nR 180,186,0\nM 64,164,24\nv_mfma_f32_16x16x32_bf16 v[80:83], v[160:163], a[12:15], 0\nR 182,186,4096\n"
"M 80,164,28\nG 4\nv_mfma_f32_16x16x32_bf16 v[84:87], v[168:171], a[12:15], 0\nR 160,191,49152\nM 84,172,28\nv_mfma_f32_16x16x32_bf16 v[68:71], v[168:171], a[8:11], 0\n"
"R 162,191,53248\nM 68,172,24\nv_mfma_f32_16x16x32_bf16 v[52:55], v[168:171], a[4:7], 0\nR 164,187,0\nM 52,172,20\nv_mfma_f32_16x16x32_bf16 v[36:39], v[168:171], a[0:3], 0\n"
"R 166,187,4096\nM 36,172,16\nG 4\nv_mfma_f32_16x16x32_bf16 v[40:43], v[176:179], a[0:3], 0\nR 168,184,8192\nM 40,180,16\n"
"v_mfma_f32_16x16x32_bf16 v[56:59], v[176:179], a[4:7], 0\nR 170,184,12288\nM 56,180,20\nv_mfma_f32_16x16x32_bf16 v[72:75], v[176:179], a[8:11], 0\nR 172,184,16384\nM 72,180,24\n"
"v_mfma_f32_16x16x32_bf16 v[88:91], v[176:179], a[12:15], 0\nR 174,184,20480\nM 88,180,28\nG 4\nv_mfma_f32_16x16x32_bf16 v[92:95], v[160:163], a[12:15], 0\nR 176,185,8192\n"
"Lpg14:\ns_cbranch_execz LretO0n\nM 92,164,28\nv_mfma_f32_16x16x32_bf16 v[76:79], v[160:163], a[8:11], 0\nR 178,185,12288\nM 76,164,24\n"
"v_mfma_f32_16x16x32_bf16 v[60:63], v[160:163], a[4:7], 0\nR 180,185,16384\nM 60,164,20\nv_mfma_f32_16x16x32_bf16 v[44:47], v[160:163], a[0:3], 0\nR 182,185,20480\nM 44,164,16\n"
"G 4\nM 32,168,32\nR 160,186,8192\nM 32,172,48\nM 48,168,36\nR 162,186,12288\n"
"M 48,172,52\nM 64,168,40\nR 164,186,16384\nM 64,172,56\nM 80,168,44\nR 166,186,20480\n"
"M 80,172,60\nG 4\nM 84,176,44\nR 168,187,8192\nM 84,180,60\nM 68,176,40\n"
"R 170,187,12288\nv_mov_b32 v226, s46\nM 68,180,56\ns_add_u32 m0, s50, 73728\nv_mov_b32 v227, s47\nM 52,176,36\n"
"R 172,187,16384\ns_add_u32 s61, s81, 385040\ns_lshl_b32 s62, s44, 5\ns_add_u32 s61, s61, s62\ns_waitcnt vmcnt(0)\ns_mov_b64 exec, 1\n"
"buffer_atomic_swap_x2 v[226:227], off, s[20:23], s61 sc1\ns_mov_b64 exec, -1\nM 52,180,52\ns_add_u32 s63, s56, 393216\nM 36,176,32\nR 174,187,20480\n"
"M 36,180,48\nG 4\nM 40,160,32\nR 176,184,24576\nM 40,164,48\nbuffer_load_dwordx4 v220, s[16:19], s63 offen lds\n"
"M 56,160,36\nR 178,184,28672\nM 56,164,52\nM 72,160,40\nR 180,184,32768\nM 72,164,56\n"
"buffer_load_dwordx4 v221, s[16:19], s63 offen offset:1024 lds\nM 88,160,44\nR 182,184,36864\ns_add_u32 m0, s50, 81920\nM 88,164,60\nG 4\n"
"M 92,168,44\nR 160,185,24576\ns_add_u32 s63, s56, 458752\nM 92,172,60\nbuffer_load_dwordx4 v220, s[16:19], s63 offen lds\nM 76,168,40\n"
"R 162,185,28672\nM 76,172,56\nM 60,168,36\nR 164,185,32768\nM 60,172,52\nbuffer_load_dwordx4 v221, s[16:19], s63 offen offset:1024 lds\n"
"M 44,168,32\nR 166,185,36864\ns_add_u32 m0, s50, 90112\nM 44,172,48\ns_barrier\nG 4\n"
"M 32,176,64\nR 168,186,24576\ns_add_u32 s63, s57, 393216\nM 32,180,80\nbuffer_load_dwordx4 v220, s[16:19], s63 offen lds\nM 48,176,68\n"
"R 170,186,28672\nM 48,180,84\nM 64,176,72\nR 172,186,32768\nM 64,180,88\nM 80,176,76\n"
"R 174,186,36864\nM 80,180,92\nG 4\nM 84,160,76\nR 176,187,24576\nM 84,164,92\n"
"buffer_load_dwordx4 v221, s[16:19], s63 offen offset:1024 lds\nM 68,160,72\nR 178,187,28672\nM 68,164,88\ns_add_u32 m0, s50, 98304\nM 52,160,68\n"
"R 180,187,32768\nv_mov_b32 v248, 0\nM 52,164,84\ns_add_u32 s63, s57, 458752\nv_mov_b32 v249, 0\nM 36,160,64\n"
"R 182,187,36864\ns_add_u32 s61, s82, 385040\ns_mov_b64 exec, 0xf\nbuffer_atomic_or_x2 v[248:249], v250, s[20:23], s61 offen sc0 sc1\ns_mov_b64 exec, -1\nM 36,164,80\n"
"G 4\nM 40,168,64\nR 160,184,40960\nM 40,172,80\nbuffer_load_dwordx4 v220, s[16:19], s63 offen lds\nM 56,168,68\n"
"R 162,184,45056\nM 56,172,84\nM 72,168,72\nR 164,184,49152\nM 72,172,88\nbuffer_load_dwordx4 v221, s[16:19], s63 offen offset:1024 lds\n"
"M 88,168,76\nR 166,184,53248\ns_add_u32 s63, s56, 262144\nM 88,172,92\nG 4\nM 92,176,76\n"
"R 168,185,40960\nM 92,180,92\ns_add_u32 m0, s50, 106496\nM 76,176,72\nR 170,185,45056\nM 76,180,88\n"
"buffer_load_dwordx4 v220, s[16:19], s63 offen lds\nM 60,176,68\nR 172,185,49152\nM 60,180,84\nM 44,176,64\nR 174,185,53248\n"
"M 44,180,80\nG 4\nM 32,160,96\nR 176,186,40960\nM 32,164,112\nbuffer_load_dwordx4 v221, s[16:19], s63 offen offset:1024 lds\n"
"M 48,160,100\nR 178,186,45056\ns_add_u32 s63, s56, 786432\nM 48,164,116\ns_add_u32 m0, s50, 114688\nM 64,160,104\n"
"R 180,186,49152\nM 64,164,120\nbuffer_load_dwordx4 v220, s[16:19], s63 offen lds\nM 80,160,108\nR 182,186,53248\nM 80,164,124\n"
"G 4\nM 84,168,108\nR 160,187,40960\nM 84,172,124\nbuffer_load_dwordx4 v221, s[16:19], s63 offen offset:1024 lds\nM 68,168,104\n"
"R 162,187,45056\ns_add_u32 s63, s56, 327680\nM 68,172,120\ns_add_u32 m0, s50, 0\nM 52,168,100\nR 164,187,49152\n"
"M 52,172,116\nbuffer_load_dwordx4 v220, s[16:19], s63 offen lds\nM 36,168,96\nR 166,187,53248\nM 36,172,112\nG 4\n"
"M 40,176,96\nR 168,184,57344\nM 40,180,112\nbuffer_load_dwordx4 v221, s[16:19], s63 offen offset:1024 lds\nM 56,176,100\nR 170,184,61440\n"
"M 56,180,116\nM 72,176,104\nR 172,188,0\nM 72,180,120\ns_add_u32 m0, s50, 8192\nM 88,176,108\n"
"R 174,188,4096\ns_waitcnt vmcnt(11)\ns_barrier\nM 88,180,124\nG 4\nM 92,160,108\n"
"R 176,185,57344\ns_add_u32 s63, s56, 851968\nM 92,164,124\ns_add_u32 s64, s68, 23072768\nM 76,160,104\nR 178,185,61440\n"
"M 76,164,120\nbuffer_load_dwordx4 v220, s[16:19], s63 offen lds\nM 60,160,100\nR 180,189,0\ns_waitcnt vmcnt(9)\nv_cmp_eq_u64 s[86:87], s[46:47], v[248:249]\n"
"M 60,164,116\ns_add_u32 s65, s68, 23187456\nM 44,160,96\nR 182,189,4096\ns_and_b32 s62, s86, 0xf\ns_cmp_eq_u32 s62, 0xf\n"
"s_cbranch_scc0 Lq474\nLq475:\nM 44,164,112\nG 4\nM 32,168,128\nR 160,186,57344\n"
"M 32,172,144\nbuffer_load_dwordx4 a[160:163], v252, s[20:23], s64 offen sc1\nM 48,168,132\nR 162,186,61440\nM 48,172,148\ns_add_u32 s66, s69, 23072768\n"
"M 64,168,136\nR 164,190,0\nM 64,172,152\nbuffer_load_dwordx4 a[164:167], v252, s[20:23], s65 offen sc1\nM 80,168,140\nR 166,190,4096\n"
"M 80,172,156\nG 4\nM 84,176,140\nR 168,187,57344\nM 84,180,156\ns_add_u32 s67, s69, 23187456\n"
"M 68,176,136\nR 170,187,61440\nM 68,180,152\nbuffer_load_dwordx4 a[168:171], v252, s[20:23], s66 offen sc1\nM 52,176,132\nR 172,191,0\n"
"M 52,180,148\ns_add_u32 s64, s68, 23079936\nM 36,176,128\nR 174,191,4096\nM 36,180,144\nbuffer_load_dwordx4 v221, s[16:19], s63 offen offset:1024 lds\n"
"G 4\nM 40,160,128\nR 176,188,8192\ns_add_u32 m0, s50, 16384\nM 40,164,144\nbuffer_load_dwordx4 a[172:175], v252, s[20:23], s67 offen sc1\n"
"M 56,160,132\nR 178,188,12288\ns_add_u32 s63, s57, 262144\nM 56,164,148\ns_add_u32 s65, s68, 23194624\nbuffer_load_dwordx4 v220, s[16:19], s63 offen lds\n"
"M 72,160,136\nR 180,188,16384\nLq587:\ns_lshr_b32 s60, s45, 3\ns_lshl2_add_u32 s60, s60, s40\ns_lshl_b32 s60, s60, 8\n"
"v_lshl_add_u32 v224, v252, 3, s60\nv_add_u32 v224, Lq587+4096-LqPC, v224\nv_min_u32 v224, LqE-LqPC-4, v224\ns_mov_b64 exec, 3\nglobal_load_dword v224, v224, s[98:99]\ns_mov_b64 exec, -1\n"
"M 72,164,152\nbuffer_load_dwordx4 a[176:179], v252, s[20:23], s64 offen sc1\nM 88,160,140\nR 182,188,20480\nM 88,164,156\nbuffer_load_dwordx4 v221, s[16:19], s63 offen offset:1024 lds\n"
"G 4\nM 92,168,140\nR 160,189,8192\ns_add_u32 m0, s50, 24576\nM 92,172,156\ns_add_u32 s66, s69, 23079936\n"
"M 76,168,136\nR 162,189,12288\ns_add_u32 s63, s57, 786432\nM 76,172,152\nbuffer_load_dwordx4 a[180:183], v252, s[20:23], s65 offen sc1\nbuffer_load_dwordx4 v220, s[16:19], s63 offen lds\n"
"M 60,168,132\nR 164,189,16384\nM 60,172,148\ns_add_u32 s67, s69, 23194624\nM 44,168,128\nR 166,189,20480\n"
"M 44,172,144\nbuffer_load_dwordx4 v221, s[16:19], s63 offen offset:1024 lds\nG 4\nM 96,176,128\nR 168,190,8192\ns_add_u32 m0, s50, 32768\n"
"M 96,180,144\nbuffer_load_dwordx4 a[184:187], v252, s[20:23], s66 offen sc1\nM 112,176,132\nR 170,190,12288\ns_add_u32 s63, s57, 327680\nM 112,180,148\n"
"s_add_u32 s64, s68, 23073792\nbuffer_load_dwordx4 v220, s[16:19], s63 offen lds\nM 128,176,136\nR 172,190,16384\nM 128,180,152\nbuffer_load_dwordx4 a[188:191], v252, s[20:23], s67 offen sc1\n"
"M 144,176,140\nR 174,190,20480\nM 144,180,156\nbuffer_load_dwordx4 v221, s[16:19], s63 offen offset:1024 lds\nG 4\nM 148,160,140\n"
"R 176,191,8192\ns_waitcnt vmcnt(19)\ns_barrier\nM 148,164,156\ns_add_u32 s65, s68, 23188480\ns_add_u32 s63, s57, 851968\n"
"M 132,160,136\nR 178,191,12288\ns_add_u32 m0, s50, 40960\nM 132,164,152\nbuffer_load_dwordx4 a[192:195], v252, s[20:23], s64 offen sc1\nM 116,160,132\n"
"R 180,191,16384\nbuffer_load_dwordx4 v220, s[16:19], s63 offen lds\nM 116,164,148\ns_add_u32 s66, s69, 23073792\nM 100,160,128\nR 182,191,20480\n"
"M 100,164,144\nG 4\nM 104,168,128\nR 160,188,24576\nbuffer_load_dwordx4 v221, s[16:19], s63 offen offset:1024 lds\nM 104,172,144\n"
"buffer_load_dwordx4 a[196:199], v252, s[20:23], s65 offen sc1\nM 120,168,132\nR 162,188,28672\nM 120,172,148\ns_add_u32 s67, s69, 23188480\nM 136,168,136\n"
"R 164,188,32768\nM 136,172,152\nbuffer_load_dwordx4 a[200:203], v252, s[20:23], s66 offen sc1\nM 152,168,140\nR 166,188,36864\nM 152,172,156\n"
"G 4\nM 156,176,140\nR 168,189,24576\nM 156,180,156\nbuffer_load_dwordx4 a[204:207], v252, s[20:23], s67 offen sc1\nM 140,176,136\n"
"R 170,189,28672\nM 140,180,152\ns_add_u32 s64, s68, 23080960\nM 124,176,132\nR 172,189,32768\nM 124,180,148\n"
"s_add_u32 s65, s68, 23195648\nM 108,176,128\nR 174,189,36864\nM 108,180,144\nG 4\nM 0,160,128\n"
"R 176,190,24576\nM 0,164,144\nbuffer_load_dwordx4 a[208:211], v252, s[20:23], s64 offen sc1\nM 16,160,132\nR 178,190,28672\nM 16,164,148\n"
"s_add_u32 s66, s69, 23080960\nv_mfma_f32_16x16x32_bf16 a[224:227], v[160:163], a[136:139], a[224:227]\nR 180,190,32768\nv_mfma_f32_16x16x32_bf16 a[224:227], v[164:167], a[152:155], a[224:227]\nbuffer_load_dwordx4 a[212:215], v252, s[20:23], s65 offen sc1\nv_mfma_f32_16x16x32_bf16 a[240:243], v[160:163], a[140:143], a[240:243]\n"
"R 182,190,36864\nv_mfma_f32_16x16x32_bf16 a[240:243], v[164:167], a[156:159], a[240:243]\nG 4\nv_mfma_f32_16x16x32_bf16 a[244:247], v[168:171], a[140:143], a[244:247]\nR 160,191,24576\nv_mfma_f32_16x16x32_bf16 a[244:247], v[172:175], a[156:159], a[244:247]\n"
"buffer_load_dwordx4 a[216:219], v252, s[20:23], s66 offen sc1\nv_mfma_f32_16x16x32_bf16 a[228:231], v[168:171], a[136:139], a[228:231]\nR 162,191,28672\nv_mfma_f32_16x16x32_bf16 a[228:231], v[172:175], a[152:155], a[228:231]\ns_add_u32 s67, s69, 23195648\nM 20,168,132\n"
"R 164,191,32768\nM 20,172,148\nM 4,168,128\nR 166,191,36864\nM 4,172,144\nG 4\n"
"M 8,176,128\nR 168,188,40960\nM 8,180,144\nbuffer_load_dwordx4 a[220:223], v252, s[20:23], s67 offen sc1\nM 24,176,132\nR 170,188,45056\n"
"M 24,180,148\ns_add_u32 m0, s50, 49152\nv_mfma_f32_16x16x32_bf16 a[232:235], v[176:179], a[136:139], a[232:235]\nR 172,188,49152\nv_mfma_f32_16x16x32_bf16 a[232:235], v[180:183], a[152:155], a[232:235]\ns_add_u32 s63, s58, 262144\n"
"v_mfma_f32_16x16x32_bf16 a[248:251], v[176:179], a[140:143], a[248:251]\nR 174,188,53248\nv_mfma_f32_16x16x32_bf16 a[248:251], v[180:183], a[156:159], a[248:251]\nG 4\nv_mfma_f32_16x16x32_bf16 a[252:255], v[160:163], a[140:143], a[252:255]\nR 176,189,40960\n"
"v_mfma_f32_16x16x32_bf16 a[252:255], v[164:167], a[156:159], a[252:255]\nbuffer_load_dwordx4 v220, s[16:19], s63 offen lds\nv_mfma_f32_16x16x32_bf16 a[236:239], v[160:163], a[136:139], a[236:239]\nR 178,189,45056\nv_mfma_f32_16x16x32_bf16 a[236:239], v[164:167], a[152:155], a[236:239]\nM 28,160,132\n"
"R 180,189,49152\nM 28,164,148\nbuffer_load_dwordx4 v221, s[16:19], s63 offen offset:1024 lds\nM 12,160,128\nR 182,189,53248\nM 12,164,144\n"
"s_waitcnt vmcnt(9)\ns_barrier\nG 4\nM 96,168,160\nR 160,190,40960\nM 96,172,176\n"
"s_add_u32 m0, s50, 57344\nM 112,168,164\nR 162,190,45056\nM 112,172,180\ns_add_u32 s63, s58, 786432\nM 128,168,168\n"
"R 164,190,49152\nv_lshrrev_b32 v230, 8, v252\nM 128,172,184\nbuffer_load_dwordx4 v220, s[16:19], s63 offen lds\nM 144,168,172\nR 166,190,53248\n"
"v_lshlrev_b32 v230, 3, v230\nM 144,172,188\nG 4\nM 148,176,172\nR 168,191,40960\nM 148,180,188\n"
"buffer_load_dwordx2 v[232:233], v230, s[36:39], s53 offen offset:0\nM 132,176,168\nR 170,191,45056\nM 132,180,184\nbuffer_load_dwordx2 v[234:235], v230, s[36:39], s53 offen offset:32\nM 116,176,164\n"
"R 172,191,49152\nM 116,180,180\nbuffer_load_dwordx4 v221, s[16:19], s63 offen offset:1024 lds\nM 100,176,160\nR 174,191,53248\nM 100,180,176\n"
"G 4\nM 104,160,160\nR 176,184,0\nM 104,164,176\ns_add_u32 m0, s50, 65536\nM 120,160,164\n"
"R 178,184,4096\nM 120,164,180\ns_add_u32 s63, s58, 327680\nM 136,160,168\nR 180,184,8192\nbuffer_load_dwordx2 v[236:237], v230, s[36:39], s53 offen offset:64\n"
"M 136,164,184\nbuffer_load_dwordx4 v220, s[16:19], s63 offen lds\nM 152,160,172\nR 182,184,12288\nM 152,164,188\nbuffer_load_dwordx2 v[238:239], v230, s[36:39], s53 offen offset:96\n"
"G 4\nM 156,168,172\nR 160,185,0\nv_bfe_u32 v242, v252, 4, 4\nM 156,172,188\nv_lshrrev_b32 v243, 8, v252\n"
"v_lshlrev_b32 v243, 2, v243\nM 140,168,168\nR 162,185,4096\nv_sub_u32 v242, v242, v243\nM 140,172,184\nv_lshlrev_b32 v243, 4, v242\n"
"s_mov_b64 s[60:61], 0x3f80\nv_lshlrev_b64 v[208:209], v243, s[60:61]\nM 124,168,164\nR 164,185,8192\nM 124,172,180\nbuffer_load_dwordx4 v221, s[16:19], s63 offen offset:1024 lds\n"
"M 108,168,160\nR 166,185,12288\nM 108,172,176\nG 4\nM 96,176,192\nR 168,186,0\n"
"s_waitcnt vmcnt(13)\nM 96,180,208\ns_add_u32 m0, s50, 73728\nM 112,176,196\nR 170,186,4096\ns_waitcnt vmcnt(12)\n"
"M 112,180,212\ns_add_u32 s63, s58, 851968\nM 128,176,200\nR 172,186,8192\ns_waitcnt vmcnt(11)\nM 128,180,216\n"
"buffer_load_dwordx4 v220, s[16:19], s63 offen lds\nM 144,176,204\nR 174,186,12288\ns_waitcnt vmcnt(11)\nM 144,180,220\nG 4\n"
"M 148,160,204\nR 176,187,0\nM 148,164,220\nv_cmp_gt_u32 s[60:61], 4, v242\ns_nop 1\nv_cndmask_b32 v212, 0, v208, s[60:61]\n"
"v_cndmask_b32 v208, 0, v208, s[60:61]\nv_cndmask_b32 v213, 0, v209, s[60:61]\nv_cndmask_b32 v209, 0, v209, s[60:61]\nM 132,160,200\nLpg15:\ns_cbranch_execz LretO0n\n"
"R 178,187,4096\nM 132,164,216\nM 116,160,196\nR 180,187,8192\nM 116,164,212\nbuffer_load_dwordx4 v221, s[16:19], s63 offen offset:1024 lds\n"
"M 100,160,192\nR 182,187,12288\nM 100,164,208\nG 4\nM 104,168,192\nR 160,184,16384\n"
"M 104,172,208\ns_add_u32 m0, s50, 81920\nM 120,168,196\nR 162,184,20480\nM 120,172,212\ns_add_u32 s63, s59, 0\n"
"M 136,168,200\nR 164,184,24576\nM 136,172,216\nbuffer_load_dwordx4 v220, s[16:19], s63 offen lds\nM 152,168,204\nR 166,184,28672\n"
"M 152,172,220\nG 4\nM 156,176,204\nR 168,185,16384\nM 156,180,220\nM 140,176,200\n"
"R 170,185,20480\nM 140,180,216\nbuffer_load_dwordx4 v221, s[16:19], s63 offen offset:1024 lds\nM 124,176,196\nR 172,185,24576\nM 124,180,212\n"
"M 108,176,192\nR 174,185,28672\nM 108,180,208\nG 4\nM 0,160,160\nR 176,186,16384\n"
"M 0,164,176\ns_mov_b32 m0, s51\nM 16,160,164\nR 178,186,20480\nv_cvt_pk_bf16_f32 v128, v128, v129\nv_cvt_pk_bf16_f32 v129, v130, v131\n"
"M 16,164,180\nds_write_addtid_b32 v128 offset:0\nv_cvt_pk_bf16_f32 v130, v132, v133\nv_cvt_pk_bf16_f32 v131, v134, v135\nv_mfma_f32_16x16x32_bf16 a[224:227], v[160:163], a[168:171], a[224:227]\nR 180,186,24576\n"
"v_cvt_pk_bf16_f32 v132, v136, v137\nv_mfma_f32_16x16x32_bf16 a[224:227], v[164:167], a[184:187], a[224:227]\nds_write_addtid_b32 v129 offset:256\nv_cvt_pk_bf16_f32 v133, v138, v139\nv_mfma_f32_16x16x32_bf16 a[240:243], v[160:163], a[172:175], a[240:243]\nR 182,186,28672\n"
"v_cvt_pk_bf16_f32 v134, v140, v141\nv_mfma_f32_16x16x32_bf16 a[240:243], v[164:167], a[188:191], a[240:243]\nds_write_addtid_b32 v130 offset:512\nG 7\nv_cvt_pk_bf16_f32 v135, v142, v143\nv_mfma_f32_16x16x32_bf16 a[244:247], v[168:171], a[172:175], a[244:247]\n"
"R 160,187,16384\nv_cvt_pk_bf16_f32 v136, v144, v145\nv_mfma_f32_16x16x32_bf16 a[244:247], v[172:175], a[188:191], a[244:247]\nds_write_addtid_b32 v131 offset:768\nv_cvt_pk_bf16_f32 v137, v146, v147\nv_mfma_f32_16x16x32_bf16 a[228:231], v[168:171], a[168:171], a[228:231]\n"
"R 162,187,20480\nv_cvt_pk_bf16_f32 v138, v148, v149\nv_mfma_f32_16x16x32_bf16 a[228:231], v[172:175], a[184:187], a[228:231]\nds_write_addtid_b32 v132 offset:1024\nv_cvt_pk_bf16_f32 v139, v150, v151\nM 20,168,164\n"
"R 164,187,24576\nv_cvt_pk_bf16_f32 v140, v152, v153\nM 20,172,180\nds_write_addtid_b32 v133 offset:1280\nv_cvt_pk_bf16_f32 v141, v154, v155\nM 4,168,160\n"
"R 166,187,28672\nv_cvt_pk_bf16_f32 v142, v156, v157\nM 4,172,176\nds_write_addtid_b32 v134 offset:1536\nG 9\nv_cvt_pk_bf16_f32 v143, v158, v159\n"
"M 8,176,160\nR 168,184,32768\nds_write_addtid_b32 v135 offset:1792\nM 8,180,176\nds_write_addtid_b32 v136 offset:2048\nds_write_addtid_b32 v137 offset:2304\n"
"M 24,176,164\nR 170,184,36864\nds_write_addtid_b32 v138 offset:2560\nM 24,180,180\nds_write_addtid_b32 v139 offset:2816\nds_write_addtid_b32 v140 offset:3072\n"
"v_mfma_f32_16x16x32_bf16 a[232:235], v[176:179], a[168:171], a[232:235]\nR 172,184,40960\nds_write_addtid_b32 v141 offset:3328\nv_mfma_f32_16x16x32_bf16 a[232:235], v[180:183], a[184:187], a[232:235]\nds_write_addtid_b32 v142 offset:3584\nds_write_addtid_b32 v143 offset:3840\n"
"v_mfma_f32_16x16x32_bf16 a[248:251], v[176:179], a[172:175], a[248:251]\nR 174,184,45056\ns_add_u32 m0, s50, 90112\nv_mfma_f32_16x16x32_bf16 a[248:251], v[180:183], a[188:191], a[248:251]\ns_add_u32 s63, s59, 524288\nG 14\n"
"v_mfma_f32_16x16x32_bf16 a[252:255], v[160:163], a[172:175], a[252:255]\nR 176,185,32768\ns_waitcnt vmcnt(8) lgkmcnt(2)\ns_barrier\nv_mfma_f32_16x16x32_bf16 a[252:255], v[164:167], a[188:191], a[252:255]\nbuffer_load_dwordx4 v220, s[16:19], s63 offen lds\n"
"v_mfma_f32_16x16x32_bf16 a[236:239], v[160:163], a[168:171], a[236:239]\nR 178,185,36864\nv_mfma_f32_16x16x32_bf16 a[236:239], v[164:167], a[184:187], a[236:239]\nds_read2st64_b32 v[128:129], v225 offset0:0 offset1:1\nM 28,160,164\nR 180,185,40960\n"
"M 28,164,180\nbuffer_load_dwordx4 v221, s[16:19], s63 offen offset:1024 lds\nM 12,160,160\nR 182,185,45056\ns_add_u32 m0, s50, 98304\nM 12,164,176\n"
"ds_read2st64_b32 v[130:131], v225 offset0:2 offset1:3\nG 6\nM 0,168,192\nR 160,186,32768\ns_add_u32 s63, s59, 65536\nM 0,172,208\n"
"buffer_load_dwordx4 v220, s[16:19], s63 offen lds\nds_read2st64_b32 v[132:133], v225 offset0:4 offset1:5\nM 16,168,196\nR 162,186,36864\nds_read2st64_b32 v[134:135], v225 offset0:6 offset1:7\nM 16,172,212\n"
"ds_read2st64_b32 v[136:137], v225 offset0:8 offset1:9\nv_mfma_f32_16x16x32_bf16 a[224:227], v[168:171], a[200:203], a[224:227]\nR 164,186,40960\nv_mfma_f32_16x16x32_bf16 a[224:227], v[172:175], a[216:219], a[224:227]\nbuffer_load_dwordx4 v221, s[16:19], s63 offen offset:1024 lds\nG 6\n"
"v_mfma_f32_16x16x32_bf16 v[96:99], v[208:211], v[128:131], v[96:99]\nv_mfma_f32_16x16x32_bf16 v[100:103], v[210:213], v[128:131], v[100:103]\nds_read2st64_b32 v[138:139], v225 offset0:10 offset1:11\nv_mfma_f32_16x16x32_bf16 a[240:243], v[168:171], a[204:207], a[240:243]\nR 166,186,45056\ns_add_u32 m0, s50, 106496\n"
"v_mfma_f32_16x16x32_bf16 a[240:243], v[172:175], a[220:223], a[240:243]\nds_read2st64_b32 v[140:141], v225 offset0:12 offset1:13\nv_mfma_f32_16x16x32_bf16 a[244:247], v[176:179], a[204:207], a[244:247]\nR 168,187,32768\ns_add_u32 s63, s59, 589824\nG 6\n"
"v_mfma_f32_16x16x32_bf16 v[104:107], v[208:211], v[132:135], v[104:107]\nv_mfma_f32_16x16x32_bf16 v[108:111], v[210:213], v[132:135], v[108:111]\nds_read2st64_b32 v[142:143], v225 offset0:14 offset1:15\nv_mfma_f32_16x16x32_bf16 a[244:247], v[180:183], a[220:223], a[244:247]\nbuffer_load_dwordx4 v220, s[16:19], s63 offen lds\nv_mfma_f32_16x16x32_bf16 a[228:231], v[176:179], a[200:203], a[228:231]\n"
"R 170,187,36864\nv_mfma_f32_16x16x32_bf16 a[228:231], v[180:183], a[216:219], a[228:231]\nG 5\nv_mfma_f32_16x16x32_bf16 v[112:115], v[208:211], v[136:139], v[112:115]\nv_mfma_f32_16x16x32_bf16 v[116:119], v[210:213], v[136:139], v[116:119]\nM 20,176,196\n"
"R 172,187,40960\nM 20,180,212\nM 4,176,192\nR 174,187,45056\nG 3\nv_mfma_f32_16x16x32_bf16 v[120:123], v[208:211], v[140:143], v[120:123]\n"
"v_mfma_f32_16x16x32_bf16 v[124:127], v[210:213], v[140:143], v[124:127]\nM 4,180,208\nM 8,160,192\nR 176,184,49152\nM 8,164,208\nbuffer_load_dwordx4 v221, s[16:19], s63 offen offset:1024 lds\n"
"M 24,160,196\nR 178,184,53248\ns_add_u32 s63, s59, 131072\nM 24,164,212\ns_add_u32 m0, s50, 114688\nv_mfma_f32_16x16x32_bf16 a[232:235], v[160:163], a[200:203], a[232:235]\n"
"R 180,184,57344\nv_cvt_pk_bf16_f32 v120, v120, v121\nv_cvt_pk_bf16_f32 v96, v96, v97\nv_cvt_pk_bf16_f32 v112, v112, v113\nv_cvt_pk_bf16_f32 v121, v122, v123\nv_mfma_f32_16x16x32_bf16 a[232:235], v[164:167], a[216:219], a[232:235]\n"
"buffer_load_dwordx4 v220, s[16:19], s63 offen lds\nv_cvt_pk_bf16_f32 v113, v114, v115\nv_cvt_pk_bf16_f32 v122, v124, v125\nv_mfma_f32_16x16x32_bf16 a[248:251], v[160:163], a[204:207], a[248:251]\nR 182,184,61440\nv_cvt_pk_bf16_f32 v123, v126, v127\n"
"v_cvt_pk_bf16_f32 v97, v98, v99\nv_cvt_pk_bf16_f32 v114, v116, v117\nv_mfma_f32_16x16x32_bf16 a[248:251], v[164:167], a[220:223], a[248:251]\nG 4\nv_cvt_pk_bf16_f32 v98, v100, v101\nv_cvt_pk_bf16_f32 v99, v102, v103\n"
"v_mfma_f32_16x16x32_bf16 a[252:255], v[168:171], a[204:207], a[252:255]\nR 160,185,49152\ns_bfe_u32 s100, s56, 0x20007\ns_mul_i32 s100, s100, 49152\ns_add_u32 s100, s100, s72\ns_add_u32 s100, s100, 52428800\n"
"v_cvt_pk_bf16_f32 v115, v118, v119\nv_mfma_f32_16x16x32_bf16 a[252:255], v[172:175], a[220:223], a[252:255]\nbuffer_store_dwordx4 v[96:99], v252, s[20:23], s100 offen offset:0 sc1\nv_mfma_f32_16x16x32_bf16 a[236:239], v[168:171], a[200:203], a[236:239]\nR 162,185,53248\nv_cvt_pk_bf16_f32 v104, v104, v105\n"
"v_mfma_f32_16x16x32_bf16 a[236:239], v[172:175], a[216:219], a[236:239]\nv_cvt_pk_bf16_f32 v105, v106, v107\nv_cvt_pk_bf16_f32 v106, v108, v109\nM 28,168,196\nR 164,185,57344\nv_cvt_pk_bf16_f32 v107, v110, v111\n"
"M 28,172,212\nbuffer_load_dwordx4 v221, s[16:19], s63 offen offset:1024 lds\nM 12,168,192\nR 166,185,61440\nM 12,172,208\nbuffer_store_dwordx4 v[104:107], v252, s[20:23], s100 offen offset:1024 sc1\n"
"s_waitcnt vmcnt(12)\ns_barrier\nG 4\nM 32,176,160\nR 168,186,49152\nM 32,180,176\n"
"v_xor_b32 v240, 0x2000, v225\nM 48,176,164\nR 170,186,53248\nds_write2st64_b32 v240, a224, a225 offset0:0 offset1:1\nM 48,180,180\nbuffer_store_dwordx4 v[112:115], v252, s[20:23], s100 offen offset:2048 sc1\n"
"ds_write2st64_b32 v240, a226, a227 offset0:2 offset1:3\nM 64,176,168\nR 172,186,57344\nds_write2st64_b32 v240, a228, a229 offset0:4 offset1:5\nM 64,180,184\nds_write2st64_b32 v240, a230, a231 offset0:6 offset1:7\n"
"M 80,176,172\nR 174,186,61440\nds_write2st64_b32 v240, a232, a233 offset0:8 offset1:9\nM 80,180,188\nds_write2st64_b32 v240, a234, a235 offset0:10 offset1:11\nG 10\n"
"M 84,160,172\nR 176,187,49152\ns_add_u32 s63, s59, 655360\nds_write2st64_b32 v240, a236, a237 offset0:12 offset1:13\nM 84,164,188\nbuffer_store_dwordx4 v[120:123], v252, s[20:23], s100 offen offset:3072 sc1\n"
"ds_write2st64_b32 v240, a238, a239 offset0:14 offset1:15\nM 68,160,168\nR 178,187,53248\nv_mfma_f32_16x16x32_bf16 v[96:99], v[208:211], v[192:195], 0\nds_write2st64_b32 v240, a240, a241 offset0:16 offset1:17\nM 68,164,184\n"
"v_mfma_f32_16x16x32_bf16 v[100:103], v[210:213], v[192:195], 0\ns_add_u32 m0, s50, 0\nds_write2st64_b32 v240, a242, a243 offset0:18 offset1:19\nM 52,160,164\nR 180,187,57344\nv_mfma_f32_16x16x32_bf16 v[104:107], v[208:211], v[196:199], 0\n"
"ds_write2st64_b32 v240, a244, a245 offset0:20 offset1:21\nM 52,164,180\nv_mfma_f32_16x16x32_bf16 v[108:111], v[210:213], v[196:199], 0\nbuffer_load_dwordx4 v220, s[16:19], s63 offen lds\nds_write2st64_b32 v240, a246, a247 offset0:22 offset1:23\nM 36,160,160\n"
"R 182,187,61440\nv_mfma_f32_16x16x32_bf16 v[112:115], v[208:211], v[200:203], 0\nds_write2st64_b32 v240, a248, a249 offset0:24 offset1:25\nM 36,164,176\nv_mfma_f32_16x16x32_bf16 v[116:119], v[210:213], v[200:203], 0\nds_write2st64_b32 v240, a250, a251 offset0:26 offset1:27\n"
"G 14\nM 40,168,160\nR 160,188,0\nv_mfma_f32_16x16x32_bf16 v[120:123], v[208:211], v[204:207], 0\nds_write2st64_b32 v240, a252, a253 offset0:28 offset1:29\nM 40,172,176\n"
"v_mfma_f32_16x16x32_bf16 v[124:127], v[210:213], v[204:207], 0\nbuffer_load_dwordx4 v221, s[16:19], s63 offen offset:1024 lds\nds_write2st64_b32 v240, a254, a255 offset0:30 offset1:31\nM 56,168,164\nR 162,188,4096\nv_mfma_f32_16x16x32_bf16 v[96:99], v[208:211], v[232:235], v[96:99]\n"
"s_add_u32 m0, s50, 8192\ns_add_u32 s63, s59, 196608\nM 56,172,180\nv_mfma_f32_16x16x32_bf16 v[100:103], v[210:213], v[232:235], v[100:103]\nbuffer_load_dwordx4 v220, s[16:19], s63 offen lds\nM 72,168,168\n"
"R 164,188,8192\nv_mfma_f32_16x16x32_bf16 v[104:107], v[208:211], v[236:239], v[104:107]\nM 72,172,184\nv_mfma_f32_16x16x32_bf16 v[108:111], v[210:213], v[236:239], v[108:111]\nbuffer_load_dwordx4 v221, s[16:19], s63 offen offset:1024 lds\nM 88,168,172\n"
"R 166,188,12288\nv_mfma_f32_16x16x32_bf16 v[112:115], v[208:211], v[232:235], v[112:115]\ns_add_u32 m0, s50, 16384\nM 88,172,188\nv_mfma_f32_16x16x32_bf16 v[116:119], v[210:213], v[232:235], v[116:119]\ns_add_u32 s63, s59, 720896\n"
"G 8\nM 92,176,172\nR 168,189,0\nv_mfma_f32_16x16x32_bf16 v[120:123], v[208:211], v[236:239], v[120:123]\ns_waitcnt vmcnt(14) lgkmcnt(4)\ns_barrier\n"
"M 92,180,188\nv_mfma_f32_16x16x32_bf16 v[124:127], v[210:213], v[236:239], v[124:127]\nbuffer_load_dwordx4 v220, s[16:19], s63 offen lds\nds_read2st64_b32 v[128:129], v225 offset0:0 offset1:1\nM 76,176,168\nR 170,189,4096\n"
"ds_read2st64_b32 v[130:131], v225 offset0:2 offset1:3\nM 76,180,184\nds_read2st64_b32 v[132:133], v225 offset0:4 offset1:5\nds_read2st64_b32 v[134:135], v225 offset0:6 offset1:7\nM 60,176,164\nR 172,189,8192\n"
"ds_read2st64_b32 v[136:137], v225 offset0:8 offset1:9\nM 60,180,180\nbuffer_load_dwordx4 v221, s[16:19], s63 offen offset:1024 lds\nds_read2st64_b32 v[138:139], v225 offset0:10 offset1:11\nM 44,176,160\nR 174,189,12288\n"
"s_add_u32 m0, s50, 24576\nds_read2st64_b32 v[140:141], v225 offset0:12 offset1:13\nM 44,180,176\nds_read2st64_b32 v[142:143], v225 offset0:14 offset1:15\nds_read2st64_b32 v[144:145], v225 offset0:16 offset1:17\nG 13\n"
"M 32,160,192\nR 176,190,0\ns_add_u32 s63, s59, 393216\nds_read2st64_b32 v[146:147], v225 offset0:18 offset1:19\nM 32,164,208\nbuffer_load_dwordx4 v220, s[16:19], s63 offen lds\n"
"ds_read2st64_b32 v[148:149], v225 offset0:20 offset1:21\nM 48,160,196\nR 178,190,4096\nds_read2st64_b32 v[150:151], v225 offset0:22 offset1:23\nG 15\nv_add_f32 v0, v0, v128\n"
"M 48,164,212\nds_read2st64_b32 v[152:153], v225 offset0:24 offset1:25\nds_read2st64_b32 v[154:155], v225 offset0:26 offset1:27\nv_add_f32 v1, v1, v129\nM 64,160,200\nR 180,190,8192\n"
"ds_read2st64_b32 v[156:157], v225 offset0:28 offset1:29\nG 15\nv_add_f32 v2, v2, v130\nM 64,164,216\nbuffer_load_dwordx4 v221, s[16:19], s63 offen offset:1024 lds\nds_read2st64_b32 v[158:159], v225 offset0:30 offset1:31\n"
"M 80,160,204\nR 182,190,12288\ns_add_u32 m0, s50, 32768\nv_add_f32 v3, v3, v131\nM 80,164,220\nG 14\n"
"v_add_f32 v4, v4, v132\nv_add_f32 v5, v5, v133\nv_add_f32 v6, v6, v134\nM 84,168,204\nR 160,191,0\ns_add_u32 s63, s59, 458752\n"
"v_add_f32 v7, v7, v135\nM 84,172,220\nbuffer_load_dwordx4 v220, s[16:19], s63 offen lds\nv_add_f32 v8, v8, v136\nM 68,168,200\nR 162,191,4096\n"
"v_add_f32 v9, v9, v137\nv_add_f32 v10, v10, v138\nM 68,172,216\nv_add_f32 v11, v11, v139\nG 15\nv_add_f32 v12, v12, v140\n"
"v_add_f32 v13, v13, v141\nM 52,168,196\nR 164,191,8192\nG 15\nv_add_f32 v14, v14, v142\nv_add_f32 v15, v15, v143\n"
"M 52,172,212\nG 14\nv_add_f32 v16, v16, v144\nv_add_f32 v17, v17, v145\nG 12\nv_add_f32 v18, v18, v146\n"
"M 36,168,192\nR 166,191,12288\nv_add_f32 v19, v19, v147\nG 12\nv_add_f32 v20, v20, v148\nM 36,172,208\n"
"G 4\nv_add_f32 v21, v21, v149\nv_add_f32 v22, v22, v150\nv_add_f32 v23, v23, v151\nM 40,176,192\nR 168,188,16384\n"
"v_add_f32 v24, v24, v152\nv_add_f32 v25, v25, v153\nM 40,180,208\nbuffer_load_dwordx4 v221, s[16:19], s63 offen offset:1024 lds\nv_add_f32 v26, v26, v154\nM 56,176,196\n"
"R 170,188,20480\ns_add_u32 s63, s59, 262144\nv_add_f32 v27, v27, v155\nM 56,180,212\ns_add_u32 m0, s50, 40960\nv_add_f32 v28, v28, v156\n"
"v_add_f32 v29, v29, v157\nv_add_f32 v30, v30, v158\nv_add_f32 v31, v31, v159\nM 72,176,200\nR 172,188,24576\nv_cvt_pk_bf16_f32 v24, v24, v25\n"
"v_cvt_pk_bf16_f32 v0, v0, v1\nv_cvt_pk_bf16_f32 v8, v8, v9\nv_cvt_pk_bf16_f32 v16, v16, v17\nv_cvt_pk_bf16_f32 v25, v26, v27\nM 72,180,216\nbuffer_load_dwordx4 v220, s[16:19], s63 offen lds\n"
"v_cvt_pk_bf16_f32 v9, v10, v11\nv_cvt_pk_bf16_f32 v17, v18, v19\nv_cvt_pk_bf16_f32 v26, v28, v29\nM 88,176,204\nR 174,188,28672\nv_cvt_pk_bf16_f32 v27, v30, v31\n"
"v_cvt_pk_bf16_f32 v1, v2, v3\nM 88,180,220\nG 4\nv_cvt_pk_bf16_f32 v2, v4, v5\nv_cvt_pk_bf16_f32 v3, v6, v7\nM 92,160,204\n"
"R 176,189,16384\ns_bfe_u32 s100, s57, 0x20007\ns_mul_i32 s100, s100, 49152\ns_add_u32 s100, s100, s72\ns_add_u32 s100, s100, 52445184\nv_cvt_pk_bf16_f32 v10, v12, v13\n"
"M 92,164,220\nbuffer_store_dwordx4 v[0:3], v252, s[20:23], s100 offen offset:0 sc1\nM 76,160,200\nR 178,189,20480\nv_cvt_pk_bf16_f32 v11, v14, v15\nM 76,164,216\n"
"buffer_load_dwordx4 v221, s[16:19], s63 offen offset:1024 lds\nM 60,160,196\nR 180,189,24576\ns_add_u32 m0, s50, 49152\ns_add_u32 s63, s59, 786432\nM 60,164,212\n"
"buffer_load_dwordx4 v220, s[16:19], s63 offen lds\nM 44,160,192\nR 182,189,28672\nv_cvt_pk_bf16_f32 v18, v20, v21\nM 44,164,208\nbuffer_store_dwordx4 v[8:11], v252, s[20:23], s100 offen offset:1024 sc1\n"
"s_waitcnt vmcnt(21)\ns_barrier\nG 4\nM 96,168,0\nR 160,190,16384\nv_cvt_pk_bf16_f32 v19, v22, v23\n"
"M 96,172,16\ns_mov_b32 m0, s51\nM 112,168,4\nR 162,190,20480\nv_cvt_pk_bf16_f32 v64, v64, v65\nv_cvt_pk_bf16_f32 v65, v66, v67\n"
"M 112,172,20\nbuffer_store_dwordx4 v[16:19], v252, s[20:23], s100 offen offset:2048 sc1\nv_cvt_pk_bf16_f32 v66, v68, v69\nv_mfma_f32_16x16x32_bf16 v[128:131], v[168:171], a[8:11], 0\nR 164,190,24576\nv_cvt_pk_bf16_f32 v67, v70, v71\n"
"v_cvt_pk_bf16_f32 v68, v72, v73\nM 128,172,24\nds_write_addtid_b32 v64 offset:0\nv_cvt_pk_bf16_f32 v69, v74, v75\nv_cvt_pk_bf16_f32 v70, v76, v77\nv_mfma_f32_16x16x32_bf16 v[144:147], v[168:171], a[12:15], 0\n"
"R 166,190,28672\nv_cvt_pk_bf16_f32 v71, v78, v79\nv_cvt_pk_bf16_f32 v72, v80, v81\nM 144,172,28\nds_write_addtid_b32 v65 offset:256\nG 6\n"
"v_cvt_pk_bf16_f32 v73, v82, v83\nv_mfma_f32_16x16x32_bf16 v[148:151], v[176:179], a[12:15], 0\nR 168,191,16384\nv_cvt_pk_bf16_f32 v74, v84, v85\nv_cvt_pk_bf16_f32 v75, v86, v87\nM 148,180,28\n"
"buffer_store_dwordx4 v[24:27], v252, s[20:23], s100 offen offset:3072 sc1\nv_mfma_f32_16x16x32_bf16 v[132:135], v[176:179], a[8:11], 0\nR 170,191,20480\nv_cvt_pk_bf16_f32 v76, v88, v89\nM 132,180,24\nds_write_addtid_b32 v66 offset:512\n"
"v_cvt_pk_bf16_f32 v77, v90, v91\nM 116,176,4\nR 172,191,24576\nv_cvt_pk_bf16_f32 v78, v92, v93\nM 116,180,20\nds_write_addtid_b32 v67 offset:768\n"
"v_cvt_pk_bf16_f32 v79, v94, v95\nM 100,176,0\nR 174,191,28672\nds_write_addtid_b32 v68 offset:1024\nM 100,180,16\nds_write_addtid_b32 v69 offset:1280\n"
"ds_write_addtid_b32 v70 offset:1536\nG 10\nM 104,160,0\nR 176,188,32768\nds_write_addtid_b32 v71 offset:1792\nM 104,164,16\n"
"ds_write_addtid_b32 v72 offset:2048\nds_write_addtid_b32 v73 offset:2304\nM 120,160,4\nR 178,188,36864\nds_write_addtid_b32 v74 offset:2560\nM 120,164,20\n"
"ds_write_addtid_b32 v75 offset:2816\nds_write_addtid_b32 v76 offset:3072\nv_mfma_f32_16x16x32_bf16 v[136:139], v[160:163], a[8:11], 0\nR 180,188,40960\nLpg16:\ns_cbranch_execz LretO0n\n"
"ds_write_addtid_b32 v77 offset:3328\nM 136,164,24\nds_write_addtid_b32 v78 offset:3584\nds_write_addtid_b32 v79 offset:3840\nv_mfma_f32_16x16x32_bf16 v[152:155], v[160:163], a[12:15], 0\nR 182,188,45056\n"
"M 152,164,28\nG 15\nv_mfma_f32_16x16x32_bf16 v[156:159], v[168:171], a[12:15], 0\nR 160,189,32768\ns_waitcnt vmcnt(15) lgkmcnt(2)\ns_barrier\n"
"M 156,172,28\nds_read2st64_b32 v[64:65], v225 offset0:0 offset1:1\nv_mfma_f32_16x16x32_bf16 v[140:143], v[168:171], a[8:11], 0\nR 162,189,36864\nM 140,172,24\nds_read2st64_b32 v[66:67], v225 offset0:2 offset1:3\n"
"M 124,168,4\nR 164,189,40960\nM 124,172,20\nds_read2st64_b32 v[68:69], v225 offset0:4 offset1:5\nM 108,168,0\nR 166,189,45056\n"
"M 108,172,16\nds_read2st64_b32 v[70:71], v225 offset0:6 offset1:7\nG 8\nM 96,176,32\nR 168,190,32768\nv_bfe_u32 v250, v252, 4, 2\n"
"v_lshlrev_b32 v250, 3, v250\nM 96,180,48\ns_lshl_b32 s60, s43, 5\nv_add_u32 v250, s60, v250\nG 5\nv_mfma_f32_16x16x32_bf16 v[32:35], v[208:211], v[64:67], v[32:35]\n"
"v_mfma_f32_16x16x32_bf16 v[36:39], v[210:213], v[64:67], v[36:39]\nM 112,176,36\nR 170,190,36864\nM 112,180,52\nds_read2st64_b32 v[72:73], v225 offset0:8 offset1:9\nM 128,176,40\n"
"R 172,190,40960\nM 128,180,56\nds_read2st64_b32 v[74:75], v225 offset0:10 offset1:11\nG 5\nv_mfma_f32_16x16x32_bf16 v[40:43], v[208:211], v[68:71], v[40:43]\nv_mfma_f32_16x16x32_bf16 v[44:47], v[210:213], v[68:71], v[44:47]\n"
"M 144,176,44\nR 174,190,45056\nM 144,180,60\nds_read2st64_b32 v[76:77], v225 offset0:12 offset1:13\nM 148,160,44\nR 176,191,32768\n"
"ds_read2st64_b32 v[78:79], v225 offset0:14 offset1:15\nM 148,164,60\nM 132,160,40\nR 178,191,36864\nM 132,164,56\nG 5\n"
"v_mfma_f32_16x16x32_bf16 v[48:51], v[208:211], v[72:75], v[48:51]\nv_mfma_f32_16x16x32_bf16 v[52:55], v[210:213], v[72:75], v[52:55]\nM 116,160,36\nR 180,191,40960\nM 116,164,52\nM 100,160,32\n"
"R 182,191,45056\nG 3\nv_mfma_f32_16x16x32_bf16 v[56:59], v[208:211], v[76:79], v[56:59]\nv_mfma_f32_16x16x32_bf16 v[60:63], v[210:213], v[76:79], v[60:63]\nM 100,164,48\nM 104,168,32\n"
"R 160,188,49152\nM 104,172,48\nM 120,168,36\nR 162,188,53248\nv_mov_b32 v226, s46\nM 120,172,52\n"
"s_add_u32 m0, s50, 49152\nv_mov_b32 v227, s47\nM 136,168,40\nR 164,184,0\ns_bfe_u32 s62, s56, 0x20007\ns_lshl_b32 s62, s62, 5\n"
"s_add_u32 s61, s74, 548864\ns_add_u32 s61, s61, s62\ns_waitcnt vmcnt(0)\ns_mov_b64 exec, 1\nbuffer_atomic_swap_x2 v[226:227], off, s[20:23], s61 sc1\ns_mov_b64 exec, -1\n"
"v_cvt_pk_bf16_f32 v32, v32, v33\nv_cvt_pk_bf16_f32 v48, v48, v49\nv_cvt_pk_bf16_f32 v56, v56, v57\nM 136,172,56\ns_bfe_u32 s62, s57, 0x20007\ns_lshl_b32 s62, s62, 5\n"
"s_add_u32 s61, s74, 548872\ns_add_u32 s61, s61, s62\ns_mov_b64 exec, 1\nbuffer_atomic_swap_x2 v[226:227], off, s[20:23], s61 sc1\ns_mov_b64 exec, -1\nv_cvt_pk_bf16_f32 v49, v50, v51\n"
"v_cvt_pk_bf16_f32 v57, v58, v59\nM 152,168,44\nR 166,184,4096\nv_cvt_pk_bf16_f32 v50, v52, v53\nv_cvt_pk_bf16_f32 v33, v34, v35\nM 152,172,60\n"
"G 4\nv_cvt_pk_bf16_f32 v34, v36, v37\nv_cvt_pk_bf16_f32 v35, v38, v39\nM 156,176,44\nR 168,189,49152\ns_bfe_u32 s100, s58, 0x20007\n"
"s_mul_i32 s100, s100, 49152\ns_add_u32 s100, s100, s72\ns_add_u32 s100, s100, 52461568\nv_cvt_pk_bf16_f32 v40, v40, v41\nM 156,180,60\nbuffer_store_dwordx4 v[32:35], v252, s[20:23], s100 offen offset:0 sc1\n"
"buffer_load_dwordx4 v221, s[16:19], s63 offen offset:1024 lds\nM 140,176,40\nR 170,189,53248\ns_add_u32 m0, s50, 57344\ns_add_u32 s63, s59, 327680\nM 140,180,56\n"
"buffer_load_dwordx4 v220, s[16:19], s63 offen lds\nM 124,176,36\nR 172,185,0\nv_cvt_pk_bf16_f32 v41, v42, v43\nM 124,180,52\nv_cvt_pk_bf16_f32 v42, v44, v45\n"
"v_cvt_pk_bf16_f32 v43, v46, v47\nM 108,176,32\nR 174,185,4096\nv_cvt_pk_bf16_f32 v51, v54, v55\nM 108,180,48\nbuffer_store_dwordx4 v[40:43], v252, s[20:23], s100 offen offset:1024 sc1\n"
"s_barrier\nG 4\nM 96,160,64\nR 176,190,49152\nv_cvt_pk_bf16_f32 v58, v60, v61\nM 96,164,80\n"
"buffer_load_dwordx4 v221, s[16:19], s63 offen offset:1024 lds\nM 112,160,68\nR 178,190,53248\ns_add_u32 m0, s50, 65536\nM 112,164,84\nbuffer_store_dwordx4 v[48:51], v252, s[20:23], s100 offen offset:2048 sc1\n"
"M 128,160,72\nR 180,186,0\ns_add_u32 s63, s59, 851968\nM 128,164,88\nbuffer_load_dwordx4 v220, s[16:19], s63 offen lds\nM 144,160,76\n"
"R 182,186,4096\nv_cvt_pk_bf16_f32 v59, v62, v63\nM 144,164,92\nG 4\nM 148,168,76\nR 160,191,49152\n"
"Lq589:\ns_lshr_b32 s60, s45, 3\ns_lshl2_add_u32 s60, s60, s40\ns_lshl_b32 s60, s60, 8\nv_lshl_add_u32 v224, v252, 3, s60\nv_add_u32 v224, Lq589+4096-LqPC, v224\n"
"v_min_u32 v224, LqE-LqPC-4, v224\ns_mov_b64 exec, 3\nglobal_load_dword v224, v224, s[98:99]\ns_mov_b64 exec, -1\nM 148,172,92\nbuffer_store_dwordx4 v[56:59], v252, s[20:23], s100 offen offset:3072 sc1\n"
"buffer_load_dwordx4 v221, s[16:19], s63 offen offset:1024 lds\nM 132,168,72\nR 162,191,53248\nM 132,172,88\nM 116,168,68\nR 164,187,0\n"
"M 116,172,84\nM 100,168,64\nR 166,187,4096\nv_mov_b32 v248, 0\nM 100,172,80\nG 4\n"
"v_mov_b32 v249, 0\nM 104,176,64\nR 168,184,8192\ns_add_u32 s61, s74, 548864\ns_mov_b64 exec, 3\nbuffer_atomic_or_x2 v[248:249], v250, s[20:23], s61 offen sc0 sc1\n"
"s_mov_b64 exec, -1\nM 104,180,80\nM 120,176,68\nR 170,184,12288\nM 120,180,84\nM 136,176,72\n"
"R 172,184,16384\nM 136,180,88\nM 152,176,76\nR 174,184,20480\nM 152,180,92\nG 4\n"
"M 156,160,76\nR 176,185,8192\nM 156,164,92\nM 140,160,72\nR 178,185,12288\nM 140,164,88\n"
"M 124,160,68\nR 180,185,16384\nM 124,164,84\nM 108,160,64\nR 182,185,20480\nM 108,164,80\n"
"G 4\nM 96,168,96\nR 160,186,8192\nM 96,172,112\nM 112,168,100\nR 162,186,12288\n"
"M 112,172,116\nM 128,168,104\nR 164,186,16384\nM 128,172,120\nM 144,168,108\nR 166,186,20480\n"
"M 144,172,124\nG 4\nM 148,176,108\nR 168,187,8192\nM 148,180,124\nM 132,176,104\n"
"R 170,187,12288\nM 132,180,120\nM 116,176,100\nR 172,187,16384\nM 116,180,116\nM 100,176,96\n"
"R 174,187,20480\nM 100,180,112\nG 4\nM 104,160,96\nR 176,184,24576\ns_waitcnt vmcnt(9)\n"
"s_barrier\nM 104,164,112\nM 120,160,100\nR 178,184,28672\nM 120,164,116\nM 136,160,104\n"
"R 180,184,32768\nM 136,164,120\nM 152,160,108\nR 182,184,36864\nM 152,164,124\nG 4\n"
"M 156,168,108\nR 160,185,24576\nM 156,172,124\nM 140,168,104\nR 162,185,28672\nM 140,172,120\n"
"M 124,168,100\nR 164,185,32768\nM 124,172,116\nM 108,168,96\nR 166,185,36864\nv_mov_b32 v226, s46\n"
"M 108,172,112\nG 4\nv_mov_b32 v227, s47\nM 96,176,128\nR 168,186,24576\ns_bfe_u32 s62, s58, 0x20007\n"
"s_lshl_b32 s62, s62, 5\ns_add_u32 s61, s74, 548880\ns_add_u32 s61, s61, s62\ns_waitcnt vmcnt(0)\ns_mov_b64 exec, 1\nbuffer_atomic_swap_x2 v[226:227], off, s[20:23], s61 sc1\n"
"s_mov_b64 exec, -1\nM 96,180,144\ns_mul_i32 s64, s43, 49152\nM 112,176,132\nR 170,186,28672\nM 112,180,148\n"
"s_add_u32 s64, s64, s72\nM 128,176,136\nR 172,186,32768\nv_cmp_eq_u64 s[86:87], s[46:47], v[248:249]\ns_lshr_b32 s60, s45, 3\ns_lshl2_add_u32 s60, s60, s40\n"
"s_lshl_b32 s60, s60, 8\nv_lshl_add_u32 v224, v252, 3, s60\nv_add_u32 v224, LqM-LqPC, v224\nv_min_u32 v224, LqE-LqPC-4, v224\ns_mov_b64 exec, 3\nglobal_load_dword v224, v224, s[98:99]\n"
"s_mov_b64 exec, -1\nM 128,180,152\ns_add_u32 s64, s64, 52428800\nM 144,176,140\nR 174,186,36864\ns_and_b32 s62, s86, 3\n"
"s_cmp_eq_u32 s62, 3\ns_cbranch_scc0 Lq514\nLq515:\nM 144,180,156\nG 4\nM 148,160,140\n"
"R 176,187,24576\nM 148,164,156\nbuffer_load_dwordx4 v[0:3], v252, s[20:23], s64 offen offset:0 sc1\nM 132,160,136\nR 178,187,28672\nM 132,164,152\n"
"buffer_load_dwordx4 v[4:7], v252, s[20:23], s64 offen offset:1024 sc1\nM 116,160,132\nR 180,187,32768\nM 116,164,148\ns_mul_i32 s65, s43, 49152\nM 100,160,128\n"
"R 182,187,36864\nM 100,164,144\nbuffer_load_dwordx4 v[8:11], v252, s[20:23], s64 offen offset:2048 sc1\nG 4\nM 104,168,128\nR 160,184,40960\n"
"M 104,172,144\nbuffer_load_dwordx4 v[12:15], v252, s[20:23], s64 offen offset:3072 sc1\nM 120,168,132\nR 162,184,45056\nM 120,172,148\ns_add_u32 s65, s65, s72\n"
"M 136,168,136\nR 164,184,49152\ns_add_u32 s65, s65, 52445184\nM 136,172,152\nbuffer_load_dwordx4 v[16:19], v252, s[20:23], s65 offen offset:0 sc1\nM 152,168,140\n"
"R 166,184,53248\nM 152,172,156\nG 4\nM 156,176,140\nR 168,185,40960\ns_barrier\n"
"M 156,180,156\nbuffer_load_dwordx4 v[20:23], v252, s[20:23], s65 offen offset:1024 sc1\nM 140,176,136\nR 170,185,45056\nv_mov_b32 v230, 0\nM 140,180,152\n"
"buffer_load_dwordx4 v[24:27], v252, s[20:23], s65 offen offset:2048 sc1\nv_mov_b32 v231, 0\nM 124,176,132\nR 172,185,49152\ns_add_u32 s61, s74, 548864\ns_mov_b64 exec, 4\n"
"buffer_atomic_or_x2 v[230:231], v250, s[20:23], s61 offen sc0 sc1\ns_mov_b64 exec, -1\nM 124,180,148\nbuffer_load_dwordx4 v[28:31], v252, s[20:23], s65 offen offset:3072 sc1\nM 108,176,128\nR 174,185,53248\n"
"M 108,180,144\nG 4\nM 128,160,168\nR 176,186,40960\nM 128,164,184\nR 178,186,45056\n"
"M 144,160,172\nR 180,186,49152\nM 144,164,188\nR 182,186,53248\nG 4\nM 148,168,172\n"
"R 160,187,40960\ns_movk_i32 s63, 6144\nv_mad_u32_u24 v220, v228, s63, v229\nv_add_u32 v221, 3144704, v220\ns_add_u32 m0, s50, 73728\ns_add_u32 s63, s52, 2048\n"
"buffer_load_dwordx4 v220, s[4:7], s63 offen lds\nM 148,172,188\nR 162,187,45056\nM 132,168,168\nR 164,187,49152\nM 132,172,184\n"
"R 166,187,53248\nG 4\nM 136,176,168\nR 168,184,57344\nbuffer_load_dwordx4 v221, s[4:7], s63 offen offset:1024 lds\nM 136,180,184\n"
"R 170,184,61440\ns_add_u32 m0, s50, 81920\nM 152,176,172\nR 172,188,0\ns_add_u32 s63, s52, 198656\nM 152,180,188\n"
"R 174,188,4096\nG 4\nM 156,160,172\nR 176,185,57344\nbuffer_load_dwordx4 v220, s[4:7], s63 offen lds\nM 156,164,188\n"
"R 178,185,61440\nM 140,160,168\nR 180,189,0\nM 140,164,184\nR 182,189,4096\nG 4\n"
"M 128,168,200\nR 160,186,57344\nbuffer_load_dwordx4 v221, s[4:7], s63 offen offset:1024 lds\nM 128,172,216\nR 162,186,61440\ns_add_u32 m0, s50, 90112\n"
"M 144,168,204\nR 164,190,0\ns_add_u32 s63, s52, 4096\nM 144,172,220\nR 166,190,4096\nG 4\n"
"M 148,176,204\nR 168,187,57344\nbuffer_load_dwordx4 v220, s[4:7], s63 offen lds\nM 148,180,220\nR 170,187,61440\nM 132,176,200\n"
"R 172,191,0\nM 132,180,216\nR 174,191,4096\nG 4\nM 136,160,200\nR 176,184,40960\n"
"buffer_load_dwordx4 v221, s[4:7], s63 offen offset:1024 lds\nM 136,164,216\nR 178,184,45056\ns_add_u32 m0, s50, 98304\nM 152,160,204\nR 180,184,49152\n"
"s_add_u32 s63, s52, 200704\nM 152,164,220\nR 182,184,53248\nG 4\nM 156,168,204\nR 160,185,40960\n"
"buffer_load_dwordx4 v220, s[4:7], s63 offen lds\nM 156,172,220\nR 162,185,45056\nM 140,168,200\nR 164,185,49152\nM 140,172,216\n"
"R 166,185,53248\nG 4\nM 96,176,160\nR 168,186,40960\nbuffer_load_dwordx4 v221, s[4:7], s63 offen offset:1024 lds\nM 96,180,176\n"
"R 170,186,45056\ns_mov_b32 m0, s51\nM 112,176,164\nR 172,186,49152\nv_cvt_pk_bf16_f32 v128, v128, v129\nv_cvt_pk_bf16_f32 v129, v130, v131\n"
"M 112,180,180\nR 174,186,53248\nG 4\nv_cvt_pk_bf16_f32 v130, v132, v133\nM 116,160,164\nR 176,187,40960\n"
"s_mul_i32 s66, s43, 49152\nv_cvt_pk_bf16_f32 v131, v134, v135\nM 116,164,180\nR 178,187,45056\ns_add_u32 s66, s66, s72\nv_cvt_pk_bf16_f32 v132, v136, v137\n"
"M 100,160,160\nR 180,187,49152\ns_add_u32 s66, s66, 52461568\nv_cvt_pk_bf16_f32 v133, v138, v139\nM 100,164,176\nR 182,187,53248\n"
"G 4\nv_cvt_pk_bf16_f32 v134, v140, v141\nM 104,168,160\nR 160,184,57344\ns_waitcnt vmcnt(9)\nv_cmp_eq_u64 s[86:87], s[46:47], v[230:231]\n"
"v_cvt_pk_bf16_f32 v135, v142, v143\nM 104,172,176\nR 162,184,61440\nv_cvt_pk_bf16_f32 v136, v144, v145\nv_cvt_pk_bf16_f32 v137, v146, v147\nM 120,168,164\n"
"R 164,188,0\ns_and_b32 s62, s86, 4\ns_cmp_eq_u32 s62, 4\ns_cbranch_scc0 Lq530\nLq531:\nv_cvt_pk_bf16_f32 v138, v148, v149\n"
"M 120,172,180\nR 166,188,4096\nG 4\nv_cvt_pk_bf16_f32 v139, v150, v151\nM 124,176,164\nR 168,185,57344\n"
"buffer_load_dwordx4 v[64:67], v252, s[20:23], s66 offen offset:0 sc1\nv_cvt_pk_bf16_f32 v140, v152, v153\nM 124,180,180\nR 170,185,61440\nv_cvt_pk_bf16_f32 v141, v154, v155\nv_cvt_pk_bf16_f32 v142, v156, v157\n"
"M 108,176,160\nR 172,189,0\nbuffer_load_dwordx4 v[68:71], v252, s[20:23], s66 offen offset:1024 sc1\nds_write_addtid_b32 v128 offset:0\nds_write_addtid_b32 v129 offset:256\nds_write_addtid_b32 v130 offset:512\n"
"ds_write_addtid_b32 v131 offset:768\nds_write_addtid_b32 v132 offset:1024\nds_write_addtid_b32 v133 offset:1280\nds_write_addtid_b32 v134 offset:1536\nds_write_addtid_b32 v135 offset:1792\nds_write_addtid_b32 v136 offset:2048\n"
"ds_write_addtid_b32 v137 offset:2304\nds_write_addtid_b32 v138 offset:2560\nds_write_addtid_b32 v139 offset:2816\nds_write_addtid_b32 v140 offset:3072\nds_write_addtid_b32 v141 offset:3328\nds_write_addtid_b32 v142 offset:3584\n"
"v_cvt_pk_bf16_f32 v143, v158, v159\nds_write_addtid_b32 v143 offset:3840\nM 108,180,176\nR 174,189,4096\ns_add_u32 m0, s50, 106496\ns_add_u32 s63, s52, 395264\n"
"G 15\nM 96,160,192\nR 176,186,57344\nbuffer_load_dwordx4 v220, s[4:7], s63 offen lds\nM 96,164,208\nR 178,186,61440\n"
"buffer_load_dwordx4 v[72:75], v252, s[20:23], s66 offen offset:2048 sc1\nG 3\ns_barrier\nM 112,160,196\nR 180,190,0\nds_read2st64_b32 v[128:129], v225 offset0:0 offset1:1\n"
"M 112,164,212\nR 182,190,4096\nds_read2st64_b32 v[130:131], v225 offset0:2 offset1:3\nG 6\nM 116,168,196\nR 160,187,57344\n"
"buffer_load_dwordx4 v[76:79], v252, s[20:23], s66 offen offset:3072 sc1\nds_read2st64_b32 v[132:133], v225 offset0:4 offset1:5\nM 116,172,212\nLpg17:\ns_cbranch_execz LretO0n\nR 162,187,61440\n"
"buffer_load_dwordx4 v221, s[4:7], s63 offen offset:1024 lds\nds_read2st64_b32 v[134:135], v225 offset0:6 offset1:7\nM 100,168,192\nR 164,191,0\ns_add_u32 m0, s50, 114688\ns_add_u32 s63, s52, 591872\n"
"ds_read2st64_b32 v[136:137], v225 offset0:8 offset1:9\nM 100,172,208\nR 166,191,4096\nds_read2st64_b32 v[138:139], v225 offset0:10 offset1:11\nG 9\nM 104,176,192\n"
"buffer_load_dwordx4 v220, s[4:7], s63 offen lds\nds_read2st64_b32 v[140:141], v225 offset0:12 offset1:13\nM 104,180,208\nds_read2st64_b32 v[142:143], v225 offset0:14 offset1:15\nM 120,176,196\nbuffer_load_dwordx4 v221, s[4:7], s63 offen offset:1024 lds\n"
"M 120,180,212\nG 3\nM 124,160,196\nM 124,164,212\nM 108,160,192\nM 108,164,208\n"
"G 0\ns_barrier\nv_mfma_f32_16x16x32_bf16 v[96:99], v[208:211], v[128:131], v[96:99]\nv_mfma_f32_16x16x32_bf16 v[100:103], v[210:213], v[128:131], v[100:103]\nv_mfma_f32_16x16x32_bf16 v[104:107], v[208:211], v[132:135], v[104:107]\nv_mfma_f32_16x16x32_bf16 v[108:111], v[210:213], v[132:135], v[108:111]\n"
"v_mfma_f32_16x16x32_bf16 v[112:115], v[208:211], v[136:139], v[112:115]\nv_mfma_f32_16x16x32_bf16 v[116:119], v[210:213], v[136:139], v[116:119]\nv_mfma_f32_16x16x32_bf16 v[120:123], v[208:211], v[140:143], v[120:123]\nv_mfma_f32_16x16x32_bf16 v[124:127], v[210:213], v[140:143], v[124:127]\ns_lshl_b32 s84, s75, 11\ns_add_u32 s84, s84, s49\n"
"s_add_u32 s84, s84, 18874368\ns_add_u32 s85, s84, 32768\nv_mov_b32 v238, s46\nv_mov_b32 v239, s47\nv_mfma_f32_16x16x32_bf16 v[96:99], v[208:211], v[0:3], v[96:99]\nv_mfma_f32_16x16x32_bf16 v[100:103], v[210:213], v[0:3], v[100:103]\n"
"v_mfma_f32_16x16x32_bf16 v[104:107], v[208:211], v[4:7], v[104:107]\nv_mfma_f32_16x16x32_bf16 v[108:111], v[210:213], v[4:7], v[108:111]\nv_mfma_f32_16x16x32_bf16 v[112:115], v[208:211], v[8:11], v[112:115]\nv_mfma_f32_16x16x32_bf16 v[116:119], v[210:213], v[8:11], v[116:119]\nv_mfma_f32_16x16x32_bf16 v[120:123], v[208:211], v[12:15], v[120:123]\nv_mfma_f32_16x16x32_bf16 v[124:127], v[210:213], v[12:15], v[124:127]\n"
"v_mfma_f32_16x16x32_bf16 v[96:99], v[208:211], v[16:19], v[96:99]\nv_mfma_f32_16x16x32_bf16 v[100:103], v[210:213], v[16:19], v[100:103]\nv_mfma_f32_16x16x32_bf16 v[104:107], v[208:211], v[20:23], v[104:107]\nv_mfma_f32_16x16x32_bf16 v[108:111], v[210:213], v[20:23], v[108:111]\nv_mfma_f32_16x16x32_bf16 v[112:115], v[208:211], v[24:27], v[112:115]\nv_mfma_f32_16x16x32_bf16 v[116:119], v[210:213], v[24:27], v[116:119]\n"
"s_waitcnt vmcnt(16)\nv_mfma_f32_16x16x32_bf16 v[120:123], v[208:211], v[28:31], v[120:123]\nv_mfma_f32_16x16x32_bf16 v[124:127], v[210:213], v[28:31], v[124:127]\ns_waitcnt vmcnt(7)\nv_mfma_f32_16x16x32_bf16 v[96:99], v[208:211], v[64:67], v[96:99]\nv_mfma_f32_16x16x32_bf16 v[100:103], v[210:213], v[64:67], v[100:103]\n"
"s_waitcnt vmcnt(6)\nv_mfma_f32_16x16x32_bf16 v[104:107], v[208:211], v[68:71], v[104:107]\nv_mfma_f32_16x16x32_bf16 v[108:111], v[210:213], v[68:71], v[108:111]\ns_waitcnt vmcnt(4)\nv_mfma_f32_16x16x32_bf16 v[112:115], v[208:211], v[72:75], v[112:115]\nv_mfma_f32_16x16x32_bf16 v[116:119], v[210:213], v[72:75], v[116:119]\n"
"s_waitcnt vmcnt(3)\nv_mfma_f32_16x16x32_bf16 v[120:123], v[208:211], v[76:79], v[120:123]\nv_mfma_f32_16x16x32_bf16 v[124:127], v[210:213], v[76:79], v[124:127]\nv_cvt_pk_bf16_f32 v192, v96, v97\nv_cvt_pk_bf16_f32 v193, v98, v99\nv_cvt_pk_bf16_f32 v194, v100, v101\n"
"v_cvt_pk_bf16_f32 v195, v102, v103\nbuffer_store_dwordx4 v[192:195], v252, s[20:23], s84 offen offset:0 sc1\nv_cvt_pk_bf16_f32 v196, v104, v105\nv_cvt_pk_bf16_f32 v197, v106, v107\nv_cvt_pk_bf16_f32 v198, v108, v109\nv_cvt_pk_bf16_f32 v199, v110, v111\n"
"buffer_store_dwordx4 v[196:199], v252, s[20:23], s84 offen offset:1024 sc1\nv_cvt_pk_bf16_f32 v200, v112, v113\nv_cvt_pk_bf16_f32 v201, v114, v115\nv_cvt_pk_bf16_f32 v202, v116, v117\nv_cvt_pk_bf16_f32 v203, v118, v119\nbuffer_store_dwordx4 v[200:203], v252, s[20:23], s85 offen offset:0 sc1\n"
"v_cvt_pk_bf16_f32 v204, v120, v121\nv_cvt_pk_bf16_f32 v205, v122, v123\nv_cvt_pk_bf16_f32 v206, v124, v125\nv_cvt_pk_bf16_f32 v207, v126, v127\nbuffer_store_dwordx4 v[204:207], v252, s[20:23], s85 offen offset:1024 sc1\ns_mov_b64 exec, 0\n"
"s_cmp_eq_u32 s40, 1\ns_cbranch_scc1 Lpg19\ns_cmp_eq_u32 s40, 2\ns_cbranch_scc1 Lpg20\ns_cmp_eq_u32 s40, 3\ns_cbranch_scc1 Lpg21\n"
"s_branch Lpg18\nLretW0n:\ns_mov_b64 exec, -1\ns_add_u32 s61, s79, 88358912\ns_lshl_b32 s62, s75, 3\ns_add_u32 s61, s61, s62\n"
"s_waitcnt vmcnt(0)\ns_mov_b64 exec, 1\nbuffer_atomic_swap_x2 v[238:239], off, s[20:23], s61 sc1\ns_mov_b64 exec, -1\ns_branch LqM\nLq116:\n"
"s_lshl_b32 s61, s42, 13\ns_add_u32 s61, s61, 84017152\nbuffer_load_dwordx4 v[128:131], v230, s[20:23], s61 offen sc1\ns_lshl_b32 s61, s42, 13\ns_add_u32 s61, s61, 84021248\nbuffer_load_dwordx4 v[132:135], v230, s[20:23], s61 offen sc1\n"
"s_waitcnt vmcnt(0)\nv_min3_u32 v232, v129, v131, v133\nv_min3_u32 v232, v232, v135, v135\nv_max3_u32 v233, v129, v131, v133\nv_max3_u32 v233, v233, v135, v135\nv_cmp_ne_u32 s[86:87], s46, v232\n"
"v_cmp_ne_u32 s[88:89], s46, v233\ns_nop 0\ns_or_b64 s[86:87], s[86:87], s[88:89]\ns_cbranch_scc0 Lq117\ns_sub_u32 s48, s48, 1\ns_cmp_gt_i32 s48, 0\n"
"s_cbranch_scc1 Lq116\ns_branch Lq117\nLq124:\ns_lshl_b32 s61, s42, 13\ns_add_u32 s61, s61, 84148224\nbuffer_load_dwordx4 v[144:147], v234, s[20:23], s61 offen sc1\n"
"s_lshl_b32 s61, s42, 13\ns_add_u32 s61, s61, 84152320\nbuffer_load_dwordx4 v[148:151], v234, s[20:23], s61 offen sc1\ns_waitcnt vmcnt(0)\nv_min3_u32 v236, v145, v147, v149\nv_min3_u32 v236, v236, v151, v151\n"
"v_max3_u32 v237, v145, v147, v149\nv_max3_u32 v237, v237, v151, v151\nv_cmp_ne_u32 s[86:87], s46, v236\nv_cmp_ne_u32 s[88:89], s46, v237\ns_nop 0\ns_or_b64 s[86:87], s[86:87], s[88:89]\n"
"s_cbranch_scc0 Lq125\ns_sub_u32 s48, s48, 1\ns_cmp_gt_i32 s48, 0\ns_cbranch_scc1 Lq124\ns_branch Lq125\nLq132:\n"
"s_lshl_b32 s61, s42, 13\ns_add_u32 s61, s61, 83886080\nbuffer_load_dwordx4 v[128:131], v230, s[20:23], s61 offen sc1\ns_lshl_b32 s61, s42, 13\ns_add_u32 s61, s61, 83890176\nbuffer_load_dwordx4 v[132:135], v230, s[20:23], s61 offen sc1\n"
"s_waitcnt vmcnt(0)\nv_min3_u32 v232, v129, v131, v133\nv_min3_u32 v232, v232, v135, v135\nv_max3_u32 v233, v129, v131, v133\nv_max3_u32 v233, v233, v135, v135\nv_cmp_ne_u32 s[86:87], s46, v232\n"
"v_cmp_ne_u32 s[88:89], s46, v233\ns_nop 0\ns_or_b64 s[86:87], s[86:87], s[88:89]\ns_cbranch_scc0 Lq133\ns_sub_u32 s48, s48, 1\ns_cmp_gt_i32 s48, 0\n"
"s_cbranch_scc1 Lq132\ns_branch Lq133\nLq140:\ns_lshr_b32 s84, s49, 3\ns_add_u32 s84, s84, 81788928\nbuffer_load_dwordx4 v[128:131], v252, s[20:23], s84 offen offset:0 sc1\n"
"buffer_load_dwordx4 v[132:135], v252, s[20:23], s84 offen offset:1024 sc1\nbuffer_load_dwordx4 v[136:139], v252, s[20:23], s84 offen offset:2048 sc1\nbuffer_load_dwordx4 v[140:143], v252, s[20:23], s84 offen offset:3072 sc1\ns_lshr_b32 s85, s49, 3\ns_add_u32 s85, s85, 81793024\nbuffer_load_dwordx4 v[144:147], v252, s[20:23], s85 offen offset:0 sc1\n"
"buffer_load_dwordx4 v[148:151], v252, s[20:23], s85 offen offset:1024 sc1\nbuffer_load_dwordx4 v[152:155], v252, s[20:23], s85 offen offset:2048 sc1\nbuffer_load_dwordx4 v[156:159], v252, s[20:23], s85 offen offset:3072 sc1\ns_waitcnt vmcnt(6)\nv_min3_u32 v230, v129, v131, v133\ns_waitcnt vmcnt(5)\n"
"v_min3_u32 v230, v230, v135, v137\ns_waitcnt vmcnt(4)\nv_min3_u32 v230, v230, v139, v141\ns_waitcnt vmcnt(3)\nv_min3_u32 v230, v230, v143, v145\ns_waitcnt vmcnt(2)\n"
"v_min3_u32 v230, v230, v147, v149\ns_waitcnt vmcnt(1)\nv_min3_u32 v230, v230, v151, v153\ns_waitcnt vmcnt(0)\nv_min3_u32 v230, v230, v155, v157\nv_min3_u32 v230, v230, v159, v159\n"
"v_max3_u32 v231, v129, v131, v133\nv_max3_u32 v231, v231, v135, v137\nv_max3_u32 v231, v231, v139, v141\nv_max3_u32 v231, v231, v143, v145\nv_max3_u32 v231, v231, v147, v149\nv_max3_u32 v231, v231, v151, v153\n"
"v_max3_u32 v231, v231, v155, v157\nv_max3_u32 v231, v231, v159, v159\nv_cmp_ne_u32 s[86:87], s46, v230\nv_cmp_ne_u32 s[88:89], s46, v231\ns_nop 0\ns_or_b64 s[86:87], s[86:87], s[88:89]\n"
"s_cbranch_scc0 Lq141\ns_sub_u32 s48, s48, 1\ns_cmp_gt_i32 s48, 0\ns_cbranch_scc1 Lq140\ns_branch Lq141\nLq193:\n"
"s_mov_b64 exec, s[66:67]\nbuffer_load_dwordx4 v[230:233], v252, s[20:23], s65 offen offset:0 sc1\nbuffer_load_dwordx2 v[64:65], v227, s[20:23], s65 offen offset:2048 sc1\nbuffer_load_dwordx2 v[70:71], v227, s[20:23], s65 offen offset:2560 sc1\ns_mov_b64 exec, -1\ns_waitcnt vmcnt(0)\n"
"v_max3_u32 v248, v233, v65, v71\nv_cmp_eq_u32 vcc, -1, v248\ns_nop 1\ns_cbranch_vccz Lq194\ns_sub_u32 s48, s48, 1\ns_cmp_gt_i32 s48, 0\n"
"s_cbranch_scc1 Lq193\ns_branch Lq194\nLq195:\ns_mov_b64 exec, s[66:67]\nbuffer_load_dwordx4 v[234:237], v252, s[20:23], s65 offen offset:1024 sc1\nbuffer_load_dwordx2 v[76:77], v227, s[20:23], s65 offen offset:3072 sc1\n"
"buffer_load_dwordx2 v[82:83], v227, s[20:23], s65 offen offset:3584 sc1\ns_mov_b64 exec, -1\ns_waitcnt vmcnt(0)\nv_max3_u32 v248, v237, v77, v83\nv_cmp_eq_u32 vcc, -1, v248\ns_nop 1\n"
"s_cbranch_vccz Lq196\ns_sub_u32 s48, s48, 1\ns_cmp_gt_i32 s48, 0\ns_cbranch_scc1 Lq195\ns_branch Lq196\nLq198:\n"
"v_mov_b32 v248, 0\nv_mov_b32 v249, 0\ns_add_u32 s61, s80, 88080384\ns_mov_b64 exec, 0xffff\nbuffer_atomic_or_x2 v[248:249], v250, s[20:23], s61 offen sc0 sc1\ns_mov_b64 exec, -1\n"
"s_waitcnt vmcnt(0)\nv_cmp_eq_u64 s[86:87], s[46:47], v[248:249]\ns_nop 1\ns_and_b32 s62, s86, 0xffff\ns_cmp_eq_u32 s62, 0xffff\ns_cbranch_scc1 Lq199\n"
"s_sleep 1\ns_sub_u32 s48, s48, 1\ns_cmp_gt_i32 s48, 0\ns_cbranch_scc1 Lq198\ns_branch Lq199\nLq260:\n"
"v_mov_b32 v248, 0\nv_mov_b32 v249, 0\ns_add_u32 s61, s80, 88219648\ns_mov_b64 exec, 0xffff\nbuffer_atomic_or_x2 v[248:249], v250, s[20:23], s61 offen sc0 sc1\ns_mov_b64 exec, -1\n"
"s_waitcnt vmcnt(0)\nv_cmp_eq_u64 s[86:87], s[46:47], v[248:249]\ns_nop 1\ns_and_b32 s62, s86, 0xffff\ns_cmp_eq_u32 s62, 0xffff\ns_cbranch_scc1 Lq261\n"
"s_sleep 1\ns_sub_u32 s48, s48, 1\ns_cmp_gt_i32 s48, 0\ns_cbranch_scc1 Lq260\ns_branch Lq261\nLq319:\n"
"s_lshr_b32 s84, s49, 3\ns_add_u32 s84, s84, 82313216\nbuffer_load_dwordx4 v[0:3], v252, s[20:23], s84 offen offset:0 sc1\nbuffer_load_dwordx4 v[4:7], v252, s[20:23], s84 offen offset:1024 sc1\nbuffer_load_dwordx4 v[8:11], v252, s[20:23], s84 offen offset:2048 sc1\nbuffer_load_dwordx4 v[12:15], v252, s[20:23], s84 offen offset:3072 sc1\n"
"s_lshr_b32 s85, s49, 3\ns_add_u32 s85, s85, 82317312\nbuffer_load_dwordx4 v[16:19], v252, s[20:23], s85 offen offset:0 sc1\nbuffer_load_dwordx4 v[20:23], v252, s[20:23], s85 offen offset:1024 sc1\nbuffer_load_dwordx4 v[24:27], v252, s[20:23], s85 offen offset:2048 sc1\nbuffer_load_dwordx4 v[28:31], v252, s[20:23], s85 offen offset:3072 sc1\n"
"s_waitcnt vmcnt(6)\nv_min3_u32 v230, v1, v3, v5\ns_waitcnt vmcnt(5)\nv_min3_u32 v230, v230, v7, v9\ns_waitcnt vmcnt(4)\nv_min3_u32 v230, v230, v11, v13\n"
"s_waitcnt vmcnt(3)\nv_min3_u32 v230, v230, v15, v17\ns_waitcnt vmcnt(2)\nv_min3_u32 v230, v230, v19, v21\ns_waitcnt vmcnt(1)\nv_min3_u32 v230, v230, v23, v25\n"
"s_waitcnt vmcnt(0)\nv_min3_u32 v230, v230, v27, v29\nv_min3_u32 v230, v230, v31, v31\nv_max3_u32 v231, v1, v3, v5\nv_max3_u32 v231, v231, v7, v9\nv_max3_u32 v231, v231, v11, v13\n"
"v_max3_u32 v231, v231, v15, v17\nv_max3_u32 v231, v231, v19, v21\nv_max3_u32 v231, v231, v23, v25\nv_max3_u32 v231, v231, v27, v29\nv_max3_u32 v231, v231, v31, v31\nv_cmp_ne_u32 s[86:87], s46, v230\n"
"v_cmp_ne_u32 s[88:89], s46, v231\ns_nop 0\ns_or_b64 s[86:87], s[86:87], s[88:89]\ns_cbranch_scc0 Lq320\ns_sub_u32 s48, s48, 1\ns_cmp_gt_i32 s48, 0\n"
"s_cbranch_scc1 Lq319\ns_branch Lq320\nLq321:\ns_lshl_b32 s61, s42, 13\ns_add_u32 s61, s61, 84279296\nbuffer_load_dwordx4 v[0:3], v238, s[20:23], s61 offen sc1\n"
"s_lshl_b32 s61, s42, 13\ns_add_u32 s61, s61, 84283392\nbuffer_load_dwordx4 v[4:7], v238, s[20:23], s61 offen sc1\ns_waitcnt vmcnt(0)\nv_min3_u32 v240, v1, v3, v5\nv_min3_u32 v240, v240, v7, v7\n"
"v_max3_u32 v241, v1, v3, v5\nv_max3_u32 v241, v241, v7, v7\nv_cmp_ne_u32 s[86:87], s46, v240\nv_cmp_ne_u32 s[88:89], s46, v241\ns_nop 0\ns_or_b64 s[86:87], s[86:87], s[88:89]\n"
"s_cbranch_scc0 Lq322\ns_sub_u32 s48, s48, 1\ns_cmp_gt_i32 s48, 0\ns_cbranch_scc1 Lq321\ns_branch Lq322\nLq331:\n"
"s_lshl_b32 s61, s42, 13\ns_add_u32 s61, s61, 84410368\nbuffer_load_dwordx4 v[16:19], v242, s[20:23], s61 offen sc1\ns_lshl_b32 s61, s42, 13\ns_add_u32 s61, s61, 84414464\nbuffer_load_dwordx4 v[20:23], v242, s[20:23], s61 offen sc1\n"
"s_waitcnt vmcnt(0)\nv_min3_u32 v244, v17, v19, v21\nv_min3_u32 v244, v244, v23, v23\nv_max3_u32 v245, v17, v19, v21\nv_max3_u32 v245, v245, v23, v23\nv_cmp_ne_u32 s[86:87], s46, v244\n"
"v_cmp_ne_u32 s[88:89], s46, v245\ns_nop 0\ns_or_b64 s[86:87], s[86:87], s[88:89]\ns_cbranch_scc0 Lq332\ns_sub_u32 s48, s48, 1\ns_cmp_gt_i32 s48, 0\n"
"s_cbranch_scc1 Lq331\ns_branch Lq332\nLq341:\ns_lshl_b32 s61, s42, 13\ns_add_u32 s61, s61, 84541440\nbuffer_load_dwordx4 v[0:3], v238, s[20:23], s61 offen sc1\n"
"s_lshl_b32 s61, s42, 13\ns_add_u32 s61, s61, 84545536\nbuffer_load_dwordx4 v[4:7], v238, s[20:23], s61 offen sc1\ns_waitcnt vmcnt(0)\nv_min3_u32 v240, v1, v3, v5\nv_min3_u32 v240, v240, v7, v7\n"
"v_max3_u32 v241, v1, v3, v5\nv_max3_u32 v241, v241, v7, v7\nv_cmp_ne_u32 s[86:87], s46, v240\nv_cmp_ne_u32 s[88:89], s46, v241\ns_nop 0\ns_or_b64 s[86:87], s[86:87], s[88:89]\n"
"s_cbranch_scc0 Lq342\ns_sub_u32 s48, s48, 1\ns_cmp_gt_i32 s48, 0\ns_cbranch_scc1 Lq341\ns_branch Lq342\nLq351:\n"
"s_lshl_b32 s61, s42, 13\ns_add_u32 s61, s61, 84672512\nbuffer_load_dwordx4 v[16:19], v242, s[20:23], s61 offen sc1\ns_lshl_b32 s61, s42, 13\ns_add_u32 s61, s61, 84676608\nbuffer_load_dwordx4 v[20:23], v242, s[20:23], s61 offen sc1\n"
"s_waitcnt vmcnt(0)\nv_min3_u32 v244, v17, v19, v21\nv_min3_u32 v244, v244, v23, v23\nv_max3_u32 v245, v17, v19, v21\nv_max3_u32 v245, v245, v23, v23\nv_cmp_ne_u32 s[86:87], s46, v244\n"
"v_cmp_ne_u32 s[88:89], s46, v245\ns_nop 0\ns_or_b64 s[86:87], s[86:87], s[88:89]\ns_cbranch_scc0 Lq352\ns_sub_u32 s48, s48, 1\ns_cmp_gt_i32 s48, 0\n"
"s_cbranch_scc1 Lq351\ns_branch Lq352\nLq385:\nv_mov_b32 v248, 0\nv_mov_b32 v249, 0\ns_add_u32 s61, s82, 385024\n"
"s_mov_b64 exec, 0xf\nbuffer_atomic_or_x2 v[248:249], v250, s[20:23], s61 offen sc0 sc1\ns_mov_b64 exec, -1\ns_waitcnt vmcnt(0)\nv_cmp_eq_u64 s[86:87], s[46:47], v[248:249]\ns_nop 1\n"
"s_and_b32 s62, s86, 0xf\ns_cmp_eq_u32 s62, 0xf\ns_cbranch_scc1 Lq386\ns_sleep 1\ns_sub_u32 s48, s48, 1\ns_cmp_gt_i32 s48, 0\n"
"s_cbranch_scc1 Lq385\ns_branch Lq386\nLq420:\nv_mov_b32 v248, 0\nv_mov_b32 v249, 0\ns_add_u32 s61, s82, 385032\n"
"s_mov_b64 exec, 0xf\nbuffer_atomic_or_x2 v[248:249], v250, s[20:23], s61 offen sc0 sc1\ns_mov_b64 exec, -1\ns_waitcnt vmcnt(0)\nv_cmp_eq_u64 s[86:87], s[46:47], v[248:249]\ns_nop 1\n"
"s_and_b32 s62, s86, 0xf\ns_cmp_eq_u32 s62, 0xf\ns_cbranch_scc1 Lq421\ns_sleep 1\ns_sub_u32 s48, s48, 1\ns_cmp_gt_i32 s48, 0\n"
"s_cbranch_scc1 Lq420\ns_branch Lq421\nLq455:\nv_mov_b32 v248, 0\nv_mov_b32 v249, 0\ns_add_u32 s61, s82, 385048\n"
"s_mov_b64 exec, 0xf\nbuffer_atomic_or_x2 v[248:249], v250, s[20:23], s61 offen sc0 sc1\ns_mov_b64 exec, -1\ns_waitcnt vmcnt(0)\nv_cmp_eq_u64 s[86:87], s[46:47], v[248:249]\ns_nop 1\n"
"s_and_b32 s62, s86, 0xf\ns_cmp_eq_u32 s62, 0xf\ns_cbranch_scc1 Lq456\ns_sleep 1\ns_sub_u32 s48, s48, 1\ns_cmp_gt_i32 s48, 0\n"
"s_cbranch_scc1 Lq455\ns_branch Lq456\nLq474:\nv_mov_b32 v248, 0\nv_mov_b32 v249, 0\ns_add_u32 s61, s82, 385040\n"
"s_mov_b64 exec, 0xf\nbuffer_atomic_or_x2 v[248:249], v250, s[20:23], s61 offen sc0 sc1\ns_mov_b64 exec, -1\ns_waitcnt vmcnt(0)\nv_cmp_eq_u64 s[86:87], s[46:47], v[248:249]\ns_nop 1\n"
"s_and_b32 s62, s86, 0xf\ns_cmp_eq_u32 s62, 0xf\ns_cbranch_scc1 Lq475\ns_sleep 1\ns_sub_u32 s48, s48, 1\ns_cmp_gt_i32 s48, 0\n"
"s_cbranch_scc1 Lq474\ns_branch Lq475\nLq514:\nv_mov_b32 v248, 0\nv_mov_b32 v249, 0\ns_add_u32 s61, s74, 548864\n"
"s_mov_b64 exec, 3\nbuffer_atomic_or_x2 v[248:249], v250, s[20:23], s61 offen sc0 sc1\ns_mov_b64 exec, -1\ns_waitcnt vmcnt(0)\nv_cmp_eq_u64 s[86:87], s[46:47], v[248:249]\ns_nop 1\n"
"s_and_b32 s62, s86, 3\ns_cmp_eq_u32 s62, 3\ns_cbranch_scc1 Lq515\ns_sleep 1\ns_sub_u32 s48, s48, 1\ns_cmp_gt_i32 s48, 0\n"
"s_cbranch_scc1 Lq514\ns_branch Lq515\nLq530:\nv_mov_b32 v230, 0\nv_mov_b32 v231, 0\ns_add_u32 s61, s74, 548864\n"
"s_mov_b64 exec, 4\nbuffer_atomic_or_x2 v[230:231], v250, s[20:23], s61 offen sc0 sc1\ns_mov_b64 exec, -1\ns_waitcnt vmcnt(0)\nv_cmp_eq_u64 s[86:87], s[46:47], v[230:231]\ns_nop 1\n"
"s_and_b32 s62, s86, 4\ns_cmp_eq_u32 s62, 4\ns_cbranch_scc1 Lq531\ns_sleep 1\ns_sub_u32 s48, s48, 1\ns_cmp_gt_i32 s48, 0\n"
"s_cbranch_scc1 Lq530\ns_branch Lq531\nLqM:\nLqq1:\nv_bfe_u32 v250, v252, 7, 1\nv_mul_u32_u24 v250, 2176, v250\n"
"v_bfe_u32 v251, v252, 4, 3\nv_lshl_add_u32 v250, v251, 3, v250\ns_lshl_b32 s60, s90, 6\nv_add_u32 v250, s60, v250\ns_sleep 5\nv_mov_b32 v248, 0\n"
"v_mov_b32 v249, 0\ns_add_u32 s61, s80, 88358912\ns_mov_b64 exec, 0xffff\nbuffer_atomic_or_x2 v[248:249], v250, s[20:23], s61 offen sc0 sc1\ns_mov_b64 exec, -1\ns_add_u32 m0, s50, 0\n"
"s_add_u32 s63, s52, 397312\nbuffer_load_dwordx4 v220, s[4:7], s63 offen lds\nbuffer_load_dwordx4 v221, s[4:7], s63 offen offset:1024 lds\ns_add_u32 m0, s50, 8192\ns_add_u32 s63, s52, 593920\nbuffer_load_dwordx4 v220, s[4:7], s63 offen lds\n"
"buffer_load_dwordx4 v221, s[4:7], s63 offen offset:1024 lds\ns_add_u32 m0, s50, 16384\ns_add_u32 s63, s52, 788480\nbuffer_load_dwordx4 v220, s[4:7], s63 offen lds\nbuffer_load_dwordx4 v221, s[4:7], s63 offen offset:1024 lds\ns_add_u32 m0, s50, 24576\n"
"s_add_u32 s63, s52, 985088\nbuffer_load_dwordx4 v220, s[4:7], s63 offen lds\nbuffer_load_dwordx4 v221, s[4:7], s63 offen offset:1024 lds\ns_add_u32 m0, s50, 32768\ns_add_u32 s63, s52, 790528\nbuffer_load_dwordx4 v220, s[4:7], s63 offen lds\n"
"buffer_load_dwordx4 v221, s[4:7], s63 offen offset:1024 lds\ns_add_u32 m0, s50, 40960\ns_add_u32 s63, s52, 987136\nbuffer_load_dwordx4 v220, s[4:7], s63 offen lds\nbuffer_load_dwordx4 v221, s[4:7], s63 offen offset:1024 lds\nv_mov_b32 v230, 0\n"
"v_mov_b32 v231, 0\nv_mov_b32 v232, 0\nv_mov_b32 v233, 0\nv_dot2c_f32_bf16 v230, s76, v192\nv_dot2c_f32_bf16 v231, v192, v192\nv_dot2c_f32_bf16 v232, s76, v200\n"
"v_dot2c_f32_bf16 v233, v200, v200\nv_dot2c_f32_bf16 v230, s76, v193\nv_dot2c_f32_bf16 v231, v193, v193\nv_dot2c_f32_bf16 v232, s76, v201\nv_dot2c_f32_bf16 v233, v201, v201\nv_dot2c_f32_bf16 v230, s76, v194\n"
"v_dot2c_f32_bf16 v231, v194, v194\nv_dot2c_f32_bf16 v232, s76, v202\nv_dot2c_f32_bf16 v233, v202, v202\nv_dot2c_f32_bf16 v230, s76, v195\nv_dot2c_f32_bf16 v231, v195, v195\nv_dot2c_f32_bf16 v232, s76, v203\n"
"v_dot2c_f32_bf16 v233, v203, v203\nv_dot2c_f32_bf16 v230, s76, v196\nv_dot2c_f32_bf16 v231, v196, v196\nv_dot2c_f32_bf16 v232, s76, v204\nv_dot2c_f32_bf16 v233, v204, v204\nv_dot2c_f32_bf16 v230, s76, v197\n"
"v_dot2c_f32_bf16 v231, v197, v197\nv_dot2c_f32_bf16 v232, s76, v205\nv_dot2c_f32_bf16 v233, v205, v205\nv_dot2c_f32_bf16 v230, s76, v198\nv_dot2c_f32_bf16 v231, v198, v198\nv_dot2c_f32_bf16 v232, s76, v206\n"
"v_dot2c_f32_bf16 v233, v206, v206\nv_dot2c_f32_bf16 v230, s76, v199\nv_dot2c_f32_bf16 v231, v199, v199\nv_dot2c_f32_bf16 v232, s76, v207\nv_dot2c_f32_bf16 v233, v207, v207\nv_lshrrev_b32 v234, 1, v252\n"
"v_permlane32_swap_b32 v230, v231\ns_nop 0\nv_permlane32_swap_b32 v232, v233\nv_add_f32 v230, v230, v231\nv_add_f32 v232, v232, v233\nv_mov_b32 v231, s46\n"
"s_nop 0\nv_permlane16_swap_b32 v230, v232\nv_add_f32 v230, v230, v232\ns_lshr_b32 s61, s49, 3\ns_lshl2_add_u32 s61, s53, s61\ns_add_u32 s61, s61, 82837504\n"
"buffer_store_dwordx2 v[230:231], v234, s[20:23], s61 offen sc1\ns_waitcnt vmcnt(13)\nv_cmp_eq_u64 s[86:87], s[46:47], v[248:249]\ns_nop 0\ns_and_b32 s62, s86, 0xffff\ns_cmp_eq_u32 s62, 0xffff\n"
"s_cbranch_scc0 Lq594\nLq595:\ns_add_u32 s64, s70, 18874368\nbuffer_load_dwordx4 a[0:3], v252, s[20:23], s64 offen offset:0 sc1\ns_add_u32 s65, s70, 18907136\nbuffer_load_dwordx4 a[4:7], v252, s[20:23], s65 offen offset:0 sc1\n"
"s_add_u32 s66, s71, 18874368\nbuffer_load_dwordx4 a[8:11], v252, s[20:23], s66 offen offset:0 sc1\ns_add_u32 s67, s71, 18907136\nbuffer_load_dwordx4 a[12:15], v252, s[20:23], s67 offen offset:0 sc1\nbuffer_load_dwordx4 a[16:19], v252, s[20:23], s64 offen offset:1024 sc1\nbuffer_load_dwordx4 a[20:23], v252, s[20:23], s65 offen offset:1024 sc1\n"
"buffer_load_dwordx4 a[24:27], v252, s[20:23], s66 offen offset:1024 sc1\nbuffer_load_dwordx4 a[28:31], v252, s[20:23], s67 offen offset:1024 sc1\ns_barrier\nR 160,188,8192\nR 162,188,12288\nR 164,188,16384\n"
"R 166,188,20480\nR 168,189,8192\nR 170,189,12288\nR 172,189,16384\nR 174,189,20480\nR 176,190,8192\n"
"R 178,190,12288\nR 180,190,16384\nR 182,190,20480\nbuffer_load_dwordx4 a[32:35], v252, s[20:23], s64 offen offset:2048 sc1\nbuffer_load_dwordx4 a[36:39], v252, s[20:23], s65 offen offset:2048 sc1\nbuffer_load_dwordx4 a[40:43], v252, s[20:23], s66 offen offset:2048 sc1\n"
"buffer_load_dwordx4 a[44:47], v252, s[20:23], s67 offen offset:2048 sc1\nbuffer_load_dwordx4 a[48:51], v252, s[20:23], s64 offen offset:3072 sc1\nbuffer_load_dwordx4 a[52:55], v252, s[20:23], s65 offen offset:3072 sc1\nbuffer_load_dwordx4 a[56:59], v252, s[20:23], s66 offen offset:3072 sc1\nbuffer_load_dwordx4 a[60:63], v252, s[20:23], s67 offen offset:3072 sc1\ns_waitcnt vmcnt(15) lgkmcnt(10)\n"
"v_mfma_f32_16x16x32_bf16 v[0:3], v[160:163], a[0:3], 0\ns_add_u32 s64, s70, 18878464\nbuffer_load_dwordx4 a[64:67], v252, s[20:23], s64 offen offset:0 sc1\ns_waitcnt vmcnt(12) lgkmcnt(8)\nM 0,164,16\nv_mfma_f32_16x16x32_bf16 v[16:19], v[160:163], a[4:7], 0\n"
"s_waitcnt vmcnt(11)\nM 16,164,20\nv_mfma_f32_16x16x32_bf16 v[32:35], v[160:163], a[8:11], 0\ns_waitcnt vmcnt(10)\nM 32,164,24\nv_mfma_f32_16x16x32_bf16 v[48:51], v[160:163], a[12:15], 0\n"
"s_add_u32 s65, s70, 18911232\nbuffer_load_dwordx4 a[68:71], v252, s[20:23], s65 offen offset:0 sc1\ns_waitcnt vmcnt(10)\nM 48,164,28\nG 4\nv_mfma_f32_16x16x32_bf16 v[52:55], v[168:171], a[12:15], 0\n"
"R 160,191,8192\nM 52,172,28\nv_mfma_f32_16x16x32_bf16 v[36:39], v[168:171], a[8:11], 0\nR 162,191,12288\nM 36,172,24\nv_mfma_f32_16x16x32_bf16 v[20:23], v[168:171], a[4:7], 0\n"
"R 164,191,16384\nM 20,172,20\ns_add_u32 s66, s71, 18878464\nbuffer_load_dwordx4 a[72:75], v252, s[20:23], s66 offen offset:0 sc1\nv_mfma_f32_16x16x32_bf16 v[4:7], v[168:171], a[0:3], 0\nR 166,191,20480\n"
"M 4,172,16\nG 4\nv_mfma_f32_16x16x32_bf16 v[8:11], v[176:179], a[0:3], 0\nR 168,188,24576\nM 8,180,16\nv_mfma_f32_16x16x32_bf16 v[24:27], v[176:179], a[4:7], 0\n"
"R 170,188,28672\nM 24,180,20\ns_add_u32 m0, s50, 49152\nv_mfma_f32_16x16x32_bf16 v[40:43], v[176:179], a[8:11], 0\nR 172,188,32768\ns_add_u32 s63, s52, 1181696\n"
"M 40,180,24\ns_add_u32 s67, s71, 18911232\nbuffer_load_dwordx4 a[76:79], v252, s[20:23], s67 offen offset:0 sc1\nv_mfma_f32_16x16x32_bf16 v[56:59], v[176:179], a[12:15], 0\nLpg18:\ns_cbranch_execz Lpg22\n"
"R 174,188,36864\nM 56,180,28\nG 4\nv_mfma_f32_16x16x32_bf16 v[60:63], v[160:163], a[12:15], 0\nR 176,189,24576\nM 60,164,28\n"
"buffer_load_dwordx4 v220, s[4:7], s63 offen lds\nv_mfma_f32_16x16x32_bf16 v[44:47], v[160:163], a[8:11], 0\nR 178,189,28672\nM 44,164,24\nbuffer_load_dwordx4 a[80:83], v252, s[20:23], s64 offen offset:1024 sc1\nv_mfma_f32_16x16x32_bf16 v[28:31], v[160:163], a[4:7], 0\n"
"R 180,189,32768\nM 28,164,20\nv_mfma_f32_16x16x32_bf16 v[12:15], v[160:163], a[0:3], 0\nR 182,189,36864\nM 12,164,16\nG 4\n"
"v_mfma_f32_16x16x32_bf16 v[64:67], a[0:3], v[168:171], 0\nR 160,190,24576\nN 64,16,172\nbuffer_load_dwordx4 a[84:87], v252, s[20:23], s65 offen offset:1024 sc1\nv_mfma_f32_16x16x32_bf16 v[68:71], a[4:7], v[168:171], 0\nR 162,190,28672\n"
"N 68,20,172\nv_mfma_f32_16x16x32_bf16 v[96:99], a[8:11], v[168:171], 0\nR 164,190,32768\nN 96,24,172\nv_mfma_f32_16x16x32_bf16 v[100:103], a[12:15], v[168:171], 0\nR 166,190,36864\n"
"N 100,28,172\ns_barrier\nG 4\nv_mfma_f32_16x16x32_bf16 v[108:111], a[12:15], v[176:179], 0\nR 168,191,24576\nN 108,28,180\n"
"buffer_load_dwordx4 a[88:91], v252, s[20:23], s66 offen offset:1024 sc1\nv_mfma_f32_16x16x32_bf16 v[104:107], a[8:11], v[176:179], 0\nR 170,191,28672\nN 104,24,180\nv_mfma_f32_16x16x32_bf16 v[76:79], a[4:7], v[176:179], 0\nR 172,191,32768\n"
"N 76,20,180\nbuffer_load_dwordx4 v221, s[4:7], s63 offen offset:1024 lds\nv_mfma_f32_16x16x32_bf16 v[72:75], a[0:3], v[176:179], 0\nR 174,191,36864\nN 72,16,180\nG 4\n"
"v_mfma_f32_16x16x32_bf16 v[80:83], a[0:3], v[160:163], 0\nR 176,188,40960\ns_add_u32 s63, s52, 1378304\nN 80,16,164\nbuffer_load_dwordx4 a[92:95], v252, s[20:23], s67 offen offset:1024 sc1\nv_mfma_f32_16x16x32_bf16 v[84:87], a[4:7], v[160:163], 0\n"
"R 178,188,45056\nN 84,20,164\ns_add_u32 m0, s50, 57344\nv_mfma_f32_16x16x32_bf16 v[112:115], a[8:11], v[160:163], 0\nR 180,188,49152\nN 112,24,164\n"
"buffer_load_dwordx4 v220, s[4:7], s63 offen lds\nv_mfma_f32_16x16x32_bf16 v[116:119], a[12:15], v[160:163], 0\nR 182,188,53248\nN 116,28,164\nG 4\nv_mfma_f32_16x16x32_bf16 v[124:127], a[12:15], v[168:171], 0\n"
"R 160,189,40960\nN 124,28,172\nbuffer_load_dwordx4 a[96:99], v252, s[20:23], s64 offen offset:2048 sc1\nv_mfma_f32_16x16x32_bf16 v[120:123], a[8:11], v[168:171], 0\nR 162,189,45056\nN 120,24,172\n"
"v_mfma_f32_16x16x32_bf16 v[92:95], a[4:7], v[168:171], 0\nR 164,189,49152\nN 92,20,172\nbuffer_load_dwordx4 a[100:103], v252, s[20:23], s65 offen offset:2048 sc1\nv_mfma_f32_16x16x32_bf16 v[88:91], a[0:3], v[168:171], 0\nR 166,189,53248\n"
"N 88,16,172\nG 4\ns_waitcnt vmcnt(20)\nM 0,176,32\nR 168,190,40960\ns_waitcnt vmcnt(16)\n"
"M 0,180,48\nM 16,176,36\nR 170,190,45056\ns_waitcnt vmcnt(15)\nM 16,180,52\nbuffer_load_dwordx4 a[104:107], v252, s[20:23], s66 offen offset:2048 sc1\n"
"M 32,176,40\nR 172,190,49152\ns_waitcnt vmcnt(15)\nM 32,180,56\nM 48,176,44\nR 174,190,53248\n"
"s_waitcnt vmcnt(14)\nM 48,180,60\ns_barrier\nG 4\nM 52,160,44\nR 176,191,40960\n"
"M 52,164,60\nbuffer_load_dwordx4 v221, s[4:7], s63 offen offset:1024 lds\nM 36,160,40\nR 178,191,45056\nM 36,164,56\nbuffer_load_dwordx4 a[108:111], v252, s[20:23], s67 offen offset:2048 sc1\n"
"M 20,160,36\nR 180,191,49152\nM 20,164,52\nM 4,160,32\nR 182,191,53248\nM 4,164,48\n"
"G 4\nM 8,168,32\nR 160,184,0\ns_add_u32 s63, s52, 1183744\nM 8,172,48\nbuffer_load_dwordx4 a[112:115], v252, s[20:23], s64 offen offset:3072 sc1\n"
"M 24,168,36\nR 162,184,4096\nM 24,172,52\ns_add_u32 m0, s50, 65536\nM 40,168,40\nR 164,184,8192\n"
"M 40,172,56\nbuffer_load_dwordx4 v220, s[4:7], s63 offen lds\nM 56,168,44\nR 166,184,12288\nM 56,172,60\nG 4\n"
"M 60,176,44\nR 168,185,0\nM 60,180,60\nbuffer_load_dwordx4 a[116:119], v252, s[20:23], s65 offen offset:3072 sc1\nM 44,176,40\nR 170,185,4096\n"
"M 44,180,56\nM 28,176,36\nR 172,185,8192\nM 28,180,52\nM 12,176,32\nR 174,185,12288\n"
"M 12,180,48\nG 4\nN 64,32,160\nR 176,186,0\nN 64,48,164\nbuffer_load_dwordx4 a[120:123], v252, s[20:23], s66 offen offset:3072 sc1\n"
"N 68,36,160\nR 178,186,4096\nN 68,52,164\nN 96,40,160\nR 180,186,8192\nN 96,56,164\n"
"buffer_load_dwordx4 a[124:127], v252, s[20:23], s67 offen offset:3072 sc1\nN 100,44,160\nR 182,186,12288\nN 100,60,164\ns_barrier\nG 4\n"
"N 108,44,168\nR 160,187,0\nN 108,60,172\nbuffer_load_dwordx4 v221, s[4:7], s63 offen offset:1024 lds\nN 104,40,168\nR 162,187,4096\n"
"N 104,56,172\ns_add_u32 m0, s50, 73728\nN 76,36,168\nR 164,187,8192\ns_add_u32 s63, s52, 1380352\nN 76,52,172\n"
"s_add_u32 s64, s70, 18882560\nbuffer_load_dwordx4 a[128:131], v252, s[20:23], s64 offen offset:0 sc1\nN 72,32,168\nR 166,187,12288\nN 72,48,172\nG 4\n"
"N 80,32,176\nR 168,184,16384\nN 80,48,180\nbuffer_load_dwordx4 v220, s[4:7], s63 offen lds\nN 84,36,176\nR 170,184,20480\n"
"N 84,52,180\ns_add_u32 s65, s70, 18915328\nbuffer_load_dwordx4 a[132:135], v252, s[20:23], s65 offen offset:0 sc1\nN 112,40,176\nR 172,184,24576\nN 112,56,180\n"
"N 116,44,176\nR 174,184,28672\nN 116,60,180\nG 4\nN 124,44,160\nR 176,185,16384\n"
"N 124,60,164\ns_add_u32 s66, s71, 18882560\nbuffer_load_dwordx4 a[136:139], v252, s[20:23], s66 offen offset:0 sc1\nN 120,40,160\nR 178,185,20480\nN 120,56,164\n"
"N 92,36,160\nR 180,185,24576\nN 92,52,164\nN 88,32,160\nR 182,185,28672\nN 88,48,164\n"
"G 4\ns_waitcnt vmcnt(25)\nM 0,168,64\nR 160,186,16384\ns_waitcnt vmcnt(20)\nM 0,172,80\n"
"s_add_u32 s67, s71, 18915328\nbuffer_load_dwordx4 a[140:143], v252, s[20:23], s67 offen offset:0 sc1\nM 16,168,68\nR 162,186,20480\ns_waitcnt vmcnt(20)\nM 16,172,84\n"
"M 32,168,72\nR 164,186,24576\ns_waitcnt vmcnt(19)\nM 32,172,88\nbuffer_load_dwordx4 v221, s[4:7], s63 offen offset:1024 lds\nM 48,168,76\n"
"R 166,186,28672\ns_waitcnt vmcnt(18)\nM 48,172,92\ns_barrier\nG 4\nM 52,176,76\n"
"R 168,187,16384\nM 52,180,92\nbuffer_load_dwordx4 a[144:147], v252, s[20:23], s64 offen offset:1024 sc1\nM 36,176,72\nR 170,187,20480\nM 36,180,88\n"
"s_add_u32 m0, s50, 81920\nM 20,176,68\nR 172,187,24576\ns_add_u32 s63, s52, 1574912\nM 20,180,84\nbuffer_load_dwordx4 a[148:151], v252, s[20:23], s65 offen offset:1024 sc1\n"
"M 4,176,64\nR 174,187,28672\nM 4,180,80\nG 4\nM 8,160,64\nR 176,184,32768\n"
"M 8,164,80\nbuffer_load_dwordx4 v220, s[4:7], s63 offen lds\nM 24,160,68\nR 178,184,36864\nM 24,164,84\nM 40,160,72\n"
"R 180,184,40960\nM 40,164,88\nbuffer_load_dwordx4 a[152:155], v252, s[20:23], s66 offen offset:1024 sc1\nM 56,160,76\nR 182,184,45056\nM 56,164,92\n"
"G 4\nM 60,168,76\nR 160,185,32768\nM 60,172,92\nM 44,168,72\nR 162,185,36864\n"
"M 44,172,88\nbuffer_load_dwordx4 a[156:159], v252, s[20:23], s67 offen offset:1024 sc1\nM 28,168,68\nR 164,185,40960\nM 28,172,84\nM 12,168,64\n"
"R 166,185,45056\nM 12,172,80\nG 4\nN 64,64,176\nR 168,186,32768\nN 64,80,180\n"
"buffer_load_dwordx4 v221, s[4:7], s63 offen offset:1024 lds\nN 68,68,176\nR 170,186,36864\nN 68,84,180\nbuffer_load_dwordx4 a[160:163], v252, s[20:23], s64 offen offset:2048 sc1\nN 96,72,176\n"
"R 172,186,40960\nN 96,88,180\nN 100,76,176\nR 174,186,45056\nN 100,92,180\ns_waitcnt vmcnt(20)\n"
"s_barrier\nG 4\nN 108,76,160\nR 176,187,32768\ns_add_u32 s63, s52, 1771520\nN 108,92,164\n"
"buffer_load_dwordx4 a[164:167], v252, s[20:23], s65 offen offset:2048 sc1\nN 104,72,160\nR 178,187,36864\nN 104,88,164\ns_add_u32 m0, s50, 90112\nN 76,68,160\n"
"R 180,187,40960\nN 76,84,164\nbuffer_load_dwordx4 v220, s[4:7], s63 offen lds\nN 72,64,160\nR 182,187,45056\nN 72,80,164\n"
"G 4\nN 80,64,168\nR 160,184,49152\nN 80,80,172\nbuffer_load_dwordx4 a[168:171], v252, s[20:23], s66 offen offset:2048 sc1\nN 84,68,168\n"
"R 162,184,53248\nN 84,84,172\nN 112,72,168\nR 164,184,57344\nN 112,88,172\nbuffer_load_dwordx4 v221, s[4:7], s63 offen offset:1024 lds\n"
"N 116,76,168\nR 166,184,61440\nN 116,92,172\nG 4\nN 124,76,176\nR 168,185,49152\n"
"N 124,92,180\nbuffer_load_dwordx4 a[172:175], v252, s[20:23], s67 offen offset:2048 sc1\nN 120,72,176\nR 170,185,53248\nN 120,88,180\nN 92,68,176\n"
"R 172,185,57344\nN 92,84,180\nbuffer_load_dwordx4 a[176:179], v252, s[20:23], s64 offen offset:3072 sc1\nN 88,64,176\nR 174,185,61440\nN 88,80,180\n"
"G 4\nM 0,160,96\nR 176,186,49152\ns_waitcnt vmcnt(24)\nM 0,164,112\nM 16,160,100\n"
"R 178,186,53248\ns_waitcnt vmcnt(22)\nM 16,164,116\nbuffer_load_dwordx4 a[180:183], v252, s[20:23], s65 offen offset:3072 sc1\nM 32,160,104\nR 180,186,57344\n"
"s_add_u32 s63, s52, 1576960\ns_waitcnt vmcnt(22)\nM 32,164,120\ns_add_u32 m0, s50, 98304\nM 48,160,108\nR 182,186,61440\n"
"s_waitcnt vmcnt(21)\nM 48,164,124\ns_waitcnt vmcnt(14)\ns_barrier\nG 4\nM 52,168,108\n"
"R 160,187,49152\nM 52,172,124\nbuffer_load_dwordx4 v220, s[4:7], s63 offen lds\nM 36,168,104\nR 162,187,53248\nM 36,172,120\n"
"buffer_load_dwordx4 a[184:187], v252, s[20:23], s66 offen offset:3072 sc1\nM 20,168,100\nR 164,187,57344\nM 20,172,116\nbuffer_load_dwordx4 v221, s[4:7], s63 offen offset:1024 lds\nM 4,168,96\n"
"R 166,187,61440\ns_add_u32 m0, s50, 106496\nM 4,172,112\nG 4\nM 8,176,96\nR 168,188,0\n"
"s_add_u32 s63, s52, 1773568\nM 8,180,112\nbuffer_load_dwordx4 a[188:191], v252, s[20:23], s67 offen offset:3072 sc1\nM 24,176,100\nR 170,188,4096\nM 24,180,116\n"
"buffer_load_dwordx4 v220, s[4:7], s63 offen lds\nM 40,176,104\nR 172,188,8192\nM 40,180,120\nbuffer_load_dwordx4 v221, s[4:7], s63 offen offset:1024 lds\nM 56,176,108\n"
"R 174,188,12288\nM 56,180,124\nG 4\nM 60,160,108\nR 176,189,0\nM 60,164,124\n"
"s_add_u32 s64, s70, 18886656\nbuffer_load_dwordx4 a[192:195], v252, s[20:23], s64 offen offset:0 sc1\nM 44,160,104\nR 178,189,4096\nM 44,164,120\nM 28,160,100\n"
"R 180,189,8192\nM 28,164,116\nM 12,160,96\nR 182,189,12288\nM 12,164,112\nG 4\n"
"N 64,96,168\nR 160,190,0\nN 64,112,172\ns_add_u32 s65, s70, 18919424\nbuffer_load_dwordx4 a[196:199], v252, s[20:23], s65 offen offset:0 sc1\nN 68,100,168\n"
"R 162,190,4096\nN 68,116,172\ns_add_u32 m0, s50, 114688\nN 96,104,168\nR 164,190,8192\ns_add_u32 s63, s52, 1968128\n"
"N 96,120,172\ns_add_u32 s66, s71, 18886656\nbuffer_load_dwordx4 a[200:203], v252, s[20:23], s66 offen offset:0 sc1\nN 100,108,168\nR 166,190,12288\nN 100,124,172\n"
"s_waitcnt vmcnt(12)\ns_barrier\nG 4\nN 108,108,176\nR 168,191,0\nN 108,124,180\n"
"buffer_load_dwordx4 v220, s[4:7], s63 offen lds\nN 104,104,176\nR 170,191,4096\nN 104,120,180\nN 76,100,176\nR 172,191,8192\n"
"N 76,116,180\ns_add_u32 s67, s71, 18919424\nbuffer_load_dwordx4 a[204:207], v252, s[20:23], s67 offen offset:0 sc1\nN 72,96,176\nR 174,191,12288\nN 72,112,180\n"
"G 4\nN 80,96,160\nR 176,188,16384\nbuffer_load_dwordx4 v221, s[4:7], s63 offen offset:1024 lds\nN 80,112,164\ns_add_u32 m0, s50, 0\n"
"s_add_u32 s63, s52, 2164736\nbuffer_load_dwordx4 v220, s[4:7], s63 offen lds\nN 84,100,160\nR 178,188,20480\nN 84,116,164\nbuffer_load_dwordx4 a[208:211], v252, s[20:23], s64 offen offset:1024 sc1\n"
"N 112,104,160\nR 180,188,24576\nN 112,120,164\nbuffer_load_dwordx4 v221, s[4:7], s63 offen offset:1024 lds\nN 116,108,160\nR 182,188,28672\n"
"N 116,124,164\nG 4\nN 124,108,168\nR 160,189,16384\nN 124,124,172\nbuffer_load_dwordx4 a[212:215], v252, s[20:23], s65 offen offset:1024 sc1\n"
"N 120,104,168\nR 162,189,20480\nN 120,120,172\nN 92,100,168\nR 164,189,24576\nN 92,116,172\n"
"N 88,96,168\nR 166,189,28672\nN 88,112,172\nG 4\nM 0,176,128\nR 168,190,16384\n"
"M 0,180,144\nbuffer_load_dwordx4 a[216:219], v252, s[20:23], s66 offen offset:1024 sc1\nM 16,176,132\nR 170,190,20480\nM 16,180,148\nM 32,176,136\n"
"R 172,190,24576\nM 32,180,152\ns_add_u32 m0, s50, 8192\nM 48,176,140\nR 174,190,28672\ns_add_u32 s63, s52, 1970176\n"
"M 48,180,156\ns_waitcnt vmcnt(11)\ns_barrier\nG 4\nM 52,160,140\nR 176,191,16384\n"
"M 52,164,156\nbuffer_load_dwordx4 a[220:223], v252, s[20:23], s67 offen offset:1024 sc1\nM 36,160,136\nR 178,191,20480\nM 36,164,152\nbuffer_load_dwordx4 v220, s[4:7], s63 offen lds\n"
"M 20,160,132\nR 180,191,24576\nM 20,164,148\nbuffer_load_dwordx4 v221, s[4:7], s63 offen offset:1024 lds\nM 4,160,128\nR 182,191,28672\n"
"s_add_u32 m0, s50, 16384\nM 4,164,144\nG 4\nM 8,168,128\nR 160,188,32768\ns_add_u32 s63, s52, 2166784\n"
"M 8,172,144\nbuffer_load_dwordx4 a[224:227], v252, s[20:23], s64 offen offset:2048 sc1\nM 24,168,132\nR 162,188,36864\nM 24,172,148\nbuffer_load_dwordx4 v220, s[4:7], s63 offen lds\n"
"M 40,168,136\nR 164,188,40960\nM 40,172,152\nbuffer_load_dwordx4 a[228:231], v252, s[20:23], s65 offen offset:2048 sc1\nM 56,168,140\nR 166,188,45056\n"
"M 56,172,156\nG 4\nM 60,176,140\nR 168,189,32768\nbuffer_load_dwordx4 v221, s[4:7], s63 offen offset:1024 lds\nM 60,180,156\n"
"M 44,176,136\nR 170,189,36864\nLpg19:\ns_cbranch_execz LretW0n\nM 44,180,152\nbuffer_load_dwordx4 a[232:235], v252, s[20:23], s66 offen offset:2048 sc1\n"
"M 28,176,132\nR 172,189,40960\nM 28,180,148\nM 12,176,128\nR 174,189,45056\nM 12,180,144\n"
"G 4\nN 64,128,160\nR 176,190,32768\nN 64,144,164\nN 68,132,160\nR 178,190,36864\n"
"N 68,148,164\nbuffer_load_dwordx4 a[236:239], v252, s[20:23], s67 offen offset:2048 sc1\nN 96,136,160\nR 180,190,40960\nN 96,152,164\ns_add_u32 m0, s50, 24576\n"
"N 100,140,160\nR 182,190,45056\ns_add_u32 s63, s52, 2361344\nN 100,156,164\ns_waitcnt vmcnt(11)\ns_barrier\n"
"G 4\nN 108,140,168\nR 160,191,32768\nN 108,156,172\nbuffer_load_dwordx4 a[240:243], v252, s[20:23], s64 offen offset:3072 sc1\nN 104,136,168\n"
"R 162,191,36864\nN 104,152,172\nbuffer_load_dwordx4 v220, s[4:7], s63 offen lds\nN 76,132,168\nR 164,191,40960\nLq700:\n"
"s_lshr_b32 s60, s45, 3\ns_lshl2_add_u32 s60, s60, s40\ns_lshl_b32 s60, s60, 8\nv_lshl_add_u32 v224, v252, 3, s60\nv_add_u32 v224, Lq700+4096-LqPC, v224\nv_min_u32 v224, LqE-LqPC-4, v224\n"
"s_mov_b64 exec, 3\nglobal_load_dword v224, v224, s[98:99]\ns_mov_b64 exec, -1\nN 76,148,172\nbuffer_load_dwordx4 v221, s[4:7], s63 offen offset:1024 lds\nN 72,128,168\n"
"R 166,191,45056\ns_add_u32 m0, s50, 32768\nN 72,144,172\nG 4\nN 80,128,176\nR 168,188,49152\n"
"s_add_u32 s63, s52, 2557952\nN 80,144,180\nbuffer_load_dwordx4 a[244:247], v252, s[20:23], s65 offen offset:3072 sc1\nN 84,132,176\nR 170,188,53248\nN 84,148,180\n"
"buffer_load_dwordx4 v220, s[4:7], s63 offen lds\nN 112,136,176\nR 172,184,0\nN 112,152,180\nbuffer_load_dwordx4 v221, s[4:7], s63 offen offset:1024 lds\nN 116,140,176\n"
"R 174,184,4096\nN 116,156,180\nG 4\nN 124,140,160\nR 176,189,49152\nN 124,156,164\n"
"buffer_load_dwordx4 a[248:251], v252, s[20:23], s66 offen offset:3072 sc1\nN 120,136,160\nR 178,189,53248\nN 120,152,164\nN 92,132,160\nR 180,185,0\n"
"N 92,148,164\nbuffer_load_dwordx4 a[252:255], v252, s[20:23], s67 offen offset:3072 sc1\nN 88,128,160\nR 182,185,4096\nN 88,144,164\nG 4\n"
"M 0,168,160\nR 160,190,49152\nM 0,172,176\nM 16,168,164\nR 162,190,53248\nM 16,172,180\n"
"s_lshr_b32 s84, s49, 3\nM 32,168,168\nR 164,186,0\ns_add_u32 m0, s50, 40960\nM 32,172,184\ns_add_u32 s84, s84, 82837504\n"
"M 48,168,172\nR 166,186,4096\ns_add_u32 s63, s52, 2754560\nM 48,172,188\ns_waitcnt vmcnt(11)\ns_barrier\n"
"G 4\nM 52,176,172\nR 168,191,49152\nM 52,180,188\nbuffer_load_dwordx4 v220, s[4:7], s63 offen lds\nM 36,176,168\n"
"R 170,191,53248\nM 36,180,184\nbuffer_load_dwordx4 v[128:131], v252, s[20:23], s84 offen offset:0 sc1\nM 20,176,164\nR 172,187,0\nM 20,180,180\n"
"buffer_load_dwordx4 v[132:135], v252, s[20:23], s84 offen offset:1024 sc1\nM 4,176,160\nR 174,187,4096\nM 4,180,176\nG 4\nM 8,160,160\n"
"R 176,184,8192\nbuffer_load_dwordx4 v221, s[4:7], s63 offen offset:1024 lds\nM 8,164,176\nbuffer_load_dwordx4 v[136:139], v252, s[20:23], s84 offen offset:2048 sc1\ns_add_u32 s63, s52, 2951168\nM 24,160,164\n"
"R 178,184,12288\ns_add_u32 m0, s50, 49152\nM 24,164,180\nbuffer_load_dwordx4 v[140:143], v252, s[20:23], s84 offen offset:3072 sc1\nM 40,160,168\nR 180,184,16384\n"
"buffer_load_dwordx4 v220, s[4:7], s63 offen lds\nM 40,164,184\ns_lshr_b32 s85, s49, 3\nM 56,160,172\nR 182,184,20480\ns_add_u32 s85, s85, 82841600\n"
"M 56,164,188\nbuffer_load_dwordx4 v[144:147], v252, s[20:23], s85 offen offset:0 sc1\nG 4\nM 60,168,172\nR 160,185,8192\nbuffer_load_dwordx4 v221, s[4:7], s63 offen offset:1024 lds\n"
"M 60,172,188\nbuffer_load_dwordx4 v[148:151], v252, s[20:23], s85 offen offset:1024 sc1\nM 44,168,168\nR 162,185,12288\nM 44,172,184\nM 28,168,164\n"
"R 164,185,16384\nM 28,172,180\nbuffer_load_dwordx4 v[152:155], v252, s[20:23], s85 offen offset:2048 sc1\nM 12,168,160\nR 166,185,20480\nM 12,172,176\n"
"G 4\nN 64,160,176\nR 168,186,8192\nN 64,176,180\nbuffer_load_dwordx4 v[156:159], v252, s[20:23], s85 offen offset:3072 sc1\nN 68,164,176\n"
"R 170,186,12288\nN 68,180,180\ns_add_u32 m0, s50, 57344\nN 96,168,176\nR 172,186,16384\nN 96,184,180\n"
"s_add_u32 s63, s52, 2363392\nN 100,172,176\nR 174,186,20480\nN 100,188,180\ns_waitcnt vmcnt(14)\ns_barrier\n"
"G 4\nN 108,172,160\nR 176,187,8192\nN 108,188,164\nbuffer_load_dwordx4 v220, s[4:7], s63 offen lds\nN 104,168,160\n"
"R 178,187,12288\nN 104,184,164\nN 76,164,160\nR 180,187,16384\nN 76,180,164\nbuffer_load_dwordx4 v221, s[4:7], s63 offen offset:1024 lds\n"
"N 72,160,160\nR 182,187,20480\ns_add_u32 m0, s50, 65536\nN 72,176,164\nG 4\nN 80,160,168\n"
"R 160,184,24576\ns_add_u32 s63, s52, 2560000\nN 80,176,172\nbuffer_load_dwordx4 v220, s[4:7], s63 offen lds\nN 84,164,168\nR 162,184,28672\n"
"N 84,180,172\nN 112,168,168\nR 164,184,32768\nN 112,184,172\nbuffer_load_dwordx4 v221, s[4:7], s63 offen offset:1024 lds\nN 116,172,168\n"
"R 166,184,36864\nN 116,188,172\nG 4\nN 124,172,176\nR 168,185,24576\nN 124,188,180\n"
"N 120,168,176\nR 170,185,28672\nN 120,184,180\ns_add_u32 m0, s50, 73728\nN 92,164,176\nR 172,185,32768\n"
"N 92,180,180\ns_add_u32 s63, s52, 2756608\nN 88,160,176\nR 174,185,36864\nN 88,176,180\nG 4\n"
"M 0,160,192\nR 176,186,24576\nM 0,164,208\nbuffer_load_dwordx4 v220, s[4:7], s63 offen lds\nM 16,160,196\nR 178,186,28672\n"
"M 16,164,212\nM 32,160,200\nR 180,186,32768\nM 32,164,216\nbuffer_load_dwordx4 v221, s[4:7], s63 offen offset:1024 lds\nM 48,160,204\n"
"R 182,186,36864\ns_add_u32 m0, s50, 81920\nM 48,164,220\ns_waitcnt vmcnt(9)\ns_barrier\nG 4\n"
"M 52,168,204\nR 160,187,24576\ns_add_u32 s63, s52, 2953216\nM 52,172,220\nbuffer_load_dwordx4 v220, s[4:7], s63 offen lds\nM 36,168,200\n"
"R 162,187,28672\nv_min3_u32 v230, v129, v131, v133\nv_min3_u32 v230, v230, v135, v137\nM 36,172,216\nv_min3_u32 v230, v230, v139, v141\nv_min3_u32 v230, v230, v143, v145\n"
"s_waitcnt vmcnt(9)\nv_min3_u32 v230, v230, v147, v149\nM 20,168,196\nR 164,187,32768\ns_waitcnt vmcnt(8)\nv_min3_u32 v230, v230, v151, v153\n"
"s_waitcnt vmcnt(7)\nv_min3_u32 v230, v230, v155, v157\nM 20,172,212\nbuffer_load_dwordx4 v221, s[4:7], s63 offen offset:1024 lds\nv_min3_u32 v230, v230, v159, v159\nM 4,168,192\n"
"R 166,187,36864\ns_add_u32 m0, s50, 90112\nv_max3_u32 v231, v129, v131, v133\nM 4,172,208\nG 4\nv_max3_u32 v231, v231, v135, v137\n"
"v_max3_u32 v231, v231, v139, v141\nv_max3_u32 v231, v231, v143, v145\nM 8,176,192\nR 168,184,40960\ns_add_u32 s63, s52, 0\nv_max3_u32 v231, v231, v147, v149\n"
"M 8,180,208\nbuffer_load_dwordx4 v220, s[4:7], s63 offen lds\nv_max3_u32 v231, v231, v151, v153\nM 24,176,196\nR 170,184,45056\nv_max3_u32 v231, v231, v155, v157\n"
"v_max3_u32 v231, v231, v159, v159\nM 24,180,212\nv_cmp_ne_u32 s[86:87], s46, v230\nv_cmp_ne_u32 s[88:89], s46, v231\nM 40,176,200\nR 172,184,49152\n"
"s_or_b64 s[86:87], s[86:87], s[88:89]\ns_cbranch_scc1 Lq651\nLq652:\nM 40,180,216\nv_add_f32 v128, v128, v144\nv_add_f32 v130, v130, v146\n"
"v_add_f32 v132, v132, v148\nM 56,176,204\nR 174,184,53248\nv_add_f32 v134, v134, v150\nv_add_f32 v136, v136, v152\nM 56,180,220\n"
"G 4\nv_add_f32 v138, v138, v154\nv_add_f32 v140, v140, v156\nv_add_f32 v142, v142, v158\nM 60,160,204\nR 176,185,40960\n"
"v_add_f32 v128, v128, v136\nv_add_f32 v130, v130, v138\nM 60,164,220\nbuffer_load_dwordx4 v221, s[4:7], s63 offen offset:1024 lds\nv_add_f32 v132, v132, v140\nM 44,160,200\n"
"R 178,185,45056\ns_add_u32 s63, s52, 196608\nv_add_f32 v134, v134, v142\nM 44,164,216\ns_add_u32 m0, s50, 98304\nv_add_f32 v128, v128, v132\n"
"v_add_f32 v130, v130, v134\nv_lshrrev_b32 v231, 1, v252\nM 28,160,196\nR 180,185,49152\nv_lshrrev_b32 v230, 2, v252\ns_lshl_b32 s62, s40, 9\n"
"s_add_u32 s62, s62, 159232\nv_add_u32 v231, s62, v231\nM 28,164,212\nbuffer_load_dwordx4 v220, s[4:7], s63 offen lds\nds_write2_b32 v231, v128, v130 offset1:1\nM 12,160,192\n"
"R 182,185,53248\ns_lshl_b32 s62, s40, 9\ns_add_u32 s62, s62, 159232\nv_add_u32 v230, s62, v230\nM 12,164,208\nds_read2st64_b32 v[232:233], v230 offset1:1\n"
"G 6\nM 0,168,224\nR 160,186,40960\nM 0,172,240\nbuffer_load_dwordx4 v221, s[4:7], s63 offen offset:1024 lds\nM 16,168,228\n"
"R 162,186,45056\ns_add_u32 s63, s52, 393216\nM 16,172,244\ns_add_u32 m0, s50, 106496\nM 32,168,232\nR 164,186,49152\n"
"M 32,172,248\nbuffer_load_dwordx4 v220, s[4:7], s63 offen lds\nG 3\nv_add_f32 v232, v232, v233\nM 48,168,236\nR 166,186,53248\n"
"v_mov_b32 v233, v232\ns_nop 1\nv_permlane32_swap_b32 v232, v233\nM 48,172,252\ns_waitcnt vmcnt(9)\ns_barrier\n"
"v_mov_b32 v230, v232\nM 52,176,236\nR 168,187,40960\nv_mov_b32 v231, v233\nv_permlane16_swap_b32 v230, v232\nM 52,180,252\n"
"buffer_load_dwordx4 v221, s[4:7], s63 offen offset:1024 lds\nv_permlane16_swap_b32 v231, v233\nM 36,176,232\nR 170,187,45056\nv_mul_f32 v230, 0xba800000, v230\nv_mul_f32 v214, v230, v230\n"
"M 36,180,248\ns_add_u32 m0, s50, 114688\nv_mul_f32 v231, 0x3a800000, v231\nv_sub_f32 v231, v231, v214\nv_max_f32 v231, 0, v231\nM 20,176,228\n"
"R 172,187,49152\nv_add_f32 v231, 0x3727c5ac, v231\nv_rsq_f32 v234, v231\nM 20,180,244\ns_add_u32 s63, s52, 589824\nv_and_b32 v208, s77, v230\n"
"v_mul_f32 v216, v231, v234\nv_sub_f32 v230, v230, v208\nM 4,176,224\nR 174,187,53248\nv_cvt_pk_bf16_f32 v209, v230, v216\nv_cvt_pk_bf16_f32 v208, v208, v208\n"
"M 4,180,240\ns_mov_b64 s[60:61], 0xffff\nv_cndmask_b32 v208, 0, v208, s[60:61]\nG 4\nv_mov_b32 v214, v234\nM 8,160,224\n"
"R 176,184,57344\nv_mul_f32 v215, 0x3e8293ee, v234\nv_mul_f32 v232, 0xba800000, v232\nM 8,164,240\nbuffer_load_dwordx4 v220, s[4:7], s63 offen lds\nv_mul_f32 v217, v232, v232\n"
"M 24,160,228\nR 178,184,61440\nv_mul_f32 v233, 0x3a800000, v233\nv_sub_f32 v233, v233, v217\nM 24,164,244\ns_mov_b64 s[60:61], 0xffff\n"
"v_cndmask_b32 v209, 0, v209, s[60:61]\nv_max_f32 v233, 0, v233\nv_add_f32 v233, 0x3727c5ac, v233\nM 40,160,232\nR 180,188,0\nv_and_b32 v212, s77, v232\n"
"v_rsq_f32 v235, v233\nM 40,164,248\nbuffer_load_dwordx4 v221, s[4:7], s63 offen offset:1024 lds\nv_mul_f32 v219, v233, v235\nM 56,160,236\nR 182,188,4096\n"
"s_add_u32 m0, s50, 0\nv_sub_f32 v232, v232, v212\nM 56,164,252\nG 4\nv_cvt_pk_bf16_f32 v213, v232, v219\nv_cvt_pk_bf16_f32 v212, v212, v212\n"
"v_mov_b32 v217, v235\nM 60,168,236\nR 160,185,57344\ns_add_u32 s63, s52, 786432\nv_mul_f32 v218, 0x3e8293ee, v235\nM 60,172,252\n"
"buffer_load_dwordx4 v220, s[4:7], s63 offen lds\nv_bfe_u32 v230, v252, 2, 6\nM 44,168,232\nR 162,185,61440\ns_mov_b64 s[60:61], 0xffff\nv_cndmask_b32 v212, 0, v212, s[60:61]\n"
"M 44,172,248\ns_mov_b64 s[60:61], 0xffff\nv_cndmask_b32 v213, 0, v213, s[60:61]\ns_lshl_b32 s60, s40, 7\ns_add_u32 s60, s60, 161280\nv_add_u32 v230, s60, v230\n"
"M 28,168,228\nR 164,189,0\nds_write_b32 v230, v214 offset:0\nM 28,172,244\nbuffer_load_dwordx4 v221, s[4:7], s63 offen offset:1024 lds\nds_write_b32 v230, v217 offset:64\n"
"M 12,168,224\nR 166,189,4096\ns_mov_b32 s60, 156160\nv_bfe_u32 v246, v252, 4, 4\nv_lshl_add_u32 v246, v246, 3, s60\nM 12,172,240\n"
"ds_read_b64 v[238:239], v246 offset:0\ns_waitcnt vmcnt(10) lgkmcnt(0)\ns_barrier\nN 64,192,176\nR 168,186,57344\nds_read_b64 v[240:241], v246 offset:128\n"
"N 64,208,180\ns_mov_b32 m0, s51\nds_read_b64 v[242:243], v246 offset:256\nN 68,196,176\nR 170,186,61440\nds_read_b64 v[244:245], v246 offset:384\n"
"ds_write_addtid_b32 v32 offset:0\nN 68,212,180\nds_read_b64 v[246:247], v246 offset:0\nds_write_addtid_b32 v33 offset:256\nN 96,200,176\nR 172,190,0\n"
"ds_write_addtid_b32 v34 offset:512\nN 96,216,180\nds_write_addtid_b32 v35 offset:768\nds_write_addtid_b32 v36 offset:1024\nN 100,204,176\nR 174,190,4096\n"
"ds_write_addtid_b32 v37 offset:1280\nN 100,220,180\nds_write_addtid_b32 v38 offset:1536\nds_write_addtid_b32 v39 offset:1792\nN 108,204,160\nR 176,187,57344\n"
"ds_write_addtid_b32 v40 offset:2048\nN 108,220,164\nds_write_addtid_b32 v41 offset:2304\nds_write_addtid_b32 v42 offset:2560\nN 104,200,160\nR 178,187,61440\n"
"ds_write_addtid_b32 v43 offset:2816\nN 104,216,164\nds_write_addtid_b32 v44 offset:3072\nds_write_addtid_b32 v45 offset:3328\nN 76,196,160\nR 180,191,0\n"
"G 15\nv_mfma_f32_16x16x32_bf16 v[0:3], v[238:241], v[208:211], v[0:3]\nds_write_addtid_b32 v46 offset:3584\nN 76,212,164\nv_mfma_f32_16x16x32_bf16 v[4:7], v[240:243], v[208:211], v[4:7]\nds_write_addtid_b32 v47 offset:3840\n"
"ds_write_addtid_b32 v48 offset:4096\nN 72,192,160\nR 182,191,4096\nv_mfma_f32_16x16x32_bf16 v[8:11], v[242:245], v[208:211], v[8:11]\nds_write_addtid_b32 v49 offset:4352\nN 72,208,164\n"
"v_mfma_f32_16x16x32_bf16 v[12:15], v[244:247], v[208:211], v[12:15]\nds_write_addtid_b32 v50 offset:4608\nds_write_addtid_b32 v51 offset:4864\nG 15\nN 80,192,168\nR 160,188,8192\n"
"v_mfma_f32_16x16x32_bf16 v[16:19], v[244:247], v[210:213], v[16:19]\nds_write_addtid_b32 v52 offset:5120\nN 80,208,172\nv_mfma_f32_16x16x32_bf16 v[20:23], v[238:241], v[210:213], v[20:23]\nds_write_addtid_b32 v53 offset:5376\nds_write_addtid_b32 v54 offset:5632\n"
"N 84,196,168\nR 162,188,12288\nv_mfma_f32_16x16x32_bf16 v[24:27], v[240:243], v[210:213], v[24:27]\nds_write_addtid_b32 v55 offset:5888\nN 84,212,172\nv_mfma_f32_16x16x32_bf16 v[28:31], v[242:245], v[210:213], v[28:31]\n"
"ds_write_addtid_b32 v56 offset:6144\nds_write_addtid_b32 v57 offset:6400\nN 112,200,168\nR 164,188,16384\nds_write_addtid_b32 v58 offset:6656\nN 112,216,172\n"
"ds_write_addtid_b32 v59 offset:6912\nds_write_addtid_b32 v60 offset:7168\nN 116,204,168\nR 166,188,20480\nds_write_addtid_b32 v61 offset:7424\nN 116,220,172\n"
"ds_write_addtid_b32 v62 offset:7680\nds_write_addtid_b32 v63 offset:7936\nG 15\nN 124,204,176\nR 168,189,8192\ns_add_u32 m0, s50, 8192\n"
"s_add_u32 s63, s52, 983040\nN 124,220,180\nbuffer_load_dwordx4 v220, s[4:7], s63 offen lds\nN 120,200,176\nR 170,189,12288\ns_waitcnt vmcnt(7) lgkmcnt(2)\n"
"s_barrier\nN 120,216,180\nds_read2st64_b32 v[32:33], v225 offset0:0 offset1:1\nds_read2st64_b32 v[34:35], v225 offset0:2 offset1:3\nN 92,196,176\nR 172,189,16384\n"
"ds_read2st64_b32 v[36:37], v225 offset0:4 offset1:5\nN 92,212,180\nLpg20:\ns_cbranch_execz LretW0n\nbuffer_load_dwordx4 v221, s[4:7], s63 offen offset:1024 lds\nds_read2st64_b32 v[38:39], v225 offset0:6 offset1:7\n"
"N 88,192,176\nR 174,189,20480\nds_read2st64_b32 v[40:41], v225 offset0:8 offset1:9\nN 88,208,180\nds_read2st64_b32 v[42:43], v225 offset0:10 offset1:11\nds_read2st64_b32 v[44:45], v225 offset0:12 offset1:13\n"
"N 64,224,160\nR 176,190,8192\ns_mov_b32 s60, 156672\nv_bfe_u32 v246, v252, 4, 4\nv_lshl_add_u32 v246, v246, 3, s60\nds_read2st64_b32 v[46:47], v225 offset0:14 offset1:15\n"
"N 64,240,164\nds_read_b64 v[238:239], v246 offset:0\nds_read2st64_b32 v[48:49], v225 offset0:16 offset1:17\nG 12\nv_add_f32 v0, v0, v32\nN 68,228,160\n"
"R 178,190,12288\nds_read_b64 v[240:241], v246 offset:128\nds_read2st64_b32 v[50:51], v225 offset0:18 offset1:19\nN 68,244,164\nds_read_b64 v[242:243], v246 offset:256\ns_add_u32 m0, s50, 16384\n"
"ds_read2st64_b32 v[52:53], v225 offset0:20 offset1:21\nN 96,232,160\nR 180,190,16384\nds_read_b64 v[244:245], v246 offset:384\nds_read2st64_b32 v[54:55], v225 offset0:22 offset1:23\nN 96,248,164\n"
"ds_read_b64 v[246:247], v246 offset:0\ns_add_u32 s63, s52, 1179648\nds_read2st64_b32 v[56:57], v225 offset0:24 offset1:25\nN 100,236,160\nR 182,190,20480\nds_read2st64_b32 v[58:59], v225 offset0:26 offset1:27\n"
"v_add_f32 v1, v1, v33\nN 100,252,164\nds_read2st64_b32 v[60:61], v225 offset0:28 offset1:29\nds_read2st64_b32 v[62:63], v225 offset0:30 offset1:31\nG 15\nN 108,236,168\n"
"R 160,191,8192\nv_add_f32 v2, v2, v34\nv_add_f32 v3, v3, v35\nN 108,252,172\nbuffer_load_dwordx4 v220, s[4:7], s63 offen lds\nv_add_f32 v4, v4, v36\n"
"N 104,232,168\nR 162,191,12288\nv_add_f32 v5, v5, v37\nv_add_f32 v6, v6, v38\nN 104,248,172\nv_add_f32 v7, v7, v39\n"
"v_add_f32 v8, v8, v40\nv_add_f32 v9, v9, v41\nN 76,228,168\nR 164,191,16384\nv_add_f32 v10, v10, v42\nv_add_f32 v11, v11, v43\n"
"N 76,244,172\nG 15\nv_mfma_f32_16x16x32_bf16 v[64:67], v[208:211], v[238:241], v[64:67]\nbuffer_load_dwordx4 v221, s[4:7], s63 offen offset:1024 lds\nv_add_f32 v12, v12, v44\nN 72,224,168\n"
"R 166,191,20480\ns_add_u32 m0, s50, 24576\nv_add_f32 v13, v13, v45\nN 72,240,172\nG 14\nv_mfma_f32_16x16x32_bf16 v[72:75], v[208:211], v[240:243], v[72:75]\n"
"G 7\nv_add_f32 v14, v14, v46\nv_add_f32 v15, v15, v47\nv_add_f32 v16, v16, v48\nN 80,224,176\nR 168,188,24576\n"
"s_add_u32 s63, s52, 1376256\nv_add_f32 v17, v17, v49\nN 80,240,180\nv_mfma_f32_16x16x32_bf16 v[80:83], v[208:211], v[242:245], v[80:83]\nbuffer_load_dwordx4 v220, s[4:7], s63 offen lds\nv_add_f32 v18, v18, v50\n"
"N 84,228,176\nR 170,188,28672\nv_add_f32 v19, v19, v51\nv_add_f32 v20, v20, v52\nN 84,244,180\nv_mfma_f32_16x16x32_bf16 v[88:91], v[208:211], v[244:247], v[88:91]\n"
"buffer_load_dwordx4 v221, s[4:7], s63 offen offset:1024 lds\nv_add_f32 v21, v21, v53\nN 112,232,176\nR 172,188,32768\ns_add_u32 s63, s52, 1572864\nv_add_f32 v22, v22, v54\n"
"N 112,248,180\nv_mfma_f32_16x16x32_bf16 v[68:71], v[210:213], v[244:247], v[68:71]\ns_add_u32 m0, s50, 49152\nv_add_f32 v23, v23, v55\nv_add_f32 v24, v24, v56\nv_add_f32 v25, v25, v57\n"
"N 116,236,176\nR 174,188,36864\nG 10\nv_add_f32 v26, v26, v58\nv_add_f32 v27, v27, v59\nN 116,252,180\n"
"v_mfma_f32_16x16x32_bf16 v[76:79], v[210:213], v[238:241], v[76:79]\nG 4\nv_add_f32 v28, v28, v60\nv_add_f32 v29, v29, v61\nv_add_f32 v30, v30, v62\nN 124,236,160\n"
"R 176,189,24576\nv_add_f32 v31, v31, v63\nN 124,252,164\nv_mfma_f32_16x16x32_bf16 v[84:87], v[210:213], v[240:243], v[84:87]\nbuffer_load_dwordx4 v220, s[4:7], s63 offen lds\nN 120,232,160\n"
"R 178,189,28672\ns_lshl4_add_u32 s64, s41, s42\nv_lshrrev_b32 v227, 1, v252\nN 120,248,164\nv_mfma_f32_16x16x32_bf16 v[92:95], v[210:213], v[242:245], v[92:95]\nbuffer_load_dwordx4 v221, s[4:7], s63 offen offset:1024 lds\n"
"v_mul_f32 v16, v16, v217\nN 92,228,160\nR 180,189,32768\ns_add_u32 m0, s50, 57344\ns_add_u32 s63, s52, 1769472\nv_mul_f32 v17, v17, v217\n"
"N 92,244,164\nbuffer_load_dwordx4 v220, s[4:7], s63 offen lds\nv_mul_f32 v18, v18, v217\nN 88,224,160\nR 182,189,36864\ns_lshl_b32 s64, s64, 12\n"
"v_mul_f32 v19, v19, v217\nN 88,240,164\ns_waitcnt vmcnt(11)\ns_barrier\nG 4\nv_mul_f32 v20, v20, v217\n"
"v_mfma_f32_16x16x32_bf16 v[128:131], v[168:171], a[0:3], 0\nR 160,190,24576\ns_add_u32 s64, s64, 78643200\nv_mul_f32 v21, v21, v217\nM 128,172,16\ns_mov_b32 m0, s51\n"
"s_cmp_eq_u32 s40, 3\ns_cselect_b64 s[66:67], -1, 0\nv_mul_f32 v22, v22, v217\nv_mfma_f32_16x16x32_bf16 v[144:147], v[168:171], a[4:7], 0\nR 162,190,28672\nv_mul_f32 v23, v23, v217\n"
"v_mul_f32 v24, v24, v217\nM 144,172,20\nv_mul_f32 v25, v25, v217\nv_mul_f32 v26, v26, v217\nv_mul_f32 v27, v27, v217\nv_mfma_f32_16x16x32_bf16 v[32:35], v[168:171], a[8:11], 0\n"
"R 164,190,32768\nv_mul_f32 v28, v28, v217\nv_mul_f32 v29, v29, v217\nM 32,172,24\nv_mul_f32 v30, v30, v217\nv_mul_f32 v31, v31, v217\n"
"v_cvt_pk_bf16_f32 v16, v16, v17\nv_mfma_f32_16x16x32_bf16 v[48:51], v[168:171], a[12:15], 0\nR 166,190,36864\nv_cvt_pk_bf16_f32 v17, v18, v19\nv_cvt_pk_bf16_f32 v18, v20, v21\nM 48,172,28\n"
"ds_write_addtid_b32 v96 offset:0\nG 5\nv_cvt_pk_bf16_f32 v19, v22, v23\nv_cvt_pk_bf16_f32 v20, v24, v25\nv_cvt_pk_bf16_f32 v21, v26, v27\nv_mfma_f32_16x16x32_bf16 v[52:55], v[176:179], a[12:15], 0\n"
"R 168,191,24576\nds_write_addtid_b32 v97 offset:256\nv_cvt_pk_bf16_f32 v22, v28, v29\nv_cvt_pk_bf16_f32 v23, v30, v31\nM 52,180,28\nds_write_addtid_b32 v98 offset:512\n"
"v_mul_f32 v0, v0, v214\nv_mul_f32 v1, v1, v214\nv_mul_f32 v2, v2, v214\nv_mfma_f32_16x16x32_bf16 v[36:39], v[176:179], a[8:11], 0\nR 170,191,28672\nds_write_addtid_b32 v99 offset:768\n"
"v_mul_f32 v3, v3, v214\nv_mul_f32 v4, v4, v214\nM 36,180,24\nds_write_addtid_b32 v100 offset:1024\nv_mul_f32 v5, v5, v214\nv_mul_f32 v6, v6, v214\n"
"v_mul_f32 v7, v7, v214\nv_mfma_f32_16x16x32_bf16 v[148:151], v[176:179], a[4:7], 0\nR 172,191,32768\nds_write_addtid_b32 v101 offset:1280\nv_mul_f32 v8, v8, v214\nv_mul_f32 v9, v9, v214\n"
"M 148,180,20\nds_write_addtid_b32 v102 offset:1536\nv_mul_f32 v10, v10, v214\nv_mul_f32 v11, v11, v214\nv_mul_f32 v12, v12, v214\nv_mfma_f32_16x16x32_bf16 v[132:135], v[176:179], a[0:3], 0\n"
"R 174,191,36864\nds_write_addtid_b32 v103 offset:1792\nv_mul_f32 v13, v13, v214\nv_mul_f32 v14, v14, v214\nM 132,180,16\nds_write_addtid_b32 v104 offset:2048\n"
"G 13\nv_mul_f32 v15, v15, v214\nv_cvt_pk_bf16_f32 v0, v0, v1\nv_cvt_pk_bf16_f32 v1, v2, v3\nv_mfma_f32_16x16x32_bf16 v[136:139], v[160:163], a[0:3], 0\nR 176,188,40960\n"
"ds_write_addtid_b32 v105 offset:2304\nv_cvt_pk_bf16_f32 v2, v4, v5\nv_cvt_pk_bf16_f32 v3, v6, v7\nM 136,164,16\nds_write_addtid_b32 v106 offset:2560\nv_cvt_pk_bf16_f32 v4, v8, v9\n"
"v_cvt_pk_bf16_f32 v5, v10, v11\nv_cvt_pk_bf16_f32 v6, v12, v13\nv_mfma_f32_16x16x32_bf16 v[152:155], v[160:163], a[4:7], 0\nR 178,188,45056\nds_write_addtid_b32 v107 offset:2816\ns_mov_b64 exec, s[66:67]\n"
"buffer_store_dwordx4 v[16:19], v252, s[20:23], s64 offen offset:0 sc1\ns_mov_b64 exec, -1\nv_cvt_pk_bf16_f32 v7, v14, v15\nM 152,164,20\nds_write_addtid_b32 v108 offset:3072\nds_write_addtid_b32 v109 offset:3328\n"
"v_mfma_f32_16x16x32_bf16 v[40:43], v[160:163], a[8:11], 0\nR 180,188,49152\nds_write_addtid_b32 v110 offset:3584\nM 40,164,24\nds_write_addtid_b32 v111 offset:3840\nds_write_addtid_b32 v112 offset:4096\n"
"v_mfma_f32_16x16x32_bf16 v[56:59], v[160:163], a[12:15], 0\nR 182,188,53248\nds_write_addtid_b32 v113 offset:4352\ns_mov_b64 exec, s[66:67]\nbuffer_store_dwordx4 v[20:23], v252, s[20:23], s64 offen offset:1024 sc1\ns_mov_b64 exec, -1\n"
"M 56,164,28\nds_write_addtid_b32 v114 offset:4608\nds_write_addtid_b32 v115 offset:4864\nG 15\nv_mfma_f32_16x16x32_bf16 v[60:63], v[168:171], a[12:15], 0\nR 160,189,40960\n"
"ds_write_addtid_b32 v116 offset:5120\nM 60,172,28\nds_write_addtid_b32 v117 offset:5376\nds_write_addtid_b32 v118 offset:5632\nv_mfma_f32_16x16x32_bf16 v[44:47], v[168:171], a[8:11], 0\nR 162,189,45056\n"
"ds_write_addtid_b32 v119 offset:5888\nM 44,172,24\nds_write_addtid_b32 v120 offset:6144\nds_write_addtid_b32 v121 offset:6400\nds_write_addtid_b32 v122 offset:6656\nds_write_addtid_b32 v123 offset:6912\n"
"ds_write_addtid_b32 v124 offset:7168\nds_write_addtid_b32 v125 offset:7424\nds_write_addtid_b32 v126 offset:7680\nds_write_addtid_b32 v127 offset:7936\nv_mfma_f32_16x16x32_bf16 v[156:159], v[168:171], a[4:7], 0\nR 164,189,49152\n"
"s_add_u32 m0, s50, 57344\nM 156,172,20\nbuffer_load_dwordx4 v221, s[4:7], s63 offen offset:1024 lds\nv_mfma_f32_16x16x32_bf16 v[140:143], v[168:171], a[0:3], 0\nR 166,189,53248\ns_waitcnt vmcnt(10) lgkmcnt(2)\n"
"s_barrier\nM 140,172,16\nds_read2st64_b32 v[96:97], v225 offset0:0 offset1:1\nds_read2st64_b32 v[98:99], v225 offset0:2 offset1:3\nv_lshrrev_b32 v226, 8, v252\nM 128,176,32\n"
"R 168,190,40960\ns_add_u32 s63, s52, 1966080\nds_read2st64_b32 v[100:101], v225 offset0:4 offset1:5\ns_lshl_b32 s60, s40, 7\ns_add_u32 s60, s60, 161280\nv_lshl_add_u32 v226, v226, 4, s60\n"
"M 128,180,48\ns_add_u32 m0, s50, 65536\nds_read2st64_b32 v[102:103], v225 offset0:6 offset1:7\nds_read2st64_b32 v[104:105], v225 offset0:8 offset1:9\nds_read_b128 v[230:233], v226 offset:0\nM 144,176,36\n"
"R 170,190,45056\nds_read2st64_b32 v[106:107], v225 offset0:10 offset1:11\nds_read_b128 v[234:237], v226 offset:64\nM 144,180,52\nbuffer_load_dwordx4 v220, s[4:7], s63 offen lds\nds_read2st64_b32 v[108:109], v225 offset0:12 offset1:13\n"
"M 32,176,40\nR 172,190,49152\nds_read2st64_b32 v[110:111], v225 offset0:14 offset1:15\nM 32,180,56\nds_read2st64_b32 v[112:113], v225 offset0:16 offset1:17\nds_read2st64_b32 v[114:115], v225 offset0:18 offset1:19\n"
"G 14\nv_add_f32 v64, v64, v96\nM 48,176,44\nR 174,190,53248\nds_read2st64_b32 v[116:117], v225 offset0:20 offset1:21\nv_add_f32 v65, v65, v97\n"
"M 48,180,60\nds_read2st64_b32 v[118:119], v225 offset0:22 offset1:23\nds_read2st64_b32 v[120:121], v225 offset0:24 offset1:25\nM 52,160,44\nR 176,191,40960\nds_read2st64_b32 v[122:123], v225 offset0:26 offset1:27\n"
"G 15\nv_add_f32 v66, v66, v98\nM 52,164,60\nbuffer_load_dwordx4 v221, s[4:7], s63 offen offset:1024 lds\nds_read2st64_b32 v[124:125], v225 offset0:28 offset1:29\nM 36,160,40\n"
"R 178,191,45056\nds_read2st64_b32 v[126:127], v225 offset0:30 offset1:31\nv_add_f32 v67, v67, v99\nM 36,164,56\ns_add_u32 m0, s50, 73728\nv_add_f32 v68, v68, v100\n"
"v_add_f32 v69, v69, v101\nv_add_f32 v70, v70, v102\nM 148,160,36\nR 180,191,49152\nv_add_f32 v71, v71, v103\nv_add_f32 v72, v72, v104\n"
"M 148,164,52\ns_add_u32 s63, s52, 2162688\nv_add_f32 v73, v73, v105\nG 15\nv_add_f32 v74, v74, v106\nv_add_f32 v75, v75, v107\n"
"M 132,160,32\nR 182,191,53248\nG 15\nv_add_f32 v76, v76, v108\nv_add_f32 v77, v77, v109\nM 132,164,48\n"
"G 10\nv_add_f32 v78, v78, v110\nv_add_f32 v79, v79, v111\nv_add_f32 v80, v80, v112\nM 136,168,32\nR 160,184,0\n"
"v_add_f32 v81, v81, v113\nv_add_f32 v82, v82, v114\nM 136,172,48\nbuffer_load_dwordx4 v220, s[4:7], s63 offen lds\nv_add_f32 v83, v83, v115\nM 152,168,36\n"
"R 162,184,4096\nG 11\nv_add_f32 v84, v84, v116\nM 152,172,52\nv_add_f32 v85, v85, v117\nG 10\n"
"v_add_f32 v86, v86, v118\nM 40,168,40\nR 164,184,8192\nv_add_f32 v87, v87, v119\nM 40,172,56\nbuffer_load_dwordx4 v221, s[4:7], s63 offen offset:1024 lds\n"
"M 56,168,44\nR 166,184,12288\ns_add_u32 m0, s50, 81920\nM 56,172,60\nG 4\nv_add_f32 v88, v88, v120\n"
"v_add_f32 v89, v89, v121\nM 60,176,44\nR 168,185,0\ns_add_u32 s63, s52, 2359296\nM 60,180,60\nbuffer_load_dwordx4 v220, s[4:7], s63 offen lds\n"
"M 44,176,40\nR 170,185,4096\nv_add_f32 v90, v90, v122\nM 44,180,56\nv_add_f32 v91, v91, v123\nv_add_f32 v92, v92, v124\n"
"M 156,176,36\nR 172,185,8192\nv_add_f32 v93, v93, v125\nM 156,180,52\nbuffer_load_dwordx4 v221, s[4:7], s63 offen offset:1024 lds\nM 140,176,32\n"
"R 174,185,12288\ns_add_u32 m0, s50, 90112\nM 140,180,48\nG 4\nv_add_f32 v94, v94, v126\nv_add_f32 v95, v95, v127\n"
"M 128,160,64\nR 176,186,0\ns_add_u32 s63, s52, 2555904\nv_mul_f32 v64, v64, v230\nM 128,164,80\nbuffer_load_dwordx4 v220, s[4:7], s63 offen lds\n"
"v_mul_f32 v65, v65, v231\nM 144,160,68\nR 178,186,4096\nv_mul_f32 v66, v66, v232\nv_mul_f32 v67, v67, v233\nM 144,164,84\n"
"v_mul_f32 v68, v68, v234\nv_mul_f32 v69, v69, v235\nv_mul_f32 v70, v70, v236\nM 32,160,72\nR 180,186,8192\nv_mul_f32 v71, v71, v237\n"
"v_cvt_pk_bf16_f32 v67, v66, v67\nM 32,164,88\nv_cvt_pk_bf16_f32 v66, v64, v65\nv_cvt_pk_bf16_f32 v68, v68, v69\nv_cvt_pk_bf16_f32 v69, v70, v71\nM 48,160,76\n"
"R 182,186,12288\nv_mul_f32 v72, v72, v230\nv_mul_f32 v73, v73, v231\nM 48,164,92\nG 4\nv_mul_f32 v74, v74, v232\n"
"v_mul_f32 v75, v75, v233\nv_mul_f32 v76, v76, v234\nM 52,168,76\nR 160,187,0\nv_mul_f32 v77, v77, v235\nv_mul_f32 v78, v78, v236\n"
"M 52,172,92\nbuffer_load_dwordx4 v221, s[4:7], s63 offen offset:1024 lds\nv_mul_f32 v79, v79, v237\nM 36,168,72\nR 162,187,4096\ns_add_u32 s63, s52, 2752512\n"
"v_cvt_pk_bf16_f32 v72, v72, v73\nM 36,172,88\ns_add_u32 m0, s50, 98304\nv_cvt_pk_bf16_f32 v73, v74, v75\nv_cvt_pk_bf16_f32 v74, v76, v77\nv_cvt_pk_bf16_f32 v75, v78, v79\n"
"M 148,168,68\nR 164,187,8192\nv_mul_f32 v80, v80, v230\nv_mul_f32 v81, v81, v231\nM 148,172,84\nbuffer_load_dwordx4 v220, s[4:7], s63 offen lds\n"
"v_mul_f32 v82, v82, v232\nM 132,168,64\nR 166,187,12288\ns_waitcnt vmcnt(9)\ns_barrier\nv_mul_f32 v83, v83, v233\n"
"M 132,172,80\nG 4\nv_mul_f32 v84, v84, v234\nv_mul_f32 v85, v85, v235\nv_mul_f32 v86, v86, v236\nM 136,176,64\n"
"R 168,184,16384\nv_mul_f32 v87, v87, v237\nv_cvt_pk_bf16_f32 v78, v80, v81\nM 136,180,80\ns_mov_b64 exec, s[66:67]\nbuffer_store_dwordx2 v[68:69], v227, s[20:23], s64 offen offset:2048 sc1\n"
"s_mov_b64 exec, -1\nv_cvt_pk_bf16_f32 v79, v82, v83\nv_cvt_pk_bf16_f32 v80, v84, v85\nM 152,176,68\nR 170,184,20480\nv_cvt_pk_bf16_f32 v81, v86, v87\n"
"v_mul_f32 v88, v88, v230\nM 152,180,84\nv_mul_f32 v89, v89, v231\nv_mul_f32 v90, v90, v232\nv_mul_f32 v91, v91, v233\nM 40,176,72\n"
"R 172,184,24576\ns_mov_b32 s60, 155648\nv_bfe_u32 v246, v252, 4, 4\nv_lshl_add_u32 v246, v246, 3, s60\nv_mul_f32 v92, v92, v234\nM 40,180,88\n"
"buffer_load_dwordx4 v221, s[4:7], s63 offen offset:1024 lds\nv_mul_f32 v93, v93, v235\nM 56,176,76\nR 174,184,28672\nds_read_b64 v[238:239], v246 offset:0\nv_mul_f32 v94, v94, v236\n"
"M 56,180,92\nG 5\nv_mul_f32 v95, v95, v237\nv_cvt_pk_bf16_f32 v84, v88, v89\nv_cvt_pk_bf16_f32 v85, v90, v91\nM 60,160,76\n"
"R 176,185,16384\nds_read_b64 v[240:241], v246 offset:128\nv_cvt_pk_bf16_f32 v86, v92, v93\nM 60,164,92\ns_add_u32 m0, s50, 106496\ns_mov_b64 exec, s[66:67]\n"
"buffer_store_dwordx2 v[74:75], v227, s[20:23], s64 offen offset:2560 sc1\ns_mov_b64 exec, -1\nv_cvt_pk_bf16_f32 v87, v94, v95\nv_add_u32 v108, 161792, v252\nM 44,160,72\nR 178,185,20480\n"
"ds_read_b64 v[242:243], v246 offset:256\ns_lshl_b32 s60, s40, 12\ns_add_u32 s60, s60, 69632\nv_add_u32 v230, s60, v252\nv_add_u32 v231, s60, v227\nM 44,164,88\n"
"s_add_u32 s63, s52, 2949120\nds_read_b128 v[112:115], v108\nM 156,160,68\nR 180,185,24576\nds_read_b64 v[244:245], v246 offset:384\nM 156,164,84\n"
"buffer_load_dwordx4 v220, s[4:7], s63 offen lds\nM 140,160,64\nR 182,185,28672\nds_read_b64 v[246:247], v246 offset:0\nM 140,164,80\nds_read_b128 v[104:107], v108 offset:1024\n"
"s_mov_b64 exec, s[66:67]\nbuffer_store_dwordx2 v[80:81], v227, s[20:23], s64 offen offset:3072 sc1\ns_mov_b64 exec, -1\nG 11\nM 128,168,96\nR 160,186,16384\n"
"M 128,172,112\nM 144,168,100\nR 162,186,20480\nM 144,172,116\nM 32,168,104\nR 164,186,24576\n"
"G 11\nv_mfma_f32_16x16x32_bf16 v[128:131], v[238:241], v[208:211], v[128:131]\nM 32,172,120\nbuffer_load_dwordx4 v221, s[4:7], s63 offen offset:1024 lds\nM 48,168,108\nR 166,186,28672\n"
"G 10\nv_mfma_f32_16x16x32_bf16 v[132:135], v[240:243], v[208:211], v[132:135]\nG 4\nv_lshlrev_b32 v100, 16, v104\nM 48,172,124\ns_mov_b64 exec, s[66:67]\n"
"buffer_store_dwordx2 v[86:87], v227, s[20:23], s64 offen offset:3584 sc1\ns_mov_b64 exec, -1\nv_and_b32 v101, s77, v104\nv_lshlrev_b32 v102, 16, v105\nM 52,176,108\nR 168,187,16384\n"
"v_mfma_f32_16x16x32_bf16 v[144:147], v[244:247], v[210:213], v[144:147]\ns_not_b64 exec, s[66:67]\nds_write_b128 v230, v[16:19] offset:0\nds_write_b128 v230, v[20:23] offset:1024\ns_mov_b64 exec, -1\nv_and_b32 v103, s77, v105\n"
"M 52,180,124\nv_lshlrev_b32 v96, 16, v106\nv_and_b32 v97, s77, v106\nM 36,176,104\nR 170,187,20480\nv_mfma_f32_16x16x32_bf16 v[148:151], v[238:241], v[210:213], v[148:151]\n"
"v_lshlrev_b32 v98, 16, v107\nM 36,180,120\ns_movk_i32 s63, 2048\nv_mad_u32_u24 v220, v228, s63, v229\nv_add_u32 v221, 1047552, v220\ns_add_u32 m0, s50, 114688\n"
"s_add_u32 s63, s53, 0\nbuffer_load_dwordx4 v220, s[8:11], s63 offen lds\nM 148,176,100\nR 172,187,24576\nv_and_b32 v99, s77, v107\nM 148,180,116\n"
"v_add_f32 v104, s78, v100\nv_add_f32 v105, s78, v101\nM 132,176,96\nR 174,187,28672\nv_add_f32 v106, s78, v102\nM 132,180,112\n"
"G 6\nv_add_f32 v107, s78, v103\nM 136,160,96\nR 176,184,49152\nM 136,164,112\ns_not_b64 exec, s[66:67]\n"
"ds_write_b64 v231, v[68:69] offset:2048\nds_write_b64 v231, v[74:75] offset:2560\ns_mov_b64 exec, -1\nM 152,160,100\nR 178,184,53248\nLpg21:\n"
"s_cbranch_execz LretW0n\nM 152,164,116\nbuffer_load_dwordx4 v221, s[8:11], s63 offen offset:1024 lds\nM 40,160,104\nR 180,184,57344\nM 40,164,120\n"
"M 56,160,108\nR 182,184,61440\nM 56,164,124\nG 6\nM 60,168,108\nR 160,185,49152\n"
"s_not_b64 exec, s[66:67]\nds_write_b64 v231, v[80:81] offset:3072\nds_write_b64 v231, v[86:87] offset:3584\ns_mov_b64 exec, -1\nM 60,172,124\nM 44,168,104\n"
"R 162,185,53248\nM 44,172,120\ns_add_u32 m0, s50, 0\nM 156,168,100\nR 164,185,57344\nM 156,172,116\n"
"s_add_u32 s63, s53, 65536\nM 140,168,96\nR 166,185,61440\nM 140,172,112\nG 6\nM 128,176,128\n"
"R 168,186,49152\ns_waitcnt vmcnt(12) lgkmcnt(4)\ns_barrier\nM 128,180,144\nbuffer_load_dwordx4 v220, s[8:11], s63 offen lds\nM 144,176,132\n"
"R 170,186,53248\nM 144,180,148\ns_cmp_eq_u32 s78, 0\ns_cbranch_scc1 Lq668\nv_mov_b32 v230, 0\nv_mov_b32 v231, 0\n"
"v_mov_b32 v232, 0\nv_mov_b32 v233, 0\nv_mov_b32 v64, 0\nv_mov_b32 v65, 0\nv_mov_b32 v70, 0\nv_mov_b32 v71, 0\n"
"v_mov_b32 v234, 0\nv_mov_b32 v235, 0\nv_mov_b32 v236, 0\nv_mov_b32 v237, 0\nv_mov_b32 v76, 0\nv_mov_b32 v77, 0\n"
"v_mov_b32 v82, 0\nv_mov_b32 v83, 0\nLq668:\ns_lshl4_add_u32 s65, s41, s42\nM 32,176,136\nR 172,186,57344\n"
"s_lshl_b32 s65, s65, 12\nM 32,180,152\ns_add_u32 s65, s65, 78577664\nM 48,176,140\nR 174,186,61440\nM 48,180,156\n"
"G 4\nM 52,160,140\nR 176,187,49152\nM 52,164,156\nbuffer_load_dwordx4 v221, s[8:11], s63 offen offset:1024 lds\nM 36,160,136\n"
"R 178,187,53248\nM 36,164,152\ns_add_u32 m0, s50, 8192\nM 148,160,132\nR 180,187,57344\nM 148,164,148\n"
"s_add_u32 s63, s53, 131072\nM 132,160,128\nR 182,187,61440\nM 132,164,144\nG 4\nM 136,168,128\n"
"R 160,188,0\nM 136,172,144\nbuffer_load_dwordx4 v220, s[8:11], s63 offen lds\nM 152,168,132\nR 162,188,4096\nM 152,172,148\n"
"M 40,168,136\nR 164,188,8192\nM 40,172,152\nM 56,168,140\nR 166,188,12288\nv_mfma_f32_16x16x32_bf16 v[136:139], v[242:245], v[208:211], v[136:139]\n"
"M 56,172,156\nG 4\nM 60,176,140\nR 168,189,0\nv_mfma_f32_16x16x32_bf16 v[140:143], v[244:247], v[208:211], v[140:143]\nM 60,180,156\n"
"buffer_load_dwordx4 v221, s[8:11], s63 offen offset:1024 lds\nM 44,176,136\nR 170,189,4096\nv_mfma_f32_16x16x32_bf16 v[152:155], v[240:243], v[210:213], v[152:155]\ns_add_u32 s63, s53, 196608\nM 44,180,152\n"
"s_add_u32 m0, s50, 16384\nM 156,176,132\nR 172,189,8192\nv_mfma_f32_16x16x32_bf16 v[156:159], v[242:245], v[210:213], v[156:159]\nM 156,180,148\nbuffer_load_dwordx4 v220, s[8:11], s63 offen lds\n"
"M 140,176,128\nR 174,189,12288\nM 140,180,144\nG 4\nM 128,160,160\nR 176,190,0\n"
"M 128,164,176\nM 144,160,164\nR 178,190,4096\nM 144,164,180\nbuffer_load_dwordx4 v221, s[8:11], s63 offen offset:1024 lds\nM 32,160,168\n"
"R 180,190,8192\ns_add_u32 s63, s53, 262144\nM 32,164,184\ns_add_u32 m0, s50, 24576\nM 48,160,172\nR 182,190,12288\n"
"M 48,164,188\nG 4\nM 52,168,172\nR 160,191,0\nM 52,172,188\nbuffer_load_dwordx4 v220, s[8:11], s63 offen lds\n"
"M 36,168,168\nR 162,191,4096\nM 36,172,184\nM 148,168,164\nR 164,191,8192\ns_lshl_b32 s60, s40, 12\n"
"s_add_u32 s60, s60, 65536\nv_add_u32 v226, s60, v252\nv_add_u32 v111, s60, v227\nM 148,172,180\nM 132,168,160\nR 166,191,12288\n"
"s_cmp_eq_u32 s40, 0\ns_cselect_b64 s[88:89], 0, -1\ns_cselect_b32 s60, s41, 0\ns_cmp_lg_u32 s60, 0\ns_cselect_b64 s[66:67], -1, 0\nM 132,172,176\n"
"G 4\nM 136,176,160\nR 168,188,16384\nM 136,180,176\ns_mov_b64 exec, s[88:89]\nds_read_b128 v[230:233], v226 offset:0\n"
"ds_read_b64 v[64:65], v111 offset:2048\ns_mov_b64 exec, -1\nM 152,176,164\nR 170,188,20480\ns_waitcnt vmcnt(10) lgkmcnt(1)\ns_barrier\n"
"M 152,180,180\nbuffer_load_dwordx4 v221, s[8:11], s63 offen offset:1024 lds\nM 40,176,168\nR 172,188,24576\nM 40,180,184\ns_mov_b64 exec, s[88:89]\n"
"ds_read_b64 v[70:71], v111 offset:2560\nds_read_b128 v[234:237], v226 offset:1024\ns_mov_b64 exec, -1\nM 56,176,172\nR 174,188,28672\nM 56,180,188\n"
"M 60,160,172\nR 176,189,16384\nM 60,164,188\ns_mov_b64 exec, s[88:89]\nds_read_b64 v[76:77], v111 offset:3072\nds_read_b64 v[82:83], v111 offset:3584\n"
"s_mov_b64 exec, -1\nM 44,160,168\nR 178,189,20480\nM 44,164,184\nM 156,160,164\nR 180,189,24576\n"
"M 156,164,180\ns_mov_b64 exec, s[66:67]\nbuffer_load_dwordx4 v[230:233], v252, s[20:23], s65 offen offset:0 sc1\ns_mov_b64 exec, -1\nM 140,160,160\nR 182,189,28672\n"
"M 140,164,176\nG 6\nM 32,168,200\nR 160,190,16384\ns_mov_b64 exec, s[66:67]\nbuffer_load_dwordx2 v[64:65], v227, s[20:23], s65 offen offset:2048 sc1\n"
"s_mov_b64 exec, -1\nM 32,172,216\nR 162,190,20480\nM 48,168,204\nR 164,190,24576\nM 48,172,220\n"
"R 166,190,28672\nG 4\nM 52,176,204\nR 168,191,16384\ns_mov_b64 exec, s[66:67]\nbuffer_load_dwordx2 v[70:71], v227, s[20:23], s65 offen offset:2560 sc1\n"
"s_mov_b64 exec, -1\nM 52,180,220\nR 170,191,20480\nM 36,176,200\nR 172,191,24576\nM 36,180,216\n"
"R 174,191,28672\nG 4\nM 40,160,200\nR 176,188,32768\ns_mov_b64 exec, s[66:67]\nbuffer_load_dwordx4 v[234:237], v252, s[20:23], s65 offen offset:1024 sc1\n"
"s_mov_b64 exec, -1\nM 40,164,216\nR 178,188,36864\nM 56,160,204\nR 180,188,40960\nM 56,164,220\n"
"R 182,188,45056\nG 4\nM 60,168,204\nR 160,189,32768\ns_mov_b64 exec, s[66:67]\nbuffer_load_dwordx2 v[76:77], v227, s[20:23], s65 offen offset:3072 sc1\n"
"s_mov_b64 exec, -1\nM 60,172,220\nR 162,189,36864\nM 44,168,200\nR 164,189,40960\nM 44,172,216\n"
"R 166,189,45056\nG 4\nM 32,176,232\nR 168,190,32768\ns_mov_b64 exec, s[66:67]\nbuffer_load_dwordx2 v[82:83], v227, s[20:23], s65 offen offset:3584 sc1\n"
"s_mov_b64 exec, -1\nLq702:\ns_lshr_b32 s60, s45, 3\ns_lshl2_add_u32 s60, s60, s40\ns_lshl_b32 s60, s60, 8\nv_lshl_add_u32 v224, v252, 3, s60\n"
"v_add_u32 v224, Lq702+4096-LqPC, v224\nv_min_u32 v224, LqE-LqPC-4, v224\ns_mov_b64 exec, 3\nglobal_load_dword v224, v224, s[98:99]\ns_mov_b64 exec, -1\nM 32,180,248\n"
"R 170,190,36864\nM 48,176,236\nR 172,190,40960\nM 48,180,252\nR 174,190,45056\nG 4\n"
"M 52,160,236\nR 176,191,32768\nM 52,164,252\nR 178,191,36864\nM 36,160,232\nR 180,191,40960\n"
"M 36,164,248\nR 182,191,45056\nG 4\nM 40,168,232\nR 160,188,16384\nM 40,172,248\n"
"R 162,188,20480\nM 56,168,236\nR 164,188,24576\nM 56,172,252\nR 166,188,28672\nG 4\n"
"M 60,176,236\nR 168,189,16384\nM 60,180,252\nR 170,189,20480\nM 44,176,232\nR 172,189,24576\n"
"M 44,180,248\nR 174,189,28672\nG 4\nM 128,160,192\nR 176,190,16384\nM 128,164,208\n"
"R 178,190,20480\ns_mov_b32 m0, s51\nM 144,160,196\nR 180,190,24576\nds_write_addtid_b32 v32 offset:0\nM 144,164,212\n"
"R 182,190,28672\nds_write_addtid_b32 v33 offset:256\nG 6\nM 148,168,196\nR 160,191,16384\nds_write_addtid_b32 v34 offset:512\n"
"M 148,172,212\nR 162,191,20480\nds_write_addtid_b32 v35 offset:768\nM 132,168,192\nR 164,191,24576\nds_write_addtid_b32 v36 offset:1024\n"
"M 132,172,208\nR 166,191,28672\nds_write_addtid_b32 v37 offset:1280\nG 9\nM 136,176,192\nR 168,188,32768\n"
"ds_write_addtid_b32 v38 offset:1536\nM 136,180,208\nR 170,188,36864\nds_write_addtid_b32 v39 offset:1792\nM 152,176,196\nR 172,188,40960\n"
"ds_write_addtid_b32 v40 offset:2048\nM 152,180,212\nR 174,188,45056\nds_write_addtid_b32 v41 offset:2304\nG 9\nM 156,160,196\n"
"R 176,189,32768\nds_write_addtid_b32 v42 offset:2560\nM 156,164,212\nR 178,189,36864\nds_write_addtid_b32 v43 offset:2816\nM 140,160,192\n"
"R 180,189,40960\nds_write_addtid_b32 v44 offset:3072\nds_write_addtid_b32 v45 offset:3328\nds_write_addtid_b32 v46 offset:3584\nds_write_addtid_b32 v47 offset:3840\nds_write_addtid_b32 v48 offset:4096\n"
"ds_write_addtid_b32 v49 offset:4352\nds_write_addtid_b32 v50 offset:4608\nds_write_addtid_b32 v51 offset:4864\nds_write_addtid_b32 v52 offset:5120\nds_write_addtid_b32 v53 offset:5376\nds_write_addtid_b32 v54 offset:5632\n"
"ds_write_addtid_b32 v55 offset:5888\nds_write_addtid_b32 v56 offset:6144\nds_write_addtid_b32 v57 offset:6400\nds_write_addtid_b32 v58 offset:6656\nds_write_addtid_b32 v59 offset:6912\nds_write_addtid_b32 v60 offset:7168\n"
"ds_write_addtid_b32 v61 offset:7424\nds_write_addtid_b32 v62 offset:7680\nds_write_addtid_b32 v63 offset:7936\nM 140,164,208\nR 182,189,45056\nG 15\n"
"M 128,168,224\nR 160,190,32768\ns_add_u32 m0, s50, 32768\nM 128,172,240\nR 162,190,36864\nG 3\n"
"s_barrier\ns_add_u32 s63, s53, 327680\nM 144,168,228\nR 164,190,40960\nbuffer_load_dwordx4 v220, s[8:11], s63 offen lds\nds_read2st64_b32 v[32:33], v225 offset0:0 offset1:1\n"
"M 144,172,244\nR 166,190,45056\nbuffer_load_dwordx4 v221, s[8:11], s63 offen offset:1024 lds\nds_read2st64_b32 v[34:35], v225 offset0:2 offset1:3\nG 6\nM 148,176,228\n"
"R 168,191,32768\ns_add_u32 m0, s50, 40960\nds_read2st64_b32 v[36:37], v225 offset0:4 offset1:5\nM 148,180,244\nR 170,191,36864\ns_add_u32 s63, s53, 393216\n"
"ds_read2st64_b32 v[38:39], v225 offset0:6 offset1:7\nM 132,176,224\nR 172,191,40960\nds_read2st64_b32 v[40:41], v225 offset0:8 offset1:9\nM 132,180,240\nR 174,191,45056\n"
"ds_read2st64_b32 v[42:43], v225 offset0:10 offset1:11\nG 9\nM 136,160,224\nbuffer_load_dwordx4 v220, s[8:11], s63 offen lds\nds_read2st64_b32 v[44:45], v225 offset0:12 offset1:13\nM 136,164,240\n"
"ds_read2st64_b32 v[46:47], v225 offset0:14 offset1:15\nds_read2st64_b32 v[48:49], v225 offset0:16 offset1:17\nM 152,160,228\nbuffer_load_dwordx4 v221, s[8:11], s63 offen offset:1024 lds\nds_read2st64_b32 v[50:51], v225 offset0:18 offset1:19\nM 152,164,244\n"
"ds_read2st64_b32 v[52:53], v225 offset0:20 offset1:21\nds_read2st64_b32 v[54:55], v225 offset0:22 offset1:23\nG 7\nM 156,168,228\nds_read2st64_b32 v[56:57], v225 offset0:24 offset1:25\nds_read2st64_b32 v[58:59], v225 offset0:26 offset1:27\n"
"M 156,172,244\nds_read2st64_b32 v[60:61], v225 offset0:28 offset1:29\nds_read2st64_b32 v[62:63], v225 offset0:30 offset1:31\nM 140,168,224\nM 140,172,240\nG 0\n"
"s_barrier\nv_pk_add_f32 v[144:145], v[144:145], v[48:49]\nv_pk_add_f32 v[146:147], v[146:147], v[50:51]\nv_pk_add_f32 v[148:149], v[148:149], v[52:53]\nv_pk_add_f32 v[150:151], v[150:151], v[54:55]\nv_pk_add_f32 v[152:153], v[152:153], v[56:57]\n"
"v_pk_add_f32 v[154:155], v[154:155], v[58:59]\nv_pk_add_f32 v[156:157], v[156:157], v[60:61]\nv_pk_add_f32 v[158:159], v[158:159], v[62:63]\nv_pk_add_f32 v[128:129], v[128:129], v[32:33]\nv_pk_add_f32 v[130:131], v[130:131], v[34:35]\nv_pk_add_f32 v[132:133], v[132:133], v[36:37]\n"
"v_pk_add_f32 v[134:135], v[134:135], v[38:39]\nv_pk_add_f32 v[136:137], v[136:137], v[40:41]\nv_pk_add_f32 v[138:139], v[138:139], v[42:43]\nv_pk_add_f32 v[140:141], v[140:141], v[44:45]\nv_pk_add_f32 v[142:143], v[142:143], v[46:47]\nv_cvt_pk_bf16_f32 v144, v144, v145\n"
"v_cvt_pk_bf16_f32 v145, v146, v147\nv_cvt_pk_bf16_f32 v146, v148, v149\nv_cvt_pk_bf16_f32 v147, v150, v151\nv_cvt_pk_bf16_f32 v152, v152, v153\nv_cvt_pk_bf16_f32 v153, v154, v155\nv_cvt_pk_bf16_f32 v154, v156, v157\n"
"v_cvt_pk_bf16_f32 v155, v158, v159\nv_cvt_pk_bf16_f32 v128, v128, v129\nv_cvt_pk_bf16_f32 v129, v130, v131\nv_cvt_pk_bf16_f32 v130, v132, v133\nv_cvt_pk_bf16_f32 v131, v134, v135\nv_cvt_pk_bf16_f32 v136, v136, v137\n"
"v_cvt_pk_bf16_f32 v137, v138, v139\nv_cvt_pk_bf16_f32 v138, v140, v141\nv_cvt_pk_bf16_f32 v139, v142, v143\nv_mfma_f32_16x16x32_bf16 v[52:55], v[16:19], v[144:147], v[96:99]\nv_mfma_f32_16x16x32_bf16 v[48:51], v[0:3], v[144:147], v[100:103]\nv_mfma_f32_16x16x32_bf16 v[60:63], v[20:23], v[152:155], v[96:99]\n"
"v_mfma_f32_16x16x32_bf16 v[56:59], v[4:7], v[152:155], v[100:103]\ns_waitcnt vmcnt(8)\nv_max3_u32 v248, v233, v65, v71\nv_cmp_eq_u32 vcc, -1, v248\ns_nop 0\ns_cbranch_vccnz Lq704\n"
"Lq705:\ns_waitcnt vmcnt(5)\nv_max3_u32 v248, v237, v77, v83\nv_cmp_eq_u32 vcc, -1, v248\ns_nop 0\ns_cbranch_vccnz Lq706\n"
"Lq707:\nv_mfma_f32_16x16x32_bf16 v[36:39], v[0:3], v[128:131], v[96:99]\nv_mfma_f32_16x16x32_bf16 v[32:35], v[230:233], v[128:131], v[104:107]\nv_mfma_f32_16x16x32_bf16 v[44:47], v[4:7], v[136:139], v[96:99]\nv_mfma_f32_16x16x32_bf16 v[40:43], v[234:237], v[136:139], v[104:107]\ns_lshl_b32 s84, s42, 11\n"
"s_add_u32 s84, s84, s49\ns_add_u32 s84, s84, 6291456\ns_add_u32 s85, s84, 32768\nv_mov_b32 v238, s46\nv_mov_b32 v239, s47\nv_max_f32 v52, v52, v53\n"
"v_max_f32 v54, v54, v55\nv_max3_f32 v52, v52, v48, v49\nv_max3_f32 v54, v54, v50, v51\nv_max_f32 v118, v52, v54\nv_max_f32 v60, v60, v61\nv_max_f32 v62, v62, v63\n"
"v_max3_f32 v60, v60, v56, v57\nv_max3_f32 v62, v62, v58, v59\nv_max_f32 v119, v60, v62\nv_max_f32 v36, v36, v37\nv_max_f32 v38, v38, v39\nv_max3_f32 v36, v36, v32, v33\n"
"v_max3_f32 v38, v38, v34, v35\nv_max_f32 v116, v36, v38\nv_max_f32 v44, v44, v45\nv_max_f32 v46, v46, v47\nv_max3_f32 v44, v44, v40, v41\nv_max3_f32 v46, v46, v42, v43\n"
"v_max_f32 v117, v44, v46\nv_permlane32_swap_b32 v116, v118\ns_nop 0\nv_permlane32_swap_b32 v117, v119\nv_max_f32 v116, v116, v118\nv_max_f32 v117, v117, v119\n"
"s_nop 1\nv_permlane16_swap_b32 v116, v117\nv_max_f32 v116, v116, v117\nv_mov_b32 v117, v116\ns_nop 1\nv_permlane16_swap_b32 v116, v117\n"
"v_mov_b32 v118, v116\nv_mov_b32 v119, v117\ns_nop 0\nv_permlane32_swap_b32 v116, v118\nv_permlane32_swap_b32 v117, v119\nv_mul_f32 v116, v116, v215\n"
"v_mul_f32 v118, v118, v218\nv_mul_f32 v117, v117, v215\nv_mul_f32 v119, v119, v218\nv_fma_f32 v52, v52, v218, -v118\nv_fma_f32 v54, v54, v218, -v118\nv_fma_f32 v60, v60, v218, -v119\n"
"v_fma_f32 v62, v62, v218, -v119\nv_fma_f32 v36, v36, v215, -v116\nv_fma_f32 v38, v38, v215, -v116\nv_fma_f32 v44, v44, v215, -v117\nv_fma_f32 v46, v46, v215, -v117\nv_exp_f32 v52, v52\n"
"v_exp_f32 v54, v54\nv_exp_f32 v60, v60\nv_exp_f32 v62, v62\nv_exp_f32 v36, v36\nv_exp_f32 v38, v38\nv_exp_f32 v44, v44\n"
"v_exp_f32 v46, v46\nv_add_f32 v118, v52, v54\nv_add_f32 v119, v60, v62\nv_add_f32 v116, v36, v38\nv_add_f32 v117, v44, v46\ns_nop 0\n"
"v_permlane32_swap_b32 v116, v118\nv_permlane32_swap_b32 v117, v119\nv_add_f32 v116, v116, v118\nv_add_f32 v117, v117, v119\ns_nop 1\nv_permlane16_swap_b32 v116, v117\n"
"v_add_f32 v116, v116, v117\nv_rcp_f32 v116, v116\ns_nop 0\nv_mov_b32 v117, v116\ns_nop 1\nv_permlane16_swap_b32 v116, v117\n"
"v_mov_b32 v118, v116\nv_mov_b32 v119, v117\ns_nop 0\nv_permlane32_swap_b32 v116, v118\nv_permlane32_swap_b32 v117, v119\nv_mul_f32 v52, v52, v118\n"
"v_mul_f32 v54, v54, v118\nv_mul_f32 v60, v60, v119\nv_mul_f32 v62, v62, v119\nv_mul_f32 v36, v36, v116\nv_mul_f32 v38, v38, v116\nv_mul_f32 v44, v44, v117\n"
"v_mul_f32 v46, v46, v117\nv_cvt_pk_bf16_f32 v52, v52, v52\nv_cvt_pk_bf16_f32 v54, v54, v54\nv_cvt_pk_bf16_f32 v60, v60, v60\nv_cvt_pk_bf16_f32 v62, v62, v62\nv_cvt_pk_bf16_f32 v36, v36, v36\n"
"v_cvt_pk_bf16_f32 v38, v38, v38\nv_cvt_pk_bf16_f32 v44, v44, v44\nv_cvt_pk_bf16_f32 v46, v46, v46\nv_and_b32 v48, v52, v112\nv_and_b32 v49, v54, v113\nv_and_b32 v50, v52, v114\n"
"v_and_b32 v51, v54, v115\nv_and_b32 v56, v60, v112\nv_and_b32 v57, v62, v113\nv_and_b32 v58, v60, v114\nv_and_b32 v59, v62, v115\nv_mfma_f32_16x16x32_bf16 v[144:147], v[66:69], v[48:51], 0\n"
"v_mfma_f32_16x16x32_bf16 v[148:151], v[72:75], v[48:51], 0\nv_and_b32 v32, v36, v112\nv_and_b32 v33, v38, v113\nv_and_b32 v34, v36, v114\nv_and_b32 v35, v38, v115\nv_mfma_f32_16x16x32_bf16 v[152:155], v[78:81], v[56:59], 0\n"
"v_mfma_f32_16x16x32_bf16 v[156:159], v[84:87], v[56:59], 0\nv_and_b32 v40, v44, v112\nv_and_b32 v41, v46, v113\nv_and_b32 v42, v44, v114\nv_and_b32 v43, v46, v115\nv_mfma_f32_16x16x32_bf16 v[128:131], v[64:67], v[32:35], 0\n"
"v_mfma_f32_16x16x32_bf16 v[132:135], v[70:73], v[32:35], 0\nv_mfma_f32_16x16x32_bf16 v[136:139], v[76:79], v[40:43], 0\nv_mfma_f32_16x16x32_bf16 v[140:143], v[82:85], v[40:43], 0\nv_cvt_pk_bf16_f32 v144, v144, v145\nv_cvt_pk_bf16_f32 v145, v146, v147\nv_cvt_pk_bf16_f32 v146, v148, v149\n"
"v_cvt_pk_bf16_f32 v147, v150, v151\nbuffer_store_dwordx4 v[144:147], v252, s[20:23], s85 offen offset:0 sc1\nv_cvt_pk_bf16_f32 v152, v152, v153\nv_cvt_pk_bf16_f32 v153, v154, v155\nv_cvt_pk_bf16_f32 v154, v156, v157\nv_cvt_pk_bf16_f32 v155, v158, v159\n"
"buffer_store_dwordx4 v[152:155], v252, s[20:23], s85 offen offset:1024 sc1\nv_cvt_pk_bf16_f32 v128, v128, v129\nv_cvt_pk_bf16_f32 v129, v130, v131\nLpg22:\ns_cbranch_execz LretW0n\nv_cvt_pk_bf16_f32 v130, v132, v133\n"
"v_cvt_pk_bf16_f32 v131, v134, v135\nbuffer_store_dwordx4 v[128:131], v252, s[20:23], s84 offen offset:0 sc1\nv_cvt_pk_bf16_f32 v136, v136, v137\nv_cvt_pk_bf16_f32 v137, v138, v139\nv_cvt_pk_bf16_f32 v138, v140, v141\nv_cvt_pk_bf16_f32 v139, v142, v143\n"
"buffer_store_dwordx4 v[136:139], v252, s[20:23], s84 offen offset:1024 sc1\ns_mov_b64 exec, 0\ns_cmp_eq_u32 s40, 1\ns_cbranch_scc1 LretO1\ns_cmp_eq_u32 s40, 2\ns_cbranch_scc1 LretO1\n"
"s_cmp_eq_u32 s40, 3\ns_cbranch_scc1 LretO1\ns_branch Lpg23\nLretO1:\ns_mov_b64 exec, -1\ns_add_u32 s61, s79, 88498176\n"
"s_lshl_b32 s62, s42, 3\ns_add_u32 s61, s61, s62\ns_waitcnt vmcnt(0)\ns_mov_b64 exec, 1\nbuffer_atomic_swap_x2 v[238:239], off, s[20:23], s61 sc1\ns_mov_b64 exec, -1\n"
"v_bfe_u32 v250, v252, 7, 1\nv_mul_u32_u24 v250, 2176, v250\nv_bfe_u32 v251, v252, 4, 3\nv_lshl_add_u32 v250, v251, 3, v250\ns_lshl_b32 s60, s90, 6\nv_add_u32 v250, s60, v250\n"
"s_sleep 5\nv_mov_b32 v248, 0\nv_mov_b32 v249, 0\ns_add_u32 s61, s80, 88498176\ns_mov_b64 exec, 0xffff\nbuffer_atomic_or_x2 v[248:249], v250, s[20:23], s61 offen sc0 sc1\n"
"s_mov_b64 exec, -1\ns_add_u32 m0, s50, 49152\ns_add_u32 s63, s53, 458752\nbuffer_load_dwordx4 v220, s[8:11], s63 offen lds\nbuffer_load_dwordx4 v221, s[8:11], s63 offen offset:1024 lds\ns_add_u32 m0, s50, 57344\n"
"s_add_u32 s63, s53, 524288\nbuffer_load_dwordx4 v220, s[8:11], s63 offen lds\nbuffer_load_dwordx4 v221, s[8:11], s63 offen offset:1024 lds\ns_add_u32 m0, s50, 65536\ns_add_u32 s63, s53, 589824\nbuffer_load_dwordx4 v220, s[8:11], s63 offen lds\n"
"buffer_load_dwordx4 v221, s[8:11], s63 offen offset:1024 lds\ns_add_u32 m0, s50, 73728\ns_add_u32 s63, s53, 655360\nbuffer_load_dwordx4 v220, s[8:11], s63 offen lds\nbuffer_load_dwordx4 v221, s[8:11], s63 offen offset:1024 lds\ns_add_u32 m0, s50, 81920\n"
"s_add_u32 s63, s53, 720896\nbuffer_load_dwordx4 v220, s[8:11], s63 offen lds\nbuffer_load_dwordx4 v221, s[8:11], s63 offen offset:1024 lds\ns_add_u32 m0, s50, 90112\ns_add_u32 s63, s53, 786432\nbuffer_load_dwordx4 v220, s[8:11], s63 offen lds\n"
"buffer_load_dwordx4 v221, s[8:11], s63 offen offset:1024 lds\ns_add_u32 m0, s50, 98304\ns_add_u32 s63, s53, 851968\nbuffer_load_dwordx4 v220, s[8:11], s63 offen lds\nbuffer_load_dwordx4 v221, s[8:11], s63 offen offset:1024 lds\ns_add_u32 m0, s50, 106496\n"
"s_add_u32 s63, s53, 917504\nbuffer_load_dwordx4 v220, s[8:11], s63 offen lds\nbuffer_load_dwordx4 v221, s[8:11], s63 offen offset:1024 lds\nv_bfe_u32 v234, v252, 4, 4\nv_lshrrev_b32 v235, 8, v252\nv_lshlrev_b32 v235, 2, v235\n"
"v_sub_u32 v234, v234, v235\nv_lshlrev_b32 v235, 4, v234\ns_mov_b64 s[60:61], 0x3f80\nv_lshlrev_b64 v[208:209], v235, s[60:61]\nv_cmp_gt_u32 s[60:61], 4, v234\ns_nop 1\n"
"v_cndmask_b32 v212, 0, v208, s[60:61]\nv_cndmask_b32 v208, 0, v208, s[60:61]\nv_cndmask_b32 v213, 0, v209, s[60:61]\nv_cndmask_b32 v209, 0, v209, s[60:61]\ns_nop 1\nv_mfma_f32_16x16x32_bf16 v[0:3], v[208:211], v[192:195], 0\n"
"v_mfma_f32_16x16x32_bf16 v[4:7], v[210:213], v[192:195], 0\nv_mfma_f32_16x16x32_bf16 v[8:11], v[208:211], v[196:199], 0\nv_mfma_f32_16x16x32_bf16 v[12:15], v[210:213], v[196:199], 0\nv_mfma_f32_16x16x32_bf16 v[16:19], v[208:211], v[200:203], 0\nv_mfma_f32_16x16x32_bf16 v[20:23], v[210:213], v[200:203], 0\nv_mfma_f32_16x16x32_bf16 v[24:27], v[208:211], v[204:207], 0\n"
"v_mfma_f32_16x16x32_bf16 v[28:31], v[210:213], v[204:207], 0\ns_waitcnt vmcnt(16)\nv_cmp_eq_u64 s[86:87], s[46:47], v[248:249]\ns_nop 0\ns_and_b32 s62, s86, 0xffff\ns_cmp_eq_u32 s62, 0xffff\n"
"s_cbranch_scc0 Lq709\nLq710:\ns_add_u32 s64, s70, 6291456\nbuffer_load_dwordx4 a[0:3], v252, s[20:23], s64 offen offset:0 sc1\ns_add_u32 s65, s70, 6324224\nbuffer_load_dwordx4 a[4:7], v252, s[20:23], s65 offen offset:0 sc1\n"
"s_add_u32 s66, s71, 6291456\nbuffer_load_dwordx4 a[8:11], v252, s[20:23], s66 offen offset:0 sc1\ns_add_u32 s67, s71, 6324224\nbuffer_load_dwordx4 a[12:15], v252, s[20:23], s67 offen offset:0 sc1\nbuffer_load_dwordx4 a[16:19], v252, s[20:23], s64 offen offset:1024 sc1\nbuffer_load_dwordx4 a[20:23], v252, s[20:23], s65 offen offset:1024 sc1\n"
"buffer_load_dwordx4 a[24:27], v252, s[20:23], s66 offen offset:1024 sc1\nbuffer_load_dwordx4 a[28:31], v252, s[20:23], s67 offen offset:1024 sc1\ns_barrier\nR 160,188,49152\nR 162,188,53248\nR 164,184,0\n"
"R 166,184,4096\nR 168,189,49152\nR 170,189,53248\nR 172,185,0\nR 174,185,4096\nR 176,190,49152\n"
"R 178,190,53248\nR 180,186,0\nR 182,186,4096\nbuffer_load_dwordx4 a[32:35], v252, s[20:23], s64 offen offset:2048 sc1\nbuffer_load_dwordx4 a[36:39], v252, s[20:23], s65 offen offset:2048 sc1\nbuffer_load_dwordx4 a[40:43], v252, s[20:23], s66 offen offset:2048 sc1\n"
"buffer_load_dwordx4 a[44:47], v252, s[20:23], s67 offen offset:2048 sc1\nbuffer_load_dwordx4 a[48:51], v252, s[20:23], s64 offen offset:3072 sc1\nbuffer_load_dwordx4 a[52:55], v252, s[20:23], s65 offen offset:3072 sc1\nbuffer_load_dwordx4 a[56:59], v252, s[20:23], s66 offen offset:3072 sc1\nbuffer_load_dwordx4 a[60:63], v252, s[20:23], s67 offen offset:3072 sc1\ns_add_u32 s64, s70, 6295552\n"
"buffer_load_dwordx4 a[64:67], v252, s[20:23], s64 offen offset:0 sc1\ns_add_u32 s65, s70, 6328320\nbuffer_load_dwordx4 a[68:71], v252, s[20:23], s65 offen offset:0 sc1\ns_add_u32 s66, s71, 6295552\nbuffer_load_dwordx4 a[72:75], v252, s[20:23], s66 offen offset:0 sc1\ns_add_u32 s67, s71, 6328320\n"
"buffer_load_dwordx4 a[76:79], v252, s[20:23], s67 offen offset:0 sc1\ns_waitcnt vmcnt(19) lgkmcnt(10)\nM 0,160,0\nbuffer_load_dwordx4 a[80:83], v252, s[20:23], s64 offen offset:1024 sc1\ns_waitcnt vmcnt(16) lgkmcnt(8)\nM 0,164,16\n"
"M 16,160,4\ns_waitcnt vmcnt(15)\nM 16,164,20\nv_mfma_f32_16x16x32_bf16 v[32:35], v[160:163], a[8:11], 0\nbuffer_load_dwordx4 a[84:87], v252, s[20:23], s65 offen offset:1024 sc1\ns_waitcnt vmcnt(15)\n"
"M 32,164,24\nv_mfma_f32_16x16x32_bf16 v[48:51], v[160:163], a[12:15], 0\ns_waitcnt vmcnt(14)\nM 48,164,28\nG 4\nv_mfma_f32_16x16x32_bf16 v[52:55], v[168:171], a[12:15], 0\n"
"R 160,191,49152\nM 52,172,28\nbuffer_load_dwordx4 a[88:91], v252, s[20:23], s66 offen offset:1024 sc1\nv_mfma_f32_16x16x32_bf16 v[36:39], v[168:171], a[8:11], 0\nR 162,191,53248\nM 36,172,24\n"
"M 20,168,4\nR 164,187,0\nM 20,172,20\nbuffer_load_dwordx4 a[92:95], v252, s[20:23], s67 offen offset:1024 sc1\nM 4,168,0\nR 166,187,4096\n"
"M 4,172,16\nG 4\nM 8,176,0\nR 168,184,8192\nM 8,180,16\nbuffer_load_dwordx4 a[96:99], v252, s[20:23], s64 offen offset:2048 sc1\n"
"M 24,176,4\nR 170,184,12288\nM 24,180,20\nv_mfma_f32_16x16x32_bf16 v[40:43], v[176:179], a[8:11], 0\nR 172,184,16384\nM 40,180,24\n"
"buffer_load_dwordx4 a[100:103], v252, s[20:23], s65 offen offset:2048 sc1\nv_mfma_f32_16x16x32_bf16 v[56:59], v[176:179], a[12:15], 0\nR 174,184,20480\nM 56,180,28\nG 4\nv_mfma_f32_16x16x32_bf16 v[60:63], v[160:163], a[12:15], 0\n"
"R 176,185,8192\nM 60,164,28\nbuffer_load_dwordx4 a[104:107], v252, s[20:23], s66 offen offset:2048 sc1\nv_mfma_f32_16x16x32_bf16 v[44:47], v[160:163], a[8:11], 0\nR 178,185,12288\nM 44,164,24\n"
"M 28,160,4\nR 180,185,16384\nM 28,164,20\nbuffer_load_dwordx4 a[108:111], v252, s[20:23], s67 offen offset:2048 sc1\nM 12,160,0\nR 182,185,20480\n"
"M 12,164,16\nG 4\ns_waitcnt vmcnt(19)\nM 0,168,32\nR 160,186,8192\ns_waitcnt vmcnt(15)\n"
"M 0,172,48\nbuffer_load_dwordx4 a[112:115], v252, s[20:23], s64 offen offset:3072 sc1\nM 16,168,36\nR 162,186,12288\ns_waitcnt vmcnt(15)\nM 16,172,52\n"
"M 32,168,40\nR 164,186,16384\ns_waitcnt vmcnt(14)\nM 32,172,56\nbuffer_load_dwordx4 a[116:119], v252, s[20:23], s65 offen offset:3072 sc1\nM 48,168,44\n"
"R 166,186,20480\ns_waitcnt vmcnt(14)\nM 48,172,60\nG 4\nM 52,176,44\nR 168,187,8192\n"
"M 52,180,60\nbuffer_load_dwordx4 a[120:123], v252, s[20:23], s66 offen offset:3072 sc1\nM 36,176,40\nR 170,187,12288\nM 36,180,56\nM 20,176,36\n"
"R 172,187,16384\nM 20,180,52\nbuffer_load_dwordx4 a[124:127], v252, s[20:23], s67 offen offset:3072 sc1\nM 4,176,32\nR 174,187,20480\nM 4,180,48\n"
"G 4\nM 8,160,32\nR 176,184,24576\nM 8,164,48\ns_add_u32 s64, s70, 6299648\nbuffer_load_dwordx4 a[128:131], v252, s[20:23], s64 offen offset:0 sc1\n"
"M 24,160,36\nR 178,184,28672\nM 24,164,52\nM 40,160,40\nR 180,184,32768\nM 40,164,56\n"
"s_add_u32 s65, s70, 6332416\nbuffer_load_dwordx4 a[132:135], v252, s[20:23], s65 offen offset:0 sc1\nM 56,160,44\nR 182,184,36864\nM 56,164,60\nG 4\n"
"M 60,168,44\nR 160,185,24576\nM 60,172,60\ns_add_u32 s66, s71, 6299648\nbuffer_load_dwordx4 a[136:139], v252, s[20:23], s66 offen offset:0 sc1\nM 44,168,40\n"
"R 162,185,28672\nM 44,172,56\nM 28,168,36\nR 164,185,32768\nM 28,172,52\ns_add_u32 s67, s71, 6332416\n"
"buffer_load_dwordx4 a[140:143], v252, s[20:23], s67 offen offset:0 sc1\nM 12,168,32\nR 166,185,36864\nM 12,172,48\ns_barrier\nG 4\n"
"s_waitcnt vmcnt(19)\nM 0,176,64\nR 168,186,24576\ns_waitcnt vmcnt(15)\nM 0,180,80\nbuffer_load_dwordx4 a[144:147], v252, s[20:23], s64 offen offset:1024 sc1\n"
"M 16,176,68\nR 170,186,28672\ns_waitcnt vmcnt(15)\nM 16,180,84\nM 32,176,72\nR 172,186,32768\n"
"s_waitcnt vmcnt(14)\nM 32,180,88\nbuffer_load_dwordx4 a[148:151], v252, s[20:23], s65 offen offset:1024 sc1\nM 48,176,76\nR 174,186,36864\ns_waitcnt vmcnt(14)\n"
"M 48,180,92\nG 4\nM 52,160,76\nR 176,187,24576\nM 52,164,92\nbuffer_load_dwordx4 a[152:155], v252, s[20:23], s66 offen offset:1024 sc1\n"
"M 36,160,72\nR 178,187,28672\nM 36,164,88\nM 20,160,68\nR 180,187,32768\nM 20,164,84\n"
"buffer_load_dwordx4 a[156:159], v252, s[20:23], s67 offen offset:1024 sc1\nM 4,160,64\nR 182,187,36864\nM 4,164,80\nG 4\nM 8,168,64\n"
"R 160,184,40960\nM 8,172,80\nbuffer_load_dwordx4 a[160:163], v252, s[20:23], s64 offen offset:2048 sc1\nM 24,168,68\nR 162,184,45056\nM 24,172,84\n"
"M 40,168,72\nR 164,184,49152\nM 40,172,88\nbuffer_load_dwordx4 a[164:167], v252, s[20:23], s65 offen offset:2048 sc1\nM 56,168,76\nR 166,184,53248\n"
"M 56,172,92\nG 4\nM 60,176,76\nR 168,185,40960\nM 60,180,92\nbuffer_load_dwordx4 a[168:171], v252, s[20:23], s66 offen offset:2048 sc1\n"
"M 44,176,72\nR 170,185,45056\nM 44,180,88\nM 28,176,68\nR 172,185,49152\nM 28,180,84\n"
"buffer_load_dwordx4 a[172:175], v252, s[20:23], s67 offen offset:2048 sc1\nM 12,176,64\nR 174,185,53248\nM 12,180,80\nG 4\ns_waitcnt vmcnt(19)\n"
"M 0,160,96\nR 176,186,40960\ns_waitcnt vmcnt(15)\nM 0,164,112\nbuffer_load_dwordx4 a[176:179], v252, s[20:23], s64 offen offset:3072 sc1\nM 16,160,100\n"
"R 178,186,45056\ns_waitcnt vmcnt(15)\nM 16,164,116\nM 32,160,104\nR 180,186,49152\ns_waitcnt vmcnt(14)\n"
"M 32,164,120\nbuffer_load_dwordx4 a[180:183], v252, s[20:23], s65 offen offset:3072 sc1\nM 48,160,108\nR 182,186,53248\ns_waitcnt vmcnt(14)\nM 48,164,124\n"
"G 4\nM 52,168,108\nR 160,187,40960\nM 52,172,124\nbuffer_load_dwordx4 a[184:187], v252, s[20:23], s66 offen offset:3072 sc1\nM 36,168,104\n"
"R 162,187,45056\nM 36,172,120\nM 20,168,100\nR 164,187,49152\nM 20,172,116\nbuffer_load_dwordx4 a[188:191], v252, s[20:23], s67 offen offset:3072 sc1\n"
"M 4,168,96\nR 166,187,53248\nM 4,172,112\nG 4\nM 8,176,96\nR 168,184,57344\n"
"M 8,180,112\ns_add_u32 s64, s70, 6303744\nbuffer_load_dwordx4 a[192:195], v252, s[20:23], s64 offen offset:0 sc1\nM 24,176,100\nR 170,184,61440\ns_add_u32 s63, s53, 983040\n"
"M 24,180,116\ns_add_u32 m0, s50, 114688\nM 40,176,104\nR 172,188,0\nM 40,180,120\ns_add_u32 s65, s70, 6336512\n"
"buffer_load_dwordx4 a[196:199], v252, s[20:23], s65 offen offset:0 sc1\nM 56,176,108\nR 174,188,4096\nM 56,180,124\nbuffer_load_dwordx4 v220, s[8:11], s63 offen lds\nG 4\n"
"M 60,160,108\nR 176,185,57344\nM 60,164,124\ns_add_u32 s66, s71, 6303744\nbuffer_load_dwordx4 a[200:203], v252, s[20:23], s66 offen offset:0 sc1\nM 44,160,104\n"
"R 178,185,61440\nM 44,164,120\nbuffer_load_dwordx4 v221, s[8:11], s63 offen offset:1024 lds\nM 28,160,100\nR 180,189,0\nM 28,164,116\n"
"s_add_u32 s67, s71, 6336512\nbuffer_load_dwordx4 a[204:207], v252, s[20:23], s67 offen offset:0 sc1\nM 12,160,96\nR 182,189,4096\nM 12,164,112\ns_barrier\n"
"G 4\ns_waitcnt vmcnt(21)\nM 0,168,128\nR 160,186,57344\ns_waitcnt vmcnt(17)\nM 0,172,144\n"
"buffer_load_dwordx4 a[208:211], v252, s[20:23], s64 offen offset:1024 sc1\nM 16,168,132\nR 162,186,61440\ns_waitcnt vmcnt(17)\nM 16,172,148\nM 32,168,136\n"
"R 164,190,0\ns_waitcnt vmcnt(16)\nM 32,172,152\nbuffer_load_dwordx4 a[212:215], v252, s[20:23], s65 offen offset:1024 sc1\nM 48,168,140\nR 166,190,4096\n"
"s_waitcnt vmcnt(16)\nM 48,172,156\nG 4\nM 52,176,140\nR 168,187,57344\nM 52,180,156\n"
"buffer_load_dwordx4 a[216:219], v252, s[20:23], s66 offen offset:1024 sc1\nM 36,176,136\nR 170,187,61440\nM 36,180,152\nM 20,176,132\nR 172,191,0\n"
"M 20,180,148\nbuffer_load_dwordx4 a[220:223], v252, s[20:23], s67 offen offset:1024 sc1\nM 4,176,128\nR 174,191,4096\nM 4,180,144\nG 4\n"
"M 8,160,128\nR 176,188,8192\nM 8,164,144\nbuffer_load_dwordx4 a[224:227], v252, s[20:23], s64 offen offset:2048 sc1\nM 24,160,132\nR 178,188,12288\n"
"M 24,164,148\nM 40,160,136\nR 180,188,16384\nM 40,164,152\nbuffer_load_dwordx4 a[228:231], v252, s[20:23], s65 offen offset:2048 sc1\nM 56,160,140\n"
"R 182,188,20480\nM 56,164,156\nG 4\nM 60,168,140\nR 160,189,8192\nM 60,172,156\n"
"buffer_load_dwordx4 a[232:235], v252, s[20:23], s66 offen offset:2048 sc1\nM 44,168,136\nR 162,189,12288\nM 44,172,152\nM 28,168,132\nR 164,189,16384\n"
"M 28,172,148\nbuffer_load_dwordx4 a[236:239], v252, s[20:23], s67 offen offset:2048 sc1\nM 12,168,128\nR 166,189,20480\nM 12,172,144\nG 4\n"
"s_waitcnt vmcnt(21)\nM 0,176,160\nR 168,190,8192\ns_waitcnt vmcnt(17)\nM 0,180,176\nbuffer_load_dwordx4 a[240:243], v252, s[20:23], s64 offen offset:3072 sc1\n"
"M 16,176,164\nR 170,190,12288\ns_waitcnt vmcnt(17)\nM 16,180,180\nM 32,176,168\nR 172,190,16384\n"
"s_waitcnt vmcnt(16)\nM 32,180,184\nbuffer_load_dwordx4 a[244:247], v252, s[20:23], s65 offen offset:3072 sc1\nM 48,176,172\nR 174,190,20480\ns_waitcnt vmcnt(16)\n"
"M 48,180,188\nG 4\nM 52,160,172\nR 176,191,8192\nM 52,164,188\nbuffer_load_dwordx4 a[248:251], v252, s[20:23], s66 offen offset:3072 sc1\n"
"M 36,160,168\nR 178,191,12288\ns_waitcnt vmcnt(12)\ns_barrier\nM 36,164,184\nM 20,160,164\n"
"R 180,191,16384\nM 20,164,180\nbuffer_load_dwordx4 a[252:255], v252, s[20:23], s67 offen offset:3072 sc1\nM 4,160,160\nR 182,191,20480\nM 4,164,176\n"
"G 4\nM 8,168,160\nR 160,188,24576\nM 8,172,176\nM 24,168,164\nR 162,188,28672\n"
"M 24,172,180\nM 40,168,168\nR 164,188,32768\nM 40,172,184\nM 56,168,172\nR 166,188,36864\n"
"M 56,172,188\nG 4\nM 60,176,172\nR 168,189,24576\nM 60,180,188\ns_movk_i32 s63, 7168\n"
"v_mad_u32_u24 v220, v228, s63, v229\nv_add_u32 v221, 3668992, v220\ns_add_u32 m0, s50, 0\ns_add_u32 s63, s54, 0\nbuffer_load_dwordx4 v220, s[12:15], s63 offen lds\nM 44,176,168\n"
"R 170,189,28672\nM 44,180,184\nM 28,176,164\nR 172,189,32768\nM 28,180,180\nbuffer_load_dwordx4 v221, s[12:15], s63 offen offset:1024 lds\n"
"M 12,176,160\nR 174,189,36864\ns_add_u32 m0, s50, 8192\nM 12,180,176\nG 4\nM 32,160,200\n"
"R 176,190,24576\ns_add_u32 s63, s54, 229376\ns_waitcnt vmcnt(11)\nM 32,164,216\nR 178,190,28672\nM 48,160,204\n"
"R 180,190,32768\ns_waitcnt vmcnt(10)\nM 48,164,220\nR 182,190,36864\nG 4\nM 52,168,204\n"
"R 160,191,24576\nbuffer_load_dwordx4 v220, s[12:15], s63 offen lds\nLpg23:\ns_cbranch_execz LretO1\nM 52,172,220\nR 162,191,28672\n"
"M 36,168,200\nR 164,191,32768\nM 36,172,216\nR 166,191,36864\nG 4\nM 40,176,200\n"
"R 168,188,40960\nbuffer_load_dwordx4 v221, s[12:15], s63 offen offset:1024 lds\nM 40,180,216\nR 170,188,45056\ns_add_u32 m0, s50, 16384\nM 56,176,204\n"
"R 172,188,49152\ns_add_u32 s63, s54, 128\nM 56,180,220\nR 174,188,53248\nG 4\nM 60,160,204\n"
"R 176,189,40960\nbuffer_load_dwordx4 v220, s[12:15], s63 offen lds\nM 60,164,220\nR 178,189,45056\nM 44,160,200\nR 180,189,49152\n"
"M 44,164,216\nR 182,189,53248\nG 4\ns_waitcnt vmcnt(10)\nM 32,168,232\nR 160,190,40960\n"
"buffer_load_dwordx4 v221, s[12:15], s63 offen offset:1024 lds\ns_waitcnt vmcnt(7)\nM 32,172,248\nR 162,190,45056\ns_add_u32 m0, s50, 24576\nM 48,168,236\n"
"R 164,190,49152\ns_add_u32 s63, s54, 229504\ns_waitcnt vmcnt(6)\nM 48,172,252\nR 166,190,53248\nG 4\n"
"M 52,176,236\nR 168,191,40960\nbuffer_load_dwordx4 v220, s[12:15], s63 offen lds\nM 52,180,252\nR 170,191,45056\nM 36,176,232\n"
"R 172,191,49152\nM 36,180,248\nR 174,191,53248\nG 4\nM 40,160,232\nR 176,188,24576\n"
"buffer_load_dwordx4 v221, s[12:15], s63 offen offset:1024 lds\nM 40,164,248\nR 178,188,28672\nM 56,160,236\nR 180,188,32768\nM 56,164,252\n"
"R 182,188,36864\nG 4\nM 60,168,236\nR 160,189,24576\nM 60,172,252\nR 162,189,28672\n"
"M 44,168,232\nR 164,189,32768\nM 44,172,248\nR 166,189,36864\nG 4\nM 0,176,192\n"
"R 168,190,24576\nM 0,180,208\nR 170,190,28672\ns_mov_b32 m0, s51\nM 16,176,196\nR 172,190,32768\n"
"v_cvt_pk_bf16_f32 v32, v32, v33\nv_cvt_pk_bf16_f32 v33, v34, v35\nM 16,180,212\nR 174,190,36864\nG 4\nv_cvt_pk_bf16_f32 v34, v36, v37\n"
"M 20,160,196\nR 176,191,24576\nv_cvt_pk_bf16_f32 v35, v38, v39\nv_cvt_pk_bf16_f32 v36, v40, v41\nM 20,164,212\nR 178,191,28672\n"
"v_cvt_pk_bf16_f32 v37, v42, v43\nv_cvt_pk_bf16_f32 v38, v44, v45\nM 4,160,192\nR 180,191,32768\ns_lshr_b32 s60, s45, 3\ns_lshl2_add_u32 s60, s60, s40\n"
"s_lshl_b32 s60, s60, 8\nv_lshl_add_u32 v224, v252, 3, s60\nv_add_u32 v224, Lqw1+24576-LqPC, v224\nv_min_u32 v224, LqE-LqPC-4, v224\ns_mov_b64 exec, 3\nglobal_load_dword v224, v224, s[98:99]\n"
"s_mov_b64 exec, -1\nv_cvt_pk_bf16_f32 v39, v46, v47\nM 4,164,208\nR 182,191,36864\nG 4\nv_cvt_pk_bf16_f32 v40, v48, v49\n"
"M 8,168,192\nR 160,188,40960\nv_cvt_pk_bf16_f32 v41, v50, v51\nv_cvt_pk_bf16_f32 v42, v52, v53\nM 8,172,208\nR 162,188,45056\n"
"v_cvt_pk_bf16_f32 v43, v54, v55\nv_cvt_pk_bf16_f32 v44, v56, v57\nM 24,168,196\nR 164,188,49152\nv_cvt_pk_bf16_f32 v45, v58, v59\nv_cvt_pk_bf16_f32 v46, v60, v61\n"
"M 24,172,212\nR 166,188,53248\nG 4\nv_cvt_pk_bf16_f32 v47, v62, v63\nM 28,176,196\nR 168,189,40960\n"
"ds_write_addtid_b32 v32 offset:0\nM 28,180,212\nR 170,189,45056\nds_write_addtid_b32 v33 offset:256\nM 12,176,192\nR 172,189,49152\n"
"ds_write_addtid_b32 v34 offset:512\nds_write_addtid_b32 v35 offset:768\nds_write_addtid_b32 v36 offset:1024\nds_write_addtid_b32 v37 offset:1280\nds_write_addtid_b32 v38 offset:1536\nds_write_addtid_b32 v39 offset:1792\n"
"ds_write_addtid_b32 v40 offset:2048\nds_write_addtid_b32 v41 offset:2304\nds_write_addtid_b32 v42 offset:2560\nds_write_addtid_b32 v43 offset:2816\nds_write_addtid_b32 v44 offset:3072\nds_write_addtid_b32 v45 offset:3328\n"
"ds_write_addtid_b32 v46 offset:3584\nds_write_addtid_b32 v47 offset:3840\nM 12,180,208\nR 174,189,53248\nG 15\nM 0,160,224\n"
"R 176,190,40960\ns_add_u32 m0, s50, 32768\nM 0,164,240\nR 178,190,45056\nG 3\ns_barrier\n"
"s_add_u32 s63, s54, 458752\nM 16,160,228\nR 180,190,49152\nbuffer_load_dwordx4 v220, s[12:15], s63 offen lds\nds_read2st64_b32 v[32:33], v225 offset0:0 offset1:1\nM 16,164,244\n"
"R 182,190,53248\nds_read2st64_b32 v[34:35], v225 offset0:2 offset1:3\nG 6\nM 20,168,228\nR 160,191,40960\nds_read2st64_b32 v[36:37], v225 offset0:4 offset1:5\n"
"M 20,172,244\nR 162,191,45056\nds_read2st64_b32 v[38:39], v225 offset0:6 offset1:7\nM 4,168,224\nR 164,191,49152\nbuffer_load_dwordx4 v221, s[12:15], s63 offen offset:1024 lds\n"
"ds_read2st64_b32 v[40:41], v225 offset0:8 offset1:9\nM 4,172,240\nR 166,191,53248\ns_add_u32 m0, s50, 40960\ns_add_u32 s63, s54, 688128\nds_read2st64_b32 v[42:43], v225 offset0:10 offset1:11\n"
"G 9\nM 8,176,224\nbuffer_load_dwordx4 v220, s[12:15], s63 offen lds\nds_read2st64_b32 v[44:45], v225 offset0:12 offset1:13\nM 8,180,240\nds_read2st64_b32 v[46:47], v225 offset0:14 offset1:15\n"
"M 24,176,228\nM 24,180,244\nG 3\nM 28,160,228\nbuffer_load_dwordx4 v221, s[12:15], s63 offen offset:1024 lds\nM 28,164,244\n"
"M 12,160,224\nM 12,164,240\nG 0\ns_barrier\nv_mfma_f32_16x16x32_bf16 v[0:3], v[208:211], v[32:35], v[0:3]\nv_mfma_f32_16x16x32_bf16 v[4:7], v[210:213], v[32:35], v[4:7]\n"
"v_mfma_f32_16x16x32_bf16 v[8:11], v[208:211], v[36:39], v[8:11]\nv_mfma_f32_16x16x32_bf16 v[12:15], v[210:213], v[36:39], v[12:15]\nv_mfma_f32_16x16x32_bf16 v[16:19], v[208:211], v[40:43], v[16:19]\nv_mfma_f32_16x16x32_bf16 v[20:23], v[210:213], v[40:43], v[20:23]\nv_mfma_f32_16x16x32_bf16 v[24:27], v[208:211], v[44:47], v[24:27]\nv_mfma_f32_16x16x32_bf16 v[28:31], v[210:213], v[44:47], v[28:31]\n"
"s_lshl_b32 s84, s75, 11\ns_add_u32 s84, s84, s49\ns_add_u32 s84, s84, 14680064\ns_add_u32 s85, s84, 32768\nv_mov_b32 v238, s46\nv_mov_b32 v239, s47\n"
"v_cvt_pk_bf16_f32 v192, v0, v1\nv_cvt_pk_bf16_f32 v193, v2, v3\nv_cvt_pk_bf16_f32 v194, v4, v5\nv_cvt_pk_bf16_f32 v195, v6, v7\nbuffer_store_dwordx4 v[192:195], v252, s[20:23], s84 offen offset:0 sc1\nv_cvt_pk_bf16_f32 v196, v8, v9\n"
"v_cvt_pk_bf16_f32 v197, v10, v11\nv_cvt_pk_bf16_f32 v198, v12, v13\nv_cvt_pk_bf16_f32 v199, v14, v15\nbuffer_store_dwordx4 v[196:199], v252, s[20:23], s84 offen offset:1024 sc1\nv_cvt_pk_bf16_f32 v200, v16, v17\nv_cvt_pk_bf16_f32 v201, v18, v19\n"
"v_cvt_pk_bf16_f32 v202, v20, v21\nv_cvt_pk_bf16_f32 v203, v22, v23\nbuffer_store_dwordx4 v[200:203], v252, s[20:23], s85 offen offset:0 sc1\nv_cvt_pk_bf16_f32 v204, v24, v25\nv_cvt_pk_bf16_f32 v205, v26, v27\nv_cvt_pk_bf16_f32 v206, v28, v29\n"
"v_cvt_pk_bf16_f32 v207, v30, v31\nbuffer_store_dwordx4 v[204:207], v252, s[20:23], s85 offen offset:1024 sc1\ns_mov_b64 exec, 0\ns_cmp_eq_u32 s40, 1\ns_cbranch_scc1 Lpg25\ns_cmp_eq_u32 s40, 2\n"
"s_cbranch_scc1 Lpg26\ns_cmp_eq_u32 s40, 3\ns_cbranch_scc1 Lpg27\ns_branch Lpg24\nLretO1n:\ns_mov_b64 exec, -1\n"
"s_add_u32 s61, s79, 88637440\ns_lshl_b32 s62, s75, 3\ns_add_u32 s61, s61, s62\ns_waitcnt vmcnt(0)\ns_mov_b64 exec, 1\nbuffer_atomic_swap_x2 v[238:239], off, s[20:23], s61 sc1\n"
"s_mov_b64 exec, -1\nLqw1:\nv_bfe_u32 v250, v252, 7, 1\nv_mul_u32_u24 v250, 2176, v250\nv_bfe_u32 v251, v252, 4, 3\nv_lshl_add_u32 v250, v251, 3, v250\n"
"s_lshl_b32 s60, s90, 6\nv_add_u32 v250, s60, v250\ns_sleep 5\nv_mov_b32 v248, 0\nv_mov_b32 v249, 0\ns_add_u32 s61, s80, 88637440\n"
"s_mov_b64 exec, 0xffff\nbuffer_atomic_or_x2 v[248:249], v250, s[20:23], s61 offen sc0 sc1\ns_mov_b64 exec, -1\ns_add_u32 m0, s50, 49152\ns_add_u32 s63, s54, 458880\nbuffer_load_dwordx4 v220, s[12:15], s63 offen lds\n"
"buffer_load_dwordx4 v221, s[12:15], s63 offen offset:1024 lds\ns_add_u32 m0, s50, 57344\ns_add_u32 s63, s54, 688256\nbuffer_load_dwordx4 v220, s[12:15], s63 offen lds\nbuffer_load_dwordx4 v221, s[12:15], s63 offen offset:1024 lds\ns_add_u32 m0, s50, 65536\n"
"s_add_u32 s63, s54, 917504\nbuffer_load_dwordx4 v220, s[12:15], s63 offen lds\nbuffer_load_dwordx4 v221, s[12:15], s63 offen offset:1024 lds\ns_add_u32 m0, s50, 73728\ns_add_u32 s63, s54, 1146880\nbuffer_load_dwordx4 v220, s[12:15], s63 offen lds\n"
"buffer_load_dwordx4 v221, s[12:15], s63 offen offset:1024 lds\ns_add_u32 m0, s50, 81920\ns_add_u32 s63, s54, 917632\nbuffer_load_dwordx4 v220, s[12:15], s63 offen lds\nbuffer_load_dwordx4 v221, s[12:15], s63 offen offset:1024 lds\ns_add_u32 m0, s50, 90112\n"
"s_add_u32 s63, s54, 1147008\nbuffer_load_dwordx4 v220, s[12:15], s63 offen lds\nbuffer_load_dwordx4 v221, s[12:15], s63 offen offset:1024 lds\nv_mov_b32 v230, 0\nv_mov_b32 v231, 0\nv_mov_b32 v232, 0\n"
"v_mov_b32 v233, 0\nv_dot2c_f32_bf16 v230, s76, v192\nv_dot2c_f32_bf16 v231, v192, v192\nv_dot2c_f32_bf16 v232, s76, v200\nv_dot2c_f32_bf16 v233, v200, v200\nv_dot2c_f32_bf16 v230, s76, v193\n"
"v_dot2c_f32_bf16 v231, v193, v193\nv_dot2c_f32_bf16 v232, s76, v201\nv_dot2c_f32_bf16 v233, v201, v201\nv_dot2c_f32_bf16 v230, s76, v194\nv_dot2c_f32_bf16 v231, v194, v194\nv_dot2c_f32_bf16 v232, s76, v202\n"
"v_dot2c_f32_bf16 v233, v202, v202\nv_dot2c_f32_bf16 v230, s76, v195\nv_dot2c_f32_bf16 v231, v195, v195\nv_dot2c_f32_bf16 v232, s76, v203\nv_dot2c_f32_bf16 v233, v203, v203\nv_dot2c_f32_bf16 v230, s76, v196\n"
"v_dot2c_f32_bf16 v231, v196, v196\nv_dot2c_f32_bf16 v232, s76, v204\nv_dot2c_f32_bf16 v233, v204, v204\nv_dot2c_f32_bf16 v230, s76, v197\nv_dot2c_f32_bf16 v231, v197, v197\nv_dot2c_f32_bf16 v232, s76, v205\n"
"v_dot2c_f32_bf16 v233, v205, v205\nv_dot2c_f32_bf16 v230, s76, v198\nv_dot2c_f32_bf16 v231, v198, v198\nv_dot2c_f32_bf16 v232, s76, v206\nv_dot2c_f32_bf16 v233, v206, v206\nv_dot2c_f32_bf16 v230, s76, v199\n"
"v_dot2c_f32_bf16 v231, v199, v199\nv_dot2c_f32_bf16 v232, s76, v207\nv_dot2c_f32_bf16 v233, v207, v207\nv_lshrrev_b32 v234, 1, v252\nv_permlane32_swap_b32 v230, v231\ns_nop 0\n"
"v_permlane32_swap_b32 v232, v233\nv_add_f32 v230, v230, v231\nv_add_f32 v232, v232, v233\nv_mov_b32 v231, s46\ns_nop 0\nv_permlane16_swap_b32 v230, v232\n"
"v_add_f32 v230, v230, v232\ns_lshr_b32 s61, s49, 3\ns_lshl2_add_u32 s61, s53, s61\ns_add_u32 s61, s61, 83361792\nbuffer_store_dwordx2 v[230:231], v234, s[20:23], s61 offen sc1\ns_waitcnt vmcnt(13)\n"
"v_cmp_eq_u64 s[86:87], s[46:47], v[248:249]\ns_nop 0\ns_and_b32 s62, s86, 0xffff\ns_cmp_eq_u32 s62, 0xffff\ns_cbranch_scc0 Lq769\nLq770:\n"
"s_add_u32 s64, s70, 14680064\nbuffer_load_dwordx4 a[0:3], v252, s[20:23], s64 offen offset:0 sc1\ns_add_u32 s65, s70, 14712832\nbuffer_load_dwordx4 a[4:7], v252, s[20:23], s65 offen offset:0 sc1\ns_add_u32 s66, s71, 14680064\nbuffer_load_dwordx4 a[8:11], v252, s[20:23], s66 offen offset:0 sc1\n"
"s_add_u32 s67, s71, 14712832\nbuffer_load_dwordx4 a[12:15], v252, s[20:23], s67 offen offset:0 sc1\nbuffer_load_dwordx4 a[16:19], v252, s[20:23], s64 offen offset:1024 sc1\nbuffer_load_dwordx4 a[20:23], v252, s[20:23], s65 offen offset:1024 sc1\nbuffer_load_dwordx4 a[24:27], v252, s[20:23], s66 offen offset:1024 sc1\nbuffer_load_dwordx4 a[28:31], v252, s[20:23], s67 offen offset:1024 sc1\n"
"s_barrier\nR 160,184,0\nR 162,184,4096\nR 164,184,8192\nR 166,184,12288\nR 168,185,0\n"
"R 170,185,4096\nR 172,185,8192\nR 174,185,12288\nR 176,186,0\nR 178,186,4096\nR 180,186,8192\n"
"R 182,186,12288\nbuffer_load_dwordx4 a[32:35], v252, s[20:23], s64 offen offset:2048 sc1\nbuffer_load_dwordx4 a[36:39], v252, s[20:23], s65 offen offset:2048 sc1\nbuffer_load_dwordx4 a[40:43], v252, s[20:23], s66 offen offset:2048 sc1\nbuffer_load_dwordx4 a[44:47], v252, s[20:23], s67 offen offset:2048 sc1\nbuffer_load_dwordx4 a[48:51], v252, s[20:23], s64 offen offset:3072 sc1\n"
"buffer_load_dwordx4 a[52:55], v252, s[20:23], s65 offen offset:3072 sc1\nbuffer_load_dwordx4 a[56:59], v252, s[20:23], s66 offen offset:3072 sc1\nbuffer_load_dwordx4 a[60:63], v252, s[20:23], s67 offen offset:3072 sc1\ns_waitcnt vmcnt(15) lgkmcnt(10)\nv_mfma_f32_16x16x32_bf16 v[32:35], v[160:163], a[0:3], 0\ns_add_u32 s64, s70, 14684160\n"
"buffer_load_dwordx4 a[64:67], v252, s[20:23], s64 offen offset:0 sc1\ns_waitcnt vmcnt(12) lgkmcnt(8)\nM 32,164,16\nv_mfma_f32_16x16x32_bf16 v[48:51], v[160:163], a[4:7], 0\ns_waitcnt vmcnt(11)\nM 48,164,20\n"
"v_mfma_f32_16x16x32_bf16 v[64:67], v[160:163], a[8:11], 0\ns_waitcnt vmcnt(10)\nM 64,164,24\ns_add_u32 s65, s70, 14716928\nbuffer_load_dwordx4 a[68:71], v252, s[20:23], s65 offen offset:0 sc1\nv_mfma_f32_16x16x32_bf16 v[80:83], v[160:163], a[12:15], 0\n"
"s_waitcnt vmcnt(10)\nM 80,164,28\nG 4\nv_mfma_f32_16x16x32_bf16 v[84:87], v[168:171], a[12:15], 0\nR 160,187,0\nM 84,172,28\n"
"v_mfma_f32_16x16x32_bf16 v[68:71], v[168:171], a[8:11], 0\nR 162,187,4096\nM 68,172,24\ns_add_u32 s66, s71, 14684160\nbuffer_load_dwordx4 a[72:75], v252, s[20:23], s66 offen offset:0 sc1\nv_mfma_f32_16x16x32_bf16 v[52:55], v[168:171], a[4:7], 0\n"
"R 164,187,8192\nM 52,172,20\nv_mfma_f32_16x16x32_bf16 v[36:39], v[168:171], a[0:3], 0\nR 166,187,12288\nM 36,172,16\nG 4\n"
"v_mfma_f32_16x16x32_bf16 v[40:43], v[176:179], a[0:3], 0\nR 168,184,16384\nM 40,180,16\ns_add_u32 s67, s71, 14716928\nbuffer_load_dwordx4 a[76:79], v252, s[20:23], s67 offen offset:0 sc1\nv_mfma_f32_16x16x32_bf16 v[56:59], v[176:179], a[4:7], 0\n"
"R 170,184,20480\nM 56,180,20\nv_mfma_f32_16x16x32_bf16 v[72:75], v[176:179], a[8:11], 0\nR 172,184,24576\nM 72,180,24\nv_mfma_f32_16x16x32_bf16 v[88:91], v[176:179], a[12:15], 0\n"
"R 174,184,28672\nM 88,180,28\nG 4\nv_mfma_f32_16x16x32_bf16 v[92:95], v[160:163], a[12:15], 0\nR 176,185,16384\nM 92,164,28\n"
"buffer_load_dwordx4 a[80:83], v252, s[20:23], s64 offen offset:1024 sc1\nv_mfma_f32_16x16x32_bf16 v[76:79], v[160:163], a[8:11], 0\nR 178,185,20480\nM 76,164,24\nv_mfma_f32_16x16x32_bf16 v[60:63], v[160:163], a[4:7], 0\nR 180,185,24576\n"
"M 60,164,20\nbuffer_load_dwordx4 a[84:87], v252, s[20:23], s65 offen offset:1024 sc1\nv_mfma_f32_16x16x32_bf16 v[44:47], v[160:163], a[0:3], 0\nR 182,185,28672\nM 44,164,16\nG 4\n"
"v_mfma_f32_16x16x32_bf16 v[96:99], v[168:171], a[0:3], 0\nR 160,186,16384\nM 96,172,16\nv_mfma_f32_16x16x32_bf16 v[112:115], v[168:171], a[4:7], 0\nR 162,186,20480\nM 112,172,20\n"
"buffer_load_dwordx4 a[88:91], v252, s[20:23], s66 offen offset:1024 sc1\nv_mfma_f32_16x16x32_bf16 v[128:131], v[168:171], a[8:11], 0\nR 164,186,24576\nM 128,172,24\nv_mfma_f32_16x16x32_bf16 v[144:147], v[168:171], a[12:15], 0\nR 166,186,28672\n"
"M 144,172,28\ns_barrier\nG 4\nv_mfma_f32_16x16x32_bf16 v[148:151], v[176:179], a[12:15], 0\nR 168,187,16384\ns_add_u32 s63, s54, 1376256\n"
"M 148,180,28\nbuffer_load_dwordx4 a[92:95], v252, s[20:23], s67 offen offset:1024 sc1\nv_mfma_f32_16x16x32_bf16 v[132:135], v[176:179], a[8:11], 0\nR 170,187,20480\nM 132,180,24\ns_add_u32 m0, s50, 98304\n"
"v_mfma_f32_16x16x32_bf16 v[116:119], v[176:179], a[4:7], 0\nR 172,187,24576\nM 116,180,20\nbuffer_load_dwordx4 v220, s[12:15], s63 offen lds\nv_mfma_f32_16x16x32_bf16 v[100:103], v[176:179], a[0:3], 0\nR 174,187,28672\n"
"M 100,180,16\nG 4\nv_mfma_f32_16x16x32_bf16 v[104:107], v[160:163], a[0:3], 0\nR 176,184,32768\nM 104,164,16\nbuffer_load_dwordx4 a[96:99], v252, s[20:23], s64 offen offset:2048 sc1\n"
"v_mfma_f32_16x16x32_bf16 v[120:123], v[160:163], a[4:7], 0\nR 178,184,36864\nM 120,164,20\nbuffer_load_dwordx4 a[100:103], v252, s[20:23], s65 offen offset:2048 sc1\nv_mfma_f32_16x16x32_bf16 v[136:139], v[160:163], a[8:11], 0\nR 180,184,40960\n"
"M 136,164,24\nv_mfma_f32_16x16x32_bf16 v[152:155], v[160:163], a[12:15], 0\nR 182,184,45056\nM 152,164,28\nG 4\nv_mfma_f32_16x16x32_bf16 v[156:159], v[168:171], a[12:15], 0\n"
"R 160,185,32768\nM 156,172,28\nbuffer_load_dwordx4 a[104:107], v252, s[20:23], s66 offen offset:2048 sc1\nv_mfma_f32_16x16x32_bf16 v[140:143], v[168:171], a[8:11], 0\nR 162,185,36864\nM 140,172,24\n"
"v_mfma_f32_16x16x32_bf16 v[124:127], v[168:171], a[4:7], 0\nR 164,185,40960\nM 124,172,20\nbuffer_load_dwordx4 v221, s[12:15], s63 offen offset:1024 lds\nv_mfma_f32_16x16x32_bf16 v[108:111], v[168:171], a[0:3], 0\nR 166,185,45056\n"
"M 108,172,16\nG 4\ns_waitcnt vmcnt(20)\nM 32,176,32\nR 168,186,32768\ns_waitcnt vmcnt(16)\n"
"M 32,180,48\nbuffer_load_dwordx4 a[108:111], v252, s[20:23], s67 offen offset:2048 sc1\nM 48,176,36\nR 170,186,36864\ns_waitcnt vmcnt(16)\nM 48,180,52\n"
"M 64,176,40\nR 172,186,40960\ns_waitcnt vmcnt(15)\nM 64,180,56\nbuffer_load_dwordx4 a[112:115], v252, s[20:23], s64 offen offset:3072 sc1\nM 80,176,44\n"
"R 174,186,45056\ns_waitcnt vmcnt(15)\nM 80,180,60\ns_barrier\nG 4\nM 84,160,44\n"
"R 176,187,32768\nM 84,164,60\nM 68,160,40\nR 178,187,36864\nM 68,164,56\nbuffer_load_dwordx4 a[116:119], v252, s[20:23], s65 offen offset:3072 sc1\n"
"M 52,160,36\nR 180,187,40960\nM 52,164,52\nM 36,160,32\nR 182,187,45056\nM 36,164,48\n"
"G 4\nM 40,168,32\nR 160,184,49152\ns_add_u32 s63, s54, 1605632\nM 40,172,48\nbuffer_load_dwordx4 a[120:123], v252, s[20:23], s66 offen offset:3072 sc1\n"
"M 56,168,36\nR 162,184,53248\nM 56,172,52\ns_add_u32 m0, s50, 106496\nM 72,168,40\nR 164,184,57344\n"
"M 72,172,56\nbuffer_load_dwordx4 v220, s[12:15], s63 offen lds\nM 88,168,44\nR 166,184,61440\nM 88,172,60\nG 4\n"
"M 92,176,44\nR 168,185,49152\nM 92,180,60\nbuffer_load_dwordx4 a[124:127], v252, s[20:23], s67 offen offset:3072 sc1\nM 76,176,40\nR 170,185,53248\n"
"M 76,180,56\nM 60,176,36\nR 172,185,57344\nM 60,180,52\ns_add_u32 s64, s70, 14688256\nbuffer_load_dwordx4 a[128:131], v252, s[20:23], s64 offen offset:0 sc1\n"
"M 44,176,32\nR 174,185,61440\nM 44,180,48\nG 4\nM 96,160,32\nR 176,186,49152\n"
"M 96,164,48\ns_add_u32 s65, s70, 14721024\nbuffer_load_dwordx4 a[132:135], v252, s[20:23], s65 offen offset:0 sc1\nM 112,160,36\nR 178,186,53248\nM 112,164,52\n"
"Lpg24:\ns_cbranch_execz Lpg28\nM 128,160,40\nR 180,186,57344\nM 128,164,56\nbuffer_load_dwordx4 v221, s[12:15], s63 offen offset:1024 lds\n"
"M 144,160,44\nR 182,186,61440\nM 144,164,60\ns_barrier\nG 4\nM 148,168,44\n"
"R 160,187,49152\nM 148,172,60\ns_add_u32 s66, s71, 14688256\nbuffer_load_dwordx4 a[136:139], v252, s[20:23], s66 offen offset:0 sc1\nM 132,168,40\nR 162,187,53248\n"
"M 132,172,56\nM 116,168,36\nR 164,187,57344\nM 116,172,52\ns_add_u32 s67, s71, 14721024\nbuffer_load_dwordx4 a[140:143], v252, s[20:23], s67 offen offset:0 sc1\n"
"M 100,168,32\nR 166,187,61440\nM 100,172,48\nG 4\nM 104,176,32\nR 168,188,0\n"
"M 104,180,48\nM 120,176,36\nR 170,188,4096\nM 120,180,52\nbuffer_load_dwordx4 a[144:147], v252, s[20:23], s64 offen offset:1024 sc1\nM 136,176,40\n"
"R 172,188,8192\nM 136,180,56\nM 152,176,44\nR 174,188,12288\nM 152,180,60\nG 4\n"
"M 156,160,44\nR 176,189,0\ns_add_u32 s63, s54, 1376384\nM 156,164,60\nbuffer_load_dwordx4 a[148:151], v252, s[20:23], s65 offen offset:1024 sc1\nM 140,160,40\n"
"R 178,189,4096\nM 140,164,56\ns_add_u32 m0, s50, 114688\nM 124,160,36\nR 180,189,8192\nM 124,164,52\n"
"buffer_load_dwordx4 v220, s[12:15], s63 offen lds\nM 108,160,32\nR 182,189,12288\nM 108,164,48\nG 4\ns_waitcnt vmcnt(26)\n"
"M 32,168,64\nR 160,190,0\ns_waitcnt vmcnt(22)\nM 32,172,80\nbuffer_load_dwordx4 a[152:155], v252, s[20:23], s66 offen offset:1024 sc1\nM 48,168,68\n"
"R 162,190,4096\ns_waitcnt vmcnt(22)\nM 48,172,84\nM 64,168,72\nR 164,190,8192\ns_waitcnt vmcnt(21)\n"
"M 64,172,88\nbuffer_load_dwordx4 a[156:159], v252, s[20:23], s67 offen offset:1024 sc1\nM 80,168,76\nR 166,190,12288\ns_waitcnt vmcnt(21)\nM 80,172,92\n"
"s_barrier\nG 4\nM 84,176,76\nR 168,191,0\nM 84,180,92\nbuffer_load_dwordx4 v221, s[12:15], s63 offen offset:1024 lds\n"
"M 68,176,72\nR 170,191,4096\nM 68,180,88\nbuffer_load_dwordx4 a[160:163], v252, s[20:23], s64 offen offset:2048 sc1\nM 52,176,68\nR 172,191,8192\n"
"M 52,180,84\ns_add_u32 m0, s50, 0\nM 36,176,64\nR 174,191,12288\ns_add_u32 s63, s54, 1605760\nM 36,180,80\n"
"G 4\nM 40,160,64\nR 176,188,16384\nM 40,164,80\nbuffer_load_dwordx4 a[164:167], v252, s[20:23], s65 offen offset:2048 sc1\nM 56,160,68\n"
"R 178,188,20480\nM 56,164,84\nbuffer_load_dwordx4 v220, s[12:15], s63 offen lds\nM 72,160,72\nR 180,188,24576\nM 72,164,88\n"
"buffer_load_dwordx4 a[168:171], v252, s[20:23], s66 offen offset:2048 sc1\nM 88,160,76\nR 182,188,28672\nM 88,164,92\nG 4\nM 92,168,76\n"
"R 160,189,16384\nbuffer_load_dwordx4 v221, s[12:15], s63 offen offset:1024 lds\nM 92,172,92\nM 76,168,72\nR 162,189,20480\nM 76,172,88\n"
"buffer_load_dwordx4 a[172:175], v252, s[20:23], s67 offen offset:2048 sc1\nM 60,168,68\nR 164,189,24576\nM 60,172,84\nM 44,168,64\nR 166,189,28672\n"
"M 44,172,80\nG 4\nM 96,176,64\nR 168,190,16384\nM 96,180,80\nbuffer_load_dwordx4 a[176:179], v252, s[20:23], s64 offen offset:3072 sc1\n"
"M 112,176,68\nR 170,190,20480\nM 112,180,84\nM 128,176,72\nR 172,190,24576\nM 128,180,88\n"
"s_add_u32 m0, s50, 8192\nM 144,176,76\nR 174,190,28672\ns_add_u32 s63, s54, 1835008\nM 144,180,92\ns_waitcnt vmcnt(15)\n"
"s_barrier\nG 4\nM 148,160,76\nR 176,191,16384\nM 148,164,92\nbuffer_load_dwordx4 a[180:183], v252, s[20:23], s65 offen offset:3072 sc1\n"
"M 132,160,72\nR 178,191,20480\nM 132,164,88\nbuffer_load_dwordx4 v220, s[12:15], s63 offen lds\nM 116,160,68\nR 180,191,24576\n"
"M 116,164,84\nbuffer_load_dwordx4 a[184:187], v252, s[20:23], s66 offen offset:3072 sc1\nM 100,160,64\nR 182,191,28672\nM 100,164,80\nG 4\n"
"M 104,168,64\nR 160,188,32768\nbuffer_load_dwordx4 v221, s[12:15], s63 offen offset:1024 lds\nM 104,172,80\ns_add_u32 m0, s50, 16384\ns_add_u32 s63, s54, 2064384\n"
"buffer_load_dwordx4 v220, s[12:15], s63 offen lds\nM 120,168,68\nR 162,188,36864\nM 120,172,84\nbuffer_load_dwordx4 a[188:191], v252, s[20:23], s67 offen offset:3072 sc1\nM 136,168,72\n"
"R 164,188,40960\nM 136,172,88\nbuffer_load_dwordx4 v221, s[12:15], s63 offen offset:1024 lds\nM 152,168,76\nR 166,188,45056\nM 152,172,92\n"
"G 4\nM 156,176,76\nR 168,189,32768\nM 156,180,92\ns_add_u32 s64, s70, 14692352\nbuffer_load_dwordx4 a[192:195], v252, s[20:23], s64 offen offset:0 sc1\n"
"M 140,176,72\nR 170,189,36864\nM 140,180,88\nM 124,176,68\nR 172,189,40960\nM 124,180,84\n"
"s_add_u32 s65, s70, 14725120\nbuffer_load_dwordx4 a[196:199], v252, s[20:23], s65 offen offset:0 sc1\nM 108,176,64\nR 174,189,45056\nM 108,180,80\nG 4\n"
"M 32,160,96\nR 176,190,32768\nM 32,164,112\nM 48,160,100\nR 178,190,36864\nM 48,164,116\n"
"s_add_u32 s66, s71, 14692352\nbuffer_load_dwordx4 a[200:203], v252, s[20:23], s66 offen offset:0 sc1\nM 64,160,104\nR 180,190,40960\nM 64,164,120\ns_add_u32 m0, s50, 24576\n"
"M 80,160,108\nR 182,190,45056\ns_add_u32 s63, s54, 2293760\nM 80,164,124\ns_waitcnt vmcnt(12)\ns_barrier\n"
"G 4\nM 84,168,108\nR 160,191,32768\nM 84,172,124\ns_add_u32 s67, s71, 14725120\nbuffer_load_dwordx4 a[204:207], v252, s[20:23], s67 offen offset:0 sc1\n"
"M 68,168,104\nR 162,191,36864\nM 68,172,120\nbuffer_load_dwordx4 v220, s[12:15], s63 offen lds\nM 52,168,100\nR 164,191,40960\n"
"M 52,172,116\nbuffer_load_dwordx4 v221, s[12:15], s63 offen offset:1024 lds\nM 36,168,96\nR 166,191,45056\ns_add_u32 m0, s50, 32768\nM 36,172,112\n"
"G 4\nM 40,176,96\nR 168,188,49152\ns_add_u32 s63, s54, 2523136\nM 40,180,112\nbuffer_load_dwordx4 a[208:211], v252, s[20:23], s64 offen offset:1024 sc1\n"
"M 56,176,100\nR 170,188,53248\nM 56,180,116\nbuffer_load_dwordx4 v220, s[12:15], s63 offen lds\nM 72,176,104\nR 172,184,0\n"
"M 72,180,120\nbuffer_load_dwordx4 a[212:215], v252, s[20:23], s65 offen offset:1024 sc1\nM 88,176,108\nR 174,184,4096\nM 88,180,124\nG 4\n"
"M 92,160,108\nR 176,189,49152\nbuffer_load_dwordx4 v221, s[12:15], s63 offen offset:1024 lds\nM 92,164,124\nM 76,160,104\nR 178,189,53248\n"
"M 76,164,120\nbuffer_load_dwordx4 a[216:219], v252, s[20:23], s66 offen offset:1024 sc1\nM 60,160,100\nR 180,185,0\nLq1048:\ns_lshr_b32 s60, s45, 3\n"
"s_lshl2_add_u32 s60, s60, s40\ns_lshl_b32 s60, s60, 8\nv_lshl_add_u32 v224, v252, 3, s60\nv_add_u32 v224, Lq1048+4096-LqPC, v224\nv_min_u32 v224, LqE-LqPC-4, v224\ns_mov_b64 exec, 3\n"
"global_load_dword v224, v224, s[98:99]\ns_mov_b64 exec, -1\nM 60,164,116\nM 44,160,96\nR 182,185,4096\nM 44,164,112\n"
"G 4\nM 96,168,96\nR 160,190,49152\nM 96,172,112\nbuffer_load_dwordx4 a[220:223], v252, s[20:23], s67 offen offset:1024 sc1\nM 112,168,100\n"
"R 162,190,53248\nM 112,172,116\nM 128,168,104\nR 164,186,0\nM 128,172,120\nM 144,168,108\n"
"R 166,186,4096\ns_add_u32 m0, s50, 40960\nM 144,172,124\ns_waitcnt vmcnt(13)\ns_barrier\nG 4\n"
"M 148,176,108\nR 168,191,49152\ns_add_u32 s63, s54, 2752512\nM 148,180,124\nbuffer_load_dwordx4 a[224:227], v252, s[20:23], s64 offen offset:2048 sc1\nM 132,176,104\n"
"R 170,191,53248\nM 132,180,120\nbuffer_load_dwordx4 a[228:231], v252, s[20:23], s65 offen offset:2048 sc1\nM 116,176,100\nR 172,187,0\nbuffer_load_dwordx4 v220, s[12:15], s63 offen lds\n"
"M 116,180,116\nbuffer_load_dwordx4 v221, s[12:15], s63 offen offset:1024 lds\nM 100,176,96\nR 174,187,4096\ns_add_u32 m0, s50, 49152\nM 100,180,112\n"
"G 4\nM 104,160,96\nR 176,184,8192\ns_add_u32 s63, s54, 2981888\nM 104,164,112\nbuffer_load_dwordx4 a[232:235], v252, s[20:23], s66 offen offset:2048 sc1\n"
"M 120,160,100\nR 178,184,12288\nM 120,164,116\nbuffer_load_dwordx4 v220, s[12:15], s63 offen lds\nM 136,160,104\nR 180,184,16384\n"
"M 136,164,120\nbuffer_load_dwordx4 v221, s[12:15], s63 offen offset:1024 lds\nM 152,160,108\nR 182,184,20480\nM 152,164,124\nG 4\n"
"M 156,168,108\nR 160,185,8192\nM 156,172,124\nbuffer_load_dwordx4 a[236:239], v252, s[20:23], s67 offen offset:2048 sc1\nM 140,168,104\nR 162,185,12288\n"
"M 140,172,120\nM 124,168,100\nR 164,185,16384\nM 124,172,116\nbuffer_load_dwordx4 a[240:243], v252, s[20:23], s64 offen offset:3072 sc1\nM 108,168,96\n"
"R 166,185,20480\nM 108,172,112\nG 4\nM 32,176,128\nR 168,186,8192\nM 32,180,144\n"
"M 48,176,132\nR 170,186,12288\nM 48,180,148\nbuffer_load_dwordx4 a[244:247], v252, s[20:23], s65 offen offset:3072 sc1\nM 64,176,136\nR 172,186,16384\n"
"M 64,180,152\nM 80,176,140\nR 174,186,20480\ns_add_u32 m0, s50, 57344\nM 80,180,156\ns_waitcnt vmcnt(13)\n"
"s_barrier\nG 4\nM 84,160,140\nR 176,187,8192\ns_add_u32 s63, s54, 3211264\nM 84,164,156\n"
"buffer_load_dwordx4 a[248:251], v252, s[20:23], s66 offen offset:3072 sc1\nM 68,160,136\nR 178,187,12288\nM 68,164,152\ns_lshr_b32 s84, s49, 3\nM 52,160,132\n"
"R 180,187,16384\nbuffer_load_dwordx4 v220, s[12:15], s63 offen lds\nM 52,164,148\ns_add_u32 s84, s84, 83361792\nM 36,160,128\nR 182,187,20480\n"
"M 36,164,144\nbuffer_load_dwordx4 v[0:3], v252, s[20:23], s84 offen offset:0 sc1\nG 4\nM 40,168,128\nR 160,184,24576\nbuffer_load_dwordx4 v221, s[12:15], s63 offen offset:1024 lds\n"
"M 40,172,144\nbuffer_load_dwordx4 a[252:255], v252, s[20:23], s67 offen offset:3072 sc1\ns_add_u32 s63, s54, 3440640\nM 56,168,132\nR 162,184,28672\ns_add_u32 m0, s50, 65536\n"
"M 56,172,148\nbuffer_load_dwordx4 v[4:7], v252, s[20:23], s84 offen offset:1024 sc1\nM 72,168,136\nR 164,184,32768\ns_lshr_b32 s85, s49, 3\nbuffer_load_dwordx4 v220, s[12:15], s63 offen lds\n"
"M 72,172,152\nbuffer_load_dwordx4 v[8:11], v252, s[20:23], s84 offen offset:2048 sc1\nM 88,168,140\nR 166,184,36864\ns_add_u32 s85, s85, 83365888\nM 88,172,156\n"
"G 4\nM 92,176,140\nR 168,185,24576\nbuffer_load_dwordx4 v221, s[12:15], s63 offen offset:1024 lds\nM 92,180,156\nbuffer_load_dwordx4 v[12:15], v252, s[20:23], s84 offen offset:3072 sc1\n"
"M 76,176,136\nR 170,185,28672\nM 76,180,152\nbuffer_load_dwordx4 v[16:19], v252, s[20:23], s85 offen offset:0 sc1\nM 60,176,132\nR 172,185,32768\n"
"M 60,180,148\nbuffer_load_dwordx4 v[20:23], v252, s[20:23], s85 offen offset:1024 sc1\nM 44,176,128\nR 174,185,36864\nM 44,180,144\nG 4\n"
"M 32,160,160\nR 176,186,24576\nM 32,164,176\nbuffer_load_dwordx4 v[24:27], v252, s[20:23], s85 offen offset:2048 sc1\nM 48,160,164\nR 178,186,28672\n"
"M 48,164,180\nbuffer_load_dwordx4 v[28:31], v252, s[20:23], s85 offen offset:3072 sc1\nM 64,160,168\nR 180,186,32768\ns_add_u32 s63, s54, 1835136\nM 64,164,184\n"
"s_add_u32 m0, s50, 73728\nM 80,160,172\nR 182,186,36864\nM 80,164,188\ns_waitcnt vmcnt(17)\ns_barrier\n"
"G 4\nM 84,168,172\nR 160,187,24576\nM 84,172,188\nbuffer_load_dwordx4 v220, s[12:15], s63 offen lds\nM 68,168,168\n"
"R 162,187,28672\nM 68,172,184\nM 52,168,164\nR 164,187,32768\nv_bfe_u32 v250, v252, 5, 1\nv_mul_u32_u24 v250, 2560, v250\n"
"M 52,172,180\nbuffer_load_dwordx4 v221, s[12:15], s63 offen offset:1024 lds\nv_and_b32 v251, 16, v252\nM 36,168,160\nR 166,187,36864\ns_add_u32 m0, s50, 81920\n"
"v_lshl_add_u32 v250, v251, 1, v250\nM 36,172,176\ns_lshl_b32 s60, s90, 6\nv_add_u32 v250, s60, v250\nG 4\nM 40,176,160\n"
"R 168,184,40960\ns_add_u32 s63, s54, 2064512\nM 40,180,176\nbuffer_load_dwordx4 v220, s[12:15], s63 offen lds\nM 56,176,164\nR 170,184,45056\n"
"M 56,180,180\nM 72,176,168\nR 172,184,49152\nM 72,180,184\nbuffer_load_dwordx4 v221, s[12:15], s63 offen offset:1024 lds\nM 88,176,172\n"
"R 174,184,53248\nM 88,180,188\nG 4\nM 92,160,172\nR 176,185,40960\nM 92,164,188\n"
"M 76,160,168\nR 178,185,45056\nM 76,164,184\ns_add_u32 m0, s50, 90112\nM 60,160,164\nR 180,185,49152\n"
"M 60,164,180\ns_add_u32 s63, s54, 2293888\nM 44,160,160\nR 182,185,53248\nM 44,164,176\nG 4\n"
"M 32,168,192\nR 160,186,40960\nM 32,172,208\nbuffer_load_dwordx4 v220, s[12:15], s63 offen lds\nM 48,168,196\nR 162,186,45056\n"
"M 48,172,212\nM 64,168,200\nR 164,186,49152\nM 64,172,216\nbuffer_load_dwordx4 v221, s[12:15], s63 offen offset:1024 lds\nM 80,168,204\n"
"R 166,186,53248\ns_add_u32 m0, s50, 98304\nM 80,172,220\ns_waitcnt vmcnt(11)\ns_barrier\nG 4\n"
"M 84,176,204\nR 168,187,40960\ns_add_u32 s63, s54, 2523264\nM 84,180,220\nbuffer_load_dwordx4 v220, s[12:15], s63 offen lds\nM 68,176,200\n"
"R 170,187,45056\nv_min3_u32 v230, v1, v3, v5\nv_min3_u32 v230, v230, v7, v9\nM 68,180,216\ns_waitcnt vmcnt(11)\nv_min3_u32 v230, v230, v11, v13\n"
"s_waitcnt vmcnt(10)\nv_min3_u32 v230, v230, v15, v17\ns_waitcnt vmcnt(9)\nv_min3_u32 v230, v230, v19, v21\nM 52,176,196\nR 172,187,49152\n"
"s_waitcnt vmcnt(8)\nv_min3_u32 v230, v230, v23, v25\ns_waitcnt vmcnt(7)\nv_min3_u32 v230, v230, v27, v29\nM 52,180,212\nbuffer_load_dwordx4 v221, s[12:15], s63 offen offset:1024 lds\n"
"v_min3_u32 v230, v230, v31, v31\nM 36,176,192\nR 174,187,53248\ns_add_u32 s63, s54, 2752640\nv_max3_u32 v231, v1, v3, v5\nM 36,180,208\n"
"G 4\nv_max3_u32 v231, v231, v7, v9\nv_max3_u32 v231, v231, v11, v13\nv_max3_u32 v231, v231, v15, v17\nM 40,160,192\nR 176,184,57344\n"
"v_max3_u32 v231, v231, v19, v21\nLpg25:\ns_cbranch_execz Lpg29\nv_max3_u32 v231, v231, v23, v25\nM 40,164,208\ns_add_u32 m0, s50, 106496\n"
"v_max3_u32 v231, v231, v27, v29\nv_max3_u32 v231, v231, v31, v31\nv_cmp_ne_u32 s[86:87], s46, v230\nM 56,160,196\nR 178,184,61440\nv_cmp_ne_u32 s[88:89], s46, v231\n"
"M 56,164,212\nbuffer_load_dwordx4 v220, s[12:15], s63 offen lds\ns_or_b64 s[86:87], s[86:87], s[88:89]\ns_cbranch_scc1 Lq826\nLq827:\nM 72,160,200\n"
"R 180,188,0\nv_add_f32 v0, v0, v16\nv_add_f32 v2, v2, v18\nM 72,164,216\nv_add_f32 v4, v4, v20\nv_add_f32 v6, v6, v22\n"
"v_add_f32 v8, v8, v24\nM 88,160,204\nR 182,188,4096\nv_add_f32 v10, v10, v26\nv_add_f32 v12, v12, v28\nM 88,164,220\n"
"G 4\nv_add_f32 v14, v14, v30\nv_add_f32 v0, v0, v8\nv_add_f32 v2, v2, v10\nM 92,168,204\nR 160,185,57344\n"
"v_add_f32 v4, v4, v12\nv_add_f32 v6, v6, v14\nM 92,172,220\nbuffer_load_dwordx4 v221, s[12:15], s63 offen offset:1024 lds\nv_add_f32 v0, v0, v4\nM 76,168,200\n"
"R 162,185,61440\ns_add_u32 s63, s54, 2982016\nv_add_f32 v2, v2, v6\nM 76,172,216\ns_add_u32 m0, s50, 114688\nv_lshrrev_b32 v231, 1, v252\n"
"v_lshrrev_b32 v230, 2, v252\ns_lshl_b32 s62, s40, 9\ns_add_u32 s62, s62, 159232\nv_add_u32 v231, s62, v231\nM 60,168,196\nR 164,189,0\n"
"ds_write2_b32 v231, v0, v2 offset1:1\nM 60,172,212\nbuffer_load_dwordx4 v220, s[12:15], s63 offen lds\ns_lshl_b32 s62, s40, 9\ns_add_u32 s62, s62, 159232\nv_add_u32 v230, s62, v230\n"
"M 44,168,192\nR 166,189,4096\nds_read2st64_b32 v[232:233], v230 offset1:1\nM 44,172,208\nG 6\nM 32,176,224\n"
"R 168,186,57344\nM 32,180,240\nbuffer_load_dwordx4 v221, s[12:15], s63 offen offset:1024 lds\nM 48,176,228\nR 170,186,61440\ns_add_u32 s63, s54, 3211392\n"
"M 48,180,244\ns_add_u32 m0, s50, 0\nM 64,176,232\nR 172,190,0\nG 3\nv_add_f32 v232, v232, v233\n"
"v_mov_b32 v233, v232\nM 64,180,248\nbuffer_load_dwordx4 v220, s[12:15], s63 offen lds\nv_permlane32_swap_b32 v232, v233\nM 80,176,236\nR 174,190,4096\n"
"v_mov_b32 v230, v232\nv_mov_b32 v231, v233\nM 80,180,252\ns_waitcnt vmcnt(9)\ns_barrier\nv_permlane16_swap_b32 v230, v232\n"
"M 84,160,236\nR 176,187,57344\nv_permlane16_swap_b32 v231, v233\nv_mul_f32 v230, 0xba800000, v230\nM 84,164,252\nbuffer_load_dwordx4 v221, s[12:15], s63 offen offset:1024 lds\n"
"v_mul_f32 v214, v230, v230\nM 68,160,232\nR 178,187,61440\ns_add_u32 s63, s54, 3440768\nv_mul_f32 v231, 0x3a800000, v231\nM 68,164,248\n"
"s_add_u32 m0, s50, 8192\nv_sub_f32 v231, v231, v214\nv_max_f32 v231, 0, v231\nv_add_f32 v231, 0x3727c5ac, v231\nM 52,160,228\nR 180,191,0\n"
"v_and_b32 v208, s77, v230\nv_rsq_f32 v234, v231\nM 52,164,244\nbuffer_load_dwordx4 v220, s[12:15], s63 offen lds\nv_mul_f32 v216, v231, v234\nM 36,160,224\n"
"R 182,191,4096\nv_sub_f32 v230, v230, v208\nv_cvt_pk_bf16_f32 v209, v230, v216\nM 36,164,240\nG 4\nv_cvt_pk_bf16_f32 v208, v208, v208\n"
"v_mul_f32 v214, 0xc0135761, v234\nv_mul_f32 v215, v234, v234\nM 40,168,224\nR 160,188,8192\nv_mul_f32 v215, v215, v214\nv_mul_f32 v215, 0x3d372713, v215\n"
"M 40,172,240\ns_mov_b64 s[60:61], 0xffff\nv_cndmask_b32 v208, 0, v208, s[60:61]\nv_mul_f32 v232, 0xba800000, v232\nv_mul_f32 v217, v232, v232\nM 56,168,228\n"
"R 162,188,12288\nv_mul_f32 v233, 0x3a800000, v233\nv_sub_f32 v233, v233, v217\nM 56,172,244\nbuffer_load_dwordx4 v221, s[12:15], s63 offen offset:1024 lds\nv_max_f32 v233, 0, v233\n"
"M 72,168,232\nR 164,188,16384\ns_add_u32 s63, s55, 0\nv_add_f32 v233, 0x3727c5ac, v233\nM 72,172,248\ns_add_u32 m0, s50, 16384\n"
"v_rsq_f32 v235, v233\nv_and_b32 v212, s77, v232\nM 88,168,236\nR 166,188,20480\nv_mul_f32 v219, v233, v235\nv_sub_f32 v232, v232, v212\n"
"M 88,172,252\ns_mov_b64 s[60:61], 0xffff\nv_cndmask_b32 v209, 0, v209, s[60:61]\nG 4\nv_cvt_pk_bf16_f32 v213, v232, v219\nM 92,176,236\n"
"R 168,189,8192\nv_cvt_pk_bf16_f32 v212, v212, v212\nv_mul_f32 v217, 0xc0135761, v235\nM 92,180,252\nbuffer_load_dwordx4 v222, s[12:15], s63 offen lds\nv_mul_f32 v218, v235, v235\n"
"M 76,176,232\nR 170,189,12288\nv_mul_f32 v218, v218, v217\nv_mul_f32 v218, 0x3d372713, v218\nM 76,180,248\ns_mov_b64 s[60:61], 0xffff\n"
"v_cndmask_b32 v212, 0, v212, s[60:61]\ns_mov_b64 s[60:61], 0xffff\nv_cndmask_b32 v213, 0, v213, s[60:61]\nM 60,176,228\nR 172,189,16384\nM 60,180,244\n"
"buffer_load_dwordx4 v223, s[12:15], s63 offen offset:1024 lds\nM 44,176,224\nR 174,189,20480\nM 44,180,240\ns_waitcnt vmcnt(10)\ns_barrier\n"
"G 4\nM 96,160,128\nR 176,190,8192\nM 96,164,144\ns_mov_b32 m0, s51\nM 112,160,132\n"
"R 178,190,12288\nM 112,164,148\nds_write_addtid_b32 v64 offset:0\nM 128,160,136\nR 180,190,16384\nM 128,164,152\n"
"ds_write_addtid_b32 v65 offset:256\nM 144,160,140\nR 182,190,20480\nds_write_addtid_b32 v66 offset:512\nM 144,164,156\nds_write_addtid_b32 v67 offset:768\n"
"ds_write_addtid_b32 v68 offset:1024\nG 9\nM 148,168,140\nR 160,191,8192\nds_write_addtid_b32 v69 offset:1280\nM 148,172,156\n"
"ds_write_addtid_b32 v70 offset:1536\nds_write_addtid_b32 v71 offset:1792\nM 132,168,136\nR 162,191,12288\nds_write_addtid_b32 v72 offset:2048\nM 132,172,152\n"
"ds_write_addtid_b32 v73 offset:2304\nds_write_addtid_b32 v74 offset:2560\nM 116,168,132\nR 164,191,16384\nds_write_addtid_b32 v75 offset:2816\nM 116,172,148\n"
"ds_write_addtid_b32 v76 offset:3072\nds_write_addtid_b32 v77 offset:3328\nM 100,168,128\nR 166,191,20480\nds_write_addtid_b32 v78 offset:3584\nM 100,172,144\n"
"ds_write_addtid_b32 v79 offset:3840\nds_write_addtid_b32 v80 offset:4096\nG 15\nM 104,176,128\nR 168,188,24576\nds_write_addtid_b32 v81 offset:4352\n"
"M 104,180,144\nds_write_addtid_b32 v82 offset:4608\nds_write_addtid_b32 v83 offset:4864\nM 120,176,132\nR 170,188,28672\nds_write_addtid_b32 v84 offset:5120\n"
"M 120,180,148\nds_write_addtid_b32 v85 offset:5376\nds_write_addtid_b32 v86 offset:5632\nM 136,176,136\nR 172,188,32768\nds_write_addtid_b32 v87 offset:5888\n"
"M 136,180,152\nds_write_addtid_b32 v88 offset:6144\nds_write_addtid_b32 v89 offset:6400\nM 152,176,140\nR 174,188,36864\nds_write_addtid_b32 v90 offset:6656\n"
"M 152,180,156\nds_write_addtid_b32 v91 offset:6912\nds_write_addtid_b32 v92 offset:7168\nG 15\nM 156,160,140\nR 176,189,24576\n"
"ds_write_addtid_b32 v93 offset:7424\nM 156,164,156\nds_write_addtid_b32 v94 offset:7680\nds_write_addtid_b32 v95 offset:7936\nM 140,160,136\nR 178,189,28672\n"
"s_add_u32 m0, s50, 24576\ns_add_u32 s63, s55, 458752\nM 140,164,152\nbuffer_load_dwordx4 v222, s[12:15], s63 offen lds\nM 124,160,132\nR 180,189,32768\n"
"s_waitcnt vmcnt(7) lgkmcnt(2)\ns_barrier\nM 124,164,148\nds_read2st64_b32 v[64:65], v225 offset0:0 offset1:1\nds_read2st64_b32 v[66:67], v225 offset0:2 offset1:3\nM 108,160,128\n"
"R 182,189,36864\nds_read2st64_b32 v[68:69], v225 offset0:4 offset1:5\nM 108,164,144\nds_read2st64_b32 v[70:71], v225 offset0:6 offset1:7\nds_read2st64_b32 v[72:73], v225 offset0:8 offset1:9\nM 96,168,160\n"
"R 160,190,24576\nds_read2st64_b32 v[74:75], v225 offset0:10 offset1:11\nM 96,172,176\nbuffer_load_dwordx4 v223, s[12:15], s63 offen offset:1024 lds\nds_read2st64_b32 v[76:77], v225 offset0:12 offset1:13\nM 112,168,164\n"
"R 162,190,28672\nds_read2st64_b32 v[78:79], v225 offset0:14 offset1:15\nM 112,172,180\ns_add_u32 m0, s50, 32768\nds_read2st64_b32 v[80:81], v225 offset0:16 offset1:17\nds_read2st64_b32 v[82:83], v225 offset0:18 offset1:19\n"
"M 128,168,168\nR 164,190,32768\nds_read2st64_b32 v[84:85], v225 offset0:20 offset1:21\nG 14\nv_add_f32 v32, v32, v64\nM 128,172,184\n"
"s_add_u32 s63, s55, 917504\nds_read2st64_b32 v[86:87], v225 offset0:22 offset1:23\nds_read2st64_b32 v[88:89], v225 offset0:24 offset1:25\nM 144,168,172\nR 166,190,36864\nds_read2st64_b32 v[90:91], v225 offset0:26 offset1:27\n"
"v_add_f32 v33, v33, v65\nM 144,172,188\nds_read2st64_b32 v[92:93], v225 offset0:28 offset1:29\nds_read2st64_b32 v[94:95], v225 offset0:30 offset1:31\nG 15\nM 148,176,172\n"
"R 168,191,24576\nv_add_f32 v34, v34, v66\nv_add_f32 v35, v35, v67\nM 148,180,188\nbuffer_load_dwordx4 v222, s[12:15], s63 offen lds\nv_add_f32 v36, v36, v68\n"
"M 132,176,168\nR 170,191,28672\nv_add_f32 v37, v37, v69\nv_add_f32 v38, v38, v70\nM 132,180,184\nv_add_f32 v39, v39, v71\n"
"v_add_f32 v40, v40, v72\nv_add_f32 v41, v41, v73\nM 116,176,164\nR 172,191,32768\nG 15\nv_add_f32 v42, v42, v74\n"
"v_add_f32 v43, v43, v75\nM 116,180,180\nv_add_f32 v44, v44, v76\nv_add_f32 v45, v45, v77\nG 13\nv_add_f32 v46, v46, v78\n"
"M 100,176,160\nR 174,191,36864\nv_add_f32 v47, v47, v79\nG 13\nv_add_f32 v48, v48, v80\nM 100,180,176\n"
"G 7\nv_add_f32 v49, v49, v81\nv_add_f32 v50, v50, v82\nM 104,160,160\nR 176,188,40960\ns_mov_b32 s60, 157184\n"
"v_bfe_u32 v246, v252, 4, 4\nv_lshl_add_u32 v246, v246, 3, s60\nM 104,164,176\nds_read_b64 v[238:239], v246 offset:0\nv_add_f32 v51, v51, v83\nM 120,160,164\n"
"R 178,188,45056\nds_read_b64 v[240:241], v246 offset:128\nM 120,164,180\nds_read_b64 v[242:243], v246 offset:256\nv_add_f32 v52, v52, v84\nM 136,160,168\n"
"R 180,188,49152\nds_read_b64 v[244:245], v246 offset:384\nM 136,164,184\nds_read_b64 v[246:247], v246 offset:0\nv_add_f32 v53, v53, v85\nM 152,160,172\n"
"R 182,188,53248\nv_add_f32 v54, v54, v86\nM 152,164,188\nG 9\nv_add_f32 v55, v55, v87\nv_add_f32 v56, v56, v88\n"
"M 156,168,172\nR 160,189,40960\nv_add_f32 v57, v57, v89\nM 156,172,188\nbuffer_load_dwordx4 v223, s[12:15], s63 offen offset:1024 lds\nM 140,168,168\n"
"R 162,189,45056\nv_add_f32 v58, v58, v90\nM 140,172,184\ns_add_u32 m0, s50, 40960\nv_add_f32 v59, v59, v91\nv_add_f32 v60, v60, v92\n"
"M 124,168,164\nR 164,189,49152\nv_add_f32 v61, v61, v93\nM 124,172,180\ns_add_u32 s63, s55, 1376256\nv_add_f32 v62, v62, v94\n"
"v_add_f32 v63, v63, v95\nM 108,168,160\nR 166,189,53248\nG 9\nv_mfma_f32_16x16x32_bf16 v[32:35], v[238:241], v[208:211], v[32:35]\nM 108,172,176\n"
"G 8\nv_mfma_f32_16x16x32_bf16 v[36:39], v[240:243], v[208:211], v[36:39]\nG 4\nM 96,176,192\nR 168,190,40960\nv_mfma_f32_16x16x32_bf16 v[40:43], v[242:245], v[208:211], v[40:43]\n"
"M 96,180,208\nv_mfma_f32_16x16x32_bf16 v[44:47], v[244:247], v[208:211], v[44:47]\nbuffer_load_dwordx4 v222, s[12:15], s63 offen lds\nM 112,176,196\nR 170,190,45056\nv_mfma_f32_16x16x32_bf16 v[48:51], v[244:247], v[210:213], v[48:51]\n"
"s_waitcnt vmcnt(5)\ns_barrier\nM 112,180,212\nv_mfma_f32_16x16x32_bf16 v[52:55], v[238:241], v[210:213], v[52:55]\nM 128,176,200\nR 172,190,49152\n"
"v_mfma_f32_16x16x32_bf16 v[56:59], v[240:243], v[210:213], v[56:59]\nM 128,180,216\nv_mfma_f32_16x16x32_bf16 v[60:63], v[242:245], v[210:213], v[60:63]\nbuffer_load_dwordx4 v223, s[12:15], s63 offen offset:1024 lds\nM 144,176,204\nR 174,190,53248\n"
"s_add_u32 s63, s55, 1835008\nM 144,180,220\nG 4\nM 148,160,204\nR 176,191,40960\nM 148,164,220\n"
"s_add_u32 m0, s50, 49152\nM 132,160,200\nR 178,191,45056\nM 132,164,216\nbuffer_load_dwordx4 v222, s[12:15], s63 offen lds\nM 116,160,196\n"
"R 180,191,49152\nv_mul_f32 v230, v32, v32\nv_mul_f32 v231, v33, v33\nM 116,164,212\ns_add_u32 s84, s73, 37748736\ns_add_u32 s85, s73, 37863424\n"
"v_mul_f32 v232, v34, v34\nv_mul_f32 v233, v35, v35\nM 100,160,192\nR 182,191,53248\nv_fma_f32 v230, v230, v215, v214\nv_fma_f32 v231, v231, v215, v214\n"
"M 100,164,208\nG 4\nv_fma_f32 v232, v232, v215, v214\nv_fma_f32 v233, v233, v215, v214\nv_mul_f32 v230, v32, v230\nM 104,168,192\n"
"R 160,184,0\nv_mul_f32 v231, v33, v231\nv_mul_f32 v232, v34, v232\nM 104,172,208\nv_exp_f32 v230, v230\nv_mul_f32 v233, v35, v233\n"
"M 120,168,196\nR 162,184,4096\nv_fma_f32 v230, v230, v216, v216\nv_exp_f32 v231, v231\nM 120,172,212\nbuffer_load_dwordx4 v223, s[12:15], s63 offen offset:1024 lds\n"
"v_exp_f32 v232, v232\nM 136,168,200\nR 164,184,8192\nv_fma_f32 v231, v231, v216, v216\nv_exp_f32 v233, v233\nM 136,172,216\n"
"s_add_u32 m0, s50, 57344\nv_rcp_f32 v230, v230\nv_fma_f32 v232, v232, v216, v216\nM 152,168,204\nR 166,184,12288\nv_fma_f32 v233, v233, v216, v216\n"
"v_rcp_f32 v231, v231\nM 152,172,220\nG 4\nv_rcp_f32 v232, v232\nv_rcp_f32 v233, v233\nM 156,176,204\n"
"R 168,185,0\nv_mul_f32 v230, v32, v230\nv_mul_f32 v231, v33, v231\nM 156,180,220\ns_add_u32 s63, s55, 2293760\nv_mul_f32 v232, v34, v232\n"
"v_mul_f32 v233, v35, v233\nv_cvt_pk_bf16_f32 v32, v230, v231\nM 140,176,200\nR 170,185,4096\nv_cvt_pk_bf16_f32 v33, v232, v233\nv_mul_f32 v234, v36, v36\n"
"M 140,180,216\nbuffer_load_dwordx4 v222, s[12:15], s63 offen lds\nv_mul_f32 v235, v37, v37\nM 124,176,196\nR 172,185,8192\nv_mul_f32 v236, v38, v38\n"
"v_mul_f32 v237, v39, v39\nM 124,180,212\nv_fma_f32 v234, v234, v215, v214\nv_fma_f32 v235, v235, v215, v214\nv_fma_f32 v236, v236, v215, v214\nM 108,176,192\n"
"R 174,185,12288\ns_mov_b32 s60, 157696\nv_bfe_u32 v246, v252, 4, 4\nv_lshl_add_u32 v246, v246, 3, s60\nv_fma_f32 v237, v237, v215, v214\nM 108,180,208\n"
"ds_read_b64 v[238:239], v246 offset:0\nG 5\nv_mul_f32 v234, v36, v234\nM 96,160,224\nR 176,186,0\nds_read_b64 v[240:241], v246 offset:128\n"
"v_mul_f32 v235, v37, v235\nM 96,164,240\nds_read_b64 v[242:243], v246 offset:256\nv_mul_f32 v236, v38, v236\nv_mul_f32 v237, v39, v237\nM 112,160,228\n"
"R 178,186,4096\nds_read_b64 v[244:245], v246 offset:384\nv_exp_f32 v234, v234\nM 112,164,244\nds_read_b64 v[246:247], v246 offset:0\nv_fma_f32 v234, v234, v216, v216\n"
"v_exp_f32 v235, v235\nM 128,160,232\nR 180,186,8192\nv_fma_f32 v235, v235, v216, v216\nv_exp_f32 v236, v236\nM 128,164,248\n"
"buffer_load_dwordx4 v223, s[12:15], s63 offen offset:1024 lds\nv_exp_f32 v237, v237\nM 144,160,236\nR 182,186,12288\ns_add_u32 s63, s55, 2752512\nv_fma_f32 v236, v236, v216, v216\n"
"M 144,164,252\nG 9\nv_rcp_f32 v234, v234\nv_fma_f32 v237, v237, v216, v216\nM 148,168,236\nR 160,187,0\n"
"v_mul_f32 v234, v36, v234\nv_rcp_f32 v235, v235\nM 148,172,252\ns_add_u32 m0, s50, 65536\nv_rcp_f32 v236, v236\nv_rcp_f32 v237, v237\n"
"M 132,168,232\nR 162,187,4096\nv_mul_f32 v235, v37, v235\nv_mul_f32 v236, v38, v236\nM 132,172,248\nbuffer_load_dwordx4 v222, s[12:15], s63 offen lds\n"
"v_mul_f32 v237, v39, v237\nM 116,168,228\nR 164,187,8192\nG 9\nv_mfma_f32_16x16x32_bf16 v[96:99], v[238:241], v[208:211], v[96:99]\nv_cvt_pk_bf16_f32 v34, v234, v235\n"
"v_cvt_pk_bf16_f32 v35, v236, v237\nM 116,172,244\nv_mul_f32 v230, v40, v40\nv_mul_f32 v231, v41, v41\nv_mul_f32 v232, v42, v42\nM 100,168,224\n"
"R 166,187,12288\nG 9\nv_mfma_f32_16x16x32_bf16 v[100:103], v[240:243], v[208:211], v[100:103]\nv_mul_f32 v233, v43, v43\nv_fma_f32 v230, v230, v215, v214\nM 100,172,240\n"
"G 4\nv_fma_f32 v231, v231, v215, v214\nv_fma_f32 v232, v232, v215, v214\nv_fma_f32 v233, v233, v215, v214\nM 104,176,224\nR 168,184,16384\n"
"v_mfma_f32_16x16x32_bf16 v[104:107], v[242:245], v[208:211], v[104:107]\nv_mul_f32 v230, v40, v230\nv_mul_f32 v231, v41, v231\nM 104,180,240\nbuffer_load_dwordx4 v223, s[12:15], s63 offen offset:1024 lds\nv_mul_f32 v232, v42, v232\n"
"M 120,176,228\nR 170,184,20480\nv_mfma_f32_16x16x32_bf16 v[108:111], v[244:247], v[208:211], v[108:111]\nv_mul_f32 v233, v43, v233\nv_exp_f32 v230, v230\nM 120,180,244\n"
"s_add_u32 m0, s50, 73728\nv_exp_f32 v231, v231\nv_exp_f32 v232, v232\nM 136,176,232\nR 172,186,16384\nv_mfma_f32_16x16x32_bf16 v[112:115], v[244:247], v[210:213], v[112:115]\n"
"v_fma_f32 v230, v230, v216, v216\nv_exp_f32 v233, v233\nM 136,180,248\ns_add_u32 s63, s55, 3211264\nv_rcp_f32 v230, v230\nv_fma_f32 v231, v231, v216, v216\n"
"Lpg26:\ns_cbranch_execz Lpg30\nM 152,176,236\nR 174,186,20480\nv_mfma_f32_16x16x32_bf16 v[116:119], v[238:241], v[210:213], v[116:119]\nv_fma_f32 v232, v232, v216, v216\n"
"v_fma_f32 v233, v233, v216, v216\nM 152,180,252\nG 4\nv_rcp_f32 v231, v231\nv_rcp_f32 v232, v232\nM 156,160,236\n"
"R 176,185,16384\nv_mfma_f32_16x16x32_bf16 v[120:123], v[240:243], v[210:213], v[120:123]\nv_mul_f32 v230, v40, v230\nv_rcp_f32 v233, v233\nM 156,164,252\nbuffer_load_dwordx4 v222, s[12:15], s63 offen lds\n"
"v_mul_f32 v231, v41, v231\nM 140,160,232\nR 178,185,20480\nv_mfma_f32_16x16x32_bf16 v[124:127], v[242:245], v[210:213], v[124:127]\nv_mul_f32 v232, v42, v232\nv_mul_f32 v233, v43, v233\n"
"M 140,164,248\nbuffer_store_dwordx4 v[32:35], v252, s[20:23], s84 offen offset:0 sc1\nv_cvt_pk_bf16_f32 v40, v230, v231\nM 124,160,228\nR 180,187,16384\nv_cvt_pk_bf16_f32 v41, v232, v233\n"
"v_mul_f32 v234, v44, v44\nM 124,164,244\nbuffer_load_dwordx4 v223, s[12:15], s63 offen offset:1024 lds\nv_mul_f32 v235, v45, v45\nM 108,160,224\nR 182,187,20480\n"
"v_mul_f32 v236, v46, v46\nv_mul_f32 v237, v47, v47\nM 108,164,240\ns_waitcnt vmcnt(11)\ns_barrier\nG 4\n"
"v_fma_f32 v234, v234, v215, v214\nv_mfma_f32_16x16x32_bf16 v[0:3], v[168:171], a[0:3], 0\nR 160,184,24576\nv_fma_f32 v235, v235, v215, v214\nv_fma_f32 v236, v236, v215, v214\nM 0,172,16\n"
"s_mov_b32 m0, s51\nv_fma_f32 v237, v237, v215, v214\nv_mul_f32 v234, v44, v234\nv_mul_f32 v235, v45, v235\nv_mfma_f32_16x16x32_bf16 v[16:19], v[168:171], a[4:7], 0\nR 162,184,28672\n"
"v_mul_f32 v236, v46, v236\nv_mul_f32 v237, v47, v237\nM 16,172,20\nv_exp_f32 v234, v234\nv_exp_f32 v235, v235\nv_mfma_f32_16x16x32_bf16 v[8:11], v[168:171], a[8:11], 0\n"
"R 164,186,24576\nv_fma_f32 v234, v234, v216, v216\nv_exp_f32 v236, v236\nM 8,172,24\nds_write_addtid_b32 v128 offset:0\nv_exp_f32 v237, v237\n"
"v_fma_f32 v235, v235, v216, v216\nv_mfma_f32_16x16x32_bf16 v[24:27], v[168:171], a[12:15], 0\nR 166,186,28672\nds_write_addtid_b32 v129 offset:256\nv_fma_f32 v236, v236, v216, v216\nv_fma_f32 v237, v237, v216, v216\n"
"M 24,172,28\nds_write_addtid_b32 v130 offset:512\nG 7\nv_rcp_f32 v234, v234\nv_rcp_f32 v235, v235\nv_mfma_f32_16x16x32_bf16 v[28:31], v[176:179], a[12:15], 0\n"
"R 168,185,24576\nds_write_addtid_b32 v131 offset:768\nv_mul_f32 v234, v44, v234\nv_rcp_f32 v236, v236\nM 28,180,28\nds_write_addtid_b32 v132 offset:1024\n"
"v_rcp_f32 v237, v237\nv_mul_f32 v235, v45, v235\nv_mfma_f32_16x16x32_bf16 v[12:15], v[176:179], a[8:11], 0\nR 170,185,28672\nds_write_addtid_b32 v133 offset:1280\nv_mul_f32 v236, v46, v236\n"
"v_mul_f32 v237, v47, v237\nM 12,180,24\nds_write_addtid_b32 v134 offset:1536\nv_cvt_pk_bf16_f32 v42, v234, v235\nv_cvt_pk_bf16_f32 v43, v236, v237\nv_mul_f32 v230, v48, v48\n"
"v_mfma_f32_16x16x32_bf16 v[20:23], v[176:179], a[4:7], 0\nR 172,187,24576\nds_write_addtid_b32 v135 offset:1792\nv_mul_f32 v231, v49, v49\nv_mul_f32 v232, v50, v50\nM 20,180,20\n"
"buffer_store_dwordx4 v[40:43], v252, s[20:23], s84 offen offset:1024 sc1\nds_write_addtid_b32 v136 offset:2048\nv_mul_f32 v233, v51, v51\nv_mfma_f32_16x16x32_bf16 v[4:7], v[176:179], a[0:3], 0\nR 174,187,28672\nds_write_addtid_b32 v137 offset:2304\n"
"v_fma_f32 v230, v230, v218, v217\nv_fma_f32 v231, v231, v218, v217\nM 4,180,16\nds_write_addtid_b32 v138 offset:2560\nG 14\nv_fma_f32 v232, v232, v218, v217\n"
"v_fma_f32 v233, v233, v218, v217\nv_mul_f32 v230, v48, v230\nM 0,160,32\nR 176,184,32768\nds_write_addtid_b32 v139 offset:2816\nv_mul_f32 v231, v49, v231\n"
"v_mul_f32 v232, v50, v232\nM 0,164,48\nds_write_addtid_b32 v140 offset:3072\nv_exp_f32 v230, v230\nv_mul_f32 v233, v51, v233\nM 16,160,36\n"
"R 178,184,36864\nds_write_addtid_b32 v141 offset:3328\nv_fma_f32 v230, v230, v219, v219\nv_exp_f32 v231, v231\nM 16,164,52\nds_write_addtid_b32 v142 offset:3584\n"
"v_exp_f32 v232, v232\nv_exp_f32 v233, v233\nM 8,160,40\nR 180,186,32768\nds_write_addtid_b32 v143 offset:3840\nv_fma_f32 v231, v231, v219, v219\n"
"v_fma_f32 v232, v232, v219, v219\nM 8,164,56\nds_write_addtid_b32 v144 offset:4096\nv_rcp_f32 v230, v230\nv_fma_f32 v233, v233, v219, v219\nM 24,160,44\n"
"R 182,186,36864\nds_write_addtid_b32 v145 offset:4352\nv_mul_f32 v230, v48, v230\nv_rcp_f32 v231, v231\nM 24,164,60\nds_write_addtid_b32 v146 offset:4608\n"
"G 14\nv_rcp_f32 v232, v232\nv_rcp_f32 v233, v233\nM 28,168,44\nR 160,185,32768\nds_write_addtid_b32 v147 offset:4864\n"
"v_mul_f32 v231, v49, v231\nv_mul_f32 v232, v50, v232\nM 28,172,60\nds_write_addtid_b32 v148 offset:5120\nds_write_addtid_b32 v149 offset:5376\nds_write_addtid_b32 v150 offset:5632\n"
"ds_write_addtid_b32 v151 offset:5888\nds_write_addtid_b32 v152 offset:6144\nds_write_addtid_b32 v153 offset:6400\nds_write_addtid_b32 v154 offset:6656\nds_write_addtid_b32 v155 offset:6912\nds_write_addtid_b32 v156 offset:7168\n"
"ds_write_addtid_b32 v157 offset:7424\nds_write_addtid_b32 v158 offset:7680\nds_write_addtid_b32 v159 offset:7936\nv_mul_f32 v233, v51, v233\nv_cvt_pk_bf16_f32 v48, v230, v231\nv_cvt_pk_bf16_f32 v49, v232, v233\n"
"M 12,168,40\nR 162,185,36864\ns_add_u32 m0, s50, 81920\ns_add_u32 s63, s54, 256\nv_mul_f32 v234, v52, v52\nM 12,172,56\n"
"buffer_load_dwordx4 v220, s[12:15], s63 offen lds\nv_mul_f32 v235, v53, v53\nM 20,168,36\nR 164,187,32768\ns_waitcnt vmcnt(5) lgkmcnt(2)\ns_barrier\n"
"v_mul_f32 v236, v54, v54\nM 20,172,52\nds_read2st64_b32 v[128:129], v225 offset0:0 offset1:1\nv_mul_f32 v237, v55, v55\nv_fma_f32 v234, v234, v218, v217\nv_fma_f32 v235, v235, v218, v217\n"
"M 4,168,32\nR 166,187,36864\nds_read2st64_b32 v[130:131], v225 offset0:2 offset1:3\nv_fma_f32 v236, v236, v218, v217\nv_fma_f32 v237, v237, v218, v217\nM 4,172,48\n"
"ds_read2st64_b32 v[132:133], v225 offset0:4 offset1:5\nv_mul_f32 v234, v52, v234\nv_mul_f32 v235, v53, v235\nv_mul_f32 v236, v54, v236\nM 0,176,64\nR 168,184,40960\n"
"ds_read2st64_b32 v[134:135], v225 offset0:6 offset1:7\nv_mul_f32 v237, v55, v237\nv_exp_f32 v234, v234\nM 0,180,80\nbuffer_load_dwordx4 v221, s[12:15], s63 offen offset:1024 lds\nds_read2st64_b32 v[136:137], v225 offset0:8 offset1:9\n"
"v_exp_f32 v235, v235\nM 16,176,68\nR 170,184,45056\ns_add_u32 m0, s50, 90112\ns_add_u32 s63, s54, 229632\nds_read2st64_b32 v[138:139], v225 offset0:10 offset1:11\n"
"v_exp_f32 v236, v236\nM 16,180,84\nbuffer_load_dwordx4 v220, s[12:15], s63 offen lds\nds_read2st64_b32 v[140:141], v225 offset0:12 offset1:13\nv_exp_f32 v237, v237\nM 8,176,72\n"
"R 172,186,40960\nds_read2st64_b32 v[142:143], v225 offset0:14 offset1:15\nv_fma_f32 v234, v234, v219, v219\nv_fma_f32 v235, v235, v219, v219\nM 8,180,88\nds_read2st64_b32 v[144:145], v225 offset0:16 offset1:17\n"
"v_rcp_f32 v234, v234\nv_fma_f32 v236, v236, v219, v219\nM 24,176,76\nR 174,186,45056\nds_read2st64_b32 v[146:147], v225 offset0:18 offset1:19\nv_fma_f32 v237, v237, v219, v219\n"
"v_rcp_f32 v235, v235\nM 24,180,92\nds_read2st64_b32 v[148:149], v225 offset0:20 offset1:21\nG 14\nv_rcp_f32 v236, v236\nv_rcp_f32 v237, v237\n"
"M 28,160,76\nR 176,185,40960\nds_read2st64_b32 v[150:151], v225 offset0:22 offset1:23\nv_mul_f32 v234, v52, v234\nv_mul_f32 v235, v53, v235\nM 28,164,92\n"
"buffer_load_dwordx4 v221, s[12:15], s63 offen offset:1024 lds\nds_read2st64_b32 v[152:153], v225 offset0:24 offset1:25\nv_mul_f32 v236, v54, v236\nM 12,160,72\nR 178,185,45056\ns_add_u32 s63, s54, 459008\n"
"ds_read2st64_b32 v[154:155], v225 offset0:26 offset1:27\nv_mul_f32 v237, v55, v237\nM 12,164,88\ns_add_u32 m0, s50, 98304\nds_read2st64_b32 v[156:157], v225 offset0:28 offset1:29\nv_cvt_pk_bf16_f32 v50, v234, v235\n"
"v_cvt_pk_bf16_f32 v51, v236, v237\nv_mul_f32 v230, v56, v56\nM 20,160,68\nR 180,187,40960\nds_read2st64_b32 v[158:159], v225 offset0:30 offset1:31\nv_mul_f32 v231, v57, v57\n"
"v_mul_f32 v232, v58, v58\nM 20,164,84\nbuffer_load_dwordx4 v220, s[12:15], s63 offen lds\nv_add_f32 v96, v96, v128\nv_mul_f32 v233, v59, v59\nM 4,160,64\n"
"R 182,187,45056\nv_fma_f32 v230, v230, v218, v217\nv_add_f32 v97, v97, v129\nv_fma_f32 v231, v231, v218, v217\nM 4,164,80\nG 11\n"
"v_fma_f32 v232, v232, v218, v217\nv_fma_f32 v233, v233, v218, v217\nv_add_f32 v98, v98, v130\nv_mul_f32 v230, v56, v230\nM 0,168,96\nR 160,184,49152\n"
"v_mul_f32 v231, v57, v231\nv_add_f32 v99, v99, v131\nv_mul_f32 v232, v58, v232\nM 0,172,112\nbuffer_load_dwordx4 v221, s[12:15], s63 offen offset:1024 lds\nv_add_f32 v100, v100, v132\n"
"v_mul_f32 v233, v59, v233\nM 16,168,100\nR 162,184,53248\ns_add_u32 s63, s54, 688384\nv_add_f32 v101, v101, v133\nv_exp_f32 v230, v230\n"
"M 16,172,116\ns_add_u32 m0, s50, 106496\nv_exp_f32 v231, v231\nv_add_f32 v102, v102, v134\nv_exp_f32 v232, v232\nM 8,168,104\n"
"R 164,186,49152\nv_fma_f32 v230, v230, v219, v219\nv_add_f32 v103, v103, v135\nv_exp_f32 v233, v233\nM 8,172,120\nbuffer_load_dwordx4 v220, s[12:15], s63 offen lds\n"
"v_add_f32 v104, v104, v136\nv_fma_f32 v231, v231, v219, v219\nM 24,168,108\nR 166,186,53248\nv_fma_f32 v232, v232, v219, v219\nv_add_f32 v105, v105, v137\n"
"v_fma_f32 v233, v233, v219, v219\nM 24,172,124\nG 4\nv_rcp_f32 v230, v230\nv_add_f32 v106, v106, v138\nv_rcp_f32 v231, v231\n"
"M 28,176,108\nR 168,185,49152\nv_mul_f32 v230, v56, v230\nv_add_f32 v107, v107, v139\nv_rcp_f32 v232, v232\nM 28,180,124\n"
"buffer_store_dwordx4 v[48:51], v252, s[20:23], s85 offen offset:0 sc1\nv_add_f32 v108, v108, v140\nv_rcp_f32 v233, v233\nM 12,176,104\nR 170,185,53248\nv_mul_f32 v231, v57, v231\n"
"v_add_f32 v109, v109, v141\nv_mul_f32 v232, v58, v232\nM 12,180,120\nbuffer_load_dwordx4 v221, s[12:15], s63 offen offset:1024 lds\nv_add_f32 v110, v110, v142\nv_mul_f32 v233, v59, v233\n"
"M 20,176,100\nR 172,187,49152\ns_add_u32 s63, s54, 917760\nv_add_f32 v111, v111, v143\nv_cvt_pk_bf16_f32 v56, v230, v231\nM 20,180,116\n"
"s_add_u32 m0, s50, 114688\nv_cvt_pk_bf16_f32 v57, v232, v233\nv_mul_f32 v234, v60, v60\nv_add_f32 v112, v112, v144\nv_add_f32 v113, v113, v145\nv_add_f32 v114, v114, v146\n"
"v_add_f32 v115, v115, v147\nv_add_f32 v116, v116, v148\nv_add_f32 v117, v117, v149\nv_add_f32 v118, v118, v150\nv_add_f32 v119, v119, v151\nv_add_f32 v120, v120, v152\n"
"v_add_f32 v121, v121, v153\nv_add_f32 v122, v122, v154\nv_add_f32 v123, v123, v155\nv_add_f32 v124, v124, v156\nv_add_f32 v125, v125, v157\nv_add_f32 v126, v126, v158\n"
"v_add_f32 v127, v127, v159\nv_mul_f32 v235, v61, v61\nM 4,176,96\nR 174,187,53248\nv_mul_f32 v236, v62, v62\nv_mul_f32 v237, v63, v63\n"
"M 4,180,112\nG 4\nv_fma_f32 v234, v234, v218, v217\nv_fma_f32 v235, v235, v218, v217\nv_fma_f32 v236, v236, v218, v217\nM 0,160,128\n"
"R 176,184,57344\nv_fma_f32 v237, v237, v218, v217\nv_mul_f32 v234, v60, v234\nM 0,164,144\nbuffer_load_dwordx4 v220, s[12:15], s63 offen lds\nv_mul_f32 v235, v61, v235\n"
"M 16,160,132\nR 178,184,61440\nv_mul_f32 v236, v62, v236\nv_mul_f32 v237, v63, v237\nM 16,164,148\nv_exp_f32 v234, v234\n"
"v_exp_f32 v235, v235\nM 8,160,136\nR 180,186,57344\nLq1050:\ns_lshr_b32 s60, s45, 3\ns_lshl2_add_u32 s60, s60, s40\n"
"s_lshl_b32 s60, s60, 8\nv_lshl_add_u32 v224, v252, 3, s60\nv_add_u32 v224, Lq1050+4096-LqPC, v224\nv_min_u32 v224, LqE-LqPC-4, v224\ns_mov_b64 exec, 3\nglobal_load_dword v224, v224, s[98:99]\n"
"s_mov_b64 exec, -1\nv_exp_f32 v236, v236\nM 8,164,152\nv_exp_f32 v237, v237\nv_fma_f32 v234, v234, v219, v219\nM 24,160,140\n"
"R 182,186,61440\nv_fma_f32 v235, v235, v219, v219\nv_fma_f32 v236, v236, v219, v219\nM 24,164,156\nG 4\nv_rcp_f32 v234, v234\n"
"v_fma_f32 v237, v237, v219, v219\nM 28,168,140\nR 160,185,57344\nv_mul_f32 v234, v60, v234\nM 28,172,156\nbuffer_load_dwordx4 v221, s[12:15], s63 offen offset:1024 lds\n"
"M 12,168,136\nR 162,185,61440\ns_add_u32 m0, s50, 0\ns_add_u32 s63, s54, 1147136\nM 12,172,152\nbuffer_load_dwordx4 v220, s[12:15], s63 offen lds\n"
"M 20,168,132\nR 164,187,57344\nv_rcp_f32 v235, v235\nM 20,172,148\nv_rcp_f32 v236, v236\nM 4,168,128\n"
"R 166,187,61440\nv_mul_f32 v235, v61, v235\nM 4,172,144\nG 4\nv_rcp_f32 v237, v237\nM 0,176,160\n"
"R 168,188,0\nv_mul_f32 v236, v62, v236\nM 0,180,176\nbuffer_load_dwordx4 v221, s[12:15], s63 offen offset:1024 lds\nM 16,176,164\nR 170,188,4096\n"
"s_add_u32 m0, s50, 8192\ns_add_u32 s63, s54, 1376512\nM 16,180,180\nbuffer_load_dwordx4 v220, s[12:15], s63 offen lds\nM 8,176,168\nR 172,190,0\n"
"v_mul_f32 v237, v63, v237\nM 8,180,184\nv_cvt_pk_bf16_f32 v58, v234, v235\nv_cvt_pk_bf16_f32 v59, v236, v237\nM 24,176,172\nR 174,190,4096\n"
"M 24,180,188\nbuffer_store_dwordx4 v[56:59], v252, s[20:23], s85 offen offset:1024 sc1\nG 4\nM 28,160,172\nR 176,189,0\ns_waitcnt vmcnt(12)\n"
"s_barrier\nv_mul_f32 v230, v96, v96\nM 28,164,188\nbuffer_load_dwordx4 v221, s[12:15], s63 offen offset:1024 lds\nv_mul_f32 v231, v97, v97\nM 12,160,168\n"
"R 178,189,4096\ns_add_u32 m0, s50, 16384\ns_add_u32 s63, s54, 1605888\nv_mul_f32 v232, v98, v98\nM 12,164,184\nbuffer_load_dwordx4 v220, s[12:15], s63 offen lds\n"
"v_mul_f32 v233, v99, v99\nM 20,160,164\nR 180,191,0\nv_fma_f32 v230, v230, v215, v214\nv_fma_f32 v231, v231, v215, v214\nM 20,164,180\n"
"s_add_u32 s84, s73, 37750784\ns_add_u32 s85, s73, 37865472\nv_fma_f32 v232, v232, v215, v214\nv_fma_f32 v233, v233, v215, v214\nM 4,160,160\nR 182,191,4096\n"
"s_mov_b32 s60, 158720\nv_bfe_u32 v242, v252, 4, 4\nv_lshl_add_u32 v242, v242, 3, s60\nv_mul_f32 v230, v96, v230\nM 4,164,176\nds_read_b64 v[238:239], v242 offset:0\n"
"G 5\nv_mul_f32 v231, v97, v231\nM 0,168,192\nR 160,188,8192\nds_read_b64 v[240:241], v242 offset:128\nv_mul_f32 v232, v98, v232\n"
"M 0,172,208\nds_read_b64 v[242:243], v242 offset:0\nv_mul_f32 v233, v99, v233\nv_exp_f32 v230, v230\nM 16,168,196\nR 162,188,12288\n"
"v_fma_f32 v230, v230, v216, v216\nv_exp_f32 v231, v231\nM 16,172,212\nv_exp_f32 v232, v232\nv_exp_f32 v233, v233\nM 8,168,200\n"
"R 164,190,8192\nv_fma_f32 v231, v231, v216, v216\nv_fma_f32 v232, v232, v216, v216\nM 8,172,216\nv_rcp_f32 v230, v230\nv_fma_f32 v233, v233, v216, v216\n"
"M 24,168,204\nR 166,190,12288\nv_mul_f32 v230, v96, v230\nv_rcp_f32 v231, v231\nM 24,172,220\nG 7\n"
"v_rcp_f32 v232, v232\nv_rcp_f32 v233, v233\nM 28,176,204\nR 168,189,8192\nv_mul_f32 v231, v97, v231\nv_mul_f32 v232, v98, v232\n"
"M 28,180,220\nv_mul_f32 v233, v99, v233\nv_cvt_pk_bf16_f32 v96, v230, v231\nv_cvt_pk_bf16_f32 v97, v232, v233\nM 12,176,200\nR 170,189,12288\n"
"v_mul_f32 v234, v100, v100\nv_mul_f32 v235, v101, v101\nM 12,180,216\nv_mul_f32 v236, v102, v102\nv_mul_f32 v237, v103, v103\nv_fma_f32 v234, v234, v215, v214\n"
"M 20,176,196\nR 172,191,8192\nG 7\nv_mfma_f32_16x16x32_bf16 v[0:3], v[238:241], v[208:211], v[0:3]\nv_fma_f32 v235, v235, v215, v214\nv_fma_f32 v236, v236, v215, v214\n"
"M 20,180,212\nv_fma_f32 v237, v237, v215, v214\nv_mul_f32 v234, v100, v234\nv_mul_f32 v235, v101, v235\nM 4,176,192\nR 174,191,12288\n"
"G 7\nv_mfma_f32_16x16x32_bf16 v[4:7], v[240:243], v[208:211], v[4:7]\nv_mul_f32 v236, v102, v236\nv_mul_f32 v237, v103, v237\nM 4,180,208\nG 4\n"
"v_exp_f32 v234, v234\nv_exp_f32 v235, v235\nM 0,160,224\nR 176,188,16384\nv_mfma_f32_16x16x32_bf16 v[16:19], v[240:243], v[210:213], v[16:19]\nv_fma_f32 v234, v234, v216, v216\n"
"v_exp_f32 v236, v236\nM 0,164,240\nv_exp_f32 v237, v237\nv_fma_f32 v235, v235, v216, v216\nM 16,160,228\nR 178,188,20480\n"
"v_mfma_f32_16x16x32_bf16 v[20:23], v[238:241], v[210:213], v[20:23]\nv_fma_f32 v236, v236, v216, v216\nv_fma_f32 v237, v237, v216, v216\nM 16,164,244\nv_rcp_f32 v234, v234\nv_rcp_f32 v235, v235\n"
"M 8,160,232\nR 180,188,24576\nv_mul_f32 v234, v100, v234\nv_rcp_f32 v236, v236\nM 8,164,248\nv_rcp_f32 v237, v237\n"
"v_mul_f32 v235, v101, v235\nM 24,160,236\nR 182,188,28672\nv_mul_f32 v236, v102, v236\nv_mul_f32 v237, v103, v237\nM 24,164,252\n"
"G 4\nv_cvt_pk_bf16_f32 v98, v234, v235\nv_cvt_pk_bf16_f32 v99, v236, v237\nv_mul_f32 v230, v104, v104\nM 28,168,236\nR 160,189,16384\n"
"v_mul_f32 v231, v105, v105\nv_mul_f32 v232, v106, v106\nM 28,172,252\nv_mul_f32 v233, v107, v107\nv_fma_f32 v230, v230, v215, v214\nv_fma_f32 v231, v231, v215, v214\n"
"M 12,168,232\nR 162,189,20480\nv_fma_f32 v232, v232, v215, v214\nv_fma_f32 v233, v233, v215, v214\nM 12,172,248\nv_mul_f32 v230, v104, v230\n"
"v_mul_f32 v231, v105, v231\nv_mul_f32 v232, v106, v232\nM 20,168,228\nR 164,189,24576\nv_mul_f32 v233, v107, v233\nv_exp_f32 v230, v230\n"
"M 20,172,244\nv_exp_f32 v231, v231\nv_exp_f32 v232, v232\nM 4,168,224\nR 166,189,28672\nv_fma_f32 v230, v230, v216, v216\n"
"v_exp_f32 v233, v233\nM 4,172,240\ns_waitcnt vmcnt(12)\ns_barrier\nG 4\nv_fma_f32 v231, v231, v216, v216\n"
"v_mfma_f32_16x16x32_bf16 v[32:35], v[176:179], a[0:3], 0\nR 168,190,16384\nv_fma_f32 v232, v232, v216, v216\nv_fma_f32 v233, v233, v216, v216\nM 32,180,16\ns_mov_b32 m0, s51\n"
"v_rcp_f32 v230, v230\nv_rcp_f32 v231, v231\nv_mfma_f32_16x16x32_bf16 v[48:51], v[176:179], a[4:7], 0\nR 170,190,20480\nds_write_addtid_b32 v8 offset:0\nLpg27:\n"
"s_cbranch_execz Lpg31\nv_mul_f32 v230, v104, v230\nv_rcp_f32 v232, v232\nM 48,180,20\nds_write_addtid_b32 v9 offset:256\nv_rcp_f32 v233, v233\n"
"v_mul_f32 v231, v105, v231\nv_mfma_f32_16x16x32_bf16 v[64:67], v[176:179], a[8:11], 0\nR 172,190,24576\nds_write_addtid_b32 v10 offset:512\nv_mov_b32 v226, s46\nv_mul_f32 v232, v106, v232\n"
"M 64,180,24\nds_write_addtid_b32 v11 offset:768\nv_mov_b32 v227, s47\nv_mul_f32 v233, v107, v233\nv_cvt_pk_bf16_f32 v104, v230, v231\nv_mfma_f32_16x16x32_bf16 v[80:83], v[176:179], a[12:15], 0\n"
"R 174,190,28672\ns_add_u32 s61, s81, 845824\ns_lshl_b32 s62, s44, 5\ns_add_u32 s61, s61, s62\ns_waitcnt vmcnt(0)\ns_mov_b64 exec, 1\n"
"buffer_atomic_swap_x2 v[226:227], off, s[20:23], s61 sc1\ns_mov_b64 exec, -1\nds_write_addtid_b32 v12 offset:1024\nv_cvt_pk_bf16_f32 v105, v232, v233\nM 80,180,28\nds_write_addtid_b32 v13 offset:1280\n"
"G 10\nv_mul_f32 v234, v108, v108\nv_mul_f32 v235, v109, v109\nv_mul_f32 v236, v110, v110\nv_mfma_f32_16x16x32_bf16 v[84:87], v[160:163], a[12:15], 0\nR 176,191,16384\n"
"ds_write_addtid_b32 v14 offset:1536\nv_mul_f32 v237, v111, v111\nv_fma_f32 v234, v234, v215, v214\nM 84,164,28\nds_write_addtid_b32 v15 offset:1792\nv_fma_f32 v235, v235, v215, v214\n"
"v_fma_f32 v236, v236, v215, v214\nv_fma_f32 v237, v237, v215, v214\nv_mfma_f32_16x16x32_bf16 v[68:71], v[160:163], a[8:11], 0\nR 178,191,20480\nds_write_addtid_b32 v24 offset:4096\nv_mul_f32 v234, v108, v234\n"
"v_mul_f32 v235, v109, v235\nM 68,164,24\nbuffer_store_dwordx4 v[96:99], v252, s[20:23], s84 offen offset:0 sc1\nds_write_addtid_b32 v25 offset:4352\nv_mul_f32 v236, v110, v236\nv_mfma_f32_16x16x32_bf16 v[52:55], v[160:163], a[4:7], 0\n"
"R 180,191,24576\nds_write_addtid_b32 v26 offset:4608\nv_mul_f32 v237, v111, v237\nv_exp_f32 v234, v234\nM 52,164,20\nds_write_addtid_b32 v27 offset:4864\n"
"ds_write_addtid_b32 v28 offset:5120\nds_write_addtid_b32 v29 offset:5376\nds_write_addtid_b32 v30 offset:5632\nds_write_addtid_b32 v31 offset:5888\nv_exp_f32 v235, v235\nv_exp_f32 v236, v236\n"
"v_mfma_f32_16x16x32_bf16 v[36:39], v[160:163], a[0:3], 0\nR 182,191,28672\ns_add_u32 m0, s50, 16384\nv_exp_f32 v237, v237\nM 36,164,16\nG 15\n"
"v_fma_f32 v234, v234, v216, v216\nv_fma_f32 v235, v235, v216, v216\nv_fma_f32 v236, v236, v216, v216\nv_mfma_f32_16x16x32_bf16 v[40:43], v[168:171], a[0:3], 0\nR 160,188,32768\nG 2\n"
"s_barrier\nv_fma_f32 v237, v237, v216, v216\nM 40,172,16\nbuffer_load_dwordx4 v221, s[12:15], s63 offen offset:1024 lds\nds_read2st64_b32 v[8:9], v225 offset0:0 offset1:1\nv_rcp_f32 v234, v234\n"
"v_mfma_f32_16x16x32_bf16 v[56:59], v[168:171], a[4:7], 0\nR 162,188,36864\ns_add_u32 m0, s50, 24576\ns_add_u32 s63, s54, 1835264\nds_read2st64_b32 v[10:11], v225 offset0:2 offset1:3\nv_rcp_f32 v235, v235\n"
"M 56,172,20\nbuffer_load_dwordx4 v220, s[12:15], s63 offen lds\nds_read2st64_b32 v[12:13], v225 offset0:4 offset1:5\nv_rcp_f32 v236, v236\nv_mfma_f32_16x16x32_bf16 v[72:75], v[168:171], a[8:11], 0\nR 164,188,40960\n"
"ds_read2st64_b32 v[14:15], v225 offset0:6 offset1:7\nv_mul_f32 v234, v108, v234\nv_rcp_f32 v237, v237\nM 72,172,24\nds_read2st64_b32 v[24:25], v225 offset0:16 offset1:17\nv_mul_f32 v235, v109, v235\n"
"v_mul_f32 v236, v110, v236\nv_mul_f32 v237, v111, v237\nv_mfma_f32_16x16x32_bf16 v[88:91], v[168:171], a[12:15], 0\nR 166,188,45056\nds_read2st64_b32 v[26:27], v225 offset0:18 offset1:19\nv_cvt_pk_bf16_f32 v106, v234, v235\n"
"v_cvt_pk_bf16_f32 v107, v236, v237\nM 88,172,28\nds_read2st64_b32 v[28:29], v225 offset0:20 offset1:21\nG 11\nv_mul_f32 v230, v112, v112\nv_mul_f32 v231, v113, v113\n"
"v_mul_f32 v232, v114, v114\nv_mfma_f32_16x16x32_bf16 v[92:95], v[176:179], a[12:15], 0\nR 168,189,32768\nds_read2st64_b32 v[30:31], v225 offset0:22 offset1:23\nv_mul_f32 v233, v115, v115\nv_fma_f32 v230, v230, v218, v217\n"
"M 92,180,28\nbuffer_load_dwordx4 v221, s[12:15], s63 offen offset:1024 lds\nG 11\nv_add_f32 v0, v0, v8\nv_fma_f32 v231, v231, v218, v217\nv_mfma_f32_16x16x32_bf16 v[76:79], v[176:179], a[8:11], 0\n"
"R 170,189,36864\ns_add_u32 m0, s50, 32768\ns_add_u32 s63, s54, 2064640\nv_add_f32 v1, v1, v9\nv_fma_f32 v232, v232, v218, v217\nM 76,180,24\n"
"buffer_load_dwordx4 v220, s[12:15], s63 offen lds\nG 10\nv_add_f32 v2, v2, v10\nv_fma_f32 v233, v233, v218, v217\nv_mfma_f32_16x16x32_bf16 v[60:63], v[176:179], a[4:7], 0\nR 172,189,40960\n"
"v_mul_f32 v230, v112, v230\nv_add_f32 v3, v3, v11\nv_mul_f32 v231, v113, v231\nM 60,180,20\nbuffer_store_dwordx4 v[104:107], v252, s[20:23], s84 offen offset:1024 sc1\nG 10\n"
"v_add_f32 v4, v4, v12\nv_mul_f32 v232, v114, v232\nv_mfma_f32_16x16x32_bf16 v[44:47], v[176:179], a[0:3], 0\nR 174,189,45056\nv_mul_f32 v233, v115, v233\nv_add_f32 v5, v5, v13\n"
"v_exp_f32 v230, v230\nM 44,180,16\nG 7\nv_exp_f32 v231, v231\nv_add_f32 v6, v6, v14\nv_exp_f32 v232, v232\n"
"M 32,160,32\nR 176,190,32768\nv_mov_b32 v248, 0\nv_add_f32 v7, v7, v15\nv_exp_f32 v233, v233\nM 32,164,48\n"
"buffer_load_dwordx4 v221, s[12:15], s63 offen offset:1024 lds\nv_mov_b32 v249, 0\nv_add_f32 v16, v16, v24\nv_fma_f32 v230, v230, v219, v219\nM 48,160,36\nR 178,190,36864\n"
"s_add_u32 s61, s82, 845824\ns_mov_b64 exec, 0xf\nbuffer_atomic_or_x2 v[248:249], v250, s[20:23], s61 offen sc0 sc1\ns_mov_b64 exec, -1\nv_add_f32 v17, v17, v25\nv_fma_f32 v231, v231, v219, v219\n"
"M 48,164,52\nv_rcp_f32 v230, v230\nG 8\nv_add_f32 v18, v18, v26\nv_fma_f32 v232, v232, v219, v219\nM 64,160,40\n"
"R 180,190,40960\nv_fma_f32 v233, v233, v219, v219\nv_add_f32 v19, v19, v27\nG 8\nv_add_f32 v20, v20, v28\nv_add_f32 v21, v21, v29\n"
"G 6\nv_add_f32 v22, v22, v30\nv_add_f32 v23, v23, v31\nv_rcp_f32 v231, v231\nM 64,164,56\ns_add_u32 m0, s50, 40960\n"
"v_rcp_f32 v232, v232\nv_rcp_f32 v233, v233\nM 80,160,44\nR 182,190,45056\nv_mul_f32 v230, v112, v230\nv_mul_f32 v231, v113, v231\n"
"M 80,164,60\nG 4\nv_mul_f32 v232, v114, v232\nv_mul_f32 v233, v115, v233\nv_cvt_pk_bf16_f32 v112, v230, v231\nM 84,168,44\n"
"R 160,191,32768\nv_cvt_pk_bf16_f32 v113, v232, v233\nv_mul_f32 v234, v116, v116\nM 84,172,60\ns_add_u32 s63, s54, 2294016\nv_mul_f32 v235, v117, v117\n"
"v_mul_f32 v236, v118, v118\nM 68,168,40\nR 162,191,36864\nv_mul_f32 v237, v119, v119\nM 68,172,56\nbuffer_load_dwordx4 v220, s[12:15], s63 offen lds\n"
"M 52,168,36\nR 164,191,40960\nv_fma_f32 v234, v234, v218, v217\nM 52,172,52\nv_fma_f32 v235, v235, v218, v217\nv_fma_f32 v236, v236, v218, v217\n"
"M 36,168,32\nR 166,191,45056\nv_fma_f32 v237, v237, v218, v217\nM 36,172,48\nG 4\nv_mul_f32 v234, v116, v234\n"
"v_mul_f32 v235, v117, v235\nM 40,176,32\nR 168,188,49152\nv_mul_f32 v236, v118, v236\nM 40,180,48\nbuffer_load_dwordx4 v221, s[12:15], s63 offen offset:1024 lds\n"
"M 56,176,36\nR 170,188,53248\ns_add_u32 s63, s54, 2523392\nM 56,180,52\ns_add_u32 m0, s50, 49152\nv_exp_f32 v234, v234\n"
"M 72,176,40\nR 172,184,0\nv_mul_f32 v237, v119, v237\nM 72,180,56\nbuffer_load_dwordx4 v220, s[12:15], s63 offen lds\nM 88,176,44\n"
"R 174,184,4096\nv_fma_f32 v234, v234, v219, v219\nM 88,180,60\nG 4\nv_exp_f32 v235, v235\nM 92,160,44\n"
"R 176,189,49152\nv_fma_f32 v235, v235, v219, v219\nM 92,164,60\nbuffer_load_dwordx4 v221, s[12:15], s63 offen offset:1024 lds\nM 76,160,40\nR 178,189,53248\n"
"s_add_u32 s63, s54, 2752768\nM 76,164,56\ns_add_u32 m0, s50, 57344\nv_exp_f32 v236, v236\nM 60,160,36\nR 180,185,0\n"
"v_fma_f32 v236, v236, v219, v219\nM 60,164,52\nbuffer_load_dwordx4 v220, s[12:15], s63 offen lds\nM 44,160,32\nR 182,185,4096\nv_mul_f32 v230, v120, v120\n"
"M 44,164,48\nG 4\nv_exp_f32 v237, v237\nM 32,168,64\nR 160,190,49152\nv_fma_f32 v237, v237, v219, v219\n"
"M 32,172,80\nbuffer_load_dwordx4 v221, s[12:15], s63 offen offset:1024 lds\nM 48,168,68\nR 162,190,53248\ns_add_u32 s63, s54, 2982144\nM 48,172,84\n"
"s_add_u32 m0, s50, 65536\nv_rcp_f32 v234, v234\nM 64,168,72\nR 164,186,0\nv_mul_f32 v234, v116, v234\nM 64,172,88\n"
"buffer_load_dwordx4 v220, s[12:15], s63 offen lds\nM 80,168,76\nR 166,186,4096\nv_mul_f32 v231, v121, v121\nM 80,172,92\nG 4\n"
"v_rcp_f32 v235, v235\nM 84,176,76\nR 168,191,49152\nv_mul_f32 v235, v117, v235\nM 84,180,92\nv_rcp_f32 v236, v236\n"
"M 68,176,72\nR 170,191,53248\nv_mul_f32 v236, v118, v236\nM 68,180,88\nbuffer_load_dwordx4 v221, s[12:15], s63 offen offset:1024 lds\nM 52,176,68\n"
"R 172,187,0\nv_cvt_pk_bf16_f32 v114, v234, v235\nM 52,180,84\ns_add_u32 s64, s68, 37748736\ns_add_u32 s63, s54, 3211520\nv_mul_f32 v232, v122, v122\n"
"M 36,176,64\nR 174,187,4096\ns_add_u32 m0, s50, 73728\nM 36,180,80\nG 4\nv_rcp_f32 v237, v237\n"
"M 40,160,64\nR 176,184,8192\ns_waitcnt vmcnt(9)\ns_barrier\ns_waitcnt vmcnt(8)\nv_cmp_eq_u64 s[86:87], s[46:47], v[248:249]\n"
"M 40,164,80\ns_add_u32 s65, s68, 37863424\nv_mul_f32 v237, v119, v237\nv_cvt_pk_bf16_f32 v115, v236, v237\nM 56,160,68\nR 178,184,12288\n"
"s_and_b32 s62, s86, 0xf\ns_cmp_eq_u32 s62, 0xf\ns_cbranch_scc0 Lq852\nLq853:\nM 56,164,84\nbuffer_load_dwordx4 v220, s[12:15], s63 offen lds\n"
"M 72,160,72\nR 180,184,16384\nv_mul_f32 v233, v123, v123\nM 72,164,88\nbuffer_load_dwordx4 a[0:3], v252, s[20:23], s64 offen sc1\nM 88,160,76\n"
"R 182,184,20480\nv_fma_f32 v230, v230, v218, v217\nM 88,164,92\nG 4\nv_fma_f32 v231, v231, v218, v217\nv_fma_f32 v232, v232, v218, v217\n"
"M 92,168,76\nR 160,185,8192\nv_fma_f32 v233, v233, v218, v217\nM 92,172,92\nbuffer_load_dwordx4 a[4:7], v252, s[20:23], s65 offen sc1\nM 76,168,72\n"
"R 162,185,12288\nv_mul_f32 v230, v120, v230\nM 76,172,88\ns_add_u32 s66, s69, 37748736\nv_exp_f32 v230, v230\nM 60,168,68\n"
"R 164,185,16384\nv_mul_f32 v231, v121, v231\nM 60,172,84\ns_add_u32 s67, s69, 37863424\nv_exp_f32 v231, v231\nM 44,168,64\n"
"R 166,185,20480\nv_mul_f32 v232, v122, v232\nM 44,172,80\nG 4\nv_exp_f32 v232, v232\nM 32,176,96\n"
"R 168,186,8192\nv_mul_f32 v233, v123, v233\nM 32,180,112\nbuffer_load_dwordx4 a[8:11], v252, s[20:23], s66 offen sc1\nM 48,176,100\nR 170,186,12288\n"
"v_fma_f32 v230, v230, v219, v219\nM 48,180,116\nbuffer_load_dwordx4 a[12:15], v252, s[20:23], s67 offen sc1\nM 64,176,104\nR 172,186,16384\nv_fma_f32 v231, v231, v219, v219\n"
"M 64,180,120\ns_add_u32 s64, s68, 37755904\nv_exp_f32 v233, v233\nM 80,176,108\nR 174,186,20480\nv_fma_f32 v232, v232, v219, v219\n"
"M 80,180,124\nG 4\nv_rcp_f32 v230, v230\nM 84,160,108\nR 176,187,8192\nv_fma_f32 v233, v233, v219, v219\n"
"M 84,164,124\nbuffer_load_dwordx4 a[16:19], v252, s[20:23], s64 offen sc1\nM 68,160,104\nR 178,187,12288\nv_mul_f32 v230, v120, v230\nM 68,164,120\n"
"s_add_u32 s65, s68, 37870592\nv_rcp_f32 v231, v231\nM 52,160,100\nR 180,187,16384\nv_mul_f32 v231, v121, v231\nM 52,164,116\n"
"s_add_u32 s66, s69, 37755904\nv_rcp_f32 v232, v232\nM 36,160,96\nR 182,187,20480\nv_mul_f32 v232, v122, v232\nM 36,164,112\n"
"G 4\nv_rcp_f32 v233, v233\nM 40,168,96\nR 160,184,24576\nv_mul_f32 v233, v123, v233\nM 40,172,112\n"
"buffer_load_dwordx4 a[20:23], v252, s[20:23], s65 offen sc1\nM 56,168,100\nR 162,184,28672\nv_cvt_pk_bf16_f32 v120, v230, v231\nM 56,172,116\nbuffer_load_dwordx4 a[24:27], v252, s[20:23], s66 offen sc1\n"
"M 72,168,104\nR 164,184,32768\nv_cvt_pk_bf16_f32 v121, v232, v233\nM 72,172,120\ns_add_u32 s67, s69, 37870592\nv_mul_f32 v234, v124, v124\n"
"v_mul_f32 v235, v125, v125\nM 88,168,108\nR 166,184,36864\nv_mul_f32 v236, v126, v126\nM 88,172,124\nG 4\n"
"v_mul_f32 v237, v127, v127\nv_fma_f32 v234, v234, v218, v217\nM 92,176,108\nR 168,185,24576\nv_fma_f32 v235, v235, v218, v217\nM 92,180,124\n"
"buffer_load_dwordx4 a[28:31], v252, s[20:23], s67 offen sc1\nM 76,176,104\nR 170,185,28672\nv_fma_f32 v236, v236, v218, v217\nM 76,180,120\ns_add_u32 s64, s68, 37749760\n"
"v_fma_f32 v237, v237, v218, v217\nv_mul_f32 v234, v124, v234\nM 60,176,100\nR 172,185,32768\nv_mul_f32 v235, v125, v235\nM 60,180,116\n"
"s_add_u32 s65, s68, 37864448\nv_exp_f32 v234, v234\nM 44,176,96\nR 174,185,36864\nv_mul_f32 v236, v126, v236\nM 44,180,112\n"
"G 4\nv_exp_f32 v235, v235\nM 32,160,128\nR 176,186,24576\nv_mul_f32 v237, v127, v237\nM 32,164,144\n"
"buffer_load_dwordx4 a[32:35], v252, s[20:23], s64 offen sc1\nbuffer_load_dwordx4 v221, s[12:15], s63 offen offset:1024 lds\nM 48,160,132\nR 178,186,28672\ns_add_u32 m0, s50, 81920\nM 48,164,148\n"
"buffer_load_dwordx4 a[36:39], v252, s[20:23], s65 offen sc1\nM 64,160,136\nR 180,186,32768\ns_add_u32 s63, s54, 3440896\nM 64,164,152\ns_add_u32 s66, s69, 37749760\n"
"buffer_load_dwordx4 v220, s[12:15], s63 offen lds\nM 80,160,140\nR 182,186,36864\nv_fma_f32 v234, v234, v219, v219\nM 80,164,156\nG 4\n"
"v_exp_f32 v236, v236\nM 84,168,140\nR 160,187,24576\nv_fma_f32 v235, v235, v219, v219\nM 84,172,156\nbuffer_load_dwordx4 a[40:43], v252, s[20:23], s66 offen sc1\n"
"buffer_load_dwordx4 v221, s[12:15], s63 offen offset:1024 lds\nM 68,168,136\nR 162,187,28672\nv_fma_f32 v236, v236, v219, v219\nM 68,172,152\ns_add_u32 s67, s69, 37864448\n"
"v_exp_f32 v237, v237\nM 52,168,132\nR 164,187,32768\ns_waitcnt vmcnt(15)\ns_barrier\nM 52,172,148\n"
"buffer_load_dwordx4 a[44:47], v252, s[20:23], s67 offen sc1\ns_movk_i32 s63, 2048\nv_mad_u32_u24 v220, v228, s63, v229\nv_add_u32 v221, 916480, v220\ns_add_u32 m0, s50, 90112\ns_add_u32 s63, s56, 0\n"
"buffer_load_dwordx4 v220, s[16:19], s63 offen lds\nM 36,168,128\nR 166,187,36864\nv_fma_f32 v237, v237, v219, v219\nM 36,172,144\nG 4\n"
"v_rcp_f32 v234, v234\nM 40,176,128\nR 168,184,40960\nv_mul_f32 v234, v124, v234\nM 40,180,144\ns_add_u32 s64, s68, 37756928\n"
"buffer_load_dwordx4 v221, s[16:19], s63 offen offset:1024 lds\nM 56,176,132\nR 170,184,45056\ns_add_u32 m0, s50, 98304\nM 56,180,148\nbuffer_load_dwordx4 a[48:51], v252, s[20:23], s64 offen sc1\n"
"M 72,176,136\nR 172,184,49152\ns_add_u32 s63, s56, 524288\nM 72,180,152\ns_add_u32 s65, s68, 37871616\nbuffer_load_dwordx4 v220, s[16:19], s63 offen lds\n"
"M 88,176,140\nR 174,184,53248\nv_rcp_f32 v235, v235\nM 88,180,156\nG 4\nv_rcp_f32 v236, v236\n"
"M 92,160,140\nR 176,185,40960\nv_mul_f32 v235, v125, v235\nM 92,164,156\nbuffer_load_dwordx4 a[52:55], v252, s[20:23], s65 offen sc1\nbuffer_load_dwordx4 v221, s[16:19], s63 offen offset:1024 lds\n"
"M 76,160,136\nR 178,185,45056\ns_add_u32 m0, s50, 106496\nM 76,164,152\ns_add_u32 s66, s69, 37756928\nv_rcp_f32 v237, v237\n"
"M 60,160,132\nR 180,185,49152\ns_add_u32 s63, s56, 65536\nM 60,164,148\nbuffer_load_dwordx4 a[56:59], v252, s[20:23], s66 offen sc1\nM 44,160,128\n"
"R 182,185,53248\nv_mul_f32 v236, v126, v236\nM 44,164,144\nbuffer_load_dwordx4 v220, s[16:19], s63 offen lds\nG 4\nM 32,168,160\n"
"R 160,186,40960\nv_mul_f32 v237, v127, v237\nM 32,172,176\ns_add_u32 s67, s69, 37871616\nv_cvt_pk_bf16_f32 v122, v234, v235\nv_cvt_pk_bf16_f32 v123, v236, v237\n"
"M 48,168,164\nR 162,186,45056\nM 48,172,180\nbuffer_load_dwordx4 a[60:63], v252, s[20:23], s67 offen sc1\nbuffer_load_dwordx4 v221, s[16:19], s63 offen offset:1024 lds\nM 64,168,168\n"
"R 164,186,49152\ns_add_u32 m0, s50, 114688\ns_add_u32 s63, s56, 589824\nM 64,172,184\nbuffer_load_dwordx4 v220, s[16:19], s63 offen lds\nM 80,168,172\n"
"R 166,186,53248\nbuffer_store_dwordx4 v[112:115], v252, s[20:23], s85 offen offset:0 sc1\nM 80,172,188\nbuffer_store_dwordx4 v[120:123], v252, s[20:23], s85 offen offset:1024 sc1\nG 4\nM 84,176,172\n"
"R 168,187,40960\nM 84,180,188\nbuffer_load_dwordx4 v221, s[16:19], s63 offen offset:1024 lds\nM 68,176,168\nR 170,187,45056\ns_add_u32 s63, s57, 0\n"
"v_mul_f32 v230, v0, v0\nM 68,180,184\ns_add_u32 m0, s50, 0\ns_add_u32 s84, s73, 37754880\ns_add_u32 s85, s73, 37869568\nv_mul_f32 v231, v1, v1\n"
"M 52,176,164\nLpg28:\ns_cbranch_execz Lpg32\nR 172,187,49152\nv_mul_f32 v232, v2, v2\nv_mul_f32 v233, v3, v3\n"
"M 52,180,180\nbuffer_load_dwordx4 v220, s[16:19], s63 offen lds\nv_fma_f32 v230, v230, v215, v214\nM 36,176,160\nR 174,187,53248\nv_fma_f32 v231, v231, v215, v214\n"
"v_fma_f32 v232, v232, v215, v214\nM 36,180,176\nG 4\nv_fma_f32 v233, v233, v215, v214\nv_mul_f32 v230, v0, v230\nv_mul_f32 v231, v1, v231\n"
"M 40,160,160\nR 176,184,57344\nv_mul_f32 v232, v2, v232\nv_mul_f32 v233, v3, v233\nM 40,164,176\nv_exp_f32 v230, v230\n"
"v_exp_f32 v231, v231\nM 56,160,164\nR 178,184,61440\nv_fma_f32 v230, v230, v216, v216\nv_exp_f32 v232, v232\nM 56,164,180\n"
"v_exp_f32 v233, v233\nv_fma_f32 v231, v231, v216, v216\nM 72,160,168\nR 180,188,0\nv_fma_f32 v232, v232, v216, v216\nv_fma_f32 v233, v233, v216, v216\n"
"M 72,164,184\nv_rcp_f32 v230, v230\nv_rcp_f32 v231, v231\nM 88,160,172\nR 182,188,4096\nv_mul_f32 v230, v0, v230\n"
"v_rcp_f32 v232, v232\nM 88,164,188\nG 4\nv_rcp_f32 v233, v233\nv_mul_f32 v231, v1, v231\nM 92,168,172\n"
"R 160,185,57344\nv_mul_f32 v232, v2, v232\nv_mul_f32 v233, v3, v233\nM 92,172,188\nv_cvt_pk_bf16_f32 v0, v230, v231\nv_cvt_pk_bf16_f32 v1, v232, v233\n"
"v_mul_f32 v234, v4, v4\nM 76,168,168\nR 162,185,61440\nv_mul_f32 v235, v5, v5\nv_mul_f32 v236, v6, v6\nM 76,172,184\n"
"v_mul_f32 v237, v7, v7\nv_fma_f32 v234, v234, v215, v214\nv_fma_f32 v235, v235, v215, v214\nM 60,168,164\nR 164,189,0\nv_fma_f32 v236, v236, v215, v214\n"
"v_fma_f32 v237, v237, v215, v214\nM 60,172,180\nv_mul_f32 v234, v4, v234\nv_mul_f32 v235, v5, v235\nv_mul_f32 v236, v6, v236\nM 44,168,160\n"
"R 166,189,4096\nv_mul_f32 v237, v7, v237\nv_exp_f32 v234, v234\nM 44,172,176\nG 4\nv_exp_f32 v235, v235\n"
"v_exp_f32 v236, v236\nM 32,176,192\nR 168,186,57344\nv_fma_f32 v234, v234, v216, v216\nv_exp_f32 v237, v237\nM 32,180,208\n"
"v_rcp_f32 v234, v234\nv_fma_f32 v235, v235, v216, v216\nM 48,176,196\nR 170,186,61440\nv_fma_f32 v236, v236, v216, v216\nv_fma_f32 v237, v237, v216, v216\n"
"M 48,180,212\nv_rcp_f32 v235, v235\nv_rcp_f32 v236, v236\nM 64,176,200\nR 172,190,0\nv_mul_f32 v234, v4, v234\n"
"v_rcp_f32 v237, v237\nM 64,180,216\nv_mul_f32 v235, v5, v235\nv_mul_f32 v236, v6, v236\nv_mul_f32 v237, v7, v237\nM 80,176,204\n"
"R 174,190,4096\nv_cvt_pk_bf16_f32 v2, v234, v235\nv_cvt_pk_bf16_f32 v3, v236, v237\nM 80,180,220\nG 4\nv_mul_f32 v230, v16, v16\n"
"v_mul_f32 v231, v17, v17\nv_mul_f32 v232, v18, v18\nM 84,160,204\nR 176,187,57344\ns_waitcnt vmcnt(9)\ns_barrier\n"
"v_mul_f32 v233, v19, v19\nM 84,164,220\nv_fma_f32 v230, v230, v218, v217\nv_fma_f32 v231, v231, v218, v217\nv_fma_f32 v232, v232, v218, v217\nM 68,160,200\n"
"R 178,187,61440\nv_fma_f32 v233, v233, v218, v217\nv_mul_f32 v230, v16, v230\nM 68,164,216\nv_exp_f32 v230, v230\nv_mul_f32 v231, v17, v231\n"
"M 52,160,196\nR 180,191,0\nv_mul_f32 v232, v18, v232\nv_mul_f32 v233, v19, v233\nM 52,164,212\nv_exp_f32 v231, v231\n"
"v_exp_f32 v232, v232\nM 36,160,192\nR 182,191,4096\nv_fma_f32 v230, v230, v219, v219\nv_exp_f32 v233, v233\nM 36,164,208\n"
"G 4\nv_rcp_f32 v230, v230\nv_fma_f32 v231, v231, v219, v219\nM 40,168,192\nR 160,188,8192\nv_fma_f32 v232, v232, v219, v219\n"
"v_fma_f32 v233, v233, v219, v219\nM 40,172,208\nv_rcp_f32 v231, v231\nv_rcp_f32 v232, v232\nM 56,168,196\nR 162,188,12288\n"
"v_mul_f32 v230, v16, v230\nv_rcp_f32 v233, v233\nM 56,172,212\nv_mul_f32 v231, v17, v231\nv_mul_f32 v232, v18, v232\nv_mul_f32 v233, v19, v233\n"
"M 72,168,200\nR 164,188,16384\nv_cvt_pk_bf16_f32 v16, v230, v231\nv_cvt_pk_bf16_f32 v17, v232, v233\nM 72,172,216\nv_mul_f32 v234, v20, v20\n"
"v_mul_f32 v235, v21, v21\nv_mul_f32 v236, v22, v22\nM 88,168,204\nR 166,188,20480\nv_mov_b32 v226, s46\nv_mul_f32 v237, v23, v23\n"
"M 88,172,220\nG 4\nv_mov_b32 v227, s47\nv_fma_f32 v234, v234, v218, v217\nv_fma_f32 v235, v235, v218, v217\nM 92,176,204\n"
"R 168,189,8192\ns_add_u32 s61, s81, 845832\ns_lshl_b32 s62, s44, 5\ns_add_u32 s61, s61, s62\ns_waitcnt vmcnt(0)\ns_mov_b64 exec, 1\n"
"buffer_atomic_swap_x2 v[226:227], off, s[20:23], s61 sc1\ns_mov_b64 exec, -1\nv_fma_f32 v236, v236, v218, v217\nM 92,180,220\nbuffer_load_dwordx4 v221, s[16:19], s63 offen offset:1024 lds\nv_fma_f32 v237, v237, v218, v217\n"
"M 76,176,200\nR 170,189,12288\ns_add_u32 m0, s50, 8192\ns_add_u32 s63, s57, 524288\nv_mul_f32 v234, v20, v234\nM 76,180,216\n"
"buffer_load_dwordx4 v220, s[16:19], s63 offen lds\nv_mul_f32 v235, v21, v235\nM 60,176,196\nR 172,189,16384\nv_mul_f32 v236, v22, v236\nv_mul_f32 v237, v23, v237\n"
"M 60,180,212\nv_exp_f32 v234, v234\nv_exp_f32 v235, v235\nM 44,176,192\nR 174,189,20480\ns_mov_b32 s60, 158208\n"
"v_bfe_u32 v246, v252, 4, 4\nv_lshl_add_u32 v246, v246, 3, s60\nv_exp_f32 v236, v236\nM 44,180,208\nds_read_b64 v[238:239], v246 offset:0\nG 5\n"
"v_exp_f32 v237, v237\nM 32,160,224\nR 176,190,8192\nds_read_b64 v[240:241], v246 offset:128\nv_fma_f32 v234, v234, v219, v219\nM 32,164,240\n"
"ds_read_b64 v[242:243], v246 offset:256\nbuffer_load_dwordx4 v221, s[16:19], s63 offen offset:1024 lds\nv_fma_f32 v235, v235, v219, v219\nM 48,160,228\nR 178,190,12288\nds_read_b64 v[244:245], v246 offset:384\n"
"v_fma_f32 v236, v236, v219, v219\nM 48,164,244\nds_read_b64 v[246:247], v246 offset:0\ns_add_u32 m0, s50, 16384\nv_fma_f32 v237, v237, v219, v219\nM 64,160,232\n"
"R 180,190,16384\ns_add_u32 s63, s57, 65536\nv_rcp_f32 v234, v234\nM 64,164,248\nbuffer_load_dwordx4 v220, s[16:19], s63 offen lds\nv_rcp_f32 v235, v235\n"
"M 80,160,236\nR 182,190,20480\nv_mul_f32 v234, v20, v234\nv_rcp_f32 v236, v236\nM 80,164,252\nG 9\n"
"v_rcp_f32 v237, v237\nv_mul_f32 v235, v21, v235\nM 84,168,236\nR 160,191,8192\nv_mul_f32 v236, v22, v236\nM 84,172,252\n"
"buffer_load_dwordx4 v221, s[16:19], s63 offen offset:1024 lds\nM 68,168,232\nR 162,191,12288\ns_add_u32 m0, s50, 24576\ns_add_u32 s63, s57, 589824\nM 68,172,248\n"
"buffer_load_dwordx4 v220, s[16:19], s63 offen lds\nM 52,168,228\nR 164,191,16384\nG 9\nv_mfma_f32_16x16x32_bf16 v[32:35], v[238:241], v[208:211], v[32:35]\nv_mul_f32 v237, v23, v237\n"
"M 52,172,244\nbuffer_store_dwordx4 v[0:3], v252, s[20:23], s84 offen offset:0 sc1\nM 36,168,224\nR 166,191,20480\nG 9\nv_mfma_f32_16x16x32_bf16 v[36:39], v[240:243], v[208:211], v[36:39]\n"
"v_cvt_pk_bf16_f32 v18, v234, v235\nM 36,172,240\nG 4\nv_cvt_pk_bf16_f32 v19, v236, v237\nM 40,176,224\nR 168,188,24576\n"
"v_mfma_f32_16x16x32_bf16 v[40:43], v[242:245], v[208:211], v[40:43]\nbuffer_load_dwordx4 v221, s[16:19], s63 offen offset:1024 lds\nbuffer_store_dwordx4 v[16:19], v252, s[20:23], s85 offen offset:0 sc1\nM 40,180,240\ns_add_u32 m0, s50, 32768\ns_add_u32 s63, s56, 131072\n"
"buffer_load_dwordx4 v220, s[16:19], s63 offen lds\nM 56,176,228\nR 170,188,28672\nv_mfma_f32_16x16x32_bf16 v[44:47], v[244:247], v[208:211], v[44:47]\nv_mov_b32 v248, 0\nM 56,180,244\n"
"v_mov_b32 v249, 0\nM 72,176,232\nR 172,188,32768\nv_mfma_f32_16x16x32_bf16 v[48:51], v[244:247], v[210:213], v[48:51]\ns_add_u32 s61, s82, 845832\ns_mov_b64 exec, 0xf\n"
"buffer_atomic_or_x2 v[248:249], v250, s[20:23], s61 offen sc0 sc1\ns_mov_b64 exec, -1\nM 72,180,248\nbuffer_load_dwordx4 v221, s[16:19], s63 offen offset:1024 lds\nM 88,176,236\nR 174,188,36864\n"
"v_mfma_f32_16x16x32_bf16 v[52:55], v[238:241], v[210:213], v[52:55]\ns_add_u32 m0, s50, 40960\nM 88,180,252\nG 4\nM 92,160,236\nR 176,189,24576\n"
"v_mfma_f32_16x16x32_bf16 v[56:59], v[240:243], v[210:213], v[56:59]\ns_add_u32 s63, s56, 655360\nM 92,164,252\nbuffer_load_dwordx4 v220, s[16:19], s63 offen lds\nM 76,160,232\nR 178,189,28672\n"
"v_mfma_f32_16x16x32_bf16 v[60:63], v[242:245], v[210:213], v[60:63]\nM 76,164,248\nM 60,160,228\nR 180,189,32768\nM 60,164,244\nM 44,160,224\n"
"R 182,189,36864\nM 44,164,240\ns_barrier\nG 4\nv_mfma_f32_16x16x32_bf16 v[96:99], v[168:171], a[0:3], 0\nR 160,190,24576\n"
"M 96,172,16\ns_mov_b32 m0, s51\nv_mfma_f32_16x16x32_bf16 v[112:115], v[168:171], a[4:7], 0\nR 162,190,28672\nM 112,172,20\nds_write_addtid_b32 v64 offset:0\n"
"v_mfma_f32_16x16x32_bf16 v[128:131], v[168:171], a[8:11], 0\nR 164,190,32768\nM 128,172,24\nds_write_addtid_b32 v65 offset:256\nv_mfma_f32_16x16x32_bf16 v[144:147], v[168:171], a[12:15], 0\nR 166,190,36864\n"
"ds_write_addtid_b32 v66 offset:512\nM 144,172,28\nds_write_addtid_b32 v67 offset:768\nds_write_addtid_b32 v68 offset:1024\nG 9\nv_mfma_f32_16x16x32_bf16 v[148:151], v[176:179], a[12:15], 0\n"
"R 168,191,24576\nds_write_addtid_b32 v69 offset:1280\nM 148,180,28\nds_write_addtid_b32 v70 offset:1536\nds_write_addtid_b32 v71 offset:1792\nv_mfma_f32_16x16x32_bf16 v[132:135], v[176:179], a[8:11], 0\n"
"R 170,191,28672\nds_write_addtid_b32 v72 offset:2048\nM 132,180,24\nds_write_addtid_b32 v73 offset:2304\nds_write_addtid_b32 v74 offset:2560\nv_mfma_f32_16x16x32_bf16 v[116:119], v[176:179], a[4:7], 0\n"
"R 172,191,32768\nds_write_addtid_b32 v75 offset:2816\nM 116,180,20\nds_write_addtid_b32 v76 offset:3072\nds_write_addtid_b32 v77 offset:3328\nv_mfma_f32_16x16x32_bf16 v[100:103], v[176:179], a[0:3], 0\n"
"R 174,191,36864\nds_write_addtid_b32 v78 offset:3584\nM 100,180,16\nds_write_addtid_b32 v79 offset:3840\nds_write_addtid_b32 v80 offset:4096\nG 15\n"
"v_mfma_f32_16x16x32_bf16 v[104:107], v[160:163], a[0:3], 0\nR 176,188,40960\nds_write_addtid_b32 v81 offset:4352\nM 104,164,16\nds_write_addtid_b32 v82 offset:4608\nds_write_addtid_b32 v83 offset:4864\n"
"v_mfma_f32_16x16x32_bf16 v[120:123], v[160:163], a[4:7], 0\nR 178,188,45056\nds_write_addtid_b32 v84 offset:5120\nM 120,164,20\nds_write_addtid_b32 v85 offset:5376\nds_write_addtid_b32 v86 offset:5632\n"
"v_mfma_f32_16x16x32_bf16 v[136:139], v[160:163], a[8:11], 0\nR 180,188,49152\nds_write_addtid_b32 v87 offset:5888\nM 136,164,24\nds_write_addtid_b32 v88 offset:6144\nds_write_addtid_b32 v89 offset:6400\n"
"v_mfma_f32_16x16x32_bf16 v[152:155], v[160:163], a[12:15], 0\nR 182,188,53248\nds_write_addtid_b32 v90 offset:6656\nM 152,164,28\nds_write_addtid_b32 v91 offset:6912\nds_write_addtid_b32 v92 offset:7168\n"
"G 15\nv_mfma_f32_16x16x32_bf16 v[156:159], v[168:171], a[12:15], 0\nR 160,189,40960\nds_write_addtid_b32 v93 offset:7424\nM 156,172,28\nds_write_addtid_b32 v94 offset:7680\n"
"ds_write_addtid_b32 v95 offset:7936\nv_mfma_f32_16x16x32_bf16 v[140:143], v[168:171], a[8:11], 0\nR 162,189,45056\nM 140,172,24\nv_mfma_f32_16x16x32_bf16 v[124:127], v[168:171], a[4:7], 0\nR 164,189,49152\n"
"s_waitcnt vmcnt(5) lgkmcnt(2)\ns_barrier\nM 124,172,20\nds_read2st64_b32 v[64:65], v225 offset0:0 offset1:1\nds_read2st64_b32 v[66:67], v225 offset0:2 offset1:3\nv_mfma_f32_16x16x32_bf16 v[108:111], v[168:171], a[0:3], 0\n"
"R 166,189,53248\nds_read2st64_b32 v[68:69], v225 offset0:4 offset1:5\nM 108,172,16\nds_read2st64_b32 v[70:71], v225 offset0:6 offset1:7\nds_read2st64_b32 v[72:73], v225 offset0:8 offset1:9\nM 96,176,32\n"
"R 168,190,40960\nds_read2st64_b32 v[74:75], v225 offset0:10 offset1:11\nM 96,180,48\ns_add_u32 s64, s68, 37750784\nds_read2st64_b32 v[76:77], v225 offset0:12 offset1:13\nv_mov_b32 v226, s46\n"
"M 112,176,36\nR 170,190,45056\nds_read2st64_b32 v[78:79], v225 offset0:14 offset1:15\nv_mov_b32 v227, s47\ns_waitcnt vmcnt(2)\nv_cmp_eq_u64 s[86:87], s[46:47], v[248:249]\n"
"M 112,180,52\ns_add_u32 s61, s81, 845848\ns_lshl_b32 s62, s44, 5\ns_add_u32 s61, s61, s62\ns_waitcnt vmcnt(0)\ns_mov_b64 exec, 1\n"
"buffer_atomic_swap_x2 v[226:227], off, s[20:23], s61 sc1\ns_mov_b64 exec, -1\nds_read2st64_b32 v[80:81], v225 offset0:16 offset1:17\nM 128,176,40\nR 172,190,49152\ns_and_b32 s62, s86, 0xf\n"
"s_cmp_eq_u32 s62, 0xf\ns_cbranch_scc0 Lq887\nLq888:\nds_read2st64_b32 v[82:83], v225 offset0:18 offset1:19\nM 128,180,56\ns_add_u32 s65, s68, 37865472\n"
"ds_read2st64_b32 v[84:85], v225 offset0:20 offset1:21\nds_read2st64_b32 v[86:87], v225 offset0:22 offset1:23\nM 144,176,44\nR 174,190,53248\ns_add_u32 m0, s50, 40960\nds_read2st64_b32 v[88:89], v225 offset0:24 offset1:25\n"
"M 144,180,60\nds_read2st64_b32 v[90:91], v225 offset0:26 offset1:27\nds_read2st64_b32 v[92:93], v225 offset0:28 offset1:29\nG 15\nM 148,160,44\nR 176,191,40960\n"
"buffer_load_dwordx4 v221, s[16:19], s63 offen offset:1024 lds\nds_read2st64_b32 v[94:95], v225 offset0:30 offset1:31\nM 148,164,60\nbuffer_load_dwordx4 a[64:67], v252, s[20:23], s64 offen sc1\nv_add_f32 v32, v32, v64\nM 132,160,40\n"
"R 178,191,45056\ns_add_u32 m0, s50, 49152\nv_add_f32 v33, v33, v65\nM 132,164,56\ns_add_u32 s66, s69, 37750784\ns_add_u32 s63, s56, 196608\n"
"v_add_f32 v34, v34, v66\nv_add_f32 v35, v35, v67\nM 116,160,36\nR 180,191,49152\nbuffer_load_dwordx4 v220, s[16:19], s63 offen lds\nv_add_f32 v36, v36, v68\n"
"M 116,164,52\nbuffer_load_dwordx4 a[68:71], v252, s[20:23], s65 offen sc1\nv_add_f32 v37, v37, v69\nM 100,160,32\nR 182,191,53248\nv_add_f32 v38, v38, v70\n"
"v_add_f32 v39, v39, v71\nM 100,164,48\nG 8\nv_add_f32 v40, v40, v72\nv_add_f32 v41, v41, v73\nv_add_f32 v42, v42, v74\n"
"M 104,168,32\nR 160,184,0\nbuffer_load_dwordx4 v221, s[16:19], s63 offen offset:1024 lds\nv_add_f32 v43, v43, v75\nM 104,172,48\ns_add_u32 s67, s69, 37865472\n"
"v_add_f32 v44, v44, v76\nv_add_f32 v45, v45, v77\nv_add_f32 v46, v46, v78\nM 120,168,36\nR 162,184,4096\nv_add_f32 v47, v47, v79\n"
"v_add_f32 v48, v48, v80\nM 120,172,52\nbuffer_load_dwordx4 a[72:75], v252, s[20:23], s66 offen sc1\nv_add_f32 v49, v49, v81\nM 136,168,40\nR 164,184,8192\n"
"v_add_f32 v50, v50, v82\nv_add_f32 v51, v51, v83\nM 136,172,56\nv_add_f32 v52, v52, v84\nv_add_f32 v53, v53, v85\nM 152,168,44\n"
"R 166,184,12288\nv_add_f32 v54, v54, v86\nM 152,172,60\nG 4\nv_add_f32 v55, v55, v87\nv_add_f32 v56, v56, v88\n"
"M 156,176,44\nR 168,185,0\nv_add_f32 v57, v57, v89\nM 156,180,60\nbuffer_load_dwordx4 a[76:79], v252, s[20:23], s67 offen sc1\nM 140,176,40\n"
"R 170,185,4096\nv_add_f32 v58, v58, v90\nM 140,180,56\ns_add_u32 s64, s68, 37757952\nv_add_f32 v59, v59, v91\nv_add_f32 v60, v60, v92\n"
"M 124,176,36\nR 172,185,8192\nv_add_f32 v61, v61, v93\nM 124,180,52\ns_add_u32 s65, s68, 37872640\nv_add_f32 v62, v62, v94\n"
"v_add_f32 v63, v63, v95\nM 108,176,32\nR 174,185,12288\nv_mul_f32 v230, v32, v32\nv_mul_f32 v231, v33, v33\nM 108,180,48\n"
"s_add_u32 s84, s73, 37752832\ns_add_u32 s85, s73, 37867520\nG 4\nv_mul_f32 v232, v34, v34\nv_mfma_f32_16x16x32_bf16 v[0:3], v[160:163], a[0:3], 0\nR 176,186,0\n"
"s_lshr_b32 s60, s45, 3\ns_lshl2_add_u32 s60, s60, s40\ns_lshl_b32 s60, s60, 8\nv_lshl_add_u32 v224, v252, 3, s60\nv_add_u32 v224, LqF-32768-LqPC, v224\nv_min_u32 v224, LqE-LqPC-4, v224\n"
"s_mov_b64 exec, 3\nglobal_load_dword v224, v224, s[98:99]\ns_mov_b64 exec, -1\nv_mul_f32 v233, v35, v35\nM 0,164,16\nbuffer_load_dwordx4 a[80:83], v252, s[20:23], s64 offen sc1\n"
"v_fma_f32 v230, v230, v215, v214\nv_mfma_f32_16x16x32_bf16 v[16:19], v[160:163], a[4:7], 0\nR 178,186,4096\nv_fma_f32 v231, v231, v215, v214\nv_fma_f32 v232, v232, v215, v214\nM 16,164,20\n"
"s_add_u32 s66, s69, 37757952\nv_fma_f32 v233, v233, v215, v214\nv_mul_f32 v230, v32, v230\nv_mul_f32 v231, v33, v231\nv_mfma_f32_16x16x32_bf16 a[224:227], v[160:163], a[8:11], 0\nR 180,186,8192\n"
"v_mul_f32 v232, v34, v232\nv_mul_f32 v233, v35, v233\nv_mfma_f32_16x16x32_bf16 a[224:227], v[164:167], a[24:27], a[224:227]\nbuffer_load_dwordx4 a[84:87], v252, s[20:23], s65 offen sc1\nv_exp_f32 v230, v230\nv_mfma_f32_16x16x32_bf16 a[240:243], v[160:163], a[12:15], 0\n"
"R 182,186,12288\nv_fma_f32 v230, v230, v216, v216\nv_exp_f32 v231, v231\nv_mfma_f32_16x16x32_bf16 a[240:243], v[164:167], a[28:31], a[240:243]\nG 4\nv_exp_f32 v232, v232\n"
"v_exp_f32 v233, v233\nv_mfma_f32_16x16x32_bf16 a[244:247], v[168:171], a[12:15], 0\nR 160,187,0\ns_add_u32 m0, s50, 57344\nv_fma_f32 v231, v231, v216, v216\nv_mfma_f32_16x16x32_bf16 a[244:247], v[172:175], a[28:31], a[244:247]\n"
"s_add_u32 s67, s69, 37872640\nv_rcp_f32 v230, v230\nv_fma_f32 v232, v232, v216, v216\nv_mfma_f32_16x16x32_bf16 a[228:231], v[168:171], a[8:11], 0\nR 162,187,4096\ns_add_u32 s63, s56, 720896\n"
"v_fma_f32 v233, v233, v216, v216\nv_mfma_f32_16x16x32_bf16 a[228:231], v[172:175], a[24:27], a[228:231]\nbuffer_load_dwordx4 a[88:91], v252, s[20:23], s66 offen sc1\nv_rcp_f32 v231, v231\nv_mfma_f32_16x16x32_bf16 v[20:23], v[168:171], a[4:7], 0\nR 164,187,8192\n"
"v_mul_f32 v230, v32, v230\nv_rcp_f32 v232, v232\nM 20,172,20\nbuffer_load_dwordx4 v220, s[16:19], s63 offen lds\nv_rcp_f32 v233, v233\nv_mfma_f32_16x16x32_bf16 v[4:7], v[168:171], a[0:3], 0\n"
"R 166,187,12288\nv_mul_f32 v231, v33, v231\nv_mul_f32 v232, v34, v232\nM 4,172,16\nG 4\nv_mul_f32 v233, v35, v233\n"
"v_cvt_pk_bf16_f32 v32, v230, v231\nv_cvt_pk_bf16_f32 v33, v232, v233\nLpg29:\ns_cbranch_execz Lpg33\nv_mfma_f32_16x16x32_bf16 v[8:11], v[176:179], a[0:3], 0\nR 168,184,16384\n"
"v_mul_f32 v234, v36, v36\nv_mul_f32 v235, v37, v37\nM 8,180,16\nbuffer_load_dwordx4 a[92:95], v252, s[20:23], s67 offen sc1\nbuffer_load_dwordx4 v221, s[16:19], s63 offen offset:1024 lds\nv_mul_f32 v236, v38, v38\n"
"v_mfma_f32_16x16x32_bf16 v[24:27], v[176:179], a[4:7], 0\nR 170,184,20480\ns_add_u32 m0, s50, 65536\nv_mul_f32 v237, v39, v39\nM 24,180,20\ns_add_u32 s64, s68, 37751808\n"
"v_fma_f32 v234, v234, v215, v214\nv_fma_f32 v235, v235, v215, v214\nv_fma_f32 v236, v236, v215, v214\nv_mfma_f32_16x16x32_bf16 a[232:235], v[176:179], a[8:11], 0\nR 172,184,24576\ns_add_u32 s63, s57, 131072\n"
"v_fma_f32 v237, v237, v215, v214\nv_mfma_f32_16x16x32_bf16 a[232:235], v[180:183], a[24:27], a[232:235]\ns_add_u32 s65, s68, 37866496\nbuffer_load_dwordx4 v220, s[16:19], s63 offen lds\nv_mul_f32 v234, v36, v234\nv_mfma_f32_16x16x32_bf16 a[248:251], v[176:179], a[12:15], 0\n"
"R 174,184,28672\nv_mul_f32 v235, v37, v235\nv_mul_f32 v236, v38, v236\nv_mfma_f32_16x16x32_bf16 a[248:251], v[180:183], a[28:31], a[248:251]\nG 4\nv_exp_f32 v234, v234\n"
"v_mul_f32 v237, v39, v237\nv_mfma_f32_16x16x32_bf16 a[252:255], v[160:163], a[12:15], 0\nR 176,185,16384\nv_fma_f32 v234, v234, v216, v216\nv_exp_f32 v235, v235\nv_mfma_f32_16x16x32_bf16 a[252:255], v[164:167], a[28:31], a[252:255]\n"
"buffer_load_dwordx4 a[96:99], v252, s[20:23], s64 offen sc1\nbuffer_load_dwordx4 v221, s[16:19], s63 offen offset:1024 lds\nv_exp_f32 v236, v236\nv_mfma_f32_16x16x32_bf16 a[236:239], v[160:163], a[8:11], 0\nR 178,185,20480\ns_add_u32 m0, s50, 73728\n"
"v_exp_f32 v237, v237\nv_mfma_f32_16x16x32_bf16 a[236:239], v[164:167], a[24:27], a[236:239]\ns_add_u32 s66, s69, 37751808\nv_rcp_f32 v234, v234\nv_fma_f32 v235, v235, v216, v216\nv_mfma_f32_16x16x32_bf16 v[28:31], v[160:163], a[4:7], 0\n"
"R 180,185,24576\ns_add_u32 s63, s57, 655360\nv_fma_f32 v236, v236, v216, v216\nM 28,164,20\nbuffer_load_dwordx4 a[100:103], v252, s[20:23], s65 offen sc1\nbuffer_load_dwordx4 v220, s[16:19], s63 offen lds\n"
"v_fma_f32 v237, v237, v216, v216\nv_mfma_f32_16x16x32_bf16 v[12:15], v[160:163], a[0:3], 0\nR 182,185,28672\nv_mul_f32 v234, v36, v234\nv_rcp_f32 v235, v235\nM 12,164,16\n"
"s_waitcnt vmcnt(14)\ns_barrier\nG 4\nv_rcp_f32 v236, v236\nM 0,168,32\nR 160,186,16384\n"
"v_mul_f32 v235, v37, v235\nv_rcp_f32 v237, v237\nM 0,172,48\ns_add_u32 s67, s69, 37866496\nbuffer_load_dwordx4 v221, s[16:19], s63 offen offset:1024 lds\nv_mul_f32 v236, v38, v236\n"
"M 16,168,36\nR 162,186,20480\nv_mul_f32 v237, v39, v237\nv_cvt_pk_bf16_f32 v34, v234, v235\nM 16,172,52\nbuffer_load_dwordx4 a[104:107], v252, s[20:23], s66 offen sc1\n"
"v_cvt_pk_bf16_f32 v35, v236, v237\nv_mfma_f32_16x16x32_bf16 a[224:227], v[168:171], a[40:43], a[224:227]\nR 164,186,24576\nv_mul_f32 v230, v40, v40\nv_mul_f32 v231, v41, v41\nv_mfma_f32_16x16x32_bf16 a[224:227], v[172:175], a[56:59], a[224:227]\n"
"v_mul_f32 v232, v42, v42\nv_mul_f32 v233, v43, v43\nv_fma_f32 v230, v230, v215, v214\nv_mfma_f32_16x16x32_bf16 a[240:243], v[168:171], a[44:47], a[240:243]\nR 166,186,28672\nv_fma_f32 v231, v231, v215, v214\n"
"v_fma_f32 v232, v232, v215, v214\nv_mfma_f32_16x16x32_bf16 a[240:243], v[172:175], a[60:63], a[240:243]\nG 4\nv_fma_f32 v233, v233, v215, v214\nv_mul_f32 v230, v40, v230\nv_mul_f32 v231, v41, v231\n"
"v_mfma_f32_16x16x32_bf16 a[244:247], v[176:179], a[44:47], a[244:247]\nR 168,187,16384\nv_mul_f32 v232, v42, v232\nv_mul_f32 v233, v43, v233\nv_mfma_f32_16x16x32_bf16 a[244:247], v[180:183], a[60:63], a[244:247]\nbuffer_load_dwordx4 a[108:111], v252, s[20:23], s67 offen sc1\n"
"v_exp_f32 v230, v230\nv_mfma_f32_16x16x32_bf16 a[228:231], v[176:179], a[40:43], a[228:231]\nR 170,187,20480\nv_fma_f32 v230, v230, v216, v216\nv_exp_f32 v231, v231\nv_mfma_f32_16x16x32_bf16 a[228:231], v[180:183], a[56:59], a[228:231]\n"
"s_add_u32 s64, s68, 37758976\nv_exp_f32 v232, v232\nv_exp_f32 v233, v233\nM 20,176,36\nR 172,187,24576\nv_fma_f32 v231, v231, v216, v216\n"
"v_fma_f32 v232, v232, v216, v216\nM 20,180,52\ns_add_u32 s65, s68, 37873664\nv_rcp_f32 v230, v230\nv_fma_f32 v233, v233, v216, v216\nM 4,176,32\n"
"R 174,187,28672\ns_add_u32 m0, s50, 81920\nv_rcp_f32 v231, v231\nM 4,180,48\nG 4\nv_rcp_f32 v232, v232\n"
"v_rcp_f32 v233, v233\nM 8,160,32\nR 176,184,32768\ns_add_u32 s63, s57, 196608\nv_mul_f32 v230, v40, v230\nM 8,164,48\n"
"buffer_load_dwordx4 a[112:115], v252, s[20:23], s64 offen sc1\nv_mul_f32 v231, v41, v231\nM 24,160,36\nR 178,184,36864\nv_mul_f32 v232, v42, v232\nv_mul_f32 v233, v43, v233\n"
"M 24,164,52\ns_add_u32 s66, s69, 37758976\nbuffer_load_dwordx4 v220, s[16:19], s63 offen lds\nv_cvt_pk_bf16_f32 v40, v230, v231\nv_mfma_f32_16x16x32_bf16 a[232:235], v[160:163], a[40:43], a[232:235]\nR 180,184,40960\n"
"v_cvt_pk_bf16_f32 v41, v232, v233\nv_mul_f32 v234, v44, v44\nv_mfma_f32_16x16x32_bf16 a[232:235], v[164:167], a[56:59], a[232:235]\nbuffer_load_dwordx4 a[116:119], v252, s[20:23], s65 offen sc1\nv_mul_f32 v235, v45, v45\nv_mfma_f32_16x16x32_bf16 a[248:251], v[160:163], a[44:47], a[248:251]\n"
"R 182,184,45056\nv_mul_f32 v236, v46, v46\nv_mul_f32 v237, v47, v47\nv_mfma_f32_16x16x32_bf16 a[248:251], v[164:167], a[60:63], a[248:251]\nbuffer_load_dwordx4 v221, s[16:19], s63 offen offset:1024 lds\nG 4\n"
"v_fma_f32 v234, v234, v215, v214\nv_mfma_f32_16x16x32_bf16 a[252:255], v[168:171], a[44:47], a[252:255]\nR 160,185,32768\ns_add_u32 m0, s50, 90112\nv_fma_f32 v235, v235, v215, v214\nv_mfma_f32_16x16x32_bf16 a[252:255], v[172:175], a[60:63], a[252:255]\n"
"s_add_u32 s67, s69, 37873664\ns_add_u32 s63, s57, 720896\nv_fma_f32 v236, v236, v215, v214\nv_fma_f32 v237, v237, v215, v214\nv_mfma_f32_16x16x32_bf16 a[236:239], v[168:171], a[40:43], a[236:239]\nR 162,185,36864\n"
"v_mov_b32 v248, 0\nv_mul_f32 v234, v44, v234\nv_mfma_f32_16x16x32_bf16 a[236:239], v[172:175], a[56:59], a[236:239]\nbuffer_load_dwordx4 a[120:123], v252, s[20:23], s66 offen sc1\nbuffer_load_dwordx4 v220, s[16:19], s63 offen lds\nv_mov_b32 v249, 0\n"
"v_mul_f32 v235, v45, v235\nM 28,168,36\nR 164,185,40960\ns_add_u32 s61, s82, 845848\ns_mov_b64 exec, 0xf\nbuffer_atomic_or_x2 v[248:249], v250, s[20:23], s61 offen sc0 sc1\n"
"s_mov_b64 exec, -1\nv_mul_f32 v236, v46, v236\nM 28,172,52\nbuffer_load_dwordx4 v221, s[16:19], s63 offen offset:1024 lds\nv_mul_f32 v237, v47, v237\nM 12,168,32\n"
"R 166,185,45056\ns_add_u32 m0, s50, 98304\nv_exp_f32 v234, v234\nM 12,172,48\nG 4\nv_exp_f32 v235, v235\n"
"v_exp_f32 v236, v236\nM 96,176,64\nR 168,186,32768\ns_add_u32 s63, s58, 0\nv_exp_f32 v237, v237\ns_waitcnt vmcnt(21)\n"
"M 96,180,80\nbuffer_load_dwordx4 a[124:127], v252, s[20:23], s67 offen sc1\nv_fma_f32 v234, v234, v216, v216\nM 112,176,68\nR 170,186,36864\nv_fma_f32 v235, v235, v216, v216\n"
"v_fma_f32 v236, v236, v216, v216\ns_waitcnt vmcnt(21)\nM 112,180,84\nbuffer_load_dwordx4 v220, s[16:19], s63 offen lds\nv_fma_f32 v237, v237, v216, v216\nM 128,176,72\n"
"R 172,186,40960\nv_mul_f32 v230, v48, v48\nv_rcp_f32 v234, v234\ns_waitcnt vmcnt(21)\nM 128,180,88\nbuffer_load_dwordx4 v221, s[16:19], s63 offen offset:1024 lds\n"
"v_rcp_f32 v235, v235\nM 144,176,76\nR 174,186,45056\ns_add_u32 m0, s50, 106496\nv_rcp_f32 v236, v236\ns_waitcnt vmcnt(20)\n"
"M 144,180,92\nG 4\nv_rcp_f32 v237, v237\nv_mul_f32 v234, v44, v234\nM 148,160,76\nR 176,187,32768\n"
"s_add_u32 s63, s58, 524288\nv_mul_f32 v235, v45, v235\nM 148,164,92\nbuffer_load_dwordx4 v220, s[16:19], s63 offen lds\nv_mul_f32 v236, v46, v236\nM 132,160,72\n"
"R 178,187,36864\nv_mul_f32 v237, v47, v237\nv_cvt_pk_bf16_f32 v42, v234, v235\nM 132,164,88\nbuffer_store_dwordx4 v[32:35], v252, s[20:23], s84 offen offset:0 sc1\nv_cvt_pk_bf16_f32 v43, v236, v237\n"
"M 116,160,68\nR 180,187,40960\nv_mul_f32 v231, v49, v49\nv_mul_f32 v232, v50, v50\nM 116,164,84\nbuffer_load_dwordx4 v221, s[16:19], s63 offen offset:1024 lds\n"
"v_mul_f32 v233, v51, v51\nM 100,160,64\nR 182,187,45056\nv_fma_f32 v230, v230, v218, v217\nv_fma_f32 v231, v231, v218, v217\nM 100,164,80\n"
"G 4\nv_fma_f32 v232, v232, v218, v217\nv_fma_f32 v233, v233, v218, v217\nv_mul_f32 v230, v48, v230\nM 104,168,64\nR 160,184,49152\n"
"s_waitcnt vmcnt(16)\ns_barrier\nv_mul_f32 v231, v49, v231\nM 104,172,80\nbuffer_store_dwordx4 v[40:43], v252, s[20:23], s84 offen offset:1024 sc1\nv_mul_f32 v232, v50, v232\n"
"M 120,168,68\nR 162,184,53248\nv_mul_f32 v233, v51, v233\nv_exp_f32 v230, v230\nM 120,172,84\ns_add_u32 m0, s50, 114688\n"
"v_exp_f32 v231, v231\nv_exp_f32 v232, v232\nM 136,168,72\nR 164,184,57344\nv_fma_f32 v230, v230, v219, v219\nv_exp_f32 v233, v233\n"
"M 136,172,88\ns_add_u32 s63, s58, 65536\nv_rcp_f32 v230, v230\nv_fma_f32 v231, v231, v219, v219\nM 152,168,76\nR 166,184,61440\n"
"v_fma_f32 v232, v232, v219, v219\nv_fma_f32 v233, v233, v219, v219\nM 152,172,92\nG 4\nv_rcp_f32 v231, v231\nv_rcp_f32 v232, v232\n"
"M 156,176,76\nR 168,185,49152\nv_mul_f32 v230, v48, v230\nv_rcp_f32 v233, v233\nM 156,180,92\nbuffer_load_dwordx4 v220, s[16:19], s63 offen lds\n"
"v_mul_f32 v231, v49, v231\nM 140,176,72\nR 170,185,53248\nv_mul_f32 v232, v50, v232\nv_mul_f32 v233, v51, v233\nM 140,180,88\n"
"v_cvt_pk_bf16_f32 v48, v230, v231\nv_cvt_pk_bf16_f32 v49, v232, v233\nv_mul_f32 v234, v52, v52\nM 124,176,68\nR 172,185,57344\nv_mul_f32 v235, v53, v53\n"
"v_mul_f32 v236, v54, v54\nM 124,180,84\nbuffer_load_dwordx4 v221, s[16:19], s63 offen offset:1024 lds\nv_mul_f32 v237, v55, v55\nM 108,176,64\nR 174,185,61440\n"
"s_add_u32 m0, s50, 0\nv_fma_f32 v234, v234, v218, v217\nM 108,180,80\nG 4\nv_fma_f32 v235, v235, v218, v217\nv_fma_f32 v236, v236, v218, v217\n"
"v_fma_f32 v237, v237, v218, v217\nM 96,160,96\nR 176,186,49152\ns_add_u32 s63, s58, 589824\nv_mul_f32 v234, v52, v234\ns_waitcnt vmcnt(16)\n"
"M 96,164,112\nbuffer_load_dwordx4 v220, s[16:19], s63 offen lds\nv_mul_f32 v235, v53, v235\nM 112,160,100\nR 178,186,53248\nv_mul_f32 v236, v54, v236\n"
"v_mul_f32 v237, v55, v237\ns_waitcnt vmcnt(15)\nM 112,164,116\nv_exp_f32 v234, v234\nv_exp_f32 v235, v235\nM 128,160,104\n"
"R 180,186,57344\nv_fma_f32 v234, v234, v219, v219\nv_exp_f32 v236, v236\ns_waitcnt vmcnt(13)\nM 128,164,120\nv_exp_f32 v237, v237\n"
"v_fma_f32 v235, v235, v219, v219\nM 144,160,108\nR 182,186,61440\nv_fma_f32 v236, v236, v219, v219\nv_fma_f32 v237, v237, v219, v219\ns_waitcnt vmcnt(9)\n"
"M 144,164,124\nG 4\nv_rcp_f32 v234, v234\nv_rcp_f32 v235, v235\nM 148,168,108\nR 160,187,49152\n"
"v_mul_f32 v234, v52, v234\nv_rcp_f32 v236, v236\nM 148,172,124\ns_add_u32 s64, s68, 37754880\nv_rcp_f32 v237, v237\nv_mul_f32 v235, v53, v235\n"
"M 132,168,104\nR 162,187,53248\nv_cmp_eq_u64 s[86:87], s[46:47], v[248:249]\nv_mul_f32 v236, v54, v236\nM 132,172,120\nbuffer_load_dwordx4 v221, s[16:19], s63 offen offset:1024 lds\n"
"v_mul_f32 v237, v55, v237\nM 116,168,100\nR 164,187,57344\ns_and_b32 s62, s86, 0xf\ns_cmp_eq_u32 s62, 0xf\ns_cbranch_scc0 Lq922\n"
"Lq923:\nv_cvt_pk_bf16_f32 v50, v234, v235\nM 116,172,116\ns_add_u32 s65, s68, 37869568\nv_cvt_pk_bf16_f32 v51, v236, v237\nv_mul_f32 v230, v56, v56\n"
"v_mul_f32 v231, v57, v57\nM 100,168,96\nR 166,187,61440\nv_mul_f32 v232, v58, v58\nv_mul_f32 v233, v59, v59\nM 100,172,112\n"
"G 4\nv_fma_f32 v230, v230, v218, v217\nv_fma_f32 v231, v231, v218, v217\nM 104,176,96\nR 168,188,0\nv_fma_f32 v232, v232, v218, v217\n"
"M 104,180,112\nbuffer_load_dwordx4 a[128:131], v252, s[20:23], s64 offen sc1\nM 120,176,100\nR 170,188,4096\nv_fma_f32 v233, v233, v218, v217\nM 120,180,116\n"
"s_add_u32 s66, s69, 37754880\nv_mul_f32 v230, v56, v230\nv_mul_f32 v231, v57, v231\nM 136,176,104\nR 172,188,8192\nv_mul_f32 v232, v58, v232\n"
"M 136,180,120\nbuffer_load_dwordx4 a[132:135], v252, s[20:23], s65 offen sc1\nM 152,176,108\nR 174,188,12288\nv_mul_f32 v233, v59, v233\nM 152,180,124\n"
"G 4\nv_exp_f32 v230, v230\nM 156,160,108\nR 176,189,0\nv_fma_f32 v230, v230, v219, v219\nM 156,164,124\n"
"s_add_u32 s67, s69, 37869568\nv_exp_f32 v231, v231\nM 140,160,104\nR 178,189,4096\nv_fma_f32 v231, v231, v219, v219\nM 140,164,120\n"
"buffer_load_dwordx4 a[136:139], v252, s[20:23], s66 offen sc1\nM 124,160,100\nR 180,189,8192\nv_mul_f32 v234, v60, v60\nM 124,164,116\nbuffer_store_dwordx4 v[48:51], v252, s[20:23], s85 offen offset:0 sc1\n"
"M 108,160,96\nR 182,189,12288\ns_add_u32 m0, s50, 8192\nM 108,164,112\ns_waitcnt vmcnt(9)\ns_barrier\n"
"G 4\nM 0,168,64\nR 160,190,0\ns_add_u32 s63, s58, 131072\nM 0,172,80\nbuffer_load_dwordx4 a[140:143], v252, s[20:23], s67 offen sc1\n"
"M 16,168,68\nR 162,190,4096\nv_mul_f32 v235, v61, v61\nM 16,172,84\ns_add_u32 s64, s68, 37762048\nv_exp_f32 v232, v232\n"
"v_mfma_f32_16x16x32_bf16 a[224:227], v[168:171], a[72:75], a[224:227]\nR 164,190,8192\nbuffer_load_dwordx4 v220, s[16:19], s63 offen lds\nv_mfma_f32_16x16x32_bf16 a[224:227], v[172:175], a[88:91], a[224:227]\ns_add_u32 s65, s68, 37876736\nv_exp_f32 v233, v233\n"
"v_mfma_f32_16x16x32_bf16 a[240:243], v[168:171], a[76:79], a[240:243]\nR 166,190,12288\nv_fma_f32 v232, v232, v219, v219\nv_mfma_f32_16x16x32_bf16 a[240:243], v[172:175], a[92:95], a[240:243]\nG 4\nv_rcp_f32 v230, v230\n"
"v_mfma_f32_16x16x32_bf16 a[244:247], v[176:179], a[76:79], a[244:247]\nR 168,191,0\nbuffer_load_dwordx4 v221, s[16:19], s63 offen offset:1024 lds\nv_mfma_f32_16x16x32_bf16 a[244:247], v[180:183], a[92:95], a[244:247]\nbuffer_load_dwordx4 a[144:147], v252, s[20:23], s64 offen sc1\nv_mfma_f32_16x16x32_bf16 a[228:231], v[176:179], a[72:75], a[228:231]\n"
"R 170,191,4096\ns_add_u32 m0, s50, 16384\nv_mfma_f32_16x16x32_bf16 a[228:231], v[180:183], a[88:91], a[228:231]\ns_add_u32 s66, s69, 37762048\ns_add_u32 s63, s58, 655360\nv_fma_f32 v233, v233, v219, v219\n"
"M 20,176,68\nR 172,191,8192\nbuffer_load_dwordx4 v220, s[16:19], s63 offen lds\nM 20,180,84\nbuffer_load_dwordx4 a[148:151], v252, s[20:23], s65 offen sc1\nM 4,176,64\n"
"R 174,191,12288\nv_mul_f32 v230, v56, v230\nM 4,180,80\nG 4\nv_rcp_f32 v231, v231\nM 8,160,64\n"
"R 176,188,16384\nbuffer_load_dwordx4 v221, s[16:19], s63 offen offset:1024 lds\nM 8,164,80\ns_add_u32 s67, s69, 37876736\nv_rcp_f32 v232, v232\nM 24,160,68\n"
"R 178,188,20480\nv_mul_f32 v231, v57, v231\nM 24,164,84\nbuffer_load_dwordx4 a[152:155], v252, s[20:23], s66 offen sc1\nv_mfma_f32_16x16x32_bf16 a[232:235], v[160:163], a[72:75], a[232:235]\nR 180,188,24576\n"
"v_mul_f32 v232, v58, v232\nv_mfma_f32_16x16x32_bf16 a[232:235], v[164:167], a[88:91], a[232:235]\nv_rcp_f32 v233, v233\nv_mfma_f32_16x16x32_bf16 a[248:251], v[160:163], a[76:79], a[248:251]\nR 182,188,28672\nv_mul_f32 v233, v59, v233\n"
"v_mfma_f32_16x16x32_bf16 a[248:251], v[164:167], a[92:95], a[248:251]\nG 4\nv_cvt_pk_bf16_f32 v56, v230, v231\nv_cvt_pk_bf16_f32 v57, v232, v233\nv_mfma_f32_16x16x32_bf16 a[252:255], v[168:171], a[76:79], a[252:255]\nR 160,189,16384\n"
"v_mul_f32 v236, v62, v62\nv_mfma_f32_16x16x32_bf16 a[252:255], v[172:175], a[92:95], a[252:255]\nbuffer_load_dwordx4 a[156:159], v252, s[20:23], s67 offen sc1\nv_mfma_f32_16x16x32_bf16 a[236:239], v[168:171], a[72:75], a[236:239]\nR 162,189,20480\nv_mul_f32 v237, v63, v63\n"
"v_mfma_f32_16x16x32_bf16 a[236:239], v[172:175], a[88:91], a[236:239]\nv_fma_f32 v234, v234, v218, v217\nv_fma_f32 v235, v235, v218, v217\nM 28,168,68\nR 164,189,24576\nv_fma_f32 v236, v236, v218, v217\n"
"M 28,172,84\ns_add_u32 m0, s50, 24576\nv_fma_f32 v237, v237, v218, v217\nv_mul_f32 v234, v60, v234\nM 12,168,64\nR 166,189,28672\n"
"v_mul_f32 v235, v61, v235\nM 12,172,80\nG 4\nv_exp_f32 v234, v234\nM 0,176,96\nR 168,190,16384\n"
"v_mul_f32 v236, v62, v236\nM 0,180,112\ns_add_u32 s63, s58, 196608\nv_exp_f32 v235, v235\nM 16,176,100\nR 170,190,20480\n"
"v_mul_f32 v237, v63, v237\nM 16,180,116\nbuffer_load_dwordx4 v220, s[16:19], s63 offen lds\nv_mfma_f32_16x16x32_bf16 a[224:227], v[176:179], a[104:107], a[224:227]\nR 172,190,24576\nv_fma_f32 v234, v234, v219, v219\n"
"v_mfma_f32_16x16x32_bf16 a[224:227], v[180:183], a[120:123], a[224:227]\nv_exp_f32 v236, v236\nv_mfma_f32_16x16x32_bf16 a[240:243], v[176:179], a[108:111], a[240:243]\nR 174,190,28672\nv_fma_f32 v235, v235, v219, v219\nv_mfma_f32_16x16x32_bf16 a[240:243], v[180:183], a[124:127], a[240:243]\n"
"G 4\nv_exp_f32 v237, v237\nv_mfma_f32_16x16x32_bf16 a[244:247], v[160:163], a[108:111], a[244:247]\nR 176,191,16384\nv_fma_f32 v236, v236, v219, v219\nv_mfma_f32_16x16x32_bf16 a[244:247], v[164:167], a[124:127], a[244:247]\n"
"buffer_load_dwordx4 v221, s[16:19], s63 offen offset:1024 lds\nv_mfma_f32_16x16x32_bf16 a[228:231], v[160:163], a[104:107], a[228:231]\nR 178,191,20480\ns_add_u32 s63, s58, 720896\nv_mfma_f32_16x16x32_bf16 a[228:231], v[164:167], a[120:123], a[228:231]\ns_add_u32 m0, s50, 32768\n"
"v_rcp_f32 v234, v234\nM 20,160,100\nR 180,191,24576\nv_fma_f32 v237, v237, v219, v219\nM 20,164,116\nbuffer_load_dwordx4 v220, s[16:19], s63 offen lds\n"
"M 4,160,96\nR 182,191,28672\nv_mul_f32 v234, v60, v234\nM 4,164,112\nG 4\nv_rcp_f32 v235, v235\n"
"M 8,168,96\nR 160,188,32768\nv_mul_f32 v235, v61, v235\nM 8,172,112\nbuffer_load_dwordx4 v221, s[16:19], s63 offen offset:1024 lds\nM 24,168,100\n"
"R 162,188,36864\ns_add_u32 s63, s58, 393216\nM 24,172,116\ns_add_u32 m0, s50, 40960\nv_rcp_f32 v236, v236\nv_mfma_f32_16x16x32_bf16 a[232:235], v[168:171], a[104:107], a[232:235]\n"
"R 164,188,40960\nv_mul_f32 v236, v62, v236\nv_mfma_f32_16x16x32_bf16 a[232:235], v[172:175], a[120:123], a[232:235]\nbuffer_load_dwordx4 v220, s[16:19], s63 offen lds\nv_mfma_f32_16x16x32_bf16 a[248:251], v[168:171], a[108:111], a[248:251]\nR 166,188,45056\n"
"v_cvt_pk_bf16_f32 v58, v234, v235\nv_mfma_f32_16x16x32_bf16 a[248:251], v[172:175], a[124:127], a[248:251]\nG 4\nv_rcp_f32 v237, v237\nv_mfma_f32_16x16x32_bf16 a[252:255], v[176:179], a[108:111], a[252:255]\nR 168,189,32768\n"
"v_mul_f32 v237, v63, v237\nv_mfma_f32_16x16x32_bf16 a[252:255], v[180:183], a[124:127], a[252:255]\nbuffer_load_dwordx4 v221, s[16:19], s63 offen offset:1024 lds\nv_mfma_f32_16x16x32_bf16 a[236:239], v[176:179], a[104:107], a[236:239]\nR 170,189,36864\ns_add_u32 s63, s58, 458752\n"
"v_mfma_f32_16x16x32_bf16 a[236:239], v[180:183], a[120:123], a[236:239]\ns_add_u32 m0, s50, 49152\nv_cvt_pk_bf16_f32 v59, v236, v237\nLpg30:\ns_cbranch_execz LretO1n\nbuffer_store_dwordx4 v[56:59], v252, s[20:23], s85 offen offset:1024 sc1\n"
"M 28,176,100\nR 172,189,40960\nM 28,180,116\nbuffer_load_dwordx4 v220, s[16:19], s63 offen lds\nM 12,176,96\nR 174,189,45056\n"
"M 12,180,112\ns_waitcnt vmcnt(10)\ns_barrier\nG 4\nv_mfma_f32_16x16x32_bf16 v[32:35], v[160:163], a[0:3], 0\nR 176,190,32768\n"
"M 32,164,16\nbuffer_load_dwordx4 v221, s[16:19], s63 offen offset:1024 lds\nv_mfma_f32_16x16x32_bf16 v[48:51], v[160:163], a[4:7], 0\nR 178,190,36864\nM 48,164,20\nv_mfma_f32_16x16x32_bf16 v[64:67], v[160:163], a[8:11], 0\n"
"R 180,190,40960\nM 64,164,24\nv_mfma_f32_16x16x32_bf16 v[80:83], v[160:163], a[12:15], 0\nR 182,190,45056\nM 80,164,28\nG 4\n"
"v_mfma_f32_16x16x32_bf16 v[84:87], v[168:171], a[12:15], 0\nR 160,191,32768\nM 84,172,28\nv_mfma_f32_16x16x32_bf16 v[68:71], v[168:171], a[8:11], 0\nR 162,191,36864\nM 68,172,24\n"
"v_mfma_f32_16x16x32_bf16 v[52:55], v[168:171], a[4:7], 0\nR 164,191,40960\nM 52,172,20\nv_mfma_f32_16x16x32_bf16 v[36:39], v[168:171], a[0:3], 0\nR 166,191,45056\nM 36,172,16\n"
"G 4\nv_mfma_f32_16x16x32_bf16 v[40:43], v[176:179], a[0:3], 0\nR 168,188,49152\nM 40,180,16\nv_mfma_f32_16x16x32_bf16 v[56:59], v[176:179], a[4:7], 0\nR 170,188,53248\n"
"M 56,180,20\nv_mfma_f32_16x16x32_bf16 v[72:75], v[176:179], a[8:11], 0\nR 172,184,0\nM 72,180,24\nv_mfma_f32_16x16x32_bf16 v[88:91], v[176:179], a[12:15], 0\nR 174,184,4096\n"
"M 88,180,28\nG 4\nv_mfma_f32_16x16x32_bf16 v[92:95], v[160:163], a[12:15], 0\nR 176,189,49152\nM 92,164,28\nv_mfma_f32_16x16x32_bf16 v[76:79], v[160:163], a[8:11], 0\n"
"R 178,189,53248\nM 76,164,24\nv_mfma_f32_16x16x32_bf16 v[60:63], v[160:163], a[4:7], 0\nR 180,185,0\nM 60,164,20\nv_mfma_f32_16x16x32_bf16 v[44:47], v[160:163], a[0:3], 0\n"
"R 182,185,4096\nM 44,164,16\nG 4\nM 32,168,32\nR 160,190,49152\nM 32,172,48\n"
"M 48,168,36\nR 162,190,53248\nM 48,172,52\nM 64,168,40\nR 164,186,0\nM 64,172,56\n"
"M 80,168,44\nR 166,186,4096\nM 80,172,60\nG 4\nM 84,176,44\nR 168,191,49152\n"
"M 84,180,60\nM 68,176,40\nR 170,191,53248\nv_mov_b32 v226, s46\nM 68,180,56\ns_add_u32 m0, s50, 57344\n"
"v_mov_b32 v227, s47\nM 52,176,36\nR 172,187,0\ns_add_u32 s61, s81, 845840\ns_lshl_b32 s62, s44, 5\ns_add_u32 s61, s61, s62\n"
"s_waitcnt vmcnt(0)\ns_mov_b64 exec, 1\nbuffer_atomic_swap_x2 v[226:227], off, s[20:23], s61 sc1\ns_mov_b64 exec, -1\nM 52,180,52\ns_add_u32 s63, s56, 393216\n"
"M 36,176,32\nR 174,187,4096\nM 36,180,48\nG 4\nM 40,160,32\nR 176,184,8192\n"
"M 40,164,48\nbuffer_load_dwordx4 v220, s[16:19], s63 offen lds\nM 56,160,36\nR 178,184,12288\nM 56,164,52\nM 72,160,40\n"
"R 180,184,16384\nM 72,164,56\nbuffer_load_dwordx4 v221, s[16:19], s63 offen offset:1024 lds\nM 88,160,44\nR 182,184,20480\ns_add_u32 m0, s50, 65536\n"
"M 88,164,60\nG 4\nM 92,168,44\nR 160,185,8192\ns_add_u32 s63, s56, 458752\nM 92,172,60\n"
"buffer_load_dwordx4 v220, s[16:19], s63 offen lds\nM 76,168,40\nR 162,185,12288\nM 76,172,56\nM 60,168,36\nR 164,185,16384\n"
"M 60,172,52\nbuffer_load_dwordx4 v221, s[16:19], s63 offen offset:1024 lds\nM 44,168,32\nR 166,185,20480\ns_add_u32 m0, s50, 73728\nM 44,172,48\n"
"s_barrier\nG 4\nM 32,176,64\nR 168,186,8192\ns_add_u32 s63, s57, 393216\nM 32,180,80\n"
"buffer_load_dwordx4 v220, s[16:19], s63 offen lds\nM 48,176,68\nR 170,186,12288\nM 48,180,84\nM 64,176,72\nR 172,186,16384\n"
"M 64,180,88\nbuffer_load_dwordx4 v221, s[16:19], s63 offen offset:1024 lds\nM 80,176,76\nR 174,186,20480\ns_add_u32 s63, s57, 458752\nM 80,180,92\n"
"G 4\nM 84,160,76\nR 176,187,8192\nM 84,164,92\ns_add_u32 m0, s50, 81920\nM 68,160,72\n"
"R 178,187,12288\nM 68,164,88\nbuffer_load_dwordx4 v220, s[16:19], s63 offen lds\nM 52,160,68\nR 180,187,16384\nv_mov_b32 v248, 0\n"
"M 52,164,84\nv_mov_b32 v249, 0\nM 36,160,64\nR 182,187,20480\ns_add_u32 s61, s82, 845840\ns_mov_b64 exec, 0xf\n"
"buffer_atomic_or_x2 v[248:249], v250, s[20:23], s61 offen sc0 sc1\ns_mov_b64 exec, -1\nM 36,164,80\nG 4\nM 40,168,64\nR 160,184,24576\n"
"M 40,172,80\nM 56,168,68\nR 162,184,28672\nM 56,172,84\nbuffer_load_dwordx4 v221, s[16:19], s63 offen offset:1024 lds\nM 72,168,72\n"
"R 164,184,32768\ns_add_u32 s63, s56, 262144\nM 72,172,88\ns_add_u32 m0, s50, 90112\nM 88,168,76\nR 166,184,36864\n"
"M 88,172,92\nG 4\nM 92,176,76\nR 168,185,24576\nM 92,180,92\nbuffer_load_dwordx4 v220, s[16:19], s63 offen lds\n"
"M 76,176,72\nR 170,185,28672\nM 76,180,88\nM 60,176,68\nR 172,185,32768\nM 60,180,84\n"
"buffer_load_dwordx4 v221, s[16:19], s63 offen offset:1024 lds\nM 44,176,64\nR 174,185,36864\ns_add_u32 m0, s50, 98304\nM 44,180,80\nG 4\n"
"M 32,160,96\nR 176,186,24576\ns_add_u32 s63, s56, 786432\nM 32,164,112\nbuffer_load_dwordx4 v220, s[16:19], s63 offen lds\nM 48,160,100\n"
"R 178,186,28672\nM 48,164,116\nM 64,160,104\nR 180,186,32768\nM 64,164,120\nbuffer_load_dwordx4 v221, s[16:19], s63 offen offset:1024 lds\n"
"M 80,160,108\nR 182,186,36864\ns_add_u32 m0, s50, 106496\nM 80,164,124\nG 4\nM 84,168,108\n"
"R 160,187,24576\ns_add_u32 s63, s56, 327680\nM 84,172,124\nbuffer_load_dwordx4 v220, s[16:19], s63 offen lds\nM 68,168,104\nR 162,187,28672\n"
"M 68,172,120\nM 52,168,100\nR 164,187,32768\nM 52,172,116\nbuffer_load_dwordx4 v221, s[16:19], s63 offen offset:1024 lds\nM 36,168,96\n"
"R 166,187,36864\ns_add_u32 s63, s56, 851968\nM 36,172,112\nG 4\nM 40,176,96\nR 168,184,40960\n"
"M 40,180,112\ns_add_u32 m0, s50, 114688\nM 56,176,100\nR 170,184,45056\nM 56,180,116\nbuffer_load_dwordx4 v220, s[16:19], s63 offen lds\n"
"M 72,176,104\nR 172,184,49152\nM 72,180,120\nM 88,176,108\nR 174,184,53248\ns_waitcnt vmcnt(12)\n"
"s_barrier\nM 88,180,124\nG 4\nM 92,160,108\nR 176,185,40960\nM 92,164,124\n"
"s_add_u32 s64, s68, 37752832\nM 76,160,104\nR 178,185,45056\nM 76,164,120\nbuffer_load_dwordx4 v221, s[16:19], s63 offen offset:1024 lds\nM 60,160,100\n"
"R 180,185,49152\ns_waitcnt vmcnt(9)\nv_cmp_eq_u64 s[86:87], s[46:47], v[248:249]\nM 60,164,116\ns_add_u32 s65, s68, 37867520\nM 44,160,96\n"
"R 182,185,53248\ns_and_b32 s62, s86, 0xf\ns_cmp_eq_u32 s62, 0xf\ns_cbranch_scc0 Lq941\nLq942:\nM 44,164,112\n"
"G 4\nM 32,168,128\nR 160,186,40960\nM 32,172,144\nbuffer_load_dwordx4 a[160:163], v252, s[20:23], s64 offen sc1\nM 48,168,132\n"
"R 162,186,45056\nM 48,172,148\ns_add_u32 s66, s69, 37752832\nM 64,168,136\nR 164,186,49152\nM 64,172,152\n"
"buffer_load_dwordx4 a[164:167], v252, s[20:23], s65 offen sc1\nM 80,168,140\nR 166,186,53248\nM 80,172,156\nG 4\nM 84,176,140\n"
"R 168,187,40960\nM 84,180,156\ns_add_u32 s67, s69, 37867520\nM 68,176,136\nR 170,187,45056\nM 68,180,152\n"
"buffer_load_dwordx4 a[168:171], v252, s[20:23], s66 offen sc1\nM 52,176,132\nR 172,187,49152\nM 52,180,148\ns_add_u32 s64, s68, 37760000\nM 36,176,128\n"
"R 174,187,53248\ns_add_u32 m0, s50, 0\nM 36,180,144\nG 4\nM 40,160,128\nR 176,184,57344\n"
"s_add_u32 s63, s57, 262144\nM 40,164,144\nbuffer_load_dwordx4 a[172:175], v252, s[20:23], s67 offen sc1\nM 56,160,132\nR 178,184,61440\nM 56,164,148\n"
"s_add_u32 s65, s68, 37874688\nbuffer_load_dwordx4 v220, s[16:19], s63 offen lds\nM 72,160,136\nR 180,188,0\nM 72,164,152\nbuffer_load_dwordx4 a[176:179], v252, s[20:23], s64 offen sc1\n"
"M 88,160,140\nR 182,188,4096\nM 88,164,156\nbuffer_load_dwordx4 v221, s[16:19], s63 offen offset:1024 lds\nG 4\nM 92,168,140\n"
"R 160,185,57344\ns_add_u32 m0, s50, 8192\nM 92,172,156\ns_add_u32 s66, s69, 37760000\nM 76,168,136\nR 162,185,61440\n"
"s_add_u32 s63, s57, 786432\nM 76,172,152\nbuffer_load_dwordx4 a[180:183], v252, s[20:23], s65 offen sc1\nbuffer_load_dwordx4 v220, s[16:19], s63 offen lds\nM 60,168,132\nR 164,189,0\n"
"M 60,172,148\ns_add_u32 s67, s69, 37874688\nM 44,168,128\nR 166,189,4096\nM 44,172,144\nbuffer_load_dwordx4 v221, s[16:19], s63 offen offset:1024 lds\n"
"G 4\nM 96,176,128\nR 168,186,57344\ns_add_u32 m0, s50, 16384\nM 96,180,144\nbuffer_load_dwordx4 a[184:187], v252, s[20:23], s66 offen sc1\n"
"M 112,176,132\nR 170,186,61440\ns_add_u32 s63, s57, 327680\nM 112,180,148\ns_add_u32 s64, s68, 37753856\nbuffer_load_dwordx4 v220, s[16:19], s63 offen lds\n"
"M 128,176,136\nR 172,190,0\nM 128,180,152\nbuffer_load_dwordx4 a[188:191], v252, s[20:23], s67 offen sc1\nM 144,176,140\nR 174,190,4096\n"
"M 144,180,156\nbuffer_load_dwordx4 v221, s[16:19], s63 offen offset:1024 lds\nG 4\nM 148,160,140\nR 176,187,57344\ns_waitcnt vmcnt(18)\n"
"s_barrier\nM 148,164,156\ns_add_u32 s65, s68, 37868544\ns_add_u32 s63, s57, 851968\nM 132,160,136\nR 178,187,61440\n"
"s_add_u32 m0, s50, 24576\nM 132,164,152\nbuffer_load_dwordx4 a[192:195], v252, s[20:23], s64 offen sc1\nM 116,160,132\nR 180,191,0\nbuffer_load_dwordx4 v220, s[16:19], s63 offen lds\n"
"M 116,164,148\ns_add_u32 s66, s69, 37753856\nM 100,160,128\nR 182,191,4096\nM 100,164,144\nG 4\n"
"M 104,168,128\nR 160,188,8192\nbuffer_load_dwordx4 v221, s[16:19], s63 offen offset:1024 lds\nM 104,172,144\nbuffer_load_dwordx4 a[196:199], v252, s[20:23], s65 offen sc1\nM 120,168,132\n"
"R 162,188,12288\nM 120,172,148\ns_add_u32 s67, s69, 37868544\nM 136,168,136\nR 164,188,16384\nM 136,172,152\n"
"buffer_load_dwordx4 a[200:203], v252, s[20:23], s66 offen sc1\nM 152,168,140\nR 166,188,20480\nM 152,172,156\nG 4\nM 156,176,140\n"
"R 168,189,8192\nM 156,180,156\nbuffer_load_dwordx4 a[204:207], v252, s[20:23], s67 offen sc1\nM 140,176,136\nR 170,189,12288\nM 140,180,152\n"
"s_add_u32 s64, s68, 37761024\nM 124,176,132\nR 172,189,16384\nM 124,180,148\ns_add_u32 s65, s68, 37875712\nM 108,176,128\n"
"R 174,189,20480\nM 108,180,144\nG 4\nM 0,160,128\nR 176,190,8192\nM 0,164,144\n"
"buffer_load_dwordx4 a[208:211], v252, s[20:23], s64 offen sc1\nM 16,160,132\nR 178,190,12288\nM 16,164,148\ns_add_u32 s66, s69, 37761024\nv_mfma_f32_16x16x32_bf16 a[224:227], v[160:163], a[136:139], a[224:227]\n"
"R 180,190,16384\nv_mfma_f32_16x16x32_bf16 a[224:227], v[164:167], a[152:155], a[224:227]\nbuffer_load_dwordx4 a[212:215], v252, s[20:23], s65 offen sc1\nv_mfma_f32_16x16x32_bf16 a[240:243], v[160:163], a[140:143], a[240:243]\nR 182,190,20480\nv_mfma_f32_16x16x32_bf16 a[240:243], v[164:167], a[156:159], a[240:243]\n"
"G 4\nv_mfma_f32_16x16x32_bf16 a[244:247], v[168:171], a[140:143], a[244:247]\nR 160,191,8192\nv_mfma_f32_16x16x32_bf16 a[244:247], v[172:175], a[156:159], a[244:247]\nbuffer_load_dwordx4 a[216:219], v252, s[20:23], s66 offen sc1\nv_mfma_f32_16x16x32_bf16 a[228:231], v[168:171], a[136:139], a[228:231]\n"
"R 162,191,12288\nv_mfma_f32_16x16x32_bf16 a[228:231], v[172:175], a[152:155], a[228:231]\ns_add_u32 s67, s69, 37875712\nM 20,168,132\nR 164,191,16384\nM 20,172,148\n"
"M 4,168,128\nR 166,191,20480\nM 4,172,144\nG 4\nM 8,176,128\nR 168,188,24576\n"
"M 8,180,144\nbuffer_load_dwordx4 a[220:223], v252, s[20:23], s67 offen sc1\nM 24,176,132\nR 170,188,28672\nM 24,180,148\ns_add_u32 m0, s50, 32768\n"
"v_mfma_f32_16x16x32_bf16 a[232:235], v[176:179], a[136:139], a[232:235]\nR 172,188,32768\nv_mfma_f32_16x16x32_bf16 a[232:235], v[180:183], a[152:155], a[232:235]\ns_add_u32 s63, s58, 262144\nv_mfma_f32_16x16x32_bf16 a[248:251], v[176:179], a[140:143], a[248:251]\nR 174,188,36864\n"
"v_mfma_f32_16x16x32_bf16 a[248:251], v[180:183], a[156:159], a[248:251]\nG 4\nv_mfma_f32_16x16x32_bf16 a[252:255], v[160:163], a[140:143], a[252:255]\nR 176,189,24576\nv_mfma_f32_16x16x32_bf16 a[252:255], v[164:167], a[156:159], a[252:255]\nbuffer_load_dwordx4 v220, s[16:19], s63 offen lds\n"
"v_mfma_f32_16x16x32_bf16 a[236:239], v[160:163], a[136:139], a[236:239]\nR 178,189,28672\nv_mfma_f32_16x16x32_bf16 a[236:239], v[164:167], a[152:155], a[236:239]\nM 28,160,132\nR 180,189,32768\nM 28,164,148\n"
"buffer_load_dwordx4 v221, s[16:19], s63 offen offset:1024 lds\nM 12,160,128\nR 182,189,36864\nM 12,164,144\ns_waitcnt vmcnt(9)\ns_barrier\n"
"G 4\nM 96,168,160\nR 160,190,24576\nM 96,172,176\ns_add_u32 m0, s50, 40960\nM 112,168,164\n"
"R 162,190,28672\nM 112,172,180\ns_add_u32 s63, s58, 786432\nM 128,168,168\nR 164,190,32768\nv_lshrrev_b32 v230, 8, v252\n"
"M 128,172,184\nbuffer_load_dwordx4 v220, s[16:19], s63 offen lds\nM 144,168,172\nR 166,190,36864\nv_lshlrev_b32 v230, 3, v230\nM 144,172,188\n"
"G 4\nM 148,176,172\nR 168,191,24576\nM 148,180,188\nbuffer_load_dwordx2 v[232:233], v230, s[36:39], s53 offen offset:0\nM 132,176,168\n"
"R 170,191,28672\nM 132,180,184\nbuffer_load_dwordx2 v[234:235], v230, s[36:39], s53 offen offset:32\nM 116,176,164\nR 172,191,32768\nM 116,180,180\n"
"buffer_load_dwordx4 v221, s[16:19], s63 offen offset:1024 lds\nM 100,176,160\nR 174,191,36864\nM 100,180,176\nG 4\nM 104,160,160\n"
"R 176,188,40960\nM 104,164,176\ns_add_u32 m0, s50, 49152\nM 120,160,164\nR 178,188,45056\nM 120,164,180\n"
"s_add_u32 s63, s58, 327680\nM 136,160,168\nR 180,188,49152\nbuffer_load_dwordx2 v[236:237], v230, s[36:39], s53 offen offset:64\nM 136,164,184\nbuffer_load_dwordx4 v220, s[16:19], s63 offen lds\n"
"M 152,160,172\nR 182,188,53248\nM 152,164,188\nbuffer_load_dwordx2 v[238:239], v230, s[36:39], s53 offen offset:96\nG 4\nM 156,168,172\n"
"R 160,189,40960\nLpg31:\ns_cbranch_execz LretO1n\nv_bfe_u32 v242, v252, 4, 4\nM 156,172,188\nv_lshrrev_b32 v243, 8, v252\n"
"v_lshlrev_b32 v243, 2, v243\nM 140,168,168\nR 162,189,45056\nv_sub_u32 v242, v242, v243\nM 140,172,184\nv_lshlrev_b32 v243, 4, v242\n"
"s_mov_b64 s[60:61], 0x3f80\nv_lshlrev_b64 v[208:209], v243, s[60:61]\nM 124,168,164\nR 164,189,49152\nM 124,172,180\nbuffer_load_dwordx4 v221, s[16:19], s63 offen offset:1024 lds\n"
"M 108,168,160\nR 166,189,53248\nM 108,172,176\nG 4\nM 96,176,192\nR 168,190,40960\n"
"s_waitcnt vmcnt(13)\nM 96,180,208\ns_add_u32 m0, s50, 57344\nM 112,176,196\nR 170,190,45056\ns_waitcnt vmcnt(12)\n"
"M 112,180,212\ns_add_u32 s63, s58, 851968\nM 128,176,200\nR 172,190,49152\ns_waitcnt vmcnt(11)\nM 128,180,216\n"
"buffer_load_dwordx4 v220, s[16:19], s63 offen lds\nM 144,176,204\nR 174,190,53248\ns_waitcnt vmcnt(11)\nM 144,180,220\nG 4\n"
"M 148,160,204\nR 176,191,40960\nM 148,164,220\nv_cmp_gt_u32 s[60:61], 4, v242\ns_nop 1\nv_cndmask_b32 v212, 0, v208, s[60:61]\n"
"v_cndmask_b32 v208, 0, v208, s[60:61]\nv_cndmask_b32 v213, 0, v209, s[60:61]\nv_cndmask_b32 v209, 0, v209, s[60:61]\nM 132,160,200\nR 178,191,45056\nM 132,164,216\n"
"M 116,160,196\nR 180,191,49152\nM 116,164,212\nbuffer_load_dwordx4 v221, s[16:19], s63 offen offset:1024 lds\nM 100,160,192\nR 182,191,53248\n"
"M 100,164,208\nG 4\nM 104,168,192\nR 160,184,0\nM 104,172,208\ns_add_u32 m0, s50, 65536\n"
"M 120,168,196\nR 162,184,4096\nM 120,172,212\ns_add_u32 s63, s59, 0\nM 136,168,200\nR 164,184,8192\n"
"M 136,172,216\nbuffer_load_dwordx4 v220, s[16:19], s63 offen lds\nM 152,168,204\nR 166,184,12288\nM 152,172,220\nG 4\n"
"M 156,176,204\nR 168,185,0\nM 156,180,220\nM 140,176,200\nR 170,185,4096\nM 140,180,216\n"
"buffer_load_dwordx4 v221, s[16:19], s63 offen offset:1024 lds\nM 124,176,196\nR 172,185,8192\nM 124,180,212\nM 108,176,192\nR 174,185,12288\n"
"M 108,180,208\nG 4\nM 0,160,160\nR 176,186,0\nM 0,164,176\ns_mov_b32 m0, s51\n"
"M 16,160,164\nR 178,186,4096\nv_cvt_pk_bf16_f32 v128, v128, v129\nv_cvt_pk_bf16_f32 v129, v130, v131\nM 16,164,180\nds_write_addtid_b32 v128 offset:0\n"
"v_cvt_pk_bf16_f32 v130, v132, v133\nv_cvt_pk_bf16_f32 v131, v134, v135\nv_mfma_f32_16x16x32_bf16 a[224:227], v[160:163], a[168:171], a[224:227]\nR 180,186,8192\nv_cvt_pk_bf16_f32 v132, v136, v137\nv_mfma_f32_16x16x32_bf16 a[224:227], v[164:167], a[184:187], a[224:227]\n"
"ds_write_addtid_b32 v129 offset:256\nv_cvt_pk_bf16_f32 v133, v138, v139\nv_mfma_f32_16x16x32_bf16 a[240:243], v[160:163], a[172:175], a[240:243]\nR 182,186,12288\nv_cvt_pk_bf16_f32 v134, v140, v141\nv_mfma_f32_16x16x32_bf16 a[240:243], v[164:167], a[188:191], a[240:243]\n"
"ds_write_addtid_b32 v130 offset:512\nG 7\nv_cvt_pk_bf16_f32 v135, v142, v143\nv_mfma_f32_16x16x32_bf16 a[244:247], v[168:171], a[172:175], a[244:247]\nR 160,187,0\nv_cvt_pk_bf16_f32 v136, v144, v145\n"
"v_mfma_f32_16x16x32_bf16 a[244:247], v[172:175], a[188:191], a[244:247]\nds_write_addtid_b32 v131 offset:768\nv_cvt_pk_bf16_f32 v137, v146, v147\nv_mfma_f32_16x16x32_bf16 a[228:231], v[168:171], a[168:171], a[228:231]\nR 162,187,4096\nv_cvt_pk_bf16_f32 v138, v148, v149\n"
"v_mfma_f32_16x16x32_bf16 a[228:231], v[172:175], a[184:187], a[228:231]\nds_write_addtid_b32 v132 offset:1024\nv_cvt_pk_bf16_f32 v139, v150, v151\nM 20,168,164\nR 164,187,8192\nv_cvt_pk_bf16_f32 v140, v152, v153\n"
"M 20,172,180\nds_write_addtid_b32 v133 offset:1280\nv_cvt_pk_bf16_f32 v141, v154, v155\nM 4,168,160\nR 166,187,12288\nv_cvt_pk_bf16_f32 v142, v156, v157\n"
"M 4,172,176\nds_write_addtid_b32 v134 offset:1536\nG 9\nv_cvt_pk_bf16_f32 v143, v158, v159\nM 8,176,160\nR 168,184,16384\n"
"ds_write_addtid_b32 v135 offset:1792\nM 8,180,176\nds_write_addtid_b32 v136 offset:2048\nds_write_addtid_b32 v137 offset:2304\nM 24,176,164\nR 170,184,20480\n"
"ds_write_addtid_b32 v138 offset:2560\nM 24,180,180\nds_write_addtid_b32 v139 offset:2816\nds_write_addtid_b32 v140 offset:3072\nv_mfma_f32_16x16x32_bf16 a[232:235], v[176:179], a[168:171], a[232:235]\nR 172,184,24576\n"
"ds_write_addtid_b32 v141 offset:3328\nv_mfma_f32_16x16x32_bf16 a[232:235], v[180:183], a[184:187], a[232:235]\nds_write_addtid_b32 v142 offset:3584\nds_write_addtid_b32 v143 offset:3840\nv_mfma_f32_16x16x32_bf16 a[248:251], v[176:179], a[172:175], a[248:251]\nR 174,184,28672\n"
"s_add_u32 m0, s50, 73728\nv_mfma_f32_16x16x32_bf16 a[248:251], v[180:183], a[188:191], a[248:251]\ns_add_u32 s63, s59, 524288\nG 14\nv_mfma_f32_16x16x32_bf16 a[252:255], v[160:163], a[172:175], a[252:255]\nR 176,185,16384\n"
"s_waitcnt vmcnt(8) lgkmcnt(2)\ns_barrier\nv_mfma_f32_16x16x32_bf16 a[252:255], v[164:167], a[188:191], a[252:255]\nbuffer_load_dwordx4 v220, s[16:19], s63 offen lds\nv_mfma_f32_16x16x32_bf16 a[236:239], v[160:163], a[168:171], a[236:239]\nR 178,185,20480\n"
"v_mfma_f32_16x16x32_bf16 a[236:239], v[164:167], a[184:187], a[236:239]\nds_read2st64_b32 v[128:129], v225 offset0:0 offset1:1\nM 28,160,164\nR 180,185,24576\nM 28,164,180\nbuffer_load_dwordx4 v221, s[16:19], s63 offen offset:1024 lds\n"
"M 12,160,160\nR 182,185,28672\ns_add_u32 m0, s50, 81920\nM 12,164,176\nds_read2st64_b32 v[130:131], v225 offset0:2 offset1:3\nG 6\n"
"M 0,168,192\nR 160,186,16384\ns_add_u32 s63, s59, 65536\nM 0,172,208\nbuffer_load_dwordx4 v220, s[16:19], s63 offen lds\nds_read2st64_b32 v[132:133], v225 offset0:4 offset1:5\n"
"M 16,168,196\nR 162,186,20480\nds_read2st64_b32 v[134:135], v225 offset0:6 offset1:7\nM 16,172,212\nds_read2st64_b32 v[136:137], v225 offset0:8 offset1:9\nv_mfma_f32_16x16x32_bf16 a[224:227], v[168:171], a[200:203], a[224:227]\n"
"R 164,186,24576\nv_mfma_f32_16x16x32_bf16 a[224:227], v[172:175], a[216:219], a[224:227]\nbuffer_load_dwordx4 v221, s[16:19], s63 offen offset:1024 lds\nG 6\nv_mfma_f32_16x16x32_bf16 v[96:99], v[208:211], v[128:131], v[96:99]\nv_mfma_f32_16x16x32_bf16 v[100:103], v[210:213], v[128:131], v[100:103]\n"
"ds_read2st64_b32 v[138:139], v225 offset0:10 offset1:11\nv_mfma_f32_16x16x32_bf16 a[240:243], v[168:171], a[204:207], a[240:243]\nR 166,186,28672\ns_add_u32 m0, s50, 90112\nv_mfma_f32_16x16x32_bf16 a[240:243], v[172:175], a[220:223], a[240:243]\nds_read2st64_b32 v[140:141], v225 offset0:12 offset1:13\n"
"v_mfma_f32_16x16x32_bf16 a[244:247], v[176:179], a[204:207], a[244:247]\nR 168,187,16384\ns_add_u32 s63, s59, 589824\nG 6\nv_mfma_f32_16x16x32_bf16 v[104:107], v[208:211], v[132:135], v[104:107]\nv_mfma_f32_16x16x32_bf16 v[108:111], v[210:213], v[132:135], v[108:111]\n"
"ds_read2st64_b32 v[142:143], v225 offset0:14 offset1:15\nv_mfma_f32_16x16x32_bf16 a[244:247], v[180:183], a[220:223], a[244:247]\nbuffer_load_dwordx4 v220, s[16:19], s63 offen lds\nv_mfma_f32_16x16x32_bf16 a[228:231], v[176:179], a[200:203], a[228:231]\nR 170,187,20480\nv_mfma_f32_16x16x32_bf16 a[228:231], v[180:183], a[216:219], a[228:231]\n"
"G 5\nv_mfma_f32_16x16x32_bf16 v[112:115], v[208:211], v[136:139], v[112:115]\nv_mfma_f32_16x16x32_bf16 v[116:119], v[210:213], v[136:139], v[116:119]\nM 20,176,196\nR 172,187,24576\nM 20,180,212\n"
"buffer_load_dwordx4 v221, s[16:19], s63 offen offset:1024 lds\nM 4,176,192\nR 174,187,28672\ns_add_u32 s63, s59, 131072\nG 3\nv_mfma_f32_16x16x32_bf16 v[120:123], v[208:211], v[140:143], v[120:123]\n"
"v_mfma_f32_16x16x32_bf16 v[124:127], v[210:213], v[140:143], v[124:127]\nM 4,180,208\nM 8,160,192\nR 176,184,32768\nM 8,164,208\ns_add_u32 m0, s50, 98304\n"
"M 24,160,196\nR 178,184,36864\nM 24,164,212\nbuffer_load_dwordx4 v220, s[16:19], s63 offen lds\nv_mfma_f32_16x16x32_bf16 a[232:235], v[160:163], a[200:203], a[232:235]\nR 180,184,40960\n"
"v_cvt_pk_bf16_f32 v120, v120, v121\nv_cvt_pk_bf16_f32 v112, v112, v113\nv_cvt_pk_bf16_f32 v121, v122, v123\nv_mfma_f32_16x16x32_bf16 a[232:235], v[164:167], a[216:219], a[232:235]\nv_cvt_pk_bf16_f32 v122, v124, v125\nv_cvt_pk_bf16_f32 v123, v126, v127\n"
"v_cvt_pk_bf16_f32 v96, v96, v97\nv_cvt_pk_bf16_f32 v113, v114, v115\nv_mfma_f32_16x16x32_bf16 a[248:251], v[160:163], a[204:207], a[248:251]\nR 182,184,45056\nv_cvt_pk_bf16_f32 v114, v116, v117\nv_cvt_pk_bf16_f32 v97, v98, v99\n"
"v_mfma_f32_16x16x32_bf16 a[248:251], v[164:167], a[220:223], a[248:251]\nG 4\nv_cvt_pk_bf16_f32 v98, v100, v101\nv_cvt_pk_bf16_f32 v99, v102, v103\nv_mfma_f32_16x16x32_bf16 a[252:255], v[168:171], a[204:207], a[252:255]\nR 160,185,32768\n"
"s_bfe_u32 s100, s56, 0x20007\ns_mul_i32 s100, s100, 49152\ns_add_u32 s100, s100, s72\ns_add_u32 s100, s100, 65011712\nv_cvt_pk_bf16_f32 v115, v118, v119\nv_mfma_f32_16x16x32_bf16 a[252:255], v[172:175], a[220:223], a[252:255]\n"
"buffer_store_dwordx4 v[96:99], v252, s[20:23], s100 offen offset:0 sc1\nv_mfma_f32_16x16x32_bf16 a[236:239], v[168:171], a[200:203], a[236:239]\nR 162,185,36864\nv_cvt_pk_bf16_f32 v104, v104, v105\nv_mfma_f32_16x16x32_bf16 a[236:239], v[172:175], a[216:219], a[236:239]\nv_cvt_pk_bf16_f32 v105, v106, v107\n"
"v_cvt_pk_bf16_f32 v106, v108, v109\nM 28,168,196\nR 164,185,40960\nv_cvt_pk_bf16_f32 v107, v110, v111\nM 28,172,212\nbuffer_load_dwordx4 v221, s[16:19], s63 offen offset:1024 lds\n"
"M 12,168,192\nR 166,185,45056\nM 12,172,208\nbuffer_store_dwordx4 v[104:107], v252, s[20:23], s100 offen offset:1024 sc1\ns_waitcnt vmcnt(12)\ns_barrier\n"
"G 4\nM 32,176,160\nR 168,186,32768\nM 32,180,176\nv_xor_b32 v240, 0x2000, v225\nM 48,176,164\n"
"R 170,186,36864\nds_write2st64_b32 v240, a224, a225 offset0:0 offset1:1\nM 48,180,180\nbuffer_store_dwordx4 v[112:115], v252, s[20:23], s100 offen offset:2048 sc1\nds_write2st64_b32 v240, a226, a227 offset0:2 offset1:3\nM 64,176,168\n"
"R 172,186,40960\nds_write2st64_b32 v240, a228, a229 offset0:4 offset1:5\nM 64,180,184\ns_add_u32 m0, s50, 106496\nds_write2st64_b32 v240, a230, a231 offset0:6 offset1:7\nM 80,176,172\n"
"R 174,186,45056\ns_add_u32 s63, s59, 655360\nds_write2st64_b32 v240, a232, a233 offset0:8 offset1:9\nM 80,180,188\nds_write2st64_b32 v240, a234, a235 offset0:10 offset1:11\nG 10\n"
"M 84,160,172\nR 176,187,32768\nds_write2st64_b32 v240, a236, a237 offset0:12 offset1:13\nM 84,164,188\nbuffer_store_dwordx4 v[120:123], v252, s[20:23], s100 offen offset:3072 sc1\nds_write2st64_b32 v240, a238, a239 offset0:14 offset1:15\n"
"M 68,160,168\nR 178,187,36864\nv_mfma_f32_16x16x32_bf16 v[96:99], v[208:211], v[192:195], 0\nds_write2st64_b32 v240, a240, a241 offset0:16 offset1:17\nM 68,164,184\nv_mfma_f32_16x16x32_bf16 v[100:103], v[210:213], v[192:195], 0\n"
"buffer_load_dwordx4 v220, s[16:19], s63 offen lds\nds_write2st64_b32 v240, a242, a243 offset0:18 offset1:19\nM 52,160,164\nR 180,187,40960\nv_mfma_f32_16x16x32_bf16 v[104:107], v[208:211], v[196:199], 0\nds_write2st64_b32 v240, a244, a245 offset0:20 offset1:21\n"
"M 52,164,180\nv_mfma_f32_16x16x32_bf16 v[108:111], v[210:213], v[196:199], 0\nds_write2st64_b32 v240, a246, a247 offset0:22 offset1:23\nM 36,160,160\nR 182,187,45056\nv_mfma_f32_16x16x32_bf16 v[112:115], v[208:211], v[200:203], 0\n"
"ds_write2st64_b32 v240, a248, a249 offset0:24 offset1:25\nM 36,164,176\nv_mfma_f32_16x16x32_bf16 v[116:119], v[210:213], v[200:203], 0\nds_write2st64_b32 v240, a250, a251 offset0:26 offset1:27\nG 14\nM 40,168,160\n"
"R 160,184,49152\nv_mfma_f32_16x16x32_bf16 v[120:123], v[208:211], v[204:207], 0\nds_write2st64_b32 v240, a252, a253 offset0:28 offset1:29\nM 40,172,176\nv_mfma_f32_16x16x32_bf16 v[124:127], v[210:213], v[204:207], 0\nbuffer_load_dwordx4 v221, s[16:19], s63 offen offset:1024 lds\n"
"ds_write2st64_b32 v240, a254, a255 offset0:30 offset1:31\nM 56,168,164\nR 162,184,53248\nv_mfma_f32_16x16x32_bf16 v[96:99], v[208:211], v[232:235], v[96:99]\ns_add_u32 m0, s50, 114688\ns_add_u32 s63, s59, 196608\n"
"M 56,172,180\nv_mfma_f32_16x16x32_bf16 v[100:103], v[210:213], v[232:235], v[100:103]\nbuffer_load_dwordx4 v220, s[16:19], s63 offen lds\nM 72,168,168\nR 164,184,57344\nv_mfma_f32_16x16x32_bf16 v[104:107], v[208:211], v[236:239], v[104:107]\n"
"M 72,172,184\nv_mfma_f32_16x16x32_bf16 v[108:111], v[210:213], v[236:239], v[108:111]\nbuffer_load_dwordx4 v221, s[16:19], s63 offen offset:1024 lds\nM 88,168,172\nR 166,184,61440\nv_mfma_f32_16x16x32_bf16 v[112:115], v[208:211], v[232:235], v[112:115]\n"
"s_add_u32 m0, s50, 0\nM 88,172,188\nv_mfma_f32_16x16x32_bf16 v[116:119], v[210:213], v[232:235], v[116:119]\ns_add_u32 s63, s59, 720896\nG 8\nM 92,176,172\n"
"R 168,185,49152\nv_mfma_f32_16x16x32_bf16 v[120:123], v[208:211], v[236:239], v[120:123]\ns_waitcnt vmcnt(14) lgkmcnt(4)\ns_barrier\nM 92,180,188\nv_mfma_f32_16x16x32_bf16 v[124:127], v[210:213], v[236:239], v[124:127]\n"
"buffer_load_dwordx4 v220, s[16:19], s63 offen lds\nds_read2st64_b32 v[128:129], v225 offset0:0 offset1:1\nM 76,176,168\nR 170,185,53248\nds_read2st64_b32 v[130:131], v225 offset0:2 offset1:3\nM 76,180,184\n"
"ds_read2st64_b32 v[132:133], v225 offset0:4 offset1:5\nds_read2st64_b32 v[134:135], v225 offset0:6 offset1:7\nM 60,176,164\nR 172,185,57344\nds_read2st64_b32 v[136:137], v225 offset0:8 offset1:9\nM 60,180,180\n"
"buffer_load_dwordx4 v221, s[16:19], s63 offen offset:1024 lds\nds_read2st64_b32 v[138:139], v225 offset0:10 offset1:11\nM 44,176,160\nR 174,185,61440\ns_add_u32 m0, s50, 8192\nds_read2st64_b32 v[140:141], v225 offset0:12 offset1:13\n"
"M 44,180,176\nds_read2st64_b32 v[142:143], v225 offset0:14 offset1:15\nds_read2st64_b32 v[144:145], v225 offset0:16 offset1:17\nG 13\nM 32,160,192\nR 176,186,49152\n"
"s_add_u32 s63, s59, 393216\nds_read2st64_b32 v[146:147], v225 offset0:18 offset1:19\nM 32,164,208\nbuffer_load_dwordx4 v220, s[16:19], s63 offen lds\nds_read2st64_b32 v[148:149], v225 offset0:20 offset1:21\nM 48,160,196\n"
"R 178,186,53248\nds_read2st64_b32 v[150:151], v225 offset0:22 offset1:23\nG 15\nv_add_f32 v0, v0, v128\nM 48,164,212\nds_read2st64_b32 v[152:153], v225 offset0:24 offset1:25\n"
"ds_read2st64_b32 v[154:155], v225 offset0:26 offset1:27\nv_add_f32 v1, v1, v129\nM 64,160,200\nR 180,186,57344\nds_read2st64_b32 v[156:157], v225 offset0:28 offset1:29\nG 15\n"
"v_add_f32 v2, v2, v130\nM 64,164,216\nbuffer_load_dwordx4 v221, s[16:19], s63 offen offset:1024 lds\nds_read2st64_b32 v[158:159], v225 offset0:30 offset1:31\nM 80,160,204\nR 182,186,61440\n"
"s_add_u32 m0, s50, 16384\nv_add_f32 v3, v3, v131\nM 80,164,220\nG 14\nv_add_f32 v4, v4, v132\nv_add_f32 v5, v5, v133\n"
"v_add_f32 v6, v6, v134\nM 84,168,204\nR 160,187,49152\ns_add_u32 s63, s59, 458752\nv_add_f32 v7, v7, v135\nM 84,172,220\n"
"buffer_load_dwordx4 v220, s[16:19], s63 offen lds\nv_add_f32 v8, v8, v136\nM 68,168,200\nR 162,187,53248\nv_add_f32 v9, v9, v137\nv_add_f32 v10, v10, v138\n"
"M 68,172,216\nv_add_f32 v11, v11, v139\nG 15\nv_add_f32 v12, v12, v140\nv_add_f32 v13, v13, v141\nM 52,168,196\n"
"R 164,187,57344\nG 15\nv_add_f32 v14, v14, v142\nv_add_f32 v15, v15, v143\nM 52,172,212\nbuffer_load_dwordx4 v221, s[16:19], s63 offen offset:1024 lds\n"
"G 14\nv_add_f32 v16, v16, v144\nM 36,168,192\nR 166,187,61440\ns_add_u32 s63, s59, 262144\nv_add_f32 v17, v17, v145\n"
"M 36,172,208\nG 4\nv_add_f32 v18, v18, v146\nv_add_f32 v19, v19, v147\nv_add_f32 v20, v20, v148\nM 40,176,192\n"
"R 168,188,0\nv_add_f32 v21, v21, v149\nv_add_f32 v22, v22, v150\nM 40,180,208\ns_add_u32 m0, s50, 24576\nv_add_f32 v23, v23, v151\n"
"v_add_f32 v24, v24, v152\nv_add_f32 v25, v25, v153\nM 56,176,196\nR 170,188,4096\nv_add_f32 v26, v26, v154\nv_add_f32 v27, v27, v155\n"
"M 56,180,212\nbuffer_load_dwordx4 v220, s[16:19], s63 offen lds\nv_add_f32 v28, v28, v156\nv_add_f32 v29, v29, v157\nv_add_f32 v30, v30, v158\nv_add_f32 v31, v31, v159\n"
"M 72,176,200\nR 172,188,8192\nv_cvt_pk_bf16_f32 v24, v24, v25\nv_cvt_pk_bf16_f32 v8, v8, v9\nv_cvt_pk_bf16_f32 v16, v16, v17\nv_cvt_pk_bf16_f32 v25, v26, v27\n"
"M 72,180,216\nv_cvt_pk_bf16_f32 v26, v28, v29\nv_cvt_pk_bf16_f32 v27, v30, v31\nv_cvt_pk_bf16_f32 v0, v0, v1\nv_cvt_pk_bf16_f32 v9, v10, v11\nv_cvt_pk_bf16_f32 v17, v18, v19\n"
"M 88,176,204\nR 174,188,12288\nv_cvt_pk_bf16_f32 v1, v2, v3\nM 88,180,220\nG 4\nv_cvt_pk_bf16_f32 v2, v4, v5\n"
"v_cvt_pk_bf16_f32 v3, v6, v7\nM 92,160,204\nR 176,189,0\ns_bfe_u32 s100, s57, 0x20007\ns_mul_i32 s100, s100, 49152\ns_add_u32 s100, s100, s72\n"
"s_add_u32 s100, s100, 65028096\nv_cvt_pk_bf16_f32 v10, v12, v13\nM 92,164,220\nbuffer_store_dwordx4 v[0:3], v252, s[20:23], s100 offen offset:0 sc1\nM 76,160,200\nR 178,189,4096\n"
"v_cvt_pk_bf16_f32 v11, v14, v15\nM 76,164,216\nbuffer_load_dwordx4 v221, s[16:19], s63 offen offset:1024 lds\nM 60,160,196\nR 180,189,8192\ns_add_u32 m0, s50, 32768\n"
"s_add_u32 s63, s59, 786432\nM 60,164,212\nbuffer_load_dwordx4 v220, s[16:19], s63 offen lds\nM 44,160,192\nR 182,189,12288\nv_cvt_pk_bf16_f32 v18, v20, v21\n"
"M 44,164,208\nbuffer_store_dwordx4 v[8:11], v252, s[20:23], s100 offen offset:1024 sc1\ns_waitcnt vmcnt(21)\ns_barrier\nG 4\nM 96,168,0\n"
"R 160,190,0\nv_cvt_pk_bf16_f32 v19, v22, v23\nM 96,172,16\ns_mov_b32 m0, s51\nM 112,168,4\nR 162,190,4096\n"
"v_cvt_pk_bf16_f32 v64, v64, v65\nv_cvt_pk_bf16_f32 v65, v66, v67\nM 112,172,20\nbuffer_store_dwordx4 v[16:19], v252, s[20:23], s100 offen offset:2048 sc1\nv_cvt_pk_bf16_f32 v66, v68, v69\nv_mfma_f32_16x16x32_bf16 v[128:131], v[168:171], a[8:11], 0\n"
"R 164,190,8192\nv_cvt_pk_bf16_f32 v67, v70, v71\nv_cvt_pk_bf16_f32 v68, v72, v73\nM 128,172,24\nds_write_addtid_b32 v64 offset:0\nv_cvt_pk_bf16_f32 v69, v74, v75\n"
"v_cvt_pk_bf16_f32 v70, v76, v77\nv_mfma_f32_16x16x32_bf16 v[144:147], v[168:171], a[12:15], 0\nR 166,190,12288\nv_cvt_pk_bf16_f32 v71, v78, v79\nLpg32:\ns_cbranch_execz LretO1n\n"
"v_cvt_pk_bf16_f32 v72, v80, v81\nM 144,172,28\nds_write_addtid_b32 v65 offset:256\nG 6\nv_cvt_pk_bf16_f32 v73, v82, v83\nv_mfma_f32_16x16x32_bf16 v[148:151], v[176:179], a[12:15], 0\n"
"R 168,191,0\nv_cvt_pk_bf16_f32 v74, v84, v85\nv_cvt_pk_bf16_f32 v75, v86, v87\nM 148,180,28\nbuffer_store_dwordx4 v[24:27], v252, s[20:23], s100 offen offset:3072 sc1\nv_mfma_f32_16x16x32_bf16 v[132:135], v[176:179], a[8:11], 0\n"
"R 170,191,4096\nv_cvt_pk_bf16_f32 v76, v88, v89\nM 132,180,24\nds_write_addtid_b32 v66 offset:512\nv_cvt_pk_bf16_f32 v77, v90, v91\nM 116,176,4\n"
"R 172,191,8192\nv_cvt_pk_bf16_f32 v78, v92, v93\nM 116,180,20\nds_write_addtid_b32 v67 offset:768\nv_cvt_pk_bf16_f32 v79, v94, v95\nM 100,176,0\n"
"R 174,191,12288\nds_write_addtid_b32 v68 offset:1024\nM 100,180,16\nds_write_addtid_b32 v69 offset:1280\nds_write_addtid_b32 v70 offset:1536\nG 10\n"
"M 104,160,0\nR 176,188,16384\nds_write_addtid_b32 v71 offset:1792\nM 104,164,16\nds_write_addtid_b32 v72 offset:2048\nds_write_addtid_b32 v73 offset:2304\n"
"M 120,160,4\nR 178,188,20480\nds_write_addtid_b32 v74 offset:2560\nM 120,164,20\nds_write_addtid_b32 v75 offset:2816\nds_write_addtid_b32 v76 offset:3072\n"
"v_mfma_f32_16x16x32_bf16 v[136:139], v[160:163], a[8:11], 0\nR 180,188,24576\nds_write_addtid_b32 v77 offset:3328\nM 136,164,24\nds_write_addtid_b32 v78 offset:3584\nds_write_addtid_b32 v79 offset:3840\n"
"v_mfma_f32_16x16x32_bf16 v[152:155], v[160:163], a[12:15], 0\nR 182,188,28672\nM 152,164,28\nG 15\nv_mfma_f32_16x16x32_bf16 v[156:159], v[168:171], a[12:15], 0\nR 160,189,16384\n"
"s_waitcnt vmcnt(15) lgkmcnt(2)\ns_barrier\nM 156,172,28\nds_read2st64_b32 v[64:65], v225 offset0:0 offset1:1\nv_mfma_f32_16x16x32_bf16 v[140:143], v[168:171], a[8:11], 0\nR 162,189,20480\n"
"M 140,172,24\nds_read2st64_b32 v[66:67], v225 offset0:2 offset1:3\nM 124,168,4\nR 164,189,24576\nM 124,172,20\nds_read2st64_b32 v[68:69], v225 offset0:4 offset1:5\n"
"M 108,168,0\nR 166,189,28672\nM 108,172,16\nds_read2st64_b32 v[70:71], v225 offset0:6 offset1:7\nG 8\nM 96,176,32\n"
"R 168,190,16384\nv_bfe_u32 v250, v252, 4, 2\nv_lshlrev_b32 v250, 3, v250\nM 96,180,48\ns_lshl_b32 s60, s43, 5\nv_add_u32 v250, s60, v250\n"
"G 5\nv_mfma_f32_16x16x32_bf16 v[32:35], v[208:211], v[64:67], v[32:35]\nv_mfma_f32_16x16x32_bf16 v[36:39], v[210:213], v[64:67], v[36:39]\nM 112,176,36\nR 170,190,20480\nM 112,180,52\n"
"ds_read2st64_b32 v[72:73], v225 offset0:8 offset1:9\nM 128,176,40\nR 172,190,24576\nM 128,180,56\nds_read2st64_b32 v[74:75], v225 offset0:10 offset1:11\nG 5\n"
"v_mfma_f32_16x16x32_bf16 v[40:43], v[208:211], v[68:71], v[40:43]\nv_mfma_f32_16x16x32_bf16 v[44:47], v[210:213], v[68:71], v[44:47]\nM 144,176,44\nR 174,190,28672\nM 144,180,60\nds_read2st64_b32 v[76:77], v225 offset0:12 offset1:13\n"
"M 148,160,44\nR 176,191,16384\nds_read2st64_b32 v[78:79], v225 offset0:14 offset1:15\nM 148,164,60\nM 132,160,40\nR 178,191,20480\n"
"M 132,164,56\nG 5\nv_mfma_f32_16x16x32_bf16 v[48:51], v[208:211], v[72:75], v[48:51]\nv_mfma_f32_16x16x32_bf16 v[52:55], v[210:213], v[72:75], v[52:55]\nM 116,160,36\nR 180,191,24576\n"
"M 116,164,52\nM 100,160,32\nR 182,191,28672\nG 3\nv_mfma_f32_16x16x32_bf16 v[56:59], v[208:211], v[76:79], v[56:59]\nv_mfma_f32_16x16x32_bf16 v[60:63], v[210:213], v[76:79], v[60:63]\n"
"M 100,164,48\nM 104,168,32\nR 160,188,32768\nM 104,172,48\nM 120,168,36\nR 162,188,36864\n"
"v_mov_b32 v226, s46\nM 120,172,52\ns_add_u32 m0, s50, 32768\nv_mov_b32 v227, s47\nM 136,168,40\nR 164,188,40960\n"
"s_bfe_u32 s62, s56, 0x20007\ns_lshl_b32 s62, s62, 5\ns_add_u32 s61, s74, 1009664\ns_add_u32 s61, s61, s62\ns_waitcnt vmcnt(0)\ns_mov_b64 exec, 1\n"
"buffer_atomic_swap_x2 v[226:227], off, s[20:23], s61 sc1\ns_mov_b64 exec, -1\nv_cvt_pk_bf16_f32 v32, v32, v33\nv_cvt_pk_bf16_f32 v48, v48, v49\nv_cvt_pk_bf16_f32 v56, v56, v57\nM 136,172,56\n"
"s_bfe_u32 s62, s57, 0x20007\ns_lshl_b32 s62, s62, 5\ns_add_u32 s61, s74, 1009672\ns_add_u32 s61, s61, s62\ns_mov_b64 exec, 1\nbuffer_atomic_swap_x2 v[226:227], off, s[20:23], s61 sc1\n"
"s_mov_b64 exec, -1\nv_cvt_pk_bf16_f32 v49, v50, v51\nM 152,168,44\nR 166,188,45056\nv_cvt_pk_bf16_f32 v50, v52, v53\nv_cvt_pk_bf16_f32 v33, v34, v35\n"
"M 152,172,60\nG 4\nv_cvt_pk_bf16_f32 v34, v36, v37\nv_cvt_pk_bf16_f32 v35, v38, v39\nM 156,176,44\nR 168,189,32768\n"
"s_bfe_u32 s100, s58, 0x20007\ns_mul_i32 s100, s100, 49152\ns_add_u32 s100, s100, s72\ns_add_u32 s100, s100, 65044480\nv_cvt_pk_bf16_f32 v40, v40, v41\nM 156,180,60\n"
"buffer_store_dwordx4 v[32:35], v252, s[20:23], s100 offen offset:0 sc1\nbuffer_load_dwordx4 v221, s[16:19], s63 offen offset:1024 lds\nM 140,176,40\nR 170,189,36864\ns_add_u32 m0, s50, 40960\ns_add_u32 s63, s59, 327680\n"
"M 140,180,56\nbuffer_load_dwordx4 v220, s[16:19], s63 offen lds\nM 124,176,36\nR 172,189,40960\nv_cvt_pk_bf16_f32 v41, v42, v43\nM 124,180,52\n"
"v_cvt_pk_bf16_f32 v42, v44, v45\nv_cvt_pk_bf16_f32 v43, v46, v47\nM 108,176,32\nR 174,189,45056\nv_cvt_pk_bf16_f32 v51, v54, v55\nM 108,180,48\n"
"buffer_store_dwordx4 v[40:43], v252, s[20:23], s100 offen offset:1024 sc1\ns_barrier\nG 4\nM 96,160,64\nR 176,190,32768\nv_cvt_pk_bf16_f32 v57, v58, v59\n"
"M 96,164,80\nbuffer_load_dwordx4 v221, s[16:19], s63 offen offset:1024 lds\nM 112,160,68\nR 178,190,36864\ns_add_u32 m0, s50, 49152\nM 112,164,84\n"
"buffer_store_dwordx4 v[48:51], v252, s[20:23], s100 offen offset:2048 sc1\nM 128,160,72\nR 180,190,40960\ns_add_u32 s63, s59, 851968\nM 128,164,88\nbuffer_load_dwordx4 v220, s[16:19], s63 offen lds\n"
"M 144,160,76\nR 182,190,45056\nv_cvt_pk_bf16_f32 v58, v60, v61\nM 144,164,92\nG 4\nv_cvt_pk_bf16_f32 v59, v62, v63\n"
"M 148,168,76\nR 160,191,32768\nM 148,172,92\nbuffer_store_dwordx4 v[56:59], v252, s[20:23], s100 offen offset:3072 sc1\nbuffer_load_dwordx4 v221, s[16:19], s63 offen offset:1024 lds\nM 132,168,72\n"
"R 162,191,36864\nM 132,172,88\nM 116,168,68\nR 164,191,40960\nM 116,172,84\nM 100,168,64\n"
"R 166,191,45056\nv_mov_b32 v248, 0\nM 100,172,80\nG 4\nv_mov_b32 v249, 0\nM 104,176,64\n"
"R 168,188,49152\ns_add_u32 s61, s74, 1009664\ns_mov_b64 exec, 3\nbuffer_atomic_or_x2 v[248:249], v250, s[20:23], s61 offen sc0 sc1\ns_mov_b64 exec, -1\nM 104,180,80\n"
"M 120,176,68\nR 170,188,53248\nM 120,180,84\nM 136,176,72\nR 172,184,0\nM 136,180,88\n"
"M 152,176,76\nR 174,184,4096\nM 152,180,92\nG 4\nM 156,160,76\nR 176,189,49152\n"
"M 156,164,92\nM 140,160,72\nR 178,189,53248\nM 140,164,88\nM 124,160,68\nR 180,185,0\n"
"M 124,164,84\nM 108,160,64\nR 182,185,4096\nM 108,164,80\nG 4\nM 96,168,96\n"
"R 160,190,49152\nM 96,172,112\nM 112,168,100\nR 162,190,53248\nM 112,172,116\nM 128,168,104\n"
"R 164,186,0\nM 128,172,120\nM 144,168,108\nR 166,186,4096\nM 144,172,124\nG 4\n"
"M 148,176,108\nR 168,191,49152\nM 148,180,124\nM 132,176,104\nR 170,191,53248\nM 132,180,120\n"
"M 116,176,100\nR 172,187,0\nM 116,180,116\nM 100,176,96\nR 174,187,4096\nM 100,180,112\n"
"G 4\nM 104,160,96\nR 176,184,8192\ns_waitcnt vmcnt(8)\ns_barrier\nM 104,164,112\n"
"M 120,160,100\nR 178,184,12288\nM 120,164,116\nM 136,160,104\nR 180,184,16384\nM 136,164,120\n"
"M 152,160,108\nR 182,184,20480\nM 152,164,124\nG 4\nM 156,168,108\nR 160,185,8192\n"
"M 156,172,124\nM 140,168,104\nR 162,185,12288\nM 140,172,120\nM 124,168,100\nR 164,185,16384\n"
"M 124,172,116\nM 108,168,96\nR 166,185,20480\nv_mov_b32 v226, s46\nM 108,172,112\nG 4\n"
"v_mov_b32 v227, s47\nM 96,176,128\nR 168,186,8192\ns_bfe_u32 s62, s58, 0x20007\ns_lshl_b32 s62, s62, 5\ns_add_u32 s61, s74, 1009680\n"
"s_add_u32 s61, s61, s62\ns_waitcnt vmcnt(0)\ns_mov_b64 exec, 1\nbuffer_atomic_swap_x2 v[226:227], off, s[20:23], s61 sc1\ns_mov_b64 exec, -1\nM 96,180,144\n"
"s_mul_i32 s64, s43, 49152\nM 112,176,132\nR 170,186,12288\nM 112,180,148\ns_add_u32 s64, s64, s72\nM 128,176,136\n"
"R 172,186,16384\nv_cmp_eq_u64 s[86:87], s[46:47], v[248:249]\nM 128,180,152\ns_add_u32 s64, s64, 65011712\nM 144,176,140\nR 174,186,20480\n"
"s_and_b32 s62, s86, 3\ns_cmp_eq_u32 s62, 3\ns_cbranch_scc0 Lq981\nLq982:\nM 144,180,156\nG 4\n"
"M 148,160,140\nR 176,187,8192\nM 148,164,156\nbuffer_load_dwordx4 v[0:3], v252, s[20:23], s64 offen offset:0 sc1\nM 132,160,136\nR 178,187,12288\n"
"M 132,164,152\nbuffer_load_dwordx4 v[4:7], v252, s[20:23], s64 offen offset:1024 sc1\nM 116,160,132\nR 180,187,16384\nM 116,164,148\ns_mul_i32 s65, s43, 49152\n"
"M 100,160,128\nR 182,187,20480\nM 100,164,144\nbuffer_load_dwordx4 v[8:11], v252, s[20:23], s64 offen offset:2048 sc1\nG 4\nM 104,168,128\n"
"R 160,184,24576\nM 104,172,144\nbuffer_load_dwordx4 v[12:15], v252, s[20:23], s64 offen offset:3072 sc1\nM 120,168,132\nR 162,184,28672\nM 120,172,148\n"
"s_add_u32 s65, s65, s72\nM 136,168,136\nR 164,184,32768\ns_add_u32 s65, s65, 65028096\nM 136,172,152\nbuffer_load_dwordx4 v[16:19], v252, s[20:23], s65 offen offset:0 sc1\n"
"M 152,168,140\nR 166,184,36864\nM 152,172,156\nG 4\nM 156,176,140\nR 168,185,24576\n"
"s_barrier\nM 156,180,156\nbuffer_load_dwordx4 v[20:23], v252, s[20:23], s65 offen offset:1024 sc1\nM 140,176,136\nR 170,185,28672\nv_mov_b32 v230, 0\n"
"M 140,180,152\nbuffer_load_dwordx4 v[24:27], v252, s[20:23], s65 offen offset:2048 sc1\nv_mov_b32 v231, 0\nM 124,176,132\nR 172,185,32768\ns_add_u32 s61, s74, 1009664\n"
"s_mov_b64 exec, 4\nbuffer_atomic_or_x2 v[230:231], v250, s[20:23], s61 offen sc0 sc1\ns_mov_b64 exec, -1\nM 124,180,148\nbuffer_load_dwordx4 v[28:31], v252, s[20:23], s65 offen offset:3072 sc1\nM 108,176,128\n"
"R 174,185,36864\nM 108,180,144\nG 4\nM 128,160,168\nR 176,186,24576\nM 128,164,184\n"
"R 178,186,28672\nM 144,160,172\nR 180,186,32768\nM 144,164,188\nR 182,186,36864\nG 4\n"
"M 148,168,172\nR 160,187,24576\nM 148,172,188\nR 162,187,28672\nM 132,168,168\nR 164,187,32768\n"
"M 132,172,184\nR 166,187,36864\nG 4\nM 136,176,168\nR 168,184,40960\nM 136,180,184\n"
"R 170,184,45056\nM 152,176,172\nR 172,184,49152\nM 152,180,188\nR 174,184,53248\nG 4\n"
"M 156,160,172\nR 176,185,40960\nM 156,164,188\nR 178,185,45056\nM 140,160,168\nR 180,185,49152\n"
"M 140,164,184\nR 182,185,53248\nG 4\nM 128,168,200\nR 160,186,40960\nM 128,172,216\n"
"R 162,186,45056\nM 144,168,204\nR 164,186,49152\nM 144,172,220\nR 166,186,53248\nG 4\n"
"M 148,176,204\nR 168,187,40960\nM 148,180,220\nR 170,187,45056\nM 132,176,200\nR 172,187,49152\n"
"M 132,180,216\nR 174,187,53248\nG 4\nM 136,160,200\nR 176,184,24576\nM 136,164,216\n"
"R 178,184,28672\nM 152,160,204\nR 180,184,32768\nM 152,164,220\nR 182,184,36864\nG 4\n"
"M 156,168,204\nR 160,185,24576\nM 156,172,220\nR 162,185,28672\nM 140,168,200\nR 164,185,32768\n"
"M 140,172,216\nR 166,185,36864\nG 4\nM 96,176,160\nR 168,186,24576\nM 96,180,176\n"
"R 170,186,28672\ns_mov_b32 m0, s51\nM 112,176,164\nR 172,186,32768\nv_cvt_pk_bf16_f32 v128, v128, v129\nv_cvt_pk_bf16_f32 v129, v130, v131\n"
"M 112,180,180\nR 174,186,36864\nG 4\nv_cvt_pk_bf16_f32 v130, v132, v133\nM 116,160,164\nR 176,187,24576\n"
"s_mul_i32 s66, s43, 49152\nv_cvt_pk_bf16_f32 v131, v134, v135\nM 116,164,180\nR 178,187,28672\ns_add_u32 s66, s66, s72\nv_cvt_pk_bf16_f32 v132, v136, v137\n"
"M 100,160,160\nR 180,187,32768\ns_add_u32 s66, s66, 65044480\nv_cvt_pk_bf16_f32 v133, v138, v139\nM 100,164,176\nR 182,187,36864\n"
"G 4\nv_cvt_pk_bf16_f32 v134, v140, v141\nM 104,168,160\nR 160,184,40960\ns_waitcnt vmcnt(1)\nv_cmp_eq_u64 s[86:87], s[46:47], v[230:231]\n"
"v_cvt_pk_bf16_f32 v135, v142, v143\nM 104,172,176\nR 162,184,45056\nv_cvt_pk_bf16_f32 v136, v144, v145\nv_cvt_pk_bf16_f32 v137, v146, v147\nM 120,168,164\n"
"R 164,184,49152\ns_and_b32 s62, s86, 4\ns_cmp_eq_u32 s62, 4\ns_cbranch_scc0 Lq997\nLq998:\nv_cvt_pk_bf16_f32 v138, v148, v149\n"
"M 120,172,180\nR 166,184,53248\nG 4\nv_cvt_pk_bf16_f32 v139, v150, v151\nM 124,176,164\nR 168,185,40960\n"
"buffer_load_dwordx4 v[64:67], v252, s[20:23], s66 offen offset:0 sc1\nv_cvt_pk_bf16_f32 v140, v152, v153\nM 124,180,180\nR 170,185,45056\nv_cvt_pk_bf16_f32 v141, v154, v155\nv_cvt_pk_bf16_f32 v142, v156, v157\n"
"M 108,176,160\nR 172,185,49152\nbuffer_load_dwordx4 v[68:71], v252, s[20:23], s66 offen offset:1024 sc1\nds_write_addtid_b32 v128 offset:0\nds_write_addtid_b32 v129 offset:256\nds_write_addtid_b32 v130 offset:512\n"
"ds_write_addtid_b32 v131 offset:768\nds_write_addtid_b32 v132 offset:1024\nds_write_addtid_b32 v133 offset:1280\nds_write_addtid_b32 v134 offset:1536\nds_write_addtid_b32 v135 offset:1792\nds_write_addtid_b32 v136 offset:2048\n"
"ds_write_addtid_b32 v137 offset:2304\nds_write_addtid_b32 v138 offset:2560\nds_write_addtid_b32 v139 offset:2816\nds_write_addtid_b32 v140 offset:3072\nds_write_addtid_b32 v141 offset:3328\nds_write_addtid_b32 v142 offset:3584\n"
"v_cvt_pk_bf16_f32 v143, v158, v159\nds_write_addtid_b32 v143 offset:3840\nM 108,180,176\nR 174,185,53248\nG 15\nM 96,160,192\n"
"R 176,186,40960\nM 96,164,208\nR 178,186,45056\nbuffer_load_dwordx4 v[72:75], v252, s[20:23], s66 offen offset:2048 sc1\nG 3\ns_barrier\n"
"M 112,160,196\nR 180,186,49152\nds_read2st64_b32 v[128:129], v225 offset0:0 offset1:1\nLpg33:\ns_cbranch_execz LretO1n\nM 112,164,212\n"
"R 182,186,53248\nds_read2st64_b32 v[130:131], v225 offset0:2 offset1:3\nG 6\nM 116,168,196\nR 160,187,40960\nbuffer_load_dwordx4 v[76:79], v252, s[20:23], s66 offen offset:3072 sc1\n"
"ds_read2st64_b32 v[132:133], v225 offset0:4 offset1:5\nM 116,172,212\nR 162,187,45056\nds_read2st64_b32 v[134:135], v225 offset0:6 offset1:7\nM 100,168,192\nR 164,187,49152\n"
"ds_read2st64_b32 v[136:137], v225 offset0:8 offset1:9\nM 100,172,208\nR 166,187,53248\nds_read2st64_b32 v[138:139], v225 offset0:10 offset1:11\nG 9\nM 104,176,192\n"
"ds_read2st64_b32 v[140:141], v225 offset0:12 offset1:13\nds_read2st64_b32 v[142:143], v225 offset0:14 offset1:15\nM 104,180,208\nM 120,176,196\nM 120,180,212\nG 3\n"
"M 124,160,196\nM 124,164,212\nM 108,160,192\nM 108,164,208\nv_mfma_f32_16x16x32_bf16 v[96:99], v[208:211], v[128:131], v[96:99]\nv_mfma_f32_16x16x32_bf16 v[100:103], v[210:213], v[128:131], v[100:103]\n"
"v_mfma_f32_16x16x32_bf16 v[104:107], v[208:211], v[132:135], v[104:107]\nv_mfma_f32_16x16x32_bf16 v[108:111], v[210:213], v[132:135], v[108:111]\nG 2\nv_mfma_f32_16x16x32_bf16 v[112:115], v[208:211], v[136:139], v[112:115]\nv_mfma_f32_16x16x32_bf16 v[116:119], v[210:213], v[136:139], v[116:119]\nG 0\n"
"v_mfma_f32_16x16x32_bf16 v[120:123], v[208:211], v[140:143], v[120:123]\nv_mfma_f32_16x16x32_bf16 v[124:127], v[210:213], v[140:143], v[124:127]\nv_mfma_f32_16x16x32_bf16 v[96:99], v[208:211], v[0:3], v[96:99]\nv_mfma_f32_16x16x32_bf16 v[100:103], v[210:213], v[0:3], v[100:103]\nv_mfma_f32_16x16x32_bf16 v[104:107], v[208:211], v[4:7], v[104:107]\nv_mfma_f32_16x16x32_bf16 v[108:111], v[210:213], v[4:7], v[108:111]\n"
"v_mfma_f32_16x16x32_bf16 v[112:115], v[208:211], v[8:11], v[112:115]\nv_mfma_f32_16x16x32_bf16 v[116:119], v[210:213], v[8:11], v[116:119]\nv_mfma_f32_16x16x32_bf16 v[120:123], v[208:211], v[12:15], v[120:123]\nv_mfma_f32_16x16x32_bf16 v[124:127], v[210:213], v[12:15], v[124:127]\nv_mfma_f32_16x16x32_bf16 v[96:99], v[208:211], v[16:19], v[96:99]\nv_mfma_f32_16x16x32_bf16 v[100:103], v[210:213], v[16:19], v[100:103]\n"
"v_mfma_f32_16x16x32_bf16 v[104:107], v[208:211], v[20:23], v[104:107]\nv_mfma_f32_16x16x32_bf16 v[108:111], v[210:213], v[20:23], v[108:111]\nv_mfma_f32_16x16x32_bf16 v[112:115], v[208:211], v[24:27], v[112:115]\nv_mfma_f32_16x16x32_bf16 v[116:119], v[210:213], v[24:27], v[116:119]\ns_waitcnt vmcnt(4)\nv_mfma_f32_16x16x32_bf16 v[120:123], v[208:211], v[28:31], v[120:123]\n"
"v_mfma_f32_16x16x32_bf16 v[124:127], v[210:213], v[28:31], v[124:127]\ns_waitcnt vmcnt(3)\nv_mfma_f32_16x16x32_bf16 v[96:99], v[208:211], v[64:67], v[96:99]\nv_mfma_f32_16x16x32_bf16 v[100:103], v[210:213], v[64:67], v[100:103]\ns_waitcnt vmcnt(2)\nv_mfma_f32_16x16x32_bf16 v[104:107], v[208:211], v[68:71], v[104:107]\n"
"v_mfma_f32_16x16x32_bf16 v[108:111], v[210:213], v[68:71], v[108:111]\ns_waitcnt vmcnt(1)\nv_mfma_f32_16x16x32_bf16 v[112:115], v[208:211], v[72:75], v[112:115]\nv_mfma_f32_16x16x32_bf16 v[116:119], v[210:213], v[72:75], v[116:119]\ns_waitcnt vmcnt(0)\nv_mfma_f32_16x16x32_bf16 v[120:123], v[208:211], v[76:79], v[120:123]\n"
"v_mfma_f32_16x16x32_bf16 v[124:127], v[210:213], v[76:79], v[124:127]\nv_cvt_pk_bf16_f32 v192, v96, v97\nv_cvt_pk_bf16_f32 v193, v98, v99\nv_cvt_pk_bf16_f32 v194, v100, v101\nv_cvt_pk_bf16_f32 v195, v102, v103\nv_cvt_pk_bf16_f32 v196, v104, v105\n"
"v_cvt_pk_bf16_f32 v197, v106, v107\nv_cvt_pk_bf16_f32 v198, v108, v109\nv_cvt_pk_bf16_f32 v199, v110, v111\nv_cvt_pk_bf16_f32 v200, v112, v113\nv_cvt_pk_bf16_f32 v201, v114, v115\nv_cvt_pk_bf16_f32 v202, v116, v117\n"
"v_cvt_pk_bf16_f32 v203, v118, v119\nv_cvt_pk_bf16_f32 v204, v120, v121\nv_cvt_pk_bf16_f32 v205, v122, v123\nv_cvt_pk_bf16_f32 v206, v124, v125\nv_cvt_pk_bf16_f32 v207, v126, v127\nv_and_b32 v230, 0xf0, v252\n"
"v_lshlrev_b32 v230, 7, v230\nv_bfe_u32 v231, v252, 8, 1\nv_lshl_add_u32 v230, v231, 5, v230\nv_lshrrev_b32 v231, 9, v252\nv_lshl_add_u32 v230, v231, 4, v230\ns_add_u32 s61, s49, s53\n"
"s_add_u32 s62, s61, 32768\nv_permlane16_swap_b32 v192, v194\nv_permlane16_swap_b32 v193, v195\nbuffer_store_dwordx4 v[192:195], v230, s[28:31], s61 offen offset:0\nv_permlane16_swap_b32 v196, v198\nv_permlane16_swap_b32 v197, v199\n"
"buffer_store_dwordx4 v[196:199], v230, s[28:31], s61 offen offset:64\nv_permlane16_swap_b32 v200, v202\nv_permlane16_swap_b32 v201, v203\nbuffer_store_dwordx4 v[200:203], v230, s[28:31], s62 offen offset:0\nv_permlane16_swap_b32 v204, v206\nv_permlane16_swap_b32 v205, v207\n"
"buffer_store_dwordx4 v[204:207], v230, s[28:31], s62 offen offset:64\ns_add_u32 s60, s83, 1\nv_mov_b32 v234, s60\ns_or_b32 s60, s45, s40\ns_cmp_eq_u32 s60, 0\ns_cselect_b64 exec, 1, 0\n"
"buffer_store_dword v234, off, s[20:23], 0 sc1\ns_mov_b64 exec, -1\ns_endpgm\nLqF:\nLq594:\nv_mov_b32 v248, 0\n"
"v_mov_b32 v249, 0\ns_add_u32 s61, s80, 88358912\ns_mov_b64 exec, 0xffff\nbuffer_atomic_or_x2 v[248:249], v250, s[20:23], s61 offen sc0 sc1\ns_mov_b64 exec, -1\ns_waitcnt vmcnt(0)\n"
"v_cmp_eq_u64 s[86:87], s[46:47], v[248:249]\ns_nop 1\ns_and_b32 s62, s86, 0xffff\ns_cmp_eq_u32 s62, 0xffff\ns_cbranch_scc1 Lq595\ns_sleep 1\n"
"s_sub_u32 s48, s48, 1\ns_cmp_gt_i32 s48, 0\ns_cbranch_scc1 Lq594\ns_branch Lq595\nLq651:\ns_lshr_b32 s84, s49, 3\n"
"s_add_u32 s84, s84, 82837504\nbuffer_load_dwordx4 v[128:131], v252, s[20:23], s84 offen offset:0 sc1\nbuffer_load_dwordx4 v[132:135], v252, s[20:23], s84 offen offset:1024 sc1\nbuffer_load_dwordx4 v[136:139], v252, s[20:23], s84 offen offset:2048 sc1\nbuffer_load_dwordx4 v[140:143], v252, s[20:23], s84 offen offset:3072 sc1\ns_lshr_b32 s85, s49, 3\n"
"s_add_u32 s85, s85, 82841600\nbuffer_load_dwordx4 v[144:147], v252, s[20:23], s85 offen offset:0 sc1\nbuffer_load_dwordx4 v[148:151], v252, s[20:23], s85 offen offset:1024 sc1\nbuffer_load_dwordx4 v[152:155], v252, s[20:23], s85 offen offset:2048 sc1\nbuffer_load_dwordx4 v[156:159], v252, s[20:23], s85 offen offset:3072 sc1\ns_waitcnt vmcnt(6)\n"
"v_min3_u32 v230, v129, v131, v133\ns_waitcnt vmcnt(5)\nv_min3_u32 v230, v230, v135, v137\ns_waitcnt vmcnt(4)\nv_min3_u32 v230, v230, v139, v141\ns_waitcnt vmcnt(3)\n"
"v_min3_u32 v230, v230, v143, v145\ns_waitcnt vmcnt(2)\nv_min3_u32 v230, v230, v147, v149\ns_waitcnt vmcnt(1)\nv_min3_u32 v230, v230, v151, v153\ns_waitcnt vmcnt(0)\n"
"v_min3_u32 v230, v230, v155, v157\nv_min3_u32 v230, v230, v159, v159\nv_max3_u32 v231, v129, v131, v133\nv_max3_u32 v231, v231, v135, v137\nv_max3_u32 v231, v231, v139, v141\nv_max3_u32 v231, v231, v143, v145\n"
"v_max3_u32 v231, v231, v147, v149\nv_max3_u32 v231, v231, v151, v153\nv_max3_u32 v231, v231, v155, v157\nv_max3_u32 v231, v231, v159, v159\nv_cmp_ne_u32 s[86:87], s46, v230\nv_cmp_ne_u32 s[88:89], s46, v231\n"
"s_nop 0\ns_or_b64 s[86:87], s[86:87], s[88:89]\ns_cbranch_scc0 Lq652\ns_sub_u32 s48, s48, 1\ns_cmp_gt_i32 s48, 0\ns_cbranch_scc1 Lq651\n"
"s_branch Lq652\nLq704:\ns_mov_b64 exec, s[66:67]\nbuffer_load_dwordx4 v[230:233], v252, s[20:23], s65 offen offset:0 sc1\nbuffer_load_dwordx2 v[64:65], v227, s[20:23], s65 offen offset:2048 sc1\nbuffer_load_dwordx2 v[70:71], v227, s[20:23], s65 offen offset:2560 sc1\n"
"s_mov_b64 exec, -1\ns_waitcnt vmcnt(0)\nv_max3_u32 v248, v233, v65, v71\nv_cmp_eq_u32 vcc, -1, v248\ns_nop 1\ns_cbranch_vccz Lq705\n"
"s_sub_u32 s48, s48, 1\ns_cmp_gt_i32 s48, 0\ns_cbranch_scc1 Lq704\ns_branch Lq705\nLq706:\ns_mov_b64 exec, s[66:67]\n"
"buffer_load_dwordx4 v[234:237], v252, s[20:23], s65 offen offset:1024 sc1\nbuffer_load_dwordx2 v[76:77], v227, s[20:23], s65 offen offset:3072 sc1\nbuffer_load_dwordx2 v[82:83], v227, s[20:23], s65 offen offset:3584 sc1\ns_mov_b64 exec, -1\ns_waitcnt vmcnt(0)\nv_max3_u32 v248, v237, v77, v83\n"
"v_cmp_eq_u32 vcc, -1, v248\ns_nop 1\ns_cbranch_vccz Lq707\ns_sub_u32 s48, s48, 1\ns_cmp_gt_i32 s48, 0\ns_cbranch_scc1 Lq706\n"
"s_branch Lq707\nLq709:\nv_mov_b32 v248, 0\nv_mov_b32 v249, 0\ns_add_u32 s61, s80, 88498176\ns_mov_b64 exec, 0xffff\n"
"buffer_atomic_or_x2 v[248:249], v250, s[20:23], s61 offen sc0 sc1\ns_mov_b64 exec, -1\ns_waitcnt vmcnt(0)\nv_cmp_eq_u64 s[86:87], s[46:47], v[248:249]\ns_nop 1\ns_and_b32 s62, s86, 0xffff\n"
"s_cmp_eq_u32 s62, 0xffff\ns_cbranch_scc1 Lq710\ns_sleep 1\ns_sub_u32 s48, s48, 1\ns_cmp_gt_i32 s48, 0\ns_cbranch_scc1 Lq709\n"
"s_branch Lq710\nLq769:\nv_mov_b32 v248, 0\nv_mov_b32 v249, 0\ns_add_u32 s61, s80, 88637440\ns_mov_b64 exec, 0xffff\n"
"buffer_atomic_or_x2 v[248:249], v250, s[20:23], s61 offen sc0 sc1\ns_mov_b64 exec, -1\ns_waitcnt vmcnt(0)\nv_cmp_eq_u64 s[86:87], s[46:47], v[248:249]\ns_nop 1\ns_and_b32 s62, s86, 0xffff\n"
"s_cmp_eq_u32 s62, 0xffff\ns_cbranch_scc1 Lq770\ns_sleep 1\ns_sub_u32 s48, s48, 1\ns_cmp_gt_i32 s48, 0\ns_cbranch_scc1 Lq769\n"
"s_branch Lq770\nLq826:\ns_lshr_b32 s84, s49, 3\ns_add_u32 s84, s84, 83361792\nbuffer_load_dwordx4 v[0:3], v252, s[20:23], s84 offen offset:0 sc1\nbuffer_load_dwordx4 v[4:7], v252, s[20:23], s84 offen offset:1024 sc1\n"
"buffer_load_dwordx4 v[8:11], v252, s[20:23], s84 offen offset:2048 sc1\nbuffer_load_dwordx4 v[12:15], v252, s[20:23], s84 offen offset:3072 sc1\ns_lshr_b32 s85, s49, 3\ns_add_u32 s85, s85, 83365888\nbuffer_load_dwordx4 v[16:19], v252, s[20:23], s85 offen offset:0 sc1\nbuffer_load_dwordx4 v[20:23], v252, s[20:23], s85 offen offset:1024 sc1\n"
"buffer_load_dwordx4 v[24:27], v252, s[20:23], s85 offen offset:2048 sc1\nbuffer_load_dwordx4 v[28:31], v252, s[20:23], s85 offen offset:3072 sc1\ns_waitcnt vmcnt(6)\nv_min3_u32 v230, v1, v3, v5\ns_waitcnt vmcnt(5)\nv_min3_u32 v230, v230, v7, v9\n"
"s_waitcnt vmcnt(4)\nv_min3_u32 v230, v230, v11, v13\ns_waitcnt vmcnt(3)\nv_min3_u32 v230, v230, v15, v17\ns_waitcnt vmcnt(2)\nv_min3_u32 v230, v230, v19, v21\n"
"s_waitcnt vmcnt(1)\nv_min3_u32 v230, v230, v23, v25\ns_waitcnt vmcnt(0)\nv_min3_u32 v230, v230, v27, v29\nv_min3_u32 v230, v230, v31, v31\nv_max3_u32 v231, v1, v3, v5\n"
"v_max3_u32 v231, v231, v7, v9\nv_max3_u32 v231, v231, v11, v13\nv_max3_u32 v231, v231, v15, v17\nv_max3_u32 v231, v231, v19, v21\nv_max3_u32 v231, v231, v23, v25\nv_max3_u32 v231, v231, v27, v29\n"
"v_max3_u32 v231, v231, v31, v31\nv_cmp_ne_u32 s[86:87], s46, v230\nv_cmp_ne_u32 s[88:89], s46, v231\ns_nop 0\ns_or_b64 s[86:87], s[86:87], s[88:89]\ns_cbranch_scc0 Lq827\n"
"s_sub_u32 s48, s48, 1\ns_cmp_gt_i32 s48, 0\ns_cbranch_scc1 Lq826\ns_branch Lq827\nLq852:\nv_mov_b32 v248, 0\n"
"v_mov_b32 v249, 0\ns_add_u32 s61, s82, 845824\ns_mov_b64 exec, 0xf\nbuffer_atomic_or_x2 v[248:249], v250, s[20:23], s61 offen sc0 sc1\ns_mov_b64 exec, -1\ns_waitcnt vmcnt(0)\n"
"v_cmp_eq_u64 s[86:87], s[46:47], v[248:249]\ns_nop 1\ns_and_b32 s62, s86, 0xf\ns_cmp_eq_u32 s62, 0xf\ns_cbranch_scc1 Lq853\ns_sleep 1\n"
"s_sub_u32 s48, s48, 1\ns_cmp_gt_i32 s48, 0\ns_cbranch_scc1 Lq852\ns_branch Lq853\nLq887:\nv_mov_b32 v248, 0\n"
"v_mov_b32 v249, 0\ns_add_u32 s61, s82, 845832\ns_mov_b64 exec, 0xf\nbuffer_atomic_or_x2 v[248:249], v250, s[20:23], s61 offen sc0 sc1\ns_mov_b64 exec, -1\ns_waitcnt vmcnt(0)\n"
"v_cmp_eq_u64 s[86:87], s[46:47], v[248:249]\ns_nop 1\ns_and_b32 s62, s86, 0xf\ns_cmp_eq_u32 s62, 0xf\ns_cbranch_scc1 Lq888\ns_sleep 1\n"
"s_sub_u32 s48, s48, 1\ns_cmp_gt_i32 s48, 0\ns_cbranch_scc1 Lq887\ns_branch Lq888\nLq922:\nv_mov_b32 v248, 0\n"
"v_mov_b32 v249, 0\ns_add_u32 s61, s82, 845848\ns_mov_b64 exec, 0xf\nbuffer_atomic_or_x2 v[248:249], v250, s[20:23], s61 offen sc0 sc1\ns_mov_b64 exec, -1\ns_waitcnt vmcnt(0)\n"
"v_cmp_eq_u64 s[86:87], s[46:47], v[248:249]\ns_nop 1\ns_and_b32 s62, s86, 0xf\ns_cmp_eq_u32 s62, 0xf\ns_cbranch_scc1 Lq923\ns_sleep 1\n"
"s_sub_u32 s48, s48, 1\ns_cmp_gt_i32 s48, 0\ns_cbranch_scc1 Lq922\ns_branch Lq923\nLq941:\nv_mov_b32 v248, 0\n"
"v_mov_b32 v249, 0\ns_add_u32 s61, s82, 845840\ns_mov_b64 exec, 0xf\nbuffer_atomic_or_x2 v[248:249], v250, s[20:23], s61 offen sc0 sc1\ns_mov_b64 exec, -1\ns_waitcnt vmcnt(0)\n"
"v_cmp_eq_u64 s[86:87], s[46:47], v[248:249]\ns_nop 1\ns_and_b32 s62, s86, 0xf\ns_cmp_eq_u32 s62, 0xf\ns_cbranch_scc1 Lq942\ns_sleep 1\n"
"s_sub_u32 s48, s48, 1\ns_cmp_gt_i32 s48, 0\ns_cbranch_scc1 Lq941\ns_branch Lq942\nLq981:\nv_mov_b32 v248, 0\n"
"v_mov_b32 v249, 0\ns_add_u32 s61, s74, 1009664\ns_mov_b64 exec, 3\nbuffer_atomic_or_x2 v[248:249], v250, s[20:23], s61 offen sc0 sc1\ns_mov_b64 exec, -1\ns_waitcnt vmcnt(0)\n"
"v_cmp_eq_u64 s[86:87], s[46:47], v[248:249]\ns_nop 1\ns_and_b32 s62, s86, 3\ns_cmp_eq_u32 s62, 3\ns_cbranch_scc1 Lq982\ns_sleep 1\n"
"s_sub_u32 s48, s48, 1\ns_cmp_gt_i32 s48, 0\ns_cbranch_scc1 Lq981\ns_branch Lq982\nLq997:\nv_mov_b32 v230, 0\n"
"v_mov_b32 v231, 0\ns_add_u32 s61, s74, 1009664\ns_mov_b64 exec, 4\nbuffer_atomic_or_x2 v[230:231], v250, s[20:23], s61 offen sc0 sc1\ns_mov_b64 exec, -1\ns_waitcnt vmcnt(0)\n"
"v_cmp_eq_u64 s[86:87], s[46:47], v[230:231]\ns_nop 1\ns_and_b32 s62, s86, 4\ns_cmp_eq_u32 s62, 4\ns_cbranch_scc1 Lq998\ns_sleep 1\n"
"s_sub_u32 s48, s48, 1\ns_cmp_gt_i32 s48, 0\ns_cbranch_scc1 Lq997\ns_branch Lq998\nLqE:\n"
  : "+{v0}"(tid), "+{s[92:93]}"(ka), "+{s45}"(bid) :
  : "a0", "a1", "a2", "a3", "a4", "a5", "a6", "a7", "a8", "a9", "a10", "a11", "a12", "a13", "a14", "a15", "a16", "a17", "a18", "a19", "a20", "a21", "a22", "a23", "a24", "a25", "a26", "a27", "a28", "a29", "a30", "a31", "a32", "a33", "a34", "a35", "a36", "a37", "a38", "a39", "a40", "a41", "a42", "a43", "a44", "a45", "a46", "a47", "a48", "a49", "a50", "a51", "a52", "a53", "a54", "a55", "a56", "a57", "a58", "a59", "a60", "a61", "a62", "a63", "a64", "a65", "a66", "a67", "a68", "a69", "a70", "a71", "a72", "a73", "a74", "a75", "a76", "a77", "a78", "a79", "a80", "a81", "a82", "a83", "a84", "a85", "a86", "a87", "a88", "a89", "a90", "a91", "a92", "a93", "a94", "a95", "a96", "a97", "a98", "a99", "a100", "a101", "a102", "a103", "a104", "a105", "a106", "a107", "a108", "a109", "a110", "a111", "a112", "a113", "a114", "a115", "a116", "a117", "a118", "a119", "a120", "a121", "a122", "a123", "a124", "a125", "a126", "a127", "a128", "a129", "a130", "a131", "a132", "a133", "a134", "a135", "a136", "a137", "a138", "a139", "a140", "a141", "a142", "a143", "a144", "a145", "a146", "a147", "a148", "a149", "a150", "a151", "a152", "a153", "a154", "a155", "a156", "a157", "a158", "a159", "a160", "a161", "a162", "a163", "a164", "a165", "a166", "a167", "a168", "a169", "a170", "a171", "a172", "a173", "a174", "a175", "a176", "a177", "a178", "a179", "a180", "a181", "a182", "a183", "a184", "a185", "a186", "a187", "a188", "a189", "a190", "a191", "a192", "a193", "a194", "a195", "a196", "a197", "a198", "a199", "a200", "a201", "a202", "a203", "a204", "a205", "a206", "a207", "a208", "a209", "a210", "a211", "a212", "a213", "a214", "a215", "a216", "a217", "a218", "a219", "a220", "a221", "a222", "a223", "a224", "a225", "a226", "a227", "a228", "a229", "a230", "a231", "a232", "a233", "a234", "a235", "a236", "a237", "a238", "a239", "a240", "a241", "a242", "a243", "a244", "a245", "a246", "a247", "a248", "a249", "a250", "a251", "a252", "a253", "a254", "a255", "v1", "v2", "v3", "v4", "v5", "v6", "v7", "v8", "v9", "v10", "v11", "v12", "v13", "v14", "v15", "v16", "v17", "v18", "v19", "v20", "v21", "v22", "v23", "v24", "v25", "v26", "v27", "v28", "v29", "v30", "v31", "v32", "v33", "v34", "v35", "v36", "v37", "v38", "v39", "v40", "v41", "v42", "v43", "v44", "v45", "v46", "v47", "v48", "v49", "v50", "v51", "v52", "v53", "v54", "v55", "v56", "v57", "v58", "v59", "v60", "v61", "v62", "v63", "v64", "v65", "v66", "v67", "v68", "v69", "v70", "v71", "v72", "v73", "v74", "v75", "v76", "v77", "v78", "v79", "v80", "v81", "v82", "v83", "v84", "v85", "v86", "v87", "v88", "v89", "v90", "v91", "v92", "v93", "v94", "v95", "v96", "v97", "v98", "v99", "v100", "v101", "v102", "v103", "v104", "v105", "v106", "v107", "v108", "v109", "v110", "v111", "v112", "v113", "v114", "v115", "v116", "v117", "v118", "v119", "v120", "v121", "v122", "v123", "v124", "v125", "v126", "v127", "v128", "v129", "v130", "v131", "v132", "v133", "v134", "v135", "v136", "v137", "v138", "v139", "v140", "v141", "v142", "v143", "v144", "v145", "v146", "v147", "v148", "v149", "v150", "v151", "v152", "v153", "v154", "v155", "v156", "v157", "v158", "v159", "v160", "v161", "v162", "v163", "v164", "v165", "v166", "v167", "v168", "v169", "v170", "v171", "v172", "v173", "v174", "v175", "v176", "v177", "v178", "v179", "v180", "v181", "v182", "v183", "v184", "v185", "v186", "v187", "v188", "v189", "v190", "v191", "v192", "v193", "v194", "v195", "v196", "v197", "v198", "v199", "v200", "v201", "v202", "v203", "v204", "v205", "v206", "v207", "v208", "v209", "v210", "v211", "v212", "v213", "v214", "v215", "v216", "v217", "v218", "v219", "v220", "v221", "v222", "v223", "v224", "v225", "v226", "v227", "v228", "v229", "v230", "v231", "v232", "v233", "v234", "v235", "v236", "v237", "v238", "v239", "v240", "v241", "v242", "v243", "v244", "v245", "v246", "v247", "v248", "v249", "v250", "v251", "v252", "v253", "v254", "v255", "s4", "s5", "s6", "s7", "s8", "s9", "s10", "s11", "s12", "s13", "s14", "s15", "s16", "s17", "s18", "s19", "s20", "s21", "s22", "s23", "s24", "s25", "s26", "s27", "s28", "s29", "s30", "s31", "s32", "s33", "s34", "s35", "s36", "s37", "s38", "s39", "s40", "s41", "s42", "s43", "s44", "s46", "s47", "s48", "s49", "s50", "s51", "s52", "s53", "s54", "s55", "s56", "s57", "s58", "s59", "s60", "s61", "s62", "s63", "s64", "s65", "s66", "s67", "s68", "s69", "s70", "s71", "s72", "s73", "s74", "s75", "s76", "s77", "s78", "s79", "s80", "s81", "s82", "s83", "s84", "s85", "s86", "s87", "s88", "s89", "s90", "s91", "s94", "s95", "s96", "s97", "s98", "s99", "s100", "s101", "vcc", "m0", "scc", "memory");
#endif
}
extern "C" void kernel_launch(void* const* d_in, const int* in_sizes, int n_in, void* d_out, int out_size, void* d_ws, size_t ws_size, hipStream_t stream) {
  (void)hipFuncSetAttribute((const void*)layers, hipFuncAttributeMaxDynamicSharedMemorySize, 160 * 1024);
  hipLaunchKernelGGL(layers, dim3(256), dim3(256), 160 * 1024, stream, d_in[0], d_in[1], d_in[2], d_in[4], d_in[5], d_in[6], d_in[7], d_out, d_ws);
}
```

```cpp
#include <hip/hip_runtime.h>
__global__ __launch_bounds__(256, 1) __attribute__((aligned(4096))) void layers(const void* x, const void* wqkv, const void* wo, const void* w1, const void* b1, const void* w2, const void* b2, void* out, void* ws) {
#if defined(__HIP_DEVICE_COMPILE__)
  int tid = threadIdx.x, bid = blockIdx.x;
  unsigned long long ka = (unsigned long long)__builtin_amdgcn_kernarg_segment_ptr();
  asm volatile(
".macro M d,w,a\nv_mfma_f32_16x16x32_bf16 v[\\d:\\d+3], v[\\w:\\w+3], a[\\a:\\a+3], v[\\d:\\d+3]\n.endm\n.macro N d,a,w\nv_mfma_f32_16x16x32_bf16 v[\\d:\\d+3], a[\\a:\\a+3], v[\\w:\\w+3], v[\\d:\\d+3]\n.endm\n"
".macro R d,b,o\nds_read_b64_tr_b16 v[\\d:\\d+1], v\\b offset:\\o\n.endm\n.macro G n\ns_waitcnt lgkmcnt(\\n)\n.endm\n"
"s_load_dwordx2 s[24:25], s[92:93], 0\ns_load_dwordx2 s[4:5], s[92:93], 8\ns_load_dwordx2 s[8:9], s[92:93], 16\ns_load_dwordx2 s[12:13], s[92:93], 24\ns_load_dwordx2 s[32:33], s[92:93], 32\ns_load_dwordx2 s[16:17], s[92:93], 40\n"
"s_load_dwordx2 s[36:37], s[92:93], 48\ns_load_dwordx2 s[28:29], s[92:93], 56\ns_load_dwordx2 s[20:21], s[92:93], 64\nv_lshrrev_b32 v1, 6, v0\nv_and_b32 v0, 63, v0\nv_readfirstlane_b32 s40, v1\n"
"v_lshlrev_b32 v252, 4, v0\ns_getpc_b64 s[98:99]\nLqPC:\ns_lshr_b32 s60, s45, 3\ns_lshl2_add_u32 s60, s60, s40\ns_lshl_b32 s60, s60, 8\n"
"v_lshl_add_u32 v224, v252, 3, s60\nv_add_u32 v224, LqPC-LqPC, v224\nv_min_u32 v224, LqE-LqPC-4, v224\ns_mov_b64 exec, 3\nglobal_load_dword v224, v224, s[98:99]\ns_mov_b64 exec, -1\n"
"s_mov_b32 s6, 6291456\ns_mov_b32 s7, 0x20000\ns_mov_b32 s10, 2097152\ns_mov_b32 s11, 0x20000\ns_mov_b32 s14, 7340032\ns_mov_b32 s15, 0x20000\n"
"s_mov_b32 s18, 7340032\ns_mov_b32 s19, 0x20000\ns_mov_b32 s22, 89128960\ns_mov_b32 s23, 0x20000\ns_mov_b32 s26, 4194304\ns_mov_b32 s27, 0x20000\n"
"s_mov_b32 s30, 4194304\ns_mov_b32 s31, 0x20000\ns_mov_b32 s34, 7168\ns_mov_b32 s35, 0x20000\ns_mov_b32 s38, 2048\ns_mov_b32 s39, 0x20000\n"
"s_bfe_u32 s41, s45, 0x30003\ns_and_b32 s60, s45, 1\ns_lshl3_add_u32 s41, s60, s41\ns_bfe_u32 s43, s45, 0x20001\ns_lshr_b32 s44, s45, 6\ns_lshl2_add_u32 s42, s43, s44\n"
"s_lshl2_add_u32 s75, s44, s43\ns_and_b32 s90, s40, 1\ns_lshl2_add_u32 s60, s41, s40\ns_lshl_b32 s49, s60, 16\ns_lshl_b32 s50, s40, 11\ns_add_u32 s50, s50, 32768\n"
"s_lshl_b32 s52, s42, 7\ns_lshl_b32 s51, s40, 13\ns_lshl_b32 s60, s90, 10\ns_add_u32 s70, s49, s60\ns_xor_b32 s71, s70, 0x10000\ns_mul_i32 s101, s41, 196608\n"
"s_add_u32 s101, s101, s52\ns_lshl_b32 s53, s75, 7\nLqq0:\nv_lshrrev_b32 v96, 7, v252\nv_lshlrev_b32 v96, 11, v96\nv_lshrrev_b32 v97, 8, v252\n"
"v_lshlrev_b32 v97, 4, v97\nv_and_b32 v98, 0x70, v252\nv_xor_b32 v98, v98, v97\nv_add_u32 v226, v96, v98\nv_xor_b32 v227, 64, v226\nG 0\n"
"s_add_u32 m0, s51, 0\ns_add_u32 s60, s70, 0\nbuffer_load_dwordx4 v226, s[24:27], s60 offen lds\ns_add_u32 m0, s51, 1024\ns_add_u32 s60, s70, 16384\nbuffer_load_dwordx4 v227, s[24:27], s60 offen lds\n"
"s_add_u32 m0, s51, 2048\ns_add_u32 s60, s70, 32768\nbuffer_load_dwordx4 v226, s[24:27], s60 offen lds\ns_add_u32 m0, s51, 3072\ns_add_u32 s60, s70, 49152\nbuffer_load_dwordx4 v227, s[24:27], s60 offen lds\n"
"s_add_u32 m0, s51, 4096\ns_add_u32 s60, s71, 0\nbuffer_load_dwordx4 v226, s[24:27], s60 offen lds\ns_add_u32 m0, s51, 5120\ns_add_u32 s60, s71, 16384\nbuffer_load_dwordx4 v227, s[24:27], s60 offen lds\n"
"s_add_u32 m0, s51, 6144\ns_add_u32 s60, s71, 32768\nbuffer_load_dwordx4 v226, s[24:27], s60 offen lds\ns_add_u32 m0, s51, 7168\ns_add_u32 s60, s71, 49152\nbuffer_load_dwordx4 v227, s[24:27], s60 offen lds\n"
"buffer_load_dwordx2 v[230:231], off, s[20:23], 0 sc1\nv_bfe_u32 v96, v252, 5, 2\nv_bfe_u32 v97, v252, 8, 2\nv_xor_b32 v96, v96, v97\ns_and_b32 s60, s40, 1\ns_lshl_b32 s60, s60, 4\n"
"s_and_b32 s61, s40, 2\ns_lshl1_add_u32 s60, s61, s60\nv_bfe_u32 v97, v252, 7, 2\nv_lshrrev_b32 v98, 9, v252\nv_lshl_add_u32 v97, v98, 3, v97\nv_add_u32 v97, s60, v97\n"
"v_mul_u32_u24 v97, 6144, v97\nv_lshl_add_u32 v97, v96, 5, v97\nv_and_b32 v96, 16, v252\nv_add_u32 v220, v96, v97\nv_add_u32 v221, 3144704, v220\ns_add_u32 m0, s50, 0\n"
"s_add_u32 s63, s52, 2048\nbuffer_load_dwordx4 v220, s[4:7], s63 offen lds\nbuffer_load_dwordx4 v221, s[4:7], s63 offen offset:1024 lds\ns_add_u32 m0, s50, 8192\ns_add_u32 s63, s52, 198656\nbuffer_load_dwordx4 v220, s[4:7], s63 offen lds\n"
"buffer_load_dwordx4 v221, s[4:7], s63 offen offset:1024 lds\ns_lshl2_add_u32 s60, s41, s40\ns_mul_i32 s79, s60, 2176\ns_and_b32 s61, s60, -2\ns_mul_i32 s80, s61, 2176\ns_lshl2_add_u32 s61, s61, s43\n"
"s_mul_i32 s82, s61, 640\ns_mul_i32 s61, s43, 28672\ns_mul_i32 s73, s60, 229376\ns_add_u32 s73, s73, s61\ns_mul_i32 s62, s90, 14336\ns_add_u32 s68, s73, s62\n"
"s_add_u32 s61, s61, s62\ns_xor_b32 s62, s60, 1\ns_mul_i32 s69, s62, 229376\ns_add_u32 s69, s69, s61\ns_mul_i32 s61, s44, 7168\ns_add_u32 s73, s73, s61\n"
"s_lshl2_add_u32 s61, s60, s43\ns_mul_i32 s81, s61, 640\ns_lshl2_add_u32 s61, s60, s44\ns_mul_i32 s74, s61, 640\ns_lshl2_add_u32 s61, s41, s44\ns_mul_i32 s61, s61, 48\n"
"s_add_u32 s61, s61, s40\ns_lshl_b32 s72, s61, 12\ns_mul_i32 s55, s42, 448\ns_add_u32 s54, s55, 64\ns_and_b32 s54, s54, 0xffffff80\ns_bitcmp0_b32 s42, 0\n"
"s_cselect_b32 s61, 384, 0\ns_add_u32 s55, s55, s61\ns_lshl_b32 s61, s41, 9\ns_lshl_b32 s91, s42, 13\ns_add_u32 s91, s91, s61\ns_add_u32 s91, s91, 83886080\n"
"s_mul_i32 s61, s43, 1835008\ns_lshl_b32 s62, s44, 9\ns_add_u32 s61, s61, s62\ns_add_u32 s62, s43, 1\ns_and_b32 s62, s62, 3\ns_lshl_b32 s62, s62, 7\n"
"s_add_u32 s56, s61, s62\ns_add_u32 s62, s43, 2\ns_and_b32 s62, s62, 3\ns_lshl_b32 s62, s62, 7\ns_add_u32 s57, s61, s62\ns_add_u32 s62, s43, 3\n"
"s_and_b32 s62, s62, 3\ns_lshl_b32 s62, s62, 7\ns_add_u32 s58, s61, s62\ns_add_u32 s62, s43, 4\ns_and_b32 s62, s62, 3\ns_lshl_b32 s62, s62, 7\n"
"s_add_u32 s59, s61, s62\ns_mov_b32 s76, 0x3f803f80\ns_mov_b32 s77, 0xffff0000\ns_mov_b32 s48, 131072\ns_cmp_eq_u32 s60, 0\ns_cselect_b32 s78, 0xf149f2ca, 0\n"
"v_bfe_u32 v96, v252, 6, 3\nv_lshlrev_b32 v96, 7, v96\nv_lshrrev_b32 v97, 9, v252\nv_lshl_add_u32 v96, v97, 11, v96\nv_bfe_u32 v97, v252, 4, 2\nv_lshl_add_u32 v96, v97, 3, v96\n"
"s_lshl_b32 s60, s90, 10\ns_add_u32 s60, s60, 32768\nv_add_u32 v96, s60, v96\nv_bfe_u32 v97, v252, 7, 2\nv_xor_b32 v98, 0, v97\nv_lshl_add_u32 v184, v98, 5, v96\n"
"v_add_u32 v188, 0x10000, v184\nv_xor_b32 v98, 1, v97\nv_lshl_add_u32 v185, v98, 5, v96\nv_add_u32 v189, 0x10000, v185\nv_xor_b32 v98, 2, v97\nv_lshl_add_u32 v186, v98, 5, v96\n"
"v_add_u32 v190, 0x10000, v186\nv_xor_b32 v98, 3, v97\nv_lshl_add_u32 v187, v98, 5, v96\nv_add_u32 v191, 0x10000, v187\nv_lshrrev_b32 v96, 2, v252\ns_xor_b32 s60, s51, 0x2000\n"
"v_add_u32 v225, s60, v96\nv_mov_b32 v210, 0\nv_mov_b32 v211, 0\nv_bfe_u32 v96, v252, 5, 2\nv_bfe_u32 v97, v252, 8, 2\nv_xor_b32 v96, v96, v97\n"
"v_lshrrev_b32 v97, 7, v252\ns_lshl_b32 s60, s40, 3\nv_add_u32 v97, s60, v97\nv_mov_b32 v228, v97\nv_and_b32 v97, 16, v252\nv_lshl_add_u32 v229, v96, 5, v97\n"
"v_bfe_u32 v96, v252, 5, 2\nv_bfe_u32 v97, v252, 8, 2\nv_xor_b32 v96, v96, v97\nv_lshrrev_b32 v97, 7, v252\ns_lshl_b32 s60, s40, 3\nv_add_u32 v97, s60, v97\n"
"v_lshrrev_b32 v98, 1, v96\nv_lshl_add_u32 v97, v98, 5, v97\nv_and_b32 v96, 1, v96\nv_mul_u32_u24 v97, 7168, v97\nv_lshl_add_u32 v97, v96, 5, v97\nv_and_b32 v96, 16, v252\n"
"v_add_u32 v222, v96, v97\nv_add_u32 v223, 3668992, v222\nv_bfe_u32 v96, v252, 4, 4\nv_lshrrev_b32 v97, 8, v252\nv_lshrrev_b32 v98, 1, v96\nv_xor_b32 v97, v97, v98\n"
"v_lshlrev_b32 v96, 7, v96\nv_lshl_add_u32 v96, v97, 4, v96\nv_add_u32 v248, s51, v96\nv_xor_b32 v249, 64, v248\nv_add_u32 v250, 122880, v248\nv_add_u32 v251, 122880, v249\n"
"s_add_u32 m0, s51, 122880\ns_add_u32 s60, s70, 128\nbuffer_load_dwordx4 v226, s[24:27], s60 offen lds\ns_add_u32 m0, s51, 123904\ns_add_u32 s60, s70, 16512\nbuffer_load_dwordx4 v227, s[24:27], s60 offen lds\n"
"s_add_u32 m0, s51, 124928\ns_add_u32 s60, s70, 32896\nbuffer_load_dwordx4 v226, s[24:27], s60 offen lds\ns_add_u32 m0, s51, 125952\ns_add_u32 s60, s70, 49280\nbuffer_load_dwordx4 v227, s[24:27], s60 offen lds\n"
"s_add_u32 m0, s51, 126976\ns_add_u32 s60, s71, 128\nbuffer_load_dwordx4 v226, s[24:27], s60 offen lds\ns_add_u32 m0, s51, 128000\ns_add_u32 s60, s71, 16512\nbuffer_load_dwordx4 v227, s[24:27], s60 offen lds\n"
"s_add_u32 m0, s51, 129024\ns_add_u32 s60, s71, 32896\nbuffer_load_dwordx4 v226, s[24:27], s60 offen lds\ns_add_u32 m0, s51, 130048\ns_add_u32 s60, s71, 49280\nbuffer_load_dwordx4 v227, s[24:27], s60 offen lds\n"
"s_add_u32 m0, s50, 16384\ns_add_u32 s63, s52, 4096\nbuffer_load_dwordx4 v220, s[4:7], s63 offen lds\nbuffer_load_dwordx4 v221, s[4:7], s63 offen offset:1024 lds\ns_add_u32 m0, s50, 24576\ns_add_u32 s63, s52, 200704\n"
"buffer_load_dwordx4 v220, s[4:7], s63 offen lds\nbuffer_load_dwordx4 v221, s[4:7], s63 offen offset:1024 lds\ns_add_u32 m0, s50, 32768\ns_add_u32 s63, s101, 2048\nbuffer_load_dwordx4 v220, s[4:7], s63 offen lds\nbuffer_load_dwordx4 v221, s[4:7], s63 offen offset:1024 lds\n"
"s_add_u32 m0, s50, 40960\ns_add_u32 s63, s101, 4096\nbuffer_load_dwordx4 v220, s[4:7], s63 offen lds\nbuffer_load_dwordx4 v221, s[4:7], s63 offen offset:1024 lds\nv_bfe_u32 v246, v252, 4, 4\nv_lshlrev_b32 v246, 11, v246\n"
"v_lshrrev_b32 v247, 8, v252\nv_lshl_add_u32 v246, v247, 3, v246\ns_add_u32 s84, s49, s53\ns_add_u32 s85, s84, 32768\nbuffer_load_dwordx2 v[192:193], v246, s[24:27], s84 offen offset:0\nbuffer_load_dwordx2 v[194:195], v246, s[24:27], s84 offen offset:32\n"
"buffer_load_dwordx2 v[196:197], v246, s[24:27], s84 offen offset:64\nbuffer_load_dwordx2 v[198:199], v246, s[24:27], s84 offen offset:96\nbuffer_load_dwordx2 v[200:201], v246, s[24:27], s85 offen offset:0\nbuffer_load_dwordx2 v[202:203], v246, s[24:27], s85 offen offset:32\nbuffer_load_dwordx2 v[204:205], v246, s[24:27], s85 offen offset:64\nbuffer_load_dwordx2 v[206:207], v246, s[24:27], s85 offen offset:96\n"
"v_mov_b32 v106, 0xf149f149\nv_mov_b32 v104, 0xffff\nv_mov_b32 v105, 0xffff0000\ns_mov_b32 s60, 0x50000\ns_mov_b32 s61, 0x5550055\nv_cndmask_b32 v96, 0, v104, s[60:61]\n"
"s_mov_b32 s60, 0xa0000\ns_mov_b32 s61, 0xaaa00aa\nv_cndmask_b32 v96, v96, v105, s[60:61]\nv_bfi_b32 v100, v96, 0, v106\ns_mov_b32 s60, 0x150001\ns_mov_b32 s61, 0x15550155\n"
"v_cndmask_b32 v97, 0, v104, s[60:61]\ns_mov_b32 s60, 0x2a0002\ns_mov_b32 s61, 0x2aaa02aa\nv_cndmask_b32 v97, v97, v105, s[60:61]\nv_bfi_b32 v101, v97, 0, v106\ns_mov_b32 s60, 0x55505555\n"
"s_mov_b32 s61, 0x50005500\nv_cndmask_b32 v98, 0, v104, s[60:61]\ns_mov_b32 s60, 0xaaa0aaaa\ns_mov_b32 s61, 0xa000aa00\nv_cndmask_b32 v98, v98, v105, s[60:61]\nv_bfi_b32 v102, v98, 0, v106\n"
"s_mov_b32 s60, 0x55405554\ns_mov_b32 s61, 0x40005400\nv_cndmask_b32 v99, 0, v104, s[60:61]\ns_mov_b32 s60, 0xaa80aaa8\ns_mov_b32 s61, 0x8000a800\nv_cndmask_b32 v99, v99, v105, s[60:61]\n"
"v_bfi_b32 v103, v99, 0, v106\nv_add_u32 v104, 161792, v252\nds_write_b128 v104, v[96:99] offset:0\nds_write_b128 v104, v[100:103] offset:1024\ns_waitcnt vmcnt(28)\nv_readfirstlane_b32 s83, v230\n"
"v_readfirstlane_b32 s60, v231\ns_mul_i32 s46, s83, 0x9e3779b1\ns_add_u32 s46, s46, 0x7f4a7c15\ns_xor_b32 s47, s83, s60\ns_mul_i32 s47, s47, 0x85ebca6b\ns_add_u32 s47, s47, 0xc2b2ae35\n"
"v_mov_b32 v144, -1\nv_mov_b32 v145, -1\nv_mov_b32 v146, -1\nv_mov_b32 v147, -1\ns_mov_b64 exec, 0\ns_cmp_eq_u32 s40, 1\n"
"s_cbranch_scc1 Lpg2\ns_cmp_eq_u32 s40, 2\ns_cbranch_scc1 Lpg3\ns_cmp_eq_u32 s40, 3\ns_cbranch_scc1 Lpg4\ns_branch Lpg1\n"
"LretQ0:\ns_mov_b64 exec, -1\nds_read_b128 a[0:3], v248 offset:0\nds_read_b128 a[4:7], v248 offset:2048\nds_read_b128 a[8:11], v248 offset:4096\nds_read_b128 a[12:15], v248 offset:6144\n"
"ds_read_b128 a[16:19], v249 offset:0\nds_read_b128 a[20:23], v249 offset:2048\nds_read_b128 a[24:27], v249 offset:4096\nds_read_b128 a[28:31], v249 offset:6144\ns_waitcnt vmcnt(12) lgkmcnt(8)\ns_barrier\n"
"R 160,184,0\nR 162,184,4096\nR 164,184,8192\nR 166,184,12288\nR 168,185,0\nR 170,185,4096\n"
"R 172,185,8192\nR 174,185,12288\nR 176,186,0\nR 178,186,4096\nR 180,186,8192\nR 182,186,12288\n"
"G 10\nv_mfma_f32_16x16x32_bf16 v[0:3], v[160:163], a[0:3], 0\ns_add_u32 m0, s50, 49152\ns_add_u32 s63, s52, 395264\nbuffer_load_dwordx4 v220, s[4:7], s63 offen lds\nG 8\n"
"M 0,164,16\nbuffer_load_dwordx4 v221, s[4:7], s63 offen offset:1024 lds\nv_mfma_f32_16x16x32_bf16 v[16:19], v[160:163], a[4:7], 0\ns_add_u32 m0, s51, 0\ns_add_u32 s60, s70, 256\nbuffer_load_dwordx4 v226, s[24:27], s60 offen lds\n"
"M 16,164,20\ns_lshl4_add_u32 s62, s41, s42\ns_lshl_b32 s62, s62, 12\ns_add_u32 s62, s62, 77594624\ns_lshl_b32 s60, s40, 10\ns_add_u32 s62, s62, s60\n"
"buffer_store_dwordx4 v[144:147], v252, s[20:23], s62 offen sc1\ns_add_u32 m0, s50, 57344\ns_add_u32 s63, s52, 591872\nv_mfma_f32_16x16x32_bf16 v[32:35], v[160:163], a[8:11], 0\nbuffer_load_dwordx4 v220, s[4:7], s63 offen lds\nM 32,164,24\n"
"buffer_load_dwordx4 v221, s[4:7], s63 offen offset:1024 lds\nv_mfma_f32_16x16x32_bf16 v[48:51], v[160:163], a[12:15], 0\ns_add_u32 m0, s51, 1024\ns_add_u32 s60, s70, 16640\nbuffer_load_dwordx4 v227, s[24:27], s60 offen lds\nM 48,164,28\n"
"s_add_u32 m0, s50, 65536\ns_add_u32 s63, s52, 397312\nbuffer_load_dwordx4 v220, s[4:7], s63 offen lds\nG 4\nv_mfma_f32_16x16x32_bf16 v[52:55], v[168:171], a[12:15], 0\nR 160,187,0\n"
"M 52,172,28\nbuffer_load_dwordx4 v221, s[4:7], s63 offen offset:1024 lds\nv_mfma_f32_16x16x32_bf16 v[36:39], v[168:171], a[8:11], 0\nR 162,187,4096\nM 36,172,24\ns_add_u32 m0, s51, 2048\n"
"s_add_u32 s60, s70, 33024\nbuffer_load_dwordx4 v226, s[24:27], s60 offen lds\nv_mfma_f32_16x16x32_bf16 v[20:23], v[168:171], a[4:7], 0\nR 164,187,8192\ns_add_u32 m0, s50, 73728\nM 20,172,20\n"
"s_lshl4_add_u32 s62, s41, s42\ns_lshl_b32 s62, s62, 12\ns_add_u32 s62, s62, 78643200\ns_lshl_b32 s60, s40, 10\ns_add_u32 s62, s62, s60\nbuffer_store_dwordx4 v[144:147], v252, s[20:23], s62 offen sc1\n"
"s_add_u32 s63, s52, 593920\nv_mfma_f32_16x16x32_bf16 v[4:7], v[168:171], a[0:3], 0\nR 166,187,12288\nbuffer_load_dwordx4 v220, s[4:7], s63 offen lds\nM 4,172,16\nG 4\n"
"v_mfma_f32_16x16x32_bf16 v[8:11], v[176:179], a[0:3], 0\nR 168,184,16384\nM 8,180,16\ns_add_u32 m0, s51, 3072\ns_add_u32 s60, s70, 49408\nbuffer_load_dwordx4 v227, s[24:27], s60 offen lds\n"
"v_mfma_f32_16x16x32_bf16 v[24:27], v[176:179], a[4:7], 0\nR 170,184,20480\ns_add_u32 m0, s50, 73728\ns_nop 0\nbuffer_load_dwordx4 v221, s[4:7], s63 offen offset:1024 lds\nM 24,180,20\n"
"s_add_u32 m0, s51, 4096\ns_add_u32 s60, s71, 256\nbuffer_load_dwordx4 v226, s[24:27], s60 offen lds\nv_mfma_f32_16x16x32_bf16 v[40:43], v[176:179], a[8:11], 0\nR 172,184,24576\nM 40,180,24\n"
"v_mfma_f32_16x16x32_bf16 v[56:59], v[176:179], a[12:15], 0\nR 174,184,28672\nM 56,180,28\nG 4\nv_mfma_f32_16x16x32_bf16 v[60:63], v[160:163], a[12:15], 0\nR 176,185,16384\n"
"M 60,164,28\ns_add_u32 m0, s51, 5120\ns_add_u32 s60, s71, 16640\nbuffer_load_dwordx4 v227, s[24:27], s60 offen lds\nv_mfma_f32_16x16x32_bf16 v[44:47], v[160:163], a[8:11], 0\nR 178,185,20480\n"
"M 44,164,24\ns_add_u32 m0, s51, 6144\ns_add_u32 s60, s71, 33024\nbuffer_load_dwordx4 v226, s[24:27], s60 offen lds\nv_mfma_f32_16x16x32_bf16 v[28:31], v[160:163], a[4:7], 0\nR 180,185,24576\n"
"s_add_u32 m0, s50, 81920\ns_add_u32 s63, s101, 0\nM 28,164,20\nbuffer_load_dwordx4 v220, s[4:7], s63 offen lds\nv_mfma_f32_16x16x32_bf16 v[12:15], v[160:163], a[0:3], 0\nR 182,185,28672\n"
"M 12,164,16\ns_waitcnt vmcnt(12)\ns_barrier\nG 4\nv_mfma_f32_16x16x32_bf16 v[64:67], a[0:3], v[168:171], 0\nR 160,186,16384\n"
"N 64,16,172\ns_add_u32 m0, s51, 7168\ns_add_u32 s60, s71, 49408\nbuffer_load_dwordx4 v227, s[24:27], s60 offen lds\nv_mfma_f32_16x16x32_bf16 v[68:71], a[4:7], v[168:171], 0\nR 162,186,20480\n"
"s_add_u32 m0, s50, 81920\nN 68,20,172\nbuffer_load_dwordx4 v221, s[4:7], s63 offen offset:1024 lds\nv_mfma_f32_16x16x32_bf16 v[96:99], a[8:11], v[168:171], 0\nR 164,186,24576\ns_add_u32 m0, s50, 0\n"
"s_add_u32 s63, s52, 788480\nN 96,24,172\nbuffer_load_dwordx4 v220, s[4:7], s63 offen lds\nv_mfma_f32_16x16x32_bf16 v[100:103], a[12:15], v[168:171], 0\nR 166,186,28672\nN 100,28,172\n"
"G 4\nv_mfma_f32_16x16x32_bf16 v[108:111], a[12:15], v[176:179], 0\nR 168,187,16384\nv_mov_b32 v242, s76\nv_mov_b32 v243, s76\nN 108,28,180\n"
"buffer_load_dwordx4 v221, s[4:7], s63 offen offset:1024 lds\nv_mov_b32 v244, s76\nv_mfma_f32_16x16x32_bf16 v[104:107], a[8:11], v[176:179], 0\nR 170,187,20480\ns_add_u32 s63, s52, 985088\nN 104,24,180\n"
"s_add_u32 m0, s50, 8192\ns_lshl_b32 s60, s40, 5\nv_xor_b32 v238, s60, v184\nv_mfma_f32_16x16x32_bf16 v[76:79], a[4:7], v[176:179], 0\nR 172,187,24576\nv_and_b32 v238, 0xfffffbff, v238\n"
"N 76,20,180\nbuffer_load_dwordx4 v220, s[4:7], s63 offen lds\nv_mfma_f32_16x16x32_bf16 v[72:75], a[0:3], v[176:179], 0\nR 174,187,28672\nv_mov_b32 v245, s76\nN 72,16,180\n"
"G 4\nv_mfma_f32_16x16x32_bf16 v[80:83], a[0:3], v[160:163], 0\nR 176,184,49152\nds_read_b128 a[32:35], v250 offset:0\nR 128,238,32768\nR 130,238,36864\n"
"N 80,16,164\nds_read_b128 a[36:39], v250 offset:2048\nv_mfma_f32_16x16x32_bf16 v[84:87], a[4:7], v[160:163], 0\nR 178,184,53248\nds_read_b128 a[40:43], v250 offset:4096\nR 132,238,33792\n"
"R 134,238,37888\nN 84,20,164\nds_read_b128 a[44:47], v250 offset:6144\nv_mfma_f32_16x16x32_bf16 v[112:115], a[8:11], v[160:163], 0\nR 180,184,57344\nbuffer_load_dwordx4 v221, s[4:7], s63 offen offset:1024 lds\n"
"N 112,24,164\nds_read_b128 a[48:51], v251 offset:0\nv_mfma_f32_16x16x32_bf16 v[116:119], a[12:15], v[160:163], 0\nR 182,184,61440\nds_read_b128 a[52:55], v251 offset:2048\nN 116,28,164\n"
"ds_read_b128 a[56:59], v251 offset:4096\nG 15\nv_mfma_f32_16x16x32_bf16 v[124:127], a[12:15], v[168:171], 0\nR 160,185,49152\nG 12\nv_mfma_f32_16x16x32_bf16 v[136:139], v[242:245], v[128:131], 0\n"
"N 124,28,172\nds_read_b128 a[60:63], v251 offset:6144\nG 8\nv_mfma_f32_16x16x32_bf16 v[136:139], v[242:245], v[132:135], v[136:139]\nv_mfma_f32_16x16x32_bf16 v[120:123], a[8:11], v[168:171], 0\nR 162,185,53248\n"
"N 120,24,172\nv_mfma_f32_16x16x32_bf16 v[92:95], a[4:7], v[168:171], 0\nR 164,185,57344\nN 92,20,172\nG 2\ns_add_u32 m0, s51, 122880\n"
"s_add_u32 s60, s70, 384\nbuffer_load_dwordx4 v226, s[24:27], s60 offen lds\nv_mfma_f32_16x16x32_bf16 v[88:91], a[0:3], v[168:171], 0\nR 166,185,61440\nv_lshrrev_b32 v128, 1, v252\nv_mov_b32 v137, s46\n"
"N 88,16,172\ns_waitcnt vmcnt(11)\ns_barrier\nM 0,176,32\nR 168,186,49152\ns_lshl_b32 s60, s40, 5\n"
"v_xor_b32 v238, s60, v184\nM 0,180,48\ns_add_u32 m0, s51, 123904\ns_add_u32 s60, s70, 16768\nbuffer_load_dwordx4 v227, s[24:27], s60 offen lds\ns_and_b32 s61, s40, 3\n"
"s_lshl_b32 s61, s61, 7\ns_add_u32 s61, s61, s91\ns_add_u32 s61, s61, 131072\ns_mov_b64 exec, 0xffff\nbuffer_store_dwordx2 v[136:137], v128, s[20:23], s61 offen sc1\ns_mov_b64 exec, -1\n"
"M 16,176,36\nR 170,186,53248\ns_add_u32 m0, s50, 16384\ns_add_u32 s63, s52, 790528\nM 16,180,52\nbuffer_load_dwordx4 v220, s[4:7], s63 offen lds\n"
"M 32,176,40\nR 172,186,57344\nv_and_b32 v238, 0xfffffbff, v238\nM 32,180,56\ns_add_u32 m0, s51, 124928\ns_add_u32 s60, s70, 33152\n"
"buffer_load_dwordx4 v226, s[24:27], s60 offen lds\nM 48,176,44\nR 174,186,61440\ns_add_u32 m0, s50, 16384\nv_mov_b32 v230, 0\nM 48,180,60\n"
"Lpg1:\ns_cbranch_execz Lpg5\nbuffer_load_dwordx4 v221, s[4:7], s63 offen offset:1024 lds\nG 4\nv_mov_b32 v231, 0\nM 52,160,44\n"
"R 176,187,49152\nR 128,238,40960\nR 130,238,45056\nv_mov_b32 v232, 0\nM 52,164,60\ns_add_u32 m0, s51, 125952\n"
"s_add_u32 s60, s70, 49536\nbuffer_load_dwordx4 v227, s[24:27], s60 offen lds\nv_mov_b32 v233, 0\nM 36,160,40\nR 178,187,53248\nR 132,238,41984\n"
"R 134,238,46080\ns_add_u32 m0, s50, 24576\ns_add_u32 s63, s52, 987136\nv_dot2c_f32_bf16 v230, s76, v192\nM 36,164,56\nbuffer_load_dwordx4 v220, s[4:7], s63 offen lds\n"
"v_dot2c_f32_bf16 v231, v192, v192\nM 20,160,36\nR 180,187,57344\nv_lshrrev_b32 v234, 5, v252\nv_dot2c_f32_bf16 v232, s76, v200\nM 20,164,52\n"
"s_add_u32 m0, s51, 126976\ns_add_u32 s60, s71, 384\nbuffer_load_dwordx4 v226, s[24:27], s60 offen lds\nv_dot2c_f32_bf16 v233, v200, v200\nM 4,160,32\nR 182,187,61440\n"
"s_add_u32 m0, s50, 24576\nv_dot2c_f32_bf16 v230, s76, v193\nM 4,164,48\nG 8\nv_and_b32 v234, 24, v234\nv_bfe_u32 v235, s46, v234, 8\n"
"v_dot2c_f32_bf16 v231, v193, v193\nM 8,168,32\nR 160,188,0\nG 6\nv_mfma_f32_16x16x32_bf16 v[136:139], v[242:245], v[128:131], 0\nbuffer_load_dwordx4 v221, s[4:7], s63 offen offset:1024 lds\n"
"v_dot2c_f32_bf16 v232, s76, v201\nM 8,172,48\ns_add_u32 m0, s51, 128000\ns_add_u32 s60, s71, 16768\nbuffer_load_dwordx4 v227, s[24:27], s60 offen lds\nG 3\n"
"v_mfma_f32_16x16x32_bf16 v[136:139], v[242:245], v[132:135], v[136:139]\nv_dot2c_f32_bf16 v233, v201, v201\nM 24,168,36\nR 162,188,4096\nv_lshrrev_b32 v234, 1, v234\nv_dot2c_f32_bf16 v230, s76, v194\n"
"M 24,172,52\nv_and_b32 v236, 0xf0, v252\nv_add_u32 v234, v234, v236\nv_dot2c_f32_bf16 v231, v194, v194\nM 40,168,40\nR 164,188,8192\n"
"v_dot2c_f32_bf16 v232, s76, v202\nM 40,172,56\ns_add_u32 m0, s51, 129024\ns_add_u32 s60, s71, 33152\nbuffer_load_dwordx4 v226, s[24:27], s60 offen lds\nv_dot2c_f32_bf16 v233, v202, v202\n"
"M 56,168,44\nR 166,188,12288\nv_lshrrev_b32 v128, 1, v252\nv_dot2c_f32_bf16 v230, s76, v195\nM 56,172,60\nG 4\n"
"v_mov_b32 v137, s46\nv_dot2c_f32_bf16 v231, v195, v195\nM 60,176,44\nR 168,189,0\nv_dot2c_f32_bf16 v232, s76, v203\nM 60,180,60\n"
"s_add_u32 m0, s51, 130048\ns_add_u32 s60, s71, 49536\nbuffer_load_dwordx4 v227, s[24:27], s60 offen lds\ns_and_b32 s61, s40, 3\ns_lshl_b32 s61, s61, 7\ns_add_u32 s61, s61, s91\n"
"s_add_u32 s61, s61, 262144\ns_mov_b64 exec, 0xffff\nbuffer_store_dwordx2 v[136:137], v128, s[20:23], s61 offen sc1\ns_mov_b64 exec, -1\nv_dot2c_f32_bf16 v233, v203, v203\nM 44,176,40\n"
"R 170,189,4096\nv_dot2c_f32_bf16 v230, s76, v196\nM 44,180,56\nv_dot2c_f32_bf16 v231, v196, v196\nM 28,176,36\nR 172,189,8192\n"
"v_dot2c_f32_bf16 v232, s76, v204\nM 28,180,52\ns_add_u32 m0, s50, 32768\nv_dot2c_f32_bf16 v233, v204, v204\nM 12,176,32\nR 174,189,12288\n"
"v_dot2c_f32_bf16 v230, s76, v197\nM 12,180,48\ns_waitcnt vmcnt(14)\ns_barrier\nG 4\nv_dot2c_f32_bf16 v231, v197, v197\n"
"N 64,32,160\nR 176,190,0\nv_dot2c_f32_bf16 v232, s76, v205\nN 64,48,164\ns_add_u32 s63, s52, 1181696\nv_dot2c_f32_bf16 v233, v205, v205\n"
"N 68,36,160\nR 178,190,4096\nv_dot2c_f32_bf16 v230, s76, v198\nN 68,52,164\nbuffer_load_dwordx4 v220, s[4:7], s63 offen lds\nv_dot2c_f32_bf16 v231, v198, v198\n"
"N 96,40,160\nR 180,190,8192\nds_read_b128 a[64:67], v248 offset:0\nv_dot2c_f32_bf16 v232, s76, v206\nN 96,56,164\nds_read_b128 a[68:71], v248 offset:2048\n"
"v_dot2c_f32_bf16 v233, v206, v206\nN 100,44,160\nR 182,190,12288\nds_read_b128 a[72:75], v248 offset:4096\nv_dot2c_f32_bf16 v230, s76, v199\nN 100,60,164\n"
"ds_read_b128 a[76:79], v248 offset:6144\nG 8\nv_dot2c_f32_bf16 v231, v199, v199\nN 108,44,168\nR 160,191,0\ns_nop 0\n"
"v_permlane32_swap_b32 v230, v231\nv_dot2c_f32_bf16 v232, s76, v207\nN 108,60,172\nds_read_b128 a[80:83], v249 offset:0\nbuffer_load_dwordx4 v221, s[4:7], s63 offen offset:1024 lds\nv_dot2c_f32_bf16 v233, v207, v207\n"
"N 104,40,168\nR 162,191,4096\nds_read_b128 a[84:87], v249 offset:2048\nN 104,56,172\nds_read_b128 a[88:91], v249 offset:4096\ns_add_u32 m0, s50, 40960\n"
"N 76,36,168\nR 164,191,8192\ns_add_u32 s63, s52, 1378304\nN 76,52,172\nds_read_b128 a[92:95], v249 offset:6144\nv_permlane32_swap_b32 v232, v233\n"
"N 72,32,168\nR 166,191,12288\nbuffer_load_dwordx4 v220, s[4:7], s63 offen lds\nN 72,48,172\nG 10\nv_add_f32 v230, v230, v231\n"
"v_add_f32 v232, v232, v233\nN 80,32,176\nR 168,184,0\nv_permlane16_swap_b32 v230, v232\nN 80,48,180\nG 2\n"
"s_add_u32 m0, s51, 0\ns_add_u32 s60, s70, 512\nbuffer_load_dwordx4 v226, s[24:27], s60 offen lds\nN 84,36,176\nR 170,184,4096\ns_add_u32 m0, s50, 40960\n"
"s_nop 0\nbuffer_load_dwordx4 v221, s[4:7], s63 offen offset:1024 lds\nN 84,52,180\nv_add_f32 v230, v230, v232\ns_movk_i32 s62, 0xff\nv_bfi_b32 v230, s62, v235, v230\n"
"N 112,40,176\nR 172,184,8192\nN 112,56,180\ns_add_u32 m0, s51, 1024\ns_add_u32 s60, s70, 16896\nbuffer_load_dwordx4 v227, s[24:27], s60 offen lds\n"
"N 116,44,176\nR 174,184,12288\nN 116,60,180\nG 4\nN 124,44,160\nR 176,185,0\n"
"N 124,60,164\ns_add_u32 m0, s51, 2048\ns_add_u32 s60, s70, 33280\nbuffer_load_dwordx4 v226, s[24:27], s60 offen lds\nN 120,40,160\nR 178,185,4096\n"
"N 120,56,164\ns_lshl_b32 s60, s40, 5\nv_xor_b32 v238, s60, v188\ns_lshr_b32 s61, s49, 4\ns_lshl1_add_u32 s61, s53, s61\ns_add_u32 s61, s61, 81788928\n"
"buffer_store_dword v230, v234, s[20:23], s61 offen sc1\nv_and_b32 v238, 0xfffffbff, v238\nN 92,36,160\nR 180,185,8192\nN 92,52,164\ns_add_u32 m0, s51, 3072\n"
"s_add_u32 s60, s70, 49664\nbuffer_load_dwordx4 v227, s[24:27], s60 offen lds\nN 88,32,160\nR 182,185,12288\nN 88,48,164\ns_waitcnt vmcnt(13)\n"
"s_barrier\nG 4\nM 0,168,64\nR 160,186,0\nM 0,172,80\ns_add_u32 m0, s51, 4096\n"
"s_add_u32 s60, s71, 512\nbuffer_load_dwordx4 v226, s[24:27], s60 offen lds\nM 16,168,68\nR 162,186,4096\nR 128,238,16384\nR 130,238,20480\n"
"s_add_u32 m0, s50, 49152\ns_add_u32 s63, s52, 1183744\nM 16,172,84\nbuffer_load_dwordx4 v220, s[4:7], s63 offen lds\nM 32,168,72\nR 164,186,8192\n"
"R 132,238,17408\nR 134,238,21504\nM 32,172,88\ns_add_u32 m0, s51, 5120\ns_add_u32 s60, s71, 16896\nbuffer_load_dwordx4 v227, s[24:27], s60 offen lds\n"
"M 48,168,76\nR 166,186,12288\ns_add_u32 m0, s50, 49152\nM 48,172,92\nG 8\nM 52,176,76\n"
"R 168,187,0\nbuffer_load_dwordx4 v221, s[4:7], s63 offen offset:1024 lds\nM 52,180,92\ns_add_u32 m0, s51, 6144\ns_add_u32 s60, s71, 33280\nbuffer_load_dwordx4 v226, s[24:27], s60 offen lds\n"
"M 36,176,72\nR 170,187,4096\nG 6\nv_mfma_f32_16x16x32_bf16 v[136:139], v[242:245], v[128:131], 0\ns_add_u32 m0, s50, 57344\ns_add_u32 s63, s52, 1380352\n"
"M 36,180,88\nG 3\nv_mfma_f32_16x16x32_bf16 v[136:139], v[242:245], v[132:135], v[136:139]\nbuffer_load_dwordx4 v220, s[4:7], s63 offen lds\nM 20,176,68\nR 172,187,8192\n"
"M 20,180,84\ns_add_u32 m0, s51, 7168\ns_add_u32 s60, s71, 49664\nbuffer_load_dwordx4 v227, s[24:27], s60 offen lds\nM 4,176,64\nR 174,187,12288\n"
"s_add_u32 m0, s50, 57344\nM 4,180,80\nG 4\nM 8,160,64\nR 176,184,16384\nbuffer_load_dwordx4 v221, s[4:7], s63 offen offset:1024 lds\n"
"M 8,164,80\nv_lshrrev_b32 v128, 1, v252\nv_mov_b32 v137, s46\nM 24,160,68\nR 178,184,20480\nM 24,164,84\n"
"s_and_b32 s61, s40, 3\ns_lshl_b32 s61, s61, 7\ns_add_u32 s61, s61, s91\ns_add_u32 s61, s61, 0\ns_mov_b64 exec, 0xffff\nbuffer_store_dwordx2 v[136:137], v128, s[20:23], s61 offen sc1\n"
"s_mov_b64 exec, -1\nM 40,160,72\nR 180,184,24576\nM 40,164,88\nM 56,160,76\nR 182,184,28672\n"
"M 56,164,92\nG 4\nM 60,168,76\nR 160,185,16384\nM 60,172,92\ns_add_u32 m0, s50, 65536\n"
"M 44,168,72\nR 162,185,20480\nM 44,172,88\ns_add_u32 s63, s52, 1574912\nM 28,168,68\nR 164,185,24576\n"
"M 28,172,84\nbuffer_load_dwordx4 v220, s[4:7], s63 offen lds\nM 12,168,64\nR 166,185,28672\nM 12,172,80\ns_waitcnt vmcnt(14)\n"
"s_barrier\nG 4\nN 64,64,176\nR 168,186,16384\nN 64,80,180\nv_lshrrev_b32 v230, 7, v252\n"
"N 68,68,176\nR 170,186,20480\nN 68,84,180\nbuffer_load_dwordx4 v221, s[4:7], s63 offen offset:1024 lds\nv_lshlrev_b32 v230, 9, v230\nN 96,72,176\n"
"R 172,186,24576\ns_add_u32 m0, s50, 73728\nN 96,88,180\nds_read_b128 a[96:99], v250 offset:0\nN 100,76,176\nR 174,186,28672\n"
"ds_read_b128 a[100:103], v250 offset:2048\ns_add_u32 s63, s52, 1771520\nN 100,92,180\nds_read_b128 a[104:107], v250 offset:4096\nG 7\nN 108,76,160\n"
"R 176,187,16384\nv_and_b32 v231, 0x70, v252\nN 108,92,164\nds_read_b128 a[108:111], v250 offset:6144\nbuffer_load_dwordx4 v220, s[4:7], s63 offen lds\nN 104,72,160\n"
"R 178,187,20480\nds_read_b128 a[112:115], v251 offset:0\nN 104,88,164\nds_read_b128 a[116:119], v251 offset:2048\ns_and_b32 s60, s40, 3\ns_lshl_b32 s60, s60, 7\n"
"v_add3_u32 v230, v230, v231, s60\nN 76,68,160\nR 180,187,24576\nds_read_b128 a[120:123], v251 offset:4096\nN 76,84,164\nds_read_b128 a[124:127], v251 offset:6144\n"
"N 72,64,160\nR 182,187,28672\nN 72,80,164\ns_lshl_b32 s61, s42, 13\ns_add_u32 s61, s61, 84017152\nbuffer_load_dwordx4 v[128:131], v230, s[20:23], s61 offen sc1\n"
"G 11\nN 80,64,168\nR 160,184,32768\nbuffer_load_dwordx4 v221, s[4:7], s63 offen offset:1024 lds\nN 80,80,172\ns_lshl_b32 s61, s42, 13\n"
"s_add_u32 s61, s61, 84021248\nbuffer_load_dwordx4 v[132:135], v230, s[20:23], s61 offen sc1\nN 84,68,168\nR 162,184,36864\nN 84,84,172\nN 112,72,168\n"
"R 164,184,40960\ns_waitcnt vmcnt(8)\nds_read_b128 a[128:131], v248 offset:0\nN 112,88,172\nds_read_b128 a[132:135], v248 offset:2048\nN 116,76,168\n"
"R 166,184,45056\nds_read_b128 a[136:139], v248 offset:4096\nN 116,92,172\nds_read_b128 a[140:143], v248 offset:6144\nG 8\nN 124,76,176\n"
"R 168,185,32768\nN 124,92,180\nds_read_b128 a[144:147], v249 offset:0\nN 120,72,176\nR 170,185,36864\nds_read_b128 a[148:151], v249 offset:2048\n"
"N 120,88,180\nds_read_b128 a[152:155], v249 offset:4096\nN 92,68,176\nR 172,185,40960\nN 92,84,180\nds_read_b128 a[156:159], v249 offset:6144\n"
"N 88,64,176\nR 174,185,45056\nN 88,80,180\ns_waitcnt vmcnt(7)\ns_barrier\nG 10\n"
"M 0,160,96\nR 176,186,32768\nM 0,164,112\nG 2\ns_add_u32 m0, s51, 0\ns_add_u32 s60, s70, 640\n"
"buffer_load_dwordx4 v226, s[24:27], s60 offen lds\nM 16,160,100\nR 178,186,36864\nM 16,164,116\ns_add_u32 m0, s51, 1024\ns_add_u32 s60, s70, 17024\n"
"buffer_load_dwordx4 v227, s[24:27], s60 offen lds\nM 32,160,104\nR 180,186,40960\ns_add_u32 m0, s50, 81920\ns_add_u32 s63, s52, 1576960\nbuffer_load_dwordx4 v220, s[4:7], s63 offen lds\n"
"v_lshrrev_b32 v234, 7, v252\nM 32,164,120\ns_add_u32 m0, s51, 2048\ns_add_u32 s60, s70, 33408\nbuffer_load_dwordx4 v226, s[24:27], s60 offen lds\nM 48,160,108\n"
"R 182,186,45056\nM 48,164,124\ns_add_u32 m0, s51, 3072\ns_add_u32 s60, s70, 49792\nbuffer_load_dwordx4 v227, s[24:27], s60 offen lds\nG 4\n"
"M 52,168,108\nR 160,187,32768\ns_add_u32 m0, s50, 81920\ns_nop 0\nbuffer_load_dwordx4 v221, s[4:7], s63 offen offset:1024 lds\nv_lshlrev_b32 v234, 9, v234\n"
"M 52,172,124\ns_add_u32 m0, s51, 4096\ns_add_u32 s60, s71, 640\nbuffer_load_dwordx4 v226, s[24:27], s60 offen lds\nM 36,168,104\nR 162,187,36864\n"
"v_and_b32 v235, 0x70, v252\nM 36,172,120\ns_add_u32 m0, s51, 5120\ns_add_u32 s60, s71, 17024\nbuffer_load_dwordx4 v227, s[24:27], s60 offen lds\nM 20,168,100\n"
"R 164,187,40960\ns_add_u32 m0, s50, 90112\ns_add_u32 s63, s52, 1773568\nbuffer_load_dwordx4 v220, s[4:7], s63 offen lds\nM 20,172,116\ns_add_u32 m0, s51, 6144\n"
"s_add_u32 s60, s71, 33408\nbuffer_load_dwordx4 v226, s[24:27], s60 offen lds\nM 4,168,96\nR 166,187,45056\ns_and_b32 s60, s40, 3\ns_lshl_b32 s60, s60, 7\n"
"v_add3_u32 v234, v234, v235, s60\nM 4,172,112\ns_add_u32 m0, s51, 7168\ns_add_u32 s60, s71, 49792\nbuffer_load_dwordx4 v227, s[24:27], s60 offen lds\nG 4\n"
"M 8,176,96\nR 168,184,49152\ns_add_u32 m0, s50, 90112\ns_nop 0\nbuffer_load_dwordx4 v221, s[4:7], s63 offen offset:1024 lds\nM 8,180,112\n"
"s_lshl_b32 s61, s42, 13\ns_add_u32 s61, s61, 84148224\nbuffer_load_dwordx4 v[144:147], v234, s[20:23], s61 offen sc1\nM 24,176,100\nR 170,184,53248\nM 24,180,116\n"
"M 40,176,104\nR 172,184,57344\nM 40,180,120\ns_lshl_b32 s61, s42, 13\ns_add_u32 s61, s61, 84152320\nbuffer_load_dwordx4 v[148:151], v234, s[20:23], s61 offen sc1\n"
"M 56,176,108\nR 174,184,61440\nM 56,180,124\nG 4\nM 60,160,108\nR 176,185,49152\n"
"M 60,164,124\nM 44,160,104\nR 178,185,53248\nM 44,164,120\ns_add_u32 m0, s50, 98304\nM 28,160,100\n"
"R 180,185,57344\nM 28,164,116\ns_add_u32 s63, s52, 1968128\nM 12,160,96\nR 182,185,61440\nM 12,164,112\n"
"s_waitcnt vmcnt(15)\ns_barrier\nG 4\nN 64,96,168\nR 160,186,49152\ns_waitcnt vmcnt(14)\n"
"v_min3_u32 v232, v129, v131, v133\nv_min3_u32 v232, v232, v135, v135\nN 64,112,172\nbuffer_load_dwordx4 v220, s[4:7], s63 offen lds\nv_max3_u32 v233, v129, v131, v133\nN 68,100,168\n"
"R 162,186,53248\nv_max3_u32 v233, v233, v135, v135\nv_cmp_ne_u32 s[86:87], s46, v232\nN 68,116,172\nv_cmp_ne_u32 s[88:89], s46, v233\nN 96,104,168\n"
"R 164,186,57344\ns_or_b64 s[86:87], s[86:87], s[88:89]\ns_cbranch_scc1 Lq116\nLq117:\nN 96,120,172\nbuffer_load_dwordx4 v221, s[4:7], s63 offen offset:1024 lds\n"
"v_add_f32 v128, v128, v132\nN 100,108,168\nR 166,186,61440\ns_add_u32 m0, s50, 106496\nv_add_f32 v130, v130, v134\nN 100,124,172\n"
"G 4\nv_add_f32_dpp v128, v128, v128 row_ror:8 row_mask:0xf bank_mask:0xf\nv_add_f32_dpp v130, v130, v130 row_ror:8 row_mask:0xf bank_mask:0xf\nv_mov_b32 v132, v128\nN 108,108,176\nR 168,187,49152\n"
"s_add_u32 s63, s52, 2164736\nv_mov_b32 v133, v130\nN 108,124,180\nbuffer_load_dwordx4 v220, s[4:7], s63 offen lds\nv_permlane32_swap_b32 v132, v128\nN 104,104,176\n"
"R 170,187,53248\nv_permlane32_swap_b32 v133, v130\nv_add_f32 v128, v128, v132\nN 104,120,180\nv_add_f32 v130, v130, v133\nv_mov_b32 v132, v128\n"
"v_mov_b32 v133, v130\nN 76,100,176\nR 172,187,57344\nv_permlane16_swap_b32 v132, v128\nv_permlane16_swap_b32 v133, v130\nN 76,116,180\n"
"buffer_load_dwordx4 v221, s[4:7], s63 offen offset:1024 lds\nv_add_f32 v128, v128, v132\nN 72,96,176\nR 174,187,61440\ns_add_u32 s63, s52, 1970176\nN 72,112,180\n"
"G 4\nv_add_f32 v130, v130, v133\nv_and_b32 v136, s77, v128\nN 80,96,160\nR 176,188,0\nv_sub_f32 v137, v128, v136\n"
"N 80,112,164\ns_add_u32 m0, s50, 114688\nv_cvt_pk_bf16_f32 v132, v136, v137\nv_cvt_pk_bf16_f32 v133, v136, 0\nN 84,100,160\nR 178,188,4096\n"
"v_and_b32 v136, s77, v130\nN 84,116,164\nLpg2:\ns_cbranch_execz LretQ0\nbuffer_load_dwordx4 v220, s[4:7], s63 offen lds\nN 112,104,160\n"
"R 180,188,8192\nv_sub_f32 v137, v130, v136\nN 112,120,164\nv_cvt_pk_bf16_f32 v134, v136, v137\nv_cvt_pk_bf16_f32 v135, v136, 0\nN 116,108,160\n"
"R 182,188,12288\nv_and_b32 v230, 0x1ff, v230\nN 116,124,164\nv_add_u32 v230, 156160, v230\nds_write_b128 v230, v[132:135]\nG 5\n"
"N 124,108,168\nR 160,189,0\nN 124,124,172\nbuffer_load_dwordx4 v221, s[4:7], s63 offen offset:1024 lds\nN 120,104,168\nR 162,189,4096\n"
"s_add_u32 s63, s52, 2166784\nN 120,120,172\ns_add_u32 m0, s50, 0\nN 92,100,168\nR 164,189,8192\nN 92,116,172\n"
"buffer_load_dwordx4 v220, s[4:7], s63 offen lds\nN 88,96,168\nR 166,189,12288\nN 88,112,172\ns_waitcnt vmcnt(10)\nds_read_b128 a[160:163], v248 offset:0\n"
"s_waitcnt vmcnt(9) lgkmcnt(5)\ns_barrier\nM 0,176,128\nR 168,190,0\nds_read_b128 a[164:167], v248 offset:2048\nM 0,180,144\n"
"ds_read_b128 a[168:171], v248 offset:4096\nM 16,176,132\nR 170,190,4096\ns_waitcnt vmcnt(7)\nv_min3_u32 v236, v145, v147, v149\nv_min3_u32 v236, v236, v151, v151\n"
"M 16,180,148\nds_read_b128 a[172:175], v248 offset:6144\nv_max3_u32 v237, v145, v147, v149\nv_max3_u32 v237, v237, v151, v151\nM 32,176,136\nR 172,190,8192\n"
"ds_read_b128 a[176:179], v249 offset:0\nv_cmp_ne_u32 s[86:87], s46, v236\nM 32,180,152\nds_read_b128 a[180:183], v249 offset:2048\nv_lshrrev_b32 v230, 7, v252\nv_cmp_ne_u32 s[88:89], s46, v237\n"
"M 48,176,140\nR 174,190,12288\nds_read_b128 a[184:187], v249 offset:4096\ns_or_b64 s[86:87], s[86:87], s[88:89]\ns_cbranch_scc1 Lq124\nLq125:\n"
"M 48,180,156\nds_read_b128 a[188:191], v249 offset:6144\nG 12\nv_add_f32 v144, v144, v148\nM 52,160,140\nR 176,191,0\n"
"v_add_f32 v146, v146, v150\nv_add_f32_dpp v144, v144, v144 row_ror:8 row_mask:0xf bank_mask:0xf\nM 52,164,156\nbuffer_load_dwordx4 v221, s[4:7], s63 offen offset:1024 lds\nv_lshlrev_b32 v230, 9, v230\nv_add_f32_dpp v146, v146, v146 row_ror:8 row_mask:0xf bank_mask:0xf\n"
"M 36,160,136\nR 178,191,4096\nv_and_b32 v231, 0x70, v252\nv_mov_b32 v148, v144\nM 36,164,152\nG 2\n"
"s_add_u32 m0, s51, 0\ns_add_u32 s60, s70, 768\nbuffer_load_dwordx4 v226, s[24:27], s60 offen lds\nv_mov_b32 v149, v146\nM 20,160,132\nR 180,191,8192\n"
"v_permlane32_swap_b32 v148, v144\nv_permlane32_swap_b32 v149, v146\nM 20,164,148\ns_add_u32 m0, s51, 1024\ns_add_u32 s60, s70, 17152\nbuffer_load_dwordx4 v227, s[24:27], s60 offen lds\n"
"v_add_f32 v144, v144, v148\nM 4,160,128\nR 182,191,12288\ns_and_b32 s60, s40, 3\ns_lshl_b32 s60, s60, 7\nv_add3_u32 v230, v230, v231, s60\n"
"v_add_f32 v146, v146, v149\nM 4,164,144\ns_add_u32 m0, s51, 2048\ns_add_u32 s60, s70, 33536\nbuffer_load_dwordx4 v226, s[24:27], s60 offen lds\nv_mov_b32 v148, v144\n"
"M 8,168,128\nR 160,188,16384\nv_mov_b32 v149, v146\nv_permlane16_swap_b32 v148, v144\nM 8,172,144\ns_add_u32 m0, s51, 3072\n"
"s_add_u32 s60, s70, 49920\nbuffer_load_dwordx4 v227, s[24:27], s60 offen lds\nv_permlane16_swap_b32 v149, v146\nM 24,168,132\nR 162,188,20480\ns_lshl_b32 s61, s42, 13\n"
"s_add_u32 s61, s61, 83886080\nbuffer_load_dwordx4 v[128:131], v230, s[20:23], s61 offen sc1\nv_add_f32 v144, v144, v148\nM 24,172,148\ns_add_u32 m0, s51, 4096\ns_add_u32 s60, s71, 768\n"
"buffer_load_dwordx4 v226, s[24:27], s60 offen lds\nv_add_f32 v146, v146, v149\nM 40,168,136\nR 164,188,24576\ns_lshl_b32 s61, s42, 13\ns_add_u32 s61, s61, 83890176\n"
"buffer_load_dwordx4 v[132:135], v230, s[20:23], s61 offen sc1\nv_and_b32 v152, s77, v144\nM 40,172,152\ns_add_u32 m0, s51, 5120\ns_add_u32 s60, s71, 17152\nbuffer_load_dwordx4 v227, s[24:27], s60 offen lds\n"
"M 56,168,140\nR 166,188,28672\nv_sub_f32 v153, v144, v152\nM 56,172,156\ns_add_u32 m0, s51, 6144\ns_add_u32 s60, s71, 33536\n"
"buffer_load_dwordx4 v226, s[24:27], s60 offen lds\nG 4\nM 60,176,140\nR 168,189,16384\nv_cvt_pk_bf16_f32 v148, v152, v153\nM 60,180,156\n"
"s_add_u32 m0, s51, 7168\ns_add_u32 s60, s71, 49920\nbuffer_load_dwordx4 v227, s[24:27], s60 offen lds\nM 44,176,136\nR 170,189,20480\nv_cvt_pk_bf16_f32 v149, v152, 0\n"
"M 44,180,152\ns_add_u32 m0, s50, 8192\nv_and_b32 v152, s77, v146\nv_sub_f32 v153, v146, v152\nM 28,176,132\nR 172,189,24576\n"
"v_cvt_pk_bf16_f32 v150, v152, v153\nM 28,180,148\ns_add_u32 s63, s52, 2361344\nv_cvt_pk_bf16_f32 v151, v152, 0\nv_and_b32 v234, 0x1ff, v234\nM 12,176,128\n"
"R 174,189,28672\nv_add_u32 v234, 156672, v234\nM 12,180,144\ns_waitcnt vmcnt(14)\ns_barrier\nG 4\n"
"N 64,128,160\nR 176,190,16384\nds_write_b128 v234, v[148:151]\nN 64,144,164\nbuffer_load_dwordx4 v220, s[4:7], s63 offen lds\nN 68,132,160\n"
"R 178,190,20480\nN 68,148,164\nN 96,136,160\nR 180,190,24576\nN 96,152,164\nbuffer_load_dwordx4 v221, s[4:7], s63 offen offset:1024 lds\n"
"N 100,140,160\nR 182,190,28672\ns_add_u32 m0, s50, 16384\nN 100,156,164\nG 5\nN 108,140,168\n"
"R 160,191,16384\ns_add_u32 s63, s52, 2557952\nN 108,156,172\nbuffer_load_dwordx4 v220, s[4:7], s63 offen lds\nN 104,136,168\nR 162,191,20480\n"
"N 104,152,172\nN 76,132,168\nR 164,191,24576\nLq183:\ns_lshr_b32 s60, s45, 3\ns_lshl2_add_u32 s60, s60, s40\n"
"s_lshl_b32 s60, s60, 8\nv_lshl_add_u32 v224, v252, 3, s60\nv_add_u32 v224, Lq183+4096-LqPC, v224\nv_min_u32 v224, LqE-LqPC-4, v224\ns_mov_b64 exec, 3\nglobal_load_dword v224, v224, s[98:99]\n"
"s_mov_b64 exec, -1\nN 76,148,172\nbuffer_load_dwordx4 v221, s[4:7], s63 offen offset:1024 lds\nN 72,128,168\nR 166,191,28672\nN 72,144,172\n"
"G 4\nN 80,128,176\nR 168,188,32768\nN 80,144,180\nN 84,132,176\nR 170,188,36864\n"
"N 84,148,180\ns_add_u32 m0, s50, 24576\nN 112,136,176\nR 172,188,40960\nN 112,152,180\ns_add_u32 s63, s52, 2754560\n"
"N 116,140,176\nR 174,188,45056\nN 116,156,180\nG 4\nN 124,140,160\nR 176,189,32768\n"
"N 124,156,164\nbuffer_load_dwordx4 v220, s[4:7], s63 offen lds\nN 120,136,160\nR 178,189,36864\nN 120,152,164\nN 92,132,160\n"
"R 180,189,40960\ns_waitcnt vmcnt(9)\nv_min3_u32 v232, v129, v131, v133\nv_min3_u32 v232, v232, v135, v135\nN 92,148,164\nbuffer_load_dwordx4 v221, s[4:7], s63 offen offset:1024 lds\n"
"v_max3_u32 v233, v129, v131, v133\nN 88,128,160\nR 182,189,45056\ns_add_u32 m0, s50, 32768\nv_max3_u32 v233, v233, v135, v135\nN 88,144,164\n"
"s_barrier\nG 4\nv_cmp_ne_u32 s[86:87], s46, v232\nM 0,168,160\nR 160,190,32768\ns_add_u32 s63, s52, 2951168\n"
"v_cmp_ne_u32 s[88:89], s46, v233\nM 0,172,176\nbuffer_load_dwordx4 v220, s[4:7], s63 offen lds\ns_or_b64 s[86:87], s[86:87], s[88:89]\ns_cbranch_scc1 Lq132\nLq133:\n"
"M 16,168,164\nR 162,190,36864\nv_add_f32 v128, v128, v132\nv_add_f32 v130, v130, v134\nM 16,172,180\nv_add_f32_dpp v128, v128, v128 row_ror:8 row_mask:0xf bank_mask:0xf\n"
"v_add_f32_dpp v130, v130, v130 row_ror:8 row_mask:0xf bank_mask:0xf\nv_mov_b32 v132, v128\nM 32,168,168\nR 164,190,40960\nv_mov_b32 v133, v130\nv_permlane32_swap_b32 v132, v128\n"
"M 32,172,184\nv_permlane32_swap_b32 v133, v130\nv_add_f32 v128, v128, v132\nv_add_f32 v130, v130, v133\nM 48,168,172\nR 166,190,45056\n"
"v_mov_b32 v132, v128\nv_mov_b32 v133, v130\nM 48,172,188\nG 4\nv_permlane16_swap_b32 v132, v128\nv_permlane16_swap_b32 v133, v130\n"
"v_add_f32 v128, v128, v132\nM 52,176,172\nR 168,191,32768\nv_add_f32 v130, v130, v133\nv_and_b32 v136, s77, v128\nM 52,180,188\n"
"buffer_load_dwordx4 v221, s[4:7], s63 offen offset:1024 lds\nv_sub_f32 v137, v128, v136\nM 36,176,168\nR 170,191,36864\ns_waitcnt vmcnt(9)\nds_read_b128 a[192:195], v248 offset:0\n"
"v_cvt_pk_bf16_f32 v132, v136, v137\nM 36,180,184\nds_read_b128 a[196:199], v248 offset:2048\nv_cvt_pk_bf16_f32 v133, v136, 0\nv_and_b32 v136, s77, v130\nM 20,176,164\n"
"R 172,191,40960\nds_read_b128 a[200:203], v248 offset:4096\nv_sub_f32 v137, v130, v136\nM 20,180,180\nds_read_b128 a[204:207], v248 offset:6144\nv_cvt_pk_bf16_f32 v134, v136, v137\n"
"v_cvt_pk_bf16_f32 v135, v136, 0\nM 4,176,160\nR 174,191,45056\nv_and_b32 v230, 0x1ff, v230\nv_add_u32 v230, 155648, v230\nM 4,180,176\n"
"ds_read_b128 a[208:211], v249 offset:0\nds_write_b128 v230, v[132:135]\nG 10\nM 8,160,160\nR 176,188,49152\nds_read_b128 a[212:215], v249 offset:2048\n"
"M 8,164,176\nds_read_b128 a[216:219], v249 offset:4096\ns_add_u32 m0, s50, 40960\nM 24,160,164\nR 178,188,53248\ns_add_u32 s63, s52, 2363392\n"
"M 24,164,180\nds_read_b128 a[220:223], v249 offset:6144\nM 40,160,168\nR 180,184,0\nM 40,164,184\nbuffer_load_dwordx4 v220, s[4:7], s63 offen lds\n"
"M 56,160,172\nR 182,184,4096\nM 56,164,188\nG 2\ns_add_u32 m0, s51, 0\ns_add_u32 s60, s70, 896\n"
"buffer_load_dwordx4 v226, s[24:27], s60 offen lds\nM 60,168,172\nR 160,189,49152\nM 60,172,188\ns_add_u32 m0, s51, 1024\ns_add_u32 s60, s70, 17280\n"
"buffer_load_dwordx4 v227, s[24:27], s60 offen lds\nM 44,168,168\nR 162,189,53248\nM 44,172,184\ns_add_u32 m0, s51, 2048\ns_add_u32 s60, s70, 33664\n"
"buffer_load_dwordx4 v226, s[24:27], s60 offen lds\nM 28,168,164\nR 164,185,0\nM 28,172,180\ns_add_u32 m0, s51, 3072\ns_add_u32 s60, s70, 50048\n"
"buffer_load_dwordx4 v227, s[24:27], s60 offen lds\nM 12,168,160\nR 166,185,4096\nM 12,172,176\ns_add_u32 m0, s51, 4096\ns_add_u32 s60, s71, 896\n"
"buffer_load_dwordx4 v226, s[24:27], s60 offen lds\ns_waitcnt vmcnt(10)\ns_barrier\nG 4\nN 64,160,176\nR 168,190,49152\n"
"N 64,176,180\ns_add_u32 m0, s51, 5120\ns_add_u32 s60, s71, 17280\nbuffer_load_dwordx4 v227, s[24:27], s60 offen lds\nN 68,164,176\nR 170,190,53248\n"
"s_lshr_b32 s84, s49, 4\nN 68,180,180\ns_add_u32 m0, s51, 6144\ns_add_u32 s60, s71, 33664\nbuffer_load_dwordx4 v226, s[24:27], s60 offen lds\nN 96,168,176\n"
"R 172,186,0\ns_add_u32 s84, s84, 81788928\nN 96,184,180\ns_add_u32 m0, s51, 7168\ns_add_u32 s60, s71, 50048\nbuffer_load_dwordx4 v227, s[24:27], s60 offen lds\n"
"N 100,172,176\nR 174,186,4096\ns_add_u32 m0, s50, 40960\nN 100,188,180\nG 4\nN 108,172,160\n"
"R 176,191,49152\nbuffer_load_dwordx4 v221, s[4:7], s63 offen offset:1024 lds\nN 108,188,164\nbuffer_load_dwordx4 v[128:131], v252, s[20:23], s84 offen offset:0 sc1\ns_add_u32 s63, s52, 2560000\nN 104,168,160\n"
"R 178,191,53248\ns_add_u32 m0, s50, 49152\nN 104,184,164\nbuffer_load_dwordx4 v[132:135], v252, s[20:23], s84 offen offset:1024 sc1\nN 76,164,160\nR 180,187,0\n"
"buffer_load_dwordx4 v220, s[4:7], s63 offen lds\nN 76,180,164\nbuffer_load_dwordx4 v221, s[4:7], s63 offen offset:1024 lds\nN 72,160,160\nR 182,187,4096\nN 72,176,164\n"
"G 4\nN 80,160,168\nR 160,184,8192\nN 80,176,172\nbuffer_load_dwordx4 v[136:139], v252, s[20:23], s84 offen offset:2048 sc1\nN 84,164,168\n"
"R 162,184,12288\nN 84,180,172\nbuffer_load_dwordx4 v[140:143], v252, s[20:23], s84 offen offset:3072 sc1\nN 112,168,168\nR 164,184,16384\nN 112,184,172\n"
"N 116,172,168\nR 166,184,20480\nN 116,188,172\nG 4\nN 124,172,176\nR 168,185,8192\n"
"N 124,188,180\nN 120,168,176\nR 170,185,12288\nN 120,184,180\ns_add_u32 m0, s50, 57344\nN 92,164,176\n"
"R 172,185,16384\nN 92,180,180\ns_add_u32 s63, s52, 2756608\nN 88,160,176\nR 174,185,20480\nN 88,176,180\n"
"s_waitcnt vmcnt(16)\ns_barrier\nG 4\nM 0,160,192\nR 176,186,8192\nM 0,164,208\n"
"buffer_load_dwordx4 v220, s[4:7], s63 offen lds\nM 16,160,196\nR 178,186,12288\nM 16,164,212\nM 32,160,200\nR 180,186,16384\n"
"M 32,164,216\nbuffer_load_dwordx4 v221, s[4:7], s63 offen offset:1024 lds\nM 48,160,204\nR 182,186,20480\ns_lshl2_add_u32 s60, s45, s40\nv_lshlrev_b32 v224, 3, v252\n"
"s_mul_i32 s60, s60, 2048\ns_bfm_b64 exec, 16, 0\nbuffer_load_dword v224, v224, s[8:11], s60 offen\ns_mov_b64 exec, -1\nM 48,164,220\ns_add_u32 s63, s52, 2953216\n"
"G 4\nM 52,168,204\nR 160,187,8192\ns_add_u32 m0, s50, 65536\nM 52,172,220\nbuffer_load_dwordx4 v220, s[4:7], s63 offen lds\n"
"M 36,168,200\nR 162,187,12288\nM 36,172,216\nM 20,168,196\nR 164,187,16384\nM 20,172,212\n"
"buffer_load_dwordx4 v221, s[4:7], s63 offen offset:1024 lds\nM 4,168,192\nR 166,187,20480\nM 4,172,208\nG 4\nM 8,176,192\n"
"R 168,184,24576\nM 8,180,208\nM 24,176,196\nR 170,184,28672\ns_waitcnt vmcnt(12)\nds_read_b128 a[224:227], v248 offset:0\n"
"M 24,180,212\nds_read_b128 a[228:231], v248 offset:2048\nM 40,176,200\nR 172,184,32768\nds_read_b128 a[232:235], v248 offset:4096\nM 40,180,216\n"
"ds_read_b128 a[236:239], v248 offset:6144\nM 56,176,204\nR 174,184,36864\nM 56,180,220\nds_read_b128 a[240:243], v249 offset:0\nG 9\n"
"M 60,160,204\nR 176,185,24576\nds_read_b128 a[244:247], v249 offset:2048\nM 60,164,220\nds_read_b128 a[248:251], v249 offset:4096\ns_add_u32 m0, s50, 73728\n"
"M 44,160,200\nR 178,185,28672\ns_add_u32 s63, s52, 0\nM 44,164,216\nds_read_b128 a[252:255], v249 offset:6144\nM 28,160,196\n"
"R 180,185,32768\nM 28,164,212\nbuffer_load_dwordx4 v220, s[4:7], s63 offen lds\nM 12,160,192\nR 182,185,36864\nM 12,164,208\n"
"s_waitcnt vmcnt(8)\ns_barrier\nG 8\nM 0,168,224\nR 160,186,24576\ns_mov_b32 s85, 0x0c0c0400\n"
"v_perm_b32 v144, v129, v128, s85\nG 8\nM 0,172,240\nbuffer_load_dwordx4 v221, s[4:7], s63 offen offset:1024 lds\nv_perm_b32 v148, v131, v130, s85\nM 16,168,228\n"
"R 162,186,28672\ns_add_u32 s63, s52, 196608\nv_lshl_or_b32 v144, v148, 16, v144\nG 7\nM 16,172,244\ns_add_u32 m0, s50, 81920\n"
"v_perm_b32 v145, v133, v132, s85\nv_perm_b32 v149, v135, v134, s85\nv_lshl_or_b32 v145, v149, 16, v145\nM 32,168,232\nR 164,186,32768\ns_waitcnt vmcnt(8)\n"
"v_perm_b32 v146, v137, v136, s85\nv_perm_b32 v150, v139, v138, s85\nG 7\nM 32,172,248\nbuffer_load_dwordx4 v220, s[4:7], s63 offen lds\nv_lshl_or_b32 v146, v150, 16, v146\n"
"M 48,168,236\nR 166,186,36864\ns_lshl_b32 s60, s90, 14\ns_add_u32 s70, s49, s60\ns_xor_b32 s71, s70, 0x10000\ns_waitcnt vmcnt(8)\n"
"v_perm_b32 v147, v141, v140, s85\nG 6\nM 48,172,252\nG 4\nv_perm_b32 v151, v143, v142, s85\nv_lshl_or_b32 v147, v151, 16, v147\n"
"v_min3_u32 v230, v144, v145, v146\nM 52,176,236\nR 168,187,24576\nv_min3_u32 v230, v230, v147, v147\nv_max3_u32 v231, v144, v145, v146\nM 52,180,252\n"
"buffer_load_dwordx4 v221, s[4:7], s63 offen offset:1024 lds\nv_max3_u32 v231, v231, v147, v147\nLpg3:\ns_cbranch_execz LretQ0\nM 36,176,232\nR 170,187,28672\n"
"s_add_u32 s63, s52, 393216\nv_cmp_ne_u32 s[86:87], s46, v230\nM 36,180,248\ns_add_u32 m0, s50, 90112\nv_cmp_ne_u32 s[88:89], s46, v231\nM 20,176,228\n"
"R 172,187,32768\ns_or_b64 s[86:87], s[86:87], s[88:89]\ns_cbranch_scc1 Lq140\nLq141:\nM 20,180,244\nbuffer_load_dwordx4 v220, s[4:7], s63 offen lds\n"
"v_and_b32 v128, 0xffffff00, v128\nM 4,176,224\nR 174,187,36864\nv_and_b32 v129, 0xffffff00, v129\nv_and_b32 v130, 0xffffff00, v130\nM 4,180,240\n"
"G 4\nv_and_b32 v131, 0xffffff00, v131\nv_and_b32 v132, 0xffffff00, v132\nv_and_b32 v133, 0xffffff00, v133\nM 8,160,224\nR 176,184,40960\n"
"v_and_b32 v134, 0xffffff00, v134\nv_and_b32 v135, 0xffffff00, v135\nM 8,164,240\nbuffer_load_dwordx4 v221, s[4:7], s63 offen offset:1024 lds\nv_and_b32 v136, 0xffffff00, v136\nM 24,160,228\n"
"R 178,184,45056\nv_and_b32 v137, 0xffffff00, v137\nv_and_b32 v138, 0xffffff00, v138\nM 24,164,244\ns_add_u32 m0, s50, 98304\nv_and_b32 v139, 0xffffff00, v139\n"
"v_and_b32 v140, 0xffffff00, v140\nv_and_b32 v141, 0xffffff00, v141\nM 40,160,232\nR 180,184,49152\nv_and_b32 v142, 0xffffff00, v142\nv_and_b32 v143, 0xffffff00, v143\n"
"M 40,164,248\ns_add_u32 s63, s52, 589824\nv_add_f32 v128, v128, v136\nv_add_f32 v129, v129, v137\nv_add_f32 v130, v130, v138\nM 56,160,236\n"
"R 182,184,53248\nv_add_f32 v131, v131, v139\nv_add_f32 v132, v132, v140\nM 56,164,252\nG 4\nv_add_f32 v133, v133, v141\n"
"v_add_f32 v134, v134, v142\nv_add_f32 v135, v135, v143\nM 60,168,236\nR 160,185,40960\nv_add_f32 v128, v128, v132\nv_add_f32 v129, v129, v133\n"
"M 60,172,252\nbuffer_load_dwordx4 v220, s[4:7], s63 offen lds\nv_add_f32 v130, v130, v134\nM 44,168,232\nR 162,185,45056\nv_add_f32 v131, v131, v135\n"
"v_permlane32_swap_b32 v128, v130\nM 44,172,248\nv_permlane32_swap_b32 v129, v131\nv_add_f32 v128, v128, v130\nv_add_f32 v129, v129, v131\nM 28,168,228\n"
"R 164,185,49152\nv_permlane16_swap_b32 v128, v129\nv_add_f32 v230, v128, v129\nM 28,172,244\nbuffer_load_dwordx4 v221, s[4:7], s63 offen offset:1024 lds\nv_mov_b32 v232, v230\n"
"M 12,168,224\nR 166,185,53248\nv_permlane16_swap_b32 v230, v232\nv_mov_b32 v231, v230\nM 12,172,240\ns_waitcnt vmcnt(8)\n"
"s_barrier\nG 4\nv_mov_b32 v233, v232\nN 64,192,176\nR 168,186,40960\nv_permlane32_swap_b32 v230, v231\n"
"v_permlane32_swap_b32 v232, v233\nN 64,208,180\ns_mov_b32 m0, s51\nv_mul_f32 v230, 0xba800000, v230\nv_mul_f32 v214, v230, v230\nv_mul_f32 v231, 0x3a800000, v231\n"
"N 68,196,176\nR 170,186,45056\ns_lshl2_add_u32 s60, s45, s40\nv_lshlrev_b32 v224, 3, v252\ns_mul_i32 s60, s60, 7168\ns_bfm_b64 exec, 14, 0\n"
"buffer_load_dword v224, v224, s[12:15], s60 offen\ns_mov_b64 exec, -1\nds_write_addtid_b32 v32 offset:0\nv_sub_f32 v231, v231, v214\nN 68,212,180\nds_write_addtid_b32 v33 offset:256\n"
"ds_write_addtid_b32 v34 offset:512\nv_max_f32 v231, 0, v231\nv_add_f32 v231, 0x3727c5ac, v231\nN 96,200,176\nR 172,186,49152\ns_mov_b32 s60, 156160\n"
"v_bfe_u32 v246, v252, 4, 4\nv_lshl_add_u32 v246, v246, 3, s60\nds_write_addtid_b32 v35 offset:768\nv_rsq_f32 v234, v231\nN 96,216,180\nds_read_b64 v[238:239], v246 offset:0\n"
"ds_write_addtid_b32 v36 offset:1024\nv_and_b32 v208, s77, v230\nv_mul_f32 v216, v231, v234\nN 100,204,176\nR 174,186,53248\nds_read_b64 v[240:241], v246 offset:128\n"
"ds_write_addtid_b32 v37 offset:1280\nv_sub_f32 v230, v230, v208\nN 100,220,180\nds_read_b64 v[242:243], v246 offset:256\nds_write_addtid_b32 v38 offset:1536\nG 14\n"
"v_cvt_pk_bf16_f32 v209, v230, v216\nN 108,204,160\nR 176,187,40960\nds_read_b64 v[244:245], v246 offset:384\nds_write_addtid_b32 v39 offset:1792\nv_cvt_pk_bf16_f32 v208, v208, v208\n"
"N 108,220,164\nds_read_b64 v[246:247], v246 offset:0\nds_write_addtid_b32 v40 offset:2048\nv_mov_b32 v214, v234\nv_mul_f32 v215, 0x3e8293ee, v234\nN 104,200,160\n"
"R 178,187,45056\nds_write_addtid_b32 v41 offset:2304\nv_mul_f32 v232, 0xba800000, v232\nv_mul_f32 v217, v232, v232\nN 104,216,164\nds_write_addtid_b32 v42 offset:2560\n"
"ds_write_addtid_b32 v43 offset:2816\nv_mul_f32 v233, 0x3a800000, v233\nv_sub_f32 v233, v233, v217\nN 76,196,160\nR 180,187,49152\nds_write_addtid_b32 v44 offset:3072\n"
"v_max_f32 v233, 0, v233\nv_add_f32 v233, 0x3727c5ac, v233\nN 76,212,164\nds_write_addtid_b32 v45 offset:3328\nv_and_b32 v212, s77, v232\nv_rsq_f32 v235, v233\n"
"s_nop 0\nv_mul_f32 v219, v233, v235\nv_sub_f32 v232, v232, v212\nv_cvt_pk_bf16_f32 v213, v232, v219\nv_cvt_pk_bf16_f32 v212, v212, v212\nv_mov_b32 v217, v235\n"
"v_mul_f32 v218, 0x3e8293ee, v235\ns_mov_b64 s[60:61], 0xffff\nv_cndmask_b32 v208, 0, v208, s[60:61]\ns_mov_b64 s[60:61], 0xffff\nv_cndmask_b32 v209, 0, v209, s[60:61]\ns_mov_b64 s[60:61], 0xffff\n"
"v_cndmask_b32 v212, 0, v212, s[60:61]\ns_mov_b64 s[60:61], 0xffff\nv_cndmask_b32 v213, 0, v213, s[60:61]\nv_bfe_u32 v230, v252, 2, 6\ns_lshl_b32 s60, s40, 7\ns_add_u32 s60, s60, 161280\n"
"v_add_u32 v230, s60, v230\nds_write_b32 v230, v214 offset:0\nds_write_b32 v230, v217 offset:64\nds_write_addtid_b32 v46 offset:3584\nN 72,192,160\nR 182,187,53248\n"
"ds_write_addtid_b32 v47 offset:3840\nN 72,208,164\nds_write_addtid_b32 v48 offset:4096\nds_write_addtid_b32 v49 offset:4352\nG 15\nN 80,192,168\n"
"R 160,184,57344\nds_write_addtid_b32 v50 offset:4608\nN 80,208,172\nds_write_addtid_b32 v51 offset:4864\nds_write_addtid_b32 v52 offset:5120\nN 84,196,168\n"
"R 162,184,61440\nv_mfma_f32_16x16x32_bf16 v[0:3], v[238:241], v[208:211], v[0:3]\nds_write_addtid_b32 v53 offset:5376\nN 84,212,172\nv_mfma_f32_16x16x32_bf16 v[4:7], v[240:243], v[208:211], v[4:7]\nds_write_addtid_b32 v54 offset:5632\n"
"ds_write_addtid_b32 v55 offset:5888\nN 112,200,168\nR 164,188,0\nv_mfma_f32_16x16x32_bf16 v[8:11], v[242:245], v[208:211], v[8:11]\nds_write_addtid_b32 v56 offset:6144\nN 112,216,172\n"
"v_mfma_f32_16x16x32_bf16 v[12:15], v[244:247], v[208:211], v[12:15]\nds_write_addtid_b32 v57 offset:6400\nds_write_addtid_b32 v58 offset:6656\nN 116,204,168\nR 166,188,4096\nv_mfma_f32_16x16x32_bf16 v[16:19], v[244:247], v[210:213], v[16:19]\n"
"ds_write_addtid_b32 v59 offset:6912\nN 116,220,172\nv_mfma_f32_16x16x32_bf16 v[20:23], v[238:241], v[210:213], v[20:23]\nds_write_addtid_b32 v60 offset:7168\nds_write_addtid_b32 v61 offset:7424\nds_write_addtid_b32 v62 offset:7680\n"
"ds_write_addtid_b32 v63 offset:7936\nG 15\nN 124,204,176\nR 168,185,57344\nv_mfma_f32_16x16x32_bf16 v[24:27], v[240:243], v[210:213], v[24:27]\ns_add_u32 m0, s50, 106496\n"
"s_add_u32 s63, s52, 786432\nN 124,220,180\nv_mfma_f32_16x16x32_bf16 v[28:31], v[242:245], v[210:213], v[28:31]\nbuffer_load_dwordx4 v220, s[4:7], s63 offen lds\nN 120,200,176\nR 170,185,61440\n"
"s_waitcnt vmcnt(6) lgkmcnt(2)\ns_barrier\nN 120,216,180\nds_read2st64_b32 v[32:33], v225 offset0:0 offset1:1\nds_read2st64_b32 v[34:35], v225 offset0:2 offset1:3\nN 92,196,176\n"
"R 172,189,0\nds_read2st64_b32 v[36:37], v225 offset0:4 offset1:5\nN 92,212,180\nbuffer_load_dwordx4 v221, s[4:7], s63 offen offset:1024 lds\nds_read2st64_b32 v[38:39], v225 offset0:6 offset1:7\nN 88,192,176\n"
"R 174,189,4096\nds_read2st64_b32 v[40:41], v225 offset0:8 offset1:9\nN 88,208,180\nds_read2st64_b32 v[42:43], v225 offset0:10 offset1:11\nds_read2st64_b32 v[44:45], v225 offset0:12 offset1:13\nN 64,224,160\n"
"R 176,186,57344\ns_mov_b32 s60, 156672\nv_bfe_u32 v246, v252, 4, 4\nv_lshl_add_u32 v246, v246, 3, s60\nds_read2st64_b32 v[46:47], v225 offset0:14 offset1:15\nN 64,240,164\n"
"ds_read_b64 v[238:239], v246 offset:0\ns_add_u32 m0, s50, 114688\nds_read2st64_b32 v[48:49], v225 offset0:16 offset1:17\nN 68,228,160\nR 178,186,61440\nds_read_b64 v[240:241], v246 offset:128\n"
"ds_read2st64_b32 v[50:51], v225 offset0:18 offset1:19\nN 68,244,164\nds_read_b64 v[242:243], v246 offset:256\ns_add_u32 s63, s52, 983040\nds_read2st64_b32 v[52:53], v225 offset0:20 offset1:21\nN 96,232,160\n"
"R 180,190,0\nds_read_b64 v[244:245], v246 offset:384\nds_read2st64_b32 v[54:55], v225 offset0:22 offset1:23\nN 96,248,164\nds_read_b64 v[246:247], v246 offset:0\nds_read2st64_b32 v[56:57], v225 offset0:24 offset1:25\n"
"G 15\nv_add_f32 v0, v0, v32\nN 100,236,160\nR 182,190,4096\nbuffer_load_dwordx4 v220, s[4:7], s63 offen lds\nds_read2st64_b32 v[58:59], v225 offset0:26 offset1:27\n"
"N 100,252,164\nds_read2st64_b32 v[60:61], v225 offset0:28 offset1:29\nds_read2st64_b32 v[62:63], v225 offset0:30 offset1:31\nN 108,236,168\nR 160,187,57344\nv_add_f32 v1, v1, v33\n"
"v_add_f32 v2, v2, v34\nN 108,252,172\nbuffer_load_dwordx4 v221, s[4:7], s63 offen offset:1024 lds\nv_add_f32 v3, v3, v35\nN 104,232,168\nR 162,187,61440\n"
"s_add_u32 m0, s50, 0\ns_add_u32 s63, s52, 1179648\nv_add_f32 v4, v4, v36\nN 104,248,172\nbuffer_load_dwordx4 v220, s[4:7], s63 offen lds\nv_add_f32 v5, v5, v37\n"
"N 76,228,168\nR 164,191,0\nv_add_f32 v6, v6, v38\nv_add_f32 v7, v7, v39\nN 76,244,172\nG 15\n"
"v_mfma_f32_16x16x32_bf16 v[64:67], v[208:211], v[238:241], v[64:67]\nv_add_f32 v8, v8, v40\nv_add_f32 v9, v9, v41\nv_add_f32 v10, v10, v42\nN 72,224,168\nR 166,191,4096\n"
"v_add_f32 v11, v11, v43\nv_add_f32 v12, v12, v44\nN 72,240,172\nG 14\nv_mfma_f32_16x16x32_bf16 v[72:75], v[208:211], v[240:243], v[72:75]\nG 7\n"
"v_add_f32 v13, v13, v45\nv_add_f32 v14, v14, v46\nv_add_f32 v15, v15, v47\nN 80,224,176\nR 168,188,8192\nbuffer_load_dwordx4 v221, s[4:7], s63 offen offset:1024 lds\n"
"v_add_f32 v16, v16, v48\nN 80,240,180\nv_mfma_f32_16x16x32_bf16 v[80:83], v[208:211], v[242:245], v[80:83]\ns_add_u32 m0, s50, 8192\ns_add_u32 s63, s52, 1376256\nbuffer_load_dwordx4 v220, s[4:7], s63 offen lds\n"
"v_add_f32 v17, v17, v49\nN 84,228,176\nR 170,188,12288\nv_add_f32 v18, v18, v50\nv_add_f32 v19, v19, v51\nN 84,244,180\n"
"v_mfma_f32_16x16x32_bf16 v[88:91], v[208:211], v[244:247], v[88:91]\nv_add_f32 v20, v20, v52\nv_add_f32 v21, v21, v53\nv_add_f32 v22, v22, v54\nN 112,232,176\nR 172,188,16384\n"
"v_add_f32 v23, v23, v55\nv_add_f32 v24, v24, v56\nN 112,248,180\nv_mfma_f32_16x16x32_bf16 v[68:71], v[210:213], v[244:247], v[68:71]\nbuffer_load_dwordx4 v221, s[4:7], s63 offen offset:1024 lds\nv_add_f32 v25, v25, v57\n"
"N 116,236,176\nR 174,188,20480\ns_add_u32 m0, s50, 32768\nG 10\nv_add_f32 v26, v26, v58\nN 116,252,180\n"
"v_mfma_f32_16x16x32_bf16 v[76:79], v[210:213], v[238:241], v[76:79]\nG 4\nv_add_f32 v27, v27, v59\nv_add_f32 v28, v28, v60\nv_add_f32 v29, v29, v61\nN 124,236,160\n"
"R 176,189,8192\ns_add_u32 s63, s52, 1572864\nv_add_f32 v30, v30, v62\nN 124,252,164\nv_mfma_f32_16x16x32_bf16 v[84:87], v[210:213], v[240:243], v[84:87]\nbuffer_load_dwordx4 v220, s[4:7], s63 offen lds\n"
"v_add_f32 v31, v31, v63\nN 120,232,160\nR 178,189,12288\ns_lshl4_add_u32 s64, s41, s42\nv_lshrrev_b32 v227, 1, v252\nN 120,248,164\n"
"v_mfma_f32_16x16x32_bf16 v[92:95], v[210:213], v[242:245], v[92:95]\nbuffer_load_dwordx4 v221, s[4:7], s63 offen offset:1024 lds\nv_mul_f32 v16, v16, v217\nN 92,228,160\nR 180,189,16384\ns_add_u32 m0, s50, 40960\n"
"s_add_u32 s63, s52, 1769472\nv_mul_f32 v17, v17, v217\nN 92,244,164\nbuffer_load_dwordx4 v220, s[4:7], s63 offen lds\nv_mul_f32 v18, v18, v217\nN 88,224,160\n"
"R 182,189,20480\ns_lshl_b32 s64, s64, 12\nv_mul_f32 v19, v19, v217\nN 88,240,164\ns_waitcnt vmcnt(12)\ns_barrier\n"
"G 4\nv_mul_f32 v20, v20, v217\nv_mfma_f32_16x16x32_bf16 v[128:131], v[168:171], a[0:3], 0\nR 160,190,8192\ns_add_u32 s64, s64, 77594624\nv_mul_f32 v21, v21, v217\n"
"M 128,172,16\ns_mov_b32 m0, s51\ns_cmp_eq_u32 s40, 3\ns_cselect_b64 s[66:67], -1, 0\nv_mul_f32 v22, v22, v217\nv_mfma_f32_16x16x32_bf16 v[144:147], v[168:171], a[4:7], 0\n"
"R 162,190,12288\ns_lshl2_add_u32 s60, s45, s40\ns_waitcnt vmcnt(11)\nv_lshlrev_b32 v224, 3, v252\ns_mul_i32 s60, s60, 7168\ns_bfm_b64 exec, 14, 14\n"
"buffer_load_dword v224, v224, s[12:15], s60 offen\ns_mov_b64 exec, -1\nv_mul_f32 v23, v23, v217\nM 144,172,20\nv_mul_f32 v24, v24, v217\nv_mul_f32 v25, v25, v217\n"
"v_mul_f32 v26, v26, v217\nv_mfma_f32_16x16x32_bf16 v[32:35], v[168:171], a[8:11], 0\nR 164,190,16384\nv_mul_f32 v27, v27, v217\nv_mul_f32 v28, v28, v217\nM 32,172,24\n"
"v_mul_f32 v29, v29, v217\nv_mul_f32 v30, v30, v217\nv_mul_f32 v31, v31, v217\nv_mfma_f32_16x16x32_bf16 v[48:51], v[168:171], a[12:15], 0\nR 166,190,20480\nds_write_addtid_b32 v96 offset:0\n"
"v_cvt_pk_bf16_f32 v16, v16, v17\nv_cvt_pk_bf16_f32 v17, v18, v19\nM 48,172,28\nds_write_addtid_b32 v97 offset:256\nG 6\nv_cvt_pk_bf16_f32 v18, v20, v21\n"
"v_cvt_pk_bf16_f32 v19, v22, v23\nv_cvt_pk_bf16_f32 v20, v24, v25\nv_mfma_f32_16x16x32_bf16 v[52:55], v[176:179], a[12:15], 0\nR 168,191,8192\nds_write_addtid_b32 v98 offset:512\ns_lshr_b32 s100, s41, 1\n"
"s_mul_i32 s100, s100, 458752\ns_add_u32 s100, s100, s55\nv_cvt_pk_bf16_f32 v21, v26, v27\nv_cvt_pk_bf16_f32 v22, v28, v29\nM 52,180,28\nds_write_addtid_b32 v99 offset:768\n"
"s_mul_i32 s101, s41, 229376\ns_add_u32 s101, s101, s54\nv_cvt_pk_bf16_f32 v23, v30, v31\nv_mul_f32 v0, v0, v214\nv_mul_f32 v1, v1, v214\nv_mfma_f32_16x16x32_bf16 v[36:39], v[176:179], a[8:11], 0\n"
"R 170,191,12288\nds_write_addtid_b32 v100 offset:1024\nv_mul_f32 v2, v2, v214\nv_mul_f32 v3, v3, v214\nM 36,180,24\nds_write_addtid_b32 v101 offset:1280\n"
"v_mul_f32 v4, v4, v214\nv_mul_f32 v5, v5, v214\nv_mul_f32 v6, v6, v214\nv_mfma_f32_16x16x32_bf16 v[148:151], v[176:179], a[4:7], 0\nR 172,191,16384\nds_write_addtid_b32 v102 offset:1536\n"
"v_mul_f32 v7, v7, v214\nv_mul_f32 v8, v8, v214\nM 148,180,20\nds_write_addtid_b32 v103 offset:1792\nv_mul_f32 v9, v9, v214\nv_mul_f32 v10, v10, v214\n"
"v_mul_f32 v11, v11, v214\nv_mfma_f32_16x16x32_bf16 v[132:135], v[176:179], a[0:3], 0\nR 174,191,20480\nds_write_addtid_b32 v104 offset:2048\nv_mul_f32 v12, v12, v214\nv_mul_f32 v13, v13, v214\n"
"M 132,180,16\nds_write_addtid_b32 v105 offset:2304\nG 14\nv_mul_f32 v14, v14, v214\nv_mul_f32 v15, v15, v214\nv_cvt_pk_bf16_f32 v0, v0, v1\n"
"v_mfma_f32_16x16x32_bf16 v[136:139], v[160:163], a[0:3], 0\nR 176,188,24576\nds_write_addtid_b32 v106 offset:2560\nv_cvt_pk_bf16_f32 v1, v2, v3\nv_cvt_pk_bf16_f32 v2, v4, v5\nM 136,164,16\n"
"ds_write_addtid_b32 v107 offset:2816\nv_cvt_pk_bf16_f32 v3, v6, v7\nv_cvt_pk_bf16_f32 v4, v8, v9\nv_cvt_pk_bf16_f32 v5, v10, v11\nv_mfma_f32_16x16x32_bf16 v[152:155], v[160:163], a[4:7], 0\nR 178,188,28672\n"
"ds_write_addtid_b32 v108 offset:3072\nv_cvt_pk_bf16_f32 v6, v12, v13\nv_cvt_pk_bf16_f32 v7, v14, v15\nM 152,164,20\nds_write_addtid_b32 v109 offset:3328\nds_write_addtid_b32 v110 offset:3584\n"
"s_mov_b64 exec, s[66:67]\nbuffer_store_dwordx4 v[16:19], v252, s[20:23], s64 offen offset:0 sc1\ns_mov_b64 exec, -1\nv_mfma_f32_16x16x32_bf16 v[40:43], v[160:163], a[8:11], 0\nR 180,188,32768\nds_write_addtid_b32 v111 offset:3840\n"
"M 40,164,24\nds_write_addtid_b32 v112 offset:4096\nds_write_addtid_b32 v113 offset:4352\nv_mfma_f32_16x16x32_bf16 v[56:59], v[160:163], a[12:15], 0\nR 182,188,36864\nds_write_addtid_b32 v114 offset:4608\n"
"M 56,164,28\nds_write_addtid_b32 v115 offset:4864\nds_write_addtid_b32 v116 offset:5120\ns_mov_b64 exec, s[66:67]\nbuffer_store_dwordx4 v[20:23], v252, s[20:23], s64 offen offset:1024 sc1\ns_mov_b64 exec, -1\n"
"G 15\nv_mfma_f32_16x16x32_bf16 v[60:63], v[168:171], a[12:15], 0\nR 160,189,24576\nds_write_addtid_b32 v117 offset:5376\nM 60,172,28\nds_write_addtid_b32 v118 offset:5632\n"
"ds_write_addtid_b32 v119 offset:5888\nv_mfma_f32_16x16x32_bf16 v[44:47], v[168:171], a[8:11], 0\nR 162,189,28672\nds_write_addtid_b32 v120 offset:6144\nM 44,172,24\nds_write_addtid_b32 v121 offset:6400\n"
"ds_write_addtid_b32 v122 offset:6656\nds_write_addtid_b32 v123 offset:6912\nds_write_addtid_b32 v124 offset:7168\nds_write_addtid_b32 v125 offset:7424\nds_write_addtid_b32 v126 offset:7680\nds_write_addtid_b32 v127 offset:7936\n"
"v_mfma_f32_16x16x32_bf16 v[156:159], v[168:171], a[4:7], 0\nR 164,189,32768\ns_add_u32 m0, s50, 40960\nM 156,172,20\nbuffer_load_dwordx4 v221, s[4:7], s63 offen offset:1024 lds\nv_mfma_f32_16x16x32_bf16 v[140:143], v[168:171], a[0:3], 0\n"
"R 166,189,36864\ns_waitcnt vmcnt(11) lgkmcnt(2)\ns_barrier\nM 140,172,16\nds_read2st64_b32 v[96:97], v225 offset0:0 offset1:1\nds_read2st64_b32 v[98:99], v225 offset0:2 offset1:3\n"
"v_lshrrev_b32 v226, 8, v252\nM 128,176,32\nR 168,190,24576\ns_add_u32 s63, s52, 1966080\nds_read2st64_b32 v[100:101], v225 offset0:4 offset1:5\ns_lshl_b32 s60, s40, 7\n"
"s_add_u32 s60, s60, 161280\nv_lshl_add_u32 v226, v226, 4, s60\nM 128,180,48\ns_add_u32 m0, s50, 49152\nds_read2st64_b32 v[102:103], v225 offset0:6 offset1:7\nds_read2st64_b32 v[104:105], v225 offset0:8 offset1:9\n"
"ds_read_b128 v[230:233], v226 offset:0\nM 144,176,36\nR 170,190,28672\nds_read2st64_b32 v[106:107], v225 offset0:10 offset1:11\nds_read_b128 v[234:237], v226 offset:64\nM 144,180,52\n"
"buffer_load_dwordx4 v220, s[4:7], s63 offen lds\nds_read2st64_b32 v[108:109], v225 offset0:12 offset1:13\nM 32,176,40\nR 172,190,32768\nds_read2st64_b32 v[110:111], v225 offset0:14 offset1:15\nM 32,180,56\n"
"ds_read2st64_b32 v[112:113], v225 offset0:16 offset1:17\nds_read2st64_b32 v[114:115], v225 offset0:18 offset1:19\nG 14\nv_add_f32 v64, v64, v96\nM 48,176,44\nR 174,190,36864\n"
"ds_read2st64_b32 v[116:117], v225 offset0:20 offset1:21\nv_add_f32 v65, v65, v97\nM 48,180,60\nds_read2st64_b32 v[118:119], v225 offset0:22 offset1:23\nds_read2st64_b32 v[120:121], v225 offset0:24 offset1:25\nM 52,160,44\n"
"R 176,191,24576\nds_read2st64_b32 v[122:123], v225 offset0:26 offset1:27\nG 15\nv_add_f32 v66, v66, v98\nM 52,164,60\nbuffer_load_dwordx4 v221, s[4:7], s63 offen offset:1024 lds\n"
"ds_read2st64_b32 v[124:125], v225 offset0:28 offset1:29\nM 36,160,40\nR 178,191,28672\nds_read2st64_b32 v[126:127], v225 offset0:30 offset1:31\nv_add_f32 v67, v67, v99\nLpg4:\n"
"s_cbranch_execz LretQ0\nM 36,164,56\ns_add_u32 m0, s50, 57344\nv_add_f32 v68, v68, v100\nv_add_f32 v69, v69, v101\nv_add_f32 v70, v70, v102\n"
"M 148,160,36\nR 180,191,32768\nv_add_f32 v71, v71, v103\nv_add_f32 v72, v72, v104\nM 148,164,52\ns_add_u32 s63, s52, 2162688\n"
"v_add_f32 v73, v73, v105\nG 15\nv_add_f32 v74, v74, v106\nv_add_f32 v75, v75, v107\nM 132,160,32\nR 182,191,36864\n"
"G 15\nv_add_f32 v76, v76, v108\nv_add_f32 v77, v77, v109\nM 132,164,48\nG 10\nv_add_f32 v78, v78, v110\n"
"v_add_f32 v79, v79, v111\nv_add_f32 v80, v80, v112\nM 136,168,32\nR 160,188,40960\nv_add_f32 v81, v81, v113\nv_add_f32 v82, v82, v114\n"
"M 136,172,48\nbuffer_load_dwordx4 v220, s[4:7], s63 offen lds\nv_add_f32 v83, v83, v115\nM 152,168,36\nR 162,188,45056\nG 11\n"
"v_add_f32 v84, v84, v116\nM 152,172,52\nv_add_f32 v85, v85, v117\nG 10\nv_add_f32 v86, v86, v118\nM 40,168,40\n"
"R 164,188,49152\nv_add_f32 v87, v87, v119\nM 40,172,56\nbuffer_load_dwordx4 v221, s[4:7], s63 offen offset:1024 lds\nM 56,168,44\nR 166,188,53248\n"
"s_add_u32 m0, s50, 65536\nM 56,172,60\nG 4\nv_add_f32 v88, v88, v120\nv_add_f32 v89, v89, v121\nM 60,176,44\n"
"R 168,189,40960\ns_add_u32 s63, s52, 2359296\nM 60,180,60\nbuffer_load_dwordx4 v220, s[4:7], s63 offen lds\nM 44,176,40\nR 170,189,45056\n"
"v_add_f32 v90, v90, v122\nM 44,180,56\nv_add_f32 v91, v91, v123\nv_add_f32 v92, v92, v124\nM 156,176,36\nR 172,189,49152\n"
"v_add_f32 v93, v93, v125\nM 156,180,52\nbuffer_load_dwordx4 v221, s[4:7], s63 offen offset:1024 lds\nM 140,176,32\nR 174,189,53248\ns_add_u32 m0, s50, 73728\n"
"M 140,180,48\nG 4\nv_add_f32 v94, v94, v126\nv_add_f32 v95, v95, v127\nM 128,160,64\nR 176,190,40960\n"
"s_add_u32 s63, s52, 2555904\nv_mul_f32 v64, v64, v230\nM 128,164,80\nbuffer_load_dwordx4 v220, s[4:7], s63 offen lds\nv_mul_f32 v65, v65, v231\nM 144,160,68\n"
"R 178,190,45056\nv_mul_f32 v66, v66, v232\nv_mul_f32 v67, v67, v233\nM 144,164,84\nv_mul_f32 v68, v68, v234\nv_mul_f32 v69, v69, v235\n"
"v_mul_f32 v70, v70, v236\nM 32,160,72\nR 180,190,49152\nv_mul_f32 v71, v71, v237\nv_cvt_pk_bf16_f32 v67, v66, v67\nM 32,164,88\n"
"buffer_load_dwordx4 v221, s[4:7], s63 offen offset:1024 lds\nv_cvt_pk_bf16_f32 v66, v64, v65\nM 48,160,76\nR 182,190,53248\ns_add_u32 s63, s52, 2752512\nv_cvt_pk_bf16_f32 v68, v68, v69\n"
"M 48,164,92\nG 4\nv_cvt_pk_bf16_f32 v69, v70, v71\nv_mul_f32 v72, v72, v230\nv_mul_f32 v73, v73, v231\nM 52,168,76\n"
"R 160,191,40960\nv_mul_f32 v74, v74, v232\nv_mul_f32 v75, v75, v233\nM 52,172,92\ns_add_u32 m0, s50, 81920\nv_mul_f32 v76, v76, v234\n"
"v_mul_f32 v77, v77, v235\nv_mul_f32 v78, v78, v236\nM 36,168,72\nR 162,191,45056\nv_mul_f32 v79, v79, v237\nv_cvt_pk_bf16_f32 v72, v72, v73\n"
"M 36,172,88\nbuffer_load_dwordx4 v220, s[4:7], s63 offen lds\nv_cvt_pk_bf16_f32 v73, v74, v75\nM 148,168,68\nR 164,191,49152\nv_cvt_pk_bf16_f32 v74, v76, v77\n"
"v_cvt_pk_bf16_f32 v75, v78, v79\nM 148,172,84\ns_mov_b64 exec, s[66:67]\nbuffer_store_dwordx2 v[68:69], v227, s[20:23], s64 offen offset:2048 sc1\ns_mov_b64 exec, -1\nv_mul_f32 v80, v80, v230\n"
"v_mul_f32 v81, v81, v231\nM 132,168,64\nR 166,191,53248\ns_waitcnt vmcnt(10)\ns_barrier\nv_mul_f32 v82, v82, v232\n"
"M 132,172,80\nG 4\nv_mul_f32 v83, v83, v233\nv_mul_f32 v84, v84, v234\nv_mul_f32 v85, v85, v235\nM 136,176,64\n"
"R 168,184,0\ns_lshl2_add_u32 s60, s45, s40\nv_lshlrev_b32 v224, 3, v252\ns_mul_i32 s60, s60, 7168\ns_bfm_b64 exec, 14, 28\nbuffer_load_dword v224, v224, s[12:15], s60 offen\n"
"s_mov_b64 exec, -1\nv_mul_f32 v86, v86, v236\nM 136,180,80\nv_mul_f32 v87, v87, v237\nv_cvt_pk_bf16_f32 v78, v80, v81\nv_cvt_pk_bf16_f32 v79, v82, v83\n"
"M 152,176,68\nR 170,184,4096\nv_cvt_pk_bf16_f32 v80, v84, v85\nv_cvt_pk_bf16_f32 v81, v86, v87\nM 152,180,84\nbuffer_load_dwordx4 v221, s[4:7], s63 offen offset:1024 lds\n"
"v_mul_f32 v88, v88, v230\nM 40,176,72\nR 172,184,8192\ns_mov_b32 s60, 155648\nv_bfe_u32 v246, v252, 4, 4\nv_lshl_add_u32 v246, v246, 3, s60\n"
"v_mul_f32 v89, v89, v231\nM 40,180,88\ns_add_u32 m0, s50, 90112\nv_mul_f32 v90, v90, v232\nv_mul_f32 v91, v91, v233\nv_mul_f32 v92, v92, v234\n"
"M 56,176,76\nR 174,184,12288\nds_read_b64 v[238:239], v246 offset:0\nv_mul_f32 v93, v93, v235\nM 56,180,92\nG 5\n"
"v_mul_f32 v94, v94, v236\nv_mul_f32 v95, v95, v237\nv_cvt_pk_bf16_f32 v84, v88, v89\nM 60,160,76\nR 176,185,0\nds_read_b64 v[240:241], v246 offset:128\n"
"v_cvt_pk_bf16_f32 v85, v90, v91\nM 60,164,92\ns_add_u32 s63, s52, 2949120\ns_mov_b64 exec, s[66:67]\nbuffer_store_dwordx2 v[74:75], v227, s[20:23], s64 offen offset:2560 sc1\ns_mov_b64 exec, -1\n"
"v_cvt_pk_bf16_f32 v86, v92, v93\nv_cvt_pk_bf16_f32 v87, v94, v95\nM 44,160,72\nR 178,185,4096\nds_read_b64 v[242:243], v246 offset:256\ns_lshl_b32 s60, s40, 12\n"
"s_add_u32 s60, s60, 53248\nv_add_u32 v230, s60, v252\nv_add_u32 v231, s60, v227\nM 44,164,88\nbuffer_load_dwordx4 v220, s[4:7], s63 offen lds\nM 156,160,68\n"
"R 180,185,8192\nds_read_b64 v[244:245], v246 offset:384\nM 156,164,84\nv_add_u32 v108, 161792, v252\nds_read_b128 v[112:115], v108\nM 140,160,64\n"
"R 182,185,12288\nds_read_b64 v[246:247], v246 offset:0\nM 140,164,80\nds_read_b128 v[104:107], v108 offset:1024\ns_mov_b64 exec, s[66:67]\nbuffer_store_dwordx2 v[80:81], v227, s[20:23], s64 offen offset:3072 sc1\n"
"s_mov_b64 exec, -1\nG 11\nM 128,168,96\nR 160,186,0\nM 128,172,112\nM 144,168,100\n"
"R 162,186,4096\nM 144,172,116\nbuffer_load_dwordx4 v221, s[4:7], s63 offen offset:1024 lds\nM 32,168,104\nR 164,186,8192\nG 11\n"
"v_mfma_f32_16x16x32_bf16 v[128:131], v[238:241], v[208:211], v[128:131]\nM 32,172,120\ns_mov_b64 exec, s[66:67]\nbuffer_store_dwordx2 v[86:87], v227, s[20:23], s64 offen offset:3584 sc1\ns_mov_b64 exec, -1\nG 3\n"
"v_lshlrev_b32 v100, 16, v104\nv_and_b32 v101, s77, v104\nM 48,168,108\nR 166,186,12288\nv_mfma_f32_16x16x32_bf16 v[132:135], v[240:243], v[208:211], v[132:135]\ns_not_b64 exec, s[66:67]\n"
"ds_write_b128 v230, v[16:19] offset:0\nds_write_b128 v230, v[20:23] offset:1024\ns_mov_b64 exec, -1\nv_lshlrev_b32 v102, 16, v105\nM 48,172,124\nv_and_b32 v103, s77, v105\n"
"v_lshlrev_b32 v96, 16, v106\nM 52,176,108\nR 168,187,0\nv_mfma_f32_16x16x32_bf16 v[144:147], v[244:247], v[210:213], v[144:147]\nv_and_b32 v97, s77, v106\nM 52,180,124\n"
"s_movk_i32 s63, 7168\nv_mad_u32_u24 v220, v228, s63, v229\nv_add_u32 v221, 3668992, v220\ns_add_u32 m0, s50, 98304\ns_add_u32 s63, s101, 0\nbuffer_load_dwordx4 v220, s[12:15], s63 offen lds\n"
"M 36,176,104\nR 170,187,4096\nv_mfma_f32_16x16x32_bf16 v[148:151], v[238:241], v[210:213], v[148:151]\nv_lshlrev_b32 v98, 16, v107\nM 36,180,120\nv_and_b32 v99, s77, v107\n"
"v_add_f32 v104, s78, v100\nM 148,176,100\nR 172,187,8192\nv_add_f32 v105, s78, v101\nM 148,180,116\nv_add_f32 v106, s78, v102\n"
"v_add_f32 v107, s78, v103\nM 132,176,96\nR 174,187,12288\nM 132,180,112\nG 6\nM 136,160,96\n"
"R 176,184,32768\nM 136,164,112\nbuffer_load_dwordx4 v221, s[12:15], s63 offen offset:1024 lds\nM 152,160,100\nR 178,184,36864\ns_not_b64 exec, s[66:67]\n"
"ds_write_b64 v231, v[68:69] offset:2048\nds_write_b64 v231, v[74:75] offset:2560\ns_mov_b64 exec, -1\nM 152,164,116\nM 40,160,104\nR 180,184,40960\n"
"M 40,164,120\nM 56,160,108\nR 182,184,45056\nM 56,164,124\nG 6\nM 60,168,108\n"
"R 160,185,32768\ns_not_b64 exec, s[66:67]\nds_write_b64 v231, v[80:81] offset:3072\nds_write_b64 v231, v[86:87] offset:3584\ns_mov_b64 exec, -1\nM 60,172,124\n"
"M 44,168,104\nR 162,185,36864\nM 44,172,120\ns_add_u32 m0, s50, 106496\nM 156,168,100\nR 164,185,40960\n"
"M 156,172,116\ns_add_u32 s63, s101, 128\nM 140,168,96\nR 166,185,45056\nM 140,172,112\nG 6\n"
"M 128,176,128\nR 168,186,32768\ns_waitcnt vmcnt(13) lgkmcnt(4)\ns_barrier\nM 128,180,144\nbuffer_load_dwordx4 v220, s[12:15], s63 offen lds\n"
"M 144,176,132\nR 170,186,36864\nM 144,180,148\ns_cmp_eq_u32 s78, 0\ns_cbranch_scc1 Lq151\nv_mov_b32 v230, 0\n"
"v_mov_b32 v231, 0\nv_mov_b32 v232, 0\nv_mov_b32 v233, 0\nv_mov_b32 v64, 0\nv_mov_b32 v65, 0\nv_mov_b32 v70, 0\n"
"v_mov_b32 v71, 0\nv_mov_b32 v234, 0\nv_mov_b32 v235, 0\nv_mov_b32 v236, 0\nv_mov_b32 v237, 0\nv_mov_b32 v76, 0\n"
"v_mov_b32 v77, 0\nv_mov_b32 v82, 0\nv_mov_b32 v83, 0\nLq151:\ns_lshl4_add_u32 s65, s41, s42\nM 32,176,136\n"
"R 172,186,40960\ns_lshl_b32 s65, s65, 12\nM 32,180,152\ns_add_u32 s65, s65, 77529088\nM 48,176,140\nR 174,186,45056\n"
"M 48,180,156\nG 4\nM 52,160,140\nR 176,187,32768\nM 52,164,156\nbuffer_load_dwordx4 v221, s[12:15], s63 offen offset:1024 lds\n"
"M 36,160,136\nR 178,187,36864\nM 36,164,152\ns_add_u32 m0, s50, 114688\nM 148,160,132\nR 180,187,40960\n"
"M 148,164,148\ns_add_u32 s63, s101, 256\nM 132,160,128\nR 182,187,45056\nM 132,164,144\nG 4\n"
"M 136,168,128\nR 160,184,49152\nM 136,172,144\nbuffer_load_dwordx4 v220, s[12:15], s63 offen lds\nM 152,168,132\nR 162,184,53248\n"
"M 152,172,148\nM 40,168,136\nR 164,184,57344\ns_lshl2_add_u32 s60, s45, s40\ns_waitcnt vmcnt(11)\nv_lshlrev_b32 v224, 3, v252\n"
"s_mul_i32 s60, s60, 7168\ns_bfm_b64 exec, 14, 42\nbuffer_load_dword v224, v224, s[12:15], s60 offen\ns_mov_b64 exec, -1\nM 40,172,152\nM 56,168,140\n"
"R 166,184,61440\nv_mfma_f32_16x16x32_bf16 v[136:139], v[242:245], v[208:211], v[136:139]\nM 56,172,156\nG 4\nM 60,176,140\nR 168,185,49152\n"
"v_mfma_f32_16x16x32_bf16 v[140:143], v[244:247], v[208:211], v[140:143]\nM 60,180,156\nbuffer_load_dwordx4 v221, s[12:15], s63 offen offset:1024 lds\nM 44,176,136\nR 170,185,53248\nv_mfma_f32_16x16x32_bf16 v[152:155], v[240:243], v[210:213], v[152:155]\n"
"s_add_u32 s63, s100, 0\nM 44,180,152\ns_add_u32 m0, s50, 0\nM 156,176,132\nR 172,185,57344\nv_mfma_f32_16x16x32_bf16 v[156:159], v[242:245], v[210:213], v[156:159]\n"
"M 156,180,148\nbuffer_load_dwordx4 v222, s[12:15], s63 offen lds\nM 140,176,128\nR 174,185,61440\nM 140,180,144\nG 4\n"
"M 128,160,160\nR 176,186,49152\nM 128,164,176\nM 144,160,164\nR 178,186,53248\nM 144,164,180\n"
"buffer_load_dwordx4 v223, s[12:15], s63 offen offset:1024 lds\nM 32,160,168\nR 180,186,57344\nM 32,164,184\nM 48,160,172\nR 182,186,61440\n"
"M 48,164,188\nG 4\nM 52,168,172\nR 160,187,49152\nM 52,172,188\nM 36,168,168\n"
"R 162,187,53248\nM 36,172,184\ns_movk_i32 s63, 2048\nv_mad_u32_u24 v220, v228, s63, v229\nv_add_u32 v221, 1047552, v220\ns_add_u32 m0, s50, 8192\n"
"s_add_u32 s63, s53, 0\nbuffer_load_dwordx4 v220, s[8:11], s63 offen lds\nM 148,168,164\nR 164,187,57344\ns_lshl_b32 s60, s40, 12\ns_add_u32 s60, s60, 49152\n"
"v_add_u32 v226, s60, v252\nv_add_u32 v111, s60, v227\nM 148,172,180\nM 132,168,160\nR 166,187,61440\ns_cmp_eq_u32 s40, 0\n"
"s_cselect_b64 s[88:89], 0, -1\ns_cselect_b32 s60, s41, 0\ns_cmp_lg_u32 s60, 0\ns_cselect_b64 s[66:67], -1, 0\nM 132,172,176\nG 4\n"
"M 136,176,160\nR 168,188,0\nM 136,180,176\ns_mov_b64 exec, s[88:89]\nds_read_b128 v[230:233], v226 offset:0\nds_read_b64 v[64:65], v111 offset:2048\n"
"s_mov_b64 exec, -1\nM 152,176,164\nR 170,188,4096\ns_waitcnt vmcnt(11) lgkmcnt(1)\ns_barrier\nM 152,180,180\n"
"buffer_load_dwordx4 v221, s[8:11], s63 offen offset:1024 lds\nM 40,176,168\nR 172,188,8192\nM 40,180,184\ns_mov_b64 exec, s[88:89]\nds_read_b64 v[70:71], v111 offset:2560\n"
"ds_read_b128 v[234:237], v226 offset:1024\ns_mov_b64 exec, -1\nM 56,176,172\nR 174,188,12288\nM 56,180,188\nM 60,160,172\n"
"R 176,189,0\nM 60,164,188\ns_mov_b64 exec, s[88:89]\nds_read_b64 v[76:77], v111 offset:3072\nds_read_b64 v[82:83], v111 offset:3584\ns_mov_b64 exec, -1\n"
"M 44,160,168\nR 178,189,4096\nM 44,164,184\nM 156,160,164\nR 180,189,8192\nM 156,164,180\n"
"s_mov_b64 exec, s[66:67]\nbuffer_load_dwordx4 v[230:233], v252, s[20:23], s65 offen offset:0 sc1\ns_mov_b64 exec, -1\nM 140,160,160\nR 182,189,12288\nM 140,164,176\n"
"G 6\nM 32,168,200\nR 160,190,0\ns_mov_b64 exec, s[66:67]\nbuffer_load_dwordx2 v[64:65], v227, s[20:23], s65 offen offset:2048 sc1\ns_mov_b64 exec, -1\n"
"M 32,172,216\nR 162,190,4096\nM 48,168,204\nR 164,190,8192\nM 48,172,220\nR 166,190,12288\n"
"G 4\nM 52,176,204\nR 168,191,0\ns_mov_b64 exec, s[66:67]\nbuffer_load_dwordx2 v[70:71], v227, s[20:23], s65 offen offset:2560 sc1\ns_mov_b64 exec, -1\n"
"M 52,180,220\nR 170,191,4096\nM 36,176,200\nR 172,191,8192\nM 36,180,216\nR 174,191,12288\n"
"G 4\nM 40,160,200\nR 176,188,16384\ns_mov_b64 exec, s[66:67]\nbuffer_load_dwordx4 v[234:237], v252, s[20:23], s65 offen offset:1024 sc1\ns_mov_b64 exec, -1\n"
"M 40,164,216\nR 178,188,20480\nM 56,160,204\nR 180,188,24576\nM 56,164,220\nR 182,188,28672\n"
"G 4\nM 60,168,204\nR 160,189,16384\ns_mov_b64 exec, s[66:67]\nbuffer_load_dwordx2 v[76:77], v227, s[20:23], s65 offen offset:3072 sc1\ns_mov_b64 exec, -1\n"
"M 60,172,220\nR 162,189,20480\nM 44,168,200\nR 164,189,24576\nM 44,172,216\nR 166,189,28672\n"
"G 4\nM 32,176,232\nR 168,190,16384\ns_mov_b64 exec, s[66:67]\nbuffer_load_dwordx2 v[82:83], v227, s[20:23], s65 offen offset:3584 sc1\ns_mov_b64 exec, -1\n"
"Lq185:\ns_lshr_b32 s60, s45, 3\ns_lshl2_add_u32 s60, s60, s40\ns_lshl_b32 s60, s60, 8\ns_waitcnt vmcnt(11)\nv_lshl_add_u32 v224, v252, 3, s60\n"
"v_add_u32 v224, Lq185+4096-LqPC, v224\nv_min_u32 v224, LqE-LqPC-4, v224\ns_mov_b64 exec, 3\nglobal_load_dword v224, v224, s[98:99]\ns_mov_b64 exec, -1\nM 32,180,248\n"
"R 170,190,20480\nM 48,176,236\nR 172,190,24576\nM 48,180,252\nR 174,190,28672\nG 4\n"
"M 52,160,236\nR 176,191,16384\nM 52,164,252\nR 178,191,20480\nM 36,160,232\nR 180,191,24576\n"
"M 36,164,248\nR 182,191,28672\nG 4\nM 40,168,232\nR 160,188,0\nM 40,172,248\n"
"R 162,188,4096\nM 56,168,236\nR 164,188,8192\nM 56,172,252\nR 166,188,12288\nG 4\n"
"M 60,176,236\nR 168,189,0\nM 60,180,252\nR 170,189,4096\nM 44,176,232\nR 172,189,8192\n"
"M 44,180,248\nR 174,189,12288\nG 4\nM 128,160,192\nR 176,190,0\nM 128,164,208\n"
"R 178,190,4096\ns_mov_b32 m0, s51\nM 144,160,196\nR 180,190,8192\nds_write_addtid_b32 v32 offset:0\nM 144,164,212\n"
"R 182,190,12288\nds_write_addtid_b32 v33 offset:256\nG 6\nM 148,168,196\nR 160,191,0\nds_write_addtid_b32 v34 offset:512\n"
"M 148,172,212\nR 162,191,4096\nds_write_addtid_b32 v35 offset:768\nM 132,168,192\nR 164,191,8192\nds_write_addtid_b32 v36 offset:1024\n"
"Lpg5:\ns_cbranch_execz LretQ0\nM 132,172,208\nR 166,191,12288\nds_write_addtid_b32 v37 offset:1280\nG 9\n"
"M 136,176,192\nR 168,188,16384\nds_write_addtid_b32 v38 offset:1536\nM 136,180,208\nR 170,188,20480\nds_write_addtid_b32 v39 offset:1792\n"
"M 152,176,196\nR 172,188,24576\nds_write_addtid_b32 v40 offset:2048\nM 152,180,212\nR 174,188,28672\nds_write_addtid_b32 v41 offset:2304\n"
"G 9\nM 156,160,196\nR 176,189,16384\nds_write_addtid_b32 v42 offset:2560\nM 156,164,212\nR 178,189,20480\n"
"ds_write_addtid_b32 v43 offset:2816\nM 140,160,192\nR 180,189,24576\nds_write_addtid_b32 v44 offset:3072\nds_write_addtid_b32 v45 offset:3328\nds_write_addtid_b32 v46 offset:3584\n"
"ds_write_addtid_b32 v47 offset:3840\nds_write_addtid_b32 v48 offset:4096\nds_write_addtid_b32 v49 offset:4352\nds_write_addtid_b32 v50 offset:4608\nds_write_addtid_b32 v51 offset:4864\nds_write_addtid_b32 v52 offset:5120\n"
"ds_write_addtid_b32 v53 offset:5376\nds_write_addtid_b32 v54 offset:5632\nds_write_addtid_b32 v55 offset:5888\nds_write_addtid_b32 v56 offset:6144\nds_write_addtid_b32 v57 offset:6400\nds_write_addtid_b32 v58 offset:6656\n"
"ds_write_addtid_b32 v59 offset:6912\nds_write_addtid_b32 v60 offset:7168\nds_write_addtid_b32 v61 offset:7424\nds_write_addtid_b32 v62 offset:7680\nds_write_addtid_b32 v63 offset:7936\nM 140,164,208\n"
"R 182,189,28672\nG 15\nM 128,168,224\nR 160,190,16384\ns_add_u32 m0, s50, 16384\nM 128,172,240\n"
"R 162,190,20480\nG 3\ns_barrier\ns_add_u32 s63, s53, 65536\nM 144,168,228\nR 164,190,24576\n"
"buffer_load_dwordx4 v220, s[8:11], s63 offen lds\nds_read2st64_b32 v[32:33], v225 offset0:0 offset1:1\nM 144,172,244\nR 166,190,28672\nbuffer_load_dwordx4 v221, s[8:11], s63 offen offset:1024 lds\nds_read2st64_b32 v[34:35], v225 offset0:2 offset1:3\n"
"G 6\nM 148,176,228\nR 168,191,16384\ns_add_u32 m0, s50, 24576\ns_add_u32 s63, s53, 131072\nbuffer_load_dwordx4 v220, s[8:11], s63 offen lds\n"
"ds_read2st64_b32 v[36:37], v225 offset0:4 offset1:5\nM 148,180,244\nR 170,191,20480\nbuffer_load_dwordx4 v221, s[8:11], s63 offen offset:1024 lds\nds_read2st64_b32 v[38:39], v225 offset0:6 offset1:7\nM 132,176,224\n"
"R 172,191,24576\ns_add_u32 m0, s50, 32768\ns_add_u32 s63, s53, 196608\nbuffer_load_dwordx4 v220, s[8:11], s63 offen lds\nds_read2st64_b32 v[40:41], v225 offset0:8 offset1:9\nM 132,180,240\n"
"R 174,191,28672\nbuffer_load_dwordx4 v221, s[8:11], s63 offen offset:1024 lds\nds_read2st64_b32 v[42:43], v225 offset0:10 offset1:11\nG 9\nM 136,160,224\ns_add_u32 m0, s50, 40960\n"
"s_add_u32 s63, s53, 262144\nbuffer_load_dwordx4 v220, s[8:11], s63 offen lds\nds_read2st64_b32 v[44:45], v225 offset0:12 offset1:13\nM 136,164,240\nbuffer_load_dwordx4 v221, s[8:11], s63 offen offset:1024 lds\nds_read2st64_b32 v[46:47], v225 offset0:14 offset1:15\n"
"M 152,160,228\ns_add_u32 m0, s50, 49152\ns_add_u32 s63, s53, 327680\nbuffer_load_dwordx4 v220, s[8:11], s63 offen lds\nds_read2st64_b32 v[48:49], v225 offset0:16 offset1:17\nM 152,164,244\n"
"ds_read2st64_b32 v[50:51], v225 offset0:18 offset1:19\nds_read2st64_b32 v[52:53], v225 offset0:20 offset1:21\nG 6\nM 156,168,228\nds_read2st64_b32 v[54:55], v225 offset0:22 offset1:23\nds_read2st64_b32 v[56:57], v225 offset0:24 offset1:25\n"
"M 156,172,244\nds_read2st64_b32 v[58:59], v225 offset0:26 offset1:27\nds_read2st64_b32 v[60:61], v225 offset0:28 offset1:29\nM 140,168,224\nds_read2st64_b32 v[62:63], v225 offset0:30 offset1:31\nM 140,172,240\n"
"G 0\ns_barrier\nv_pk_add_f32 v[144:145], v[144:145], v[48:49]\nv_pk_add_f32 v[146:147], v[146:147], v[50:51]\nv_pk_add_f32 v[148:149], v[148:149], v[52:53]\nv_pk_add_f32 v[150:151], v[150:151], v[54:55]\n"
"v_pk_add_f32 v[152:153], v[152:153], v[56:57]\nv_pk_add_f32 v[154:155], v[154:155], v[58:59]\nv_pk_add_f32 v[156:157], v[156:157], v[60:61]\nv_pk_add_f32 v[158:159], v[158:159], v[62:63]\nv_pk_add_f32 v[128:129], v[128:129], v[32:33]\nv_pk_add_f32 v[130:131], v[130:131], v[34:35]\n"
"v_pk_add_f32 v[132:133], v[132:133], v[36:37]\nv_pk_add_f32 v[134:135], v[134:135], v[38:39]\nv_pk_add_f32 v[136:137], v[136:137], v[40:41]\nv_pk_add_f32 v[138:139], v[138:139], v[42:43]\nv_pk_add_f32 v[140:141], v[140:141], v[44:45]\nv_pk_add_f32 v[142:143], v[142:143], v[46:47]\n"
"v_cvt_pk_bf16_f32 v144, v144, v145\nv_cvt_pk_bf16_f32 v145, v146, v147\nv_cvt_pk_bf16_f32 v146, v148, v149\nv_cvt_pk_bf16_f32 v147, v150, v151\nv_cvt_pk_bf16_f32 v152, v152, v153\nv_cvt_pk_bf16_f32 v153, v154, v155\n"
"v_cvt_pk_bf16_f32 v154, v156, v157\nv_cvt_pk_bf16_f32 v155, v158, v159\nv_cvt_pk_bf16_f32 v128, v128, v129\nv_cvt_pk_bf16_f32 v129, v130, v131\nv_cvt_pk_bf16_f32 v130, v132, v133\nv_cvt_pk_bf16_f32 v131, v134, v135\n"
"v_cvt_pk_bf16_f32 v136, v136, v137\nv_cvt_pk_bf16_f32 v137, v138, v139\nv_cvt_pk_bf16_f32 v138, v140, v141\nv_cvt_pk_bf16_f32 v139, v142, v143\nv_mfma_f32_16x16x32_bf16 v[52:55], v[16:19], v[144:147], v[96:99]\nv_mfma_f32_16x16x32_bf16 v[48:51], v[0:3], v[144:147], v[100:103]\n"
"v_mfma_f32_16x16x32_bf16 v[60:63], v[20:23], v[152:155], v[96:99]\nv_mfma_f32_16x16x32_bf16 v[56:59], v[4:7], v[152:155], v[100:103]\ns_waitcnt vmcnt(13)\nv_max3_u32 v248, v233, v65, v71\nv_cmp_eq_u32 vcc, -1, v248\ns_nop 0\n"
"s_cbranch_vccnz Lq187\nLq188:\ns_waitcnt vmcnt(10)\nv_max3_u32 v248, v237, v77, v83\nv_cmp_eq_u32 vcc, -1, v248\ns_nop 0\n"
"s_cbranch_vccnz Lq189\nLq190:\nv_mfma_f32_16x16x32_bf16 v[36:39], v[0:3], v[128:131], v[96:99]\nv_mfma_f32_16x16x32_bf16 v[32:35], v[230:233], v[128:131], v[104:107]\nv_mfma_f32_16x16x32_bf16 v[44:47], v[4:7], v[136:139], v[96:99]\nv_mfma_f32_16x16x32_bf16 v[40:43], v[234:237], v[136:139], v[104:107]\n"
"s_lshl_b32 s84, s42, 11\ns_add_u32 s84, s84, s49\ns_add_u32 s84, s84, 2097152\ns_add_u32 s85, s84, 32768\ns_lshl2_add_u32 s60, s45, s40\ns_waitcnt vmcnt(9)\n"
"v_lshlrev_b32 v224, 3, v252\ns_mul_i32 s60, s60, 7168\ns_bfm_b64 exec, 28, 28\nbuffer_load_dword v224, v224, s[16:19], s60 offen\ns_mov_b64 exec, -1\nv_mov_b32 v238, s46\n"
"v_mov_b32 v239, s47\nv_max_f32 v52, v52, v53\nv_max_f32 v54, v54, v55\nv_max3_f32 v52, v52, v48, v49\nv_max3_f32 v54, v54, v50, v51\nv_max_f32 v118, v52, v54\n"
"v_max_f32 v60, v60, v61\nv_max_f32 v62, v62, v63\nv_max3_f32 v60, v60, v56, v57\nv_max3_f32 v62, v62, v58, v59\nv_max_f32 v119, v60, v62\nv_max_f32 v36, v36, v37\n"
"v_max_f32 v38, v38, v39\nv_max3_f32 v36, v36, v32, v33\nv_max3_f32 v38, v38, v34, v35\nv_max_f32 v116, v36, v38\nv_max_f32 v44, v44, v45\nv_max_f32 v46, v46, v47\n"
"v_max3_f32 v44, v44, v40, v41\nv_max3_f32 v46, v46, v42, v43\nv_max_f32 v117, v44, v46\nv_permlane32_swap_b32 v116, v118\ns_nop 0\nv_permlane32_swap_b32 v117, v119\n"
"v_max_f32 v116, v116, v118\nv_max_f32 v117, v117, v119\ns_nop 1\nv_permlane16_swap_b32 v116, v117\nv_max_f32 v116, v116, v117\nv_mov_b32 v117, v116\n"
"s_nop 1\nv_permlane16_swap_b32 v116, v117\nv_mov_b32 v118, v116\nv_mov_b32 v119, v117\ns_nop 0\nv_permlane32_swap_b32 v116, v118\n"
"v_permlane32_swap_b32 v117, v119\nv_mul_f32 v116, v116, v215\nv_mul_f32 v118, v118, v218\nv_mul_f32 v117, v117, v215\nv_mul_f32 v119, v119, v218\nv_fma_f32 v52, v52, v218, -v118\n"
"v_fma_f32 v54, v54, v218, -v118\nv_fma_f32 v60, v60, v218, -v119\nv_fma_f32 v62, v62, v218, -v119\nv_fma_f32 v36, v36, v215, -v116\nv_fma_f32 v38, v38, v215, -v116\nv_fma_f32 v44, v44, v215, -v117\n"
"v_fma_f32 v46, v46, v215, -v117\nv_exp_f32 v52, v52\nv_exp_f32 v54, v54\nv_exp_f32 v60, v60\nv_exp_f32 v62, v62\nv_exp_f32 v36, v36\n"
"v_exp_f32 v38, v38\nv_exp_f32 v44, v44\nv_exp_f32 v46, v46\nv_add_f32 v118, v52, v54\nv_add_f32 v119, v60, v62\nv_add_f32 v116, v36, v38\n"
"v_add_f32 v117, v44, v46\ns_nop 0\nv_permlane32_swap_b32 v116, v118\nv_permlane32_swap_b32 v117, v119\nv_add_f32 v116, v116, v118\nv_add_f32 v117, v117, v119\n"
"s_nop 1\nv_permlane16_swap_b32 v116, v117\nv_add_f32 v116, v116, v117\nv_rcp_f32 v116, v116\ns_nop 0\nv_mov_b32 v117, v116\n"
"s_nop 1\nv_permlane16_swap_b32 v116, v117\nv_mov_b32 v118, v116\nv_mov_b32 v119, v117\ns_nop 0\nv_permlane32_swap_b32 v116, v118\n"
"v_permlane32_swap_b32 v117, v119\nv_mul_f32 v52, v52, v118\nv_mul_f32 v54, v54, v118\nv_mul_f32 v60, v60, v119\nv_mul_f32 v62, v62, v119\nv_mul_f32 v36, v36, v116\n"
"v_mul_f32 v38, v38, v116\nv_mul_f32 v44, v44, v117\nv_mul_f32 v46, v46, v117\nv_cvt_pk_bf16_f32 v52, v52, v52\nv_cvt_pk_bf16_f32 v54, v54, v54\nv_cvt_pk_bf16_f32 v60, v60, v60\n"
"v_cvt_pk_bf16_f32 v62, v62, v62\nv_cvt_pk_bf16_f32 v36, v36, v36\nv_cvt_pk_bf16_f32 v38, v38, v38\nv_cvt_pk_bf16_f32 v44, v44, v44\nv_cvt_pk_bf16_f32 v46, v46, v46\nv_and_b32 v48, v52, v112\n"
"v_and_b32 v49, v54, v113\nv_and_b32 v50, v52, v114\nv_and_b32 v51, v54, v115\nv_and_b32 v56, v60, v112\nv_and_b32 v57, v62, v113\nv_and_b32 v58, v60, v114\n"
"v_and_b32 v59, v62, v115\nv_mfma_f32_16x16x32_bf16 v[144:147], v[66:69], v[48:51], 0\nv_mfma_f32_16x16x32_bf16 v[148:151], v[72:75], v[48:51], 0\nv_and_b32 v32, v36, v112\nv_and_b32 v33, v38, v113\nv_and_b32 v34, v36, v114\n"
"v_and_b32 v35, v38, v115\nv_mfma_f32_16x16x32_bf16 v[152:155], v[78:81], v[56:59], 0\nv_mfma_f32_16x16x32_bf16 v[156:159], v[84:87], v[56:59], 0\nv_and_b32 v40, v44, v112\nv_and_b32 v41, v46, v113\nv_and_b32 v42, v44, v114\n"
"v_and_b32 v43, v46, v115\nv_mfma_f32_16x16x32_bf16 v[128:131], v[64:67], v[32:35], 0\nv_mfma_f32_16x16x32_bf16 v[132:135], v[70:73], v[32:35], 0\nv_mfma_f32_16x16x32_bf16 v[136:139], v[76:79], v[40:43], 0\nv_mfma_f32_16x16x32_bf16 v[140:143], v[82:85], v[40:43], 0\nv_cvt_pk_bf16_f32 v144, v144, v145\n"
"v_cvt_pk_bf16_f32 v145, v146, v147\nv_cvt_pk_bf16_f32 v146, v148, v149\nv_cvt_pk_bf16_f32 v147, v150, v151\nbuffer_store_dwordx4 v[144:147], v252, s[20:23], s85 offen offset:0 sc1\nv_cvt_pk_bf16_f32 v152, v152, v153\nv_cvt_pk_bf16_f32 v153, v154, v155\n"
"v_cvt_pk_bf16_f32 v154, v156, v157\nv_cvt_pk_bf16_f32 v155, v158, v159\nbuffer_store_dwordx4 v[152:155], v252, s[20:23], s85 offen offset:1024 sc1\nv_cvt_pk_bf16_f32 v128, v128, v129\nv_cvt_pk_bf16_f32 v129, v130, v131\nv_cvt_pk_bf16_f32 v130, v132, v133\n"
"v_cvt_pk_bf16_f32 v131, v134, v135\nbuffer_store_dwordx4 v[128:131], v252, s[20:23], s84 offen offset:0 sc1\nv_cvt_pk_bf16_f32 v136, v136, v137\nv_cvt_pk_bf16_f32 v137, v138, v139\nv_cvt_pk_bf16_f32 v138, v140, v141\nv_cvt_pk_bf16_f32 v139, v142, v143\n"
"buffer_store_dwordx4 v[136:139], v252, s[20:23], s84 offen offset:1024 sc1\ns_mov_b64 exec, 0\ns_cmp_eq_u32 s40, 1\ns_cbranch_scc1 Lpg7\ns_cmp_eq_u32 s40, 2\ns_cbranch_scc1 LretO0\n"
"s_cmp_eq_u32 s40, 3\ns_cbranch_scc1 LretO0\ns_branch Lpg6\nLretO0:\ns_mov_b64 exec, -1\ns_add_u32 s61, s79, 88080384\n"
"s_lshl_b32 s62, s42, 3\ns_add_u32 s61, s61, s62\ns_waitcnt vmcnt(0)\ns_mov_b64 exec, 1\nbuffer_atomic_swap_x2 v[238:239], off, s[20:23], s61 sc1\ns_mov_b64 exec, -1\n"
"v_bfe_u32 v250, v252, 7, 1\nv_mul_u32_u24 v250, 2176, v250\nv_bfe_u32 v251, v252, 4, 3\nv_lshl_add_u32 v250, v251, 3, v250\ns_lshl_b32 s60, s90, 6\nv_add_u32 v250, s60, v250\n"
"s_sleep 5\nv_mov_b32 v248, 0\nv_mov_b32 v249, 0\ns_add_u32 s61, s80, 88080384\ns_mov_b64 exec, 0xffff\nbuffer_atomic_or_x2 v[248:249], v250, s[20:23], s61 offen sc0 sc1\n"
"s_mov_b64 exec, -1\nbuffer_load_dwordx4 v221, s[8:11], s63 offen offset:1024 lds\ns_add_u32 m0, s50, 57344\ns_add_u32 s63, s53, 393216\nbuffer_load_dwordx4 v220, s[8:11], s63 offen lds\nbuffer_load_dwordx4 v221, s[8:11], s63 offen offset:1024 lds\n"
"s_add_u32 m0, s50, 65536\ns_add_u32 s63, s53, 458752\nbuffer_load_dwordx4 v220, s[8:11], s63 offen lds\nbuffer_load_dwordx4 v221, s[8:11], s63 offen offset:1024 lds\ns_add_u32 m0, s50, 73728\ns_add_u32 s63, s53, 524288\n"
"buffer_load_dwordx4 v220, s[8:11], s63 offen lds\nbuffer_load_dwordx4 v221, s[8:11], s63 offen offset:1024 lds\ns_add_u32 m0, s50, 81920\ns_add_u32 s63, s53, 589824\nbuffer_load_dwordx4 v220, s[8:11], s63 offen lds\nbuffer_load_dwordx4 v221, s[8:11], s63 offen offset:1024 lds\n"
"s_add_u32 m0, s50, 90112\ns_add_u32 s63, s53, 655360\nbuffer_load_dwordx4 v220, s[8:11], s63 offen lds\nbuffer_load_dwordx4 v221, s[8:11], s63 offen offset:1024 lds\nv_bfe_u32 v234, v252, 4, 4\nv_lshrrev_b32 v235, 8, v252\n"
"v_lshlrev_b32 v235, 2, v235\nv_sub_u32 v234, v234, v235\nv_lshlrev_b32 v235, 4, v234\ns_mov_b64 s[60:61], 0x3f80\nv_lshlrev_b64 v[208:209], v235, s[60:61]\nv_cmp_gt_u32 s[60:61], 4, v234\n"
"s_nop 1\nv_cndmask_b32 v212, 0, v208, s[60:61]\nv_cndmask_b32 v208, 0, v208, s[60:61]\nv_cndmask_b32 v213, 0, v209, s[60:61]\nv_cndmask_b32 v209, 0, v209, s[60:61]\ns_nop 1\n"
"v_mfma_f32_16x16x32_bf16 v[0:3], v[208:211], v[192:195], 0\nv_mfma_f32_16x16x32_bf16 v[4:7], v[210:213], v[192:195], 0\nv_mfma_f32_16x16x32_bf16 v[8:11], v[208:211], v[196:199], 0\nv_mfma_f32_16x16x32_bf16 v[12:15], v[210:213], v[196:199], 0\nv_mfma_f32_16x16x32_bf16 v[16:19], v[208:211], v[200:203], 0\nv_mfma_f32_16x16x32_bf16 v[20:23], v[210:213], v[200:203], 0\n"
"v_mfma_f32_16x16x32_bf16 v[24:27], v[208:211], v[204:207], 0\nv_mfma_f32_16x16x32_bf16 v[28:31], v[210:213], v[204:207], 0\ns_waitcnt vmcnt(11)\nv_cmp_eq_u64 s[86:87], s[46:47], v[248:249]\ns_nop 1\ns_and_b32 s62, s86, 0xffff\n"
"s_cmp_eq_u32 s62, 0xffff\ns_cbranch_scc1 Lq193\nLq192:\nv_mov_b32 v248, 0\nv_mov_b32 v249, 0\ns_add_u32 s61, s80, 88080384\n"
"s_mov_b64 exec, 0xffff\nbuffer_atomic_or_x2 v[248:249], v250, s[20:23], s61 offen sc0 sc1\ns_mov_b64 exec, -1\ns_waitcnt vmcnt(0)\nv_cmp_eq_u64 s[86:87], s[46:47], v[248:249]\ns_nop 1\n"
"s_and_b32 s62, s86, 0xffff\ns_cmp_eq_u32 s62, 0xffff\ns_cbranch_scc1 Lq193\ns_sub_u32 s48, s48, 1\ns_cmp_gt_i32 s48, 0\ns_cbranch_scc1 Lq192\n"
"Lq193:\ns_add_u32 s64, s70, 2097152\nbuffer_load_dwordx4 a[0:3], v252, s[20:23], s64 offen offset:0 sc1\ns_add_u32 s65, s70, 2129920\nbuffer_load_dwordx4 a[4:7], v252, s[20:23], s65 offen offset:0 sc1\ns_add_u32 s66, s71, 2097152\n"
"buffer_load_dwordx4 a[8:11], v252, s[20:23], s66 offen offset:0 sc1\ns_add_u32 s67, s71, 2129920\nbuffer_load_dwordx4 a[12:15], v252, s[20:23], s67 offen offset:0 sc1\nbuffer_load_dwordx4 a[16:19], v252, s[20:23], s64 offen offset:1024 sc1\nbuffer_load_dwordx4 a[20:23], v252, s[20:23], s65 offen offset:1024 sc1\nbuffer_load_dwordx4 a[24:27], v252, s[20:23], s66 offen offset:1024 sc1\n"
"buffer_load_dwordx4 a[28:31], v252, s[20:23], s67 offen offset:1024 sc1\ns_barrier\nR 160,184,8192\nR 162,184,12288\nR 164,184,16384\nR 166,184,20480\n"
"R 168,185,8192\nR 170,185,12288\nR 172,185,16384\nR 174,185,20480\nR 176,186,8192\nR 178,186,12288\n"
"R 180,186,16384\nR 182,186,20480\nbuffer_load_dwordx4 a[32:35], v252, s[20:23], s64 offen offset:2048 sc1\nbuffer_load_dwordx4 a[36:39], v252, s[20:23], s65 offen offset:2048 sc1\nbuffer_load_dwordx4 a[40:43], v252, s[20:23], s66 offen offset:2048 sc1\nbuffer_load_dwordx4 a[44:47], v252, s[20:23], s67 offen offset:2048 sc1\n"
"buffer_load_dwordx4 a[48:51], v252, s[20:23], s64 offen offset:3072 sc1\nbuffer_load_dwordx4 a[52:55], v252, s[20:23], s65 offen offset:3072 sc1\nbuffer_load_dwordx4 a[56:59], v252, s[20:23], s66 offen offset:3072 sc1\nbuffer_load_dwordx4 a[60:63], v252, s[20:23], s67 offen offset:3072 sc1\ns_add_u32 s64, s70, 2101248\nbuffer_load_dwordx4 a[64:67], v252, s[20:23], s64 offen offset:0 sc1\n"
"s_add_u32 s65, s70, 2134016\nbuffer_load_dwordx4 a[68:71], v252, s[20:23], s65 offen offset:0 sc1\ns_add_u32 s66, s71, 2101248\nbuffer_load_dwordx4 a[72:75], v252, s[20:23], s66 offen offset:0 sc1\ns_add_u32 s67, s71, 2134016\nbuffer_load_dwordx4 a[76:79], v252, s[20:23], s67 offen offset:0 sc1\n"
"s_waitcnt vmcnt(19) lgkmcnt(10)\nM 0,160,0\nbuffer_load_dwordx4 a[80:83], v252, s[20:23], s64 offen offset:1024 sc1\ns_waitcnt vmcnt(16) lgkmcnt(8)\nM 0,164,16\nM 16,160,4\n"
"s_lshl_b32 s60, s40, 5\nv_xor_b32 v238, s60, v188\nv_and_b32 v238, 0xfffffbff, v238\ns_waitcnt vmcnt(15)\nM 16,164,20\nv_mov_b32 v242, s76\n"
"v_mov_b32 v243, s76\nv_mfma_f32_16x16x32_bf16 v[32:35], v[160:163], a[8:11], 0\nbuffer_load_dwordx4 a[84:87], v252, s[20:23], s65 offen offset:1024 sc1\ns_waitcnt vmcnt(15)\nM 32,164,24\nv_mov_b32 v244, s76\n"
"v_mov_b32 v245, s76\nv_mfma_f32_16x16x32_bf16 v[48:51], v[160:163], a[12:15], 0\ns_waitcnt vmcnt(14)\nM 48,164,28\nG 4\nv_mfma_f32_16x16x32_bf16 v[52:55], v[168:171], a[12:15], 0\n"
"R 160,187,8192\nM 52,172,28\nbuffer_load_dwordx4 a[88:91], v252, s[20:23], s66 offen offset:1024 sc1\nv_mfma_f32_16x16x32_bf16 v[36:39], v[168:171], a[8:11], 0\nR 162,187,12288\nM 36,172,24\n"
"M 20,168,4\nR 164,187,16384\nR 64,238,32768\nR 66,238,36864\nM 20,172,20\nbuffer_load_dwordx4 a[92:95], v252, s[20:23], s67 offen offset:1024 sc1\n"
"M 4,168,0\nR 166,187,20480\nR 68,238,33792\nR 70,238,37888\nM 4,172,16\nG 8\n"
"M 8,176,0\nR 168,184,24576\nM 8,180,16\nbuffer_load_dwordx4 a[96:99], v252, s[20:23], s64 offen offset:2048 sc1\nM 24,176,4\nR 170,184,28672\n"
"M 24,180,20\nv_mfma_f32_16x16x32_bf16 v[40:43], v[176:179], a[8:11], 0\nR 172,184,32768\nG 6\nv_mfma_f32_16x16x32_bf16 v[72:75], v[242:245], v[64:67], 0\nM 40,180,24\n"
"G 3\nv_mfma_f32_16x16x32_bf16 v[72:75], v[242:245], v[68:71], v[72:75]\nbuffer_load_dwordx4 a[100:103], v252, s[20:23], s65 offen offset:2048 sc1\nv_mfma_f32_16x16x32_bf16 v[56:59], v[176:179], a[12:15], 0\nR 174,184,36864\nM 56,180,28\n"
"v_mfma_f32_16x16x32_bf16 v[60:63], v[160:163], a[12:15], 0\nR 176,185,24576\nM 60,164,28\nbuffer_load_dwordx4 a[104:107], v252, s[20:23], s66 offen offset:2048 sc1\nv_lshrrev_b32 v64, 1, v252\nv_mfma_f32_16x16x32_bf16 v[44:47], v[160:163], a[8:11], 0\n"
"R 178,185,28672\nv_mov_b32 v73, s46\ns_and_b32 s61, s40, 3\ns_lshl_b32 s61, s61, 7\ns_add_u32 s61, s61, s91\ns_add_u32 s61, s61, 393216\n"
"s_mov_b64 exec, 0xffff\nbuffer_store_dwordx2 v[72:73], v64, s[20:23], s61 offen sc1\ns_mov_b64 exec, -1\nM 44,164,24\nM 28,160,4\nR 180,185,32768\n"
"M 28,164,20\nbuffer_load_dwordx4 a[108:111], v252, s[20:23], s67 offen offset:2048 sc1\nM 12,160,0\nR 182,185,36864\nM 12,164,16\ns_lshl_b32 s60, s40, 5\n"
"v_xor_b32 v238, s60, v188\nG 4\ns_waitcnt vmcnt(20)\nM 0,168,32\nR 160,186,24576\nv_and_b32 v238, 0xfffffbff, v238\n"
"s_waitcnt vmcnt(16)\nM 0,172,48\nbuffer_load_dwordx4 a[112:115], v252, s[20:23], s64 offen offset:3072 sc1\nM 16,168,36\nR 162,186,28672\ns_waitcnt vmcnt(16)\n"
"M 16,172,52\nM 32,168,40\nR 164,186,32768\ns_waitcnt vmcnt(15)\nM 32,172,56\nbuffer_load_dwordx4 a[116:119], v252, s[20:23], s65 offen offset:3072 sc1\n"
"s_barrier\nM 48,168,44\nR 166,186,36864\nR 64,238,40960\nR 66,238,45056\ns_waitcnt vmcnt(15)\n"
"M 48,172,60\nG 6\nM 52,176,44\nR 168,187,24576\nR 68,238,41984\nR 70,238,46080\n"
"M 52,180,60\nbuffer_load_dwordx4 a[120:123], v252, s[20:23], s66 offen offset:3072 sc1\nM 36,176,40\nR 170,187,28672\nM 36,180,56\nM 20,176,36\n"
"R 172,187,32768\nM 20,180,52\nbuffer_load_dwordx4 a[124:127], v252, s[20:23], s67 offen offset:3072 sc1\nM 4,176,32\nR 174,187,36864\nG 6\n"
"v_mfma_f32_16x16x32_bf16 v[72:75], v[242:245], v[64:67], 0\nM 4,180,48\nG 3\nv_mfma_f32_16x16x32_bf16 v[72:75], v[242:245], v[68:71], v[72:75]\nM 8,160,32\nR 176,184,40960\n"
"M 8,164,48\ns_add_u32 s64, s70, 2105344\nbuffer_load_dwordx4 a[128:131], v252, s[20:23], s64 offen offset:0 sc1\nM 24,160,36\nR 178,184,45056\nM 24,164,52\n"
"v_lshrrev_b32 v64, 1, v252\nv_mov_b32 v73, s46\ns_and_b32 s61, s40, 3\ns_lshl_b32 s61, s61, 7\ns_add_u32 s61, s61, s91\ns_add_u32 s61, s61, 524288\n"
"s_mov_b64 exec, 0xffff\nbuffer_store_dwordx2 v[72:73], v64, s[20:23], s61 offen sc1\ns_mov_b64 exec, -1\nM 40,160,40\nR 180,184,49152\nM 40,164,56\n"
"s_add_u32 s65, s70, 2138112\nbuffer_load_dwordx4 a[132:135], v252, s[20:23], s65 offen offset:0 sc1\nM 56,160,44\nR 182,184,53248\nM 56,164,60\ns_lshl_b32 s60, s40, 5\n"
"v_xor_b32 v238, s60, v188\nG 4\nM 60,168,44\nR 160,185,40960\nLpg6:\ns_cbranch_execz LretO0\n"
"v_and_b32 v238, 0xfffffbff, v238\nM 60,172,60\ns_add_u32 s66, s71, 2105344\nbuffer_load_dwordx4 a[136:139], v252, s[20:23], s66 offen offset:0 sc1\nM 44,168,40\nR 162,185,45056\n"
"M 44,172,56\nM 28,168,36\nR 164,185,49152\nM 28,172,52\ns_add_u32 s67, s71, 2138112\nbuffer_load_dwordx4 a[140:143], v252, s[20:23], s67 offen offset:0 sc1\n"
"M 12,168,32\nR 166,185,53248\nM 12,172,48\ns_barrier\nG 4\ns_waitcnt vmcnt(21)\n"
"M 0,176,64\nR 168,186,40960\nR 64,238,49152\nR 66,238,53248\ns_waitcnt vmcnt(17)\nM 0,180,80\n"
"buffer_load_dwordx4 a[144:147], v252, s[20:23], s64 offen offset:1024 sc1\nM 16,176,68\nR 170,186,45056\nR 68,238,50176\nR 70,238,54272\ns_waitcnt vmcnt(17)\n"
"M 16,180,84\nM 32,176,72\nR 172,186,49152\ns_waitcnt vmcnt(16)\nM 32,180,88\nbuffer_load_dwordx4 a[148:151], v252, s[20:23], s65 offen offset:1024 sc1\n"
"M 48,176,76\nR 174,186,53248\ns_add_u32 m0, s50, 98304\ns_waitcnt vmcnt(16)\nM 48,180,92\nG 8\n"
"M 52,160,76\nR 176,187,40960\nG 6\nv_mfma_f32_16x16x32_bf16 v[72:75], v[242:245], v[64:67], 0\ns_add_u32 s63, s53, 720896\nM 52,164,92\n"
"G 3\nv_mfma_f32_16x16x32_bf16 v[72:75], v[242:245], v[68:71], v[72:75]\nbuffer_load_dwordx4 a[152:155], v252, s[20:23], s66 offen offset:1024 sc1\nM 36,160,72\nR 178,187,45056\nM 36,164,88\n"
"buffer_load_dwordx4 v220, s[8:11], s63 offen lds\nM 20,160,68\nR 180,187,49152\nM 20,164,84\nbuffer_load_dwordx4 a[156:159], v252, s[20:23], s67 offen offset:1024 sc1\nv_lshrrev_b32 v64, 1, v252\n"
"M 4,160,64\nR 182,187,53248\nv_mov_b32 v73, s46\ns_and_b32 s61, s40, 3\ns_lshl_b32 s61, s61, 7\ns_add_u32 s61, s61, s91\n"
"s_add_u32 s61, s61, 655360\ns_mov_b64 exec, 0xffff\nbuffer_store_dwordx2 v[72:73], v64, s[20:23], s61 offen sc1\ns_mov_b64 exec, -1\nM 4,164,80\nG 4\n"
"M 8,168,64\nR 160,184,57344\nM 8,172,80\nbuffer_load_dwordx4 a[160:163], v252, s[20:23], s64 offen offset:2048 sc1\nM 24,168,68\nR 162,184,61440\n"
"buffer_load_dwordx4 v221, s[8:11], s63 offen offset:1024 lds\nM 24,172,84\nM 40,168,72\nR 164,188,0\nM 40,172,88\nbuffer_load_dwordx4 a[164:167], v252, s[20:23], s65 offen offset:2048 sc1\n"
"M 56,168,76\nR 166,188,4096\nM 56,172,92\ns_and_b32 s60, s41, 1\ns_lshl1_add_u32 s60, s60, s90\ns_lshl_b32 s60, s60, 5\n"
"v_xor_b32 v238, s60, v184\nG 4\nM 60,176,76\nR 168,185,57344\nv_and_b32 v238, 0xfffffbff, v238\nM 60,180,92\n"
"buffer_load_dwordx4 a[168:171], v252, s[20:23], s66 offen offset:2048 sc1\nM 44,176,72\nR 170,185,61440\nR 64,238,0\nR 66,238,4096\nM 44,180,88\n"
"s_add_u32 m0, s50, 106496\nM 28,176,68\nR 172,189,0\nR 68,238,1024\nR 70,238,5120\nM 28,180,84\n"
"buffer_load_dwordx4 a[172:175], v252, s[20:23], s67 offen offset:2048 sc1\nM 12,176,64\nR 174,189,4096\ns_add_u32 s63, s53, 786432\nM 12,180,80\ns_barrier\n"
"G 8\ns_waitcnt vmcnt(24)\nM 0,160,96\nR 176,186,57344\ns_waitcnt vmcnt(19)\nM 0,164,112\n"
"buffer_load_dwordx4 a[176:179], v252, s[20:23], s64 offen offset:3072 sc1\nbuffer_load_dwordx4 v220, s[8:11], s63 offen lds\nM 16,160,100\nR 178,186,61440\nG 6\nv_mfma_f32_16x16x32_bf16 v[72:75], v[242:245], v[64:67], 0\n"
"s_waitcnt vmcnt(20)\nM 16,164,116\nG 3\nv_mfma_f32_16x16x32_bf16 v[72:75], v[242:245], v[68:71], v[72:75]\nbuffer_load_dwordx4 v221, s[8:11], s63 offen offset:1024 lds\nM 32,160,104\n"
"R 180,190,0\ns_lshr_b32 s60, s45, 3\ns_lshl2_add_u32 s60, s60, s40\ns_lshl_b32 s60, s60, 8\nv_lshl_add_u32 v224, v252, 3, s60\nv_add_u32 v224, Lqw0+24576-LqPC, v224\n"
"v_min_u32 v224, LqE-LqPC-4, v224\ns_mov_b64 exec, 3\nglobal_load_dword v224, v224, s[98:99]\ns_mov_b64 exec, -1\ns_add_u32 s63, s53, 851968\ns_waitcnt vmcnt(21)\n"
"M 32,164,120\nbuffer_load_dwordx4 a[180:183], v252, s[20:23], s65 offen offset:3072 sc1\nM 48,160,108\nR 182,190,4096\ns_add_u32 m0, s50, 114688\ns_waitcnt vmcnt(21)\n"
"M 48,164,124\nbuffer_load_dwordx4 v220, s[8:11], s63 offen lds\nG 4\nv_lshrrev_b32 v64, 1, v252\nM 52,168,108\nR 160,187,57344\n"
"v_mov_b32 v73, s46\ns_and_b32 s61, s40, 1\ns_lshl_b32 s61, s61, 7\ns_add_u32 s61, s61, s91\ns_add_u32 s61, s61, 786432\ns_mov_b64 exec, 0xffff\n"
"buffer_store_dwordx2 v[72:73], v64, s[20:23], s61 offen sc1\ns_mov_b64 exec, -1\nM 52,172,124\nbuffer_load_dwordx4 a[184:187], v252, s[20:23], s66 offen offset:3072 sc1\nM 36,168,104\nR 162,187,61440\n"
"M 36,172,120\nbuffer_load_dwordx4 v221, s[8:11], s63 offen offset:1024 lds\nM 20,168,100\nR 164,191,0\nM 20,172,116\nbuffer_load_dwordx4 a[188:191], v252, s[20:23], s67 offen offset:3072 sc1\n"
"M 4,168,96\nR 166,191,4096\nM 4,172,112\nG 4\nM 8,176,96\nR 168,188,8192\n"
"M 8,180,112\ns_add_u32 s64, s70, 2109440\nbuffer_load_dwordx4 a[192:195], v252, s[20:23], s64 offen offset:0 sc1\nM 24,176,100\nR 170,188,12288\nM 24,180,116\n"
"M 40,176,104\nR 172,188,16384\nM 40,180,120\ns_add_u32 s65, s70, 2142208\nbuffer_load_dwordx4 a[196:199], v252, s[20:23], s65 offen offset:0 sc1\nM 56,176,108\n"
"R 174,188,20480\nM 56,180,124\nG 4\nM 60,160,108\nR 176,189,8192\nM 60,164,124\n"
"s_add_u32 s66, s71, 2109440\nbuffer_load_dwordx4 a[200:203], v252, s[20:23], s66 offen offset:0 sc1\nM 44,160,104\nR 178,189,12288\ns_add_u32 s63, s53, 917504\nM 44,164,120\n"
"s_add_u32 m0, s50, 0\nM 28,160,100\nR 180,189,16384\nM 28,164,116\ns_add_u32 s67, s71, 2142208\nbuffer_load_dwordx4 a[204:207], v252, s[20:23], s67 offen offset:0 sc1\n"
"M 12,160,96\nR 182,189,20480\nM 12,164,112\ns_waitcnt vmcnt(17)\ns_barrier\nbuffer_load_dwordx4 v220, s[8:11], s63 offen lds\n"
"G 4\nM 0,168,128\nR 160,190,8192\nbuffer_load_dwordx4 v221, s[8:11], s63 offen offset:1024 lds\nM 0,172,144\nbuffer_load_dwordx4 a[208:211], v252, s[20:23], s64 offen offset:1024 sc1\n"
"s_add_u32 m0, s50, 8192\ns_add_u32 s63, s53, 983040\nbuffer_load_dwordx4 v220, s[8:11], s63 offen lds\nM 16,168,132\nR 162,190,12288\nbuffer_load_dwordx4 v221, s[8:11], s63 offen offset:1024 lds\n"
"M 16,172,148\nM 32,168,136\nR 164,190,16384\nM 32,172,152\nbuffer_load_dwordx4 a[212:215], v252, s[20:23], s65 offen offset:1024 sc1\nM 48,168,140\n"
"R 166,190,20480\nM 48,172,156\nG 4\nM 52,176,140\nR 168,191,8192\nM 52,180,156\n"
"buffer_load_dwordx4 a[216:219], v252, s[20:23], s66 offen offset:1024 sc1\nM 36,176,136\nR 170,191,12288\nM 36,180,152\nM 20,176,132\nR 172,191,16384\n"
"M 20,180,148\nbuffer_load_dwordx4 a[220:223], v252, s[20:23], s67 offen offset:1024 sc1\nM 4,176,128\nR 174,191,20480\nM 4,180,144\nG 4\n"
"M 8,160,128\nR 176,188,24576\nM 8,164,144\nbuffer_load_dwordx4 a[224:227], v252, s[20:23], s64 offen offset:2048 sc1\nM 24,160,132\nR 178,188,28672\n"
"M 24,164,148\nM 40,160,136\nR 180,188,32768\nM 40,164,152\nbuffer_load_dwordx4 a[228:231], v252, s[20:23], s65 offen offset:2048 sc1\nM 56,160,140\n"
"R 182,188,36864\nM 56,164,156\nG 4\nM 60,168,140\nR 160,189,24576\nM 60,172,156\n"
"buffer_load_dwordx4 a[232:235], v252, s[20:23], s66 offen offset:2048 sc1\nM 44,168,136\nR 162,189,28672\nM 44,172,152\nM 28,168,132\nR 164,189,32768\n"
"M 28,172,148\nbuffer_load_dwordx4 a[236:239], v252, s[20:23], s67 offen offset:2048 sc1\nM 12,168,128\nR 166,189,36864\nM 12,172,144\nG 4\n"
"M 0,176,160\nR 168,190,24576\ns_waitcnt vmcnt(25)\nM 0,180,176\nbuffer_load_dwordx4 a[240:243], v252, s[20:23], s64 offen offset:3072 sc1\nM 16,176,164\n"
"R 170,190,28672\ns_waitcnt vmcnt(22)\nM 16,180,180\nM 32,176,168\nR 172,190,32768\ns_waitcnt vmcnt(19)\n"
"M 32,180,184\nbuffer_load_dwordx4 a[244:247], v252, s[20:23], s65 offen offset:3072 sc1\nM 48,176,172\nR 174,190,36864\ns_waitcnt vmcnt(18)\nM 48,180,188\n"
"G 4\nM 52,160,172\nR 176,191,24576\nM 52,164,188\nbuffer_load_dwordx4 a[248:251], v252, s[20:23], s66 offen offset:3072 sc1\nM 36,160,168\n"
"R 178,191,28672\ns_waitcnt vmcnt(10)\ns_barrier\nM 36,164,184\nM 20,160,164\nR 180,191,32768\n"
"M 20,164,180\nbuffer_load_dwordx4 a[252:255], v252, s[20:23], s67 offen offset:3072 sc1\nM 4,160,160\nR 182,191,36864\nM 4,164,176\nG 4\n"
"M 8,168,160\nR 160,188,40960\nM 8,172,176\nM 24,168,164\nR 162,188,45056\nM 24,172,180\n"
"M 40,168,168\nR 164,188,49152\nM 40,172,184\nM 56,168,172\nR 166,188,53248\nM 56,172,188\n"
"G 4\nM 60,176,172\nR 168,189,40960\nM 60,180,188\ns_movk_i32 s63, 7168\nv_mad_u32_u24 v220, v228, s63, v229\n"
"v_add_u32 v221, 3668992, v220\ns_add_u32 m0, s50, 16384\ns_add_u32 s63, s54, 0\nbuffer_load_dwordx4 v220, s[12:15], s63 offen lds\nM 44,176,168\nR 170,189,45056\n"
"M 44,180,184\nM 28,176,164\nR 172,189,49152\nM 28,180,180\nbuffer_load_dwordx4 v221, s[12:15], s63 offen offset:1024 lds\nM 12,176,160\n"
"R 174,189,53248\ns_add_u32 m0, s50, 24576\nM 12,180,176\nG 4\nM 32,160,200\nR 176,190,40960\n"
"s_add_u32 s63, s54, 229376\ns_waitcnt vmcnt(11)\nM 32,164,216\nR 178,190,45056\nM 48,160,204\nR 180,190,49152\n"
"s_waitcnt vmcnt(10)\nM 48,164,220\nR 182,190,53248\nG 4\nM 52,168,204\nR 160,191,40960\n"
"buffer_load_dwordx4 v220, s[12:15], s63 offen lds\nM 52,172,220\nR 162,191,45056\nM 36,168,200\nR 164,191,49152\nM 36,172,216\n"
"R 166,191,53248\nG 4\nM 40,176,200\nR 168,184,0\nbuffer_load_dwordx4 v221, s[12:15], s63 offen offset:1024 lds\nM 40,180,216\n"
"R 170,184,4096\ns_add_u32 m0, s50, 32768\nM 56,176,204\nR 172,184,8192\ns_add_u32 s63, s54, 128\nM 56,180,220\n"
"R 174,184,12288\nG 4\nM 60,160,204\nR 176,185,0\nbuffer_load_dwordx4 v220, s[12:15], s63 offen lds\nM 60,164,220\n"
"R 178,185,4096\nM 44,160,200\nR 180,185,8192\nM 44,164,216\nR 182,185,12288\nG 4\n"
"s_waitcnt vmcnt(10)\nM 32,168,232\nR 160,186,0\nbuffer_load_dwordx4 v221, s[12:15], s63 offen offset:1024 lds\ns_waitcnt vmcnt(7)\nM 32,172,248\n"
"R 162,186,4096\ns_add_u32 m0, s50, 40960\nM 48,168,236\nR 164,186,8192\ns_add_u32 s63, s54, 229504\ns_waitcnt vmcnt(6)\n"
"M 48,172,252\nR 166,186,12288\nG 4\nM 52,176,236\nR 168,187,0\nbuffer_load_dwordx4 v220, s[12:15], s63 offen lds\n"
"M 52,180,252\nR 170,187,4096\nM 36,176,232\nR 172,187,8192\nM 36,180,248\nR 174,187,12288\n"
"G 4\nM 40,160,232\nR 176,188,40960\nbuffer_load_dwordx4 v221, s[12:15], s63 offen offset:1024 lds\nM 40,164,248\nR 178,188,45056\n"
"M 56,160,236\nR 180,188,49152\nM 56,164,252\nR 182,188,53248\nG 4\nM 60,168,236\n"
"R 160,189,40960\nM 60,172,252\nR 162,189,45056\nM 44,168,232\nR 164,189,49152\nM 44,172,248\n"
"R 166,189,53248\nG 4\nM 0,176,192\nR 168,190,40960\nM 0,180,208\nR 170,190,45056\n"
"s_mov_b32 m0, s51\nM 16,176,196\nR 172,190,49152\nv_cvt_pk_bf16_f32 v32, v32, v33\nv_cvt_pk_bf16_f32 v33, v34, v35\nM 16,180,212\n"
"R 174,190,53248\nG 4\nv_cvt_pk_bf16_f32 v34, v36, v37\nM 20,160,196\nR 176,191,40960\nv_cvt_pk_bf16_f32 v35, v38, v39\n"
"v_cvt_pk_bf16_f32 v36, v40, v41\nM 20,164,212\nR 178,191,45056\nv_cvt_pk_bf16_f32 v37, v42, v43\nv_cvt_pk_bf16_f32 v38, v44, v45\nM 4,160,192\n"
"R 180,191,49152\nv_cvt_pk_bf16_f32 v39, v46, v47\nv_cvt_pk_bf16_f32 v40, v48, v49\nM 4,164,208\nR 182,191,53248\nG 4\n"
"v_cvt_pk_bf16_f32 v41, v50, v51\nM 8,168,192\nR 160,184,0\nv_cvt_pk_bf16_f32 v42, v52, v53\nv_cvt_pk_bf16_f32 v43, v54, v55\nM 8,172,208\n"
"R 162,184,4096\nv_cvt_pk_bf16_f32 v44, v56, v57\nv_cvt_pk_bf16_f32 v45, v58, v59\nM 24,168,196\nR 164,184,8192\nv_cvt_pk_bf16_f32 v46, v60, v61\n"
"v_cvt_pk_bf16_f32 v47, v62, v63\nM 24,172,212\nR 166,184,12288\nds_write_addtid_b32 v32 offset:0\nG 5\nM 28,176,196\n"
"R 168,185,0\nds_write_addtid_b32 v33 offset:256\nM 28,180,212\nR 170,185,4096\nds_write_addtid_b32 v34 offset:512\nM 12,176,192\n"
"R 172,185,8192\nds_write_addtid_b32 v35 offset:768\nds_write_addtid_b32 v36 offset:1024\nds_write_addtid_b32 v37 offset:1280\nds_write_addtid_b32 v38 offset:1536\nds_write_addtid_b32 v39 offset:1792\n"
"ds_write_addtid_b32 v40 offset:2048\nds_write_addtid_b32 v41 offset:2304\nds_write_addtid_b32 v42 offset:2560\nds_write_addtid_b32 v43 offset:2816\nds_write_addtid_b32 v44 offset:3072\nds_write_addtid_b32 v45 offset:3328\n"
"ds_write_addtid_b32 v46 offset:3584\nds_write_addtid_b32 v47 offset:3840\nM 12,180,208\nR 174,185,12288\nG 15\nM 0,160,224\n"
"R 176,186,0\ns_add_u32 m0, s50, 49152\nM 0,164,240\nR 178,186,4096\nG 3\ns_barrier\n"
"s_add_u32 s63, s54, 458752\nM 16,160,228\nR 180,186,8192\nbuffer_load_dwordx4 v220, s[12:15], s63 offen lds\nds_read2st64_b32 v[32:33], v225 offset0:0 offset1:1\nM 16,164,244\n"
"R 182,186,12288\nds_read2st64_b32 v[34:35], v225 offset0:2 offset1:3\nG 6\nM 20,168,228\nR 160,187,0\nds_read2st64_b32 v[36:37], v225 offset0:4 offset1:5\n"
"M 20,172,244\nR 162,187,4096\nds_read2st64_b32 v[38:39], v225 offset0:6 offset1:7\nM 4,168,224\nR 164,187,8192\nbuffer_load_dwordx4 v221, s[12:15], s63 offen offset:1024 lds\n"
"ds_read2st64_b32 v[40:41], v225 offset0:8 offset1:9\nM 4,172,240\nR 166,187,12288\ns_add_u32 m0, s50, 57344\ns_add_u32 s63, s54, 688128\nds_read2st64_b32 v[42:43], v225 offset0:10 offset1:11\n"
"G 9\nM 8,176,224\nbuffer_load_dwordx4 v220, s[12:15], s63 offen lds\nds_read2st64_b32 v[44:45], v225 offset0:12 offset1:13\nM 8,180,240\nds_read2st64_b32 v[46:47], v225 offset0:14 offset1:15\n"
"M 24,176,228\nM 24,180,244\nG 3\nM 28,160,228\nbuffer_load_dwordx4 v221, s[12:15], s63 offen offset:1024 lds\nM 28,164,244\n"
"M 12,160,224\nM 12,164,240\nG 0\ns_barrier\nLpg7:\ns_cbranch_execz LretO0\n"
"v_mfma_f32_16x16x32_bf16 v[0:3], v[208:211], v[32:35], v[0:3]\nv_mfma_f32_16x16x32_bf16 v[4:7], v[210:213], v[32:35], v[4:7]\nv_mfma_f32_16x16x32_bf16 v[8:11], v[208:211], v[36:39], v[8:11]\nv_mfma_f32_16x16x32_bf16 v[12:15], v[210:213], v[36:39], v[12:15]\nv_mfma_f32_16x16x32_bf16 v[16:19], v[208:211], v[40:43], v[16:19]\nv_mfma_f32_16x16x32_bf16 v[20:23], v[210:213], v[40:43], v[20:23]\n"
"v_mfma_f32_16x16x32_bf16 v[24:27], v[208:211], v[44:47], v[24:27]\nv_mfma_f32_16x16x32_bf16 v[28:31], v[210:213], v[44:47], v[28:31]\ns_lshl_b32 s84, s75, 11\ns_add_u32 s84, s84, s49\ns_add_u32 s84, s84, 10485760\ns_add_u32 s85, s84, 32768\n"
"v_mov_b32 v238, s46\nv_mov_b32 v239, s47\nv_cvt_pk_bf16_f32 v192, v0, v1\nv_cvt_pk_bf16_f32 v193, v2, v3\nv_cvt_pk_bf16_f32 v194, v4, v5\nv_cvt_pk_bf16_f32 v195, v6, v7\n"
"buffer_store_dwordx4 v[192:195], v252, s[20:23], s84 offen offset:0 sc1\nv_cvt_pk_bf16_f32 v196, v8, v9\nv_cvt_pk_bf16_f32 v197, v10, v11\nv_cvt_pk_bf16_f32 v198, v12, v13\nv_cvt_pk_bf16_f32 v199, v14, v15\nbuffer_store_dwordx4 v[196:199], v252, s[20:23], s84 offen offset:1024 sc1\n"
"v_cvt_pk_bf16_f32 v200, v16, v17\nv_cvt_pk_bf16_f32 v201, v18, v19\nv_cvt_pk_bf16_f32 v202, v20, v21\nv_cvt_pk_bf16_f32 v203, v22, v23\nbuffer_store_dwordx4 v[200:203], v252, s[20:23], s85 offen offset:0 sc1\nv_cvt_pk_bf16_f32 v204, v24, v25\n"
"v_cvt_pk_bf16_f32 v205, v26, v27\nv_cvt_pk_bf16_f32 v206, v28, v29\nv_cvt_pk_bf16_f32 v207, v30, v31\nbuffer_store_dwordx4 v[204:207], v252, s[20:23], s85 offen offset:1024 sc1\ns_mov_b64 exec, 0\ns_cmp_eq_u32 s40, 1\n"
"s_cbranch_scc1 Lpg9\ns_cmp_eq_u32 s40, 2\ns_cbranch_scc1 Lpg10\ns_cmp_eq_u32 s40, 3\ns_cbranch_scc1 Lpg11\ns_branch Lpg8\n"
"LretO0n:\ns_mov_b64 exec, -1\ns_add_u32 s61, s79, 88219648\ns_lshl_b32 s62, s75, 3\ns_add_u32 s61, s61, s62\ns_waitcnt vmcnt(0)\n"
"s_mov_b64 exec, 1\nbuffer_atomic_swap_x2 v[238:239], off, s[20:23], s61 sc1\ns_mov_b64 exec, -1\nLqw0:\nv_bfe_u32 v250, v252, 7, 1\nv_mul_u32_u24 v250, 2176, v250\n"
"v_bfe_u32 v251, v252, 4, 3\nv_lshl_add_u32 v250, v251, 3, v250\ns_lshl_b32 s60, s90, 6\nv_add_u32 v250, s60, v250\ns_sleep 5\nv_mov_b32 v248, 0\n"
"v_mov_b32 v249, 0\ns_add_u32 s61, s80, 88219648\ns_mov_b64 exec, 0xffff\nbuffer_atomic_or_x2 v[248:249], v250, s[20:23], s61 offen sc0 sc1\ns_mov_b64 exec, -1\ns_add_u32 m0, s50, 65536\n"
"s_add_u32 s63, s54, 458880\nbuffer_load_dwordx4 v220, s[12:15], s63 offen lds\nbuffer_load_dwordx4 v221, s[12:15], s63 offen offset:1024 lds\ns_add_u32 m0, s50, 73728\ns_add_u32 s63, s54, 688256\nbuffer_load_dwordx4 v220, s[12:15], s63 offen lds\n"
"buffer_load_dwordx4 v221, s[12:15], s63 offen offset:1024 lds\ns_add_u32 m0, s50, 81920\ns_add_u32 s63, s54, 917504\nbuffer_load_dwordx4 v220, s[12:15], s63 offen lds\nbuffer_load_dwordx4 v221, s[12:15], s63 offen offset:1024 lds\ns_add_u32 m0, s50, 90112\n"
"s_add_u32 s63, s54, 1146880\nbuffer_load_dwordx4 v220, s[12:15], s63 offen lds\nbuffer_load_dwordx4 v221, s[12:15], s63 offen offset:1024 lds\ns_add_u32 m0, s50, 98304\ns_add_u32 s63, s54, 917632\nbuffer_load_dwordx4 v220, s[12:15], s63 offen lds\n"
"buffer_load_dwordx4 v221, s[12:15], s63 offen offset:1024 lds\ns_add_u32 m0, s50, 106496\ns_add_u32 s63, s54, 1147008\nbuffer_load_dwordx4 v220, s[12:15], s63 offen lds\nbuffer_load_dwordx4 v221, s[12:15], s63 offen offset:1024 lds\ns_mov_b64 exec, 0\n"
"s_cmp_eq_u32 s40, 1\ns_cbranch_scc1 Lpg17\ns_cmp_eq_u32 s40, 2\ns_cbranch_scc1 LretW0z\ns_cmp_eq_u32 s40, 3\ns_cbranch_scc1 LretW0z\n"
"s_branch Lpg16\nLretW0z:\ns_mov_b64 exec, -1\nv_mov_b32 v230, 0\nv_mov_b32 v231, 0\nv_mov_b32 v232, 0\n"
"v_mov_b32 v233, 0\nv_dot2c_f32_bf16 v230, s76, v192\nv_dot2c_f32_bf16 v231, v192, v192\nv_dot2c_f32_bf16 v232, s76, v200\nv_dot2c_f32_bf16 v233, v200, v200\nv_dot2c_f32_bf16 v230, s76, v193\n"
"v_dot2c_f32_bf16 v231, v193, v193\nv_dot2c_f32_bf16 v232, s76, v201\nv_dot2c_f32_bf16 v233, v201, v201\nv_dot2c_f32_bf16 v230, s76, v194\nv_dot2c_f32_bf16 v231, v194, v194\nv_dot2c_f32_bf16 v232, s76, v202\n"
"v_dot2c_f32_bf16 v233, v202, v202\nv_dot2c_f32_bf16 v230, s76, v195\nv_dot2c_f32_bf16 v231, v195, v195\nv_dot2c_f32_bf16 v232, s76, v203\nv_dot2c_f32_bf16 v233, v203, v203\nv_dot2c_f32_bf16 v230, s76, v196\n"
"v_dot2c_f32_bf16 v231, v196, v196\nv_dot2c_f32_bf16 v232, s76, v204\nv_dot2c_f32_bf16 v233, v204, v204\nv_dot2c_f32_bf16 v230, s76, v197\nv_dot2c_f32_bf16 v231, v197, v197\nv_dot2c_f32_bf16 v232, s76, v205\n"
"v_dot2c_f32_bf16 v233, v205, v205\nv_dot2c_f32_bf16 v230, s76, v198\nv_dot2c_f32_bf16 v231, v198, v198\nv_dot2c_f32_bf16 v232, s76, v206\nv_dot2c_f32_bf16 v233, v206, v206\nv_dot2c_f32_bf16 v230, s76, v199\n"
"v_dot2c_f32_bf16 v231, v199, v199\nv_dot2c_f32_bf16 v232, s76, v207\nv_dot2c_f32_bf16 v233, v207, v207\nv_lshrrev_b32 v234, 5, v252\nv_and_b32 v234, 24, v234\nv_bfe_u32 v235, s46, v234, 8\n"
"v_lshrrev_b32 v234, 1, v234\nv_and_b32 v236, 0xf0, v252\nv_add_u32 v234, v234, v236\nv_permlane32_swap_b32 v230, v231\nv_permlane32_swap_b32 v232, v233\nv_add_f32 v230, v230, v231\n"
"v_add_f32 v232, v232, v233\ns_nop 1\nv_permlane16_swap_b32 v230, v232\nv_add_f32 v230, v230, v232\ns_movk_i32 s62, 0xff\nv_bfi_b32 v230, s62, v235, v230\n"
"s_lshr_b32 s61, s49, 4\ns_lshl1_add_u32 s61, s53, s61\ns_add_u32 s61, s61, 82313216\nbuffer_store_dword v230, v234, s[20:23], s61 offen sc1\ns_waitcnt vmcnt(13)\nv_cmp_eq_u64 s[86:87], s[46:47], v[248:249]\n"
"s_nop 1\ns_and_b32 s62, s86, 0xffff\ns_cmp_eq_u32 s62, 0xffff\ns_cbranch_scc1 Lq255\nLq254:\nv_mov_b32 v248, 0\n"
"v_mov_b32 v249, 0\ns_add_u32 s61, s80, 88219648\ns_mov_b64 exec, 0xffff\nbuffer_atomic_or_x2 v[248:249], v250, s[20:23], s61 offen sc0 sc1\ns_mov_b64 exec, -1\ns_waitcnt vmcnt(0)\n"
"v_cmp_eq_u64 s[86:87], s[46:47], v[248:249]\ns_nop 1\ns_and_b32 s62, s86, 0xffff\ns_cmp_eq_u32 s62, 0xffff\ns_cbranch_scc1 Lq255\ns_sub_u32 s48, s48, 1\n"
"s_cmp_gt_i32 s48, 0\ns_cbranch_scc1 Lq254\nLq255:\ns_add_u32 s64, s70, 10485760\nbuffer_load_dwordx4 a[0:3], v252, s[20:23], s64 offen offset:0 sc1\ns_add_u32 s65, s70, 10518528\n"
"buffer_load_dwordx4 a[4:7], v252, s[20:23], s65 offen offset:0 sc1\ns_add_u32 s66, s71, 10485760\nbuffer_load_dwordx4 a[8:11], v252, s[20:23], s66 offen offset:0 sc1\ns_add_u32 s67, s71, 10518528\nbuffer_load_dwordx4 a[12:15], v252, s[20:23], s67 offen offset:0 sc1\nbuffer_load_dwordx4 a[16:19], v252, s[20:23], s64 offen offset:1024 sc1\n"
"buffer_load_dwordx4 a[20:23], v252, s[20:23], s65 offen offset:1024 sc1\nbuffer_load_dwordx4 a[24:27], v252, s[20:23], s66 offen offset:1024 sc1\nbuffer_load_dwordx4 a[28:31], v252, s[20:23], s67 offen offset:1024 sc1\ns_barrier\nR 160,184,16384\nR 162,184,20480\n"
"R 164,184,24576\nR 166,184,28672\nR 168,185,16384\nR 170,185,20480\nR 172,185,24576\nR 174,185,28672\n"
"R 176,186,16384\nR 178,186,20480\nR 180,186,24576\nR 182,186,28672\nbuffer_load_dwordx4 a[32:35], v252, s[20:23], s64 offen offset:2048 sc1\nbuffer_load_dwordx4 a[36:39], v252, s[20:23], s65 offen offset:2048 sc1\n"
"buffer_load_dwordx4 a[40:43], v252, s[20:23], s66 offen offset:2048 sc1\nbuffer_load_dwordx4 a[44:47], v252, s[20:23], s67 offen offset:2048 sc1\nbuffer_load_dwordx4 a[48:51], v252, s[20:23], s64 offen offset:3072 sc1\nbuffer_load_dwordx4 a[52:55], v252, s[20:23], s65 offen offset:3072 sc1\nbuffer_load_dwordx4 a[56:59], v252, s[20:23], s66 offen offset:3072 sc1\nbuffer_load_dwordx4 a[60:63], v252, s[20:23], s67 offen offset:3072 sc1\n"
"s_waitcnt vmcnt(15) lgkmcnt(10)\nv_mfma_f32_16x16x32_bf16 v[32:35], v[160:163], a[0:3], 0\ns_add_u32 s64, s70, 10489856\nbuffer_load_dwordx4 a[64:67], v252, s[20:23], s64 offen offset:0 sc1\ns_waitcnt vmcnt(12) lgkmcnt(8)\nM 32,164,16\n"
"v_mfma_f32_16x16x32_bf16 v[48:51], v[160:163], a[4:7], 0\ns_waitcnt vmcnt(11)\nM 48,164,20\nv_mfma_f32_16x16x32_bf16 v[64:67], v[160:163], a[8:11], 0\nv_lshrrev_b32 v238, 7, v252\ns_waitcnt vmcnt(10)\n"
"M 64,164,24\nv_lshlrev_b32 v238, 9, v238\nv_mfma_f32_16x16x32_bf16 v[80:83], v[160:163], a[12:15], 0\ns_add_u32 s65, s70, 10522624\nbuffer_load_dwordx4 a[68:71], v252, s[20:23], s65 offen offset:0 sc1\ns_waitcnt vmcnt(10)\n"
"M 80,164,28\nG 4\nv_and_b32 v239, 0x70, v252\nv_mfma_f32_16x16x32_bf16 v[84:87], v[168:171], a[12:15], 0\nR 160,187,16384\nM 84,172,28\n"
"s_and_b32 s60, s40, 3\ns_lshl_b32 s60, s60, 7\nv_add3_u32 v238, v238, v239, s60\nv_mfma_f32_16x16x32_bf16 v[68:71], v[168:171], a[8:11], 0\nR 162,187,20480\ns_lshl_b32 s61, s42, 13\n"
"s_add_u32 s61, s61, 84279296\nbuffer_load_dwordx4 v[0:3], v238, s[20:23], s61 offen sc1\nM 68,172,24\ns_lshl_b32 s61, s42, 13\ns_add_u32 s61, s61, 84283392\nbuffer_load_dwordx4 v[4:7], v238, s[20:23], s61 offen sc1\n"
"v_mfma_f32_16x16x32_bf16 v[52:55], v[168:171], a[4:7], 0\nR 164,187,24576\nM 52,172,20\ns_add_u32 s66, s71, 10489856\nbuffer_load_dwordx4 a[72:75], v252, s[20:23], s66 offen offset:0 sc1\nv_bfe_u32 v239, v238, 2, 7\n"
"v_mfma_f32_16x16x32_bf16 v[36:39], v[168:171], a[0:3], 0\nR 166,187,28672\nM 36,172,16\nbuffer_load_dword v239, v239, s[32:35], s54 offen offset:0\nG 4\nv_mfma_f32_16x16x32_bf16 v[40:43], v[176:179], a[0:3], 0\n"
"R 168,184,32768\nM 40,180,16\nv_mfma_f32_16x16x32_bf16 v[56:59], v[176:179], a[4:7], 0\nR 170,184,36864\nM 56,180,20\ns_add_u32 s67, s71, 10522624\n"
"buffer_load_dwordx4 a[76:79], v252, s[20:23], s67 offen offset:0 sc1\nv_mfma_f32_16x16x32_bf16 v[72:75], v[176:179], a[8:11], 0\nR 172,184,40960\nM 72,180,24\nv_mfma_f32_16x16x32_bf16 v[88:91], v[176:179], a[12:15], 0\nR 174,184,45056\n"
"M 88,180,28\nG 4\nv_mfma_f32_16x16x32_bf16 v[92:95], v[160:163], a[12:15], 0\nR 176,185,32768\nM 92,164,28\nbuffer_load_dwordx4 a[80:83], v252, s[20:23], s64 offen offset:1024 sc1\n"
"v_mfma_f32_16x16x32_bf16 v[76:79], v[160:163], a[8:11], 0\nR 178,185,36864\nM 76,164,24\nv_mfma_f32_16x16x32_bf16 v[60:63], v[160:163], a[4:7], 0\nR 180,185,40960\nM 60,164,20\n"
"v_mfma_f32_16x16x32_bf16 v[44:47], v[160:163], a[0:3], 0\nR 182,185,45056\nM 44,164,16\nG 4\nv_mfma_f32_16x16x32_bf16 v[96:99], v[168:171], a[0:3], 0\nR 160,186,32768\n"
"M 96,172,16\nbuffer_load_dwordx4 a[84:87], v252, s[20:23], s65 offen offset:1024 sc1\nv_mfma_f32_16x16x32_bf16 v[112:115], v[168:171], a[4:7], 0\nR 162,186,36864\nM 112,172,20\nv_mfma_f32_16x16x32_bf16 v[128:131], v[168:171], a[8:11], 0\n"
"R 164,186,40960\nM 128,172,24\nbuffer_load_dwordx4 a[88:91], v252, s[20:23], s66 offen offset:1024 sc1\nv_mfma_f32_16x16x32_bf16 v[144:147], v[168:171], a[12:15], 0\nR 166,186,45056\nM 144,172,28\n"
"s_barrier\nG 4\nv_mfma_f32_16x16x32_bf16 v[148:151], v[176:179], a[12:15], 0\nR 168,187,32768\nM 148,180,28\nv_mfma_f32_16x16x32_bf16 v[132:135], v[176:179], a[8:11], 0\n"
"R 170,187,36864\nM 132,180,24\nbuffer_load_dwordx4 a[92:95], v252, s[20:23], s67 offen offset:1024 sc1\nv_mfma_f32_16x16x32_bf16 v[116:119], v[176:179], a[4:7], 0\nR 172,187,40960\nM 116,180,20\n"
"v_mfma_f32_16x16x32_bf16 v[100:103], v[176:179], a[0:3], 0\nR 174,187,45056\nM 100,180,16\nG 4\nv_mfma_f32_16x16x32_bf16 v[104:107], v[160:163], a[0:3], 0\nR 176,184,49152\n"
"v_lshrrev_b32 v242, 7, v252\nM 104,164,16\nbuffer_load_dwordx4 a[96:99], v252, s[20:23], s64 offen offset:2048 sc1\nv_mfma_f32_16x16x32_bf16 v[120:123], v[160:163], a[4:7], 0\nR 178,184,53248\nv_lshlrev_b32 v242, 9, v242\n"
"s_waitcnt vmcnt(8)\nv_min3_u32 v240, v1, v3, v5\nM 120,164,20\nv_and_b32 v243, 0x70, v252\nv_min3_u32 v240, v240, v7, v7\nv_max3_u32 v241, v1, v3, v5\n"
"v_mfma_f32_16x16x32_bf16 v[136:139], v[160:163], a[8:11], 0\nR 180,184,57344\nv_max3_u32 v241, v241, v7, v7\nv_cmp_ne_u32 s[86:87], s46, v240\nM 136,164,24\ns_and_b32 s60, s40, 3\n"
"s_lshl_b32 s60, s60, 7\nv_add3_u32 v242, v242, v243, s60\nv_cmp_ne_u32 s[88:89], s46, v241\nv_mfma_f32_16x16x32_bf16 v[152:155], v[160:163], a[12:15], 0\nR 182,184,61440\ns_lshl_b32 s61, s42, 13\n"
"s_add_u32 s61, s61, 84410368\nbuffer_load_dwordx4 v[16:19], v242, s[20:23], s61 offen sc1\ns_or_b64 s[86:87], s[86:87], s[88:89]\ns_cbranch_scc1 Lq315\nLq316:\nM 152,164,28\n"
"s_lshl_b32 s61, s42, 13\ns_add_u32 s61, s61, 84414464\nbuffer_load_dwordx4 v[20:23], v242, s[20:23], s61 offen sc1\nG 4\nv_mfma_f32_16x16x32_bf16 v[156:159], v[168:171], a[12:15], 0\nR 160,185,49152\n"
"v_add_f32 v0, v0, v4\nM 156,172,28\nbuffer_load_dwordx4 a[100:103], v252, s[20:23], s65 offen offset:2048 sc1\nv_bfe_u32 v243, v242, 2, 7\nv_mfma_f32_16x16x32_bf16 v[140:143], v[168:171], a[8:11], 0\nR 162,185,53248\n"
"v_add_f32 v2, v2, v6\nM 140,172,24\nbuffer_load_dword v243, v243, s[32:35], s54 offen offset:128\nv_mfma_f32_16x16x32_bf16 v[124:127], v[168:171], a[4:7], 0\nR 164,185,57344\nv_add_f32_dpp v0, v0, v0 row_ror:8 row_mask:0xf bank_mask:0xf\n"
"M 124,172,20\nbuffer_load_dwordx4 a[104:107], v252, s[20:23], s66 offen offset:2048 sc1\nv_mfma_f32_16x16x32_bf16 v[108:111], v[168:171], a[0:3], 0\nR 166,185,61440\nv_add_f32_dpp v2, v2, v2 row_ror:8 row_mask:0xf bank_mask:0xf\nM 108,172,16\n"
"G 4\nv_mov_b32 v4, v0\nv_mov_b32 v5, v2\nM 32,176,32\nR 168,186,49152\nv_permlane32_swap_b32 v4, v0\n"
"M 32,180,48\nv_permlane32_swap_b32 v5, v2\nv_add_f32 v0, v0, v4\nM 48,176,36\nR 170,186,53248\nv_add_f32 v2, v2, v5\n"
"M 48,180,52\nbuffer_load_dwordx4 a[108:111], v252, s[20:23], s67 offen offset:2048 sc1\nM 64,176,40\nR 172,186,57344\nv_mov_b32 v4, v0\nM 64,180,56\n"
"v_mov_b32 v5, v2\nv_permlane16_swap_b32 v4, v0\nM 80,176,44\nR 174,186,61440\nv_permlane16_swap_b32 v5, v2\nM 80,180,60\n"
"s_barrier\nG 4\nM 84,160,44\nR 176,187,49152\nv_add_f32 v0, v0, v4\nM 84,164,60\n"
"buffer_load_dwordx4 a[112:115], v252, s[20:23], s64 offen offset:3072 sc1\nM 68,160,40\nR 178,187,53248\nv_add_f32 v2, v2, v5\nM 68,164,56\nv_and_b32 v8, s77, v0\n"
"v_sub_f32 v9, v0, v8\nM 52,160,36\nR 180,187,57344\ns_waitcnt vmcnt(13)\nv_lshlrev_b32 v10, 16, v239\nM 52,164,52\n"
"v_cvt_pk_bf16_f32 v4, v8, v9\nv_cvt_pk_bf16_f32 v5, v8, v10\nM 36,160,32\nR 182,187,61440\nv_and_b32 v8, s77, v2\nM 36,164,48\n"
"G 4\nv_sub_f32 v9, v2, v8\nv_and_b32 v10, s77, v239\nM 40,168,32\nR 160,188,0\nv_cvt_pk_bf16_f32 v6, v8, v9\n"
"M 40,172,48\nbuffer_load_dwordx4 a[116:119], v252, s[20:23], s65 offen offset:3072 sc1\nM 56,168,36\nR 162,188,4096\nv_cvt_pk_bf16_f32 v7, v8, v10\nM 56,172,52\n"
"v_and_b32 v238, 0x1ff, v238\nv_add_u32 v238, 157184, v238\nM 72,168,40\nR 164,188,8192\nds_write_b128 v238, v[4:7]\nM 72,172,56\n"
"buffer_load_dwordx4 a[120:123], v252, s[20:23], s66 offen offset:3072 sc1\nM 88,168,44\nR 166,188,12288\nM 88,172,60\nG 5\nM 92,176,44\n"
"R 168,189,0\nM 92,180,60\nM 76,176,40\nR 170,189,4096\nM 76,180,56\nbuffer_load_dwordx4 a[124:127], v252, s[20:23], s67 offen offset:3072 sc1\n"
"M 60,176,36\nR 172,189,8192\nv_lshrrev_b32 v238, 7, v252\nM 60,180,52\nv_lshlrev_b32 v238, 9, v238\nM 44,176,32\n"
"R 174,189,12288\ns_waitcnt vmcnt(8)\nv_min3_u32 v244, v17, v19, v21\nv_min3_u32 v244, v244, v23, v23\nM 44,180,48\nG 4\n"
"v_and_b32 v239, 0x70, v252\nv_max3_u32 v245, v17, v19, v21\nv_max3_u32 v245, v245, v23, v23\nM 96,160,32\nR 176,190,0\ns_and_b32 s60, s40, 3\n"
"s_lshl_b32 s60, s60, 7\nv_add3_u32 v238, v238, v239, s60\nv_cmp_ne_u32 s[86:87], s46, v244\nM 96,164,48\ns_add_u32 s64, s70, 10493952\nbuffer_load_dwordx4 a[128:131], v252, s[20:23], s64 offen offset:0 sc1\n"
"v_cmp_ne_u32 s[88:89], s46, v245\nM 112,160,36\nR 178,190,4096\ns_lshl_b32 s61, s42, 13\ns_add_u32 s61, s61, 84541440\nbuffer_load_dwordx4 v[0:3], v238, s[20:23], s61 offen sc1\n"
"s_or_b64 s[86:87], s[86:87], s[88:89]\ns_cbranch_scc1 Lq325\nLq326:\nM 112,164,52\ns_lshl_b32 s61, s42, 13\ns_add_u32 s61, s61, 84545536\n"
"buffer_load_dwordx4 v[4:7], v238, s[20:23], s61 offen sc1\nv_add_f32 v16, v16, v20\nM 128,160,40\nR 180,190,8192\nv_add_f32 v18, v18, v22\nv_add_f32_dpp v16, v16, v16 row_ror:8 row_mask:0xf bank_mask:0xf\n"
"M 128,164,56\ns_add_u32 m0, s50, 114688\nv_bfe_u32 v239, v238, 2, 7\nv_add_f32_dpp v18, v18, v18 row_ror:8 row_mask:0xf bank_mask:0xf\nv_mov_b32 v20, v16\nM 144,160,44\n"
"R 182,190,12288\ns_add_u32 s63, s54, 1376256\nv_mov_b32 v21, v18\nM 144,164,60\nbuffer_load_dword v239, v239, s[32:35], s54 offen offset:256\ns_barrier\n"
"G 4\nv_permlane32_swap_b32 v20, v16\nM 148,168,44\nR 160,191,0\nv_permlane32_swap_b32 v21, v18\nv_add_f32 v16, v16, v20\n"
"M 148,172,60\ns_add_u32 s65, s70, 10526720\nbuffer_load_dwordx4 a[132:135], v252, s[20:23], s65 offen offset:0 sc1\nv_add_f32 v18, v18, v21\nM 132,168,40\nR 162,191,4096\n"
"v_mov_b32 v20, v16\nv_mov_b32 v21, v18\nM 132,172,56\nbuffer_load_dwordx4 v220, s[12:15], s63 offen lds\nM 116,168,36\nR 164,191,8192\n"
"v_permlane16_swap_b32 v20, v16\nM 116,172,52\ns_add_u32 s66, s71, 10493952\nbuffer_load_dwordx4 a[136:139], v252, s[20:23], s66 offen offset:0 sc1\nM 100,168,32\nR 166,191,12288\n"
"v_permlane16_swap_b32 v21, v18\nM 100,172,48\nG 4\nv_add_f32 v16, v16, v20\nv_add_f32 v18, v18, v21\nM 104,176,32\n"
"R 168,188,16384\nbuffer_load_dwordx4 v221, s[12:15], s63 offen offset:1024 lds\nM 104,180,48\ns_add_u32 m0, s50, 0\ns_add_u32 s63, s54, 1605632\nbuffer_load_dwordx4 v220, s[12:15], s63 offen lds\n"
"M 120,176,36\nR 170,188,20480\nv_and_b32 v24, s77, v16\nM 120,180,52\ns_add_u32 s67, s71, 10526720\nbuffer_load_dwordx4 a[140:143], v252, s[20:23], s67 offen offset:0 sc1\n"
"M 136,176,40\nR 172,188,24576\nv_sub_f32 v25, v16, v24\nM 136,180,56\nbuffer_load_dwordx4 v221, s[12:15], s63 offen offset:1024 lds\nM 152,176,44\n"
"R 174,188,28672\ns_waitcnt vmcnt(17)\nv_lshlrev_b32 v26, 16, v243\nM 152,180,60\nG 4\nv_cvt_pk_bf16_f32 v20, v24, v25\n"
"v_cvt_pk_bf16_f32 v21, v24, v26\nM 156,160,44\nR 176,189,16384\nv_and_b32 v24, s77, v18\nM 156,164,60\nbuffer_load_dwordx4 a[144:147], v252, s[20:23], s64 offen offset:1024 sc1\n"
"M 140,160,40\nR 178,189,20480\nv_sub_f32 v25, v18, v24\nM 140,164,56\nv_and_b32 v26, s77, v243\nv_cvt_pk_bf16_f32 v22, v24, v25\n"
"M 124,160,36\nR 180,189,24576\nv_cvt_pk_bf16_f32 v23, v24, v26\nM 124,164,52\nv_and_b32 v242, 0x1ff, v242\nv_add_u32 v242, 157696, v242\n"
"M 108,160,32\nR 182,189,28672\nds_write_b128 v242, v[20:23]\nM 108,164,48\nG 5\nM 32,168,64\n"
"R 160,190,16384\nM 32,172,80\nbuffer_load_dwordx4 a[148:151], v252, s[20:23], s65 offen offset:1024 sc1\nM 48,168,68\nR 162,190,20480\nM 48,172,84\n"
"s_add_u32 m0, s50, 8192\nM 64,168,72\nR 164,190,24576\ns_add_u32 s63, s54, 1376384\nM 64,172,88\nbuffer_load_dwordx4 a[152:155], v252, s[20:23], s66 offen offset:1024 sc1\n"
"M 80,168,76\nR 166,190,28672\nM 80,172,92\nG 4\ns_barrier\nM 84,176,76\n"
"R 168,191,16384\nv_lshrrev_b32 v242, 7, v252\nM 84,180,92\nbuffer_load_dwordx4 v220, s[12:15], s63 offen lds\nv_lshlrev_b32 v242, 9, v242\nM 68,176,72\n"
"R 170,191,20480\ns_waitcnt vmcnt(12)\nv_min3_u32 v240, v1, v3, v5\nv_min3_u32 v240, v240, v7, v7\nM 68,180,88\nbuffer_load_dwordx4 a[156:159], v252, s[20:23], s67 offen offset:1024 sc1\n"
"v_and_b32 v243, 0x70, v252\nv_max3_u32 v241, v1, v3, v5\nM 52,176,68\nLpg8:\ns_cbranch_execz Lpg12\nR 172,191,24576\n"
"v_max3_u32 v241, v241, v7, v7\nv_cmp_ne_u32 s[86:87], s46, v240\nM 52,180,84\ns_and_b32 s60, s40, 1\ns_lshl_b32 s60, s60, 7\nv_add3_u32 v242, v242, v243, s60\n"
"v_cmp_ne_u32 s[88:89], s46, v241\nM 36,176,64\nR 174,191,28672\ns_lshl_b32 s61, s42, 13\ns_add_u32 s61, s61, 84672512\nbuffer_load_dwordx4 v[16:19], v242, s[20:23], s61 offen sc1\n"
"s_or_b64 s[86:87], s[86:87], s[88:89]\ns_cbranch_scc1 Lq335\nLq336:\nM 36,180,80\ns_lshl_b32 s61, s42, 13\ns_add_u32 s61, s61, 84676608\n"
"buffer_load_dwordx4 v[20:23], v242, s[20:23], s61 offen sc1\nG 4\nv_add_f32 v0, v0, v4\nM 40,160,64\nR 176,188,32768\nbuffer_load_dwordx4 v221, s[12:15], s63 offen offset:1024 lds\n"
"v_add_f32 v2, v2, v6\nM 40,164,80\nbuffer_load_dwordx4 a[160:163], v252, s[20:23], s64 offen offset:2048 sc1\ns_add_u32 s63, s54, 1605760\nv_bfe_u32 v243, v242, 2, 7\nv_add_f32_dpp v0, v0, v0 row_ror:8 row_mask:0xf bank_mask:0xf\n"
"M 56,160,68\nR 178,188,36864\ns_add_u32 m0, s50, 16384\nv_add_f32_dpp v2, v2, v2 row_ror:8 row_mask:0xf bank_mask:0xf\nM 56,164,84\nbuffer_load_dword v243, v243, s[32:35], s55 offen offset:0\n"
"v_mov_b32 v4, v0\nM 72,160,72\nR 180,188,40960\nbuffer_load_dwordx4 v220, s[12:15], s63 offen lds\nv_mov_b32 v5, v2\nM 72,164,88\n"
"buffer_load_dwordx4 v221, s[12:15], s63 offen offset:1024 lds\nv_permlane32_swap_b32 v4, v0\nM 88,160,76\nR 182,188,45056\nv_permlane32_swap_b32 v5, v2\nv_add_f32 v0, v0, v4\n"
"M 88,164,92\nG 4\nv_add_f32 v2, v2, v5\nv_mov_b32 v4, v0\nv_mov_b32 v5, v2\nM 92,168,76\n"
"R 160,189,32768\nv_permlane16_swap_b32 v4, v0\nv_permlane16_swap_b32 v5, v2\nM 92,172,92\nbuffer_load_dwordx4 a[164:167], v252, s[20:23], s65 offen offset:2048 sc1\nv_add_f32 v0, v0, v4\n"
"M 76,168,72\nR 162,189,36864\nv_add_f32 v2, v2, v5\nv_and_b32 v8, s77, v0\nM 76,172,88\nv_sub_f32 v9, v0, v8\n"
"s_waitcnt vmcnt(20)\nv_lshlrev_b32 v10, 16, v239\nM 60,168,68\nR 164,189,40960\nv_cvt_pk_bf16_f32 v4, v8, v9\nM 60,172,84\n"
"buffer_load_dwordx4 a[168:171], v252, s[20:23], s66 offen offset:2048 sc1\nM 44,168,64\nR 166,189,45056\nv_cvt_pk_bf16_f32 v5, v8, v10\nM 44,172,80\nG 4\n"
"v_and_b32 v8, s77, v2\nv_sub_f32 v9, v2, v8\nM 96,176,64\nR 168,190,32768\nv_and_b32 v10, s77, v239\nM 96,180,80\n"
"v_cvt_pk_bf16_f32 v6, v8, v9\nv_cvt_pk_bf16_f32 v7, v8, v10\nM 112,176,68\nR 170,190,36864\nv_and_b32 v238, 0x1ff, v238\nM 112,180,84\n"
"buffer_load_dwordx4 a[172:175], v252, s[20:23], s67 offen offset:2048 sc1\nM 128,176,72\nR 172,190,40960\nv_add_u32 v238, 158208, v238\nM 128,180,88\ns_add_u32 m0, s50, 24576\n"
"ds_write_b128 v238, v[4:7]\nM 144,176,76\nR 174,190,45056\ns_add_u32 s63, s54, 1835008\nM 144,180,92\ns_waitcnt vmcnt(15) lgkmcnt(1)\n"
"s_barrier\nM 148,160,76\nR 176,191,32768\nM 148,164,92\nbuffer_load_dwordx4 a[176:179], v252, s[20:23], s64 offen offset:3072 sc1\nM 132,160,72\n"
"R 178,191,36864\nM 132,164,88\nbuffer_load_dwordx4 v220, s[12:15], s63 offen lds\nM 116,160,68\nR 180,191,40960\nM 116,164,84\n"
"buffer_load_dwordx4 v221, s[12:15], s63 offen offset:1024 lds\nM 100,160,64\nR 182,191,45056\ns_add_u32 m0, s50, 32768\nM 100,164,80\nG 4\n"
"M 104,168,64\nR 160,188,49152\ns_add_u32 s63, s54, 2064384\nM 104,172,80\nbuffer_load_dwordx4 a[180:183], v252, s[20:23], s65 offen offset:3072 sc1\nM 120,168,68\n"
"R 162,188,53248\nM 120,172,84\nbuffer_load_dwordx4 v220, s[12:15], s63 offen lds\nM 136,168,72\nR 164,184,0\nM 136,172,88\n"
"buffer_load_dwordx4 a[184:187], v252, s[20:23], s66 offen offset:3072 sc1\nM 152,168,76\nR 166,184,4096\ns_waitcnt vmcnt(14)\nv_min3_u32 v244, v17, v19, v21\nv_min3_u32 v244, v244, v23, v23\n"
"M 152,172,92\nG 4\nv_max3_u32 v245, v17, v19, v21\nv_max3_u32 v245, v245, v23, v23\nv_cmp_ne_u32 s[86:87], s46, v244\nM 156,176,76\n"
"R 168,189,49152\nbuffer_load_dwordx4 v221, s[12:15], s63 offen offset:1024 lds\nv_cmp_ne_u32 s[88:89], s46, v245\nM 156,180,92\ns_or_b64 s[86:87], s[86:87], s[88:89]\ns_cbranch_scc1 Lq345\n"
"Lq346:\nv_add_f32 v16, v16, v20\nv_add_f32 v18, v18, v22\nM 140,176,72\nR 170,189,53248\nv_add_f32_dpp v16, v16, v16 row_ror:8 row_mask:0xf bank_mask:0xf\n"
"v_add_f32_dpp v18, v18, v18 row_ror:8 row_mask:0xf bank_mask:0xf\nM 140,180,88\nbuffer_load_dwordx4 a[188:191], v252, s[20:23], s67 offen offset:3072 sc1\nv_mov_b32 v20, v16\nM 124,176,68\nR 172,185,0\n"
"v_mov_b32 v21, v18\nM 124,180,84\nv_permlane32_swap_b32 v20, v16\nv_permlane32_swap_b32 v21, v18\nM 108,176,64\nR 174,185,4096\n"
"v_add_f32 v16, v16, v20\nM 108,180,80\nG 4\nv_add_f32 v18, v18, v21\nv_mov_b32 v20, v16\nM 32,160,96\n"
"R 176,190,49152\nv_mov_b32 v21, v18\nM 32,164,112\ns_add_u32 s64, s70, 10498048\nbuffer_load_dwordx4 a[192:195], v252, s[20:23], s64 offen offset:0 sc1\nM 48,160,100\n"
"R 178,190,53248\nv_permlane16_swap_b32 v20, v16\nM 48,164,116\nv_permlane16_swap_b32 v21, v18\nv_add_f32 v16, v16, v20\nM 64,160,104\n"
"R 180,186,0\nv_add_f32 v18, v18, v21\nM 64,164,120\ns_add_u32 m0, s50, 40960\nv_and_b32 v24, s77, v16\nv_sub_f32 v25, v16, v24\n"
"M 80,160,108\nR 182,186,4096\ns_add_u32 s63, s54, 2293760\nM 80,164,124\ns_waitcnt vmcnt(12)\ns_barrier\n"
"G 4\nM 84,168,108\nR 160,191,49152\nv_lshlrev_b32 v26, 16, v243\nM 84,172,124\ns_add_u32 s65, s70, 10530816\n"
"buffer_load_dwordx4 a[196:199], v252, s[20:23], s65 offen offset:0 sc1\nM 68,168,104\nR 162,191,53248\nv_cvt_pk_bf16_f32 v20, v24, v25\nM 68,172,120\nbuffer_load_dwordx4 v220, s[12:15], s63 offen lds\n"
"M 52,168,100\nR 164,187,0\nv_cvt_pk_bf16_f32 v21, v24, v26\nM 52,172,116\ns_add_u32 s66, s71, 10498048\nbuffer_load_dwordx4 a[200:203], v252, s[20:23], s66 offen offset:0 sc1\n"
"M 36,168,96\nR 166,187,4096\nv_and_b32 v24, s77, v18\nM 36,172,112\nG 4\nv_sub_f32 v25, v18, v24\n"
"v_and_b32 v26, s77, v243\nM 40,176,96\nR 168,184,8192\nbuffer_load_dwordx4 v221, s[12:15], s63 offen offset:1024 lds\nM 40,180,112\ns_add_u32 m0, s50, 49152\n"
"s_add_u32 s63, s54, 2523136\nbuffer_load_dwordx4 v220, s[12:15], s63 offen lds\nM 56,176,100\nR 170,184,12288\nv_cvt_pk_bf16_f32 v22, v24, v25\nM 56,180,116\n"
"s_add_u32 s67, s71, 10530816\nbuffer_load_dwordx4 a[204:207], v252, s[20:23], s67 offen offset:0 sc1\nM 72,176,104\nR 172,184,16384\nv_cvt_pk_bf16_f32 v23, v24, v26\nM 72,180,120\n"
"buffer_load_dwordx4 v221, s[12:15], s63 offen offset:1024 lds\nM 88,176,108\nR 174,184,20480\nv_and_b32 v242, 0x1ff, v242\nM 88,180,124\nv_add_u32 v242, 158720, v242\n"
"ds_write_b128 v242, v[20:23]\nG 5\nM 92,160,108\nR 176,185,8192\nM 92,164,124\nbuffer_load_dwordx4 a[208:211], v252, s[20:23], s64 offen offset:1024 sc1\n"
"M 76,160,104\nR 178,185,12288\nM 76,164,120\ns_lshr_b32 s84, s49, 4\nM 60,160,100\nR 180,185,16384\n"
"Lq569:\ns_lshr_b32 s60, s45, 3\ns_lshl2_add_u32 s60, s60, s40\ns_lshl_b32 s60, s60, 8\nv_lshl_add_u32 v224, v252, 3, s60\nv_add_u32 v224, Lq569+4096-LqPC, v224\n"
"v_min_u32 v224, LqE-LqPC-4, v224\ns_mov_b64 exec, 3\nglobal_load_dword v224, v224, s[98:99]\ns_mov_b64 exec, -1\nM 60,164,116\ns_add_u32 s84, s84, 82313216\n"
"M 44,160,96\nR 182,185,20480\nM 44,164,112\nG 5\nM 96,168,96\nR 160,186,8192\n"
"M 96,172,112\nbuffer_load_dwordx4 a[212:215], v252, s[20:23], s65 offen offset:1024 sc1\nM 112,168,100\nR 162,186,12288\nM 112,172,116\nbuffer_load_dwordx4 v[0:3], v252, s[20:23], s84 offen offset:0 sc1\n"
"M 128,168,104\nR 164,186,16384\nM 128,172,120\nbuffer_load_dwordx4 a[216:219], v252, s[20:23], s66 offen offset:1024 sc1\nM 144,168,108\nR 166,186,20480\n"
"s_add_u32 m0, s50, 57344\nM 144,172,124\nbuffer_load_dwordx4 v[4:7], v252, s[20:23], s84 offen offset:1024 sc1\ns_waitcnt vmcnt(15) lgkmcnt(8)\ns_barrier\nG 4\n"
"M 148,176,108\nR 168,187,8192\ns_add_u32 s63, s54, 2752512\nM 148,180,124\nbuffer_load_dwordx4 v[8:11], v252, s[20:23], s84 offen offset:2048 sc1\nM 132,176,104\n"
"R 170,187,12288\nM 132,180,120\nbuffer_load_dwordx4 a[220:223], v252, s[20:23], s67 offen offset:1024 sc1\nM 116,176,100\nR 172,187,16384\nbuffer_load_dwordx4 v220, s[12:15], s63 offen lds\n"
"M 116,180,116\nbuffer_load_dwordx4 v[12:15], v252, s[20:23], s84 offen offset:3072 sc1\nM 100,176,96\nR 174,187,20480\nM 100,180,112\nG 4\n"
"M 104,160,96\nR 176,184,24576\nbuffer_load_dwordx4 v221, s[12:15], s63 offen offset:1024 lds\nM 104,164,112\nbuffer_load_dwordx4 a[224:227], v252, s[20:23], s64 offen offset:2048 sc1\ns_add_u32 s63, s54, 2981888\n"
"M 120,160,100\nR 178,184,28672\ns_add_u32 m0, s50, 65536\nM 120,164,116\nbuffer_load_dwordx4 v220, s[12:15], s63 offen lds\nM 136,160,104\n"
"R 180,184,32768\nM 136,164,120\nbuffer_load_dwordx4 v221, s[12:15], s63 offen offset:1024 lds\nM 152,160,108\nR 182,184,36864\nM 152,164,124\n"
"G 4\nM 156,168,108\nR 160,185,24576\nM 156,172,124\nbuffer_load_dwordx4 a[228:231], v252, s[20:23], s65 offen offset:2048 sc1\nM 140,168,104\n"
"R 162,185,28672\nM 140,172,120\nM 124,168,100\nR 164,185,32768\nM 124,172,116\nbuffer_load_dwordx4 a[232:235], v252, s[20:23], s66 offen offset:2048 sc1\n"
"M 108,168,96\nR 166,185,36864\nM 108,172,112\nG 4\nM 32,176,128\nR 168,186,24576\n"
"M 32,180,144\nM 48,176,132\nR 170,186,28672\nM 48,180,148\nbuffer_load_dwordx4 a[236:239], v252, s[20:23], s67 offen offset:2048 sc1\nM 64,176,136\n"
"R 172,186,32768\nM 64,180,152\ns_add_u32 m0, s50, 73728\nM 80,176,140\nR 174,186,36864\ns_add_u32 s63, s54, 3211264\n"
"M 80,180,156\ns_waitcnt vmcnt(17)\ns_barrier\nG 4\nM 84,160,140\nR 176,187,24576\n"
"M 84,164,156\nbuffer_load_dwordx4 a[240:243], v252, s[20:23], s64 offen offset:3072 sc1\nM 68,160,136\nR 178,187,28672\nM 68,164,152\nbuffer_load_dwordx4 v220, s[12:15], s63 offen lds\n"
"M 52,160,132\nR 180,187,32768\nM 52,164,148\nbuffer_load_dwordx4 v221, s[12:15], s63 offen offset:1024 lds\nM 36,160,128\nR 182,187,36864\n"
"s_add_u32 m0, s50, 81920\nM 36,164,144\nG 4\nM 40,168,128\nR 160,184,40960\ns_add_u32 s63, s54, 3440640\n"
"M 40,172,144\nbuffer_load_dwordx4 a[244:247], v252, s[20:23], s65 offen offset:3072 sc1\nM 56,168,132\nR 162,184,45056\nM 56,172,148\nbuffer_load_dwordx4 v220, s[12:15], s63 offen lds\n"
"M 72,168,136\nR 164,184,49152\nM 72,172,152\nbuffer_load_dwordx4 a[248:251], v252, s[20:23], s66 offen offset:3072 sc1\nM 88,168,140\nR 166,184,53248\n"
"s_mov_b32 s85, 0x0c0c0400\ns_waitcnt vmcnt(19)\nv_perm_b32 v16, v1, v0, s85\nM 88,172,156\nG 4\nv_perm_b32 v20, v3, v2, s85\n"
"v_lshl_or_b32 v16, v20, 16, v16\ns_waitcnt vmcnt(17)\nv_perm_b32 v17, v5, v4, s85\nM 92,176,140\nR 168,185,40960\nbuffer_load_dwordx4 v221, s[12:15], s63 offen offset:1024 lds\n"
"v_perm_b32 v21, v7, v6, s85\nM 92,180,156\nv_lshl_or_b32 v17, v21, 16, v17\ns_waitcnt vmcnt(17)\nv_perm_b32 v18, v9, v8, s85\nv_perm_b32 v22, v11, v10, s85\n"
"M 76,176,136\nR 170,185,45056\nv_lshl_or_b32 v18, v22, 16, v18\ns_waitcnt vmcnt(14)\nv_perm_b32 v19, v13, v12, s85\nM 76,180,152\n"
"buffer_load_dwordx4 a[252:255], v252, s[20:23], s67 offen offset:3072 sc1\nv_perm_b32 v23, v15, v14, s85\nM 60,176,132\nR 172,185,49152\nv_lshl_or_b32 v19, v23, 16, v19\nv_min3_u32 v230, v16, v17, v18\n"
"M 60,180,148\nv_min3_u32 v230, v230, v19, v19\nv_max3_u32 v231, v16, v17, v18\nv_max3_u32 v231, v231, v19, v19\nM 44,176,128\nR 174,185,53248\n"
"v_cmp_ne_u32 s[86:87], s46, v230\nv_cmp_ne_u32 s[88:89], s46, v231\nM 44,180,144\ns_or_b64 s[86:87], s[86:87], s[88:89]\ns_cbranch_scc1 Lq313\nLq314:\n"
"G 4\nv_and_b32 v0, 0xffffff00, v0\nM 32,160,160\nR 176,186,40960\nv_and_b32 v1, 0xffffff00, v1\nv_and_b32 v2, 0xffffff00, v2\n"
"M 32,164,176\nv_and_b32 v3, 0xffffff00, v3\nv_and_b32 v4, 0xffffff00, v4\nv_and_b32 v5, 0xffffff00, v5\nM 48,160,164\nR 178,186,45056\n"
"v_and_b32 v6, 0xffffff00, v6\nv_and_b32 v7, 0xffffff00, v7\nM 48,164,180\ns_add_u32 m0, s50, 90112\nv_and_b32 v8, 0xffffff00, v8\nv_and_b32 v9, 0xffffff00, v9\n"
"v_and_b32 v10, 0xffffff00, v10\nM 64,160,168\nR 180,186,49152\nv_and_b32 v11, 0xffffff00, v11\nv_and_b32 v12, 0xffffff00, v12\nM 64,164,184\n"
"s_add_u32 s63, s54, 1835136\nv_and_b32 v13, 0xffffff00, v13\nv_and_b32 v14, 0xffffff00, v14\nv_and_b32 v15, 0xffffff00, v15\nM 80,160,172\nR 182,186,53248\n"
"v_add_f32 v0, v0, v8\nv_add_f32 v1, v1, v9\nM 80,164,188\ns_waitcnt vmcnt(11)\ns_barrier\nG 4\n"
"v_add_f32 v2, v2, v10\nM 84,168,172\nR 160,187,40960\nv_add_f32 v3, v3, v11\nv_add_f32 v4, v4, v12\nM 84,172,188\n"
"buffer_load_dwordx4 v220, s[12:15], s63 offen lds\nv_add_f32 v5, v5, v13\nM 68,168,168\nR 162,187,45056\nv_add_f32 v6, v6, v14\nv_add_f32 v7, v7, v15\n"
"M 68,172,184\nv_add_f32 v0, v0, v4\nv_add_f32 v1, v1, v5\nv_add_f32 v2, v2, v6\nM 52,168,164\nR 164,187,49152\n"
"v_bfe_u32 v250, v252, 5, 1\nv_add_f32 v3, v3, v7\nv_mul_u32_u24 v250, 2560, v250\nM 52,172,180\nbuffer_load_dwordx4 v221, s[12:15], s63 offen offset:1024 lds\nv_permlane32_swap_b32 v0, v2\n"
"v_and_b32 v251, 16, v252\nM 36,168,160\nR 166,187,53248\ns_add_u32 m0, s50, 98304\nv_permlane32_swap_b32 v1, v3\nv_lshl_add_u32 v250, v251, 1, v250\n"
"M 36,172,176\ns_lshl_b32 s60, s90, 6\nv_add_u32 v250, s60, v250\nG 4\nv_add_f32 v0, v0, v2\nM 40,176,160\n"
"R 168,184,57344\ns_add_u32 s63, s54, 2064512\nv_add_f32 v1, v1, v3\nM 40,180,176\nbuffer_load_dwordx4 v220, s[12:15], s63 offen lds\nv_permlane16_swap_b32 v0, v1\n"
"M 56,176,164\nR 170,184,61440\nv_add_f32 v230, v0, v1\nv_mov_b32 v232, v230\nM 56,180,180\ns_nop 0\n"
"v_permlane16_swap_b32 v230, v232\nv_mov_b32 v231, v230\nv_mov_b32 v233, v232\nM 72,176,168\nR 172,188,0\nv_permlane32_swap_b32 v230, v231\n"
"v_permlane32_swap_b32 v232, v233\nM 72,180,184\nbuffer_load_dwordx4 v221, s[12:15], s63 offen offset:1024 lds\nv_mul_f32 v230, 0xba800000, v230\nM 88,176,172\nR 174,188,4096\n"
"s_add_u32 m0, s50, 106496\nM 88,180,188\nG 4\nv_mul_f32 v214, v230, v230\nv_mul_f32 v231, 0x3a800000, v231\nM 92,160,172\n"
"R 176,185,57344\ns_add_u32 s63, s54, 2293888\nM 92,164,188\nbuffer_load_dwordx4 v220, s[12:15], s63 offen lds\nM 76,160,168\nR 178,185,61440\n"
"v_sub_f32 v231, v231, v214\nM 76,164,184\nv_max_f32 v231, 0, v231\nv_add_f32 v231, 0x3727c5ac, v231\nM 60,160,164\nR 180,189,0\n"
"v_and_b32 v208, s77, v230\nM 60,164,180\nv_rsq_f32 v234, v231\nM 44,160,160\nR 182,189,4096\nv_mul_f32 v216, v231, v234\n"
"M 44,164,176\nG 4\nv_sub_f32 v230, v230, v208\nv_cvt_pk_bf16_f32 v209, v230, v216\nM 32,168,192\nR 160,186,57344\n"
"v_cvt_pk_bf16_f32 v208, v208, v208\nM 32,172,208\nbuffer_load_dwordx4 v221, s[12:15], s63 offen offset:1024 lds\nM 48,168,196\nR 162,186,61440\ns_add_u32 s63, s54, 2523264\n"
"M 48,172,212\ns_add_u32 m0, s50, 114688\ns_mov_b64 s[60:61], 0xffff\nv_cndmask_b32 v208, 0, v208, s[60:61]\nv_mul_f32 v214, 0xc0135761, v234\nM 64,168,200\n"
"R 164,190,0\nv_mul_f32 v215, v234, v234\nM 64,172,216\nLpg9:\ns_cbranch_execz Lpg13\nbuffer_load_dwordx4 v220, s[12:15], s63 offen lds\n"
"M 80,168,204\nR 166,190,4096\nv_mul_f32 v215, v215, v214\nM 80,172,220\ns_waitcnt vmcnt(8)\ns_barrier\n"
"G 4\nM 84,176,204\nR 168,187,57344\nv_mul_f32 v215, 0x3d372713, v215\nM 84,180,220\nbuffer_load_dwordx4 v221, s[12:15], s63 offen offset:1024 lds\n"
"M 68,176,200\nR 170,187,61440\ns_add_u32 s63, s54, 2752640\nM 68,180,216\ns_add_u32 m0, s50, 0\ns_mov_b64 s[60:61], 0xffff\n"
"v_cndmask_b32 v209, 0, v209, s[60:61]\nv_mul_f32 v232, 0xba800000, v232\nM 52,176,196\nR 172,191,0\nv_mul_f32 v217, v232, v232\nM 52,180,212\n"
"buffer_load_dwordx4 v220, s[12:15], s63 offen lds\nM 36,176,192\nR 174,191,4096\nv_mul_f32 v233, 0x3a800000, v233\nM 36,180,208\nG 4\n"
"v_sub_f32 v233, v233, v217\nv_max_f32 v233, 0, v233\nM 40,160,192\nR 176,188,8192\nv_add_f32 v233, 0x3727c5ac, v233\nM 40,164,208\n"
"buffer_load_dwordx4 v221, s[12:15], s63 offen offset:1024 lds\nM 56,160,196\nR 178,188,12288\nv_and_b32 v212, s77, v232\nM 56,164,212\ns_add_u32 m0, s50, 8192\n"
"v_rsq_f32 v235, v233\nM 72,160,200\nR 180,188,16384\nv_mul_f32 v219, v233, v235\nM 72,164,216\ns_add_u32 s63, s54, 2982016\n"
"v_sub_f32 v232, v232, v212\nv_cvt_pk_bf16_f32 v213, v232, v219\nM 88,160,204\nR 182,188,20480\nv_cvt_pk_bf16_f32 v212, v212, v212\nM 88,164,220\n"
"s_mov_b64 s[60:61], 0xffff\nv_cndmask_b32 v212, 0, v212, s[60:61]\nG 4\nv_mul_f32 v217, 0xc0135761, v235\nM 92,168,204\nR 160,189,8192\n"
"v_mul_f32 v218, v235, v235\nM 92,172,220\nbuffer_load_dwordx4 v220, s[12:15], s63 offen lds\nM 76,168,200\nR 162,189,12288\nv_mul_f32 v218, v218, v217\n"
"M 76,172,216\ns_mov_b64 s[60:61], 0xffff\nv_cndmask_b32 v213, 0, v213, s[60:61]\nv_mul_f32 v218, 0x3d372713, v218\nM 60,168,196\nR 164,189,16384\n"
"M 60,172,212\nbuffer_load_dwordx4 v221, s[12:15], s63 offen offset:1024 lds\nM 44,168,192\nR 166,189,20480\ns_mov_b32 s60, 157184\nv_bfe_u32 v246, v252, 4, 4\n"
"v_lshl_add_u32 v246, v246, 3, s60\nM 44,172,208\nds_read_b64 v[238:239], v246 offset:0\nG 5\nM 32,176,224\nR 168,190,8192\n"
"ds_read_b64 v[240:241], v246 offset:128\nM 32,180,240\nds_read_b64 v[242:243], v246 offset:256\ns_add_u32 m0, s50, 16384\nM 48,176,228\nR 170,190,12288\n"
"ds_read_b64 v[244:245], v246 offset:384\nM 48,180,244\nds_read_b64 v[246:247], v246 offset:0\ns_add_u32 s63, s54, 3211392\nM 64,176,232\nR 172,190,16384\n"
"M 64,180,248\nbuffer_load_dwordx4 v220, s[12:15], s63 offen lds\nM 80,176,236\nR 174,190,20480\ns_waitcnt vmcnt(13)\nM 80,180,252\n"
"s_waitcnt vmcnt(9) lgkmcnt(2)\ns_barrier\nM 84,160,236\nR 176,191,8192\nM 84,164,252\nbuffer_load_dwordx4 v221, s[12:15], s63 offen offset:1024 lds\n"
"M 68,160,232\nR 178,191,12288\ns_add_u32 s63, s54, 3440768\nM 68,164,248\ns_add_u32 m0, s50, 24576\nM 52,160,228\n"
"R 180,191,16384\nv_mfma_f32_16x16x32_bf16 v[32:35], v[238:241], v[208:211], v[32:35]\nM 52,164,244\nbuffer_load_dwordx4 v220, s[12:15], s63 offen lds\nM 36,160,224\nR 182,191,20480\n"
"v_mfma_f32_16x16x32_bf16 v[36:39], v[240:243], v[208:211], v[36:39]\nM 36,164,240\nG 4\nM 40,168,224\nR 160,188,24576\nv_mfma_f32_16x16x32_bf16 v[40:43], v[242:245], v[208:211], v[40:43]\n"
"M 40,172,240\nbuffer_load_dwordx4 v221, s[12:15], s63 offen offset:1024 lds\nM 56,168,228\nR 162,188,28672\nv_mfma_f32_16x16x32_bf16 v[44:47], v[244:247], v[208:211], v[44:47]\ns_add_u32 m0, s50, 32768\n"
"s_add_u32 s63, s55, 0\nM 56,172,244\nbuffer_load_dwordx4 v222, s[12:15], s63 offen lds\nM 72,168,232\nR 164,188,32768\nv_mfma_f32_16x16x32_bf16 v[48:51], v[244:247], v[210:213], v[48:51]\n"
"M 72,172,248\nM 88,168,236\nR 166,188,36864\nv_mfma_f32_16x16x32_bf16 v[52:55], v[238:241], v[210:213], v[52:55]\nM 88,172,252\nG 4\n"
"M 92,176,236\nR 168,189,24576\nv_mfma_f32_16x16x32_bf16 v[56:59], v[240:243], v[210:213], v[56:59]\nM 92,180,252\nbuffer_load_dwordx4 v223, s[12:15], s63 offen offset:1024 lds\nM 76,176,232\n"
"R 170,189,28672\nv_mfma_f32_16x16x32_bf16 v[60:63], v[242:245], v[210:213], v[60:63]\nM 76,180,248\nM 60,176,228\nR 172,189,32768\nM 60,180,244\n"
"M 44,176,224\nR 174,189,36864\nM 44,180,240\ns_waitcnt vmcnt(10)\ns_barrier\nG 4\n"
"M 96,160,128\nR 176,190,24576\nM 96,164,144\ns_mov_b32 m0, s51\nM 112,160,132\nR 178,190,28672\n"
"s_lshl2_add_u32 s60, s45, s40\nv_lshlrev_b32 v224, 3, v252\ns_mul_i32 s60, s60, 7168\ns_bfm_b64 exec, 14, 0\nbuffer_load_dword v224, v224, s[16:19], s60 offen\ns_mov_b64 exec, -1\n"
"M 112,164,148\nds_write_addtid_b32 v64 offset:0\nM 128,160,136\nR 180,190,32768\nM 128,164,152\nds_write_addtid_b32 v65 offset:256\n"
"M 144,160,140\nR 182,190,36864\nds_write_addtid_b32 v66 offset:512\nM 144,164,156\nds_write_addtid_b32 v67 offset:768\nds_write_addtid_b32 v68 offset:1024\n"
"G 9\nM 148,168,140\nR 160,191,24576\nds_write_addtid_b32 v69 offset:1280\nM 148,172,156\nds_write_addtid_b32 v70 offset:1536\n"
"ds_write_addtid_b32 v71 offset:1792\nM 132,168,136\nR 162,191,28672\nds_write_addtid_b32 v72 offset:2048\nM 132,172,152\nds_write_addtid_b32 v73 offset:2304\n"
"ds_write_addtid_b32 v74 offset:2560\nM 116,168,132\nR 164,191,32768\nds_write_addtid_b32 v75 offset:2816\nM 116,172,148\nds_write_addtid_b32 v76 offset:3072\n"
"ds_write_addtid_b32 v77 offset:3328\nM 100,168,128\nR 166,191,36864\nds_write_addtid_b32 v78 offset:3584\nM 100,172,144\nds_write_addtid_b32 v79 offset:3840\n"
"ds_write_addtid_b32 v80 offset:4096\nG 15\nM 104,176,128\nR 168,188,40960\nds_write_addtid_b32 v81 offset:4352\nM 104,180,144\n"
"ds_write_addtid_b32 v82 offset:4608\nds_write_addtid_b32 v83 offset:4864\nM 120,176,132\nR 170,188,45056\nds_write_addtid_b32 v84 offset:5120\nM 120,180,148\n"
"ds_write_addtid_b32 v85 offset:5376\nds_write_addtid_b32 v86 offset:5632\nM 136,176,136\nR 172,188,49152\nds_write_addtid_b32 v87 offset:5888\nM 136,180,152\n"
"ds_write_addtid_b32 v88 offset:6144\nds_write_addtid_b32 v89 offset:6400\nM 152,176,140\nR 174,188,53248\nds_write_addtid_b32 v90 offset:6656\nM 152,180,156\n"
"ds_write_addtid_b32 v91 offset:6912\nds_write_addtid_b32 v92 offset:7168\nG 15\nM 156,160,140\nR 176,189,40960\nds_write_addtid_b32 v93 offset:7424\n"
"M 156,164,156\nds_write_addtid_b32 v94 offset:7680\nds_write_addtid_b32 v95 offset:7936\nM 140,160,136\nR 178,189,45056\ns_add_u32 m0, s50, 40960\n"
"s_add_u32 s63, s55, 458752\nM 140,164,152\nbuffer_load_dwordx4 v222, s[12:15], s63 offen lds\nM 124,160,132\nR 180,189,49152\ns_waitcnt vmcnt(8) lgkmcnt(2)\n"
"s_barrier\nM 124,164,148\nds_read2st64_b32 v[64:65], v225 offset0:0 offset1:1\nds_read2st64_b32 v[66:67], v225 offset0:2 offset1:3\nM 108,160,128\nR 182,189,53248\n"
"ds_read2st64_b32 v[68:69], v225 offset0:4 offset1:5\nM 108,164,144\nds_read2st64_b32 v[70:71], v225 offset0:6 offset1:7\nds_read2st64_b32 v[72:73], v225 offset0:8 offset1:9\nM 96,168,160\nR 160,190,40960\n"
"ds_read2st64_b32 v[74:75], v225 offset0:10 offset1:11\nM 96,172,176\nbuffer_load_dwordx4 v223, s[12:15], s63 offen offset:1024 lds\nds_read2st64_b32 v[76:77], v225 offset0:12 offset1:13\nM 112,168,164\nR 162,190,45056\n"
"ds_read2st64_b32 v[78:79], v225 offset0:14 offset1:15\nM 112,172,180\ns_add_u32 m0, s50, 49152\nds_read2st64_b32 v[80:81], v225 offset0:16 offset1:17\nds_read2st64_b32 v[82:83], v225 offset0:18 offset1:19\nM 128,168,168\n"
"R 164,190,49152\nds_read2st64_b32 v[84:85], v225 offset0:20 offset1:21\nG 14\nv_add_f32 v32, v32, v64\nM 128,172,184\ns_add_u32 s63, s55, 917504\n"
"ds_read2st64_b32 v[86:87], v225 offset0:22 offset1:23\nds_read2st64_b32 v[88:89], v225 offset0:24 offset1:25\nM 144,168,172\nR 166,190,53248\nds_read2st64_b32 v[90:91], v225 offset0:26 offset1:27\nv_add_f32 v33, v33, v65\n"
"M 144,172,188\nds_read2st64_b32 v[92:93], v225 offset0:28 offset1:29\nds_read2st64_b32 v[94:95], v225 offset0:30 offset1:31\nG 15\nM 148,176,172\nR 168,191,40960\n"
"v_add_f32 v34, v34, v66\nv_add_f32 v35, v35, v67\nM 148,180,188\nbuffer_load_dwordx4 v222, s[12:15], s63 offen lds\nv_add_f32 v36, v36, v68\nM 132,176,168\n"
"R 170,191,45056\nv_add_f32 v37, v37, v69\nv_add_f32 v38, v38, v70\nM 132,180,184\nv_add_f32 v39, v39, v71\nv_add_f32 v40, v40, v72\n"
"M 116,176,164\nR 172,191,49152\nv_add_f32 v41, v41, v73\nM 116,180,180\nG 15\nv_add_f32 v42, v42, v74\n"
"v_add_f32 v43, v43, v75\nM 100,176,160\nR 174,191,53248\nv_add_f32 v44, v44, v76\nM 100,180,176\nG 7\n"
"v_add_f32 v45, v45, v77\nv_add_f32 v46, v46, v78\nM 104,160,160\nR 176,184,0\nv_add_f32 v47, v47, v79\nM 104,164,176\n"
"buffer_load_dwordx4 v223, s[12:15], s63 offen offset:1024 lds\nM 120,160,164\nR 178,184,4096\nv_add_f32 v48, v48, v80\nM 120,164,180\ns_add_u32 m0, s50, 57344\n"
"v_add_f32 v49, v49, v81\nv_add_f32 v50, v50, v82\nM 136,160,168\nR 180,184,8192\nv_add_f32 v51, v51, v83\nM 136,164,184\n"
"s_add_u32 s63, s55, 1376256\nv_add_f32 v52, v52, v84\nv_add_f32 v53, v53, v85\nM 152,160,172\nR 182,184,12288\nv_add_f32 v54, v54, v86\n"
"M 152,164,188\nG 4\nv_add_f32 v55, v55, v87\nv_add_f32 v56, v56, v88\nM 156,168,172\nR 160,185,0\n"
"v_add_f32 v57, v57, v89\nM 156,172,188\nbuffer_load_dwordx4 v222, s[12:15], s63 offen lds\nM 140,168,168\nR 162,185,4096\nv_add_f32 v58, v58, v90\n"
"M 140,172,184\nv_add_f32 v59, v59, v91\nv_add_f32 v60, v60, v92\nM 124,168,164\nR 164,185,8192\nv_add_f32 v61, v61, v93\n"
"M 124,172,180\nv_add_f32 v62, v62, v94\nv_add_f32 v63, v63, v95\nM 108,168,160\nR 166,185,12288\nv_mul_f32 v230, v32, v32\n"
"v_mul_f32 v231, v33, v33\nM 108,172,176\ns_add_u32 s84, s73, 23068672\ns_add_u32 s85, s73, 23183360\nG 4\nv_mul_f32 v232, v34, v34\n"
"M 96,176,192\nR 168,186,0\nv_mul_f32 v233, v35, v35\nv_fma_f32 v230, v230, v215, v214\nM 96,180,208\nv_fma_f32 v231, v231, v215, v214\n"
"v_fma_f32 v232, v232, v215, v214\nv_fma_f32 v233, v233, v215, v214\nM 112,176,196\nR 170,186,4096\ns_lshl2_add_u32 s60, s45, s40\ns_waitcnt vmcnt(5)\n"
"v_lshlrev_b32 v224, 3, v252\ns_mul_i32 s60, s60, 7168\ns_bfm_b64 exec, 14, 14\nbuffer_load_dword v224, v224, s[16:19], s60 offen\ns_mov_b64 exec, -1\ns_barrier\n"
"v_mul_f32 v230, v32, v230\nM 112,180,212\nv_exp_f32 v230, v230\nv_mul_f32 v231, v33, v231\nM 128,176,200\nR 172,186,8192\n"
"v_mul_f32 v232, v34, v232\nv_mul_f32 v233, v35, v233\nM 128,180,216\nv_exp_f32 v231, v231\nv_exp_f32 v232, v232\nM 144,176,204\n"
"R 174,186,12288\nv_fma_f32 v230, v230, v216, v216\nv_exp_f32 v233, v233\nM 144,180,220\nG 4\nv_rcp_f32 v230, v230\n"
"v_fma_f32 v231, v231, v216, v216\nM 148,160,204\nR 176,187,0\nv_fma_f32 v232, v232, v216, v216\nv_fma_f32 v233, v233, v216, v216\nM 148,164,220\n"
"buffer_load_dwordx4 v223, s[12:15], s63 offen offset:1024 lds\nv_rcp_f32 v231, v231\nM 132,160,200\nR 178,187,4096\nv_mul_f32 v230, v32, v230\nv_rcp_f32 v232, v232\n"
"M 132,164,216\ns_add_u32 m0, s50, 65536\nv_rcp_f32 v233, v233\nv_mul_f32 v231, v33, v231\nM 116,160,196\nR 180,187,8192\n"
"v_mul_f32 v232, v34, v232\nv_mul_f32 v233, v35, v233\nM 116,164,212\ns_add_u32 s63, s55, 1835008\nv_cvt_pk_bf16_f32 v32, v230, v231\nv_cvt_pk_bf16_f32 v33, v232, v233\n"
"v_mul_f32 v234, v36, v36\nM 100,160,192\nR 182,187,12288\nv_mul_f32 v235, v37, v37\nv_mul_f32 v236, v38, v38\nM 100,164,208\n"
"G 4\nv_mul_f32 v237, v39, v39\nv_fma_f32 v234, v234, v215, v214\nv_fma_f32 v235, v235, v215, v214\nM 104,168,192\nR 160,184,16384\n"
"v_fma_f32 v236, v236, v215, v214\nv_fma_f32 v237, v237, v215, v214\nM 104,172,208\nbuffer_load_dwordx4 v222, s[12:15], s63 offen lds\nv_mul_f32 v234, v36, v234\nM 120,168,196\n"
"R 162,184,20480\nv_mul_f32 v235, v37, v235\nv_mul_f32 v236, v38, v236\nM 120,172,212\nv_exp_f32 v234, v234\nv_mul_f32 v237, v39, v237\n"
"M 136,168,200\nR 164,184,24576\nv_fma_f32 v234, v234, v216, v216\nv_exp_f32 v235, v235\nM 136,172,216\nv_exp_f32 v236, v236\n"
"v_exp_f32 v237, v237\nM 152,168,204\nR 166,184,28672\nv_fma_f32 v235, v235, v216, v216\nv_fma_f32 v236, v236, v216, v216\nM 152,172,220\n"
"G 4\nv_rcp_f32 v234, v234\nv_fma_f32 v237, v237, v216, v216\nM 156,176,204\nR 168,185,16384\nv_mul_f32 v234, v36, v234\n"
"v_rcp_f32 v235, v235\nM 156,180,220\nbuffer_load_dwordx4 v223, s[12:15], s63 offen offset:1024 lds\nv_rcp_f32 v236, v236\nM 140,176,200\nR 170,185,20480\n"
"v_mul_f32 v235, v37, v235\nv_rcp_f32 v237, v237\nM 140,180,216\nv_mul_f32 v236, v38, v236\nv_mul_f32 v237, v39, v237\nv_cvt_pk_bf16_f32 v34, v234, v235\n"
"M 124,176,196\nR 172,185,24576\nv_cvt_pk_bf16_f32 v35, v236, v237\nv_mul_f32 v230, v40, v40\nM 124,180,212\nbuffer_store_dwordx4 v[32:35], v252, s[20:23], s84 offen offset:0 sc1\n"
"v_mul_f32 v231, v41, v41\nM 108,176,192\nR 174,185,28672\ns_mov_b32 s60, 157696\nv_bfe_u32 v246, v252, 4, 4\nv_lshl_add_u32 v246, v246, 3, s60\n"
"v_mul_f32 v232, v42, v42\nM 108,180,208\nds_read_b64 v[238:239], v246 offset:0\nG 5\nv_mul_f32 v233, v43, v43\nM 96,160,224\n"
"R 176,186,16384\nds_read_b64 v[240:241], v246 offset:128\nv_fma_f32 v230, v230, v215, v214\nM 96,164,240\nds_read_b64 v[242:243], v246 offset:256\ns_add_u32 m0, s50, 73728\n"
"v_fma_f32 v231, v231, v215, v214\nM 112,160,228\nR 178,186,20480\nds_read_b64 v[244:245], v246 offset:384\nv_fma_f32 v232, v232, v215, v214\nM 112,164,244\n"
"ds_read_b64 v[246:247], v246 offset:0\ns_add_u32 s63, s55, 2293760\nv_fma_f32 v233, v233, v215, v214\nM 128,160,232\nR 180,186,24576\nv_mul_f32 v230, v40, v230\n"
"v_mul_f32 v231, v41, v231\nM 128,164,248\nbuffer_load_dwordx4 v222, s[12:15], s63 offen lds\nv_mul_f32 v232, v42, v232\nM 144,160,236\nR 182,186,28672\n"
"v_mul_f32 v233, v43, v233\nv_exp_f32 v230, v230\nM 144,164,252\nG 9\nv_exp_f32 v231, v231\nv_exp_f32 v232, v232\n"
"M 148,168,236\nR 160,187,16384\nv_fma_f32 v230, v230, v216, v216\nv_exp_f32 v233, v233\nM 148,172,252\nbuffer_load_dwordx4 v223, s[12:15], s63 offen offset:1024 lds\n"
"v_fma_f32 v231, v231, v216, v216\nM 132,168,232\nR 162,187,20480\ns_add_u32 s63, s55, 2752512\nv_fma_f32 v232, v232, v216, v216\nM 132,172,248\n"
"s_add_u32 m0, s50, 81920\nv_rcp_f32 v230, v230\nv_fma_f32 v233, v233, v216, v216\nM 116,168,228\nR 164,187,24576\nG 9\n"
"v_mfma_f32_16x16x32_bf16 v[96:99], v[238:241], v[208:211], v[96:99]\nv_mul_f32 v230, v40, v230\nv_rcp_f32 v231, v231\nM 116,172,244\nbuffer_load_dwordx4 v222, s[12:15], s63 offen lds\nv_rcp_f32 v232, v232\n"
"M 100,168,224\nR 166,187,28672\nG 9\nv_mfma_f32_16x16x32_bf16 v[100:103], v[240:243], v[208:211], v[100:103]\nv_mul_f32 v231, v41, v231\nv_rcp_f32 v233, v233\n"
"M 100,172,240\nG 4\nv_mul_f32 v232, v42, v232\nv_mul_f32 v233, v43, v233\nv_cvt_pk_bf16_f32 v40, v230, v231\nM 104,176,224\n"
"R 168,184,32768\nv_mfma_f32_16x16x32_bf16 v[104:107], v[242:245], v[208:211], v[104:107]\nv_cvt_pk_bf16_f32 v41, v232, v233\nv_mul_f32 v234, v44, v44\nM 104,180,240\nbuffer_load_dwordx4 v223, s[12:15], s63 offen offset:1024 lds\n"
"v_mul_f32 v235, v45, v45\nM 120,176,228\nR 170,184,36864\nv_mfma_f32_16x16x32_bf16 v[108:111], v[244:247], v[208:211], v[108:111]\nv_mul_f32 v236, v46, v46\nv_mul_f32 v237, v47, v47\n"
"M 120,180,244\ns_add_u32 m0, s50, 90112\nv_fma_f32 v234, v234, v215, v214\nv_fma_f32 v235, v235, v215, v214\nv_fma_f32 v236, v236, v215, v214\nLpg10:\n"
"s_cbranch_execz Lpg14\nM 136,176,232\nR 172,186,32768\nv_mfma_f32_16x16x32_bf16 v[112:115], v[244:247], v[210:213], v[112:115]\nv_fma_f32 v237, v237, v215, v214\nv_mul_f32 v234, v44, v234\n"
"M 136,180,248\ns_add_u32 s63, s55, 3211264\nv_exp_f32 v234, v234\nv_mul_f32 v235, v45, v235\nM 152,176,236\nR 174,186,36864\n"
"v_mfma_f32_16x16x32_bf16 v[116:119], v[238:241], v[210:213], v[116:119]\nv_mul_f32 v236, v46, v236\nv_mul_f32 v237, v47, v237\nM 152,180,252\nG 4\nv_exp_f32 v235, v235\n"
"v_exp_f32 v236, v236\nM 156,160,236\nR 176,185,32768\nv_mfma_f32_16x16x32_bf16 v[120:123], v[240:243], v[210:213], v[120:123]\nv_fma_f32 v234, v234, v216, v216\nv_exp_f32 v237, v237\n"
"M 156,164,252\nbuffer_load_dwordx4 v222, s[12:15], s63 offen lds\nv_fma_f32 v235, v235, v216, v216\nM 140,160,232\nR 178,185,36864\nv_mfma_f32_16x16x32_bf16 v[124:127], v[242:245], v[210:213], v[124:127]\n"
"v_fma_f32 v236, v236, v216, v216\nv_fma_f32 v237, v237, v216, v216\nM 140,164,248\nv_rcp_f32 v234, v234\nv_rcp_f32 v235, v235\nM 124,160,228\n"
"R 180,187,32768\nv_mul_f32 v234, v44, v234\nv_rcp_f32 v236, v236\nM 124,164,244\nbuffer_load_dwordx4 v223, s[12:15], s63 offen offset:1024 lds\nv_rcp_f32 v237, v237\n"
"M 108,160,224\nR 182,187,36864\nv_mul_f32 v235, v45, v235\nv_mul_f32 v236, v46, v236\nM 108,164,240\ns_waitcnt vmcnt(12)\n"
"s_barrier\nG 4\nv_mul_f32 v237, v47, v237\nv_mfma_f32_16x16x32_bf16 v[0:3], v[168:171], a[0:3], 0\nR 160,184,40960\nv_cvt_pk_bf16_f32 v42, v234, v235\n"
"v_cvt_pk_bf16_f32 v43, v236, v237\nM 0,172,16\ns_mov_b32 m0, s51\nv_mul_f32 v230, v48, v48\nv_mul_f32 v231, v49, v49\nv_mul_f32 v232, v50, v50\n"
"v_mfma_f32_16x16x32_bf16 v[16:19], v[168:171], a[4:7], 0\nR 162,184,45056\nv_mul_f32 v233, v51, v51\nv_fma_f32 v230, v230, v218, v217\nM 16,172,20\nbuffer_store_dwordx4 v[40:43], v252, s[20:23], s84 offen offset:1024 sc1\n"
"v_fma_f32 v231, v231, v218, v217\nv_mfma_f32_16x16x32_bf16 v[8:11], v[168:171], a[8:11], 0\nR 164,186,40960\nv_fma_f32 v232, v232, v218, v217\nv_fma_f32 v233, v233, v218, v217\nM 8,172,24\n"
"ds_write_addtid_b32 v128 offset:0\nv_mul_f32 v230, v48, v230\nv_mul_f32 v231, v49, v231\nv_mul_f32 v232, v50, v232\nv_mfma_f32_16x16x32_bf16 v[24:27], v[168:171], a[12:15], 0\nR 166,186,45056\n"
"ds_write_addtid_b32 v129 offset:256\nv_mul_f32 v233, v51, v233\nv_exp_f32 v230, v230\nM 24,172,28\nds_write_addtid_b32 v130 offset:512\nG 7\n"
"v_exp_f32 v231, v231\nv_exp_f32 v232, v232\nv_mfma_f32_16x16x32_bf16 v[28:31], v[176:179], a[12:15], 0\nR 168,185,40960\nds_write_addtid_b32 v131 offset:768\nv_fma_f32 v230, v230, v219, v219\n"
"v_exp_f32 v233, v233\nM 28,180,28\nds_write_addtid_b32 v132 offset:1024\nv_rcp_f32 v230, v230\nv_fma_f32 v231, v231, v219, v219\nv_mfma_f32_16x16x32_bf16 v[12:15], v[176:179], a[8:11], 0\n"
"R 170,185,45056\nds_write_addtid_b32 v133 offset:1280\nv_fma_f32 v232, v232, v219, v219\nv_fma_f32 v233, v233, v219, v219\nM 12,180,24\nds_write_addtid_b32 v134 offset:1536\n"
"v_rcp_f32 v231, v231\nv_rcp_f32 v232, v232\nv_mfma_f32_16x16x32_bf16 v[20:23], v[176:179], a[4:7], 0\nR 172,187,40960\nds_write_addtid_b32 v135 offset:1792\nv_mul_f32 v230, v48, v230\n"
"v_rcp_f32 v233, v233\nM 20,180,20\nds_write_addtid_b32 v136 offset:2048\nv_mul_f32 v231, v49, v231\nv_mul_f32 v232, v50, v232\nv_mul_f32 v233, v51, v233\n"
"v_mfma_f32_16x16x32_bf16 v[4:7], v[176:179], a[0:3], 0\nR 174,187,45056\nds_write_addtid_b32 v137 offset:2304\nv_cvt_pk_bf16_f32 v48, v230, v231\nv_cvt_pk_bf16_f32 v49, v232, v233\nM 4,180,16\n"
"ds_write_addtid_b32 v138 offset:2560\nG 14\nv_mul_f32 v234, v52, v52\nv_mul_f32 v235, v53, v53\nv_mul_f32 v236, v54, v54\nM 0,160,32\n"
"R 176,184,49152\nds_write_addtid_b32 v139 offset:2816\nv_mul_f32 v237, v55, v55\nv_fma_f32 v234, v234, v218, v217\nM 0,164,48\nds_write_addtid_b32 v140 offset:3072\n"
"v_fma_f32 v235, v235, v218, v217\nv_fma_f32 v236, v236, v218, v217\nv_fma_f32 v237, v237, v218, v217\nM 16,160,36\nR 178,184,53248\nds_write_addtid_b32 v141 offset:3328\n"
"v_mul_f32 v234, v52, v234\nv_mul_f32 v235, v53, v235\nM 16,164,52\nds_write_addtid_b32 v142 offset:3584\nv_exp_f32 v234, v234\nv_mul_f32 v236, v54, v236\n"
"M 8,160,40\nR 180,186,49152\nds_write_addtid_b32 v143 offset:3840\nv_mul_f32 v237, v55, v237\nv_exp_f32 v235, v235\nM 8,164,56\n"
"ds_write_addtid_b32 v144 offset:4096\nv_exp_f32 v236, v236\nv_exp_f32 v237, v237\nM 24,160,44\nR 182,186,53248\nds_write_addtid_b32 v145 offset:4352\n"
"v_fma_f32 v234, v234, v219, v219\nv_fma_f32 v235, v235, v219, v219\nM 24,164,60\nds_write_addtid_b32 v146 offset:4608\nG 14\nv_rcp_f32 v234, v234\n"
"v_fma_f32 v236, v236, v219, v219\nM 28,168,44\nR 160,185,49152\nds_write_addtid_b32 v147 offset:4864\nv_fma_f32 v237, v237, v219, v219\nv_rcp_f32 v235, v235\n"
"M 28,172,60\nds_write_addtid_b32 v148 offset:5120\nds_write_addtid_b32 v149 offset:5376\nds_write_addtid_b32 v150 offset:5632\nds_write_addtid_b32 v151 offset:5888\nds_write_addtid_b32 v152 offset:6144\n"
"ds_write_addtid_b32 v153 offset:6400\nds_write_addtid_b32 v154 offset:6656\nds_write_addtid_b32 v155 offset:6912\nds_write_addtid_b32 v156 offset:7168\nds_write_addtid_b32 v157 offset:7424\nds_write_addtid_b32 v158 offset:7680\n"
"ds_write_addtid_b32 v159 offset:7936\nv_rcp_f32 v236, v236\nv_rcp_f32 v237, v237\nM 12,168,40\nR 162,185,53248\ns_add_u32 m0, s50, 98304\n"
"s_add_u32 s63, s54, 256\nv_mul_f32 v234, v52, v234\nM 12,172,56\nbuffer_load_dwordx4 v220, s[12:15], s63 offen lds\nv_mul_f32 v235, v53, v235\nM 20,168,36\n"
"R 164,187,49152\ns_waitcnt vmcnt(4) lgkmcnt(2)\ns_barrier\nv_mul_f32 v236, v54, v236\nM 20,172,52\nds_read2st64_b32 v[128:129], v225 offset0:0 offset1:1\n"
"v_mul_f32 v237, v55, v237\nv_cvt_pk_bf16_f32 v50, v234, v235\nv_cvt_pk_bf16_f32 v51, v236, v237\nM 4,168,32\nR 166,187,53248\nds_read2st64_b32 v[130:131], v225 offset0:2 offset1:3\n"
"v_mul_f32 v230, v56, v56\nv_mul_f32 v231, v57, v57\nM 4,172,48\nds_read2st64_b32 v[132:133], v225 offset0:4 offset1:5\nv_mul_f32 v232, v58, v58\nv_mul_f32 v233, v59, v59\n"
"v_fma_f32 v230, v230, v218, v217\nM 0,176,64\nR 168,184,57344\nds_read2st64_b32 v[134:135], v225 offset0:6 offset1:7\nv_fma_f32 v231, v231, v218, v217\nv_fma_f32 v232, v232, v218, v217\n"
"M 0,180,80\nbuffer_load_dwordx4 v221, s[12:15], s63 offen offset:1024 lds\nds_read2st64_b32 v[136:137], v225 offset0:8 offset1:9\nv_fma_f32 v233, v233, v218, v217\nM 16,176,68\nR 170,184,61440\n"
"s_add_u32 m0, s50, 106496\ns_add_u32 s63, s54, 229632\nds_read2st64_b32 v[138:139], v225 offset0:10 offset1:11\nv_mul_f32 v230, v56, v230\nM 16,180,84\nbuffer_load_dwordx4 v220, s[12:15], s63 offen lds\n"
"ds_read2st64_b32 v[140:141], v225 offset0:12 offset1:13\nv_mul_f32 v231, v57, v231\nM 8,176,72\nR 172,186,57344\nds_read2st64_b32 v[142:143], v225 offset0:14 offset1:15\nv_mul_f32 v232, v58, v232\n"
"v_mul_f32 v233, v59, v233\nM 8,180,88\nbuffer_store_dwordx4 v[48:51], v252, s[20:23], s85 offen offset:0 sc1\nds_read2st64_b32 v[144:145], v225 offset0:16 offset1:17\nv_exp_f32 v230, v230\nM 24,176,76\n"
"R 174,186,61440\nds_read2st64_b32 v[146:147], v225 offset0:18 offset1:19\nv_fma_f32 v230, v230, v219, v219\nv_exp_f32 v231, v231\nM 24,180,92\nds_read2st64_b32 v[148:149], v225 offset0:20 offset1:21\n"
"G 14\nv_exp_f32 v232, v232\nv_exp_f32 v233, v233\nM 28,160,76\nR 176,185,57344\nds_read2st64_b32 v[150:151], v225 offset0:22 offset1:23\n"
"v_fma_f32 v231, v231, v219, v219\nv_fma_f32 v232, v232, v219, v219\nM 28,164,92\nbuffer_load_dwordx4 v221, s[12:15], s63 offen offset:1024 lds\nds_read2st64_b32 v[152:153], v225 offset0:24 offset1:25\nv_fma_f32 v233, v233, v219, v219\n"
"M 12,160,72\nR 178,185,61440\ns_add_u32 s63, s54, 459008\nds_read2st64_b32 v[154:155], v225 offset0:26 offset1:27\nv_rcp_f32 v230, v230\nM 12,164,88\n"
"s_add_u32 m0, s50, 114688\nds_read2st64_b32 v[156:157], v225 offset0:28 offset1:29\nv_rcp_f32 v231, v231\nv_rcp_f32 v232, v232\nM 20,160,68\nR 180,187,57344\n"
"ds_read2st64_b32 v[158:159], v225 offset0:30 offset1:31\nv_mul_f32 v230, v56, v230\nv_rcp_f32 v233, v233\nM 20,164,84\nbuffer_load_dwordx4 v220, s[12:15], s63 offen lds\nv_add_f32 v96, v96, v128\n"
"v_mul_f32 v231, v57, v231\nM 4,160,64\nR 182,187,61440\nv_mul_f32 v232, v58, v232\nv_add_f32 v97, v97, v129\nv_mul_f32 v233, v59, v233\n"
"M 4,164,80\nG 11\nv_cvt_pk_bf16_f32 v56, v230, v231\nv_cvt_pk_bf16_f32 v57, v232, v233\nv_add_f32 v98, v98, v130\nv_mul_f32 v234, v60, v60\n"
"M 0,168,96\nR 160,188,0\nv_mul_f32 v235, v61, v61\nv_add_f32 v99, v99, v131\nv_mul_f32 v236, v62, v62\nM 0,172,112\n"
"buffer_load_dwordx4 v221, s[12:15], s63 offen offset:1024 lds\nv_add_f32 v100, v100, v132\nv_mul_f32 v237, v63, v63\nM 16,168,100\nR 162,188,4096\nv_fma_f32 v234, v234, v218, v217\n"
"v_add_f32 v101, v101, v133\nv_fma_f32 v235, v235, v218, v217\nM 16,172,116\ns_add_u32 m0, s50, 0\nv_fma_f32 v236, v236, v218, v217\nv_fma_f32 v237, v237, v218, v217\n"
"v_add_f32 v102, v102, v134\nv_mul_f32 v234, v60, v234\nM 8,168,104\nR 164,190,0\nv_mul_f32 v235, v61, v235\nv_add_f32 v103, v103, v135\n"
"v_mul_f32 v236, v62, v236\nM 8,172,120\ns_add_u32 s63, s54, 688384\nv_exp_f32 v234, v234\nv_add_f32 v104, v104, v136\nv_mul_f32 v237, v63, v237\n"
"M 24,168,108\nR 166,190,4096\nv_fma_f32 v234, v234, v219, v219\nv_add_f32 v105, v105, v137\nv_exp_f32 v235, v235\nM 24,172,124\n"
"G 4\nv_add_f32 v106, v106, v138\nv_add_f32 v107, v107, v139\nv_add_f32 v108, v108, v140\nv_exp_f32 v236, v236\nM 28,176,108\n"
"R 168,189,0\nv_add_f32 v109, v109, v141\nv_add_f32 v110, v110, v142\nv_exp_f32 v237, v237\nM 28,180,124\nbuffer_load_dwordx4 v220, s[12:15], s63 offen lds\n"
"v_add_f32 v111, v111, v143\nv_fma_f32 v235, v235, v219, v219\nM 12,176,104\nR 170,189,4096\nv_add_f32 v112, v112, v144\nv_add_f32 v113, v113, v145\n"
"v_fma_f32 v236, v236, v219, v219\nM 12,180,120\nv_add_f32 v114, v114, v146\nv_add_f32 v115, v115, v147\nv_add_f32 v116, v116, v148\nv_fma_f32 v237, v237, v219, v219\n"
"M 20,176,100\nR 172,191,0\nv_add_f32 v117, v117, v149\nv_add_f32 v118, v118, v150\nv_rcp_f32 v234, v234\nM 20,180,116\n"
"buffer_load_dwordx4 v221, s[12:15], s63 offen offset:1024 lds\nv_add_f32 v119, v119, v151\nv_add_f32 v120, v120, v152\nv_add_f32 v121, v121, v153\nv_add_f32 v122, v122, v154\nv_add_f32 v123, v123, v155\n"
"v_add_f32 v124, v124, v156\nv_add_f32 v125, v125, v157\nv_add_f32 v126, v126, v158\nv_add_f32 v127, v127, v159\nM 4,176,96\nR 174,191,4096\n"
"s_add_u32 m0, s50, 8192\nM 4,180,112\nG 4\nv_rcp_f32 v235, v235\nM 0,160,128\nR 176,188,8192\n"
"s_add_u32 s63, s54, 917760\nM 0,164,144\nbuffer_load_dwordx4 v220, s[12:15], s63 offen lds\nM 16,160,132\nR 178,188,12288\nv_mul_f32 v234, v60, v234\n"
"M 16,164,148\nv_rcp_f32 v236, v236\nM 8,160,136\nR 180,190,8192\nLq571:\ns_lshr_b32 s60, s45, 3\n"
"s_lshl2_add_u32 s60, s60, s40\ns_lshl_b32 s60, s60, 8\nv_lshl_add_u32 v224, v252, 3, s60\nv_add_u32 v224, Lq571+4096-LqPC, v224\nv_min_u32 v224, LqE-LqPC-4, v224\ns_mov_b64 exec, 3\n"
"global_load_dword v224, v224, s[98:99]\ns_mov_b64 exec, -1\nM 8,164,152\nv_rcp_f32 v237, v237\nM 24,160,140\nR 182,190,12288\n"
"v_mul_f32 v235, v61, v235\nM 24,164,156\nG 4\nv_mul_f32 v236, v62, v236\nv_mul_f32 v237, v63, v237\nM 28,168,140\n"
"R 160,189,8192\nv_cvt_pk_bf16_f32 v58, v234, v235\nM 28,172,156\nbuffer_load_dwordx4 v221, s[12:15], s63 offen offset:1024 lds\nM 12,168,136\nR 162,189,12288\n"
"s_add_u32 m0, s50, 16384\ns_add_u32 s63, s54, 1147136\nM 12,172,152\nbuffer_load_dwordx4 v220, s[12:15], s63 offen lds\nM 20,168,132\nR 164,191,8192\n"
"v_cvt_pk_bf16_f32 v59, v236, v237\nM 20,172,148\nM 4,168,128\nR 166,191,12288\nM 4,172,144\nG 4\n"
"M 0,176,160\nR 168,188,16384\nM 0,180,176\nbuffer_load_dwordx4 v221, s[12:15], s63 offen offset:1024 lds\nM 16,176,164\nR 170,188,20480\n"
"s_add_u32 m0, s50, 24576\ns_add_u32 s63, s54, 1376512\nM 16,180,180\nbuffer_load_dwordx4 v220, s[12:15], s63 offen lds\nM 8,176,168\nR 172,190,16384\n"
"M 8,180,184\nbuffer_store_dwordx4 v[56:59], v252, s[20:23], s85 offen offset:1024 sc1\nM 24,176,172\nR 174,190,20480\nM 24,180,188\nG 4\n"
"M 28,160,172\nR 176,189,16384\ns_waitcnt vmcnt(11)\ns_barrier\nv_mul_f32 v230, v96, v96\nM 28,164,188\n"
"buffer_load_dwordx4 v221, s[12:15], s63 offen offset:1024 lds\nv_mul_f32 v231, v97, v97\nM 12,160,168\nR 178,189,20480\ns_add_u32 m0, s50, 32768\ns_add_u32 s63, s54, 1605888\n"
"v_mul_f32 v232, v98, v98\nM 12,164,184\nbuffer_load_dwordx4 v220, s[12:15], s63 offen lds\nv_mul_f32 v233, v99, v99\nM 20,160,164\nR 180,191,16384\n"
"v_fma_f32 v230, v230, v215, v214\nv_fma_f32 v231, v231, v215, v214\nM 20,164,180\ns_add_u32 s84, s73, 23070720\ns_add_u32 s85, s73, 23185408\nv_fma_f32 v232, v232, v215, v214\n"
"v_fma_f32 v233, v233, v215, v214\nM 4,160,160\nR 182,191,20480\ns_mov_b32 s60, 158720\nv_bfe_u32 v242, v252, 4, 4\nv_lshl_add_u32 v242, v242, 3, s60\n"
"v_mul_f32 v230, v96, v230\nM 4,164,176\nds_read_b64 v[238:239], v242 offset:0\nG 5\nv_mul_f32 v231, v97, v231\nM 0,168,192\n"
"R 160,188,24576\nds_read_b64 v[240:241], v242 offset:128\nv_mul_f32 v232, v98, v232\nM 0,172,208\nds_read_b64 v[242:243], v242 offset:0\nv_mul_f32 v233, v99, v233\n"
"v_exp_f32 v230, v230\nM 16,168,196\nR 162,188,28672\nv_fma_f32 v230, v230, v216, v216\nv_exp_f32 v231, v231\nM 16,172,212\n"
"v_exp_f32 v232, v232\nv_exp_f32 v233, v233\nM 8,168,200\nR 164,190,24576\nv_fma_f32 v231, v231, v216, v216\nv_fma_f32 v232, v232, v216, v216\n"
"M 8,172,216\nv_rcp_f32 v230, v230\nv_fma_f32 v233, v233, v216, v216\nM 24,168,204\nR 166,190,28672\nv_mul_f32 v230, v96, v230\n"
"v_rcp_f32 v231, v231\nM 24,172,220\nG 7\nv_rcp_f32 v232, v232\nv_rcp_f32 v233, v233\nM 28,176,204\n"
"R 168,189,24576\nv_mul_f32 v231, v97, v231\nv_mul_f32 v232, v98, v232\nM 28,180,220\nv_mul_f32 v233, v99, v233\nv_cvt_pk_bf16_f32 v96, v230, v231\n"
"v_cvt_pk_bf16_f32 v97, v232, v233\nM 12,176,200\nR 170,189,28672\nv_mul_f32 v234, v100, v100\nv_mul_f32 v235, v101, v101\nM 12,180,216\n"
"v_mul_f32 v236, v102, v102\nv_mul_f32 v237, v103, v103\nv_fma_f32 v234, v234, v215, v214\nM 20,176,196\nR 172,191,24576\nG 7\n"
"v_mfma_f32_16x16x32_bf16 v[0:3], v[238:241], v[208:211], v[0:3]\nv_fma_f32 v235, v235, v215, v214\nv_fma_f32 v236, v236, v215, v214\nM 20,180,212\nv_fma_f32 v237, v237, v215, v214\nv_mul_f32 v234, v100, v234\n"
"v_mul_f32 v235, v101, v235\nM 4,176,192\nR 174,191,28672\nG 7\nv_mfma_f32_16x16x32_bf16 v[4:7], v[240:243], v[208:211], v[4:7]\nv_mul_f32 v236, v102, v236\n"
"v_mul_f32 v237, v103, v237\nM 4,180,208\nG 4\nv_exp_f32 v234, v234\nv_exp_f32 v235, v235\nM 0,160,224\n"
"R 176,188,32768\nv_mfma_f32_16x16x32_bf16 v[16:19], v[240:243], v[210:213], v[16:19]\nv_fma_f32 v234, v234, v216, v216\nv_exp_f32 v236, v236\nM 0,164,240\nv_exp_f32 v237, v237\n"
"v_fma_f32 v235, v235, v216, v216\nM 16,160,228\nR 178,188,36864\nv_mfma_f32_16x16x32_bf16 v[20:23], v[238:241], v[210:213], v[20:23]\nv_fma_f32 v236, v236, v216, v216\nv_fma_f32 v237, v237, v216, v216\n"
"M 16,164,244\nv_rcp_f32 v234, v234\nv_rcp_f32 v235, v235\nM 8,160,232\nR 180,188,40960\nv_mul_f32 v234, v100, v234\n"
"v_rcp_f32 v236, v236\nM 8,164,248\nv_rcp_f32 v237, v237\nv_mul_f32 v235, v101, v235\nM 24,160,236\nR 182,188,45056\n"
"v_mul_f32 v236, v102, v236\nv_mul_f32 v237, v103, v237\nM 24,164,252\nG 4\nv_cvt_pk_bf16_f32 v98, v234, v235\nv_cvt_pk_bf16_f32 v99, v236, v237\n"
"v_mul_f32 v230, v104, v104\nM 28,168,236\nR 160,189,32768\nv_mul_f32 v231, v105, v105\nv_mul_f32 v232, v106, v106\nM 28,172,252\n"
"v_mul_f32 v233, v107, v107\nv_fma_f32 v230, v230, v215, v214\nv_fma_f32 v231, v231, v215, v214\nM 12,168,232\nR 162,189,36864\nv_fma_f32 v232, v232, v215, v214\n"
"v_fma_f32 v233, v233, v215, v214\nM 12,172,248\nv_mul_f32 v230, v104, v230\nv_mul_f32 v231, v105, v231\nv_mul_f32 v232, v106, v232\nM 20,168,228\n"
"R 164,189,40960\nv_mul_f32 v233, v107, v233\nv_exp_f32 v230, v230\nM 20,172,244\nv_exp_f32 v231, v231\nv_exp_f32 v232, v232\n"
"M 4,168,224\nR 166,189,45056\nv_fma_f32 v230, v230, v216, v216\nv_exp_f32 v233, v233\nM 4,172,240\ns_waitcnt vmcnt(11)\n"
"s_barrier\nG 4\nv_fma_f32 v231, v231, v216, v216\nv_mfma_f32_16x16x32_bf16 v[32:35], v[176:179], a[0:3], 0\nR 168,190,32768\nv_fma_f32 v232, v232, v216, v216\n"
"v_fma_f32 v233, v233, v216, v216\nM 32,180,16\ns_mov_b32 m0, s51\nv_rcp_f32 v230, v230\nv_rcp_f32 v231, v231\nv_mfma_f32_16x16x32_bf16 v[48:51], v[176:179], a[4:7], 0\n"
"R 170,190,36864\nds_write_addtid_b32 v8 offset:0\nv_mul_f32 v230, v104, v230\nv_rcp_f32 v232, v232\nM 48,180,20\nds_write_addtid_b32 v9 offset:256\n"
"v_rcp_f32 v233, v233\nv_mul_f32 v231, v105, v231\nv_mfma_f32_16x16x32_bf16 v[64:67], v[176:179], a[8:11], 0\nR 172,190,40960\nds_write_addtid_b32 v10 offset:512\nv_mov_b32 v226, s46\n"
"Lpg11:\ns_cbranch_execz Lpg15\nv_mul_f32 v232, v106, v232\nM 64,180,24\nds_write_addtid_b32 v11 offset:768\nv_mov_b32 v227, s47\n"
"v_mul_f32 v233, v107, v233\nv_cvt_pk_bf16_f32 v104, v230, v231\nv_mfma_f32_16x16x32_bf16 v[80:83], v[176:179], a[12:15], 0\nR 174,190,45056\ns_add_u32 s61, s81, 385024\ns_lshl_b32 s62, s44, 5\n"
"s_add_u32 s61, s61, s62\ns_waitcnt vmcnt(0)\ns_mov_b64 exec, 1\nbuffer_atomic_swap_x2 v[226:227], off, s[20:23], s61 sc1\ns_mov_b64 exec, -1\nds_write_addtid_b32 v12 offset:1024\n"
"v_cvt_pk_bf16_f32 v105, v232, v233\nM 80,180,28\nds_write_addtid_b32 v13 offset:1280\nG 10\nv_mul_f32 v234, v108, v108\nv_mul_f32 v235, v109, v109\n"
"v_mul_f32 v236, v110, v110\nv_mfma_f32_16x16x32_bf16 v[84:87], v[160:163], a[12:15], 0\nR 176,191,32768\nds_write_addtid_b32 v14 offset:1536\nv_mul_f32 v237, v111, v111\nv_fma_f32 v234, v234, v215, v214\n"
"M 84,164,28\nds_write_addtid_b32 v15 offset:1792\nv_fma_f32 v235, v235, v215, v214\nv_fma_f32 v236, v236, v215, v214\nv_fma_f32 v237, v237, v215, v214\nv_mfma_f32_16x16x32_bf16 v[68:71], v[160:163], a[8:11], 0\n"
"R 178,191,36864\nds_write_addtid_b32 v24 offset:4096\nv_mul_f32 v234, v108, v234\nv_mul_f32 v235, v109, v235\nM 68,164,24\nbuffer_store_dwordx4 v[96:99], v252, s[20:23], s84 offen offset:0 sc1\n"
"ds_write_addtid_b32 v25 offset:4352\nv_mul_f32 v236, v110, v236\nv_mfma_f32_16x16x32_bf16 v[52:55], v[160:163], a[4:7], 0\nR 180,191,40960\nds_write_addtid_b32 v26 offset:4608\nv_mul_f32 v237, v111, v237\n"
"v_exp_f32 v234, v234\nM 52,164,20\nds_write_addtid_b32 v27 offset:4864\nds_write_addtid_b32 v28 offset:5120\nds_write_addtid_b32 v29 offset:5376\nds_write_addtid_b32 v30 offset:5632\n"
"ds_write_addtid_b32 v31 offset:5888\nv_exp_f32 v235, v235\nv_exp_f32 v236, v236\nv_mfma_f32_16x16x32_bf16 v[36:39], v[160:163], a[0:3], 0\nR 182,191,45056\ns_add_u32 m0, s50, 32768\n"
"v_exp_f32 v237, v237\nM 36,164,16\nG 15\nv_fma_f32 v234, v234, v216, v216\nv_fma_f32 v235, v235, v216, v216\nv_fma_f32 v236, v236, v216, v216\n"
"v_mfma_f32_16x16x32_bf16 v[40:43], v[168:171], a[0:3], 0\nR 160,188,49152\nG 2\ns_barrier\nv_fma_f32 v237, v237, v216, v216\nM 40,172,16\n"
"buffer_load_dwordx4 v221, s[12:15], s63 offen offset:1024 lds\nds_read2st64_b32 v[8:9], v225 offset0:0 offset1:1\nv_rcp_f32 v234, v234\nv_mfma_f32_16x16x32_bf16 v[56:59], v[168:171], a[4:7], 0\nR 162,188,53248\ns_add_u32 m0, s50, 40960\n"
"s_add_u32 s63, s54, 1835264\nds_read2st64_b32 v[10:11], v225 offset0:2 offset1:3\nv_rcp_f32 v235, v235\nM 56,172,20\nbuffer_load_dwordx4 v220, s[12:15], s63 offen lds\nds_read2st64_b32 v[12:13], v225 offset0:4 offset1:5\n"
"v_rcp_f32 v236, v236\nv_mfma_f32_16x16x32_bf16 v[72:75], v[168:171], a[8:11], 0\nR 164,184,0\nds_read2st64_b32 v[14:15], v225 offset0:6 offset1:7\nv_mul_f32 v234, v108, v234\nv_rcp_f32 v237, v237\n"
"M 72,172,24\nds_read2st64_b32 v[24:25], v225 offset0:16 offset1:17\nv_mul_f32 v235, v109, v235\nv_mul_f32 v236, v110, v236\nv_mul_f32 v237, v111, v237\nv_mfma_f32_16x16x32_bf16 v[88:91], v[168:171], a[12:15], 0\n"
"R 166,184,4096\nds_read2st64_b32 v[26:27], v225 offset0:18 offset1:19\nv_cvt_pk_bf16_f32 v106, v234, v235\nv_cvt_pk_bf16_f32 v107, v236, v237\nM 88,172,28\nds_read2st64_b32 v[28:29], v225 offset0:20 offset1:21\n"
"G 11\nv_mul_f32 v230, v112, v112\nv_mul_f32 v231, v113, v113\nv_mul_f32 v232, v114, v114\nv_mfma_f32_16x16x32_bf16 v[92:95], v[176:179], a[12:15], 0\nR 168,189,49152\n"
"ds_read2st64_b32 v[30:31], v225 offset0:22 offset1:23\nv_mul_f32 v233, v115, v115\nv_fma_f32 v230, v230, v218, v217\nM 92,180,28\nbuffer_load_dwordx4 v221, s[12:15], s63 offen offset:1024 lds\nG 11\n"
"v_add_f32 v0, v0, v8\nv_fma_f32 v231, v231, v218, v217\nv_mfma_f32_16x16x32_bf16 v[76:79], v[176:179], a[8:11], 0\nR 170,189,53248\ns_add_u32 m0, s50, 49152\ns_add_u32 s63, s54, 2064640\n"
"v_add_f32 v1, v1, v9\nv_fma_f32 v232, v232, v218, v217\nM 76,180,24\nbuffer_load_dwordx4 v220, s[12:15], s63 offen lds\nG 10\nv_add_f32 v2, v2, v10\n"
"v_fma_f32 v233, v233, v218, v217\nv_mfma_f32_16x16x32_bf16 v[60:63], v[176:179], a[4:7], 0\nR 172,185,0\nv_mul_f32 v230, v112, v230\nv_add_f32 v3, v3, v11\nv_mul_f32 v231, v113, v231\n"
"M 60,180,20\nbuffer_store_dwordx4 v[104:107], v252, s[20:23], s84 offen offset:1024 sc1\nG 10\nv_add_f32 v4, v4, v12\nv_mul_f32 v232, v114, v232\nv_mfma_f32_16x16x32_bf16 v[44:47], v[176:179], a[0:3], 0\n"
"R 174,185,4096\nv_mul_f32 v233, v115, v233\nv_add_f32 v5, v5, v13\nv_exp_f32 v230, v230\nM 44,180,16\nG 7\n"
"v_exp_f32 v231, v231\nv_add_f32 v6, v6, v14\nv_exp_f32 v232, v232\nM 32,160,32\nR 176,190,49152\nv_mov_b32 v248, 0\n"
"v_add_f32 v7, v7, v15\nv_exp_f32 v233, v233\nM 32,164,48\nbuffer_load_dwordx4 v221, s[12:15], s63 offen offset:1024 lds\nv_mov_b32 v249, 0\nv_add_f32 v16, v16, v24\n"
"v_fma_f32 v230, v230, v219, v219\nM 48,160,36\nR 178,190,53248\ns_add_u32 s61, s82, 385024\ns_mov_b64 exec, 0xf\nbuffer_atomic_or_x2 v[248:249], v250, s[20:23], s61 offen sc0 sc1\n"
"s_mov_b64 exec, -1\nv_add_f32 v17, v17, v25\nv_fma_f32 v231, v231, v219, v219\nM 48,164,52\nv_rcp_f32 v230, v230\nG 8\n"
"v_add_f32 v18, v18, v26\nv_fma_f32 v232, v232, v219, v219\nM 64,160,40\nR 180,186,0\nv_fma_f32 v233, v233, v219, v219\nv_add_f32 v19, v19, v27\n"
"G 8\nv_add_f32 v20, v20, v28\nv_add_f32 v21, v21, v29\nG 6\nv_add_f32 v22, v22, v30\nv_add_f32 v23, v23, v31\n"
"v_rcp_f32 v231, v231\nM 64,164,56\ns_add_u32 m0, s50, 57344\nv_rcp_f32 v232, v232\nv_rcp_f32 v233, v233\nM 80,160,44\n"
"R 182,186,4096\nv_mul_f32 v230, v112, v230\nv_mul_f32 v231, v113, v231\nM 80,164,60\nG 4\nv_mul_f32 v232, v114, v232\n"
"v_mul_f32 v233, v115, v233\nv_cvt_pk_bf16_f32 v112, v230, v231\nM 84,168,44\nR 160,191,49152\nv_cvt_pk_bf16_f32 v113, v232, v233\nM 84,172,60\n"
"s_add_u32 s63, s54, 2294016\nv_mul_f32 v234, v116, v116\nv_mul_f32 v235, v117, v117\nM 68,168,40\nR 162,191,53248\nv_mul_f32 v236, v118, v118\n"
"M 68,172,56\nbuffer_load_dwordx4 v220, s[12:15], s63 offen lds\nM 52,168,36\nR 164,187,0\nv_mul_f32 v237, v119, v119\nM 52,172,52\n"
"v_fma_f32 v234, v234, v218, v217\nv_fma_f32 v235, v235, v218, v217\nM 36,168,32\nR 166,187,4096\nv_fma_f32 v236, v236, v218, v217\nM 36,172,48\n"
"G 4\nv_fma_f32 v237, v237, v218, v217\nv_mul_f32 v234, v116, v234\nM 40,176,32\nR 168,184,8192\nv_mul_f32 v235, v117, v235\n"
"M 40,180,48\nbuffer_load_dwordx4 v221, s[12:15], s63 offen offset:1024 lds\nM 56,176,36\nR 170,184,12288\ns_add_u32 s63, s54, 2523392\nM 56,180,52\n"
"s_add_u32 m0, s50, 65536\nv_exp_f32 v234, v234\nM 72,176,40\nR 172,184,16384\nv_mul_f32 v236, v118, v236\nM 72,180,56\n"
"buffer_load_dwordx4 v220, s[12:15], s63 offen lds\nM 88,176,44\nR 174,184,20480\nv_mul_f32 v237, v119, v237\nM 88,180,60\nG 4\n"
"v_exp_f32 v235, v235\nM 92,160,44\nR 176,185,8192\nv_fma_f32 v234, v234, v219, v219\nM 92,164,60\nbuffer_load_dwordx4 v221, s[12:15], s63 offen offset:1024 lds\n"
"M 76,160,40\nR 178,185,12288\ns_add_u32 s63, s54, 2752768\nM 76,164,56\ns_add_u32 m0, s50, 73728\nv_exp_f32 v236, v236\n"
"M 60,160,36\nR 180,185,16384\nv_fma_f32 v235, v235, v219, v219\nM 60,164,52\nbuffer_load_dwordx4 v220, s[12:15], s63 offen lds\nM 44,160,32\n"
"R 182,185,20480\nv_fma_f32 v236, v236, v219, v219\nM 44,164,48\nG 4\nv_exp_f32 v237, v237\nM 32,168,64\n"
"R 160,186,8192\nv_fma_f32 v237, v237, v219, v219\nM 32,172,80\nbuffer_load_dwordx4 v221, s[12:15], s63 offen offset:1024 lds\nM 48,168,68\nR 162,186,12288\n"
"v_mul_f32 v230, v120, v120\nM 48,172,84\ns_add_u32 m0, s50, 81920\nv_rcp_f32 v234, v234\nM 64,168,72\nR 164,186,16384\n"
"v_mul_f32 v234, v116, v234\nM 64,172,88\ns_add_u32 s63, s54, 2982144\nv_rcp_f32 v235, v235\nM 80,168,76\nR 166,186,20480\n"
"v_mul_f32 v235, v117, v235\nM 80,172,92\nG 4\nv_rcp_f32 v236, v236\nM 84,176,76\nR 168,187,8192\n"
"v_mul_f32 v236, v118, v236\nM 84,180,92\nbuffer_load_dwordx4 v220, s[12:15], s63 offen lds\nM 68,176,72\nR 170,187,12288\nv_cvt_pk_bf16_f32 v114, v234, v235\n"
"M 68,180,88\nv_rcp_f32 v237, v237\nM 52,176,68\nR 172,187,16384\nv_mul_f32 v237, v119, v237\nM 52,180,84\n"
"s_add_u32 s64, s68, 23068672\nv_cvt_pk_bf16_f32 v115, v236, v237\nv_mul_f32 v231, v121, v121\nM 36,176,64\nR 174,187,20480\nv_mul_f32 v232, v122, v122\n"
"M 36,180,80\nG 4\nv_mul_f32 v233, v123, v123\nv_fma_f32 v230, v230, v218, v217\nM 40,160,64\nR 176,184,24576\n"
"s_waitcnt vmcnt(8)\ns_barrier\ns_waitcnt vmcnt(7)\nv_cmp_eq_u64 s[86:87], s[46:47], v[248:249]\nM 40,164,80\ns_add_u32 s65, s68, 23183360\n"
"v_fma_f32 v231, v231, v218, v217\nv_fma_f32 v232, v232, v218, v217\nM 56,160,68\nR 178,184,28672\ns_and_b32 s62, s86, 0xf\ns_cmp_eq_u32 s62, 0xf\n"
"s_cbranch_scc0 Lq373\nLq374:\nM 56,164,84\nbuffer_load_dwordx4 v221, s[12:15], s63 offen offset:1024 lds\nM 72,160,72\nR 180,184,32768\n"
"v_fma_f32 v233, v233, v218, v217\nM 72,164,88\nbuffer_load_dwordx4 a[0:3], v252, s[20:23], s64 offen sc1\nM 88,160,76\nR 182,184,36864\nv_mul_f32 v230, v120, v230\n"
"M 88,164,92\nG 4\nv_exp_f32 v230, v230\nM 92,168,76\nR 160,185,24576\nv_mul_f32 v231, v121, v231\n"
"M 92,172,92\nbuffer_load_dwordx4 a[4:7], v252, s[20:23], s65 offen sc1\nM 76,168,72\nR 162,185,28672\nv_mul_f32 v232, v122, v232\nM 76,172,88\n"
"s_add_u32 s66, s69, 23068672\nv_exp_f32 v231, v231\nM 60,168,68\nR 164,185,32768\nv_mul_f32 v233, v123, v233\nM 60,172,84\n"
"s_add_u32 s67, s69, 23183360\nv_exp_f32 v232, v232\nM 44,168,64\nR 166,185,36864\nv_fma_f32 v230, v230, v219, v219\nM 44,172,80\n"
"G 4\nv_exp_f32 v233, v233\nM 32,176,96\nR 168,186,24576\nv_fma_f32 v231, v231, v219, v219\nM 32,180,112\n"
"buffer_load_dwordx4 a[8:11], v252, s[20:23], s66 offen sc1\nM 48,176,100\nR 170,186,28672\nv_fma_f32 v232, v232, v219, v219\nM 48,180,116\nbuffer_load_dwordx4 a[12:15], v252, s[20:23], s67 offen sc1\n"
"M 64,176,104\nR 172,186,32768\nv_fma_f32 v233, v233, v219, v219\nM 64,180,120\ns_add_u32 s64, s68, 23075840\nv_rcp_f32 v230, v230\n"
"M 80,176,108\nR 174,186,36864\nv_mul_f32 v230, v120, v230\nM 80,180,124\nG 4\nv_rcp_f32 v231, v231\n"
"M 84,160,108\nR 176,187,24576\nv_mul_f32 v231, v121, v231\nM 84,164,124\nbuffer_load_dwordx4 a[16:19], v252, s[20:23], s64 offen sc1\nM 68,160,104\n"
"R 178,187,28672\nv_cvt_pk_bf16_f32 v120, v230, v231\nM 68,164,120\ns_add_u32 s65, s68, 23190528\nv_rcp_f32 v232, v232\nM 52,160,100\n"
"R 180,187,32768\nv_mul_f32 v232, v122, v232\nM 52,164,116\ns_add_u32 s66, s69, 23075840\nv_rcp_f32 v233, v233\nM 36,160,96\n"
"R 182,187,36864\nv_mul_f32 v233, v123, v233\nM 36,164,112\nG 4\nv_cvt_pk_bf16_f32 v121, v232, v233\nv_mul_f32 v234, v124, v124\n"
"M 40,168,96\nR 160,184,40960\nv_mul_f32 v235, v125, v125\nM 40,172,112\nbuffer_load_dwordx4 a[20:23], v252, s[20:23], s65 offen sc1\nM 56,168,100\n"
"R 162,184,45056\nv_mul_f32 v236, v126, v126\nM 56,172,116\nbuffer_load_dwordx4 a[24:27], v252, s[20:23], s66 offen sc1\nM 72,168,104\nR 164,184,49152\n"
"v_mul_f32 v237, v127, v127\nM 72,172,120\ns_add_u32 s67, s69, 23190528\nv_fma_f32 v234, v234, v218, v217\nv_fma_f32 v235, v235, v218, v217\nM 88,168,108\n"
"R 166,184,53248\nv_fma_f32 v236, v236, v218, v217\nM 88,172,124\nG 4\nv_fma_f32 v237, v237, v218, v217\nv_mul_f32 v234, v124, v234\n"
"M 92,176,108\nR 168,185,40960\ns_add_u32 m0, s50, 90112\nM 92,180,124\nbuffer_load_dwordx4 a[28:31], v252, s[20:23], s67 offen sc1\nM 76,176,104\n"
"R 170,185,45056\ns_add_u32 s63, s54, 3211520\nM 76,180,120\ns_add_u32 s64, s68, 23069696\nv_exp_f32 v234, v234\nM 60,176,100\n"
"R 172,185,49152\nv_mul_f32 v235, v125, v235\nM 60,180,116\ns_add_u32 s65, s68, 23184384\nbuffer_load_dwordx4 v220, s[12:15], s63 offen lds\nM 44,176,96\n"
"R 174,185,53248\nv_mul_f32 v236, v126, v236\nM 44,180,112\nG 4\nv_exp_f32 v235, v235\nM 32,160,128\n"
"R 176,186,40960\nv_mul_f32 v237, v127, v237\nM 32,164,144\nbuffer_load_dwordx4 a[32:35], v252, s[20:23], s64 offen sc1\nbuffer_load_dwordx4 v221, s[12:15], s63 offen offset:1024 lds\nM 48,160,132\n"
"R 178,186,45056\ns_add_u32 m0, s50, 98304\nM 48,164,148\nbuffer_load_dwordx4 a[36:39], v252, s[20:23], s65 offen sc1\nM 64,160,136\nR 180,186,49152\n"
"s_add_u32 s63, s54, 3440896\nM 64,164,152\ns_add_u32 s66, s69, 23069696\nbuffer_load_dwordx4 v220, s[12:15], s63 offen lds\nM 80,160,140\nR 182,186,53248\n"
"v_fma_f32 v234, v234, v219, v219\nM 80,164,156\nG 4\nv_exp_f32 v236, v236\nM 84,168,140\nR 160,187,40960\n"
"v_fma_f32 v235, v235, v219, v219\nM 84,172,156\nbuffer_load_dwordx4 a[40:43], v252, s[20:23], s66 offen sc1\nbuffer_load_dwordx4 v221, s[12:15], s63 offen offset:1024 lds\nM 68,168,136\nR 162,187,45056\n"
"v_fma_f32 v236, v236, v219, v219\nM 68,172,152\ns_add_u32 s67, s69, 23184384\nv_exp_f32 v237, v237\nM 52,168,132\nR 164,187,49152\n"
"s_waitcnt vmcnt(15)\ns_barrier\nM 52,172,148\nbuffer_load_dwordx4 a[44:47], v252, s[20:23], s67 offen sc1\ns_movk_i32 s63, 2048\nv_mad_u32_u24 v220, v228, s63, v229\n"
"v_add_u32 v221, 916480, v220\ns_add_u32 m0, s50, 106496\ns_add_u32 s63, s56, 0\nbuffer_load_dwordx4 v220, s[16:19], s63 offen lds\nM 36,168,128\nR 166,187,53248\n"
"v_fma_f32 v237, v237, v219, v219\nM 36,172,144\nG 4\nv_rcp_f32 v234, v234\nM 40,176,128\nR 168,184,57344\n"
"v_mul_f32 v234, v124, v234\nM 40,180,144\ns_add_u32 s64, s68, 23076864\nbuffer_load_dwordx4 v221, s[16:19], s63 offen offset:1024 lds\nM 56,176,132\nR 170,184,61440\n"
"s_add_u32 m0, s50, 114688\nM 56,180,148\nbuffer_load_dwordx4 a[48:51], v252, s[20:23], s64 offen sc1\nM 72,176,136\nR 172,188,0\ns_add_u32 s63, s56, 524288\n"
"M 72,180,152\ns_add_u32 s65, s68, 23191552\nbuffer_load_dwordx4 v220, s[16:19], s63 offen lds\nM 88,176,140\nR 174,188,4096\nv_rcp_f32 v235, v235\n"
"M 88,180,156\nG 4\nv_rcp_f32 v236, v236\nM 92,160,140\nR 176,185,57344\nv_mul_f32 v235, v125, v235\n"
"M 92,164,156\nbuffer_load_dwordx4 a[52:55], v252, s[20:23], s65 offen sc1\nbuffer_load_dwordx4 v221, s[16:19], s63 offen offset:1024 lds\nM 76,160,136\nR 178,185,61440\ns_add_u32 m0, s50, 0\n"
"M 76,164,152\ns_add_u32 s66, s69, 23076864\nv_rcp_f32 v237, v237\nM 60,160,132\nR 180,189,0\ns_add_u32 s63, s56, 65536\n"
"M 60,164,148\nbuffer_load_dwordx4 a[56:59], v252, s[20:23], s66 offen sc1\nM 44,160,128\nR 182,189,4096\nv_mul_f32 v236, v126, v236\nM 44,164,144\n"
"buffer_load_dwordx4 v220, s[16:19], s63 offen lds\nG 4\nM 32,168,160\nR 160,186,57344\nv_mul_f32 v237, v127, v237\nM 32,172,176\n"
"s_add_u32 s67, s69, 23191552\nv_cvt_pk_bf16_f32 v122, v234, v235\nv_cvt_pk_bf16_f32 v123, v236, v237\nM 48,168,164\nR 162,186,61440\nM 48,172,180\n"
"buffer_load_dwordx4 a[60:63], v252, s[20:23], s67 offen sc1\nbuffer_load_dwordx4 v221, s[16:19], s63 offen offset:1024 lds\nM 64,168,168\nR 164,190,0\ns_add_u32 m0, s50, 8192\ns_add_u32 s63, s56, 589824\n"
"M 64,172,184\nbuffer_load_dwordx4 v220, s[16:19], s63 offen lds\nM 80,168,172\nR 166,190,4096\nbuffer_store_dwordx4 v[112:115], v252, s[20:23], s85 offen offset:0 sc1\nM 80,172,188\n"
"buffer_store_dwordx4 v[120:123], v252, s[20:23], s85 offen offset:1024 sc1\nG 4\nM 84,176,172\nR 168,187,57344\nM 84,180,188\nbuffer_load_dwordx4 v221, s[16:19], s63 offen offset:1024 lds\n"
"M 68,176,168\nR 170,187,61440\ns_add_u32 s63, s57, 0\nv_mul_f32 v230, v0, v0\nM 68,180,184\ns_add_u32 m0, s50, 16384\n"
"s_add_u32 s84, s73, 23074816\ns_add_u32 s85, s73, 23189504\nv_mul_f32 v231, v1, v1\nM 52,176,164\nR 172,191,0\nv_mul_f32 v232, v2, v2\n"
"v_mul_f32 v233, v3, v3\nM 52,180,180\nbuffer_load_dwordx4 v220, s[16:19], s63 offen lds\nv_fma_f32 v230, v230, v215, v214\nM 36,176,160\nR 174,191,4096\n"
"v_fma_f32 v231, v231, v215, v214\nLpg12:\ns_cbranch_execz LretO0n\nv_fma_f32 v232, v232, v215, v214\nM 36,180,176\nG 4\n"
"v_fma_f32 v233, v233, v215, v214\nv_mul_f32 v230, v0, v230\nv_mul_f32 v231, v1, v231\nM 40,160,160\nR 176,188,8192\nv_mul_f32 v232, v2, v232\n"
"v_mul_f32 v233, v3, v233\nM 40,164,176\nv_exp_f32 v230, v230\nv_exp_f32 v231, v231\nM 56,160,164\nR 178,188,12288\n"
"v_fma_f32 v230, v230, v216, v216\nv_exp_f32 v232, v232\nM 56,164,180\nv_exp_f32 v233, v233\nv_fma_f32 v231, v231, v216, v216\nM 72,160,168\n"
"R 180,188,16384\nv_fma_f32 v232, v232, v216, v216\nv_fma_f32 v233, v233, v216, v216\nM 72,164,184\nv_rcp_f32 v230, v230\nv_rcp_f32 v231, v231\n"
"M 88,160,172\nR 182,188,20480\nv_mul_f32 v230, v0, v230\nv_rcp_f32 v232, v232\nM 88,164,188\nG 4\n"
"v_rcp_f32 v233, v233\nv_mul_f32 v231, v1, v231\nM 92,168,172\nR 160,189,8192\nv_mul_f32 v232, v2, v232\nv_mul_f32 v233, v3, v233\n"
"M 92,172,188\nv_cvt_pk_bf16_f32 v0, v230, v231\nv_cvt_pk_bf16_f32 v1, v232, v233\nv_mul_f32 v234, v4, v4\nM 76,168,168\nR 162,189,12288\n"
"v_mul_f32 v235, v5, v5\nv_mul_f32 v236, v6, v6\nM 76,172,184\nv_mul_f32 v237, v7, v7\nv_fma_f32 v234, v234, v215, v214\nv_fma_f32 v235, v235, v215, v214\n"
"M 60,168,164\nR 164,189,16384\nv_fma_f32 v236, v236, v215, v214\nv_fma_f32 v237, v237, v215, v214\nM 60,172,180\nv_mul_f32 v234, v4, v234\n"
"v_mul_f32 v235, v5, v235\nv_mul_f32 v236, v6, v236\nM 44,168,160\nR 166,189,20480\nv_mul_f32 v237, v7, v237\nv_exp_f32 v234, v234\n"
"M 44,172,176\nG 4\nv_exp_f32 v235, v235\nv_exp_f32 v236, v236\nM 32,176,192\nR 168,190,8192\n"
"v_fma_f32 v234, v234, v216, v216\nv_exp_f32 v237, v237\nM 32,180,208\nv_rcp_f32 v234, v234\nv_fma_f32 v235, v235, v216, v216\nM 48,176,196\n"
"R 170,190,12288\nv_fma_f32 v236, v236, v216, v216\nv_fma_f32 v237, v237, v216, v216\nM 48,180,212\nv_rcp_f32 v235, v235\nv_rcp_f32 v236, v236\n"
"M 64,176,200\nR 172,190,16384\nv_mul_f32 v234, v4, v234\nv_rcp_f32 v237, v237\nM 64,180,216\nv_mul_f32 v235, v5, v235\n"
"v_mul_f32 v236, v6, v236\nv_mul_f32 v237, v7, v237\nM 80,176,204\nR 174,190,20480\nv_cvt_pk_bf16_f32 v2, v234, v235\nv_cvt_pk_bf16_f32 v3, v236, v237\n"
"M 80,180,220\nG 4\nv_mul_f32 v230, v16, v16\nv_mul_f32 v231, v17, v17\nv_mul_f32 v232, v18, v18\nM 84,160,204\n"
"R 176,191,8192\ns_waitcnt vmcnt(9)\ns_barrier\nv_mul_f32 v233, v19, v19\nM 84,164,220\nv_fma_f32 v230, v230, v218, v217\n"
"v_fma_f32 v231, v231, v218, v217\nv_fma_f32 v232, v232, v218, v217\nM 68,160,200\nR 178,191,12288\nv_fma_f32 v233, v233, v218, v217\nv_mul_f32 v230, v16, v230\n"
"M 68,164,216\nv_exp_f32 v230, v230\nv_mul_f32 v231, v17, v231\nM 52,160,196\nR 180,191,16384\nv_mul_f32 v232, v18, v232\n"
"v_mul_f32 v233, v19, v233\nM 52,164,212\nv_exp_f32 v231, v231\nv_exp_f32 v232, v232\nM 36,160,192\nR 182,191,20480\n"
"v_fma_f32 v230, v230, v219, v219\nv_exp_f32 v233, v233\nM 36,164,208\nG 4\nv_rcp_f32 v230, v230\nv_fma_f32 v231, v231, v219, v219\n"
"M 40,168,192\nR 160,188,24576\nv_fma_f32 v232, v232, v219, v219\nv_fma_f32 v233, v233, v219, v219\nM 40,172,208\nv_rcp_f32 v231, v231\n"
"v_rcp_f32 v232, v232\nM 56,168,196\nR 162,188,28672\nv_mul_f32 v230, v16, v230\nv_rcp_f32 v233, v233\nM 56,172,212\n"
"v_mul_f32 v231, v17, v231\nv_mul_f32 v232, v18, v232\nv_mul_f32 v233, v19, v233\nM 72,168,200\nR 164,188,32768\nv_cvt_pk_bf16_f32 v16, v230, v231\n"
"v_cvt_pk_bf16_f32 v17, v232, v233\nM 72,172,216\nv_mul_f32 v234, v20, v20\nv_mul_f32 v235, v21, v21\nv_mul_f32 v236, v22, v22\nM 88,168,204\n"
"R 166,188,36864\nv_mov_b32 v226, s46\nv_mul_f32 v237, v23, v23\nM 88,172,220\nG 4\nv_mov_b32 v227, s47\n"
"v_fma_f32 v234, v234, v218, v217\nv_fma_f32 v235, v235, v218, v217\nM 92,176,204\nR 168,189,24576\ns_add_u32 s61, s81, 385032\ns_lshl_b32 s62, s44, 5\n"
"s_add_u32 s61, s61, s62\ns_waitcnt vmcnt(0)\ns_mov_b64 exec, 1\nbuffer_atomic_swap_x2 v[226:227], off, s[20:23], s61 sc1\ns_mov_b64 exec, -1\nv_fma_f32 v236, v236, v218, v217\n"
"M 92,180,220\nbuffer_load_dwordx4 v221, s[16:19], s63 offen offset:1024 lds\nv_fma_f32 v237, v237, v218, v217\nM 76,176,200\nR 170,189,28672\ns_add_u32 m0, s50, 24576\n"
"s_add_u32 s63, s57, 524288\nv_mul_f32 v234, v20, v234\nM 76,180,216\nbuffer_load_dwordx4 v220, s[16:19], s63 offen lds\nv_mul_f32 v235, v21, v235\nM 60,176,196\n"
"R 172,189,32768\nv_mul_f32 v236, v22, v236\nv_mul_f32 v237, v23, v237\nM 60,180,212\nv_exp_f32 v234, v234\nv_exp_f32 v235, v235\n"
"M 44,176,192\nR 174,189,36864\ns_mov_b32 s60, 158208\nv_bfe_u32 v246, v252, 4, 4\nv_lshl_add_u32 v246, v246, 3, s60\nv_exp_f32 v236, v236\n"
"M 44,180,208\nds_read_b64 v[238:239], v246 offset:0\nG 5\nv_exp_f32 v237, v237\nM 32,160,224\nR 176,190,24576\n"
"ds_read_b64 v[240:241], v246 offset:128\nv_fma_f32 v234, v234, v219, v219\nM 32,164,240\nds_read_b64 v[242:243], v246 offset:256\nbuffer_load_dwordx4 v221, s[16:19], s63 offen offset:1024 lds\nv_fma_f32 v235, v235, v219, v219\n"
"M 48,160,228\nR 178,190,28672\nds_read_b64 v[244:245], v246 offset:384\nv_fma_f32 v236, v236, v219, v219\nM 48,164,244\nds_read_b64 v[246:247], v246 offset:0\n"
"s_add_u32 m0, s50, 32768\nv_fma_f32 v237, v237, v219, v219\nM 64,160,232\nR 180,190,32768\ns_add_u32 s63, s57, 65536\nv_rcp_f32 v234, v234\n"
"M 64,164,248\nbuffer_load_dwordx4 v220, s[16:19], s63 offen lds\nv_rcp_f32 v235, v235\nM 80,160,236\nR 182,190,36864\nv_mul_f32 v234, v20, v234\n"
"v_rcp_f32 v236, v236\nM 80,164,252\nG 9\nv_rcp_f32 v237, v237\nv_mul_f32 v235, v21, v235\nM 84,168,236\n"
"R 160,191,24576\nv_mul_f32 v236, v22, v236\nM 84,172,252\nbuffer_load_dwordx4 v221, s[16:19], s63 offen offset:1024 lds\nM 68,168,232\nR 162,191,28672\n"
"s_add_u32 m0, s50, 40960\ns_add_u32 s63, s57, 589824\nM 68,172,248\nbuffer_load_dwordx4 v220, s[16:19], s63 offen lds\nM 52,168,228\nR 164,191,32768\n"
"G 9\nv_mfma_f32_16x16x32_bf16 v[32:35], v[238:241], v[208:211], v[32:35]\nv_mul_f32 v237, v23, v237\nM 52,172,244\nbuffer_store_dwordx4 v[0:3], v252, s[20:23], s84 offen offset:0 sc1\nM 36,168,224\n"
"R 166,191,36864\nG 9\nv_mfma_f32_16x16x32_bf16 v[36:39], v[240:243], v[208:211], v[36:39]\nv_cvt_pk_bf16_f32 v18, v234, v235\nM 36,172,240\nG 4\n"
"v_cvt_pk_bf16_f32 v19, v236, v237\nM 40,176,224\nR 168,188,40960\nv_mfma_f32_16x16x32_bf16 v[40:43], v[242:245], v[208:211], v[40:43]\nbuffer_load_dwordx4 v221, s[16:19], s63 offen offset:1024 lds\nbuffer_store_dwordx4 v[16:19], v252, s[20:23], s85 offen offset:0 sc1\n"
"M 40,180,240\ns_add_u32 m0, s50, 49152\ns_add_u32 s63, s56, 131072\nbuffer_load_dwordx4 v220, s[16:19], s63 offen lds\nM 56,176,228\nR 170,188,45056\n"
"v_mfma_f32_16x16x32_bf16 v[44:47], v[244:247], v[208:211], v[44:47]\nv_mov_b32 v248, 0\nM 56,180,244\nv_mov_b32 v249, 0\nM 72,176,232\nR 172,188,49152\n"
"v_mfma_f32_16x16x32_bf16 v[48:51], v[244:247], v[210:213], v[48:51]\ns_add_u32 s61, s82, 385032\ns_mov_b64 exec, 0xf\nbuffer_atomic_or_x2 v[248:249], v250, s[20:23], s61 offen sc0 sc1\ns_mov_b64 exec, -1\nM 72,180,248\n"
"buffer_load_dwordx4 v221, s[16:19], s63 offen offset:1024 lds\nM 88,176,236\nR 174,188,53248\nv_mfma_f32_16x16x32_bf16 v[52:55], v[238:241], v[210:213], v[52:55]\ns_add_u32 m0, s50, 57344\nM 88,180,252\n"
"G 4\nM 92,160,236\nR 176,189,40960\nv_mfma_f32_16x16x32_bf16 v[56:59], v[240:243], v[210:213], v[56:59]\ns_add_u32 s63, s56, 655360\nM 92,164,252\n"
"buffer_load_dwordx4 v220, s[16:19], s63 offen lds\nM 76,160,232\nR 178,189,45056\nv_mfma_f32_16x16x32_bf16 v[60:63], v[242:245], v[210:213], v[60:63]\nM 76,164,248\nM 60,160,228\n"
"R 180,189,49152\nM 60,164,244\nM 44,160,224\nR 182,189,53248\nM 44,164,240\ns_barrier\n"
"G 4\nv_mfma_f32_16x16x32_bf16 v[96:99], v[168:171], a[0:3], 0\nR 160,190,40960\nM 96,172,16\ns_mov_b32 m0, s51\nv_mfma_f32_16x16x32_bf16 v[112:115], v[168:171], a[4:7], 0\n"
"R 162,190,45056\nM 112,172,20\nds_write_addtid_b32 v64 offset:0\nv_mfma_f32_16x16x32_bf16 v[128:131], v[168:171], a[8:11], 0\nR 164,190,49152\nM 128,172,24\n"
"ds_write_addtid_b32 v65 offset:256\nv_mfma_f32_16x16x32_bf16 v[144:147], v[168:171], a[12:15], 0\nR 166,190,53248\nds_write_addtid_b32 v66 offset:512\nM 144,172,28\nds_write_addtid_b32 v67 offset:768\n"
"ds_write_addtid_b32 v68 offset:1024\nG 9\nv_mfma_f32_16x16x32_bf16 v[148:151], v[176:179], a[12:15], 0\nR 168,191,40960\nds_write_addtid_b32 v69 offset:1280\nM 148,180,28\n"
"ds_write_addtid_b32 v70 offset:1536\nds_write_addtid_b32 v71 offset:1792\nv_mfma_f32_16x16x32_bf16 v[132:135], v[176:179], a[8:11], 0\nR 170,191,45056\nds_write_addtid_b32 v72 offset:2048\nM 132,180,24\n"
"ds_write_addtid_b32 v73 offset:2304\nds_write_addtid_b32 v74 offset:2560\nv_mfma_f32_16x16x32_bf16 v[116:119], v[176:179], a[4:7], 0\nR 172,191,49152\nds_write_addtid_b32 v75 offset:2816\nM 116,180,20\n"
"ds_write_addtid_b32 v76 offset:3072\nds_write_addtid_b32 v77 offset:3328\nv_mfma_f32_16x16x32_bf16 v[100:103], v[176:179], a[0:3], 0\nR 174,191,53248\nds_write_addtid_b32 v78 offset:3584\nM 100,180,16\n"
"ds_write_addtid_b32 v79 offset:3840\nds_write_addtid_b32 v80 offset:4096\nG 15\nv_mfma_f32_16x16x32_bf16 v[104:107], v[160:163], a[0:3], 0\nR 176,184,0\nds_write_addtid_b32 v81 offset:4352\n"
"M 104,164,16\nds_write_addtid_b32 v82 offset:4608\nds_write_addtid_b32 v83 offset:4864\nv_mfma_f32_16x16x32_bf16 v[120:123], v[160:163], a[4:7], 0\nR 178,184,4096\nds_write_addtid_b32 v84 offset:5120\n"
"M 120,164,20\nds_write_addtid_b32 v85 offset:5376\nds_write_addtid_b32 v86 offset:5632\nv_mfma_f32_16x16x32_bf16 v[136:139], v[160:163], a[8:11], 0\nR 180,184,8192\nds_write_addtid_b32 v87 offset:5888\n"
"M 136,164,24\nds_write_addtid_b32 v88 offset:6144\nds_write_addtid_b32 v89 offset:6400\nv_mfma_f32_16x16x32_bf16 v[152:155], v[160:163], a[12:15], 0\nR 182,184,12288\nds_write_addtid_b32 v90 offset:6656\n"
"M 152,164,28\nds_write_addtid_b32 v91 offset:6912\nds_write_addtid_b32 v92 offset:7168\nG 15\nv_mfma_f32_16x16x32_bf16 v[156:159], v[168:171], a[12:15], 0\nR 160,185,0\n"
"ds_write_addtid_b32 v93 offset:7424\nM 156,172,28\nds_write_addtid_b32 v94 offset:7680\nds_write_addtid_b32 v95 offset:7936\nv_mfma_f32_16x16x32_bf16 v[140:143], v[168:171], a[8:11], 0\nR 162,185,4096\n"
"M 140,172,24\nv_mfma_f32_16x16x32_bf16 v[124:127], v[168:171], a[4:7], 0\nR 164,185,8192\ns_waitcnt vmcnt(5) lgkmcnt(2)\ns_barrier\nM 124,172,20\n"
"ds_read2st64_b32 v[64:65], v225 offset0:0 offset1:1\nds_read2st64_b32 v[66:67], v225 offset0:2 offset1:3\nv_mfma_f32_16x16x32_bf16 v[108:111], v[168:171], a[0:3], 0\nR 166,185,12288\nds_read2st64_b32 v[68:69], v225 offset0:4 offset1:5\nM 108,172,16\n"
"ds_read2st64_b32 v[70:71], v225 offset0:6 offset1:7\nds_read2st64_b32 v[72:73], v225 offset0:8 offset1:9\nM 96,176,32\nR 168,186,0\nds_read2st64_b32 v[74:75], v225 offset0:10 offset1:11\nM 96,180,48\n"
"s_add_u32 s64, s68, 23070720\nds_read2st64_b32 v[76:77], v225 offset0:12 offset1:13\nv_mov_b32 v226, s46\nM 112,176,36\nR 170,186,4096\nds_read2st64_b32 v[78:79], v225 offset0:14 offset1:15\n"
"v_mov_b32 v227, s47\ns_waitcnt vmcnt(2)\nv_cmp_eq_u64 s[86:87], s[46:47], v[248:249]\nM 112,180,52\ns_add_u32 s61, s81, 385048\ns_lshl_b32 s62, s44, 5\n"
"s_add_u32 s61, s61, s62\ns_waitcnt vmcnt(0)\ns_mov_b64 exec, 1\nbuffer_atomic_swap_x2 v[226:227], off, s[20:23], s61 sc1\ns_mov_b64 exec, -1\nds_read2st64_b32 v[80:81], v225 offset0:16 offset1:17\n"
"M 128,176,40\nR 172,186,8192\ns_and_b32 s62, s86, 0xf\ns_cmp_eq_u32 s62, 0xf\ns_cbranch_scc0 Lq408\nLq409:\n"
"ds_read2st64_b32 v[82:83], v225 offset0:18 offset1:19\nM 128,180,56\ns_add_u32 s65, s68, 23185408\nds_read2st64_b32 v[84:85], v225 offset0:20 offset1:21\nds_read2st64_b32 v[86:87], v225 offset0:22 offset1:23\nM 144,176,44\n"
"R 174,186,12288\ns_add_u32 m0, s50, 57344\nds_read2st64_b32 v[88:89], v225 offset0:24 offset1:25\nM 144,180,60\nds_read2st64_b32 v[90:91], v225 offset0:26 offset1:27\nds_read2st64_b32 v[92:93], v225 offset0:28 offset1:29\n"
"G 15\nM 148,160,44\nR 176,187,0\nbuffer_load_dwordx4 v221, s[16:19], s63 offen offset:1024 lds\nds_read2st64_b32 v[94:95], v225 offset0:30 offset1:31\nM 148,164,60\n"
"buffer_load_dwordx4 a[64:67], v252, s[20:23], s64 offen sc1\nv_add_f32 v32, v32, v64\nM 132,160,40\nR 178,187,4096\ns_add_u32 m0, s50, 65536\nv_add_f32 v33, v33, v65\n"
"M 132,164,56\ns_add_u32 s66, s69, 23070720\ns_add_u32 s63, s56, 196608\nv_add_f32 v34, v34, v66\nv_add_f32 v35, v35, v67\nM 116,160,36\n"
"R 180,187,8192\nbuffer_load_dwordx4 v220, s[16:19], s63 offen lds\nv_add_f32 v36, v36, v68\nM 116,164,52\nbuffer_load_dwordx4 a[68:71], v252, s[20:23], s65 offen sc1\nv_add_f32 v37, v37, v69\n"
"M 100,160,32\nR 182,187,12288\nv_add_f32 v38, v38, v70\nv_add_f32 v39, v39, v71\nM 100,164,48\nG 8\n"
"v_add_f32 v40, v40, v72\nv_add_f32 v41, v41, v73\nv_add_f32 v42, v42, v74\nM 104,168,32\nR 160,184,16384\nbuffer_load_dwordx4 v221, s[16:19], s63 offen offset:1024 lds\n"
"v_add_f32 v43, v43, v75\nM 104,172,48\ns_add_u32 s67, s69, 23185408\nv_add_f32 v44, v44, v76\nv_add_f32 v45, v45, v77\nv_add_f32 v46, v46, v78\n"
"M 120,168,36\nR 162,184,20480\nv_add_f32 v47, v47, v79\nv_add_f32 v48, v48, v80\nM 120,172,52\nbuffer_load_dwordx4 a[72:75], v252, s[20:23], s66 offen sc1\n"
"v_add_f32 v49, v49, v81\nM 136,168,40\nR 164,184,24576\nv_add_f32 v50, v50, v82\nv_add_f32 v51, v51, v83\nM 136,172,56\n"
"v_add_f32 v52, v52, v84\nv_add_f32 v53, v53, v85\nM 152,168,44\nR 166,184,28672\nv_add_f32 v54, v54, v86\nM 152,172,60\n"
"G 4\nv_add_f32 v55, v55, v87\nv_add_f32 v56, v56, v88\nM 156,176,44\nR 168,185,16384\nv_add_f32 v57, v57, v89\n"
"M 156,180,60\nbuffer_load_dwordx4 a[76:79], v252, s[20:23], s67 offen sc1\nM 140,176,40\nR 170,185,20480\nv_add_f32 v58, v58, v90\nM 140,180,56\n"
"s_add_u32 s64, s68, 23077888\nv_add_f32 v59, v59, v91\nv_add_f32 v60, v60, v92\nM 124,176,36\nR 172,185,24576\nv_add_f32 v61, v61, v93\n"
"M 124,180,52\ns_add_u32 s65, s68, 23192576\nv_add_f32 v62, v62, v94\nv_add_f32 v63, v63, v95\nM 108,176,32\nR 174,185,28672\n"
"v_mul_f32 v230, v32, v32\nv_mul_f32 v231, v33, v33\nM 108,180,48\ns_add_u32 s84, s73, 23072768\ns_add_u32 s85, s73, 23187456\nG 4\n"
"v_mul_f32 v232, v34, v34\nv_mfma_f32_16x16x32_bf16 v[0:3], v[160:163], a[0:3], 0\nR 176,186,16384\nLq573:\ns_lshr_b32 s60, s45, 3\ns_lshl2_add_u32 s60, s60, s40\n"
"s_lshl_b32 s60, s60, 8\nv_lshl_add_u32 v224, v252, 3, s60\nv_add_u32 v224, Lq573+4096-LqPC, v224\nv_min_u32 v224, LqE-LqPC-4, v224\ns_mov_b64 exec, 3\nglobal_load_dword v224, v224, s[98:99]\n"
"s_mov_b64 exec, -1\nv_mul_f32 v233, v35, v35\nM 0,164,16\nbuffer_load_dwordx4 a[80:83], v252, s[20:23], s64 offen sc1\nv_fma_f32 v230, v230, v215, v214\nv_mfma_f32_16x16x32_bf16 v[16:19], v[160:163], a[4:7], 0\n"
"R 178,186,20480\nv_fma_f32 v231, v231, v215, v214\nv_fma_f32 v232, v232, v215, v214\nM 16,164,20\ns_add_u32 s66, s69, 23077888\nv_fma_f32 v233, v233, v215, v214\n"
"v_mul_f32 v230, v32, v230\nv_mul_f32 v231, v33, v231\nv_mfma_f32_16x16x32_bf16 a[224:227], v[160:163], a[8:11], 0\nR 180,186,24576\nv_mul_f32 v232, v34, v232\nv_mul_f32 v233, v35, v233\n"
"v_mfma_f32_16x16x32_bf16 a[224:227], v[164:167], a[24:27], a[224:227]\nbuffer_load_dwordx4 a[84:87], v252, s[20:23], s65 offen sc1\nv_exp_f32 v230, v230\nv_mfma_f32_16x16x32_bf16 a[240:243], v[160:163], a[12:15], 0\nR 182,186,28672\nv_fma_f32 v230, v230, v216, v216\n"
"v_exp_f32 v231, v231\nv_mfma_f32_16x16x32_bf16 a[240:243], v[164:167], a[28:31], a[240:243]\nG 4\nv_exp_f32 v232, v232\nv_exp_f32 v233, v233\nv_mfma_f32_16x16x32_bf16 a[244:247], v[168:171], a[12:15], 0\n"
"R 160,187,16384\ns_add_u32 m0, s50, 73728\nv_fma_f32 v231, v231, v216, v216\nv_mfma_f32_16x16x32_bf16 a[244:247], v[172:175], a[28:31], a[244:247]\ns_add_u32 s67, s69, 23192576\nv_rcp_f32 v230, v230\n"
"v_fma_f32 v232, v232, v216, v216\nv_mfma_f32_16x16x32_bf16 a[228:231], v[168:171], a[8:11], 0\nR 162,187,20480\ns_add_u32 s63, s56, 720896\nv_fma_f32 v233, v233, v216, v216\nv_mfma_f32_16x16x32_bf16 a[228:231], v[172:175], a[24:27], a[228:231]\n"
"buffer_load_dwordx4 a[88:91], v252, s[20:23], s66 offen sc1\nv_rcp_f32 v231, v231\nv_mfma_f32_16x16x32_bf16 v[20:23], v[168:171], a[4:7], 0\nR 164,187,24576\nv_mul_f32 v230, v32, v230\nv_rcp_f32 v232, v232\n"
"M 20,172,20\nbuffer_load_dwordx4 v220, s[16:19], s63 offen lds\nv_rcp_f32 v233, v233\nv_mfma_f32_16x16x32_bf16 v[4:7], v[168:171], a[0:3], 0\nR 166,187,28672\nv_mul_f32 v231, v33, v231\n"
"v_mul_f32 v232, v34, v232\nM 4,172,16\nG 4\nv_mul_f32 v233, v35, v233\nv_cvt_pk_bf16_f32 v32, v230, v231\nv_cvt_pk_bf16_f32 v33, v232, v233\n"
"v_mfma_f32_16x16x32_bf16 v[8:11], v[176:179], a[0:3], 0\nR 168,184,32768\nv_mul_f32 v234, v36, v36\nv_mul_f32 v235, v37, v37\nM 8,180,16\nbuffer_load_dwordx4 a[92:95], v252, s[20:23], s67 offen sc1\n"
"buffer_load_dwordx4 v221, s[16:19], s63 offen offset:1024 lds\nv_mul_f32 v236, v38, v38\nv_mfma_f32_16x16x32_bf16 v[24:27], v[176:179], a[4:7], 0\nR 170,184,36864\nLpg13:\ns_cbranch_execz LretO0n\n"
"s_add_u32 m0, s50, 81920\nv_mul_f32 v237, v39, v39\nM 24,180,20\ns_add_u32 s64, s68, 23071744\nv_fma_f32 v234, v234, v215, v214\nv_fma_f32 v235, v235, v215, v214\n"
"v_fma_f32 v236, v236, v215, v214\nv_mfma_f32_16x16x32_bf16 a[232:235], v[176:179], a[8:11], 0\nR 172,184,40960\ns_add_u32 s63, s57, 131072\nv_fma_f32 v237, v237, v215, v214\nv_mfma_f32_16x16x32_bf16 a[232:235], v[180:183], a[24:27], a[232:235]\n"
"s_add_u32 s65, s68, 23186432\nbuffer_load_dwordx4 v220, s[16:19], s63 offen lds\nv_mul_f32 v234, v36, v234\nv_mfma_f32_16x16x32_bf16 a[248:251], v[176:179], a[12:15], 0\nR 174,184,45056\nv_mul_f32 v235, v37, v235\n"
"v_mul_f32 v236, v38, v236\nv_mfma_f32_16x16x32_bf16 a[248:251], v[180:183], a[28:31], a[248:251]\nG 4\nv_exp_f32 v234, v234\nv_mul_f32 v237, v39, v237\nv_mfma_f32_16x16x32_bf16 a[252:255], v[160:163], a[12:15], 0\n"
"R 176,185,32768\nv_fma_f32 v234, v234, v216, v216\nv_exp_f32 v235, v235\nv_mfma_f32_16x16x32_bf16 a[252:255], v[164:167], a[28:31], a[252:255]\nbuffer_load_dwordx4 a[96:99], v252, s[20:23], s64 offen sc1\nbuffer_load_dwordx4 v221, s[16:19], s63 offen offset:1024 lds\n"
"v_exp_f32 v236, v236\nv_mfma_f32_16x16x32_bf16 a[236:239], v[160:163], a[8:11], 0\nR 178,185,36864\ns_add_u32 m0, s50, 90112\nv_exp_f32 v237, v237\nv_mfma_f32_16x16x32_bf16 a[236:239], v[164:167], a[24:27], a[236:239]\n"
"s_add_u32 s66, s69, 23071744\nv_rcp_f32 v234, v234\nv_fma_f32 v235, v235, v216, v216\nv_mfma_f32_16x16x32_bf16 v[28:31], v[160:163], a[4:7], 0\nR 180,185,40960\ns_add_u32 s63, s57, 655360\n"
"v_fma_f32 v236, v236, v216, v216\nM 28,164,20\nbuffer_load_dwordx4 a[100:103], v252, s[20:23], s65 offen sc1\nbuffer_load_dwordx4 v220, s[16:19], s63 offen lds\nv_fma_f32 v237, v237, v216, v216\nv_mfma_f32_16x16x32_bf16 v[12:15], v[160:163], a[0:3], 0\n"
"R 182,185,45056\nv_mul_f32 v234, v36, v234\nv_rcp_f32 v235, v235\nM 12,164,16\ns_waitcnt vmcnt(14)\ns_barrier\n"
"G 4\nv_rcp_f32 v236, v236\nM 0,168,32\nR 160,186,32768\nv_mul_f32 v235, v37, v235\nv_rcp_f32 v237, v237\n"
"M 0,172,48\ns_add_u32 s67, s69, 23186432\nbuffer_load_dwordx4 v221, s[16:19], s63 offen offset:1024 lds\nv_mul_f32 v236, v38, v236\nM 16,168,36\nR 162,186,36864\n"
"v_mul_f32 v237, v39, v237\nv_cvt_pk_bf16_f32 v34, v234, v235\nM 16,172,52\nbuffer_load_dwordx4 a[104:107], v252, s[20:23], s66 offen sc1\nv_cvt_pk_bf16_f32 v35, v236, v237\nv_mfma_f32_16x16x32_bf16 a[224:227], v[168:171], a[40:43], a[224:227]\n"
"R 164,186,40960\nv_mul_f32 v230, v40, v40\nv_mul_f32 v231, v41, v41\nv_mfma_f32_16x16x32_bf16 a[224:227], v[172:175], a[56:59], a[224:227]\nv_mul_f32 v232, v42, v42\nv_mul_f32 v233, v43, v43\n"
"v_fma_f32 v230, v230, v215, v214\nv_mfma_f32_16x16x32_bf16 a[240:243], v[168:171], a[44:47], a[240:243]\nR 166,186,45056\nv_fma_f32 v231, v231, v215, v214\nv_fma_f32 v232, v232, v215, v214\nv_mfma_f32_16x16x32_bf16 a[240:243], v[172:175], a[60:63], a[240:243]\n"
"G 4\nv_fma_f32 v233, v233, v215, v214\nv_mul_f32 v230, v40, v230\nv_mul_f32 v231, v41, v231\nv_mfma_f32_16x16x32_bf16 a[244:247], v[176:179], a[44:47], a[244:247]\nR 168,187,32768\n"
"v_mul_f32 v232, v42, v232\nv_mul_f32 v233, v43, v233\nv_mfma_f32_16x16x32_bf16 a[244:247], v[180:183], a[60:63], a[244:247]\nbuffer_load_dwordx4 a[108:111], v252, s[20:23], s67 offen sc1\nv_exp_f32 v230, v230\nv_mfma_f32_16x16x32_bf16 a[228:231], v[176:179], a[40:43], a[228:231]\n"
"R 170,187,36864\nv_fma_f32 v230, v230, v216, v216\nv_exp_f32 v231, v231\nv_mfma_f32_16x16x32_bf16 a[228:231], v[180:183], a[56:59], a[228:231]\ns_add_u32 s64, s68, 23078912\nv_exp_f32 v232, v232\n"
"v_exp_f32 v233, v233\nM 20,176,36\nR 172,187,40960\nv_fma_f32 v231, v231, v216, v216\nv_fma_f32 v232, v232, v216, v216\nM 20,180,52\n"
"s_add_u32 s65, s68, 23193600\nv_rcp_f32 v230, v230\nv_fma_f32 v233, v233, v216, v216\nM 4,176,32\nR 174,187,45056\ns_add_u32 m0, s50, 98304\n"
"v_rcp_f32 v231, v231\nM 4,180,48\nG 4\nv_rcp_f32 v232, v232\nv_rcp_f32 v233, v233\nM 8,160,32\n"
"R 176,184,49152\ns_add_u32 s63, s57, 196608\nv_mul_f32 v230, v40, v230\nM 8,164,48\nbuffer_load_dwordx4 a[112:115], v252, s[20:23], s64 offen sc1\nv_mul_f32 v231, v41, v231\n"
"M 24,160,36\nR 178,184,53248\nv_mul_f32 v232, v42, v232\nv_mul_f32 v233, v43, v233\nM 24,164,52\ns_add_u32 s66, s69, 23078912\n"
"buffer_load_dwordx4 v220, s[16:19], s63 offen lds\nv_cvt_pk_bf16_f32 v40, v230, v231\nv_mfma_f32_16x16x32_bf16 a[232:235], v[160:163], a[40:43], a[232:235]\nR 180,184,57344\nv_cvt_pk_bf16_f32 v41, v232, v233\nv_mul_f32 v234, v44, v44\n"
"v_mfma_f32_16x16x32_bf16 a[232:235], v[164:167], a[56:59], a[232:235]\nbuffer_load_dwordx4 a[116:119], v252, s[20:23], s65 offen sc1\nv_mul_f32 v235, v45, v45\nv_mfma_f32_16x16x32_bf16 a[248:251], v[160:163], a[44:47], a[248:251]\nR 182,184,61440\nv_mul_f32 v236, v46, v46\n"
"v_mul_f32 v237, v47, v47\nv_mfma_f32_16x16x32_bf16 a[248:251], v[164:167], a[60:63], a[248:251]\nbuffer_load_dwordx4 v221, s[16:19], s63 offen offset:1024 lds\nG 4\nv_fma_f32 v234, v234, v215, v214\nv_mfma_f32_16x16x32_bf16 a[252:255], v[168:171], a[44:47], a[252:255]\n"
"R 160,185,49152\ns_add_u32 m0, s50, 106496\nv_fma_f32 v235, v235, v215, v214\nv_mfma_f32_16x16x32_bf16 a[252:255], v[172:175], a[60:63], a[252:255]\ns_add_u32 s67, s69, 23193600\ns_add_u32 s63, s57, 720896\n"
"v_fma_f32 v236, v236, v215, v214\nv_fma_f32 v237, v237, v215, v214\nv_mfma_f32_16x16x32_bf16 a[236:239], v[168:171], a[40:43], a[236:239]\nR 162,185,53248\nv_mov_b32 v248, 0\nv_mul_f32 v234, v44, v234\n"
"v_mfma_f32_16x16x32_bf16 a[236:239], v[172:175], a[56:59], a[236:239]\nbuffer_load_dwordx4 a[120:123], v252, s[20:23], s66 offen sc1\nbuffer_load_dwordx4 v220, s[16:19], s63 offen lds\nv_mov_b32 v249, 0\nv_mul_f32 v235, v45, v235\nM 28,168,36\n"
"R 164,185,57344\ns_add_u32 s61, s82, 385048\ns_mov_b64 exec, 0xf\nbuffer_atomic_or_x2 v[248:249], v250, s[20:23], s61 offen sc0 sc1\ns_mov_b64 exec, -1\nv_mul_f32 v236, v46, v236\n"
"M 28,172,52\nbuffer_load_dwordx4 v221, s[16:19], s63 offen offset:1024 lds\nv_mul_f32 v237, v47, v237\nM 12,168,32\nR 166,185,61440\ns_add_u32 m0, s50, 114688\n"
"v_exp_f32 v234, v234\nM 12,172,48\nG 4\nv_exp_f32 v235, v235\nv_exp_f32 v236, v236\nM 96,176,64\n"
"R 168,186,49152\ns_add_u32 s63, s58, 0\nv_exp_f32 v237, v237\ns_waitcnt vmcnt(21)\nM 96,180,80\nbuffer_load_dwordx4 a[124:127], v252, s[20:23], s67 offen sc1\n"
"v_fma_f32 v234, v234, v216, v216\nM 112,176,68\nR 170,186,53248\nv_fma_f32 v235, v235, v216, v216\nv_fma_f32 v236, v236, v216, v216\ns_waitcnt vmcnt(21)\n"
"M 112,180,84\nbuffer_load_dwordx4 v220, s[16:19], s63 offen lds\nv_fma_f32 v237, v237, v216, v216\nM 128,176,72\nR 172,186,57344\nv_mul_f32 v230, v48, v48\n"
"v_rcp_f32 v234, v234\ns_waitcnt vmcnt(21)\nM 128,180,88\nbuffer_load_dwordx4 v221, s[16:19], s63 offen offset:1024 lds\nv_rcp_f32 v235, v235\nM 144,176,76\n"
"R 174,186,61440\ns_add_u32 m0, s50, 0\nv_rcp_f32 v236, v236\ns_waitcnt vmcnt(20)\nM 144,180,92\nG 4\n"
"v_rcp_f32 v237, v237\nv_mul_f32 v234, v44, v234\nM 148,160,76\nR 176,187,49152\ns_add_u32 s63, s58, 524288\nv_mul_f32 v235, v45, v235\n"
"M 148,164,92\nbuffer_load_dwordx4 v220, s[16:19], s63 offen lds\nv_mul_f32 v236, v46, v236\nM 132,160,72\nR 178,187,53248\nv_mul_f32 v237, v47, v237\n"
"v_cvt_pk_bf16_f32 v42, v234, v235\nM 132,164,88\nbuffer_store_dwordx4 v[32:35], v252, s[20:23], s84 offen offset:0 sc1\nv_cvt_pk_bf16_f32 v43, v236, v237\nM 116,160,68\nR 180,187,57344\n"
"v_mul_f32 v231, v49, v49\nv_mul_f32 v232, v50, v50\nM 116,164,84\nbuffer_load_dwordx4 v221, s[16:19], s63 offen offset:1024 lds\nv_mul_f32 v233, v51, v51\nM 100,160,64\n"
"R 182,187,61440\nv_fma_f32 v230, v230, v218, v217\nv_fma_f32 v231, v231, v218, v217\nM 100,164,80\nG 4\nv_fma_f32 v232, v232, v218, v217\n"
"v_fma_f32 v233, v233, v218, v217\nv_mul_f32 v230, v48, v230\nM 104,168,64\nR 160,188,0\ns_waitcnt vmcnt(16)\ns_barrier\n"
"v_mul_f32 v231, v49, v231\nM 104,172,80\nbuffer_store_dwordx4 v[40:43], v252, s[20:23], s84 offen offset:1024 sc1\nv_mul_f32 v232, v50, v232\nM 120,168,68\nR 162,188,4096\n"
"v_mul_f32 v233, v51, v233\nv_exp_f32 v230, v230\nM 120,172,84\ns_add_u32 m0, s50, 8192\nv_exp_f32 v231, v231\nv_exp_f32 v232, v232\n"
"M 136,168,72\nR 164,188,8192\nv_fma_f32 v230, v230, v219, v219\nv_exp_f32 v233, v233\nM 136,172,88\ns_add_u32 s63, s58, 65536\n"
"v_rcp_f32 v230, v230\nv_fma_f32 v231, v231, v219, v219\nM 152,168,76\nR 166,188,12288\nv_fma_f32 v232, v232, v219, v219\nv_fma_f32 v233, v233, v219, v219\n"
"M 152,172,92\nG 4\nv_rcp_f32 v231, v231\nv_rcp_f32 v232, v232\nM 156,176,76\nR 168,189,0\n"
"v_mul_f32 v230, v48, v230\nv_rcp_f32 v233, v233\nM 156,180,92\nbuffer_load_dwordx4 v220, s[16:19], s63 offen lds\nv_mul_f32 v231, v49, v231\nM 140,176,72\n"
"R 170,189,4096\nv_mul_f32 v232, v50, v232\nv_mul_f32 v233, v51, v233\nM 140,180,88\nv_cvt_pk_bf16_f32 v48, v230, v231\nv_cvt_pk_bf16_f32 v49, v232, v233\n"
"v_mul_f32 v234, v52, v52\nM 124,176,68\nR 172,189,8192\nv_mul_f32 v235, v53, v53\nv_mul_f32 v236, v54, v54\nM 124,180,84\n"
"buffer_load_dwordx4 v221, s[16:19], s63 offen offset:1024 lds\nv_mul_f32 v237, v55, v55\nM 108,176,64\nR 174,189,12288\ns_add_u32 m0, s50, 16384\nv_fma_f32 v234, v234, v218, v217\n"
"M 108,180,80\nG 4\nv_fma_f32 v235, v235, v218, v217\nv_fma_f32 v236, v236, v218, v217\nv_fma_f32 v237, v237, v218, v217\nM 96,160,96\n"
"R 176,190,0\ns_add_u32 s63, s58, 589824\nv_mul_f32 v234, v52, v234\ns_waitcnt vmcnt(16)\nM 96,164,112\nbuffer_load_dwordx4 v220, s[16:19], s63 offen lds\n"
"v_mul_f32 v235, v53, v235\nM 112,160,100\nR 178,190,4096\nv_mul_f32 v236, v54, v236\nv_mul_f32 v237, v55, v237\ns_waitcnt vmcnt(15)\n"
"M 112,164,116\nv_exp_f32 v234, v234\nv_exp_f32 v235, v235\nM 128,160,104\nR 180,190,8192\nv_fma_f32 v234, v234, v219, v219\n"
"v_exp_f32 v236, v236\ns_waitcnt vmcnt(13)\nM 128,164,120\nv_exp_f32 v237, v237\nv_fma_f32 v235, v235, v219, v219\nM 144,160,108\n"
"R 182,190,12288\nv_fma_f32 v236, v236, v219, v219\nv_fma_f32 v237, v237, v219, v219\ns_waitcnt vmcnt(9)\nM 144,164,124\nG 4\n"
"v_rcp_f32 v234, v234\nv_rcp_f32 v235, v235\nM 148,168,108\nR 160,191,0\nv_mul_f32 v234, v52, v234\nv_rcp_f32 v236, v236\n"
"M 148,172,124\ns_add_u32 s64, s68, 23074816\nv_rcp_f32 v237, v237\nv_mul_f32 v235, v53, v235\nM 132,168,104\nR 162,191,4096\n"
"v_cmp_eq_u64 s[86:87], s[46:47], v[248:249]\nv_mul_f32 v236, v54, v236\nM 132,172,120\nbuffer_load_dwordx4 v221, s[16:19], s63 offen offset:1024 lds\nv_mul_f32 v237, v55, v237\nM 116,168,100\n"
"R 164,191,8192\ns_and_b32 s62, s86, 0xf\ns_cmp_eq_u32 s62, 0xf\ns_cbranch_scc0 Lq443\nLq444:\nv_cvt_pk_bf16_f32 v50, v234, v235\n"
"M 116,172,116\ns_add_u32 s65, s68, 23189504\nv_cvt_pk_bf16_f32 v51, v236, v237\nv_mul_f32 v230, v56, v56\nv_mul_f32 v231, v57, v57\nM 100,168,96\n"
"R 166,191,12288\nv_mul_f32 v232, v58, v58\nv_mul_f32 v233, v59, v59\nM 100,172,112\nG 4\nv_fma_f32 v230, v230, v218, v217\n"
"v_fma_f32 v231, v231, v218, v217\nM 104,176,96\nR 168,188,16384\nv_fma_f32 v232, v232, v218, v217\nM 104,180,112\nbuffer_load_dwordx4 a[128:131], v252, s[20:23], s64 offen sc1\n"
"M 120,176,100\nR 170,188,20480\nv_fma_f32 v233, v233, v218, v217\nM 120,180,116\ns_add_u32 s66, s69, 23074816\nv_mul_f32 v230, v56, v230\n"
"v_mul_f32 v231, v57, v231\nM 136,176,104\nR 172,188,24576\nv_mul_f32 v232, v58, v232\nM 136,180,120\nbuffer_load_dwordx4 a[132:135], v252, s[20:23], s65 offen sc1\n"
"M 152,176,108\nR 174,188,28672\nv_mul_f32 v233, v59, v233\nM 152,180,124\nG 4\nv_exp_f32 v230, v230\n"
"M 156,160,108\nR 176,189,16384\nv_fma_f32 v230, v230, v219, v219\nM 156,164,124\ns_add_u32 s67, s69, 23189504\nv_exp_f32 v231, v231\n"
"M 140,160,104\nR 178,189,20480\nv_fma_f32 v231, v231, v219, v219\nM 140,164,120\nbuffer_load_dwordx4 a[136:139], v252, s[20:23], s66 offen sc1\nM 124,160,100\n"
"R 180,189,24576\nv_mul_f32 v234, v60, v60\nM 124,164,116\nbuffer_store_dwordx4 v[48:51], v252, s[20:23], s85 offen offset:0 sc1\nM 108,160,96\nR 182,189,28672\n"
"s_add_u32 m0, s50, 24576\nM 108,164,112\ns_waitcnt vmcnt(9)\ns_barrier\nG 4\nM 0,168,64\n"
"R 160,190,16384\ns_add_u32 s63, s58, 131072\nM 0,172,80\nbuffer_load_dwordx4 a[140:143], v252, s[20:23], s67 offen sc1\nM 16,168,68\nR 162,190,20480\n"
"v_mul_f32 v235, v61, v61\nM 16,172,84\ns_add_u32 s64, s68, 23081984\nv_exp_f32 v232, v232\nv_mfma_f32_16x16x32_bf16 a[224:227], v[168:171], a[72:75], a[224:227]\nR 164,190,24576\n"
"buffer_load_dwordx4 v220, s[16:19], s63 offen lds\nv_mfma_f32_16x16x32_bf16 a[224:227], v[172:175], a[88:91], a[224:227]\ns_add_u32 s65, s68, 23196672\nv_exp_f32 v233, v233\nv_mfma_f32_16x16x32_bf16 a[240:243], v[168:171], a[76:79], a[240:243]\nR 166,190,28672\n"
"v_fma_f32 v232, v232, v219, v219\nv_mfma_f32_16x16x32_bf16 a[240:243], v[172:175], a[92:95], a[240:243]\nG 4\nv_rcp_f32 v230, v230\nv_mfma_f32_16x16x32_bf16 a[244:247], v[176:179], a[76:79], a[244:247]\nR 168,191,16384\n"
"buffer_load_dwordx4 v221, s[16:19], s63 offen offset:1024 lds\nv_mfma_f32_16x16x32_bf16 a[244:247], v[180:183], a[92:95], a[244:247]\nbuffer_load_dwordx4 a[144:147], v252, s[20:23], s64 offen sc1\nv_mfma_f32_16x16x32_bf16 a[228:231], v[176:179], a[72:75], a[228:231]\nR 170,191,20480\ns_add_u32 m0, s50, 32768\n"
"v_mfma_f32_16x16x32_bf16 a[228:231], v[180:183], a[88:91], a[228:231]\ns_add_u32 s66, s69, 23081984\ns_add_u32 s63, s58, 655360\nv_fma_f32 v233, v233, v219, v219\nM 20,176,68\nR 172,191,24576\n"
"buffer_load_dwordx4 v220, s[16:19], s63 offen lds\nM 20,180,84\nbuffer_load_dwordx4 a[148:151], v252, s[20:23], s65 offen sc1\nM 4,176,64\nR 174,191,28672\nv_mul_f32 v230, v56, v230\n"
"M 4,180,80\nG 4\nv_rcp_f32 v231, v231\nM 8,160,64\nR 176,188,32768\nbuffer_load_dwordx4 v221, s[16:19], s63 offen offset:1024 lds\n"
"M 8,164,80\ns_add_u32 s67, s69, 23196672\nv_rcp_f32 v232, v232\nM 24,160,68\nR 178,188,36864\nv_mul_f32 v231, v57, v231\n"
"M 24,164,84\nbuffer_load_dwordx4 a[152:155], v252, s[20:23], s66 offen sc1\nv_mfma_f32_16x16x32_bf16 a[232:235], v[160:163], a[72:75], a[232:235]\nR 180,188,40960\nv_mul_f32 v232, v58, v232\nv_mfma_f32_16x16x32_bf16 a[232:235], v[164:167], a[88:91], a[232:235]\n"
"v_rcp_f32 v233, v233\nv_mfma_f32_16x16x32_bf16 a[248:251], v[160:163], a[76:79], a[248:251]\nR 182,188,45056\nv_mul_f32 v233, v59, v233\nv_mfma_f32_16x16x32_bf16 a[248:251], v[164:167], a[92:95], a[248:251]\nG 4\n"
"v_cvt_pk_bf16_f32 v56, v230, v231\nv_cvt_pk_bf16_f32 v57, v232, v233\nv_mfma_f32_16x16x32_bf16 a[252:255], v[168:171], a[76:79], a[252:255]\nR 160,189,32768\nv_mul_f32 v236, v62, v62\nv_mfma_f32_16x16x32_bf16 a[252:255], v[172:175], a[92:95], a[252:255]\n"
"buffer_load_dwordx4 a[156:159], v252, s[20:23], s67 offen sc1\nv_mfma_f32_16x16x32_bf16 a[236:239], v[168:171], a[72:75], a[236:239]\nR 162,189,36864\nv_mul_f32 v237, v63, v63\nv_mfma_f32_16x16x32_bf16 a[236:239], v[172:175], a[88:91], a[236:239]\nv_fma_f32 v234, v234, v218, v217\n"
"v_fma_f32 v235, v235, v218, v217\nM 28,168,68\nR 164,189,40960\nv_fma_f32 v236, v236, v218, v217\nM 28,172,84\ns_add_u32 m0, s50, 40960\n"
"v_fma_f32 v237, v237, v218, v217\nv_mul_f32 v234, v60, v234\nM 12,168,64\nR 166,189,45056\nv_mul_f32 v235, v61, v235\nM 12,172,80\n"
"G 4\nv_exp_f32 v234, v234\nM 0,176,96\nR 168,190,32768\nv_mul_f32 v236, v62, v236\nM 0,180,112\n"
"s_add_u32 s63, s58, 196608\nv_exp_f32 v235, v235\nM 16,176,100\nR 170,190,36864\nv_mul_f32 v237, v63, v237\nM 16,180,116\n"
"buffer_load_dwordx4 v220, s[16:19], s63 offen lds\nv_mfma_f32_16x16x32_bf16 a[224:227], v[176:179], a[104:107], a[224:227]\nR 172,190,40960\nv_fma_f32 v234, v234, v219, v219\nv_mfma_f32_16x16x32_bf16 a[224:227], v[180:183], a[120:123], a[224:227]\nv_exp_f32 v236, v236\n"
"v_mfma_f32_16x16x32_bf16 a[240:243], v[176:179], a[108:111], a[240:243]\nR 174,190,45056\nv_fma_f32 v235, v235, v219, v219\nv_mfma_f32_16x16x32_bf16 a[240:243], v[180:183], a[124:127], a[240:243]\nG 4\nv_exp_f32 v237, v237\n"
"v_mfma_f32_16x16x32_bf16 a[244:247], v[160:163], a[108:111], a[244:247]\nR 176,191,32768\nv_fma_f32 v236, v236, v219, v219\nv_mfma_f32_16x16x32_bf16 a[244:247], v[164:167], a[124:127], a[244:247]\nbuffer_load_dwordx4 v221, s[16:19], s63 offen offset:1024 lds\nv_mfma_f32_16x16x32_bf16 a[228:231], v[160:163], a[104:107], a[228:231]\n"
"R 178,191,36864\ns_add_u32 s63, s58, 720896\nv_mfma_f32_16x16x32_bf16 a[228:231], v[164:167], a[120:123], a[228:231]\ns_add_u32 m0, s50, 49152\nv_rcp_f32 v234, v234\nM 20,160,100\n"
"R 180,191,40960\nv_fma_f32 v237, v237, v219, v219\nM 20,164,116\nbuffer_load_dwordx4 v220, s[16:19], s63 offen lds\nM 4,160,96\nR 182,191,45056\n"
"v_mul_f32 v234, v60, v234\nM 4,164,112\nG 4\nv_rcp_f32 v235, v235\nM 8,168,96\nR 160,188,49152\n"
"v_mul_f32 v235, v61, v235\nM 8,172,112\nbuffer_load_dwordx4 v221, s[16:19], s63 offen offset:1024 lds\nM 24,168,100\nR 162,188,53248\ns_add_u32 s63, s58, 393216\n"
"M 24,172,116\ns_add_u32 m0, s50, 57344\nv_rcp_f32 v236, v236\nv_mfma_f32_16x16x32_bf16 a[232:235], v[168:171], a[104:107], a[232:235]\nR 164,184,0\nv_mul_f32 v236, v62, v236\n"
"v_mfma_f32_16x16x32_bf16 a[232:235], v[172:175], a[120:123], a[232:235]\nbuffer_load_dwordx4 v220, s[16:19], s63 offen lds\nv_mfma_f32_16x16x32_bf16 a[248:251], v[168:171], a[108:111], a[248:251]\nR 166,184,4096\nv_cvt_pk_bf16_f32 v58, v234, v235\nv_mfma_f32_16x16x32_bf16 a[248:251], v[172:175], a[124:127], a[248:251]\n"
"G 4\nv_rcp_f32 v237, v237\nv_mfma_f32_16x16x32_bf16 a[252:255], v[176:179], a[108:111], a[252:255]\nR 168,189,49152\nv_mul_f32 v237, v63, v237\nv_mfma_f32_16x16x32_bf16 a[252:255], v[180:183], a[124:127], a[252:255]\n"
"buffer_load_dwordx4 v221, s[16:19], s63 offen offset:1024 lds\nv_mfma_f32_16x16x32_bf16 a[236:239], v[176:179], a[104:107], a[236:239]\nR 170,189,53248\ns_add_u32 s63, s58, 458752\nv_mfma_f32_16x16x32_bf16 a[236:239], v[180:183], a[120:123], a[236:239]\ns_add_u32 m0, s50, 65536\n"
"v_cvt_pk_bf16_f32 v59, v236, v237\nbuffer_store_dwordx4 v[56:59], v252, s[20:23], s85 offen offset:1024 sc1\nM 28,176,100\nR 172,185,0\nM 28,180,116\nbuffer_load_dwordx4 v220, s[16:19], s63 offen lds\n"
"M 12,176,96\nR 174,185,4096\nM 12,180,112\nLpg14:\ns_cbranch_execz LretO0n\ns_waitcnt vmcnt(10)\n"
"s_barrier\nG 4\nv_mfma_f32_16x16x32_bf16 v[32:35], v[160:163], a[0:3], 0\nR 176,190,49152\nM 32,164,16\nbuffer_load_dwordx4 v221, s[16:19], s63 offen offset:1024 lds\n"
"v_mfma_f32_16x16x32_bf16 v[48:51], v[160:163], a[4:7], 0\nR 178,190,53248\nM 48,164,20\nv_mfma_f32_16x16x32_bf16 v[64:67], v[160:163], a[8:11], 0\nR 180,186,0\nM 64,164,24\n"
"v_mfma_f32_16x16x32_bf16 v[80:83], v[160:163], a[12:15], 0\nR 182,186,4096\nM 80,164,28\nG 4\nv_mfma_f32_16x16x32_bf16 v[84:87], v[168:171], a[12:15], 0\nR 160,191,49152\n"
"M 84,172,28\nv_mfma_f32_16x16x32_bf16 v[68:71], v[168:171], a[8:11], 0\nR 162,191,53248\nM 68,172,24\nv_mfma_f32_16x16x32_bf16 v[52:55], v[168:171], a[4:7], 0\nR 164,187,0\n"
"M 52,172,20\nv_mfma_f32_16x16x32_bf16 v[36:39], v[168:171], a[0:3], 0\nR 166,187,4096\nM 36,172,16\nG 4\nv_mfma_f32_16x16x32_bf16 v[40:43], v[176:179], a[0:3], 0\n"
"R 168,184,8192\nM 40,180,16\nv_mfma_f32_16x16x32_bf16 v[56:59], v[176:179], a[4:7], 0\nR 170,184,12288\nM 56,180,20\nv_mfma_f32_16x16x32_bf16 v[72:75], v[176:179], a[8:11], 0\n"
"R 172,184,16384\nM 72,180,24\nv_mfma_f32_16x16x32_bf16 v[88:91], v[176:179], a[12:15], 0\nR 174,184,20480\nM 88,180,28\nG 4\n"
"v_mfma_f32_16x16x32_bf16 v[92:95], v[160:163], a[12:15], 0\nR 176,185,8192\nM 92,164,28\nv_mfma_f32_16x16x32_bf16 v[76:79], v[160:163], a[8:11], 0\nR 178,185,12288\nM 76,164,24\n"
"v_mfma_f32_16x16x32_bf16 v[60:63], v[160:163], a[4:7], 0\nR 180,185,16384\nM 60,164,20\nv_mfma_f32_16x16x32_bf16 v[44:47], v[160:163], a[0:3], 0\nR 182,185,20480\nM 44,164,16\n"
"G 4\nM 32,168,32\nR 160,186,8192\nM 32,172,48\nM 48,168,36\nR 162,186,12288\n"
"M 48,172,52\nM 64,168,40\nR 164,186,16384\nM 64,172,56\nM 80,168,44\nR 166,186,20480\n"
"M 80,172,60\nG 4\nM 84,176,44\nR 168,187,8192\nM 84,180,60\nM 68,176,40\n"
"R 170,187,12288\nv_mov_b32 v226, s46\nM 68,180,56\ns_add_u32 m0, s50, 73728\nv_mov_b32 v227, s47\nM 52,176,36\n"
"R 172,187,16384\ns_add_u32 s61, s81, 385040\ns_lshl_b32 s62, s44, 5\ns_add_u32 s61, s61, s62\ns_waitcnt vmcnt(0)\ns_mov_b64 exec, 1\n"
"buffer_atomic_swap_x2 v[226:227], off, s[20:23], s61 sc1\ns_mov_b64 exec, -1\nM 52,180,52\ns_add_u32 s63, s56, 393216\nM 36,176,32\nR 174,187,20480\n"
"M 36,180,48\nG 4\nM 40,160,32\nR 176,184,24576\nM 40,164,48\nbuffer_load_dwordx4 v220, s[16:19], s63 offen lds\n"
"M 56,160,36\nR 178,184,28672\nM 56,164,52\nM 72,160,40\nR 180,184,32768\nM 72,164,56\n"
"buffer_load_dwordx4 v221, s[16:19], s63 offen offset:1024 lds\nM 88,160,44\nR 182,184,36864\ns_add_u32 m0, s50, 81920\nM 88,164,60\nG 4\n"
"M 92,168,44\nR 160,185,24576\ns_add_u32 s63, s56, 458752\nM 92,172,60\nbuffer_load_dwordx4 v220, s[16:19], s63 offen lds\nM 76,168,40\n"
"R 162,185,28672\nM 76,172,56\nM 60,168,36\nR 164,185,32768\nM 60,172,52\nbuffer_load_dwordx4 v221, s[16:19], s63 offen offset:1024 lds\n"
"M 44,168,32\nR 166,185,36864\ns_add_u32 m0, s50, 90112\nM 44,172,48\ns_barrier\nG 4\n"
"M 32,176,64\nR 168,186,24576\ns_add_u32 s63, s57, 393216\nM 32,180,80\nbuffer_load_dwordx4 v220, s[16:19], s63 offen lds\nM 48,176,68\n"
"R 170,186,28672\nM 48,180,84\nM 64,176,72\nR 172,186,32768\nM 64,180,88\nM 80,176,76\n"
"R 174,186,36864\nM 80,180,92\nG 4\nM 84,160,76\nR 176,187,24576\nM 84,164,92\n"
"buffer_load_dwordx4 v221, s[16:19], s63 offen offset:1024 lds\nM 68,160,72\nR 178,187,28672\nM 68,164,88\ns_add_u32 m0, s50, 98304\nM 52,160,68\n"
"R 180,187,32768\nv_mov_b32 v248, 0\nM 52,164,84\ns_add_u32 s63, s57, 458752\nv_mov_b32 v249, 0\nM 36,160,64\n"
"R 182,187,36864\ns_add_u32 s61, s82, 385040\ns_mov_b64 exec, 0xf\nbuffer_atomic_or_x2 v[248:249], v250, s[20:23], s61 offen sc0 sc1\ns_mov_b64 exec, -1\nM 36,164,80\n"
"G 4\nM 40,168,64\nR 160,184,40960\nM 40,172,80\nbuffer_load_dwordx4 v220, s[16:19], s63 offen lds\nM 56,168,68\n"
"R 162,184,45056\nM 56,172,84\nM 72,168,72\nR 164,184,49152\nM 72,172,88\nbuffer_load_dwordx4 v221, s[16:19], s63 offen offset:1024 lds\n"
"M 88,168,76\nR 166,184,53248\ns_add_u32 s63, s56, 262144\nM 88,172,92\nG 4\nM 92,176,76\n"
"R 168,185,40960\nM 92,180,92\ns_add_u32 m0, s50, 106496\nM 76,176,72\nR 170,185,45056\nM 76,180,88\n"
"buffer_load_dwordx4 v220, s[16:19], s63 offen lds\nM 60,176,68\nR 172,185,49152\nM 60,180,84\nM 44,176,64\nR 174,185,53248\n"
"M 44,180,80\nG 4\nM 32,160,96\nR 176,186,40960\nM 32,164,112\nbuffer_load_dwordx4 v221, s[16:19], s63 offen offset:1024 lds\n"
"M 48,160,100\nR 178,186,45056\ns_add_u32 s63, s56, 786432\nM 48,164,116\ns_add_u32 m0, s50, 114688\nM 64,160,104\n"
"R 180,186,49152\nM 64,164,120\nbuffer_load_dwordx4 v220, s[16:19], s63 offen lds\nM 80,160,108\nR 182,186,53248\nM 80,164,124\n"
"G 4\nM 84,168,108\nR 160,187,40960\nM 84,172,124\nbuffer_load_dwordx4 v221, s[16:19], s63 offen offset:1024 lds\nM 68,168,104\n"
"R 162,187,45056\ns_add_u32 s63, s56, 327680\nM 68,172,120\ns_add_u32 m0, s50, 0\nM 52,168,100\nR 164,187,49152\n"
"M 52,172,116\nbuffer_load_dwordx4 v220, s[16:19], s63 offen lds\nM 36,168,96\nR 166,187,53248\nM 36,172,112\nG 4\n"
"M 40,176,96\nR 168,184,57344\nM 40,180,112\nbuffer_load_dwordx4 v221, s[16:19], s63 offen offset:1024 lds\nM 56,176,100\nR 170,184,61440\n"
"M 56,180,116\nM 72,176,104\nR 172,188,0\nM 72,180,120\ns_add_u32 m0, s50, 8192\nM 88,176,108\n"
"R 174,188,4096\ns_waitcnt vmcnt(11)\ns_barrier\nM 88,180,124\nG 4\nM 92,160,108\n"
"R 176,185,57344\ns_add_u32 s63, s56, 851968\nM 92,164,124\ns_add_u32 s64, s68, 23072768\nM 76,160,104\nR 178,185,61440\n"
"M 76,164,120\nbuffer_load_dwordx4 v220, s[16:19], s63 offen lds\nM 60,160,100\nR 180,189,0\ns_waitcnt vmcnt(9)\nv_cmp_eq_u64 s[86:87], s[46:47], v[248:249]\n"
"M 60,164,116\ns_add_u32 s65, s68, 23187456\nM 44,160,96\nR 182,189,4096\ns_and_b32 s62, s86, 0xf\ns_cmp_eq_u32 s62, 0xf\n"
"s_cbranch_scc0 Lq462\nLq463:\nM 44,164,112\nG 4\nM 32,168,128\nR 160,186,57344\n"
"M 32,172,144\nbuffer_load_dwordx4 a[160:163], v252, s[20:23], s64 offen sc1\nM 48,168,132\nR 162,186,61440\nM 48,172,148\ns_add_u32 s66, s69, 23072768\n"
"M 64,168,136\nR 164,190,0\nM 64,172,152\nbuffer_load_dwordx4 a[164:167], v252, s[20:23], s65 offen sc1\nM 80,168,140\nR 166,190,4096\n"
"M 80,172,156\nG 4\nM 84,176,140\nR 168,187,57344\nM 84,180,156\ns_add_u32 s67, s69, 23187456\n"
"M 68,176,136\nR 170,187,61440\nM 68,180,152\nbuffer_load_dwordx4 a[168:171], v252, s[20:23], s66 offen sc1\nM 52,176,132\nR 172,191,0\n"
"M 52,180,148\ns_add_u32 s64, s68, 23079936\nM 36,176,128\nR 174,191,4096\nM 36,180,144\nbuffer_load_dwordx4 v221, s[16:19], s63 offen offset:1024 lds\n"
"G 4\nM 40,160,128\nR 176,188,8192\ns_add_u32 m0, s50, 16384\nM 40,164,144\nbuffer_load_dwordx4 a[172:175], v252, s[20:23], s67 offen sc1\n"
"M 56,160,132\nR 178,188,12288\ns_add_u32 s63, s57, 262144\nM 56,164,148\ns_add_u32 s65, s68, 23194624\nbuffer_load_dwordx4 v220, s[16:19], s63 offen lds\n"
"M 72,160,136\nR 180,188,16384\nLq575:\ns_lshr_b32 s60, s45, 3\ns_lshl2_add_u32 s60, s60, s40\ns_lshl_b32 s60, s60, 8\n"
"v_lshl_add_u32 v224, v252, 3, s60\nv_add_u32 v224, Lq575+4096-LqPC, v224\nv_min_u32 v224, LqE-LqPC-4, v224\ns_mov_b64 exec, 3\nglobal_load_dword v224, v224, s[98:99]\ns_mov_b64 exec, -1\n"
"M 72,164,152\nbuffer_load_dwordx4 a[176:179], v252, s[20:23], s64 offen sc1\nM 88,160,140\nR 182,188,20480\nM 88,164,156\nbuffer_load_dwordx4 v221, s[16:19], s63 offen offset:1024 lds\n"
"G 4\nM 92,168,140\nR 160,189,8192\ns_add_u32 m0, s50, 24576\nM 92,172,156\ns_add_u32 s66, s69, 23079936\n"
"M 76,168,136\nR 162,189,12288\ns_add_u32 s63, s57, 786432\nM 76,172,152\nbuffer_load_dwordx4 a[180:183], v252, s[20:23], s65 offen sc1\nbuffer_load_dwordx4 v220, s[16:19], s63 offen lds\n"
"M 60,168,132\nR 164,189,16384\nM 60,172,148\ns_add_u32 s67, s69, 23194624\nM 44,168,128\nR 166,189,20480\n"
"M 44,172,144\nbuffer_load_dwordx4 v221, s[16:19], s63 offen offset:1024 lds\nG 4\nM 96,176,128\nR 168,190,8192\ns_add_u32 m0, s50, 32768\n"
"M 96,180,144\nbuffer_load_dwordx4 a[184:187], v252, s[20:23], s66 offen sc1\nM 112,176,132\nR 170,190,12288\ns_add_u32 s63, s57, 327680\nM 112,180,148\n"
"s_add_u32 s64, s68, 23073792\nbuffer_load_dwordx4 v220, s[16:19], s63 offen lds\nM 128,176,136\nR 172,190,16384\nM 128,180,152\nbuffer_load_dwordx4 a[188:191], v252, s[20:23], s67 offen sc1\n"
"M 144,176,140\nR 174,190,20480\nM 144,180,156\nbuffer_load_dwordx4 v221, s[16:19], s63 offen offset:1024 lds\nG 4\nM 148,160,140\n"
"R 176,191,8192\ns_waitcnt vmcnt(19)\ns_barrier\nM 148,164,156\ns_add_u32 s65, s68, 23188480\ns_add_u32 s63, s57, 851968\n"
"M 132,160,136\nR 178,191,12288\ns_add_u32 m0, s50, 40960\nM 132,164,152\nbuffer_load_dwordx4 a[192:195], v252, s[20:23], s64 offen sc1\nM 116,160,132\n"
"R 180,191,16384\nbuffer_load_dwordx4 v220, s[16:19], s63 offen lds\nM 116,164,148\ns_add_u32 s66, s69, 23073792\nM 100,160,128\nR 182,191,20480\n"
"M 100,164,144\nG 4\nM 104,168,128\nR 160,188,24576\nbuffer_load_dwordx4 v221, s[16:19], s63 offen offset:1024 lds\nM 104,172,144\n"
"buffer_load_dwordx4 a[196:199], v252, s[20:23], s65 offen sc1\nM 120,168,132\nR 162,188,28672\nM 120,172,148\ns_add_u32 s67, s69, 23188480\nM 136,168,136\n"
"R 164,188,32768\nM 136,172,152\nbuffer_load_dwordx4 a[200:203], v252, s[20:23], s66 offen sc1\nM 152,168,140\nR 166,188,36864\nM 152,172,156\n"
"G 4\nM 156,176,140\nR 168,189,24576\nM 156,180,156\nbuffer_load_dwordx4 a[204:207], v252, s[20:23], s67 offen sc1\nM 140,176,136\n"
"R 170,189,28672\nM 140,180,152\ns_add_u32 s64, s68, 23080960\nM 124,176,132\nR 172,189,32768\nM 124,180,148\n"
"s_add_u32 s65, s68, 23195648\nM 108,176,128\nR 174,189,36864\nM 108,180,144\nG 4\nM 0,160,128\n"
"R 176,190,24576\nM 0,164,144\nbuffer_load_dwordx4 a[208:211], v252, s[20:23], s64 offen sc1\nM 16,160,132\nR 178,190,28672\nM 16,164,148\n"
"s_add_u32 s66, s69, 23080960\nv_mfma_f32_16x16x32_bf16 a[224:227], v[160:163], a[136:139], a[224:227]\nR 180,190,32768\nv_mfma_f32_16x16x32_bf16 a[224:227], v[164:167], a[152:155], a[224:227]\nbuffer_load_dwordx4 a[212:215], v252, s[20:23], s65 offen sc1\nv_mfma_f32_16x16x32_bf16 a[240:243], v[160:163], a[140:143], a[240:243]\n"
"R 182,190,36864\nv_mfma_f32_16x16x32_bf16 a[240:243], v[164:167], a[156:159], a[240:243]\nG 4\nv_mfma_f32_16x16x32_bf16 a[244:247], v[168:171], a[140:143], a[244:247]\nR 160,191,24576\nv_mfma_f32_16x16x32_bf16 a[244:247], v[172:175], a[156:159], a[244:247]\n"
"buffer_load_dwordx4 a[216:219], v252, s[20:23], s66 offen sc1\nv_mfma_f32_16x16x32_bf16 a[228:231], v[168:171], a[136:139], a[228:231]\nR 162,191,28672\nv_mfma_f32_16x16x32_bf16 a[228:231], v[172:175], a[152:155], a[228:231]\ns_add_u32 s67, s69, 23195648\nM 20,168,132\n"
"R 164,191,32768\nM 20,172,148\nM 4,168,128\nR 166,191,36864\nM 4,172,144\nG 4\n"
"M 8,176,128\nR 168,188,40960\nM 8,180,144\nbuffer_load_dwordx4 a[220:223], v252, s[20:23], s67 offen sc1\nM 24,176,132\nR 170,188,45056\n"
"M 24,180,148\ns_add_u32 m0, s50, 49152\nv_mfma_f32_16x16x32_bf16 a[232:235], v[176:179], a[136:139], a[232:235]\nR 172,188,49152\nv_mfma_f32_16x16x32_bf16 a[232:235], v[180:183], a[152:155], a[232:235]\ns_add_u32 s63, s58, 262144\n"
"v_mfma_f32_16x16x32_bf16 a[248:251], v[176:179], a[140:143], a[248:251]\nR 174,188,53248\nv_mfma_f32_16x16x32_bf16 a[248:251], v[180:183], a[156:159], a[248:251]\nG 4\nv_mfma_f32_16x16x32_bf16 a[252:255], v[160:163], a[140:143], a[252:255]\nR 176,189,40960\n"
"v_mfma_f32_16x16x32_bf16 a[252:255], v[164:167], a[156:159], a[252:255]\nbuffer_load_dwordx4 v220, s[16:19], s63 offen lds\nv_mfma_f32_16x16x32_bf16 a[236:239], v[160:163], a[136:139], a[236:239]\nR 178,189,45056\ns_add_u32 s84, s52, 2048\nv_mfma_f32_16x16x32_bf16 a[236:239], v[164:167], a[152:155], a[236:239]\n"
"v_lshrrev_b32 v244, 5, v252\nv_lshlrev_b32 v244, 5, v244\nM 28,160,132\nR 180,189,49152\nv_bfe_u32 v245, v252, 4, 1\nM 28,164,148\n"
"buffer_load_dwordx4 v221, s[16:19], s63 offen offset:1024 lds\nM 12,160,128\nR 182,189,53248\nM 12,164,144\ns_waitcnt vmcnt(9)\ns_barrier\n"
"G 4\nM 96,168,160\nR 160,190,40960\nM 96,172,176\ns_add_u32 m0, s50, 57344\nM 112,168,164\n"
"R 162,190,45056\nM 112,172,180\ns_add_u32 s63, s58, 786432\nM 128,168,168\nR 164,190,49152\nv_lshrrev_b32 v230, 8, v252\n"
"M 128,172,184\nbuffer_load_dwordx4 v220, s[16:19], s63 offen lds\nM 144,168,172\nR 166,190,53248\nv_lshlrev_b32 v230, 3, v230\nM 144,172,188\n"
"G 4\nM 148,176,172\nR 168,191,40960\nM 148,180,188\ns_and_b32 s60, s41, 7\ns_lshl2_add_u32 s60, s60, s40\n"
"v_add_u32 v244, s60, v244\nM 132,176,168\nR 170,191,45056\nv_mul_u32_u24 v244, 6144, v244\nM 132,180,184\ns_sub_u32 s60, s84, s84\n"
"s_add_u32 s60, s60, 2048\nv_mul_lo_u32 v245, v245, s60\nM 116,176,164\nR 172,191,49152\nv_add_u32 v244, v244, v245\nM 116,180,180\n"
"buffer_load_dwordx2 v[232:233], v230, s[36:39], s53 offen offset:0\nM 100,176,160\nR 174,191,53248\ns_bfm_b64 exec, 16, 0\nbuffer_load_dword v245, v244, s[4:7], s84 offen\ns_mov_b64 exec, -1\n"
"M 100,180,176\nG 4\nM 104,160,160\nR 176,184,0\nM 104,164,176\nbuffer_load_dwordx4 v221, s[16:19], s63 offen offset:1024 lds\n"
"M 120,160,164\nR 178,184,4096\nM 120,164,180\ns_add_u32 m0, s50, 65536\nM 136,160,168\nR 180,184,8192\n"
"buffer_load_dwordx2 v[234:235], v230, s[36:39], s53 offen offset:32\nM 136,164,184\nLpg15:\ns_cbranch_execz LretO0n\ns_add_u32 s63, s58, 327680\nbuffer_load_dwordx2 v[236:237], v230, s[36:39], s53 offen offset:64\n"
"M 152,160,172\nR 182,184,12288\nM 152,164,188\nbuffer_load_dwordx2 v[238:239], v230, s[36:39], s53 offen offset:96\nG 4\nM 156,168,172\n"
"R 160,185,0\nv_bfe_u32 v242, v252, 4, 4\nM 156,172,188\nbuffer_load_dwordx4 v220, s[16:19], s63 offen lds\nM 140,168,168\nR 162,185,4096\n"
"v_lshrrev_b32 v243, 8, v252\nM 140,172,184\nv_lshlrev_b32 v243, 2, v243\nv_sub_u32 v242, v242, v243\nM 124,168,164\nR 164,185,8192\n"
"v_lshlrev_b32 v243, 4, v242\nM 124,172,180\ns_mov_b64 s[60:61], 0x3f80\nv_lshlrev_b64 v[208:209], v243, s[60:61]\nM 108,168,160\nR 166,185,12288\n"
"s_bfm_b64 exec, 16, 16\nbuffer_load_dword v245, v244, s[4:7], s84 offen\ns_mov_b64 exec, -1\nM 108,172,176\nG 4\nM 96,176,192\n"
"R 168,186,0\ns_waitcnt vmcnt(14)\nM 96,180,208\nbuffer_load_dwordx4 v221, s[16:19], s63 offen offset:1024 lds\nM 112,176,196\nR 170,186,4096\n"
"s_waitcnt vmcnt(14)\nM 112,180,212\ns_add_u32 m0, s50, 73728\nM 128,176,200\nR 172,186,8192\ns_waitcnt vmcnt(13)\n"
"M 128,180,216\ns_add_u32 s63, s58, 851968\nM 144,176,204\nR 174,186,12288\ns_waitcnt vmcnt(12)\nM 144,180,220\n"
"G 4\nv_cmp_gt_u32 s[60:61], 4, v242\ns_nop 1\nv_cndmask_b32 v212, 0, v208, s[60:61]\nv_cndmask_b32 v208, 0, v208, s[60:61]\nv_cndmask_b32 v213, 0, v209, s[60:61]\n"
"v_cndmask_b32 v209, 0, v209, s[60:61]\nM 148,160,204\nR 176,187,0\nM 148,164,220\nbuffer_load_dwordx4 v220, s[16:19], s63 offen lds\nM 132,160,200\n"
"R 178,187,4096\nM 132,164,216\nM 116,160,196\nR 180,187,8192\nM 116,164,212\nM 100,160,192\n"
"R 182,187,12288\ns_bfm_b64 exec, 16, 32\nbuffer_load_dword v245, v244, s[4:7], s84 offen\ns_mov_b64 exec, -1\nM 100,164,208\nG 4\n"
"M 104,168,192\nR 160,184,16384\nM 104,172,208\nbuffer_load_dwordx4 v221, s[16:19], s63 offen offset:1024 lds\nM 120,168,196\nR 162,184,20480\n"
"M 120,172,212\ns_add_u32 m0, s50, 81920\nM 136,168,200\nR 164,184,24576\nM 136,172,216\ns_add_u32 s63, s59, 0\n"
"M 152,168,204\nR 166,184,28672\nM 152,172,220\nG 4\nM 156,176,204\nR 168,185,16384\n"
"M 156,180,220\nbuffer_load_dwordx4 v220, s[16:19], s63 offen lds\nM 140,176,200\nR 170,185,20480\nM 140,180,216\nM 124,176,196\n"
"R 172,185,24576\nM 124,180,212\nM 108,176,192\nR 174,185,28672\ns_bfm_b64 exec, 16, 48\nbuffer_load_dword v245, v244, s[4:7], s84 offen\n"
"s_mov_b64 exec, -1\nM 108,180,208\nG 4\nM 0,160,160\nR 176,186,16384\nM 0,164,176\n"
"s_mov_b32 m0, s51\nM 16,160,164\nR 178,186,20480\nv_cvt_pk_bf16_f32 v128, v128, v129\nv_cvt_pk_bf16_f32 v129, v130, v131\nM 16,164,180\n"
"ds_write_addtid_b32 v128 offset:0\nv_cvt_pk_bf16_f32 v130, v132, v133\nv_cvt_pk_bf16_f32 v131, v134, v135\nv_mfma_f32_16x16x32_bf16 a[224:227], v[160:163], a[168:171], a[224:227]\nR 180,186,24576\nv_cvt_pk_bf16_f32 v132, v136, v137\n"
"v_mfma_f32_16x16x32_bf16 a[224:227], v[164:167], a[184:187], a[224:227]\nds_write_addtid_b32 v129 offset:256\nv_cvt_pk_bf16_f32 v133, v138, v139\nv_mfma_f32_16x16x32_bf16 a[240:243], v[160:163], a[172:175], a[240:243]\nR 182,186,28672\nv_cvt_pk_bf16_f32 v134, v140, v141\n"
"v_mfma_f32_16x16x32_bf16 a[240:243], v[164:167], a[188:191], a[240:243]\nds_write_addtid_b32 v130 offset:512\nG 7\nv_cvt_pk_bf16_f32 v135, v142, v143\nv_mfma_f32_16x16x32_bf16 a[244:247], v[168:171], a[172:175], a[244:247]\nR 160,187,16384\n"
"v_cvt_pk_bf16_f32 v136, v144, v145\nv_mfma_f32_16x16x32_bf16 a[244:247], v[172:175], a[188:191], a[244:247]\nds_write_addtid_b32 v131 offset:768\nv_cvt_pk_bf16_f32 v137, v146, v147\nv_mfma_f32_16x16x32_bf16 a[228:231], v[168:171], a[168:171], a[228:231]\nR 162,187,20480\n"
"v_cvt_pk_bf16_f32 v138, v148, v149\nv_mfma_f32_16x16x32_bf16 a[228:231], v[172:175], a[184:187], a[228:231]\nds_write_addtid_b32 v132 offset:1024\nv_cvt_pk_bf16_f32 v139, v150, v151\nM 20,168,164\nR 164,187,24576\n"
"v_cvt_pk_bf16_f32 v140, v152, v153\nM 20,172,180\nds_write_addtid_b32 v133 offset:1280\nv_cvt_pk_bf16_f32 v141, v154, v155\nM 4,168,160\nR 166,187,28672\n"
"v_cvt_pk_bf16_f32 v142, v156, v157\nM 4,172,176\nds_write_addtid_b32 v134 offset:1536\nG 9\nv_cvt_pk_bf16_f32 v143, v158, v159\nM 8,176,160\n"
"R 168,184,32768\nds_write_addtid_b32 v135 offset:1792\nM 8,180,176\nds_write_addtid_b32 v136 offset:2048\nds_write_addtid_b32 v137 offset:2304\nM 24,176,164\n"
"R 170,184,36864\nds_write_addtid_b32 v138 offset:2560\nM 24,180,180\nds_write_addtid_b32 v139 offset:2816\nds_write_addtid_b32 v140 offset:3072\nv_mfma_f32_16x16x32_bf16 a[232:235], v[176:179], a[168:171], a[232:235]\n"
"R 172,184,40960\nds_write_addtid_b32 v141 offset:3328\nv_mfma_f32_16x16x32_bf16 a[232:235], v[180:183], a[184:187], a[232:235]\nds_write_addtid_b32 v142 offset:3584\nds_write_addtid_b32 v143 offset:3840\nv_mfma_f32_16x16x32_bf16 a[248:251], v[176:179], a[172:175], a[248:251]\n"
"R 174,184,45056\ns_add_u32 m0, s50, 81920\nv_mfma_f32_16x16x32_bf16 a[248:251], v[180:183], a[188:191], a[248:251]\nG 14\nv_mfma_f32_16x16x32_bf16 a[252:255], v[160:163], a[172:175], a[252:255]\nR 176,185,32768\n"
"s_waitcnt vmcnt(11) lgkmcnt(2)\ns_barrier\nv_mfma_f32_16x16x32_bf16 a[252:255], v[164:167], a[188:191], a[252:255]\nbuffer_load_dwordx4 v221, s[16:19], s63 offen offset:1024 lds\nv_mfma_f32_16x16x32_bf16 a[236:239], v[160:163], a[168:171], a[236:239]\nR 178,185,36864\n"
"s_add_u32 s63, s59, 524288\nv_mfma_f32_16x16x32_bf16 a[236:239], v[164:167], a[184:187], a[236:239]\ns_add_u32 m0, s50, 90112\nds_read2st64_b32 v[128:129], v225 offset0:0 offset1:1\nM 28,160,164\nR 180,185,40960\n"
"M 28,164,180\nbuffer_load_dwordx4 v220, s[16:19], s63 offen lds\nM 12,160,160\nR 182,185,45056\nM 12,164,176\nds_read2st64_b32 v[130:131], v225 offset0:2 offset1:3\n"
"G 6\nM 0,168,192\nR 160,186,32768\nM 0,172,208\nbuffer_load_dwordx4 v221, s[16:19], s63 offen offset:1024 lds\nds_read2st64_b32 v[132:133], v225 offset0:4 offset1:5\n"
"M 16,168,196\nR 162,186,36864\ns_add_u32 s63, s59, 65536\nds_read2st64_b32 v[134:135], v225 offset0:6 offset1:7\nM 16,172,212\ns_add_u32 m0, s50, 98304\n"
"ds_read2st64_b32 v[136:137], v225 offset0:8 offset1:9\nv_mfma_f32_16x16x32_bf16 a[224:227], v[168:171], a[200:203], a[224:227]\nR 164,186,40960\nv_mfma_f32_16x16x32_bf16 a[224:227], v[172:175], a[216:219], a[224:227]\nbuffer_load_dwordx4 v220, s[16:19], s63 offen lds\nG 6\n"
"v_mfma_f32_16x16x32_bf16 v[96:99], v[208:211], v[128:131], v[96:99]\nv_mfma_f32_16x16x32_bf16 v[100:103], v[210:213], v[128:131], v[100:103]\nds_read2st64_b32 v[138:139], v225 offset0:10 offset1:11\nv_mfma_f32_16x16x32_bf16 a[240:243], v[168:171], a[204:207], a[240:243]\nR 166,186,45056\nv_mfma_f32_16x16x32_bf16 a[240:243], v[172:175], a[220:223], a[240:243]\n"
"ds_read2st64_b32 v[140:141], v225 offset0:12 offset1:13\nv_mfma_f32_16x16x32_bf16 a[244:247], v[176:179], a[204:207], a[244:247]\nR 168,187,32768\nG 6\nv_mfma_f32_16x16x32_bf16 v[104:107], v[208:211], v[132:135], v[104:107]\nv_mfma_f32_16x16x32_bf16 v[108:111], v[210:213], v[132:135], v[108:111]\n"
"ds_read2st64_b32 v[142:143], v225 offset0:14 offset1:15\nv_mfma_f32_16x16x32_bf16 a[244:247], v[180:183], a[220:223], a[244:247]\nbuffer_load_dwordx4 v221, s[16:19], s63 offen offset:1024 lds\nv_mfma_f32_16x16x32_bf16 a[228:231], v[176:179], a[200:203], a[228:231]\nR 170,187,36864\nv_mfma_f32_16x16x32_bf16 a[228:231], v[180:183], a[216:219], a[228:231]\n"
"s_add_u32 m0, s50, 106496\nG 5\nv_mfma_f32_16x16x32_bf16 v[112:115], v[208:211], v[136:139], v[112:115]\nv_mfma_f32_16x16x32_bf16 v[116:119], v[210:213], v[136:139], v[116:119]\nM 20,176,196\nR 172,187,40960\n"
"M 20,180,212\ns_add_u32 s63, s59, 589824\nM 4,176,192\nR 174,187,45056\nG 3\nv_mfma_f32_16x16x32_bf16 v[120:123], v[208:211], v[140:143], v[120:123]\n"
"v_mfma_f32_16x16x32_bf16 v[124:127], v[210:213], v[140:143], v[124:127]\nM 4,180,208\nM 8,160,192\nR 176,184,49152\nM 8,164,208\nbuffer_load_dwordx4 v220, s[16:19], s63 offen lds\n"
"M 24,160,196\nR 178,184,53248\nM 24,164,212\nv_mfma_f32_16x16x32_bf16 a[232:235], v[160:163], a[200:203], a[232:235]\nR 180,184,57344\nv_cvt_pk_bf16_f32 v120, v120, v121\n"
"v_cvt_pk_bf16_f32 v96, v96, v97\nv_cvt_pk_bf16_f32 v112, v112, v113\nv_cvt_pk_bf16_f32 v121, v122, v123\nv_mfma_f32_16x16x32_bf16 a[232:235], v[164:167], a[216:219], a[232:235]\nbuffer_load_dwordx4 v221, s[16:19], s63 offen offset:1024 lds\nv_cvt_pk_bf16_f32 v97, v98, v99\n"
"v_cvt_pk_bf16_f32 v113, v114, v115\nv_cvt_pk_bf16_f32 v122, v124, v125\nv_mfma_f32_16x16x32_bf16 a[248:251], v[160:163], a[204:207], a[248:251]\nR 182,184,61440\nv_cvt_pk_bf16_f32 v123, v126, v127\nv_cvt_pk_bf16_f32 v98, v100, v101\n"
"v_cvt_pk_bf16_f32 v114, v116, v117\nv_mfma_f32_16x16x32_bf16 a[248:251], v[164:167], a[220:223], a[248:251]\ns_bfe_u32 s100, s56, 0x20007\ns_mul_i32 s100, s100, 49152\ns_add_u32 s100, s100, s72\ns_add_u32 s100, s100, 52428800\n"
"G 4\nv_cvt_pk_bf16_f32 v99, v102, v103\nv_cvt_pk_bf16_f32 v115, v118, v119\nv_mfma_f32_16x16x32_bf16 a[252:255], v[168:171], a[204:207], a[252:255]\nR 160,185,49152\ns_add_u32 s63, s59, 131072\n"
"v_mfma_f32_16x16x32_bf16 a[252:255], v[172:175], a[220:223], a[252:255]\nbuffer_store_dwordx4 v[96:99], v252, s[20:23], s100 offen offset:0 sc1\nv_mfma_f32_16x16x32_bf16 a[236:239], v[168:171], a[200:203], a[236:239]\nR 162,185,53248\nv_cvt_pk_bf16_f32 v104, v104, v105\nv_mfma_f32_16x16x32_bf16 a[236:239], v[172:175], a[216:219], a[236:239]\n"
"s_add_u32 m0, s50, 114688\nv_cvt_pk_bf16_f32 v105, v106, v107\nv_cvt_pk_bf16_f32 v106, v108, v109\nM 28,168,196\nR 164,185,57344\nv_cvt_pk_bf16_f32 v107, v110, v111\n"
"M 28,172,212\nbuffer_load_dwordx4 v220, s[16:19], s63 offen lds\nM 12,168,192\nR 166,185,61440\nM 12,172,208\nbuffer_store_dwordx4 v[104:107], v252, s[20:23], s100 offen offset:1024 sc1\n"
"s_waitcnt vmcnt(12)\ns_barrier\nG 4\nM 32,176,160\nR 168,186,49152\nM 32,180,176\n"
"v_xor_b32 v240, 0x2000, v225\nM 48,176,164\nR 170,186,53248\nds_write2st64_b32 v240, a224, a225 offset0:0 offset1:1\nM 48,180,180\nbuffer_store_dwordx4 v[112:115], v252, s[20:23], s100 offen offset:2048 sc1\n"
"ds_write2st64_b32 v240, a226, a227 offset0:2 offset1:3\nM 64,176,168\nR 172,186,57344\nds_write2st64_b32 v240, a228, a229 offset0:4 offset1:5\nM 64,180,184\nds_write2st64_b32 v240, a230, a231 offset0:6 offset1:7\n"
"M 80,176,172\nR 174,186,61440\nds_write2st64_b32 v240, a232, a233 offset0:8 offset1:9\nM 80,180,188\nds_write2st64_b32 v240, a234, a235 offset0:10 offset1:11\nG 10\n"
"M 84,160,172\nR 176,187,49152\nds_write2st64_b32 v240, a236, a237 offset0:12 offset1:13\nM 84,164,188\nbuffer_store_dwordx4 v[120:123], v252, s[20:23], s100 offen offset:3072 sc1\nds_write2st64_b32 v240, a238, a239 offset0:14 offset1:15\n"
"M 68,160,168\nR 178,187,53248\nv_mfma_f32_16x16x32_bf16 v[96:99], v[208:211], v[192:195], 0\nds_write2st64_b32 v240, a240, a241 offset0:16 offset1:17\nM 68,164,184\nv_mfma_f32_16x16x32_bf16 v[100:103], v[210:213], v[192:195], 0\n"
"ds_write2st64_b32 v240, a242, a243 offset0:18 offset1:19\nM 52,160,164\nR 180,187,57344\nv_mfma_f32_16x16x32_bf16 v[104:107], v[208:211], v[196:199], 0\nds_write2st64_b32 v240, a244, a245 offset0:20 offset1:21\nM 52,164,180\n"
"v_mfma_f32_16x16x32_bf16 v[108:111], v[210:213], v[196:199], 0\nbuffer_load_dwordx4 v221, s[16:19], s63 offen offset:1024 lds\nds_write2st64_b32 v240, a246, a247 offset0:22 offset1:23\nM 36,160,160\nR 182,187,61440\nv_mfma_f32_16x16x32_bf16 v[112:115], v[208:211], v[200:203], 0\n"
"s_add_u32 m0, s50, 0\nds_write2st64_b32 v240, a248, a249 offset0:24 offset1:25\nM 36,164,176\nv_mfma_f32_16x16x32_bf16 v[116:119], v[210:213], v[200:203], 0\nds_write2st64_b32 v240, a250, a251 offset0:26 offset1:27\nG 14\n"
"M 40,168,160\nR 160,188,0\nv_mfma_f32_16x16x32_bf16 v[120:123], v[208:211], v[204:207], 0\ns_add_u32 s63, s59, 655360\nds_write2st64_b32 v240, a252, a253 offset0:28 offset1:29\nM 40,172,176\n"
"v_mfma_f32_16x16x32_bf16 v[124:127], v[210:213], v[204:207], 0\nbuffer_load_dwordx4 v220, s[16:19], s63 offen lds\nds_write2st64_b32 v240, a254, a255 offset0:30 offset1:31\nM 56,168,164\nR 162,188,4096\nv_mfma_f32_16x16x32_bf16 v[96:99], v[208:211], v[232:235], v[96:99]\n"
"M 56,172,180\nv_mfma_f32_16x16x32_bf16 v[100:103], v[210:213], v[232:235], v[100:103]\nbuffer_load_dwordx4 v221, s[16:19], s63 offen offset:1024 lds\nM 72,168,168\nR 164,188,8192\nv_mfma_f32_16x16x32_bf16 v[104:107], v[208:211], v[236:239], v[104:107]\n"
"s_add_u32 m0, s50, 8192\ns_add_u32 s63, s59, 196608\nM 72,172,184\nv_mfma_f32_16x16x32_bf16 v[108:111], v[210:213], v[236:239], v[108:111]\nbuffer_load_dwordx4 v220, s[16:19], s63 offen lds\nM 88,168,172\n"
"R 166,188,12288\nv_mfma_f32_16x16x32_bf16 v[112:115], v[208:211], v[232:235], v[112:115]\nM 88,172,188\nv_mfma_f32_16x16x32_bf16 v[116:119], v[210:213], v[232:235], v[116:119]\nG 8\nM 92,176,172\n"
"R 168,189,0\nv_mfma_f32_16x16x32_bf16 v[120:123], v[208:211], v[236:239], v[120:123]\ns_waitcnt vmcnt(13) lgkmcnt(4)\ns_barrier\nM 92,180,188\nv_mfma_f32_16x16x32_bf16 v[124:127], v[210:213], v[236:239], v[124:127]\n"
"buffer_load_dwordx4 v221, s[16:19], s63 offen offset:1024 lds\nds_read2st64_b32 v[128:129], v225 offset0:0 offset1:1\nM 76,176,168\nR 170,189,4096\ns_add_u32 s63, s59, 720896\nds_read2st64_b32 v[130:131], v225 offset0:2 offset1:3\n"
"M 76,180,184\ns_add_u32 m0, s50, 16384\nds_read2st64_b32 v[132:133], v225 offset0:4 offset1:5\nds_read2st64_b32 v[134:135], v225 offset0:6 offset1:7\nM 60,176,164\nR 172,189,8192\n"
"ds_read2st64_b32 v[136:137], v225 offset0:8 offset1:9\nM 60,180,180\nbuffer_load_dwordx4 v220, s[16:19], s63 offen lds\nds_read2st64_b32 v[138:139], v225 offset0:10 offset1:11\nM 44,176,160\nR 174,189,12288\n"
"ds_read2st64_b32 v[140:141], v225 offset0:12 offset1:13\nM 44,180,176\nds_read2st64_b32 v[142:143], v225 offset0:14 offset1:15\nds_read2st64_b32 v[144:145], v225 offset0:16 offset1:17\nG 13\nM 32,160,192\n"
"R 176,190,0\nds_read2st64_b32 v[146:147], v225 offset0:18 offset1:19\nG 13\nv_add_f32 v0, v0, v128\nM 32,164,208\nbuffer_load_dwordx4 v221, s[16:19], s63 offen offset:1024 lds\n"
"ds_read2st64_b32 v[148:149], v225 offset0:20 offset1:21\nM 48,160,196\nR 178,190,4096\ns_add_u32 s63, s59, 393216\nds_read2st64_b32 v[150:151], v225 offset0:22 offset1:23\nM 48,164,212\n"
"s_add_u32 m0, s50, 24576\nds_read2st64_b32 v[152:153], v225 offset0:24 offset1:25\nds_read2st64_b32 v[154:155], v225 offset0:26 offset1:27\nM 64,160,200\nR 180,190,8192\nds_read2st64_b32 v[156:157], v225 offset0:28 offset1:29\n"
"v_add_f32 v1, v1, v129\nM 64,164,216\nbuffer_load_dwordx4 v220, s[16:19], s63 offen lds\nds_read2st64_b32 v[158:159], v225 offset0:30 offset1:31\nM 80,160,204\nR 182,190,12288\n"
"G 15\nv_add_f32 v2, v2, v130\nv_add_f32 v3, v3, v131\nM 80,164,220\nG 14\nv_add_f32 v4, v4, v132\n"
"v_add_f32 v5, v5, v133\nv_add_f32 v6, v6, v134\nM 84,168,204\nR 160,191,0\nv_add_f32 v7, v7, v135\nv_add_f32 v8, v8, v136\n"
"M 84,172,220\nbuffer_load_dwordx4 v221, s[16:19], s63 offen offset:1024 lds\nv_add_f32 v9, v9, v137\nM 68,168,200\nR 162,191,4096\ns_add_u32 s63, s59, 458752\n"
"v_add_f32 v10, v10, v138\nM 68,172,216\ns_add_u32 m0, s50, 32768\nv_add_f32 v11, v11, v139\nG 15\nv_add_f32 v12, v12, v140\n"
"v_add_f32 v13, v13, v141\nM 52,168,196\nR 164,191,8192\nG 15\nv_add_f32 v14, v14, v142\nv_add_f32 v15, v15, v143\n"
"M 52,172,212\nbuffer_load_dwordx4 v220, s[16:19], s63 offen lds\nG 14\nv_add_f32 v16, v16, v144\nM 36,168,192\nR 166,191,12288\n"
"v_add_f32 v17, v17, v145\nG 13\nv_add_f32 v18, v18, v146\nM 36,172,208\nG 4\nv_add_f32 v19, v19, v147\n"
"v_add_f32 v20, v20, v148\nv_add_f32 v21, v21, v149\nM 40,176,192\nR 168,188,16384\nv_add_f32 v22, v22, v150\nv_add_f32 v23, v23, v151\n"
"M 40,180,208\nbuffer_load_dwordx4 v221, s[16:19], s63 offen offset:1024 lds\nv_add_f32 v24, v24, v152\nM 56,176,196\nR 170,188,20480\ns_add_u32 s63, s59, 262144\n"
"v_add_f32 v25, v25, v153\nM 56,180,212\ns_add_u32 m0, s50, 40960\nv_add_f32 v26, v26, v154\nv_add_f32 v27, v27, v155\nv_add_f32 v28, v28, v156\n"
"v_add_f32 v29, v29, v157\nv_add_f32 v30, v30, v158\nv_add_f32 v31, v31, v159\nM 72,176,200\nR 172,188,24576\nv_cvt_pk_bf16_f32 v24, v24, v25\n"
"v_cvt_pk_bf16_f32 v0, v0, v1\nv_cvt_pk_bf16_f32 v8, v8, v9\nv_cvt_pk_bf16_f32 v16, v16, v17\nv_cvt_pk_bf16_f32 v25, v26, v27\nM 72,180,216\nbuffer_load_dwordx4 v220, s[16:19], s63 offen lds\n"
"v_cvt_pk_bf16_f32 v9, v10, v11\nv_cvt_pk_bf16_f32 v17, v18, v19\nv_cvt_pk_bf16_f32 v26, v28, v29\nM 88,176,204\nR 174,188,28672\nv_cvt_pk_bf16_f32 v27, v30, v31\n"
"v_cvt_pk_bf16_f32 v1, v2, v3\nM 88,180,220\nG 4\nv_cvt_pk_bf16_f32 v2, v4, v5\nv_cvt_pk_bf16_f32 v3, v6, v7\nM 92,160,204\n"
"R 176,189,16384\ns_bfe_u32 s100, s57, 0x20007\ns_mul_i32 s100, s100, 49152\ns_add_u32 s100, s100, s72\ns_add_u32 s100, s100, 52445184\nv_cvt_pk_bf16_f32 v10, v12, v13\n"
"M 92,164,220\nbuffer_store_dwordx4 v[0:3], v252, s[20:23], s100 offen offset:0 sc1\nM 76,160,200\nR 178,189,20480\nv_cvt_pk_bf16_f32 v11, v14, v15\nM 76,164,216\n"
"buffer_load_dwordx4 v221, s[16:19], s63 offen offset:1024 lds\nM 60,160,196\nR 180,189,24576\ns_add_u32 m0, s50, 49152\ns_add_u32 s63, s59, 786432\nM 60,164,212\n"
"buffer_load_dwordx4 v220, s[16:19], s63 offen lds\nM 44,160,192\nR 182,189,28672\nv_cvt_pk_bf16_f32 v18, v20, v21\nM 44,164,208\nbuffer_store_dwordx4 v[8:11], v252, s[20:23], s100 offen offset:1024 sc1\n"
"s_waitcnt vmcnt(21)\ns_barrier\nG 4\nM 96,168,0\nR 160,190,16384\nv_cvt_pk_bf16_f32 v19, v22, v23\n"
"M 96,172,16\ns_mov_b32 m0, s51\nM 112,168,4\nLpg16:\ns_cbranch_execz LretW0z\nR 162,190,20480\n"
"v_cvt_pk_bf16_f32 v64, v64, v65\nv_cvt_pk_bf16_f32 v65, v66, v67\nM 112,172,20\nbuffer_store_dwordx4 v[16:19], v252, s[20:23], s100 offen offset:2048 sc1\nv_cvt_pk_bf16_f32 v66, v68, v69\nv_mfma_f32_16x16x32_bf16 v[128:131], v[168:171], a[8:11], 0\n"
"R 164,190,24576\nv_cvt_pk_bf16_f32 v67, v70, v71\nv_cvt_pk_bf16_f32 v68, v72, v73\nM 128,172,24\nds_write_addtid_b32 v64 offset:0\nv_cvt_pk_bf16_f32 v69, v74, v75\n"
"v_cvt_pk_bf16_f32 v70, v76, v77\nv_mfma_f32_16x16x32_bf16 v[144:147], v[168:171], a[12:15], 0\nR 166,190,28672\nv_cvt_pk_bf16_f32 v71, v78, v79\nv_cvt_pk_bf16_f32 v72, v80, v81\nM 144,172,28\n"
"ds_write_addtid_b32 v65 offset:256\nG 6\nv_cvt_pk_bf16_f32 v73, v82, v83\nv_mfma_f32_16x16x32_bf16 v[148:151], v[176:179], a[12:15], 0\nR 168,191,16384\nv_cvt_pk_bf16_f32 v74, v84, v85\n"
"v_cvt_pk_bf16_f32 v75, v86, v87\nM 148,180,28\nbuffer_store_dwordx4 v[24:27], v252, s[20:23], s100 offen offset:3072 sc1\nv_mfma_f32_16x16x32_bf16 v[132:135], v[176:179], a[8:11], 0\nR 170,191,20480\nv_cvt_pk_bf16_f32 v76, v88, v89\n"
"M 132,180,24\nds_write_addtid_b32 v66 offset:512\nv_cvt_pk_bf16_f32 v77, v90, v91\nM 116,176,4\nR 172,191,24576\nv_cvt_pk_bf16_f32 v78, v92, v93\n"
"M 116,180,20\nds_write_addtid_b32 v67 offset:768\nv_cvt_pk_bf16_f32 v79, v94, v95\nM 100,176,0\nR 174,191,28672\nds_write_addtid_b32 v68 offset:1024\n"
"M 100,180,16\nds_write_addtid_b32 v69 offset:1280\nds_write_addtid_b32 v70 offset:1536\nG 10\nM 104,160,0\nR 176,188,32768\n"
"ds_write_addtid_b32 v71 offset:1792\nM 104,164,16\nds_write_addtid_b32 v72 offset:2048\nds_write_addtid_b32 v73 offset:2304\nM 120,160,4\nR 178,188,36864\n"
"ds_write_addtid_b32 v74 offset:2560\nM 120,164,20\nds_write_addtid_b32 v75 offset:2816\nds_write_addtid_b32 v76 offset:3072\nv_mfma_f32_16x16x32_bf16 v[136:139], v[160:163], a[8:11], 0\nR 180,188,40960\n"
"ds_write_addtid_b32 v77 offset:3328\nM 136,164,24\nds_write_addtid_b32 v78 offset:3584\nds_write_addtid_b32 v79 offset:3840\nv_mfma_f32_16x16x32_bf16 v[152:155], v[160:163], a[12:15], 0\nR 182,188,45056\n"
"M 152,164,28\nG 15\nv_mfma_f32_16x16x32_bf16 v[156:159], v[168:171], a[12:15], 0\nR 160,189,32768\ns_waitcnt vmcnt(15) lgkmcnt(2)\ns_barrier\n"
"M 156,172,28\nds_read2st64_b32 v[64:65], v225 offset0:0 offset1:1\nv_mfma_f32_16x16x32_bf16 v[140:143], v[168:171], a[8:11], 0\nR 162,189,36864\nM 140,172,24\nds_read2st64_b32 v[66:67], v225 offset0:2 offset1:3\n"
"M 124,168,4\nR 164,189,40960\nM 124,172,20\nds_read2st64_b32 v[68:69], v225 offset0:4 offset1:5\nM 108,168,0\nR 166,189,45056\n"
"M 108,172,16\nds_read2st64_b32 v[70:71], v225 offset0:6 offset1:7\nG 8\nM 96,176,32\nR 168,190,32768\nv_bfe_u32 v250, v252, 4, 2\n"
"v_lshlrev_b32 v250, 3, v250\nM 96,180,48\ns_lshl_b32 s60, s43, 5\nv_add_u32 v250, s60, v250\nG 5\nv_mfma_f32_16x16x32_bf16 v[32:35], v[208:211], v[64:67], v[32:35]\n"
"v_mfma_f32_16x16x32_bf16 v[36:39], v[210:213], v[64:67], v[36:39]\nM 112,176,36\nR 170,190,36864\nM 112,180,52\nds_read2st64_b32 v[72:73], v225 offset0:8 offset1:9\nM 128,176,40\n"
"R 172,190,40960\nM 128,180,56\nds_read2st64_b32 v[74:75], v225 offset0:10 offset1:11\nG 5\nv_mfma_f32_16x16x32_bf16 v[40:43], v[208:211], v[68:71], v[40:43]\nv_mfma_f32_16x16x32_bf16 v[44:47], v[210:213], v[68:71], v[44:47]\n"
"M 144,176,44\nR 174,190,45056\nM 144,180,60\nds_read2st64_b32 v[76:77], v225 offset0:12 offset1:13\nM 148,160,44\nR 176,191,32768\n"
"ds_read2st64_b32 v[78:79], v225 offset0:14 offset1:15\nM 148,164,60\nM 132,160,40\nR 178,191,36864\nM 132,164,56\nG 5\n"
"v_mfma_f32_16x16x32_bf16 v[48:51], v[208:211], v[72:75], v[48:51]\nv_mfma_f32_16x16x32_bf16 v[52:55], v[210:213], v[72:75], v[52:55]\nM 116,160,36\nR 180,191,40960\nM 116,164,52\nM 100,160,32\n"
"R 182,191,45056\nG 3\nv_mfma_f32_16x16x32_bf16 v[56:59], v[208:211], v[76:79], v[56:59]\nv_mfma_f32_16x16x32_bf16 v[60:63], v[210:213], v[76:79], v[60:63]\nM 100,164,48\nM 104,168,32\n"
"R 160,188,49152\nM 104,172,48\nM 120,168,36\nR 162,188,53248\nv_mov_b32 v226, s46\nM 120,172,52\n"
"s_add_u32 m0, s50, 49152\nv_mov_b32 v227, s47\nM 136,168,40\nR 164,184,0\ns_bfe_u32 s62, s56, 0x20007\ns_lshl_b32 s62, s62, 5\n"
"s_add_u32 s61, s74, 548864\ns_add_u32 s61, s61, s62\ns_waitcnt vmcnt(0)\ns_mov_b64 exec, 1\nbuffer_atomic_swap_x2 v[226:227], off, s[20:23], s61 sc1\ns_mov_b64 exec, -1\n"
"v_cvt_pk_bf16_f32 v32, v32, v33\nv_cvt_pk_bf16_f32 v48, v48, v49\nv_cvt_pk_bf16_f32 v56, v56, v57\nM 136,172,56\ns_bfe_u32 s62, s57, 0x20007\ns_lshl_b32 s62, s62, 5\n"
"s_add_u32 s61, s74, 548872\ns_add_u32 s61, s61, s62\ns_mov_b64 exec, 1\nbuffer_atomic_swap_x2 v[226:227], off, s[20:23], s61 sc1\ns_mov_b64 exec, -1\nv_cvt_pk_bf16_f32 v49, v50, v51\n"
"v_cvt_pk_bf16_f32 v57, v58, v59\nM 152,168,44\nR 166,184,4096\nv_cvt_pk_bf16_f32 v50, v52, v53\nv_cvt_pk_bf16_f32 v33, v34, v35\nM 152,172,60\n"
"G 4\nv_cvt_pk_bf16_f32 v34, v36, v37\nv_cvt_pk_bf16_f32 v35, v38, v39\nM 156,176,44\nR 168,189,49152\ns_bfe_u32 s100, s58, 0x20007\n"
"s_mul_i32 s100, s100, 49152\ns_add_u32 s100, s100, s72\ns_add_u32 s100, s100, 52461568\nv_cvt_pk_bf16_f32 v40, v40, v41\nM 156,180,60\nbuffer_store_dwordx4 v[32:35], v252, s[20:23], s100 offen offset:0 sc1\n"
"buffer_load_dwordx4 v221, s[16:19], s63 offen offset:1024 lds\nM 140,176,40\nR 170,189,53248\ns_add_u32 m0, s50, 57344\ns_add_u32 s63, s59, 327680\nM 140,180,56\n"
"buffer_load_dwordx4 v220, s[16:19], s63 offen lds\nM 124,176,36\nR 172,185,0\nv_cvt_pk_bf16_f32 v41, v42, v43\nM 124,180,52\nv_cvt_pk_bf16_f32 v42, v44, v45\n"
"v_cvt_pk_bf16_f32 v43, v46, v47\nM 108,176,32\nR 174,185,4096\nv_cvt_pk_bf16_f32 v51, v54, v55\nM 108,180,48\nbuffer_store_dwordx4 v[40:43], v252, s[20:23], s100 offen offset:1024 sc1\n"
"s_barrier\nG 4\nM 96,160,64\nR 176,190,49152\nv_cvt_pk_bf16_f32 v58, v60, v61\nM 96,164,80\n"
"buffer_load_dwordx4 v221, s[16:19], s63 offen offset:1024 lds\nM 112,160,68\nR 178,190,53248\ns_add_u32 m0, s50, 65536\nM 112,164,84\nbuffer_store_dwordx4 v[48:51], v252, s[20:23], s100 offen offset:2048 sc1\n"
"M 128,160,72\nR 180,186,0\ns_add_u32 s63, s59, 851968\nM 128,164,88\nbuffer_load_dwordx4 v220, s[16:19], s63 offen lds\nM 144,160,76\n"
"R 182,186,4096\nv_cvt_pk_bf16_f32 v59, v62, v63\nM 144,164,92\nG 4\nM 148,168,76\nR 160,191,49152\n"
"Lq577:\ns_lshr_b32 s60, s45, 3\ns_lshl2_add_u32 s60, s60, s40\ns_lshl_b32 s60, s60, 8\nv_lshl_add_u32 v224, v252, 3, s60\nv_add_u32 v224, Lq577+4096-LqPC, v224\n"
"v_min_u32 v224, LqE-LqPC-4, v224\ns_mov_b64 exec, 3\nglobal_load_dword v224, v224, s[98:99]\ns_mov_b64 exec, -1\nM 148,172,92\nbuffer_store_dwordx4 v[56:59], v252, s[20:23], s100 offen offset:3072 sc1\n"
"buffer_load_dwordx4 v221, s[16:19], s63 offen offset:1024 lds\nM 132,168,72\nR 162,191,53248\nM 132,172,88\nM 116,168,68\nR 164,187,0\n"
"M 116,172,84\nM 100,168,64\nR 166,187,4096\nv_mov_b32 v248, 0\nM 100,172,80\nG 4\n"
"v_mov_b32 v249, 0\nM 104,176,64\nR 168,184,8192\ns_add_u32 s61, s74, 548864\ns_mov_b64 exec, 3\nbuffer_atomic_or_x2 v[248:249], v250, s[20:23], s61 offen sc0 sc1\n"
"s_mov_b64 exec, -1\nM 104,180,80\nM 120,176,68\nR 170,184,12288\nM 120,180,84\nM 136,176,72\n"
"R 172,184,16384\nM 136,180,88\nM 152,176,76\nR 174,184,20480\nM 152,180,92\nG 4\n"
"M 156,160,76\nR 176,185,8192\nM 156,164,92\nM 140,160,72\nR 178,185,12288\nM 140,164,88\n"
"M 124,160,68\nR 180,185,16384\nM 124,164,84\nM 108,160,64\nR 182,185,20480\nM 108,164,80\n"
"G 4\nM 96,168,96\nR 160,186,8192\nM 96,172,112\nM 112,168,100\nR 162,186,12288\n"
"M 112,172,116\nM 128,168,104\nR 164,186,16384\nM 128,172,120\nM 144,168,108\nR 166,186,20480\n"
"M 144,172,124\nG 4\nM 148,176,108\nR 168,187,8192\nM 148,180,124\nM 132,176,104\n"
"R 170,187,12288\nM 132,180,120\nM 116,176,100\nR 172,187,16384\nM 116,180,116\nM 100,176,96\n"
"R 174,187,20480\nM 100,180,112\nG 4\nM 104,160,96\nR 176,184,24576\ns_waitcnt vmcnt(9)\n"
"s_barrier\nM 104,164,112\nM 120,160,100\nR 178,184,28672\nM 120,164,116\nM 136,160,104\n"
"R 180,184,32768\nM 136,164,120\nM 152,160,108\nR 182,184,36864\nM 152,164,124\nG 4\n"
"M 156,168,108\nR 160,185,24576\nM 156,172,124\nM 140,168,104\nR 162,185,28672\nM 140,172,120\n"
"M 124,168,100\nR 164,185,32768\nM 124,172,116\nM 108,168,96\nR 166,185,36864\nv_mov_b32 v226, s46\n"
"M 108,172,112\nG 4\nv_mov_b32 v227, s47\nM 96,176,128\nR 168,186,24576\ns_bfe_u32 s62, s58, 0x20007\n"
"s_lshl_b32 s62, s62, 5\ns_add_u32 s61, s74, 548880\ns_add_u32 s61, s61, s62\ns_waitcnt vmcnt(0)\ns_mov_b64 exec, 1\nbuffer_atomic_swap_x2 v[226:227], off, s[20:23], s61 sc1\n"
"s_mov_b64 exec, -1\nM 96,180,144\ns_mul_i32 s64, s43, 49152\nM 112,176,132\nR 170,186,28672\nM 112,180,148\n"
"s_add_u32 s64, s64, s72\nM 128,176,136\nR 172,186,32768\nv_cmp_eq_u64 s[86:87], s[46:47], v[248:249]\ns_lshr_b32 s60, s45, 3\ns_lshl2_add_u32 s60, s60, s40\n"
"s_lshl_b32 s60, s60, 8\nv_lshl_add_u32 v224, v252, 3, s60\nv_add_u32 v224, LqM-LqPC, v224\nv_min_u32 v224, LqE-LqPC-4, v224\ns_mov_b64 exec, 3\nglobal_load_dword v224, v224, s[98:99]\n"
"s_mov_b64 exec, -1\nM 128,180,152\ns_add_u32 s64, s64, 52428800\nM 144,176,140\nR 174,186,36864\ns_and_b32 s62, s86, 3\n"
"s_cmp_eq_u32 s62, 3\ns_cbranch_scc0 Lq502\nLq503:\nM 144,180,156\nG 4\nM 148,160,140\n"
"R 176,187,24576\nM 148,164,156\nbuffer_load_dwordx4 v[0:3], v252, s[20:23], s64 offen offset:0 sc1\nM 132,160,136\nR 178,187,28672\nM 132,164,152\n"
"buffer_load_dwordx4 v[4:7], v252, s[20:23], s64 offen offset:1024 sc1\nM 116,160,132\nR 180,187,32768\nM 116,164,148\ns_mul_i32 s65, s43, 49152\nM 100,160,128\n"
"R 182,187,36864\nM 100,164,144\nbuffer_load_dwordx4 v[8:11], v252, s[20:23], s64 offen offset:2048 sc1\nG 4\nM 104,168,128\nR 160,184,40960\n"
"M 104,172,144\nbuffer_load_dwordx4 v[12:15], v252, s[20:23], s64 offen offset:3072 sc1\nM 120,168,132\nR 162,184,45056\nM 120,172,148\ns_add_u32 s65, s65, s72\n"
"M 136,168,136\nR 164,184,49152\ns_add_u32 s65, s65, 52445184\nM 136,172,152\nbuffer_load_dwordx4 v[16:19], v252, s[20:23], s65 offen offset:0 sc1\nM 152,168,140\n"
"R 166,184,53248\nM 152,172,156\nG 4\nM 156,176,140\nR 168,185,40960\ns_barrier\n"
"M 156,180,156\nbuffer_load_dwordx4 v[20:23], v252, s[20:23], s65 offen offset:1024 sc1\nM 140,176,136\nR 170,185,45056\nv_mov_b32 v230, 0\nM 140,180,152\n"
"buffer_load_dwordx4 v[24:27], v252, s[20:23], s65 offen offset:2048 sc1\nv_mov_b32 v231, 0\nM 124,176,132\nR 172,185,49152\ns_add_u32 s61, s74, 548864\ns_mov_b64 exec, 4\n"
"buffer_atomic_or_x2 v[230:231], v250, s[20:23], s61 offen sc0 sc1\ns_mov_b64 exec, -1\nM 124,180,148\nbuffer_load_dwordx4 v[28:31], v252, s[20:23], s65 offen offset:3072 sc1\nM 108,176,128\nR 174,185,53248\n"
"M 108,180,144\nG 4\nM 128,160,168\nR 176,186,40960\nM 128,164,184\nR 178,186,45056\n"
"M 144,160,172\nR 180,186,49152\nM 144,164,188\nR 182,186,53248\nG 4\nM 148,168,172\n"
"R 160,187,40960\ns_movk_i32 s63, 6144\nv_mad_u32_u24 v220, v228, s63, v229\nv_add_u32 v221, 3144704, v220\ns_add_u32 m0, s50, 73728\ns_add_u32 s63, s52, 2048\n"
"buffer_load_dwordx4 v220, s[4:7], s63 offen lds\nM 148,172,188\nR 162,187,45056\nM 132,168,168\nR 164,187,49152\nM 132,172,184\n"
"R 166,187,53248\nG 4\nM 136,176,168\nR 168,184,57344\nbuffer_load_dwordx4 v221, s[4:7], s63 offen offset:1024 lds\nM 136,180,184\n"
"R 170,184,61440\ns_add_u32 m0, s50, 81920\nM 152,176,172\nR 172,188,0\ns_add_u32 s63, s52, 198656\nM 152,180,188\n"
"R 174,188,4096\nG 4\nM 156,160,172\nR 176,185,57344\nbuffer_load_dwordx4 v220, s[4:7], s63 offen lds\nM 156,164,188\n"
"R 178,185,61440\nM 140,160,168\nR 180,189,0\nM 140,164,184\nR 182,189,4096\nG 4\n"
"M 128,168,200\nR 160,186,57344\nbuffer_load_dwordx4 v221, s[4:7], s63 offen offset:1024 lds\nM 128,172,216\nR 162,186,61440\ns_add_u32 m0, s50, 90112\n"
"M 144,168,204\nR 164,190,0\ns_add_u32 s63, s52, 4096\nM 144,172,220\nR 166,190,4096\nG 4\n"
"M 148,176,204\nR 168,187,57344\nbuffer_load_dwordx4 v220, s[4:7], s63 offen lds\nM 148,180,220\nR 170,187,61440\nM 132,176,200\n"
"R 172,191,0\nM 132,180,216\nR 174,191,4096\nG 4\nM 136,160,200\nR 176,184,40960\n"
"buffer_load_dwordx4 v221, s[4:7], s63 offen offset:1024 lds\nM 136,164,216\nR 178,184,45056\ns_add_u32 m0, s50, 98304\nM 152,160,204\nR 180,184,49152\n"
"s_add_u32 s63, s52, 200704\nM 152,164,220\nR 182,184,53248\nG 4\nM 156,168,204\nR 160,185,40960\n"
"buffer_load_dwordx4 v220, s[4:7], s63 offen lds\nM 156,172,220\nR 162,185,45056\nM 140,168,200\nR 164,185,49152\nM 140,172,216\n"
"R 166,185,53248\nG 4\nM 96,176,160\nR 168,186,40960\nbuffer_load_dwordx4 v221, s[4:7], s63 offen offset:1024 lds\nM 96,180,176\n"
"R 170,186,45056\ns_mov_b32 m0, s51\nM 112,176,164\nR 172,186,49152\nv_cvt_pk_bf16_f32 v128, v128, v129\nv_cvt_pk_bf16_f32 v129, v130, v131\n"
"M 112,180,180\nR 174,186,53248\nG 4\nv_cvt_pk_bf16_f32 v130, v132, v133\nM 116,160,164\nR 176,187,40960\n"
"s_mul_i32 s66, s43, 49152\nv_cvt_pk_bf16_f32 v131, v134, v135\nM 116,164,180\nR 178,187,45056\ns_add_u32 s66, s66, s72\nv_cvt_pk_bf16_f32 v132, v136, v137\n"
"M 100,160,160\nR 180,187,49152\ns_add_u32 s66, s66, 52461568\nv_cvt_pk_bf16_f32 v133, v138, v139\nM 100,164,176\nR 182,187,53248\n"
"G 4\nv_cvt_pk_bf16_f32 v134, v140, v141\nM 104,168,160\nR 160,184,57344\ns_waitcnt vmcnt(9)\nv_cmp_eq_u64 s[86:87], s[46:47], v[230:231]\n"
"v_cvt_pk_bf16_f32 v135, v142, v143\nM 104,172,176\nR 162,184,61440\nv_cvt_pk_bf16_f32 v136, v144, v145\nLpg17:\ns_cbranch_execz LretW0z\n"
"v_cvt_pk_bf16_f32 v137, v146, v147\nM 120,168,164\nR 164,188,0\ns_and_b32 s62, s86, 4\ns_cmp_eq_u32 s62, 4\ns_cbranch_scc0 Lq518\n"
"Lq519:\nv_cvt_pk_bf16_f32 v138, v148, v149\nM 120,172,180\nR 166,188,4096\nG 4\nv_cvt_pk_bf16_f32 v139, v150, v151\n"
"M 124,176,164\nR 168,185,57344\nbuffer_load_dwordx4 v[64:67], v252, s[20:23], s66 offen offset:0 sc1\nv_cvt_pk_bf16_f32 v140, v152, v153\nM 124,180,180\nR 170,185,61440\n"
"v_cvt_pk_bf16_f32 v141, v154, v155\nv_cvt_pk_bf16_f32 v142, v156, v157\nM 108,176,160\nR 172,189,0\nbuffer_load_dwordx4 v[68:71], v252, s[20:23], s66 offen offset:1024 sc1\nds_write_addtid_b32 v128 offset:0\n"
"ds_write_addtid_b32 v129 offset:256\nds_write_addtid_b32 v130 offset:512\nds_write_addtid_b32 v131 offset:768\nds_write_addtid_b32 v132 offset:1024\nds_write_addtid_b32 v133 offset:1280\nds_write_addtid_b32 v134 offset:1536\n"
"ds_write_addtid_b32 v135 offset:1792\nds_write_addtid_b32 v136 offset:2048\nds_write_addtid_b32 v137 offset:2304\nds_write_addtid_b32 v138 offset:2560\nds_write_addtid_b32 v139 offset:2816\nds_write_addtid_b32 v140 offset:3072\n"
"ds_write_addtid_b32 v141 offset:3328\nds_write_addtid_b32 v142 offset:3584\nv_cvt_pk_bf16_f32 v143, v158, v159\nds_write_addtid_b32 v143 offset:3840\nM 108,180,176\nR 174,189,4096\n"
"s_add_u32 m0, s50, 106496\ns_add_u32 s63, s52, 395264\nG 15\nM 96,160,192\nR 176,186,57344\nbuffer_load_dwordx4 v220, s[4:7], s63 offen lds\n"
"M 96,164,208\nR 178,186,61440\nbuffer_load_dwordx4 v[72:75], v252, s[20:23], s66 offen offset:2048 sc1\nG 3\ns_barrier\nM 112,160,196\n"
"R 180,190,0\nds_read2st64_b32 v[128:129], v225 offset0:0 offset1:1\nM 112,164,212\nR 182,190,4096\nds_read2st64_b32 v[130:131], v225 offset0:2 offset1:3\nG 6\n"
"M 116,168,196\nR 160,187,57344\nbuffer_load_dwordx4 v[76:79], v252, s[20:23], s66 offen offset:3072 sc1\nds_read2st64_b32 v[132:133], v225 offset0:4 offset1:5\nM 116,172,212\nR 162,187,61440\n"
"buffer_load_dwordx4 v221, s[4:7], s63 offen offset:1024 lds\nds_read2st64_b32 v[134:135], v225 offset0:6 offset1:7\nM 100,168,192\nR 164,191,0\ns_add_u32 m0, s50, 114688\ns_add_u32 s63, s52, 591872\n"
"ds_read2st64_b32 v[136:137], v225 offset0:8 offset1:9\nM 100,172,208\nR 166,191,4096\nds_read2st64_b32 v[138:139], v225 offset0:10 offset1:11\nG 9\nM 104,176,192\n"
"buffer_load_dwordx4 v220, s[4:7], s63 offen lds\nds_read2st64_b32 v[140:141], v225 offset0:12 offset1:13\nM 104,180,208\nds_read2st64_b32 v[142:143], v225 offset0:14 offset1:15\nM 120,176,196\nbuffer_load_dwordx4 v221, s[4:7], s63 offen offset:1024 lds\n"
"M 120,180,212\nG 3\nM 124,160,196\nM 124,164,212\nM 108,160,192\nM 108,164,208\n"
"G 0\ns_barrier\nv_mfma_f32_16x16x32_bf16 v[96:99], v[208:211], v[128:131], v[96:99]\nv_mfma_f32_16x16x32_bf16 v[100:103], v[210:213], v[128:131], v[100:103]\nv_mfma_f32_16x16x32_bf16 v[104:107], v[208:211], v[132:135], v[104:107]\nv_mfma_f32_16x16x32_bf16 v[108:111], v[210:213], v[132:135], v[108:111]\n"
"v_mfma_f32_16x16x32_bf16 v[112:115], v[208:211], v[136:139], v[112:115]\nv_mfma_f32_16x16x32_bf16 v[116:119], v[210:213], v[136:139], v[116:119]\nv_mfma_f32_16x16x32_bf16 v[120:123], v[208:211], v[140:143], v[120:123]\nv_mfma_f32_16x16x32_bf16 v[124:127], v[210:213], v[140:143], v[124:127]\ns_lshl_b32 s84, s75, 11\ns_add_u32 s84, s84, s49\n"
"s_add_u32 s84, s84, 18874368\ns_add_u32 s85, s84, 32768\nv_mov_b32 v238, s46\nv_mov_b32 v239, s47\nv_mfma_f32_16x16x32_bf16 v[96:99], v[208:211], v[0:3], v[96:99]\nv_mfma_f32_16x16x32_bf16 v[100:103], v[210:213], v[0:3], v[100:103]\n"
"v_mfma_f32_16x16x32_bf16 v[104:107], v[208:211], v[4:7], v[104:107]\nv_mfma_f32_16x16x32_bf16 v[108:111], v[210:213], v[4:7], v[108:111]\nv_mfma_f32_16x16x32_bf16 v[112:115], v[208:211], v[8:11], v[112:115]\nv_mfma_f32_16x16x32_bf16 v[116:119], v[210:213], v[8:11], v[116:119]\nv_mfma_f32_16x16x32_bf16 v[120:123], v[208:211], v[12:15], v[120:123]\nv_mfma_f32_16x16x32_bf16 v[124:127], v[210:213], v[12:15], v[124:127]\n"
"v_mfma_f32_16x16x32_bf16 v[96:99], v[208:211], v[16:19], v[96:99]\nv_mfma_f32_16x16x32_bf16 v[100:103], v[210:213], v[16:19], v[100:103]\nv_mfma_f32_16x16x32_bf16 v[104:107], v[208:211], v[20:23], v[104:107]\nv_mfma_f32_16x16x32_bf16 v[108:111], v[210:213], v[20:23], v[108:111]\nv_mfma_f32_16x16x32_bf16 v[112:115], v[208:211], v[24:27], v[112:115]\nv_mfma_f32_16x16x32_bf16 v[116:119], v[210:213], v[24:27], v[116:119]\n"
"s_waitcnt vmcnt(16)\nv_mfma_f32_16x16x32_bf16 v[120:123], v[208:211], v[28:31], v[120:123]\nv_mfma_f32_16x16x32_bf16 v[124:127], v[210:213], v[28:31], v[124:127]\ns_waitcnt vmcnt(7)\nv_mfma_f32_16x16x32_bf16 v[96:99], v[208:211], v[64:67], v[96:99]\nv_mfma_f32_16x16x32_bf16 v[100:103], v[210:213], v[64:67], v[100:103]\n"
"s_waitcnt vmcnt(6)\nv_mfma_f32_16x16x32_bf16 v[104:107], v[208:211], v[68:71], v[104:107]\nv_mfma_f32_16x16x32_bf16 v[108:111], v[210:213], v[68:71], v[108:111]\ns_waitcnt vmcnt(4)\nv_mfma_f32_16x16x32_bf16 v[112:115], v[208:211], v[72:75], v[112:115]\nv_mfma_f32_16x16x32_bf16 v[116:119], v[210:213], v[72:75], v[116:119]\n"
"s_waitcnt vmcnt(3)\nv_mfma_f32_16x16x32_bf16 v[120:123], v[208:211], v[76:79], v[120:123]\nv_mfma_f32_16x16x32_bf16 v[124:127], v[210:213], v[76:79], v[124:127]\nv_cvt_pk_bf16_f32 v192, v96, v97\nv_cvt_pk_bf16_f32 v193, v98, v99\nv_cvt_pk_bf16_f32 v194, v100, v101\n"
"v_cvt_pk_bf16_f32 v195, v102, v103\nbuffer_store_dwordx4 v[192:195], v252, s[20:23], s84 offen offset:0 sc1\nv_cvt_pk_bf16_f32 v196, v104, v105\nv_cvt_pk_bf16_f32 v197, v106, v107\nv_cvt_pk_bf16_f32 v198, v108, v109\nv_cvt_pk_bf16_f32 v199, v110, v111\n"
"buffer_store_dwordx4 v[196:199], v252, s[20:23], s84 offen offset:1024 sc1\nv_cvt_pk_bf16_f32 v200, v112, v113\nv_cvt_pk_bf16_f32 v201, v114, v115\nv_cvt_pk_bf16_f32 v202, v116, v117\nv_cvt_pk_bf16_f32 v203, v118, v119\nbuffer_store_dwordx4 v[200:203], v252, s[20:23], s85 offen offset:0 sc1\n"
"v_cvt_pk_bf16_f32 v204, v120, v121\nv_cvt_pk_bf16_f32 v205, v122, v123\nv_cvt_pk_bf16_f32 v206, v124, v125\nv_cvt_pk_bf16_f32 v207, v126, v127\nbuffer_store_dwordx4 v[204:207], v252, s[20:23], s85 offen offset:1024 sc1\ns_mov_b64 exec, 0\n"
"s_cmp_eq_u32 s40, 1\ns_cbranch_scc1 Lpg19\ns_cmp_eq_u32 s40, 2\ns_cbranch_scc1 Lpg20\ns_cmp_eq_u32 s40, 3\ns_cbranch_scc1 Lpg21\n"
"s_branch Lpg18\nLretW0n:\ns_mov_b64 exec, -1\ns_branch LqM\nLq116:\ns_lshl_b32 s61, s42, 13\n"
"s_add_u32 s61, s61, 84017152\nbuffer_load_dwordx4 v[128:131], v230, s[20:23], s61 offen sc1\ns_lshl_b32 s61, s42, 13\ns_add_u32 s61, s61, 84021248\nbuffer_load_dwordx4 v[132:135], v230, s[20:23], s61 offen sc1\ns_waitcnt vmcnt(0)\n"
"v_min3_u32 v232, v129, v131, v133\nv_min3_u32 v232, v232, v135, v135\nv_max3_u32 v233, v129, v131, v133\nv_max3_u32 v233, v233, v135, v135\nv_cmp_ne_u32 s[86:87], s46, v232\nv_cmp_ne_u32 s[88:89], s46, v233\n"
"s_nop 0\ns_or_b64 s[86:87], s[86:87], s[88:89]\ns_cbranch_scc0 Lq117\ns_sub_u32 s48, s48, 1\ns_cmp_gt_i32 s48, 0\ns_cbranch_scc1 Lq116\n"
"s_branch Lq117\nLq124:\ns_lshl_b32 s61, s42, 13\ns_add_u32 s61, s61, 84148224\nbuffer_load_dwordx4 v[144:147], v234, s[20:23], s61 offen sc1\ns_lshl_b32 s61, s42, 13\n"
"s_add_u32 s61, s61, 84152320\nbuffer_load_dwordx4 v[148:151], v234, s[20:23], s61 offen sc1\ns_waitcnt vmcnt(0)\nv_min3_u32 v236, v145, v147, v149\nv_min3_u32 v236, v236, v151, v151\nv_max3_u32 v237, v145, v147, v149\n"
"v_max3_u32 v237, v237, v151, v151\nv_cmp_ne_u32 s[86:87], s46, v236\nv_cmp_ne_u32 s[88:89], s46, v237\ns_nop 0\ns_or_b64 s[86:87], s[86:87], s[88:89]\ns_cbranch_scc0 Lq125\n"
"s_sub_u32 s48, s48, 1\ns_cmp_gt_i32 s48, 0\ns_cbranch_scc1 Lq124\ns_branch Lq125\nLq132:\ns_lshl_b32 s61, s42, 13\n"
"s_add_u32 s61, s61, 83886080\nbuffer_load_dwordx4 v[128:131], v230, s[20:23], s61 offen sc1\ns_lshl_b32 s61, s42, 13\ns_add_u32 s61, s61, 83890176\nbuffer_load_dwordx4 v[132:135], v230, s[20:23], s61 offen sc1\ns_waitcnt vmcnt(0)\n"
"v_min3_u32 v232, v129, v131, v133\nv_min3_u32 v232, v232, v135, v135\nv_max3_u32 v233, v129, v131, v133\nv_max3_u32 v233, v233, v135, v135\nv_cmp_ne_u32 s[86:87], s46, v232\nv_cmp_ne_u32 s[88:89], s46, v233\n"
"s_nop 0\ns_or_b64 s[86:87], s[86:87], s[88:89]\ns_cbranch_scc0 Lq133\ns_sub_u32 s48, s48, 1\ns_cmp_gt_i32 s48, 0\ns_cbranch_scc1 Lq132\n"
"s_branch Lq133\nLq140:\ns_lshr_b32 s84, s49, 4\ns_add_u32 s84, s84, 81788928\nbuffer_load_dwordx4 v[128:131], v252, s[20:23], s84 offen offset:0 sc1\nbuffer_load_dwordx4 v[132:135], v252, s[20:23], s84 offen offset:1024 sc1\n"
"buffer_load_dwordx4 v[136:139], v252, s[20:23], s84 offen offset:2048 sc1\nbuffer_load_dwordx4 v[140:143], v252, s[20:23], s84 offen offset:3072 sc1\ns_mov_b32 s85, 0x0c0c0400\ns_waitcnt vmcnt(3)\nv_perm_b32 v144, v129, v128, s85\nv_perm_b32 v148, v131, v130, s85\n"
"v_lshl_or_b32 v144, v148, 16, v144\ns_waitcnt vmcnt(2)\nv_perm_b32 v145, v133, v132, s85\nv_perm_b32 v149, v135, v134, s85\nv_lshl_or_b32 v145, v149, 16, v145\ns_waitcnt vmcnt(1)\n"
"v_perm_b32 v146, v137, v136, s85\nv_perm_b32 v150, v139, v138, s85\nv_lshl_or_b32 v146, v150, 16, v146\ns_waitcnt vmcnt(0)\nv_perm_b32 v147, v141, v140, s85\nv_perm_b32 v151, v143, v142, s85\n"
"v_lshl_or_b32 v147, v151, 16, v147\nv_min3_u32 v230, v144, v145, v146\nv_min3_u32 v230, v230, v147, v147\nv_max3_u32 v231, v144, v145, v146\nv_max3_u32 v231, v231, v147, v147\nv_cmp_ne_u32 s[86:87], s46, v230\n"
"v_cmp_ne_u32 s[88:89], s46, v231\ns_nop 0\ns_or_b64 s[86:87], s[86:87], s[88:89]\ns_cbranch_scc0 Lq141\ns_sub_u32 s48, s48, 1\ns_cmp_gt_i32 s48, 0\n"
"s_cbranch_scc1 Lq140\ns_branch Lq141\nLq187:\ns_mov_b64 exec, s[66:67]\nbuffer_load_dwordx4 v[230:233], v252, s[20:23], s65 offen offset:0 sc1\nbuffer_load_dwordx2 v[64:65], v227, s[20:23], s65 offen offset:2048 sc1\n"
"buffer_load_dwordx2 v[70:71], v227, s[20:23], s65 offen offset:2560 sc1\ns_mov_b64 exec, -1\ns_waitcnt vmcnt(0)\nv_max3_u32 v248, v233, v65, v71\nv_cmp_eq_u32 vcc, -1, v248\ns_nop 1\n"
"s_cbranch_vccz Lq188\ns_sub_u32 s48, s48, 1\ns_cmp_gt_i32 s48, 0\ns_cbranch_scc1 Lq187\ns_branch Lq188\nLq189:\n"
"s_mov_b64 exec, s[66:67]\nbuffer_load_dwordx4 v[234:237], v252, s[20:23], s65 offen offset:1024 sc1\nbuffer_load_dwordx2 v[76:77], v227, s[20:23], s65 offen offset:3072 sc1\nbuffer_load_dwordx2 v[82:83], v227, s[20:23], s65 offen offset:3584 sc1\ns_mov_b64 exec, -1\ns_waitcnt vmcnt(0)\n"
"v_max3_u32 v248, v237, v77, v83\nv_cmp_eq_u32 vcc, -1, v248\ns_nop 1\ns_cbranch_vccz Lq190\ns_sub_u32 s48, s48, 1\ns_cmp_gt_i32 s48, 0\n"
"s_cbranch_scc1 Lq189\ns_branch Lq190\nLq313:\ns_lshr_b32 s84, s49, 4\ns_add_u32 s84, s84, 82313216\nbuffer_load_dwordx4 v[0:3], v252, s[20:23], s84 offen offset:0 sc1\n"
"buffer_load_dwordx4 v[4:7], v252, s[20:23], s84 offen offset:1024 sc1\nbuffer_load_dwordx4 v[8:11], v252, s[20:23], s84 offen offset:2048 sc1\nbuffer_load_dwordx4 v[12:15], v252, s[20:23], s84 offen offset:3072 sc1\ns_mov_b32 s85, 0x0c0c0400\ns_waitcnt vmcnt(3)\nv_perm_b32 v16, v1, v0, s85\n"
"v_perm_b32 v20, v3, v2, s85\nv_lshl_or_b32 v16, v20, 16, v16\ns_waitcnt vmcnt(2)\nv_perm_b32 v17, v5, v4, s85\nv_perm_b32 v21, v7, v6, s85\nv_lshl_or_b32 v17, v21, 16, v17\n"
"s_waitcnt vmcnt(1)\nv_perm_b32 v18, v9, v8, s85\nv_perm_b32 v22, v11, v10, s85\nv_lshl_or_b32 v18, v22, 16, v18\ns_waitcnt vmcnt(0)\nv_perm_b32 v19, v13, v12, s85\n"
"v_perm_b32 v23, v15, v14, s85\nv_lshl_or_b32 v19, v23, 16, v19\nv_min3_u32 v230, v16, v17, v18\nv_min3_u32 v230, v230, v19, v19\nv_max3_u32 v231, v16, v17, v18\nv_max3_u32 v231, v231, v19, v19\n"
"v_cmp_ne_u32 s[86:87], s46, v230\nv_cmp_ne_u32 s[88:89], s46, v231\ns_nop 0\ns_or_b64 s[86:87], s[86:87], s[88:89]\ns_cbranch_scc0 Lq314\ns_sub_u32 s48, s48, 1\n"
"s_cmp_gt_i32 s48, 0\ns_cbranch_scc1 Lq313\ns_branch Lq314\nLq315:\ns_lshl_b32 s61, s42, 13\ns_add_u32 s61, s61, 84279296\n"
"buffer_load_dwordx4 v[0:3], v238, s[20:23], s61 offen sc1\ns_lshl_b32 s61, s42, 13\ns_add_u32 s61, s61, 84283392\nbuffer_load_dwordx4 v[4:7], v238, s[20:23], s61 offen sc1\ns_waitcnt vmcnt(0)\nv_min3_u32 v240, v1, v3, v5\n"
"v_min3_u32 v240, v240, v7, v7\nv_max3_u32 v241, v1, v3, v5\nv_max3_u32 v241, v241, v7, v7\nv_cmp_ne_u32 s[86:87], s46, v240\nv_cmp_ne_u32 s[88:89], s46, v241\ns_nop 0\n"
"s_or_b64 s[86:87], s[86:87], s[88:89]\ns_cbranch_scc0 Lq316\ns_sub_u32 s48, s48, 1\ns_cmp_gt_i32 s48, 0\ns_cbranch_scc1 Lq315\ns_branch Lq316\n"
"Lq325:\ns_lshl_b32 s61, s42, 13\ns_add_u32 s61, s61, 84410368\nbuffer_load_dwordx4 v[16:19], v242, s[20:23], s61 offen sc1\ns_lshl_b32 s61, s42, 13\ns_add_u32 s61, s61, 84414464\n"
"buffer_load_dwordx4 v[20:23], v242, s[20:23], s61 offen sc1\ns_waitcnt vmcnt(0)\nv_min3_u32 v244, v17, v19, v21\nv_min3_u32 v244, v244, v23, v23\nv_max3_u32 v245, v17, v19, v21\nv_max3_u32 v245, v245, v23, v23\n"
"v_cmp_ne_u32 s[86:87], s46, v244\nv_cmp_ne_u32 s[88:89], s46, v245\ns_nop 0\ns_or_b64 s[86:87], s[86:87], s[88:89]\ns_cbranch_scc0 Lq326\ns_sub_u32 s48, s48, 1\n"
"s_cmp_gt_i32 s48, 0\ns_cbranch_scc1 Lq325\ns_branch Lq326\nLq335:\ns_lshl_b32 s61, s42, 13\ns_add_u32 s61, s61, 84541440\n"
"buffer_load_dwordx4 v[0:3], v238, s[20:23], s61 offen sc1\ns_lshl_b32 s61, s42, 13\ns_add_u32 s61, s61, 84545536\nbuffer_load_dwordx4 v[4:7], v238, s[20:23], s61 offen sc1\ns_waitcnt vmcnt(0)\nv_min3_u32 v240, v1, v3, v5\n"
"v_min3_u32 v240, v240, v7, v7\nv_max3_u32 v241, v1, v3, v5\nv_max3_u32 v241, v241, v7, v7\nv_cmp_ne_u32 s[86:87], s46, v240\nv_cmp_ne_u32 s[88:89], s46, v241\ns_nop 0\n"
"s_or_b64 s[86:87], s[86:87], s[88:89]\ns_cbranch_scc0 Lq336\ns_sub_u32 s48, s48, 1\ns_cmp_gt_i32 s48, 0\ns_cbranch_scc1 Lq335\ns_branch Lq336\n"
"Lq345:\ns_lshl_b32 s61, s42, 13\ns_add_u32 s61, s61, 84672512\nbuffer_load_dwordx4 v[16:19], v242, s[20:23], s61 offen sc1\ns_lshl_b32 s61, s42, 13\ns_add_u32 s61, s61, 84676608\n"
"buffer_load_dwordx4 v[20:23], v242, s[20:23], s61 offen sc1\ns_waitcnt vmcnt(0)\nv_min3_u32 v244, v17, v19, v21\nv_min3_u32 v244, v244, v23, v23\nv_max3_u32 v245, v17, v19, v21\nv_max3_u32 v245, v245, v23, v23\n"
"v_cmp_ne_u32 s[86:87], s46, v244\nv_cmp_ne_u32 s[88:89], s46, v245\ns_nop 0\ns_or_b64 s[86:87], s[86:87], s[88:89]\ns_cbranch_scc0 Lq346\ns_sub_u32 s48, s48, 1\n"
"s_cmp_gt_i32 s48, 0\ns_cbranch_scc1 Lq345\ns_branch Lq346\nLq373:\nv_mov_b32 v248, 0\nv_mov_b32 v249, 0\n"
"s_add_u32 s61, s82, 385024\ns_mov_b64 exec, 0xf\nbuffer_atomic_or_x2 v[248:249], v250, s[20:23], s61 offen sc0 sc1\ns_mov_b64 exec, -1\ns_waitcnt vmcnt(0)\nv_cmp_eq_u64 s[86:87], s[46:47], v[248:249]\n"
"s_nop 1\ns_and_b32 s62, s86, 0xf\ns_cmp_eq_u32 s62, 0xf\ns_cbranch_scc1 Lq374\ns_sleep 1\ns_sub_u32 s48, s48, 1\n"
"s_cmp_gt_i32 s48, 0\ns_cbranch_scc1 Lq373\ns_branch Lq374\nLq408:\nv_mov_b32 v248, 0\nv_mov_b32 v249, 0\n"
"s_add_u32 s61, s82, 385032\ns_mov_b64 exec, 0xf\nbuffer_atomic_or_x2 v[248:249], v250, s[20:23], s61 offen sc0 sc1\ns_mov_b64 exec, -1\ns_waitcnt vmcnt(0)\nv_cmp_eq_u64 s[86:87], s[46:47], v[248:249]\n"
"s_nop 1\ns_and_b32 s62, s86, 0xf\ns_cmp_eq_u32 s62, 0xf\ns_cbranch_scc1 Lq409\ns_sleep 1\ns_sub_u32 s48, s48, 1\n"
"s_cmp_gt_i32 s48, 0\ns_cbranch_scc1 Lq408\ns_branch Lq409\nLq443:\nv_mov_b32 v248, 0\nv_mov_b32 v249, 0\n"
"s_add_u32 s61, s82, 385048\ns_mov_b64 exec, 0xf\nbuffer_atomic_or_x2 v[248:249], v250, s[20:23], s61 offen sc0 sc1\ns_mov_b64 exec, -1\ns_waitcnt vmcnt(0)\nv_cmp_eq_u64 s[86:87], s[46:47], v[248:249]\n"
"s_nop 1\ns_and_b32 s62, s86, 0xf\ns_cmp_eq_u32 s62, 0xf\ns_cbranch_scc1 Lq444\ns_sleep 1\ns_sub_u32 s48, s48, 1\n"
"s_cmp_gt_i32 s48, 0\ns_cbranch_scc1 Lq443\ns_branch Lq444\nLq462:\nv_mov_b32 v248, 0\nv_mov_b32 v249, 0\n"
"s_add_u32 s61, s82, 385040\ns_mov_b64 exec, 0xf\nbuffer_atomic_or_x2 v[248:249], v250, s[20:23], s61 offen sc0 sc1\ns_mov_b64 exec, -1\ns_waitcnt vmcnt(0)\nv_cmp_eq_u64 s[86:87], s[46:47], v[248:249]\n"
"s_nop 1\ns_and_b32 s62, s86, 0xf\ns_cmp_eq_u32 s62, 0xf\ns_cbranch_scc1 Lq463\ns_sleep 1\ns_sub_u32 s48, s48, 1\n"
"s_cmp_gt_i32 s48, 0\ns_cbranch_scc1 Lq462\ns_branch Lq463\nLq502:\nv_mov_b32 v248, 0\nv_mov_b32 v249, 0\n"
"s_add_u32 s61, s74, 548864\ns_mov_b64 exec, 3\nbuffer_atomic_or_x2 v[248:249], v250, s[20:23], s61 offen sc0 sc1\ns_mov_b64 exec, -1\ns_waitcnt vmcnt(0)\nv_cmp_eq_u64 s[86:87], s[46:47], v[248:249]\n"
"s_nop 1\ns_and_b32 s62, s86, 3\ns_cmp_eq_u32 s62, 3\ns_cbranch_scc1 Lq503\ns_sleep 1\ns_sub_u32 s48, s48, 1\n"
"s_cmp_gt_i32 s48, 0\ns_cbranch_scc1 Lq502\ns_branch Lq503\nLq518:\nv_mov_b32 v230, 0\nv_mov_b32 v231, 0\n"
"s_add_u32 s61, s74, 548864\ns_mov_b64 exec, 4\nbuffer_atomic_or_x2 v[230:231], v250, s[20:23], s61 offen sc0 sc1\ns_mov_b64 exec, -1\ns_waitcnt vmcnt(0)\nv_cmp_eq_u64 s[86:87], s[46:47], v[230:231]\n"
"s_nop 1\ns_and_b32 s62, s86, 4\ns_cmp_eq_u32 s62, 4\ns_cbranch_scc1 Lq519\ns_sleep 1\ns_sub_u32 s48, s48, 1\n"
"s_cmp_gt_i32 s48, 0\ns_cbranch_scc1 Lq518\ns_branch Lq519\nLqM:\ns_add_u32 s61, s79, 88358912\ns_lshl_b32 s62, s75, 3\n"
"s_add_u32 s61, s61, s62\ns_waitcnt vmcnt(0)\ns_mov_b64 exec, 1\nbuffer_atomic_swap_x2 v[238:239], off, s[20:23], s61 sc1\ns_mov_b64 exec, -1\nLqq1:\n"
"v_bfe_u32 v250, v252, 7, 1\nv_mul_u32_u24 v250, 2176, v250\nv_bfe_u32 v251, v252, 4, 3\nv_lshl_add_u32 v250, v251, 3, v250\ns_lshl_b32 s60, s90, 6\nv_add_u32 v250, s60, v250\n"
"s_sleep 5\nv_mov_b32 v248, 0\nv_mov_b32 v249, 0\ns_add_u32 s61, s80, 88358912\ns_mov_b64 exec, 0xffff\nbuffer_atomic_or_x2 v[248:249], v250, s[20:23], s61 offen sc0 sc1\n"
"s_mov_b64 exec, -1\ns_add_u32 m0, s50, 0\ns_add_u32 s63, s52, 397312\nbuffer_load_dwordx4 v220, s[4:7], s63 offen lds\nbuffer_load_dwordx4 v221, s[4:7], s63 offen offset:1024 lds\ns_add_u32 m0, s50, 8192\n"
"s_add_u32 s63, s52, 593920\nbuffer_load_dwordx4 v220, s[4:7], s63 offen lds\nbuffer_load_dwordx4 v221, s[4:7], s63 offen offset:1024 lds\ns_add_u32 m0, s50, 16384\ns_add_u32 s63, s52, 788480\nbuffer_load_dwordx4 v220, s[4:7], s63 offen lds\n"
"buffer_load_dwordx4 v221, s[4:7], s63 offen offset:1024 lds\ns_add_u32 m0, s50, 24576\ns_add_u32 s63, s52, 985088\nbuffer_load_dwordx4 v220, s[4:7], s63 offen lds\nbuffer_load_dwordx4 v221, s[4:7], s63 offen offset:1024 lds\ns_add_u32 m0, s50, 32768\n"
"s_add_u32 s63, s52, 790528\nbuffer_load_dwordx4 v220, s[4:7], s63 offen lds\nbuffer_load_dwordx4 v221, s[4:7], s63 offen offset:1024 lds\ns_add_u32 m0, s50, 40960\ns_add_u32 s63, s52, 987136\nbuffer_load_dwordx4 v220, s[4:7], s63 offen lds\n"
"buffer_load_dwordx4 v221, s[4:7], s63 offen offset:1024 lds\ns_mov_b64 exec, 0\ns_cmp_eq_u32 s40, 1\ns_cbranch_scc1 LretQ1z\ns_cmp_eq_u32 s40, 2\ns_cbranch_scc1 LretQ1z\n"
"s_cmp_eq_u32 s40, 3\ns_cbranch_scc1 LretQ1z\ns_branch LretQ1z\nLretQ1z:\ns_mov_b64 exec, -1\nv_mov_b32 v230, 0\n"
"v_mov_b32 v231, 0\nv_mov_b32 v232, 0\nv_mov_b32 v233, 0\nv_dot2c_f32_bf16 v230, s76, v192\nv_dot2c_f32_bf16 v231, v192, v192\nv_dot2c_f32_bf16 v232, s76, v200\n"
"v_dot2c_f32_bf16 v233, v200, v200\nv_dot2c_f32_bf16 v230, s76, v193\nv_dot2c_f32_bf16 v231, v193, v193\nv_dot2c_f32_bf16 v232, s76, v201\nv_dot2c_f32_bf16 v233, v201, v201\nv_dot2c_f32_bf16 v230, s76, v194\n"
"v_dot2c_f32_bf16 v231, v194, v194\nv_dot2c_f32_bf16 v232, s76, v202\nv_dot2c_f32_bf16 v233, v202, v202\nv_dot2c_f32_bf16 v230, s76, v195\nv_dot2c_f32_bf16 v231, v195, v195\nv_dot2c_f32_bf16 v232, s76, v203\n"
"v_dot2c_f32_bf16 v233, v203, v203\nv_dot2c_f32_bf16 v230, s76, v196\nv_dot2c_f32_bf16 v231, v196, v196\nv_dot2c_f32_bf16 v232, s76, v204\nv_dot2c_f32_bf16 v233, v204, v204\nv_dot2c_f32_bf16 v230, s76, v197\n"
"v_dot2c_f32_bf16 v231, v197, v197\nv_dot2c_f32_bf16 v232, s76, v205\nv_dot2c_f32_bf16 v233, v205, v205\nv_dot2c_f32_bf16 v230, s76, v198\nv_dot2c_f32_bf16 v231, v198, v198\nv_dot2c_f32_bf16 v232, s76, v206\n"
"v_dot2c_f32_bf16 v233, v206, v206\nv_dot2c_f32_bf16 v230, s76, v199\nv_dot2c_f32_bf16 v231, v199, v199\nv_dot2c_f32_bf16 v232, s76, v207\nv_dot2c_f32_bf16 v233, v207, v207\nv_lshrrev_b32 v234, 5, v252\n"
"v_and_b32 v234, 24, v234\nv_bfe_u32 v235, s46, v234, 8\nv_lshrrev_b32 v234, 1, v234\nv_and_b32 v236, 0xf0, v252\nv_add_u32 v234, v234, v236\nv_permlane32_swap_b32 v230, v231\n"
"v_permlane32_swap_b32 v232, v233\nv_add_f32 v230, v230, v231\nv_add_f32 v232, v232, v233\ns_nop 1\nv_permlane16_swap_b32 v230, v232\nv_add_f32 v230, v230, v232\n"
"s_movk_i32 s62, 0xff\nv_bfi_b32 v230, s62, v235, v230\ns_lshr_b32 s61, s49, 4\ns_lshl1_add_u32 s61, s53, s61\ns_add_u32 s61, s61, 82837504\nbuffer_store_dword v230, v234, s[20:23], s61 offen sc1\n"
"s_waitcnt vmcnt(13)\nv_cmp_eq_u64 s[86:87], s[46:47], v[248:249]\ns_nop 1\ns_and_b32 s62, s86, 0xffff\ns_cmp_eq_u32 s62, 0xffff\ns_cbranch_scc1 Lq583\n"
"Lq582:\nv_mov_b32 v248, 0\nv_mov_b32 v249, 0\ns_add_u32 s61, s80, 88358912\ns_mov_b64 exec, 0xffff\nbuffer_atomic_or_x2 v[248:249], v250, s[20:23], s61 offen sc0 sc1\n"
"s_mov_b64 exec, -1\ns_waitcnt vmcnt(0)\nv_cmp_eq_u64 s[86:87], s[46:47], v[248:249]\ns_nop 1\ns_and_b32 s62, s86, 0xffff\ns_cmp_eq_u32 s62, 0xffff\n"
"s_cbranch_scc1 Lq583\ns_sub_u32 s48, s48, 1\ns_cmp_gt_i32 s48, 0\ns_cbranch_scc1 Lq582\nLq583:\ns_add_u32 s64, s70, 18874368\n"
"buffer_load_dwordx4 a[0:3], v252, s[20:23], s64 offen offset:0 sc1\ns_add_u32 s65, s70, 18907136\nbuffer_load_dwordx4 a[4:7], v252, s[20:23], s65 offen offset:0 sc1\ns_add_u32 s66, s71, 18874368\nbuffer_load_dwordx4 a[8:11], v252, s[20:23], s66 offen offset:0 sc1\ns_add_u32 s67, s71, 18907136\n"
"buffer_load_dwordx4 a[12:15], v252, s[20:23], s67 offen offset:0 sc1\nbuffer_load_dwordx4 a[16:19], v252, s[20:23], s64 offen offset:1024 sc1\nbuffer_load_dwordx4 a[20:23], v252, s[20:23], s65 offen offset:1024 sc1\nbuffer_load_dwordx4 a[24:27], v252, s[20:23], s66 offen offset:1024 sc1\nbuffer_load_dwordx4 a[28:31], v252, s[20:23], s67 offen offset:1024 sc1\ns_barrier\n"
"R 160,188,8192\nR 162,188,12288\nR 164,188,16384\nR 166,188,20480\nR 168,189,8192\nR 170,189,12288\n"
"R 172,189,16384\nR 174,189,20480\nR 176,190,8192\nR 178,190,12288\nR 180,190,16384\nR 182,190,20480\n"
"buffer_load_dwordx4 a[32:35], v252, s[20:23], s64 offen offset:2048 sc1\nbuffer_load_dwordx4 a[36:39], v252, s[20:23], s65 offen offset:2048 sc1\nbuffer_load_dwordx4 a[40:43], v252, s[20:23], s66 offen offset:2048 sc1\nbuffer_load_dwordx4 a[44:47], v252, s[20:23], s67 offen offset:2048 sc1\nbuffer_load_dwordx4 a[48:51], v252, s[20:23], s64 offen offset:3072 sc1\nbuffer_load_dwordx4 a[52:55], v252, s[20:23], s65 offen offset:3072 sc1\n"
"buffer_load_dwordx4 a[56:59], v252, s[20:23], s66 offen offset:3072 sc1\nbuffer_load_dwordx4 a[60:63], v252, s[20:23], s67 offen offset:3072 sc1\nLpg18:\ns_cbranch_execz Lpg22\ns_waitcnt vmcnt(15) lgkmcnt(10)\nv_mfma_f32_16x16x32_bf16 v[0:3], v[160:163], a[0:3], 0\n"
"s_add_u32 s64, s70, 18878464\nbuffer_load_dwordx4 a[64:67], v252, s[20:23], s64 offen offset:0 sc1\ns_waitcnt vmcnt(12) lgkmcnt(8)\nM 0,164,16\nv_mfma_f32_16x16x32_bf16 v[16:19], v[160:163], a[4:7], 0\ns_waitcnt vmcnt(11)\n"
"M 16,164,20\nv_mfma_f32_16x16x32_bf16 v[32:35], v[160:163], a[8:11], 0\ns_waitcnt vmcnt(10)\nM 32,164,24\nv_mfma_f32_16x16x32_bf16 v[48:51], v[160:163], a[12:15], 0\ns_waitcnt vmcnt(9)\n"
"M 48,164,28\nG 4\nv_mfma_f32_16x16x32_bf16 v[52:55], v[168:171], a[12:15], 0\nR 160,191,8192\nM 52,172,28\ns_add_u32 s65, s70, 18911232\n"
"buffer_load_dwordx4 a[68:71], v252, s[20:23], s65 offen offset:0 sc1\nv_mfma_f32_16x16x32_bf16 v[36:39], v[168:171], a[8:11], 0\nR 162,191,12288\nM 36,172,24\nv_mfma_f32_16x16x32_bf16 v[20:23], v[168:171], a[4:7], 0\nR 164,191,16384\n"
"M 20,172,20\nv_mfma_f32_16x16x32_bf16 v[4:7], v[168:171], a[0:3], 0\nR 166,191,20480\nM 4,172,16\nG 4\nv_mfma_f32_16x16x32_bf16 v[8:11], v[176:179], a[0:3], 0\n"
"R 168,188,24576\nM 8,180,16\ns_add_u32 s66, s71, 18878464\nbuffer_load_dwordx4 a[72:75], v252, s[20:23], s66 offen offset:0 sc1\nv_mfma_f32_16x16x32_bf16 v[24:27], v[176:179], a[4:7], 0\nR 170,188,28672\n"
"M 24,180,20\ns_add_u32 m0, s50, 49152\nv_mfma_f32_16x16x32_bf16 v[40:43], v[176:179], a[8:11], 0\nR 172,188,32768\ns_add_u32 s63, s52, 1181696\nM 40,180,24\n"
"s_add_u32 s67, s71, 18911232\nbuffer_load_dwordx4 a[76:79], v252, s[20:23], s67 offen offset:0 sc1\nv_mfma_f32_16x16x32_bf16 v[56:59], v[176:179], a[12:15], 0\nR 174,188,36864\nM 56,180,28\nG 4\n"
"v_mfma_f32_16x16x32_bf16 v[60:63], v[160:163], a[12:15], 0\nR 176,189,24576\nM 60,164,28\nbuffer_load_dwordx4 v220, s[4:7], s63 offen lds\nv_mfma_f32_16x16x32_bf16 v[44:47], v[160:163], a[8:11], 0\nR 178,189,28672\n"
"M 44,164,24\nv_mfma_f32_16x16x32_bf16 v[28:31], v[160:163], a[4:7], 0\nR 180,189,32768\nM 28,164,20\nbuffer_load_dwordx4 a[80:83], v252, s[20:23], s64 offen offset:1024 sc1\nv_mfma_f32_16x16x32_bf16 v[12:15], v[160:163], a[0:3], 0\n"
"R 182,189,36864\nM 12,164,16\nG 4\nv_mfma_f32_16x16x32_bf16 v[64:67], a[0:3], v[168:171], 0\nR 160,190,24576\nN 64,16,172\n"
"v_mfma_f32_16x16x32_bf16 v[68:71], a[4:7], v[168:171], 0\nR 162,190,28672\nN 68,20,172\nbuffer_load_dwordx4 a[84:87], v252, s[20:23], s65 offen offset:1024 sc1\nv_mfma_f32_16x16x32_bf16 v[96:99], a[8:11], v[168:171], 0\nR 164,190,32768\n"
"N 96,24,172\nv_mfma_f32_16x16x32_bf16 v[100:103], a[12:15], v[168:171], 0\nR 166,190,36864\nN 100,28,172\ns_barrier\nG 4\n"
"v_mfma_f32_16x16x32_bf16 v[108:111], a[12:15], v[176:179], 0\nR 168,191,24576\nN 108,28,180\nbuffer_load_dwordx4 v221, s[4:7], s63 offen offset:1024 lds\nv_mfma_f32_16x16x32_bf16 v[104:107], a[8:11], v[176:179], 0\nR 170,191,28672\n"
"N 104,24,180\nbuffer_load_dwordx4 a[88:91], v252, s[20:23], s66 offen offset:1024 sc1\nv_mfma_f32_16x16x32_bf16 v[76:79], a[4:7], v[176:179], 0\nR 172,191,32768\nN 76,20,180\nv_mfma_f32_16x16x32_bf16 v[72:75], a[0:3], v[176:179], 0\n"
"R 174,191,36864\nN 72,16,180\nG 4\nv_mfma_f32_16x16x32_bf16 v[80:83], a[0:3], v[160:163], 0\nR 176,188,40960\ns_add_u32 s63, s52, 1378304\n"
"N 80,16,164\nbuffer_load_dwordx4 a[92:95], v252, s[20:23], s67 offen offset:1024 sc1\nv_mfma_f32_16x16x32_bf16 v[84:87], a[4:7], v[160:163], 0\nR 178,188,45056\nN 84,20,164\ns_add_u32 m0, s50, 57344\n"
"v_mfma_f32_16x16x32_bf16 v[112:115], a[8:11], v[160:163], 0\nR 180,188,49152\nN 112,24,164\nbuffer_load_dwordx4 v220, s[4:7], s63 offen lds\nv_mfma_f32_16x16x32_bf16 v[116:119], a[12:15], v[160:163], 0\nR 182,188,53248\n"
"N 116,28,164\nG 4\nv_mfma_f32_16x16x32_bf16 v[124:127], a[12:15], v[168:171], 0\nR 160,189,40960\nN 124,28,172\nbuffer_load_dwordx4 a[96:99], v252, s[20:23], s64 offen offset:2048 sc1\n"
"v_mfma_f32_16x16x32_bf16 v[120:123], a[8:11], v[168:171], 0\nR 162,189,45056\nN 120,24,172\nv_mfma_f32_16x16x32_bf16 v[92:95], a[4:7], v[168:171], 0\nR 164,189,49152\nN 92,20,172\n"
"v_mfma_f32_16x16x32_bf16 v[88:91], a[0:3], v[168:171], 0\nR 166,189,53248\nN 88,16,172\nG 4\ns_waitcnt vmcnt(19)\nM 0,176,32\n"
"R 168,190,40960\ns_waitcnt vmcnt(15)\nM 0,180,48\nbuffer_load_dwordx4 a[100:103], v252, s[20:23], s65 offen offset:2048 sc1\nM 16,176,36\nR 170,190,45056\n"
"s_waitcnt vmcnt(15)\nM 16,180,52\nM 32,176,40\nR 172,190,49152\ns_waitcnt vmcnt(14)\nM 32,180,56\n"
"buffer_load_dwordx4 v221, s[4:7], s63 offen offset:1024 lds\nM 48,176,44\nR 174,190,53248\ns_waitcnt vmcnt(14)\nM 48,180,60\ns_barrier\n"
"G 4\nM 52,160,44\nR 176,191,40960\nM 52,164,60\nbuffer_load_dwordx4 a[104:107], v252, s[20:23], s66 offen offset:2048 sc1\nM 36,160,40\n"
"R 178,191,45056\nM 36,164,56\ns_add_u32 m0, s50, 65536\nM 20,160,36\nR 180,191,49152\ns_add_u32 s63, s52, 1183744\n"
"M 20,164,52\nbuffer_load_dwordx4 a[108:111], v252, s[20:23], s67 offen offset:2048 sc1\nM 4,160,32\nR 182,191,53248\nM 4,164,48\nG 4\n"
"M 8,168,32\nR 160,184,0\nM 8,172,48\nbuffer_load_dwordx4 v220, s[4:7], s63 offen lds\nM 24,168,36\nR 162,184,4096\n"
"M 24,172,52\nM 40,168,40\nR 164,184,8192\nM 40,172,56\nbuffer_load_dwordx4 a[112:115], v252, s[20:23], s64 offen offset:3072 sc1\nM 56,168,44\n"
"R 166,184,12288\nM 56,172,60\nG 4\nM 60,176,44\nR 168,185,0\nM 60,180,60\n"
"M 44,176,40\nR 170,185,4096\nM 44,180,56\nbuffer_load_dwordx4 a[116:119], v252, s[20:23], s65 offen offset:3072 sc1\nM 28,176,36\nR 172,185,8192\n"
"M 28,180,52\nM 12,176,32\nR 174,185,12288\nM 12,180,48\nG 4\nN 64,32,160\n"
"R 176,186,0\nN 64,48,164\nbuffer_load_dwordx4 v221, s[4:7], s63 offen offset:1024 lds\nN 68,36,160\nR 178,186,4096\nN 68,52,164\n"
"buffer_load_dwordx4 a[120:123], v252, s[20:23], s66 offen offset:3072 sc1\nN 96,40,160\nR 180,186,8192\nN 96,56,164\ns_lshr_b32 s84, s49, 4\nN 100,44,160\n"
"R 182,186,12288\ns_add_u32 s84, s84, 82837504\nN 100,60,164\ns_barrier\nG 4\nN 108,44,168\n"
"R 160,187,0\nN 108,60,172\nbuffer_load_dwordx4 a[124:127], v252, s[20:23], s67 offen offset:3072 sc1\nN 104,40,168\nR 162,187,4096\nN 104,56,172\n"
"buffer_load_dwordx4 v[128:131], v252, s[20:23], s84 offen offset:0 sc1\nN 76,36,168\nR 164,187,8192\nN 76,52,172\nN 72,32,168\nR 166,187,12288\n"
"N 72,48,172\nbuffer_load_dwordx4 v[132:135], v252, s[20:23], s84 offen offset:1024 sc1\nG 4\nN 80,32,176\nR 168,184,16384\nN 80,48,180\n"
"s_add_u32 s64, s70, 18882560\nbuffer_load_dwordx4 a[128:131], v252, s[20:23], s64 offen offset:0 sc1\nN 84,36,176\nR 170,184,20480\nN 84,52,180\nbuffer_load_dwordx4 v[136:139], v252, s[20:23], s84 offen offset:2048 sc1\n"
"N 112,40,176\nR 172,184,24576\nN 112,56,180\nN 116,44,176\nR 174,184,28672\nN 116,60,180\n"
"buffer_load_dwordx4 v[140:143], v252, s[20:23], s84 offen offset:3072 sc1\nG 4\nN 124,44,160\nR 176,185,16384\nN 124,60,164\ns_add_u32 s65, s70, 18915328\n"
"buffer_load_dwordx4 a[132:135], v252, s[20:23], s65 offen offset:0 sc1\nN 120,40,160\nR 178,185,20480\nN 120,56,164\nN 92,36,160\nR 180,185,24576\n"
"N 92,52,164\nN 88,32,160\nR 182,185,28672\nN 88,48,164\nG 4\ns_waitcnt vmcnt(27)\n"
"M 0,168,64\nR 160,186,16384\ns_waitcnt vmcnt(22)\nM 0,172,80\ns_add_u32 s66, s71, 18882560\nbuffer_load_dwordx4 a[136:139], v252, s[20:23], s66 offen offset:0 sc1\n"
"M 16,168,68\nR 162,186,20480\ns_waitcnt vmcnt(22)\nM 16,172,84\nM 32,168,72\nR 164,186,24576\n"
"s_waitcnt vmcnt(20)\nM 32,172,88\ns_add_u32 s67, s71, 18915328\nbuffer_load_dwordx4 a[140:143], v252, s[20:23], s67 offen offset:0 sc1\nM 48,168,76\nR 166,186,28672\n"
"s_waitcnt vmcnt(20)\nM 48,172,92\ns_barrier\nG 4\nM 52,176,76\nR 168,187,16384\n"
"M 52,180,92\nM 36,176,72\nR 170,187,20480\nM 36,180,88\ns_add_u32 m0, s50, 73728\nM 20,176,68\n"
"R 172,187,24576\ns_add_u32 s63, s52, 1380352\nM 20,180,84\nbuffer_load_dwordx4 a[144:147], v252, s[20:23], s64 offen offset:1024 sc1\nM 4,176,64\nR 174,187,28672\n"
"M 4,180,80\nG 4\nM 8,160,64\nR 176,184,32768\nM 8,164,80\nbuffer_load_dwordx4 v220, s[4:7], s63 offen lds\n"
"M 24,160,68\nR 178,184,36864\nM 24,164,84\nbuffer_load_dwordx4 a[148:151], v252, s[20:23], s65 offen offset:1024 sc1\nM 40,160,72\nR 180,184,40960\n"
"M 40,164,88\nbuffer_load_dwordx4 v221, s[4:7], s63 offen offset:1024 lds\nM 56,160,76\nR 182,184,45056\nM 56,164,92\nG 4\n"
"M 60,168,76\nR 160,185,32768\nM 60,172,92\nM 44,168,72\nR 162,185,36864\nM 44,172,88\n"
"buffer_load_dwordx4 a[152:155], v252, s[20:23], s66 offen offset:1024 sc1\nM 28,168,68\nR 164,185,40960\nM 28,172,84\nM 12,168,64\nR 166,185,45056\n"
"M 12,172,80\nG 4\nN 64,64,176\nR 168,186,32768\nN 64,80,180\nbuffer_load_dwordx4 a[156:159], v252, s[20:23], s67 offen offset:1024 sc1\n"
"N 68,68,176\nR 170,186,36864\nN 68,84,180\nN 96,72,176\nR 172,186,40960\ns_mov_b32 s85, 0x0c0c0400\n"
"N 96,88,180\ns_add_u32 m0, s50, 81920\ns_waitcnt vmcnt(13)\nv_perm_b32 v144, v129, v128, s85\nv_perm_b32 v148, v131, v130, s85\nN 100,76,176\n"
"R 174,186,45056\ns_add_u32 s63, s52, 1574912\nN 100,92,180\ns_barrier\nG 4\nN 108,76,160\n"
"R 176,187,32768\nv_lshl_or_b32 v144, v148, 16, v144\nN 108,92,164\nbuffer_load_dwordx4 a[160:163], v252, s[20:23], s64 offen offset:2048 sc1\nN 104,72,160\nR 178,187,36864\n"
"s_waitcnt vmcnt(13)\nv_perm_b32 v145, v133, v132, s85\nN 104,88,164\nbuffer_load_dwordx4 v220, s[4:7], s63 offen lds\nN 76,68,160\nR 180,187,40960\n"
"v_perm_b32 v149, v135, v134, s85\nN 76,84,164\nbuffer_load_dwordx4 v221, s[4:7], s63 offen offset:1024 lds\nN 72,64,160\nR 182,187,45056\ns_add_u32 m0, s50, 90112\n"
"N 72,80,164\nG 4\nv_lshl_or_b32 v145, v149, 16, v145\ns_waitcnt vmcnt(13)\nv_perm_b32 v146, v137, v136, s85\nN 80,64,168\n"
"R 160,184,49152\ns_add_u32 s63, s52, 1771520\nN 80,80,172\nbuffer_load_dwordx4 a[164:167], v252, s[20:23], s65 offen offset:2048 sc1\nN 84,68,168\nR 162,184,53248\n"
"v_perm_b32 v150, v139, v138, s85\nN 84,84,172\nbuffer_load_dwordx4 v220, s[4:7], s63 offen lds\nN 112,72,168\nR 164,184,57344\nv_lshl_or_b32 v146, v150, 16, v146\n"
"N 112,88,172\nbuffer_load_dwordx4 v221, s[4:7], s63 offen offset:1024 lds\nN 116,76,168\nR 166,184,61440\ns_waitcnt vmcnt(15)\nv_perm_b32 v147, v141, v140, s85\n"
"N 116,92,172\nG 4\nv_perm_b32 v151, v143, v142, s85\nv_lshl_or_b32 v147, v151, 16, v147\nN 124,76,176\nR 168,185,49152\n"
"v_min3_u32 v230, v144, v145, v146\nN 124,92,180\nbuffer_load_dwordx4 a[168:171], v252, s[20:23], s66 offen offset:2048 sc1\nN 120,72,176\nR 170,185,53248\nv_min3_u32 v230, v230, v147, v147\n"
"N 120,88,180\nv_max3_u32 v231, v144, v145, v146\nv_max3_u32 v231, v231, v147, v147\nN 92,68,176\nR 172,185,57344\nv_cmp_ne_u32 s[86:87], s46, v230\n"
"N 92,84,180\nbuffer_load_dwordx4 a[172:175], v252, s[20:23], s67 offen offset:2048 sc1\nN 88,64,176\nR 174,185,61440\nv_cmp_ne_u32 s[88:89], s46, v231\nN 88,80,180\n"
"s_or_b64 s[86:87], s[86:87], s[88:89]\ns_cbranch_scc1 Lq639\nLq640:\nG 4\nM 0,160,96\nR 176,186,49152\n"
"v_and_b32 v128, 0xffffff00, v128\nM 0,164,112\nv_and_b32 v129, 0xffffff00, v129\nv_and_b32 v130, 0xffffff00, v130\nM 16,160,100\nR 178,186,53248\n"
"v_and_b32 v131, 0xffffff00, v131\nM 16,164,116\ns_add_u32 m0, s50, 98304\nv_and_b32 v132, 0xffffff00, v132\nv_and_b32 v133, 0xffffff00, v133\nM 32,160,104\n"
"R 180,186,57344\ns_add_u32 s63, s52, 1576960\nM 32,164,120\nbuffer_load_dwordx4 a[176:179], v252, s[20:23], s64 offen offset:3072 sc1\nM 48,160,108\nR 182,186,61440\n"
"v_and_b32 v134, 0xffffff00, v134\nM 48,164,124\ns_waitcnt vmcnt(11)\ns_barrier\nG 4\nM 52,168,108\n"
"R 160,187,49152\nv_and_b32 v135, 0xffffff00, v135\nM 52,172,124\nbuffer_load_dwordx4 v220, s[4:7], s63 offen lds\nM 36,168,104\nR 162,187,53248\n"
"v_and_b32 v136, 0xffffff00, v136\nM 36,172,120\nbuffer_load_dwordx4 a[180:183], v252, s[20:23], s65 offen offset:3072 sc1\nM 20,168,100\nR 164,187,57344\nv_and_b32 v137, 0xffffff00, v137\n"
"M 20,172,116\nbuffer_load_dwordx4 v221, s[4:7], s63 offen offset:1024 lds\nM 4,168,96\nR 166,187,61440\ns_add_u32 m0, s50, 106496\nM 4,172,112\n"
"G 4\nv_and_b32 v138, 0xffffff00, v138\nv_and_b32 v139, 0xffffff00, v139\nM 8,176,96\nR 168,188,0\ns_add_u32 s63, s52, 1773568\n"
"M 8,180,112\nbuffer_load_dwordx4 v220, s[4:7], s63 offen lds\nM 24,176,100\nR 170,188,4096\nv_and_b32 v140, 0xffffff00, v140\nM 24,180,116\n"
"buffer_load_dwordx4 a[184:187], v252, s[20:23], s66 offen offset:3072 sc1\nM 40,176,104\nR 172,188,8192\nv_and_b32 v141, 0xffffff00, v141\nM 40,180,120\nbuffer_load_dwordx4 v221, s[4:7], s63 offen offset:1024 lds\n"
"M 56,176,108\nR 174,188,12288\nv_and_b32 v142, 0xffffff00, v142\nM 56,180,124\nG 4\nv_and_b32 v143, 0xffffff00, v143\n"
"v_add_f32 v128, v128, v136\nM 60,160,108\nR 176,189,0\nv_add_f32 v129, v129, v137\nM 60,164,124\nbuffer_load_dwordx4 a[188:191], v252, s[20:23], s67 offen offset:3072 sc1\n"
"M 44,160,104\nR 178,189,4096\nv_add_f32 v130, v130, v138\nM 44,164,120\nv_add_f32 v131, v131, v139\nv_add_f32 v132, v132, v140\n"
"M 28,160,100\nR 180,189,8192\nv_add_f32 v133, v133, v141\nM 28,164,116\nv_add_f32 v134, v134, v142\nv_add_f32 v135, v135, v143\n"
"M 12,160,96\nR 182,189,12288\nv_add_f32 v128, v128, v132\nM 12,164,112\nG 4\nv_add_f32 v129, v129, v133\n"
"v_add_f32 v130, v130, v134\nN 64,96,168\nR 160,190,0\nv_add_f32 v131, v131, v135\nN 64,112,172\ns_add_u32 s64, s70, 18886656\n"
"buffer_load_dwordx4 a[192:195], v252, s[20:23], s64 offen offset:0 sc1\nN 68,100,168\nR 162,190,4096\nv_permlane32_swap_b32 v128, v130\nN 68,116,172\nv_permlane32_swap_b32 v129, v131\n"
"v_add_f32 v128, v128, v130\nN 96,104,168\nR 164,190,8192\nv_add_f32 v129, v129, v131\nN 96,120,172\ns_add_u32 m0, s50, 114688\n"
"v_permlane16_swap_b32 v128, v129\nv_add_f32 v230, v128, v129\nN 100,108,168\nR 166,190,12288\ns_add_u32 s63, s52, 1968128\nN 100,124,172\n"
"s_waitcnt vmcnt(11)\ns_barrier\nG 4\nN 108,108,176\nR 168,191,0\nv_mov_b32 v232, v230\n"
"N 108,124,180\ns_add_u32 s65, s70, 18919424\nbuffer_load_dwordx4 a[196:199], v252, s[20:23], s65 offen offset:0 sc1\nN 104,104,176\nR 170,191,4096\nv_permlane16_swap_b32 v230, v232\n"
"N 104,120,180\nbuffer_load_dwordx4 v220, s[4:7], s63 offen lds\nN 76,100,176\nR 172,191,8192\nv_mov_b32 v231, v230\nLpg19:\n"
"s_cbranch_execz LretW0n\nN 76,116,180\nbuffer_load_dwordx4 v221, s[4:7], s63 offen offset:1024 lds\nN 72,96,176\nR 174,191,12288\ns_add_u32 m0, s50, 0\n"
"N 72,112,180\nG 4\nv_mov_b32 v233, v232\nv_permlane32_swap_b32 v230, v231\nN 80,96,160\nR 176,188,16384\n"
"s_add_u32 s63, s52, 2164736\nN 80,112,164\ns_add_u32 s66, s71, 18886656\nbuffer_load_dwordx4 a[200:203], v252, s[20:23], s66 offen offset:0 sc1\nN 84,100,160\nR 178,188,20480\n"
"v_permlane32_swap_b32 v232, v233\nN 84,116,164\nbuffer_load_dwordx4 v220, s[4:7], s63 offen lds\nN 112,104,160\nR 180,188,24576\nv_mul_f32 v230, 0xba800000, v230\n"
"N 112,120,164\ns_add_u32 s67, s71, 18919424\nbuffer_load_dwordx4 a[204:207], v252, s[20:23], s67 offen offset:0 sc1\nN 116,108,160\nR 182,188,28672\nv_mul_f32 v214, v230, v230\n"
"N 116,124,164\nG 4\nv_mul_f32 v231, 0x3a800000, v231\nv_sub_f32 v231, v231, v214\nN 124,108,168\nR 160,189,16384\n"
"buffer_load_dwordx4 v221, s[4:7], s63 offen offset:1024 lds\nN 124,124,172\nv_max_f32 v231, 0, v231\nv_add_f32 v231, 0x3727c5ac, v231\nN 120,104,168\nR 162,189,20480\n"
"v_and_b32 v208, s77, v230\nN 120,120,172\nv_rsq_f32 v234, v231\nN 92,100,168\nR 164,189,24576\nv_mul_f32 v216, v231, v234\n"
"N 92,116,172\nbuffer_load_dwordx4 a[208:211], v252, s[20:23], s64 offen offset:1024 sc1\nN 88,96,168\nR 166,189,28672\nv_sub_f32 v230, v230, v208\nN 88,112,172\n"
"G 4\nv_cvt_pk_bf16_f32 v209, v230, v216\nv_cvt_pk_bf16_f32 v208, v208, v208\nM 0,176,128\nR 168,190,16384\nv_mov_b32 v214, v234\n"
"M 0,180,144\ns_mov_b64 s[60:61], 0xffff\nv_cndmask_b32 v208, 0, v208, s[60:61]\nv_mul_f32 v215, 0x3e8293ee, v234\nM 16,176,132\nR 170,190,20480\n"
"v_mul_f32 v232, 0xba800000, v232\nM 16,180,148\nbuffer_load_dwordx4 a[212:215], v252, s[20:23], s65 offen offset:1024 sc1\nM 32,176,136\nR 172,190,24576\ns_add_u32 s63, s52, 1970176\n"
"M 32,180,152\ns_add_u32 m0, s50, 8192\ns_mov_b64 s[60:61], 0xffff\nv_cndmask_b32 v209, 0, v209, s[60:61]\nM 48,176,140\nR 174,190,28672\n"
"v_mul_f32 v217, v232, v232\nM 48,180,156\ns_waitcnt vmcnt(11)\ns_barrier\nG 4\nM 52,160,140\n"
"R 176,191,16384\nv_mul_f32 v233, 0x3a800000, v233\nM 52,164,156\nbuffer_load_dwordx4 v220, s[4:7], s63 offen lds\nM 36,160,136\nR 178,191,20480\n"
"v_sub_f32 v233, v233, v217\nM 36,164,152\nbuffer_load_dwordx4 a[216:219], v252, s[20:23], s66 offen offset:1024 sc1\nM 20,160,132\nR 180,191,24576\nv_max_f32 v233, 0, v233\n"
"M 20,164,148\nbuffer_load_dwordx4 v221, s[4:7], s63 offen offset:1024 lds\nM 4,160,128\nR 182,191,28672\ns_add_u32 m0, s50, 16384\nM 4,164,144\n"
"G 4\nv_add_f32 v233, 0x3727c5ac, v233\nv_and_b32 v212, s77, v232\nM 8,168,128\nR 160,188,32768\ns_add_u32 s63, s52, 2166784\n"
"M 8,172,144\nbuffer_load_dwordx4 a[220:223], v252, s[20:23], s67 offen offset:1024 sc1\nM 24,168,132\nR 162,188,36864\nv_sub_f32 v232, v232, v212\nM 24,172,148\n"
"buffer_load_dwordx4 v220, s[4:7], s63 offen lds\nM 40,168,136\nR 164,188,40960\nv_cvt_pk_bf16_f32 v212, v212, v212\nM 40,172,152\nbuffer_load_dwordx4 v221, s[4:7], s63 offen offset:1024 lds\n"
"M 56,168,140\nR 166,188,45056\nv_bfe_u32 v230, v252, 2, 6\nM 56,172,156\nG 4\nv_rsq_f32 v235, v233\n"
"M 60,176,140\nR 168,189,32768\nv_mul_f32 v219, v233, v235\nM 60,180,156\nbuffer_load_dwordx4 a[224:227], v252, s[20:23], s64 offen offset:2048 sc1\nM 44,176,136\n"
"R 170,189,36864\nv_cvt_pk_bf16_f32 v213, v232, v219\nM 44,180,152\ns_mov_b64 s[60:61], 0xffff\nv_cndmask_b32 v212, 0, v212, s[60:61]\nv_mov_b32 v217, v235\n"
"M 28,176,132\nR 172,189,40960\nv_mul_f32 v218, 0x3e8293ee, v235\nM 28,180,148\ns_mov_b64 s[60:61], 0xffff\nv_cndmask_b32 v213, 0, v213, s[60:61]\n"
"M 12,176,128\nR 174,189,45056\nM 12,180,144\nG 4\nN 64,128,160\nR 176,190,32768\n"
"N 64,144,164\nbuffer_load_dwordx4 a[228:231], v252, s[20:23], s65 offen offset:2048 sc1\nN 68,132,160\nR 178,190,36864\nN 68,148,164\ns_lshl_b32 s60, s40, 7\n"
"s_add_u32 s60, s60, 161280\nv_add_u32 v230, s60, v230\nN 96,136,160\nR 180,190,40960\nN 96,152,164\ns_add_u32 m0, s50, 24576\n"
"ds_write_b32 v230, v214 offset:0\nN 100,140,160\nR 182,190,45056\ns_add_u32 s63, s52, 2361344\nN 100,156,164\ns_waitcnt vmcnt(10)\n"
"s_barrier\nG 5\nN 108,140,168\nR 160,191,32768\nN 108,156,172\nbuffer_load_dwordx4 a[232:235], v252, s[20:23], s66 offen offset:2048 sc1\n"
"N 104,136,168\nR 162,191,36864\nN 104,152,172\nbuffer_load_dwordx4 v220, s[4:7], s63 offen lds\nN 76,132,168\nR 164,191,40960\n"
"Lq682:\ns_lshr_b32 s60, s45, 3\ns_lshl2_add_u32 s60, s60, s40\ns_lshl_b32 s60, s60, 8\nv_lshl_add_u32 v224, v252, 3, s60\nv_add_u32 v224, Lq682+4096-LqPC, v224\n"
"v_min_u32 v224, LqE-LqPC-4, v224\ns_mov_b64 exec, 3\nglobal_load_dword v224, v224, s[98:99]\ns_mov_b64 exec, -1\nN 76,148,172\nbuffer_load_dwordx4 a[236:239], v252, s[20:23], s67 offen offset:2048 sc1\n"
"N 72,128,168\nR 166,191,45056\nN 72,144,172\nds_write_b32 v230, v217 offset:64\nG 5\nN 80,128,176\n"
"R 168,188,49152\nbuffer_load_dwordx4 v221, s[4:7], s63 offen offset:1024 lds\nN 80,144,180\ns_add_u32 m0, s50, 32768\ns_add_u32 s63, s52, 2557952\nbuffer_load_dwordx4 v220, s[4:7], s63 offen lds\n"
"N 84,132,176\nR 170,188,53248\nN 84,148,180\nN 112,136,176\nR 172,184,0\nN 112,152,180\n"
"buffer_load_dwordx4 a[240:243], v252, s[20:23], s64 offen offset:3072 sc1\nN 116,140,176\nR 174,184,4096\nN 116,156,180\nG 5\nN 124,140,160\n"
"R 176,189,49152\nbuffer_load_dwordx4 v221, s[4:7], s63 offen offset:1024 lds\nN 124,156,164\nN 120,136,160\nR 178,189,53248\nN 120,152,164\n"
"buffer_load_dwordx4 a[244:247], v252, s[20:23], s65 offen offset:3072 sc1\nN 92,132,160\nR 180,185,0\nN 92,148,164\nN 88,128,160\nR 182,185,4096\n"
"N 88,144,164\nG 4\nM 0,168,160\nR 160,190,49152\nM 0,172,176\nM 16,168,164\n"
"R 162,190,53248\nM 16,172,180\nbuffer_load_dwordx4 a[248:251], v252, s[20:23], s66 offen offset:3072 sc1\nM 32,168,168\nR 164,186,0\nM 32,172,184\n"
"s_add_u32 m0, s50, 40960\nM 48,168,172\nR 166,186,4096\ns_add_u32 s63, s52, 2754560\nM 48,172,188\ns_waitcnt vmcnt(12)\n"
"s_barrier\nG 4\nM 52,176,172\nR 168,191,49152\nM 52,180,188\nbuffer_load_dwordx4 a[252:255], v252, s[20:23], s67 offen offset:3072 sc1\n"
"M 36,176,168\nR 170,191,53248\nM 36,180,184\nbuffer_load_dwordx4 v220, s[4:7], s63 offen lds\nM 20,176,164\nR 172,187,0\n"
"M 20,180,180\nbuffer_load_dwordx4 v221, s[4:7], s63 offen offset:1024 lds\nM 4,176,160\nR 174,187,4096\ns_add_u32 m0, s50, 49152\nM 4,180,176\n"
"G 4\nM 8,160,160\nR 176,184,8192\ns_add_u32 s63, s52, 2951168\nM 8,164,176\nbuffer_load_dwordx4 v220, s[4:7], s63 offen lds\n"
"M 24,160,164\nR 178,184,12288\nM 24,164,180\nM 40,160,168\nR 180,184,16384\nM 40,164,184\n"
"buffer_load_dwordx4 v221, s[4:7], s63 offen offset:1024 lds\nM 56,160,172\nR 182,184,20480\nM 56,164,188\nG 4\nM 60,168,172\n"
"R 160,185,8192\nM 60,172,188\nM 44,168,168\nR 162,185,12288\nM 44,172,184\ns_add_u32 m0, s50, 57344\n"
"M 28,168,164\nR 164,185,16384\nM 28,172,180\ns_add_u32 s63, s52, 2363392\nM 12,168,160\nR 166,185,20480\n"
"M 12,172,176\nG 4\nN 64,160,176\nR 168,186,8192\nN 64,176,180\nbuffer_load_dwordx4 v220, s[4:7], s63 offen lds\n"
"N 68,164,176\nR 170,186,12288\nN 68,180,180\nN 96,168,176\nR 172,186,16384\nN 96,184,180\n"
"buffer_load_dwordx4 v221, s[4:7], s63 offen offset:1024 lds\nN 100,172,176\nR 174,186,20480\ns_add_u32 m0, s50, 65536\nN 100,188,180\ns_waitcnt vmcnt(9)\n"
"s_barrier\nG 4\nN 108,172,160\nR 176,187,8192\ns_add_u32 s63, s52, 2560000\nN 108,188,164\n"
"buffer_load_dwordx4 v220, s[4:7], s63 offen lds\nN 104,168,160\nR 178,187,12288\nN 104,184,164\nN 76,164,160\nR 180,187,16384\n"
"N 76,180,164\nN 72,160,160\nR 182,187,20480\nN 72,176,164\nG 4\nN 80,160,168\n"
"R 160,184,24576\nN 80,176,172\nbuffer_load_dwordx4 v221, s[4:7], s63 offen offset:1024 lds\nN 84,164,168\nR 162,184,28672\ns_add_u32 s63, s52, 2756608\n"
"N 84,180,172\ns_add_u32 m0, s50, 73728\nN 112,168,168\nR 164,184,32768\nN 112,184,172\nbuffer_load_dwordx4 v220, s[4:7], s63 offen lds\n"
"N 116,172,168\nR 166,184,36864\nN 116,188,172\nG 4\nN 124,172,176\nR 168,185,24576\n"
"N 124,188,180\nbuffer_load_dwordx4 v221, s[4:7], s63 offen offset:1024 lds\nN 120,168,176\nR 170,185,28672\ns_add_u32 s63, s52, 2953216\nN 120,184,180\n"
"s_add_u32 m0, s50, 81920\nN 92,164,176\nR 172,185,32768\nN 92,180,180\nbuffer_load_dwordx4 v220, s[4:7], s63 offen lds\nN 88,160,176\n"
"R 174,185,36864\nN 88,176,180\nG 4\nM 0,160,192\nR 176,186,24576\ns_mov_b32 s60, 156160\n"
"v_bfe_u32 v246, v252, 4, 4\nv_lshl_add_u32 v246, v246, 3, s60\nM 0,164,208\nds_read_b64 v[238:239], v246 offset:0\nM 16,160,196\nR 178,186,28672\n"
"ds_read_b64 v[240:241], v246 offset:128\nM 16,164,212\nds_read_b64 v[242:243], v246 offset:256\nM 32,160,200\nR 180,186,32768\nds_read_b64 v[244:245], v246 offset:384\n"
"M 32,164,216\nds_read_b64 v[246:247], v246 offset:0\nM 48,160,204\nR 182,186,36864\nM 48,164,220\ns_waitcnt vmcnt(7) lgkmcnt(1)\n"
"s_barrier\nM 52,168,204\nR 160,187,24576\nM 52,172,220\nbuffer_load_dwordx4 v221, s[4:7], s63 offen offset:1024 lds\nM 36,168,200\n"
"R 162,187,28672\ns_add_u32 s63, s52, 0\nM 36,172,216\ns_add_u32 m0, s50, 90112\nM 20,168,196\nR 164,187,32768\n"
"M 20,172,212\nbuffer_load_dwordx4 v220, s[4:7], s63 offen lds\nM 4,168,192\nR 166,187,36864\nv_mfma_f32_16x16x32_bf16 v[0:3], v[238:241], v[208:211], v[0:3]\nM 4,172,208\n"
"G 4\nM 8,176,192\nR 168,184,40960\nv_mfma_f32_16x16x32_bf16 v[4:7], v[240:243], v[208:211], v[4:7]\nM 8,180,208\nbuffer_load_dwordx4 v221, s[4:7], s63 offen offset:1024 lds\n"
"M 24,176,196\nR 170,184,45056\nv_mfma_f32_16x16x32_bf16 v[8:11], v[242:245], v[208:211], v[8:11]\ns_add_u32 m0, s50, 98304\ns_add_u32 s63, s52, 196608\nM 24,180,212\n"
"buffer_load_dwordx4 v220, s[4:7], s63 offen lds\nM 40,176,200\nR 172,184,49152\nv_mfma_f32_16x16x32_bf16 v[12:15], v[244:247], v[208:211], v[12:15]\nM 40,180,216\nM 56,176,204\n"
"R 174,184,53248\nv_mfma_f32_16x16x32_bf16 v[16:19], v[244:247], v[210:213], v[16:19]\nM 56,180,220\nG 4\nM 60,160,204\nR 176,185,40960\n"
"v_mfma_f32_16x16x32_bf16 v[20:23], v[238:241], v[210:213], v[20:23]\nM 60,164,220\nbuffer_load_dwordx4 v221, s[4:7], s63 offen offset:1024 lds\nM 44,160,200\nR 178,185,45056\nv_mfma_f32_16x16x32_bf16 v[24:27], v[240:243], v[210:213], v[24:27]\n"
"s_add_u32 m0, s50, 106496\ns_add_u32 s63, s52, 393216\nM 44,164,216\nbuffer_load_dwordx4 v220, s[4:7], s63 offen lds\nM 28,160,196\nR 180,185,49152\n"
"v_mfma_f32_16x16x32_bf16 v[28:31], v[242:245], v[210:213], v[28:31]\nM 28,164,212\nM 12,160,192\nR 182,185,53248\nM 12,164,208\nG 4\n"
"M 0,168,224\nR 160,186,40960\nM 0,172,240\nbuffer_load_dwordx4 v221, s[4:7], s63 offen offset:1024 lds\nM 16,168,228\nR 162,186,45056\n"
"s_add_u32 s63, s52, 589824\nM 16,172,244\ns_add_u32 m0, s50, 114688\nM 32,168,232\nR 164,186,49152\nM 32,172,248\n"
"buffer_load_dwordx4 v220, s[4:7], s63 offen lds\nM 48,168,236\nR 166,186,53248\nM 48,172,252\ns_waitcnt vmcnt(11)\ns_barrier\n"
"G 4\nM 52,176,236\nR 168,187,40960\nM 52,180,252\nbuffer_load_dwordx4 v221, s[4:7], s63 offen offset:1024 lds\nM 36,176,232\n"
"R 170,187,45056\ns_add_u32 s63, s52, 786432\nM 36,180,248\ns_add_u32 m0, s50, 0\nM 20,176,228\nR 172,187,49152\n"
"M 20,180,244\nbuffer_load_dwordx4 v220, s[4:7], s63 offen lds\nM 4,176,224\nR 174,187,53248\nM 4,180,240\nG 4\n"
"M 8,160,224\nR 176,184,57344\nM 8,164,240\nbuffer_load_dwordx4 v221, s[4:7], s63 offen offset:1024 lds\nM 24,160,228\nR 178,184,61440\n"
"M 24,164,244\ns_add_u32 m0, s50, 8192\nM 40,160,232\nR 180,188,0\nM 40,164,248\ns_add_u32 s63, s52, 983040\n"
"M 56,160,236\nR 182,188,4096\nM 56,164,252\nG 4\nM 60,168,236\nR 160,185,57344\n"
"M 60,172,252\nbuffer_load_dwordx4 v220, s[4:7], s63 offen lds\nM 44,168,232\nR 162,185,61440\nM 44,172,248\nM 28,168,228\n"
"R 164,189,0\nM 28,172,244\nbuffer_load_dwordx4 v221, s[4:7], s63 offen offset:1024 lds\nM 12,168,224\nR 166,189,4096\nM 12,172,240\n"
"s_waitcnt vmcnt(12)\ns_barrier\nG 4\nN 64,192,176\nR 168,186,57344\nN 64,208,180\n"
"s_mov_b32 m0, s51\nN 68,196,176\nR 170,186,61440\nN 68,212,180\nds_write_addtid_b32 v32 offset:0\nds_write_addtid_b32 v33 offset:256\n"
"N 96,200,176\nR 172,190,0\nds_write_addtid_b32 v34 offset:512\nN 96,216,180\nds_write_addtid_b32 v35 offset:768\nds_write_addtid_b32 v36 offset:1024\n"
"N 100,204,176\nR 174,190,4096\nds_write_addtid_b32 v37 offset:1280\nN 100,220,180\nds_write_addtid_b32 v38 offset:1536\nds_write_addtid_b32 v39 offset:1792\n"
"G 12\nN 108,204,160\nR 176,187,57344\nds_write_addtid_b32 v40 offset:2048\nN 108,220,164\nds_write_addtid_b32 v41 offset:2304\n"
"ds_write_addtid_b32 v42 offset:2560\nN 104,200,160\nR 178,187,61440\nds_write_addtid_b32 v43 offset:2816\nN 104,216,164\nds_write_addtid_b32 v44 offset:3072\n"
"ds_write_addtid_b32 v45 offset:3328\nN 76,196,160\nR 180,191,0\nLpg20:\ns_cbranch_execz LretW0n\nds_write_addtid_b32 v46 offset:3584\n"
"N 76,212,164\nds_write_addtid_b32 v47 offset:3840\nds_write_addtid_b32 v48 offset:4096\nN 72,192,160\nR 182,191,4096\nds_write_addtid_b32 v49 offset:4352\n"
"N 72,208,164\nds_write_addtid_b32 v50 offset:4608\nds_write_addtid_b32 v51 offset:4864\nG 15\nN 80,192,168\nR 160,188,8192\n"
"ds_write_addtid_b32 v52 offset:5120\nN 80,208,172\nds_write_addtid_b32 v53 offset:5376\nds_write_addtid_b32 v54 offset:5632\nN 84,196,168\nR 162,188,12288\n"
"ds_write_addtid_b32 v55 offset:5888\nN 84,212,172\nds_write_addtid_b32 v56 offset:6144\nds_write_addtid_b32 v57 offset:6400\nN 112,200,168\nR 164,188,16384\n"
"ds_write_addtid_b32 v58 offset:6656\nN 112,216,172\nds_write_addtid_b32 v59 offset:6912\nds_write_addtid_b32 v60 offset:7168\nN 116,204,168\nR 166,188,20480\n"
"ds_write_addtid_b32 v61 offset:7424\nN 116,220,172\nds_write_addtid_b32 v62 offset:7680\nds_write_addtid_b32 v63 offset:7936\nG 15\nN 124,204,176\n"
"R 168,189,8192\ns_add_u32 m0, s50, 16384\ns_add_u32 s63, s52, 1179648\nN 124,220,180\nbuffer_load_dwordx4 v220, s[4:7], s63 offen lds\nN 120,200,176\n"
"R 170,189,12288\ns_waitcnt vmcnt(9) lgkmcnt(2)\ns_barrier\nN 120,216,180\nds_read2st64_b32 v[32:33], v225 offset0:0 offset1:1\nds_read2st64_b32 v[34:35], v225 offset0:2 offset1:3\n"
"N 92,196,176\nR 172,189,16384\nds_read2st64_b32 v[36:37], v225 offset0:4 offset1:5\nN 92,212,180\nds_read2st64_b32 v[38:39], v225 offset0:6 offset1:7\nds_read2st64_b32 v[40:41], v225 offset0:8 offset1:9\n"
"N 88,192,176\nR 174,189,20480\nds_read2st64_b32 v[42:43], v225 offset0:10 offset1:11\nN 88,208,180\nds_read2st64_b32 v[44:45], v225 offset0:12 offset1:13\nds_read2st64_b32 v[46:47], v225 offset0:14 offset1:15\n"
"N 64,224,160\nR 176,190,8192\ns_mov_b32 s60, 156672\nv_bfe_u32 v246, v252, 4, 4\nv_lshl_add_u32 v246, v246, 3, s60\nds_read2st64_b32 v[48:49], v225 offset0:16 offset1:17\n"
"N 64,240,164\nds_read_b64 v[238:239], v246 offset:0\nds_read2st64_b32 v[50:51], v225 offset0:18 offset1:19\nG 13\nv_add_f32 v0, v0, v32\nN 68,228,160\n"
"R 178,190,12288\nds_read_b64 v[240:241], v246 offset:128\nds_read2st64_b32 v[52:53], v225 offset0:20 offset1:21\nN 68,244,164\nds_read_b64 v[242:243], v246 offset:256\nds_read2st64_b32 v[54:55], v225 offset0:22 offset1:23\n"
"v_add_f32 v1, v1, v33\nN 96,232,160\nR 180,190,16384\nds_read_b64 v[244:245], v246 offset:384\nds_read2st64_b32 v[56:57], v225 offset0:24 offset1:25\nN 96,248,164\n"
"ds_read_b64 v[246:247], v246 offset:0\nds_read2st64_b32 v[58:59], v225 offset0:26 offset1:27\nG 15\nv_add_f32 v2, v2, v34\nN 100,236,160\nR 182,190,20480\n"
"ds_read2st64_b32 v[60:61], v225 offset0:28 offset1:29\nv_add_f32 v3, v3, v35\nN 100,252,164\nds_read2st64_b32 v[62:63], v225 offset0:30 offset1:31\nv_add_f32 v4, v4, v36\nN 108,236,168\n"
"R 160,191,8192\nv_add_f32 v5, v5, v37\nv_add_f32 v6, v6, v38\nN 108,252,172\nbuffer_load_dwordx4 v221, s[4:7], s63 offen offset:1024 lds\nv_add_f32 v7, v7, v39\n"
"N 104,232,168\nR 162,191,12288\ns_add_u32 s63, s52, 1376256\nv_add_f32 v8, v8, v40\nN 104,248,172\ns_add_u32 m0, s50, 24576\n"
"v_add_f32 v9, v9, v41\nv_add_f32 v10, v10, v42\nv_add_f32 v11, v11, v43\nN 76,228,168\nR 164,191,16384\nv_add_f32 v12, v12, v44\n"
"v_add_f32 v13, v13, v45\nN 76,244,172\nG 14\nv_mfma_f32_16x16x32_bf16 v[64:67], v[208:211], v[238:241], v[64:67]\nbuffer_load_dwordx4 v220, s[4:7], s63 offen lds\nv_add_f32 v14, v14, v46\n"
"N 72,224,168\nR 166,191,20480\nv_add_f32 v15, v15, v47\nv_add_f32 v16, v16, v48\nN 72,240,172\nG 13\n"
"v_mfma_f32_16x16x32_bf16 v[72:75], v[208:211], v[240:243], v[72:75]\nG 6\nv_add_f32 v17, v17, v49\nv_add_f32 v18, v18, v50\nv_add_f32 v19, v19, v51\nN 80,224,176\n"
"R 168,188,24576\nv_add_f32 v20, v20, v52\nv_add_f32 v21, v21, v53\nN 80,240,180\nv_mfma_f32_16x16x32_bf16 v[80:83], v[208:211], v[242:245], v[80:83]\nbuffer_load_dwordx4 v221, s[4:7], s63 offen offset:1024 lds\n"
"v_add_f32 v22, v22, v54\nN 84,228,176\nR 170,188,28672\ns_add_u32 m0, s50, 49152\ns_add_u32 s63, s52, 1572864\nv_add_f32 v23, v23, v55\n"
"N 84,244,180\nv_mfma_f32_16x16x32_bf16 v[88:91], v[208:211], v[244:247], v[88:91]\nbuffer_load_dwordx4 v220, s[4:7], s63 offen lds\nv_add_f32 v24, v24, v56\nN 112,232,176\nR 172,188,32768\n"
"v_add_f32 v25, v25, v57\nN 112,248,180\nv_mfma_f32_16x16x32_bf16 v[68:71], v[210:213], v[244:247], v[68:71]\nv_add_f32 v26, v26, v58\nv_add_f32 v27, v27, v59\nN 116,236,176\n"
"R 174,188,36864\nG 9\nv_add_f32 v28, v28, v60\nN 116,252,180\nv_mfma_f32_16x16x32_bf16 v[76:79], v[210:213], v[238:241], v[76:79]\nG 4\n"
"v_add_f32 v29, v29, v61\nv_add_f32 v30, v30, v62\nN 124,236,160\nR 176,189,24576\nv_add_f32 v31, v31, v63\nN 124,252,164\n"
"v_mfma_f32_16x16x32_bf16 v[84:87], v[210:213], v[240:243], v[84:87]\nbuffer_load_dwordx4 v221, s[4:7], s63 offen offset:1024 lds\nN 120,232,160\nR 178,189,28672\ns_add_u32 m0, s50, 57344\ns_add_u32 s63, s52, 1769472\n"
"v_lshrrev_b32 v227, 1, v252\nN 120,248,164\nv_mfma_f32_16x16x32_bf16 v[92:95], v[210:213], v[242:245], v[92:95]\nbuffer_load_dwordx4 v220, s[4:7], s63 offen lds\nv_mul_f32 v16, v16, v217\nN 92,228,160\n"
"R 180,189,32768\ns_lshl4_add_u32 s64, s41, s42\nv_mul_f32 v17, v17, v217\nN 92,244,164\ns_cmp_eq_u32 s40, 3\ns_cselect_b64 s[66:67], -1, 0\n"
"s_lshl_b32 s64, s64, 12\nv_mul_f32 v18, v18, v217\nN 88,224,160\nR 182,189,36864\ns_add_u32 s64, s64, 78643200\nv_mul_f32 v19, v19, v217\n"
"N 88,240,164\ns_waitcnt vmcnt(11)\ns_barrier\nG 4\nv_mul_f32 v20, v20, v217\nv_mfma_f32_16x16x32_bf16 v[128:131], v[168:171], a[0:3], 0\n"
"R 160,190,24576\nv_mul_f32 v21, v21, v217\nv_mul_f32 v22, v22, v217\nM 128,172,16\ns_mov_b32 m0, s51\nv_mul_f32 v23, v23, v217\n"
"v_mul_f32 v24, v24, v217\nv_mul_f32 v25, v25, v217\nv_mfma_f32_16x16x32_bf16 v[144:147], v[168:171], a[4:7], 0\nR 162,190,28672\nv_mul_f32 v26, v26, v217\nv_mul_f32 v27, v27, v217\n"
"M 144,172,20\nv_mul_f32 v28, v28, v217\nv_mul_f32 v29, v29, v217\nv_mul_f32 v30, v30, v217\nv_mfma_f32_16x16x32_bf16 v[32:35], v[168:171], a[8:11], 0\nR 164,190,32768\n"
"v_mul_f32 v31, v31, v217\nv_cvt_pk_bf16_f32 v16, v16, v17\nM 32,172,24\nv_cvt_pk_bf16_f32 v17, v18, v19\nv_cvt_pk_bf16_f32 v18, v20, v21\nv_cvt_pk_bf16_f32 v19, v22, v23\n"
"v_mfma_f32_16x16x32_bf16 v[48:51], v[168:171], a[12:15], 0\nR 166,190,36864\nv_cvt_pk_bf16_f32 v20, v24, v25\nv_cvt_pk_bf16_f32 v21, v26, v27\nM 48,172,28\nds_write_addtid_b32 v96 offset:0\n"
"G 5\nv_cvt_pk_bf16_f32 v22, v28, v29\nv_cvt_pk_bf16_f32 v23, v30, v31\nv_mul_f32 v0, v0, v214\nv_mfma_f32_16x16x32_bf16 v[52:55], v[176:179], a[12:15], 0\nR 168,191,24576\n"
"ds_write_addtid_b32 v97 offset:256\nv_mul_f32 v1, v1, v214\nv_mul_f32 v2, v2, v214\nM 52,180,28\nds_write_addtid_b32 v98 offset:512\nv_mul_f32 v3, v3, v214\n"
"v_mul_f32 v4, v4, v214\nv_mul_f32 v5, v5, v214\nv_mfma_f32_16x16x32_bf16 v[36:39], v[176:179], a[8:11], 0\nR 170,191,28672\nds_write_addtid_b32 v99 offset:768\nv_mul_f32 v6, v6, v214\n"
"v_mul_f32 v7, v7, v214\nM 36,180,24\nds_write_addtid_b32 v100 offset:1024\nv_mul_f32 v8, v8, v214\nv_mul_f32 v9, v9, v214\nv_mul_f32 v10, v10, v214\n"
"v_mfma_f32_16x16x32_bf16 v[148:151], v[176:179], a[4:7], 0\nR 172,191,32768\nds_write_addtid_b32 v101 offset:1280\nv_mul_f32 v11, v11, v214\nv_mul_f32 v12, v12, v214\nM 148,180,20\n"
"ds_write_addtid_b32 v102 offset:1536\nv_mul_f32 v13, v13, v214\nv_mul_f32 v14, v14, v214\nv_mul_f32 v15, v15, v214\nv_mfma_f32_16x16x32_bf16 v[132:135], v[176:179], a[0:3], 0\nR 174,191,36864\n"
"ds_write_addtid_b32 v103 offset:1792\nv_cvt_pk_bf16_f32 v0, v0, v1\nv_cvt_pk_bf16_f32 v1, v2, v3\nM 132,180,16\nds_write_addtid_b32 v104 offset:2048\nG 13\n"
"v_cvt_pk_bf16_f32 v2, v4, v5\nv_cvt_pk_bf16_f32 v3, v6, v7\nv_cvt_pk_bf16_f32 v4, v8, v9\nv_mfma_f32_16x16x32_bf16 v[136:139], v[160:163], a[0:3], 0\nR 176,188,40960\nds_write_addtid_b32 v105 offset:2304\n"
"v_cvt_pk_bf16_f32 v5, v10, v11\nv_cvt_pk_bf16_f32 v6, v12, v13\nM 136,164,16\nds_write_addtid_b32 v106 offset:2560\nds_write_addtid_b32 v107 offset:2816\ns_mov_b64 exec, s[66:67]\n"
"buffer_store_dwordx4 v[16:19], v252, s[20:23], s64 offen offset:0 sc1\ns_mov_b64 exec, -1\nv_cvt_pk_bf16_f32 v7, v14, v15\nv_mfma_f32_16x16x32_bf16 v[152:155], v[160:163], a[4:7], 0\nR 178,188,45056\nds_write_addtid_b32 v108 offset:3072\n"
"M 152,164,20\nds_write_addtid_b32 v109 offset:3328\nds_write_addtid_b32 v110 offset:3584\nv_mfma_f32_16x16x32_bf16 v[40:43], v[160:163], a[8:11], 0\nR 180,188,49152\nds_write_addtid_b32 v111 offset:3840\n"
"M 40,164,24\nds_write_addtid_b32 v112 offset:4096\nds_write_addtid_b32 v113 offset:4352\ns_mov_b64 exec, s[66:67]\nbuffer_store_dwordx4 v[20:23], v252, s[20:23], s64 offen offset:1024 sc1\ns_mov_b64 exec, -1\n"
"v_mfma_f32_16x16x32_bf16 v[56:59], v[160:163], a[12:15], 0\nR 182,188,53248\nds_write_addtid_b32 v114 offset:4608\nM 56,164,28\nds_write_addtid_b32 v115 offset:4864\nds_write_addtid_b32 v116 offset:5120\n"
"G 15\nv_mfma_f32_16x16x32_bf16 v[60:63], v[168:171], a[12:15], 0\nR 160,189,40960\nds_write_addtid_b32 v117 offset:5376\nM 60,172,28\nds_write_addtid_b32 v118 offset:5632\n"
"ds_write_addtid_b32 v119 offset:5888\nv_mfma_f32_16x16x32_bf16 v[44:47], v[168:171], a[8:11], 0\nR 162,189,45056\nds_write_addtid_b32 v120 offset:6144\nM 44,172,24\nds_write_addtid_b32 v121 offset:6400\n"
"ds_write_addtid_b32 v122 offset:6656\nds_write_addtid_b32 v123 offset:6912\nds_write_addtid_b32 v124 offset:7168\nds_write_addtid_b32 v125 offset:7424\nds_write_addtid_b32 v126 offset:7680\nds_write_addtid_b32 v127 offset:7936\n"
"v_mfma_f32_16x16x32_bf16 v[156:159], v[168:171], a[4:7], 0\nR 164,189,49152\ns_add_u32 m0, s50, 57344\nM 156,172,20\nbuffer_load_dwordx4 v221, s[4:7], s63 offen offset:1024 lds\nv_mfma_f32_16x16x32_bf16 v[140:143], v[168:171], a[0:3], 0\n"
"R 166,189,53248\ns_waitcnt vmcnt(10) lgkmcnt(2)\ns_barrier\nM 140,172,16\nds_read2st64_b32 v[96:97], v225 offset0:0 offset1:1\nds_read2st64_b32 v[98:99], v225 offset0:2 offset1:3\n"
"v_lshrrev_b32 v226, 8, v252\nM 128,176,32\nR 168,190,40960\ns_add_u32 s63, s52, 1966080\nds_read2st64_b32 v[100:101], v225 offset0:4 offset1:5\ns_lshl_b32 s60, s40, 7\n"
"s_add_u32 s60, s60, 161280\nv_lshl_add_u32 v226, v226, 4, s60\nM 128,180,48\ns_add_u32 m0, s50, 65536\nds_read2st64_b32 v[102:103], v225 offset0:6 offset1:7\nds_read2st64_b32 v[104:105], v225 offset0:8 offset1:9\n"
"ds_read_b128 v[230:233], v226 offset:0\nM 144,176,36\nR 170,190,45056\nds_read2st64_b32 v[106:107], v225 offset0:10 offset1:11\nds_read_b128 v[234:237], v226 offset:64\nM 144,180,52\n"
"buffer_load_dwordx4 v220, s[4:7], s63 offen lds\nds_read2st64_b32 v[108:109], v225 offset0:12 offset1:13\nM 32,176,40\nR 172,190,49152\nds_read2st64_b32 v[110:111], v225 offset0:14 offset1:15\nM 32,180,56\n"
"ds_read2st64_b32 v[112:113], v225 offset0:16 offset1:17\nds_read2st64_b32 v[114:115], v225 offset0:18 offset1:19\nG 14\nv_add_f32 v64, v64, v96\nM 48,176,44\nR 174,190,53248\n"
"ds_read2st64_b32 v[116:117], v225 offset0:20 offset1:21\nv_add_f32 v65, v65, v97\nM 48,180,60\nds_read2st64_b32 v[118:119], v225 offset0:22 offset1:23\nds_read2st64_b32 v[120:121], v225 offset0:24 offset1:25\nM 52,160,44\n"
"R 176,191,40960\nds_read2st64_b32 v[122:123], v225 offset0:26 offset1:27\nG 15\nv_add_f32 v66, v66, v98\nM 52,164,60\nbuffer_load_dwordx4 v221, s[4:7], s63 offen offset:1024 lds\n"
"ds_read2st64_b32 v[124:125], v225 offset0:28 offset1:29\nM 36,160,40\nR 178,191,45056\nds_read2st64_b32 v[126:127], v225 offset0:30 offset1:31\nv_add_f32 v67, v67, v99\nM 36,164,56\n"
"s_add_u32 m0, s50, 73728\nv_add_f32 v68, v68, v100\nv_add_f32 v69, v69, v101\nv_add_f32 v70, v70, v102\nM 148,160,36\nR 180,191,49152\n"
"v_add_f32 v71, v71, v103\nv_add_f32 v72, v72, v104\nM 148,164,52\ns_add_u32 s63, s52, 2162688\nv_add_f32 v73, v73, v105\nG 15\n"
"v_add_f32 v74, v74, v106\nv_add_f32 v75, v75, v107\nM 132,160,32\nR 182,191,53248\nG 15\nv_add_f32 v76, v76, v108\n"
"v_add_f32 v77, v77, v109\nM 132,164,48\nG 10\nv_add_f32 v78, v78, v110\nv_add_f32 v79, v79, v111\nv_add_f32 v80, v80, v112\n"
"M 136,168,32\nR 160,184,0\nv_add_f32 v81, v81, v113\nv_add_f32 v82, v82, v114\nM 136,172,48\nbuffer_load_dwordx4 v220, s[4:7], s63 offen lds\n"
"v_add_f32 v83, v83, v115\nM 152,168,36\nR 162,184,4096\nG 11\nv_add_f32 v84, v84, v116\nM 152,172,52\n"
"v_add_f32 v85, v85, v117\nG 10\nv_add_f32 v86, v86, v118\nM 40,168,40\nR 164,184,8192\nv_add_f32 v87, v87, v119\n"
"M 40,172,56\nbuffer_load_dwordx4 v221, s[4:7], s63 offen offset:1024 lds\nM 56,168,44\nR 166,184,12288\ns_add_u32 m0, s50, 81920\nM 56,172,60\n"
"G 4\nv_add_f32 v88, v88, v120\nv_add_f32 v89, v89, v121\nM 60,176,44\nR 168,185,0\ns_add_u32 s63, s52, 2359296\n"
"M 60,180,60\nbuffer_load_dwordx4 v220, s[4:7], s63 offen lds\nM 44,176,40\nR 170,185,4096\nv_add_f32 v90, v90, v122\nM 44,180,56\n"
"v_add_f32 v91, v91, v123\nv_add_f32 v92, v92, v124\nM 156,176,36\nR 172,185,8192\nv_add_f32 v93, v93, v125\nM 156,180,52\n"
"buffer_load_dwordx4 v221, s[4:7], s63 offen offset:1024 lds\nM 140,176,32\nR 174,185,12288\ns_add_u32 m0, s50, 90112\nM 140,180,48\nG 4\n"
"v_add_f32 v94, v94, v126\nv_add_f32 v95, v95, v127\nM 128,160,64\nR 176,186,0\ns_add_u32 s63, s52, 2555904\nv_mul_f32 v64, v64, v230\n"
"M 128,164,80\nbuffer_load_dwordx4 v220, s[4:7], s63 offen lds\nv_mul_f32 v65, v65, v231\nv_add_u32 v108, 161792, v252\nM 144,160,68\nR 178,186,4096\n"
"ds_read_b128 v[112:115], v108\nv_mul_f32 v66, v66, v232\nv_mul_f32 v67, v67, v233\nM 144,164,84\nds_read_b128 v[104:107], v108 offset:1024\nv_mul_f32 v68, v68, v234\n"
"v_mul_f32 v69, v69, v235\nv_mul_f32 v70, v70, v236\nM 32,160,72\nR 180,186,8192\nv_mul_f32 v71, v71, v237\nv_cvt_pk_bf16_f32 v67, v66, v67\n"
"M 32,164,88\nv_cvt_pk_bf16_f32 v66, v64, v65\nv_cvt_pk_bf16_f32 v68, v68, v69\nv_cvt_pk_bf16_f32 v69, v70, v71\nM 48,160,76\nR 182,186,12288\n"
"v_mul_f32 v72, v72, v230\nv_mul_f32 v73, v73, v231\nM 48,164,92\nv_mul_f32 v74, v74, v232\nv_mul_f32 v75, v75, v233\nG 6\n"
"v_mul_f32 v76, v76, v234\nM 52,168,76\nR 160,187,0\nv_mul_f32 v77, v77, v235\nv_mul_f32 v78, v78, v236\nM 52,172,92\n"
"buffer_load_dwordx4 v221, s[4:7], s63 offen offset:1024 lds\nv_mul_f32 v79, v79, v237\nG 3\nv_lshlrev_b32 v100, 16, v104\nM 36,168,72\nR 162,187,4096\n"
"s_add_u32 s63, s52, 2752512\nv_cvt_pk_bf16_f32 v72, v72, v73\nv_and_b32 v101, s77, v104\nM 36,172,88\ns_add_u32 m0, s50, 98304\nv_cvt_pk_bf16_f32 v73, v74, v75\n"
"v_cvt_pk_bf16_f32 v74, v76, v77\nv_cvt_pk_bf16_f32 v75, v78, v79\nM 148,168,68\nR 164,187,8192\nv_mul_f32 v80, v80, v230\nv_mul_f32 v81, v81, v231\n"
"M 148,172,84\nbuffer_load_dwordx4 v220, s[4:7], s63 offen lds\nv_mul_f32 v82, v82, v232\nM 132,168,64\nR 166,187,12288\ns_waitcnt vmcnt(9)\n"
"s_barrier\nv_mul_f32 v83, v83, v233\nM 132,172,80\nG 4\nv_mul_f32 v84, v84, v234\nv_mul_f32 v85, v85, v235\n"
"v_mul_f32 v86, v86, v236\nM 136,176,64\nR 168,184,16384\nv_mul_f32 v87, v87, v237\nv_cvt_pk_bf16_f32 v78, v80, v81\nM 136,180,80\n"
"s_mov_b64 exec, s[66:67]\nbuffer_store_dwordx2 v[68:69], v227, s[20:23], s64 offen offset:2048 sc1\ns_mov_b64 exec, -1\nv_cvt_pk_bf16_f32 v79, v82, v83\nv_cvt_pk_bf16_f32 v80, v84, v85\nM 152,176,68\n"
"R 170,184,20480\nv_cvt_pk_bf16_f32 v81, v86, v87\nv_mul_f32 v88, v88, v230\nM 152,180,84\nv_mul_f32 v89, v89, v231\nv_mul_f32 v90, v90, v232\n"
"v_mul_f32 v91, v91, v233\nM 40,176,72\nR 172,184,24576\ns_mov_b32 s60, 155648\nv_bfe_u32 v246, v252, 4, 4\nv_lshl_add_u32 v246, v246, 3, s60\n"
"v_mul_f32 v92, v92, v234\nM 40,180,88\nbuffer_load_dwordx4 v221, s[4:7], s63 offen offset:1024 lds\nv_mul_f32 v93, v93, v235\nM 56,176,76\nR 174,184,28672\n"
"ds_read_b64 v[238:239], v246 offset:0\nv_mul_f32 v94, v94, v236\nM 56,180,92\nG 5\nv_mul_f32 v95, v95, v237\nv_cvt_pk_bf16_f32 v84, v88, v89\n"
"v_cvt_pk_bf16_f32 v85, v90, v91\nM 60,160,76\nR 176,185,16384\nds_read_b64 v[240:241], v246 offset:128\nv_cvt_pk_bf16_f32 v86, v92, v93\nM 60,164,92\n"
"s_add_u32 m0, s50, 106496\ns_mov_b64 exec, s[66:67]\nbuffer_store_dwordx2 v[74:75], v227, s[20:23], s64 offen offset:2560 sc1\ns_mov_b64 exec, -1\nv_cvt_pk_bf16_f32 v87, v94, v95\nv_lshlrev_b32 v102, 16, v105\n"
"M 44,160,72\nR 178,185,20480\nds_read_b64 v[242:243], v246 offset:256\ns_lshl_b32 s60, s40, 12\ns_add_u32 s60, s60, 69632\nv_add_u32 v230, s60, v252\n"
"v_add_u32 v231, s60, v227\nM 44,164,88\ns_add_u32 s63, s52, 2949120\nv_and_b32 v103, s77, v105\nv_lshlrev_b32 v96, 16, v106\nM 156,160,68\n"
"R 180,185,24576\nds_read_b64 v[244:245], v246 offset:384\nM 156,164,84\nbuffer_load_dwordx4 v220, s[4:7], s63 offen lds\nM 140,160,64\nR 182,185,28672\n"
"ds_read_b64 v[246:247], v246 offset:0\nM 140,164,80\ns_mov_b64 exec, s[66:67]\nbuffer_store_dwordx2 v[80:81], v227, s[20:23], s64 offen offset:3072 sc1\ns_mov_b64 exec, -1\nG 9\n"
"v_and_b32 v97, s77, v106\nv_lshlrev_b32 v98, 16, v107\nM 128,168,96\nR 160,186,16384\nv_and_b32 v99, s77, v107\nM 128,172,112\n"
"v_add_f32 v104, s78, v100\nLpg21:\ns_cbranch_execz LretW0n\nv_add_f32 v105, s78, v101\nM 144,168,100\nR 162,186,20480\n"
"v_add_f32 v106, s78, v102\nM 144,172,116\nv_add_f32 v107, s78, v103\nM 32,168,104\nR 164,186,24576\nG 9\n"
"v_mfma_f32_16x16x32_bf16 v[128:131], v[238:241], v[208:211], v[128:131]\nM 32,172,120\nbuffer_load_dwordx4 v221, s[4:7], s63 offen offset:1024 lds\nM 48,168,108\nR 166,186,28672\nG 8\n"
"v_mfma_f32_16x16x32_bf16 v[132:135], v[240:243], v[208:211], v[132:135]\nM 48,172,124\ns_mov_b64 exec, s[66:67]\nbuffer_store_dwordx2 v[86:87], v227, s[20:23], s64 offen offset:3584 sc1\ns_mov_b64 exec, -1\nG 5\n"
"M 52,176,108\nR 168,187,16384\nG 5\nv_mfma_f32_16x16x32_bf16 v[144:147], v[244:247], v[210:213], v[144:147]\ns_not_b64 exec, s[66:67]\nds_write_b128 v230, v[16:19] offset:0\n"
"ds_write_b128 v230, v[20:23] offset:1024\ns_mov_b64 exec, -1\nM 52,180,124\nM 36,176,104\nR 170,187,20480\nv_mfma_f32_16x16x32_bf16 v[148:151], v[238:241], v[210:213], v[148:151]\n"
"M 36,180,120\ns_movk_i32 s63, 2048\nv_mad_u32_u24 v220, v228, s63, v229\nv_add_u32 v221, 1047552, v220\ns_add_u32 m0, s50, 114688\ns_add_u32 s63, s53, 0\n"
"buffer_load_dwordx4 v220, s[8:11], s63 offen lds\nM 148,176,100\nR 172,187,24576\nM 148,180,116\ns_not_b64 exec, s[66:67]\nds_write_b64 v231, v[68:69] offset:2048\n"
"ds_write_b64 v231, v[74:75] offset:2560\ns_mov_b64 exec, -1\nM 132,176,96\nR 174,187,28672\nM 132,180,112\nG 8\n"
"M 136,160,96\nR 176,184,49152\nM 136,164,112\nM 152,160,100\nR 178,184,53248\nv_lshrrev_b32 v248, 5, v252\n"
"M 152,164,116\nbuffer_load_dwordx4 v221, s[8:11], s63 offen offset:1024 lds\nM 40,160,104\nR 180,184,57344\nv_lshlrev_b32 v248, 5, v248\nM 40,164,120\n"
"s_and_b32 s60, s41, 7\ns_lshl2_add_u32 s60, s60, s40\nv_add_u32 v248, s60, v248\nM 56,160,108\nR 182,184,61440\nv_mul_u32_u24 v248, 7168, v248\n"
"M 56,164,124\nG 4\nv_bfe_u32 v249, v252, 4, 1\nM 60,168,108\nR 160,185,49152\ns_not_b64 exec, s[66:67]\n"
"ds_write_b64 v231, v[80:81] offset:3072\nds_write_b64 v231, v[86:87] offset:3584\ns_mov_b64 exec, -1\nM 60,172,124\ns_sub_u32 s60, s54, s54\ns_add_u32 s60, s60, 128\n"
"v_mul_lo_u32 v249, v249, s60\nM 44,168,104\nR 162,185,53248\nv_add_u32 v248, v248, v249\nM 44,172,120\ns_add_u32 m0, s50, 0\n"
"M 156,168,100\nR 164,185,57344\nM 156,172,116\ns_add_u32 s63, s53, 65536\nM 140,168,96\nR 166,185,61440\n"
"M 140,172,112\nG 6\nM 128,176,128\nR 168,186,49152\ns_waitcnt vmcnt(12) lgkmcnt(4)\ns_barrier\n"
"M 128,180,144\nbuffer_load_dwordx4 v220, s[8:11], s63 offen lds\nM 144,176,132\nR 170,186,53248\nM 144,180,148\ns_cmp_eq_u32 s78, 0\n"
"s_cbranch_scc1 Lq650\nv_mov_b32 v230, 0\nv_mov_b32 v231, 0\nv_mov_b32 v232, 0\nv_mov_b32 v233, 0\nv_mov_b32 v64, 0\n"
"v_mov_b32 v65, 0\nv_mov_b32 v70, 0\nv_mov_b32 v71, 0\nv_mov_b32 v234, 0\nv_mov_b32 v235, 0\nv_mov_b32 v236, 0\n"
"v_mov_b32 v237, 0\nv_mov_b32 v76, 0\nv_mov_b32 v77, 0\nv_mov_b32 v82, 0\nv_mov_b32 v83, 0\nLq650:\n"
"s_lshl4_add_u32 s65, s41, s42\nM 32,176,136\nR 172,186,57344\ns_lshl_b32 s65, s65, 12\nM 32,180,152\ns_add_u32 s65, s65, 78577664\n"
"M 48,176,140\nR 174,186,61440\ns_bfm_b64 exec, 16, 0\nbuffer_load_dword v249, v248, s[12:15], s54 offen\ns_mov_b64 exec, -1\nM 48,180,156\n"
"G 4\nM 52,160,140\nR 176,187,49152\nM 52,164,156\nbuffer_load_dwordx4 v221, s[8:11], s63 offen offset:1024 lds\nM 36,160,136\n"
"R 178,187,53248\nM 36,164,152\ns_add_u32 m0, s50, 8192\nM 148,160,132\nR 180,187,57344\nM 148,164,148\n"
"s_add_u32 s63, s53, 131072\nM 132,160,128\nR 182,187,61440\nM 132,164,144\nG 4\nM 136,168,128\n"
"R 160,188,0\nM 136,172,144\nbuffer_load_dwordx4 v220, s[8:11], s63 offen lds\nM 152,168,132\nR 162,188,4096\nM 152,172,148\n"
"M 40,168,136\nR 164,188,8192\nM 40,172,152\nM 56,168,140\nR 166,188,12288\nv_mfma_f32_16x16x32_bf16 v[136:139], v[242:245], v[208:211], v[136:139]\n"
"s_bfm_b64 exec, 16, 16\nbuffer_load_dword v249, v248, s[12:15], s54 offen\ns_mov_b64 exec, -1\nM 56,172,156\nG 4\nM 60,176,140\n"
"R 168,189,0\nv_mfma_f32_16x16x32_bf16 v[140:143], v[244:247], v[208:211], v[140:143]\nM 60,180,156\nbuffer_load_dwordx4 v221, s[8:11], s63 offen offset:1024 lds\nM 44,176,136\nR 170,189,4096\n"
"v_mfma_f32_16x16x32_bf16 v[152:155], v[240:243], v[210:213], v[152:155]\ns_add_u32 s63, s53, 196608\nM 44,180,152\ns_add_u32 m0, s50, 16384\nM 156,176,132\nR 172,189,8192\n"
"v_mfma_f32_16x16x32_bf16 v[156:159], v[242:245], v[210:213], v[156:159]\nM 156,180,148\nbuffer_load_dwordx4 v220, s[8:11], s63 offen lds\nM 140,176,128\nR 174,189,12288\nM 140,180,144\n"
"G 4\nM 128,160,160\nR 176,190,0\nM 128,164,176\nM 144,160,164\nR 178,190,4096\n"
"M 144,164,180\nbuffer_load_dwordx4 v221, s[8:11], s63 offen offset:1024 lds\nM 32,160,168\nR 180,190,8192\nM 32,164,184\ns_add_u32 m0, s50, 24576\n"
"M 48,160,172\nR 182,190,12288\ns_bfm_b64 exec, 16, 32\nbuffer_load_dword v249, v248, s[12:15], s54 offen\ns_mov_b64 exec, -1\nM 48,164,188\n"
"G 4\nM 52,168,172\nR 160,191,0\nM 52,172,188\ns_add_u32 s63, s53, 262144\nM 36,168,168\n"
"R 162,191,4096\nM 36,172,184\nbuffer_load_dwordx4 v220, s[8:11], s63 offen lds\nM 148,168,164\nR 164,191,8192\ns_lshl_b32 s60, s40, 12\n"
"s_add_u32 s60, s60, 65536\nv_add_u32 v226, s60, v252\nv_add_u32 v111, s60, v227\nM 148,172,180\nM 132,168,160\nR 166,191,12288\n"
"s_cmp_eq_u32 s40, 0\ns_cselect_b64 s[88:89], 0, -1\ns_cselect_b32 s60, s41, 0\ns_cmp_lg_u32 s60, 0\ns_cselect_b64 s[66:67], -1, 0\nM 132,172,176\n"
"G 4\nM 136,176,160\nR 168,188,16384\nM 136,180,176\ns_mov_b64 exec, s[88:89]\nds_read_b128 v[230:233], v226 offset:0\n"
"ds_read_b64 v[64:65], v111 offset:2048\ns_mov_b64 exec, -1\nM 152,176,164\nR 170,188,20480\ns_waitcnt vmcnt(13) lgkmcnt(1)\ns_barrier\n"
"M 152,180,180\nbuffer_load_dwordx4 v221, s[8:11], s63 offen offset:1024 lds\nM 40,176,168\nR 172,188,24576\nM 40,180,184\ns_mov_b64 exec, s[88:89]\n"
"ds_read_b64 v[70:71], v111 offset:2560\nds_read_b128 v[234:237], v226 offset:1024\ns_mov_b64 exec, -1\nM 56,176,172\nR 174,188,28672\ns_bfm_b64 exec, 16, 48\n"
"buffer_load_dword v249, v248, s[12:15], s54 offen\ns_mov_b64 exec, -1\nM 56,180,188\nM 60,160,172\nR 176,189,16384\nM 60,164,188\n"
"s_mov_b64 exec, s[88:89]\nds_read_b64 v[76:77], v111 offset:3072\nds_read_b64 v[82:83], v111 offset:3584\ns_mov_b64 exec, -1\nM 44,160,168\nR 178,189,20480\n"
"M 44,164,184\nM 156,160,164\nR 180,189,24576\nM 156,164,180\ns_mov_b64 exec, s[66:67]\nbuffer_load_dwordx4 v[230:233], v252, s[20:23], s65 offen offset:0 sc1\n"
"s_mov_b64 exec, -1\nM 140,160,160\nR 182,189,28672\nM 140,164,176\nG 6\nM 32,168,200\n"
"R 160,190,16384\ns_mov_b64 exec, s[66:67]\nbuffer_load_dwordx2 v[64:65], v227, s[20:23], s65 offen offset:2048 sc1\ns_mov_b64 exec, -1\nM 32,172,216\nR 162,190,20480\n"
"M 48,168,204\nR 164,190,24576\nM 48,172,220\nR 166,190,28672\nG 4\nM 52,176,204\n"
"R 168,191,16384\ns_mov_b64 exec, s[66:67]\nbuffer_load_dwordx2 v[70:71], v227, s[20:23], s65 offen offset:2560 sc1\ns_mov_b64 exec, -1\nM 52,180,220\nR 170,191,20480\n"
"M 36,176,200\nR 172,191,24576\nM 36,180,216\nR 174,191,28672\nG 4\nM 40,160,200\n"
"R 176,188,32768\ns_mov_b64 exec, s[66:67]\nbuffer_load_dwordx4 v[234:237], v252, s[20:23], s65 offen offset:1024 sc1\ns_mov_b64 exec, -1\nM 40,164,216\nR 178,188,36864\n"
"M 56,160,204\nR 180,188,40960\nM 56,164,220\nR 182,188,45056\nG 4\nM 60,168,204\n"
"R 160,189,32768\ns_mov_b64 exec, s[66:67]\nbuffer_load_dwordx2 v[76:77], v227, s[20:23], s65 offen offset:3072 sc1\ns_mov_b64 exec, -1\nM 60,172,220\nR 162,189,36864\n"
"M 44,168,200\nR 164,189,40960\nM 44,172,216\nR 166,189,45056\nG 4\nM 32,176,232\n"
"R 168,190,32768\ns_mov_b64 exec, s[66:67]\nbuffer_load_dwordx2 v[82:83], v227, s[20:23], s65 offen offset:3584 sc1\ns_mov_b64 exec, -1\nLq684:\ns_lshr_b32 s60, s45, 3\n"
"s_lshl2_add_u32 s60, s60, s40\ns_lshl_b32 s60, s60, 8\nv_lshl_add_u32 v224, v252, 3, s60\nv_add_u32 v224, Lq684+4096-LqPC, v224\nv_min_u32 v224, LqE-LqPC-4, v224\ns_mov_b64 exec, 3\n"
"global_load_dword v224, v224, s[98:99]\ns_mov_b64 exec, -1\nM 32,180,248\nR 170,190,36864\nM 48,176,236\nR 172,190,40960\n"
"M 48,180,252\nR 174,190,45056\nG 4\nM 52,160,236\nR 176,191,32768\nM 52,164,252\n"
"R 178,191,36864\nM 36,160,232\nR 180,191,40960\nM 36,164,248\nR 182,191,45056\nG 4\n"
"M 40,168,232\nR 160,188,16384\nM 40,172,248\nR 162,188,20480\nM 56,168,236\nR 164,188,24576\n"
"M 56,172,252\nR 166,188,28672\nG 4\nM 60,176,236\nR 168,189,16384\nM 60,180,252\n"
"R 170,189,20480\nM 44,176,232\nR 172,189,24576\nM 44,180,248\nR 174,189,28672\nG 4\n"
"M 128,160,192\nR 176,190,16384\nM 128,164,208\nR 178,190,20480\ns_mov_b32 m0, s51\nM 144,160,196\n"
"R 180,190,24576\nds_write_addtid_b32 v32 offset:0\nM 144,164,212\nR 182,190,28672\nds_write_addtid_b32 v33 offset:256\nG 6\n"
"M 148,168,196\nR 160,191,16384\nds_write_addtid_b32 v34 offset:512\nM 148,172,212\nR 162,191,20480\nds_write_addtid_b32 v35 offset:768\n"
"M 132,168,192\nR 164,191,24576\nds_write_addtid_b32 v36 offset:1024\nM 132,172,208\nR 166,191,28672\nds_write_addtid_b32 v37 offset:1280\n"
"G 9\nM 136,176,192\nR 168,188,32768\nds_write_addtid_b32 v38 offset:1536\nM 136,180,208\nR 170,188,36864\n"
"ds_write_addtid_b32 v39 offset:1792\nM 152,176,196\nR 172,188,40960\nds_write_addtid_b32 v40 offset:2048\nM 152,180,212\nR 174,188,45056\n"
"ds_write_addtid_b32 v41 offset:2304\nG 9\nM 156,160,196\nR 176,189,32768\nds_write_addtid_b32 v42 offset:2560\nM 156,164,212\n"
"R 178,189,36864\nds_write_addtid_b32 v43 offset:2816\nM 140,160,192\nR 180,189,40960\nds_write_addtid_b32 v44 offset:3072\nds_write_addtid_b32 v45 offset:3328\n"
"ds_write_addtid_b32 v46 offset:3584\nds_write_addtid_b32 v47 offset:3840\nds_write_addtid_b32 v48 offset:4096\nds_write_addtid_b32 v49 offset:4352\nds_write_addtid_b32 v50 offset:4608\nds_write_addtid_b32 v51 offset:4864\n"
"ds_write_addtid_b32 v52 offset:5120\nds_write_addtid_b32 v53 offset:5376\nds_write_addtid_b32 v54 offset:5632\nds_write_addtid_b32 v55 offset:5888\nds_write_addtid_b32 v56 offset:6144\nds_write_addtid_b32 v57 offset:6400\n"
"ds_write_addtid_b32 v58 offset:6656\nds_write_addtid_b32 v59 offset:6912\nds_write_addtid_b32 v60 offset:7168\nds_write_addtid_b32 v61 offset:7424\nds_write_addtid_b32 v62 offset:7680\nds_write_addtid_b32 v63 offset:7936\n"
"M 140,164,208\nR 182,189,45056\nG 15\nM 128,168,224\nR 160,190,32768\ns_add_u32 m0, s50, 32768\n"
"M 128,172,240\nR 162,190,36864\nG 3\ns_barrier\ns_add_u32 s63, s53, 327680\nM 144,168,228\n"
"R 164,190,40960\nbuffer_load_dwordx4 v220, s[8:11], s63 offen lds\nds_read2st64_b32 v[32:33], v225 offset0:0 offset1:1\nM 144,172,244\nR 166,190,45056\nbuffer_load_dwordx4 v221, s[8:11], s63 offen offset:1024 lds\n"
"ds_read2st64_b32 v[34:35], v225 offset0:2 offset1:3\nG 6\nM 148,176,228\nR 168,191,32768\ns_add_u32 m0, s50, 40960\nds_read2st64_b32 v[36:37], v225 offset0:4 offset1:5\n"
"M 148,180,244\nR 170,191,36864\ns_add_u32 s63, s53, 393216\nds_read2st64_b32 v[38:39], v225 offset0:6 offset1:7\nM 132,176,224\nR 172,191,40960\n"
"ds_read2st64_b32 v[40:41], v225 offset0:8 offset1:9\nM 132,180,240\nR 174,191,45056\nds_read2st64_b32 v[42:43], v225 offset0:10 offset1:11\nG 9\nM 136,160,224\n"
"buffer_load_dwordx4 v220, s[8:11], s63 offen lds\nds_read2st64_b32 v[44:45], v225 offset0:12 offset1:13\nM 136,164,240\nds_read2st64_b32 v[46:47], v225 offset0:14 offset1:15\nds_read2st64_b32 v[48:49], v225 offset0:16 offset1:17\nM 152,160,228\n"
"buffer_load_dwordx4 v221, s[8:11], s63 offen offset:1024 lds\nds_read2st64_b32 v[50:51], v225 offset0:18 offset1:19\nM 152,164,244\nds_read2st64_b32 v[52:53], v225 offset0:20 offset1:21\nds_read2st64_b32 v[54:55], v225 offset0:22 offset1:23\nG 7\n"
"M 156,168,228\nds_read2st64_b32 v[56:57], v225 offset0:24 offset1:25\nds_read2st64_b32 v[58:59], v225 offset0:26 offset1:27\nM 156,172,244\nds_read2st64_b32 v[60:61], v225 offset0:28 offset1:29\nds_read2st64_b32 v[62:63], v225 offset0:30 offset1:31\n"
"M 140,168,224\nM 140,172,240\nG 0\ns_barrier\nv_pk_add_f32 v[144:145], v[144:145], v[48:49]\nv_pk_add_f32 v[146:147], v[146:147], v[50:51]\n"
"v_pk_add_f32 v[148:149], v[148:149], v[52:53]\nv_pk_add_f32 v[150:151], v[150:151], v[54:55]\nv_pk_add_f32 v[152:153], v[152:153], v[56:57]\nv_pk_add_f32 v[154:155], v[154:155], v[58:59]\nv_pk_add_f32 v[156:157], v[156:157], v[60:61]\nv_pk_add_f32 v[158:159], v[158:159], v[62:63]\n"
"v_pk_add_f32 v[128:129], v[128:129], v[32:33]\nv_pk_add_f32 v[130:131], v[130:131], v[34:35]\nv_pk_add_f32 v[132:133], v[132:133], v[36:37]\nv_pk_add_f32 v[134:135], v[134:135], v[38:39]\nv_pk_add_f32 v[136:137], v[136:137], v[40:41]\nv_pk_add_f32 v[138:139], v[138:139], v[42:43]\n"
"v_pk_add_f32 v[140:141], v[140:141], v[44:45]\nv_pk_add_f32 v[142:143], v[142:143], v[46:47]\nv_cvt_pk_bf16_f32 v144, v144, v145\nv_cvt_pk_bf16_f32 v145, v146, v147\nv_cvt_pk_bf16_f32 v146, v148, v149\nv_cvt_pk_bf16_f32 v147, v150, v151\n"
"v_cvt_pk_bf16_f32 v152, v152, v153\nv_cvt_pk_bf16_f32 v153, v154, v155\nv_cvt_pk_bf16_f32 v154, v156, v157\nv_cvt_pk_bf16_f32 v155, v158, v159\nv_cvt_pk_bf16_f32 v128, v128, v129\nv_cvt_pk_bf16_f32 v129, v130, v131\n"
"v_cvt_pk_bf16_f32 v130, v132, v133\nv_cvt_pk_bf16_f32 v131, v134, v135\nv_cvt_pk_bf16_f32 v136, v136, v137\nv_cvt_pk_bf16_f32 v137, v138, v139\nv_cvt_pk_bf16_f32 v138, v140, v141\nv_cvt_pk_bf16_f32 v139, v142, v143\n"
"v_mfma_f32_16x16x32_bf16 v[52:55], v[16:19], v[144:147], v[96:99]\nv_mfma_f32_16x16x32_bf16 v[48:51], v[0:3], v[144:147], v[100:103]\nv_mfma_f32_16x16x32_bf16 v[60:63], v[20:23], v[152:155], v[96:99]\nv_mfma_f32_16x16x32_bf16 v[56:59], v[4:7], v[152:155], v[100:103]\ns_waitcnt vmcnt(8)\nv_max3_u32 v248, v233, v65, v71\n"
"v_cmp_eq_u32 vcc, -1, v248\ns_nop 0\ns_cbranch_vccnz Lq686\nLq687:\ns_waitcnt vmcnt(5)\nv_max3_u32 v248, v237, v77, v83\n"
"v_cmp_eq_u32 vcc, -1, v248\ns_nop 0\ns_cbranch_vccnz Lq688\nLq689:\nv_mfma_f32_16x16x32_bf16 v[36:39], v[0:3], v[128:131], v[96:99]\nv_mfma_f32_16x16x32_bf16 v[32:35], v[230:233], v[128:131], v[104:107]\n"
"v_mfma_f32_16x16x32_bf16 v[44:47], v[4:7], v[136:139], v[96:99]\nv_mfma_f32_16x16x32_bf16 v[40:43], v[234:237], v[136:139], v[104:107]\ns_lshl_b32 s84, s42, 11\ns_add_u32 s84, s84, s49\ns_add_u32 s84, s84, 6291456\ns_add_u32 s85, s84, 32768\n"
"v_mov_b32 v238, s46\nv_mov_b32 v239, s47\nv_max_f32 v52, v52, v53\nv_max_f32 v54, v54, v55\nv_max3_f32 v52, v52, v48, v49\nv_max3_f32 v54, v54, v50, v51\n"
"v_max_f32 v118, v52, v54\nv_max_f32 v60, v60, v61\nv_max_f32 v62, v62, v63\nv_max3_f32 v60, v60, v56, v57\nv_max3_f32 v62, v62, v58, v59\nv_max_f32 v119, v60, v62\n"
"v_max_f32 v36, v36, v37\nv_max_f32 v38, v38, v39\nv_max3_f32 v36, v36, v32, v33\nv_max3_f32 v38, v38, v34, v35\nv_max_f32 v116, v36, v38\nv_max_f32 v44, v44, v45\n"
"v_max_f32 v46, v46, v47\nv_max3_f32 v44, v44, v40, v41\nv_max3_f32 v46, v46, v42, v43\nv_max_f32 v117, v44, v46\nv_permlane32_swap_b32 v116, v118\ns_nop 0\n"
"v_permlane32_swap_b32 v117, v119\nv_max_f32 v116, v116, v118\nv_max_f32 v117, v117, v119\ns_nop 1\nv_permlane16_swap_b32 v116, v117\nv_max_f32 v116, v116, v117\n"
"v_mov_b32 v117, v116\ns_nop 1\nv_permlane16_swap_b32 v116, v117\nv_mov_b32 v118, v116\nv_mov_b32 v119, v117\ns_nop 0\n"
"v_permlane32_swap_b32 v116, v118\nv_permlane32_swap_b32 v117, v119\nv_mul_f32 v116, v116, v215\nv_mul_f32 v118, v118, v218\nv_mul_f32 v117, v117, v215\nLpg22:\n"
"s_cbranch_execz LretW0n\nv_mul_f32 v119, v119, v218\nv_fma_f32 v52, v52, v218, -v118\nv_fma_f32 v54, v54, v218, -v118\nv_fma_f32 v60, v60, v218, -v119\nv_fma_f32 v62, v62, v218, -v119\n"
"v_fma_f32 v36, v36, v215, -v116\nv_fma_f32 v38, v38, v215, -v116\nv_fma_f32 v44, v44, v215, -v117\nv_fma_f32 v46, v46, v215, -v117\nv_exp_f32 v52, v52\nv_exp_f32 v54, v54\n"
"v_exp_f32 v60, v60\nv_exp_f32 v62, v62\nv_exp_f32 v36, v36\nv_exp_f32 v38, v38\nv_exp_f32 v44, v44\nv_exp_f32 v46, v46\n"
"v_add_f32 v118, v52, v54\nv_add_f32 v119, v60, v62\nv_add_f32 v116, v36, v38\nv_add_f32 v117, v44, v46\ns_nop 0\nv_permlane32_swap_b32 v116, v118\n"
"v_permlane32_swap_b32 v117, v119\nv_add_f32 v116, v116, v118\nv_add_f32 v117, v117, v119\ns_nop 1\nv_permlane16_swap_b32 v116, v117\nv_add_f32 v116, v116, v117\n"
"v_rcp_f32 v116, v116\ns_nop 0\nv_mov_b32 v117, v116\ns_nop 1\nv_permlane16_swap_b32 v116, v117\nv_mov_b32 v118, v116\n"
"v_mov_b32 v119, v117\ns_nop 0\nv_permlane32_swap_b32 v116, v118\nv_permlane32_swap_b32 v117, v119\nv_mul_f32 v52, v52, v118\nv_mul_f32 v54, v54, v118\n"
"v_mul_f32 v60, v60, v119\nv_mul_f32 v62, v62, v119\nv_mul_f32 v36, v36, v116\nv_mul_f32 v38, v38, v116\nv_mul_f32 v44, v44, v117\nv_mul_f32 v46, v46, v117\n"
"v_cvt_pk_bf16_f32 v52, v52, v52\nv_cvt_pk_bf16_f32 v54, v54, v54\nv_cvt_pk_bf16_f32 v60, v60, v60\nv_cvt_pk_bf16_f32 v62, v62, v62\nv_cvt_pk_bf16_f32 v36, v36, v36\nv_cvt_pk_bf16_f32 v38, v38, v38\n"
"v_cvt_pk_bf16_f32 v44, v44, v44\nv_cvt_pk_bf16_f32 v46, v46, v46\nv_and_b32 v48, v52, v112\nv_and_b32 v49, v54, v113\nv_and_b32 v50, v52, v114\nv_and_b32 v51, v54, v115\n"
"v_and_b32 v56, v60, v112\nv_and_b32 v57, v62, v113\nv_and_b32 v58, v60, v114\nv_and_b32 v59, v62, v115\nv_mfma_f32_16x16x32_bf16 v[144:147], v[66:69], v[48:51], 0\nv_mfma_f32_16x16x32_bf16 v[148:151], v[72:75], v[48:51], 0\n"
"v_and_b32 v32, v36, v112\nv_and_b32 v33, v38, v113\nv_and_b32 v34, v36, v114\nv_and_b32 v35, v38, v115\nv_mfma_f32_16x16x32_bf16 v[152:155], v[78:81], v[56:59], 0\nv_mfma_f32_16x16x32_bf16 v[156:159], v[84:87], v[56:59], 0\n"
"v_and_b32 v40, v44, v112\nv_and_b32 v41, v46, v113\nv_and_b32 v42, v44, v114\nv_and_b32 v43, v46, v115\nv_mfma_f32_16x16x32_bf16 v[128:131], v[64:67], v[32:35], 0\nv_mfma_f32_16x16x32_bf16 v[132:135], v[70:73], v[32:35], 0\n"
"v_mfma_f32_16x16x32_bf16 v[136:139], v[76:79], v[40:43], 0\nv_mfma_f32_16x16x32_bf16 v[140:143], v[82:85], v[40:43], 0\nv_cvt_pk_bf16_f32 v144, v144, v145\nv_cvt_pk_bf16_f32 v145, v146, v147\nv_cvt_pk_bf16_f32 v146, v148, v149\nv_cvt_pk_bf16_f32 v147, v150, v151\n"
"buffer_store_dwordx4 v[144:147], v252, s[20:23], s85 offen offset:0 sc1\nv_cvt_pk_bf16_f32 v152, v152, v153\nv_cvt_pk_bf16_f32 v153, v154, v155\nv_cvt_pk_bf16_f32 v154, v156, v157\nv_cvt_pk_bf16_f32 v155, v158, v159\nbuffer_store_dwordx4 v[152:155], v252, s[20:23], s85 offen offset:1024 sc1\n"
"v_cvt_pk_bf16_f32 v128, v128, v129\nv_cvt_pk_bf16_f32 v129, v130, v131\nv_cvt_pk_bf16_f32 v130, v132, v133\nv_cvt_pk_bf16_f32 v131, v134, v135\nbuffer_store_dwordx4 v[128:131], v252, s[20:23], s84 offen offset:0 sc1\nv_cvt_pk_bf16_f32 v136, v136, v137\n"
"v_cvt_pk_bf16_f32 v137, v138, v139\nv_cvt_pk_bf16_f32 v138, v140, v141\nv_cvt_pk_bf16_f32 v139, v142, v143\nbuffer_store_dwordx4 v[136:139], v252, s[20:23], s84 offen offset:1024 sc1\ns_mov_b64 exec, 0\ns_cmp_eq_u32 s40, 1\n"
"s_cbranch_scc1 LretO1\ns_cmp_eq_u32 s40, 2\ns_cbranch_scc1 LretO1\ns_cmp_eq_u32 s40, 3\ns_cbranch_scc1 LretO1\ns_branch Lpg23\n"
"LretO1:\ns_mov_b64 exec, -1\ns_add_u32 s61, s79, 88498176\ns_lshl_b32 s62, s42, 3\ns_add_u32 s61, s61, s62\ns_waitcnt vmcnt(0)\n"
"s_mov_b64 exec, 1\nbuffer_atomic_swap_x2 v[238:239], off, s[20:23], s61 sc1\ns_mov_b64 exec, -1\nv_bfe_u32 v250, v252, 7, 1\nv_mul_u32_u24 v250, 2176, v250\nv_bfe_u32 v251, v252, 4, 3\n"
"v_lshl_add_u32 v250, v251, 3, v250\ns_lshl_b32 s60, s90, 6\nv_add_u32 v250, s60, v250\ns_sleep 5\nv_mov_b32 v248, 0\nv_mov_b32 v249, 0\n"
"s_add_u32 s61, s80, 88498176\ns_mov_b64 exec, 0xffff\nbuffer_atomic_or_x2 v[248:249], v250, s[20:23], s61 offen sc0 sc1\ns_mov_b64 exec, -1\ns_add_u32 m0, s50, 49152\ns_add_u32 s63, s53, 458752\n"
"buffer_load_dwordx4 v220, s[8:11], s63 offen lds\nbuffer_load_dwordx4 v221, s[8:11], s63 offen offset:1024 lds\ns_add_u32 m0, s50, 57344\ns_add_u32 s63, s53, 524288\nbuffer_load_dwordx4 v220, s[8:11], s63 offen lds\nbuffer_load_dwordx4 v221, s[8:11], s63 offen offset:1024 lds\n"
"s_add_u32 m0, s50, 65536\ns_add_u32 s63, s53, 589824\nbuffer_load_dwordx4 v220, s[8:11], s63 offen lds\nbuffer_load_dwordx4 v221, s[8:11], s63 offen offset:1024 lds\ns_add_u32 m0, s50, 73728\ns_add_u32 s63, s53, 655360\n"
"buffer_load_dwordx4 v220, s[8:11], s63 offen lds\nbuffer_load_dwordx4 v221, s[8:11], s63 offen offset:1024 lds\ns_add_u32 m0, s50, 81920\ns_add_u32 s63, s53, 720896\nbuffer_load_dwordx4 v220, s[8:11], s63 offen lds\nbuffer_load_dwordx4 v221, s[8:11], s63 offen offset:1024 lds\n"
"s_add_u32 m0, s50, 90112\ns_add_u32 s63, s53, 786432\nbuffer_load_dwordx4 v220, s[8:11], s63 offen lds\nbuffer_load_dwordx4 v221, s[8:11], s63 offen offset:1024 lds\ns_add_u32 m0, s50, 98304\ns_add_u32 s63, s53, 851968\n"
"buffer_load_dwordx4 v220, s[8:11], s63 offen lds\nbuffer_load_dwordx4 v221, s[8:11], s63 offen offset:1024 lds\ns_add_u32 m0, s50, 106496\ns_add_u32 s63, s53, 917504\nbuffer_load_dwordx4 v220, s[8:11], s63 offen lds\nbuffer_load_dwordx4 v221, s[8:11], s63 offen offset:1024 lds\n"
"v_bfe_u32 v234, v252, 4, 4\nv_lshrrev_b32 v235, 8, v252\nv_lshlrev_b32 v235, 2, v235\nv_sub_u32 v234, v234, v235\nv_lshlrev_b32 v235, 4, v234\ns_mov_b64 s[60:61], 0x3f80\n"
"v_lshlrev_b64 v[208:209], v235, s[60:61]\nv_cmp_gt_u32 s[60:61], 4, v234\ns_nop 1\nv_cndmask_b32 v212, 0, v208, s[60:61]\nv_cndmask_b32 v208, 0, v208, s[60:61]\nv_cndmask_b32 v213, 0, v209, s[60:61]\n"
"v_cndmask_b32 v209, 0, v209, s[60:61]\ns_nop 1\nv_mfma_f32_16x16x32_bf16 v[0:3], v[208:211], v[192:195], 0\nv_mfma_f32_16x16x32_bf16 v[4:7], v[210:213], v[192:195], 0\nv_mfma_f32_16x16x32_bf16 v[8:11], v[208:211], v[196:199], 0\nv_mfma_f32_16x16x32_bf16 v[12:15], v[210:213], v[196:199], 0\n"
"v_mfma_f32_16x16x32_bf16 v[16:19], v[208:211], v[200:203], 0\nv_mfma_f32_16x16x32_bf16 v[20:23], v[210:213], v[200:203], 0\nv_mfma_f32_16x16x32_bf16 v[24:27], v[208:211], v[204:207], 0\nv_mfma_f32_16x16x32_bf16 v[28:31], v[210:213], v[204:207], 0\ns_waitcnt vmcnt(16)\nv_cmp_eq_u64 s[86:87], s[46:47], v[248:249]\n"
"s_nop 1\ns_and_b32 s62, s86, 0xffff\ns_cmp_eq_u32 s62, 0xffff\ns_cbranch_scc1 Lq692\nLq691:\nv_mov_b32 v248, 0\n"
"v_mov_b32 v249, 0\ns_add_u32 s61, s80, 88498176\ns_mov_b64 exec, 0xffff\nbuffer_atomic_or_x2 v[248:249], v250, s[20:23], s61 offen sc0 sc1\ns_mov_b64 exec, -1\ns_waitcnt vmcnt(0)\n"
"v_cmp_eq_u64 s[86:87], s[46:47], v[248:249]\ns_nop 1\ns_and_b32 s62, s86, 0xffff\ns_cmp_eq_u32 s62, 0xffff\ns_cbranch_scc1 Lq692\ns_sub_u32 s48, s48, 1\n"
"s_cmp_gt_i32 s48, 0\ns_cbranch_scc1 Lq691\nLq692:\ns_add_u32 s64, s70, 6291456\nbuffer_load_dwordx4 a[0:3], v252, s[20:23], s64 offen offset:0 sc1\ns_add_u32 s65, s70, 6324224\n"
"buffer_load_dwordx4 a[4:7], v252, s[20:23], s65 offen offset:0 sc1\ns_add_u32 s66, s71, 6291456\nbuffer_load_dwordx4 a[8:11], v252, s[20:23], s66 offen offset:0 sc1\ns_add_u32 s67, s71, 6324224\nbuffer_load_dwordx4 a[12:15], v252, s[20:23], s67 offen offset:0 sc1\nbuffer_load_dwordx4 a[16:19], v252, s[20:23], s64 offen offset:1024 sc1\n"
"buffer_load_dwordx4 a[20:23], v252, s[20:23], s65 offen offset:1024 sc1\nbuffer_load_dwordx4 a[24:27], v252, s[20:23], s66 offen offset:1024 sc1\nbuffer_load_dwordx4 a[28:31], v252, s[20:23], s67 offen offset:1024 sc1\ns_barrier\nR 160,188,49152\nR 162,188,53248\n"
"R 164,184,0\nR 166,184,4096\nR 168,189,49152\nR 170,189,53248\nR 172,185,0\nR 174,185,4096\n"
"R 176,190,49152\nR 178,190,53248\nR 180,186,0\nR 182,186,4096\nbuffer_load_dwordx4 a[32:35], v252, s[20:23], s64 offen offset:2048 sc1\nbuffer_load_dwordx4 a[36:39], v252, s[20:23], s65 offen offset:2048 sc1\n"
"buffer_load_dwordx4 a[40:43], v252, s[20:23], s66 offen offset:2048 sc1\nbuffer_load_dwordx4 a[44:47], v252, s[20:23], s67 offen offset:2048 sc1\nbuffer_load_dwordx4 a[48:51], v252, s[20:23], s64 offen offset:3072 sc1\nbuffer_load_dwordx4 a[52:55], v252, s[20:23], s65 offen offset:3072 sc1\nbuffer_load_dwordx4 a[56:59], v252, s[20:23], s66 offen offset:3072 sc1\nbuffer_load_dwordx4 a[60:63], v252, s[20:23], s67 offen offset:3072 sc1\n"
"s_add_u32 s64, s70, 6295552\nbuffer_load_dwordx4 a[64:67], v252, s[20:23], s64 offen offset:0 sc1\ns_add_u32 s65, s70, 6328320\nbuffer_load_dwordx4 a[68:71], v252, s[20:23], s65 offen offset:0 sc1\ns_add_u32 s66, s71, 6295552\nbuffer_load_dwordx4 a[72:75], v252, s[20:23], s66 offen offset:0 sc1\n"
"s_add_u32 s67, s71, 6328320\nbuffer_load_dwordx4 a[76:79], v252, s[20:23], s67 offen offset:0 sc1\ns_waitcnt vmcnt(19) lgkmcnt(10)\nM 0,160,0\nbuffer_load_dwordx4 a[80:83], v252, s[20:23], s64 offen offset:1024 sc1\ns_waitcnt vmcnt(16) lgkmcnt(8)\n"
"M 0,164,16\nM 16,160,4\ns_waitcnt vmcnt(15)\nM 16,164,20\nv_mfma_f32_16x16x32_bf16 v[32:35], v[160:163], a[8:11], 0\nbuffer_load_dwordx4 a[84:87], v252, s[20:23], s65 offen offset:1024 sc1\n"
"s_waitcnt vmcnt(15)\nM 32,164,24\nv_mfma_f32_16x16x32_bf16 v[48:51], v[160:163], a[12:15], 0\ns_waitcnt vmcnt(14)\nM 48,164,28\nG 4\n"
"v_mfma_f32_16x16x32_bf16 v[52:55], v[168:171], a[12:15], 0\nR 160,191,49152\nM 52,172,28\nbuffer_load_dwordx4 a[88:91], v252, s[20:23], s66 offen offset:1024 sc1\nv_mfma_f32_16x16x32_bf16 v[36:39], v[168:171], a[8:11], 0\nR 162,191,53248\n"
"M 36,172,24\nM 20,168,4\nR 164,187,0\nM 20,172,20\nbuffer_load_dwordx4 a[92:95], v252, s[20:23], s67 offen offset:1024 sc1\nM 4,168,0\n"
"R 166,187,4096\nM 4,172,16\nG 4\nM 8,176,0\nR 168,184,8192\nM 8,180,16\n"
"buffer_load_dwordx4 a[96:99], v252, s[20:23], s64 offen offset:2048 sc1\nM 24,176,4\nR 170,184,12288\nM 24,180,20\nv_mfma_f32_16x16x32_bf16 v[40:43], v[176:179], a[8:11], 0\nR 172,184,16384\n"
"M 40,180,24\nbuffer_load_dwordx4 a[100:103], v252, s[20:23], s65 offen offset:2048 sc1\nv_mfma_f32_16x16x32_bf16 v[56:59], v[176:179], a[12:15], 0\nR 174,184,20480\nM 56,180,28\nG 4\n"
"v_mfma_f32_16x16x32_bf16 v[60:63], v[160:163], a[12:15], 0\nR 176,185,8192\nM 60,164,28\nbuffer_load_dwordx4 a[104:107], v252, s[20:23], s66 offen offset:2048 sc1\nv_mfma_f32_16x16x32_bf16 v[44:47], v[160:163], a[8:11], 0\nR 178,185,12288\n"
"M 44,164,24\nM 28,160,4\nR 180,185,16384\nM 28,164,20\nbuffer_load_dwordx4 a[108:111], v252, s[20:23], s67 offen offset:2048 sc1\nM 12,160,0\n"
"R 182,185,20480\nM 12,164,16\nG 4\ns_waitcnt vmcnt(19)\nM 0,168,32\nR 160,186,8192\n"
"s_waitcnt vmcnt(15)\nM 0,172,48\nbuffer_load_dwordx4 a[112:115], v252, s[20:23], s64 offen offset:3072 sc1\nM 16,168,36\nR 162,186,12288\ns_waitcnt vmcnt(15)\n"
"M 16,172,52\nM 32,168,40\nR 164,186,16384\ns_waitcnt vmcnt(14)\nM 32,172,56\nbuffer_load_dwordx4 a[116:119], v252, s[20:23], s65 offen offset:3072 sc1\n"
"M 48,168,44\nR 166,186,20480\ns_waitcnt vmcnt(14)\nM 48,172,60\nG 4\nM 52,176,44\n"
"R 168,187,8192\nM 52,180,60\nbuffer_load_dwordx4 a[120:123], v252, s[20:23], s66 offen offset:3072 sc1\nM 36,176,40\nR 170,187,12288\nM 36,180,56\n"
"M 20,176,36\nR 172,187,16384\nM 20,180,52\nbuffer_load_dwordx4 a[124:127], v252, s[20:23], s67 offen offset:3072 sc1\nM 4,176,32\nR 174,187,20480\n"
"M 4,180,48\nG 4\nM 8,160,32\nR 176,184,24576\nM 8,164,48\ns_add_u32 s64, s70, 6299648\n"
"buffer_load_dwordx4 a[128:131], v252, s[20:23], s64 offen offset:0 sc1\nM 24,160,36\nR 178,184,28672\nM 24,164,52\nM 40,160,40\nR 180,184,32768\n"
"M 40,164,56\ns_add_u32 s65, s70, 6332416\nbuffer_load_dwordx4 a[132:135], v252, s[20:23], s65 offen offset:0 sc1\nM 56,160,44\nR 182,184,36864\nM 56,164,60\n"
"G 4\nM 60,168,44\nR 160,185,24576\nM 60,172,60\ns_add_u32 s66, s71, 6299648\nbuffer_load_dwordx4 a[136:139], v252, s[20:23], s66 offen offset:0 sc1\n"
"M 44,168,40\nR 162,185,28672\nM 44,172,56\nM 28,168,36\nR 164,185,32768\nM 28,172,52\n"
"s_add_u32 s67, s71, 6332416\nbuffer_load_dwordx4 a[140:143], v252, s[20:23], s67 offen offset:0 sc1\nM 12,168,32\nR 166,185,36864\nM 12,172,48\ns_barrier\n"
"G 4\ns_waitcnt vmcnt(19)\nM 0,176,64\nR 168,186,24576\ns_waitcnt vmcnt(15)\nM 0,180,80\n"
"buffer_load_dwordx4 a[144:147], v252, s[20:23], s64 offen offset:1024 sc1\nM 16,176,68\nR 170,186,28672\ns_waitcnt vmcnt(15)\nM 16,180,84\nM 32,176,72\n"
"R 172,186,32768\ns_waitcnt vmcnt(14)\nM 32,180,88\nbuffer_load_dwordx4 a[148:151], v252, s[20:23], s65 offen offset:1024 sc1\nM 48,176,76\nR 174,186,36864\n"
"s_waitcnt vmcnt(14)\nM 48,180,92\nG 4\nM 52,160,76\nR 176,187,24576\nM 52,164,92\n"
"buffer_load_dwordx4 a[152:155], v252, s[20:23], s66 offen offset:1024 sc1\nM 36,160,72\nR 178,187,28672\nM 36,164,88\nM 20,160,68\nR 180,187,32768\n"
"M 20,164,84\nbuffer_load_dwordx4 a[156:159], v252, s[20:23], s67 offen offset:1024 sc1\nM 4,160,64\nR 182,187,36864\nM 4,164,80\nG 4\n"
"M 8,168,64\nR 160,184,40960\nM 8,172,80\nbuffer_load_dwordx4 a[160:163], v252, s[20:23], s64 offen offset:2048 sc1\nM 24,168,68\nR 162,184,45056\n"
"M 24,172,84\nM 40,168,72\nR 164,184,49152\nM 40,172,88\nbuffer_load_dwordx4 a[164:167], v252, s[20:23], s65 offen offset:2048 sc1\nM 56,168,76\n"
"R 166,184,53248\nM 56,172,92\nG 4\nM 60,176,76\nR 168,185,40960\nM 60,180,92\n"
"buffer_load_dwordx4 a[168:171], v252, s[20:23], s66 offen offset:2048 sc1\nM 44,176,72\nR 170,185,45056\nM 44,180,88\nM 28,176,68\nR 172,185,49152\n"
"M 28,180,84\nbuffer_load_dwordx4 a[172:175], v252, s[20:23], s67 offen offset:2048 sc1\nM 12,176,64\nR 174,185,53248\nM 12,180,80\nG 4\n"
"s_waitcnt vmcnt(19)\nM 0,160,96\nR 176,186,40960\ns_waitcnt vmcnt(15)\nM 0,164,112\nbuffer_load_dwordx4 a[176:179], v252, s[20:23], s64 offen offset:3072 sc1\n"
"M 16,160,100\nR 178,186,45056\ns_waitcnt vmcnt(15)\nM 16,164,116\nM 32,160,104\nR 180,186,49152\n"
"s_lshr_b32 s60, s45, 3\ns_lshl2_add_u32 s60, s60, s40\ns_lshl_b32 s60, s60, 8\nv_lshl_add_u32 v224, v252, 3, s60\nv_add_u32 v224, Lqw1+24576-LqPC, v224\nv_min_u32 v224, LqE-LqPC-4, v224\n"
"s_mov_b64 exec, 3\nglobal_load_dword v224, v224, s[98:99]\ns_mov_b64 exec, -1\ns_waitcnt vmcnt(15)\nM 32,164,120\nbuffer_load_dwordx4 a[180:183], v252, s[20:23], s65 offen offset:3072 sc1\n"
"M 48,160,108\nR 182,186,53248\ns_waitcnt vmcnt(15)\nM 48,164,124\nG 4\nM 52,168,108\n"
"R 160,187,40960\nM 52,172,124\nbuffer_load_dwordx4 a[184:187], v252, s[20:23], s66 offen offset:3072 sc1\nM 36,168,104\nR 162,187,45056\nM 36,172,120\n"
"M 20,168,100\nR 164,187,49152\nM 20,172,116\nbuffer_load_dwordx4 a[188:191], v252, s[20:23], s67 offen offset:3072 sc1\nM 4,168,96\nR 166,187,53248\n"
"M 4,172,112\nG 4\nM 8,176,96\nR 168,184,57344\nM 8,180,112\ns_add_u32 s64, s70, 6303744\n"
"buffer_load_dwordx4 a[192:195], v252, s[20:23], s64 offen offset:0 sc1\nM 24,176,100\nR 170,184,61440\ns_add_u32 s63, s53, 983040\nM 24,180,116\ns_add_u32 m0, s50, 114688\n"
"M 40,176,104\nR 172,188,0\nM 40,180,120\ns_add_u32 s65, s70, 6336512\nbuffer_load_dwordx4 a[196:199], v252, s[20:23], s65 offen offset:0 sc1\nM 56,176,108\n"
"R 174,188,4096\nM 56,180,124\nbuffer_load_dwordx4 v220, s[8:11], s63 offen lds\nG 4\nM 60,160,108\nR 176,185,57344\n"
"M 60,164,124\ns_add_u32 s66, s71, 6303744\nbuffer_load_dwordx4 a[200:203], v252, s[20:23], s66 offen offset:0 sc1\nM 44,160,104\nR 178,185,61440\nM 44,164,120\n"
"buffer_load_dwordx4 v221, s[8:11], s63 offen offset:1024 lds\nM 28,160,100\nR 180,189,0\nM 28,164,116\ns_add_u32 s67, s71, 6336512\nbuffer_load_dwordx4 a[204:207], v252, s[20:23], s67 offen offset:0 sc1\n"
"M 12,160,96\nR 182,189,4096\nM 12,164,112\ns_barrier\nG 4\ns_waitcnt vmcnt(22)\n"
"M 0,168,128\nR 160,186,57344\ns_waitcnt vmcnt(18)\nM 0,172,144\nbuffer_load_dwordx4 a[208:211], v252, s[20:23], s64 offen offset:1024 sc1\nM 16,168,132\n"
"R 162,186,61440\ns_waitcnt vmcnt(18)\nM 16,172,148\nM 32,168,136\nR 164,190,0\ns_waitcnt vmcnt(17)\n"
"M 32,172,152\nbuffer_load_dwordx4 a[212:215], v252, s[20:23], s65 offen offset:1024 sc1\nM 48,168,140\nR 166,190,4096\ns_waitcnt vmcnt(17)\nM 48,172,156\n"
"G 4\nM 52,176,140\nR 168,187,57344\nM 52,180,156\nbuffer_load_dwordx4 a[216:219], v252, s[20:23], s66 offen offset:1024 sc1\nM 36,176,136\n"
"R 170,187,61440\nM 36,180,152\nM 20,176,132\nR 172,191,0\nM 20,180,148\nbuffer_load_dwordx4 a[220:223], v252, s[20:23], s67 offen offset:1024 sc1\n"
"M 4,176,128\nR 174,191,4096\nM 4,180,144\nG 4\nM 8,160,128\nR 176,188,8192\n"
"M 8,164,144\nbuffer_load_dwordx4 a[224:227], v252, s[20:23], s64 offen offset:2048 sc1\nM 24,160,132\nR 178,188,12288\nM 24,164,148\nM 40,160,136\n"
"R 180,188,16384\nM 40,164,152\nbuffer_load_dwordx4 a[228:231], v252, s[20:23], s65 offen offset:2048 sc1\nM 56,160,140\nR 182,188,20480\nM 56,164,156\n"
"G 4\nM 60,168,140\nR 160,189,8192\nM 60,172,156\nbuffer_load_dwordx4 a[232:235], v252, s[20:23], s66 offen offset:2048 sc1\nM 44,168,136\n"
"R 162,189,12288\nM 44,172,152\nM 28,168,132\nR 164,189,16384\nM 28,172,148\nbuffer_load_dwordx4 a[236:239], v252, s[20:23], s67 offen offset:2048 sc1\n"
"Lpg23:\ns_cbranch_execz LretO1\nM 12,168,128\nR 166,189,20480\nM 12,172,144\nG 4\n"
"s_waitcnt vmcnt(22)\nM 0,176,160\nR 168,190,8192\ns_waitcnt vmcnt(18)\nM 0,180,176\nbuffer_load_dwordx4 a[240:243], v252, s[20:23], s64 offen offset:3072 sc1\n"
"M 16,176,164\nR 170,190,12288\ns_waitcnt vmcnt(17)\nM 16,180,180\nM 32,176,168\nR 172,190,16384\n"
"s_waitcnt vmcnt(16)\nM 32,180,184\nbuffer_load_dwordx4 a[244:247], v252, s[20:23], s65 offen offset:3072 sc1\nM 48,176,172\nR 174,190,20480\ns_waitcnt vmcnt(16)\n"
"M 48,180,188\nG 4\nM 52,160,172\nR 176,191,8192\nM 52,164,188\nbuffer_load_dwordx4 a[248:251], v252, s[20:23], s66 offen offset:3072 sc1\n"
"M 36,160,168\nR 178,191,12288\ns_waitcnt vmcnt(12)\ns_barrier\nM 36,164,184\nM 20,160,164\n"
"R 180,191,16384\nM 20,164,180\nbuffer_load_dwordx4 a[252:255], v252, s[20:23], s67 offen offset:3072 sc1\nM 4,160,160\nR 182,191,20480\nM 4,164,176\n"
"G 4\nM 8,168,160\nR 160,188,24576\nM 8,172,176\nM 24,168,164\nR 162,188,28672\n"
"M 24,172,180\nM 40,168,168\nR 164,188,32768\nM 40,172,184\nM 56,168,172\nR 166,188,36864\n"
"M 56,172,188\nG 4\nM 60,176,172\nR 168,189,24576\nM 60,180,188\ns_movk_i32 s63, 7168\n"
"v_mad_u32_u24 v220, v228, s63, v229\nv_add_u32 v221, 3668992, v220\ns_add_u32 m0, s50, 0\ns_add_u32 s63, s54, 0\nbuffer_load_dwordx4 v220, s[12:15], s63 offen lds\nM 44,176,168\n"
"R 170,189,28672\nM 44,180,184\nM 28,176,164\nR 172,189,32768\nM 28,180,180\nbuffer_load_dwordx4 v221, s[12:15], s63 offen offset:1024 lds\n"
"M 12,176,160\nR 174,189,36864\ns_add_u32 m0, s50, 8192\nM 12,180,176\nG 4\nM 32,160,200\n"
"R 176,190,24576\ns_add_u32 s63, s54, 229376\ns_waitcnt vmcnt(11)\nM 32,164,216\nR 178,190,28672\nM 48,160,204\n"
"R 180,190,32768\ns_waitcnt vmcnt(10)\nM 48,164,220\nR 182,190,36864\nG 4\nM 52,168,204\n"
"R 160,191,24576\nbuffer_load_dwordx4 v220, s[12:15], s63 offen lds\nM 52,172,220\nR 162,191,28672\nM 36,168,200\nR 164,191,32768\n"
"M 36,172,216\nR 166,191,36864\nG 4\nM 40,176,200\nR 168,188,40960\nbuffer_load_dwordx4 v221, s[12:15], s63 offen offset:1024 lds\n"
"M 40,180,216\nR 170,188,45056\ns_add_u32 m0, s50, 16384\nM 56,176,204\nR 172,188,49152\ns_add_u32 s63, s54, 128\n"
"M 56,180,220\nR 174,188,53248\nG 4\nM 60,160,204\nR 176,189,40960\nbuffer_load_dwordx4 v220, s[12:15], s63 offen lds\n"
"M 60,164,220\nR 178,189,45056\nM 44,160,200\nR 180,189,49152\nM 44,164,216\nR 182,189,53248\n"
"G 4\ns_waitcnt vmcnt(10)\nM 32,168,232\nR 160,190,40960\nbuffer_load_dwordx4 v221, s[12:15], s63 offen offset:1024 lds\ns_waitcnt vmcnt(7)\n"
"M 32,172,248\nR 162,190,45056\ns_add_u32 m0, s50, 24576\nM 48,168,236\nR 164,190,49152\ns_add_u32 s63, s54, 229504\n"
"s_waitcnt vmcnt(6)\nM 48,172,252\nR 166,190,53248\nG 4\nM 52,176,236\nR 168,191,40960\n"
"buffer_load_dwordx4 v220, s[12:15], s63 offen lds\nM 52,180,252\nR 170,191,45056\nM 36,176,232\nR 172,191,49152\nM 36,180,248\n"
"R 174,191,53248\nG 4\nM 40,160,232\nR 176,188,24576\nbuffer_load_dwordx4 v221, s[12:15], s63 offen offset:1024 lds\nM 40,164,248\n"
"R 178,188,28672\nM 56,160,236\nR 180,188,32768\nM 56,164,252\nR 182,188,36864\nG 4\n"
"M 60,168,236\nR 160,189,24576\nM 60,172,252\nR 162,189,28672\nM 44,168,232\nR 164,189,32768\n"
"M 44,172,248\nR 166,189,36864\nG 4\nM 0,176,192\nR 168,190,24576\nM 0,180,208\n"
"R 170,190,28672\ns_mov_b32 m0, s51\nM 16,176,196\nR 172,190,32768\nv_cvt_pk_bf16_f32 v32, v32, v33\nv_cvt_pk_bf16_f32 v33, v34, v35\n"
"M 16,180,212\nR 174,190,36864\nG 4\nv_cvt_pk_bf16_f32 v34, v36, v37\nM 20,160,196\nR 176,191,24576\n"
"v_cvt_pk_bf16_f32 v35, v38, v39\nv_cvt_pk_bf16_f32 v36, v40, v41\nM 20,164,212\nR 178,191,28672\nv_cvt_pk_bf16_f32 v37, v42, v43\nv_cvt_pk_bf16_f32 v38, v44, v45\n"
"M 4,160,192\nR 180,191,32768\nv_cvt_pk_bf16_f32 v39, v46, v47\nv_cvt_pk_bf16_f32 v40, v48, v49\nM 4,164,208\nR 182,191,36864\n"
"G 4\nv_cvt_pk_bf16_f32 v41, v50, v51\nM 8,168,192\nR 160,188,40960\nv_cvt_pk_bf16_f32 v42, v52, v53\nv_cvt_pk_bf16_f32 v43, v54, v55\n"
"M 8,172,208\nR 162,188,45056\nv_cvt_pk_bf16_f32 v44, v56, v57\nv_cvt_pk_bf16_f32 v45, v58, v59\nM 24,168,196\nR 164,188,49152\n"
"v_cvt_pk_bf16_f32 v46, v60, v61\nv_cvt_pk_bf16_f32 v47, v62, v63\nM 24,172,212\nR 166,188,53248\nds_write_addtid_b32 v32 offset:0\nG 5\n"
"M 28,176,196\nR 168,189,40960\nds_write_addtid_b32 v33 offset:256\nM 28,180,212\nR 170,189,45056\nds_write_addtid_b32 v34 offset:512\n"
"M 12,176,192\nR 172,189,49152\nds_write_addtid_b32 v35 offset:768\nds_write_addtid_b32 v36 offset:1024\nds_write_addtid_b32 v37 offset:1280\nds_write_addtid_b32 v38 offset:1536\n"
"ds_write_addtid_b32 v39 offset:1792\nds_write_addtid_b32 v40 offset:2048\nds_write_addtid_b32 v41 offset:2304\nds_write_addtid_b32 v42 offset:2560\nds_write_addtid_b32 v43 offset:2816\nds_write_addtid_b32 v44 offset:3072\n"
"ds_write_addtid_b32 v45 offset:3328\nds_write_addtid_b32 v46 offset:3584\nds_write_addtid_b32 v47 offset:3840\nM 12,180,208\nR 174,189,53248\nG 15\n"
"M 0,160,224\nR 176,190,40960\ns_add_u32 m0, s50, 32768\nM 0,164,240\nR 178,190,45056\nG 3\n"
"s_barrier\ns_add_u32 s63, s54, 458752\nM 16,160,228\nR 180,190,49152\nbuffer_load_dwordx4 v220, s[12:15], s63 offen lds\nds_read2st64_b32 v[32:33], v225 offset0:0 offset1:1\n"
"M 16,164,244\nR 182,190,53248\nds_read2st64_b32 v[34:35], v225 offset0:2 offset1:3\nG 6\nM 20,168,228\nR 160,191,40960\n"
"ds_read2st64_b32 v[36:37], v225 offset0:4 offset1:5\nM 20,172,244\nR 162,191,45056\nds_read2st64_b32 v[38:39], v225 offset0:6 offset1:7\nM 4,168,224\nR 164,191,49152\n"
"buffer_load_dwordx4 v221, s[12:15], s63 offen offset:1024 lds\nds_read2st64_b32 v[40:41], v225 offset0:8 offset1:9\nM 4,172,240\nR 166,191,53248\ns_add_u32 m0, s50, 40960\ns_add_u32 s63, s54, 688128\n"
"ds_read2st64_b32 v[42:43], v225 offset0:10 offset1:11\nG 9\nM 8,176,224\nbuffer_load_dwordx4 v220, s[12:15], s63 offen lds\nds_read2st64_b32 v[44:45], v225 offset0:12 offset1:13\nM 8,180,240\n"
"ds_read2st64_b32 v[46:47], v225 offset0:14 offset1:15\nM 24,176,228\nM 24,180,244\nG 3\nM 28,160,228\nbuffer_load_dwordx4 v221, s[12:15], s63 offen offset:1024 lds\n"
"M 28,164,244\nM 12,160,224\nM 12,164,240\nG 0\ns_barrier\nv_mfma_f32_16x16x32_bf16 v[0:3], v[208:211], v[32:35], v[0:3]\n"
"v_mfma_f32_16x16x32_bf16 v[4:7], v[210:213], v[32:35], v[4:7]\nv_mfma_f32_16x16x32_bf16 v[8:11], v[208:211], v[36:39], v[8:11]\nv_mfma_f32_16x16x32_bf16 v[12:15], v[210:213], v[36:39], v[12:15]\nv_mfma_f32_16x16x32_bf16 v[16:19], v[208:211], v[40:43], v[16:19]\nv_mfma_f32_16x16x32_bf16 v[20:23], v[210:213], v[40:43], v[20:23]\nv_mfma_f32_16x16x32_bf16 v[24:27], v[208:211], v[44:47], v[24:27]\n"
"v_mfma_f32_16x16x32_bf16 v[28:31], v[210:213], v[44:47], v[28:31]\ns_lshl_b32 s84, s75, 11\ns_add_u32 s84, s84, s49\ns_add_u32 s84, s84, 14680064\ns_add_u32 s85, s84, 32768\nv_mov_b32 v238, s46\n"
"v_mov_b32 v239, s47\nv_cvt_pk_bf16_f32 v192, v0, v1\nv_cvt_pk_bf16_f32 v193, v2, v3\nv_cvt_pk_bf16_f32 v194, v4, v5\nv_cvt_pk_bf16_f32 v195, v6, v7\nbuffer_store_dwordx4 v[192:195], v252, s[20:23], s84 offen offset:0 sc1\n"
"v_cvt_pk_bf16_f32 v196, v8, v9\nv_cvt_pk_bf16_f32 v197, v10, v11\nv_cvt_pk_bf16_f32 v198, v12, v13\nv_cvt_pk_bf16_f32 v199, v14, v15\nbuffer_store_dwordx4 v[196:199], v252, s[20:23], s84 offen offset:1024 sc1\nv_cvt_pk_bf16_f32 v200, v16, v17\n"
"v_cvt_pk_bf16_f32 v201, v18, v19\nv_cvt_pk_bf16_f32 v202, v20, v21\nv_cvt_pk_bf16_f32 v203, v22, v23\nbuffer_store_dwordx4 v[200:203], v252, s[20:23], s85 offen offset:0 sc1\nv_cvt_pk_bf16_f32 v204, v24, v25\nv_cvt_pk_bf16_f32 v205, v26, v27\n"
"v_cvt_pk_bf16_f32 v206, v28, v29\nv_cvt_pk_bf16_f32 v207, v30, v31\nbuffer_store_dwordx4 v[204:207], v252, s[20:23], s85 offen offset:1024 sc1\ns_mov_b64 exec, 0\ns_cmp_eq_u32 s40, 1\ns_cbranch_scc1 Lpg25\n"
"s_cmp_eq_u32 s40, 2\ns_cbranch_scc1 Lpg26\ns_cmp_eq_u32 s40, 3\ns_cbranch_scc1 Lpg27\ns_branch Lpg24\nLretO1n:\n"
"s_mov_b64 exec, -1\ns_add_u32 s61, s79, 88637440\ns_lshl_b32 s62, s75, 3\ns_add_u32 s61, s61, s62\ns_waitcnt vmcnt(0)\ns_mov_b64 exec, 1\n"
"buffer_atomic_swap_x2 v[238:239], off, s[20:23], s61 sc1\ns_mov_b64 exec, -1\nLqw1:\nv_bfe_u32 v250, v252, 7, 1\nv_mul_u32_u24 v250, 2176, v250\nv_bfe_u32 v251, v252, 4, 3\n"
"v_lshl_add_u32 v250, v251, 3, v250\ns_lshl_b32 s60, s90, 6\nv_add_u32 v250, s60, v250\ns_sleep 5\nv_mov_b32 v248, 0\nv_mov_b32 v249, 0\n"
"s_add_u32 s61, s80, 88637440\ns_mov_b64 exec, 0xffff\nbuffer_atomic_or_x2 v[248:249], v250, s[20:23], s61 offen sc0 sc1\ns_mov_b64 exec, -1\ns_add_u32 m0, s50, 49152\ns_add_u32 s63, s54, 458880\n"
"buffer_load_dwordx4 v220, s[12:15], s63 offen lds\nbuffer_load_dwordx4 v221, s[12:15], s63 offen offset:1024 lds\ns_add_u32 m0, s50, 57344\ns_add_u32 s63, s54, 688256\nbuffer_load_dwordx4 v220, s[12:15], s63 offen lds\nbuffer_load_dwordx4 v221, s[12:15], s63 offen offset:1024 lds\n"
"s_add_u32 m0, s50, 65536\ns_add_u32 s63, s54, 917504\nbuffer_load_dwordx4 v220, s[12:15], s63 offen lds\nbuffer_load_dwordx4 v221, s[12:15], s63 offen offset:1024 lds\ns_add_u32 m0, s50, 73728\ns_add_u32 s63, s54, 1146880\n"
"buffer_load_dwordx4 v220, s[12:15], s63 offen lds\nbuffer_load_dwordx4 v221, s[12:15], s63 offen offset:1024 lds\ns_add_u32 m0, s50, 81920\ns_add_u32 s63, s54, 917632\nbuffer_load_dwordx4 v220, s[12:15], s63 offen lds\nbuffer_load_dwordx4 v221, s[12:15], s63 offen offset:1024 lds\n"
"s_add_u32 m0, s50, 90112\ns_add_u32 s63, s54, 1147008\nbuffer_load_dwordx4 v220, s[12:15], s63 offen lds\nbuffer_load_dwordx4 v221, s[12:15], s63 offen offset:1024 lds\ns_mov_b64 exec, 0\ns_cmp_eq_u32 s40, 1\n"
"s_cbranch_scc1 Lpg33\ns_cmp_eq_u32 s40, 2\ns_cbranch_scc1 LretW1z\ns_cmp_eq_u32 s40, 3\ns_cbranch_scc1 LretW1z\ns_branch Lpg32\n"
"LretW1z:\ns_mov_b64 exec, -1\nv_mov_b32 v230, 0\nv_mov_b32 v231, 0\nv_mov_b32 v232, 0\nv_mov_b32 v233, 0\n"
"v_dot2c_f32_bf16 v230, s76, v192\nv_dot2c_f32_bf16 v231, v192, v192\nv_dot2c_f32_bf16 v232, s76, v200\nv_dot2c_f32_bf16 v233, v200, v200\nv_dot2c_f32_bf16 v230, s76, v193\nv_dot2c_f32_bf16 v231, v193, v193\n"
"v_dot2c_f32_bf16 v232, s76, v201\nv_dot2c_f32_bf16 v233, v201, v201\nv_dot2c_f32_bf16 v230, s76, v194\nv_dot2c_f32_bf16 v231, v194, v194\nv_dot2c_f32_bf16 v232, s76, v202\nv_dot2c_f32_bf16 v233, v202, v202\n"
"v_dot2c_f32_bf16 v230, s76, v195\nv_dot2c_f32_bf16 v231, v195, v195\nv_dot2c_f32_bf16 v232, s76, v203\nv_dot2c_f32_bf16 v233, v203, v203\nv_dot2c_f32_bf16 v230, s76, v196\nv_dot2c_f32_bf16 v231, v196, v196\n"
"v_dot2c_f32_bf16 v232, s76, v204\nv_dot2c_f32_bf16 v233, v204, v204\nv_dot2c_f32_bf16 v230, s76, v197\nv_dot2c_f32_bf16 v231, v197, v197\nv_dot2c_f32_bf16 v232, s76, v205\nv_dot2c_f32_bf16 v233, v205, v205\n"
"v_dot2c_f32_bf16 v230, s76, v198\nv_dot2c_f32_bf16 v231, v198, v198\nv_dot2c_f32_bf16 v232, s76, v206\nv_dot2c_f32_bf16 v233, v206, v206\nv_dot2c_f32_bf16 v230, s76, v199\nv_dot2c_f32_bf16 v231, v199, v199\n"
"v_dot2c_f32_bf16 v232, s76, v207\nv_dot2c_f32_bf16 v233, v207, v207\nv_lshrrev_b32 v234, 5, v252\nv_and_b32 v234, 24, v234\nv_bfe_u32 v235, s46, v234, 8\nv_lshrrev_b32 v234, 1, v234\n"
"v_and_b32 v236, 0xf0, v252\nv_add_u32 v234, v234, v236\nv_permlane32_swap_b32 v230, v231\nv_permlane32_swap_b32 v232, v233\nv_add_f32 v230, v230, v231\nv_add_f32 v232, v232, v233\n"
"s_nop 1\nv_permlane16_swap_b32 v230, v232\nv_add_f32 v230, v230, v232\ns_movk_i32 s62, 0xff\nv_bfi_b32 v230, s62, v235, v230\ns_lshr_b32 s61, s49, 4\n"
"s_lshl1_add_u32 s61, s53, s61\ns_add_u32 s61, s61, 83361792\nbuffer_store_dword v230, v234, s[20:23], s61 offen sc1\ns_waitcnt vmcnt(13)\nv_cmp_eq_u64 s[86:87], s[46:47], v[248:249]\ns_nop 1\n"
"s_and_b32 s62, s86, 0xffff\ns_cmp_eq_u32 s62, 0xffff\ns_cbranch_scc1 Lq752\nLq751:\nv_mov_b32 v248, 0\nv_mov_b32 v249, 0\n"
"s_add_u32 s61, s80, 88637440\ns_mov_b64 exec, 0xffff\nbuffer_atomic_or_x2 v[248:249], v250, s[20:23], s61 offen sc0 sc1\ns_mov_b64 exec, -1\ns_waitcnt vmcnt(0)\nv_cmp_eq_u64 s[86:87], s[46:47], v[248:249]\n"
"s_nop 1\ns_and_b32 s62, s86, 0xffff\ns_cmp_eq_u32 s62, 0xffff\ns_cbranch_scc1 Lq752\ns_sub_u32 s48, s48, 1\ns_cmp_gt_i32 s48, 0\n"
"s_cbranch_scc1 Lq751\nLq752:\ns_add_u32 s64, s70, 14680064\nbuffer_load_dwordx4 a[0:3], v252, s[20:23], s64 offen offset:0 sc1\ns_add_u32 s65, s70, 14712832\nbuffer_load_dwordx4 a[4:7], v252, s[20:23], s65 offen offset:0 sc1\n"
"s_add_u32 s66, s71, 14680064\nbuffer_load_dwordx4 a[8:11], v252, s[20:23], s66 offen offset:0 sc1\ns_add_u32 s67, s71, 14712832\nbuffer_load_dwordx4 a[12:15], v252, s[20:23], s67 offen offset:0 sc1\nbuffer_load_dwordx4 a[16:19], v252, s[20:23], s64 offen offset:1024 sc1\nbuffer_load_dwordx4 a[20:23], v252, s[20:23], s65 offen offset:1024 sc1\n"
"buffer_load_dwordx4 a[24:27], v252, s[20:23], s66 offen offset:1024 sc1\nbuffer_load_dwordx4 a[28:31], v252, s[20:23], s67 offen offset:1024 sc1\ns_barrier\nR 160,184,0\nR 162,184,4096\nR 164,184,8192\n"
"R 166,184,12288\nR 168,185,0\nR 170,185,4096\nR 172,185,8192\nR 174,185,12288\nR 176,186,0\n"
"R 178,186,4096\nR 180,186,8192\nR 182,186,12288\nbuffer_load_dwordx4 a[32:35], v252, s[20:23], s64 offen offset:2048 sc1\nbuffer_load_dwordx4 a[36:39], v252, s[20:23], s65 offen offset:2048 sc1\nbuffer_load_dwordx4 a[40:43], v252, s[20:23], s66 offen offset:2048 sc1\n"
"buffer_load_dwordx4 a[44:47], v252, s[20:23], s67 offen offset:2048 sc1\nbuffer_load_dwordx4 a[48:51], v252, s[20:23], s64 offen offset:3072 sc1\nbuffer_load_dwordx4 a[52:55], v252, s[20:23], s65 offen offset:3072 sc1\nbuffer_load_dwordx4 a[56:59], v252, s[20:23], s66 offen offset:3072 sc1\nbuffer_load_dwordx4 a[60:63], v252, s[20:23], s67 offen offset:3072 sc1\ns_waitcnt vmcnt(15) lgkmcnt(10)\n"
"v_mfma_f32_16x16x32_bf16 v[32:35], v[160:163], a[0:3], 0\ns_add_u32 s64, s70, 14684160\nbuffer_load_dwordx4 a[64:67], v252, s[20:23], s64 offen offset:0 sc1\ns_waitcnt vmcnt(12) lgkmcnt(8)\nM 32,164,16\nv_mfma_f32_16x16x32_bf16 v[48:51], v[160:163], a[4:7], 0\n"
"s_waitcnt vmcnt(11)\nM 48,164,20\nv_mfma_f32_16x16x32_bf16 v[64:67], v[160:163], a[8:11], 0\ns_waitcnt vmcnt(10)\nM 64,164,24\nv_mfma_f32_16x16x32_bf16 v[80:83], v[160:163], a[12:15], 0\n"
"s_add_u32 s65, s70, 14716928\nbuffer_load_dwordx4 a[68:71], v252, s[20:23], s65 offen offset:0 sc1\ns_waitcnt vmcnt(10)\nM 80,164,28\nG 4\nv_mfma_f32_16x16x32_bf16 v[84:87], v[168:171], a[12:15], 0\n"
"R 160,187,0\nM 84,172,28\nv_mfma_f32_16x16x32_bf16 v[68:71], v[168:171], a[8:11], 0\nR 162,187,4096\nM 68,172,24\nv_mfma_f32_16x16x32_bf16 v[52:55], v[168:171], a[4:7], 0\n"
"R 164,187,8192\nM 52,172,20\ns_add_u32 s66, s71, 14684160\nbuffer_load_dwordx4 a[72:75], v252, s[20:23], s66 offen offset:0 sc1\nv_mfma_f32_16x16x32_bf16 v[36:39], v[168:171], a[0:3], 0\nR 166,187,12288\n"
"M 36,172,16\nG 4\nv_mfma_f32_16x16x32_bf16 v[40:43], v[176:179], a[0:3], 0\nR 168,184,16384\nM 40,180,16\nv_mfma_f32_16x16x32_bf16 v[56:59], v[176:179], a[4:7], 0\n"
"R 170,184,20480\nM 56,180,20\ns_add_u32 s67, s71, 14716928\nbuffer_load_dwordx4 a[76:79], v252, s[20:23], s67 offen offset:0 sc1\nv_mfma_f32_16x16x32_bf16 v[72:75], v[176:179], a[8:11], 0\nR 172,184,24576\n"
"M 72,180,24\nv_mfma_f32_16x16x32_bf16 v[88:91], v[176:179], a[12:15], 0\nR 174,184,28672\nM 88,180,28\nG 4\nv_mfma_f32_16x16x32_bf16 v[92:95], v[160:163], a[12:15], 0\n"
"R 176,185,16384\ns_add_u32 s63, s54, 1376256\nM 92,164,28\nbuffer_load_dwordx4 a[80:83], v252, s[20:23], s64 offen offset:1024 sc1\nv_mfma_f32_16x16x32_bf16 v[76:79], v[160:163], a[8:11], 0\nR 178,185,20480\n"
"M 76,164,24\ns_add_u32 m0, s50, 98304\nv_mfma_f32_16x16x32_bf16 v[60:63], v[160:163], a[4:7], 0\nR 180,185,24576\nM 60,164,20\nbuffer_load_dwordx4 v220, s[12:15], s63 offen lds\n"
"v_mfma_f32_16x16x32_bf16 v[44:47], v[160:163], a[0:3], 0\nR 182,185,28672\nM 44,164,16\nG 4\nv_mfma_f32_16x16x32_bf16 v[96:99], v[168:171], a[0:3], 0\nR 160,186,16384\n"
"M 96,172,16\nbuffer_load_dwordx4 a[84:87], v252, s[20:23], s65 offen offset:1024 sc1\nv_mfma_f32_16x16x32_bf16 v[112:115], v[168:171], a[4:7], 0\nR 162,186,20480\nM 112,172,20\nv_mfma_f32_16x16x32_bf16 v[128:131], v[168:171], a[8:11], 0\n"
"R 164,186,24576\nM 128,172,24\nbuffer_load_dwordx4 a[88:91], v252, s[20:23], s66 offen offset:1024 sc1\nv_mfma_f32_16x16x32_bf16 v[144:147], v[168:171], a[12:15], 0\nR 166,186,28672\nM 144,172,28\n"
"s_barrier\nG 4\nv_mfma_f32_16x16x32_bf16 v[148:151], v[176:179], a[12:15], 0\nR 168,187,16384\nM 148,180,28\nv_mfma_f32_16x16x32_bf16 v[132:135], v[176:179], a[8:11], 0\n"
"R 170,187,20480\nM 132,180,24\nbuffer_load_dwordx4 a[92:95], v252, s[20:23], s67 offen offset:1024 sc1\nv_mfma_f32_16x16x32_bf16 v[116:119], v[176:179], a[4:7], 0\nR 172,187,24576\nM 116,180,20\n"
"v_mfma_f32_16x16x32_bf16 v[100:103], v[176:179], a[0:3], 0\nR 174,187,28672\nM 100,180,16\nG 4\nv_mfma_f32_16x16x32_bf16 v[104:107], v[160:163], a[0:3], 0\nR 176,184,32768\n"
"M 104,164,16\nbuffer_load_dwordx4 a[96:99], v252, s[20:23], s64 offen offset:2048 sc1\nv_mfma_f32_16x16x32_bf16 v[120:123], v[160:163], a[4:7], 0\nR 178,184,36864\nM 120,164,20\nLpg24:\n"
"s_cbranch_execz Lpg28\nv_mfma_f32_16x16x32_bf16 v[136:139], v[160:163], a[8:11], 0\nR 180,184,40960\nM 136,164,24\nbuffer_load_dwordx4 v221, s[12:15], s63 offen offset:1024 lds\nv_mfma_f32_16x16x32_bf16 v[152:155], v[160:163], a[12:15], 0\n"
"R 182,184,45056\nM 152,164,28\nG 4\nv_mfma_f32_16x16x32_bf16 v[156:159], v[168:171], a[12:15], 0\nR 160,185,32768\nM 156,172,28\n"
"buffer_load_dwordx4 a[100:103], v252, s[20:23], s65 offen offset:2048 sc1\nv_mfma_f32_16x16x32_bf16 v[140:143], v[168:171], a[8:11], 0\nR 162,185,36864\nM 140,172,24\ns_add_u32 m0, s50, 106496\nv_mfma_f32_16x16x32_bf16 v[124:127], v[168:171], a[4:7], 0\n"
"R 164,185,40960\ns_add_u32 s63, s54, 1605632\nM 124,172,20\nbuffer_load_dwordx4 a[104:107], v252, s[20:23], s66 offen offset:2048 sc1\nv_mfma_f32_16x16x32_bf16 v[108:111], v[168:171], a[0:3], 0\nR 166,185,45056\n"
"M 108,172,16\nG 4\ns_waitcnt vmcnt(20)\nM 32,176,32\nR 168,186,32768\ns_waitcnt vmcnt(16)\n"
"M 32,180,48\nbuffer_load_dwordx4 v220, s[12:15], s63 offen lds\nM 48,176,36\nR 170,186,36864\ns_waitcnt vmcnt(16)\nM 48,180,52\n"
"buffer_load_dwordx4 a[108:111], v252, s[20:23], s67 offen offset:2048 sc1\nM 64,176,40\nR 172,186,40960\ns_waitcnt vmcnt(16)\nM 64,180,56\nM 80,176,44\n"
"R 174,186,45056\ns_waitcnt vmcnt(15)\nM 80,180,60\ns_barrier\nG 4\nM 84,160,44\n"
"R 176,187,32768\nM 84,164,60\nbuffer_load_dwordx4 a[112:115], v252, s[20:23], s64 offen offset:3072 sc1\nM 68,160,40\nR 178,187,36864\nM 68,164,56\n"
"M 52,160,36\nR 180,187,40960\nM 52,164,52\nM 36,160,32\nR 182,187,45056\nM 36,164,48\n"
"G 4\nM 40,168,32\nR 160,184,49152\nM 40,172,48\nbuffer_load_dwordx4 a[116:119], v252, s[20:23], s65 offen offset:3072 sc1\nM 56,168,36\n"
"R 162,184,53248\nM 56,172,52\nM 72,168,40\nR 164,184,57344\nM 72,172,56\nbuffer_load_dwordx4 a[120:123], v252, s[20:23], s66 offen offset:3072 sc1\n"
"M 88,168,44\nR 166,184,61440\nM 88,172,60\nG 4\nM 92,176,44\nR 168,185,49152\n"
"M 92,180,60\nbuffer_load_dwordx4 v221, s[12:15], s63 offen offset:1024 lds\nM 76,176,40\nR 170,185,53248\nM 76,180,56\nbuffer_load_dwordx4 a[124:127], v252, s[20:23], s67 offen offset:3072 sc1\n"
"M 60,176,36\nR 172,185,57344\nM 60,180,52\nM 44,176,32\nR 174,185,61440\nM 44,180,48\n"
"G 4\nM 96,160,32\nR 176,186,49152\nM 96,164,48\ns_add_u32 s64, s70, 14688256\nbuffer_load_dwordx4 a[128:131], v252, s[20:23], s64 offen offset:0 sc1\n"
"M 112,160,36\nR 178,186,53248\nM 112,164,52\ns_lshr_b32 s84, s49, 4\nM 128,160,40\nR 180,186,57344\n"
"M 128,164,56\ns_add_u32 s84, s84, 83361792\nM 144,160,44\nR 182,186,61440\nM 144,164,60\ns_barrier\n"
"G 4\nM 148,168,44\nR 160,187,49152\nM 148,172,60\ns_add_u32 s65, s70, 14721024\nbuffer_load_dwordx4 a[132:135], v252, s[20:23], s65 offen offset:0 sc1\n"
"M 132,168,40\nR 162,187,53248\nM 132,172,56\nbuffer_load_dwordx4 v[0:3], v252, s[20:23], s84 offen offset:0 sc1\nM 116,168,36\nR 164,187,57344\n"
"M 116,172,52\ns_add_u32 s66, s71, 14688256\nbuffer_load_dwordx4 a[136:139], v252, s[20:23], s66 offen offset:0 sc1\nM 100,168,32\nR 166,187,61440\nM 100,172,48\n"
"G 4\nM 104,176,32\nR 168,188,0\nM 104,180,48\nbuffer_load_dwordx4 v[4:7], v252, s[20:23], s84 offen offset:1024 sc1\nM 120,176,36\n"
"R 170,188,4096\nM 120,180,52\ns_add_u32 s67, s71, 14721024\nbuffer_load_dwordx4 a[140:143], v252, s[20:23], s67 offen offset:0 sc1\nM 136,176,40\nR 172,188,8192\n"
"M 136,180,56\nbuffer_load_dwordx4 v[8:11], v252, s[20:23], s84 offen offset:2048 sc1\nM 152,176,44\nR 174,188,12288\nM 152,180,60\nbuffer_load_dwordx4 v[12:15], v252, s[20:23], s84 offen offset:3072 sc1\n"
"G 4\nM 156,160,44\nR 176,189,0\nM 156,164,60\nbuffer_load_dwordx4 a[144:147], v252, s[20:23], s64 offen offset:1024 sc1\nM 140,160,40\n"
"R 178,189,4096\nM 140,164,56\nM 124,160,36\nR 180,189,8192\nM 124,164,52\nM 108,160,32\n"
"R 182,189,12288\nM 108,164,48\nG 4\ns_waitcnt vmcnt(28)\nM 32,168,64\nR 160,190,0\n"
"s_waitcnt vmcnt(24)\nM 32,172,80\nbuffer_load_dwordx4 a[148:151], v252, s[20:23], s65 offen offset:1024 sc1\nM 48,168,68\nR 162,190,4096\ns_waitcnt vmcnt(23)\n"
"M 48,172,84\ns_add_u32 m0, s50, 114688\nM 64,168,72\nR 164,190,8192\ns_add_u32 s63, s54, 1376384\ns_waitcnt vmcnt(22)\n"
"M 64,172,88\nbuffer_load_dwordx4 a[152:155], v252, s[20:23], s66 offen offset:1024 sc1\nM 80,168,76\nR 166,190,12288\ns_waitcnt vmcnt(22)\nM 80,172,92\n"
"s_barrier\nG 4\nM 84,176,76\nR 168,191,0\nM 84,180,92\nbuffer_load_dwordx4 v220, s[12:15], s63 offen lds\n"
"M 68,176,72\nR 170,191,4096\nM 68,180,88\nbuffer_load_dwordx4 a[156:159], v252, s[20:23], s67 offen offset:1024 sc1\nM 52,176,68\nR 172,191,8192\n"
"M 52,180,84\nbuffer_load_dwordx4 v221, s[12:15], s63 offen offset:1024 lds\nM 36,176,64\nR 174,191,12288\ns_add_u32 m0, s50, 0\nM 36,180,80\n"
"G 4\nM 40,160,64\nR 176,188,16384\ns_add_u32 s63, s54, 1605760\nM 40,164,80\nbuffer_load_dwordx4 a[160:163], v252, s[20:23], s64 offen offset:2048 sc1\n"
"M 56,160,68\nR 178,188,20480\nM 56,164,84\nbuffer_load_dwordx4 v220, s[12:15], s63 offen lds\nM 72,160,72\nR 180,188,24576\n"
"M 72,164,88\nbuffer_load_dwordx4 v221, s[12:15], s63 offen offset:1024 lds\nM 88,160,76\nR 182,188,28672\nM 88,164,92\nG 4\n"
"M 92,168,76\nR 160,189,16384\nM 92,172,92\nbuffer_load_dwordx4 a[164:167], v252, s[20:23], s65 offen offset:2048 sc1\nM 76,168,72\nR 162,189,20480\n"
"M 76,172,88\nM 60,168,68\nR 164,189,24576\nM 60,172,84\nbuffer_load_dwordx4 a[168:171], v252, s[20:23], s66 offen offset:2048 sc1\nM 44,168,64\n"
"R 166,189,28672\nM 44,172,80\nG 4\nM 96,176,64\nR 168,190,16384\ns_mov_b32 s85, 0x0c0c0400\n"
"M 96,180,80\ns_waitcnt vmcnt(16)\nv_perm_b32 v16, v1, v0, s85\nv_perm_b32 v20, v3, v2, s85\nM 112,176,68\nR 170,190,20480\n"
"v_lshl_or_b32 v16, v20, 16, v16\nM 112,180,84\nbuffer_load_dwordx4 a[172:175], v252, s[20:23], s67 offen offset:2048 sc1\nM 128,176,72\nR 172,190,24576\ns_waitcnt vmcnt(15)\n"
"v_perm_b32 v17, v5, v4, s85\nM 128,180,88\ns_add_u32 m0, s50, 8192\nv_perm_b32 v21, v7, v6, s85\nv_lshl_or_b32 v17, v21, 16, v17\nM 144,176,76\n"
"R 174,190,28672\ns_add_u32 s63, s54, 1835008\nM 144,180,92\ns_barrier\nG 4\nM 148,160,76\n"
"R 176,191,16384\ns_waitcnt vmcnt(13)\nv_perm_b32 v18, v9, v8, s85\nM 148,164,92\nbuffer_load_dwordx4 a[176:179], v252, s[20:23], s64 offen offset:3072 sc1\nM 132,160,72\n"
"R 178,191,20480\nv_perm_b32 v22, v11, v10, s85\nM 132,164,88\nbuffer_load_dwordx4 v220, s[12:15], s63 offen lds\nM 116,160,68\nR 180,191,24576\n"
"v_lshl_or_b32 v18, v22, 16, v18\nM 116,164,84\nbuffer_load_dwordx4 v221, s[12:15], s63 offen offset:1024 lds\nM 100,160,64\nR 182,191,28672\ns_add_u32 m0, s50, 16384\n"
"M 100,164,80\nG 4\ns_waitcnt vmcnt(15)\nv_perm_b32 v19, v13, v12, s85\nv_perm_b32 v23, v15, v14, s85\nM 104,168,64\n"
"R 160,188,32768\ns_add_u32 s63, s54, 2064384\nM 104,172,80\nbuffer_load_dwordx4 a[180:183], v252, s[20:23], s65 offen offset:3072 sc1\nM 120,168,68\nR 162,188,36864\n"
"v_lshl_or_b32 v19, v23, 16, v19\nM 120,172,84\nbuffer_load_dwordx4 v220, s[12:15], s63 offen lds\nM 136,168,72\nR 164,188,40960\nv_min3_u32 v230, v16, v17, v18\n"
"M 136,172,88\nbuffer_load_dwordx4 a[184:187], v252, s[20:23], s66 offen offset:3072 sc1\nM 152,168,76\nR 166,188,45056\nv_min3_u32 v230, v230, v19, v19\nM 152,172,92\n"
"G 4\nv_max3_u32 v231, v16, v17, v18\nv_max3_u32 v231, v231, v19, v19\nM 156,176,76\nR 168,189,32768\nbuffer_load_dwordx4 v221, s[12:15], s63 offen offset:1024 lds\n"
"M 156,180,92\nv_cmp_ne_u32 s[86:87], s46, v230\nv_cmp_ne_u32 s[88:89], s46, v231\nM 140,176,72\nR 170,189,36864\nM 140,180,88\n"
"buffer_load_dwordx4 a[188:191], v252, s[20:23], s67 offen offset:3072 sc1\nM 124,176,68\nR 172,189,40960\nM 124,180,84\ns_or_b64 s[86:87], s[86:87], s[88:89]\ns_cbranch_scc1 Lq808\n"
"Lq809:\nv_and_b32 v0, 0xffffff00, v0\nM 108,176,64\nR 174,189,45056\nv_and_b32 v1, 0xffffff00, v1\nM 108,180,80\n"
"G 4\nv_and_b32 v2, 0xffffff00, v2\nv_and_b32 v3, 0xffffff00, v3\nM 32,160,96\nR 176,190,32768\nv_and_b32 v4, 0xffffff00, v4\n"
"M 32,164,112\ns_add_u32 s64, s70, 14692352\nbuffer_load_dwordx4 a[192:195], v252, s[20:23], s64 offen offset:0 sc1\nM 48,160,100\nR 178,190,36864\nv_and_b32 v5, 0xffffff00, v5\n"
"M 48,164,116\nv_and_b32 v6, 0xffffff00, v6\nv_and_b32 v7, 0xffffff00, v7\nM 64,160,104\nR 180,190,40960\nv_and_b32 v8, 0xffffff00, v8\n"
"M 64,164,120\ns_add_u32 m0, s50, 24576\nv_and_b32 v9, 0xffffff00, v9\nv_and_b32 v10, 0xffffff00, v10\nM 80,160,108\nR 182,190,45056\n"
"s_add_u32 s63, s54, 2293760\nM 80,164,124\ns_waitcnt vmcnt(12)\ns_barrier\nG 4\nM 84,168,108\n"
"R 160,191,32768\nv_and_b32 v11, 0xffffff00, v11\nM 84,172,124\ns_add_u32 s65, s70, 14725120\nbuffer_load_dwordx4 a[196:199], v252, s[20:23], s65 offen offset:0 sc1\nM 68,168,104\n"
"R 162,191,36864\nv_and_b32 v12, 0xffffff00, v12\nM 68,172,120\nbuffer_load_dwordx4 v220, s[12:15], s63 offen lds\nM 52,168,100\nR 164,191,40960\n"
"v_and_b32 v13, 0xffffff00, v13\nM 52,172,116\ns_add_u32 s66, s71, 14692352\nbuffer_load_dwordx4 a[200:203], v252, s[20:23], s66 offen offset:0 sc1\nM 36,168,96\nR 166,191,45056\n"
"v_and_b32 v14, 0xffffff00, v14\nM 36,172,112\nG 4\nv_and_b32 v15, 0xffffff00, v15\nv_add_f32 v0, v0, v8\nM 40,176,96\n"
"R 168,188,49152\nbuffer_load_dwordx4 v221, s[12:15], s63 offen offset:1024 lds\nM 40,180,112\ns_add_u32 m0, s50, 32768\ns_add_u32 s63, s54, 2523136\nbuffer_load_dwordx4 v220, s[12:15], s63 offen lds\n"
"M 56,176,100\nR 170,188,53248\nv_add_f32 v1, v1, v9\nM 56,180,116\ns_add_u32 s67, s71, 14725120\nbuffer_load_dwordx4 a[204:207], v252, s[20:23], s67 offen offset:0 sc1\n"
"M 72,176,104\nR 172,184,0\nv_add_f32 v2, v2, v10\nM 72,180,120\nbuffer_load_dwordx4 v221, s[12:15], s63 offen offset:1024 lds\nM 88,176,108\n"
"R 174,184,4096\nv_add_f32 v3, v3, v11\nM 88,180,124\nG 4\nv_add_f32 v4, v4, v12\nv_add_f32 v5, v5, v13\n"
"M 92,160,108\nR 176,189,49152\nv_add_f32 v6, v6, v14\nM 92,164,124\nbuffer_load_dwordx4 a[208:211], v252, s[20:23], s64 offen offset:1024 sc1\nM 76,160,104\n"
"R 178,189,53248\nv_add_f32 v7, v7, v15\nM 76,164,120\nv_add_f32 v0, v0, v4\nv_add_f32 v1, v1, v5\nM 60,160,100\n"
"R 180,185,0\nLq1024:\ns_lshr_b32 s60, s45, 3\ns_lshl2_add_u32 s60, s60, s40\ns_lshl_b32 s60, s60, 8\nv_lshl_add_u32 v224, v252, 3, s60\n"
"v_add_u32 v224, Lq1024+4096-LqPC, v224\nv_min_u32 v224, LqE-LqPC-4, v224\ns_mov_b64 exec, 3\nglobal_load_dword v224, v224, s[98:99]\ns_mov_b64 exec, -1\nM 60,164,116\n"
"v_add_f32 v2, v2, v6\nv_add_f32 v3, v3, v7\nM 44,160,96\nR 182,185,4096\nv_permlane32_swap_b32 v0, v2\nM 44,164,112\n"
"G 4\nv_permlane32_swap_b32 v1, v3\nv_add_f32 v0, v0, v2\nM 96,168,96\nR 160,190,49152\nv_add_f32 v1, v1, v3\n"
"M 96,172,112\nbuffer_load_dwordx4 a[212:215], v252, s[20:23], s65 offen offset:1024 sc1\nM 112,168,100\nR 162,190,53248\nv_permlane16_swap_b32 v0, v1\nM 112,172,116\n"
"s_add_u32 m0, s50, 40960\nv_add_f32 v230, v0, v1\nv_mov_b32 v232, v230\nM 128,168,104\nR 164,186,0\ns_add_u32 s63, s54, 2752512\n"
"M 128,172,120\nbuffer_load_dwordx4 a[216:219], v252, s[20:23], s66 offen offset:1024 sc1\nM 144,168,108\nR 166,186,4096\nv_permlane16_swap_b32 v230, v232\nM 144,172,124\n"
"s_waitcnt vmcnt(13)\ns_barrier\nG 4\nM 148,176,108\nR 168,191,49152\nv_mov_b32 v231, v230\n"
"M 148,180,124\nbuffer_load_dwordx4 v220, s[12:15], s63 offen lds\nM 132,176,104\nR 170,191,53248\nv_mov_b32 v233, v232\nM 132,180,120\n"
"buffer_load_dwordx4 a[220:223], v252, s[20:23], s67 offen offset:1024 sc1\nM 116,176,100\nR 172,187,0\nv_permlane32_swap_b32 v230, v231\nM 116,180,116\nbuffer_load_dwordx4 v221, s[12:15], s63 offen offset:1024 lds\n"
"M 100,176,96\nR 174,187,4096\ns_add_u32 m0, s50, 49152\nM 100,180,112\nG 4\nv_permlane32_swap_b32 v232, v233\n"
"v_mul_f32 v230, 0xba800000, v230\nM 104,160,96\nR 176,184,8192\ns_add_u32 s63, s54, 2981888\nM 104,164,112\nbuffer_load_dwordx4 a[224:227], v252, s[20:23], s64 offen offset:2048 sc1\n"
"M 120,160,100\nR 178,184,12288\nv_mul_f32 v214, v230, v230\nM 120,164,116\nbuffer_load_dwordx4 v220, s[12:15], s63 offen lds\nM 136,160,104\n"
"R 180,184,16384\nv_mul_f32 v231, 0x3a800000, v231\nM 136,164,120\nbuffer_load_dwordx4 v221, s[12:15], s63 offen offset:1024 lds\nM 152,160,108\nR 182,184,20480\n"
"v_sub_f32 v231, v231, v214\nM 152,164,124\nG 4\nv_max_f32 v231, 0, v231\nv_add_f32 v231, 0x3727c5ac, v231\nM 156,168,108\n"
"R 160,185,8192\nv_and_b32 v208, s77, v230\nM 156,172,124\nbuffer_load_dwordx4 a[228:231], v252, s[20:23], s65 offen offset:2048 sc1\nM 140,168,104\nR 162,185,12288\n"
"v_sub_f32 v230, v230, v208\nM 140,172,120\nv_rsq_f32 v234, v231\nM 124,168,100\nR 164,185,16384\nv_mul_f32 v216, v231, v234\n"
"M 124,172,116\nbuffer_load_dwordx4 a[232:235], v252, s[20:23], s66 offen offset:2048 sc1\nM 108,168,96\nR 166,185,20480\nv_cvt_pk_bf16_f32 v209, v230, v216\nM 108,172,112\n"
"G 4\nv_cvt_pk_bf16_f32 v208, v208, v208\nv_mul_f32 v214, 0xc0135761, v234\nM 32,176,128\nR 168,186,8192\nv_mul_f32 v215, v234, v234\n"
"M 32,180,144\ns_mov_b64 s[60:61], 0xffff\nv_cndmask_b32 v208, 0, v208, s[60:61]\nv_mul_f32 v215, v215, v214\nM 48,176,132\nR 170,186,12288\n"
"v_mul_f32 v215, 0x3d372713, v215\nM 48,180,148\nbuffer_load_dwordx4 a[236:239], v252, s[20:23], s67 offen offset:2048 sc1\nM 64,176,136\nR 172,186,16384\nv_mul_f32 v232, 0xba800000, v232\n"
"M 64,180,152\ns_add_u32 m0, s50, 57344\ns_mov_b64 s[60:61], 0xffff\nv_cndmask_b32 v209, 0, v209, s[60:61]\nM 80,176,140\nR 174,186,20480\n"
"s_add_u32 s63, s54, 3211264\nM 80,180,156\ns_waitcnt vmcnt(13)\ns_barrier\nG 4\nM 84,160,140\n"
"R 176,187,8192\nv_mul_f32 v217, v232, v232\nM 84,164,156\nbuffer_load_dwordx4 a[240:243], v252, s[20:23], s64 offen offset:3072 sc1\nM 68,160,136\nR 178,187,12288\n"
"v_mul_f32 v233, 0x3a800000, v233\nM 68,164,152\nbuffer_load_dwordx4 v220, s[12:15], s63 offen lds\nLpg25:\ns_cbranch_execz Lpg29\nM 52,160,132\n"
"R 180,187,16384\nv_sub_f32 v233, v233, v217\nM 52,164,148\nbuffer_load_dwordx4 v221, s[12:15], s63 offen offset:1024 lds\nM 36,160,128\nR 182,187,20480\n"
"s_add_u32 m0, s50, 65536\nM 36,164,144\nG 4\nv_max_f32 v233, 0, v233\nv_add_f32 v233, 0x3727c5ac, v233\nM 40,168,128\n"
"R 160,184,24576\ns_add_u32 s63, s54, 3440640\nM 40,172,144\nbuffer_load_dwordx4 a[244:247], v252, s[20:23], s65 offen offset:3072 sc1\nM 56,168,132\nR 162,184,28672\n"
"v_and_b32 v212, s77, v232\nM 56,172,148\nbuffer_load_dwordx4 v220, s[12:15], s63 offen lds\nM 72,168,136\nR 164,184,32768\nv_sub_f32 v232, v232, v212\n"
"M 72,172,152\nbuffer_load_dwordx4 a[248:251], v252, s[20:23], s66 offen offset:3072 sc1\nM 88,168,140\nR 166,184,36864\nv_cvt_pk_bf16_f32 v212, v212, v212\nM 88,172,156\n"
"G 4\nv_rsq_f32 v235, v233\nM 92,176,140\nR 168,185,24576\nbuffer_load_dwordx4 v221, s[12:15], s63 offen offset:1024 lds\nM 92,180,156\n"
"s_mov_b64 s[60:61], 0xffff\nv_cndmask_b32 v212, 0, v212, s[60:61]\nv_mul_f32 v219, v233, v235\nM 76,176,136\nR 170,185,28672\nv_cvt_pk_bf16_f32 v213, v232, v219\n"
"M 76,180,152\nbuffer_load_dwordx4 a[252:255], v252, s[20:23], s67 offen offset:3072 sc1\nM 60,176,132\nR 172,185,32768\nv_mul_f32 v217, 0xc0135761, v235\nM 60,180,148\n"
"s_mov_b64 s[60:61], 0xffff\nv_cndmask_b32 v213, 0, v213, s[60:61]\nv_mul_f32 v218, v235, v235\nM 44,176,128\nR 174,185,36864\nv_mul_f32 v218, v218, v217\n"
"M 44,180,144\nG 4\nv_mul_f32 v218, 0x3d372713, v218\nM 32,160,160\nR 176,186,24576\nM 32,164,176\n"
"M 48,160,164\nR 178,186,28672\nM 48,164,180\ns_add_u32 m0, s50, 73728\nM 64,160,168\nR 180,186,32768\n"
"M 64,164,184\ns_add_u32 s63, s54, 1835136\nM 80,160,172\nR 182,186,36864\nM 80,164,188\ns_waitcnt vmcnt(11)\n"
"s_barrier\nG 4\nM 84,168,172\nR 160,187,24576\nM 84,172,188\nbuffer_load_dwordx4 v220, s[12:15], s63 offen lds\n"
"M 68,168,168\nR 162,187,28672\nM 68,172,184\nM 52,168,164\nR 164,187,32768\nv_bfe_u32 v250, v252, 5, 1\n"
"v_mul_u32_u24 v250, 2560, v250\nM 52,172,180\nbuffer_load_dwordx4 v221, s[12:15], s63 offen offset:1024 lds\nv_and_b32 v251, 16, v252\nM 36,168,160\nR 166,187,36864\n"
"s_add_u32 m0, s50, 81920\nv_lshl_add_u32 v250, v251, 1, v250\nM 36,172,176\ns_lshl_b32 s60, s90, 6\nv_add_u32 v250, s60, v250\nG 4\n"
"M 40,176,160\nR 168,184,40960\ns_add_u32 s63, s54, 2064512\nM 40,180,176\nbuffer_load_dwordx4 v220, s[12:15], s63 offen lds\nM 56,176,164\n"
"R 170,184,45056\nM 56,180,180\nM 72,176,168\nR 172,184,49152\nM 72,180,184\nbuffer_load_dwordx4 v221, s[12:15], s63 offen offset:1024 lds\n"
"M 88,176,172\nR 174,184,53248\ns_add_u32 m0, s50, 90112\nM 88,180,188\nG 4\nM 92,160,172\n"
"R 176,185,40960\ns_add_u32 s63, s54, 2293888\nM 92,164,188\nbuffer_load_dwordx4 v220, s[12:15], s63 offen lds\nM 76,160,168\nR 178,185,45056\n"
"M 76,164,184\nM 60,160,164\nR 180,185,49152\nM 60,164,180\nbuffer_load_dwordx4 v221, s[12:15], s63 offen offset:1024 lds\nM 44,160,160\n"
"R 182,185,53248\ns_add_u32 s63, s54, 2523264\nM 44,164,176\nG 4\nM 32,168,192\nR 160,186,40960\n"
"M 32,172,208\ns_add_u32 m0, s50, 98304\nM 48,168,196\nR 162,186,45056\nM 48,172,212\nbuffer_load_dwordx4 v220, s[12:15], s63 offen lds\n"
"M 64,168,200\nR 164,186,49152\nM 64,172,216\nM 80,168,204\nR 166,186,53248\nM 80,172,220\n"
"s_waitcnt vmcnt(8)\ns_barrier\nG 4\nM 84,176,204\nR 168,187,40960\nM 84,180,220\n"
"buffer_load_dwordx4 v221, s[12:15], s63 offen offset:1024 lds\nM 68,176,200\nR 170,187,45056\ns_add_u32 s63, s54, 2752640\nM 68,180,216\ns_add_u32 m0, s50, 106496\n"
"M 52,176,196\nR 172,187,49152\nM 52,180,212\nbuffer_load_dwordx4 v220, s[12:15], s63 offen lds\nM 36,176,192\nR 174,187,53248\n"
"M 36,180,208\nG 4\nM 40,160,192\nR 176,184,57344\nM 40,164,208\nbuffer_load_dwordx4 v221, s[12:15], s63 offen offset:1024 lds\n"
"M 56,160,196\nR 178,184,61440\ns_add_u32 s63, s54, 2982016\nM 56,164,212\ns_add_u32 m0, s50, 114688\nM 72,160,200\n"
"R 180,188,0\nM 72,164,216\nbuffer_load_dwordx4 v220, s[12:15], s63 offen lds\nM 88,160,204\nR 182,188,4096\nM 88,164,220\n"
"G 4\nM 92,168,204\nR 160,185,57344\nM 92,172,220\nbuffer_load_dwordx4 v221, s[12:15], s63 offen offset:1024 lds\nM 76,168,200\n"
"R 162,185,61440\ns_add_u32 s63, s54, 3211392\nM 76,172,216\ns_add_u32 m0, s50, 0\nM 60,168,196\nR 164,189,0\n"
"M 60,172,212\nbuffer_load_dwordx4 v220, s[12:15], s63 offen lds\nM 44,168,192\nR 166,189,4096\ns_mov_b32 s60, 157184\nv_bfe_u32 v246, v252, 4, 4\n"
"v_lshl_add_u32 v246, v246, 3, s60\nM 44,172,208\nds_read_b64 v[238:239], v246 offset:0\nG 5\nM 32,176,224\nR 168,186,57344\n"
"ds_read_b64 v[240:241], v246 offset:128\nM 32,180,240\nds_read_b64 v[242:243], v246 offset:256\nM 48,176,228\nR 170,186,61440\nds_read_b64 v[244:245], v246 offset:384\n"
"M 48,180,244\nds_read_b64 v[246:247], v246 offset:0\nM 64,176,232\nR 172,190,0\nM 64,180,248\nbuffer_load_dwordx4 v221, s[12:15], s63 offen offset:1024 lds\n"
"M 80,176,236\nR 174,190,4096\ns_add_u32 m0, s50, 8192\ns_waitcnt vmcnt(14)\nM 80,180,252\ns_waitcnt vmcnt(10) lgkmcnt(2)\n"
"s_barrier\nM 84,160,236\nR 176,187,57344\ns_add_u32 s63, s54, 3440768\nM 84,164,252\nbuffer_load_dwordx4 v220, s[12:15], s63 offen lds\n"
"M 68,160,232\nR 178,187,61440\nM 68,164,248\nM 52,160,228\nR 180,191,0\nv_mfma_f32_16x16x32_bf16 v[32:35], v[238:241], v[208:211], v[32:35]\n"
"M 52,164,244\nbuffer_load_dwordx4 v221, s[12:15], s63 offen offset:1024 lds\nM 36,160,224\nR 182,191,4096\nv_mfma_f32_16x16x32_bf16 v[36:39], v[240:243], v[208:211], v[36:39]\ns_add_u32 m0, s50, 16384\n"
"M 36,164,240\nG 4\nM 40,168,224\nR 160,188,8192\nv_mfma_f32_16x16x32_bf16 v[40:43], v[242:245], v[208:211], v[40:43]\ns_add_u32 s63, s55, 0\n"
"M 40,172,240\nbuffer_load_dwordx4 v222, s[12:15], s63 offen lds\nM 56,168,228\nR 162,188,12288\nv_mfma_f32_16x16x32_bf16 v[44:47], v[244:247], v[208:211], v[44:47]\nM 56,172,244\n"
"M 72,168,232\nR 164,188,16384\nv_mfma_f32_16x16x32_bf16 v[48:51], v[244:247], v[210:213], v[48:51]\nM 72,172,248\nbuffer_load_dwordx4 v223, s[12:15], s63 offen offset:1024 lds\nM 88,168,236\n"
"R 166,188,20480\nv_mfma_f32_16x16x32_bf16 v[52:55], v[238:241], v[210:213], v[52:55]\ns_add_u32 s63, s55, 458752\nM 88,172,252\nG 4\nM 92,176,236\n"
"R 168,189,8192\nv_mfma_f32_16x16x32_bf16 v[56:59], v[240:243], v[210:213], v[56:59]\nM 92,180,252\ns_add_u32 m0, s50, 24576\nM 76,176,232\nR 170,189,12288\n"
"v_mfma_f32_16x16x32_bf16 v[60:63], v[242:245], v[210:213], v[60:63]\nM 76,180,248\nbuffer_load_dwordx4 v222, s[12:15], s63 offen lds\nM 60,176,228\nR 172,189,16384\nM 60,180,244\n"
"M 44,176,224\nR 174,189,20480\nM 44,180,240\ns_waitcnt vmcnt(11)\ns_barrier\nG 4\n"
"M 96,160,128\nR 176,190,8192\nM 96,164,144\ns_mov_b32 m0, s51\nM 112,160,132\nR 178,190,12288\n"
"M 112,164,148\nds_write_addtid_b32 v64 offset:0\nM 128,160,136\nR 180,190,16384\nM 128,164,152\nds_write_addtid_b32 v65 offset:256\n"
"M 144,160,140\nR 182,190,20480\nds_write_addtid_b32 v66 offset:512\nM 144,164,156\nds_write_addtid_b32 v67 offset:768\nds_write_addtid_b32 v68 offset:1024\n"
"G 9\nM 148,168,140\nR 160,191,8192\nds_write_addtid_b32 v69 offset:1280\nM 148,172,156\nds_write_addtid_b32 v70 offset:1536\n"
"ds_write_addtid_b32 v71 offset:1792\nM 132,168,136\nR 162,191,12288\nds_write_addtid_b32 v72 offset:2048\nM 132,172,152\nds_write_addtid_b32 v73 offset:2304\n"
"ds_write_addtid_b32 v74 offset:2560\nM 116,168,132\nR 164,191,16384\nds_write_addtid_b32 v75 offset:2816\nM 116,172,148\nds_write_addtid_b32 v76 offset:3072\n"
"ds_write_addtid_b32 v77 offset:3328\nM 100,168,128\nR 166,191,20480\nds_write_addtid_b32 v78 offset:3584\nM 100,172,144\nds_write_addtid_b32 v79 offset:3840\n"
"ds_write_addtid_b32 v80 offset:4096\nG 15\nM 104,176,128\nR 168,188,24576\nds_write_addtid_b32 v81 offset:4352\nM 104,180,144\n"
"ds_write_addtid_b32 v82 offset:4608\nds_write_addtid_b32 v83 offset:4864\nM 120,176,132\nR 170,188,28672\nds_write_addtid_b32 v84 offset:5120\nM 120,180,148\n"
"ds_write_addtid_b32 v85 offset:5376\nds_write_addtid_b32 v86 offset:5632\nM 136,176,136\nR 172,188,32768\nds_write_addtid_b32 v87 offset:5888\nM 136,180,152\n"
"ds_write_addtid_b32 v88 offset:6144\nds_write_addtid_b32 v89 offset:6400\nM 152,176,140\nR 174,188,36864\nds_write_addtid_b32 v90 offset:6656\nM 152,180,156\n"
"ds_write_addtid_b32 v91 offset:6912\nds_write_addtid_b32 v92 offset:7168\nG 15\nM 156,160,140\nR 176,189,24576\nds_write_addtid_b32 v93 offset:7424\n"
"M 156,164,156\nds_write_addtid_b32 v94 offset:7680\nds_write_addtid_b32 v95 offset:7936\nM 140,160,136\nR 178,189,28672\ns_add_u32 m0, s50, 24576\n"
"M 140,164,152\nbuffer_load_dwordx4 v223, s[12:15], s63 offen offset:1024 lds\nM 124,160,132\nR 180,189,32768\ns_waitcnt vmcnt(8) lgkmcnt(2)\ns_barrier\n"
"M 124,164,148\ns_add_u32 m0, s50, 32768\nds_read2st64_b32 v[64:65], v225 offset0:0 offset1:1\nds_read2st64_b32 v[66:67], v225 offset0:2 offset1:3\nM 108,160,128\nR 182,189,36864\n"
"s_add_u32 s63, s55, 917504\nds_read2st64_b32 v[68:69], v225 offset0:4 offset1:5\nM 108,164,144\nds_read2st64_b32 v[70:71], v225 offset0:6 offset1:7\nds_read2st64_b32 v[72:73], v225 offset0:8 offset1:9\nM 96,168,160\n"
"R 160,190,24576\nds_read2st64_b32 v[74:75], v225 offset0:10 offset1:11\nM 96,172,176\nbuffer_load_dwordx4 v222, s[12:15], s63 offen lds\nds_read2st64_b32 v[76:77], v225 offset0:12 offset1:13\nM 112,168,164\n"
"R 162,190,28672\nds_read2st64_b32 v[78:79], v225 offset0:14 offset1:15\nM 112,172,180\nds_read2st64_b32 v[80:81], v225 offset0:16 offset1:17\nds_read2st64_b32 v[82:83], v225 offset0:18 offset1:19\nG 12\n"
"v_add_f32 v32, v32, v64\nM 128,168,168\nR 164,190,32768\nds_read2st64_b32 v[84:85], v225 offset0:20 offset1:21\nv_add_f32 v33, v33, v65\nM 128,172,184\n"
"ds_read2st64_b32 v[86:87], v225 offset0:22 offset1:23\nds_read2st64_b32 v[88:89], v225 offset0:24 offset1:25\nG 15\nv_add_f32 v34, v34, v66\nM 144,168,172\nR 166,190,36864\n"
"ds_read2st64_b32 v[90:91], v225 offset0:26 offset1:27\nv_add_f32 v35, v35, v67\nM 144,172,188\nds_read2st64_b32 v[92:93], v225 offset0:28 offset1:29\nds_read2st64_b32 v[94:95], v225 offset0:30 offset1:31\nG 15\n"
"M 148,176,172\nR 168,191,24576\nv_add_f32 v36, v36, v68\nv_add_f32 v37, v37, v69\nM 148,180,188\nbuffer_load_dwordx4 v223, s[12:15], s63 offen offset:1024 lds\n"
"M 132,176,168\nR 170,191,28672\nv_add_f32 v38, v38, v70\nM 132,180,184\ns_add_u32 m0, s50, 40960\nv_add_f32 v39, v39, v71\n"
"v_add_f32 v40, v40, v72\nM 116,176,164\nR 172,191,32768\nv_add_f32 v41, v41, v73\nM 116,180,180\ns_add_u32 s63, s55, 1376256\n"
"G 15\nv_add_f32 v42, v42, v74\nv_add_f32 v43, v43, v75\nM 100,176,160\nR 174,191,36864\nv_add_f32 v44, v44, v76\n"
"M 100,180,176\nG 7\nv_add_f32 v45, v45, v77\nv_add_f32 v46, v46, v78\nM 104,160,160\nR 176,188,40960\n"
"v_add_f32 v47, v47, v79\nM 104,164,176\nbuffer_load_dwordx4 v222, s[12:15], s63 offen lds\nM 120,160,164\nR 178,188,45056\nv_add_f32 v48, v48, v80\n"
"M 120,164,180\nv_add_f32 v49, v49, v81\nv_add_f32 v50, v50, v82\nM 136,160,168\nR 180,188,49152\nv_add_f32 v51, v51, v83\n"
"M 136,164,184\nv_add_f32 v52, v52, v84\nv_add_f32 v53, v53, v85\nM 152,160,172\nR 182,188,53248\nv_add_f32 v54, v54, v86\n"
"M 152,164,188\nG 4\nv_add_f32 v55, v55, v87\nv_add_f32 v56, v56, v88\nM 156,168,172\nR 160,189,40960\n"
"v_add_f32 v57, v57, v89\nM 156,172,188\nbuffer_load_dwordx4 v223, s[12:15], s63 offen offset:1024 lds\nM 140,168,168\nR 162,189,45056\nv_add_f32 v58, v58, v90\n"
"M 140,172,184\ns_add_u32 m0, s50, 49152\nv_add_f32 v59, v59, v91\nv_add_f32 v60, v60, v92\nM 124,168,164\nR 164,189,49152\n"
"v_add_f32 v61, v61, v93\nM 124,172,180\ns_add_u32 s63, s55, 1835008\nv_add_f32 v62, v62, v94\nv_add_f32 v63, v63, v95\nM 108,168,160\n"
"R 166,189,53248\nv_mul_f32 v230, v32, v32\nv_mul_f32 v231, v33, v33\nM 108,172,176\ns_add_u32 s84, s73, 37748736\ns_add_u32 s85, s73, 37863424\n"
"G 4\nv_mul_f32 v232, v34, v34\nM 96,176,192\nR 168,190,40960\nv_mul_f32 v233, v35, v35\nv_fma_f32 v230, v230, v215, v214\n"
"M 96,180,208\nbuffer_load_dwordx4 v222, s[12:15], s63 offen lds\nv_fma_f32 v231, v231, v215, v214\nM 112,176,196\nR 170,190,45056\ns_waitcnt vmcnt(7)\n"
"s_barrier\nv_fma_f32 v232, v232, v215, v214\nM 112,180,212\nv_fma_f32 v233, v233, v215, v214\nv_mul_f32 v230, v32, v230\nv_mul_f32 v231, v33, v231\n"
"M 128,176,200\nR 172,190,49152\nv_mul_f32 v232, v34, v232\nv_mul_f32 v233, v35, v233\nM 128,180,216\nv_exp_f32 v230, v230\n"
"v_exp_f32 v231, v231\nM 144,176,204\nR 174,190,53248\nv_fma_f32 v230, v230, v216, v216\nv_exp_f32 v232, v232\nM 144,180,220\n"
"G 4\nv_exp_f32 v233, v233\nv_fma_f32 v231, v231, v216, v216\nM 148,160,204\nR 176,191,40960\nv_fma_f32 v232, v232, v216, v216\n"
"v_fma_f32 v233, v233, v216, v216\nM 148,164,220\nbuffer_load_dwordx4 v223, s[12:15], s63 offen offset:1024 lds\nv_rcp_f32 v230, v230\nM 132,160,200\nR 178,191,45056\n"
"v_mul_f32 v230, v32, v230\nv_rcp_f32 v231, v231\nM 132,164,216\ns_add_u32 m0, s50, 57344\nv_rcp_f32 v232, v232\nv_rcp_f32 v233, v233\n"
"M 116,160,196\nR 180,191,49152\nv_mul_f32 v231, v33, v231\nv_mul_f32 v232, v34, v232\nM 116,164,212\ns_add_u32 s63, s55, 2293760\n"
"v_mul_f32 v233, v35, v233\nv_cvt_pk_bf16_f32 v32, v230, v231\nv_cvt_pk_bf16_f32 v33, v232, v233\nM 100,160,192\nR 182,191,53248\nv_mul_f32 v234, v36, v36\n"
"v_mul_f32 v235, v37, v37\nM 100,164,208\nG 4\nv_mul_f32 v236, v38, v38\nv_mul_f32 v237, v39, v39\nv_fma_f32 v234, v234, v215, v214\n"
"M 104,168,192\nR 160,184,0\nv_fma_f32 v235, v235, v215, v214\nv_fma_f32 v236, v236, v215, v214\nM 104,172,208\nbuffer_load_dwordx4 v222, s[12:15], s63 offen lds\n"
"v_fma_f32 v237, v237, v215, v214\nM 120,168,196\nR 162,184,4096\nv_mul_f32 v234, v36, v234\nv_mul_f32 v235, v37, v235\nM 120,172,212\n"
"v_exp_f32 v234, v234\nv_mul_f32 v236, v38, v236\nM 136,168,200\nLpg26:\ns_cbranch_execz Lpg30\nR 164,184,8192\n"
"v_mul_f32 v237, v39, v237\nv_exp_f32 v235, v235\nM 136,172,216\nv_exp_f32 v236, v236\nv_exp_f32 v237, v237\nM 152,168,204\n"
"R 166,184,12288\nv_fma_f32 v234, v234, v216, v216\nv_fma_f32 v235, v235, v216, v216\nM 152,172,220\nG 4\nv_rcp_f32 v234, v234\n"
"v_fma_f32 v236, v236, v216, v216\nM 156,176,204\nR 168,185,0\nv_fma_f32 v237, v237, v216, v216\nv_rcp_f32 v235, v235\nM 156,180,220\n"
"buffer_load_dwordx4 v223, s[12:15], s63 offen offset:1024 lds\nv_rcp_f32 v236, v236\nM 140,176,200\nR 170,185,4096\nv_mul_f32 v234, v36, v234\nv_rcp_f32 v237, v237\n"
"M 140,180,216\nv_mul_f32 v235, v37, v235\nv_mul_f32 v236, v38, v236\nv_mul_f32 v237, v39, v237\nM 124,176,196\nR 172,185,8192\n"
"v_cvt_pk_bf16_f32 v34, v234, v235\nv_cvt_pk_bf16_f32 v35, v236, v237\nM 124,180,212\nbuffer_store_dwordx4 v[32:35], v252, s[20:23], s84 offen offset:0 sc1\nv_mul_f32 v230, v40, v40\nM 108,176,192\n"
"R 174,185,12288\ns_mov_b32 s60, 157696\nv_bfe_u32 v246, v252, 4, 4\nv_lshl_add_u32 v246, v246, 3, s60\nv_mul_f32 v231, v41, v41\nM 108,180,208\n"
"ds_read_b64 v[238:239], v246 offset:0\nG 5\nv_mul_f32 v232, v42, v42\nM 96,160,224\nR 176,186,0\nds_read_b64 v[240:241], v246 offset:128\n"
"v_mul_f32 v233, v43, v43\nM 96,164,240\nds_read_b64 v[242:243], v246 offset:256\ns_add_u32 m0, s50, 65536\nv_fma_f32 v230, v230, v215, v214\nM 112,160,228\n"
"R 178,186,4096\nds_read_b64 v[244:245], v246 offset:384\nv_fma_f32 v231, v231, v215, v214\nM 112,164,244\nds_read_b64 v[246:247], v246 offset:0\ns_add_u32 s63, s55, 2752512\n"
"v_fma_f32 v232, v232, v215, v214\nM 128,160,232\nR 180,186,8192\nv_fma_f32 v233, v233, v215, v214\nv_mul_f32 v230, v40, v230\nM 128,164,248\n"
"buffer_load_dwordx4 v222, s[12:15], s63 offen lds\nv_mul_f32 v231, v41, v231\nM 144,160,236\nR 182,186,12288\nv_mul_f32 v232, v42, v232\nv_mul_f32 v233, v43, v233\n"
"M 144,164,252\nG 9\nv_exp_f32 v230, v230\nv_exp_f32 v231, v231\nM 148,168,236\nR 160,187,0\n"
"v_fma_f32 v230, v230, v216, v216\nv_exp_f32 v232, v232\nM 148,172,252\nv_exp_f32 v233, v233\nv_fma_f32 v231, v231, v216, v216\nM 132,168,232\n"
"R 162,187,4096\nv_fma_f32 v232, v232, v216, v216\nv_fma_f32 v233, v233, v216, v216\nM 132,172,248\nbuffer_load_dwordx4 v223, s[12:15], s63 offen offset:1024 lds\nv_rcp_f32 v230, v230\n"
"M 116,168,228\nR 164,187,8192\nG 9\nv_mfma_f32_16x16x32_bf16 v[96:99], v[238:241], v[208:211], v[96:99]\ns_add_u32 s63, s55, 3211264\nv_rcp_f32 v231, v231\n"
"M 116,172,244\ns_add_u32 m0, s50, 73728\nv_rcp_f32 v232, v232\nv_rcp_f32 v233, v233\nM 100,168,224\nR 166,187,12288\n"
"G 9\nv_mfma_f32_16x16x32_bf16 v[100:103], v[240:243], v[208:211], v[100:103]\nv_mul_f32 v230, v40, v230\nv_mul_f32 v231, v41, v231\nM 100,172,240\nG 4\n"
"v_mul_f32 v232, v42, v232\nv_mul_f32 v233, v43, v233\nv_cvt_pk_bf16_f32 v40, v230, v231\nM 104,176,224\nR 168,184,16384\nv_mfma_f32_16x16x32_bf16 v[104:107], v[242:245], v[208:211], v[104:107]\n"
"v_cvt_pk_bf16_f32 v41, v232, v233\nv_mul_f32 v234, v44, v44\nM 104,180,240\nbuffer_load_dwordx4 v222, s[12:15], s63 offen lds\nv_mul_f32 v235, v45, v45\nM 120,176,228\n"
"R 170,184,20480\nv_mfma_f32_16x16x32_bf16 v[108:111], v[244:247], v[208:211], v[108:111]\nv_mul_f32 v236, v46, v46\nv_mul_f32 v237, v47, v47\nM 120,180,244\nv_fma_f32 v234, v234, v215, v214\n"
"v_fma_f32 v235, v235, v215, v214\nv_fma_f32 v236, v236, v215, v214\nM 136,176,232\nR 172,186,16384\nv_mfma_f32_16x16x32_bf16 v[112:115], v[244:247], v[210:213], v[112:115]\nv_fma_f32 v237, v237, v215, v214\n"
"v_mul_f32 v234, v44, v234\nM 136,180,248\nv_exp_f32 v234, v234\nv_mul_f32 v235, v45, v235\nM 152,176,236\nR 174,186,20480\n"
"v_mfma_f32_16x16x32_bf16 v[116:119], v[238:241], v[210:213], v[116:119]\nv_mul_f32 v236, v46, v236\nv_mul_f32 v237, v47, v237\nM 152,180,252\nG 4\nv_exp_f32 v235, v235\n"
"v_exp_f32 v236, v236\nM 156,160,236\nR 176,185,16384\nv_mfma_f32_16x16x32_bf16 v[120:123], v[240:243], v[210:213], v[120:123]\nv_fma_f32 v234, v234, v216, v216\nv_exp_f32 v237, v237\n"
"M 156,164,252\nbuffer_load_dwordx4 v223, s[12:15], s63 offen offset:1024 lds\nv_fma_f32 v235, v235, v216, v216\nM 140,160,232\nR 178,185,20480\nv_mfma_f32_16x16x32_bf16 v[124:127], v[242:245], v[210:213], v[124:127]\n"
"v_fma_f32 v236, v236, v216, v216\nv_fma_f32 v237, v237, v216, v216\nM 140,164,248\nv_rcp_f32 v234, v234\nv_rcp_f32 v235, v235\nM 124,160,228\n"
"R 180,187,16384\nv_mul_f32 v234, v44, v234\nv_rcp_f32 v236, v236\nM 124,164,244\nv_rcp_f32 v237, v237\nv_mul_f32 v235, v45, v235\n"
"M 108,160,224\nR 182,187,20480\nv_mul_f32 v236, v46, v236\nv_mul_f32 v237, v47, v237\nM 108,164,240\ns_waitcnt vmcnt(11)\n"
"s_barrier\nG 4\nv_cvt_pk_bf16_f32 v42, v234, v235\nv_mfma_f32_16x16x32_bf16 v[0:3], v[168:171], a[0:3], 0\nR 160,184,24576\nv_cvt_pk_bf16_f32 v43, v236, v237\n"
"v_mul_f32 v230, v48, v48\nM 0,172,16\ns_mov_b32 m0, s51\nv_mul_f32 v231, v49, v49\nv_mul_f32 v232, v50, v50\nv_mul_f32 v233, v51, v51\n"
"v_mfma_f32_16x16x32_bf16 v[16:19], v[168:171], a[4:7], 0\nR 162,184,28672\nv_fma_f32 v230, v230, v218, v217\nv_fma_f32 v231, v231, v218, v217\nM 16,172,20\nbuffer_store_dwordx4 v[40:43], v252, s[20:23], s84 offen offset:1024 sc1\n"
"v_fma_f32 v232, v232, v218, v217\nv_mfma_f32_16x16x32_bf16 v[8:11], v[168:171], a[8:11], 0\nR 164,186,24576\nv_fma_f32 v233, v233, v218, v217\nv_mul_f32 v230, v48, v230\nM 8,172,24\n"
"ds_write_addtid_b32 v128 offset:0\nv_exp_f32 v230, v230\nv_mul_f32 v231, v49, v231\nv_mfma_f32_16x16x32_bf16 v[24:27], v[168:171], a[12:15], 0\nR 166,186,28672\nds_write_addtid_b32 v129 offset:256\n"
"v_mul_f32 v232, v50, v232\nv_mul_f32 v233, v51, v233\nM 24,172,28\nds_write_addtid_b32 v130 offset:512\nG 7\nv_exp_f32 v231, v231\n"
"v_exp_f32 v232, v232\nv_mfma_f32_16x16x32_bf16 v[28:31], v[176:179], a[12:15], 0\nR 168,185,24576\nds_write_addtid_b32 v131 offset:768\nv_fma_f32 v230, v230, v219, v219\nv_exp_f32 v233, v233\n"
"M 28,180,28\nds_write_addtid_b32 v132 offset:1024\nv_rcp_f32 v230, v230\nv_fma_f32 v231, v231, v219, v219\nv_mfma_f32_16x16x32_bf16 v[12:15], v[176:179], a[8:11], 0\nR 170,185,28672\n"
"ds_write_addtid_b32 v133 offset:1280\nv_fma_f32 v232, v232, v219, v219\nv_fma_f32 v233, v233, v219, v219\nM 12,180,24\nds_write_addtid_b32 v134 offset:1536\nv_rcp_f32 v231, v231\n"
"v_rcp_f32 v232, v232\nv_mfma_f32_16x16x32_bf16 v[20:23], v[176:179], a[4:7], 0\nR 172,187,24576\nds_write_addtid_b32 v135 offset:1792\nv_mul_f32 v230, v48, v230\nv_rcp_f32 v233, v233\n"
"M 20,180,20\nds_write_addtid_b32 v136 offset:2048\nv_mul_f32 v231, v49, v231\nv_mul_f32 v232, v50, v232\nv_mul_f32 v233, v51, v233\nv_mfma_f32_16x16x32_bf16 v[4:7], v[176:179], a[0:3], 0\n"
"R 174,187,28672\nds_write_addtid_b32 v137 offset:2304\nv_cvt_pk_bf16_f32 v48, v230, v231\nv_cvt_pk_bf16_f32 v49, v232, v233\nM 4,180,16\nds_write_addtid_b32 v138 offset:2560\n"
"G 14\nv_mul_f32 v234, v52, v52\nv_mul_f32 v235, v53, v53\nv_mul_f32 v236, v54, v54\nM 0,160,32\nR 176,184,32768\n"
"ds_write_addtid_b32 v139 offset:2816\nv_mul_f32 v237, v55, v55\nv_fma_f32 v234, v234, v218, v217\nM 0,164,48\nds_write_addtid_b32 v140 offset:3072\nv_fma_f32 v235, v235, v218, v217\n"
"v_fma_f32 v236, v236, v218, v217\nv_fma_f32 v237, v237, v218, v217\nM 16,160,36\nR 178,184,36864\nds_write_addtid_b32 v141 offset:3328\nv_mul_f32 v234, v52, v234\n"
"v_mul_f32 v235, v53, v235\nM 16,164,52\nds_write_addtid_b32 v142 offset:3584\nv_exp_f32 v234, v234\nv_mul_f32 v236, v54, v236\nM 8,160,40\n"
"R 180,186,32768\nds_write_addtid_b32 v143 offset:3840\nv_mul_f32 v237, v55, v237\nv_exp_f32 v235, v235\nM 8,164,56\nds_write_addtid_b32 v144 offset:4096\n"
"v_exp_f32 v236, v236\nv_exp_f32 v237, v237\nM 24,160,44\nR 182,186,36864\nds_write_addtid_b32 v145 offset:4352\nv_fma_f32 v234, v234, v219, v219\n"
"v_fma_f32 v235, v235, v219, v219\nM 24,164,60\nds_write_addtid_b32 v146 offset:4608\nG 14\nv_rcp_f32 v234, v234\nv_fma_f32 v236, v236, v219, v219\n"
"M 28,168,44\nR 160,185,32768\nds_write_addtid_b32 v147 offset:4864\nv_fma_f32 v237, v237, v219, v219\nv_rcp_f32 v235, v235\nM 28,172,60\n"
"ds_write_addtid_b32 v148 offset:5120\nds_write_addtid_b32 v149 offset:5376\nds_write_addtid_b32 v150 offset:5632\nds_write_addtid_b32 v151 offset:5888\nds_write_addtid_b32 v152 offset:6144\nds_write_addtid_b32 v153 offset:6400\n"
"ds_write_addtid_b32 v154 offset:6656\nds_write_addtid_b32 v155 offset:6912\nds_write_addtid_b32 v156 offset:7168\nds_write_addtid_b32 v157 offset:7424\nds_write_addtid_b32 v158 offset:7680\nds_write_addtid_b32 v159 offset:7936\n"
"v_rcp_f32 v236, v236\nv_rcp_f32 v237, v237\nM 12,168,40\nR 162,185,36864\ns_add_u32 m0, s50, 81920\ns_add_u32 s63, s54, 256\n"
"v_mul_f32 v234, v52, v234\nM 12,172,56\nbuffer_load_dwordx4 v220, s[12:15], s63 offen lds\nv_mul_f32 v235, v53, v235\nM 20,168,36\nR 164,187,32768\n"
"s_waitcnt vmcnt(4) lgkmcnt(2)\ns_barrier\nv_mul_f32 v236, v54, v236\nM 20,172,52\nds_read2st64_b32 v[128:129], v225 offset0:0 offset1:1\nv_mul_f32 v237, v55, v237\n"
"v_cvt_pk_bf16_f32 v50, v234, v235\nv_cvt_pk_bf16_f32 v51, v236, v237\nM 4,168,32\nR 166,187,36864\nds_read2st64_b32 v[130:131], v225 offset0:2 offset1:3\nv_mul_f32 v230, v56, v56\n"
"v_mul_f32 v231, v57, v57\nM 4,172,48\nds_read2st64_b32 v[132:133], v225 offset0:4 offset1:5\nv_mul_f32 v232, v58, v58\nv_mul_f32 v233, v59, v59\nv_fma_f32 v230, v230, v218, v217\n"
"M 0,176,64\nR 168,184,40960\nds_read2st64_b32 v[134:135], v225 offset0:6 offset1:7\nv_fma_f32 v231, v231, v218, v217\nv_fma_f32 v232, v232, v218, v217\nM 0,180,80\n"
"buffer_load_dwordx4 v221, s[12:15], s63 offen offset:1024 lds\nds_read2st64_b32 v[136:137], v225 offset0:8 offset1:9\nv_fma_f32 v233, v233, v218, v217\nM 16,176,68\nR 170,184,45056\ns_add_u32 m0, s50, 90112\n"
"s_add_u32 s63, s54, 229632\nds_read2st64_b32 v[138:139], v225 offset0:10 offset1:11\nv_mul_f32 v230, v56, v230\nM 16,180,84\nbuffer_load_dwordx4 v220, s[12:15], s63 offen lds\nds_read2st64_b32 v[140:141], v225 offset0:12 offset1:13\n"
"v_mul_f32 v231, v57, v231\nM 8,176,72\nR 172,186,40960\nds_read2st64_b32 v[142:143], v225 offset0:14 offset1:15\nv_mul_f32 v232, v58, v232\nv_mul_f32 v233, v59, v233\n"
"M 8,180,88\nbuffer_store_dwordx4 v[48:51], v252, s[20:23], s85 offen offset:0 sc1\nds_read2st64_b32 v[144:145], v225 offset0:16 offset1:17\nv_exp_f32 v230, v230\nM 24,176,76\nR 174,186,45056\n"
"ds_read2st64_b32 v[146:147], v225 offset0:18 offset1:19\nv_fma_f32 v230, v230, v219, v219\nv_exp_f32 v231, v231\nM 24,180,92\nds_read2st64_b32 v[148:149], v225 offset0:20 offset1:21\nG 14\n"
"v_exp_f32 v232, v232\nv_exp_f32 v233, v233\nM 28,160,76\nR 176,185,40960\nds_read2st64_b32 v[150:151], v225 offset0:22 offset1:23\nv_fma_f32 v231, v231, v219, v219\n"
"v_fma_f32 v232, v232, v219, v219\nM 28,164,92\nbuffer_load_dwordx4 v221, s[12:15], s63 offen offset:1024 lds\nds_read2st64_b32 v[152:153], v225 offset0:24 offset1:25\nv_fma_f32 v233, v233, v219, v219\nM 12,160,72\n"
"R 178,185,45056\ns_add_u32 s63, s54, 459008\nds_read2st64_b32 v[154:155], v225 offset0:26 offset1:27\nv_rcp_f32 v230, v230\nM 12,164,88\ns_add_u32 m0, s50, 98304\n"
"ds_read2st64_b32 v[156:157], v225 offset0:28 offset1:29\nv_rcp_f32 v231, v231\nv_rcp_f32 v232, v232\nM 20,160,68\nR 180,187,40960\nds_read2st64_b32 v[158:159], v225 offset0:30 offset1:31\n"
"v_mul_f32 v230, v56, v230\nv_rcp_f32 v233, v233\nM 20,164,84\nbuffer_load_dwordx4 v220, s[12:15], s63 offen lds\nv_add_f32 v96, v96, v128\nv_mul_f32 v231, v57, v231\n"
"M 4,160,64\nR 182,187,45056\nv_mul_f32 v232, v58, v232\nv_add_f32 v97, v97, v129\nv_mul_f32 v233, v59, v233\nM 4,164,80\n"
"G 11\nv_cvt_pk_bf16_f32 v56, v230, v231\nv_cvt_pk_bf16_f32 v57, v232, v233\nv_add_f32 v98, v98, v130\nv_mul_f32 v234, v60, v60\nM 0,168,96\n"
"R 160,184,49152\nv_mul_f32 v235, v61, v61\nv_add_f32 v99, v99, v131\nv_mul_f32 v236, v62, v62\nM 0,172,112\nbuffer_load_dwordx4 v221, s[12:15], s63 offen offset:1024 lds\n"
"v_add_f32 v100, v100, v132\nv_mul_f32 v237, v63, v63\nM 16,168,100\nR 162,184,53248\ns_add_u32 s63, s54, 688384\nv_add_f32 v101, v101, v133\n"
"v_fma_f32 v234, v234, v218, v217\nM 16,172,116\ns_add_u32 m0, s50, 106496\nv_fma_f32 v235, v235, v218, v217\nv_fma_f32 v236, v236, v218, v217\nv_add_f32 v102, v102, v134\n"
"v_fma_f32 v237, v237, v218, v217\nM 8,168,104\nR 164,186,49152\nv_mul_f32 v234, v60, v234\nv_add_f32 v103, v103, v135\nv_mul_f32 v235, v61, v235\n"
"M 8,172,120\nbuffer_load_dwordx4 v220, s[12:15], s63 offen lds\nv_add_f32 v104, v104, v136\nv_mul_f32 v236, v62, v236\nM 24,168,108\nR 166,186,53248\n"
"v_mul_f32 v237, v63, v237\nv_add_f32 v105, v105, v137\nv_exp_f32 v234, v234\nM 24,172,124\nG 4\nv_add_f32 v106, v106, v138\n"
"v_add_f32 v107, v107, v139\nv_add_f32 v108, v108, v140\nv_exp_f32 v235, v235\nM 28,176,108\nR 168,185,49152\nv_add_f32 v109, v109, v141\n"
"v_add_f32 v110, v110, v142\nv_exp_f32 v236, v236\nM 28,180,124\nv_add_f32 v111, v111, v143\nv_add_f32 v112, v112, v144\nv_add_f32 v113, v113, v145\n"
"v_exp_f32 v237, v237\nM 12,176,104\nR 170,185,53248\nv_add_f32 v114, v114, v146\nv_add_f32 v115, v115, v147\nv_fma_f32 v234, v234, v219, v219\n"
"M 12,180,120\nbuffer_load_dwordx4 v221, s[12:15], s63 offen offset:1024 lds\nv_add_f32 v116, v116, v148\nv_fma_f32 v235, v235, v219, v219\nM 20,176,100\nR 172,187,49152\n"
"s_add_u32 s63, s54, 917760\nv_add_f32 v117, v117, v149\nM 20,180,116\ns_add_u32 m0, s50, 114688\nv_add_f32 v118, v118, v150\nv_add_f32 v119, v119, v151\n"
"v_add_f32 v120, v120, v152\nv_add_f32 v121, v121, v153\nv_add_f32 v122, v122, v154\nv_add_f32 v123, v123, v155\nv_add_f32 v124, v124, v156\nv_add_f32 v125, v125, v157\n"
"v_add_f32 v126, v126, v158\nv_add_f32 v127, v127, v159\nM 4,176,96\nR 174,187,53248\nv_fma_f32 v236, v236, v219, v219\nM 4,180,112\n"
"G 4\nv_rcp_f32 v234, v234\nM 0,160,128\nR 176,184,57344\nv_fma_f32 v237, v237, v219, v219\nM 0,164,144\n"
"buffer_load_dwordx4 v220, s[12:15], s63 offen lds\nM 16,160,132\nR 178,184,61440\nv_mul_f32 v234, v60, v234\nM 16,164,148\nv_rcp_f32 v235, v235\n"
"M 8,160,136\nR 180,186,57344\nLq1026:\ns_lshr_b32 s60, s45, 3\ns_lshl2_add_u32 s60, s60, s40\ns_lshl_b32 s60, s60, 8\n"
"v_lshl_add_u32 v224, v252, 3, s60\nv_add_u32 v224, Lq1026+4096-LqPC, v224\nv_min_u32 v224, LqE-LqPC-4, v224\ns_mov_b64 exec, 3\nglobal_load_dword v224, v224, s[98:99]\ns_mov_b64 exec, -1\n"
"M 8,164,152\nv_rcp_f32 v236, v236\nM 24,160,140\nR 182,186,61440\nv_mul_f32 v235, v61, v235\nM 24,164,156\n"
"G 4\nv_rcp_f32 v237, v237\nM 28,168,140\nR 160,185,57344\nv_mul_f32 v236, v62, v236\nM 28,172,156\n"
"buffer_load_dwordx4 v221, s[12:15], s63 offen offset:1024 lds\nM 12,168,136\nR 162,185,61440\ns_add_u32 m0, s50, 0\ns_add_u32 s63, s54, 1147136\nM 12,172,152\n"
"buffer_load_dwordx4 v220, s[12:15], s63 offen lds\nM 20,168,132\nR 164,187,57344\nv_mul_f32 v237, v63, v237\nM 20,172,148\nv_cvt_pk_bf16_f32 v58, v234, v235\n"
"v_cvt_pk_bf16_f32 v59, v236, v237\nM 4,168,128\nR 166,187,61440\nM 4,172,144\nG 4\nM 0,176,160\n"
"R 168,188,0\nM 0,180,176\nbuffer_load_dwordx4 v221, s[12:15], s63 offen offset:1024 lds\nM 16,176,164\nR 170,188,4096\ns_add_u32 m0, s50, 8192\n"
"s_add_u32 s63, s54, 1376512\nM 16,180,180\nbuffer_load_dwordx4 v220, s[12:15], s63 offen lds\nM 8,176,168\nR 172,190,0\nM 8,180,184\n"
"buffer_store_dwordx4 v[56:59], v252, s[20:23], s85 offen offset:1024 sc1\nM 24,176,172\nR 174,190,4096\nM 24,180,188\nG 4\nM 28,160,172\n"
"R 176,189,0\ns_waitcnt vmcnt(11)\ns_barrier\nv_mul_f32 v230, v96, v96\nM 28,164,188\nbuffer_load_dwordx4 v221, s[12:15], s63 offen offset:1024 lds\n"
"v_mul_f32 v231, v97, v97\nM 12,160,168\nR 178,189,4096\ns_add_u32 m0, s50, 16384\ns_add_u32 s63, s54, 1605888\nv_mul_f32 v232, v98, v98\n"
"M 12,164,184\nbuffer_load_dwordx4 v220, s[12:15], s63 offen lds\nv_mul_f32 v233, v99, v99\nM 20,160,164\nR 180,191,0\nv_fma_f32 v230, v230, v215, v214\n"
"v_fma_f32 v231, v231, v215, v214\nM 20,164,180\ns_add_u32 s84, s73, 37750784\ns_add_u32 s85, s73, 37865472\nv_fma_f32 v232, v232, v215, v214\nv_fma_f32 v233, v233, v215, v214\n"
"M 4,160,160\nR 182,191,4096\ns_mov_b32 s60, 158720\nv_bfe_u32 v242, v252, 4, 4\nv_lshl_add_u32 v242, v242, 3, s60\nv_mul_f32 v230, v96, v230\n"
"M 4,164,176\nds_read_b64 v[238:239], v242 offset:0\nG 5\nv_mul_f32 v231, v97, v231\nM 0,168,192\nR 160,188,8192\n"
"ds_read_b64 v[240:241], v242 offset:128\nv_mul_f32 v232, v98, v232\nM 0,172,208\nds_read_b64 v[242:243], v242 offset:0\nv_mul_f32 v233, v99, v233\nv_exp_f32 v230, v230\n"
"M 16,168,196\nR 162,188,12288\nv_fma_f32 v230, v230, v216, v216\nv_exp_f32 v231, v231\nM 16,172,212\nv_exp_f32 v232, v232\n"
"v_exp_f32 v233, v233\nM 8,168,200\nR 164,190,8192\nv_fma_f32 v231, v231, v216, v216\nLpg27:\ns_cbranch_execz Lpg31\n"
"v_fma_f32 v232, v232, v216, v216\nM 8,172,216\nv_rcp_f32 v230, v230\nv_fma_f32 v233, v233, v216, v216\nM 24,168,204\nR 166,190,12288\n"
"v_mul_f32 v230, v96, v230\nv_rcp_f32 v231, v231\nM 24,172,220\nG 7\nv_rcp_f32 v232, v232\nv_rcp_f32 v233, v233\n"
"M 28,176,204\nR 168,189,8192\nv_mul_f32 v231, v97, v231\nv_mul_f32 v232, v98, v232\nM 28,180,220\nv_mul_f32 v233, v99, v233\n"
"v_cvt_pk_bf16_f32 v96, v230, v231\nv_cvt_pk_bf16_f32 v97, v232, v233\nM 12,176,200\nR 170,189,12288\nv_mul_f32 v234, v100, v100\nv_mul_f32 v235, v101, v101\n"
"M 12,180,216\nv_mul_f32 v236, v102, v102\nv_mul_f32 v237, v103, v103\nv_fma_f32 v234, v234, v215, v214\nM 20,176,196\nR 172,191,8192\n"
"G 7\nv_mfma_f32_16x16x32_bf16 v[0:3], v[238:241], v[208:211], v[0:3]\nv_fma_f32 v235, v235, v215, v214\nv_fma_f32 v236, v236, v215, v214\nM 20,180,212\nv_fma_f32 v237, v237, v215, v214\n"
"v_mul_f32 v234, v100, v234\nv_mul_f32 v235, v101, v235\nM 4,176,192\nR 174,191,12288\nG 7\nv_mfma_f32_16x16x32_bf16 v[4:7], v[240:243], v[208:211], v[4:7]\n"
"v_mul_f32 v236, v102, v236\nv_mul_f32 v237, v103, v237\nM 4,180,208\nG 4\nv_exp_f32 v234, v234\nv_exp_f32 v235, v235\n"
"M 0,160,224\nR 176,188,16384\nv_mfma_f32_16x16x32_bf16 v[16:19], v[240:243], v[210:213], v[16:19]\nv_fma_f32 v234, v234, v216, v216\nv_exp_f32 v236, v236\nM 0,164,240\n"
"v_exp_f32 v237, v237\nv_fma_f32 v235, v235, v216, v216\nM 16,160,228\nR 178,188,20480\nv_mfma_f32_16x16x32_bf16 v[20:23], v[238:241], v[210:213], v[20:23]\nv_fma_f32 v236, v236, v216, v216\n"
"v_fma_f32 v237, v237, v216, v216\nM 16,164,244\nv_rcp_f32 v234, v234\nv_rcp_f32 v235, v235\nM 8,160,232\nR 180,188,24576\n"
"v_mul_f32 v234, v100, v234\nv_rcp_f32 v236, v236\nM 8,164,248\nv_rcp_f32 v237, v237\nv_mul_f32 v235, v101, v235\nM 24,160,236\n"
"R 182,188,28672\nv_mul_f32 v236, v102, v236\nv_mul_f32 v237, v103, v237\nM 24,164,252\nG 4\nv_cvt_pk_bf16_f32 v98, v234, v235\n"
"v_cvt_pk_bf16_f32 v99, v236, v237\nv_mul_f32 v230, v104, v104\nM 28,168,236\nR 160,189,16384\nv_mul_f32 v231, v105, v105\nv_mul_f32 v232, v106, v106\n"
"M 28,172,252\nv_mul_f32 v233, v107, v107\nv_fma_f32 v230, v230, v215, v214\nv_fma_f32 v231, v231, v215, v214\nM 12,168,232\nR 162,189,20480\n"
"v_fma_f32 v232, v232, v215, v214\nv_fma_f32 v233, v233, v215, v214\nM 12,172,248\nv_mul_f32 v230, v104, v230\nv_mul_f32 v231, v105, v231\nv_mul_f32 v232, v106, v232\n"
"M 20,168,228\nR 164,189,24576\nv_mul_f32 v233, v107, v233\nv_exp_f32 v230, v230\nM 20,172,244\nv_exp_f32 v231, v231\n"
"v_exp_f32 v232, v232\nM 4,168,224\nR 166,189,28672\nv_fma_f32 v230, v230, v216, v216\nv_exp_f32 v233, v233\nM 4,172,240\n"
"s_waitcnt vmcnt(11)\ns_barrier\nG 4\nv_fma_f32 v231, v231, v216, v216\nv_mfma_f32_16x16x32_bf16 v[32:35], v[176:179], a[0:3], 0\nR 168,190,16384\n"
"v_fma_f32 v232, v232, v216, v216\nv_fma_f32 v233, v233, v216, v216\nM 32,180,16\ns_mov_b32 m0, s51\nv_rcp_f32 v230, v230\nv_rcp_f32 v231, v231\n"
"v_mfma_f32_16x16x32_bf16 v[48:51], v[176:179], a[4:7], 0\nR 170,190,20480\nds_write_addtid_b32 v8 offset:0\nv_mul_f32 v230, v104, v230\nv_rcp_f32 v232, v232\nM 48,180,20\n"
"ds_write_addtid_b32 v9 offset:256\nv_rcp_f32 v233, v233\nv_mul_f32 v231, v105, v231\nv_mfma_f32_16x16x32_bf16 v[64:67], v[176:179], a[8:11], 0\nR 172,190,24576\nds_write_addtid_b32 v10 offset:512\n"
"v_mov_b32 v226, s46\nv_mul_f32 v232, v106, v232\nM 64,180,24\nds_write_addtid_b32 v11 offset:768\nv_mov_b32 v227, s47\nv_mul_f32 v233, v107, v233\n"
"v_cvt_pk_bf16_f32 v104, v230, v231\nv_mfma_f32_16x16x32_bf16 v[80:83], v[176:179], a[12:15], 0\nR 174,190,28672\ns_add_u32 s61, s81, 845824\ns_lshl_b32 s62, s44, 5\ns_add_u32 s61, s61, s62\n"
"s_waitcnt vmcnt(0)\ns_mov_b64 exec, 1\nbuffer_atomic_swap_x2 v[226:227], off, s[20:23], s61 sc1\ns_mov_b64 exec, -1\nds_write_addtid_b32 v12 offset:1024\nv_cvt_pk_bf16_f32 v105, v232, v233\n"
"M 80,180,28\nds_write_addtid_b32 v13 offset:1280\nG 10\nv_mul_f32 v234, v108, v108\nv_mul_f32 v235, v109, v109\nv_mul_f32 v236, v110, v110\n"
"v_mfma_f32_16x16x32_bf16 v[84:87], v[160:163], a[12:15], 0\nR 176,191,16384\nds_write_addtid_b32 v14 offset:1536\nv_mul_f32 v237, v111, v111\nv_fma_f32 v234, v234, v215, v214\nM 84,164,28\n"
"ds_write_addtid_b32 v15 offset:1792\nv_fma_f32 v235, v235, v215, v214\nv_fma_f32 v236, v236, v215, v214\nv_fma_f32 v237, v237, v215, v214\nv_mfma_f32_16x16x32_bf16 v[68:71], v[160:163], a[8:11], 0\nR 178,191,20480\n"
"ds_write_addtid_b32 v24 offset:4096\nv_mul_f32 v234, v108, v234\nv_mul_f32 v235, v109, v235\nM 68,164,24\nbuffer_store_dwordx4 v[96:99], v252, s[20:23], s84 offen offset:0 sc1\nds_write_addtid_b32 v25 offset:4352\n"
"v_mul_f32 v236, v110, v236\nv_mfma_f32_16x16x32_bf16 v[52:55], v[160:163], a[4:7], 0\nR 180,191,24576\nds_write_addtid_b32 v26 offset:4608\nv_mul_f32 v237, v111, v237\nv_exp_f32 v234, v234\n"
"M 52,164,20\nds_write_addtid_b32 v27 offset:4864\nds_write_addtid_b32 v28 offset:5120\nds_write_addtid_b32 v29 offset:5376\nds_write_addtid_b32 v30 offset:5632\nds_write_addtid_b32 v31 offset:5888\n"
"v_exp_f32 v235, v235\nv_exp_f32 v236, v236\nv_mfma_f32_16x16x32_bf16 v[36:39], v[160:163], a[0:3], 0\nR 182,191,28672\ns_add_u32 m0, s50, 16384\nv_exp_f32 v237, v237\n"
"M 36,164,16\nG 15\nv_fma_f32 v234, v234, v216, v216\nv_fma_f32 v235, v235, v216, v216\nv_fma_f32 v236, v236, v216, v216\nv_mfma_f32_16x16x32_bf16 v[40:43], v[168:171], a[0:3], 0\n"
"R 160,188,32768\nG 2\ns_barrier\nv_fma_f32 v237, v237, v216, v216\nM 40,172,16\nbuffer_load_dwordx4 v221, s[12:15], s63 offen offset:1024 lds\n"
"ds_read2st64_b32 v[8:9], v225 offset0:0 offset1:1\nv_rcp_f32 v234, v234\nv_mfma_f32_16x16x32_bf16 v[56:59], v[168:171], a[4:7], 0\nR 162,188,36864\ns_add_u32 m0, s50, 24576\ns_add_u32 s63, s54, 1835264\n"
"ds_read2st64_b32 v[10:11], v225 offset0:2 offset1:3\nv_rcp_f32 v235, v235\nM 56,172,20\nbuffer_load_dwordx4 v220, s[12:15], s63 offen lds\nds_read2st64_b32 v[12:13], v225 offset0:4 offset1:5\nv_rcp_f32 v236, v236\n"
"v_mfma_f32_16x16x32_bf16 v[72:75], v[168:171], a[8:11], 0\nR 164,188,40960\nds_read2st64_b32 v[14:15], v225 offset0:6 offset1:7\nv_mul_f32 v234, v108, v234\nv_rcp_f32 v237, v237\nM 72,172,24\n"
"ds_read2st64_b32 v[24:25], v225 offset0:16 offset1:17\nv_mul_f32 v235, v109, v235\nv_mul_f32 v236, v110, v236\nv_mul_f32 v237, v111, v237\nv_mfma_f32_16x16x32_bf16 v[88:91], v[168:171], a[12:15], 0\nR 166,188,45056\n"
"ds_read2st64_b32 v[26:27], v225 offset0:18 offset1:19\nv_cvt_pk_bf16_f32 v106, v234, v235\nv_cvt_pk_bf16_f32 v107, v236, v237\nM 88,172,28\nds_read2st64_b32 v[28:29], v225 offset0:20 offset1:21\nG 11\n"
"v_mul_f32 v230, v112, v112\nv_mul_f32 v231, v113, v113\nv_mul_f32 v232, v114, v114\nv_mfma_f32_16x16x32_bf16 v[92:95], v[176:179], a[12:15], 0\nR 168,189,32768\nds_read2st64_b32 v[30:31], v225 offset0:22 offset1:23\n"
"v_mul_f32 v233, v115, v115\nv_fma_f32 v230, v230, v218, v217\nM 92,180,28\nbuffer_load_dwordx4 v221, s[12:15], s63 offen offset:1024 lds\nG 11\nv_add_f32 v0, v0, v8\n"
"v_fma_f32 v231, v231, v218, v217\nv_mfma_f32_16x16x32_bf16 v[76:79], v[176:179], a[8:11], 0\nR 170,189,36864\ns_add_u32 m0, s50, 32768\ns_add_u32 s63, s54, 2064640\nv_add_f32 v1, v1, v9\n"
"v_fma_f32 v232, v232, v218, v217\nM 76,180,24\nbuffer_load_dwordx4 v220, s[12:15], s63 offen lds\nG 10\nv_add_f32 v2, v2, v10\nv_fma_f32 v233, v233, v218, v217\n"
"v_mfma_f32_16x16x32_bf16 v[60:63], v[176:179], a[4:7], 0\nR 172,189,40960\nv_mul_f32 v230, v112, v230\nv_add_f32 v3, v3, v11\nv_mul_f32 v231, v113, v231\nM 60,180,20\n"
"buffer_store_dwordx4 v[104:107], v252, s[20:23], s84 offen offset:1024 sc1\nG 10\nv_add_f32 v4, v4, v12\nv_mul_f32 v232, v114, v232\nv_mfma_f32_16x16x32_bf16 v[44:47], v[176:179], a[0:3], 0\nR 174,189,45056\n"
"v_mul_f32 v233, v115, v233\nv_add_f32 v5, v5, v13\nv_exp_f32 v230, v230\nM 44,180,16\nG 7\nv_exp_f32 v231, v231\n"
"v_add_f32 v6, v6, v14\nv_exp_f32 v232, v232\nM 32,160,32\nR 176,190,32768\nv_mov_b32 v248, 0\nv_add_f32 v7, v7, v15\n"
"v_exp_f32 v233, v233\nM 32,164,48\nbuffer_load_dwordx4 v221, s[12:15], s63 offen offset:1024 lds\nv_mov_b32 v249, 0\nv_add_f32 v16, v16, v24\nv_fma_f32 v230, v230, v219, v219\n"
"M 48,160,36\nR 178,190,36864\ns_add_u32 s61, s82, 845824\ns_mov_b64 exec, 0xf\nbuffer_atomic_or_x2 v[248:249], v250, s[20:23], s61 offen sc0 sc1\ns_mov_b64 exec, -1\n"
"v_add_f32 v17, v17, v25\nv_fma_f32 v231, v231, v219, v219\nM 48,164,52\nv_rcp_f32 v230, v230\nG 8\nv_add_f32 v18, v18, v26\n"
"v_fma_f32 v232, v232, v219, v219\nM 64,160,40\nR 180,190,40960\nv_fma_f32 v233, v233, v219, v219\nv_add_f32 v19, v19, v27\nG 8\n"
"v_add_f32 v20, v20, v28\nv_add_f32 v21, v21, v29\nG 6\nv_add_f32 v22, v22, v30\nv_add_f32 v23, v23, v31\nv_rcp_f32 v231, v231\n"
"M 64,164,56\ns_add_u32 m0, s50, 40960\nv_rcp_f32 v232, v232\nv_rcp_f32 v233, v233\nM 80,160,44\nR 182,190,45056\n"
"v_mul_f32 v230, v112, v230\nv_mul_f32 v231, v113, v231\nM 80,164,60\nG 4\nv_mul_f32 v232, v114, v232\nv_mul_f32 v233, v115, v233\n"
"v_cvt_pk_bf16_f32 v112, v230, v231\nM 84,168,44\nR 160,191,32768\nv_cvt_pk_bf16_f32 v113, v232, v233\nv_mul_f32 v234, v116, v116\nM 84,172,60\n"
"s_add_u32 s63, s54, 2294016\nv_mul_f32 v235, v117, v117\nv_mul_f32 v236, v118, v118\nM 68,168,40\nR 162,191,36864\nv_mul_f32 v237, v119, v119\n"
"M 68,172,56\nbuffer_load_dwordx4 v220, s[12:15], s63 offen lds\nM 52,168,36\nR 164,191,40960\nv_fma_f32 v234, v234, v218, v217\nM 52,172,52\n"
"v_fma_f32 v235, v235, v218, v217\nv_fma_f32 v236, v236, v218, v217\nM 36,168,32\nR 166,191,45056\nv_fma_f32 v237, v237, v218, v217\nM 36,172,48\n"
"G 4\nv_mul_f32 v234, v116, v234\nv_mul_f32 v235, v117, v235\nM 40,176,32\nR 168,188,49152\nv_mul_f32 v236, v118, v236\n"
"M 40,180,48\nbuffer_load_dwordx4 v221, s[12:15], s63 offen offset:1024 lds\nM 56,176,36\nR 170,188,53248\ns_add_u32 s63, s54, 2523392\nM 56,180,52\n"
"s_add_u32 m0, s50, 49152\nv_exp_f32 v234, v234\nM 72,176,40\nR 172,184,0\nv_mul_f32 v237, v119, v237\nM 72,180,56\n"
"buffer_load_dwordx4 v220, s[12:15], s63 offen lds\nM 88,176,44\nR 174,184,4096\nv_fma_f32 v234, v234, v219, v219\nM 88,180,60\nG 4\n"
"v_exp_f32 v235, v235\nM 92,160,44\nR 176,189,49152\nv_fma_f32 v235, v235, v219, v219\nM 92,164,60\nbuffer_load_dwordx4 v221, s[12:15], s63 offen offset:1024 lds\n"
"M 76,160,40\nR 178,189,53248\ns_add_u32 s63, s54, 2752768\nM 76,164,56\ns_add_u32 m0, s50, 57344\nv_exp_f32 v236, v236\n"
"M 60,160,36\nR 180,185,0\nv_fma_f32 v236, v236, v219, v219\nM 60,164,52\nbuffer_load_dwordx4 v220, s[12:15], s63 offen lds\nM 44,160,32\n"
"R 182,185,4096\nv_mul_f32 v230, v120, v120\nM 44,164,48\nG 4\nv_exp_f32 v237, v237\nM 32,168,64\n"
"R 160,190,49152\nv_fma_f32 v237, v237, v219, v219\nM 32,172,80\nbuffer_load_dwordx4 v221, s[12:15], s63 offen offset:1024 lds\nM 48,168,68\nR 162,190,53248\n"
"s_add_u32 s63, s54, 2982144\nM 48,172,84\ns_add_u32 m0, s50, 65536\nv_rcp_f32 v234, v234\nM 64,168,72\nR 164,186,0\n"
"v_mul_f32 v234, v116, v234\nM 64,172,88\nbuffer_load_dwordx4 v220, s[12:15], s63 offen lds\nM 80,168,76\nR 166,186,4096\nv_mul_f32 v231, v121, v121\n"
"M 80,172,92\nG 4\nv_rcp_f32 v235, v235\nM 84,176,76\nR 168,191,49152\nv_mul_f32 v235, v117, v235\n"
"M 84,180,92\nv_rcp_f32 v236, v236\nM 68,176,72\nR 170,191,53248\nv_mul_f32 v236, v118, v236\nM 68,180,88\n"
"buffer_load_dwordx4 v221, s[12:15], s63 offen offset:1024 lds\nM 52,176,68\nR 172,187,0\nv_cvt_pk_bf16_f32 v114, v234, v235\nM 52,180,84\ns_add_u32 s64, s68, 37748736\n"
"s_add_u32 s63, s54, 3211520\nv_mul_f32 v232, v122, v122\nM 36,176,64\nR 174,187,4096\ns_add_u32 m0, s50, 73728\nM 36,180,80\n"
"G 4\nv_rcp_f32 v237, v237\nM 40,160,64\nR 176,184,8192\ns_waitcnt vmcnt(9)\ns_barrier\n"
"s_waitcnt vmcnt(8)\nv_cmp_eq_u64 s[86:87], s[46:47], v[248:249]\nM 40,164,80\ns_add_u32 s65, s68, 37863424\nv_mul_f32 v237, v119, v237\nv_cvt_pk_bf16_f32 v115, v236, v237\n"
"M 56,160,68\nR 178,184,12288\ns_and_b32 s62, s86, 0xf\ns_cmp_eq_u32 s62, 0xf\ns_cbranch_scc0 Lq828\nLq829:\n"
"M 56,164,84\nbuffer_load_dwordx4 v220, s[12:15], s63 offen lds\nM 72,160,72\nR 180,184,16384\nv_mul_f32 v233, v123, v123\nM 72,164,88\n"
"buffer_load_dwordx4 a[0:3], v252, s[20:23], s64 offen sc1\nM 88,160,76\nR 182,184,20480\nv_fma_f32 v230, v230, v218, v217\nM 88,164,92\nG 4\n"
"v_fma_f32 v231, v231, v218, v217\nv_fma_f32 v232, v232, v218, v217\nM 92,168,76\nR 160,185,8192\nv_fma_f32 v233, v233, v218, v217\nM 92,172,92\n"
"buffer_load_dwordx4 a[4:7], v252, s[20:23], s65 offen sc1\nM 76,168,72\nR 162,185,12288\nv_mul_f32 v230, v120, v230\nM 76,172,88\ns_add_u32 s66, s69, 37748736\n"
"v_exp_f32 v230, v230\nM 60,168,68\nR 164,185,16384\nv_mul_f32 v231, v121, v231\nM 60,172,84\ns_add_u32 s67, s69, 37863424\n"
"v_exp_f32 v231, v231\nM 44,168,64\nR 166,185,20480\nv_mul_f32 v232, v122, v232\nM 44,172,80\nG 4\n"
"v_exp_f32 v232, v232\nM 32,176,96\nR 168,186,8192\nv_mul_f32 v233, v123, v233\nM 32,180,112\nbuffer_load_dwordx4 a[8:11], v252, s[20:23], s66 offen sc1\n"
"M 48,176,100\nR 170,186,12288\nv_fma_f32 v230, v230, v219, v219\nM 48,180,116\nbuffer_load_dwordx4 a[12:15], v252, s[20:23], s67 offen sc1\nM 64,176,104\n"
"R 172,186,16384\nv_fma_f32 v231, v231, v219, v219\nM 64,180,120\ns_add_u32 s64, s68, 37755904\nv_exp_f32 v233, v233\nM 80,176,108\n"
"R 174,186,20480\nv_fma_f32 v232, v232, v219, v219\nM 80,180,124\nG 4\nv_rcp_f32 v230, v230\nM 84,160,108\n"
"R 176,187,8192\nv_fma_f32 v233, v233, v219, v219\nM 84,164,124\nbuffer_load_dwordx4 a[16:19], v252, s[20:23], s64 offen sc1\nM 68,160,104\nR 178,187,12288\n"
"v_mul_f32 v230, v120, v230\nM 68,164,120\ns_add_u32 s65, s68, 37870592\nv_rcp_f32 v231, v231\nM 52,160,100\nR 180,187,16384\n"
"v_mul_f32 v231, v121, v231\nM 52,164,116\ns_add_u32 s66, s69, 37755904\nv_rcp_f32 v232, v232\nM 36,160,96\nR 182,187,20480\n"
"v_mul_f32 v232, v122, v232\nM 36,164,112\nG 4\nv_rcp_f32 v233, v233\nM 40,168,96\nR 160,184,24576\n"
"v_mul_f32 v233, v123, v233\nM 40,172,112\nbuffer_load_dwordx4 a[20:23], v252, s[20:23], s65 offen sc1\nM 56,168,100\nR 162,184,28672\nv_cvt_pk_bf16_f32 v120, v230, v231\n"
"M 56,172,116\nbuffer_load_dwordx4 a[24:27], v252, s[20:23], s66 offen sc1\nM 72,168,104\nR 164,184,32768\nv_cvt_pk_bf16_f32 v121, v232, v233\nM 72,172,120\n"
"s_add_u32 s67, s69, 37870592\nv_mul_f32 v234, v124, v124\nv_mul_f32 v235, v125, v125\nM 88,168,108\nR 166,184,36864\nv_mul_f32 v236, v126, v126\n"
"M 88,172,124\nG 4\nv_mul_f32 v237, v127, v127\nv_fma_f32 v234, v234, v218, v217\nM 92,176,108\nR 168,185,24576\n"
"v_fma_f32 v235, v235, v218, v217\nM 92,180,124\nbuffer_load_dwordx4 a[28:31], v252, s[20:23], s67 offen sc1\nM 76,176,104\nR 170,185,28672\nv_fma_f32 v236, v236, v218, v217\n"
"M 76,180,120\ns_add_u32 s64, s68, 37749760\nv_fma_f32 v237, v237, v218, v217\nv_mul_f32 v234, v124, v234\nM 60,176,100\nR 172,185,32768\n"
"v_mul_f32 v235, v125, v235\nM 60,180,116\ns_add_u32 s65, s68, 37864448\nv_exp_f32 v234, v234\nM 44,176,96\nR 174,185,36864\n"
"v_mul_f32 v236, v126, v236\nM 44,180,112\nG 4\nv_exp_f32 v235, v235\nM 32,160,128\nR 176,186,24576\n"
"v_mul_f32 v237, v127, v237\nM 32,164,144\nbuffer_load_dwordx4 a[32:35], v252, s[20:23], s64 offen sc1\nbuffer_load_dwordx4 v221, s[12:15], s63 offen offset:1024 lds\nM 48,160,132\nR 178,186,28672\n"
"s_add_u32 m0, s50, 81920\nM 48,164,148\nbuffer_load_dwordx4 a[36:39], v252, s[20:23], s65 offen sc1\nM 64,160,136\nR 180,186,32768\ns_add_u32 s63, s54, 3440896\n"
"M 64,164,152\ns_add_u32 s66, s69, 37749760\nbuffer_load_dwordx4 v220, s[12:15], s63 offen lds\nM 80,160,140\nR 182,186,36864\nv_fma_f32 v234, v234, v219, v219\n"
"M 80,164,156\nG 4\nv_exp_f32 v236, v236\nM 84,168,140\nR 160,187,24576\nv_fma_f32 v235, v235, v219, v219\n"
"M 84,172,156\nbuffer_load_dwordx4 a[40:43], v252, s[20:23], s66 offen sc1\nbuffer_load_dwordx4 v221, s[12:15], s63 offen offset:1024 lds\nLpg28:\ns_cbranch_execz LretO1n\nM 68,168,136\n"
"R 162,187,28672\nv_fma_f32 v236, v236, v219, v219\nM 68,172,152\ns_add_u32 s67, s69, 37864448\nv_exp_f32 v237, v237\nM 52,168,132\n"
"R 164,187,32768\ns_waitcnt vmcnt(15)\ns_barrier\nM 52,172,148\nbuffer_load_dwordx4 a[44:47], v252, s[20:23], s67 offen sc1\ns_movk_i32 s63, 2048\n"
"v_mad_u32_u24 v220, v228, s63, v229\nv_add_u32 v221, 916480, v220\ns_add_u32 m0, s50, 90112\ns_add_u32 s63, s56, 0\nbuffer_load_dwordx4 v220, s[16:19], s63 offen lds\nM 36,168,128\n"
"R 166,187,36864\nv_fma_f32 v237, v237, v219, v219\nM 36,172,144\nG 4\nv_rcp_f32 v234, v234\nM 40,176,128\n"
"R 168,184,40960\nv_mul_f32 v234, v124, v234\nM 40,180,144\ns_add_u32 s64, s68, 37756928\nbuffer_load_dwordx4 v221, s[16:19], s63 offen offset:1024 lds\nM 56,176,132\n"
"R 170,184,45056\ns_add_u32 m0, s50, 98304\nM 56,180,148\nbuffer_load_dwordx4 a[48:51], v252, s[20:23], s64 offen sc1\nM 72,176,136\nR 172,184,49152\n"
"s_add_u32 s63, s56, 524288\nM 72,180,152\ns_add_u32 s65, s68, 37871616\nbuffer_load_dwordx4 v220, s[16:19], s63 offen lds\nM 88,176,140\nR 174,184,53248\n"
"v_rcp_f32 v235, v235\nM 88,180,156\nG 4\nv_rcp_f32 v236, v236\nM 92,160,140\nR 176,185,40960\n"
"v_mul_f32 v235, v125, v235\nM 92,164,156\nbuffer_load_dwordx4 a[52:55], v252, s[20:23], s65 offen sc1\nbuffer_load_dwordx4 v221, s[16:19], s63 offen offset:1024 lds\nM 76,160,136\nR 178,185,45056\n"
"s_add_u32 m0, s50, 106496\nM 76,164,152\ns_add_u32 s66, s69, 37756928\nv_rcp_f32 v237, v237\nM 60,160,132\nR 180,185,49152\n"
"s_add_u32 s63, s56, 65536\nM 60,164,148\nbuffer_load_dwordx4 a[56:59], v252, s[20:23], s66 offen sc1\nM 44,160,128\nR 182,185,53248\nv_mul_f32 v236, v126, v236\n"
"M 44,164,144\nbuffer_load_dwordx4 v220, s[16:19], s63 offen lds\nG 4\nM 32,168,160\nR 160,186,40960\nv_mul_f32 v237, v127, v237\n"
"M 32,172,176\ns_add_u32 s67, s69, 37871616\nv_cvt_pk_bf16_f32 v122, v234, v235\nv_cvt_pk_bf16_f32 v123, v236, v237\nM 48,168,164\nR 162,186,45056\n"
"M 48,172,180\nbuffer_load_dwordx4 a[60:63], v252, s[20:23], s67 offen sc1\nbuffer_load_dwordx4 v221, s[16:19], s63 offen offset:1024 lds\nM 64,168,168\nR 164,186,49152\ns_add_u32 m0, s50, 114688\n"
"s_add_u32 s63, s56, 589824\nM 64,172,184\nbuffer_load_dwordx4 v220, s[16:19], s63 offen lds\nM 80,168,172\nR 166,186,53248\nbuffer_store_dwordx4 v[112:115], v252, s[20:23], s85 offen offset:0 sc1\n"
"M 80,172,188\nbuffer_store_dwordx4 v[120:123], v252, s[20:23], s85 offen offset:1024 sc1\nG 4\nM 84,176,172\nR 168,187,40960\nM 84,180,188\n"
"buffer_load_dwordx4 v221, s[16:19], s63 offen offset:1024 lds\nM 68,176,168\nR 170,187,45056\ns_add_u32 s63, s57, 0\nv_mul_f32 v230, v0, v0\nM 68,180,184\n"
"s_add_u32 m0, s50, 0\ns_add_u32 s84, s73, 37754880\ns_add_u32 s85, s73, 37869568\nv_mul_f32 v231, v1, v1\nM 52,176,164\nR 172,187,49152\n"
"v_mul_f32 v232, v2, v2\nv_mul_f32 v233, v3, v3\nM 52,180,180\nbuffer_load_dwordx4 v220, s[16:19], s63 offen lds\nv_fma_f32 v230, v230, v215, v214\nM 36,176,160\n"
"R 174,187,53248\nv_fma_f32 v231, v231, v215, v214\nv_fma_f32 v232, v232, v215, v214\nM 36,180,176\nG 4\nv_fma_f32 v233, v233, v215, v214\n"
"v_mul_f32 v230, v0, v230\nv_mul_f32 v231, v1, v231\nM 40,160,160\nR 176,184,57344\nv_mul_f32 v232, v2, v232\nv_mul_f32 v233, v3, v233\n"
"M 40,164,176\nv_exp_f32 v230, v230\nv_exp_f32 v231, v231\nM 56,160,164\nR 178,184,61440\nv_fma_f32 v230, v230, v216, v216\n"
"v_exp_f32 v232, v232\nM 56,164,180\nv_exp_f32 v233, v233\nv_fma_f32 v231, v231, v216, v216\nM 72,160,168\nR 180,188,0\n"
"v_fma_f32 v232, v232, v216, v216\nv_fma_f32 v233, v233, v216, v216\nM 72,164,184\nv_rcp_f32 v230, v230\nv_rcp_f32 v231, v231\nM 88,160,172\n"
"R 182,188,4096\nv_mul_f32 v230, v0, v230\nv_rcp_f32 v232, v232\nM 88,164,188\nG 4\nv_rcp_f32 v233, v233\n"
"v_mul_f32 v231, v1, v231\nM 92,168,172\nR 160,185,57344\nv_mul_f32 v232, v2, v232\nv_mul_f32 v233, v3, v233\nM 92,172,188\n"
"v_cvt_pk_bf16_f32 v0, v230, v231\nv_cvt_pk_bf16_f32 v1, v232, v233\nv_mul_f32 v234, v4, v4\nM 76,168,168\nR 162,185,61440\nv_mul_f32 v235, v5, v5\n"
"v_mul_f32 v236, v6, v6\nM 76,172,184\nv_mul_f32 v237, v7, v7\nv_fma_f32 v234, v234, v215, v214\nv_fma_f32 v235, v235, v215, v214\nM 60,168,164\n"
"R 164,189,0\nv_fma_f32 v236, v236, v215, v214\nv_fma_f32 v237, v237, v215, v214\nM 60,172,180\nv_mul_f32 v234, v4, v234\nv_mul_f32 v235, v5, v235\n"
"v_mul_f32 v236, v6, v236\nM 44,168,160\nR 166,189,4096\nv_mul_f32 v237, v7, v237\nv_exp_f32 v234, v234\nM 44,172,176\n"
"G 4\nv_exp_f32 v235, v235\nv_exp_f32 v236, v236\nM 32,176,192\nR 168,186,57344\nv_fma_f32 v234, v234, v216, v216\n"
"v_exp_f32 v237, v237\nM 32,180,208\nv_rcp_f32 v234, v234\nv_fma_f32 v235, v235, v216, v216\nM 48,176,196\nR 170,186,61440\n"
"v_fma_f32 v236, v236, v216, v216\nv_fma_f32 v237, v237, v216, v216\nM 48,180,212\nv_rcp_f32 v235, v235\nv_rcp_f32 v236, v236\nM 64,176,200\n"
"R 172,190,0\nv_mul_f32 v234, v4, v234\nv_rcp_f32 v237, v237\nM 64,180,216\nv_mul_f32 v235, v5, v235\nv_mul_f32 v236, v6, v236\n"
"v_mul_f32 v237, v7, v237\nM 80,176,204\nR 174,190,4096\nv_cvt_pk_bf16_f32 v2, v234, v235\nv_cvt_pk_bf16_f32 v3, v236, v237\nM 80,180,220\n"
"G 4\nv_mul_f32 v230, v16, v16\nv_mul_f32 v231, v17, v17\nv_mul_f32 v232, v18, v18\nM 84,160,204\nR 176,187,57344\n"
"s_waitcnt vmcnt(9)\ns_barrier\nv_mul_f32 v233, v19, v19\nM 84,164,220\nv_fma_f32 v230, v230, v218, v217\nv_fma_f32 v231, v231, v218, v217\n"
"v_fma_f32 v232, v232, v218, v217\nM 68,160,200\nR 178,187,61440\nv_fma_f32 v233, v233, v218, v217\nv_mul_f32 v230, v16, v230\nM 68,164,216\n"
"v_exp_f32 v230, v230\nv_mul_f32 v231, v17, v231\nM 52,160,196\nR 180,191,0\nv_mul_f32 v232, v18, v232\nv_mul_f32 v233, v19, v233\n"
"M 52,164,212\nv_exp_f32 v231, v231\nv_exp_f32 v232, v232\nM 36,160,192\nR 182,191,4096\nv_fma_f32 v230, v230, v219, v219\n"
"v_exp_f32 v233, v233\nM 36,164,208\nG 4\nv_rcp_f32 v230, v230\nv_fma_f32 v231, v231, v219, v219\nM 40,168,192\n"
"R 160,188,8192\nv_fma_f32 v232, v232, v219, v219\nv_fma_f32 v233, v233, v219, v219\nM 40,172,208\nv_rcp_f32 v231, v231\nv_rcp_f32 v232, v232\n"
"M 56,168,196\nR 162,188,12288\nv_mul_f32 v230, v16, v230\nv_rcp_f32 v233, v233\nM 56,172,212\nv_mul_f32 v231, v17, v231\n"
"v_mul_f32 v232, v18, v232\nv_mul_f32 v233, v19, v233\nM 72,168,200\nR 164,188,16384\nv_cvt_pk_bf16_f32 v16, v230, v231\nv_cvt_pk_bf16_f32 v17, v232, v233\n"
"M 72,172,216\nv_mul_f32 v234, v20, v20\nv_mul_f32 v235, v21, v21\nv_mul_f32 v236, v22, v22\nM 88,168,204\nR 166,188,20480\n"
"v_mov_b32 v226, s46\nv_mul_f32 v237, v23, v23\nM 88,172,220\nG 4\nv_mov_b32 v227, s47\nv_fma_f32 v234, v234, v218, v217\n"
"v_fma_f32 v235, v235, v218, v217\nM 92,176,204\nR 168,189,8192\ns_add_u32 s61, s81, 845832\ns_lshl_b32 s62, s44, 5\ns_add_u32 s61, s61, s62\n"
"s_waitcnt vmcnt(0)\ns_mov_b64 exec, 1\nbuffer_atomic_swap_x2 v[226:227], off, s[20:23], s61 sc1\ns_mov_b64 exec, -1\nv_fma_f32 v236, v236, v218, v217\nM 92,180,220\n"
"buffer_load_dwordx4 v221, s[16:19], s63 offen offset:1024 lds\nv_fma_f32 v237, v237, v218, v217\nM 76,176,200\nR 170,189,12288\ns_add_u32 m0, s50, 8192\ns_add_u32 s63, s57, 524288\n"
"v_mul_f32 v234, v20, v234\nM 76,180,216\nbuffer_load_dwordx4 v220, s[16:19], s63 offen lds\nv_mul_f32 v235, v21, v235\nM 60,176,196\nR 172,189,16384\n"
"v_mul_f32 v236, v22, v236\nv_mul_f32 v237, v23, v237\nM 60,180,212\nv_exp_f32 v234, v234\nv_exp_f32 v235, v235\nM 44,176,192\n"
"R 174,189,20480\ns_mov_b32 s60, 158208\nv_bfe_u32 v246, v252, 4, 4\nv_lshl_add_u32 v246, v246, 3, s60\nv_exp_f32 v236, v236\nM 44,180,208\n"
"ds_read_b64 v[238:239], v246 offset:0\nG 5\nv_exp_f32 v237, v237\nM 32,160,224\nR 176,190,8192\nds_read_b64 v[240:241], v246 offset:128\n"
"v_fma_f32 v234, v234, v219, v219\nM 32,164,240\nds_read_b64 v[242:243], v246 offset:256\nbuffer_load_dwordx4 v221, s[16:19], s63 offen offset:1024 lds\nv_fma_f32 v235, v235, v219, v219\nM 48,160,228\n"
"R 178,190,12288\nds_read_b64 v[244:245], v246 offset:384\nv_fma_f32 v236, v236, v219, v219\nM 48,164,244\nds_read_b64 v[246:247], v246 offset:0\ns_add_u32 m0, s50, 16384\n"
"v_fma_f32 v237, v237, v219, v219\nM 64,160,232\nR 180,190,16384\ns_add_u32 s63, s57, 65536\nv_rcp_f32 v234, v234\nM 64,164,248\n"
"buffer_load_dwordx4 v220, s[16:19], s63 offen lds\nv_rcp_f32 v235, v235\nM 80,160,236\nR 182,190,20480\nv_mul_f32 v234, v20, v234\nv_rcp_f32 v236, v236\n"
"M 80,164,252\nG 9\nv_rcp_f32 v237, v237\nv_mul_f32 v235, v21, v235\nM 84,168,236\nR 160,191,8192\n"
"v_mul_f32 v236, v22, v236\nM 84,172,252\nbuffer_load_dwordx4 v221, s[16:19], s63 offen offset:1024 lds\nM 68,168,232\nR 162,191,12288\ns_add_u32 m0, s50, 24576\n"
"s_add_u32 s63, s57, 589824\nM 68,172,248\nbuffer_load_dwordx4 v220, s[16:19], s63 offen lds\nM 52,168,228\nR 164,191,16384\nG 9\n"
"v_mfma_f32_16x16x32_bf16 v[32:35], v[238:241], v[208:211], v[32:35]\nv_mul_f32 v237, v23, v237\nM 52,172,244\nbuffer_store_dwordx4 v[0:3], v252, s[20:23], s84 offen offset:0 sc1\nM 36,168,224\nR 166,191,20480\n"
"G 9\nv_mfma_f32_16x16x32_bf16 v[36:39], v[240:243], v[208:211], v[36:39]\nv_cvt_pk_bf16_f32 v18, v234, v235\nM 36,172,240\nG 4\nv_cvt_pk_bf16_f32 v19, v236, v237\n"
"M 40,176,224\nR 168,188,24576\nv_mfma_f32_16x16x32_bf16 v[40:43], v[242:245], v[208:211], v[40:43]\nbuffer_load_dwordx4 v221, s[16:19], s63 offen offset:1024 lds\nbuffer_store_dwordx4 v[16:19], v252, s[20:23], s85 offen offset:0 sc1\nM 40,180,240\n"
"s_add_u32 m0, s50, 32768\ns_add_u32 s63, s56, 131072\nbuffer_load_dwordx4 v220, s[16:19], s63 offen lds\nM 56,176,228\nR 170,188,28672\nv_mfma_f32_16x16x32_bf16 v[44:47], v[244:247], v[208:211], v[44:47]\n"
"v_mov_b32 v248, 0\nM 56,180,244\nv_mov_b32 v249, 0\nM 72,176,232\nR 172,188,32768\nv_mfma_f32_16x16x32_bf16 v[48:51], v[244:247], v[210:213], v[48:51]\n"
"s_add_u32 s61, s82, 845832\ns_mov_b64 exec, 0xf\nbuffer_atomic_or_x2 v[248:249], v250, s[20:23], s61 offen sc0 sc1\ns_mov_b64 exec, -1\nM 72,180,248\nbuffer_load_dwordx4 v221, s[16:19], s63 offen offset:1024 lds\n"
"M 88,176,236\nR 174,188,36864\nv_mfma_f32_16x16x32_bf16 v[52:55], v[238:241], v[210:213], v[52:55]\ns_add_u32 m0, s50, 40960\nM 88,180,252\nG 4\n"
"M 92,160,236\nR 176,189,24576\nv_mfma_f32_16x16x32_bf16 v[56:59], v[240:243], v[210:213], v[56:59]\ns_add_u32 s63, s56, 655360\nM 92,164,252\nbuffer_load_dwordx4 v220, s[16:19], s63 offen lds\n"
"M 76,160,232\nR 178,189,28672\nv_mfma_f32_16x16x32_bf16 v[60:63], v[242:245], v[210:213], v[60:63]\nM 76,164,248\nM 60,160,228\nR 180,189,32768\n"
"M 60,164,244\nM 44,160,224\nR 182,189,36864\nM 44,164,240\ns_barrier\nG 4\n"
"v_mfma_f32_16x16x32_bf16 v[96:99], v[168:171], a[0:3], 0\nR 160,190,24576\nM 96,172,16\ns_mov_b32 m0, s51\nv_mfma_f32_16x16x32_bf16 v[112:115], v[168:171], a[4:7], 0\nR 162,190,28672\n"
"M 112,172,20\nds_write_addtid_b32 v64 offset:0\nv_mfma_f32_16x16x32_bf16 v[128:131], v[168:171], a[8:11], 0\nR 164,190,32768\nM 128,172,24\nds_write_addtid_b32 v65 offset:256\n"
"v_mfma_f32_16x16x32_bf16 v[144:147], v[168:171], a[12:15], 0\nR 166,190,36864\nds_write_addtid_b32 v66 offset:512\nM 144,172,28\nds_write_addtid_b32 v67 offset:768\nds_write_addtid_b32 v68 offset:1024\n"
"G 9\nv_mfma_f32_16x16x32_bf16 v[148:151], v[176:179], a[12:15], 0\nR 168,191,24576\nds_write_addtid_b32 v69 offset:1280\nM 148,180,28\nds_write_addtid_b32 v70 offset:1536\n"
"ds_write_addtid_b32 v71 offset:1792\nv_mfma_f32_16x16x32_bf16 v[132:135], v[176:179], a[8:11], 0\nR 170,191,28672\nds_write_addtid_b32 v72 offset:2048\nM 132,180,24\nds_write_addtid_b32 v73 offset:2304\n"
"ds_write_addtid_b32 v74 offset:2560\nv_mfma_f32_16x16x32_bf16 v[116:119], v[176:179], a[4:7], 0\nR 172,191,32768\nds_write_addtid_b32 v75 offset:2816\nM 116,180,20\nds_write_addtid_b32 v76 offset:3072\n"
"ds_write_addtid_b32 v77 offset:3328\nv_mfma_f32_16x16x32_bf16 v[100:103], v[176:179], a[0:3], 0\nR 174,191,36864\nds_write_addtid_b32 v78 offset:3584\nM 100,180,16\nds_write_addtid_b32 v79 offset:3840\n"
"ds_write_addtid_b32 v80 offset:4096\nG 15\nv_mfma_f32_16x16x32_bf16 v[104:107], v[160:163], a[0:3], 0\nR 176,188,40960\nds_write_addtid_b32 v81 offset:4352\nM 104,164,16\n"
"ds_write_addtid_b32 v82 offset:4608\nds_write_addtid_b32 v83 offset:4864\nv_mfma_f32_16x16x32_bf16 v[120:123], v[160:163], a[4:7], 0\nR 178,188,45056\nds_write_addtid_b32 v84 offset:5120\nM 120,164,20\n"
"ds_write_addtid_b32 v85 offset:5376\nds_write_addtid_b32 v86 offset:5632\nv_mfma_f32_16x16x32_bf16 v[136:139], v[160:163], a[8:11], 0\nR 180,188,49152\nds_write_addtid_b32 v87 offset:5888\nM 136,164,24\n"
"ds_write_addtid_b32 v88 offset:6144\nds_write_addtid_b32 v89 offset:6400\nv_mfma_f32_16x16x32_bf16 v[152:155], v[160:163], a[12:15], 0\nR 182,188,53248\nds_write_addtid_b32 v90 offset:6656\nM 152,164,28\n"
"ds_write_addtid_b32 v91 offset:6912\nds_write_addtid_b32 v92 offset:7168\nG 15\nv_mfma_f32_16x16x32_bf16 v[156:159], v[168:171], a[12:15], 0\nR 160,189,40960\nds_write_addtid_b32 v93 offset:7424\n"
"M 156,172,28\nds_write_addtid_b32 v94 offset:7680\nds_write_addtid_b32 v95 offset:7936\nv_mfma_f32_16x16x32_bf16 v[140:143], v[168:171], a[8:11], 0\nR 162,189,45056\nM 140,172,24\n"
"v_mfma_f32_16x16x32_bf16 v[124:127], v[168:171], a[4:7], 0\nR 164,189,49152\ns_waitcnt vmcnt(5) lgkmcnt(2)\ns_barrier\nM 124,172,20\nds_read2st64_b32 v[64:65], v225 offset0:0 offset1:1\n"
"ds_read2st64_b32 v[66:67], v225 offset0:2 offset1:3\nv_mfma_f32_16x16x32_bf16 v[108:111], v[168:171], a[0:3], 0\nR 166,189,53248\nds_read2st64_b32 v[68:69], v225 offset0:4 offset1:5\nM 108,172,16\nds_read2st64_b32 v[70:71], v225 offset0:6 offset1:7\n"
"ds_read2st64_b32 v[72:73], v225 offset0:8 offset1:9\nM 96,176,32\nR 168,190,40960\nds_read2st64_b32 v[74:75], v225 offset0:10 offset1:11\nM 96,180,48\ns_add_u32 s64, s68, 37750784\n"
"ds_read2st64_b32 v[76:77], v225 offset0:12 offset1:13\nv_mov_b32 v226, s46\nM 112,176,36\nR 170,190,45056\nds_read2st64_b32 v[78:79], v225 offset0:14 offset1:15\nv_mov_b32 v227, s47\n"
"s_waitcnt vmcnt(2)\nv_cmp_eq_u64 s[86:87], s[46:47], v[248:249]\nM 112,180,52\ns_add_u32 s61, s81, 845848\ns_lshl_b32 s62, s44, 5\ns_add_u32 s61, s61, s62\n"
"s_waitcnt vmcnt(0)\ns_mov_b64 exec, 1\nbuffer_atomic_swap_x2 v[226:227], off, s[20:23], s61 sc1\ns_mov_b64 exec, -1\nds_read2st64_b32 v[80:81], v225 offset0:16 offset1:17\nM 128,176,40\n"
"R 172,190,49152\ns_and_b32 s62, s86, 0xf\ns_cmp_eq_u32 s62, 0xf\ns_cbranch_scc0 Lq863\nLq864:\nds_read2st64_b32 v[82:83], v225 offset0:18 offset1:19\n"
"M 128,180,56\ns_add_u32 s65, s68, 37865472\nds_read2st64_b32 v[84:85], v225 offset0:20 offset1:21\nds_read2st64_b32 v[86:87], v225 offset0:22 offset1:23\nM 144,176,44\nR 174,190,53248\n"
"s_add_u32 m0, s50, 40960\nds_read2st64_b32 v[88:89], v225 offset0:24 offset1:25\nM 144,180,60\nds_read2st64_b32 v[90:91], v225 offset0:26 offset1:27\nds_read2st64_b32 v[92:93], v225 offset0:28 offset1:29\nG 15\n"
"M 148,160,44\nR 176,191,40960\nbuffer_load_dwordx4 v221, s[16:19], s63 offen offset:1024 lds\nds_read2st64_b32 v[94:95], v225 offset0:30 offset1:31\nM 148,164,60\nbuffer_load_dwordx4 a[64:67], v252, s[20:23], s64 offen sc1\n"
"v_add_f32 v32, v32, v64\nM 132,160,40\nR 178,191,45056\ns_add_u32 m0, s50, 49152\nv_add_f32 v33, v33, v65\nM 132,164,56\n"
"s_add_u32 s66, s69, 37750784\ns_add_u32 s63, s56, 196608\nv_add_f32 v34, v34, v66\nv_add_f32 v35, v35, v67\nM 116,160,36\nR 180,191,49152\n"
"buffer_load_dwordx4 v220, s[16:19], s63 offen lds\nv_add_f32 v36, v36, v68\nM 116,164,52\nbuffer_load_dwordx4 a[68:71], v252, s[20:23], s65 offen sc1\nv_add_f32 v37, v37, v69\nM 100,160,32\n"
"R 182,191,53248\nv_add_f32 v38, v38, v70\nv_add_f32 v39, v39, v71\nM 100,164,48\nG 8\nv_add_f32 v40, v40, v72\n"
"v_add_f32 v41, v41, v73\nv_add_f32 v42, v42, v74\nM 104,168,32\nR 160,184,0\nbuffer_load_dwordx4 v221, s[16:19], s63 offen offset:1024 lds\nv_add_f32 v43, v43, v75\n"
"M 104,172,48\ns_add_u32 s67, s69, 37865472\nLpg29:\ns_cbranch_execz LretO1n\nv_add_f32 v44, v44, v76\nv_add_f32 v45, v45, v77\n"
"v_add_f32 v46, v46, v78\nM 120,168,36\nR 162,184,4096\nv_add_f32 v47, v47, v79\nv_add_f32 v48, v48, v80\nM 120,172,52\n"
"buffer_load_dwordx4 a[72:75], v252, s[20:23], s66 offen sc1\nv_add_f32 v49, v49, v81\nM 136,168,40\nR 164,184,8192\nv_add_f32 v50, v50, v82\nv_add_f32 v51, v51, v83\n"
"M 136,172,56\nv_add_f32 v52, v52, v84\nv_add_f32 v53, v53, v85\nM 152,168,44\nR 166,184,12288\nv_add_f32 v54, v54, v86\n"
"M 152,172,60\nG 4\nv_add_f32 v55, v55, v87\nv_add_f32 v56, v56, v88\nM 156,176,44\nR 168,185,0\n"
"v_add_f32 v57, v57, v89\nM 156,180,60\nbuffer_load_dwordx4 a[76:79], v252, s[20:23], s67 offen sc1\nM 140,176,40\nR 170,185,4096\nv_add_f32 v58, v58, v90\n"
"M 140,180,56\ns_add_u32 s64, s68, 37757952\nv_add_f32 v59, v59, v91\nv_add_f32 v60, v60, v92\nM 124,176,36\nR 172,185,8192\n"
"v_add_f32 v61, v61, v93\nM 124,180,52\ns_add_u32 s65, s68, 37872640\nv_add_f32 v62, v62, v94\nv_add_f32 v63, v63, v95\nM 108,176,32\n"
"R 174,185,12288\nv_mul_f32 v230, v32, v32\nv_mul_f32 v231, v33, v33\nM 108,180,48\ns_add_u32 s84, s73, 37752832\ns_add_u32 s85, s73, 37867520\n"
"G 4\nv_mul_f32 v232, v34, v34\nv_mfma_f32_16x16x32_bf16 v[0:3], v[160:163], a[0:3], 0\nR 176,186,0\ns_lshr_b32 s60, s45, 3\ns_lshl2_add_u32 s60, s60, s40\n"
"s_lshl_b32 s60, s60, 8\nv_lshl_add_u32 v224, v252, 3, s60\nv_add_u32 v224, LqF-32768-LqPC, v224\nv_min_u32 v224, LqE-LqPC-4, v224\ns_mov_b64 exec, 3\nglobal_load_dword v224, v224, s[98:99]\n"
"s_mov_b64 exec, -1\nv_mul_f32 v233, v35, v35\nM 0,164,16\nbuffer_load_dwordx4 a[80:83], v252, s[20:23], s64 offen sc1\nv_fma_f32 v230, v230, v215, v214\nv_mfma_f32_16x16x32_bf16 v[16:19], v[160:163], a[4:7], 0\n"
"R 178,186,4096\nv_fma_f32 v231, v231, v215, v214\nv_fma_f32 v232, v232, v215, v214\nM 16,164,20\ns_add_u32 s66, s69, 37757952\nv_fma_f32 v233, v233, v215, v214\n"
"v_mul_f32 v230, v32, v230\nv_mul_f32 v231, v33, v231\nv_mfma_f32_16x16x32_bf16 a[224:227], v[160:163], a[8:11], 0\nR 180,186,8192\nv_mul_f32 v232, v34, v232\nv_mul_f32 v233, v35, v233\n"
"v_mfma_f32_16x16x32_bf16 a[224:227], v[164:167], a[24:27], a[224:227]\nbuffer_load_dwordx4 a[84:87], v252, s[20:23], s65 offen sc1\nv_exp_f32 v230, v230\nv_mfma_f32_16x16x32_bf16 a[240:243], v[160:163], a[12:15], 0\nR 182,186,12288\nv_fma_f32 v230, v230, v216, v216\n"
"v_exp_f32 v231, v231\nv_mfma_f32_16x16x32_bf16 a[240:243], v[164:167], a[28:31], a[240:243]\nG 4\nv_exp_f32 v232, v232\nv_exp_f32 v233, v233\nv_mfma_f32_16x16x32_bf16 a[244:247], v[168:171], a[12:15], 0\n"
"R 160,187,0\ns_add_u32 m0, s50, 57344\nv_fma_f32 v231, v231, v216, v216\nv_mfma_f32_16x16x32_bf16 a[244:247], v[172:175], a[28:31], a[244:247]\ns_add_u32 s67, s69, 37872640\nv_rcp_f32 v230, v230\n"
"v_fma_f32 v232, v232, v216, v216\nv_mfma_f32_16x16x32_bf16 a[228:231], v[168:171], a[8:11], 0\nR 162,187,4096\ns_add_u32 s63, s56, 720896\nv_fma_f32 v233, v233, v216, v216\nv_mfma_f32_16x16x32_bf16 a[228:231], v[172:175], a[24:27], a[228:231]\n"
"buffer_load_dwordx4 a[88:91], v252, s[20:23], s66 offen sc1\nv_rcp_f32 v231, v231\nv_mfma_f32_16x16x32_bf16 v[20:23], v[168:171], a[4:7], 0\nR 164,187,8192\nv_mul_f32 v230, v32, v230\nv_rcp_f32 v232, v232\n"
"M 20,172,20\nbuffer_load_dwordx4 v220, s[16:19], s63 offen lds\nv_rcp_f32 v233, v233\nv_mfma_f32_16x16x32_bf16 v[4:7], v[168:171], a[0:3], 0\nR 166,187,12288\nv_mul_f32 v231, v33, v231\n"
"v_mul_f32 v232, v34, v232\nM 4,172,16\nG 4\nv_mul_f32 v233, v35, v233\nv_cvt_pk_bf16_f32 v32, v230, v231\nv_cvt_pk_bf16_f32 v33, v232, v233\n"
"v_mfma_f32_16x16x32_bf16 v[8:11], v[176:179], a[0:3], 0\nR 168,184,16384\nv_mul_f32 v234, v36, v36\nv_mul_f32 v235, v37, v37\nM 8,180,16\nbuffer_load_dwordx4 a[92:95], v252, s[20:23], s67 offen sc1\n"
"buffer_load_dwordx4 v221, s[16:19], s63 offen offset:1024 lds\nv_mul_f32 v236, v38, v38\nv_mfma_f32_16x16x32_bf16 v[24:27], v[176:179], a[4:7], 0\nR 170,184,20480\ns_add_u32 m0, s50, 65536\nv_mul_f32 v237, v39, v39\n"
"M 24,180,20\ns_add_u32 s64, s68, 37751808\nv_fma_f32 v234, v234, v215, v214\nv_fma_f32 v235, v235, v215, v214\nv_fma_f32 v236, v236, v215, v214\nv_mfma_f32_16x16x32_bf16 a[232:235], v[176:179], a[8:11], 0\n"
"R 172,184,24576\ns_add_u32 s63, s57, 131072\nv_fma_f32 v237, v237, v215, v214\nv_mfma_f32_16x16x32_bf16 a[232:235], v[180:183], a[24:27], a[232:235]\ns_add_u32 s65, s68, 37866496\nbuffer_load_dwordx4 v220, s[16:19], s63 offen lds\n"
"v_mul_f32 v234, v36, v234\nv_mfma_f32_16x16x32_bf16 a[248:251], v[176:179], a[12:15], 0\nR 174,184,28672\nv_mul_f32 v235, v37, v235\nv_mul_f32 v236, v38, v236\nv_mfma_f32_16x16x32_bf16 a[248:251], v[180:183], a[28:31], a[248:251]\n"
"G 4\nv_exp_f32 v234, v234\nv_mul_f32 v237, v39, v237\nv_mfma_f32_16x16x32_bf16 a[252:255], v[160:163], a[12:15], 0\nR 176,185,16384\nv_fma_f32 v234, v234, v216, v216\n"
"v_exp_f32 v235, v235\nv_mfma_f32_16x16x32_bf16 a[252:255], v[164:167], a[28:31], a[252:255]\nbuffer_load_dwordx4 a[96:99], v252, s[20:23], s64 offen sc1\nbuffer_load_dwordx4 v221, s[16:19], s63 offen offset:1024 lds\nv_exp_f32 v236, v236\nv_mfma_f32_16x16x32_bf16 a[236:239], v[160:163], a[8:11], 0\n"
"R 178,185,20480\ns_add_u32 m0, s50, 73728\nv_exp_f32 v237, v237\nv_mfma_f32_16x16x32_bf16 a[236:239], v[164:167], a[24:27], a[236:239]\ns_add_u32 s66, s69, 37751808\nv_rcp_f32 v234, v234\n"
"v_fma_f32 v235, v235, v216, v216\nv_mfma_f32_16x16x32_bf16 v[28:31], v[160:163], a[4:7], 0\nR 180,185,24576\ns_add_u32 s63, s57, 655360\nv_fma_f32 v236, v236, v216, v216\nM 28,164,20\n"
"buffer_load_dwordx4 a[100:103], v252, s[20:23], s65 offen sc1\nbuffer_load_dwordx4 v220, s[16:19], s63 offen lds\nv_fma_f32 v237, v237, v216, v216\nv_mfma_f32_16x16x32_bf16 v[12:15], v[160:163], a[0:3], 0\nR 182,185,28672\nv_mul_f32 v234, v36, v234\n"
"v_rcp_f32 v235, v235\nM 12,164,16\ns_waitcnt vmcnt(14)\ns_barrier\nG 4\nv_rcp_f32 v236, v236\n"
"M 0,168,32\nR 160,186,16384\nv_mul_f32 v235, v37, v235\nv_rcp_f32 v237, v237\nM 0,172,48\ns_add_u32 s67, s69, 37866496\n"
"buffer_load_dwordx4 v221, s[16:19], s63 offen offset:1024 lds\nv_mul_f32 v236, v38, v236\nM 16,168,36\nR 162,186,20480\nv_mul_f32 v237, v39, v237\nv_cvt_pk_bf16_f32 v34, v234, v235\n"
"M 16,172,52\nbuffer_load_dwordx4 a[104:107], v252, s[20:23], s66 offen sc1\nv_cvt_pk_bf16_f32 v35, v236, v237\nv_mfma_f32_16x16x32_bf16 a[224:227], v[168:171], a[40:43], a[224:227]\nR 164,186,24576\nv_mul_f32 v230, v40, v40\n"
"v_mul_f32 v231, v41, v41\nv_mfma_f32_16x16x32_bf16 a[224:227], v[172:175], a[56:59], a[224:227]\nv_mul_f32 v232, v42, v42\nv_mul_f32 v233, v43, v43\nv_fma_f32 v230, v230, v215, v214\nv_mfma_f32_16x16x32_bf16 a[240:243], v[168:171], a[44:47], a[240:243]\n"
"R 166,186,28672\nv_fma_f32 v231, v231, v215, v214\nv_fma_f32 v232, v232, v215, v214\nv_mfma_f32_16x16x32_bf16 a[240:243], v[172:175], a[60:63], a[240:243]\nG 4\nv_fma_f32 v233, v233, v215, v214\n"
"v_mul_f32 v230, v40, v230\nv_mul_f32 v231, v41, v231\nv_mfma_f32_16x16x32_bf16 a[244:247], v[176:179], a[44:47], a[244:247]\nR 168,187,16384\nv_mul_f32 v232, v42, v232\nv_mul_f32 v233, v43, v233\n"
"v_mfma_f32_16x16x32_bf16 a[244:247], v[180:183], a[60:63], a[244:247]\nbuffer_load_dwordx4 a[108:111], v252, s[20:23], s67 offen sc1\nv_exp_f32 v230, v230\nv_mfma_f32_16x16x32_bf16 a[228:231], v[176:179], a[40:43], a[228:231]\nR 170,187,20480\nv_fma_f32 v230, v230, v216, v216\n"
"v_exp_f32 v231, v231\nv_mfma_f32_16x16x32_bf16 a[228:231], v[180:183], a[56:59], a[228:231]\ns_add_u32 s64, s68, 37758976\nv_exp_f32 v232, v232\nv_exp_f32 v233, v233\nM 20,176,36\n"
"R 172,187,24576\nv_fma_f32 v231, v231, v216, v216\nv_fma_f32 v232, v232, v216, v216\nM 20,180,52\ns_add_u32 s65, s68, 37873664\nv_rcp_f32 v230, v230\n"
"v_fma_f32 v233, v233, v216, v216\nM 4,176,32\nR 174,187,28672\ns_add_u32 m0, s50, 81920\nv_rcp_f32 v231, v231\nM 4,180,48\n"
"G 4\nv_rcp_f32 v232, v232\nv_rcp_f32 v233, v233\nM 8,160,32\nR 176,184,32768\ns_add_u32 s63, s57, 196608\n"
"v_mul_f32 v230, v40, v230\nM 8,164,48\nbuffer_load_dwordx4 a[112:115], v252, s[20:23], s64 offen sc1\nv_mul_f32 v231, v41, v231\nM 24,160,36\nR 178,184,36864\n"
"v_mul_f32 v232, v42, v232\nv_mul_f32 v233, v43, v233\nM 24,164,52\ns_add_u32 s66, s69, 37758976\nbuffer_load_dwordx4 v220, s[16:19], s63 offen lds\nv_cvt_pk_bf16_f32 v40, v230, v231\n"
"v_mfma_f32_16x16x32_bf16 a[232:235], v[160:163], a[40:43], a[232:235]\nR 180,184,40960\nv_cvt_pk_bf16_f32 v41, v232, v233\nv_mul_f32 v234, v44, v44\nv_mfma_f32_16x16x32_bf16 a[232:235], v[164:167], a[56:59], a[232:235]\nbuffer_load_dwordx4 a[116:119], v252, s[20:23], s65 offen sc1\n"
"v_mul_f32 v235, v45, v45\nv_mfma_f32_16x16x32_bf16 a[248:251], v[160:163], a[44:47], a[248:251]\nR 182,184,45056\nv_mul_f32 v236, v46, v46\nv_mul_f32 v237, v47, v47\nv_mfma_f32_16x16x32_bf16 a[248:251], v[164:167], a[60:63], a[248:251]\n"
"buffer_load_dwordx4 v221, s[16:19], s63 offen offset:1024 lds\nG 4\nv_fma_f32 v234, v234, v215, v214\nv_mfma_f32_16x16x32_bf16 a[252:255], v[168:171], a[44:47], a[252:255]\nR 160,185,32768\ns_add_u32 m0, s50, 90112\n"
"v_fma_f32 v235, v235, v215, v214\nv_mfma_f32_16x16x32_bf16 a[252:255], v[172:175], a[60:63], a[252:255]\ns_add_u32 s67, s69, 37873664\ns_add_u32 s63, s57, 720896\nv_fma_f32 v236, v236, v215, v214\nv_fma_f32 v237, v237, v215, v214\n"
"v_mfma_f32_16x16x32_bf16 a[236:239], v[168:171], a[40:43], a[236:239]\nR 162,185,36864\nv_mov_b32 v248, 0\nv_mul_f32 v234, v44, v234\nv_mfma_f32_16x16x32_bf16 a[236:239], v[172:175], a[56:59], a[236:239]\nbuffer_load_dwordx4 a[120:123], v252, s[20:23], s66 offen sc1\n"
"buffer_load_dwordx4 v220, s[16:19], s63 offen lds\nv_mov_b32 v249, 0\nv_mul_f32 v235, v45, v235\nM 28,168,36\nR 164,185,40960\ns_add_u32 s61, s82, 845848\n"
"s_mov_b64 exec, 0xf\nbuffer_atomic_or_x2 v[248:249], v250, s[20:23], s61 offen sc0 sc1\ns_mov_b64 exec, -1\nv_mul_f32 v236, v46, v236\nM 28,172,52\nbuffer_load_dwordx4 v221, s[16:19], s63 offen offset:1024 lds\n"
"v_mul_f32 v237, v47, v237\nM 12,168,32\nR 166,185,45056\ns_add_u32 m0, s50, 98304\nv_exp_f32 v234, v234\nM 12,172,48\n"
"G 4\nv_exp_f32 v235, v235\nv_exp_f32 v236, v236\nM 96,176,64\nR 168,186,32768\ns_add_u32 s63, s58, 0\n"
"v_exp_f32 v237, v237\ns_waitcnt vmcnt(21)\nM 96,180,80\nbuffer_load_dwordx4 a[124:127], v252, s[20:23], s67 offen sc1\nv_fma_f32 v234, v234, v216, v216\nM 112,176,68\n"
"R 170,186,36864\nv_fma_f32 v235, v235, v216, v216\nv_fma_f32 v236, v236, v216, v216\ns_waitcnt vmcnt(21)\nM 112,180,84\nbuffer_load_dwordx4 v220, s[16:19], s63 offen lds\n"
"v_fma_f32 v237, v237, v216, v216\nM 128,176,72\nR 172,186,40960\nv_mul_f32 v230, v48, v48\nv_rcp_f32 v234, v234\ns_waitcnt vmcnt(21)\n"
"M 128,180,88\nbuffer_load_dwordx4 v221, s[16:19], s63 offen offset:1024 lds\nv_rcp_f32 v235, v235\nM 144,176,76\nR 174,186,45056\ns_add_u32 m0, s50, 106496\n"
"v_rcp_f32 v236, v236\ns_waitcnt vmcnt(20)\nM 144,180,92\nG 4\nv_rcp_f32 v237, v237\nv_mul_f32 v234, v44, v234\n"
"M 148,160,76\nR 176,187,32768\ns_add_u32 s63, s58, 524288\nv_mul_f32 v235, v45, v235\nM 148,164,92\nbuffer_load_dwordx4 v220, s[16:19], s63 offen lds\n"
"v_mul_f32 v236, v46, v236\nM 132,160,72\nR 178,187,36864\nv_mul_f32 v237, v47, v237\nv_cvt_pk_bf16_f32 v42, v234, v235\nM 132,164,88\n"
"buffer_store_dwordx4 v[32:35], v252, s[20:23], s84 offen offset:0 sc1\nv_cvt_pk_bf16_f32 v43, v236, v237\nM 116,160,68\nR 180,187,40960\nv_mul_f32 v231, v49, v49\nv_mul_f32 v232, v50, v50\n"
"M 116,164,84\nbuffer_load_dwordx4 v221, s[16:19], s63 offen offset:1024 lds\nv_mul_f32 v233, v51, v51\nM 100,160,64\nR 182,187,45056\nv_fma_f32 v230, v230, v218, v217\n"
"v_fma_f32 v231, v231, v218, v217\nM 100,164,80\nG 4\nv_fma_f32 v232, v232, v218, v217\nv_fma_f32 v233, v233, v218, v217\nv_mul_f32 v230, v48, v230\n"
"M 104,168,64\nR 160,184,49152\ns_waitcnt vmcnt(16)\ns_barrier\nv_mul_f32 v231, v49, v231\nM 104,172,80\n"
"buffer_store_dwordx4 v[40:43], v252, s[20:23], s84 offen offset:1024 sc1\nv_mul_f32 v232, v50, v232\nM 120,168,68\nR 162,184,53248\nv_mul_f32 v233, v51, v233\nv_exp_f32 v230, v230\n"
"M 120,172,84\ns_add_u32 m0, s50, 114688\nv_exp_f32 v231, v231\nv_exp_f32 v232, v232\nM 136,168,72\nR 164,184,57344\n"
"v_fma_f32 v230, v230, v219, v219\nv_exp_f32 v233, v233\nM 136,172,88\ns_add_u32 s63, s58, 65536\nv_rcp_f32 v230, v230\nv_fma_f32 v231, v231, v219, v219\n"
"M 152,168,76\nR 166,184,61440\nv_fma_f32 v232, v232, v219, v219\nv_fma_f32 v233, v233, v219, v219\nM 152,172,92\nG 4\n"
"v_rcp_f32 v231, v231\nv_rcp_f32 v232, v232\nM 156,176,76\nR 168,185,49152\nv_mul_f32 v230, v48, v230\nv_rcp_f32 v233, v233\n"
"M 156,180,92\nbuffer_load_dwordx4 v220, s[16:19], s63 offen lds\nv_mul_f32 v231, v49, v231\nM 140,176,72\nR 170,185,53248\nv_mul_f32 v232, v50, v232\n"
"v_mul_f32 v233, v51, v233\nM 140,180,88\nv_cvt_pk_bf16_f32 v48, v230, v231\nv_cvt_pk_bf16_f32 v49, v232, v233\nv_mul_f32 v234, v52, v52\nM 124,176,68\n"
"R 172,185,57344\nv_mul_f32 v235, v53, v53\nv_mul_f32 v236, v54, v54\nM 124,180,84\nbuffer_load_dwordx4 v221, s[16:19], s63 offen offset:1024 lds\nv_mul_f32 v237, v55, v55\n"
"M 108,176,64\nR 174,185,61440\ns_add_u32 m0, s50, 0\nv_fma_f32 v234, v234, v218, v217\nM 108,180,80\nG 4\n"
"v_fma_f32 v235, v235, v218, v217\nv_fma_f32 v236, v236, v218, v217\nv_fma_f32 v237, v237, v218, v217\nM 96,160,96\nR 176,186,49152\ns_add_u32 s63, s58, 589824\n"
"v_mul_f32 v234, v52, v234\ns_waitcnt vmcnt(16)\nM 96,164,112\nbuffer_load_dwordx4 v220, s[16:19], s63 offen lds\nv_mul_f32 v235, v53, v235\nM 112,160,100\n"
"R 178,186,53248\nv_mul_f32 v236, v54, v236\nv_mul_f32 v237, v55, v237\ns_waitcnt vmcnt(15)\nM 112,164,116\nv_exp_f32 v234, v234\n"
"v_exp_f32 v235, v235\nM 128,160,104\nR 180,186,57344\nv_fma_f32 v234, v234, v219, v219\nv_exp_f32 v236, v236\ns_waitcnt vmcnt(13)\n"
"M 128,164,120\nv_exp_f32 v237, v237\nv_fma_f32 v235, v235, v219, v219\nM 144,160,108\nR 182,186,61440\nv_fma_f32 v236, v236, v219, v219\n"
"v_fma_f32 v237, v237, v219, v219\ns_waitcnt vmcnt(9)\nM 144,164,124\nG 4\nv_rcp_f32 v234, v234\nv_rcp_f32 v235, v235\n"
"M 148,168,108\nR 160,187,49152\nv_mul_f32 v234, v52, v234\nv_rcp_f32 v236, v236\nM 148,172,124\ns_add_u32 s64, s68, 37754880\n"
"v_rcp_f32 v237, v237\nv_mul_f32 v235, v53, v235\nM 132,168,104\nR 162,187,53248\nv_cmp_eq_u64 s[86:87], s[46:47], v[248:249]\nv_mul_f32 v236, v54, v236\n"
"M 132,172,120\nbuffer_load_dwordx4 v221, s[16:19], s63 offen offset:1024 lds\nv_mul_f32 v237, v55, v237\nM 116,168,100\nR 164,187,57344\ns_and_b32 s62, s86, 0xf\n"
"s_cmp_eq_u32 s62, 0xf\ns_cbranch_scc0 Lq898\nLq899:\nv_cvt_pk_bf16_f32 v50, v234, v235\nM 116,172,116\ns_add_u32 s65, s68, 37869568\n"
"v_cvt_pk_bf16_f32 v51, v236, v237\nv_mul_f32 v230, v56, v56\nv_mul_f32 v231, v57, v57\nM 100,168,96\nR 166,187,61440\nv_mul_f32 v232, v58, v58\n"
"v_mul_f32 v233, v59, v59\nM 100,172,112\nG 4\nv_fma_f32 v230, v230, v218, v217\nv_fma_f32 v231, v231, v218, v217\nM 104,176,96\n"
"R 168,188,0\nv_fma_f32 v232, v232, v218, v217\nM 104,180,112\nbuffer_load_dwordx4 a[128:131], v252, s[20:23], s64 offen sc1\nM 120,176,100\nR 170,188,4096\n"
"v_fma_f32 v233, v233, v218, v217\nM 120,180,116\ns_add_u32 s66, s69, 37754880\nv_mul_f32 v230, v56, v230\nv_mul_f32 v231, v57, v231\nM 136,176,104\n"
"R 172,188,8192\nv_mul_f32 v232, v58, v232\nM 136,180,120\nbuffer_load_dwordx4 a[132:135], v252, s[20:23], s65 offen sc1\nM 152,176,108\nR 174,188,12288\n"
"v_mul_f32 v233, v59, v233\nM 152,180,124\nG 4\nv_exp_f32 v230, v230\nM 156,160,108\nR 176,189,0\n"
"v_fma_f32 v230, v230, v219, v219\nM 156,164,124\ns_add_u32 s67, s69, 37869568\nv_exp_f32 v231, v231\nM 140,160,104\nR 178,189,4096\n"
"v_fma_f32 v231, v231, v219, v219\nM 140,164,120\nbuffer_load_dwordx4 a[136:139], v252, s[20:23], s66 offen sc1\nM 124,160,100\nR 180,189,8192\nv_mul_f32 v234, v60, v60\n"
"M 124,164,116\nbuffer_store_dwordx4 v[48:51], v252, s[20:23], s85 offen offset:0 sc1\nM 108,160,96\nR 182,189,12288\ns_add_u32 m0, s50, 8192\nM 108,164,112\n"
"s_waitcnt vmcnt(9)\ns_barrier\nG 4\nM 0,168,64\nR 160,190,0\ns_add_u32 s63, s58, 131072\n"
"M 0,172,80\nbuffer_load_dwordx4 a[140:143], v252, s[20:23], s67 offen sc1\nM 16,168,68\nR 162,190,4096\nv_mul_f32 v235, v61, v61\nM 16,172,84\n"
"s_add_u32 s64, s68, 37762048\nv_exp_f32 v232, v232\nv_mfma_f32_16x16x32_bf16 a[224:227], v[168:171], a[72:75], a[224:227]\nR 164,190,8192\nbuffer_load_dwordx4 v220, s[16:19], s63 offen lds\nv_mfma_f32_16x16x32_bf16 a[224:227], v[172:175], a[88:91], a[224:227]\n"
"s_add_u32 s65, s68, 37876736\nv_exp_f32 v233, v233\nv_mfma_f32_16x16x32_bf16 a[240:243], v[168:171], a[76:79], a[240:243]\nR 166,190,12288\nv_fma_f32 v232, v232, v219, v219\nv_mfma_f32_16x16x32_bf16 a[240:243], v[172:175], a[92:95], a[240:243]\n"
"G 4\nv_rcp_f32 v230, v230\nv_mfma_f32_16x16x32_bf16 a[244:247], v[176:179], a[76:79], a[244:247]\nR 168,191,0\nbuffer_load_dwordx4 v221, s[16:19], s63 offen offset:1024 lds\nv_mfma_f32_16x16x32_bf16 a[244:247], v[180:183], a[92:95], a[244:247]\n"
"buffer_load_dwordx4 a[144:147], v252, s[20:23], s64 offen sc1\nv_mfma_f32_16x16x32_bf16 a[228:231], v[176:179], a[72:75], a[228:231]\nR 170,191,4096\ns_add_u32 m0, s50, 16384\nv_mfma_f32_16x16x32_bf16 a[228:231], v[180:183], a[88:91], a[228:231]\ns_add_u32 s66, s69, 37762048\n"
"s_add_u32 s63, s58, 655360\nv_fma_f32 v233, v233, v219, v219\nM 20,176,68\nR 172,191,8192\nbuffer_load_dwordx4 v220, s[16:19], s63 offen lds\nM 20,180,84\n"
"buffer_load_dwordx4 a[148:151], v252, s[20:23], s65 offen sc1\nM 4,176,64\nR 174,191,12288\nv_mul_f32 v230, v56, v230\nM 4,180,80\nG 4\n"
"v_rcp_f32 v231, v231\nM 8,160,64\nR 176,188,16384\nbuffer_load_dwordx4 v221, s[16:19], s63 offen offset:1024 lds\nM 8,164,80\ns_add_u32 s67, s69, 37876736\n"
"v_rcp_f32 v232, v232\nM 24,160,68\nR 178,188,20480\nv_mul_f32 v231, v57, v231\nM 24,164,84\nbuffer_load_dwordx4 a[152:155], v252, s[20:23], s66 offen sc1\n"
"v_mfma_f32_16x16x32_bf16 a[232:235], v[160:163], a[72:75], a[232:235]\nR 180,188,24576\nLpg30:\ns_cbranch_execz LretO1n\nv_mul_f32 v232, v58, v232\nv_mfma_f32_16x16x32_bf16 a[232:235], v[164:167], a[88:91], a[232:235]\n"
"v_rcp_f32 v233, v233\nv_mfma_f32_16x16x32_bf16 a[248:251], v[160:163], a[76:79], a[248:251]\nR 182,188,28672\nv_mul_f32 v233, v59, v233\nv_mfma_f32_16x16x32_bf16 a[248:251], v[164:167], a[92:95], a[248:251]\nG 4\n"
"v_cvt_pk_bf16_f32 v56, v230, v231\nv_cvt_pk_bf16_f32 v57, v232, v233\nv_mfma_f32_16x16x32_bf16 a[252:255], v[168:171], a[76:79], a[252:255]\nR 160,189,16384\nv_mul_f32 v236, v62, v62\nv_mfma_f32_16x16x32_bf16 a[252:255], v[172:175], a[92:95], a[252:255]\n"
"buffer_load_dwordx4 a[156:159], v252, s[20:23], s67 offen sc1\nv_mfma_f32_16x16x32_bf16 a[236:239], v[168:171], a[72:75], a[236:239]\nR 162,189,20480\nv_mul_f32 v237, v63, v63\nv_mfma_f32_16x16x32_bf16 a[236:239], v[172:175], a[88:91], a[236:239]\nv_fma_f32 v234, v234, v218, v217\n"
"v_fma_f32 v235, v235, v218, v217\nM 28,168,68\nR 164,189,24576\nv_fma_f32 v236, v236, v218, v217\nM 28,172,84\ns_add_u32 m0, s50, 24576\n"
"v_fma_f32 v237, v237, v218, v217\nv_mul_f32 v234, v60, v234\nM 12,168,64\nR 166,189,28672\nv_mul_f32 v235, v61, v235\nM 12,172,80\n"
"G 4\nv_exp_f32 v234, v234\nM 0,176,96\nR 168,190,16384\nv_mul_f32 v236, v62, v236\nM 0,180,112\n"
"s_add_u32 s63, s58, 196608\nv_exp_f32 v235, v235\nM 16,176,100\nR 170,190,20480\nv_mul_f32 v237, v63, v237\nM 16,180,116\n"
"buffer_load_dwordx4 v220, s[16:19], s63 offen lds\nv_mfma_f32_16x16x32_bf16 a[224:227], v[176:179], a[104:107], a[224:227]\nR 172,190,24576\nv_fma_f32 v234, v234, v219, v219\nv_mfma_f32_16x16x32_bf16 a[224:227], v[180:183], a[120:123], a[224:227]\nv_exp_f32 v236, v236\n"
"v_mfma_f32_16x16x32_bf16 a[240:243], v[176:179], a[108:111], a[240:243]\nR 174,190,28672\nv_fma_f32 v235, v235, v219, v219\nv_mfma_f32_16x16x32_bf16 a[240:243], v[180:183], a[124:127], a[240:243]\nG 4\nv_exp_f32 v237, v237\n"
"v_mfma_f32_16x16x32_bf16 a[244:247], v[160:163], a[108:111], a[244:247]\nR 176,191,16384\nv_fma_f32 v236, v236, v219, v219\nv_mfma_f32_16x16x32_bf16 a[244:247], v[164:167], a[124:127], a[244:247]\nbuffer_load_dwordx4 v221, s[16:19], s63 offen offset:1024 lds\nv_mfma_f32_16x16x32_bf16 a[228:231], v[160:163], a[104:107], a[228:231]\n"
"R 178,191,20480\ns_add_u32 s63, s58, 720896\nv_mfma_f32_16x16x32_bf16 a[228:231], v[164:167], a[120:123], a[228:231]\ns_add_u32 m0, s50, 32768\nv_rcp_f32 v234, v234\nM 20,160,100\n"
"R 180,191,24576\nv_fma_f32 v237, v237, v219, v219\nM 20,164,116\nbuffer_load_dwordx4 v220, s[16:19], s63 offen lds\nM 4,160,96\nR 182,191,28672\n"
"v_mul_f32 v234, v60, v234\nM 4,164,112\nG 4\nv_rcp_f32 v235, v235\nM 8,168,96\nR 160,188,32768\n"
"v_mul_f32 v235, v61, v235\nM 8,172,112\nbuffer_load_dwordx4 v221, s[16:19], s63 offen offset:1024 lds\nM 24,168,100\nR 162,188,36864\ns_add_u32 s63, s58, 393216\n"
"M 24,172,116\ns_add_u32 m0, s50, 40960\nv_rcp_f32 v236, v236\nv_mfma_f32_16x16x32_bf16 a[232:235], v[168:171], a[104:107], a[232:235]\nR 164,188,40960\nv_mul_f32 v236, v62, v236\n"
"v_mfma_f32_16x16x32_bf16 a[232:235], v[172:175], a[120:123], a[232:235]\nbuffer_load_dwordx4 v220, s[16:19], s63 offen lds\nv_mfma_f32_16x16x32_bf16 a[248:251], v[168:171], a[108:111], a[248:251]\nR 166,188,45056\nv_cvt_pk_bf16_f32 v58, v234, v235\nv_mfma_f32_16x16x32_bf16 a[248:251], v[172:175], a[124:127], a[248:251]\n"
"G 4\nv_rcp_f32 v237, v237\nv_mfma_f32_16x16x32_bf16 a[252:255], v[176:179], a[108:111], a[252:255]\nR 168,189,32768\nv_mul_f32 v237, v63, v237\nv_mfma_f32_16x16x32_bf16 a[252:255], v[180:183], a[124:127], a[252:255]\n"
"buffer_load_dwordx4 v221, s[16:19], s63 offen offset:1024 lds\nv_mfma_f32_16x16x32_bf16 a[236:239], v[176:179], a[104:107], a[236:239]\nR 170,189,36864\ns_add_u32 s63, s58, 458752\nv_mfma_f32_16x16x32_bf16 a[236:239], v[180:183], a[120:123], a[236:239]\ns_add_u32 m0, s50, 49152\n"
"v_cvt_pk_bf16_f32 v59, v236, v237\nbuffer_store_dwordx4 v[56:59], v252, s[20:23], s85 offen offset:1024 sc1\nM 28,176,100\nR 172,189,40960\nM 28,180,116\nbuffer_load_dwordx4 v220, s[16:19], s63 offen lds\n"
"M 12,176,96\nR 174,189,45056\nM 12,180,112\ns_waitcnt vmcnt(10)\ns_barrier\nG 4\n"
"v_mfma_f32_16x16x32_bf16 v[32:35], v[160:163], a[0:3], 0\nR 176,190,32768\nM 32,164,16\nbuffer_load_dwordx4 v221, s[16:19], s63 offen offset:1024 lds\nv_mfma_f32_16x16x32_bf16 v[48:51], v[160:163], a[4:7], 0\nR 178,190,36864\n"
"M 48,164,20\nv_mfma_f32_16x16x32_bf16 v[64:67], v[160:163], a[8:11], 0\nR 180,190,40960\nM 64,164,24\nv_mfma_f32_16x16x32_bf16 v[80:83], v[160:163], a[12:15], 0\nR 182,190,45056\n"
"M 80,164,28\nG 4\nv_mfma_f32_16x16x32_bf16 v[84:87], v[168:171], a[12:15], 0\nR 160,191,32768\nM 84,172,28\nv_mfma_f32_16x16x32_bf16 v[68:71], v[168:171], a[8:11], 0\n"
"R 162,191,36864\nM 68,172,24\nv_mfma_f32_16x16x32_bf16 v[52:55], v[168:171], a[4:7], 0\nR 164,191,40960\nM 52,172,20\nv_mfma_f32_16x16x32_bf16 v[36:39], v[168:171], a[0:3], 0\n"
"R 166,191,45056\nM 36,172,16\nG 4\nv_mfma_f32_16x16x32_bf16 v[40:43], v[176:179], a[0:3], 0\nR 168,188,49152\nM 40,180,16\n"
"v_mfma_f32_16x16x32_bf16 v[56:59], v[176:179], a[4:7], 0\nR 170,188,53248\nM 56,180,20\nv_mfma_f32_16x16x32_bf16 v[72:75], v[176:179], a[8:11], 0\nR 172,184,0\nM 72,180,24\n"
"v_mfma_f32_16x16x32_bf16 v[88:91], v[176:179], a[12:15], 0\nR 174,184,4096\nM 88,180,28\nG 4\nv_mfma_f32_16x16x32_bf16 v[92:95], v[160:163], a[12:15], 0\nR 176,189,49152\n"
"M 92,164,28\nv_mfma_f32_16x16x32_bf16 v[76:79], v[160:163], a[8:11], 0\nR 178,189,53248\nM 76,164,24\nv_mfma_f32_16x16x32_bf16 v[60:63], v[160:163], a[4:7], 0\nR 180,185,0\n"
"M 60,164,20\nv_mfma_f32_16x16x32_bf16 v[44:47], v[160:163], a[0:3], 0\nR 182,185,4096\nM 44,164,16\nG 4\nM 32,168,32\n"
"R 160,190,49152\nM 32,172,48\nM 48,168,36\nR 162,190,53248\nM 48,172,52\nM 64,168,40\n"
"R 164,186,0\nM 64,172,56\nM 80,168,44\nR 166,186,4096\nM 80,172,60\nG 4\n"
"M 84,176,44\nR 168,191,49152\nM 84,180,60\nM 68,176,40\nR 170,191,53248\nv_mov_b32 v226, s46\n"
"M 68,180,56\ns_add_u32 m0, s50, 57344\nv_mov_b32 v227, s47\nM 52,176,36\nR 172,187,0\ns_add_u32 s61, s81, 845840\n"
"s_lshl_b32 s62, s44, 5\ns_add_u32 s61, s61, s62\ns_waitcnt vmcnt(0)\ns_mov_b64 exec, 1\nbuffer_atomic_swap_x2 v[226:227], off, s[20:23], s61 sc1\ns_mov_b64 exec, -1\n"
"M 52,180,52\ns_add_u32 s63, s56, 393216\nM 36,176,32\nR 174,187,4096\nM 36,180,48\nG 4\n"
"M 40,160,32\nR 176,184,8192\nM 40,164,48\nbuffer_load_dwordx4 v220, s[16:19], s63 offen lds\nM 56,160,36\nR 178,184,12288\n"
"M 56,164,52\nM 72,160,40\nR 180,184,16384\nM 72,164,56\nbuffer_load_dwordx4 v221, s[16:19], s63 offen offset:1024 lds\nM 88,160,44\n"
"R 182,184,20480\ns_add_u32 m0, s50, 65536\nM 88,164,60\nG 4\nM 92,168,44\nR 160,185,8192\n"
"s_add_u32 s63, s56, 458752\nM 92,172,60\nbuffer_load_dwordx4 v220, s[16:19], s63 offen lds\nM 76,168,40\nR 162,185,12288\nM 76,172,56\n"
"M 60,168,36\nR 164,185,16384\nM 60,172,52\nbuffer_load_dwordx4 v221, s[16:19], s63 offen offset:1024 lds\nM 44,168,32\nR 166,185,20480\n"
"s_add_u32 m0, s50, 73728\nM 44,172,48\ns_barrier\nG 4\nM 32,176,64\nR 168,186,8192\n"
"s_add_u32 s63, s57, 393216\nM 32,180,80\nbuffer_load_dwordx4 v220, s[16:19], s63 offen lds\nM 48,176,68\nR 170,186,12288\nM 48,180,84\n"
"M 64,176,72\nR 172,186,16384\nM 64,180,88\nbuffer_load_dwordx4 v221, s[16:19], s63 offen offset:1024 lds\nM 80,176,76\nR 174,186,20480\n"
"s_add_u32 s63, s57, 458752\nM 80,180,92\nG 4\nM 84,160,76\nR 176,187,8192\nM 84,164,92\n"
"s_add_u32 m0, s50, 81920\nM 68,160,72\nR 178,187,12288\nM 68,164,88\nbuffer_load_dwordx4 v220, s[16:19], s63 offen lds\nM 52,160,68\n"
"R 180,187,16384\nv_mov_b32 v248, 0\nM 52,164,84\nv_mov_b32 v249, 0\nM 36,160,64\nR 182,187,20480\n"
"s_add_u32 s61, s82, 845840\ns_mov_b64 exec, 0xf\nbuffer_atomic_or_x2 v[248:249], v250, s[20:23], s61 offen sc0 sc1\ns_mov_b64 exec, -1\nM 36,164,80\nG 4\n"
"M 40,168,64\nR 160,184,24576\nM 40,172,80\nM 56,168,68\nR 162,184,28672\nM 56,172,84\n"
"buffer_load_dwordx4 v221, s[16:19], s63 offen offset:1024 lds\nM 72,168,72\nR 164,184,32768\ns_add_u32 s63, s56, 262144\nM 72,172,88\ns_add_u32 m0, s50, 90112\n"
"M 88,168,76\nR 166,184,36864\nM 88,172,92\nG 4\nM 92,176,76\nR 168,185,24576\n"
"M 92,180,92\nbuffer_load_dwordx4 v220, s[16:19], s63 offen lds\nM 76,176,72\nR 170,185,28672\nM 76,180,88\nM 60,176,68\n"
"R 172,185,32768\nM 60,180,84\nbuffer_load_dwordx4 v221, s[16:19], s63 offen offset:1024 lds\nM 44,176,64\nR 174,185,36864\ns_add_u32 m0, s50, 98304\n"
"M 44,180,80\nG 4\nM 32,160,96\nR 176,186,24576\ns_add_u32 s63, s56, 786432\nM 32,164,112\n"
"buffer_load_dwordx4 v220, s[16:19], s63 offen lds\nM 48,160,100\nR 178,186,28672\nM 48,164,116\nM 64,160,104\nR 180,186,32768\n"
"M 64,164,120\nbuffer_load_dwordx4 v221, s[16:19], s63 offen offset:1024 lds\nM 80,160,108\nR 182,186,36864\ns_add_u32 m0, s50, 106496\nM 80,164,124\n"
"G 4\nM 84,168,108\nR 160,187,24576\ns_add_u32 s63, s56, 327680\nM 84,172,124\nbuffer_load_dwordx4 v220, s[16:19], s63 offen lds\n"
"M 68,168,104\nR 162,187,28672\nM 68,172,120\nM 52,168,100\nR 164,187,32768\nM 52,172,116\n"
"buffer_load_dwordx4 v221, s[16:19], s63 offen offset:1024 lds\nM 36,168,96\nR 166,187,36864\ns_add_u32 s63, s56, 851968\nM 36,172,112\nG 4\n"
"M 40,176,96\nR 168,184,40960\nM 40,180,112\ns_add_u32 m0, s50, 114688\nM 56,176,100\nR 170,184,45056\n"
"M 56,180,116\nbuffer_load_dwordx4 v220, s[16:19], s63 offen lds\nM 72,176,104\nR 172,184,49152\nM 72,180,120\nM 88,176,108\n"
"R 174,184,53248\ns_waitcnt vmcnt(12)\ns_barrier\nM 88,180,124\nG 4\nM 92,160,108\n"
"R 176,185,40960\nM 92,164,124\ns_add_u32 s64, s68, 37752832\nM 76,160,104\nR 178,185,45056\nM 76,164,120\n"
"buffer_load_dwordx4 v221, s[16:19], s63 offen offset:1024 lds\nM 60,160,100\nR 180,185,49152\ns_waitcnt vmcnt(9)\nv_cmp_eq_u64 s[86:87], s[46:47], v[248:249]\nM 60,164,116\n"
"s_add_u32 s65, s68, 37867520\nM 44,160,96\nR 182,185,53248\ns_and_b32 s62, s86, 0xf\ns_cmp_eq_u32 s62, 0xf\ns_cbranch_scc0 Lq917\n"
"Lq918:\nM 44,164,112\nG 4\nM 32,168,128\nR 160,186,40960\nM 32,172,144\n"
"buffer_load_dwordx4 a[160:163], v252, s[20:23], s64 offen sc1\nM 48,168,132\nR 162,186,45056\nM 48,172,148\ns_add_u32 s66, s69, 37752832\nM 64,168,136\n"
"R 164,186,49152\nM 64,172,152\nbuffer_load_dwordx4 a[164:167], v252, s[20:23], s65 offen sc1\nM 80,168,140\nR 166,186,53248\nM 80,172,156\n"
"G 4\nM 84,176,140\nR 168,187,40960\nM 84,180,156\ns_add_u32 s67, s69, 37867520\nM 68,176,136\n"
"R 170,187,45056\nM 68,180,152\nbuffer_load_dwordx4 a[168:171], v252, s[20:23], s66 offen sc1\nM 52,176,132\nR 172,187,49152\nM 52,180,148\n"
"s_add_u32 s64, s68, 37760000\nM 36,176,128\nR 174,187,53248\ns_add_u32 m0, s50, 0\nM 36,180,144\nG 4\n"
"M 40,160,128\nR 176,184,57344\ns_add_u32 s63, s57, 262144\nM 40,164,144\nbuffer_load_dwordx4 a[172:175], v252, s[20:23], s67 offen sc1\nM 56,160,132\n"
"R 178,184,61440\nM 56,164,148\ns_add_u32 s65, s68, 37874688\nbuffer_load_dwordx4 v220, s[16:19], s63 offen lds\nM 72,160,136\nR 180,188,0\n"
"M 72,164,152\nbuffer_load_dwordx4 a[176:179], v252, s[20:23], s64 offen sc1\nM 88,160,140\nR 182,188,4096\nM 88,164,156\nbuffer_load_dwordx4 v221, s[16:19], s63 offen offset:1024 lds\n"
"G 4\nM 92,168,140\nR 160,185,57344\ns_add_u32 m0, s50, 8192\nM 92,172,156\ns_add_u32 s66, s69, 37760000\n"
"M 76,168,136\nR 162,185,61440\ns_add_u32 s63, s57, 786432\nM 76,172,152\nbuffer_load_dwordx4 a[180:183], v252, s[20:23], s65 offen sc1\nbuffer_load_dwordx4 v220, s[16:19], s63 offen lds\n"
"M 60,168,132\nR 164,189,0\nM 60,172,148\ns_add_u32 s67, s69, 37874688\nM 44,168,128\nR 166,189,4096\n"
"M 44,172,144\nbuffer_load_dwordx4 v221, s[16:19], s63 offen offset:1024 lds\nG 4\nM 96,176,128\nR 168,186,57344\ns_add_u32 m0, s50, 16384\n"
"M 96,180,144\nbuffer_load_dwordx4 a[184:187], v252, s[20:23], s66 offen sc1\nM 112,176,132\nR 170,186,61440\ns_add_u32 s63, s57, 327680\nM 112,180,148\n"
"s_add_u32 s64, s68, 37753856\nbuffer_load_dwordx4 v220, s[16:19], s63 offen lds\nM 128,176,136\nR 172,190,0\nM 128,180,152\nbuffer_load_dwordx4 a[188:191], v252, s[20:23], s67 offen sc1\n"
"M 144,176,140\nR 174,190,4096\nM 144,180,156\nbuffer_load_dwordx4 v221, s[16:19], s63 offen offset:1024 lds\nG 4\nM 148,160,140\n"
"R 176,187,57344\ns_waitcnt vmcnt(18)\ns_barrier\nM 148,164,156\ns_add_u32 s65, s68, 37868544\ns_add_u32 s63, s57, 851968\n"
"M 132,160,136\nR 178,187,61440\ns_add_u32 m0, s50, 24576\nM 132,164,152\nbuffer_load_dwordx4 a[192:195], v252, s[20:23], s64 offen sc1\nM 116,160,132\n"
"R 180,191,0\nbuffer_load_dwordx4 v220, s[16:19], s63 offen lds\nM 116,164,148\ns_add_u32 s66, s69, 37753856\nM 100,160,128\nR 182,191,4096\n"
"M 100,164,144\nG 4\nM 104,168,128\nR 160,188,8192\nbuffer_load_dwordx4 v221, s[16:19], s63 offen offset:1024 lds\nM 104,172,144\n"
"buffer_load_dwordx4 a[196:199], v252, s[20:23], s65 offen sc1\nM 120,168,132\nR 162,188,12288\nM 120,172,148\ns_add_u32 s67, s69, 37868544\nM 136,168,136\n"
"R 164,188,16384\nM 136,172,152\nbuffer_load_dwordx4 a[200:203], v252, s[20:23], s66 offen sc1\nM 152,168,140\nR 166,188,20480\nM 152,172,156\n"
"G 4\nM 156,176,140\nR 168,189,8192\nM 156,180,156\nbuffer_load_dwordx4 a[204:207], v252, s[20:23], s67 offen sc1\nM 140,176,136\n"
"R 170,189,12288\nM 140,180,152\ns_add_u32 s64, s68, 37761024\nM 124,176,132\nR 172,189,16384\nM 124,180,148\n"
"s_add_u32 s65, s68, 37875712\nM 108,176,128\nR 174,189,20480\nM 108,180,144\nG 4\nM 0,160,128\n"
"R 176,190,8192\nM 0,164,144\nbuffer_load_dwordx4 a[208:211], v252, s[20:23], s64 offen sc1\nM 16,160,132\nR 178,190,12288\nM 16,164,148\n"
"s_add_u32 s66, s69, 37761024\nv_mfma_f32_16x16x32_bf16 a[224:227], v[160:163], a[136:139], a[224:227]\nR 180,190,16384\nv_mfma_f32_16x16x32_bf16 a[224:227], v[164:167], a[152:155], a[224:227]\nbuffer_load_dwordx4 a[212:215], v252, s[20:23], s65 offen sc1\nv_mfma_f32_16x16x32_bf16 a[240:243], v[160:163], a[140:143], a[240:243]\n"
"R 182,190,20480\nv_mfma_f32_16x16x32_bf16 a[240:243], v[164:167], a[156:159], a[240:243]\nLpg31:\ns_cbranch_execz LretO1n\nG 4\nv_mfma_f32_16x16x32_bf16 a[244:247], v[168:171], a[140:143], a[244:247]\n"
"R 160,191,8192\nv_mfma_f32_16x16x32_bf16 a[244:247], v[172:175], a[156:159], a[244:247]\nbuffer_load_dwordx4 a[216:219], v252, s[20:23], s66 offen sc1\nv_mfma_f32_16x16x32_bf16 a[228:231], v[168:171], a[136:139], a[228:231]\nR 162,191,12288\nv_mfma_f32_16x16x32_bf16 a[228:231], v[172:175], a[152:155], a[228:231]\n"
"s_add_u32 s67, s69, 37875712\nM 20,168,132\nR 164,191,16384\nM 20,172,148\nM 4,168,128\nR 166,191,20480\n"
"M 4,172,144\nG 4\nM 8,176,128\nR 168,188,24576\nM 8,180,144\nbuffer_load_dwordx4 a[220:223], v252, s[20:23], s67 offen sc1\n"
"M 24,176,132\nR 170,188,28672\nM 24,180,148\ns_add_u32 m0, s50, 32768\nv_mfma_f32_16x16x32_bf16 a[232:235], v[176:179], a[136:139], a[232:235]\nR 172,188,32768\n"
"v_mfma_f32_16x16x32_bf16 a[232:235], v[180:183], a[152:155], a[232:235]\ns_add_u32 s63, s58, 262144\nv_mfma_f32_16x16x32_bf16 a[248:251], v[176:179], a[140:143], a[248:251]\nR 174,188,36864\nv_mfma_f32_16x16x32_bf16 a[248:251], v[180:183], a[156:159], a[248:251]\nG 4\n"
"v_mfma_f32_16x16x32_bf16 a[252:255], v[160:163], a[140:143], a[252:255]\nR 176,189,24576\nv_mfma_f32_16x16x32_bf16 a[252:255], v[164:167], a[156:159], a[252:255]\nbuffer_load_dwordx4 v220, s[16:19], s63 offen lds\nv_mfma_f32_16x16x32_bf16 a[236:239], v[160:163], a[136:139], a[236:239]\nR 178,189,28672\n"
"v_mfma_f32_16x16x32_bf16 a[236:239], v[164:167], a[152:155], a[236:239]\nM 28,160,132\nR 180,189,32768\nM 28,164,148\nbuffer_load_dwordx4 v221, s[16:19], s63 offen offset:1024 lds\nM 12,160,128\n"
"R 182,189,36864\nM 12,164,144\ns_waitcnt vmcnt(9)\ns_barrier\nG 4\nM 96,168,160\n"
"R 160,190,24576\nM 96,172,176\ns_add_u32 m0, s50, 40960\nM 112,168,164\nR 162,190,28672\nM 112,172,180\n"
"s_add_u32 s63, s58, 786432\nM 128,168,168\nR 164,190,32768\nv_lshrrev_b32 v230, 8, v252\nM 128,172,184\nbuffer_load_dwordx4 v220, s[16:19], s63 offen lds\n"
"M 144,168,172\nR 166,190,36864\nv_lshlrev_b32 v230, 3, v230\nM 144,172,188\nG 4\nM 148,176,172\n"
"R 168,191,24576\nM 148,180,188\nbuffer_load_dwordx2 v[232:233], v230, s[36:39], s53 offen offset:0\nM 132,176,168\nR 170,191,28672\nM 132,180,184\n"
"buffer_load_dwordx2 v[234:235], v230, s[36:39], s53 offen offset:32\nM 116,176,164\nR 172,191,32768\nM 116,180,180\nbuffer_load_dwordx4 v221, s[16:19], s63 offen offset:1024 lds\nM 100,176,160\n"
"R 174,191,36864\nM 100,180,176\nG 4\nM 104,160,160\nR 176,188,40960\nM 104,164,176\n"
"s_add_u32 m0, s50, 49152\nM 120,160,164\nR 178,188,45056\nM 120,164,180\ns_add_u32 s63, s58, 327680\nM 136,160,168\n"
"R 180,188,49152\nbuffer_load_dwordx2 v[236:237], v230, s[36:39], s53 offen offset:64\nM 136,164,184\nbuffer_load_dwordx4 v220, s[16:19], s63 offen lds\nM 152,160,172\nR 182,188,53248\n"
"M 152,164,188\nbuffer_load_dwordx2 v[238:239], v230, s[36:39], s53 offen offset:96\nG 4\nM 156,168,172\nR 160,189,40960\nv_bfe_u32 v242, v252, 4, 4\n"
"M 156,172,188\nv_lshrrev_b32 v243, 8, v252\nv_lshlrev_b32 v243, 2, v243\nM 140,168,168\nR 162,189,45056\nv_sub_u32 v242, v242, v243\n"
"M 140,172,184\nv_lshlrev_b32 v243, 4, v242\ns_mov_b64 s[60:61], 0x3f80\nv_lshlrev_b64 v[208:209], v243, s[60:61]\nM 124,168,164\nR 164,189,49152\n"
"M 124,172,180\nbuffer_load_dwordx4 v221, s[16:19], s63 offen offset:1024 lds\nM 108,168,160\nR 166,189,53248\nM 108,172,176\nG 4\n"
"M 96,176,192\nR 168,190,40960\ns_waitcnt vmcnt(13)\nM 96,180,208\ns_add_u32 m0, s50, 57344\nM 112,176,196\n"
"R 170,190,45056\ns_waitcnt vmcnt(12)\nM 112,180,212\ns_add_u32 s63, s58, 851968\nM 128,176,200\nR 172,190,49152\n"
"s_waitcnt vmcnt(11)\nM 128,180,216\nbuffer_load_dwordx4 v220, s[16:19], s63 offen lds\nM 144,176,204\nR 174,190,53248\ns_waitcnt vmcnt(11)\n"
"M 144,180,220\nG 4\nM 148,160,204\nR 176,191,40960\nM 148,164,220\nv_cmp_gt_u32 s[60:61], 4, v242\n"
"s_nop 1\nv_cndmask_b32 v212, 0, v208, s[60:61]\nv_cndmask_b32 v208, 0, v208, s[60:61]\nv_cndmask_b32 v213, 0, v209, s[60:61]\nv_cndmask_b32 v209, 0, v209, s[60:61]\nM 132,160,200\n"
"R 178,191,45056\nM 132,164,216\nM 116,160,196\nR 180,191,49152\nM 116,164,212\nbuffer_load_dwordx4 v221, s[16:19], s63 offen offset:1024 lds\n"
"M 100,160,192\nR 182,191,53248\nM 100,164,208\nG 4\nM 104,168,192\nR 160,184,0\n"
"M 104,172,208\ns_add_u32 m0, s50, 65536\nM 120,168,196\nR 162,184,4096\nM 120,172,212\ns_add_u32 s63, s59, 0\n"
"M 136,168,200\nR 164,184,8192\nM 136,172,216\nbuffer_load_dwordx4 v220, s[16:19], s63 offen lds\nM 152,168,204\nR 166,184,12288\n"
"M 152,172,220\nG 4\nM 156,176,204\nR 168,185,0\nM 156,180,220\nM 140,176,200\n"
"R 170,185,4096\nM 140,180,216\nbuffer_load_dwordx4 v221, s[16:19], s63 offen offset:1024 lds\nM 124,176,196\nR 172,185,8192\nM 124,180,212\n"
"M 108,176,192\nR 174,185,12288\nM 108,180,208\nG 4\nM 0,160,160\nR 176,186,0\n"
"M 0,164,176\ns_mov_b32 m0, s51\nM 16,160,164\nR 178,186,4096\nv_cvt_pk_bf16_f32 v128, v128, v129\nv_cvt_pk_bf16_f32 v129, v130, v131\n"
"M 16,164,180\nds_write_addtid_b32 v128 offset:0\nv_cvt_pk_bf16_f32 v130, v132, v133\nv_cvt_pk_bf16_f32 v131, v134, v135\nv_mfma_f32_16x16x32_bf16 a[224:227], v[160:163], a[168:171], a[224:227]\nR 180,186,8192\n"
"v_cvt_pk_bf16_f32 v132, v136, v137\nv_mfma_f32_16x16x32_bf16 a[224:227], v[164:167], a[184:187], a[224:227]\nds_write_addtid_b32 v129 offset:256\nv_cvt_pk_bf16_f32 v133, v138, v139\nv_mfma_f32_16x16x32_bf16 a[240:243], v[160:163], a[172:175], a[240:243]\nR 182,186,12288\n"
"v_cvt_pk_bf16_f32 v134, v140, v141\nv_mfma_f32_16x16x32_bf16 a[240:243], v[164:167], a[188:191], a[240:243]\nds_write_addtid_b32 v130 offset:512\nG 7\nv_cvt_pk_bf16_f32 v135, v142, v143\nv_mfma_f32_16x16x32_bf16 a[244:247], v[168:171], a[172:175], a[244:247]\n"
"R 160,187,0\nv_cvt_pk_bf16_f32 v136, v144, v145\nv_mfma_f32_16x16x32_bf16 a[244:247], v[172:175], a[188:191], a[244:247]\nds_write_addtid_b32 v131 offset:768\nv_cvt_pk_bf16_f32 v137, v146, v147\nv_mfma_f32_16x16x32_bf16 a[228:231], v[168:171], a[168:171], a[228:231]\n"
"R 162,187,4096\nv_cvt_pk_bf16_f32 v138, v148, v149\nv_mfma_f32_16x16x32_bf16 a[228:231], v[172:175], a[184:187], a[228:231]\nds_write_addtid_b32 v132 offset:1024\nv_cvt_pk_bf16_f32 v139, v150, v151\nM 20,168,164\n"
"R 164,187,8192\nv_cvt_pk_bf16_f32 v140, v152, v153\nM 20,172,180\nds_write_addtid_b32 v133 offset:1280\nv_cvt_pk_bf16_f32 v141, v154, v155\nM 4,168,160\n"
"R 166,187,12288\nv_cvt_pk_bf16_f32 v142, v156, v157\nM 4,172,176\nds_write_addtid_b32 v134 offset:1536\nG 9\nv_cvt_pk_bf16_f32 v143, v158, v159\n"
"M 8,176,160\nR 168,184,16384\nds_write_addtid_b32 v135 offset:1792\nM 8,180,176\nds_write_addtid_b32 v136 offset:2048\nds_write_addtid_b32 v137 offset:2304\n"
"M 24,176,164\nR 170,184,20480\nds_write_addtid_b32 v138 offset:2560\nM 24,180,180\nds_write_addtid_b32 v139 offset:2816\nds_write_addtid_b32 v140 offset:3072\n"
"v_mfma_f32_16x16x32_bf16 a[232:235], v[176:179], a[168:171], a[232:235]\nR 172,184,24576\nds_write_addtid_b32 v141 offset:3328\nv_mfma_f32_16x16x32_bf16 a[232:235], v[180:183], a[184:187], a[232:235]\nds_write_addtid_b32 v142 offset:3584\nds_write_addtid_b32 v143 offset:3840\n"
"v_mfma_f32_16x16x32_bf16 a[248:251], v[176:179], a[172:175], a[248:251]\nR 174,184,28672\ns_add_u32 m0, s50, 73728\nv_mfma_f32_16x16x32_bf16 a[248:251], v[180:183], a[188:191], a[248:251]\ns_add_u32 s63, s59, 524288\nG 14\n"
"v_mfma_f32_16x16x32_bf16 a[252:255], v[160:163], a[172:175], a[252:255]\nR 176,185,16384\ns_waitcnt vmcnt(8) lgkmcnt(2)\ns_barrier\nv_mfma_f32_16x16x32_bf16 a[252:255], v[164:167], a[188:191], a[252:255]\nbuffer_load_dwordx4 v220, s[16:19], s63 offen lds\n"
"v_mfma_f32_16x16x32_bf16 a[236:239], v[160:163], a[168:171], a[236:239]\nR 178,185,20480\nv_mfma_f32_16x16x32_bf16 a[236:239], v[164:167], a[184:187], a[236:239]\nds_read2st64_b32 v[128:129], v225 offset0:0 offset1:1\nM 28,160,164\nR 180,185,24576\n"
"M 28,164,180\nbuffer_load_dwordx4 v221, s[16:19], s63 offen offset:1024 lds\nM 12,160,160\nR 182,185,28672\ns_add_u32 m0, s50, 81920\nM 12,164,176\n"
"ds_read2st64_b32 v[130:131], v225 offset0:2 offset1:3\nG 6\nM 0,168,192\nR 160,186,16384\ns_add_u32 s63, s59, 65536\nM 0,172,208\n"
"buffer_load_dwordx4 v220, s[16:19], s63 offen lds\nds_read2st64_b32 v[132:133], v225 offset0:4 offset1:5\nM 16,168,196\nR 162,186,20480\nds_read2st64_b32 v[134:135], v225 offset0:6 offset1:7\nM 16,172,212\n"
"ds_read2st64_b32 v[136:137], v225 offset0:8 offset1:9\nv_mfma_f32_16x16x32_bf16 a[224:227], v[168:171], a[200:203], a[224:227]\nR 164,186,24576\nv_mfma_f32_16x16x32_bf16 a[224:227], v[172:175], a[216:219], a[224:227]\nbuffer_load_dwordx4 v221, s[16:19], s63 offen offset:1024 lds\nG 6\n"
"v_mfma_f32_16x16x32_bf16 v[96:99], v[208:211], v[128:131], v[96:99]\nv_mfma_f32_16x16x32_bf16 v[100:103], v[210:213], v[128:131], v[100:103]\nds_read2st64_b32 v[138:139], v225 offset0:10 offset1:11\nv_mfma_f32_16x16x32_bf16 a[240:243], v[168:171], a[204:207], a[240:243]\nR 166,186,28672\ns_add_u32 m0, s50, 90112\n"
"v_mfma_f32_16x16x32_bf16 a[240:243], v[172:175], a[220:223], a[240:243]\nds_read2st64_b32 v[140:141], v225 offset0:12 offset1:13\nv_mfma_f32_16x16x32_bf16 a[244:247], v[176:179], a[204:207], a[244:247]\nR 168,187,16384\ns_add_u32 s63, s59, 589824\nG 6\n"
"v_mfma_f32_16x16x32_bf16 v[104:107], v[208:211], v[132:135], v[104:107]\nv_mfma_f32_16x16x32_bf16 v[108:111], v[210:213], v[132:135], v[108:111]\nds_read2st64_b32 v[142:143], v225 offset0:14 offset1:15\nv_mfma_f32_16x16x32_bf16 a[244:247], v[180:183], a[220:223], a[244:247]\nbuffer_load_dwordx4 v220, s[16:19], s63 offen lds\nv_mfma_f32_16x16x32_bf16 a[228:231], v[176:179], a[200:203], a[228:231]\n"
"R 170,187,20480\nv_mfma_f32_16x16x32_bf16 a[228:231], v[180:183], a[216:219], a[228:231]\nG 5\nv_mfma_f32_16x16x32_bf16 v[112:115], v[208:211], v[136:139], v[112:115]\nv_mfma_f32_16x16x32_bf16 v[116:119], v[210:213], v[136:139], v[116:119]\nM 20,176,196\n"
"R 172,187,24576\nM 20,180,212\nbuffer_load_dwordx4 v221, s[16:19], s63 offen offset:1024 lds\nM 4,176,192\nR 174,187,28672\ns_add_u32 s63, s59, 131072\n"
"G 3\nv_mfma_f32_16x16x32_bf16 v[120:123], v[208:211], v[140:143], v[120:123]\nv_mfma_f32_16x16x32_bf16 v[124:127], v[210:213], v[140:143], v[124:127]\nM 4,180,208\nM 8,160,192\nR 176,184,32768\n"
"M 8,164,208\ns_add_u32 m0, s50, 98304\nM 24,160,196\nR 178,184,36864\nM 24,164,212\nbuffer_load_dwordx4 v220, s[16:19], s63 offen lds\n"
"v_mfma_f32_16x16x32_bf16 a[232:235], v[160:163], a[200:203], a[232:235]\nR 180,184,40960\nv_cvt_pk_bf16_f32 v120, v120, v121\nv_cvt_pk_bf16_f32 v112, v112, v113\nv_cvt_pk_bf16_f32 v121, v122, v123\nv_mfma_f32_16x16x32_bf16 a[232:235], v[164:167], a[216:219], a[232:235]\n"
"v_cvt_pk_bf16_f32 v122, v124, v125\nv_cvt_pk_bf16_f32 v123, v126, v127\nv_cvt_pk_bf16_f32 v96, v96, v97\nv_cvt_pk_bf16_f32 v113, v114, v115\nv_mfma_f32_16x16x32_bf16 a[248:251], v[160:163], a[204:207], a[248:251]\nR 182,184,45056\n"
"v_cvt_pk_bf16_f32 v114, v116, v117\nv_cvt_pk_bf16_f32 v97, v98, v99\nv_mfma_f32_16x16x32_bf16 a[248:251], v[164:167], a[220:223], a[248:251]\nG 4\nv_cvt_pk_bf16_f32 v98, v100, v101\nv_cvt_pk_bf16_f32 v99, v102, v103\n"
"v_mfma_f32_16x16x32_bf16 a[252:255], v[168:171], a[204:207], a[252:255]\nR 160,185,32768\ns_bfe_u32 s100, s56, 0x20007\ns_mul_i32 s100, s100, 49152\ns_add_u32 s100, s100, s72\ns_add_u32 s100, s100, 65011712\n"
"v_cvt_pk_bf16_f32 v115, v118, v119\nv_mfma_f32_16x16x32_bf16 a[252:255], v[172:175], a[220:223], a[252:255]\nbuffer_store_dwordx4 v[96:99], v252, s[20:23], s100 offen offset:0 sc1\nv_mfma_f32_16x16x32_bf16 a[236:239], v[168:171], a[200:203], a[236:239]\nR 162,185,36864\nv_cvt_pk_bf16_f32 v104, v104, v105\n"
"v_mfma_f32_16x16x32_bf16 a[236:239], v[172:175], a[216:219], a[236:239]\nv_cvt_pk_bf16_f32 v105, v106, v107\nv_cvt_pk_bf16_f32 v106, v108, v109\nM 28,168,196\nR 164,185,40960\nv_cvt_pk_bf16_f32 v107, v110, v111\n"
"M 28,172,212\nbuffer_load_dwordx4 v221, s[16:19], s63 offen offset:1024 lds\nM 12,168,192\nR 166,185,45056\nM 12,172,208\nbuffer_store_dwordx4 v[104:107], v252, s[20:23], s100 offen offset:1024 sc1\n"
"s_waitcnt vmcnt(12)\ns_barrier\nG 4\nM 32,176,160\nR 168,186,32768\nM 32,180,176\n"
"v_xor_b32 v240, 0x2000, v225\nM 48,176,164\nR 170,186,36864\nds_write2st64_b32 v240, a224, a225 offset0:0 offset1:1\nM 48,180,180\nbuffer_store_dwordx4 v[112:115], v252, s[20:23], s100 offen offset:2048 sc1\n"
"ds_write2st64_b32 v240, a226, a227 offset0:2 offset1:3\nM 64,176,168\nR 172,186,40960\nds_write2st64_b32 v240, a228, a229 offset0:4 offset1:5\nM 64,180,184\ns_add_u32 m0, s50, 106496\n"
"ds_write2st64_b32 v240, a230, a231 offset0:6 offset1:7\nM 80,176,172\nR 174,186,45056\ns_add_u32 s63, s59, 655360\nds_write2st64_b32 v240, a232, a233 offset0:8 offset1:9\nM 80,180,188\n"
"ds_write2st64_b32 v240, a234, a235 offset0:10 offset1:11\nG 10\nM 84,160,172\nR 176,187,32768\nds_write2st64_b32 v240, a236, a237 offset0:12 offset1:13\nM 84,164,188\n"
"buffer_store_dwordx4 v[120:123], v252, s[20:23], s100 offen offset:3072 sc1\nds_write2st64_b32 v240, a238, a239 offset0:14 offset1:15\nM 68,160,168\nR 178,187,36864\nv_mfma_f32_16x16x32_bf16 v[96:99], v[208:211], v[192:195], 0\nds_write2st64_b32 v240, a240, a241 offset0:16 offset1:17\n"
"M 68,164,184\nv_mfma_f32_16x16x32_bf16 v[100:103], v[210:213], v[192:195], 0\nbuffer_load_dwordx4 v220, s[16:19], s63 offen lds\nds_write2st64_b32 v240, a242, a243 offset0:18 offset1:19\nM 52,160,164\nR 180,187,40960\n"
"v_mfma_f32_16x16x32_bf16 v[104:107], v[208:211], v[196:199], 0\nds_write2st64_b32 v240, a244, a245 offset0:20 offset1:21\nM 52,164,180\nv_mfma_f32_16x16x32_bf16 v[108:111], v[210:213], v[196:199], 0\nds_write2st64_b32 v240, a246, a247 offset0:22 offset1:23\nM 36,160,160\n"
"R 182,187,45056\nv_mfma_f32_16x16x32_bf16 v[112:115], v[208:211], v[200:203], 0\nds_write2st64_b32 v240, a248, a249 offset0:24 offset1:25\nM 36,164,176\nv_mfma_f32_16x16x32_bf16 v[116:119], v[210:213], v[200:203], 0\nds_write2st64_b32 v240, a250, a251 offset0:26 offset1:27\n"
"G 14\nM 40,168,160\nR 160,184,49152\nv_mfma_f32_16x16x32_bf16 v[120:123], v[208:211], v[204:207], 0\nds_write2st64_b32 v240, a252, a253 offset0:28 offset1:29\nM 40,172,176\n"
"v_mfma_f32_16x16x32_bf16 v[124:127], v[210:213], v[204:207], 0\nbuffer_load_dwordx4 v221, s[16:19], s63 offen offset:1024 lds\nds_write2st64_b32 v240, a254, a255 offset0:30 offset1:31\nM 56,168,164\nR 162,184,53248\nv_mfma_f32_16x16x32_bf16 v[96:99], v[208:211], v[232:235], v[96:99]\n"
"s_add_u32 m0, s50, 114688\ns_add_u32 s63, s59, 196608\nM 56,172,180\nv_mfma_f32_16x16x32_bf16 v[100:103], v[210:213], v[232:235], v[100:103]\nbuffer_load_dwordx4 v220, s[16:19], s63 offen lds\nM 72,168,168\n"
"R 164,184,57344\nv_mfma_f32_16x16x32_bf16 v[104:107], v[208:211], v[236:239], v[104:107]\nM 72,172,184\nv_mfma_f32_16x16x32_bf16 v[108:111], v[210:213], v[236:239], v[108:111]\nbuffer_load_dwordx4 v221, s[16:19], s63 offen offset:1024 lds\nM 88,168,172\n"
"R 166,184,61440\nv_mfma_f32_16x16x32_bf16 v[112:115], v[208:211], v[232:235], v[112:115]\ns_add_u32 m0, s50, 0\nM 88,172,188\nv_mfma_f32_16x16x32_bf16 v[116:119], v[210:213], v[232:235], v[116:119]\ns_add_u32 s63, s59, 720896\n"
"G 8\nM 92,176,172\nR 168,185,49152\nv_mfma_f32_16x16x32_bf16 v[120:123], v[208:211], v[236:239], v[120:123]\ns_waitcnt vmcnt(14) lgkmcnt(4)\ns_barrier\n"
"M 92,180,188\nv_mfma_f32_16x16x32_bf16 v[124:127], v[210:213], v[236:239], v[124:127]\nbuffer_load_dwordx4 v220, s[16:19], s63 offen lds\nds_read2st64_b32 v[128:129], v225 offset0:0 offset1:1\nM 76,176,168\nR 170,185,53248\n"
"ds_read2st64_b32 v[130:131], v225 offset0:2 offset1:3\nM 76,180,184\nds_read2st64_b32 v[132:133], v225 offset0:4 offset1:5\nds_read2st64_b32 v[134:135], v225 offset0:6 offset1:7\nM 60,176,164\nR 172,185,57344\n"
"ds_read2st64_b32 v[136:137], v225 offset0:8 offset1:9\nM 60,180,180\nbuffer_load_dwordx4 v221, s[16:19], s63 offen offset:1024 lds\nds_read2st64_b32 v[138:139], v225 offset0:10 offset1:11\nM 44,176,160\nR 174,185,61440\n"
"s_add_u32 m0, s50, 8192\nds_read2st64_b32 v[140:141], v225 offset0:12 offset1:13\nM 44,180,176\nds_read2st64_b32 v[142:143], v225 offset0:14 offset1:15\nds_read2st64_b32 v[144:145], v225 offset0:16 offset1:17\nG 13\n"
"M 32,160,192\nR 176,186,49152\ns_add_u32 s63, s59, 393216\nds_read2st64_b32 v[146:147], v225 offset0:18 offset1:19\nM 32,164,208\nbuffer_load_dwordx4 v220, s[16:19], s63 offen lds\n"
"ds_read2st64_b32 v[148:149], v225 offset0:20 offset1:21\nM 48,160,196\nR 178,186,53248\nds_read2st64_b32 v[150:151], v225 offset0:22 offset1:23\nG 15\nv_add_f32 v0, v0, v128\n"
"M 48,164,212\nds_read2st64_b32 v[152:153], v225 offset0:24 offset1:25\nds_read2st64_b32 v[154:155], v225 offset0:26 offset1:27\nv_add_f32 v1, v1, v129\nM 64,160,200\nR 180,186,57344\n"
"ds_read2st64_b32 v[156:157], v225 offset0:28 offset1:29\nG 15\nv_add_f32 v2, v2, v130\nM 64,164,216\nbuffer_load_dwordx4 v221, s[16:19], s63 offen offset:1024 lds\nds_read2st64_b32 v[158:159], v225 offset0:30 offset1:31\n"
"M 80,160,204\nR 182,186,61440\ns_add_u32 m0, s50, 16384\nv_add_f32 v3, v3, v131\nM 80,164,220\nG 14\n"
"v_add_f32 v4, v4, v132\nv_add_f32 v5, v5, v133\nv_add_f32 v6, v6, v134\nM 84,168,204\nR 160,187,49152\ns_add_u32 s63, s59, 458752\n"
"v_add_f32 v7, v7, v135\nM 84,172,220\nbuffer_load_dwordx4 v220, s[16:19], s63 offen lds\nv_add_f32 v8, v8, v136\nM 68,168,200\nR 162,187,53248\n"
"Lpg32:\ns_cbranch_execz LretW1z\nv_add_f32 v9, v9, v137\nv_add_f32 v10, v10, v138\nM 68,172,216\nv_add_f32 v11, v11, v139\n"
"G 15\nv_add_f32 v12, v12, v140\nv_add_f32 v13, v13, v141\nM 52,168,196\nR 164,187,57344\nG 15\n"
"v_add_f32 v14, v14, v142\nv_add_f32 v15, v15, v143\nM 52,172,212\nbuffer_load_dwordx4 v221, s[16:19], s63 offen offset:1024 lds\nG 14\nv_add_f32 v16, v16, v144\n"
"M 36,168,192\nR 166,187,61440\ns_add_u32 s63, s59, 262144\nv_add_f32 v17, v17, v145\nM 36,172,208\nG 4\n"
"v_add_f32 v18, v18, v146\nv_add_f32 v19, v19, v147\nv_add_f32 v20, v20, v148\nM 40,176,192\nR 168,188,0\nv_add_f32 v21, v21, v149\n"
"v_add_f32 v22, v22, v150\nM 40,180,208\ns_add_u32 m0, s50, 24576\nv_add_f32 v23, v23, v151\nv_add_f32 v24, v24, v152\nv_add_f32 v25, v25, v153\n"
"M 56,176,196\nR 170,188,4096\nv_add_f32 v26, v26, v154\nv_add_f32 v27, v27, v155\nM 56,180,212\nbuffer_load_dwordx4 v220, s[16:19], s63 offen lds\n"
"v_add_f32 v28, v28, v156\nv_add_f32 v29, v29, v157\nv_add_f32 v30, v30, v158\nv_add_f32 v31, v31, v159\nM 72,176,200\nR 172,188,8192\n"
"v_cvt_pk_bf16_f32 v24, v24, v25\nv_cvt_pk_bf16_f32 v8, v8, v9\nv_cvt_pk_bf16_f32 v16, v16, v17\nv_cvt_pk_bf16_f32 v25, v26, v27\nM 72,180,216\nv_cvt_pk_bf16_f32 v26, v28, v29\n"
"v_cvt_pk_bf16_f32 v27, v30, v31\nv_cvt_pk_bf16_f32 v0, v0, v1\nv_cvt_pk_bf16_f32 v9, v10, v11\nv_cvt_pk_bf16_f32 v17, v18, v19\nM 88,176,204\nR 174,188,12288\n"
"v_cvt_pk_bf16_f32 v1, v2, v3\nM 88,180,220\nG 4\nv_cvt_pk_bf16_f32 v2, v4, v5\nv_cvt_pk_bf16_f32 v3, v6, v7\nM 92,160,204\n"
"R 176,189,0\ns_bfe_u32 s100, s57, 0x20007\ns_mul_i32 s100, s100, 49152\ns_add_u32 s100, s100, s72\ns_add_u32 s100, s100, 65028096\nv_cvt_pk_bf16_f32 v10, v12, v13\n"
"M 92,164,220\nbuffer_store_dwordx4 v[0:3], v252, s[20:23], s100 offen offset:0 sc1\nM 76,160,200\nR 178,189,4096\nv_cvt_pk_bf16_f32 v11, v14, v15\nM 76,164,216\n"
"buffer_load_dwordx4 v221, s[16:19], s63 offen offset:1024 lds\nM 60,160,196\nR 180,189,8192\ns_add_u32 m0, s50, 32768\ns_add_u32 s63, s59, 786432\nM 60,164,212\n"
"buffer_load_dwordx4 v220, s[16:19], s63 offen lds\nM 44,160,192\nR 182,189,12288\nv_cvt_pk_bf16_f32 v18, v20, v21\nM 44,164,208\nbuffer_store_dwordx4 v[8:11], v252, s[20:23], s100 offen offset:1024 sc1\n"
"s_waitcnt vmcnt(21)\ns_barrier\nG 4\nM 96,168,0\nR 160,190,0\nv_cvt_pk_bf16_f32 v19, v22, v23\n"
"M 96,172,16\ns_mov_b32 m0, s51\nM 112,168,4\nR 162,190,4096\nv_cvt_pk_bf16_f32 v64, v64, v65\nv_cvt_pk_bf16_f32 v65, v66, v67\n"
"M 112,172,20\nbuffer_store_dwordx4 v[16:19], v252, s[20:23], s100 offen offset:2048 sc1\nv_cvt_pk_bf16_f32 v66, v68, v69\nv_mfma_f32_16x16x32_bf16 v[128:131], v[168:171], a[8:11], 0\nR 164,190,8192\nv_cvt_pk_bf16_f32 v67, v70, v71\n"
"v_cvt_pk_bf16_f32 v68, v72, v73\nM 128,172,24\nds_write_addtid_b32 v64 offset:0\nv_cvt_pk_bf16_f32 v69, v74, v75\nv_cvt_pk_bf16_f32 v70, v76, v77\nv_mfma_f32_16x16x32_bf16 v[144:147], v[168:171], a[12:15], 0\n"
"R 166,190,12288\nv_cvt_pk_bf16_f32 v71, v78, v79\nv_cvt_pk_bf16_f32 v72, v80, v81\nM 144,172,28\nds_write_addtid_b32 v65 offset:256\nG 6\n"
"v_cvt_pk_bf16_f32 v73, v82, v83\nv_mfma_f32_16x16x32_bf16 v[148:151], v[176:179], a[12:15], 0\nR 168,191,0\nv_cvt_pk_bf16_f32 v74, v84, v85\nv_cvt_pk_bf16_f32 v75, v86, v87\nM 148,180,28\n"
"buffer_store_dwordx4 v[24:27], v252, s[20:23], s100 offen offset:3072 sc1\nv_mfma_f32_16x16x32_bf16 v[132:135], v[176:179], a[8:11], 0\nR 170,191,4096\nv_cvt_pk_bf16_f32 v76, v88, v89\nM 132,180,24\nds_write_addtid_b32 v66 offset:512\n"
"v_cvt_pk_bf16_f32 v77, v90, v91\nM 116,176,4\nR 172,191,8192\nv_cvt_pk_bf16_f32 v78, v92, v93\nM 116,180,20\nds_write_addtid_b32 v67 offset:768\n"
"v_cvt_pk_bf16_f32 v79, v94, v95\nM 100,176,0\nR 174,191,12288\nds_write_addtid_b32 v68 offset:1024\nM 100,180,16\nds_write_addtid_b32 v69 offset:1280\n"
"ds_write_addtid_b32 v70 offset:1536\nG 10\nM 104,160,0\nR 176,188,16384\nds_write_addtid_b32 v71 offset:1792\nM 104,164,16\n"
"ds_write_addtid_b32 v72 offset:2048\nds_write_addtid_b32 v73 offset:2304\nM 120,160,4\nR 178,188,20480\nds_write_addtid_b32 v74 offset:2560\nM 120,164,20\n"
"ds_write_addtid_b32 v75 offset:2816\nds_write_addtid_b32 v76 offset:3072\nv_mfma_f32_16x16x32_bf16 v[136:139], v[160:163], a[8:11], 0\nR 180,188,24576\nds_write_addtid_b32 v77 offset:3328\nM 136,164,24\n"
"ds_write_addtid_b32 v78 offset:3584\nds_write_addtid_b32 v79 offset:3840\nv_mfma_f32_16x16x32_bf16 v[152:155], v[160:163], a[12:15], 0\nR 182,188,28672\nM 152,164,28\nG 15\n"
"v_mfma_f32_16x16x32_bf16 v[156:159], v[168:171], a[12:15], 0\nR 160,189,16384\ns_waitcnt vmcnt(15) lgkmcnt(2)\ns_barrier\nM 156,172,28\nds_read2st64_b32 v[64:65], v225 offset0:0 offset1:1\n"
"v_mfma_f32_16x16x32_bf16 v[140:143], v[168:171], a[8:11], 0\nR 162,189,20480\nM 140,172,24\nds_read2st64_b32 v[66:67], v225 offset0:2 offset1:3\nM 124,168,4\nR 164,189,24576\n"
"M 124,172,20\nds_read2st64_b32 v[68:69], v225 offset0:4 offset1:5\nM 108,168,0\nR 166,189,28672\nM 108,172,16\nds_read2st64_b32 v[70:71], v225 offset0:6 offset1:7\n"
"G 8\nM 96,176,32\nR 168,190,16384\nv_bfe_u32 v250, v252, 4, 2\nv_lshlrev_b32 v250, 3, v250\nM 96,180,48\n"
"s_lshl_b32 s60, s43, 5\nv_add_u32 v250, s60, v250\nG 5\nv_mfma_f32_16x16x32_bf16 v[32:35], v[208:211], v[64:67], v[32:35]\nv_mfma_f32_16x16x32_bf16 v[36:39], v[210:213], v[64:67], v[36:39]\nM 112,176,36\n"
"R 170,190,20480\nM 112,180,52\nds_read2st64_b32 v[72:73], v225 offset0:8 offset1:9\nM 128,176,40\nR 172,190,24576\nM 128,180,56\n"
"ds_read2st64_b32 v[74:75], v225 offset0:10 offset1:11\nG 5\nv_mfma_f32_16x16x32_bf16 v[40:43], v[208:211], v[68:71], v[40:43]\nv_mfma_f32_16x16x32_bf16 v[44:47], v[210:213], v[68:71], v[44:47]\nM 144,176,44\nR 174,190,28672\n"
"M 144,180,60\nds_read2st64_b32 v[76:77], v225 offset0:12 offset1:13\nM 148,160,44\nR 176,191,16384\nds_read2st64_b32 v[78:79], v225 offset0:14 offset1:15\nM 148,164,60\n"
"M 132,160,40\nR 178,191,20480\nM 132,164,56\nG 5\nv_mfma_f32_16x16x32_bf16 v[48:51], v[208:211], v[72:75], v[48:51]\nv_mfma_f32_16x16x32_bf16 v[52:55], v[210:213], v[72:75], v[52:55]\n"
"M 116,160,36\nR 180,191,24576\nM 116,164,52\nM 100,160,32\nR 182,191,28672\nG 3\n"
"v_mfma_f32_16x16x32_bf16 v[56:59], v[208:211], v[76:79], v[56:59]\nv_mfma_f32_16x16x32_bf16 v[60:63], v[210:213], v[76:79], v[60:63]\nM 100,164,48\nM 104,168,32\nR 160,188,32768\nM 104,172,48\n"
"M 120,168,36\nR 162,188,36864\nv_mov_b32 v226, s46\nM 120,172,52\ns_add_u32 m0, s50, 32768\nv_mov_b32 v227, s47\n"
"M 136,168,40\nR 164,188,40960\ns_bfe_u32 s62, s56, 0x20007\ns_lshl_b32 s62, s62, 5\ns_add_u32 s61, s74, 1009664\ns_add_u32 s61, s61, s62\n"
"s_waitcnt vmcnt(0)\ns_mov_b64 exec, 1\nbuffer_atomic_swap_x2 v[226:227], off, s[20:23], s61 sc1\ns_mov_b64 exec, -1\nv_cvt_pk_bf16_f32 v32, v32, v33\nv_cvt_pk_bf16_f32 v48, v48, v49\n"
"v_cvt_pk_bf16_f32 v56, v56, v57\nM 136,172,56\ns_bfe_u32 s62, s57, 0x20007\ns_lshl_b32 s62, s62, 5\ns_add_u32 s61, s74, 1009672\ns_add_u32 s61, s61, s62\n"
"s_mov_b64 exec, 1\nbuffer_atomic_swap_x2 v[226:227], off, s[20:23], s61 sc1\ns_mov_b64 exec, -1\nv_cvt_pk_bf16_f32 v49, v50, v51\nM 152,168,44\nR 166,188,45056\n"
"v_cvt_pk_bf16_f32 v50, v52, v53\nv_cvt_pk_bf16_f32 v33, v34, v35\nM 152,172,60\nG 4\nv_cvt_pk_bf16_f32 v34, v36, v37\nv_cvt_pk_bf16_f32 v35, v38, v39\n"
"M 156,176,44\nR 168,189,32768\ns_bfe_u32 s100, s58, 0x20007\ns_mul_i32 s100, s100, 49152\ns_add_u32 s100, s100, s72\ns_add_u32 s100, s100, 65044480\n"
"v_cvt_pk_bf16_f32 v40, v40, v41\nM 156,180,60\nbuffer_store_dwordx4 v[32:35], v252, s[20:23], s100 offen offset:0 sc1\nbuffer_load_dwordx4 v221, s[16:19], s63 offen offset:1024 lds\nM 140,176,40\nR 170,189,36864\n"
"s_add_u32 m0, s50, 40960\ns_add_u32 s63, s59, 327680\nM 140,180,56\nbuffer_load_dwordx4 v220, s[16:19], s63 offen lds\nM 124,176,36\nR 172,189,40960\n"
"v_cvt_pk_bf16_f32 v41, v42, v43\nM 124,180,52\nv_cvt_pk_bf16_f32 v42, v44, v45\nv_cvt_pk_bf16_f32 v43, v46, v47\nM 108,176,32\nR 174,189,45056\n"
"v_cvt_pk_bf16_f32 v51, v54, v55\nM 108,180,48\nbuffer_store_dwordx4 v[40:43], v252, s[20:23], s100 offen offset:1024 sc1\ns_barrier\nG 4\nM 96,160,64\n"
"R 176,190,32768\nv_cvt_pk_bf16_f32 v57, v58, v59\nM 96,164,80\nbuffer_load_dwordx4 v221, s[16:19], s63 offen offset:1024 lds\nM 112,160,68\nR 178,190,36864\n"
"s_add_u32 m0, s50, 49152\nM 112,164,84\nbuffer_store_dwordx4 v[48:51], v252, s[20:23], s100 offen offset:2048 sc1\nM 128,160,72\nR 180,190,40960\ns_add_u32 s63, s59, 851968\n"
"M 128,164,88\nbuffer_load_dwordx4 v220, s[16:19], s63 offen lds\nM 144,160,76\nR 182,190,45056\nv_cvt_pk_bf16_f32 v58, v60, v61\nM 144,164,92\n"
"G 4\nv_cvt_pk_bf16_f32 v59, v62, v63\nM 148,168,76\nR 160,191,32768\nM 148,172,92\nbuffer_store_dwordx4 v[56:59], v252, s[20:23], s100 offen offset:3072 sc1\n"
"buffer_load_dwordx4 v221, s[16:19], s63 offen offset:1024 lds\nM 132,168,72\nR 162,191,36864\nM 132,172,88\nM 116,168,68\nR 164,191,40960\n"
"M 116,172,84\nM 100,168,64\nR 166,191,45056\nv_mov_b32 v248, 0\nM 100,172,80\nG 4\n"
"v_mov_b32 v249, 0\nM 104,176,64\nR 168,188,49152\ns_add_u32 s61, s74, 1009664\ns_mov_b64 exec, 3\nbuffer_atomic_or_x2 v[248:249], v250, s[20:23], s61 offen sc0 sc1\n"
"s_mov_b64 exec, -1\nM 104,180,80\nM 120,176,68\nR 170,188,53248\nM 120,180,84\nM 136,176,72\n"
"R 172,184,0\nM 136,180,88\nM 152,176,76\nR 174,184,4096\nM 152,180,92\nG 4\n"
"M 156,160,76\nR 176,189,49152\nM 156,164,92\nM 140,160,72\nR 178,189,53248\nM 140,164,88\n"
"M 124,160,68\nR 180,185,0\nM 124,164,84\nM 108,160,64\nR 182,185,4096\nM 108,164,80\n"
"G 4\nM 96,168,96\nR 160,190,49152\nM 96,172,112\nM 112,168,100\nR 162,190,53248\n"
"M 112,172,116\nM 128,168,104\nR 164,186,0\nM 128,172,120\nM 144,168,108\nR 166,186,4096\n"
"M 144,172,124\nG 4\nM 148,176,108\nR 168,191,49152\nM 148,180,124\nM 132,176,104\n"
"R 170,191,53248\nM 132,180,120\nM 116,176,100\nR 172,187,0\nM 116,180,116\nM 100,176,96\n"
"R 174,187,4096\nM 100,180,112\nG 4\nM 104,160,96\nR 176,184,8192\ns_waitcnt vmcnt(8)\n"
"s_barrier\nM 104,164,112\nM 120,160,100\nR 178,184,12288\nM 120,164,116\nM 136,160,104\n"
"R 180,184,16384\nM 136,164,120\nM 152,160,108\nR 182,184,20480\nM 152,164,124\nG 4\n"
"M 156,168,108\nR 160,185,8192\nM 156,172,124\nM 140,168,104\nR 162,185,12288\nM 140,172,120\n"
"M 124,168,100\nR 164,185,16384\nM 124,172,116\nM 108,168,96\nR 166,185,20480\nv_mov_b32 v226, s46\n"
"M 108,172,112\nG 4\nv_mov_b32 v227, s47\nM 96,176,128\nR 168,186,8192\ns_bfe_u32 s62, s58, 0x20007\n"
"s_lshl_b32 s62, s62, 5\ns_add_u32 s61, s74, 1009680\ns_add_u32 s61, s61, s62\ns_waitcnt vmcnt(0)\ns_mov_b64 exec, 1\nbuffer_atomic_swap_x2 v[226:227], off, s[20:23], s61 sc1\n"
"s_mov_b64 exec, -1\nM 96,180,144\ns_mul_i32 s64, s43, 49152\nM 112,176,132\nR 170,186,12288\nM 112,180,148\n"
"s_add_u32 s64, s64, s72\nM 128,176,136\nR 172,186,16384\nv_cmp_eq_u64 s[86:87], s[46:47], v[248:249]\nM 128,180,152\ns_add_u32 s64, s64, 65011712\n"
"M 144,176,140\nR 174,186,20480\ns_and_b32 s62, s86, 3\ns_cmp_eq_u32 s62, 3\ns_cbranch_scc0 Lq957\nLq958:\n"
"M 144,180,156\nG 4\nM 148,160,140\nR 176,187,8192\nM 148,164,156\nbuffer_load_dwordx4 v[0:3], v252, s[20:23], s64 offen offset:0 sc1\n"
"M 132,160,136\nR 178,187,12288\nM 132,164,152\nbuffer_load_dwordx4 v[4:7], v252, s[20:23], s64 offen offset:1024 sc1\nM 116,160,132\nR 180,187,16384\n"
"M 116,164,148\ns_mul_i32 s65, s43, 49152\nM 100,160,128\nR 182,187,20480\nM 100,164,144\nbuffer_load_dwordx4 v[8:11], v252, s[20:23], s64 offen offset:2048 sc1\n"
"G 4\nM 104,168,128\nR 160,184,24576\nM 104,172,144\nbuffer_load_dwordx4 v[12:15], v252, s[20:23], s64 offen offset:3072 sc1\nM 120,168,132\n"
"R 162,184,28672\nM 120,172,148\ns_add_u32 s65, s65, s72\nM 136,168,136\nR 164,184,32768\ns_add_u32 s65, s65, 65028096\n"
"M 136,172,152\nbuffer_load_dwordx4 v[16:19], v252, s[20:23], s65 offen offset:0 sc1\nM 152,168,140\nR 166,184,36864\nM 152,172,156\nG 4\n"
"M 156,176,140\nR 168,185,24576\ns_barrier\nM 156,180,156\nbuffer_load_dwordx4 v[20:23], v252, s[20:23], s65 offen offset:1024 sc1\nM 140,176,136\n"
"R 170,185,28672\nv_mov_b32 v230, 0\nM 140,180,152\nbuffer_load_dwordx4 v[24:27], v252, s[20:23], s65 offen offset:2048 sc1\nv_mov_b32 v231, 0\nM 124,176,132\n"
"R 172,185,32768\ns_add_u32 s61, s74, 1009664\ns_mov_b64 exec, 4\nbuffer_atomic_or_x2 v[230:231], v250, s[20:23], s61 offen sc0 sc1\ns_mov_b64 exec, -1\nM 124,180,148\n"
"buffer_load_dwordx4 v[28:31], v252, s[20:23], s65 offen offset:3072 sc1\nM 108,176,128\nR 174,185,36864\nM 108,180,144\nG 4\nM 128,160,168\n"
"R 176,186,24576\nM 128,164,184\nR 178,186,28672\nM 144,160,172\nR 180,186,32768\nM 144,164,188\n"
"R 182,186,36864\nG 4\nM 148,168,172\nR 160,187,24576\nM 148,172,188\nR 162,187,28672\n"
"M 132,168,168\nR 164,187,32768\nM 132,172,184\nR 166,187,36864\nG 4\nM 136,176,168\n"
"R 168,184,40960\nM 136,180,184\nR 170,184,45056\nM 152,176,172\nR 172,184,49152\nM 152,180,188\n"
"R 174,184,53248\nG 4\nM 156,160,172\nR 176,185,40960\nM 156,164,188\nR 178,185,45056\n"
"M 140,160,168\nR 180,185,49152\nM 140,164,184\nR 182,185,53248\nG 4\nM 128,168,200\n"
"R 160,186,40960\nM 128,172,216\nR 162,186,45056\nM 144,168,204\nR 164,186,49152\nM 144,172,220\n"
"R 166,186,53248\nG 4\nM 148,176,204\nR 168,187,40960\nM 148,180,220\nR 170,187,45056\n"
"M 132,176,200\nR 172,187,49152\nM 132,180,216\nR 174,187,53248\nG 4\nM 136,160,200\n"
"R 176,184,24576\nLpg33:\ns_cbranch_execz LretW1z\nM 136,164,216\nR 178,184,28672\nM 152,160,204\n"
"R 180,184,32768\nM 152,164,220\nR 182,184,36864\nG 4\nM 156,168,204\nR 160,185,24576\n"
"M 156,172,220\nR 162,185,28672\nM 140,168,200\nR 164,185,32768\nM 140,172,216\nR 166,185,36864\n"
"G 4\nM 96,176,160\nR 168,186,24576\nM 96,180,176\nR 170,186,28672\ns_mov_b32 m0, s51\n"
"M 112,176,164\nR 172,186,32768\nv_cvt_pk_bf16_f32 v128, v128, v129\nv_cvt_pk_bf16_f32 v129, v130, v131\nM 112,180,180\nR 174,186,36864\n"
"G 4\nv_cvt_pk_bf16_f32 v130, v132, v133\nM 116,160,164\nR 176,187,24576\ns_mul_i32 s66, s43, 49152\nv_cvt_pk_bf16_f32 v131, v134, v135\n"
"M 116,164,180\nR 178,187,28672\ns_add_u32 s66, s66, s72\nv_cvt_pk_bf16_f32 v132, v136, v137\nM 100,160,160\nR 180,187,32768\n"
"s_add_u32 s66, s66, 65044480\nv_cvt_pk_bf16_f32 v133, v138, v139\nM 100,164,176\nR 182,187,36864\nG 4\nv_cvt_pk_bf16_f32 v134, v140, v141\n"
"M 104,168,160\nR 160,184,40960\ns_waitcnt vmcnt(1)\nv_cmp_eq_u64 s[86:87], s[46:47], v[230:231]\nv_cvt_pk_bf16_f32 v135, v142, v143\nM 104,172,176\n"
"R 162,184,45056\nv_cvt_pk_bf16_f32 v136, v144, v145\nv_cvt_pk_bf16_f32 v137, v146, v147\nM 120,168,164\nR 164,184,49152\ns_and_b32 s62, s86, 4\n"
"s_cmp_eq_u32 s62, 4\ns_cbranch_scc0 Lq973\nLq974:\nv_cvt_pk_bf16_f32 v138, v148, v149\nM 120,172,180\nR 166,184,53248\n"
"G 4\nv_cvt_pk_bf16_f32 v139, v150, v151\nM 124,176,164\nR 168,185,40960\nbuffer_load_dwordx4 v[64:67], v252, s[20:23], s66 offen offset:0 sc1\nv_cvt_pk_bf16_f32 v140, v152, v153\n"
"M 124,180,180\nR 170,185,45056\nv_cvt_pk_bf16_f32 v141, v154, v155\nv_cvt_pk_bf16_f32 v142, v156, v157\nM 108,176,160\nR 172,185,49152\n"
"buffer_load_dwordx4 v[68:71], v252, s[20:23], s66 offen offset:1024 sc1\nds_write_addtid_b32 v128 offset:0\nds_write_addtid_b32 v129 offset:256\nds_write_addtid_b32 v130 offset:512\nds_write_addtid_b32 v131 offset:768\nds_write_addtid_b32 v132 offset:1024\n"
"ds_write_addtid_b32 v133 offset:1280\nds_write_addtid_b32 v134 offset:1536\nds_write_addtid_b32 v135 offset:1792\nds_write_addtid_b32 v136 offset:2048\nds_write_addtid_b32 v137 offset:2304\nds_write_addtid_b32 v138 offset:2560\n"
"ds_write_addtid_b32 v139 offset:2816\nds_write_addtid_b32 v140 offset:3072\nds_write_addtid_b32 v141 offset:3328\nds_write_addtid_b32 v142 offset:3584\nv_cvt_pk_bf16_f32 v143, v158, v159\nds_write_addtid_b32 v143 offset:3840\n"
"M 108,180,176\nR 174,185,53248\nG 15\nM 96,160,192\nR 176,186,40960\nM 96,164,208\n"
"R 178,186,45056\nbuffer_load_dwordx4 v[72:75], v252, s[20:23], s66 offen offset:2048 sc1\nG 3\ns_barrier\nM 112,160,196\nR 180,186,49152\n"
"ds_read2st64_b32 v[128:129], v225 offset0:0 offset1:1\nM 112,164,212\nR 182,186,53248\nds_read2st64_b32 v[130:131], v225 offset0:2 offset1:3\nG 6\nM 116,168,196\n"
"R 160,187,40960\nbuffer_load_dwordx4 v[76:79], v252, s[20:23], s66 offen offset:3072 sc1\nds_read2st64_b32 v[132:133], v225 offset0:4 offset1:5\nM 116,172,212\nR 162,187,45056\nds_read2st64_b32 v[134:135], v225 offset0:6 offset1:7\n"
"M 100,168,192\nR 164,187,49152\nds_read2st64_b32 v[136:137], v225 offset0:8 offset1:9\nM 100,172,208\nR 166,187,53248\nds_read2st64_b32 v[138:139], v225 offset0:10 offset1:11\n"
"G 9\nM 104,176,192\nds_read2st64_b32 v[140:141], v225 offset0:12 offset1:13\nds_read2st64_b32 v[142:143], v225 offset0:14 offset1:15\nM 104,180,208\nM 120,176,196\n"
"M 120,180,212\nG 3\nM 124,160,196\nM 124,164,212\nM 108,160,192\nM 108,164,208\n"
"v_mfma_f32_16x16x32_bf16 v[96:99], v[208:211], v[128:131], v[96:99]\nv_mfma_f32_16x16x32_bf16 v[100:103], v[210:213], v[128:131], v[100:103]\nv_mfma_f32_16x16x32_bf16 v[104:107], v[208:211], v[132:135], v[104:107]\nv_mfma_f32_16x16x32_bf16 v[108:111], v[210:213], v[132:135], v[108:111]\nG 2\nv_mfma_f32_16x16x32_bf16 v[112:115], v[208:211], v[136:139], v[112:115]\n"
"v_mfma_f32_16x16x32_bf16 v[116:119], v[210:213], v[136:139], v[116:119]\nG 0\nv_mfma_f32_16x16x32_bf16 v[120:123], v[208:211], v[140:143], v[120:123]\nv_mfma_f32_16x16x32_bf16 v[124:127], v[210:213], v[140:143], v[124:127]\nv_mfma_f32_16x16x32_bf16 v[96:99], v[208:211], v[0:3], v[96:99]\nv_mfma_f32_16x16x32_bf16 v[100:103], v[210:213], v[0:3], v[100:103]\n"
"v_mfma_f32_16x16x32_bf16 v[104:107], v[208:211], v[4:7], v[104:107]\nv_mfma_f32_16x16x32_bf16 v[108:111], v[210:213], v[4:7], v[108:111]\nv_mfma_f32_16x16x32_bf16 v[112:115], v[208:211], v[8:11], v[112:115]\nv_mfma_f32_16x16x32_bf16 v[116:119], v[210:213], v[8:11], v[116:119]\nv_mfma_f32_16x16x32_bf16 v[120:123], v[208:211], v[12:15], v[120:123]\nv_mfma_f32_16x16x32_bf16 v[124:127], v[210:213], v[12:15], v[124:127]\n"
"v_mfma_f32_16x16x32_bf16 v[96:99], v[208:211], v[16:19], v[96:99]\nv_mfma_f32_16x16x32_bf16 v[100:103], v[210:213], v[16:19], v[100:103]\nv_mfma_f32_16x16x32_bf16 v[104:107], v[208:211], v[20:23], v[104:107]\nv_mfma_f32_16x16x32_bf16 v[108:111], v[210:213], v[20:23], v[108:111]\nv_mfma_f32_16x16x32_bf16 v[112:115], v[208:211], v[24:27], v[112:115]\nv_mfma_f32_16x16x32_bf16 v[116:119], v[210:213], v[24:27], v[116:119]\n"
"s_waitcnt vmcnt(4)\nv_mfma_f32_16x16x32_bf16 v[120:123], v[208:211], v[28:31], v[120:123]\nv_mfma_f32_16x16x32_bf16 v[124:127], v[210:213], v[28:31], v[124:127]\ns_waitcnt vmcnt(3)\nv_mfma_f32_16x16x32_bf16 v[96:99], v[208:211], v[64:67], v[96:99]\nv_mfma_f32_16x16x32_bf16 v[100:103], v[210:213], v[64:67], v[100:103]\n"
"s_waitcnt vmcnt(2)\nv_mfma_f32_16x16x32_bf16 v[104:107], v[208:211], v[68:71], v[104:107]\nv_mfma_f32_16x16x32_bf16 v[108:111], v[210:213], v[68:71], v[108:111]\ns_waitcnt vmcnt(1)\nv_mfma_f32_16x16x32_bf16 v[112:115], v[208:211], v[72:75], v[112:115]\nv_mfma_f32_16x16x32_bf16 v[116:119], v[210:213], v[72:75], v[116:119]\n"
"s_waitcnt vmcnt(0)\nv_mfma_f32_16x16x32_bf16 v[120:123], v[208:211], v[76:79], v[120:123]\nv_mfma_f32_16x16x32_bf16 v[124:127], v[210:213], v[76:79], v[124:127]\nv_cvt_pk_bf16_f32 v192, v96, v97\nv_cvt_pk_bf16_f32 v193, v98, v99\nv_cvt_pk_bf16_f32 v194, v100, v101\n"
"v_cvt_pk_bf16_f32 v195, v102, v103\nv_cvt_pk_bf16_f32 v196, v104, v105\nv_cvt_pk_bf16_f32 v197, v106, v107\nv_cvt_pk_bf16_f32 v198, v108, v109\nv_cvt_pk_bf16_f32 v199, v110, v111\nv_cvt_pk_bf16_f32 v200, v112, v113\n"
"v_cvt_pk_bf16_f32 v201, v114, v115\nv_cvt_pk_bf16_f32 v202, v116, v117\nv_cvt_pk_bf16_f32 v203, v118, v119\nv_cvt_pk_bf16_f32 v204, v120, v121\nv_cvt_pk_bf16_f32 v205, v122, v123\nv_cvt_pk_bf16_f32 v206, v124, v125\n"
"v_cvt_pk_bf16_f32 v207, v126, v127\nv_and_b32 v230, 0xf0, v252\nv_lshlrev_b32 v230, 7, v230\nv_bfe_u32 v231, v252, 8, 1\nv_lshl_add_u32 v230, v231, 5, v230\nv_lshrrev_b32 v231, 9, v252\n"
"v_lshl_add_u32 v230, v231, 4, v230\ns_add_u32 s61, s49, s53\ns_add_u32 s62, s61, 32768\nv_permlane16_swap_b32 v192, v194\nv_permlane16_swap_b32 v193, v195\nbuffer_store_dwordx4 v[192:195], v230, s[28:31], s61 offen offset:0\n"
"v_permlane16_swap_b32 v196, v198\nv_permlane16_swap_b32 v197, v199\nbuffer_store_dwordx4 v[196:199], v230, s[28:31], s61 offen offset:64\nv_permlane16_swap_b32 v200, v202\nv_permlane16_swap_b32 v201, v203\nbuffer_store_dwordx4 v[200:203], v230, s[28:31], s62 offen offset:0\n"
"v_permlane16_swap_b32 v204, v206\nv_permlane16_swap_b32 v205, v207\nbuffer_store_dwordx4 v[204:207], v230, s[28:31], s62 offen offset:64\ns_add_u32 s60, s83, 1\nv_mov_b32 v234, s60\ns_or_b32 s60, s45, s40\n"
"s_cmp_eq_u32 s60, 0\ns_cselect_b64 exec, 1, 0\nbuffer_store_dword v234, off, s[20:23], 0 sc1\ns_mov_b64 exec, -1\ns_endpgm\nLqF:\n"
"Lq639:\ns_lshr_b32 s84, s49, 4\ns_add_u32 s84, s84, 82837504\nbuffer_load_dwordx4 v[128:131], v252, s[20:23], s84 offen offset:0 sc1\nbuffer_load_dwordx4 v[132:135], v252, s[20:23], s84 offen offset:1024 sc1\nbuffer_load_dwordx4 v[136:139], v252, s[20:23], s84 offen offset:2048 sc1\n"
"buffer_load_dwordx4 v[140:143], v252, s[20:23], s84 offen offset:3072 sc1\ns_mov_b32 s85, 0x0c0c0400\ns_waitcnt vmcnt(3)\nv_perm_b32 v144, v129, v128, s85\nv_perm_b32 v148, v131, v130, s85\nv_lshl_or_b32 v144, v148, 16, v144\n"
"s_waitcnt vmcnt(2)\nv_perm_b32 v145, v133, v132, s85\nv_perm_b32 v149, v135, v134, s85\nv_lshl_or_b32 v145, v149, 16, v145\ns_waitcnt vmcnt(1)\nv_perm_b32 v146, v137, v136, s85\n"
"v_perm_b32 v150, v139, v138, s85\nv_lshl_or_b32 v146, v150, 16, v146\ns_waitcnt vmcnt(0)\nv_perm_b32 v147, v141, v140, s85\nv_perm_b32 v151, v143, v142, s85\nv_lshl_or_b32 v147, v151, 16, v147\n"
"v_min3_u32 v230, v144, v145, v146\nv_min3_u32 v230, v230, v147, v147\nv_max3_u32 v231, v144, v145, v146\nv_max3_u32 v231, v231, v147, v147\nv_cmp_ne_u32 s[86:87], s46, v230\nv_cmp_ne_u32 s[88:89], s46, v231\n"
"s_nop 0\ns_or_b64 s[86:87], s[86:87], s[88:89]\ns_cbranch_scc0 Lq640\ns_sub_u32 s48, s48, 1\ns_cmp_gt_i32 s48, 0\ns_cbranch_scc1 Lq639\n"
"s_branch Lq640\nLq686:\ns_mov_b64 exec, s[66:67]\nbuffer_load_dwordx4 v[230:233], v252, s[20:23], s65 offen offset:0 sc1\nbuffer_load_dwordx2 v[64:65], v227, s[20:23], s65 offen offset:2048 sc1\nbuffer_load_dwordx2 v[70:71], v227, s[20:23], s65 offen offset:2560 sc1\n"
"s_mov_b64 exec, -1\ns_waitcnt vmcnt(0)\nv_max3_u32 v248, v233, v65, v71\nv_cmp_eq_u32 vcc, -1, v248\ns_nop 1\ns_cbranch_vccz Lq687\n"
"s_sub_u32 s48, s48, 1\ns_cmp_gt_i32 s48, 0\ns_cbranch_scc1 Lq686\ns_branch Lq687\nLq688:\ns_mov_b64 exec, s[66:67]\n"
"buffer_load_dwordx4 v[234:237], v252, s[20:23], s65 offen offset:1024 sc1\nbuffer_load_dwordx2 v[76:77], v227, s[20:23], s65 offen offset:3072 sc1\nbuffer_load_dwordx2 v[82:83], v227, s[20:23], s65 offen offset:3584 sc1\ns_mov_b64 exec, -1\ns_waitcnt vmcnt(0)\nv_max3_u32 v248, v237, v77, v83\n"
"v_cmp_eq_u32 vcc, -1, v248\ns_nop 1\ns_cbranch_vccz Lq689\ns_sub_u32 s48, s48, 1\ns_cmp_gt_i32 s48, 0\ns_cbranch_scc1 Lq688\n"
"s_branch Lq689\nLq808:\ns_lshr_b32 s84, s49, 4\ns_add_u32 s84, s84, 83361792\nbuffer_load_dwordx4 v[0:3], v252, s[20:23], s84 offen offset:0 sc1\nbuffer_load_dwordx4 v[4:7], v252, s[20:23], s84 offen offset:1024 sc1\n"
"buffer_load_dwordx4 v[8:11], v252, s[20:23], s84 offen offset:2048 sc1\nbuffer_load_dwordx4 v[12:15], v252, s[20:23], s84 offen offset:3072 sc1\ns_mov_b32 s85, 0x0c0c0400\ns_waitcnt vmcnt(3)\nv_perm_b32 v16, v1, v0, s85\nv_perm_b32 v20, v3, v2, s85\n"
"v_lshl_or_b32 v16, v20, 16, v16\ns_waitcnt vmcnt(2)\nv_perm_b32 v17, v5, v4, s85\nv_perm_b32 v21, v7, v6, s85\nv_lshl_or_b32 v17, v21, 16, v17\ns_waitcnt vmcnt(1)\n"
"v_perm_b32 v18, v9, v8, s85\nv_perm_b32 v22, v11, v10, s85\nv_lshl_or_b32 v18, v22, 16, v18\ns_waitcnt vmcnt(0)\nv_perm_b32 v19, v13, v12, s85\nv_perm_b32 v23, v15, v14, s85\n"
"v_lshl_or_b32 v19, v23, 16, v19\nv_min3_u32 v230, v16, v17, v18\nv_min3_u32 v230, v230, v19, v19\nv_max3_u32 v231, v16, v17, v18\nv_max3_u32 v231, v231, v19, v19\nv_cmp_ne_u32 s[86:87], s46, v230\n"
"v_cmp_ne_u32 s[88:89], s46, v231\ns_nop 0\ns_or_b64 s[86:87], s[86:87], s[88:89]\ns_cbranch_scc0 Lq809\ns_sub_u32 s48, s48, 1\ns_cmp_gt_i32 s48, 0\n"
"s_cbranch_scc1 Lq808\ns_branch Lq809\nLq828:\nv_mov_b32 v248, 0\nv_mov_b32 v249, 0\ns_add_u32 s61, s82, 845824\n"
"s_mov_b64 exec, 0xf\nbuffer_atomic_or_x2 v[248:249], v250, s[20:23], s61 offen sc0 sc1\ns_mov_b64 exec, -1\ns_waitcnt vmcnt(0)\nv_cmp_eq_u64 s[86:87], s[46:47], v[248:249]\ns_nop 1\n"
"s_and_b32 s62, s86, 0xf\ns_cmp_eq_u32 s62, 0xf\ns_cbranch_scc1 Lq829\ns_sleep 1\ns_sub_u32 s48, s48, 1\ns_cmp_gt_i32 s48, 0\n"
"s_cbranch_scc1 Lq828\ns_branch Lq829\nLq863:\nv_mov_b32 v248, 0\nv_mov_b32 v249, 0\ns_add_u32 s61, s82, 845832\n"
"s_mov_b64 exec, 0xf\nbuffer_atomic_or_x2 v[248:249], v250, s[20:23], s61 offen sc0 sc1\ns_mov_b64 exec, -1\ns_waitcnt vmcnt(0)\nv_cmp_eq_u64 s[86:87], s[46:47], v[248:249]\ns_nop 1\n"
"s_and_b32 s62, s86, 0xf\ns_cmp_eq_u32 s62, 0xf\ns_cbranch_scc1 Lq864\ns_sleep 1\ns_sub_u32 s48, s48, 1\ns_cmp_gt_i32 s48, 0\n"
"s_cbranch_scc1 Lq863\ns_branch Lq864\nLq898:\nv_mov_b32 v248, 0\nv_mov_b32 v249, 0\ns_add_u32 s61, s82, 845848\n"
"s_mov_b64 exec, 0xf\nbuffer_atomic_or_x2 v[248:249], v250, s[20:23], s61 offen sc0 sc1\ns_mov_b64 exec, -1\ns_waitcnt vmcnt(0)\nv_cmp_eq_u64 s[86:87], s[46:47], v[248:249]\ns_nop 1\n"
"s_and_b32 s62, s86, 0xf\ns_cmp_eq_u32 s62, 0xf\ns_cbranch_scc1 Lq899\ns_sleep 1\ns_sub_u32 s48, s48, 1\ns_cmp_gt_i32 s48, 0\n"
"s_cbranch_scc1 Lq898\ns_branch Lq899\nLq917:\nv_mov_b32 v248, 0\nv_mov_b32 v249, 0\ns_add_u32 s61, s82, 845840\n"
"s_mov_b64 exec, 0xf\nbuffer_atomic_or_x2 v[248:249], v250, s[20:23], s61 offen sc0 sc1\ns_mov_b64 exec, -1\ns_waitcnt vmcnt(0)\nv_cmp_eq_u64 s[86:87], s[46:47], v[248:249]\ns_nop 1\n"
"s_and_b32 s62, s86, 0xf\ns_cmp_eq_u32 s62, 0xf\ns_cbranch_scc1 Lq918\ns_sleep 1\ns_sub_u32 s48, s48, 1\ns_cmp_gt_i32 s48, 0\n"
"s_cbranch_scc1 Lq917\ns_branch Lq918\nLq957:\nv_mov_b32 v248, 0\nv_mov_b32 v249, 0\ns_add_u32 s61, s74, 1009664\n"
"s_mov_b64 exec, 3\nbuffer_atomic_or_x2 v[248:249], v250, s[20:23], s61 offen sc0 sc1\ns_mov_b64 exec, -1\ns_waitcnt vmcnt(0)\nv_cmp_eq_u64 s[86:87], s[46:47], v[248:249]\ns_nop 1\n"
"s_and_b32 s62, s86, 3\ns_cmp_eq_u32 s62, 3\ns_cbranch_scc1 Lq958\ns_sleep 1\ns_sub_u32 s48, s48, 1\ns_cmp_gt_i32 s48, 0\n"
"s_cbranch_scc1 Lq957\ns_branch Lq958\nLq973:\nv_mov_b32 v230, 0\nv_mov_b32 v231, 0\ns_add_u32 s61, s74, 1009664\n"
"s_mov_b64 exec, 4\nbuffer_atomic_or_x2 v[230:231], v250, s[20:23], s61 offen sc0 sc1\ns_mov_b64 exec, -1\ns_waitcnt vmcnt(0)\nv_cmp_eq_u64 s[86:87], s[46:47], v[230:231]\ns_nop 1\n"
"s_and_b32 s62, s86, 4\ns_cmp_eq_u32 s62, 4\ns_cbranch_scc1 Lq974\ns_sleep 1\ns_sub_u32 s48, s48, 1\ns_cmp_gt_i32 s48, 0\n"
"s_cbranch_scc1 Lq973\ns_branch Lq974\nLqE:\n"
  : "+{v0}"(tid), "+{s[92:93]}"(ka), "+{s45}"(bid) :
  : "a0", "a1", "a2", "a3", "a4", "a5", "a6", "a7", "a8", "a9", "a10", "a11", "a12", "a13", "a14", "a15", "a16", "a17", "a18", "a19", "a20", "a21", "a22", "a23", "a24", "a25", "a26", "a27", "a28", "a29", "a30", "a31", "a32", "a33", "a34", "a35", "a36", "a37", "a38", "a39", "a40", "a41", "a42", "a43", "a44", "a45", "a46", "a47", "a48", "a49", "a50", "a51", "a52", "a53", "a54", "a55", "a56", "a57", "a58", "a59", "a60", "a61", "a62", "a63", "a64", "a65", "a66", "a67", "a68", "a69", "a70", "a71", "a72", "a73", "a74", "a75", "a76", "a77", "a78", "a79", "a80", "a81", "a82", "a83", "a84", "a85", "a86", "a87", "a88", "a89", "a90", "a91", "a92", "a93", "a94", "a95", "a96", "a97", "a98", "a99", "a100", "a101", "a102", "a103", "a104", "a105", "a106", "a107", "a108", "a109", "a110", "a111", "a112", "a113", "a114", "a115", "a116", "a117", "a118", "a119", "a120", "a121", "a122", "a123", "a124", "a125", "a126", "a127", "a128", "a129", "a130", "a131", "a132", "a133", "a134", "a135", "a136", "a137", "a138", "a139", "a140", "a141", "a142", "a143", "a144", "a145", "a146", "a147", "a148", "a149", "a150", "a151", "a152", "a153", "a154", "a155", "a156", "a157", "a158", "a159", "a160", "a161", "a162", "a163", "a164", "a165", "a166", "a167", "a168", "a169", "a170", "a171", "a172", "a173", "a174", "a175", "a176", "a177", "a178", "a179", "a180", "a181", "a182", "a183", "a184", "a185", "a186", "a187", "a188", "a189", "a190", "a191", "a192", "a193", "a194", "a195", "a196", "a197", "a198", "a199", "a200", "a201", "a202", "a203", "a204", "a205", "a206", "a207", "a208", "a209", "a210", "a211", "a212", "a213", "a214", "a215", "a216", "a217", "a218", "a219", "a220", "a221", "a222", "a223", "a224", "a225", "a226", "a227", "a228", "a229", "a230", "a231", "a232", "a233", "a234", "a235", "a236", "a237", "a238", "a239", "a240", "a241", "a242", "a243", "a244", "a245", "a246", "a247", "a248", "a249", "a250", "a251", "a252", "a253", "a254", "a255", "v1", "v2", "v3", "v4", "v5", "v6", "v7", "v8", "v9", "v10", "v11", "v12", "v13", "v14", "v15", "v16", "v17", "v18", "v19", "v20", "v21", "v22", "v23", "v24", "v25", "v26", "v27", "v28", "v29", "v30", "v31", "v32", "v33", "v34", "v35", "v36", "v37", "v38", "v39", "v40", "v41", "v42", "v43", "v44", "v45", "v46", "v47", "v48", "v49", "v50", "v51", "v52", "v53", "v54", "v55", "v56", "v57", "v58", "v59", "v60", "v61", "v62", "v63", "v64", "v65", "v66", "v67", "v68", "v69", "v70", "v71", "v72", "v73", "v74", "v75", "v76", "v77", "v78", "v79", "v80", "v81", "v82", "v83", "v84", "v85", "v86", "v87", "v88", "v89", "v90", "v91", "v92", "v93", "v94", "v95", "v96", "v97", "v98", "v99", "v100", "v101", "v102", "v103", "v104", "v105", "v106", "v107", "v108", "v109", "v110", "v111", "v112", "v113", "v114", "v115", "v116", "v117", "v118", "v119", "v120", "v121", "v122", "v123", "v124", "v125", "v126", "v127", "v128", "v129", "v130", "v131", "v132", "v133", "v134", "v135", "v136", "v137", "v138", "v139", "v140", "v141", "v142", "v143", "v144", "v145", "v146", "v147", "v148", "v149", "v150", "v151", "v152", "v153", "v154", "v155", "v156", "v157", "v158", "v159", "v160", "v161", "v162", "v163", "v164", "v165", "v166", "v167", "v168", "v169", "v170", "v171", "v172", "v173", "v174", "v175", "v176", "v177", "v178", "v179", "v180", "v181", "v182", "v183", "v184", "v185", "v186", "v187", "v188", "v189", "v190", "v191", "v192", "v193", "v194", "v195", "v196", "v197", "v198", "v199", "v200", "v201", "v202", "v203", "v204", "v205", "v206", "v207", "v208", "v209", "v210", "v211", "v212", "v213", "v214", "v215", "v216", "v217", "v218", "v219", "v220", "v221", "v222", "v223", "v224", "v225", "v226", "v227", "v228", "v229", "v230", "v231", "v232", "v233", "v234", "v235", "v236", "v237", "v238", "v239", "v240", "v241", "v242", "v243", "v244", "v245", "v246", "v247", "v248", "v249", "v250", "v251", "v252", "v253", "v254", "v255", "s4", "s5", "s6", "s7", "s8", "s9", "s10", "s11", "s12", "s13", "s14", "s15", "s16", "s17", "s18", "s19", "s20", "s21", "s22", "s23", "s24", "s25", "s26", "s27", "s28", "s29", "s30", "s31", "s32", "s33", "s34", "s35", "s36", "s37", "s38", "s39", "s40", "s41", "s42", "s43", "s44", "s46", "s47", "s48", "s49", "s50", "s51", "s52", "s53", "s54", "s55", "s56", "s57", "s58", "s59", "s60", "s61", "s62", "s63", "s64", "s65", "s66", "s67", "s68", "s69", "s70", "s71", "s72", "s73", "s74", "s75", "s76", "s77", "s78", "s79", "s80", "s81", "s82", "s83", "s84", "s85", "s86", "s87", "s88", "s89", "s90", "s91", "s94", "s95", "s96", "s97", "s98", "s99", "s100", "s101", "vcc", "m0", "scc", "memory");
#endif
}
extern "C" void kernel_launch(void* const* d_in, const int* in_sizes, int n_in, void* d_out, int out_size, void* d_ws, size_t ws_size, hipStream_t stream) {
  (void)hipFuncSetAttribute((const void*)layers, hipFuncAttributeMaxDynamicSharedMemorySize, 160 * 1024);
  hipLaunchKernelGGL(layers, dim3(256), dim3(256), 160 * 1024, stream, d_in[0], d_in[1], d_in[2], d_in[4], d_in[5], d_in[6], d_in[7], d_out, d_ws);
}
```

```cpp
#include <hip/hip_runtime.h>
__global__ __launch_bounds__(256, 1) __attribute__((aligned(4096))) void layers(const void* x, const void* wqkv, const void* wo, const void* w1, const void* b1, const void* w2, const void* b2, void* out, void* ws) {
#if defined(__HIP_DEVICE_COMPILE__)
  int tid = threadIdx.x, bid = blockIdx.x;
  unsigned long long ka = (unsigned long long)__builtin_amdgcn_kernarg_segment_ptr();
  asm volatile(
".macro M d,w,a\nv_mfma_f32_16x16x32_bf16 v[\\d:\\d+3], v[\\w:\\w+3], a[\\a:\\a+3], v[\\d:\\d+3]\n.endm\n.macro N d,a,w\nv_mfma_f32_16x16x32_bf16 v[\\d:\\d+3], a[\\a:\\a+3], v[\\w:\\w+3], v[\\d:\\d+3]\n.endm\n"
".macro R d,b,o\nds_read_b64_tr_b16 v[\\d:\\d+1], v\\b offset:\\o\n.endm\n.macro G n\ns_waitcnt lgkmcnt(\\n)\n.endm\n"
"s_load_dwordx2 s[24:25], s[92:93], 0\ns_load_dwordx2 s[4:5], s[92:93], 8\ns_load_dwordx2 s[8:9], s[92:93], 16\ns_load_dwordx2 s[12:13], s[92:93], 24\ns_load_dwordx2 s[32:33], s[92:93], 32\ns_load_dwordx2 s[16:17], s[92:93], 40\n"
"s_load_dwordx2 s[36:37], s[92:93], 48\ns_load_dwordx2 s[28:29], s[92:93], 56\ns_load_dwordx2 s[20:21], s[92:93], 64\nv_lshrrev_b32 v1, 6, v0\nv_and_b32 v0, 63, v0\nv_readfirstlane_b32 s40, v1\n"
"v_lshlrev_b32 v252, 4, v0\ns_getpc_b64 s[98:99]\nLqPC:\ns_lshr_b32 s60, s45, 3\ns_lshl2_add_u32 s60, s60, s40\ns_lshl_b32 s60, s60, 8\n"
"v_lshl_add_u32 v224, v252, 3, s60\nv_add_u32 v224, LqPC-LqPC, v224\nv_min_u32 v224, LqE-LqPC-4, v224\ns_mov_b64 exec, 3\nglobal_load_dword v224, v224, s[98:99]\ns_mov_b64 exec, -1\n"
"s_mov_b32 s6, 6291456\ns_mov_b32 s7, 0x20000\ns_mov_b32 s10, 2097152\ns_mov_b32 s11, 0x20000\ns_mov_b32 s14, 7340032\ns_mov_b32 s15, 0x20000\n"
"s_mov_b32 s18, 7340032\ns_mov_b32 s19, 0x20000\ns_mov_b32 s22, 89128960\ns_mov_b32 s23, 0x20000\ns_mov_b32 s26, 4194304\ns_mov_b32 s27, 0x20000\n"
"s_mov_b32 s30, 4194304\ns_mov_b32 s31, 0x20000\ns_mov_b32 s34, 7168\ns_mov_b32 s35, 0x20000\ns_mov_b32 s38, 2048\ns_mov_b32 s39, 0x20000\n"
"s_bfe_u32 s41, s45, 0x30003\ns_and_b32 s60, s45, 1\ns_lshl3_add_u32 s41, s60, s41\ns_bfe_u32 s43, s45, 0x20001\ns_lshr_b32 s44, s45, 6\ns_lshl2_add_u32 s42, s43, s44\n"
"s_lshl2_add_u32 s75, s44, s43\ns_and_b32 s90, s40, 1\ns_lshl2_add_u32 s60, s41, s40\ns_lshl_b32 s49, s60, 16\ns_lshl_b32 s50, s40, 11\ns_add_u32 s50, s50, 32768\n"
"s_lshl_b32 s52, s42, 7\ns_lshl_b32 s51, s40, 13\ns_lshl_b32 s60, s90, 10\ns_add_u32 s70, s49, s60\ns_xor_b32 s71, s70, 0x10000\ns_mul_i32 s101, s41, 196608\n"
"s_add_u32 s101, s101, s52\ns_lshl_b32 s53, s75, 7\nLqq0:\nv_lshrrev_b32 v96, 7, v252\nv_lshlrev_b32 v96, 11, v96\nv_lshrrev_b32 v97, 8, v252\n"
"v_lshlrev_b32 v97, 4, v97\nv_and_b32 v98, 0x70, v252\nv_xor_b32 v98, v98, v97\nv_add_u32 v226, v96, v98\nv_xor_b32 v227, 64, v226\nG 0\n"
"s_add_u32 m0, s51, 0\ns_add_u32 s60, s70, 0\nbuffer_load_dwordx4 v226, s[24:27], s60 offen lds\ns_add_u32 m0, s51, 1024\ns_add_u32 s60, s70, 16384\nbuffer_load_dwordx4 v227, s[24:27], s60 offen lds\n"
"s_add_u32 m0, s51, 2048\ns_add_u32 s60, s70, 32768\nbuffer_load_dwordx4 v226, s[24:27], s60 offen lds\ns_add_u32 m0, s51, 3072\ns_add_u32 s60, s70, 49152\nbuffer_load_dwordx4 v227, s[24:27], s60 offen lds\n"
"s_add_u32 m0, s51, 4096\ns_add_u32 s60, s71, 0\nbuffer_load_dwordx4 v226, s[24:27], s60 offen lds\ns_add_u32 m0, s51, 5120\ns_add_u32 s60, s71, 16384\nbuffer_load_dwordx4 v227, s[24:27], s60 offen lds\n"
"s_add_u32 m0, s51, 6144\ns_add_u32 s60, s71, 32768\nbuffer_load_dwordx4 v226, s[24:27], s60 offen lds\ns_add_u32 m0, s51, 7168\ns_add_u32 s60, s71, 49152\nbuffer_load_dwordx4 v227, s[24:27], s60 offen lds\n"
"buffer_load_dwordx2 v[230:231], off, s[20:23], 0 sc1\nv_bfe_u32 v96, v252, 5, 2\nv_bfe_u32 v97, v252, 8, 2\nv_xor_b32 v96, v96, v97\ns_and_b32 s60, s40, 1\ns_lshl_b32 s60, s60, 4\n"
"s_and_b32 s61, s40, 2\ns_lshl1_add_u32 s60, s61, s60\nv_bfe_u32 v97, v252, 7, 2\nv_lshrrev_b32 v98, 9, v252\nv_lshl_add_u32 v97, v98, 3, v97\nv_add_u32 v97, s60, v97\n"
"v_mul_u32_u24 v97, 6144, v97\nv_lshl_add_u32 v97, v96, 5, v97\nv_and_b32 v96, 16, v252\nv_add_u32 v220, v96, v97\nv_add_u32 v221, 3144704, v220\ns_add_u32 m0, s50, 0\n"
"s_add_u32 s63, s52, 2048\nbuffer_load_dwordx4 v220, s[4:7], s63 offen lds\nbuffer_load_dwordx4 v221, s[4:7], s63 offen offset:1024 lds\ns_add_u32 m0, s50, 8192\ns_add_u32 s63, s52, 198656\nbuffer_load_dwordx4 v220, s[4:7], s63 offen lds\n"
"buffer_load_dwordx4 v221, s[4:7], s63 offen offset:1024 lds\ns_lshl2_add_u32 s60, s41, s40\ns_mul_i32 s79, s60, 2176\ns_and_b32 s61, s60, -2\ns_mul_i32 s80, s61, 2176\ns_lshl2_add_u32 s61, s61, s43\n"
"s_mul_i32 s82, s61, 640\ns_mul_i32 s61, s43, 28672\ns_mul_i32 s73, s60, 229376\ns_add_u32 s73, s73, s61\ns_mul_i32 s62, s90, 14336\ns_add_u32 s68, s73, s62\n"
"s_add_u32 s61, s61, s62\ns_xor_b32 s62, s60, 1\ns_mul_i32 s69, s62, 229376\ns_add_u32 s69, s69, s61\ns_mul_i32 s61, s44, 7168\ns_add_u32 s73, s73, s61\n"
"s_lshl2_add_u32 s61, s60, s43\ns_mul_i32 s81, s61, 640\ns_lshl2_add_u32 s61, s60, s44\ns_mul_i32 s74, s61, 640\ns_lshl2_add_u32 s61, s41, s44\ns_mul_i32 s61, s61, 48\n"
"s_add_u32 s61, s61, s40\ns_lshl_b32 s72, s61, 12\ns_mul_i32 s55, s42, 448\ns_add_u32 s54, s55, 64\ns_and_b32 s54, s54, 0xffffff80\ns_bitcmp0_b32 s42, 0\n"
"s_cselect_b32 s61, 384, 0\ns_add_u32 s55, s55, s61\ns_lshl_b32 s61, s41, 9\ns_lshl_b32 s91, s42, 13\ns_add_u32 s91, s91, s61\ns_add_u32 s91, s91, 83886080\n"
"s_mul_i32 s61, s43, 1835008\ns_lshl_b32 s62, s44, 9\ns_add_u32 s61, s61, s62\ns_add_u32 s62, s43, 1\ns_and_b32 s62, s62, 3\ns_lshl_b32 s62, s62, 7\n"
"s_add_u32 s56, s61, s62\ns_add_u32 s62, s43, 2\ns_and_b32 s62, s62, 3\ns_lshl_b32 s62, s62, 7\ns_add_u32 s57, s61, s62\ns_add_u32 s62, s43, 3\n"
"s_and_b32 s62, s62, 3\ns_lshl_b32 s62, s62, 7\ns_add_u32 s58, s61, s62\ns_add_u32 s62, s43, 4\ns_and_b32 s62, s62, 3\ns_lshl_b32 s62, s62, 7\n"
"s_add_u32 s59, s61, s62\ns_mov_b32 s76, 0x3f803f80\ns_mov_b32 s77, 0xffff0000\ns_mov_b32 s48, 131072\ns_cmp_eq_u32 s60, 0\ns_cselect_b32 s78, 0xf149f2ca, 0\n"
"v_bfe_u32 v96, v252, 6, 3\nv_lshlrev_b32 v96, 7, v96\nv_lshrrev_b32 v97, 9, v252\nv_lshl_add_u32 v96, v97, 11, v96\nv_bfe_u32 v97, v252, 4, 2\nv_lshl_add_u32 v96, v97, 3, v96\n"
"s_lshl_b32 s60, s90, 10\ns_add_u32 s60, s60, 32768\nv_add_u32 v96, s60, v96\nv_bfe_u32 v97, v252, 7, 2\nv_xor_b32 v98, 0, v97\nv_lshl_add_u32 v184, v98, 5, v96\n"
"v_add_u32 v188, 0x10000, v184\nv_xor_b32 v98, 1, v97\nv_lshl_add_u32 v185, v98, 5, v96\nv_add_u32 v189, 0x10000, v185\nv_xor_b32 v98, 2, v97\nv_lshl_add_u32 v186, v98, 5, v96\n"
"v_add_u32 v190, 0x10000, v186\nv_xor_b32 v98, 3, v97\nv_lshl_add_u32 v187, v98, 5, v96\nv_add_u32 v191, 0x10000, v187\nv_lshrrev_b32 v96, 2, v252\ns_xor_b32 s60, s51, 0x2000\n"
"v_add_u32 v225, s60, v96\nv_mov_b32 v210, 0\nv_mov_b32 v211, 0\nv_bfe_u32 v96, v252, 5, 2\nv_bfe_u32 v97, v252, 8, 2\nv_xor_b32 v96, v96, v97\n"
"v_lshrrev_b32 v97, 7, v252\ns_lshl_b32 s60, s40, 3\nv_add_u32 v97, s60, v97\nv_mov_b32 v228, v97\nv_and_b32 v97, 16, v252\nv_lshl_add_u32 v229, v96, 5, v97\n"
"v_bfe_u32 v96, v252, 5, 2\nv_bfe_u32 v97, v252, 8, 2\nv_xor_b32 v96, v96, v97\nv_lshrrev_b32 v97, 7, v252\ns_lshl_b32 s60, s40, 3\nv_add_u32 v97, s60, v97\n"
"v_lshrrev_b32 v98, 1, v96\nv_lshl_add_u32 v97, v98, 5, v97\nv_and_b32 v96, 1, v96\nv_mul_u32_u24 v97, 7168, v97\nv_lshl_add_u32 v97, v96, 5, v97\nv_and_b32 v96, 16, v252\n"
"v_add_u32 v222, v96, v97\nv_add_u32 v223, 3668992, v222\nv_bfe_u32 v96, v252, 4, 4\nv_lshrrev_b32 v97, 8, v252\nv_lshrrev_b32 v98, 1, v96\nv_xor_b32 v97, v97, v98\n"
"v_lshlrev_b32 v96, 7, v96\nv_lshl_add_u32 v96, v97, 4, v96\nv_add_u32 v248, s51, v96\nv_xor_b32 v249, 64, v248\nv_add_u32 v250, 122880, v248\nv_add_u32 v251, 122880, v249\n"
"s_add_u32 m0, s51, 122880\ns_add_u32 s60, s70, 128\nbuffer_load_dwordx4 v226, s[24:27], s60 offen lds\ns_add_u32 m0, s51, 123904\ns_add_u32 s60, s70, 16512\nbuffer_load_dwordx4 v227, s[24:27], s60 offen lds\n"
"s_add_u32 m0, s51, 124928\ns_add_u32 s60, s70, 32896\nbuffer_load_dwordx4 v226, s[24:27], s60 offen lds\ns_add_u32 m0, s51, 125952\ns_add_u32 s60, s70, 49280\nbuffer_load_dwordx4 v227, s[24:27], s60 offen lds\n"
"s_add_u32 m0, s51, 126976\ns_add_u32 s60, s71, 128\nbuffer_load_dwordx4 v226, s[24:27], s60 offen lds\ns_add_u32 m0, s51, 128000\ns_add_u32 s60, s71, 16512\nbuffer_load_dwordx4 v227, s[24:27], s60 offen lds\n"
"s_add_u32 m0, s51, 129024\ns_add_u32 s60, s71, 32896\nbuffer_load_dwordx4 v226, s[24:27], s60 offen lds\ns_add_u32 m0, s51, 130048\ns_add_u32 s60, s71, 49280\nbuffer_load_dwordx4 v227, s[24:27], s60 offen lds\n"
"s_add_u32 m0, s50, 16384\ns_add_u32 s63, s52, 4096\nbuffer_load_dwordx4 v220, s[4:7], s63 offen lds\nbuffer_load_dwordx4 v221, s[4:7], s63 offen offset:1024 lds\ns_add_u32 m0, s50, 24576\ns_add_u32 s63, s52, 200704\n"
"buffer_load_dwordx4 v220, s[4:7], s63 offen lds\nbuffer_load_dwordx4 v221, s[4:7], s63 offen offset:1024 lds\ns_add_u32 m0, s50, 32768\ns_add_u32 s63, s101, 2048\nbuffer_load_dwordx4 v220, s[4:7], s63 offen lds\nbuffer_load_dwordx4 v221, s[4:7], s63 offen offset:1024 lds\n"
"s_add_u32 m0, s50, 40960\ns_add_u32 s63, s101, 4096\nbuffer_load_dwordx4 v220, s[4:7], s63 offen lds\nbuffer_load_dwordx4 v221, s[4:7], s63 offen offset:1024 lds\nv_bfe_u32 v246, v252, 4, 4\nv_lshlrev_b32 v246, 11, v246\n"
"v_lshrrev_b32 v247, 8, v252\nv_lshl_add_u32 v246, v247, 3, v246\ns_add_u32 s84, s49, s53\ns_add_u32 s85, s84, 32768\nbuffer_load_dwordx2 v[192:193], v246, s[24:27], s84 offen offset:0\nbuffer_load_dwordx2 v[194:195], v246, s[24:27], s84 offen offset:32\n"
"buffer_load_dwordx2 v[196:197], v246, s[24:27], s84 offen offset:64\nbuffer_load_dwordx2 v[198:199], v246, s[24:27], s84 offen offset:96\nbuffer_load_dwordx2 v[200:201], v246, s[24:27], s85 offen offset:0\nbuffer_load_dwordx2 v[202:203], v246, s[24:27], s85 offen offset:32\nbuffer_load_dwordx2 v[204:205], v246, s[24:27], s85 offen offset:64\nbuffer_load_dwordx2 v[206:207], v246, s[24:27], s85 offen offset:96\n"
"v_mov_b32 v106, 0xf149f149\nv_mov_b32 v104, 0xffff\nv_mov_b32 v105, 0xffff0000\ns_mov_b32 s60, 0x50000\ns_mov_b32 s61, 0x5550055\nv_cndmask_b32 v96, 0, v104, s[60:61]\n"
"s_mov_b32 s60, 0xa0000\ns_mov_b32 s61, 0xaaa00aa\nv_cndmask_b32 v96, v96, v105, s[60:61]\nv_bfi_b32 v100, v96, 0, v106\ns_mov_b32 s60, 0x150001\ns_mov_b32 s61, 0x15550155\n"
"v_cndmask_b32 v97, 0, v104, s[60:61]\ns_mov_b32 s60, 0x2a0002\ns_mov_b32 s61, 0x2aaa02aa\nv_cndmask_b32 v97, v97, v105, s[60:61]\nv_bfi_b32 v101, v97, 0, v106\ns_mov_b32 s60, 0x55505555\n"
"s_mov_b32 s61, 0x50005500\nv_cndmask_b32 v98, 0, v104, s[60:61]\ns_mov_b32 s60, 0xaaa0aaaa\ns_mov_b32 s61, 0xa000aa00\nv_cndmask_b32 v98, v98, v105, s[60:61]\nv_bfi_b32 v102, v98, 0, v106\n"
"s_mov_b32 s60, 0x55405554\ns_mov_b32 s61, 0x40005400\nv_cndmask_b32 v99, 0, v104, s[60:61]\ns_mov_b32 s60, 0xaa80aaa8\ns_mov_b32 s61, 0x8000a800\nv_cndmask_b32 v99, v99, v105, s[60:61]\n"
"v_bfi_b32 v103, v99, 0, v106\nv_add_u32 v104, 161792, v252\nds_write_b128 v104, v[96:99] offset:0\nds_write_b128 v104, v[100:103] offset:1024\ns_waitcnt vmcnt(28)\nv_readfirstlane_b32 s83, v230\n"
"v_readfirstlane_b32 s60, v231\ns_mul_i32 s46, s83, 0x9e3779b1\ns_add_u32 s46, s46, 0x7f4a7c15\ns_xor_b32 s47, s83, s60\ns_mul_i32 s47, s47, 0x85ebca6b\ns_add_u32 s47, s47, 0xc2b2ae35\n"
"v_mov_b32 v144, -1\nv_mov_b32 v145, -1\nv_mov_b32 v146, -1\nv_mov_b32 v147, -1\ns_mov_b64 exec, 0\ns_cmp_eq_u32 s40, 1\n"
"s_cbranch_scc1 Lpg2\ns_cmp_eq_u32 s40, 2\ns_cbranch_scc1 Lpg3\ns_cmp_eq_u32 s40, 3\ns_cbranch_scc1 Lpg4\ns_branch Lpg1\n"
"LretQ0:\ns_mov_b64 exec, -1\nds_read_b128 a[0:3], v248 offset:0\nds_read_b128 a[4:7], v248 offset:2048\nds_read_b128 a[8:11], v248 offset:4096\nds_read_b128 a[12:15], v248 offset:6144\n"
"ds_read_b128 a[16:19], v249 offset:0\nds_read_b128 a[20:23], v249 offset:2048\nds_read_b128 a[24:27], v249 offset:4096\nds_read_b128 a[28:31], v249 offset:6144\ns_waitcnt vmcnt(12) lgkmcnt(8)\ns_barrier\n"
"R 160,184,0\nR 162,184,4096\nR 164,184,8192\nR 166,184,12288\nR 168,185,0\nR 170,185,4096\n"
"R 172,185,8192\nR 174,185,12288\nR 176,186,0\nR 178,186,4096\nR 180,186,8192\nR 182,186,12288\n"
"G 10\nv_mfma_f32_16x16x32_bf16 v[0:3], v[160:163], a[0:3], 0\ns_add_u32 m0, s50, 49152\ns_add_u32 s63, s52, 395264\nbuffer_load_dwordx4 v220, s[4:7], s63 offen lds\nG 8\n"
"M 0,164,16\nbuffer_load_dwordx4 v221, s[4:7], s63 offen offset:1024 lds\nv_mfma_f32_16x16x32_bf16 v[16:19], v[160:163], a[4:7], 0\ns_add_u32 m0, s51, 0\ns_add_u32 s60, s70, 256\nbuffer_load_dwordx4 v226, s[24:27], s60 offen lds\n"
"M 16,164,20\ns_lshl4_add_u32 s62, s41, s42\ns_lshl_b32 s62, s62, 12\ns_add_u32 s62, s62, 77594624\ns_lshl_b32 s60, s40, 10\ns_add_u32 s62, s62, s60\n"
"buffer_store_dwordx4 v[144:147], v252, s[20:23], s62 offen sc1\ns_add_u32 m0, s50, 57344\ns_add_u32 s63, s52, 591872\nv_mfma_f32_16x16x32_bf16 v[32:35], v[160:163], a[8:11], 0\nbuffer_load_dwordx4 v220, s[4:7], s63 offen lds\nM 32,164,24\n"
"buffer_load_dwordx4 v221, s[4:7], s63 offen offset:1024 lds\nv_mfma_f32_16x16x32_bf16 v[48:51], v[160:163], a[12:15], 0\ns_add_u32 m0, s51, 1024\ns_add_u32 s60, s70, 16640\nbuffer_load_dwordx4 v227, s[24:27], s60 offen lds\nM 48,164,28\n"
"s_add_u32 m0, s50, 65536\ns_add_u32 s63, s52, 397312\nbuffer_load_dwordx4 v220, s[4:7], s63 offen lds\nG 4\nv_mfma_f32_16x16x32_bf16 v[52:55], v[168:171], a[12:15], 0\nR 160,187,0\n"
"M 52,172,28\nbuffer_load_dwordx4 v221, s[4:7], s63 offen offset:1024 lds\nv_mfma_f32_16x16x32_bf16 v[36:39], v[168:171], a[8:11], 0\nR 162,187,4096\nM 36,172,24\ns_add_u32 m0, s51, 2048\n"
"s_add_u32 s60, s70, 33024\nbuffer_load_dwordx4 v226, s[24:27], s60 offen lds\nv_mfma_f32_16x16x32_bf16 v[20:23], v[168:171], a[4:7], 0\nR 164,187,8192\ns_add_u32 m0, s50, 73728\nM 20,172,20\n"
"s_lshl4_add_u32 s62, s41, s42\ns_lshl_b32 s62, s62, 12\ns_add_u32 s62, s62, 78643200\ns_lshl_b32 s60, s40, 10\ns_add_u32 s62, s62, s60\nbuffer_store_dwordx4 v[144:147], v252, s[20:23], s62 offen sc1\n"
"s_add_u32 s63, s52, 593920\nv_mfma_f32_16x16x32_bf16 v[4:7], v[168:171], a[0:3], 0\nR 166,187,12288\nbuffer_load_dwordx4 v220, s[4:7], s63 offen lds\nM 4,172,16\nG 4\n"
"v_mfma_f32_16x16x32_bf16 v[8:11], v[176:179], a[0:3], 0\nR 168,184,16384\nM 8,180,16\ns_add_u32 m0, s51, 3072\ns_add_u32 s60, s70, 49408\nbuffer_load_dwordx4 v227, s[24:27], s60 offen lds\n"
"v_mfma_f32_16x16x32_bf16 v[24:27], v[176:179], a[4:7], 0\nR 170,184,20480\ns_add_u32 m0, s50, 73728\ns_nop 0\nbuffer_load_dwordx4 v221, s[4:7], s63 offen offset:1024 lds\nM 24,180,20\n"
"s_add_u32 m0, s51, 4096\ns_add_u32 s60, s71, 256\nbuffer_load_dwordx4 v226, s[24:27], s60 offen lds\nv_mfma_f32_16x16x32_bf16 v[40:43], v[176:179], a[8:11], 0\nR 172,184,24576\nM 40,180,24\n"
"v_mfma_f32_16x16x32_bf16 v[56:59], v[176:179], a[12:15], 0\nR 174,184,28672\nM 56,180,28\nG 4\nv_mfma_f32_16x16x32_bf16 v[60:63], v[160:163], a[12:15], 0\nR 176,185,16384\n"
"M 60,164,28\ns_add_u32 m0, s51, 5120\ns_add_u32 s60, s71, 16640\nbuffer_load_dwordx4 v227, s[24:27], s60 offen lds\nv_mfma_f32_16x16x32_bf16 v[44:47], v[160:163], a[8:11], 0\nR 178,185,20480\n"
"M 44,164,24\ns_add_u32 m0, s51, 6144\ns_add_u32 s60, s71, 33024\nbuffer_load_dwordx4 v226, s[24:27], s60 offen lds\nv_mfma_f32_16x16x32_bf16 v[28:31], v[160:163], a[4:7], 0\nR 180,185,24576\n"
"s_add_u32 m0, s50, 81920\ns_add_u32 s63, s101, 0\nM 28,164,20\nbuffer_load_dwordx4 v220, s[4:7], s63 offen lds\nv_mfma_f32_16x16x32_bf16 v[12:15], v[160:163], a[0:3], 0\nR 182,185,28672\n"
"M 12,164,16\ns_waitcnt vmcnt(12)\ns_barrier\nG 4\nv_mfma_f32_16x16x32_bf16 v[64:67], a[0:3], v[168:171], 0\nR 160,186,16384\n"
"N 64,16,172\ns_add_u32 m0, s51, 7168\ns_add_u32 s60, s71, 49408\nbuffer_load_dwordx4 v227, s[24:27], s60 offen lds\nv_mfma_f32_16x16x32_bf16 v[68:71], a[4:7], v[168:171], 0\nR 162,186,20480\n"
"s_add_u32 m0, s50, 81920\nN 68,20,172\nbuffer_load_dwordx4 v221, s[4:7], s63 offen offset:1024 lds\nv_mfma_f32_16x16x32_bf16 v[96:99], a[8:11], v[168:171], 0\nR 164,186,24576\ns_add_u32 m0, s50, 0\n"
"s_add_u32 s63, s52, 788480\nN 96,24,172\nbuffer_load_dwordx4 v220, s[4:7], s63 offen lds\nv_mfma_f32_16x16x32_bf16 v[100:103], a[12:15], v[168:171], 0\nR 166,186,28672\nN 100,28,172\n"
"G 4\nv_mfma_f32_16x16x32_bf16 v[108:111], a[12:15], v[176:179], 0\nR 168,187,16384\nv_mov_b32 v242, s76\nv_mov_b32 v243, s76\nN 108,28,180\n"
"buffer_load_dwordx4 v221, s[4:7], s63 offen offset:1024 lds\nv_mov_b32 v244, s76\nv_mfma_f32_16x16x32_bf16 v[104:107], a[8:11], v[176:179], 0\nR 170,187,20480\ns_add_u32 s63, s52, 985088\nN 104,24,180\n"
"s_add_u32 m0, s50, 8192\ns_lshl_b32 s60, s40, 5\nv_xor_b32 v238, s60, v184\nv_mfma_f32_16x16x32_bf16 v[76:79], a[4:7], v[176:179], 0\nR 172,187,24576\nv_and_b32 v238, 0xfffffbff, v238\n"
"N 76,20,180\nbuffer_load_dwordx4 v220, s[4:7], s63 offen lds\nv_mfma_f32_16x16x32_bf16 v[72:75], a[0:3], v[176:179], 0\nR 174,187,28672\nv_mov_b32 v245, s76\nN 72,16,180\n"
"G 4\nv_mfma_f32_16x16x32_bf16 v[80:83], a[0:3], v[160:163], 0\nR 176,184,49152\nds_read_b128 a[32:35], v250 offset:0\nR 128,238,32768\nR 130,238,36864\n"
"N 80,16,164\nds_read_b128 a[36:39], v250 offset:2048\nv_mfma_f32_16x16x32_bf16 v[84:87], a[4:7], v[160:163], 0\nR 178,184,53248\nds_read_b128 a[40:43], v250 offset:4096\nR 132,238,33792\n"
"R 134,238,37888\nN 84,20,164\nds_read_b128 a[44:47], v250 offset:6144\nv_mfma_f32_16x16x32_bf16 v[112:115], a[8:11], v[160:163], 0\nR 180,184,57344\nbuffer_load_dwordx4 v221, s[4:7], s63 offen offset:1024 lds\n"
"N 112,24,164\nds_read_b128 a[48:51], v251 offset:0\nv_mfma_f32_16x16x32_bf16 v[116:119], a[12:15], v[160:163], 0\nR 182,184,61440\nds_read_b128 a[52:55], v251 offset:2048\nN 116,28,164\n"
"ds_read_b128 a[56:59], v251 offset:4096\nG 15\nv_mfma_f32_16x16x32_bf16 v[124:127], a[12:15], v[168:171], 0\nR 160,185,49152\nG 12\nv_mfma_f32_16x16x32_bf16 v[136:139], v[242:245], v[128:131], 0\n"
"N 124,28,172\nds_read_b128 a[60:63], v251 offset:6144\nG 8\nv_mfma_f32_16x16x32_bf16 v[136:139], v[242:245], v[132:135], v[136:139]\nv_mfma_f32_16x16x32_bf16 v[120:123], a[8:11], v[168:171], 0\nR 162,185,53248\n"
"N 120,24,172\nv_mfma_f32_16x16x32_bf16 v[92:95], a[4:7], v[168:171], 0\nR 164,185,57344\nN 92,20,172\nG 2\ns_add_u32 m0, s51, 122880\n"
"s_add_u32 s60, s70, 384\nbuffer_load_dwordx4 v226, s[24:27], s60 offen lds\nv_mfma_f32_16x16x32_bf16 v[88:91], a[0:3], v[168:171], 0\nR 166,185,61440\nv_lshrrev_b32 v128, 1, v252\nv_mov_b32 v137, s46\n"
"N 88,16,172\ns_waitcnt vmcnt(11)\ns_barrier\nM 0,176,32\nR 168,186,49152\ns_lshl_b32 s60, s40, 5\n"
"v_xor_b32 v238, s60, v184\nM 0,180,48\ns_add_u32 m0, s51, 123904\ns_add_u32 s60, s70, 16768\nbuffer_load_dwordx4 v227, s[24:27], s60 offen lds\ns_and_b32 s61, s40, 3\n"
"s_lshl_b32 s61, s61, 7\ns_add_u32 s61, s61, s91\ns_add_u32 s61, s61, 131072\ns_mov_b64 exec, 0xffff\nbuffer_store_dwordx2 v[136:137], v128, s[20:23], s61 offen sc1\ns_mov_b64 exec, -1\n"
"M 16,176,36\nR 170,186,53248\ns_add_u32 m0, s50, 16384\ns_add_u32 s63, s52, 790528\nM 16,180,52\nbuffer_load_dwordx4 v220, s[4:7], s63 offen lds\n"
"M 32,176,40\nR 172,186,57344\nv_and_b32 v238, 0xfffffbff, v238\nM 32,180,56\ns_add_u32 m0, s51, 124928\ns_add_u32 s60, s70, 33152\n"
"buffer_load_dwordx4 v226, s[24:27], s60 offen lds\nM 48,176,44\nR 174,186,61440\ns_add_u32 m0, s50, 16384\nv_mov_b32 v230, 0\nM 48,180,60\n"
"Lpg1:\ns_cbranch_execz Lpg5\nbuffer_load_dwordx4 v221, s[4:7], s63 offen offset:1024 lds\nG 4\nv_mov_b32 v231, 0\nM 52,160,44\n"
"R 176,187,49152\nR 128,238,40960\nR 130,238,45056\nv_mov_b32 v232, 0\nM 52,164,60\ns_add_u32 m0, s51, 125952\n"
"s_add_u32 s60, s70, 49536\nbuffer_load_dwordx4 v227, s[24:27], s60 offen lds\nv_mov_b32 v233, 0\nM 36,160,40\nR 178,187,53248\nR 132,238,41984\n"
"R 134,238,46080\ns_add_u32 m0, s50, 24576\ns_add_u32 s63, s52, 987136\nv_dot2c_f32_bf16 v230, s76, v192\nM 36,164,56\nbuffer_load_dwordx4 v220, s[4:7], s63 offen lds\n"
"v_dot2c_f32_bf16 v231, v192, v192\nM 20,160,36\nR 180,187,57344\nv_lshrrev_b32 v234, 5, v252\nv_dot2c_f32_bf16 v232, s76, v200\nM 20,164,52\n"
"s_add_u32 m0, s51, 126976\ns_add_u32 s60, s71, 384\nbuffer_load_dwordx4 v226, s[24:27], s60 offen lds\nv_dot2c_f32_bf16 v233, v200, v200\nM 4,160,32\nR 182,187,61440\n"
"s_add_u32 m0, s50, 24576\nv_dot2c_f32_bf16 v230, s76, v193\nM 4,164,48\nG 8\nv_and_b32 v234, 24, v234\nv_bfe_u32 v235, s46, v234, 8\n"
"v_dot2c_f32_bf16 v231, v193, v193\nM 8,168,32\nR 160,188,0\nG 6\nv_mfma_f32_16x16x32_bf16 v[136:139], v[242:245], v[128:131], 0\nbuffer_load_dwordx4 v221, s[4:7], s63 offen offset:1024 lds\n"
"v_dot2c_f32_bf16 v232, s76, v201\nM 8,172,48\ns_add_u32 m0, s51, 128000\ns_add_u32 s60, s71, 16768\nbuffer_load_dwordx4 v227, s[24:27], s60 offen lds\nG 3\n"
"v_mfma_f32_16x16x32_bf16 v[136:139], v[242:245], v[132:135], v[136:139]\nv_dot2c_f32_bf16 v233, v201, v201\nM 24,168,36\nR 162,188,4096\nv_lshrrev_b32 v234, 1, v234\nv_dot2c_f32_bf16 v230, s76, v194\n"
"M 24,172,52\nv_and_b32 v236, 0xf0, v252\nv_add_u32 v234, v234, v236\nv_dot2c_f32_bf16 v231, v194, v194\nM 40,168,40\nR 164,188,8192\n"
"v_dot2c_f32_bf16 v232, s76, v202\nM 40,172,56\ns_add_u32 m0, s51, 129024\ns_add_u32 s60, s71, 33152\nbuffer_load_dwordx4 v226, s[24:27], s60 offen lds\nv_dot2c_f32_bf16 v233, v202, v202\n"
"M 56,168,44\nR 166,188,12288\nv_lshrrev_b32 v128, 1, v252\nv_dot2c_f32_bf16 v230, s76, v195\nM 56,172,60\nG 4\n"
"v_mov_b32 v137, s46\nv_dot2c_f32_bf16 v231, v195, v195\nM 60,176,44\nR 168,189,0\nv_dot2c_f32_bf16 v232, s76, v203\nM 60,180,60\n"
"s_add_u32 m0, s51, 130048\ns_add_u32 s60, s71, 49536\nbuffer_load_dwordx4 v227, s[24:27], s60 offen lds\ns_and_b32 s61, s40, 3\ns_lshl_b32 s61, s61, 7\ns_add_u32 s61, s61, s91\n"
"s_add_u32 s61, s61, 262144\ns_mov_b64 exec, 0xffff\nbuffer_store_dwordx2 v[136:137], v128, s[20:23], s61 offen sc1\ns_mov_b64 exec, -1\nv_dot2c_f32_bf16 v233, v203, v203\nM 44,176,40\n"
"R 170,189,4096\nv_dot2c_f32_bf16 v230, s76, v196\nM 44,180,56\nv_dot2c_f32_bf16 v231, v196, v196\nM 28,176,36\nR 172,189,8192\n"
"v_dot2c_f32_bf16 v232, s76, v204\nM 28,180,52\ns_add_u32 m0, s50, 32768\nv_dot2c_f32_bf16 v233, v204, v204\nM 12,176,32\nR 174,189,12288\n"
"v_dot2c_f32_bf16 v230, s76, v197\nM 12,180,48\ns_waitcnt vmcnt(14)\ns_barrier\nG 4\nv_dot2c_f32_bf16 v231, v197, v197\n"
"N 64,32,160\nR 176,190,0\nv_dot2c_f32_bf16 v232, s76, v205\nN 64,48,164\ns_add_u32 s63, s52, 1181696\nv_dot2c_f32_bf16 v233, v205, v205\n"
"N 68,36,160\nR 178,190,4096\nv_dot2c_f32_bf16 v230, s76, v198\nN 68,52,164\nbuffer_load_dwordx4 v220, s[4:7], s63 offen lds\nv_dot2c_f32_bf16 v231, v198, v198\n"
"N 96,40,160\nR 180,190,8192\nds_read_b128 a[64:67], v248 offset:0\nv_dot2c_f32_bf16 v232, s76, v206\nN 96,56,164\nds_read_b128 a[68:71], v248 offset:2048\n"
"v_dot2c_f32_bf16 v233, v206, v206\nN 100,44,160\nR 182,190,12288\nds_read_b128 a[72:75], v248 offset:4096\nv_dot2c_f32_bf16 v230, s76, v199\nN 100,60,164\n"
"ds_read_b128 a[76:79], v248 offset:6144\nG 8\nv_dot2c_f32_bf16 v231, v199, v199\nN 108,44,168\nR 160,191,0\ns_nop 0\n"
"v_permlane32_swap_b32 v230, v231\nv_dot2c_f32_bf16 v232, s76, v207\nN 108,60,172\nds_read_b128 a[80:83], v249 offset:0\nbuffer_load_dwordx4 v221, s[4:7], s63 offen offset:1024 lds\nv_dot2c_f32_bf16 v233, v207, v207\n"
"N 104,40,168\nR 162,191,4096\nds_read_b128 a[84:87], v249 offset:2048\nN 104,56,172\nds_read_b128 a[88:91], v249 offset:4096\ns_add_u32 m0, s50, 40960\n"
"N 76,36,168\nR 164,191,8192\ns_add_u32 s63, s52, 1378304\nN 76,52,172\nds_read_b128 a[92:95], v249 offset:6144\nv_permlane32_swap_b32 v232, v233\n"
"N 72,32,168\nR 166,191,12288\nbuffer_load_dwordx4 v220, s[4:7], s63 offen lds\nN 72,48,172\nG 10\nv_add_f32 v230, v230, v231\n"
"v_add_f32 v232, v232, v233\nN 80,32,176\nR 168,184,0\nv_permlane16_swap_b32 v230, v232\nN 80,48,180\nG 2\n"
"s_add_u32 m0, s51, 0\ns_add_u32 s60, s70, 512\nbuffer_load_dwordx4 v226, s[24:27], s60 offen lds\nN 84,36,176\nR 170,184,4096\ns_add_u32 m0, s50, 40960\n"
"s_nop 0\nbuffer_load_dwordx4 v221, s[4:7], s63 offen offset:1024 lds\nN 84,52,180\nv_add_f32 v230, v230, v232\ns_movk_i32 s62, 0xff\nv_bfi_b32 v230, s62, v235, v230\n"
"N 112,40,176\nR 172,184,8192\nN 112,56,180\ns_add_u32 m0, s51, 1024\ns_add_u32 s60, s70, 16896\nbuffer_load_dwordx4 v227, s[24:27], s60 offen lds\n"
"N 116,44,176\nR 174,184,12288\nN 116,60,180\nG 4\nN 124,44,160\nR 176,185,0\n"
"N 124,60,164\ns_add_u32 m0, s51, 2048\ns_add_u32 s60, s70, 33280\nbuffer_load_dwordx4 v226, s[24:27], s60 offen lds\nN 120,40,160\nR 178,185,4096\n"
"N 120,56,164\ns_lshl_b32 s60, s40, 5\nv_xor_b32 v238, s60, v188\ns_lshr_b32 s61, s49, 4\ns_lshl1_add_u32 s61, s53, s61\ns_add_u32 s61, s61, 81788928\n"
"buffer_store_dword v230, v234, s[20:23], s61 offen sc1\nv_and_b32 v238, 0xfffffbff, v238\nN 92,36,160\nR 180,185,8192\nN 92,52,164\ns_add_u32 m0, s51, 3072\n"
"s_add_u32 s60, s70, 49664\nbuffer_load_dwordx4 v227, s[24:27], s60 offen lds\nN 88,32,160\nR 182,185,12288\nN 88,48,164\ns_waitcnt vmcnt(13)\n"
"s_barrier\nG 4\nM 0,168,64\nR 160,186,0\nM 0,172,80\ns_add_u32 m0, s51, 4096\n"
"s_add_u32 s60, s71, 512\nbuffer_load_dwordx4 v226, s[24:27], s60 offen lds\nM 16,168,68\nR 162,186,4096\nR 128,238,16384\nR 130,238,20480\n"
"s_add_u32 m0, s50, 49152\ns_add_u32 s63, s52, 1183744\nM 16,172,84\nbuffer_load_dwordx4 v220, s[4:7], s63 offen lds\nM 32,168,72\nR 164,186,8192\n"
"R 132,238,17408\nR 134,238,21504\nM 32,172,88\ns_add_u32 m0, s51, 5120\ns_add_u32 s60, s71, 16896\nbuffer_load_dwordx4 v227, s[24:27], s60 offen lds\n"
"M 48,168,76\nR 166,186,12288\ns_add_u32 m0, s50, 49152\nM 48,172,92\nG 8\nM 52,176,76\n"
"R 168,187,0\nbuffer_load_dwordx4 v221, s[4:7], s63 offen offset:1024 lds\nM 52,180,92\ns_add_u32 m0, s51, 6144\ns_add_u32 s60, s71, 33280\nbuffer_load_dwordx4 v226, s[24:27], s60 offen lds\n"
"M 36,176,72\nR 170,187,4096\nG 6\nv_mfma_f32_16x16x32_bf16 v[136:139], v[242:245], v[128:131], 0\ns_add_u32 m0, s50, 57344\ns_add_u32 s63, s52, 1380352\n"
"M 36,180,88\nG 3\nv_mfma_f32_16x16x32_bf16 v[136:139], v[242:245], v[132:135], v[136:139]\nbuffer_load_dwordx4 v220, s[4:7], s63 offen lds\nM 20,176,68\nR 172,187,8192\n"
"M 20,180,84\ns_add_u32 m0, s51, 7168\ns_add_u32 s60, s71, 49664\nbuffer_load_dwordx4 v227, s[24:27], s60 offen lds\nM 4,176,64\nR 174,187,12288\n"
"s_add_u32 m0, s50, 57344\nM 4,180,80\nG 4\nM 8,160,64\nR 176,184,16384\nbuffer_load_dwordx4 v221, s[4:7], s63 offen offset:1024 lds\n"
"M 8,164,80\nv_lshrrev_b32 v128, 1, v252\nv_mov_b32 v137, s46\nM 24,160,68\nR 178,184,20480\nM 24,164,84\n"
"s_and_b32 s61, s40, 3\ns_lshl_b32 s61, s61, 7\ns_add_u32 s61, s61, s91\ns_add_u32 s61, s61, 0\ns_mov_b64 exec, 0xffff\nbuffer_store_dwordx2 v[136:137], v128, s[20:23], s61 offen sc1\n"
"s_mov_b64 exec, -1\nM 40,160,72\nR 180,184,24576\nM 40,164,88\nM 56,160,76\nR 182,184,28672\n"
"M 56,164,92\nG 4\nM 60,168,76\nR 160,185,16384\nM 60,172,92\ns_add_u32 m0, s50, 65536\n"
"M 44,168,72\nR 162,185,20480\nM 44,172,88\ns_add_u32 s63, s52, 1574912\nM 28,168,68\nR 164,185,24576\n"
"M 28,172,84\nbuffer_load_dwordx4 v220, s[4:7], s63 offen lds\nM 12,168,64\nR 166,185,28672\nM 12,172,80\ns_waitcnt vmcnt(14)\n"
"s_barrier\nG 4\nN 64,64,176\nR 168,186,16384\nN 64,80,180\nv_lshrrev_b32 v230, 7, v252\n"
"N 68,68,176\nR 170,186,20480\nN 68,84,180\nbuffer_load_dwordx4 v221, s[4:7], s63 offen offset:1024 lds\nv_lshlrev_b32 v230, 9, v230\nN 96,72,176\n"
"R 172,186,24576\ns_add_u32 m0, s50, 73728\nN 96,88,180\nds_read_b128 a[96:99], v250 offset:0\nN 100,76,176\nR 174,186,28672\n"
"ds_read_b128 a[100:103], v250 offset:2048\ns_add_u32 s63, s52, 1771520\nN 100,92,180\nds_read_b128 a[104:107], v250 offset:4096\nG 7\nN 108,76,160\n"
"R 176,187,16384\nv_and_b32 v231, 0x70, v252\nN 108,92,164\nds_read_b128 a[108:111], v250 offset:6144\nbuffer_load_dwordx4 v220, s[4:7], s63 offen lds\nN 104,72,160\n"
"R 178,187,20480\nds_read_b128 a[112:115], v251 offset:0\nN 104,88,164\nds_read_b128 a[116:119], v251 offset:2048\ns_and_b32 s60, s40, 3\ns_lshl_b32 s60, s60, 7\n"
"v_add3_u32 v230, v230, v231, s60\nN 76,68,160\nR 180,187,24576\nds_read_b128 a[120:123], v251 offset:4096\nN 76,84,164\nds_read_b128 a[124:127], v251 offset:6144\n"
"N 72,64,160\nR 182,187,28672\nN 72,80,164\ns_lshl_b32 s61, s42, 13\ns_add_u32 s61, s61, 84017152\nbuffer_load_dwordx4 v[128:131], v230, s[20:23], s61 offen sc1\n"
"G 11\nN 80,64,168\nR 160,184,32768\nbuffer_load_dwordx4 v221, s[4:7], s63 offen offset:1024 lds\nN 80,80,172\ns_lshl_b32 s61, s42, 13\n"
"s_add_u32 s61, s61, 84021248\nbuffer_load_dwordx4 v[132:135], v230, s[20:23], s61 offen sc1\nN 84,68,168\nR 162,184,36864\nN 84,84,172\nN 112,72,168\n"
"R 164,184,40960\ns_waitcnt vmcnt(8)\nds_read_b128 a[128:131], v248 offset:0\nN 112,88,172\nds_read_b128 a[132:135], v248 offset:2048\nN 116,76,168\n"
"R 166,184,45056\nds_read_b128 a[136:139], v248 offset:4096\nN 116,92,172\nds_read_b128 a[140:143], v248 offset:6144\nG 8\nN 124,76,176\n"
"R 168,185,32768\nN 124,92,180\nds_read_b128 a[144:147], v249 offset:0\nN 120,72,176\nR 170,185,36864\nds_read_b128 a[148:151], v249 offset:2048\n"
"N 120,88,180\nds_read_b128 a[152:155], v249 offset:4096\nN 92,68,176\nR 172,185,40960\nN 92,84,180\nds_read_b128 a[156:159], v249 offset:6144\n"
"N 88,64,176\nR 174,185,45056\nN 88,80,180\ns_waitcnt vmcnt(7)\ns_barrier\nG 10\n"
"M 0,160,96\nR 176,186,32768\nM 0,164,112\nG 2\ns_add_u32 m0, s51, 0\ns_add_u32 s60, s70, 640\n"
"buffer_load_dwordx4 v226, s[24:27], s60 offen lds\nM 16,160,100\nR 178,186,36864\nM 16,164,116\ns_add_u32 m0, s51, 1024\ns_add_u32 s60, s70, 17024\n"
"buffer_load_dwordx4 v227, s[24:27], s60 offen lds\nM 32,160,104\nR 180,186,40960\ns_add_u32 m0, s50, 81920\ns_add_u32 s63, s52, 1576960\nbuffer_load_dwordx4 v220, s[4:7], s63 offen lds\n"
"v_lshrrev_b32 v234, 7, v252\nM 32,164,120\ns_add_u32 m0, s51, 2048\ns_add_u32 s60, s70, 33408\nbuffer_load_dwordx4 v226, s[24:27], s60 offen lds\nM 48,160,108\n"
"R 182,186,45056\nM 48,164,124\ns_add_u32 m0, s51, 3072\ns_add_u32 s60, s70, 49792\nbuffer_load_dwordx4 v227, s[24:27], s60 offen lds\nG 4\n"
"M 52,168,108\nR 160,187,32768\ns_add_u32 m0, s50, 81920\ns_nop 0\nbuffer_load_dwordx4 v221, s[4:7], s63 offen offset:1024 lds\nv_lshlrev_b32 v234, 9, v234\n"
"M 52,172,124\ns_add_u32 m0, s51, 4096\ns_add_u32 s60, s71, 640\nbuffer_load_dwordx4 v226, s[24:27], s60 offen lds\nM 36,168,104\nR 162,187,36864\n"
"v_and_b32 v235, 0x70, v252\nM 36,172,120\ns_add_u32 m0, s51, 5120\ns_add_u32 s60, s71, 17024\nbuffer_load_dwordx4 v227, s[24:27], s60 offen lds\nM 20,168,100\n"
"R 164,187,40960\ns_add_u32 m0, s50, 90112\ns_add_u32 s63, s52, 1773568\nbuffer_load_dwordx4 v220, s[4:7], s63 offen lds\nM 20,172,116\ns_add_u32 m0, s51, 6144\n"
"s_add_u32 s60, s71, 33408\nbuffer_load_dwordx4 v226, s[24:27], s60 offen lds\nM 4,168,96\nR 166,187,45056\ns_and_b32 s60, s40, 3\ns_lshl_b32 s60, s60, 7\n"
"v_add3_u32 v234, v234, v235, s60\nM 4,172,112\ns_add_u32 m0, s51, 7168\ns_add_u32 s60, s71, 49792\nbuffer_load_dwordx4 v227, s[24:27], s60 offen lds\nG 4\n"
"M 8,176,96\nR 168,184,49152\ns_add_u32 m0, s50, 90112\ns_nop 0\nbuffer_load_dwordx4 v221, s[4:7], s63 offen offset:1024 lds\nM 8,180,112\n"
"s_lshl_b32 s61, s42, 13\ns_add_u32 s61, s61, 84148224\nbuffer_load_dwordx4 v[144:147], v234, s[20:23], s61 offen sc1\nM 24,176,100\nR 170,184,53248\nM 24,180,116\n"
"M 40,176,104\nR 172,184,57344\nM 40,180,120\ns_lshl_b32 s61, s42, 13\ns_add_u32 s61, s61, 84152320\nbuffer_load_dwordx4 v[148:151], v234, s[20:23], s61 offen sc1\n"
"M 56,176,108\nR 174,184,61440\nM 56,180,124\nG 4\nM 60,160,108\nR 176,185,49152\n"
"M 60,164,124\nM 44,160,104\nR 178,185,53248\nM 44,164,120\ns_add_u32 m0, s50, 98304\nM 28,160,100\n"
"R 180,185,57344\nM 28,164,116\ns_add_u32 s63, s52, 1968128\nM 12,160,96\nR 182,185,61440\nM 12,164,112\n"
"s_waitcnt vmcnt(15)\ns_barrier\nG 4\nN 64,96,168\nR 160,186,49152\ns_waitcnt vmcnt(14)\n"
"v_min3_u32 v232, v129, v131, v133\nv_min3_u32 v232, v232, v135, v135\nN 64,112,172\nbuffer_load_dwordx4 v220, s[4:7], s63 offen lds\nv_max3_u32 v233, v129, v131, v133\nN 68,100,168\n"
"R 162,186,53248\nv_max3_u32 v233, v233, v135, v135\nv_cmp_ne_u32 s[86:87], s46, v232\nN 68,116,172\nv_cmp_ne_u32 s[88:89], s46, v233\nN 96,104,168\n"
"R 164,186,57344\ns_or_b64 s[86:87], s[86:87], s[88:89]\ns_cbranch_scc1 Lq116\nLq117:\nN 96,120,172\nbuffer_load_dwordx4 v221, s[4:7], s63 offen offset:1024 lds\n"
"v_add_f32 v128, v128, v132\nN 100,108,168\nR 166,186,61440\ns_add_u32 m0, s50, 106496\nv_add_f32 v130, v130, v134\nN 100,124,172\n"
"G 4\nv_add_f32_dpp v128, v128, v128 row_ror:8 row_mask:0xf bank_mask:0xf\nv_add_f32_dpp v130, v130, v130 row_ror:8 row_mask:0xf bank_mask:0xf\nv_mov_b32 v132, v128\nN 108,108,176\nR 168,187,49152\n"
"s_add_u32 s63, s52, 2164736\nv_mov_b32 v133, v130\nN 108,124,180\nbuffer_load_dwordx4 v220, s[4:7], s63 offen lds\nv_permlane32_swap_b32 v132, v128\nN 104,104,176\n"
"R 170,187,53248\nv_permlane32_swap_b32 v133, v130\nv_add_f32 v128, v128, v132\nN 104,120,180\nv_add_f32 v130, v130, v133\nv_mov_b32 v132, v128\n"
"v_mov_b32 v133, v130\nN 76,100,176\nR 172,187,57344\nv_permlane16_swap_b32 v132, v128\nv_permlane16_swap_b32 v133, v130\nN 76,116,180\n"
"buffer_load_dwordx4 v221, s[4:7], s63 offen offset:1024 lds\nv_add_f32 v128, v128, v132\nN 72,96,176\nR 174,187,61440\ns_add_u32 s63, s52, 1970176\nN 72,112,180\n"
"G 4\nv_add_f32 v130, v130, v133\nv_and_b32 v136, s77, v128\nN 80,96,160\nR 176,188,0\nv_sub_f32 v137, v128, v136\n"
"N 80,112,164\ns_add_u32 m0, s50, 114688\nv_cvt_pk_bf16_f32 v132, v136, v137\nv_cvt_pk_bf16_f32 v133, v136, 0\nN 84,100,160\nR 178,188,4096\n"
"v_and_b32 v136, s77, v130\nN 84,116,164\nLpg2:\ns_cbranch_execz LretQ0\nbuffer_load_dwordx4 v220, s[4:7], s63 offen lds\nN 112,104,160\n"
"R 180,188,8192\nv_sub_f32 v137, v130, v136\nN 112,120,164\nv_cvt_pk_bf16_f32 v134, v136, v137\nv_cvt_pk_bf16_f32 v135, v136, 0\nN 116,108,160\n"
"R 182,188,12288\nv_and_b32 v230, 0x1ff, v230\nN 116,124,164\nv_add_u32 v230, 156160, v230\nds_write_b128 v230, v[132:135]\nG 5\n"
"N 124,108,168\nR 160,189,0\nN 124,124,172\nbuffer_load_dwordx4 v221, s[4:7], s63 offen offset:1024 lds\nN 120,104,168\nR 162,189,4096\n"
"s_add_u32 s63, s52, 2166784\nN 120,120,172\ns_add_u32 m0, s50, 0\nN 92,100,168\nR 164,189,8192\nN 92,116,172\n"
"buffer_load_dwordx4 v220, s[4:7], s63 offen lds\nN 88,96,168\nR 166,189,12288\nN 88,112,172\ns_waitcnt vmcnt(10)\nds_read_b128 a[160:163], v248 offset:0\n"
"s_waitcnt vmcnt(9) lgkmcnt(5)\ns_barrier\nM 0,176,128\nR 168,190,0\nds_read_b128 a[164:167], v248 offset:2048\nM 0,180,144\n"
"ds_read_b128 a[168:171], v248 offset:4096\nM 16,176,132\nR 170,190,4096\ns_waitcnt vmcnt(7)\nv_min3_u32 v236, v145, v147, v149\nv_min3_u32 v236, v236, v151, v151\n"
"M 16,180,148\nds_read_b128 a[172:175], v248 offset:6144\nv_max3_u32 v237, v145, v147, v149\nv_max3_u32 v237, v237, v151, v151\nM 32,176,136\nR 172,190,8192\n"
"ds_read_b128 a[176:179], v249 offset:0\nv_cmp_ne_u32 s[86:87], s46, v236\nM 32,180,152\nds_read_b128 a[180:183], v249 offset:2048\nv_lshrrev_b32 v230, 7, v252\nv_cmp_ne_u32 s[88:89], s46, v237\n"
"M 48,176,140\nR 174,190,12288\nds_read_b128 a[184:187], v249 offset:4096\ns_or_b64 s[86:87], s[86:87], s[88:89]\ns_cbranch_scc1 Lq124\nLq125:\n"
"M 48,180,156\nds_read_b128 a[188:191], v249 offset:6144\nG 12\nv_add_f32 v144, v144, v148\nM 52,160,140\nR 176,191,0\n"
"v_add_f32 v146, v146, v150\nv_add_f32_dpp v144, v144, v144 row_ror:8 row_mask:0xf bank_mask:0xf\nM 52,164,156\nbuffer_load_dwordx4 v221, s[4:7], s63 offen offset:1024 lds\nv_lshlrev_b32 v230, 9, v230\nv_add_f32_dpp v146, v146, v146 row_ror:8 row_mask:0xf bank_mask:0xf\n"
"M 36,160,136\nR 178,191,4096\nv_and_b32 v231, 0x70, v252\nv_mov_b32 v148, v144\nM 36,164,152\nG 2\n"
"s_add_u32 m0, s51, 0\ns_add_u32 s60, s70, 768\nbuffer_load_dwordx4 v226, s[24:27], s60 offen lds\nv_mov_b32 v149, v146\nM 20,160,132\nR 180,191,8192\n"
"v_permlane32_swap_b32 v148, v144\nv_permlane32_swap_b32 v149, v146\nM 20,164,148\ns_add_u32 m0, s51, 1024\ns_add_u32 s60, s70, 17152\nbuffer_load_dwordx4 v227, s[24:27], s60 offen lds\n"
"v_add_f32 v144, v144, v148\nM 4,160,128\nR 182,191,12288\ns_and_b32 s60, s40, 3\ns_lshl_b32 s60, s60, 7\nv_add3_u32 v230, v230, v231, s60\n"
"v_add_f32 v146, v146, v149\nM 4,164,144\ns_add_u32 m0, s51, 2048\ns_add_u32 s60, s70, 33536\nbuffer_load_dwordx4 v226, s[24:27], s60 offen lds\nv_mov_b32 v148, v144\n"
"M 8,168,128\nR 160,188,16384\nv_mov_b32 v149, v146\nv_permlane16_swap_b32 v148, v144\nM 8,172,144\ns_add_u32 m0, s51, 3072\n"
"s_add_u32 s60, s70, 49920\nbuffer_load_dwordx4 v227, s[24:27], s60 offen lds\nv_permlane16_swap_b32 v149, v146\nM 24,168,132\nR 162,188,20480\ns_lshl_b32 s61, s42, 13\n"
"s_add_u32 s61, s61, 83886080\nbuffer_load_dwordx4 v[128:131], v230, s[20:23], s61 offen sc1\nv_add_f32 v144, v144, v148\nM 24,172,148\ns_add_u32 m0, s51, 4096\ns_add_u32 s60, s71, 768\n"
"buffer_load_dwordx4 v226, s[24:27], s60 offen lds\nv_add_f32 v146, v146, v149\nM 40,168,136\nR 164,188,24576\ns_lshl_b32 s61, s42, 13\ns_add_u32 s61, s61, 83890176\n"
"buffer_load_dwordx4 v[132:135], v230, s[20:23], s61 offen sc1\nv_and_b32 v152, s77, v144\nM 40,172,152\ns_add_u32 m0, s51, 5120\ns_add_u32 s60, s71, 17152\nbuffer_load_dwordx4 v227, s[24:27], s60 offen lds\n"
"M 56,168,140\nR 166,188,28672\nv_sub_f32 v153, v144, v152\nM 56,172,156\ns_add_u32 m0, s51, 6144\ns_add_u32 s60, s71, 33536\n"
"buffer_load_dwordx4 v226, s[24:27], s60 offen lds\nG 4\nM 60,176,140\nR 168,189,16384\nv_cvt_pk_bf16_f32 v148, v152, v153\nM 60,180,156\n"
"s_add_u32 m0, s51, 7168\ns_add_u32 s60, s71, 49920\nbuffer_load_dwordx4 v227, s[24:27], s60 offen lds\nM 44,176,136\nR 170,189,20480\nv_cvt_pk_bf16_f32 v149, v152, 0\n"
"M 44,180,152\ns_add_u32 m0, s50, 8192\nv_and_b32 v152, s77, v146\nv_sub_f32 v153, v146, v152\nM 28,176,132\nR 172,189,24576\n"
"v_cvt_pk_bf16_f32 v150, v152, v153\nM 28,180,148\ns_add_u32 s63, s52, 2361344\nv_cvt_pk_bf16_f32 v151, v152, 0\nv_and_b32 v234, 0x1ff, v234\nM 12,176,128\n"
"R 174,189,28672\nv_add_u32 v234, 156672, v234\nM 12,180,144\ns_waitcnt vmcnt(14)\ns_barrier\nG 4\n"
"N 64,128,160\nR 176,190,16384\nds_write_b128 v234, v[148:151]\nN 64,144,164\nbuffer_load_dwordx4 v220, s[4:7], s63 offen lds\nN 68,132,160\n"
"R 178,190,20480\nN 68,148,164\nN 96,136,160\nR 180,190,24576\nN 96,152,164\nbuffer_load_dwordx4 v221, s[4:7], s63 offen offset:1024 lds\n"
"N 100,140,160\nR 182,190,28672\ns_add_u32 m0, s50, 16384\nN 100,156,164\nG 5\nN 108,140,168\n"
"R 160,191,16384\ns_add_u32 s63, s52, 2557952\nN 108,156,172\nbuffer_load_dwordx4 v220, s[4:7], s63 offen lds\nN 104,136,168\nR 162,191,20480\n"
"N 104,152,172\nN 76,132,168\nR 164,191,24576\nLq183:\ns_lshr_b32 s60, s45, 3\ns_lshl2_add_u32 s60, s60, s40\n"
"s_lshl_b32 s60, s60, 8\nv_lshl_add_u32 v224, v252, 3, s60\nv_add_u32 v224, Lq183+4096-LqPC, v224\nv_min_u32 v224, LqE-LqPC-4, v224\ns_mov_b64 exec, 3\nglobal_load_dword v224, v224, s[98:99]\n"
"s_mov_b64 exec, -1\nN 76,148,172\nbuffer_load_dwordx4 v221, s[4:7], s63 offen offset:1024 lds\nN 72,128,168\nR 166,191,28672\nN 72,144,172\n"
"G 4\nN 80,128,176\nR 168,188,32768\nN 80,144,180\nN 84,132,176\nR 170,188,36864\n"
"N 84,148,180\ns_add_u32 m0, s50, 24576\nN 112,136,176\nR 172,188,40960\nN 112,152,180\ns_add_u32 s63, s52, 2754560\n"
"N 116,140,176\nR 174,188,45056\nN 116,156,180\nG 4\nN 124,140,160\nR 176,189,32768\n"
"N 124,156,164\nbuffer_load_dwordx4 v220, s[4:7], s63 offen lds\nN 120,136,160\nR 178,189,36864\nN 120,152,164\nN 92,132,160\n"
"R 180,189,40960\ns_waitcnt vmcnt(9)\nv_min3_u32 v232, v129, v131, v133\nv_min3_u32 v232, v232, v135, v135\nN 92,148,164\nbuffer_load_dwordx4 v221, s[4:7], s63 offen offset:1024 lds\n"
"v_max3_u32 v233, v129, v131, v133\nN 88,128,160\nR 182,189,45056\ns_add_u32 m0, s50, 32768\nv_max3_u32 v233, v233, v135, v135\nN 88,144,164\n"
"s_barrier\nG 4\nv_cmp_ne_u32 s[86:87], s46, v232\nM 0,168,160\nR 160,190,32768\ns_add_u32 s63, s52, 2951168\n"
"v_cmp_ne_u32 s[88:89], s46, v233\nM 0,172,176\nbuffer_load_dwordx4 v220, s[4:7], s63 offen lds\ns_or_b64 s[86:87], s[86:87], s[88:89]\ns_cbranch_scc1 Lq132\nLq133:\n"
"M 16,168,164\nR 162,190,36864\nv_add_f32 v128, v128, v132\nv_add_f32 v130, v130, v134\nM 16,172,180\nv_add_f32_dpp v128, v128, v128 row_ror:8 row_mask:0xf bank_mask:0xf\n"
"v_add_f32_dpp v130, v130, v130 row_ror:8 row_mask:0xf bank_mask:0xf\nv_mov_b32 v132, v128\nM 32,168,168\nR 164,190,40960\nv_mov_b32 v133, v130\nv_permlane32_swap_b32 v132, v128\n"
"M 32,172,184\nv_permlane32_swap_b32 v133, v130\nv_add_f32 v128, v128, v132\nv_add_f32 v130, v130, v133\nM 48,168,172\nR 166,190,45056\n"
"v_mov_b32 v132, v128\nv_mov_b32 v133, v130\nM 48,172,188\nG 4\nv_permlane16_swap_b32 v132, v128\nv_permlane16_swap_b32 v133, v130\n"
"v_add_f32 v128, v128, v132\nM 52,176,172\nR 168,191,32768\nv_add_f32 v130, v130, v133\nv_and_b32 v136, s77, v128\nM 52,180,188\n"
"buffer_load_dwordx4 v221, s[4:7], s63 offen offset:1024 lds\nv_sub_f32 v137, v128, v136\nM 36,176,168\nR 170,191,36864\ns_waitcnt vmcnt(9)\nds_read_b128 a[192:195], v248 offset:0\n"
"v_cvt_pk_bf16_f32 v132, v136, v137\nM 36,180,184\nds_read_b128 a[196:199], v248 offset:2048\nv_cvt_pk_bf16_f32 v133, v136, 0\nv_and_b32 v136, s77, v130\nM 20,176,164\n"
"R 172,191,40960\nds_read_b128 a[200:203], v248 offset:4096\nv_sub_f32 v137, v130, v136\nM 20,180,180\nds_read_b128 a[204:207], v248 offset:6144\nv_cvt_pk_bf16_f32 v134, v136, v137\n"
"v_cvt_pk_bf16_f32 v135, v136, 0\nM 4,176,160\nR 174,191,45056\nv_and_b32 v230, 0x1ff, v230\nv_add_u32 v230, 155648, v230\nM 4,180,176\n"
"ds_read_b128 a[208:211], v249 offset:0\nds_write_b128 v230, v[132:135]\nG 10\nM 8,160,160\nR 176,188,49152\nds_read_b128 a[212:215], v249 offset:2048\n"
"M 8,164,176\nds_read_b128 a[216:219], v249 offset:4096\ns_add_u32 m0, s50, 40960\nM 24,160,164\nR 178,188,53248\ns_add_u32 s63, s52, 2363392\n"
"M 24,164,180\nds_read_b128 a[220:223], v249 offset:6144\nM 40,160,168\nR 180,184,0\nM 40,164,184\nbuffer_load_dwordx4 v220, s[4:7], s63 offen lds\n"
"M 56,160,172\nR 182,184,4096\nM 56,164,188\nG 2\ns_add_u32 m0, s51, 0\ns_add_u32 s60, s70, 896\n"
"buffer_load_dwordx4 v226, s[24:27], s60 offen lds\nM 60,168,172\nR 160,189,49152\nM 60,172,188\ns_add_u32 m0, s51, 1024\ns_add_u32 s60, s70, 17280\n"
"buffer_load_dwordx4 v227, s[24:27], s60 offen lds\nM 44,168,168\nR 162,189,53248\nM 44,172,184\ns_add_u32 m0, s51, 2048\ns_add_u32 s60, s70, 33664\n"
"buffer_load_dwordx4 v226, s[24:27], s60 offen lds\nM 28,168,164\nR 164,185,0\nM 28,172,180\ns_add_u32 m0, s51, 3072\ns_add_u32 s60, s70, 50048\n"
"buffer_load_dwordx4 v227, s[24:27], s60 offen lds\nM 12,168,160\nR 166,185,4096\nM 12,172,176\ns_add_u32 m0, s51, 4096\ns_add_u32 s60, s71, 896\n"
"buffer_load_dwordx4 v226, s[24:27], s60 offen lds\ns_waitcnt vmcnt(10)\ns_barrier\nG 4\nN 64,160,176\nR 168,190,49152\n"
"N 64,176,180\ns_add_u32 m0, s51, 5120\ns_add_u32 s60, s71, 17280\nbuffer_load_dwordx4 v227, s[24:27], s60 offen lds\nN 68,164,176\nR 170,190,53248\n"
"s_lshr_b32 s84, s49, 4\nN 68,180,180\ns_add_u32 m0, s51, 6144\ns_add_u32 s60, s71, 33664\nbuffer_load_dwordx4 v226, s[24:27], s60 offen lds\nN 96,168,176\n"
"R 172,186,0\ns_add_u32 s84, s84, 81788928\nN 96,184,180\ns_add_u32 m0, s51, 7168\ns_add_u32 s60, s71, 50048\nbuffer_load_dwordx4 v227, s[24:27], s60 offen lds\n"
"N 100,172,176\nR 174,186,4096\ns_add_u32 m0, s50, 40960\nN 100,188,180\nG 4\nN 108,172,160\n"
"R 176,191,49152\nbuffer_load_dwordx4 v221, s[4:7], s63 offen offset:1024 lds\nN 108,188,164\nbuffer_load_dwordx4 v[128:131], v252, s[20:23], s84 offen offset:0 sc1\ns_add_u32 s63, s52, 2560000\nN 104,168,160\n"
"R 178,191,53248\ns_add_u32 m0, s50, 49152\nN 104,184,164\nbuffer_load_dwordx4 v[132:135], v252, s[20:23], s84 offen offset:1024 sc1\nN 76,164,160\nR 180,187,0\n"
"buffer_load_dwordx4 v220, s[4:7], s63 offen lds\nN 76,180,164\nbuffer_load_dwordx4 v221, s[4:7], s63 offen offset:1024 lds\nN 72,160,160\nR 182,187,4096\nN 72,176,164\n"
"G 4\nN 80,160,168\nR 160,184,8192\nN 80,176,172\nbuffer_load_dwordx4 v[136:139], v252, s[20:23], s84 offen offset:2048 sc1\nN 84,164,168\n"
"R 162,184,12288\nN 84,180,172\nbuffer_load_dwordx4 v[140:143], v252, s[20:23], s84 offen offset:3072 sc1\nN 112,168,168\nR 164,184,16384\nN 112,184,172\n"
"N 116,172,168\nR 166,184,20480\nN 116,188,172\nG 4\nN 124,172,176\nR 168,185,8192\n"
"N 124,188,180\nN 120,168,176\nR 170,185,12288\nN 120,184,180\ns_add_u32 m0, s50, 57344\nN 92,164,176\n"
"R 172,185,16384\nN 92,180,180\ns_add_u32 s63, s52, 2756608\nN 88,160,176\nR 174,185,20480\nN 88,176,180\n"
"s_waitcnt vmcnt(16)\ns_barrier\nG 4\nM 0,160,192\nR 176,186,8192\nM 0,164,208\n"
"buffer_load_dwordx4 v220, s[4:7], s63 offen lds\nM 16,160,196\nR 178,186,12288\nM 16,164,212\nM 32,160,200\nR 180,186,16384\n"
"M 32,164,216\nbuffer_load_dwordx4 v221, s[4:7], s63 offen offset:1024 lds\nM 48,160,204\nR 182,186,20480\ns_lshl2_add_u32 s60, s45, s40\nv_lshlrev_b32 v224, 3, v252\n"
"s_mul_i32 s60, s60, 2048\ns_bfm_b64 exec, 16, 0\nbuffer_load_dword v224, v224, s[8:11], s60 offen\ns_mov_b64 exec, -1\nM 48,164,220\ns_add_u32 s63, s52, 2953216\n"
"G 4\nM 52,168,204\nR 160,187,8192\ns_add_u32 m0, s50, 65536\nM 52,172,220\nbuffer_load_dwordx4 v220, s[4:7], s63 offen lds\n"
"M 36,168,200\nR 162,187,12288\nM 36,172,216\nM 20,168,196\nR 164,187,16384\nM 20,172,212\n"
"buffer_load_dwordx4 v221, s[4:7], s63 offen offset:1024 lds\nM 4,168,192\nR 166,187,20480\nM 4,172,208\nG 4\nM 8,176,192\n"
"R 168,184,24576\nM 8,180,208\nM 24,176,196\nR 170,184,28672\ns_waitcnt vmcnt(12)\nds_read_b128 a[224:227], v248 offset:0\n"
"M 24,180,212\nds_read_b128 a[228:231], v248 offset:2048\nM 40,176,200\nR 172,184,32768\nds_read_b128 a[232:235], v248 offset:4096\nM 40,180,216\n"
"ds_read_b128 a[236:239], v248 offset:6144\nM 56,176,204\nR 174,184,36864\nM 56,180,220\nds_read_b128 a[240:243], v249 offset:0\nG 9\n"
"M 60,160,204\nR 176,185,24576\nds_read_b128 a[244:247], v249 offset:2048\nM 60,164,220\nds_read_b128 a[248:251], v249 offset:4096\ns_add_u32 m0, s50, 73728\n"
"M 44,160,200\nR 178,185,28672\ns_add_u32 s63, s52, 0\nM 44,164,216\nds_read_b128 a[252:255], v249 offset:6144\nM 28,160,196\n"
"R 180,185,32768\nM 28,164,212\nbuffer_load_dwordx4 v220, s[4:7], s63 offen lds\nM 12,160,192\nR 182,185,36864\nM 12,164,208\n"
"s_waitcnt vmcnt(8)\ns_barrier\nG 8\nM 0,168,224\nR 160,186,24576\ns_mov_b32 s85, 0x0c0c0400\n"
"v_perm_b32 v144, v129, v128, s85\nG 8\nM 0,172,240\nbuffer_load_dwordx4 v221, s[4:7], s63 offen offset:1024 lds\nv_perm_b32 v148, v131, v130, s85\nM 16,168,228\n"
"R 162,186,28672\ns_add_u32 s63, s52, 196608\nv_lshl_or_b32 v144, v148, 16, v144\nG 7\nM 16,172,244\ns_add_u32 m0, s50, 81920\n"
"v_perm_b32 v145, v133, v132, s85\nv_perm_b32 v149, v135, v134, s85\nv_lshl_or_b32 v145, v149, 16, v145\nM 32,168,232\nR 164,186,32768\ns_waitcnt vmcnt(8)\n"
"v_perm_b32 v146, v137, v136, s85\nv_perm_b32 v150, v139, v138, s85\nG 7\nM 32,172,248\nbuffer_load_dwordx4 v220, s[4:7], s63 offen lds\nv_lshl_or_b32 v146, v150, 16, v146\n"
"M 48,168,236\nR 166,186,36864\ns_lshl_b32 s60, s90, 14\ns_add_u32 s70, s49, s60\ns_xor_b32 s71, s70, 0x10000\ns_waitcnt vmcnt(8)\n"
"v_perm_b32 v147, v141, v140, s85\nG 6\nM 48,172,252\nG 4\nv_perm_b32 v151, v143, v142, s85\nv_lshl_or_b32 v147, v151, 16, v147\n"
"v_min3_u32 v230, v144, v145, v146\nM 52,176,236\nR 168,187,24576\nv_min3_u32 v230, v230, v147, v147\nv_max3_u32 v231, v144, v145, v146\nM 52,180,252\n"
"buffer_load_dwordx4 v221, s[4:7], s63 offen offset:1024 lds\nv_max3_u32 v231, v231, v147, v147\nLpg3:\ns_cbranch_execz LretQ0\nM 36,176,232\nR 170,187,28672\n"
"s_add_u32 s63, s52, 393216\nv_cmp_ne_u32 s[86:87], s46, v230\nM 36,180,248\ns_add_u32 m0, s50, 90112\nv_cmp_ne_u32 s[88:89], s46, v231\nM 20,176,228\n"
"R 172,187,32768\ns_or_b64 s[86:87], s[86:87], s[88:89]\ns_cbranch_scc1 Lq140\nLq141:\nM 20,180,244\nbuffer_load_dwordx4 v220, s[4:7], s63 offen lds\n"
"v_and_b32 v128, 0xffffff00, v128\nM 4,176,224\nR 174,187,36864\nv_and_b32 v129, 0xffffff00, v129\nv_and_b32 v130, 0xffffff00, v130\nM 4,180,240\n"
"G 4\nv_and_b32 v131, 0xffffff00, v131\nv_and_b32 v132, 0xffffff00, v132\nv_and_b32 v133, 0xffffff00, v133\nM 8,160,224\nR 176,184,40960\n"
"v_and_b32 v134, 0xffffff00, v134\nv_and_b32 v135, 0xffffff00, v135\nM 8,164,240\nbuffer_load_dwordx4 v221, s[4:7], s63 offen offset:1024 lds\nv_and_b32 v136, 0xffffff00, v136\nM 24,160,228\n"
"R 178,184,45056\nv_and_b32 v137, 0xffffff00, v137\nv_and_b32 v138, 0xffffff00, v138\nM 24,164,244\ns_add_u32 m0, s50, 98304\nv_and_b32 v139, 0xffffff00, v139\n"
"v_and_b32 v140, 0xffffff00, v140\nv_and_b32 v141, 0xffffff00, v141\nM 40,160,232\nR 180,184,49152\nv_and_b32 v142, 0xffffff00, v142\nv_and_b32 v143, 0xffffff00, v143\n"
"M 40,164,248\ns_add_u32 s63, s52, 589824\nv_add_f32 v128, v128, v136\nv_add_f32 v129, v129, v137\nv_add_f32 v130, v130, v138\nM 56,160,236\n"
"R 182,184,53248\nv_add_f32 v131, v131, v139\nv_add_f32 v132, v132, v140\nM 56,164,252\nG 4\nv_add_f32 v133, v133, v141\n"
"v_add_f32 v134, v134, v142\nv_add_f32 v135, v135, v143\nM 60,168,236\nR 160,185,40960\nv_add_f32 v128, v128, v132\nv_add_f32 v129, v129, v133\n"
"M 60,172,252\nbuffer_load_dwordx4 v220, s[4:7], s63 offen lds\nv_add_f32 v130, v130, v134\nM 44,168,232\nR 162,185,45056\nv_add_f32 v131, v131, v135\n"
"v_permlane32_swap_b32 v128, v130\nM 44,172,248\nv_permlane32_swap_b32 v129, v131\nv_add_f32 v128, v128, v130\nv_add_f32 v129, v129, v131\nM 28,168,228\n"
"R 164,185,49152\nv_permlane16_swap_b32 v128, v129\nv_add_f32 v230, v128, v129\nM 28,172,244\nbuffer_load_dwordx4 v221, s[4:7], s63 offen offset:1024 lds\nv_mov_b32 v232, v230\n"
"M 12,168,224\nR 166,185,53248\nv_permlane16_swap_b32 v230, v232\nv_mov_b32 v231, v230\nM 12,172,240\ns_waitcnt vmcnt(8)\n"
"s_barrier\nG 4\nv_mov_b32 v233, v232\nN 64,192,176\nR 168,186,40960\nv_permlane32_swap_b32 v230, v231\n"
"v_permlane32_swap_b32 v232, v233\nN 64,208,180\ns_mov_b32 m0, s51\nv_mul_f32 v230, 0xba800000, v230\nv_mul_f32 v214, v230, v230\nv_mul_f32 v231, 0x3a800000, v231\n"
"N 68,196,176\nR 170,186,45056\ns_lshl2_add_u32 s60, s45, s40\nv_lshlrev_b32 v224, 3, v252\ns_mul_i32 s60, s60, 7168\ns_bfm_b64 exec, 14, 0\n"
"buffer_load_dword v224, v224, s[12:15], s60 offen\ns_mov_b64 exec, -1\nds_write_addtid_b32 v32 offset:0\nv_sub_f32 v231, v231, v214\nN 68,212,180\nds_write_addtid_b32 v33 offset:256\n"
"ds_write_addtid_b32 v34 offset:512\nv_max_f32 v231, 0, v231\nv_add_f32 v231, 0x3727c5ac, v231\nN 96,200,176\nR 172,186,49152\ns_mov_b32 s60, 156160\n"
"v_bfe_u32 v246, v252, 4, 4\nv_lshl_add_u32 v246, v246, 3, s60\nds_write_addtid_b32 v35 offset:768\nv_rsq_f32 v234, v231\nN 96,216,180\nds_read_b64 v[238:239], v246 offset:0\n"
"ds_write_addtid_b32 v36 offset:1024\nv_and_b32 v208, s77, v230\nv_mul_f32 v216, v231, v234\nN 100,204,176\nR 174,186,53248\nds_read_b64 v[240:241], v246 offset:128\n"
"ds_write_addtid_b32 v37 offset:1280\nv_sub_f32 v230, v230, v208\nN 100,220,180\nds_read_b64 v[242:243], v246 offset:256\nds_write_addtid_b32 v38 offset:1536\nG 14\n"
"v_cvt_pk_bf16_f32 v209, v230, v216\nN 108,204,160\nR 176,187,40960\nds_read_b64 v[244:245], v246 offset:384\nds_write_addtid_b32 v39 offset:1792\nv_cvt_pk_bf16_f32 v208, v208, v208\n"
"N 108,220,164\nds_read_b64 v[246:247], v246 offset:0\nds_write_addtid_b32 v40 offset:2048\nv_mov_b32 v214, v234\nv_mul_f32 v215, 0x3e8293ee, v234\nN 104,200,160\n"
"R 178,187,45056\nds_write_addtid_b32 v41 offset:2304\nv_mul_f32 v232, 0xba800000, v232\nv_mul_f32 v217, v232, v232\nN 104,216,164\nds_write_addtid_b32 v42 offset:2560\n"
"ds_write_addtid_b32 v43 offset:2816\nv_mul_f32 v233, 0x3a800000, v233\nv_sub_f32 v233, v233, v217\nN 76,196,160\nR 180,187,49152\nds_write_addtid_b32 v44 offset:3072\n"
"v_max_f32 v233, 0, v233\nv_add_f32 v233, 0x3727c5ac, v233\nN 76,212,164\nds_write_addtid_b32 v45 offset:3328\nv_and_b32 v212, s77, v232\nv_rsq_f32 v235, v233\n"
"s_nop 0\nv_mul_f32 v219, v233, v235\nv_sub_f32 v232, v232, v212\nv_cvt_pk_bf16_f32 v213, v232, v219\nv_cvt_pk_bf16_f32 v212, v212, v212\nv_mov_b32 v217, v235\n"
"v_mul_f32 v218, 0x3e8293ee, v235\ns_mov_b64 s[60:61], 0xffff\nv_cndmask_b32 v208, 0, v208, s[60:61]\ns_mov_b64 s[60:61], 0xffff\nv_cndmask_b32 v209, 0, v209, s[60:61]\ns_mov_b64 s[60:61], 0xffff\n"
"v_cndmask_b32 v212, 0, v212, s[60:61]\ns_mov_b64 s[60:61], 0xffff\nv_cndmask_b32 v213, 0, v213, s[60:61]\nv_bfe_u32 v230, v252, 2, 6\ns_lshl_b32 s60, s40, 7\ns_add_u32 s60, s60, 161280\n"
"v_add_u32 v230, s60, v230\nds_write_b32 v230, v214 offset:0\nds_write_b32 v230, v217 offset:64\nds_write_addtid_b32 v46 offset:3584\nN 72,192,160\nR 182,187,53248\n"
"ds_write_addtid_b32 v47 offset:3840\nN 72,208,164\nds_write_addtid_b32 v48 offset:4096\nds_write_addtid_b32 v49 offset:4352\nG 15\nN 80,192,168\n"
"R 160,184,57344\nds_write_addtid_b32 v50 offset:4608\nN 80,208,172\nds_write_addtid_b32 v51 offset:4864\nds_write_addtid_b32 v52 offset:5120\nN 84,196,168\n"
"R 162,184,61440\nv_mfma_f32_16x16x32_bf16 v[0:3], v[238:241], v[208:211], v[0:3]\nds_write_addtid_b32 v53 offset:5376\nN 84,212,172\nv_mfma_f32_16x16x32_bf16 v[4:7], v[240:243], v[208:211], v[4:7]\nds_write_addtid_b32 v54 offset:5632\n"
"ds_write_addtid_b32 v55 offset:5888\nN 112,200,168\nR 164,188,0\nv_mfma_f32_16x16x32_bf16 v[8:11], v[242:245], v[208:211], v[8:11]\nds_write_addtid_b32 v56 offset:6144\nN 112,216,172\n"
"v_mfma_f32_16x16x32_bf16 v[12:15], v[244:247], v[208:211], v[12:15]\nds_write_addtid_b32 v57 offset:6400\nds_write_addtid_b32 v58 offset:6656\nN 116,204,168\nR 166,188,4096\nv_mfma_f32_16x16x32_bf16 v[16:19], v[244:247], v[210:213], v[16:19]\n"
"ds_write_addtid_b32 v59 offset:6912\nN 116,220,172\nv_mfma_f32_16x16x32_bf16 v[20:23], v[238:241], v[210:213], v[20:23]\nds_write_addtid_b32 v60 offset:7168\nds_write_addtid_b32 v61 offset:7424\nds_write_addtid_b32 v62 offset:7680\n"
"ds_write_addtid_b32 v63 offset:7936\nG 15\nN 124,204,176\nR 168,185,57344\nv_mfma_f32_16x16x32_bf16 v[24:27], v[240:243], v[210:213], v[24:27]\ns_add_u32 m0, s50, 106496\n"
"s_add_u32 s63, s52, 786432\nN 124,220,180\nv_mfma_f32_16x16x32_bf16 v[28:31], v[242:245], v[210:213], v[28:31]\nbuffer_load_dwordx4 v220, s[4:7], s63 offen lds\nN 120,200,176\nR 170,185,61440\n"
"s_waitcnt vmcnt(6) lgkmcnt(2)\ns_barrier\nN 120,216,180\nds_read2st64_b32 v[32:33], v225 offset0:0 offset1:1\nds_read2st64_b32 v[34:35], v225 offset0:2 offset1:3\nN 92,196,176\n"
"R 172,189,0\nds_read2st64_b32 v[36:37], v225 offset0:4 offset1:5\nN 92,212,180\nbuffer_load_dwordx4 v221, s[4:7], s63 offen offset:1024 lds\nds_read2st64_b32 v[38:39], v225 offset0:6 offset1:7\nN 88,192,176\n"
"R 174,189,4096\nds_read2st64_b32 v[40:41], v225 offset0:8 offset1:9\nN 88,208,180\nds_read2st64_b32 v[42:43], v225 offset0:10 offset1:11\nds_read2st64_b32 v[44:45], v225 offset0:12 offset1:13\nN 64,224,160\n"
"R 176,186,57344\ns_mov_b32 s60, 156672\nv_bfe_u32 v246, v252, 4, 4\nv_lshl_add_u32 v246, v246, 3, s60\nds_read2st64_b32 v[46:47], v225 offset0:14 offset1:15\nN 64,240,164\n"
"ds_read_b64 v[238:239], v246 offset:0\ns_add_u32 m0, s50, 114688\nds_read2st64_b32 v[48:49], v225 offset0:16 offset1:17\nN 68,228,160\nR 178,186,61440\nds_read_b64 v[240:241], v246 offset:128\n"
"ds_read2st64_b32 v[50:51], v225 offset0:18 offset1:19\nN 68,244,164\nds_read_b64 v[242:243], v246 offset:256\ns_add_u32 s63, s52, 983040\nds_read2st64_b32 v[52:53], v225 offset0:20 offset1:21\nN 96,232,160\n"
"R 180,190,0\nds_read_b64 v[244:245], v246 offset:384\nds_read2st64_b32 v[54:55], v225 offset0:22 offset1:23\nN 96,248,164\nds_read_b64 v[246:247], v246 offset:0\nds_read2st64_b32 v[56:57], v225 offset0:24 offset1:25\n"
"G 15\nv_add_f32 v0, v0, v32\nN 100,236,160\nR 182,190,4096\nbuffer_load_dwordx4 v220, s[4:7], s63 offen lds\nds_read2st64_b32 v[58:59], v225 offset0:26 offset1:27\n"
"N 100,252,164\nds_read2st64_b32 v[60:61], v225 offset0:28 offset1:29\nds_read2st64_b32 v[62:63], v225 offset0:30 offset1:31\nN 108,236,168\nR 160,187,57344\nv_add_f32 v1, v1, v33\n"
"v_add_f32 v2, v2, v34\nN 108,252,172\nbuffer_load_dwordx4 v221, s[4:7], s63 offen offset:1024 lds\nv_add_f32 v3, v3, v35\nN 104,232,168\nR 162,187,61440\n"
"s_add_u32 m0, s50, 0\ns_add_u32 s63, s52, 1179648\nv_add_f32 v4, v4, v36\nN 104,248,172\nbuffer_load_dwordx4 v220, s[4:7], s63 offen lds\nv_add_f32 v5, v5, v37\n"
"N 76,228,168\nR 164,191,0\nv_add_f32 v6, v6, v38\nv_add_f32 v7, v7, v39\nN 76,244,172\nG 15\n"
"v_mfma_f32_16x16x32_bf16 v[64:67], v[208:211], v[238:241], v[64:67]\nv_add_f32 v8, v8, v40\nv_add_f32 v9, v9, v41\nv_add_f32 v10, v10, v42\nN 72,224,168\nR 166,191,4096\n"
"v_add_f32 v11, v11, v43\nv_add_f32 v12, v12, v44\nN 72,240,172\nG 14\nv_mfma_f32_16x16x32_bf16 v[72:75], v[208:211], v[240:243], v[72:75]\nG 7\n"
"v_add_f32 v13, v13, v45\nv_add_f32 v14, v14, v46\nv_add_f32 v15, v15, v47\nN 80,224,176\nR 168,188,8192\nbuffer_load_dwordx4 v221, s[4:7], s63 offen offset:1024 lds\n"
"v_add_f32 v16, v16, v48\nN 80,240,180\nv_mfma_f32_16x16x32_bf16 v[80:83], v[208:211], v[242:245], v[80:83]\ns_add_u32 m0, s50, 8192\ns_add_u32 s63, s52, 1376256\nbuffer_load_dwordx4 v220, s[4:7], s63 offen lds\n"
"v_add_f32 v17, v17, v49\nN 84,228,176\nR 170,188,12288\nv_add_f32 v18, v18, v50\nv_add_f32 v19, v19, v51\nN 84,244,180\n"
"v_mfma_f32_16x16x32_bf16 v[88:91], v[208:211], v[244:247], v[88:91]\nv_add_f32 v20, v20, v52\nv_add_f32 v21, v21, v53\nv_add_f32 v22, v22, v54\nN 112,232,176\nR 172,188,16384\n"
"v_add_f32 v23, v23, v55\nv_add_f32 v24, v24, v56\nN 112,248,180\nv_mfma_f32_16x16x32_bf16 v[68:71], v[210:213], v[244:247], v[68:71]\nbuffer_load_dwordx4 v221, s[4:7], s63 offen offset:1024 lds\nv_add_f32 v25, v25, v57\n"
"N 116,236,176\nR 174,188,20480\ns_add_u32 m0, s50, 32768\nG 10\nv_add_f32 v26, v26, v58\nN 116,252,180\n"
"v_mfma_f32_16x16x32_bf16 v[76:79], v[210:213], v[238:241], v[76:79]\nG 4\nv_add_f32 v27, v27, v59\nv_add_f32 v28, v28, v60\nv_add_f32 v29, v29, v61\nN 124,236,160\n"
"R 176,189,8192\ns_add_u32 s63, s52, 1572864\nv_add_f32 v30, v30, v62\nN 124,252,164\nv_mfma_f32_16x16x32_bf16 v[84:87], v[210:213], v[240:243], v[84:87]\nbuffer_load_dwordx4 v220, s[4:7], s63 offen lds\n"
"v_add_f32 v31, v31, v63\nN 120,232,160\nR 178,189,12288\ns_lshl4_add_u32 s64, s41, s42\nv_lshrrev_b32 v227, 1, v252\nN 120,248,164\n"
"v_mfma_f32_16x16x32_bf16 v[92:95], v[210:213], v[242:245], v[92:95]\nbuffer_load_dwordx4 v221, s[4:7], s63 offen offset:1024 lds\nv_mul_f32 v16, v16, v217\nN 92,228,160\nR 180,189,16384\ns_add_u32 m0, s50, 40960\n"
"s_add_u32 s63, s52, 1769472\nv_mul_f32 v17, v17, v217\nN 92,244,164\nbuffer_load_dwordx4 v220, s[4:7], s63 offen lds\nv_mul_f32 v18, v18, v217\nN 88,224,160\n"
"R 182,189,20480\ns_lshl_b32 s64, s64, 12\nv_mul_f32 v19, v19, v217\nN 88,240,164\ns_waitcnt vmcnt(12)\ns_barrier\n"
"G 4\nv_mul_f32 v20, v20, v217\nv_mfma_f32_16x16x32_bf16 v[128:131], v[168:171], a[0:3], 0\nR 160,190,8192\ns_add_u32 s64, s64, 77594624\nv_mul_f32 v21, v21, v217\n"
"M 128,172,16\ns_mov_b32 m0, s51\ns_cmp_eq_u32 s40, 3\ns_cselect_b64 s[66:67], -1, 0\nv_mul_f32 v22, v22, v217\nv_mfma_f32_16x16x32_bf16 v[144:147], v[168:171], a[4:7], 0\n"
"R 162,190,12288\ns_lshl2_add_u32 s60, s45, s40\ns_waitcnt vmcnt(11)\nv_lshlrev_b32 v224, 3, v252\ns_mul_i32 s60, s60, 7168\ns_bfm_b64 exec, 14, 14\n"
"buffer_load_dword v224, v224, s[12:15], s60 offen\ns_mov_b64 exec, -1\nv_mul_f32 v23, v23, v217\nM 144,172,20\nv_mul_f32 v24, v24, v217\nv_mul_f32 v25, v25, v217\n"
"v_mul_f32 v26, v26, v217\nv_mfma_f32_16x16x32_bf16 v[32:35], v[168:171], a[8:11], 0\nR 164,190,16384\nv_mul_f32 v27, v27, v217\nv_mul_f32 v28, v28, v217\nM 32,172,24\n"
"v_mul_f32 v29, v29, v217\nv_mul_f32 v30, v30, v217\nv_mul_f32 v31, v31, v217\nv_mfma_f32_16x16x32_bf16 v[48:51], v[168:171], a[12:15], 0\nR 166,190,20480\nds_write_addtid_b32 v96 offset:0\n"
"v_cvt_pk_bf16_f32 v16, v16, v17\nv_cvt_pk_bf16_f32 v17, v18, v19\nM 48,172,28\nds_write_addtid_b32 v97 offset:256\nG 6\nv_cvt_pk_bf16_f32 v18, v20, v21\n"
"v_cvt_pk_bf16_f32 v19, v22, v23\nv_cvt_pk_bf16_f32 v20, v24, v25\nv_mfma_f32_16x16x32_bf16 v[52:55], v[176:179], a[12:15], 0\nR 168,191,8192\nds_write_addtid_b32 v98 offset:512\ns_lshr_b32 s100, s41, 1\n"
"s_mul_i32 s100, s100, 458752\ns_add_u32 s100, s100, s55\nv_cvt_pk_bf16_f32 v21, v26, v27\nv_cvt_pk_bf16_f32 v22, v28, v29\nM 52,180,28\nds_write_addtid_b32 v99 offset:768\n"
"s_mul_i32 s101, s41, 229376\ns_add_u32 s101, s101, s54\nv_cvt_pk_bf16_f32 v23, v30, v31\nv_mul_f32 v0, v0, v214\nv_mul_f32 v1, v1, v214\nv_mfma_f32_16x16x32_bf16 v[36:39], v[176:179], a[8:11], 0\n"
"R 170,191,12288\nds_write_addtid_b32 v100 offset:1024\nv_mul_f32 v2, v2, v214\nv_mul_f32 v3, v3, v214\nM 36,180,24\nds_write_addtid_b32 v101 offset:1280\n"
"v_mul_f32 v4, v4, v214\nv_mul_f32 v5, v5, v214\nv_mul_f32 v6, v6, v214\nv_mfma_f32_16x16x32_bf16 v[148:151], v[176:179], a[4:7], 0\nR 172,191,16384\nds_write_addtid_b32 v102 offset:1536\n"
"v_mul_f32 v7, v7, v214\nv_mul_f32 v8, v8, v214\nM 148,180,20\nds_write_addtid_b32 v103 offset:1792\nv_mul_f32 v9, v9, v214\nv_mul_f32 v10, v10, v214\n"
"v_mul_f32 v11, v11, v214\nv_mfma_f32_16x16x32_bf16 v[132:135], v[176:179], a[0:3], 0\nR 174,191,20480\nds_write_addtid_b32 v104 offset:2048\nv_mul_f32 v12, v12, v214\nv_mul_f32 v13, v13, v214\n"
"M 132,180,16\nds_write_addtid_b32 v105 offset:2304\nG 14\nv_mul_f32 v14, v14, v214\nv_mul_f32 v15, v15, v214\nv_cvt_pk_bf16_f32 v0, v0, v1\n"
"v_mfma_f32_16x16x32_bf16 v[136:139], v[160:163], a[0:3], 0\nR 176,188,24576\nds_write_addtid_b32 v106 offset:2560\nv_cvt_pk_bf16_f32 v1, v2, v3\nv_cvt_pk_bf16_f32 v2, v4, v5\nM 136,164,16\n"
"ds_write_addtid_b32 v107 offset:2816\nv_cvt_pk_bf16_f32 v3, v6, v7\nv_cvt_pk_bf16_f32 v4, v8, v9\nv_cvt_pk_bf16_f32 v5, v10, v11\nv_mfma_f32_16x16x32_bf16 v[152:155], v[160:163], a[4:7], 0\nR 178,188,28672\n"
"ds_write_addtid_b32 v108 offset:3072\nv_cvt_pk_bf16_f32 v6, v12, v13\nv_cvt_pk_bf16_f32 v7, v14, v15\nM 152,164,20\nds_write_addtid_b32 v109 offset:3328\nds_write_addtid_b32 v110 offset:3584\n"
"s_mov_b64 exec, s[66:67]\nbuffer_store_dwordx4 v[16:19], v252, s[20:23], s64 offen offset:0 sc1\ns_mov_b64 exec, -1\nv_mfma_f32_16x16x32_bf16 v[40:43], v[160:163], a[8:11], 0\nR 180,188,32768\nds_write_addtid_b32 v111 offset:3840\n"
"M 40,164,24\nds_write_addtid_b32 v112 offset:4096\nds_write_addtid_b32 v113 offset:4352\nv_mfma_f32_16x16x32_bf16 v[56:59], v[160:163], a[12:15], 0\nR 182,188,36864\nds_write_addtid_b32 v114 offset:4608\n"
"M 56,164,28\nds_write_addtid_b32 v115 offset:4864\nds_write_addtid_b32 v116 offset:5120\ns_mov_b64 exec, s[66:67]\nbuffer_store_dwordx4 v[20:23], v252, s[20:23], s64 offen offset:1024 sc1\ns_mov_b64 exec, -1\n"
"G 15\nv_mfma_f32_16x16x32_bf16 v[60:63], v[168:171], a[12:15], 0\nR 160,189,24576\nds_write_addtid_b32 v117 offset:5376\nM 60,172,28\nds_write_addtid_b32 v118 offset:5632\n"
"ds_write_addtid_b32 v119 offset:5888\nv_mfma_f32_16x16x32_bf16 v[44:47], v[168:171], a[8:11], 0\nR 162,189,28672\nds_write_addtid_b32 v120 offset:6144\nM 44,172,24\nds_write_addtid_b32 v121 offset:6400\n"
"ds_write_addtid_b32 v122 offset:6656\nds_write_addtid_b32 v123 offset:6912\nds_write_addtid_b32 v124 offset:7168\nds_write_addtid_b32 v125 offset:7424\nds_write_addtid_b32 v126 offset:7680\nds_write_addtid_b32 v127 offset:7936\n"
"v_mfma_f32_16x16x32_bf16 v[156:159], v[168:171], a[4:7], 0\nR 164,189,32768\ns_add_u32 m0, s50, 40960\nM 156,172,20\nbuffer_load_dwordx4 v221, s[4:7], s63 offen offset:1024 lds\nv_mfma_f32_16x16x32_bf16 v[140:143], v[168:171], a[0:3], 0\n"
"R 166,189,36864\ns_waitcnt vmcnt(11) lgkmcnt(2)\ns_barrier\nM 140,172,16\nds_read2st64_b32 v[96:97], v225 offset0:0 offset1:1\nds_read2st64_b32 v[98:99], v225 offset0:2 offset1:3\n"
"v_lshrrev_b32 v226, 8, v252\nM 128,176,32\nR 168,190,24576\ns_add_u32 s63, s52, 1966080\nds_read2st64_b32 v[100:101], v225 offset0:4 offset1:5\ns_lshl_b32 s60, s40, 7\n"
"s_add_u32 s60, s60, 161280\nv_lshl_add_u32 v226, v226, 4, s60\nM 128,180,48\ns_add_u32 m0, s50, 49152\nds_read2st64_b32 v[102:103], v225 offset0:6 offset1:7\nds_read2st64_b32 v[104:105], v225 offset0:8 offset1:9\n"
"ds_read_b128 v[230:233], v226 offset:0\nM 144,176,36\nR 170,190,28672\nds_read2st64_b32 v[106:107], v225 offset0:10 offset1:11\nds_read_b128 v[234:237], v226 offset:64\nM 144,180,52\n"
"buffer_load_dwordx4 v220, s[4:7], s63 offen lds\nds_read2st64_b32 v[108:109], v225 offset0:12 offset1:13\nM 32,176,40\nR 172,190,32768\nds_read2st64_b32 v[110:111], v225 offset0:14 offset1:15\nM 32,180,56\n"
"ds_read2st64_b32 v[112:113], v225 offset0:16 offset1:17\nds_read2st64_b32 v[114:115], v225 offset0:18 offset1:19\nG 14\nv_add_f32 v64, v64, v96\nM 48,176,44\nR 174,190,36864\n"
"ds_read2st64_b32 v[116:117], v225 offset0:20 offset1:21\nv_add_f32 v65, v65, v97\nM 48,180,60\nds_read2st64_b32 v[118:119], v225 offset0:22 offset1:23\nds_read2st64_b32 v[120:121], v225 offset0:24 offset1:25\nM 52,160,44\n"
"R 176,191,24576\nds_read2st64_b32 v[122:123], v225 offset0:26 offset1:27\nG 15\nv_add_f32 v66, v66, v98\nM 52,164,60\nbuffer_load_dwordx4 v221, s[4:7], s63 offen offset:1024 lds\n"
"ds_read2st64_b32 v[124:125], v225 offset0:28 offset1:29\nM 36,160,40\nR 178,191,28672\nds_read2st64_b32 v[126:127], v225 offset0:30 offset1:31\nv_add_f32 v67, v67, v99\nLpg4:\n"
"s_cbranch_execz LretQ0\nM 36,164,56\ns_add_u32 m0, s50, 57344\nv_add_f32 v68, v68, v100\nv_add_f32 v69, v69, v101\nv_add_f32 v70, v70, v102\n"
"M 148,160,36\nR 180,191,32768\nv_add_f32 v71, v71, v103\nv_add_f32 v72, v72, v104\nM 148,164,52\ns_add_u32 s63, s52, 2162688\n"
"v_add_f32 v73, v73, v105\nG 15\nv_add_f32 v74, v74, v106\nv_add_f32 v75, v75, v107\nM 132,160,32\nR 182,191,36864\n"
"G 15\nv_add_f32 v76, v76, v108\nv_add_f32 v77, v77, v109\nM 132,164,48\nG 10\nv_add_f32 v78, v78, v110\n"
"v_add_f32 v79, v79, v111\nv_add_f32 v80, v80, v112\nM 136,168,32\nR 160,188,40960\nv_add_f32 v81, v81, v113\nv_add_f32 v82, v82, v114\n"
"M 136,172,48\nbuffer_load_dwordx4 v220, s[4:7], s63 offen lds\nv_add_f32 v83, v83, v115\nM 152,168,36\nR 162,188,45056\nG 11\n"
"v_add_f32 v84, v84, v116\nM 152,172,52\nv_add_f32 v85, v85, v117\nG 10\nv_add_f32 v86, v86, v118\nM 40,168,40\n"
"R 164,188,49152\nv_add_f32 v87, v87, v119\nM 40,172,56\nbuffer_load_dwordx4 v221, s[4:7], s63 offen offset:1024 lds\nM 56,168,44\nR 166,188,53248\n"
"s_add_u32 m0, s50, 65536\nM 56,172,60\nG 4\nv_add_f32 v88, v88, v120\nv_add_f32 v89, v89, v121\nM 60,176,44\n"
"R 168,189,40960\ns_add_u32 s63, s52, 2359296\nM 60,180,60\nbuffer_load_dwordx4 v220, s[4:7], s63 offen lds\nM 44,176,40\nR 170,189,45056\n"
"v_add_f32 v90, v90, v122\nM 44,180,56\nv_add_f32 v91, v91, v123\nv_add_f32 v92, v92, v124\nM 156,176,36\nR 172,189,49152\n"
"v_add_f32 v93, v93, v125\nM 156,180,52\nbuffer_load_dwordx4 v221, s[4:7], s63 offen offset:1024 lds\nM 140,176,32\nR 174,189,53248\ns_add_u32 m0, s50, 73728\n"
"M 140,180,48\nG 4\nv_add_f32 v94, v94, v126\nv_add_f32 v95, v95, v127\nM 128,160,64\nR 176,190,40960\n"
"s_add_u32 s63, s52, 2555904\nv_mul_f32 v64, v64, v230\nM 128,164,80\nbuffer_load_dwordx4 v220, s[4:7], s63 offen lds\nv_mul_f32 v65, v65, v231\nM 144,160,68\n"
"R 178,190,45056\nv_mul_f32 v66, v66, v232\nv_mul_f32 v67, v67, v233\nM 144,164,84\nv_mul_f32 v68, v68, v234\nv_mul_f32 v69, v69, v235\n"
"v_mul_f32 v70, v70, v236\nM 32,160,72\nR 180,190,49152\nv_mul_f32 v71, v71, v237\nv_cvt_pk_bf16_f32 v67, v66, v67\nM 32,164,88\n"
"buffer_load_dwordx4 v221, s[4:7], s63 offen offset:1024 lds\nv_cvt_pk_bf16_f32 v66, v64, v65\nM 48,160,76\nR 182,190,53248\ns_add_u32 s63, s52, 2752512\nv_cvt_pk_bf16_f32 v68, v68, v69\n"
"M 48,164,92\nG 4\nv_cvt_pk_bf16_f32 v69, v70, v71\nv_mul_f32 v72, v72, v230\nv_mul_f32 v73, v73, v231\nM 52,168,76\n"
"R 160,191,40960\nv_mul_f32 v74, v74, v232\nv_mul_f32 v75, v75, v233\nM 52,172,92\ns_add_u32 m0, s50, 81920\nv_mul_f32 v76, v76, v234\n"
"v_mul_f32 v77, v77, v235\nv_mul_f32 v78, v78, v236\nM 36,168,72\nR 162,191,45056\nv_mul_f32 v79, v79, v237\nv_cvt_pk_bf16_f32 v72, v72, v73\n"
"M 36,172,88\nbuffer_load_dwordx4 v220, s[4:7], s63 offen lds\nv_cvt_pk_bf16_f32 v73, v74, v75\nM 148,168,68\nR 164,191,49152\nv_cvt_pk_bf16_f32 v74, v76, v77\n"
"v_cvt_pk_bf16_f32 v75, v78, v79\nM 148,172,84\ns_mov_b64 exec, s[66:67]\nbuffer_store_dwordx2 v[68:69], v227, s[20:23], s64 offen offset:2048 sc1\ns_mov_b64 exec, -1\nv_mul_f32 v80, v80, v230\n"
"v_mul_f32 v81, v81, v231\nM 132,168,64\nR 166,191,53248\ns_waitcnt vmcnt(10)\ns_barrier\nv_mul_f32 v82, v82, v232\n"
"M 132,172,80\nG 4\nv_mul_f32 v83, v83, v233\nv_mul_f32 v84, v84, v234\nv_mul_f32 v85, v85, v235\nM 136,176,64\n"
"R 168,184,0\ns_lshl2_add_u32 s60, s45, s40\nv_lshlrev_b32 v224, 3, v252\ns_mul_i32 s60, s60, 7168\ns_bfm_b64 exec, 14, 28\nbuffer_load_dword v224, v224, s[12:15], s60 offen\n"
"s_mov_b64 exec, -1\nv_mul_f32 v86, v86, v236\nM 136,180,80\nv_mul_f32 v87, v87, v237\nv_cvt_pk_bf16_f32 v78, v80, v81\nv_cvt_pk_bf16_f32 v79, v82, v83\n"
"M 152,176,68\nR 170,184,4096\nv_cvt_pk_bf16_f32 v80, v84, v85\nv_cvt_pk_bf16_f32 v81, v86, v87\nM 152,180,84\nbuffer_load_dwordx4 v221, s[4:7], s63 offen offset:1024 lds\n"
"v_mul_f32 v88, v88, v230\nM 40,176,72\nR 172,184,8192\ns_mov_b32 s60, 155648\nv_bfe_u32 v246, v252, 4, 4\nv_lshl_add_u32 v246, v246, 3, s60\n"
"v_mul_f32 v89, v89, v231\nM 40,180,88\ns_add_u32 m0, s50, 90112\nv_mul_f32 v90, v90, v232\nv_mul_f32 v91, v91, v233\nv_mul_f32 v92, v92, v234\n"
"M 56,176,76\nR 174,184,12288\nds_read_b64 v[238:239], v246 offset:0\nv_mul_f32 v93, v93, v235\nM 56,180,92\nG 5\n"
"v_mul_f32 v94, v94, v236\nv_mul_f32 v95, v95, v237\nv_cvt_pk_bf16_f32 v84, v88, v89\nM 60,160,76\nR 176,185,0\nds_read_b64 v[240:241], v246 offset:128\n"
"v_cvt_pk_bf16_f32 v85, v90, v91\nM 60,164,92\ns_add_u32 s63, s52, 2949120\ns_mov_b64 exec, s[66:67]\nbuffer_store_dwordx2 v[74:75], v227, s[20:23], s64 offen offset:2560 sc1\ns_mov_b64 exec, -1\n"
"v_cvt_pk_bf16_f32 v86, v92, v93\nv_cvt_pk_bf16_f32 v87, v94, v95\nM 44,160,72\nR 178,185,4096\nds_read_b64 v[242:243], v246 offset:256\ns_lshl_b32 s60, s40, 12\n"
"s_add_u32 s60, s60, 53248\nv_add_u32 v230, s60, v252\nv_add_u32 v231, s60, v227\nM 44,164,88\nbuffer_load_dwordx4 v220, s[4:7], s63 offen lds\nM 156,160,68\n"
"R 180,185,8192\nds_read_b64 v[244:245], v246 offset:384\nM 156,164,84\nv_add_u32 v108, 161792, v252\nds_read_b128 v[112:115], v108\nM 140,160,64\n"
"R 182,185,12288\nds_read_b64 v[246:247], v246 offset:0\nM 140,164,80\nds_read_b128 v[104:107], v108 offset:1024\ns_mov_b64 exec, s[66:67]\nbuffer_store_dwordx2 v[80:81], v227, s[20:23], s64 offen offset:3072 sc1\n"
"s_mov_b64 exec, -1\nG 11\nM 128,168,96\nR 160,186,0\nM 128,172,112\nM 144,168,100\n"
"R 162,186,4096\nM 144,172,116\nbuffer_load_dwordx4 v221, s[4:7], s63 offen offset:1024 lds\nM 32,168,104\nR 164,186,8192\nG 11\n"
"v_mfma_f32_16x16x32_bf16 v[128:131], v[238:241], v[208:211], v[128:131]\nM 32,172,120\ns_mov_b64 exec, s[66:67]\nbuffer_store_dwordx2 v[86:87], v227, s[20:23], s64 offen offset:3584 sc1\ns_mov_b64 exec, -1\nG 3\n"
"v_lshlrev_b32 v100, 16, v104\nv_and_b32 v101, s77, v104\nM 48,168,108\nR 166,186,12288\nv_mfma_f32_16x16x32_bf16 v[132:135], v[240:243], v[208:211], v[132:135]\ns_not_b64 exec, s[66:67]\n"
"ds_write_b128 v230, v[16:19] offset:0\nds_write_b128 v230, v[20:23] offset:1024\ns_mov_b64 exec, -1\nv_lshlrev_b32 v102, 16, v105\nM 48,172,124\nv_and_b32 v103, s77, v105\n"
"v_lshlrev_b32 v96, 16, v106\nM 52,176,108\nR 168,187,0\nv_mfma_f32_16x16x32_bf16 v[144:147], v[244:247], v[210:213], v[144:147]\nv_and_b32 v97, s77, v106\nM 52,180,124\n"
"s_movk_i32 s63, 7168\nv_mad_u32_u24 v220, v228, s63, v229\nv_add_u32 v221, 3668992, v220\ns_add_u32 m0, s50, 98304\ns_add_u32 s63, s101, 0\nbuffer_load_dwordx4 v220, s[12:15], s63 offen lds\n"
"M 36,176,104\nR 170,187,4096\nv_mfma_f32_16x16x32_bf16 v[148:151], v[238:241], v[210:213], v[148:151]\nv_lshlrev_b32 v98, 16, v107\nM 36,180,120\nv_and_b32 v99, s77, v107\n"
"v_add_f32 v104, s78, v100\nM 148,176,100\nR 172,187,8192\nv_add_f32 v105, s78, v101\nM 148,180,116\nv_add_f32 v106, s78, v102\n"
"v_add_f32 v107, s78, v103\nM 132,176,96\nR 174,187,12288\nM 132,180,112\nG 6\nM 136,160,96\n"
"R 176,184,32768\nM 136,164,112\nbuffer_load_dwordx4 v221, s[12:15], s63 offen offset:1024 lds\nM 152,160,100\nR 178,184,36864\ns_not_b64 exec, s[66:67]\n"
"ds_write_b64 v231, v[68:69] offset:2048\nds_write_b64 v231, v[74:75] offset:2560\ns_mov_b64 exec, -1\nM 152,164,116\nM 40,160,104\nR 180,184,40960\n"
"M 40,164,120\nM 56,160,108\nR 182,184,45056\nM 56,164,124\nG 6\nM 60,168,108\n"
"R 160,185,32768\ns_not_b64 exec, s[66:67]\nds_write_b64 v231, v[80:81] offset:3072\nds_write_b64 v231, v[86:87] offset:3584\ns_mov_b64 exec, -1\nM 60,172,124\n"
"M 44,168,104\nR 162,185,36864\nM 44,172,120\ns_add_u32 m0, s50, 106496\nM 156,168,100\nR 164,185,40960\n"
"M 156,172,116\ns_add_u32 s63, s101, 128\nM 140,168,96\nR 166,185,45056\nM 140,172,112\nG 6\n"
"M 128,176,128\nR 168,186,32768\ns_waitcnt vmcnt(13) lgkmcnt(4)\ns_barrier\nM 128,180,144\nbuffer_load_dwordx4 v220, s[12:15], s63 offen lds\n"
"M 144,176,132\nR 170,186,36864\nM 144,180,148\ns_cmp_eq_u32 s78, 0\ns_cbranch_scc1 Lq151\nv_mov_b32 v230, 0\n"
"v_mov_b32 v231, 0\nv_mov_b32 v232, 0\nv_mov_b32 v233, 0\nv_mov_b32 v64, 0\nv_mov_b32 v65, 0\nv_mov_b32 v70, 0\n"
"v_mov_b32 v71, 0\nv_mov_b32 v234, 0\nv_mov_b32 v235, 0\nv_mov_b32 v236, 0\nv_mov_b32 v237, 0\nv_mov_b32 v76, 0\n"
"v_mov_b32 v77, 0\nv_mov_b32 v82, 0\nv_mov_b32 v83, 0\nLq151:\ns_lshl4_add_u32 s65, s41, s42\nM 32,176,136\n"
"R 172,186,40960\ns_lshl_b32 s65, s65, 12\nM 32,180,152\ns_add_u32 s65, s65, 77529088\nM 48,176,140\nR 174,186,45056\n"
"M 48,180,156\nG 4\nM 52,160,140\nR 176,187,32768\nM 52,164,156\nbuffer_load_dwordx4 v221, s[12:15], s63 offen offset:1024 lds\n"
"M 36,160,136\nR 178,187,36864\nM 36,164,152\ns_add_u32 m0, s50, 114688\nM 148,160,132\nR 180,187,40960\n"
"M 148,164,148\ns_add_u32 s63, s101, 256\nM 132,160,128\nR 182,187,45056\nM 132,164,144\nG 4\n"
"M 136,168,128\nR 160,184,49152\nM 136,172,144\nbuffer_load_dwordx4 v220, s[12:15], s63 offen lds\nM 152,168,132\nR 162,184,53248\n"
"M 152,172,148\nM 40,168,136\nR 164,184,57344\ns_lshl2_add_u32 s60, s45, s40\ns_waitcnt vmcnt(11)\nv_lshlrev_b32 v224, 3, v252\n"
"s_mul_i32 s60, s60, 7168\ns_bfm_b64 exec, 14, 42\nbuffer_load_dword v224, v224, s[12:15], s60 offen\ns_mov_b64 exec, -1\nM 40,172,152\nM 56,168,140\n"
"R 166,184,61440\nv_mfma_f32_16x16x32_bf16 v[136:139], v[242:245], v[208:211], v[136:139]\nM 56,172,156\nG 4\nM 60,176,140\nR 168,185,49152\n"
"v_mfma_f32_16x16x32_bf16 v[140:143], v[244:247], v[208:211], v[140:143]\nM 60,180,156\nbuffer_load_dwordx4 v221, s[12:15], s63 offen offset:1024 lds\nM 44,176,136\nR 170,185,53248\nv_mfma_f32_16x16x32_bf16 v[152:155], v[240:243], v[210:213], v[152:155]\n"
"s_add_u32 s63, s100, 0\nM 44,180,152\ns_add_u32 m0, s50, 0\nM 156,176,132\nR 172,185,57344\nv_mfma_f32_16x16x32_bf16 v[156:159], v[242:245], v[210:213], v[156:159]\n"
"M 156,180,148\nbuffer_load_dwordx4 v222, s[12:15], s63 offen lds\nM 140,176,128\nR 174,185,61440\nM 140,180,144\nG 4\n"
"M 128,160,160\nR 176,186,49152\nM 128,164,176\nM 144,160,164\nR 178,186,53248\nM 144,164,180\n"
"buffer_load_dwordx4 v223, s[12:15], s63 offen offset:1024 lds\nM 32,160,168\nR 180,186,57344\nM 32,164,184\nM 48,160,172\nR 182,186,61440\n"
"M 48,164,188\nG 4\nM 52,168,172\nR 160,187,49152\nM 52,172,188\nM 36,168,168\n"
"R 162,187,53248\nM 36,172,184\ns_movk_i32 s63, 2048\nv_mad_u32_u24 v220, v228, s63, v229\nv_add_u32 v221, 1047552, v220\ns_add_u32 m0, s50, 8192\n"
"s_add_u32 s63, s53, 0\nbuffer_load_dwordx4 v220, s[8:11], s63 offen lds\nM 148,168,164\nR 164,187,57344\ns_lshl_b32 s60, s40, 12\ns_add_u32 s60, s60, 49152\n"
"v_add_u32 v226, s60, v252\nv_add_u32 v111, s60, v227\nM 148,172,180\nM 132,168,160\nR 166,187,61440\ns_cmp_eq_u32 s40, 0\n"
"s_cselect_b64 s[88:89], 0, -1\ns_cselect_b32 s60, s41, 0\ns_cmp_lg_u32 s60, 0\ns_cselect_b64 s[66:67], -1, 0\nM 132,172,176\nG 4\n"
"M 136,176,160\nR 168,188,0\nM 136,180,176\ns_mov_b64 exec, s[88:89]\nds_read_b128 v[230:233], v226 offset:0\nds_read_b64 v[64:65], v111 offset:2048\n"
"s_mov_b64 exec, -1\nM 152,176,164\nR 170,188,4096\ns_waitcnt vmcnt(11) lgkmcnt(1)\ns_barrier\nM 152,180,180\n"
"buffer_load_dwordx4 v221, s[8:11], s63 offen offset:1024 lds\nM 40,176,168\nR 172,188,8192\nM 40,180,184\ns_mov_b64 exec, s[88:89]\nds_read_b64 v[70:71], v111 offset:2560\n"
"ds_read_b128 v[234:237], v226 offset:1024\ns_mov_b64 exec, -1\nM 56,176,172\nR 174,188,12288\nM 56,180,188\nM 60,160,172\n"
"R 176,189,0\nM 60,164,188\ns_mov_b64 exec, s[88:89]\nds_read_b64 v[76:77], v111 offset:3072\nds_read_b64 v[82:83], v111 offset:3584\ns_mov_b64 exec, -1\n"
"M 44,160,168\nR 178,189,4096\nM 44,164,184\nM 156,160,164\nR 180,189,8192\nM 156,164,180\n"
"s_mov_b64 exec, s[66:67]\nbuffer_load_dwordx4 v[230:233], v252, s[20:23], s65 offen offset:0 sc1\ns_mov_b64 exec, -1\nM 140,160,160\nR 182,189,12288\nM 140,164,176\n"
"G 6\nM 32,168,200\nR 160,190,0\ns_mov_b64 exec, s[66:67]\nbuffer_load_dwordx2 v[64:65], v227, s[20:23], s65 offen offset:2048 sc1\ns_mov_b64 exec, -1\n"
"M 32,172,216\nR 162,190,4096\nM 48,168,204\nR 164,190,8192\nM 48,172,220\nR 166,190,12288\n"
"G 4\nM 52,176,204\nR 168,191,0\ns_mov_b64 exec, s[66:67]\nbuffer_load_dwordx2 v[70:71], v227, s[20:23], s65 offen offset:2560 sc1\ns_mov_b64 exec, -1\n"
"M 52,180,220\nR 170,191,4096\nM 36,176,200\nR 172,191,8192\nM 36,180,216\nR 174,191,12288\n"
"G 4\nM 40,160,200\nR 176,188,16384\ns_mov_b64 exec, s[66:67]\nbuffer_load_dwordx4 v[234:237], v252, s[20:23], s65 offen offset:1024 sc1\ns_mov_b64 exec, -1\n"
"M 40,164,216\nR 178,188,20480\nM 56,160,204\nR 180,188,24576\nM 56,164,220\nR 182,188,28672\n"
"G 4\nM 60,168,204\nR 160,189,16384\ns_mov_b64 exec, s[66:67]\nbuffer_load_dwordx2 v[76:77], v227, s[20:23], s65 offen offset:3072 sc1\ns_mov_b64 exec, -1\n"
"M 60,172,220\nR 162,189,20480\nM 44,168,200\nR 164,189,24576\nM 44,172,216\nR 166,189,28672\n"
"G 4\nM 32,176,232\nR 168,190,16384\ns_mov_b64 exec, s[66:67]\nbuffer_load_dwordx2 v[82:83], v227, s[20:23], s65 offen offset:3584 sc1\ns_mov_b64 exec, -1\n"
"Lq185:\ns_lshr_b32 s60, s45, 3\ns_lshl2_add_u32 s60, s60, s40\ns_lshl_b32 s60, s60, 8\ns_waitcnt vmcnt(11)\nv_lshl_add_u32 v224, v252, 3, s60\n"
"v_add_u32 v224, Lq185+4096-LqPC, v224\nv_min_u32 v224, LqE-LqPC-4, v224\ns_mov_b64 exec, 3\nglobal_load_dword v224, v224, s[98:99]\ns_mov_b64 exec, -1\nM 32,180,248\n"
"R 170,190,20480\nM 48,176,236\nR 172,190,24576\nM 48,180,252\nR 174,190,28672\nG 4\n"
"M 52,160,236\nR 176,191,16384\nM 52,164,252\nR 178,191,20480\nM 36,160,232\nR 180,191,24576\n"
"M 36,164,248\nR 182,191,28672\nG 4\nM 40,168,232\nR 160,188,0\nM 40,172,248\n"
"R 162,188,4096\nM 56,168,236\nR 164,188,8192\nM 56,172,252\nR 166,188,12288\nG 4\n"
"M 60,176,236\nR 168,189,0\nM 60,180,252\nR 170,189,4096\nM 44,176,232\nR 172,189,8192\n"
"M 44,180,248\nR 174,189,12288\nG 4\nM 128,160,192\nR 176,190,0\nM 128,164,208\n"
"R 178,190,4096\ns_mov_b32 m0, s51\nM 144,160,196\nR 180,190,8192\nds_write_addtid_b32 v32 offset:0\nM 144,164,212\n"
"R 182,190,12288\nds_write_addtid_b32 v33 offset:256\nG 6\nM 148,168,196\nR 160,191,0\nds_write_addtid_b32 v34 offset:512\n"
"M 148,172,212\nR 162,191,4096\nds_write_addtid_b32 v35 offset:768\nM 132,168,192\nR 164,191,8192\nds_write_addtid_b32 v36 offset:1024\n"
"Lpg5:\ns_cbranch_execz LretQ0\nM 132,172,208\nR 166,191,12288\nds_write_addtid_b32 v37 offset:1280\nG 9\n"
"M 136,176,192\nR 168,188,16384\nds_write_addtid_b32 v38 offset:1536\nM 136,180,208\nR 170,188,20480\nds_write_addtid_b32 v39 offset:1792\n"
"M 152,176,196\nR 172,188,24576\nds_write_addtid_b32 v40 offset:2048\nM 152,180,212\nR 174,188,28672\nds_write_addtid_b32 v41 offset:2304\n"
"G 9\nM 156,160,196\nR 176,189,16384\nds_write_addtid_b32 v42 offset:2560\nM 156,164,212\nR 178,189,20480\n"
"ds_write_addtid_b32 v43 offset:2816\nM 140,160,192\nR 180,189,24576\nds_write_addtid_b32 v44 offset:3072\nds_write_addtid_b32 v45 offset:3328\nds_write_addtid_b32 v46 offset:3584\n"
"ds_write_addtid_b32 v47 offset:3840\nds_write_addtid_b32 v48 offset:4096\nds_write_addtid_b32 v49 offset:4352\nds_write_addtid_b32 v50 offset:4608\nds_write_addtid_b32 v51 offset:4864\nds_write_addtid_b32 v52 offset:5120\n"
"ds_write_addtid_b32 v53 offset:5376\nds_write_addtid_b32 v54 offset:5632\nds_write_addtid_b32 v55 offset:5888\nds_write_addtid_b32 v56 offset:6144\nds_write_addtid_b32 v57 offset:6400\nds_write_addtid_b32 v58 offset:6656\n"
"ds_write_addtid_b32 v59 offset:6912\nds_write_addtid_b32 v60 offset:7168\nds_write_addtid_b32 v61 offset:7424\nds_write_addtid_b32 v62 offset:7680\nds_write_addtid_b32 v63 offset:7936\nM 140,164,208\n"
"R 182,189,28672\nG 15\nM 128,168,224\nR 160,190,16384\ns_add_u32 m0, s50, 16384\nM 128,172,240\n"
"R 162,190,20480\nG 3\ns_barrier\ns_add_u32 s63, s53, 65536\nM 144,168,228\nR 164,190,24576\n"
"buffer_load_dwordx4 v220, s[8:11], s63 offen lds\nds_read2st64_b32 v[32:33], v225 offset0:0 offset1:1\nM 144,172,244\nR 166,190,28672\nbuffer_load_dwordx4 v221, s[8:11], s63 offen offset:1024 lds\nds_read2st64_b32 v[34:35], v225 offset0:2 offset1:3\n"
"G 6\nM 148,176,228\nR 168,191,16384\ns_add_u32 m0, s50, 24576\ns_add_u32 s63, s53, 131072\nbuffer_load_dwordx4 v220, s[8:11], s63 offen lds\n"
"ds_read2st64_b32 v[36:37], v225 offset0:4 offset1:5\nM 148,180,244\nR 170,191,20480\nbuffer_load_dwordx4 v221, s[8:11], s63 offen offset:1024 lds\nds_read2st64_b32 v[38:39], v225 offset0:6 offset1:7\nM 132,176,224\n"
"R 172,191,24576\ns_add_u32 m0, s50, 32768\ns_add_u32 s63, s53, 196608\nbuffer_load_dwordx4 v220, s[8:11], s63 offen lds\nds_read2st64_b32 v[40:41], v225 offset0:8 offset1:9\nM 132,180,240\n"
"R 174,191,28672\nbuffer_load_dwordx4 v221, s[8:11], s63 offen offset:1024 lds\nds_read2st64_b32 v[42:43], v225 offset0:10 offset1:11\nG 9\nM 136,160,224\ns_add_u32 m0, s50, 40960\n"
"s_add_u32 s63, s53, 262144\nbuffer_load_dwordx4 v220, s[8:11], s63 offen lds\nds_read2st64_b32 v[44:45], v225 offset0:12 offset1:13\nM 136,164,240\nbuffer_load_dwordx4 v221, s[8:11], s63 offen offset:1024 lds\nds_read2st64_b32 v[46:47], v225 offset0:14 offset1:15\n"
"M 152,160,228\ns_add_u32 m0, s50, 49152\ns_add_u32 s63, s53, 327680\nbuffer_load_dwordx4 v220, s[8:11], s63 offen lds\nds_read2st64_b32 v[48:49], v225 offset0:16 offset1:17\nM 152,164,244\n"
"ds_read2st64_b32 v[50:51], v225 offset0:18 offset1:19\nds_read2st64_b32 v[52:53], v225 offset0:20 offset1:21\nG 6\nM 156,168,228\nds_read2st64_b32 v[54:55], v225 offset0:22 offset1:23\nds_read2st64_b32 v[56:57], v225 offset0:24 offset1:25\n"
"M 156,172,244\nds_read2st64_b32 v[58:59], v225 offset0:26 offset1:27\nds_read2st64_b32 v[60:61], v225 offset0:28 offset1:29\nM 140,168,224\nds_read2st64_b32 v[62:63], v225 offset0:30 offset1:31\nM 140,172,240\n"
"G 7\nv_pk_add_f32 v[144:145], v[144:145], v[48:49]\nG 6\nv_pk_add_f32 v[146:147], v[146:147], v[50:51]\nG 5\nv_pk_add_f32 v[148:149], v[148:149], v[52:53]\n"
"G 4\nv_pk_add_f32 v[150:151], v[150:151], v[54:55]\nG 3\nv_pk_add_f32 v[152:153], v[152:153], v[56:57]\nG 2\nv_pk_add_f32 v[154:155], v[154:155], v[58:59]\n"
"G 1\nv_pk_add_f32 v[156:157], v[156:157], v[60:61]\nG 0\nv_pk_add_f32 v[158:159], v[158:159], v[62:63]\nv_pk_add_f32 v[128:129], v[128:129], v[32:33]\nv_pk_add_f32 v[130:131], v[130:131], v[34:35]\n"
"v_pk_add_f32 v[132:133], v[132:133], v[36:37]\nv_pk_add_f32 v[134:135], v[134:135], v[38:39]\nv_pk_add_f32 v[136:137], v[136:137], v[40:41]\nv_pk_add_f32 v[138:139], v[138:139], v[42:43]\nv_pk_add_f32 v[140:141], v[140:141], v[44:45]\nv_pk_add_f32 v[142:143], v[142:143], v[46:47]\n"
"v_cvt_pk_bf16_f32 v144, v144, v145\nv_cvt_pk_bf16_f32 v145, v146, v147\nv_cvt_pk_bf16_f32 v146, v148, v149\nv_cvt_pk_bf16_f32 v147, v150, v151\nv_cvt_pk_bf16_f32 v152, v152, v153\nv_cvt_pk_bf16_f32 v153, v154, v155\n"
"v_cvt_pk_bf16_f32 v154, v156, v157\nv_cvt_pk_bf16_f32 v155, v158, v159\nv_cvt_pk_bf16_f32 v128, v128, v129\nv_cvt_pk_bf16_f32 v129, v130, v131\nv_cvt_pk_bf16_f32 v130, v132, v133\nv_cvt_pk_bf16_f32 v131, v134, v135\n"
"v_cvt_pk_bf16_f32 v136, v136, v137\nv_cvt_pk_bf16_f32 v137, v138, v139\nv_cvt_pk_bf16_f32 v138, v140, v141\nv_cvt_pk_bf16_f32 v139, v142, v143\nv_mfma_f32_16x16x32_bf16 v[52:55], v[16:19], v[144:147], v[96:99]\nv_mfma_f32_16x16x32_bf16 v[48:51], v[0:3], v[144:147], v[100:103]\n"
"v_mfma_f32_16x16x32_bf16 v[60:63], v[20:23], v[152:155], v[96:99]\nv_mfma_f32_16x16x32_bf16 v[56:59], v[4:7], v[152:155], v[100:103]\ns_waitcnt vmcnt(13)\nv_max3_u32 v248, v233, v65, v71\nv_cmp_eq_u32 vcc, -1, v248\ns_nop 0\n"
"s_cbranch_vccnz Lq187\nLq188:\ns_waitcnt vmcnt(10)\nv_max3_u32 v248, v237, v77, v83\nv_cmp_eq_u32 vcc, -1, v248\ns_nop 0\n"
"s_cbranch_vccnz Lq189\nLq190:\nv_mfma_f32_16x16x32_bf16 v[36:39], v[0:3], v[128:131], v[96:99]\nv_mfma_f32_16x16x32_bf16 v[32:35], v[230:233], v[128:131], v[104:107]\nv_mfma_f32_16x16x32_bf16 v[44:47], v[4:7], v[136:139], v[96:99]\nv_mfma_f32_16x16x32_bf16 v[40:43], v[234:237], v[136:139], v[104:107]\n"
"s_lshl_b32 s84, s42, 11\ns_add_u32 s84, s84, s49\ns_add_u32 s84, s84, 2097152\ns_add_u32 s85, s84, 32768\ns_lshl2_add_u32 s60, s45, s40\ns_waitcnt vmcnt(9)\n"
"v_lshlrev_b32 v224, 3, v252\ns_mul_i32 s60, s60, 7168\ns_bfm_b64 exec, 28, 28\nbuffer_load_dword v224, v224, s[16:19], s60 offen\ns_mov_b64 exec, -1\nv_mov_b32 v238, s46\n"
"v_mov_b32 v239, s47\nv_max_f32 v52, v52, v53\nv_max_f32 v54, v54, v55\nv_max3_f32 v52, v52, v48, v49\nv_max3_f32 v54, v54, v50, v51\nv_max_f32 v118, v52, v54\n"
"v_max_f32 v60, v60, v61\nv_max_f32 v62, v62, v63\nv_max3_f32 v60, v60, v56, v57\nv_max3_f32 v62, v62, v58, v59\nv_max_f32 v119, v60, v62\nv_max_f32 v36, v36, v37\n"
"v_max_f32 v38, v38, v39\nv_max3_f32 v36, v36, v32, v33\nv_max3_f32 v38, v38, v34, v35\nv_max_f32 v116, v36, v38\nv_max_f32 v44, v44, v45\nv_max_f32 v46, v46, v47\n"
"v_max3_f32 v44, v44, v40, v41\nv_max3_f32 v46, v46, v42, v43\nv_max_f32 v117, v44, v46\nv_permlane32_swap_b32 v116, v118\ns_nop 0\nv_permlane32_swap_b32 v117, v119\n"
"v_max_f32 v116, v116, v118\nv_max_f32 v117, v117, v119\ns_nop 1\nv_permlane16_swap_b32 v116, v117\nv_max_f32 v116, v116, v117\nv_mov_b32 v117, v116\n"
"s_nop 1\nv_permlane16_swap_b32 v116, v117\nv_mov_b32 v118, v116\nv_mov_b32 v119, v117\ns_nop 0\nv_permlane32_swap_b32 v116, v118\n"
"v_permlane32_swap_b32 v117, v119\nv_mul_f32 v116, v116, v215\nv_mul_f32 v118, v118, v218\nv_mul_f32 v117, v117, v215\nv_mul_f32 v119, v119, v218\nv_fma_f32 v52, v52, v218, -v118\n"
"v_fma_f32 v54, v54, v218, -v118\nv_fma_f32 v60, v60, v218, -v119\nv_fma_f32 v62, v62, v218, -v119\nv_fma_f32 v36, v36, v215, -v116\nv_fma_f32 v38, v38, v215, -v116\nv_fma_f32 v44, v44, v215, -v117\n"
"v_fma_f32 v46, v46, v215, -v117\nv_exp_f32 v52, v52\nv_exp_f32 v54, v54\nv_exp_f32 v60, v60\nv_exp_f32 v62, v62\nv_exp_f32 v36, v36\n"
"v_exp_f32 v38, v38\nv_exp_f32 v44, v44\nv_exp_f32 v46, v46\nv_add_f32 v118, v52, v54\nv_add_f32 v119, v60, v62\nv_add_f32 v116, v36, v38\n"
"v_add_f32 v117, v44, v46\ns_nop 0\nv_permlane32_swap_b32 v116, v118\nv_permlane32_swap_b32 v117, v119\nv_add_f32 v116, v116, v118\nv_add_f32 v117, v117, v119\n"
"s_nop 1\nv_permlane16_swap_b32 v116, v117\nv_add_f32 v116, v116, v117\ns_barrier\nv_rcp_f32 v116, v116\ns_nop 0\n"
"v_mov_b32 v117, v116\ns_nop 1\nv_permlane16_swap_b32 v116, v117\nv_mov_b32 v118, v116\nv_mov_b32 v119, v117\ns_nop 0\n"
"v_permlane32_swap_b32 v116, v118\nv_permlane32_swap_b32 v117, v119\nv_mul_f32 v52, v52, v118\nv_mul_f32 v54, v54, v118\nv_mul_f32 v60, v60, v119\nv_mul_f32 v62, v62, v119\n"
"v_mul_f32 v36, v36, v116\nv_mul_f32 v38, v38, v116\nv_mul_f32 v44, v44, v117\nv_mul_f32 v46, v46, v117\nv_cvt_pk_bf16_f32 v52, v52, v52\nv_cvt_pk_bf16_f32 v54, v54, v54\n"
"v_cvt_pk_bf16_f32 v60, v60, v60\nv_cvt_pk_bf16_f32 v62, v62, v62\nv_cvt_pk_bf16_f32 v36, v36, v36\nv_cvt_pk_bf16_f32 v38, v38, v38\nv_cvt_pk_bf16_f32 v44, v44, v44\nv_cvt_pk_bf16_f32 v46, v46, v46\n"
"v_and_b32 v48, v52, v112\nv_and_b32 v49, v54, v113\nv_and_b32 v50, v52, v114\nv_and_b32 v51, v54, v115\nv_and_b32 v56, v60, v112\nv_and_b32 v57, v62, v113\n"
"v_and_b32 v58, v60, v114\nv_and_b32 v59, v62, v115\nv_mfma_f32_16x16x32_bf16 v[144:147], v[66:69], v[48:51], 0\nv_mfma_f32_16x16x32_bf16 v[148:151], v[72:75], v[48:51], 0\nv_and_b32 v32, v36, v112\nv_and_b32 v33, v38, v113\n"
"v_and_b32 v34, v36, v114\nv_and_b32 v35, v38, v115\nv_mfma_f32_16x16x32_bf16 v[152:155], v[78:81], v[56:59], 0\nv_mfma_f32_16x16x32_bf16 v[156:159], v[84:87], v[56:59], 0\nv_and_b32 v40, v44, v112\nv_and_b32 v41, v46, v113\n"
"v_and_b32 v42, v44, v114\nv_and_b32 v43, v46, v115\nv_mfma_f32_16x16x32_bf16 v[128:131], v[64:67], v[32:35], 0\nv_mfma_f32_16x16x32_bf16 v[132:135], v[70:73], v[32:35], 0\nv_mfma_f32_16x16x32_bf16 v[136:139], v[76:79], v[40:43], 0\nv_mfma_f32_16x16x32_bf16 v[140:143], v[82:85], v[40:43], 0\n"
"v_cvt_pk_bf16_f32 v144, v144, v145\nv_cvt_pk_bf16_f32 v145, v146, v147\nv_cvt_pk_bf16_f32 v146, v148, v149\nv_cvt_pk_bf16_f32 v147, v150, v151\nbuffer_store_dwordx4 v[144:147], v252, s[20:23], s85 offen offset:0 sc1\nv_cvt_pk_bf16_f32 v152, v152, v153\n"
"v_cvt_pk_bf16_f32 v153, v154, v155\nv_cvt_pk_bf16_f32 v154, v156, v157\nv_cvt_pk_bf16_f32 v155, v158, v159\nbuffer_store_dwordx4 v[152:155], v252, s[20:23], s85 offen offset:1024 sc1\nv_cvt_pk_bf16_f32 v128, v128, v129\nv_cvt_pk_bf16_f32 v129, v130, v131\n"
"v_cvt_pk_bf16_f32 v130, v132, v133\nv_cvt_pk_bf16_f32 v131, v134, v135\nbuffer_store_dwordx4 v[128:131], v252, s[20:23], s84 offen offset:0 sc1\nv_cvt_pk_bf16_f32 v136, v136, v137\nv_cvt_pk_bf16_f32 v137, v138, v139\nv_cvt_pk_bf16_f32 v138, v140, v141\n"
"v_cvt_pk_bf16_f32 v139, v142, v143\nbuffer_store_dwordx4 v[136:139], v252, s[20:23], s84 offen offset:1024 sc1\ns_mov_b64 exec, 0\ns_cmp_eq_u32 s40, 1\ns_cbranch_scc1 Lpg7\ns_cmp_eq_u32 s40, 2\n"
"s_cbranch_scc1 LretO0\ns_cmp_eq_u32 s40, 3\ns_cbranch_scc1 LretO0\ns_branch Lpg6\nLretO0:\ns_mov_b64 exec, -1\n"
"s_add_u32 s61, s79, 88080384\ns_lshl_b32 s62, s42, 3\ns_add_u32 s61, s61, s62\ns_waitcnt vmcnt(0)\ns_mov_b64 exec, 1\nbuffer_atomic_swap_x2 v[238:239], off, s[20:23], s61 sc1\n"
"s_mov_b64 exec, -1\nv_bfe_u32 v250, v252, 7, 1\nv_mul_u32_u24 v250, 2176, v250\nv_bfe_u32 v251, v252, 4, 3\nv_lshl_add_u32 v250, v251, 3, v250\ns_lshl_b32 s60, s90, 6\n"
"v_add_u32 v250, s60, v250\ns_sleep 3\nv_mov_b32 v248, 0\nv_mov_b32 v249, 0\ns_add_u32 s61, s80, 88080384\ns_mov_b64 exec, 0xffff\n"
"buffer_atomic_or_x2 v[248:249], v250, s[20:23], s61 offen sc0 sc1\ns_mov_b64 exec, -1\nbuffer_load_dwordx4 v221, s[8:11], s63 offen offset:1024 lds\ns_add_u32 m0, s50, 57344\ns_add_u32 s63, s53, 393216\nbuffer_load_dwordx4 v220, s[8:11], s63 offen lds\n"
"buffer_load_dwordx4 v221, s[8:11], s63 offen offset:1024 lds\ns_add_u32 m0, s50, 65536\ns_add_u32 s63, s53, 458752\nbuffer_load_dwordx4 v220, s[8:11], s63 offen lds\nbuffer_load_dwordx4 v221, s[8:11], s63 offen offset:1024 lds\ns_add_u32 m0, s50, 73728\n"
"s_add_u32 s63, s53, 524288\nbuffer_load_dwordx4 v220, s[8:11], s63 offen lds\nbuffer_load_dwordx4 v221, s[8:11], s63 offen offset:1024 lds\ns_add_u32 m0, s50, 81920\ns_add_u32 s63, s53, 589824\nbuffer_load_dwordx4 v220, s[8:11], s63 offen lds\n"
"buffer_load_dwordx4 v221, s[8:11], s63 offen offset:1024 lds\ns_add_u32 m0, s50, 90112\ns_add_u32 s63, s53, 655360\nbuffer_load_dwordx4 v220, s[8:11], s63 offen lds\ns_waitcnt vmcnt(10)\nv_cmp_eq_u64 s[86:87], s[46:47], v[248:249]\n"
"s_nop 1\ns_and_b32 s88, s86, 0xffff\ns_cmp_eq_u32 s88, 0xffff\ns_cbranch_scc1 Lq201\nv_mov_b32 v246, 0\nv_mov_b32 v247, 0\n"
"s_add_u32 s61, s80, 88080384\ns_mov_b64 exec, 0xffff\nbuffer_atomic_or_x2 v[246:247], v250, s[20:23], s61 offen sc0 sc1\ns_mov_b64 exec, -1\nLq201:\nbuffer_load_dwordx4 v221, s[8:11], s63 offen offset:1024 lds\n"
"v_bfe_u32 v234, v252, 4, 4\nv_lshrrev_b32 v235, 8, v252\nv_lshlrev_b32 v235, 2, v235\nv_sub_u32 v234, v234, v235\nv_lshlrev_b32 v235, 4, v234\ns_mov_b64 s[60:61], 0x3f80\n"
"v_lshlrev_b64 v[208:209], v235, s[60:61]\nv_cmp_gt_u32 s[60:61], 4, v234\ns_nop 1\nv_cndmask_b32 v212, 0, v208, s[60:61]\nv_cndmask_b32 v208, 0, v208, s[60:61]\nv_cndmask_b32 v213, 0, v209, s[60:61]\n"
"v_cndmask_b32 v209, 0, v209, s[60:61]\ns_nop 1\nv_mfma_f32_16x16x32_bf16 v[0:3], v[208:211], v[192:195], 0\nv_mfma_f32_16x16x32_bf16 v[4:7], v[210:213], v[192:195], 0\nv_mfma_f32_16x16x32_bf16 v[8:11], v[208:211], v[196:199], 0\nv_mfma_f32_16x16x32_bf16 v[12:15], v[210:213], v[196:199], 0\n"
"v_mfma_f32_16x16x32_bf16 v[16:19], v[208:211], v[200:203], 0\nv_mfma_f32_16x16x32_bf16 v[20:23], v[210:213], v[200:203], 0\nv_mfma_f32_16x16x32_bf16 v[24:27], v[208:211], v[204:207], 0\nv_mfma_f32_16x16x32_bf16 v[28:31], v[210:213], v[204:207], 0\ns_cmp_eq_u32 s88, 0xffff\ns_cbranch_scc1 Lq202\n"
"s_waitcnt vmcnt(1)\nv_mov_b32 v248, v246\nv_mov_b32 v249, v247\nv_cmp_eq_u64 s[86:87], s[46:47], v[248:249]\ns_nop 1\ns_and_b32 s62, s86, 0xffff\n"
"s_cmp_eq_u32 s62, 0xffff\ns_cbranch_scc1 Lq193\nLq192:\nv_mov_b32 v248, 0\nv_mov_b32 v249, 0\ns_add_u32 s61, s80, 88080384\n"
"s_mov_b64 exec, 0xffff\nbuffer_atomic_or_x2 v[248:249], v250, s[20:23], s61 offen sc0 sc1\ns_mov_b64 exec, -1\ns_waitcnt vmcnt(0)\nv_cmp_eq_u64 s[86:87], s[46:47], v[248:249]\ns_nop 1\n"
"s_and_b32 s62, s86, 0xffff\ns_cmp_eq_u32 s62, 0xffff\ns_cbranch_scc1 Lq193\ns_sub_u32 s48, s48, 1\ns_cmp_gt_i32 s48, 0\ns_cbranch_scc1 Lq192\n"
"Lq193:\nLq202:\ns_add_u32 s64, s70, 2097152\nbuffer_load_dwordx4 a[0:3], v252, s[20:23], s64 offen offset:0 sc1\ns_add_u32 s65, s70, 2129920\nbuffer_load_dwordx4 a[4:7], v252, s[20:23], s65 offen offset:0 sc1\n"
"s_add_u32 s66, s71, 2097152\nbuffer_load_dwordx4 a[8:11], v252, s[20:23], s66 offen offset:0 sc1\ns_add_u32 s67, s71, 2129920\nbuffer_load_dwordx4 a[12:15], v252, s[20:23], s67 offen offset:0 sc1\nbuffer_load_dwordx4 a[16:19], v252, s[20:23], s64 offen offset:1024 sc1\nbuffer_load_dwordx4 a[20:23], v252, s[20:23], s65 offen offset:1024 sc1\n"
"buffer_load_dwordx4 a[24:27], v252, s[20:23], s66 offen offset:1024 sc1\nbuffer_load_dwordx4 a[28:31], v252, s[20:23], s67 offen offset:1024 sc1\ns_barrier\nR 160,184,8192\nR 162,184,12288\nR 164,184,16384\n"
"R 166,184,20480\nR 168,185,8192\nR 170,185,12288\nR 172,185,16384\nR 174,185,20480\nR 176,186,8192\n"
"R 178,186,12288\nR 180,186,16384\nR 182,186,20480\nbuffer_load_dwordx4 a[32:35], v252, s[20:23], s64 offen offset:2048 sc1\nbuffer_load_dwordx4 a[36:39], v252, s[20:23], s65 offen offset:2048 sc1\nbuffer_load_dwordx4 a[40:43], v252, s[20:23], s66 offen offset:2048 sc1\n"
"buffer_load_dwordx4 a[44:47], v252, s[20:23], s67 offen offset:2048 sc1\nbuffer_load_dwordx4 a[48:51], v252, s[20:23], s64 offen offset:3072 sc1\nbuffer_load_dwordx4 a[52:55], v252, s[20:23], s65 offen offset:3072 sc1\nbuffer_load_dwordx4 a[56:59], v252, s[20:23], s66 offen offset:3072 sc1\nbuffer_load_dwordx4 a[60:63], v252, s[20:23], s67 offen offset:3072 sc1\ns_add_u32 s64, s70, 2101248\n"
"buffer_load_dwordx4 a[64:67], v252, s[20:23], s64 offen offset:0 sc1\ns_add_u32 s65, s70, 2134016\nbuffer_load_dwordx4 a[68:71], v252, s[20:23], s65 offen offset:0 sc1\ns_add_u32 s66, s71, 2101248\nbuffer_load_dwordx4 a[72:75], v252, s[20:23], s66 offen offset:0 sc1\ns_add_u32 s67, s71, 2134016\n"
"buffer_load_dwordx4 a[76:79], v252, s[20:23], s67 offen offset:0 sc1\ns_waitcnt vmcnt(19) lgkmcnt(10)\nM 0,160,0\nbuffer_load_dwordx4 a[80:83], v252, s[20:23], s64 offen offset:1024 sc1\ns_waitcnt vmcnt(16) lgkmcnt(8)\nM 0,164,16\n"
"M 16,160,4\ns_lshl_b32 s60, s40, 5\nv_xor_b32 v238, s60, v188\nv_and_b32 v238, 0xfffffbff, v238\ns_waitcnt vmcnt(15)\nM 16,164,20\n"
"v_mov_b32 v242, s76\nv_mov_b32 v243, s76\nv_mfma_f32_16x16x32_bf16 v[32:35], v[160:163], a[8:11], 0\nbuffer_load_dwordx4 a[84:87], v252, s[20:23], s65 offen offset:1024 sc1\ns_waitcnt vmcnt(15)\nM 32,164,24\n"
"v_mov_b32 v244, s76\nv_mov_b32 v245, s76\nv_mfma_f32_16x16x32_bf16 v[48:51], v[160:163], a[12:15], 0\ns_waitcnt vmcnt(14)\nM 48,164,28\nG 4\n"
"v_mfma_f32_16x16x32_bf16 v[52:55], v[168:171], a[12:15], 0\nR 160,187,8192\nM 52,172,28\nbuffer_load_dwordx4 a[88:91], v252, s[20:23], s66 offen offset:1024 sc1\nv_mfma_f32_16x16x32_bf16 v[36:39], v[168:171], a[8:11], 0\nR 162,187,12288\n"
"M 36,172,24\nM 20,168,4\nR 164,187,16384\nR 64,238,32768\nR 66,238,36864\nM 20,172,20\n"
"buffer_load_dwordx4 a[92:95], v252, s[20:23], s67 offen offset:1024 sc1\nM 4,168,0\nR 166,187,20480\nR 68,238,33792\nR 70,238,37888\nM 4,172,16\n"
"G 8\nM 8,176,0\nR 168,184,24576\nM 8,180,16\nbuffer_load_dwordx4 a[96:99], v252, s[20:23], s64 offen offset:2048 sc1\nM 24,176,4\n"
"R 170,184,28672\nM 24,180,20\nv_mfma_f32_16x16x32_bf16 v[40:43], v[176:179], a[8:11], 0\nR 172,184,32768\nG 6\nv_mfma_f32_16x16x32_bf16 v[72:75], v[242:245], v[64:67], 0\n"
"M 40,180,24\nG 3\nv_mfma_f32_16x16x32_bf16 v[72:75], v[242:245], v[68:71], v[72:75]\nbuffer_load_dwordx4 a[100:103], v252, s[20:23], s65 offen offset:2048 sc1\nv_mfma_f32_16x16x32_bf16 v[56:59], v[176:179], a[12:15], 0\nR 174,184,36864\n"
"M 56,180,28\nv_mfma_f32_16x16x32_bf16 v[60:63], v[160:163], a[12:15], 0\nR 176,185,24576\nM 60,164,28\nbuffer_load_dwordx4 a[104:107], v252, s[20:23], s66 offen offset:2048 sc1\nv_lshrrev_b32 v64, 1, v252\n"
"v_mfma_f32_16x16x32_bf16 v[44:47], v[160:163], a[8:11], 0\nR 178,185,28672\nv_mov_b32 v73, s46\ns_and_b32 s61, s40, 3\ns_lshl_b32 s61, s61, 7\ns_add_u32 s61, s61, s91\n"
"s_add_u32 s61, s61, 393216\ns_mov_b64 exec, 0xffff\nbuffer_store_dwordx2 v[72:73], v64, s[20:23], s61 offen sc1\ns_mov_b64 exec, -1\nM 44,164,24\nM 28,160,4\n"
"R 180,185,32768\nM 28,164,20\nbuffer_load_dwordx4 a[108:111], v252, s[20:23], s67 offen offset:2048 sc1\nM 12,160,0\nR 182,185,36864\nM 12,164,16\n"
"s_lshl_b32 s60, s40, 5\nv_xor_b32 v238, s60, v188\nG 4\ns_waitcnt vmcnt(20)\nM 0,168,32\nR 160,186,24576\n"
"v_and_b32 v238, 0xfffffbff, v238\ns_waitcnt vmcnt(16)\nM 0,172,48\nbuffer_load_dwordx4 a[112:115], v252, s[20:23], s64 offen offset:3072 sc1\nM 16,168,36\nR 162,186,28672\n"
"s_waitcnt vmcnt(16)\nM 16,172,52\nM 32,168,40\nR 164,186,32768\ns_waitcnt vmcnt(15)\nM 32,172,56\n"
"buffer_load_dwordx4 a[116:119], v252, s[20:23], s65 offen offset:3072 sc1\ns_barrier\nM 48,168,44\nR 166,186,36864\nR 64,238,40960\nR 66,238,45056\n"
"s_waitcnt vmcnt(15)\nM 48,172,60\nG 6\nM 52,176,44\nR 168,187,24576\nR 68,238,41984\n"
"R 70,238,46080\nM 52,180,60\nbuffer_load_dwordx4 a[120:123], v252, s[20:23], s66 offen offset:3072 sc1\nM 36,176,40\nR 170,187,28672\nM 36,180,56\n"
"M 20,176,36\nR 172,187,32768\nM 20,180,52\nbuffer_load_dwordx4 a[124:127], v252, s[20:23], s67 offen offset:3072 sc1\nM 4,176,32\nR 174,187,36864\n"
"G 6\nv_mfma_f32_16x16x32_bf16 v[72:75], v[242:245], v[64:67], 0\nM 4,180,48\nG 3\nv_mfma_f32_16x16x32_bf16 v[72:75], v[242:245], v[68:71], v[72:75]\nM 8,160,32\n"
"R 176,184,40960\nM 8,164,48\ns_add_u32 s64, s70, 2105344\nbuffer_load_dwordx4 a[128:131], v252, s[20:23], s64 offen offset:0 sc1\nM 24,160,36\nR 178,184,45056\n"
"M 24,164,52\nv_lshrrev_b32 v64, 1, v252\nv_mov_b32 v73, s46\ns_and_b32 s61, s40, 3\ns_lshl_b32 s61, s61, 7\nLpg6:\n"
"s_cbranch_execz LretO0\ns_add_u32 s61, s61, s91\ns_add_u32 s61, s61, 524288\ns_mov_b64 exec, 0xffff\nbuffer_store_dwordx2 v[72:73], v64, s[20:23], s61 offen sc1\ns_mov_b64 exec, -1\n"
"M 40,160,40\nR 180,184,49152\nM 40,164,56\ns_add_u32 s65, s70, 2138112\nbuffer_load_dwordx4 a[132:135], v252, s[20:23], s65 offen offset:0 sc1\nM 56,160,44\n"
"R 182,184,53248\nM 56,164,60\ns_lshl_b32 s60, s40, 5\nv_xor_b32 v238, s60, v188\nG 4\nM 60,168,44\n"
"R 160,185,40960\nv_and_b32 v238, 0xfffffbff, v238\nM 60,172,60\ns_add_u32 s66, s71, 2105344\nbuffer_load_dwordx4 a[136:139], v252, s[20:23], s66 offen offset:0 sc1\nM 44,168,40\n"
"R 162,185,45056\nM 44,172,56\nM 28,168,36\nR 164,185,49152\nM 28,172,52\ns_add_u32 s67, s71, 2138112\n"
"buffer_load_dwordx4 a[140:143], v252, s[20:23], s67 offen offset:0 sc1\nM 12,168,32\nR 166,185,53248\nM 12,172,48\ns_barrier\nG 4\n"
"s_waitcnt vmcnt(21)\nM 0,176,64\nR 168,186,40960\nR 64,238,49152\nR 66,238,53248\ns_waitcnt vmcnt(17)\n"
"M 0,180,80\nbuffer_load_dwordx4 a[144:147], v252, s[20:23], s64 offen offset:1024 sc1\nM 16,176,68\nR 170,186,45056\nR 68,238,50176\nR 70,238,54272\n"
"s_waitcnt vmcnt(17)\nM 16,180,84\nM 32,176,72\nR 172,186,49152\ns_waitcnt vmcnt(16)\nM 32,180,88\n"
"buffer_load_dwordx4 a[148:151], v252, s[20:23], s65 offen offset:1024 sc1\nM 48,176,76\nR 174,186,53248\ns_add_u32 m0, s50, 98304\ns_waitcnt vmcnt(16)\nM 48,180,92\n"
"G 8\nM 52,160,76\nR 176,187,40960\nG 6\nv_mfma_f32_16x16x32_bf16 v[72:75], v[242:245], v[64:67], 0\ns_add_u32 s63, s53, 720896\n"
"M 52,164,92\nG 3\nv_mfma_f32_16x16x32_bf16 v[72:75], v[242:245], v[68:71], v[72:75]\nbuffer_load_dwordx4 a[152:155], v252, s[20:23], s66 offen offset:1024 sc1\nM 36,160,72\nR 178,187,45056\n"
"M 36,164,88\nbuffer_load_dwordx4 v220, s[8:11], s63 offen lds\nM 20,160,68\nR 180,187,49152\nM 20,164,84\nbuffer_load_dwordx4 a[156:159], v252, s[20:23], s67 offen offset:1024 sc1\n"
"v_lshrrev_b32 v64, 1, v252\nM 4,160,64\nR 182,187,53248\nv_mov_b32 v73, s46\ns_and_b32 s61, s40, 3\ns_lshl_b32 s61, s61, 7\n"
"s_add_u32 s61, s61, s91\ns_add_u32 s61, s61, 655360\ns_mov_b64 exec, 0xffff\nbuffer_store_dwordx2 v[72:73], v64, s[20:23], s61 offen sc1\ns_mov_b64 exec, -1\nM 4,164,80\n"
"G 4\nM 8,168,64\nR 160,184,57344\nM 8,172,80\nbuffer_load_dwordx4 a[160:163], v252, s[20:23], s64 offen offset:2048 sc1\nM 24,168,68\n"
"R 162,184,61440\nbuffer_load_dwordx4 v221, s[8:11], s63 offen offset:1024 lds\nM 24,172,84\nM 40,168,72\nR 164,188,0\nM 40,172,88\n"
"buffer_load_dwordx4 a[164:167], v252, s[20:23], s65 offen offset:2048 sc1\nM 56,168,76\nR 166,188,4096\nM 56,172,92\ns_and_b32 s60, s41, 1\ns_lshl1_add_u32 s60, s60, s90\n"
"s_lshl_b32 s60, s60, 5\nv_xor_b32 v238, s60, v184\nG 4\nM 60,176,76\nR 168,185,57344\nv_and_b32 v238, 0xfffffbff, v238\n"
"M 60,180,92\nbuffer_load_dwordx4 a[168:171], v252, s[20:23], s66 offen offset:2048 sc1\nM 44,176,72\nR 170,185,61440\nR 64,238,0\nR 66,238,4096\n"
"M 44,180,88\ns_add_u32 m0, s50, 106496\nM 28,176,68\nR 172,189,0\nR 68,238,1024\nR 70,238,5120\n"
"M 28,180,84\nbuffer_load_dwordx4 a[172:175], v252, s[20:23], s67 offen offset:2048 sc1\nM 12,176,64\nR 174,189,4096\ns_add_u32 s63, s53, 786432\nM 12,180,80\n"
"s_barrier\nG 8\ns_waitcnt vmcnt(24)\nM 0,160,96\nR 176,186,57344\ns_waitcnt vmcnt(19)\n"
"M 0,164,112\nbuffer_load_dwordx4 a[176:179], v252, s[20:23], s64 offen offset:3072 sc1\nbuffer_load_dwordx4 v220, s[8:11], s63 offen lds\nM 16,160,100\nR 178,186,61440\nG 6\n"
"v_mfma_f32_16x16x32_bf16 v[72:75], v[242:245], v[64:67], 0\ns_waitcnt vmcnt(20)\nM 16,164,116\nG 3\nv_mfma_f32_16x16x32_bf16 v[72:75], v[242:245], v[68:71], v[72:75]\nbuffer_load_dwordx4 v221, s[8:11], s63 offen offset:1024 lds\n"
"M 32,160,104\nR 180,190,0\ns_lshr_b32 s60, s45, 3\ns_lshl2_add_u32 s60, s60, s40\ns_lshl_b32 s60, s60, 8\nv_lshl_add_u32 v224, v252, 3, s60\n"
"v_add_u32 v224, Lqw0+24576-LqPC, v224\nv_min_u32 v224, LqE-LqPC-4, v224\ns_mov_b64 exec, 3\nglobal_load_dword v224, v224, s[98:99]\ns_mov_b64 exec, -1\ns_add_u32 s63, s53, 851968\n"
"s_waitcnt vmcnt(21)\nM 32,164,120\nbuffer_load_dwordx4 a[180:183], v252, s[20:23], s65 offen offset:3072 sc1\nM 48,160,108\nR 182,190,4096\ns_add_u32 m0, s50, 114688\n"
"s_waitcnt vmcnt(21)\nM 48,164,124\nbuffer_load_dwordx4 v220, s[8:11], s63 offen lds\nG 4\nv_lshrrev_b32 v64, 1, v252\nM 52,168,108\n"
"R 160,187,57344\nv_mov_b32 v73, s46\ns_and_b32 s61, s40, 1\ns_lshl_b32 s61, s61, 7\ns_add_u32 s61, s61, s91\ns_add_u32 s61, s61, 786432\n"
"s_mov_b64 exec, 0xffff\nbuffer_store_dwordx2 v[72:73], v64, s[20:23], s61 offen sc1\ns_mov_b64 exec, -1\nM 52,172,124\nbuffer_load_dwordx4 a[184:187], v252, s[20:23], s66 offen offset:3072 sc1\nM 36,168,104\n"
"R 162,187,61440\nM 36,172,120\nbuffer_load_dwordx4 v221, s[8:11], s63 offen offset:1024 lds\nM 20,168,100\nR 164,191,0\nM 20,172,116\n"
"buffer_load_dwordx4 a[188:191], v252, s[20:23], s67 offen offset:3072 sc1\nM 4,168,96\nR 166,191,4096\nM 4,172,112\nG 4\nM 8,176,96\n"
"R 168,188,8192\nM 8,180,112\ns_add_u32 s64, s70, 2109440\nbuffer_load_dwordx4 a[192:195], v252, s[20:23], s64 offen offset:0 sc1\nM 24,176,100\nR 170,188,12288\n"
"M 24,180,116\nM 40,176,104\nR 172,188,16384\nM 40,180,120\ns_add_u32 s65, s70, 2142208\nbuffer_load_dwordx4 a[196:199], v252, s[20:23], s65 offen offset:0 sc1\n"
"M 56,176,108\nR 174,188,20480\nM 56,180,124\nG 4\nM 60,160,108\nR 176,189,8192\n"
"M 60,164,124\ns_add_u32 s66, s71, 2109440\nbuffer_load_dwordx4 a[200:203], v252, s[20:23], s66 offen offset:0 sc1\nM 44,160,104\nR 178,189,12288\ns_add_u32 s63, s53, 917504\n"
"M 44,164,120\ns_add_u32 m0, s50, 0\nM 28,160,100\nR 180,189,16384\nM 28,164,116\ns_add_u32 s67, s71, 2142208\n"
"buffer_load_dwordx4 a[204:207], v252, s[20:23], s67 offen offset:0 sc1\nM 12,160,96\nR 182,189,20480\nM 12,164,112\ns_waitcnt vmcnt(17)\ns_barrier\n"
"buffer_load_dwordx4 v220, s[8:11], s63 offen lds\nG 4\nM 0,168,128\nR 160,190,8192\nbuffer_load_dwordx4 v221, s[8:11], s63 offen offset:1024 lds\nM 0,172,144\n"
"buffer_load_dwordx4 a[208:211], v252, s[20:23], s64 offen offset:1024 sc1\ns_add_u32 m0, s50, 8192\ns_add_u32 s63, s53, 983040\nbuffer_load_dwordx4 v220, s[8:11], s63 offen lds\nM 16,168,132\nR 162,190,12288\n"
"buffer_load_dwordx4 v221, s[8:11], s63 offen offset:1024 lds\nM 16,172,148\nM 32,168,136\nR 164,190,16384\nM 32,172,152\nbuffer_load_dwordx4 a[212:215], v252, s[20:23], s65 offen offset:1024 sc1\n"
"M 48,168,140\nR 166,190,20480\nM 48,172,156\nG 4\nM 52,176,140\nR 168,191,8192\n"
"M 52,180,156\nbuffer_load_dwordx4 a[216:219], v252, s[20:23], s66 offen offset:1024 sc1\nM 36,176,136\nR 170,191,12288\nM 36,180,152\nM 20,176,132\n"
"R 172,191,16384\nM 20,180,148\nbuffer_load_dwordx4 a[220:223], v252, s[20:23], s67 offen offset:1024 sc1\nM 4,176,128\nR 174,191,20480\nM 4,180,144\n"
"G 4\nM 8,160,128\nR 176,188,24576\nM 8,164,144\nbuffer_load_dwordx4 a[224:227], v252, s[20:23], s64 offen offset:2048 sc1\nM 24,160,132\n"
"R 178,188,28672\nM 24,164,148\nM 40,160,136\nR 180,188,32768\nM 40,164,152\nbuffer_load_dwordx4 a[228:231], v252, s[20:23], s65 offen offset:2048 sc1\n"
"M 56,160,140\nR 182,188,36864\nM 56,164,156\nG 4\nM 60,168,140\nR 160,189,24576\n"
"M 60,172,156\nbuffer_load_dwordx4 a[232:235], v252, s[20:23], s66 offen offset:2048 sc1\nM 44,168,136\nR 162,189,28672\nM 44,172,152\nM 28,168,132\n"
"R 164,189,32768\nM 28,172,148\nbuffer_load_dwordx4 a[236:239], v252, s[20:23], s67 offen offset:2048 sc1\nM 12,168,128\nR 166,189,36864\nM 12,172,144\n"
"G 4\nM 0,176,160\nR 168,190,24576\ns_waitcnt vmcnt(25)\nM 0,180,176\nbuffer_load_dwordx4 a[240:243], v252, s[20:23], s64 offen offset:3072 sc1\n"
"M 16,176,164\nR 170,190,28672\ns_waitcnt vmcnt(22)\nM 16,180,180\nM 32,176,168\nR 172,190,32768\n"
"s_waitcnt vmcnt(19)\nM 32,180,184\nbuffer_load_dwordx4 a[244:247], v252, s[20:23], s65 offen offset:3072 sc1\nM 48,176,172\nR 174,190,36864\ns_waitcnt vmcnt(18)\n"
"M 48,180,188\nG 4\nM 52,160,172\nR 176,191,24576\nM 52,164,188\nbuffer_load_dwordx4 a[248:251], v252, s[20:23], s66 offen offset:3072 sc1\n"
"M 36,160,168\nR 178,191,28672\ns_waitcnt vmcnt(10)\ns_barrier\nM 36,164,184\nM 20,160,164\n"
"R 180,191,32768\nM 20,164,180\nbuffer_load_dwordx4 a[252:255], v252, s[20:23], s67 offen offset:3072 sc1\nM 4,160,160\nR 182,191,36864\nM 4,164,176\n"
"G 4\nM 8,168,160\nR 160,188,40960\nM 8,172,176\nM 24,168,164\nR 162,188,45056\n"
"M 24,172,180\nM 40,168,168\nR 164,188,49152\nM 40,172,184\nM 56,168,172\nR 166,188,53248\n"
"M 56,172,188\nG 4\nM 60,176,172\nR 168,189,40960\nM 60,180,188\ns_movk_i32 s63, 7168\n"
"v_mad_u32_u24 v220, v228, s63, v229\nv_add_u32 v221, 3668992, v220\ns_add_u32 m0, s50, 16384\ns_add_u32 s63, s54, 0\nbuffer_load_dwordx4 v220, s[12:15], s63 offen lds\nM 44,176,168\n"
"R 170,189,45056\nM 44,180,184\nM 28,176,164\nR 172,189,49152\nM 28,180,180\nbuffer_load_dwordx4 v221, s[12:15], s63 offen offset:1024 lds\n"
"M 12,176,160\nR 174,189,53248\ns_add_u32 m0, s50, 24576\nM 12,180,176\nG 4\nM 32,160,200\n"
"R 176,190,40960\ns_add_u32 s63, s54, 229376\ns_waitcnt vmcnt(11)\nM 32,164,216\nR 178,190,45056\nM 48,160,204\n"
"R 180,190,49152\ns_waitcnt vmcnt(10)\nM 48,164,220\nR 182,190,53248\nG 4\nM 52,168,204\n"
"R 160,191,40960\nbuffer_load_dwordx4 v220, s[12:15], s63 offen lds\nM 52,172,220\nR 162,191,45056\nM 36,168,200\nR 164,191,49152\n"
"M 36,172,216\nR 166,191,53248\nG 4\nM 40,176,200\nR 168,184,0\nbuffer_load_dwordx4 v221, s[12:15], s63 offen offset:1024 lds\n"
"M 40,180,216\nR 170,184,4096\ns_add_u32 m0, s50, 32768\nM 56,176,204\nR 172,184,8192\ns_add_u32 s63, s54, 128\n"
"M 56,180,220\nR 174,184,12288\nG 4\nM 60,160,204\nR 176,185,0\nbuffer_load_dwordx4 v220, s[12:15], s63 offen lds\n"
"M 60,164,220\nR 178,185,4096\nM 44,160,200\nR 180,185,8192\nM 44,164,216\nR 182,185,12288\n"
"G 4\ns_waitcnt vmcnt(10)\nM 32,168,232\nR 160,186,0\nbuffer_load_dwordx4 v221, s[12:15], s63 offen offset:1024 lds\ns_waitcnt vmcnt(7)\n"
"M 32,172,248\nR 162,186,4096\ns_add_u32 m0, s50, 40960\nM 48,168,236\nR 164,186,8192\ns_add_u32 s63, s54, 229504\n"
"s_waitcnt vmcnt(6)\nM 48,172,252\nR 166,186,12288\nG 4\nM 52,176,236\nR 168,187,0\n"
"buffer_load_dwordx4 v220, s[12:15], s63 offen lds\nM 52,180,252\nR 170,187,4096\nM 36,176,232\nR 172,187,8192\nM 36,180,248\n"
"R 174,187,12288\nG 4\nM 40,160,232\nR 176,188,40960\nbuffer_load_dwordx4 v221, s[12:15], s63 offen offset:1024 lds\nM 40,164,248\n"
"R 178,188,45056\nM 56,160,236\nR 180,188,49152\nM 56,164,252\nR 182,188,53248\nG 4\n"
"M 60,168,236\nR 160,189,40960\nM 60,172,252\nR 162,189,45056\nM 44,168,232\nR 164,189,49152\n"
"M 44,172,248\nR 166,189,53248\nG 4\nM 0,176,192\nR 168,190,40960\nM 0,180,208\n"
"R 170,190,45056\ns_mov_b32 m0, s51\nM 16,176,196\nR 172,190,49152\nv_cvt_pk_bf16_f32 v32, v32, v33\nv_cvt_pk_bf16_f32 v33, v34, v35\n"
"M 16,180,212\nR 174,190,53248\nG 4\nv_cvt_pk_bf16_f32 v34, v36, v37\nM 20,160,196\nR 176,191,40960\n"
"v_cvt_pk_bf16_f32 v35, v38, v39\nv_cvt_pk_bf16_f32 v36, v40, v41\nM 20,164,212\nR 178,191,45056\nv_cvt_pk_bf16_f32 v37, v42, v43\nv_cvt_pk_bf16_f32 v38, v44, v45\n"
"M 4,160,192\nR 180,191,49152\nv_cvt_pk_bf16_f32 v39, v46, v47\nv_cvt_pk_bf16_f32 v40, v48, v49\nM 4,164,208\nR 182,191,53248\n"
"G 4\nv_cvt_pk_bf16_f32 v41, v50, v51\nM 8,168,192\nR 160,184,0\nv_cvt_pk_bf16_f32 v42, v52, v53\nv_cvt_pk_bf16_f32 v43, v54, v55\n"
"M 8,172,208\nR 162,184,4096\nv_cvt_pk_bf16_f32 v44, v56, v57\nv_cvt_pk_bf16_f32 v45, v58, v59\nM 24,168,196\nR 164,184,8192\n"
"v_cvt_pk_bf16_f32 v46, v60, v61\nv_cvt_pk_bf16_f32 v47, v62, v63\nM 24,172,212\nR 166,184,12288\nds_write_addtid_b32 v32 offset:0\nG 5\n"
"M 28,176,196\nR 168,185,0\nds_write_addtid_b32 v33 offset:256\nM 28,180,212\nR 170,185,4096\nds_write_addtid_b32 v34 offset:512\n"
"M 12,176,192\nR 172,185,8192\nds_write_addtid_b32 v35 offset:768\nds_write_addtid_b32 v36 offset:1024\nds_write_addtid_b32 v37 offset:1280\nds_write_addtid_b32 v38 offset:1536\n"
"ds_write_addtid_b32 v39 offset:1792\nds_write_addtid_b32 v40 offset:2048\nds_write_addtid_b32 v41 offset:2304\nds_write_addtid_b32 v42 offset:2560\nds_write_addtid_b32 v43 offset:2816\nds_write_addtid_b32 v44 offset:3072\n"
"ds_write_addtid_b32 v45 offset:3328\nds_write_addtid_b32 v46 offset:3584\nds_write_addtid_b32 v47 offset:3840\nM 12,180,208\nR 174,185,12288\nG 15\n"
"M 0,160,224\nR 176,186,0\ns_add_u32 m0, s50, 49152\nM 0,164,240\nR 178,186,4096\nG 3\n"
"s_barrier\ns_add_u32 s63, s54, 458752\nM 16,160,228\nR 180,186,8192\nbuffer_load_dwordx4 v220, s[12:15], s63 offen lds\nds_read2st64_b32 v[32:33], v225 offset0:0 offset1:1\n"
"M 16,164,244\nR 182,186,12288\nds_read2st64_b32 v[34:35], v225 offset0:2 offset1:3\nG 6\nM 20,168,228\nR 160,187,0\n"
"ds_read2st64_b32 v[36:37], v225 offset0:4 offset1:5\nM 20,172,244\nR 162,187,4096\nds_read2st64_b32 v[38:39], v225 offset0:6 offset1:7\nM 4,168,224\nR 164,187,8192\n"
"buffer_load_dwordx4 v221, s[12:15], s63 offen offset:1024 lds\nds_read2st64_b32 v[40:41], v225 offset0:8 offset1:9\nM 4,172,240\nR 166,187,12288\ns_add_u32 m0, s50, 57344\ns_add_u32 s63, s54, 688128\n"
"Lpg7:\ns_cbranch_execz LretO0\nds_read2st64_b32 v[42:43], v225 offset0:10 offset1:11\nG 9\nM 8,176,224\nbuffer_load_dwordx4 v220, s[12:15], s63 offen lds\n"
"ds_read2st64_b32 v[44:45], v225 offset0:12 offset1:13\nM 8,180,240\nds_read2st64_b32 v[46:47], v225 offset0:14 offset1:15\nM 24,176,228\nM 24,180,244\nG 3\n"
"M 28,160,228\nbuffer_load_dwordx4 v221, s[12:15], s63 offen offset:1024 lds\nM 28,164,244\nM 12,160,224\nM 12,164,240\nG 0\n"
"s_barrier\nv_mfma_f32_16x16x32_bf16 v[0:3], v[208:211], v[32:35], v[0:3]\nv_mfma_f32_16x16x32_bf16 v[4:7], v[210:213], v[32:35], v[4:7]\nv_mfma_f32_16x16x32_bf16 v[8:11], v[208:211], v[36:39], v[8:11]\nv_mfma_f32_16x16x32_bf16 v[12:15], v[210:213], v[36:39], v[12:15]\nv_mfma_f32_16x16x32_bf16 v[16:19], v[208:211], v[40:43], v[16:19]\n"
"v_mfma_f32_16x16x32_bf16 v[20:23], v[210:213], v[40:43], v[20:23]\nv_mfma_f32_16x16x32_bf16 v[24:27], v[208:211], v[44:47], v[24:27]\nv_mfma_f32_16x16x32_bf16 v[28:31], v[210:213], v[44:47], v[28:31]\ns_lshl_b32 s84, s75, 11\ns_add_u32 s84, s84, s49\ns_add_u32 s84, s84, 10485760\n"
"s_add_u32 s85, s84, 32768\nv_mov_b32 v238, s46\nv_mov_b32 v239, s47\nv_cvt_pk_bf16_f32 v192, v0, v1\nv_cvt_pk_bf16_f32 v193, v2, v3\nv_cvt_pk_bf16_f32 v194, v4, v5\n"
"v_cvt_pk_bf16_f32 v195, v6, v7\nbuffer_store_dwordx4 v[192:195], v252, s[20:23], s84 offen offset:0 sc1\nv_cvt_pk_bf16_f32 v196, v8, v9\nv_cvt_pk_bf16_f32 v197, v10, v11\nv_cvt_pk_bf16_f32 v198, v12, v13\nv_cvt_pk_bf16_f32 v199, v14, v15\n"
"buffer_store_dwordx4 v[196:199], v252, s[20:23], s84 offen offset:1024 sc1\nv_cvt_pk_bf16_f32 v200, v16, v17\nv_cvt_pk_bf16_f32 v201, v18, v19\nv_cvt_pk_bf16_f32 v202, v20, v21\nv_cvt_pk_bf16_f32 v203, v22, v23\nbuffer_store_dwordx4 v[200:203], v252, s[20:23], s85 offen offset:0 sc1\n"
"v_cvt_pk_bf16_f32 v204, v24, v25\nv_cvt_pk_bf16_f32 v205, v26, v27\nv_cvt_pk_bf16_f32 v206, v28, v29\nv_cvt_pk_bf16_f32 v207, v30, v31\nbuffer_store_dwordx4 v[204:207], v252, s[20:23], s85 offen offset:1024 sc1\ns_mov_b64 exec, 0\n"
"s_cmp_eq_u32 s40, 1\ns_cbranch_scc1 Lpg9\ns_cmp_eq_u32 s40, 2\ns_cbranch_scc1 Lpg10\ns_cmp_eq_u32 s40, 3\ns_cbranch_scc1 Lpg11\n"
"s_branch Lpg8\nLretO0n:\ns_mov_b64 exec, -1\ns_add_u32 s61, s79, 88219648\ns_lshl_b32 s62, s75, 3\ns_add_u32 s61, s61, s62\n"
"s_waitcnt vmcnt(0)\ns_mov_b64 exec, 1\nbuffer_atomic_swap_x2 v[238:239], off, s[20:23], s61 sc1\ns_mov_b64 exec, -1\nLqw0:\nv_bfe_u32 v250, v252, 7, 1\n"
"v_mul_u32_u24 v250, 2176, v250\nv_bfe_u32 v251, v252, 4, 3\nv_lshl_add_u32 v250, v251, 3, v250\ns_lshl_b32 s60, s90, 6\nv_add_u32 v250, s60, v250\ns_sleep 3\n"
"v_mov_b32 v248, 0\nv_mov_b32 v249, 0\ns_add_u32 s61, s80, 88219648\ns_mov_b64 exec, 0xffff\nbuffer_atomic_or_x2 v[248:249], v250, s[20:23], s61 offen sc0 sc1\ns_mov_b64 exec, -1\n"
"s_add_u32 m0, s50, 65536\ns_add_u32 s63, s54, 458880\nbuffer_load_dwordx4 v220, s[12:15], s63 offen lds\nbuffer_load_dwordx4 v221, s[12:15], s63 offen offset:1024 lds\ns_add_u32 m0, s50, 73728\ns_add_u32 s63, s54, 688256\n"
"buffer_load_dwordx4 v220, s[12:15], s63 offen lds\nbuffer_load_dwordx4 v221, s[12:15], s63 offen offset:1024 lds\ns_add_u32 m0, s50, 81920\ns_add_u32 s63, s54, 917504\nbuffer_load_dwordx4 v220, s[12:15], s63 offen lds\nbuffer_load_dwordx4 v221, s[12:15], s63 offen offset:1024 lds\n"
"s_add_u32 m0, s50, 90112\ns_add_u32 s63, s54, 1146880\nbuffer_load_dwordx4 v220, s[12:15], s63 offen lds\nbuffer_load_dwordx4 v221, s[12:15], s63 offen offset:1024 lds\ns_add_u32 m0, s50, 98304\ns_add_u32 s63, s54, 917632\n"
"buffer_load_dwordx4 v220, s[12:15], s63 offen lds\nbuffer_load_dwordx4 v221, s[12:15], s63 offen offset:1024 lds\ns_waitcnt vmcnt(10)\nv_cmp_eq_u64 s[86:87], s[46:47], v[248:249]\ns_nop 1\ns_and_b32 s88, s86, 0xffff\n"
"s_cmp_eq_u32 s88, 0xffff\ns_cbranch_scc1 Lq265\nv_mov_b32 v246, 0\nv_mov_b32 v247, 0\ns_add_u32 s61, s80, 88219648\ns_mov_b64 exec, 0xffff\n"
"buffer_atomic_or_x2 v[246:247], v250, s[20:23], s61 offen sc0 sc1\ns_mov_b64 exec, -1\nLq265:\ns_add_u32 m0, s50, 106496\ns_add_u32 s63, s54, 1147008\nbuffer_load_dwordx4 v220, s[12:15], s63 offen lds\n"
"buffer_load_dwordx4 v221, s[12:15], s63 offen offset:1024 lds\ns_mov_b64 exec, 0\ns_cmp_eq_u32 s40, 1\ns_cbranch_scc1 Lpg17\ns_cmp_eq_u32 s40, 2\ns_cbranch_scc1 LretW0z\n"
"s_cmp_eq_u32 s40, 3\ns_cbranch_scc1 LretW0z\ns_branch Lpg16\nLretW0z:\ns_mov_b64 exec, -1\nv_mov_b32 v230, 0\n"
"v_mov_b32 v231, 0\nv_mov_b32 v232, 0\nv_mov_b32 v233, 0\nv_dot2c_f32_bf16 v230, s76, v192\nv_dot2c_f32_bf16 v231, v192, v192\nv_dot2c_f32_bf16 v232, s76, v200\n"
"v_dot2c_f32_bf16 v233, v200, v200\nv_dot2c_f32_bf16 v230, s76, v193\nv_dot2c_f32_bf16 v231, v193, v193\nv_dot2c_f32_bf16 v232, s76, v201\nv_dot2c_f32_bf16 v233, v201, v201\nv_dot2c_f32_bf16 v230, s76, v194\n"
"v_dot2c_f32_bf16 v231, v194, v194\nv_dot2c_f32_bf16 v232, s76, v202\nv_dot2c_f32_bf16 v233, v202, v202\nv_dot2c_f32_bf16 v230, s76, v195\nv_dot2c_f32_bf16 v231, v195, v195\nv_dot2c_f32_bf16 v232, s76, v203\n"
"v_dot2c_f32_bf16 v233, v203, v203\nv_dot2c_f32_bf16 v230, s76, v196\nv_dot2c_f32_bf16 v231, v196, v196\nv_dot2c_f32_bf16 v232, s76, v204\nv_dot2c_f32_bf16 v233, v204, v204\nv_dot2c_f32_bf16 v230, s76, v197\n"
"v_dot2c_f32_bf16 v231, v197, v197\nv_dot2c_f32_bf16 v232, s76, v205\nv_dot2c_f32_bf16 v233, v205, v205\nv_dot2c_f32_bf16 v230, s76, v198\nv_dot2c_f32_bf16 v231, v198, v198\nv_dot2c_f32_bf16 v232, s76, v206\n"
"v_dot2c_f32_bf16 v233, v206, v206\nv_dot2c_f32_bf16 v230, s76, v199\nv_dot2c_f32_bf16 v231, v199, v199\nv_dot2c_f32_bf16 v232, s76, v207\nv_dot2c_f32_bf16 v233, v207, v207\nv_lshrrev_b32 v234, 5, v252\n"
"v_and_b32 v234, 24, v234\nv_bfe_u32 v235, s46, v234, 8\nv_lshrrev_b32 v234, 1, v234\nv_and_b32 v236, 0xf0, v252\nv_add_u32 v234, v234, v236\nv_permlane32_swap_b32 v230, v231\n"
"v_permlane32_swap_b32 v232, v233\nv_add_f32 v230, v230, v231\nv_add_f32 v232, v232, v233\ns_nop 1\nv_permlane16_swap_b32 v230, v232\nv_add_f32 v230, v230, v232\n"
"s_movk_i32 s62, 0xff\nv_bfi_b32 v230, s62, v235, v230\ns_lshr_b32 s61, s49, 4\ns_lshl1_add_u32 s61, s53, s61\ns_add_u32 s61, s61, 82313216\nbuffer_store_dword v230, v234, s[20:23], s61 offen sc1\n"
"s_cmp_eq_u32 s88, 0xffff\ns_cbranch_scc1 Lq266\ns_waitcnt vmcnt(3)\nv_mov_b32 v248, v246\nv_mov_b32 v249, v247\nv_cmp_eq_u64 s[86:87], s[46:47], v[248:249]\n"
"s_nop 1\ns_and_b32 s62, s86, 0xffff\ns_cmp_eq_u32 s62, 0xffff\ns_cbranch_scc1 Lq257\nLq256:\nv_mov_b32 v248, 0\n"
"v_mov_b32 v249, 0\ns_add_u32 s61, s80, 88219648\ns_mov_b64 exec, 0xffff\nbuffer_atomic_or_x2 v[248:249], v250, s[20:23], s61 offen sc0 sc1\ns_mov_b64 exec, -1\ns_waitcnt vmcnt(0)\n"
"v_cmp_eq_u64 s[86:87], s[46:47], v[248:249]\ns_nop 1\ns_and_b32 s62, s86, 0xffff\ns_cmp_eq_u32 s62, 0xffff\ns_cbranch_scc1 Lq257\ns_sub_u32 s48, s48, 1\n"
"s_cmp_gt_i32 s48, 0\ns_cbranch_scc1 Lq256\nLq257:\nLq266:\ns_add_u32 s64, s70, 10485760\nbuffer_load_dwordx4 a[0:3], v252, s[20:23], s64 offen offset:0 sc1\n"
"s_add_u32 s65, s70, 10518528\nbuffer_load_dwordx4 a[4:7], v252, s[20:23], s65 offen offset:0 sc1\ns_add_u32 s66, s71, 10485760\nbuffer_load_dwordx4 a[8:11], v252, s[20:23], s66 offen offset:0 sc1\ns_add_u32 s67, s71, 10518528\nbuffer_load_dwordx4 a[12:15], v252, s[20:23], s67 offen offset:0 sc1\n"
"buffer_load_dwordx4 a[16:19], v252, s[20:23], s64 offen offset:1024 sc1\nbuffer_load_dwordx4 a[20:23], v252, s[20:23], s65 offen offset:1024 sc1\nbuffer_load_dwordx4 a[24:27], v252, s[20:23], s66 offen offset:1024 sc1\nbuffer_load_dwordx4 a[28:31], v252, s[20:23], s67 offen offset:1024 sc1\ns_barrier\nR 160,184,16384\n"
"R 162,184,20480\nR 164,184,24576\nR 166,184,28672\nR 168,185,16384\nR 170,185,20480\nR 172,185,24576\n"
"R 174,185,28672\nR 176,186,16384\nR 178,186,20480\nR 180,186,24576\nR 182,186,28672\nbuffer_load_dwordx4 a[32:35], v252, s[20:23], s64 offen offset:2048 sc1\n"
"buffer_load_dwordx4 a[36:39], v252, s[20:23], s65 offen offset:2048 sc1\nbuffer_load_dwordx4 a[40:43], v252, s[20:23], s66 offen offset:2048 sc1\nbuffer_load_dwordx4 a[44:47], v252, s[20:23], s67 offen offset:2048 sc1\nbuffer_load_dwordx4 a[48:51], v252, s[20:23], s64 offen offset:3072 sc1\nbuffer_load_dwordx4 a[52:55], v252, s[20:23], s65 offen offset:3072 sc1\nbuffer_load_dwordx4 a[56:59], v252, s[20:23], s66 offen offset:3072 sc1\n"
"buffer_load_dwordx4 a[60:63], v252, s[20:23], s67 offen offset:3072 sc1\ns_waitcnt vmcnt(15) lgkmcnt(10)\nv_mfma_f32_16x16x32_bf16 v[32:35], v[160:163], a[0:3], 0\ns_add_u32 s64, s70, 10489856\nbuffer_load_dwordx4 a[64:67], v252, s[20:23], s64 offen offset:0 sc1\ns_waitcnt vmcnt(12) lgkmcnt(8)\n"
"M 32,164,16\nv_mfma_f32_16x16x32_bf16 v[48:51], v[160:163], a[4:7], 0\ns_waitcnt vmcnt(11)\nM 48,164,20\nv_mfma_f32_16x16x32_bf16 v[64:67], v[160:163], a[8:11], 0\nv_lshrrev_b32 v238, 7, v252\n"
"s_waitcnt vmcnt(10)\nM 64,164,24\nv_lshlrev_b32 v238, 9, v238\nv_mfma_f32_16x16x32_bf16 v[80:83], v[160:163], a[12:15], 0\ns_add_u32 s65, s70, 10522624\nbuffer_load_dwordx4 a[68:71], v252, s[20:23], s65 offen offset:0 sc1\n"
"s_waitcnt vmcnt(10)\nM 80,164,28\nG 4\nv_and_b32 v239, 0x70, v252\nv_mfma_f32_16x16x32_bf16 v[84:87], v[168:171], a[12:15], 0\nR 160,187,16384\n"
"M 84,172,28\ns_and_b32 s60, s40, 3\ns_lshl_b32 s60, s60, 7\nv_add3_u32 v238, v238, v239, s60\nv_mfma_f32_16x16x32_bf16 v[68:71], v[168:171], a[8:11], 0\nR 162,187,20480\n"
"s_lshl_b32 s61, s42, 13\ns_add_u32 s61, s61, 84279296\nbuffer_load_dwordx4 v[0:3], v238, s[20:23], s61 offen sc1\nM 68,172,24\ns_lshl_b32 s61, s42, 13\ns_add_u32 s61, s61, 84283392\n"
"buffer_load_dwordx4 v[4:7], v238, s[20:23], s61 offen sc1\nv_mfma_f32_16x16x32_bf16 v[52:55], v[168:171], a[4:7], 0\nR 164,187,24576\nM 52,172,20\ns_add_u32 s66, s71, 10489856\nbuffer_load_dwordx4 a[72:75], v252, s[20:23], s66 offen offset:0 sc1\n"
"v_bfe_u32 v239, v238, 2, 7\nv_mfma_f32_16x16x32_bf16 v[36:39], v[168:171], a[0:3], 0\nR 166,187,28672\nM 36,172,16\nbuffer_load_dword v239, v239, s[32:35], s54 offen offset:0\nG 4\n"
"v_mfma_f32_16x16x32_bf16 v[40:43], v[176:179], a[0:3], 0\nR 168,184,32768\nM 40,180,16\nv_mfma_f32_16x16x32_bf16 v[56:59], v[176:179], a[4:7], 0\nR 170,184,36864\nM 56,180,20\n"
"s_add_u32 s67, s71, 10522624\nbuffer_load_dwordx4 a[76:79], v252, s[20:23], s67 offen offset:0 sc1\nv_mfma_f32_16x16x32_bf16 v[72:75], v[176:179], a[8:11], 0\nR 172,184,40960\nM 72,180,24\nv_mfma_f32_16x16x32_bf16 v[88:91], v[176:179], a[12:15], 0\n"
"R 174,184,45056\nM 88,180,28\nG 4\nv_mfma_f32_16x16x32_bf16 v[92:95], v[160:163], a[12:15], 0\nR 176,185,32768\nM 92,164,28\n"
"buffer_load_dwordx4 a[80:83], v252, s[20:23], s64 offen offset:1024 sc1\nv_mfma_f32_16x16x32_bf16 v[76:79], v[160:163], a[8:11], 0\nR 178,185,36864\nM 76,164,24\nv_mfma_f32_16x16x32_bf16 v[60:63], v[160:163], a[4:7], 0\nR 180,185,40960\n"
"M 60,164,20\nv_mfma_f32_16x16x32_bf16 v[44:47], v[160:163], a[0:3], 0\nR 182,185,45056\nM 44,164,16\nG 4\nv_mfma_f32_16x16x32_bf16 v[96:99], v[168:171], a[0:3], 0\n"
"R 160,186,32768\nM 96,172,16\nbuffer_load_dwordx4 a[84:87], v252, s[20:23], s65 offen offset:1024 sc1\nv_mfma_f32_16x16x32_bf16 v[112:115], v[168:171], a[4:7], 0\nR 162,186,36864\nM 112,172,20\n"
"v_mfma_f32_16x16x32_bf16 v[128:131], v[168:171], a[8:11], 0\nR 164,186,40960\nM 128,172,24\nbuffer_load_dwordx4 a[88:91], v252, s[20:23], s66 offen offset:1024 sc1\nv_mfma_f32_16x16x32_bf16 v[144:147], v[168:171], a[12:15], 0\nR 166,186,45056\n"
"M 144,172,28\ns_barrier\nG 4\nv_mfma_f32_16x16x32_bf16 v[148:151], v[176:179], a[12:15], 0\nR 168,187,32768\nM 148,180,28\n"
"v_mfma_f32_16x16x32_bf16 v[132:135], v[176:179], a[8:11], 0\nR 170,187,36864\nM 132,180,24\nbuffer_load_dwordx4 a[92:95], v252, s[20:23], s67 offen offset:1024 sc1\nv_mfma_f32_16x16x32_bf16 v[116:119], v[176:179], a[4:7], 0\nR 172,187,40960\n"
"M 116,180,20\nv_mfma_f32_16x16x32_bf16 v[100:103], v[176:179], a[0:3], 0\nR 174,187,45056\nM 100,180,16\nG 4\nv_mfma_f32_16x16x32_bf16 v[104:107], v[160:163], a[0:3], 0\n"
"R 176,184,49152\nv_lshrrev_b32 v242, 7, v252\nM 104,164,16\nbuffer_load_dwordx4 a[96:99], v252, s[20:23], s64 offen offset:2048 sc1\nv_mfma_f32_16x16x32_bf16 v[120:123], v[160:163], a[4:7], 0\nR 178,184,53248\n"
"v_lshlrev_b32 v242, 9, v242\ns_waitcnt vmcnt(8)\nv_min3_u32 v240, v1, v3, v5\nM 120,164,20\nv_and_b32 v243, 0x70, v252\nv_min3_u32 v240, v240, v7, v7\n"
"v_max3_u32 v241, v1, v3, v5\nv_mfma_f32_16x16x32_bf16 v[136:139], v[160:163], a[8:11], 0\nR 180,184,57344\nv_max3_u32 v241, v241, v7, v7\nv_cmp_ne_u32 s[86:87], s46, v240\nM 136,164,24\n"
"s_and_b32 s60, s40, 3\ns_lshl_b32 s60, s60, 7\nv_add3_u32 v242, v242, v243, s60\nv_cmp_ne_u32 s[88:89], s46, v241\nv_mfma_f32_16x16x32_bf16 v[152:155], v[160:163], a[12:15], 0\nR 182,184,61440\n"
"s_lshl_b32 s61, s42, 13\ns_add_u32 s61, s61, 84410368\nbuffer_load_dwordx4 v[16:19], v242, s[20:23], s61 offen sc1\ns_or_b64 s[86:87], s[86:87], s[88:89]\ns_cbranch_scc1 Lq319\nLq320:\n"
"M 152,164,28\ns_lshl_b32 s61, s42, 13\ns_add_u32 s61, s61, 84414464\nbuffer_load_dwordx4 v[20:23], v242, s[20:23], s61 offen sc1\nG 4\nv_mfma_f32_16x16x32_bf16 v[156:159], v[168:171], a[12:15], 0\n"
"R 160,185,49152\nv_add_f32 v0, v0, v4\nM 156,172,28\nbuffer_load_dwordx4 a[100:103], v252, s[20:23], s65 offen offset:2048 sc1\nv_bfe_u32 v243, v242, 2, 7\nv_mfma_f32_16x16x32_bf16 v[140:143], v[168:171], a[8:11], 0\n"
"R 162,185,53248\nv_add_f32 v2, v2, v6\nM 140,172,24\nbuffer_load_dword v243, v243, s[32:35], s54 offen offset:128\nv_mfma_f32_16x16x32_bf16 v[124:127], v[168:171], a[4:7], 0\nR 164,185,57344\n"
"v_add_f32_dpp v0, v0, v0 row_ror:8 row_mask:0xf bank_mask:0xf\nM 124,172,20\nbuffer_load_dwordx4 a[104:107], v252, s[20:23], s66 offen offset:2048 sc1\nv_mfma_f32_16x16x32_bf16 v[108:111], v[168:171], a[0:3], 0\nR 166,185,61440\nv_add_f32_dpp v2, v2, v2 row_ror:8 row_mask:0xf bank_mask:0xf\n"
"M 108,172,16\nG 4\nv_mov_b32 v4, v0\nv_mov_b32 v5, v2\nM 32,176,32\nR 168,186,49152\n"
"v_permlane32_swap_b32 v4, v0\nM 32,180,48\nv_permlane32_swap_b32 v5, v2\nv_add_f32 v0, v0, v4\nM 48,176,36\nR 170,186,53248\n"
"v_add_f32 v2, v2, v5\nM 48,180,52\nbuffer_load_dwordx4 a[108:111], v252, s[20:23], s67 offen offset:2048 sc1\nM 64,176,40\nR 172,186,57344\nv_mov_b32 v4, v0\n"
"M 64,180,56\nv_mov_b32 v5, v2\nv_permlane16_swap_b32 v4, v0\nM 80,176,44\nR 174,186,61440\nv_permlane16_swap_b32 v5, v2\n"
"M 80,180,60\ns_barrier\nG 4\nM 84,160,44\nR 176,187,49152\nv_add_f32 v0, v0, v4\n"
"M 84,164,60\nbuffer_load_dwordx4 a[112:115], v252, s[20:23], s64 offen offset:3072 sc1\nM 68,160,40\nR 178,187,53248\nv_add_f32 v2, v2, v5\nM 68,164,56\n"
"v_and_b32 v8, s77, v0\nv_sub_f32 v9, v0, v8\nM 52,160,36\nR 180,187,57344\ns_waitcnt vmcnt(13)\nv_lshlrev_b32 v10, 16, v239\n"
"M 52,164,52\nv_cvt_pk_bf16_f32 v4, v8, v9\nv_cvt_pk_bf16_f32 v5, v8, v10\nM 36,160,32\nR 182,187,61440\nv_and_b32 v8, s77, v2\n"
"M 36,164,48\nG 4\nv_sub_f32 v9, v2, v8\nv_and_b32 v10, s77, v239\nM 40,168,32\nR 160,188,0\n"
"v_cvt_pk_bf16_f32 v6, v8, v9\nM 40,172,48\nbuffer_load_dwordx4 a[116:119], v252, s[20:23], s65 offen offset:3072 sc1\nM 56,168,36\nR 162,188,4096\nv_cvt_pk_bf16_f32 v7, v8, v10\n"
"M 56,172,52\nv_and_b32 v238, 0x1ff, v238\nv_add_u32 v238, 157184, v238\nM 72,168,40\nR 164,188,8192\nds_write_b128 v238, v[4:7]\n"
"M 72,172,56\nbuffer_load_dwordx4 a[120:123], v252, s[20:23], s66 offen offset:3072 sc1\nM 88,168,44\nR 166,188,12288\nM 88,172,60\nG 5\n"
"M 92,176,44\nR 168,189,0\nM 92,180,60\nM 76,176,40\nR 170,189,4096\nM 76,180,56\n"
"buffer_load_dwordx4 a[124:127], v252, s[20:23], s67 offen offset:3072 sc1\nM 60,176,36\nR 172,189,8192\nv_lshrrev_b32 v238, 7, v252\nM 60,180,52\nv_lshlrev_b32 v238, 9, v238\n"
"M 44,176,32\nR 174,189,12288\ns_waitcnt vmcnt(8)\nv_min3_u32 v244, v17, v19, v21\nv_min3_u32 v244, v244, v23, v23\nM 44,180,48\n"
"G 4\nv_and_b32 v239, 0x70, v252\nv_max3_u32 v245, v17, v19, v21\nv_max3_u32 v245, v245, v23, v23\nM 96,160,32\nR 176,190,0\n"
"s_and_b32 s60, s40, 3\ns_lshl_b32 s60, s60, 7\nv_add3_u32 v238, v238, v239, s60\nv_cmp_ne_u32 s[86:87], s46, v244\nM 96,164,48\ns_add_u32 s64, s70, 10493952\n"
"buffer_load_dwordx4 a[128:131], v252, s[20:23], s64 offen offset:0 sc1\nv_cmp_ne_u32 s[88:89], s46, v245\nM 112,160,36\nR 178,190,4096\ns_lshl_b32 s61, s42, 13\ns_add_u32 s61, s61, 84541440\n"
"buffer_load_dwordx4 v[0:3], v238, s[20:23], s61 offen sc1\ns_or_b64 s[86:87], s[86:87], s[88:89]\ns_cbranch_scc1 Lq329\nLq330:\nM 112,164,52\ns_lshl_b32 s61, s42, 13\n"
"s_add_u32 s61, s61, 84545536\nbuffer_load_dwordx4 v[4:7], v238, s[20:23], s61 offen sc1\nv_add_f32 v16, v16, v20\nM 128,160,40\nR 180,190,8192\nv_add_f32 v18, v18, v22\n"
"v_add_f32_dpp v16, v16, v16 row_ror:8 row_mask:0xf bank_mask:0xf\nM 128,164,56\ns_add_u32 m0, s50, 114688\nv_bfe_u32 v239, v238, 2, 7\nv_add_f32_dpp v18, v18, v18 row_ror:8 row_mask:0xf bank_mask:0xf\nv_mov_b32 v20, v16\n"
"M 144,160,44\nR 182,190,12288\ns_add_u32 s63, s54, 1376256\nv_mov_b32 v21, v18\nM 144,164,60\nbuffer_load_dword v239, v239, s[32:35], s54 offen offset:256\n"
"s_barrier\nG 4\nv_permlane32_swap_b32 v20, v16\nM 148,168,44\nR 160,191,0\nv_permlane32_swap_b32 v21, v18\n"
"v_add_f32 v16, v16, v20\nM 148,172,60\ns_add_u32 s65, s70, 10526720\nbuffer_load_dwordx4 a[132:135], v252, s[20:23], s65 offen offset:0 sc1\nv_add_f32 v18, v18, v21\nM 132,168,40\n"
"R 162,191,4096\nv_mov_b32 v20, v16\nv_mov_b32 v21, v18\nM 132,172,56\nbuffer_load_dwordx4 v220, s[12:15], s63 offen lds\nM 116,168,36\n"
"R 164,191,8192\nv_permlane16_swap_b32 v20, v16\nM 116,172,52\ns_add_u32 s66, s71, 10493952\nbuffer_load_dwordx4 a[136:139], v252, s[20:23], s66 offen offset:0 sc1\nM 100,168,32\n"
"R 166,191,12288\nv_permlane16_swap_b32 v21, v18\nM 100,172,48\nG 4\nv_add_f32 v16, v16, v20\nv_add_f32 v18, v18, v21\n"
"M 104,176,32\nR 168,188,16384\nbuffer_load_dwordx4 v221, s[12:15], s63 offen offset:1024 lds\nM 104,180,48\ns_add_u32 m0, s50, 0\ns_add_u32 s63, s54, 1605632\n"
"buffer_load_dwordx4 v220, s[12:15], s63 offen lds\nM 120,176,36\nR 170,188,20480\nv_and_b32 v24, s77, v16\nM 120,180,52\ns_add_u32 s67, s71, 10526720\n"
"buffer_load_dwordx4 a[140:143], v252, s[20:23], s67 offen offset:0 sc1\nM 136,176,40\nR 172,188,24576\nv_sub_f32 v25, v16, v24\nM 136,180,56\nbuffer_load_dwordx4 v221, s[12:15], s63 offen offset:1024 lds\n"
"M 152,176,44\nR 174,188,28672\ns_waitcnt vmcnt(17)\nv_lshlrev_b32 v26, 16, v243\nM 152,180,60\nG 4\n"
"v_cvt_pk_bf16_f32 v20, v24, v25\nv_cvt_pk_bf16_f32 v21, v24, v26\nM 156,160,44\nR 176,189,16384\nv_and_b32 v24, s77, v18\nM 156,164,60\n"
"buffer_load_dwordx4 a[144:147], v252, s[20:23], s64 offen offset:1024 sc1\nM 140,160,40\nR 178,189,20480\nv_sub_f32 v25, v18, v24\nM 140,164,56\nv_and_b32 v26, s77, v243\n"
"v_cvt_pk_bf16_f32 v22, v24, v25\nM 124,160,36\nR 180,189,24576\nv_cvt_pk_bf16_f32 v23, v24, v26\nM 124,164,52\nv_and_b32 v242, 0x1ff, v242\n"
"v_add_u32 v242, 157696, v242\nM 108,160,32\nR 182,189,28672\nds_write_b128 v242, v[20:23]\nM 108,164,48\nG 5\n"
"M 32,168,64\nR 160,190,16384\nM 32,172,80\nbuffer_load_dwordx4 a[148:151], v252, s[20:23], s65 offen offset:1024 sc1\nM 48,168,68\nLpg8:\n"
"s_cbranch_execz Lpg12\nR 162,190,20480\nM 48,172,84\ns_add_u32 m0, s50, 8192\nM 64,168,72\nR 164,190,24576\n"
"s_add_u32 s63, s54, 1376384\nM 64,172,88\nbuffer_load_dwordx4 a[152:155], v252, s[20:23], s66 offen offset:1024 sc1\nM 80,168,76\nR 166,190,28672\nM 80,172,92\n"
"G 4\ns_barrier\nM 84,176,76\nR 168,191,16384\nv_lshrrev_b32 v242, 7, v252\nM 84,180,92\n"
"buffer_load_dwordx4 v220, s[12:15], s63 offen lds\nv_lshlrev_b32 v242, 9, v242\nM 68,176,72\nR 170,191,20480\ns_waitcnt vmcnt(12)\nv_min3_u32 v240, v1, v3, v5\n"
"v_min3_u32 v240, v240, v7, v7\nM 68,180,88\nbuffer_load_dwordx4 a[156:159], v252, s[20:23], s67 offen offset:1024 sc1\nv_and_b32 v243, 0x70, v252\nv_max3_u32 v241, v1, v3, v5\nM 52,176,68\n"
"R 172,191,24576\nv_max3_u32 v241, v241, v7, v7\nv_cmp_ne_u32 s[86:87], s46, v240\nM 52,180,84\ns_and_b32 s60, s40, 1\ns_lshl_b32 s60, s60, 7\n"
"v_add3_u32 v242, v242, v243, s60\nv_cmp_ne_u32 s[88:89], s46, v241\nM 36,176,64\nR 174,191,28672\ns_lshl_b32 s61, s42, 13\ns_add_u32 s61, s61, 84672512\n"
"buffer_load_dwordx4 v[16:19], v242, s[20:23], s61 offen sc1\ns_or_b64 s[86:87], s[86:87], s[88:89]\ns_cbranch_scc1 Lq339\nLq340:\nM 36,180,80\ns_lshl_b32 s61, s42, 13\n"
"s_add_u32 s61, s61, 84676608\nbuffer_load_dwordx4 v[20:23], v242, s[20:23], s61 offen sc1\nG 4\nv_add_f32 v0, v0, v4\nM 40,160,64\nR 176,188,32768\n"
"buffer_load_dwordx4 v221, s[12:15], s63 offen offset:1024 lds\nv_add_f32 v2, v2, v6\nM 40,164,80\nbuffer_load_dwordx4 a[160:163], v252, s[20:23], s64 offen offset:2048 sc1\ns_add_u32 s63, s54, 1605760\nv_bfe_u32 v243, v242, 2, 7\n"
"v_add_f32_dpp v0, v0, v0 row_ror:8 row_mask:0xf bank_mask:0xf\nM 56,160,68\nR 178,188,36864\ns_add_u32 m0, s50, 16384\nv_add_f32_dpp v2, v2, v2 row_ror:8 row_mask:0xf bank_mask:0xf\nM 56,164,84\n"
"buffer_load_dword v243, v243, s[32:35], s55 offen offset:0\nv_mov_b32 v4, v0\nM 72,160,72\nR 180,188,40960\nbuffer_load_dwordx4 v220, s[12:15], s63 offen lds\nv_mov_b32 v5, v2\n"
"M 72,164,88\nbuffer_load_dwordx4 v221, s[12:15], s63 offen offset:1024 lds\nv_permlane32_swap_b32 v4, v0\nM 88,160,76\nR 182,188,45056\nv_permlane32_swap_b32 v5, v2\n"
"v_add_f32 v0, v0, v4\nM 88,164,92\nG 4\nv_add_f32 v2, v2, v5\nv_mov_b32 v4, v0\nv_mov_b32 v5, v2\n"
"M 92,168,76\nR 160,189,32768\nv_permlane16_swap_b32 v4, v0\nv_permlane16_swap_b32 v5, v2\nM 92,172,92\nbuffer_load_dwordx4 a[164:167], v252, s[20:23], s65 offen offset:2048 sc1\n"
"v_add_f32 v0, v0, v4\nM 76,168,72\nR 162,189,36864\nv_add_f32 v2, v2, v5\nv_and_b32 v8, s77, v0\nM 76,172,88\n"
"v_sub_f32 v9, v0, v8\ns_waitcnt vmcnt(20)\nv_lshlrev_b32 v10, 16, v239\nM 60,168,68\nR 164,189,40960\nv_cvt_pk_bf16_f32 v4, v8, v9\n"
"M 60,172,84\nbuffer_load_dwordx4 a[168:171], v252, s[20:23], s66 offen offset:2048 sc1\nM 44,168,64\nR 166,189,45056\nv_cvt_pk_bf16_f32 v5, v8, v10\nM 44,172,80\n"
"G 4\nv_and_b32 v8, s77, v2\nv_sub_f32 v9, v2, v8\nM 96,176,64\nR 168,190,32768\nv_and_b32 v10, s77, v239\n"
"M 96,180,80\nv_cvt_pk_bf16_f32 v6, v8, v9\nv_cvt_pk_bf16_f32 v7, v8, v10\nM 112,176,68\nR 170,190,36864\nv_and_b32 v238, 0x1ff, v238\n"
"M 112,180,84\nbuffer_load_dwordx4 a[172:175], v252, s[20:23], s67 offen offset:2048 sc1\nM 128,176,72\nR 172,190,40960\nv_add_u32 v238, 158208, v238\nM 128,180,88\n"
"s_add_u32 m0, s50, 24576\nds_write_b128 v238, v[4:7]\nM 144,176,76\nR 174,190,45056\ns_add_u32 s63, s54, 1835008\nM 144,180,92\n"
"s_waitcnt vmcnt(15) lgkmcnt(1)\ns_barrier\nM 148,160,76\nR 176,191,32768\nM 148,164,92\nbuffer_load_dwordx4 a[176:179], v252, s[20:23], s64 offen offset:3072 sc1\n"
"M 132,160,72\nR 178,191,36864\nM 132,164,88\nbuffer_load_dwordx4 v220, s[12:15], s63 offen lds\nM 116,160,68\nR 180,191,40960\n"
"M 116,164,84\nbuffer_load_dwordx4 v221, s[12:15], s63 offen offset:1024 lds\nM 100,160,64\nR 182,191,45056\ns_add_u32 m0, s50, 32768\nM 100,164,80\n"
"G 4\nM 104,168,64\nR 160,188,49152\ns_add_u32 s63, s54, 2064384\nM 104,172,80\nbuffer_load_dwordx4 a[180:183], v252, s[20:23], s65 offen offset:3072 sc1\n"
"M 120,168,68\nR 162,188,53248\nM 120,172,84\nbuffer_load_dwordx4 v220, s[12:15], s63 offen lds\nM 136,168,72\nR 164,184,0\n"
"M 136,172,88\nbuffer_load_dwordx4 a[184:187], v252, s[20:23], s66 offen offset:3072 sc1\nM 152,168,76\nR 166,184,4096\ns_waitcnt vmcnt(14)\nv_min3_u32 v244, v17, v19, v21\n"
"v_min3_u32 v244, v244, v23, v23\nM 152,172,92\nG 4\nv_max3_u32 v245, v17, v19, v21\nv_max3_u32 v245, v245, v23, v23\nv_cmp_ne_u32 s[86:87], s46, v244\n"
"M 156,176,76\nR 168,189,49152\nbuffer_load_dwordx4 v221, s[12:15], s63 offen offset:1024 lds\nv_cmp_ne_u32 s[88:89], s46, v245\nM 156,180,92\ns_or_b64 s[86:87], s[86:87], s[88:89]\n"
"s_cbranch_scc1 Lq349\nLq350:\nv_add_f32 v16, v16, v20\nv_add_f32 v18, v18, v22\nM 140,176,72\nR 170,189,53248\n"
"v_add_f32_dpp v16, v16, v16 row_ror:8 row_mask:0xf bank_mask:0xf\nv_add_f32_dpp v18, v18, v18 row_ror:8 row_mask:0xf bank_mask:0xf\nM 140,180,88\nbuffer_load_dwordx4 a[188:191], v252, s[20:23], s67 offen offset:3072 sc1\nv_mov_b32 v20, v16\nM 124,176,68\n"
"R 172,185,0\nv_mov_b32 v21, v18\nM 124,180,84\nv_permlane32_swap_b32 v20, v16\nv_permlane32_swap_b32 v21, v18\nM 108,176,64\n"
"R 174,185,4096\nv_add_f32 v16, v16, v20\nM 108,180,80\nG 4\nv_add_f32 v18, v18, v21\nv_mov_b32 v20, v16\n"
"M 32,160,96\nR 176,190,49152\nv_mov_b32 v21, v18\nM 32,164,112\ns_add_u32 s64, s70, 10498048\nbuffer_load_dwordx4 a[192:195], v252, s[20:23], s64 offen offset:0 sc1\n"
"M 48,160,100\nR 178,190,53248\nv_permlane16_swap_b32 v20, v16\nM 48,164,116\nv_permlane16_swap_b32 v21, v18\nv_add_f32 v16, v16, v20\n"
"M 64,160,104\nR 180,186,0\nv_add_f32 v18, v18, v21\nM 64,164,120\ns_add_u32 m0, s50, 40960\nv_and_b32 v24, s77, v16\n"
"v_sub_f32 v25, v16, v24\nM 80,160,108\nR 182,186,4096\ns_add_u32 s63, s54, 2293760\nM 80,164,124\ns_waitcnt vmcnt(12)\n"
"s_barrier\nG 4\nM 84,168,108\nR 160,191,49152\nv_lshlrev_b32 v26, 16, v243\nM 84,172,124\n"
"s_add_u32 s65, s70, 10530816\nbuffer_load_dwordx4 a[196:199], v252, s[20:23], s65 offen offset:0 sc1\nM 68,168,104\nR 162,191,53248\nv_cvt_pk_bf16_f32 v20, v24, v25\nM 68,172,120\n"
"buffer_load_dwordx4 v220, s[12:15], s63 offen lds\nM 52,168,100\nR 164,187,0\nv_cvt_pk_bf16_f32 v21, v24, v26\nM 52,172,116\ns_add_u32 s66, s71, 10498048\n"
"buffer_load_dwordx4 a[200:203], v252, s[20:23], s66 offen offset:0 sc1\nM 36,168,96\nR 166,187,4096\nv_and_b32 v24, s77, v18\nM 36,172,112\nG 4\n"
"v_sub_f32 v25, v18, v24\nv_and_b32 v26, s77, v243\nM 40,176,96\nR 168,184,8192\nbuffer_load_dwordx4 v221, s[12:15], s63 offen offset:1024 lds\nM 40,180,112\n"
"s_add_u32 m0, s50, 49152\ns_add_u32 s63, s54, 2523136\nbuffer_load_dwordx4 v220, s[12:15], s63 offen lds\nM 56,176,100\nR 170,184,12288\nv_cvt_pk_bf16_f32 v22, v24, v25\n"
"M 56,180,116\ns_add_u32 s67, s71, 10530816\nbuffer_load_dwordx4 a[204:207], v252, s[20:23], s67 offen offset:0 sc1\nM 72,176,104\nR 172,184,16384\nv_cvt_pk_bf16_f32 v23, v24, v26\n"
"M 72,180,120\nbuffer_load_dwordx4 v221, s[12:15], s63 offen offset:1024 lds\nM 88,176,108\nR 174,184,20480\nv_and_b32 v242, 0x1ff, v242\nM 88,180,124\n"
"v_add_u32 v242, 158720, v242\nds_write_b128 v242, v[20:23]\nG 5\nM 92,160,108\nR 176,185,8192\nM 92,164,124\n"
"buffer_load_dwordx4 a[208:211], v252, s[20:23], s64 offen offset:1024 sc1\nM 76,160,104\nR 178,185,12288\nM 76,164,120\ns_lshr_b32 s84, s49, 4\nM 60,160,100\n"
"R 180,185,16384\nLq573:\ns_lshr_b32 s60, s45, 3\ns_lshl2_add_u32 s60, s60, s40\ns_lshl_b32 s60, s60, 8\nv_lshl_add_u32 v224, v252, 3, s60\n"
"v_add_u32 v224, Lq573+4096-LqPC, v224\nv_min_u32 v224, LqE-LqPC-4, v224\ns_mov_b64 exec, 3\nglobal_load_dword v224, v224, s[98:99]\ns_mov_b64 exec, -1\nM 60,164,116\n"
"s_add_u32 s84, s84, 82313216\nM 44,160,96\nR 182,185,20480\nM 44,164,112\nG 5\nM 96,168,96\n"
"R 160,186,8192\nM 96,172,112\nbuffer_load_dwordx4 a[212:215], v252, s[20:23], s65 offen offset:1024 sc1\nM 112,168,100\nR 162,186,12288\nM 112,172,116\n"
"buffer_load_dwordx4 v[0:3], v252, s[20:23], s84 offen offset:0 sc1\nM 128,168,104\nR 164,186,16384\nM 128,172,120\nbuffer_load_dwordx4 a[216:219], v252, s[20:23], s66 offen offset:1024 sc1\nM 144,168,108\n"
"R 166,186,20480\ns_add_u32 m0, s50, 57344\nM 144,172,124\nbuffer_load_dwordx4 v[4:7], v252, s[20:23], s84 offen offset:1024 sc1\ns_waitcnt vmcnt(15) lgkmcnt(8)\ns_barrier\n"
"G 4\nM 148,176,108\nR 168,187,8192\ns_add_u32 s63, s54, 2752512\nM 148,180,124\nbuffer_load_dwordx4 v[8:11], v252, s[20:23], s84 offen offset:2048 sc1\n"
"M 132,176,104\nR 170,187,12288\nM 132,180,120\nbuffer_load_dwordx4 a[220:223], v252, s[20:23], s67 offen offset:1024 sc1\nM 116,176,100\nR 172,187,16384\n"
"buffer_load_dwordx4 v220, s[12:15], s63 offen lds\nM 116,180,116\nbuffer_load_dwordx4 v[12:15], v252, s[20:23], s84 offen offset:3072 sc1\nM 100,176,96\nR 174,187,20480\nM 100,180,112\n"
"G 4\nM 104,160,96\nR 176,184,24576\nbuffer_load_dwordx4 v221, s[12:15], s63 offen offset:1024 lds\nM 104,164,112\nbuffer_load_dwordx4 a[224:227], v252, s[20:23], s64 offen offset:2048 sc1\n"
"s_add_u32 s63, s54, 2981888\nM 120,160,100\nR 178,184,28672\ns_add_u32 m0, s50, 65536\nM 120,164,116\nbuffer_load_dwordx4 v220, s[12:15], s63 offen lds\n"
"M 136,160,104\nR 180,184,32768\nM 136,164,120\nbuffer_load_dwordx4 v221, s[12:15], s63 offen offset:1024 lds\nM 152,160,108\nR 182,184,36864\n"
"M 152,164,124\nG 4\nM 156,168,108\nR 160,185,24576\nM 156,172,124\nbuffer_load_dwordx4 a[228:231], v252, s[20:23], s65 offen offset:2048 sc1\n"
"M 140,168,104\nR 162,185,28672\nM 140,172,120\nM 124,168,100\nR 164,185,32768\nM 124,172,116\n"
"buffer_load_dwordx4 a[232:235], v252, s[20:23], s66 offen offset:2048 sc1\nM 108,168,96\nR 166,185,36864\nM 108,172,112\nG 4\nM 32,176,128\n"
"R 168,186,24576\nM 32,180,144\nM 48,176,132\nR 170,186,28672\nM 48,180,148\nbuffer_load_dwordx4 a[236:239], v252, s[20:23], s67 offen offset:2048 sc1\n"
"M 64,176,136\nR 172,186,32768\nM 64,180,152\ns_add_u32 m0, s50, 73728\nM 80,176,140\nR 174,186,36864\n"
"s_add_u32 s63, s54, 3211264\nM 80,180,156\ns_waitcnt vmcnt(17)\ns_barrier\nG 4\nM 84,160,140\n"
"R 176,187,24576\nM 84,164,156\nbuffer_load_dwordx4 a[240:243], v252, s[20:23], s64 offen offset:3072 sc1\nM 68,160,136\nR 178,187,28672\nM 68,164,152\n"
"buffer_load_dwordx4 v220, s[12:15], s63 offen lds\nM 52,160,132\nR 180,187,32768\nM 52,164,148\nbuffer_load_dwordx4 v221, s[12:15], s63 offen offset:1024 lds\nM 36,160,128\n"
"R 182,187,36864\ns_add_u32 m0, s50, 81920\nM 36,164,144\nG 4\nM 40,168,128\nR 160,184,40960\n"
"s_add_u32 s63, s54, 3440640\nM 40,172,144\nbuffer_load_dwordx4 a[244:247], v252, s[20:23], s65 offen offset:3072 sc1\nM 56,168,132\nR 162,184,45056\nM 56,172,148\n"
"buffer_load_dwordx4 v220, s[12:15], s63 offen lds\nM 72,168,136\nR 164,184,49152\nM 72,172,152\nbuffer_load_dwordx4 a[248:251], v252, s[20:23], s66 offen offset:3072 sc1\nM 88,168,140\n"
"R 166,184,53248\ns_mov_b32 s85, 0x0c0c0400\ns_waitcnt vmcnt(19)\nv_perm_b32 v16, v1, v0, s85\nM 88,172,156\nG 4\n"
"v_perm_b32 v20, v3, v2, s85\nv_lshl_or_b32 v16, v20, 16, v16\ns_waitcnt vmcnt(17)\nv_perm_b32 v17, v5, v4, s85\nM 92,176,140\nR 168,185,40960\n"
"buffer_load_dwordx4 v221, s[12:15], s63 offen offset:1024 lds\nv_perm_b32 v21, v7, v6, s85\nM 92,180,156\nv_lshl_or_b32 v17, v21, 16, v17\ns_waitcnt vmcnt(17)\nv_perm_b32 v18, v9, v8, s85\n"
"v_perm_b32 v22, v11, v10, s85\nM 76,176,136\nR 170,185,45056\nv_lshl_or_b32 v18, v22, 16, v18\ns_waitcnt vmcnt(14)\nv_perm_b32 v19, v13, v12, s85\n"
"M 76,180,152\nbuffer_load_dwordx4 a[252:255], v252, s[20:23], s67 offen offset:3072 sc1\nv_perm_b32 v23, v15, v14, s85\nM 60,176,132\nR 172,185,49152\nv_lshl_or_b32 v19, v23, 16, v19\n"
"v_min3_u32 v230, v16, v17, v18\nM 60,180,148\nv_min3_u32 v230, v230, v19, v19\nv_max3_u32 v231, v16, v17, v18\nv_max3_u32 v231, v231, v19, v19\nM 44,176,128\n"
"R 174,185,53248\nv_cmp_ne_u32 s[86:87], s46, v230\nv_cmp_ne_u32 s[88:89], s46, v231\nM 44,180,144\ns_or_b64 s[86:87], s[86:87], s[88:89]\ns_cbranch_scc1 Lq317\n"
"Lq318:\nG 4\nv_and_b32 v0, 0xffffff00, v0\nM 32,160,160\nR 176,186,40960\nv_and_b32 v1, 0xffffff00, v1\n"
"v_and_b32 v2, 0xffffff00, v2\nM 32,164,176\nv_and_b32 v3, 0xffffff00, v3\nv_and_b32 v4, 0xffffff00, v4\nv_and_b32 v5, 0xffffff00, v5\nM 48,160,164\n"
"R 178,186,45056\nv_and_b32 v6, 0xffffff00, v6\nv_and_b32 v7, 0xffffff00, v7\nM 48,164,180\ns_add_u32 m0, s50, 90112\nv_and_b32 v8, 0xffffff00, v8\n"
"v_and_b32 v9, 0xffffff00, v9\nv_and_b32 v10, 0xffffff00, v10\nM 64,160,168\nR 180,186,49152\nv_and_b32 v11, 0xffffff00, v11\nv_and_b32 v12, 0xffffff00, v12\n"
"M 64,164,184\ns_add_u32 s63, s54, 1835136\nv_and_b32 v13, 0xffffff00, v13\nv_and_b32 v14, 0xffffff00, v14\nv_and_b32 v15, 0xffffff00, v15\nM 80,160,172\n"
"R 182,186,53248\nv_add_f32 v0, v0, v8\nv_add_f32 v1, v1, v9\nM 80,164,188\ns_waitcnt vmcnt(11)\ns_barrier\n"
"G 4\nv_add_f32 v2, v2, v10\nM 84,168,172\nR 160,187,40960\nv_add_f32 v3, v3, v11\nv_add_f32 v4, v4, v12\n"
"M 84,172,188\nbuffer_load_dwordx4 v220, s[12:15], s63 offen lds\nv_add_f32 v5, v5, v13\nM 68,168,168\nR 162,187,45056\nv_add_f32 v6, v6, v14\n"
"v_add_f32 v7, v7, v15\nM 68,172,184\nv_add_f32 v0, v0, v4\nv_add_f32 v1, v1, v5\nv_add_f32 v2, v2, v6\nM 52,168,164\n"
"R 164,187,49152\nv_bfe_u32 v250, v252, 5, 1\nv_add_f32 v3, v3, v7\nv_mul_u32_u24 v250, 2560, v250\nM 52,172,180\nbuffer_load_dwordx4 v221, s[12:15], s63 offen offset:1024 lds\n"
"v_permlane32_swap_b32 v0, v2\nv_and_b32 v251, 16, v252\nM 36,168,160\nR 166,187,53248\ns_add_u32 m0, s50, 98304\nv_permlane32_swap_b32 v1, v3\n"
"v_lshl_add_u32 v250, v251, 1, v250\nM 36,172,176\ns_lshl_b32 s60, s90, 6\nv_add_u32 v250, s60, v250\nG 4\nv_add_f32 v0, v0, v2\n"
"M 40,176,160\nR 168,184,57344\ns_add_u32 s63, s54, 2064512\nv_add_f32 v1, v1, v3\nM 40,180,176\nbuffer_load_dwordx4 v220, s[12:15], s63 offen lds\n"
"v_permlane16_swap_b32 v0, v1\nM 56,176,164\nR 170,184,61440\nv_add_f32 v230, v0, v1\nv_mov_b32 v232, v230\nM 56,180,180\n"
"s_nop 0\nv_permlane16_swap_b32 v230, v232\nv_mov_b32 v231, v230\nv_mov_b32 v233, v232\nM 72,176,168\nR 172,188,0\n"
"v_permlane32_swap_b32 v230, v231\nv_permlane32_swap_b32 v232, v233\nM 72,180,184\nbuffer_load_dwordx4 v221, s[12:15], s63 offen offset:1024 lds\nv_mul_f32 v230, 0xba800000, v230\nM 88,176,172\n"
"R 174,188,4096\ns_add_u32 m0, s50, 106496\nM 88,180,188\nG 4\nv_mul_f32 v214, v230, v230\nv_mul_f32 v231, 0x3a800000, v231\n"
"M 92,160,172\nR 176,185,57344\ns_add_u32 s63, s54, 2293888\nM 92,164,188\nbuffer_load_dwordx4 v220, s[12:15], s63 offen lds\nM 76,160,168\n"
"R 178,185,61440\nv_sub_f32 v231, v231, v214\nM 76,164,184\nv_max_f32 v231, 0, v231\nv_add_f32 v231, 0x3727c5ac, v231\nLpg9:\n"
"s_cbranch_execz Lpg13\nM 60,160,164\nR 180,189,0\nv_and_b32 v208, s77, v230\nM 60,164,180\nv_rsq_f32 v234, v231\n"
"M 44,160,160\nR 182,189,4096\nv_mul_f32 v216, v231, v234\nM 44,164,176\nG 4\nv_sub_f32 v230, v230, v208\n"
"v_cvt_pk_bf16_f32 v209, v230, v216\nM 32,168,192\nR 160,186,57344\nv_cvt_pk_bf16_f32 v208, v208, v208\nM 32,172,208\nbuffer_load_dwordx4 v221, s[12:15], s63 offen offset:1024 lds\n"
"M 48,168,196\nR 162,186,61440\ns_add_u32 s63, s54, 2523264\nM 48,172,212\ns_add_u32 m0, s50, 114688\ns_mov_b64 s[60:61], 0xffff\n"
"v_cndmask_b32 v208, 0, v208, s[60:61]\nv_mul_f32 v214, 0xc0135761, v234\nM 64,168,200\nR 164,190,0\nv_mul_f32 v215, v234, v234\nM 64,172,216\n"
"buffer_load_dwordx4 v220, s[12:15], s63 offen lds\nM 80,168,204\nR 166,190,4096\nv_mul_f32 v215, v215, v214\nM 80,172,220\ns_waitcnt vmcnt(8)\n"
"s_barrier\nG 4\nM 84,176,204\nR 168,187,57344\nv_mul_f32 v215, 0x3d372713, v215\nM 84,180,220\n"
"buffer_load_dwordx4 v221, s[12:15], s63 offen offset:1024 lds\nM 68,176,200\nR 170,187,61440\ns_add_u32 s63, s54, 2752640\nM 68,180,216\ns_add_u32 m0, s50, 0\n"
"s_mov_b64 s[60:61], 0xffff\nv_cndmask_b32 v209, 0, v209, s[60:61]\nv_mul_f32 v232, 0xba800000, v232\nM 52,176,196\nR 172,191,0\nv_mul_f32 v217, v232, v232\n"
"M 52,180,212\nbuffer_load_dwordx4 v220, s[12:15], s63 offen lds\nM 36,176,192\nR 174,191,4096\nv_mul_f32 v233, 0x3a800000, v233\nM 36,180,208\n"
"G 4\nv_sub_f32 v233, v233, v217\nv_max_f32 v233, 0, v233\nM 40,160,192\nR 176,188,8192\nv_add_f32 v233, 0x3727c5ac, v233\n"
"M 40,164,208\nbuffer_load_dwordx4 v221, s[12:15], s63 offen offset:1024 lds\nM 56,160,196\nR 178,188,12288\nv_and_b32 v212, s77, v232\nM 56,164,212\n"
"s_add_u32 m0, s50, 8192\nv_rsq_f32 v235, v233\nM 72,160,200\nR 180,188,16384\nv_mul_f32 v219, v233, v235\nM 72,164,216\n"
"s_add_u32 s63, s54, 2982016\nv_sub_f32 v232, v232, v212\nv_cvt_pk_bf16_f32 v213, v232, v219\nM 88,160,204\nR 182,188,20480\nv_cvt_pk_bf16_f32 v212, v212, v212\n"
"M 88,164,220\ns_mov_b64 s[60:61], 0xffff\nv_cndmask_b32 v212, 0, v212, s[60:61]\nG 4\nv_mul_f32 v217, 0xc0135761, v235\nM 92,168,204\n"
"R 160,189,8192\nv_mul_f32 v218, v235, v235\nM 92,172,220\nbuffer_load_dwordx4 v220, s[12:15], s63 offen lds\nM 76,168,200\nR 162,189,12288\n"
"v_mul_f32 v218, v218, v217\nM 76,172,216\ns_mov_b64 s[60:61], 0xffff\nv_cndmask_b32 v213, 0, v213, s[60:61]\nv_mul_f32 v218, 0x3d372713, v218\nM 60,168,196\n"
"R 164,189,16384\nM 60,172,212\nbuffer_load_dwordx4 v221, s[12:15], s63 offen offset:1024 lds\nM 44,168,192\nR 166,189,20480\ns_mov_b32 s60, 157184\n"
"v_bfe_u32 v246, v252, 4, 4\nv_lshl_add_u32 v246, v246, 3, s60\nM 44,172,208\nds_read_b64 v[238:239], v246 offset:0\nG 5\nM 32,176,224\n"
"R 168,190,8192\nds_read_b64 v[240:241], v246 offset:128\nM 32,180,240\nds_read_b64 v[242:243], v246 offset:256\ns_add_u32 m0, s50, 16384\nM 48,176,228\n"
"R 170,190,12288\nds_read_b64 v[244:245], v246 offset:384\nM 48,180,244\nds_read_b64 v[246:247], v246 offset:0\ns_add_u32 s63, s54, 3211392\nM 64,176,232\n"
"R 172,190,16384\nM 64,180,248\nbuffer_load_dwordx4 v220, s[12:15], s63 offen lds\nM 80,176,236\nR 174,190,20480\ns_waitcnt vmcnt(13)\n"
"M 80,180,252\ns_waitcnt vmcnt(9) lgkmcnt(2)\ns_barrier\nM 84,160,236\nR 176,191,8192\nM 84,164,252\n"
"buffer_load_dwordx4 v221, s[12:15], s63 offen offset:1024 lds\nM 68,160,232\nR 178,191,12288\ns_add_u32 s63, s54, 3440768\nM 68,164,248\ns_add_u32 m0, s50, 24576\n"
"M 52,160,228\nR 180,191,16384\nv_mfma_f32_16x16x32_bf16 v[32:35], v[238:241], v[208:211], v[32:35]\nM 52,164,244\nbuffer_load_dwordx4 v220, s[12:15], s63 offen lds\nM 36,160,224\n"
"R 182,191,20480\nv_mfma_f32_16x16x32_bf16 v[36:39], v[240:243], v[208:211], v[36:39]\nM 36,164,240\nG 4\nM 40,168,224\nR 160,188,24576\n"
"v_mfma_f32_16x16x32_bf16 v[40:43], v[242:245], v[208:211], v[40:43]\nM 40,172,240\nbuffer_load_dwordx4 v221, s[12:15], s63 offen offset:1024 lds\nM 56,168,228\nR 162,188,28672\nv_mfma_f32_16x16x32_bf16 v[44:47], v[244:247], v[208:211], v[44:47]\n"
"s_add_u32 m0, s50, 32768\ns_add_u32 s63, s55, 0\nM 56,172,244\nbuffer_load_dwordx4 v222, s[12:15], s63 offen lds\nM 72,168,232\nR 164,188,32768\n"
"v_mfma_f32_16x16x32_bf16 v[48:51], v[244:247], v[210:213], v[48:51]\nM 72,172,248\nM 88,168,236\nR 166,188,36864\nv_mfma_f32_16x16x32_bf16 v[52:55], v[238:241], v[210:213], v[52:55]\nM 88,172,252\n"
"G 4\nM 92,176,236\nR 168,189,24576\nv_mfma_f32_16x16x32_bf16 v[56:59], v[240:243], v[210:213], v[56:59]\nM 92,180,252\nbuffer_load_dwordx4 v223, s[12:15], s63 offen offset:1024 lds\n"
"M 76,176,232\nR 170,189,28672\nv_mfma_f32_16x16x32_bf16 v[60:63], v[242:245], v[210:213], v[60:63]\nM 76,180,248\nM 60,176,228\nR 172,189,32768\n"
"M 60,180,244\nM 44,176,224\nR 174,189,36864\nM 44,180,240\ns_waitcnt vmcnt(10)\ns_barrier\n"
"G 4\nM 96,160,128\nR 176,190,24576\nM 96,164,144\ns_mov_b32 m0, s51\nM 112,160,132\n"
"R 178,190,28672\ns_lshl2_add_u32 s60, s45, s40\nv_lshlrev_b32 v224, 3, v252\ns_mul_i32 s60, s60, 7168\ns_bfm_b64 exec, 14, 0\nbuffer_load_dword v224, v224, s[16:19], s60 offen\n"
"s_mov_b64 exec, -1\nM 112,164,148\nds_write_addtid_b32 v64 offset:0\nM 128,160,136\nR 180,190,32768\nM 128,164,152\n"
"ds_write_addtid_b32 v65 offset:256\nM 144,160,140\nR 182,190,36864\nds_write_addtid_b32 v66 offset:512\nM 144,164,156\nds_write_addtid_b32 v67 offset:768\n"
"ds_write_addtid_b32 v68 offset:1024\nG 9\nM 148,168,140\nR 160,191,24576\nds_write_addtid_b32 v69 offset:1280\nM 148,172,156\n"
"ds_write_addtid_b32 v70 offset:1536\nds_write_addtid_b32 v71 offset:1792\nM 132,168,136\nR 162,191,28672\nds_write_addtid_b32 v72 offset:2048\nM 132,172,152\n"
"ds_write_addtid_b32 v73 offset:2304\nds_write_addtid_b32 v74 offset:2560\nM 116,168,132\nR 164,191,32768\nds_write_addtid_b32 v75 offset:2816\nM 116,172,148\n"
"ds_write_addtid_b32 v76 offset:3072\nds_write_addtid_b32 v77 offset:3328\nM 100,168,128\nR 166,191,36864\nds_write_addtid_b32 v78 offset:3584\nM 100,172,144\n"
"ds_write_addtid_b32 v79 offset:3840\nds_write_addtid_b32 v80 offset:4096\nG 15\nM 104,176,128\nR 168,188,40960\nds_write_addtid_b32 v81 offset:4352\n"
"M 104,180,144\nds_write_addtid_b32 v82 offset:4608\nds_write_addtid_b32 v83 offset:4864\nM 120,176,132\nR 170,188,45056\nds_write_addtid_b32 v84 offset:5120\n"
"M 120,180,148\nds_write_addtid_b32 v85 offset:5376\nds_write_addtid_b32 v86 offset:5632\nM 136,176,136\nR 172,188,49152\nds_write_addtid_b32 v87 offset:5888\n"
"M 136,180,152\nds_write_addtid_b32 v88 offset:6144\nds_write_addtid_b32 v89 offset:6400\nM 152,176,140\nR 174,188,53248\nds_write_addtid_b32 v90 offset:6656\n"
"M 152,180,156\nds_write_addtid_b32 v91 offset:6912\nds_write_addtid_b32 v92 offset:7168\nG 15\nM 156,160,140\nR 176,189,40960\n"
"ds_write_addtid_b32 v93 offset:7424\nM 156,164,156\nds_write_addtid_b32 v94 offset:7680\nds_write_addtid_b32 v95 offset:7936\nM 140,160,136\nR 178,189,45056\n"
"s_add_u32 m0, s50, 40960\ns_add_u32 s63, s55, 458752\nM 140,164,152\nbuffer_load_dwordx4 v222, s[12:15], s63 offen lds\nM 124,160,132\nR 180,189,49152\n"
"s_waitcnt vmcnt(8) lgkmcnt(2)\ns_barrier\nM 124,164,148\nds_read2st64_b32 v[64:65], v225 offset0:0 offset1:1\nds_read2st64_b32 v[66:67], v225 offset0:2 offset1:3\nM 108,160,128\n"
"R 182,189,53248\nds_read2st64_b32 v[68:69], v225 offset0:4 offset1:5\nM 108,164,144\nds_read2st64_b32 v[70:71], v225 offset0:6 offset1:7\nds_read2st64_b32 v[72:73], v225 offset0:8 offset1:9\nM 96,168,160\n"
"R 160,190,40960\nds_read2st64_b32 v[74:75], v225 offset0:10 offset1:11\nM 96,172,176\nbuffer_load_dwordx4 v223, s[12:15], s63 offen offset:1024 lds\nds_read2st64_b32 v[76:77], v225 offset0:12 offset1:13\nM 112,168,164\n"
"R 162,190,45056\nds_read2st64_b32 v[78:79], v225 offset0:14 offset1:15\nM 112,172,180\ns_add_u32 m0, s50, 49152\nds_read2st64_b32 v[80:81], v225 offset0:16 offset1:17\nds_read2st64_b32 v[82:83], v225 offset0:18 offset1:19\n"
"M 128,168,168\nR 164,190,49152\nds_read2st64_b32 v[84:85], v225 offset0:20 offset1:21\nG 14\nv_add_f32 v32, v32, v64\nM 128,172,184\n"
"s_add_u32 s63, s55, 917504\nds_read2st64_b32 v[86:87], v225 offset0:22 offset1:23\nds_read2st64_b32 v[88:89], v225 offset0:24 offset1:25\nM 144,168,172\nR 166,190,53248\nds_read2st64_b32 v[90:91], v225 offset0:26 offset1:27\n"
"v_add_f32 v33, v33, v65\nM 144,172,188\nds_read2st64_b32 v[92:93], v225 offset0:28 offset1:29\nds_read2st64_b32 v[94:95], v225 offset0:30 offset1:31\nG 15\nM 148,176,172\n"
"R 168,191,40960\nv_add_f32 v34, v34, v66\nv_add_f32 v35, v35, v67\nM 148,180,188\nbuffer_load_dwordx4 v222, s[12:15], s63 offen lds\nv_add_f32 v36, v36, v68\n"
"M 132,176,168\nR 170,191,45056\nv_add_f32 v37, v37, v69\nv_add_f32 v38, v38, v70\nM 132,180,184\nv_add_f32 v39, v39, v71\n"
"v_add_f32 v40, v40, v72\nM 116,176,164\nR 172,191,49152\nv_add_f32 v41, v41, v73\nM 116,180,180\nG 15\n"
"v_add_f32 v42, v42, v74\nv_add_f32 v43, v43, v75\nM 100,176,160\nR 174,191,53248\nv_add_f32 v44, v44, v76\nM 100,180,176\n"
"G 7\nv_add_f32 v45, v45, v77\nv_add_f32 v46, v46, v78\nM 104,160,160\nR 176,184,0\nv_add_f32 v47, v47, v79\n"
"M 104,164,176\nbuffer_load_dwordx4 v223, s[12:15], s63 offen offset:1024 lds\nM 120,160,164\nR 178,184,4096\nv_add_f32 v48, v48, v80\nM 120,164,180\n"
"s_add_u32 m0, s50, 57344\nv_add_f32 v49, v49, v81\nv_add_f32 v50, v50, v82\nM 136,160,168\nR 180,184,8192\nv_add_f32 v51, v51, v83\n"
"M 136,164,184\ns_add_u32 s63, s55, 1376256\nv_add_f32 v52, v52, v84\nv_add_f32 v53, v53, v85\nM 152,160,172\nR 182,184,12288\n"
"v_add_f32 v54, v54, v86\nM 152,164,188\nG 4\nv_add_f32 v55, v55, v87\nv_add_f32 v56, v56, v88\nM 156,168,172\n"
"R 160,185,0\nv_add_f32 v57, v57, v89\nM 156,172,188\nbuffer_load_dwordx4 v222, s[12:15], s63 offen lds\nM 140,168,168\nR 162,185,4096\n"
"v_add_f32 v58, v58, v90\nM 140,172,184\nv_add_f32 v59, v59, v91\nv_add_f32 v60, v60, v92\nM 124,168,164\nR 164,185,8192\n"
"v_add_f32 v61, v61, v93\nM 124,172,180\nv_add_f32 v62, v62, v94\nv_add_f32 v63, v63, v95\nM 108,168,160\nR 166,185,12288\n"
"v_mul_f32 v230, v32, v32\nv_mul_f32 v231, v33, v33\nM 108,172,176\ns_add_u32 s84, s73, 23068672\ns_add_u32 s85, s73, 23183360\nG 4\n"
"v_mul_f32 v232, v34, v34\nM 96,176,192\nR 168,186,0\nv_mul_f32 v233, v35, v35\nv_fma_f32 v230, v230, v215, v214\nM 96,180,208\n"
"v_fma_f32 v231, v231, v215, v214\nv_fma_f32 v232, v232, v215, v214\nv_fma_f32 v233, v233, v215, v214\nM 112,176,196\nR 170,186,4096\ns_lshl2_add_u32 s60, s45, s40\n"
"s_waitcnt vmcnt(5)\nv_lshlrev_b32 v224, 3, v252\ns_mul_i32 s60, s60, 7168\ns_bfm_b64 exec, 14, 14\nbuffer_load_dword v224, v224, s[16:19], s60 offen\ns_mov_b64 exec, -1\n"
"s_barrier\nv_mul_f32 v230, v32, v230\nM 112,180,212\nv_exp_f32 v230, v230\nv_mul_f32 v231, v33, v231\nM 128,176,200\n"
"R 172,186,8192\nv_mul_f32 v232, v34, v232\nv_mul_f32 v233, v35, v233\nM 128,180,216\nv_exp_f32 v231, v231\nv_exp_f32 v232, v232\n"
"M 144,176,204\nR 174,186,12288\nv_fma_f32 v230, v230, v216, v216\nv_exp_f32 v233, v233\nM 144,180,220\nG 4\n"
"v_rcp_f32 v230, v230\nv_fma_f32 v231, v231, v216, v216\nM 148,160,204\nR 176,187,0\nv_fma_f32 v232, v232, v216, v216\nv_fma_f32 v233, v233, v216, v216\n"
"M 148,164,220\nbuffer_load_dwordx4 v223, s[12:15], s63 offen offset:1024 lds\nv_rcp_f32 v231, v231\nM 132,160,200\nR 178,187,4096\nv_mul_f32 v230, v32, v230\n"
"v_rcp_f32 v232, v232\nM 132,164,216\ns_add_u32 m0, s50, 65536\nv_rcp_f32 v233, v233\nv_mul_f32 v231, v33, v231\nM 116,160,196\n"
"R 180,187,8192\nv_mul_f32 v232, v34, v232\nv_mul_f32 v233, v35, v233\nM 116,164,212\ns_add_u32 s63, s55, 1835008\nv_cvt_pk_bf16_f32 v32, v230, v231\n"
"v_cvt_pk_bf16_f32 v33, v232, v233\nv_mul_f32 v234, v36, v36\nM 100,160,192\nR 182,187,12288\nv_mul_f32 v235, v37, v37\nv_mul_f32 v236, v38, v38\n"
"M 100,164,208\nG 4\nv_mul_f32 v237, v39, v39\nv_fma_f32 v234, v234, v215, v214\nv_fma_f32 v235, v235, v215, v214\nM 104,168,192\n"
"R 160,184,16384\nv_fma_f32 v236, v236, v215, v214\nv_fma_f32 v237, v237, v215, v214\nM 104,172,208\nbuffer_load_dwordx4 v222, s[12:15], s63 offen lds\nv_mul_f32 v234, v36, v234\n"
"M 120,168,196\nR 162,184,20480\nv_mul_f32 v235, v37, v235\nv_mul_f32 v236, v38, v236\nM 120,172,212\nv_exp_f32 v234, v234\n"
"v_mul_f32 v237, v39, v237\nM 136,168,200\nR 164,184,24576\nv_fma_f32 v234, v234, v216, v216\nv_exp_f32 v235, v235\nM 136,172,216\n"
"v_exp_f32 v236, v236\nv_exp_f32 v237, v237\nM 152,168,204\nR 166,184,28672\nv_fma_f32 v235, v235, v216, v216\nv_fma_f32 v236, v236, v216, v216\n"
"M 152,172,220\nG 4\nv_rcp_f32 v234, v234\nv_fma_f32 v237, v237, v216, v216\nM 156,176,204\nR 168,185,16384\n"
"v_mul_f32 v234, v36, v234\nv_rcp_f32 v235, v235\nM 156,180,220\nbuffer_load_dwordx4 v223, s[12:15], s63 offen offset:1024 lds\nv_rcp_f32 v236, v236\nM 140,176,200\n"
"R 170,185,20480\nv_mul_f32 v235, v37, v235\nv_rcp_f32 v237, v237\nM 140,180,216\nv_mul_f32 v236, v38, v236\nv_mul_f32 v237, v39, v237\n"
"v_cvt_pk_bf16_f32 v34, v234, v235\nM 124,176,196\nR 172,185,24576\nv_cvt_pk_bf16_f32 v35, v236, v237\nv_mul_f32 v230, v40, v40\nM 124,180,212\n"
"buffer_store_dwordx4 v[32:35], v252, s[20:23], s84 offen offset:0 sc1\nv_mul_f32 v231, v41, v41\nM 108,176,192\nR 174,185,28672\ns_mov_b32 s60, 157696\nv_bfe_u32 v246, v252, 4, 4\n"
"v_lshl_add_u32 v246, v246, 3, s60\nv_mul_f32 v232, v42, v42\nM 108,180,208\nds_read_b64 v[238:239], v246 offset:0\nG 5\nv_mul_f32 v233, v43, v43\n"
"M 96,160,224\nR 176,186,16384\nds_read_b64 v[240:241], v246 offset:128\nv_fma_f32 v230, v230, v215, v214\nM 96,164,240\nds_read_b64 v[242:243], v246 offset:256\n"
"s_add_u32 m0, s50, 73728\nv_fma_f32 v231, v231, v215, v214\nM 112,160,228\nR 178,186,20480\nds_read_b64 v[244:245], v246 offset:384\nv_fma_f32 v232, v232, v215, v214\n"
"M 112,164,244\nds_read_b64 v[246:247], v246 offset:0\ns_add_u32 s63, s55, 2293760\nv_fma_f32 v233, v233, v215, v214\nM 128,160,232\nR 180,186,24576\n"
"v_mul_f32 v230, v40, v230\nv_mul_f32 v231, v41, v231\nM 128,164,248\nbuffer_load_dwordx4 v222, s[12:15], s63 offen lds\nv_mul_f32 v232, v42, v232\nM 144,160,236\n"
"R 182,186,28672\nv_mul_f32 v233, v43, v233\nv_exp_f32 v230, v230\nM 144,164,252\nG 9\nv_exp_f32 v231, v231\n"
"v_exp_f32 v232, v232\nM 148,168,236\nR 160,187,16384\nv_fma_f32 v230, v230, v216, v216\nv_exp_f32 v233, v233\nM 148,172,252\n"
"buffer_load_dwordx4 v223, s[12:15], s63 offen offset:1024 lds\nv_fma_f32 v231, v231, v216, v216\nM 132,168,232\nR 162,187,20480\ns_add_u32 s63, s55, 2752512\nv_fma_f32 v232, v232, v216, v216\n"
"M 132,172,248\ns_add_u32 m0, s50, 81920\nv_rcp_f32 v230, v230\nv_fma_f32 v233, v233, v216, v216\nM 116,168,228\nR 164,187,24576\n"
"G 9\nv_mfma_f32_16x16x32_bf16 v[96:99], v[238:241], v[208:211], v[96:99]\nv_mul_f32 v230, v40, v230\nLpg10:\ns_cbranch_execz Lpg14\nv_rcp_f32 v231, v231\n"
"M 116,172,244\nbuffer_load_dwordx4 v222, s[12:15], s63 offen lds\nv_rcp_f32 v232, v232\nM 100,168,224\nR 166,187,28672\nG 9\n"
"v_mfma_f32_16x16x32_bf16 v[100:103], v[240:243], v[208:211], v[100:103]\nv_mul_f32 v231, v41, v231\nv_rcp_f32 v233, v233\nM 100,172,240\nG 4\nv_mul_f32 v232, v42, v232\n"
"v_mul_f32 v233, v43, v233\nv_cvt_pk_bf16_f32 v40, v230, v231\nM 104,176,224\nR 168,184,32768\nv_mfma_f32_16x16x32_bf16 v[104:107], v[242:245], v[208:211], v[104:107]\nv_cvt_pk_bf16_f32 v41, v232, v233\n"
"v_mul_f32 v234, v44, v44\nM 104,180,240\nbuffer_load_dwordx4 v223, s[12:15], s63 offen offset:1024 lds\nv_mul_f32 v235, v45, v45\nM 120,176,228\nR 170,184,36864\n"
"v_mfma_f32_16x16x32_bf16 v[108:111], v[244:247], v[208:211], v[108:111]\nv_mul_f32 v236, v46, v46\nv_mul_f32 v237, v47, v47\nM 120,180,244\ns_add_u32 m0, s50, 90112\nv_fma_f32 v234, v234, v215, v214\n"
"v_fma_f32 v235, v235, v215, v214\nv_fma_f32 v236, v236, v215, v214\nM 136,176,232\nR 172,186,32768\nv_mfma_f32_16x16x32_bf16 v[112:115], v[244:247], v[210:213], v[112:115]\nv_fma_f32 v237, v237, v215, v214\n"
"v_mul_f32 v234, v44, v234\nM 136,180,248\ns_add_u32 s63, s55, 3211264\nv_exp_f32 v234, v234\nv_mul_f32 v235, v45, v235\nM 152,176,236\n"
"R 174,186,36864\nv_mfma_f32_16x16x32_bf16 v[116:119], v[238:241], v[210:213], v[116:119]\nv_mul_f32 v236, v46, v236\nv_mul_f32 v237, v47, v237\nM 152,180,252\nG 4\n"
"v_exp_f32 v235, v235\nv_exp_f32 v236, v236\nM 156,160,236\nR 176,185,32768\nv_mfma_f32_16x16x32_bf16 v[120:123], v[240:243], v[210:213], v[120:123]\nv_fma_f32 v234, v234, v216, v216\n"
"v_exp_f32 v237, v237\nM 156,164,252\nbuffer_load_dwordx4 v222, s[12:15], s63 offen lds\nv_fma_f32 v235, v235, v216, v216\nM 140,160,232\nR 178,185,36864\n"
"v_mfma_f32_16x16x32_bf16 v[124:127], v[242:245], v[210:213], v[124:127]\nv_fma_f32 v236, v236, v216, v216\nv_fma_f32 v237, v237, v216, v216\nM 140,164,248\nv_rcp_f32 v234, v234\nv_rcp_f32 v235, v235\n"
"M 124,160,228\nR 180,187,32768\nv_mul_f32 v234, v44, v234\nv_rcp_f32 v236, v236\nM 124,164,244\nbuffer_load_dwordx4 v223, s[12:15], s63 offen offset:1024 lds\n"
"v_rcp_f32 v237, v237\nM 108,160,224\nR 182,187,36864\nv_mul_f32 v235, v45, v235\nv_mul_f32 v236, v46, v236\nM 108,164,240\n"
"s_waitcnt vmcnt(12)\ns_barrier\nG 4\nv_mul_f32 v237, v47, v237\nv_mfma_f32_16x16x32_bf16 v[0:3], v[168:171], a[0:3], 0\nR 160,184,40960\n"
"v_cvt_pk_bf16_f32 v42, v234, v235\nv_cvt_pk_bf16_f32 v43, v236, v237\nM 0,172,16\ns_mov_b32 m0, s51\nv_mul_f32 v230, v48, v48\nv_mul_f32 v231, v49, v49\n"
"v_mul_f32 v232, v50, v50\nv_mfma_f32_16x16x32_bf16 v[16:19], v[168:171], a[4:7], 0\nR 162,184,45056\nv_mul_f32 v233, v51, v51\nv_fma_f32 v230, v230, v218, v217\nM 16,172,20\n"
"buffer_store_dwordx4 v[40:43], v252, s[20:23], s84 offen offset:1024 sc1\nv_fma_f32 v231, v231, v218, v217\nv_mfma_f32_16x16x32_bf16 v[8:11], v[168:171], a[8:11], 0\nR 164,186,40960\nv_fma_f32 v232, v232, v218, v217\nv_fma_f32 v233, v233, v218, v217\n"
"M 8,172,24\nds_write_addtid_b32 v128 offset:0\nv_mul_f32 v230, v48, v230\nv_mul_f32 v231, v49, v231\nv_mul_f32 v232, v50, v232\nv_mfma_f32_16x16x32_bf16 v[24:27], v[168:171], a[12:15], 0\n"
"R 166,186,45056\nds_write_addtid_b32 v129 offset:256\nv_mul_f32 v233, v51, v233\nv_exp_f32 v230, v230\nM 24,172,28\nds_write_addtid_b32 v130 offset:512\n"
"G 7\nv_exp_f32 v231, v231\nv_exp_f32 v232, v232\nv_mfma_f32_16x16x32_bf16 v[28:31], v[176:179], a[12:15], 0\nR 168,185,40960\nds_write_addtid_b32 v131 offset:768\n"
"v_fma_f32 v230, v230, v219, v219\nv_exp_f32 v233, v233\nM 28,180,28\nds_write_addtid_b32 v132 offset:1024\nv_rcp_f32 v230, v230\nv_fma_f32 v231, v231, v219, v219\n"
"v_mfma_f32_16x16x32_bf16 v[12:15], v[176:179], a[8:11], 0\nR 170,185,45056\nds_write_addtid_b32 v133 offset:1280\nv_fma_f32 v232, v232, v219, v219\nv_fma_f32 v233, v233, v219, v219\nM 12,180,24\n"
"ds_write_addtid_b32 v134 offset:1536\nv_rcp_f32 v231, v231\nv_rcp_f32 v232, v232\nv_mfma_f32_16x16x32_bf16 v[20:23], v[176:179], a[4:7], 0\nR 172,187,40960\nds_write_addtid_b32 v135 offset:1792\n"
"v_mul_f32 v230, v48, v230\nv_rcp_f32 v233, v233\nM 20,180,20\nds_write_addtid_b32 v136 offset:2048\nv_mul_f32 v231, v49, v231\nv_mul_f32 v232, v50, v232\n"
"v_mul_f32 v233, v51, v233\nv_mfma_f32_16x16x32_bf16 v[4:7], v[176:179], a[0:3], 0\nR 174,187,45056\nds_write_addtid_b32 v137 offset:2304\nv_cvt_pk_bf16_f32 v48, v230, v231\nv_cvt_pk_bf16_f32 v49, v232, v233\n"
"M 4,180,16\nds_write_addtid_b32 v138 offset:2560\nG 14\nv_mul_f32 v234, v52, v52\nv_mul_f32 v235, v53, v53\nv_mul_f32 v236, v54, v54\n"
"M 0,160,32\nR 176,184,49152\nds_write_addtid_b32 v139 offset:2816\nv_mul_f32 v237, v55, v55\nv_fma_f32 v234, v234, v218, v217\nM 0,164,48\n"
"ds_write_addtid_b32 v140 offset:3072\nv_fma_f32 v235, v235, v218, v217\nv_fma_f32 v236, v236, v218, v217\nv_fma_f32 v237, v237, v218, v217\nM 16,160,36\nR 178,184,53248\n"
"ds_write_addtid_b32 v141 offset:3328\nv_mul_f32 v234, v52, v234\nv_mul_f32 v235, v53, v235\nM 16,164,52\nds_write_addtid_b32 v142 offset:3584\nv_exp_f32 v234, v234\n"
"v_mul_f32 v236, v54, v236\nM 8,160,40\nR 180,186,49152\nds_write_addtid_b32 v143 offset:3840\nv_mul_f32 v237, v55, v237\nv_exp_f32 v235, v235\n"
"M 8,164,56\nds_write_addtid_b32 v144 offset:4096\nv_exp_f32 v236, v236\nv_exp_f32 v237, v237\nM 24,160,44\nR 182,186,53248\n"
"ds_write_addtid_b32 v145 offset:4352\nv_fma_f32 v234, v234, v219, v219\nv_fma_f32 v235, v235, v219, v219\nM 24,164,60\nds_write_addtid_b32 v146 offset:4608\nG 14\n"
"v_rcp_f32 v234, v234\nv_fma_f32 v236, v236, v219, v219\nM 28,168,44\nR 160,185,49152\nds_write_addtid_b32 v147 offset:4864\nv_fma_f32 v237, v237, v219, v219\n"
"v_rcp_f32 v235, v235\nM 28,172,60\nds_write_addtid_b32 v148 offset:5120\nds_write_addtid_b32 v149 offset:5376\nds_write_addtid_b32 v150 offset:5632\nds_write_addtid_b32 v151 offset:5888\n"
"ds_write_addtid_b32 v152 offset:6144\nds_write_addtid_b32 v153 offset:6400\nds_write_addtid_b32 v154 offset:6656\nds_write_addtid_b32 v155 offset:6912\nds_write_addtid_b32 v156 offset:7168\nds_write_addtid_b32 v157 offset:7424\n"
"ds_write_addtid_b32 v158 offset:7680\nds_write_addtid_b32 v159 offset:7936\nv_rcp_f32 v236, v236\nv_rcp_f32 v237, v237\nM 12,168,40\nR 162,185,53248\n"
"s_add_u32 m0, s50, 98304\ns_add_u32 s63, s54, 256\nv_mul_f32 v234, v52, v234\nM 12,172,56\nbuffer_load_dwordx4 v220, s[12:15], s63 offen lds\nv_mul_f32 v235, v53, v235\n"
"M 20,168,36\nR 164,187,49152\ns_waitcnt vmcnt(4) lgkmcnt(2)\ns_barrier\nv_mul_f32 v236, v54, v236\nM 20,172,52\n"
"ds_read2st64_b32 v[128:129], v225 offset0:0 offset1:1\nv_mul_f32 v237, v55, v237\nv_cvt_pk_bf16_f32 v50, v234, v235\nv_cvt_pk_bf16_f32 v51, v236, v237\nM 4,168,32\nR 166,187,53248\n"
"ds_read2st64_b32 v[130:131], v225 offset0:2 offset1:3\nv_mul_f32 v230, v56, v56\nv_mul_f32 v231, v57, v57\nM 4,172,48\nds_read2st64_b32 v[132:133], v225 offset0:4 offset1:5\nv_mul_f32 v232, v58, v58\n"
"v_mul_f32 v233, v59, v59\nv_fma_f32 v230, v230, v218, v217\nM 0,176,64\nR 168,184,57344\nds_read2st64_b32 v[134:135], v225 offset0:6 offset1:7\nv_fma_f32 v231, v231, v218, v217\n"
"v_fma_f32 v232, v232, v218, v217\nM 0,180,80\nbuffer_load_dwordx4 v221, s[12:15], s63 offen offset:1024 lds\nds_read2st64_b32 v[136:137], v225 offset0:8 offset1:9\nv_fma_f32 v233, v233, v218, v217\nM 16,176,68\n"
"R 170,184,61440\ns_add_u32 m0, s50, 106496\ns_add_u32 s63, s54, 229632\nds_read2st64_b32 v[138:139], v225 offset0:10 offset1:11\nv_mul_f32 v230, v56, v230\nM 16,180,84\n"
"buffer_load_dwordx4 v220, s[12:15], s63 offen lds\nds_read2st64_b32 v[140:141], v225 offset0:12 offset1:13\nv_mul_f32 v231, v57, v231\nM 8,176,72\nR 172,186,57344\nds_read2st64_b32 v[142:143], v225 offset0:14 offset1:15\n"
"v_mul_f32 v232, v58, v232\nv_mul_f32 v233, v59, v233\nM 8,180,88\nbuffer_store_dwordx4 v[48:51], v252, s[20:23], s85 offen offset:0 sc1\nds_read2st64_b32 v[144:145], v225 offset0:16 offset1:17\nv_exp_f32 v230, v230\n"
"M 24,176,76\nR 174,186,61440\nds_read2st64_b32 v[146:147], v225 offset0:18 offset1:19\nv_fma_f32 v230, v230, v219, v219\nv_exp_f32 v231, v231\nM 24,180,92\n"
"ds_read2st64_b32 v[148:149], v225 offset0:20 offset1:21\nG 14\nv_exp_f32 v232, v232\nv_exp_f32 v233, v233\nM 28,160,76\nR 176,185,57344\n"
"ds_read2st64_b32 v[150:151], v225 offset0:22 offset1:23\nv_fma_f32 v231, v231, v219, v219\nv_fma_f32 v232, v232, v219, v219\nM 28,164,92\nbuffer_load_dwordx4 v221, s[12:15], s63 offen offset:1024 lds\nds_read2st64_b32 v[152:153], v225 offset0:24 offset1:25\n"
"v_fma_f32 v233, v233, v219, v219\nM 12,160,72\nR 178,185,61440\ns_add_u32 s63, s54, 459008\nds_read2st64_b32 v[154:155], v225 offset0:26 offset1:27\nv_rcp_f32 v230, v230\n"
"M 12,164,88\ns_add_u32 m0, s50, 114688\nds_read2st64_b32 v[156:157], v225 offset0:28 offset1:29\nv_rcp_f32 v231, v231\nv_rcp_f32 v232, v232\nM 20,160,68\n"
"R 180,187,57344\nds_read2st64_b32 v[158:159], v225 offset0:30 offset1:31\nv_mul_f32 v230, v56, v230\nv_rcp_f32 v233, v233\nM 20,164,84\nbuffer_load_dwordx4 v220, s[12:15], s63 offen lds\n"
"v_add_f32 v96, v96, v128\nv_mul_f32 v231, v57, v231\nM 4,160,64\nR 182,187,61440\nv_mul_f32 v232, v58, v232\nv_add_f32 v97, v97, v129\n"
"v_mul_f32 v233, v59, v233\nM 4,164,80\nG 11\nv_cvt_pk_bf16_f32 v56, v230, v231\nv_cvt_pk_bf16_f32 v57, v232, v233\nv_add_f32 v98, v98, v130\n"
"v_mul_f32 v234, v60, v60\nM 0,168,96\nR 160,188,0\nv_mul_f32 v235, v61, v61\nv_add_f32 v99, v99, v131\nv_mul_f32 v236, v62, v62\n"
"M 0,172,112\nbuffer_load_dwordx4 v221, s[12:15], s63 offen offset:1024 lds\nv_add_f32 v100, v100, v132\nv_mul_f32 v237, v63, v63\nM 16,168,100\nR 162,188,4096\n"
"v_fma_f32 v234, v234, v218, v217\nv_add_f32 v101, v101, v133\nv_fma_f32 v235, v235, v218, v217\nM 16,172,116\ns_add_u32 m0, s50, 0\nv_fma_f32 v236, v236, v218, v217\n"
"v_fma_f32 v237, v237, v218, v217\nv_add_f32 v102, v102, v134\nv_mul_f32 v234, v60, v234\nM 8,168,104\nR 164,190,0\nv_mul_f32 v235, v61, v235\n"
"v_add_f32 v103, v103, v135\nv_mul_f32 v236, v62, v236\nM 8,172,120\ns_add_u32 s63, s54, 688384\nv_exp_f32 v234, v234\nv_add_f32 v104, v104, v136\n"
"v_mul_f32 v237, v63, v237\nM 24,168,108\nR 166,190,4096\nv_fma_f32 v234, v234, v219, v219\nv_add_f32 v105, v105, v137\nv_exp_f32 v235, v235\n"
"M 24,172,124\nG 4\nv_add_f32 v106, v106, v138\nv_add_f32 v107, v107, v139\nv_add_f32 v108, v108, v140\nv_exp_f32 v236, v236\n"
"M 28,176,108\nR 168,189,0\nv_add_f32 v109, v109, v141\nv_add_f32 v110, v110, v142\nv_exp_f32 v237, v237\nM 28,180,124\n"
"buffer_load_dwordx4 v220, s[12:15], s63 offen lds\nv_add_f32 v111, v111, v143\nv_fma_f32 v235, v235, v219, v219\nM 12,176,104\nR 170,189,4096\nv_add_f32 v112, v112, v144\n"
"v_add_f32 v113, v113, v145\nv_fma_f32 v236, v236, v219, v219\nM 12,180,120\nv_add_f32 v114, v114, v146\nv_add_f32 v115, v115, v147\nv_add_f32 v116, v116, v148\n"
"v_fma_f32 v237, v237, v219, v219\nM 20,176,100\nR 172,191,0\nv_add_f32 v117, v117, v149\nv_add_f32 v118, v118, v150\nv_rcp_f32 v234, v234\n"
"M 20,180,116\nbuffer_load_dwordx4 v221, s[12:15], s63 offen offset:1024 lds\nv_add_f32 v119, v119, v151\nv_add_f32 v120, v120, v152\nv_add_f32 v121, v121, v153\nv_add_f32 v122, v122, v154\n"
"v_add_f32 v123, v123, v155\nv_add_f32 v124, v124, v156\nv_add_f32 v125, v125, v157\nv_add_f32 v126, v126, v158\nv_add_f32 v127, v127, v159\nM 4,176,96\n"
"R 174,191,4096\ns_add_u32 m0, s50, 8192\nM 4,180,112\nG 4\nv_rcp_f32 v235, v235\nM 0,160,128\n"
"R 176,188,8192\ns_add_u32 s63, s54, 917760\nM 0,164,144\nbuffer_load_dwordx4 v220, s[12:15], s63 offen lds\nM 16,160,132\nR 178,188,12288\n"
"v_mul_f32 v234, v60, v234\nM 16,164,148\nv_rcp_f32 v236, v236\nM 8,160,136\nR 180,190,8192\nLq575:\n"
"s_lshr_b32 s60, s45, 3\ns_lshl2_add_u32 s60, s60, s40\ns_lshl_b32 s60, s60, 8\nv_lshl_add_u32 v224, v252, 3, s60\nv_add_u32 v224, Lq575+4096-LqPC, v224\nv_min_u32 v224, LqE-LqPC-4, v224\n"
"s_mov_b64 exec, 3\nglobal_load_dword v224, v224, s[98:99]\ns_mov_b64 exec, -1\nM 8,164,152\nv_rcp_f32 v237, v237\nM 24,160,140\n"
"R 182,190,12288\nv_mul_f32 v235, v61, v235\nM 24,164,156\nG 4\nv_mul_f32 v236, v62, v236\nv_mul_f32 v237, v63, v237\n"
"M 28,168,140\nR 160,189,8192\nv_cvt_pk_bf16_f32 v58, v234, v235\nM 28,172,156\nbuffer_load_dwordx4 v221, s[12:15], s63 offen offset:1024 lds\nM 12,168,136\n"
"R 162,189,12288\ns_add_u32 m0, s50, 16384\ns_add_u32 s63, s54, 1147136\nM 12,172,152\nbuffer_load_dwordx4 v220, s[12:15], s63 offen lds\nM 20,168,132\n"
"R 164,191,8192\nv_cvt_pk_bf16_f32 v59, v236, v237\nM 20,172,148\nM 4,168,128\nR 166,191,12288\nM 4,172,144\n"
"G 4\nM 0,176,160\nR 168,188,16384\nM 0,180,176\nbuffer_load_dwordx4 v221, s[12:15], s63 offen offset:1024 lds\nM 16,176,164\n"
"R 170,188,20480\ns_add_u32 m0, s50, 24576\ns_add_u32 s63, s54, 1376512\nM 16,180,180\nbuffer_load_dwordx4 v220, s[12:15], s63 offen lds\nM 8,176,168\n"
"R 172,190,16384\nM 8,180,184\nbuffer_store_dwordx4 v[56:59], v252, s[20:23], s85 offen offset:1024 sc1\nM 24,176,172\nR 174,190,20480\nM 24,180,188\n"
"G 4\nM 28,160,172\nR 176,189,16384\ns_waitcnt vmcnt(11)\ns_barrier\nv_mul_f32 v230, v96, v96\n"
"M 28,164,188\nbuffer_load_dwordx4 v221, s[12:15], s63 offen offset:1024 lds\nv_mul_f32 v231, v97, v97\nM 12,160,168\nR 178,189,20480\ns_add_u32 m0, s50, 32768\n"
"s_add_u32 s63, s54, 1605888\nv_mul_f32 v232, v98, v98\nM 12,164,184\nbuffer_load_dwordx4 v220, s[12:15], s63 offen lds\nv_mul_f32 v233, v99, v99\nM 20,160,164\n"
"R 180,191,16384\nv_fma_f32 v230, v230, v215, v214\nv_fma_f32 v231, v231, v215, v214\nM 20,164,180\ns_add_u32 s84, s73, 23070720\ns_add_u32 s85, s73, 23185408\n"
"v_fma_f32 v232, v232, v215, v214\nv_fma_f32 v233, v233, v215, v214\nM 4,160,160\nR 182,191,20480\ns_mov_b32 s60, 158720\nv_bfe_u32 v242, v252, 4, 4\n"
"v_lshl_add_u32 v242, v242, 3, s60\nv_mul_f32 v230, v96, v230\nM 4,164,176\nds_read_b64 v[238:239], v242 offset:0\nG 5\nv_mul_f32 v231, v97, v231\n"
"M 0,168,192\nR 160,188,24576\nds_read_b64 v[240:241], v242 offset:128\nv_mul_f32 v232, v98, v232\nM 0,172,208\nds_read_b64 v[242:243], v242 offset:0\n"
"v_mul_f32 v233, v99, v233\nv_exp_f32 v230, v230\nM 16,168,196\nR 162,188,28672\nv_fma_f32 v230, v230, v216, v216\nv_exp_f32 v231, v231\n"
"M 16,172,212\nv_exp_f32 v232, v232\nv_exp_f32 v233, v233\nM 8,168,200\nR 164,190,24576\nv_fma_f32 v231, v231, v216, v216\n"
"v_fma_f32 v232, v232, v216, v216\nM 8,172,216\nv_rcp_f32 v230, v230\nv_fma_f32 v233, v233, v216, v216\nM 24,168,204\nR 166,190,28672\n"
"v_mul_f32 v230, v96, v230\nv_rcp_f32 v231, v231\nM 24,172,220\nG 7\nv_rcp_f32 v232, v232\nv_rcp_f32 v233, v233\n"
"M 28,176,204\nR 168,189,24576\nv_mul_f32 v231, v97, v231\nv_mul_f32 v232, v98, v232\nM 28,180,220\nv_mul_f32 v233, v99, v233\n"
"v_cvt_pk_bf16_f32 v96, v230, v231\nv_cvt_pk_bf16_f32 v97, v232, v233\nM 12,176,200\nR 170,189,28672\nv_mul_f32 v234, v100, v100\nv_mul_f32 v235, v101, v101\n"
"M 12,180,216\nv_mul_f32 v236, v102, v102\nv_mul_f32 v237, v103, v103\nv_fma_f32 v234, v234, v215, v214\nM 20,176,196\nR 172,191,24576\n"
"G 7\nv_mfma_f32_16x16x32_bf16 v[0:3], v[238:241], v[208:211], v[0:3]\nv_fma_f32 v235, v235, v215, v214\nv_fma_f32 v236, v236, v215, v214\nM 20,180,212\nv_fma_f32 v237, v237, v215, v214\n"
"v_mul_f32 v234, v100, v234\nv_mul_f32 v235, v101, v235\nM 4,176,192\nR 174,191,28672\nG 7\nv_mfma_f32_16x16x32_bf16 v[4:7], v[240:243], v[208:211], v[4:7]\n"
"v_mul_f32 v236, v102, v236\nv_mul_f32 v237, v103, v237\nM 4,180,208\nG 4\nv_exp_f32 v234, v234\nv_exp_f32 v235, v235\n"
"M 0,160,224\nR 176,188,32768\nv_mfma_f32_16x16x32_bf16 v[16:19], v[240:243], v[210:213], v[16:19]\nv_fma_f32 v234, v234, v216, v216\nv_exp_f32 v236, v236\nM 0,164,240\n"
"v_exp_f32 v237, v237\nv_fma_f32 v235, v235, v216, v216\nM 16,160,228\nR 178,188,36864\nv_mfma_f32_16x16x32_bf16 v[20:23], v[238:241], v[210:213], v[20:23]\nv_fma_f32 v236, v236, v216, v216\n"
"v_fma_f32 v237, v237, v216, v216\nM 16,164,244\nv_rcp_f32 v234, v234\nv_rcp_f32 v235, v235\nM 8,160,232\nR 180,188,40960\n"
"v_mul_f32 v234, v100, v234\nv_rcp_f32 v236, v236\nM 8,164,248\nv_rcp_f32 v237, v237\nv_mul_f32 v235, v101, v235\nM 24,160,236\n"
"R 182,188,45056\nv_mul_f32 v236, v102, v236\nv_mul_f32 v237, v103, v237\nM 24,164,252\nG 4\nv_cvt_pk_bf16_f32 v98, v234, v235\n"
"v_cvt_pk_bf16_f32 v99, v236, v237\nv_mul_f32 v230, v104, v104\nM 28,168,236\nR 160,189,32768\nv_mul_f32 v231, v105, v105\nv_mul_f32 v232, v106, v106\n"
"M 28,172,252\nv_mul_f32 v233, v107, v107\nv_fma_f32 v230, v230, v215, v214\nv_fma_f32 v231, v231, v215, v214\nM 12,168,232\nR 162,189,36864\n"
"v_fma_f32 v232, v232, v215, v214\nv_fma_f32 v233, v233, v215, v214\nM 12,172,248\nv_mul_f32 v230, v104, v230\nv_mul_f32 v231, v105, v231\nv_mul_f32 v232, v106, v232\n"
"M 20,168,228\nR 164,189,40960\nv_mul_f32 v233, v107, v233\nLpg11:\ns_cbranch_execz Lpg15\nv_exp_f32 v230, v230\n"
"M 20,172,244\nv_exp_f32 v231, v231\nv_exp_f32 v232, v232\nM 4,168,224\nR 166,189,45056\nv_fma_f32 v230, v230, v216, v216\n"
"v_exp_f32 v233, v233\nM 4,172,240\ns_waitcnt vmcnt(11)\ns_barrier\nG 4\nv_fma_f32 v231, v231, v216, v216\n"
"v_mfma_f32_16x16x32_bf16 v[32:35], v[176:179], a[0:3], 0\nR 168,190,32768\nv_fma_f32 v232, v232, v216, v216\nv_fma_f32 v233, v233, v216, v216\nM 32,180,16\ns_mov_b32 m0, s51\n"
"v_rcp_f32 v230, v230\nv_rcp_f32 v231, v231\nv_mfma_f32_16x16x32_bf16 v[48:51], v[176:179], a[4:7], 0\nR 170,190,36864\nds_write_addtid_b32 v8 offset:0\nv_mul_f32 v230, v104, v230\n"
"v_rcp_f32 v232, v232\nM 48,180,20\nds_write_addtid_b32 v9 offset:256\nv_rcp_f32 v233, v233\nv_mul_f32 v231, v105, v231\nv_mfma_f32_16x16x32_bf16 v[64:67], v[176:179], a[8:11], 0\n"
"R 172,190,40960\nds_write_addtid_b32 v10 offset:512\nv_mov_b32 v226, s46\nv_mul_f32 v232, v106, v232\nM 64,180,24\nds_write_addtid_b32 v11 offset:768\n"
"v_mov_b32 v227, s47\nv_mul_f32 v233, v107, v233\nv_cvt_pk_bf16_f32 v104, v230, v231\nv_mfma_f32_16x16x32_bf16 v[80:83], v[176:179], a[12:15], 0\nR 174,190,45056\ns_add_u32 s61, s81, 385024\n"
"s_lshl_b32 s62, s44, 5\ns_add_u32 s61, s61, s62\ns_waitcnt vmcnt(0)\ns_mov_b64 exec, 1\nbuffer_atomic_swap_x2 v[226:227], off, s[20:23], s61 sc1\ns_mov_b64 exec, -1\n"
"ds_write_addtid_b32 v12 offset:1024\nv_cvt_pk_bf16_f32 v105, v232, v233\nM 80,180,28\nds_write_addtid_b32 v13 offset:1280\nG 10\nv_mul_f32 v234, v108, v108\n"
"v_mul_f32 v235, v109, v109\nv_mul_f32 v236, v110, v110\nv_mfma_f32_16x16x32_bf16 v[84:87], v[160:163], a[12:15], 0\nR 176,191,32768\nds_write_addtid_b32 v14 offset:1536\nv_mul_f32 v237, v111, v111\n"
"v_fma_f32 v234, v234, v215, v214\nM 84,164,28\nds_write_addtid_b32 v15 offset:1792\nv_fma_f32 v235, v235, v215, v214\nv_fma_f32 v236, v236, v215, v214\nv_fma_f32 v237, v237, v215, v214\n"
"v_mfma_f32_16x16x32_bf16 v[68:71], v[160:163], a[8:11], 0\nR 178,191,36864\nds_write_addtid_b32 v24 offset:4096\nv_mul_f32 v234, v108, v234\nv_mul_f32 v235, v109, v235\nM 68,164,24\n"
"buffer_store_dwordx4 v[96:99], v252, s[20:23], s84 offen offset:0 sc1\nds_write_addtid_b32 v25 offset:4352\nv_mul_f32 v236, v110, v236\nv_mfma_f32_16x16x32_bf16 v[52:55], v[160:163], a[4:7], 0\nR 180,191,40960\nds_write_addtid_b32 v26 offset:4608\n"
"v_mul_f32 v237, v111, v237\nv_exp_f32 v234, v234\nM 52,164,20\nds_write_addtid_b32 v27 offset:4864\nds_write_addtid_b32 v28 offset:5120\nds_write_addtid_b32 v29 offset:5376\n"
"ds_write_addtid_b32 v30 offset:5632\nds_write_addtid_b32 v31 offset:5888\nv_exp_f32 v235, v235\nv_exp_f32 v236, v236\nv_mfma_f32_16x16x32_bf16 v[36:39], v[160:163], a[0:3], 0\nR 182,191,45056\n"
"s_add_u32 m0, s50, 32768\nv_exp_f32 v237, v237\nM 36,164,16\nG 15\nv_fma_f32 v234, v234, v216, v216\nv_fma_f32 v235, v235, v216, v216\n"
"v_fma_f32 v236, v236, v216, v216\nv_mfma_f32_16x16x32_bf16 v[40:43], v[168:171], a[0:3], 0\nR 160,188,49152\nG 2\ns_barrier\nv_fma_f32 v237, v237, v216, v216\n"
"M 40,172,16\nbuffer_load_dwordx4 v221, s[12:15], s63 offen offset:1024 lds\nds_read2st64_b32 v[8:9], v225 offset0:0 offset1:1\nv_rcp_f32 v234, v234\nv_mfma_f32_16x16x32_bf16 v[56:59], v[168:171], a[4:7], 0\nR 162,188,53248\n"
"s_add_u32 m0, s50, 40960\ns_add_u32 s63, s54, 1835264\nds_read2st64_b32 v[10:11], v225 offset0:2 offset1:3\nv_rcp_f32 v235, v235\nM 56,172,20\nbuffer_load_dwordx4 v220, s[12:15], s63 offen lds\n"
"ds_read2st64_b32 v[12:13], v225 offset0:4 offset1:5\nv_rcp_f32 v236, v236\nv_mfma_f32_16x16x32_bf16 v[72:75], v[168:171], a[8:11], 0\nR 164,184,0\nds_read2st64_b32 v[14:15], v225 offset0:6 offset1:7\nv_mul_f32 v234, v108, v234\n"
"v_rcp_f32 v237, v237\nM 72,172,24\nds_read2st64_b32 v[24:25], v225 offset0:16 offset1:17\nv_mul_f32 v235, v109, v235\nv_mul_f32 v236, v110, v236\nv_mul_f32 v237, v111, v237\n"
"v_mfma_f32_16x16x32_bf16 v[88:91], v[168:171], a[12:15], 0\nR 166,184,4096\nds_read2st64_b32 v[26:27], v225 offset0:18 offset1:19\nv_cvt_pk_bf16_f32 v106, v234, v235\nv_cvt_pk_bf16_f32 v107, v236, v237\nM 88,172,28\n"
"ds_read2st64_b32 v[28:29], v225 offset0:20 offset1:21\nG 11\nv_mul_f32 v230, v112, v112\nv_mul_f32 v231, v113, v113\nv_mul_f32 v232, v114, v114\nv_mfma_f32_16x16x32_bf16 v[92:95], v[176:179], a[12:15], 0\n"
"R 168,189,49152\nds_read2st64_b32 v[30:31], v225 offset0:22 offset1:23\nv_mul_f32 v233, v115, v115\nv_fma_f32 v230, v230, v218, v217\nM 92,180,28\nbuffer_load_dwordx4 v221, s[12:15], s63 offen offset:1024 lds\n"
"G 11\nv_add_f32 v0, v0, v8\nv_fma_f32 v231, v231, v218, v217\nv_mfma_f32_16x16x32_bf16 v[76:79], v[176:179], a[8:11], 0\nR 170,189,53248\ns_add_u32 m0, s50, 49152\n"
"s_add_u32 s63, s54, 2064640\nv_add_f32 v1, v1, v9\nv_fma_f32 v232, v232, v218, v217\nM 76,180,24\nbuffer_load_dwordx4 v220, s[12:15], s63 offen lds\nG 10\n"
"v_add_f32 v2, v2, v10\nv_fma_f32 v233, v233, v218, v217\nv_mfma_f32_16x16x32_bf16 v[60:63], v[176:179], a[4:7], 0\nR 172,185,0\nv_mul_f32 v230, v112, v230\nv_add_f32 v3, v3, v11\n"
"v_mul_f32 v231, v113, v231\nM 60,180,20\nbuffer_store_dwordx4 v[104:107], v252, s[20:23], s84 offen offset:1024 sc1\nG 10\nv_add_f32 v4, v4, v12\nv_mul_f32 v232, v114, v232\n"
"v_mfma_f32_16x16x32_bf16 v[44:47], v[176:179], a[0:3], 0\nR 174,185,4096\nv_mul_f32 v233, v115, v233\nv_add_f32 v5, v5, v13\nv_exp_f32 v230, v230\nM 44,180,16\n"
"G 7\nv_exp_f32 v231, v231\nv_add_f32 v6, v6, v14\nv_exp_f32 v232, v232\nM 32,160,32\nR 176,190,49152\n"
"v_mov_b32 v248, 0\nv_add_f32 v7, v7, v15\nv_exp_f32 v233, v233\nM 32,164,48\nbuffer_load_dwordx4 v221, s[12:15], s63 offen offset:1024 lds\nv_mov_b32 v249, 0\n"
"v_add_f32 v16, v16, v24\nv_fma_f32 v230, v230, v219, v219\nM 48,160,36\nR 178,190,53248\ns_add_u32 s61, s82, 385024\ns_mov_b64 exec, 0xf\n"
"buffer_atomic_or_x2 v[248:249], v250, s[20:23], s61 offen sc0 sc1\ns_mov_b64 exec, -1\nv_add_f32 v17, v17, v25\nv_fma_f32 v231, v231, v219, v219\nM 48,164,52\nv_rcp_f32 v230, v230\n"
"G 8\nv_add_f32 v18, v18, v26\nv_fma_f32 v232, v232, v219, v219\nM 64,160,40\nR 180,186,0\nv_fma_f32 v233, v233, v219, v219\n"
"v_add_f32 v19, v19, v27\nG 8\nv_add_f32 v20, v20, v28\nv_add_f32 v21, v21, v29\nG 6\nv_add_f32 v22, v22, v30\n"
"v_add_f32 v23, v23, v31\nv_rcp_f32 v231, v231\nM 64,164,56\ns_add_u32 m0, s50, 57344\nv_rcp_f32 v232, v232\nv_rcp_f32 v233, v233\n"
"M 80,160,44\nR 182,186,4096\nv_mul_f32 v230, v112, v230\nv_mul_f32 v231, v113, v231\nM 80,164,60\nG 4\n"
"v_mul_f32 v232, v114, v232\nv_mul_f32 v233, v115, v233\nv_cvt_pk_bf16_f32 v112, v230, v231\nM 84,168,44\nR 160,191,49152\nv_cvt_pk_bf16_f32 v113, v232, v233\n"
"M 84,172,60\ns_add_u32 s63, s54, 2294016\nv_mul_f32 v234, v116, v116\nv_mul_f32 v235, v117, v117\nM 68,168,40\nR 162,191,53248\n"
"v_mul_f32 v236, v118, v118\nM 68,172,56\nbuffer_load_dwordx4 v220, s[12:15], s63 offen lds\nM 52,168,36\nR 164,187,0\nv_mul_f32 v237, v119, v119\n"
"M 52,172,52\nv_fma_f32 v234, v234, v218, v217\nv_fma_f32 v235, v235, v218, v217\nM 36,168,32\nR 166,187,4096\nv_fma_f32 v236, v236, v218, v217\n"
"M 36,172,48\nG 4\nv_fma_f32 v237, v237, v218, v217\nv_mul_f32 v234, v116, v234\nM 40,176,32\nR 168,184,8192\n"
"v_mul_f32 v235, v117, v235\nM 40,180,48\nbuffer_load_dwordx4 v221, s[12:15], s63 offen offset:1024 lds\nM 56,176,36\nR 170,184,12288\ns_add_u32 s63, s54, 2523392\n"
"M 56,180,52\ns_add_u32 m0, s50, 65536\nv_exp_f32 v234, v234\nM 72,176,40\nR 172,184,16384\nv_mul_f32 v236, v118, v236\n"
"M 72,180,56\nbuffer_load_dwordx4 v220, s[12:15], s63 offen lds\nM 88,176,44\nR 174,184,20480\nv_mul_f32 v237, v119, v237\nM 88,180,60\n"
"G 4\nv_exp_f32 v235, v235\nM 92,160,44\nR 176,185,8192\nv_fma_f32 v234, v234, v219, v219\nM 92,164,60\n"
"buffer_load_dwordx4 v221, s[12:15], s63 offen offset:1024 lds\nM 76,160,40\nR 178,185,12288\ns_add_u32 s63, s54, 2752768\nM 76,164,56\ns_add_u32 m0, s50, 73728\n"
"v_exp_f32 v236, v236\nM 60,160,36\nR 180,185,16384\nv_fma_f32 v235, v235, v219, v219\nM 60,164,52\nbuffer_load_dwordx4 v220, s[12:15], s63 offen lds\n"
"M 44,160,32\nR 182,185,20480\nv_fma_f32 v236, v236, v219, v219\nM 44,164,48\nG 4\nv_exp_f32 v237, v237\n"
"M 32,168,64\nR 160,186,8192\nv_fma_f32 v237, v237, v219, v219\nM 32,172,80\nbuffer_load_dwordx4 v221, s[12:15], s63 offen offset:1024 lds\nM 48,168,68\n"
"R 162,186,12288\nv_mul_f32 v230, v120, v120\nM 48,172,84\ns_add_u32 m0, s50, 81920\nv_rcp_f32 v234, v234\nM 64,168,72\n"
"R 164,186,16384\nv_mul_f32 v234, v116, v234\nM 64,172,88\ns_add_u32 s63, s54, 2982144\nv_rcp_f32 v235, v235\nM 80,168,76\n"
"R 166,186,20480\nv_mul_f32 v235, v117, v235\nM 80,172,92\nG 4\nv_rcp_f32 v236, v236\nM 84,176,76\n"
"R 168,187,8192\nv_mul_f32 v236, v118, v236\nM 84,180,92\nbuffer_load_dwordx4 v220, s[12:15], s63 offen lds\nM 68,176,72\nR 170,187,12288\n"
"v_cvt_pk_bf16_f32 v114, v234, v235\nM 68,180,88\nv_rcp_f32 v237, v237\nM 52,176,68\nR 172,187,16384\nv_mul_f32 v237, v119, v237\n"
"M 52,180,84\ns_add_u32 s64, s68, 23068672\nv_cvt_pk_bf16_f32 v115, v236, v237\nv_mul_f32 v231, v121, v121\nM 36,176,64\nR 174,187,20480\n"
"v_mul_f32 v232, v122, v122\nM 36,180,80\nG 4\nv_mul_f32 v233, v123, v123\nv_fma_f32 v230, v230, v218, v217\nM 40,160,64\n"
"R 176,184,24576\ns_waitcnt vmcnt(8)\ns_barrier\ns_waitcnt vmcnt(7)\nv_cmp_eq_u64 s[86:87], s[46:47], v[248:249]\nM 40,164,80\n"
"s_add_u32 s65, s68, 23183360\nv_fma_f32 v231, v231, v218, v217\nv_fma_f32 v232, v232, v218, v217\nM 56,160,68\nR 178,184,28672\ns_and_b32 s62, s86, 0xf\n"
"s_cmp_eq_u32 s62, 0xf\ns_cbranch_scc0 Lq377\nLq378:\nM 56,164,84\nbuffer_load_dwordx4 v221, s[12:15], s63 offen offset:1024 lds\nM 72,160,72\n"
"R 180,184,32768\nv_fma_f32 v233, v233, v218, v217\nM 72,164,88\nbuffer_load_dwordx4 a[0:3], v252, s[20:23], s64 offen sc1\nM 88,160,76\nR 182,184,36864\n"
"v_mul_f32 v230, v120, v230\nM 88,164,92\nG 4\nv_exp_f32 v230, v230\nM 92,168,76\nR 160,185,24576\n"
"v_mul_f32 v231, v121, v231\nM 92,172,92\nbuffer_load_dwordx4 a[4:7], v252, s[20:23], s65 offen sc1\nM 76,168,72\nR 162,185,28672\nv_mul_f32 v232, v122, v232\n"
"M 76,172,88\ns_add_u32 s66, s69, 23068672\nv_exp_f32 v231, v231\nM 60,168,68\nR 164,185,32768\nv_mul_f32 v233, v123, v233\n"
"M 60,172,84\ns_add_u32 s67, s69, 23183360\nv_exp_f32 v232, v232\nM 44,168,64\nR 166,185,36864\nv_fma_f32 v230, v230, v219, v219\n"
"M 44,172,80\nG 4\nv_exp_f32 v233, v233\nM 32,176,96\nR 168,186,24576\nv_fma_f32 v231, v231, v219, v219\n"
"M 32,180,112\nbuffer_load_dwordx4 a[8:11], v252, s[20:23], s66 offen sc1\nM 48,176,100\nR 170,186,28672\nv_fma_f32 v232, v232, v219, v219\nM 48,180,116\n"
"buffer_load_dwordx4 a[12:15], v252, s[20:23], s67 offen sc1\nM 64,176,104\nR 172,186,32768\nv_fma_f32 v233, v233, v219, v219\nM 64,180,120\ns_add_u32 s64, s68, 23075840\n"
"v_rcp_f32 v230, v230\nM 80,176,108\nR 174,186,36864\nv_mul_f32 v230, v120, v230\nM 80,180,124\nG 4\n"
"v_rcp_f32 v231, v231\nM 84,160,108\nR 176,187,24576\nv_mul_f32 v231, v121, v231\nM 84,164,124\nbuffer_load_dwordx4 a[16:19], v252, s[20:23], s64 offen sc1\n"
"M 68,160,104\nR 178,187,28672\nv_cvt_pk_bf16_f32 v120, v230, v231\nM 68,164,120\ns_add_u32 s65, s68, 23190528\nv_rcp_f32 v232, v232\n"
"M 52,160,100\nR 180,187,32768\nv_mul_f32 v232, v122, v232\nM 52,164,116\ns_add_u32 s66, s69, 23075840\nv_rcp_f32 v233, v233\n"
"M 36,160,96\nR 182,187,36864\nv_mul_f32 v233, v123, v233\nM 36,164,112\nG 4\nv_cvt_pk_bf16_f32 v121, v232, v233\n"
"v_mul_f32 v234, v124, v124\nM 40,168,96\nR 160,184,40960\nv_mul_f32 v235, v125, v125\nM 40,172,112\nbuffer_load_dwordx4 a[20:23], v252, s[20:23], s65 offen sc1\n"
"M 56,168,100\nR 162,184,45056\nv_mul_f32 v236, v126, v126\nM 56,172,116\nbuffer_load_dwordx4 a[24:27], v252, s[20:23], s66 offen sc1\nM 72,168,104\n"
"R 164,184,49152\nv_mul_f32 v237, v127, v127\nM 72,172,120\ns_add_u32 s67, s69, 23190528\nv_fma_f32 v234, v234, v218, v217\nv_fma_f32 v235, v235, v218, v217\n"
"M 88,168,108\nR 166,184,53248\nv_fma_f32 v236, v236, v218, v217\nM 88,172,124\nG 4\nv_fma_f32 v237, v237, v218, v217\n"
"v_mul_f32 v234, v124, v234\nM 92,176,108\nR 168,185,40960\ns_add_u32 m0, s50, 90112\nM 92,180,124\nbuffer_load_dwordx4 a[28:31], v252, s[20:23], s67 offen sc1\n"
"M 76,176,104\nR 170,185,45056\ns_add_u32 s63, s54, 3211520\nM 76,180,120\ns_add_u32 s64, s68, 23069696\nv_exp_f32 v234, v234\n"
"M 60,176,100\nR 172,185,49152\nv_mul_f32 v235, v125, v235\nM 60,180,116\ns_add_u32 s65, s68, 23184384\nbuffer_load_dwordx4 v220, s[12:15], s63 offen lds\n"
"M 44,176,96\nR 174,185,53248\nv_mul_f32 v236, v126, v236\nM 44,180,112\nG 4\nv_exp_f32 v235, v235\n"
"M 32,160,128\nR 176,186,40960\nv_mul_f32 v237, v127, v237\nM 32,164,144\nbuffer_load_dwordx4 a[32:35], v252, s[20:23], s64 offen sc1\nbuffer_load_dwordx4 v221, s[12:15], s63 offen offset:1024 lds\n"
"M 48,160,132\nR 178,186,45056\ns_add_u32 m0, s50, 98304\nM 48,164,148\nbuffer_load_dwordx4 a[36:39], v252, s[20:23], s65 offen sc1\nM 64,160,136\n"
"R 180,186,49152\ns_add_u32 s63, s54, 3440896\nM 64,164,152\ns_add_u32 s66, s69, 23069696\nbuffer_load_dwordx4 v220, s[12:15], s63 offen lds\nM 80,160,140\n"
"R 182,186,53248\nv_fma_f32 v234, v234, v219, v219\nM 80,164,156\nG 4\nv_exp_f32 v236, v236\nM 84,168,140\n"
"R 160,187,40960\nv_fma_f32 v235, v235, v219, v219\nM 84,172,156\nbuffer_load_dwordx4 a[40:43], v252, s[20:23], s66 offen sc1\nbuffer_load_dwordx4 v221, s[12:15], s63 offen offset:1024 lds\nM 68,168,136\n"
"R 162,187,45056\nv_fma_f32 v236, v236, v219, v219\nM 68,172,152\ns_add_u32 s67, s69, 23184384\nv_exp_f32 v237, v237\nM 52,168,132\n"
"R 164,187,49152\ns_waitcnt vmcnt(15)\ns_barrier\nM 52,172,148\nbuffer_load_dwordx4 a[44:47], v252, s[20:23], s67 offen sc1\ns_movk_i32 s63, 2048\n"
"v_mad_u32_u24 v220, v228, s63, v229\nv_add_u32 v221, 916480, v220\ns_add_u32 m0, s50, 106496\ns_add_u32 s63, s56, 0\nbuffer_load_dwordx4 v220, s[16:19], s63 offen lds\nM 36,168,128\n"
"R 166,187,53248\nv_fma_f32 v237, v237, v219, v219\nM 36,172,144\nG 4\nv_rcp_f32 v234, v234\nM 40,176,128\n"
"R 168,184,57344\nv_mul_f32 v234, v124, v234\nM 40,180,144\ns_add_u32 s64, s68, 23076864\nbuffer_load_dwordx4 v221, s[16:19], s63 offen offset:1024 lds\nM 56,176,132\n"
"R 170,184,61440\ns_add_u32 m0, s50, 114688\nM 56,180,148\nbuffer_load_dwordx4 a[48:51], v252, s[20:23], s64 offen sc1\nM 72,176,136\nR 172,188,0\n"
"s_add_u32 s63, s56, 524288\nM 72,180,152\ns_add_u32 s65, s68, 23191552\nbuffer_load_dwordx4 v220, s[16:19], s63 offen lds\nM 88,176,140\nR 174,188,4096\n"
"v_rcp_f32 v235, v235\nM 88,180,156\nG 4\nv_rcp_f32 v236, v236\nM 92,160,140\nR 176,185,57344\n"
"v_mul_f32 v235, v125, v235\nM 92,164,156\nbuffer_load_dwordx4 a[52:55], v252, s[20:23], s65 offen sc1\nbuffer_load_dwordx4 v221, s[16:19], s63 offen offset:1024 lds\nM 76,160,136\nR 178,185,61440\n"
"s_add_u32 m0, s50, 0\nM 76,164,152\ns_add_u32 s66, s69, 23076864\nv_rcp_f32 v237, v237\nM 60,160,132\nR 180,189,0\n"
"s_add_u32 s63, s56, 65536\nM 60,164,148\nbuffer_load_dwordx4 a[56:59], v252, s[20:23], s66 offen sc1\nM 44,160,128\nR 182,189,4096\nv_mul_f32 v236, v126, v236\n"
"M 44,164,144\nbuffer_load_dwordx4 v220, s[16:19], s63 offen lds\nG 4\nM 32,168,160\nR 160,186,57344\nv_mul_f32 v237, v127, v237\n"
"M 32,172,176\ns_add_u32 s67, s69, 23191552\nv_cvt_pk_bf16_f32 v122, v234, v235\nv_cvt_pk_bf16_f32 v123, v236, v237\nM 48,168,164\nR 162,186,61440\n"
"M 48,172,180\nbuffer_load_dwordx4 a[60:63], v252, s[20:23], s67 offen sc1\nbuffer_load_dwordx4 v221, s[16:19], s63 offen offset:1024 lds\nM 64,168,168\nR 164,190,0\ns_add_u32 m0, s50, 8192\n"
"s_add_u32 s63, s56, 589824\nM 64,172,184\nbuffer_load_dwordx4 v220, s[16:19], s63 offen lds\nLpg12:\ns_cbranch_execz LretO0n\nM 80,168,172\n"
"R 166,190,4096\nbuffer_store_dwordx4 v[112:115], v252, s[20:23], s85 offen offset:0 sc1\nM 80,172,188\nbuffer_store_dwordx4 v[120:123], v252, s[20:23], s85 offen offset:1024 sc1\nG 4\nM 84,176,172\n"
"R 168,187,57344\nM 84,180,188\nbuffer_load_dwordx4 v221, s[16:19], s63 offen offset:1024 lds\nM 68,176,168\nR 170,187,61440\ns_add_u32 s63, s57, 0\n"
"v_mul_f32 v230, v0, v0\nM 68,180,184\ns_add_u32 m0, s50, 16384\ns_add_u32 s84, s73, 23074816\ns_add_u32 s85, s73, 23189504\nv_mul_f32 v231, v1, v1\n"
"M 52,176,164\nR 172,191,0\nv_mul_f32 v232, v2, v2\nv_mul_f32 v233, v3, v3\nM 52,180,180\nbuffer_load_dwordx4 v220, s[16:19], s63 offen lds\n"
"v_fma_f32 v230, v230, v215, v214\nM 36,176,160\nR 174,191,4096\nv_fma_f32 v231, v231, v215, v214\nv_fma_f32 v232, v232, v215, v214\nM 36,180,176\n"
"G 4\nv_fma_f32 v233, v233, v215, v214\nv_mul_f32 v230, v0, v230\nv_mul_f32 v231, v1, v231\nM 40,160,160\nR 176,188,8192\n"
"v_mul_f32 v232, v2, v232\nv_mul_f32 v233, v3, v233\nM 40,164,176\nv_exp_f32 v230, v230\nv_exp_f32 v231, v231\nM 56,160,164\n"
"R 178,188,12288\nv_fma_f32 v230, v230, v216, v216\nv_exp_f32 v232, v232\nM 56,164,180\nv_exp_f32 v233, v233\nv_fma_f32 v231, v231, v216, v216\n"
"M 72,160,168\nR 180,188,16384\nv_fma_f32 v232, v232, v216, v216\nv_fma_f32 v233, v233, v216, v216\nM 72,164,184\nv_rcp_f32 v230, v230\n"
"v_rcp_f32 v231, v231\nM 88,160,172\nR 182,188,20480\nv_mul_f32 v230, v0, v230\nv_rcp_f32 v232, v232\nM 88,164,188\n"
"G 4\nv_rcp_f32 v233, v233\nv_mul_f32 v231, v1, v231\nM 92,168,172\nR 160,189,8192\nv_mul_f32 v232, v2, v232\n"
"v_mul_f32 v233, v3, v233\nM 92,172,188\nv_cvt_pk_bf16_f32 v0, v230, v231\nv_cvt_pk_bf16_f32 v1, v232, v233\nv_mul_f32 v234, v4, v4\nM 76,168,168\n"
"R 162,189,12288\nv_mul_f32 v235, v5, v5\nv_mul_f32 v236, v6, v6\nM 76,172,184\nv_mul_f32 v237, v7, v7\nv_fma_f32 v234, v234, v215, v214\n"
"v_fma_f32 v235, v235, v215, v214\nM 60,168,164\nR 164,189,16384\nv_fma_f32 v236, v236, v215, v214\nv_fma_f32 v237, v237, v215, v214\nM 60,172,180\n"
"v_mul_f32 v234, v4, v234\nv_mul_f32 v235, v5, v235\nv_mul_f32 v236, v6, v236\nM 44,168,160\nR 166,189,20480\nv_mul_f32 v237, v7, v237\n"
"v_exp_f32 v234, v234\nM 44,172,176\nG 4\nv_exp_f32 v235, v235\nv_exp_f32 v236, v236\nM 32,176,192\n"
"R 168,190,8192\nv_fma_f32 v234, v234, v216, v216\nv_exp_f32 v237, v237\nM 32,180,208\nv_rcp_f32 v234, v234\nv_fma_f32 v235, v235, v216, v216\n"
"M 48,176,196\nR 170,190,12288\nv_fma_f32 v236, v236, v216, v216\nv_fma_f32 v237, v237, v216, v216\nM 48,180,212\nv_rcp_f32 v235, v235\n"
"v_rcp_f32 v236, v236\nM 64,176,200\nR 172,190,16384\nv_mul_f32 v234, v4, v234\nv_rcp_f32 v237, v237\nM 64,180,216\n"
"v_mul_f32 v235, v5, v235\nv_mul_f32 v236, v6, v236\nv_mul_f32 v237, v7, v237\nM 80,176,204\nR 174,190,20480\nv_cvt_pk_bf16_f32 v2, v234, v235\n"
"v_cvt_pk_bf16_f32 v3, v236, v237\nM 80,180,220\nG 4\nv_mul_f32 v230, v16, v16\nv_mul_f32 v231, v17, v17\nv_mul_f32 v232, v18, v18\n"
"M 84,160,204\nR 176,191,8192\ns_waitcnt vmcnt(9)\ns_barrier\nv_mul_f32 v233, v19, v19\nM 84,164,220\n"
"v_fma_f32 v230, v230, v218, v217\nv_fma_f32 v231, v231, v218, v217\nv_fma_f32 v232, v232, v218, v217\nM 68,160,200\nR 178,191,12288\nv_fma_f32 v233, v233, v218, v217\n"
"v_mul_f32 v230, v16, v230\nM 68,164,216\nv_exp_f32 v230, v230\nv_mul_f32 v231, v17, v231\nM 52,160,196\nR 180,191,16384\n"
"v_mul_f32 v232, v18, v232\nv_mul_f32 v233, v19, v233\nM 52,164,212\nv_exp_f32 v231, v231\nv_exp_f32 v232, v232\nM 36,160,192\n"
"R 182,191,20480\nv_fma_f32 v230, v230, v219, v219\nv_exp_f32 v233, v233\nM 36,164,208\nG 4\nv_rcp_f32 v230, v230\n"
"v_fma_f32 v231, v231, v219, v219\nM 40,168,192\nR 160,188,24576\nv_fma_f32 v232, v232, v219, v219\nv_fma_f32 v233, v233, v219, v219\nM 40,172,208\n"
"v_rcp_f32 v231, v231\nv_rcp_f32 v232, v232\nM 56,168,196\nR 162,188,28672\nv_mul_f32 v230, v16, v230\nv_rcp_f32 v233, v233\n"
"M 56,172,212\nv_mul_f32 v231, v17, v231\nv_mul_f32 v232, v18, v232\nv_mul_f32 v233, v19, v233\nM 72,168,200\nR 164,188,32768\n"
"v_cvt_pk_bf16_f32 v16, v230, v231\nv_cvt_pk_bf16_f32 v17, v232, v233\nM 72,172,216\nv_mul_f32 v234, v20, v20\nv_mul_f32 v235, v21, v21\nv_mul_f32 v236, v22, v22\n"
"M 88,168,204\nR 166,188,36864\nv_mov_b32 v226, s46\nv_mul_f32 v237, v23, v23\nM 88,172,220\nG 4\n"
"v_mov_b32 v227, s47\nv_fma_f32 v234, v234, v218, v217\nv_fma_f32 v235, v235, v218, v217\nM 92,176,204\nR 168,189,24576\ns_add_u32 s61, s81, 385032\n"
"s_lshl_b32 s62, s44, 5\ns_add_u32 s61, s61, s62\ns_waitcnt vmcnt(0)\ns_mov_b64 exec, 1\nbuffer_atomic_swap_x2 v[226:227], off, s[20:23], s61 sc1\ns_mov_b64 exec, -1\n"
"v_fma_f32 v236, v236, v218, v217\nM 92,180,220\nbuffer_load_dwordx4 v221, s[16:19], s63 offen offset:1024 lds\nv_fma_f32 v237, v237, v218, v217\nM 76,176,200\nR 170,189,28672\n"
"s_add_u32 m0, s50, 24576\ns_add_u32 s63, s57, 524288\nv_mul_f32 v234, v20, v234\nM 76,180,216\nbuffer_load_dwordx4 v220, s[16:19], s63 offen lds\nv_mul_f32 v235, v21, v235\n"
"M 60,176,196\nR 172,189,32768\nv_mul_f32 v236, v22, v236\nv_mul_f32 v237, v23, v237\nM 60,180,212\nv_exp_f32 v234, v234\n"
"v_exp_f32 v235, v235\nM 44,176,192\nR 174,189,36864\ns_mov_b32 s60, 158208\nv_bfe_u32 v246, v252, 4, 4\nv_lshl_add_u32 v246, v246, 3, s60\n"
"v_exp_f32 v236, v236\nM 44,180,208\nds_read_b64 v[238:239], v246 offset:0\nG 5\nv_exp_f32 v237, v237\nM 32,160,224\n"
"R 176,190,24576\nds_read_b64 v[240:241], v246 offset:128\nv_fma_f32 v234, v234, v219, v219\nM 32,164,240\nds_read_b64 v[242:243], v246 offset:256\nbuffer_load_dwordx4 v221, s[16:19], s63 offen offset:1024 lds\n"
"v_fma_f32 v235, v235, v219, v219\nM 48,160,228\nR 178,190,28672\nds_read_b64 v[244:245], v246 offset:384\nv_fma_f32 v236, v236, v219, v219\nM 48,164,244\n"
"ds_read_b64 v[246:247], v246 offset:0\ns_add_u32 m0, s50, 32768\nv_fma_f32 v237, v237, v219, v219\nM 64,160,232\nR 180,190,32768\ns_add_u32 s63, s57, 65536\n"
"v_rcp_f32 v234, v234\nM 64,164,248\nbuffer_load_dwordx4 v220, s[16:19], s63 offen lds\nv_rcp_f32 v235, v235\nM 80,160,236\nR 182,190,36864\n"
"v_mul_f32 v234, v20, v234\nv_rcp_f32 v236, v236\nM 80,164,252\nG 9\nv_rcp_f32 v237, v237\nv_mul_f32 v235, v21, v235\n"
"M 84,168,236\nR 160,191,24576\nv_mul_f32 v236, v22, v236\nM 84,172,252\nbuffer_load_dwordx4 v221, s[16:19], s63 offen offset:1024 lds\nM 68,168,232\n"
"R 162,191,28672\ns_add_u32 m0, s50, 40960\ns_add_u32 s63, s57, 589824\nM 68,172,248\nbuffer_load_dwordx4 v220, s[16:19], s63 offen lds\nM 52,168,228\n"
"R 164,191,32768\nG 9\nv_mfma_f32_16x16x32_bf16 v[32:35], v[238:241], v[208:211], v[32:35]\nv_mul_f32 v237, v23, v237\nM 52,172,244\nbuffer_store_dwordx4 v[0:3], v252, s[20:23], s84 offen offset:0 sc1\n"
"M 36,168,224\nR 166,191,36864\nG 9\nv_mfma_f32_16x16x32_bf16 v[36:39], v[240:243], v[208:211], v[36:39]\nv_cvt_pk_bf16_f32 v18, v234, v235\nM 36,172,240\n"
"G 4\nv_cvt_pk_bf16_f32 v19, v236, v237\nM 40,176,224\nR 168,188,40960\nv_mfma_f32_16x16x32_bf16 v[40:43], v[242:245], v[208:211], v[40:43]\nbuffer_load_dwordx4 v221, s[16:19], s63 offen offset:1024 lds\n"
"buffer_store_dwordx4 v[16:19], v252, s[20:23], s85 offen offset:0 sc1\nM 40,180,240\ns_add_u32 m0, s50, 49152\ns_add_u32 s63, s56, 131072\nbuffer_load_dwordx4 v220, s[16:19], s63 offen lds\nM 56,176,228\n"
"R 170,188,45056\nv_mfma_f32_16x16x32_bf16 v[44:47], v[244:247], v[208:211], v[44:47]\nv_mov_b32 v248, 0\nM 56,180,244\nv_mov_b32 v249, 0\nM 72,176,232\n"
"R 172,188,49152\nv_mfma_f32_16x16x32_bf16 v[48:51], v[244:247], v[210:213], v[48:51]\ns_add_u32 s61, s82, 385032\ns_mov_b64 exec, 0xf\nbuffer_atomic_or_x2 v[248:249], v250, s[20:23], s61 offen sc0 sc1\ns_mov_b64 exec, -1\n"
"M 72,180,248\nbuffer_load_dwordx4 v221, s[16:19], s63 offen offset:1024 lds\nM 88,176,236\nR 174,188,53248\nv_mfma_f32_16x16x32_bf16 v[52:55], v[238:241], v[210:213], v[52:55]\ns_add_u32 m0, s50, 57344\n"
"M 88,180,252\nG 4\nM 92,160,236\nR 176,189,40960\nv_mfma_f32_16x16x32_bf16 v[56:59], v[240:243], v[210:213], v[56:59]\ns_add_u32 s63, s56, 655360\n"
"M 92,164,252\nbuffer_load_dwordx4 v220, s[16:19], s63 offen lds\nM 76,160,232\nR 178,189,45056\nv_mfma_f32_16x16x32_bf16 v[60:63], v[242:245], v[210:213], v[60:63]\nM 76,164,248\n"
"M 60,160,228\nR 180,189,49152\nM 60,164,244\nM 44,160,224\nR 182,189,53248\nM 44,164,240\n"
"s_barrier\nG 4\nv_mfma_f32_16x16x32_bf16 v[96:99], v[168:171], a[0:3], 0\nR 160,190,40960\nM 96,172,16\ns_mov_b32 m0, s51\n"
"v_mfma_f32_16x16x32_bf16 v[112:115], v[168:171], a[4:7], 0\nR 162,190,45056\nM 112,172,20\nds_write_addtid_b32 v64 offset:0\nv_mfma_f32_16x16x32_bf16 v[128:131], v[168:171], a[8:11], 0\nR 164,190,49152\n"
"M 128,172,24\nds_write_addtid_b32 v65 offset:256\nv_mfma_f32_16x16x32_bf16 v[144:147], v[168:171], a[12:15], 0\nR 166,190,53248\nds_write_addtid_b32 v66 offset:512\nM 144,172,28\n"
"ds_write_addtid_b32 v67 offset:768\nds_write_addtid_b32 v68 offset:1024\nG 9\nv_mfma_f32_16x16x32_bf16 v[148:151], v[176:179], a[12:15], 0\nR 168,191,40960\nds_write_addtid_b32 v69 offset:1280\n"
"M 148,180,28\nds_write_addtid_b32 v70 offset:1536\nds_write_addtid_b32 v71 offset:1792\nv_mfma_f32_16x16x32_bf16 v[132:135], v[176:179], a[8:11], 0\nR 170,191,45056\nds_write_addtid_b32 v72 offset:2048\n"
"M 132,180,24\nds_write_addtid_b32 v73 offset:2304\nds_write_addtid_b32 v74 offset:2560\nv_mfma_f32_16x16x32_bf16 v[116:119], v[176:179], a[4:7], 0\nR 172,191,49152\nds_write_addtid_b32 v75 offset:2816\n"
"M 116,180,20\nds_write_addtid_b32 v76 offset:3072\nds_write_addtid_b32 v77 offset:3328\nv_mfma_f32_16x16x32_bf16 v[100:103], v[176:179], a[0:3], 0\nR 174,191,53248\nds_write_addtid_b32 v78 offset:3584\n"
"M 100,180,16\nds_write_addtid_b32 v79 offset:3840\nds_write_addtid_b32 v80 offset:4096\nG 15\nv_mfma_f32_16x16x32_bf16 v[104:107], v[160:163], a[0:3], 0\nR 176,184,0\n"
"ds_write_addtid_b32 v81 offset:4352\nM 104,164,16\nds_write_addtid_b32 v82 offset:4608\nds_write_addtid_b32 v83 offset:4864\nv_mfma_f32_16x16x32_bf16 v[120:123], v[160:163], a[4:7], 0\nR 178,184,4096\n"
"ds_write_addtid_b32 v84 offset:5120\nM 120,164,20\nds_write_addtid_b32 v85 offset:5376\nds_write_addtid_b32 v86 offset:5632\nv_mfma_f32_16x16x32_bf16 v[136:139], v[160:163], a[8:11], 0\nR 180,184,8192\n"
"ds_write_addtid_b32 v87 offset:5888\nM 136,164,24\nds_write_addtid_b32 v88 offset:6144\nds_write_addtid_b32 v89 offset:6400\nv_mfma_f32_16x16x32_bf16 v[152:155], v[160:163], a[12:15], 0\nR 182,184,12288\n"
"ds_write_addtid_b32 v90 offset:6656\nM 152,164,28\nds_write_addtid_b32 v91 offset:6912\nds_write_addtid_b32 v92 offset:7168\nG 15\nv_mfma_f32_16x16x32_bf16 v[156:159], v[168:171], a[12:15], 0\n"
"R 160,185,0\nds_write_addtid_b32 v93 offset:7424\nM 156,172,28\nds_write_addtid_b32 v94 offset:7680\nds_write_addtid_b32 v95 offset:7936\nv_mfma_f32_16x16x32_bf16 v[140:143], v[168:171], a[8:11], 0\n"
"R 162,185,4096\nM 140,172,24\nv_mfma_f32_16x16x32_bf16 v[124:127], v[168:171], a[4:7], 0\nR 164,185,8192\ns_waitcnt vmcnt(5) lgkmcnt(2)\ns_barrier\n"
"M 124,172,20\nds_read2st64_b32 v[64:65], v225 offset0:0 offset1:1\nds_read2st64_b32 v[66:67], v225 offset0:2 offset1:3\nv_mfma_f32_16x16x32_bf16 v[108:111], v[168:171], a[0:3], 0\nR 166,185,12288\nds_read2st64_b32 v[68:69], v225 offset0:4 offset1:5\n"
"M 108,172,16\nds_read2st64_b32 v[70:71], v225 offset0:6 offset1:7\nds_read2st64_b32 v[72:73], v225 offset0:8 offset1:9\nM 96,176,32\nR 168,186,0\nds_read2st64_b32 v[74:75], v225 offset0:10 offset1:11\n"
"M 96,180,48\ns_add_u32 s64, s68, 23070720\nds_read2st64_b32 v[76:77], v225 offset0:12 offset1:13\nv_mov_b32 v226, s46\nM 112,176,36\nR 170,186,4096\n"
"ds_read2st64_b32 v[78:79], v225 offset0:14 offset1:15\nv_mov_b32 v227, s47\ns_waitcnt vmcnt(2)\nv_cmp_eq_u64 s[86:87], s[46:47], v[248:249]\nM 112,180,52\ns_add_u32 s61, s81, 385048\n"
"s_lshl_b32 s62, s44, 5\ns_add_u32 s61, s61, s62\ns_waitcnt vmcnt(0)\ns_mov_b64 exec, 1\nbuffer_atomic_swap_x2 v[226:227], off, s[20:23], s61 sc1\ns_mov_b64 exec, -1\n"
"ds_read2st64_b32 v[80:81], v225 offset0:16 offset1:17\nM 128,176,40\nR 172,186,8192\ns_and_b32 s62, s86, 0xf\ns_cmp_eq_u32 s62, 0xf\ns_cbranch_scc0 Lq412\n"
"Lq413:\nds_read2st64_b32 v[82:83], v225 offset0:18 offset1:19\nM 128,180,56\ns_add_u32 s65, s68, 23185408\nds_read2st64_b32 v[84:85], v225 offset0:20 offset1:21\nds_read2st64_b32 v[86:87], v225 offset0:22 offset1:23\n"
"M 144,176,44\nR 174,186,12288\ns_add_u32 m0, s50, 57344\nds_read2st64_b32 v[88:89], v225 offset0:24 offset1:25\nM 144,180,60\nds_read2st64_b32 v[90:91], v225 offset0:26 offset1:27\n"
"ds_read2st64_b32 v[92:93], v225 offset0:28 offset1:29\nG 15\nM 148,160,44\nR 176,187,0\nbuffer_load_dwordx4 v221, s[16:19], s63 offen offset:1024 lds\nds_read2st64_b32 v[94:95], v225 offset0:30 offset1:31\n"
"M 148,164,60\nbuffer_load_dwordx4 a[64:67], v252, s[20:23], s64 offen sc1\nv_add_f32 v32, v32, v64\nM 132,160,40\nR 178,187,4096\ns_add_u32 m0, s50, 65536\n"
"v_add_f32 v33, v33, v65\nM 132,164,56\ns_add_u32 s66, s69, 23070720\ns_add_u32 s63, s56, 196608\nv_add_f32 v34, v34, v66\nv_add_f32 v35, v35, v67\n"
"M 116,160,36\nR 180,187,8192\nbuffer_load_dwordx4 v220, s[16:19], s63 offen lds\nv_add_f32 v36, v36, v68\nM 116,164,52\nbuffer_load_dwordx4 a[68:71], v252, s[20:23], s65 offen sc1\n"
"v_add_f32 v37, v37, v69\nM 100,160,32\nR 182,187,12288\nv_add_f32 v38, v38, v70\nv_add_f32 v39, v39, v71\nM 100,164,48\n"
"G 8\nv_add_f32 v40, v40, v72\nv_add_f32 v41, v41, v73\nv_add_f32 v42, v42, v74\nM 104,168,32\nR 160,184,16384\n"
"buffer_load_dwordx4 v221, s[16:19], s63 offen offset:1024 lds\nv_add_f32 v43, v43, v75\nM 104,172,48\ns_add_u32 s67, s69, 23185408\nv_add_f32 v44, v44, v76\nv_add_f32 v45, v45, v77\n"
"v_add_f32 v46, v46, v78\nM 120,168,36\nR 162,184,20480\nv_add_f32 v47, v47, v79\nv_add_f32 v48, v48, v80\nM 120,172,52\n"
"buffer_load_dwordx4 a[72:75], v252, s[20:23], s66 offen sc1\nv_add_f32 v49, v49, v81\nM 136,168,40\nR 164,184,24576\nv_add_f32 v50, v50, v82\nv_add_f32 v51, v51, v83\n"
"M 136,172,56\nv_add_f32 v52, v52, v84\nv_add_f32 v53, v53, v85\nM 152,168,44\nR 166,184,28672\nv_add_f32 v54, v54, v86\n"
"M 152,172,60\nG 4\nv_add_f32 v55, v55, v87\nv_add_f32 v56, v56, v88\nM 156,176,44\nR 168,185,16384\n"
"v_add_f32 v57, v57, v89\nM 156,180,60\nbuffer_load_dwordx4 a[76:79], v252, s[20:23], s67 offen sc1\nM 140,176,40\nR 170,185,20480\nv_add_f32 v58, v58, v90\n"
"M 140,180,56\ns_add_u32 s64, s68, 23077888\nv_add_f32 v59, v59, v91\nv_add_f32 v60, v60, v92\nM 124,176,36\nR 172,185,24576\n"
"v_add_f32 v61, v61, v93\nM 124,180,52\ns_add_u32 s65, s68, 23192576\nv_add_f32 v62, v62, v94\nv_add_f32 v63, v63, v95\nM 108,176,32\n"
"R 174,185,28672\nv_mul_f32 v230, v32, v32\nv_mul_f32 v231, v33, v33\nM 108,180,48\ns_add_u32 s84, s73, 23072768\ns_add_u32 s85, s73, 23187456\n"
"G 4\nv_mul_f32 v232, v34, v34\nv_mfma_f32_16x16x32_bf16 v[0:3], v[160:163], a[0:3], 0\nR 176,186,16384\nLq577:\ns_lshr_b32 s60, s45, 3\n"
"s_lshl2_add_u32 s60, s60, s40\ns_lshl_b32 s60, s60, 8\nv_lshl_add_u32 v224, v252, 3, s60\nv_add_u32 v224, Lq577+4096-LqPC, v224\nv_min_u32 v224, LqE-LqPC-4, v224\ns_mov_b64 exec, 3\n"
"global_load_dword v224, v224, s[98:99]\ns_mov_b64 exec, -1\nv_mul_f32 v233, v35, v35\nM 0,164,16\nbuffer_load_dwordx4 a[80:83], v252, s[20:23], s64 offen sc1\nv_fma_f32 v230, v230, v215, v214\n"
"v_mfma_f32_16x16x32_bf16 v[16:19], v[160:163], a[4:7], 0\nR 178,186,20480\nv_fma_f32 v231, v231, v215, v214\nv_fma_f32 v232, v232, v215, v214\nM 16,164,20\ns_add_u32 s66, s69, 23077888\n"
"v_fma_f32 v233, v233, v215, v214\nv_mul_f32 v230, v32, v230\nv_mul_f32 v231, v33, v231\nv_mfma_f32_16x16x32_bf16 a[224:227], v[160:163], a[8:11], 0\nR 180,186,24576\nv_mul_f32 v232, v34, v232\n"
"v_mul_f32 v233, v35, v233\nv_mfma_f32_16x16x32_bf16 a[224:227], v[164:167], a[24:27], a[224:227]\nbuffer_load_dwordx4 a[84:87], v252, s[20:23], s65 offen sc1\nv_exp_f32 v230, v230\nv_mfma_f32_16x16x32_bf16 a[240:243], v[160:163], a[12:15], 0\nR 182,186,28672\n"
"v_fma_f32 v230, v230, v216, v216\nv_exp_f32 v231, v231\nv_mfma_f32_16x16x32_bf16 a[240:243], v[164:167], a[28:31], a[240:243]\nG 4\nv_exp_f32 v232, v232\nv_exp_f32 v233, v233\n"
"v_mfma_f32_16x16x32_bf16 a[244:247], v[168:171], a[12:15], 0\nR 160,187,16384\ns_add_u32 m0, s50, 73728\nv_fma_f32 v231, v231, v216, v216\nv_mfma_f32_16x16x32_bf16 a[244:247], v[172:175], a[28:31], a[244:247]\ns_add_u32 s67, s69, 23192576\n"
"v_rcp_f32 v230, v230\nv_fma_f32 v232, v232, v216, v216\nv_mfma_f32_16x16x32_bf16 a[228:231], v[168:171], a[8:11], 0\nLpg13:\ns_cbranch_execz LretO0n\nR 162,187,20480\n"
"s_add_u32 s63, s56, 720896\nv_fma_f32 v233, v233, v216, v216\nv_mfma_f32_16x16x32_bf16 a[228:231], v[172:175], a[24:27], a[228:231]\nbuffer_load_dwordx4 a[88:91], v252, s[20:23], s66 offen sc1\nv_rcp_f32 v231, v231\nv_mfma_f32_16x16x32_bf16 v[20:23], v[168:171], a[4:7], 0\n"
"R 164,187,24576\nv_mul_f32 v230, v32, v230\nv_rcp_f32 v232, v232\nM 20,172,20\nbuffer_load_dwordx4 v220, s[16:19], s63 offen lds\nv_rcp_f32 v233, v233\n"
"v_mfma_f32_16x16x32_bf16 v[4:7], v[168:171], a[0:3], 0\nR 166,187,28672\nv_mul_f32 v231, v33, v231\nv_mul_f32 v232, v34, v232\nM 4,172,16\nG 4\n"
"v_mul_f32 v233, v35, v233\nv_cvt_pk_bf16_f32 v32, v230, v231\nv_cvt_pk_bf16_f32 v33, v232, v233\nv_mfma_f32_16x16x32_bf16 v[8:11], v[176:179], a[0:3], 0\nR 168,184,32768\nv_mul_f32 v234, v36, v36\n"
"v_mul_f32 v235, v37, v37\nM 8,180,16\nbuffer_load_dwordx4 a[92:95], v252, s[20:23], s67 offen sc1\nbuffer_load_dwordx4 v221, s[16:19], s63 offen offset:1024 lds\nv_mul_f32 v236, v38, v38\nv_mfma_f32_16x16x32_bf16 v[24:27], v[176:179], a[4:7], 0\n"
"R 170,184,36864\ns_add_u32 m0, s50, 81920\nv_mul_f32 v237, v39, v39\nM 24,180,20\ns_add_u32 s64, s68, 23071744\nv_fma_f32 v234, v234, v215, v214\n"
"v_fma_f32 v235, v235, v215, v214\nv_fma_f32 v236, v236, v215, v214\nv_mfma_f32_16x16x32_bf16 a[232:235], v[176:179], a[8:11], 0\nR 172,184,40960\ns_add_u32 s63, s57, 131072\nv_fma_f32 v237, v237, v215, v214\n"
"v_mfma_f32_16x16x32_bf16 a[232:235], v[180:183], a[24:27], a[232:235]\ns_add_u32 s65, s68, 23186432\nbuffer_load_dwordx4 v220, s[16:19], s63 offen lds\nv_mul_f32 v234, v36, v234\nv_mfma_f32_16x16x32_bf16 a[248:251], v[176:179], a[12:15], 0\nR 174,184,45056\n"
"v_mul_f32 v235, v37, v235\nv_mul_f32 v236, v38, v236\nv_mfma_f32_16x16x32_bf16 a[248:251], v[180:183], a[28:31], a[248:251]\nG 4\nv_exp_f32 v234, v234\nv_mul_f32 v237, v39, v237\n"
"v_mfma_f32_16x16x32_bf16 a[252:255], v[160:163], a[12:15], 0\nR 176,185,32768\nv_fma_f32 v234, v234, v216, v216\nv_exp_f32 v235, v235\nv_mfma_f32_16x16x32_bf16 a[252:255], v[164:167], a[28:31], a[252:255]\nbuffer_load_dwordx4 a[96:99], v252, s[20:23], s64 offen sc1\n"
"buffer_load_dwordx4 v221, s[16:19], s63 offen offset:1024 lds\nv_exp_f32 v236, v236\nv_mfma_f32_16x16x32_bf16 a[236:239], v[160:163], a[8:11], 0\nR 178,185,36864\ns_add_u32 m0, s50, 90112\nv_exp_f32 v237, v237\n"
"v_mfma_f32_16x16x32_bf16 a[236:239], v[164:167], a[24:27], a[236:239]\ns_add_u32 s66, s69, 23071744\nv_rcp_f32 v234, v234\nv_fma_f32 v235, v235, v216, v216\nv_mfma_f32_16x16x32_bf16 v[28:31], v[160:163], a[4:7], 0\nR 180,185,40960\n"
"s_add_u32 s63, s57, 655360\nv_fma_f32 v236, v236, v216, v216\nM 28,164,20\nbuffer_load_dwordx4 a[100:103], v252, s[20:23], s65 offen sc1\nbuffer_load_dwordx4 v220, s[16:19], s63 offen lds\nv_fma_f32 v237, v237, v216, v216\n"
"v_mfma_f32_16x16x32_bf16 v[12:15], v[160:163], a[0:3], 0\nR 182,185,45056\nv_mul_f32 v234, v36, v234\nv_rcp_f32 v235, v235\nM 12,164,16\ns_waitcnt vmcnt(14)\n"
"s_barrier\nG 4\nv_rcp_f32 v236, v236\nM 0,168,32\nR 160,186,32768\nv_mul_f32 v235, v37, v235\n"
"v_rcp_f32 v237, v237\nM 0,172,48\ns_add_u32 s67, s69, 23186432\nbuffer_load_dwordx4 v221, s[16:19], s63 offen offset:1024 lds\nv_mul_f32 v236, v38, v236\nM 16,168,36\n"
"R 162,186,36864\nv_mul_f32 v237, v39, v237\nv_cvt_pk_bf16_f32 v34, v234, v235\nM 16,172,52\nbuffer_load_dwordx4 a[104:107], v252, s[20:23], s66 offen sc1\nv_cvt_pk_bf16_f32 v35, v236, v237\n"
"v_mfma_f32_16x16x32_bf16 a[224:227], v[168:171], a[40:43], a[224:227]\nR 164,186,40960\nv_mul_f32 v230, v40, v40\nv_mul_f32 v231, v41, v41\nv_mfma_f32_16x16x32_bf16 a[224:227], v[172:175], a[56:59], a[224:227]\nv_mul_f32 v232, v42, v42\n"
"v_mul_f32 v233, v43, v43\nv_fma_f32 v230, v230, v215, v214\nv_mfma_f32_16x16x32_bf16 a[240:243], v[168:171], a[44:47], a[240:243]\nR 166,186,45056\nv_fma_f32 v231, v231, v215, v214\nv_fma_f32 v232, v232, v215, v214\n"
"v_mfma_f32_16x16x32_bf16 a[240:243], v[172:175], a[60:63], a[240:243]\nG 4\nv_fma_f32 v233, v233, v215, v214\nv_mul_f32 v230, v40, v230\nv_mul_f32 v231, v41, v231\nv_mfma_f32_16x16x32_bf16 a[244:247], v[176:179], a[44:47], a[244:247]\n"
"R 168,187,32768\nv_mul_f32 v232, v42, v232\nv_mul_f32 v233, v43, v233\nv_mfma_f32_16x16x32_bf16 a[244:247], v[180:183], a[60:63], a[244:247]\nbuffer_load_dwordx4 a[108:111], v252, s[20:23], s67 offen sc1\nv_exp_f32 v230, v230\n"
"v_mfma_f32_16x16x32_bf16 a[228:231], v[176:179], a[40:43], a[228:231]\nR 170,187,36864\nv_fma_f32 v230, v230, v216, v216\nv_exp_f32 v231, v231\nv_mfma_f32_16x16x32_bf16 a[228:231], v[180:183], a[56:59], a[228:231]\ns_add_u32 s64, s68, 23078912\n"
"v_exp_f32 v232, v232\nv_exp_f32 v233, v233\nM 20,176,36\nR 172,187,40960\nv_fma_f32 v231, v231, v216, v216\nv_fma_f32 v232, v232, v216, v216\n"
"M 20,180,52\ns_add_u32 s65, s68, 23193600\nv_rcp_f32 v230, v230\nv_fma_f32 v233, v233, v216, v216\nM 4,176,32\nR 174,187,45056\n"
"s_add_u32 m0, s50, 98304\nv_rcp_f32 v231, v231\nM 4,180,48\nG 4\nv_rcp_f32 v232, v232\nv_rcp_f32 v233, v233\n"
"M 8,160,32\nR 176,184,49152\ns_add_u32 s63, s57, 196608\nv_mul_f32 v230, v40, v230\nM 8,164,48\nbuffer_load_dwordx4 a[112:115], v252, s[20:23], s64 offen sc1\n"
"v_mul_f32 v231, v41, v231\nM 24,160,36\nR 178,184,53248\nv_mul_f32 v232, v42, v232\nv_mul_f32 v233, v43, v233\nM 24,164,52\n"
"s_add_u32 s66, s69, 23078912\nbuffer_load_dwordx4 v220, s[16:19], s63 offen lds\nv_cvt_pk_bf16_f32 v40, v230, v231\nv_mfma_f32_16x16x32_bf16 a[232:235], v[160:163], a[40:43], a[232:235]\nR 180,184,57344\nv_cvt_pk_bf16_f32 v41, v232, v233\n"
"v_mul_f32 v234, v44, v44\nv_mfma_f32_16x16x32_bf16 a[232:235], v[164:167], a[56:59], a[232:235]\nbuffer_load_dwordx4 a[116:119], v252, s[20:23], s65 offen sc1\nv_mul_f32 v235, v45, v45\nv_mfma_f32_16x16x32_bf16 a[248:251], v[160:163], a[44:47], a[248:251]\nR 182,184,61440\n"
"v_mul_f32 v236, v46, v46\nv_mul_f32 v237, v47, v47\nv_mfma_f32_16x16x32_bf16 a[248:251], v[164:167], a[60:63], a[248:251]\nbuffer_load_dwordx4 v221, s[16:19], s63 offen offset:1024 lds\nG 4\nv_fma_f32 v234, v234, v215, v214\n"
"v_mfma_f32_16x16x32_bf16 a[252:255], v[168:171], a[44:47], a[252:255]\nR 160,185,49152\ns_add_u32 m0, s50, 106496\nv_fma_f32 v235, v235, v215, v214\nv_mfma_f32_16x16x32_bf16 a[252:255], v[172:175], a[60:63], a[252:255]\ns_add_u32 s67, s69, 23193600\n"
"s_add_u32 s63, s57, 720896\nv_fma_f32 v236, v236, v215, v214\nv_fma_f32 v237, v237, v215, v214\nv_mfma_f32_16x16x32_bf16 a[236:239], v[168:171], a[40:43], a[236:239]\nR 162,185,53248\nv_mov_b32 v248, 0\n"
"v_mul_f32 v234, v44, v234\nv_mfma_f32_16x16x32_bf16 a[236:239], v[172:175], a[56:59], a[236:239]\nbuffer_load_dwordx4 a[120:123], v252, s[20:23], s66 offen sc1\nbuffer_load_dwordx4 v220, s[16:19], s63 offen lds\nv_mov_b32 v249, 0\nv_mul_f32 v235, v45, v235\n"
"M 28,168,36\nR 164,185,57344\ns_add_u32 s61, s82, 385048\ns_mov_b64 exec, 0xf\nbuffer_atomic_or_x2 v[248:249], v250, s[20:23], s61 offen sc0 sc1\ns_mov_b64 exec, -1\n"
"v_mul_f32 v236, v46, v236\nM 28,172,52\nbuffer_load_dwordx4 v221, s[16:19], s63 offen offset:1024 lds\nv_mul_f32 v237, v47, v237\nM 12,168,32\nR 166,185,61440\n"
"s_add_u32 m0, s50, 114688\nv_exp_f32 v234, v234\nM 12,172,48\nG 4\nv_exp_f32 v235, v235\nv_exp_f32 v236, v236\n"
"M 96,176,64\nR 168,186,49152\ns_add_u32 s63, s58, 0\nv_exp_f32 v237, v237\ns_waitcnt vmcnt(21)\nM 96,180,80\n"
"buffer_load_dwordx4 a[124:127], v252, s[20:23], s67 offen sc1\nv_fma_f32 v234, v234, v216, v216\nM 112,176,68\nR 170,186,53248\nv_fma_f32 v235, v235, v216, v216\nv_fma_f32 v236, v236, v216, v216\n"
"s_waitcnt vmcnt(21)\nM 112,180,84\nbuffer_load_dwordx4 v220, s[16:19], s63 offen lds\nv_fma_f32 v237, v237, v216, v216\nM 128,176,72\nR 172,186,57344\n"
"v_mul_f32 v230, v48, v48\nv_rcp_f32 v234, v234\ns_waitcnt vmcnt(21)\nM 128,180,88\nbuffer_load_dwordx4 v221, s[16:19], s63 offen offset:1024 lds\nv_rcp_f32 v235, v235\n"
"M 144,176,76\nR 174,186,61440\ns_add_u32 m0, s50, 0\nv_rcp_f32 v236, v236\ns_waitcnt vmcnt(20)\nM 144,180,92\n"
"G 4\nv_rcp_f32 v237, v237\nv_mul_f32 v234, v44, v234\nM 148,160,76\nR 176,187,49152\ns_add_u32 s63, s58, 524288\n"
"v_mul_f32 v235, v45, v235\nM 148,164,92\nbuffer_load_dwordx4 v220, s[16:19], s63 offen lds\nv_mul_f32 v236, v46, v236\nM 132,160,72\nR 178,187,53248\n"
"v_mul_f32 v237, v47, v237\nv_cvt_pk_bf16_f32 v42, v234, v235\nM 132,164,88\nbuffer_store_dwordx4 v[32:35], v252, s[20:23], s84 offen offset:0 sc1\nv_cvt_pk_bf16_f32 v43, v236, v237\nM 116,160,68\n"
"R 180,187,57344\nv_mul_f32 v231, v49, v49\nv_mul_f32 v232, v50, v50\nM 116,164,84\nbuffer_load_dwordx4 v221, s[16:19], s63 offen offset:1024 lds\nv_mul_f32 v233, v51, v51\n"
"M 100,160,64\nR 182,187,61440\nv_fma_f32 v230, v230, v218, v217\nv_fma_f32 v231, v231, v218, v217\nM 100,164,80\nG 4\n"
"v_fma_f32 v232, v232, v218, v217\nv_fma_f32 v233, v233, v218, v217\nv_mul_f32 v230, v48, v230\nM 104,168,64\nR 160,188,0\ns_waitcnt vmcnt(16)\n"
"s_barrier\nv_mul_f32 v231, v49, v231\nM 104,172,80\nbuffer_store_dwordx4 v[40:43], v252, s[20:23], s84 offen offset:1024 sc1\nv_mul_f32 v232, v50, v232\nM 120,168,68\n"
"R 162,188,4096\nv_mul_f32 v233, v51, v233\nv_exp_f32 v230, v230\nM 120,172,84\ns_add_u32 m0, s50, 8192\nv_exp_f32 v231, v231\n"
"v_exp_f32 v232, v232\nM 136,168,72\nR 164,188,8192\nv_fma_f32 v230, v230, v219, v219\nv_exp_f32 v233, v233\nM 136,172,88\n"
"s_add_u32 s63, s58, 65536\nv_rcp_f32 v230, v230\nv_fma_f32 v231, v231, v219, v219\nM 152,168,76\nR 166,188,12288\nv_fma_f32 v232, v232, v219, v219\n"
"v_fma_f32 v233, v233, v219, v219\nM 152,172,92\nG 4\nv_rcp_f32 v231, v231\nv_rcp_f32 v232, v232\nM 156,176,76\n"
"R 168,189,0\nv_mul_f32 v230, v48, v230\nv_rcp_f32 v233, v233\nM 156,180,92\nbuffer_load_dwordx4 v220, s[16:19], s63 offen lds\nv_mul_f32 v231, v49, v231\n"
"M 140,176,72\nR 170,189,4096\nv_mul_f32 v232, v50, v232\nv_mul_f32 v233, v51, v233\nM 140,180,88\nv_cvt_pk_bf16_f32 v48, v230, v231\n"
"v_cvt_pk_bf16_f32 v49, v232, v233\nv_mul_f32 v234, v52, v52\nM 124,176,68\nR 172,189,8192\nv_mul_f32 v235, v53, v53\nv_mul_f32 v236, v54, v54\n"
"M 124,180,84\nbuffer_load_dwordx4 v221, s[16:19], s63 offen offset:1024 lds\nv_mul_f32 v237, v55, v55\nM 108,176,64\nR 174,189,12288\ns_add_u32 m0, s50, 16384\n"
"v_fma_f32 v234, v234, v218, v217\nM 108,180,80\nG 4\nv_fma_f32 v235, v235, v218, v217\nv_fma_f32 v236, v236, v218, v217\nv_fma_f32 v237, v237, v218, v217\n"
"M 96,160,96\nR 176,190,0\ns_add_u32 s63, s58, 589824\nv_mul_f32 v234, v52, v234\ns_waitcnt vmcnt(16)\nM 96,164,112\n"
"buffer_load_dwordx4 v220, s[16:19], s63 offen lds\nv_mul_f32 v235, v53, v235\nM 112,160,100\nR 178,190,4096\nv_mul_f32 v236, v54, v236\nv_mul_f32 v237, v55, v237\n"
"s_waitcnt vmcnt(15)\nM 112,164,116\nv_exp_f32 v234, v234\nv_exp_f32 v235, v235\nM 128,160,104\nR 180,190,8192\n"
"v_fma_f32 v234, v234, v219, v219\nv_exp_f32 v236, v236\ns_waitcnt vmcnt(13)\nM 128,164,120\nv_exp_f32 v237, v237\nv_fma_f32 v235, v235, v219, v219\n"
"M 144,160,108\nR 182,190,12288\nv_fma_f32 v236, v236, v219, v219\nv_fma_f32 v237, v237, v219, v219\ns_waitcnt vmcnt(9)\nM 144,164,124\n"
"G 4\nv_rcp_f32 v234, v234\nv_rcp_f32 v235, v235\nM 148,168,108\nR 160,191,0\nv_mul_f32 v234, v52, v234\n"
"v_rcp_f32 v236, v236\nM 148,172,124\ns_add_u32 s64, s68, 23074816\nv_rcp_f32 v237, v237\nv_mul_f32 v235, v53, v235\nM 132,168,104\n"
"R 162,191,4096\nv_cmp_eq_u64 s[86:87], s[46:47], v[248:249]\nv_mul_f32 v236, v54, v236\nM 132,172,120\nbuffer_load_dwordx4 v221, s[16:19], s63 offen offset:1024 lds\nv_mul_f32 v237, v55, v237\n"
"M 116,168,100\nR 164,191,8192\ns_and_b32 s62, s86, 0xf\ns_cmp_eq_u32 s62, 0xf\ns_cbranch_scc0 Lq447\nLq448:\n"
"v_cvt_pk_bf16_f32 v50, v234, v235\nM 116,172,116\ns_add_u32 s65, s68, 23189504\nv_cvt_pk_bf16_f32 v51, v236, v237\nv_mul_f32 v230, v56, v56\nv_mul_f32 v231, v57, v57\n"
"M 100,168,96\nR 166,191,12288\nv_mul_f32 v232, v58, v58\nv_mul_f32 v233, v59, v59\nM 100,172,112\nG 4\n"
"v_fma_f32 v230, v230, v218, v217\nv_fma_f32 v231, v231, v218, v217\nM 104,176,96\nR 168,188,16384\nv_fma_f32 v232, v232, v218, v217\nM 104,180,112\n"
"buffer_load_dwordx4 a[128:131], v252, s[20:23], s64 offen sc1\nM 120,176,100\nR 170,188,20480\nv_fma_f32 v233, v233, v218, v217\nM 120,180,116\ns_add_u32 s66, s69, 23074816\n"
"v_mul_f32 v230, v56, v230\nv_mul_f32 v231, v57, v231\nM 136,176,104\nR 172,188,24576\nv_mul_f32 v232, v58, v232\nM 136,180,120\n"
"buffer_load_dwordx4 a[132:135], v252, s[20:23], s65 offen sc1\nM 152,176,108\nR 174,188,28672\nv_mul_f32 v233, v59, v233\nM 152,180,124\nG 4\n"
"v_exp_f32 v230, v230\nM 156,160,108\nR 176,189,16384\nv_fma_f32 v230, v230, v219, v219\nM 156,164,124\ns_add_u32 s67, s69, 23189504\n"
"v_exp_f32 v231, v231\nM 140,160,104\nR 178,189,20480\nv_fma_f32 v231, v231, v219, v219\nM 140,164,120\nbuffer_load_dwordx4 a[136:139], v252, s[20:23], s66 offen sc1\n"
"M 124,160,100\nR 180,189,24576\nv_mul_f32 v234, v60, v60\nM 124,164,116\nbuffer_store_dwordx4 v[48:51], v252, s[20:23], s85 offen offset:0 sc1\nM 108,160,96\n"
"R 182,189,28672\ns_add_u32 m0, s50, 24576\nM 108,164,112\ns_waitcnt vmcnt(9)\ns_barrier\nG 4\n"
"M 0,168,64\nR 160,190,16384\ns_add_u32 s63, s58, 131072\nM 0,172,80\nbuffer_load_dwordx4 a[140:143], v252, s[20:23], s67 offen sc1\nM 16,168,68\n"
"R 162,190,20480\nv_mul_f32 v235, v61, v61\nM 16,172,84\ns_add_u32 s64, s68, 23081984\nv_exp_f32 v232, v232\nv_mfma_f32_16x16x32_bf16 a[224:227], v[168:171], a[72:75], a[224:227]\n"
"R 164,190,24576\nbuffer_load_dwordx4 v220, s[16:19], s63 offen lds\nv_mfma_f32_16x16x32_bf16 a[224:227], v[172:175], a[88:91], a[224:227]\ns_add_u32 s65, s68, 23196672\nv_exp_f32 v233, v233\nv_mfma_f32_16x16x32_bf16 a[240:243], v[168:171], a[76:79], a[240:243]\n"
"R 166,190,28672\nv_fma_f32 v232, v232, v219, v219\nv_mfma_f32_16x16x32_bf16 a[240:243], v[172:175], a[92:95], a[240:243]\nG 4\nv_rcp_f32 v230, v230\nv_mfma_f32_16x16x32_bf16 a[244:247], v[176:179], a[76:79], a[244:247]\n"
"R 168,191,16384\nbuffer_load_dwordx4 v221, s[16:19], s63 offen offset:1024 lds\nv_mfma_f32_16x16x32_bf16 a[244:247], v[180:183], a[92:95], a[244:247]\nbuffer_load_dwordx4 a[144:147], v252, s[20:23], s64 offen sc1\nv_mfma_f32_16x16x32_bf16 a[228:231], v[176:179], a[72:75], a[228:231]\nR 170,191,20480\n"
"s_add_u32 m0, s50, 32768\nv_mfma_f32_16x16x32_bf16 a[228:231], v[180:183], a[88:91], a[228:231]\ns_add_u32 s66, s69, 23081984\ns_add_u32 s63, s58, 655360\nv_fma_f32 v233, v233, v219, v219\nM 20,176,68\n"
"R 172,191,24576\nbuffer_load_dwordx4 v220, s[16:19], s63 offen lds\nM 20,180,84\nbuffer_load_dwordx4 a[148:151], v252, s[20:23], s65 offen sc1\nM 4,176,64\nR 174,191,28672\n"
"v_mul_f32 v230, v56, v230\nM 4,180,80\nG 4\nv_rcp_f32 v231, v231\nM 8,160,64\nR 176,188,32768\n"
"buffer_load_dwordx4 v221, s[16:19], s63 offen offset:1024 lds\nM 8,164,80\ns_add_u32 s67, s69, 23196672\nv_rcp_f32 v232, v232\nM 24,160,68\nR 178,188,36864\n"
"v_mul_f32 v231, v57, v231\nM 24,164,84\nbuffer_load_dwordx4 a[152:155], v252, s[20:23], s66 offen sc1\nv_mfma_f32_16x16x32_bf16 a[232:235], v[160:163], a[72:75], a[232:235]\nR 180,188,40960\nv_mul_f32 v232, v58, v232\n"
"v_mfma_f32_16x16x32_bf16 a[232:235], v[164:167], a[88:91], a[232:235]\nv_rcp_f32 v233, v233\nv_mfma_f32_16x16x32_bf16 a[248:251], v[160:163], a[76:79], a[248:251]\nR 182,188,45056\nv_mul_f32 v233, v59, v233\nv_mfma_f32_16x16x32_bf16 a[248:251], v[164:167], a[92:95], a[248:251]\n"
"G 4\nv_cvt_pk_bf16_f32 v56, v230, v231\nv_cvt_pk_bf16_f32 v57, v232, v233\nv_mfma_f32_16x16x32_bf16 a[252:255], v[168:171], a[76:79], a[252:255]\nR 160,189,32768\nv_mul_f32 v236, v62, v62\n"
"v_mfma_f32_16x16x32_bf16 a[252:255], v[172:175], a[92:95], a[252:255]\nbuffer_load_dwordx4 a[156:159], v252, s[20:23], s67 offen sc1\nv_mfma_f32_16x16x32_bf16 a[236:239], v[168:171], a[72:75], a[236:239]\nR 162,189,36864\nv_mul_f32 v237, v63, v63\nv_mfma_f32_16x16x32_bf16 a[236:239], v[172:175], a[88:91], a[236:239]\n"
"v_fma_f32 v234, v234, v218, v217\nv_fma_f32 v235, v235, v218, v217\nM 28,168,68\nR 164,189,40960\nv_fma_f32 v236, v236, v218, v217\nM 28,172,84\n"
"s_add_u32 m0, s50, 40960\nv_fma_f32 v237, v237, v218, v217\nv_mul_f32 v234, v60, v234\nM 12,168,64\nR 166,189,45056\nv_mul_f32 v235, v61, v235\n"
"M 12,172,80\nG 4\nv_exp_f32 v234, v234\nM 0,176,96\nR 168,190,32768\nv_mul_f32 v236, v62, v236\n"
"M 0,180,112\ns_add_u32 s63, s58, 196608\nv_exp_f32 v235, v235\nM 16,176,100\nR 170,190,36864\nv_mul_f32 v237, v63, v237\n"
"M 16,180,116\nbuffer_load_dwordx4 v220, s[16:19], s63 offen lds\nv_mfma_f32_16x16x32_bf16 a[224:227], v[176:179], a[104:107], a[224:227]\nR 172,190,40960\nv_fma_f32 v234, v234, v219, v219\nv_mfma_f32_16x16x32_bf16 a[224:227], v[180:183], a[120:123], a[224:227]\n"
"v_exp_f32 v236, v236\nv_mfma_f32_16x16x32_bf16 a[240:243], v[176:179], a[108:111], a[240:243]\nR 174,190,45056\nv_fma_f32 v235, v235, v219, v219\nv_mfma_f32_16x16x32_bf16 a[240:243], v[180:183], a[124:127], a[240:243]\nG 4\n"
"v_exp_f32 v237, v237\nv_mfma_f32_16x16x32_bf16 a[244:247], v[160:163], a[108:111], a[244:247]\nR 176,191,32768\nv_fma_f32 v236, v236, v219, v219\nv_mfma_f32_16x16x32_bf16 a[244:247], v[164:167], a[124:127], a[244:247]\nbuffer_load_dwordx4 v221, s[16:19], s63 offen offset:1024 lds\n"
"v_mfma_f32_16x16x32_bf16 a[228:231], v[160:163], a[104:107], a[228:231]\nR 178,191,36864\ns_add_u32 s63, s58, 720896\nv_mfma_f32_16x16x32_bf16 a[228:231], v[164:167], a[120:123], a[228:231]\ns_add_u32 m0, s50, 49152\nv_rcp_f32 v234, v234\n"
"M 20,160,100\nR 180,191,40960\nv_fma_f32 v237, v237, v219, v219\nM 20,164,116\nbuffer_load_dwordx4 v220, s[16:19], s63 offen lds\nM 4,160,96\n"
"R 182,191,45056\nv_mul_f32 v234, v60, v234\nM 4,164,112\nG 4\nv_rcp_f32 v235, v235\nM 8,168,96\n"
"R 160,188,49152\nv_mul_f32 v235, v61, v235\nM 8,172,112\nbuffer_load_dwordx4 v221, s[16:19], s63 offen offset:1024 lds\nM 24,168,100\nR 162,188,53248\n"
"s_add_u32 s63, s58, 393216\nM 24,172,116\ns_add_u32 m0, s50, 57344\nv_rcp_f32 v236, v236\nv_mfma_f32_16x16x32_bf16 a[232:235], v[168:171], a[104:107], a[232:235]\nLpg14:\n"
"s_cbranch_execz LretO0n\nR 164,184,0\nv_mul_f32 v236, v62, v236\nv_mfma_f32_16x16x32_bf16 a[232:235], v[172:175], a[120:123], a[232:235]\nbuffer_load_dwordx4 v220, s[16:19], s63 offen lds\nv_mfma_f32_16x16x32_bf16 a[248:251], v[168:171], a[108:111], a[248:251]\n"
"R 166,184,4096\nv_cvt_pk_bf16_f32 v58, v234, v235\nv_mfma_f32_16x16x32_bf16 a[248:251], v[172:175], a[124:127], a[248:251]\nG 4\nv_rcp_f32 v237, v237\nv_mfma_f32_16x16x32_bf16 a[252:255], v[176:179], a[108:111], a[252:255]\n"
"R 168,189,49152\nv_mul_f32 v237, v63, v237\nv_mfma_f32_16x16x32_bf16 a[252:255], v[180:183], a[124:127], a[252:255]\nbuffer_load_dwordx4 v221, s[16:19], s63 offen offset:1024 lds\nv_mfma_f32_16x16x32_bf16 a[236:239], v[176:179], a[104:107], a[236:239]\nR 170,189,53248\n"
"s_add_u32 s63, s58, 458752\nv_mfma_f32_16x16x32_bf16 a[236:239], v[180:183], a[120:123], a[236:239]\ns_add_u32 m0, s50, 65536\nv_cvt_pk_bf16_f32 v59, v236, v237\nbuffer_store_dwordx4 v[56:59], v252, s[20:23], s85 offen offset:1024 sc1\nM 28,176,100\n"
"R 172,185,0\nM 28,180,116\nbuffer_load_dwordx4 v220, s[16:19], s63 offen lds\nM 12,176,96\nR 174,185,4096\nM 12,180,112\n"
"s_waitcnt vmcnt(10)\ns_barrier\nG 4\nv_mfma_f32_16x16x32_bf16 v[32:35], v[160:163], a[0:3], 0\nR 176,190,49152\nM 32,164,16\n"
"buffer_load_dwordx4 v221, s[16:19], s63 offen offset:1024 lds\nv_mfma_f32_16x16x32_bf16 v[48:51], v[160:163], a[4:7], 0\nR 178,190,53248\nM 48,164,20\nv_mfma_f32_16x16x32_bf16 v[64:67], v[160:163], a[8:11], 0\nR 180,186,0\n"
"M 64,164,24\nv_mfma_f32_16x16x32_bf16 v[80:83], v[160:163], a[12:15], 0\nR 182,186,4096\nM 80,164,28\nG 4\nv_mfma_f32_16x16x32_bf16 v[84:87], v[168:171], a[12:15], 0\n"
"R 160,191,49152\nM 84,172,28\nv_mfma_f32_16x16x32_bf16 v[68:71], v[168:171], a[8:11], 0\nR 162,191,53248\nM 68,172,24\nv_mfma_f32_16x16x32_bf16 v[52:55], v[168:171], a[4:7], 0\n"
"R 164,187,0\nM 52,172,20\nv_mfma_f32_16x16x32_bf16 v[36:39], v[168:171], a[0:3], 0\nR 166,187,4096\nM 36,172,16\nG 4\n"
"v_mfma_f32_16x16x32_bf16 v[40:43], v[176:179], a[0:3], 0\nR 168,184,8192\nM 40,180,16\nv_mfma_f32_16x16x32_bf16 v[56:59], v[176:179], a[4:7], 0\nR 170,184,12288\nM 56,180,20\n"
"v_mfma_f32_16x16x32_bf16 v[72:75], v[176:179], a[8:11], 0\nR 172,184,16384\nM 72,180,24\nv_mfma_f32_16x16x32_bf16 v[88:91], v[176:179], a[12:15], 0\nR 174,184,20480\nM 88,180,28\n"
"G 4\nv_mfma_f32_16x16x32_bf16 v[92:95], v[160:163], a[12:15], 0\nR 176,185,8192\nM 92,164,28\nv_mfma_f32_16x16x32_bf16 v[76:79], v[160:163], a[8:11], 0\nR 178,185,12288\n"
"M 76,164,24\nv_mfma_f32_16x16x32_bf16 v[60:63], v[160:163], a[4:7], 0\nR 180,185,16384\nM 60,164,20\nv_mfma_f32_16x16x32_bf16 v[44:47], v[160:163], a[0:3], 0\nR 182,185,20480\n"
"M 44,164,16\nG 4\nM 32,168,32\nR 160,186,8192\nM 32,172,48\nM 48,168,36\n"
"R 162,186,12288\nM 48,172,52\nM 64,168,40\nR 164,186,16384\nM 64,172,56\nM 80,168,44\n"
"R 166,186,20480\nM 80,172,60\nG 4\nM 84,176,44\nR 168,187,8192\nM 84,180,60\n"
"M 68,176,40\nR 170,187,12288\nv_mov_b32 v226, s46\nM 68,180,56\ns_add_u32 m0, s50, 73728\nv_mov_b32 v227, s47\n"
"M 52,176,36\nR 172,187,16384\ns_add_u32 s61, s81, 385040\ns_lshl_b32 s62, s44, 5\ns_add_u32 s61, s61, s62\ns_waitcnt vmcnt(0)\n"
"s_mov_b64 exec, 1\nbuffer_atomic_swap_x2 v[226:227], off, s[20:23], s61 sc1\ns_mov_b64 exec, -1\nM 52,180,52\ns_add_u32 s63, s56, 393216\nM 36,176,32\n"
"R 174,187,20480\nM 36,180,48\nG 4\nM 40,160,32\nR 176,184,24576\nM 40,164,48\n"
"buffer_load_dwordx4 v220, s[16:19], s63 offen lds\nM 56,160,36\nR 178,184,28672\nM 56,164,52\nM 72,160,40\nR 180,184,32768\n"
"M 72,164,56\nbuffer_load_dwordx4 v221, s[16:19], s63 offen offset:1024 lds\nM 88,160,44\nR 182,184,36864\ns_add_u32 m0, s50, 81920\nM 88,164,60\n"
"G 4\nM 92,168,44\nR 160,185,24576\ns_add_u32 s63, s56, 458752\nM 92,172,60\nbuffer_load_dwordx4 v220, s[16:19], s63 offen lds\n"
"M 76,168,40\nR 162,185,28672\nM 76,172,56\nM 60,168,36\nR 164,185,32768\nM 60,172,52\n"
"buffer_load_dwordx4 v221, s[16:19], s63 offen offset:1024 lds\nM 44,168,32\nR 166,185,36864\ns_add_u32 m0, s50, 90112\nM 44,172,48\ns_barrier\n"
"G 4\nM 32,176,64\nR 168,186,24576\ns_add_u32 s63, s57, 393216\nM 32,180,80\nbuffer_load_dwordx4 v220, s[16:19], s63 offen lds\n"
"M 48,176,68\nR 170,186,28672\nM 48,180,84\nM 64,176,72\nR 172,186,32768\nM 64,180,88\n"
"M 80,176,76\nR 174,186,36864\nM 80,180,92\nG 4\nM 84,160,76\nR 176,187,24576\n"
"M 84,164,92\nbuffer_load_dwordx4 v221, s[16:19], s63 offen offset:1024 lds\nM 68,160,72\nR 178,187,28672\nM 68,164,88\ns_add_u32 m0, s50, 98304\n"
"M 52,160,68\nR 180,187,32768\nv_mov_b32 v248, 0\nM 52,164,84\ns_add_u32 s63, s57, 458752\nv_mov_b32 v249, 0\n"
"M 36,160,64\nR 182,187,36864\ns_add_u32 s61, s82, 385040\ns_mov_b64 exec, 0xf\nbuffer_atomic_or_x2 v[248:249], v250, s[20:23], s61 offen sc0 sc1\ns_mov_b64 exec, -1\n"
"M 36,164,80\nG 4\nM 40,168,64\nR 160,184,40960\nM 40,172,80\nbuffer_load_dwordx4 v220, s[16:19], s63 offen lds\n"
"M 56,168,68\nR 162,184,45056\nM 56,172,84\nM 72,168,72\nR 164,184,49152\nM 72,172,88\n"
"buffer_load_dwordx4 v221, s[16:19], s63 offen offset:1024 lds\nM 88,168,76\nR 166,184,53248\ns_add_u32 s63, s56, 262144\nM 88,172,92\nG 4\n"
"M 92,176,76\nR 168,185,40960\nM 92,180,92\ns_add_u32 m0, s50, 106496\nM 76,176,72\nR 170,185,45056\n"
"M 76,180,88\nbuffer_load_dwordx4 v220, s[16:19], s63 offen lds\nM 60,176,68\nR 172,185,49152\nM 60,180,84\nM 44,176,64\n"
"R 174,185,53248\nM 44,180,80\nG 4\nM 32,160,96\nR 176,186,40960\nM 32,164,112\n"
"buffer_load_dwordx4 v221, s[16:19], s63 offen offset:1024 lds\nM 48,160,100\nR 178,186,45056\ns_add_u32 s63, s56, 786432\nM 48,164,116\ns_add_u32 m0, s50, 114688\n"
"M 64,160,104\nR 180,186,49152\nM 64,164,120\nbuffer_load_dwordx4 v220, s[16:19], s63 offen lds\nM 80,160,108\nR 182,186,53248\n"
"M 80,164,124\nG 4\nM 84,168,108\nR 160,187,40960\nM 84,172,124\nbuffer_load_dwordx4 v221, s[16:19], s63 offen offset:1024 lds\n"
"M 68,168,104\nR 162,187,45056\ns_add_u32 s63, s56, 327680\nM 68,172,120\ns_add_u32 m0, s50, 0\nM 52,168,100\n"
"R 164,187,49152\nM 52,172,116\nbuffer_load_dwordx4 v220, s[16:19], s63 offen lds\nM 36,168,96\nR 166,187,53248\nM 36,172,112\n"
"G 4\nM 40,176,96\nR 168,184,57344\nM 40,180,112\nbuffer_load_dwordx4 v221, s[16:19], s63 offen offset:1024 lds\nM 56,176,100\n"
"R 170,184,61440\nM 56,180,116\nM 72,176,104\nR 172,188,0\nM 72,180,120\ns_add_u32 m0, s50, 8192\n"
"M 88,176,108\nR 174,188,4096\ns_waitcnt vmcnt(11)\ns_barrier\nM 88,180,124\nG 4\n"
"M 92,160,108\nR 176,185,57344\ns_add_u32 s63, s56, 851968\nM 92,164,124\ns_add_u32 s64, s68, 23072768\nM 76,160,104\n"
"R 178,185,61440\nM 76,164,120\nbuffer_load_dwordx4 v220, s[16:19], s63 offen lds\nM 60,160,100\nR 180,189,0\ns_waitcnt vmcnt(9)\n"
"v_cmp_eq_u64 s[86:87], s[46:47], v[248:249]\nM 60,164,116\ns_add_u32 s65, s68, 23187456\nM 44,160,96\nR 182,189,4096\ns_and_b32 s62, s86, 0xf\n"
"s_cmp_eq_u32 s62, 0xf\ns_cbranch_scc0 Lq466\nLq467:\nM 44,164,112\nG 4\nM 32,168,128\n"
"R 160,186,57344\nM 32,172,144\nbuffer_load_dwordx4 a[160:163], v252, s[20:23], s64 offen sc1\nM 48,168,132\nR 162,186,61440\nM 48,172,148\n"
"s_add_u32 s66, s69, 23072768\nM 64,168,136\nR 164,190,0\nM 64,172,152\nbuffer_load_dwordx4 a[164:167], v252, s[20:23], s65 offen sc1\nM 80,168,140\n"
"R 166,190,4096\nM 80,172,156\nG 4\nM 84,176,140\nR 168,187,57344\nM 84,180,156\n"
"s_add_u32 s67, s69, 23187456\nM 68,176,136\nR 170,187,61440\nM 68,180,152\nbuffer_load_dwordx4 a[168:171], v252, s[20:23], s66 offen sc1\nM 52,176,132\n"
"R 172,191,0\nM 52,180,148\ns_add_u32 s64, s68, 23079936\nM 36,176,128\nR 174,191,4096\nM 36,180,144\n"
"buffer_load_dwordx4 v221, s[16:19], s63 offen offset:1024 lds\nG 4\nM 40,160,128\nR 176,188,8192\ns_add_u32 m0, s50, 16384\nM 40,164,144\n"
"buffer_load_dwordx4 a[172:175], v252, s[20:23], s67 offen sc1\nM 56,160,132\nR 178,188,12288\ns_add_u32 s63, s57, 262144\nM 56,164,148\ns_add_u32 s65, s68, 23194624\n"
"buffer_load_dwordx4 v220, s[16:19], s63 offen lds\nM 72,160,136\nR 180,188,16384\nLq579:\ns_lshr_b32 s60, s45, 3\ns_lshl2_add_u32 s60, s60, s40\n"
"s_lshl_b32 s60, s60, 8\nv_lshl_add_u32 v224, v252, 3, s60\nv_add_u32 v224, Lq579+4096-LqPC, v224\nv_min_u32 v224, LqE-LqPC-4, v224\ns_mov_b64 exec, 3\nglobal_load_dword v224, v224, s[98:99]\n"
"s_mov_b64 exec, -1\nM 72,164,152\nbuffer_load_dwordx4 a[176:179], v252, s[20:23], s64 offen sc1\nM 88,160,140\nR 182,188,20480\nM 88,164,156\n"
"buffer_load_dwordx4 v221, s[16:19], s63 offen offset:1024 lds\nG 4\nM 92,168,140\nR 160,189,8192\ns_add_u32 m0, s50, 24576\nM 92,172,156\n"
"s_add_u32 s66, s69, 23079936\nM 76,168,136\nR 162,189,12288\ns_add_u32 s63, s57, 786432\nM 76,172,152\nbuffer_load_dwordx4 a[180:183], v252, s[20:23], s65 offen sc1\n"
"buffer_load_dwordx4 v220, s[16:19], s63 offen lds\nM 60,168,132\nR 164,189,16384\nM 60,172,148\ns_add_u32 s67, s69, 23194624\nM 44,168,128\n"
"R 166,189,20480\nM 44,172,144\nbuffer_load_dwordx4 v221, s[16:19], s63 offen offset:1024 lds\nG 4\nM 96,176,128\nR 168,190,8192\n"
"s_add_u32 m0, s50, 32768\nM 96,180,144\nbuffer_load_dwordx4 a[184:187], v252, s[20:23], s66 offen sc1\nM 112,176,132\nR 170,190,12288\ns_add_u32 s63, s57, 327680\n"
"M 112,180,148\ns_add_u32 s64, s68, 23073792\nbuffer_load_dwordx4 v220, s[16:19], s63 offen lds\nM 128,176,136\nR 172,190,16384\nM 128,180,152\n"
"buffer_load_dwordx4 a[188:191], v252, s[20:23], s67 offen sc1\nM 144,176,140\nR 174,190,20480\nM 144,180,156\nbuffer_load_dwordx4 v221, s[16:19], s63 offen offset:1024 lds\nG 4\n"
"M 148,160,140\nR 176,191,8192\ns_waitcnt vmcnt(19)\ns_barrier\nM 148,164,156\ns_add_u32 s65, s68, 23188480\n"
"s_add_u32 s63, s57, 851968\nM 132,160,136\nR 178,191,12288\ns_add_u32 m0, s50, 40960\nM 132,164,152\nbuffer_load_dwordx4 a[192:195], v252, s[20:23], s64 offen sc1\n"
"M 116,160,132\nR 180,191,16384\nbuffer_load_dwordx4 v220, s[16:19], s63 offen lds\nM 116,164,148\ns_add_u32 s66, s69, 23073792\nM 100,160,128\n"
"R 182,191,20480\nM 100,164,144\nG 4\nM 104,168,128\nR 160,188,24576\nbuffer_load_dwordx4 v221, s[16:19], s63 offen offset:1024 lds\n"
"M 104,172,144\nbuffer_load_dwordx4 a[196:199], v252, s[20:23], s65 offen sc1\nM 120,168,132\nR 162,188,28672\nM 120,172,148\ns_add_u32 s67, s69, 23188480\n"
"M 136,168,136\nR 164,188,32768\nM 136,172,152\nbuffer_load_dwordx4 a[200:203], v252, s[20:23], s66 offen sc1\nM 152,168,140\nR 166,188,36864\n"
"M 152,172,156\nG 4\nM 156,176,140\nR 168,189,24576\nM 156,180,156\nbuffer_load_dwordx4 a[204:207], v252, s[20:23], s67 offen sc1\n"
"M 140,176,136\nR 170,189,28672\nM 140,180,152\ns_add_u32 s64, s68, 23080960\nM 124,176,132\nR 172,189,32768\n"
"M 124,180,148\ns_add_u32 s65, s68, 23195648\nM 108,176,128\nR 174,189,36864\nM 108,180,144\nG 4\n"
"M 0,160,128\nR 176,190,24576\nM 0,164,144\nbuffer_load_dwordx4 a[208:211], v252, s[20:23], s64 offen sc1\nM 16,160,132\nR 178,190,28672\n"
"M 16,164,148\ns_add_u32 s66, s69, 23080960\nv_mfma_f32_16x16x32_bf16 a[224:227], v[160:163], a[136:139], a[224:227]\nR 180,190,32768\nv_mfma_f32_16x16x32_bf16 a[224:227], v[164:167], a[152:155], a[224:227]\nbuffer_load_dwordx4 a[212:215], v252, s[20:23], s65 offen sc1\n"
"v_mfma_f32_16x16x32_bf16 a[240:243], v[160:163], a[140:143], a[240:243]\nR 182,190,36864\nv_mfma_f32_16x16x32_bf16 a[240:243], v[164:167], a[156:159], a[240:243]\nG 4\nv_mfma_f32_16x16x32_bf16 a[244:247], v[168:171], a[140:143], a[244:247]\nR 160,191,24576\n"
"v_mfma_f32_16x16x32_bf16 a[244:247], v[172:175], a[156:159], a[244:247]\nbuffer_load_dwordx4 a[216:219], v252, s[20:23], s66 offen sc1\nv_mfma_f32_16x16x32_bf16 a[228:231], v[168:171], a[136:139], a[228:231]\nR 162,191,28672\nv_mfma_f32_16x16x32_bf16 a[228:231], v[172:175], a[152:155], a[228:231]\ns_add_u32 s67, s69, 23195648\n"
"M 20,168,132\nR 164,191,32768\nM 20,172,148\nM 4,168,128\nR 166,191,36864\nM 4,172,144\n"
"G 4\nM 8,176,128\nR 168,188,40960\nM 8,180,144\nbuffer_load_dwordx4 a[220:223], v252, s[20:23], s67 offen sc1\nM 24,176,132\n"
"R 170,188,45056\nM 24,180,148\ns_add_u32 m0, s50, 49152\nv_mfma_f32_16x16x32_bf16 a[232:235], v[176:179], a[136:139], a[232:235]\nR 172,188,49152\nv_mfma_f32_16x16x32_bf16 a[232:235], v[180:183], a[152:155], a[232:235]\n"
"s_add_u32 s63, s58, 262144\nv_mfma_f32_16x16x32_bf16 a[248:251], v[176:179], a[140:143], a[248:251]\nR 174,188,53248\nv_mfma_f32_16x16x32_bf16 a[248:251], v[180:183], a[156:159], a[248:251]\nG 4\nv_mfma_f32_16x16x32_bf16 a[252:255], v[160:163], a[140:143], a[252:255]\n"
"R 176,189,40960\nv_mfma_f32_16x16x32_bf16 a[252:255], v[164:167], a[156:159], a[252:255]\nbuffer_load_dwordx4 v220, s[16:19], s63 offen lds\nv_mfma_f32_16x16x32_bf16 a[236:239], v[160:163], a[136:139], a[236:239]\nR 178,189,45056\ns_add_u32 s84, s52, 2048\n"
"v_mfma_f32_16x16x32_bf16 a[236:239], v[164:167], a[152:155], a[236:239]\nv_lshrrev_b32 v244, 5, v252\nv_lshlrev_b32 v244, 5, v244\nM 28,160,132\nR 180,189,49152\nv_bfe_u32 v245, v252, 4, 1\n"
"M 28,164,148\nbuffer_load_dwordx4 v221, s[16:19], s63 offen offset:1024 lds\nM 12,160,128\nR 182,189,53248\nM 12,164,144\ns_waitcnt vmcnt(9)\n"
"s_barrier\nG 4\nM 96,168,160\nR 160,190,40960\nM 96,172,176\ns_add_u32 m0, s50, 57344\n"
"M 112,168,164\nR 162,190,45056\nM 112,172,180\ns_add_u32 s63, s58, 786432\nM 128,168,168\nR 164,190,49152\n"
"v_lshrrev_b32 v230, 8, v252\nM 128,172,184\nbuffer_load_dwordx4 v220, s[16:19], s63 offen lds\nM 144,168,172\nR 166,190,53248\nv_lshlrev_b32 v230, 3, v230\n"
"M 144,172,188\nG 4\nM 148,176,172\nR 168,191,40960\nM 148,180,188\ns_and_b32 s60, s41, 7\n"
"s_lshl2_add_u32 s60, s60, s40\nv_add_u32 v244, s60, v244\nM 132,176,168\nR 170,191,45056\nLpg15:\ns_cbranch_execz LretO0n\n"
"v_mul_u32_u24 v244, 6144, v244\nM 132,180,184\ns_sub_u32 s60, s84, s84\ns_add_u32 s60, s60, 2048\nv_mul_lo_u32 v245, v245, s60\nM 116,176,164\n"
"R 172,191,49152\nv_add_u32 v244, v244, v245\nM 116,180,180\nbuffer_load_dwordx2 v[232:233], v230, s[36:39], s53 offen offset:0\nM 100,176,160\nR 174,191,53248\n"
"s_bfm_b64 exec, 16, 0\nbuffer_load_dword v245, v244, s[4:7], s84 offen\ns_mov_b64 exec, -1\nM 100,180,176\nG 4\nM 104,160,160\n"
"R 176,184,0\nM 104,164,176\nbuffer_load_dwordx4 v221, s[16:19], s63 offen offset:1024 lds\nM 120,160,164\nR 178,184,4096\nM 120,164,180\n"
"s_add_u32 m0, s50, 65536\nM 136,160,168\nR 180,184,8192\nbuffer_load_dwordx2 v[234:235], v230, s[36:39], s53 offen offset:32\nM 136,164,184\ns_add_u32 s63, s58, 327680\n"
"buffer_load_dwordx2 v[236:237], v230, s[36:39], s53 offen offset:64\nM 152,160,172\nR 182,184,12288\nM 152,164,188\nbuffer_load_dwordx2 v[238:239], v230, s[36:39], s53 offen offset:96\nG 4\n"
"M 156,168,172\nR 160,185,0\nv_bfe_u32 v242, v252, 4, 4\nM 156,172,188\nbuffer_load_dwordx4 v220, s[16:19], s63 offen lds\nM 140,168,168\n"
"R 162,185,4096\nv_lshrrev_b32 v243, 8, v252\nM 140,172,184\nv_lshlrev_b32 v243, 2, v243\nv_sub_u32 v242, v242, v243\nM 124,168,164\n"
"R 164,185,8192\nv_lshlrev_b32 v243, 4, v242\nM 124,172,180\ns_mov_b64 s[60:61], 0x3f80\nv_lshlrev_b64 v[208:209], v243, s[60:61]\nM 108,168,160\n"
"R 166,185,12288\ns_bfm_b64 exec, 16, 16\nbuffer_load_dword v245, v244, s[4:7], s84 offen\ns_mov_b64 exec, -1\nM 108,172,176\nG 4\n"
"M 96,176,192\nR 168,186,0\ns_waitcnt vmcnt(14)\nM 96,180,208\nbuffer_load_dwordx4 v221, s[16:19], s63 offen offset:1024 lds\nM 112,176,196\n"
"R 170,186,4096\ns_waitcnt vmcnt(14)\nM 112,180,212\ns_add_u32 m0, s50, 73728\nM 128,176,200\nR 172,186,8192\n"
"s_waitcnt vmcnt(13)\nM 128,180,216\ns_add_u32 s63, s58, 851968\nM 144,176,204\nR 174,186,12288\ns_waitcnt vmcnt(12)\n"
"M 144,180,220\nG 4\nv_cmp_gt_u32 s[60:61], 4, v242\ns_nop 1\nv_cndmask_b32 v212, 0, v208, s[60:61]\nv_cndmask_b32 v208, 0, v208, s[60:61]\n"
"v_cndmask_b32 v213, 0, v209, s[60:61]\nv_cndmask_b32 v209, 0, v209, s[60:61]\nM 148,160,204\nR 176,187,0\nM 148,164,220\nbuffer_load_dwordx4 v220, s[16:19], s63 offen lds\n"
"M 132,160,200\nR 178,187,4096\nM 132,164,216\nM 116,160,196\nR 180,187,8192\nM 116,164,212\n"
"M 100,160,192\nR 182,187,12288\ns_bfm_b64 exec, 16, 32\nbuffer_load_dword v245, v244, s[4:7], s84 offen\ns_mov_b64 exec, -1\nM 100,164,208\n"
"G 4\nM 104,168,192\nR 160,184,16384\nM 104,172,208\nbuffer_load_dwordx4 v221, s[16:19], s63 offen offset:1024 lds\nM 120,168,196\n"
"R 162,184,20480\nM 120,172,212\ns_add_u32 m0, s50, 81920\nM 136,168,200\nR 164,184,24576\nM 136,172,216\n"
"s_add_u32 s63, s59, 0\nM 152,168,204\nR 166,184,28672\nM 152,172,220\nG 4\nM 156,176,204\n"
"R 168,185,16384\nM 156,180,220\nbuffer_load_dwordx4 v220, s[16:19], s63 offen lds\nM 140,176,200\nR 170,185,20480\nM 140,180,216\n"
"M 124,176,196\nR 172,185,24576\nM 124,180,212\nM 108,176,192\nR 174,185,28672\ns_bfm_b64 exec, 16, 48\n"
"buffer_load_dword v245, v244, s[4:7], s84 offen\ns_mov_b64 exec, -1\nM 108,180,208\nG 4\nM 0,160,160\nR 176,186,16384\n"
"M 0,164,176\ns_mov_b32 m0, s51\nM 16,160,164\nR 178,186,20480\nv_cvt_pk_bf16_f32 v128, v128, v129\nv_cvt_pk_bf16_f32 v129, v130, v131\n"
"M 16,164,180\nds_write_addtid_b32 v128 offset:0\nv_cvt_pk_bf16_f32 v130, v132, v133\nv_cvt_pk_bf16_f32 v131, v134, v135\nv_mfma_f32_16x16x32_bf16 a[224:227], v[160:163], a[168:171], a[224:227]\nR 180,186,24576\n"
"v_cvt_pk_bf16_f32 v132, v136, v137\nv_mfma_f32_16x16x32_bf16 a[224:227], v[164:167], a[184:187], a[224:227]\nds_write_addtid_b32 v129 offset:256\nv_cvt_pk_bf16_f32 v133, v138, v139\nv_mfma_f32_16x16x32_bf16 a[240:243], v[160:163], a[172:175], a[240:243]\nR 182,186,28672\n"
"v_cvt_pk_bf16_f32 v134, v140, v141\nv_mfma_f32_16x16x32_bf16 a[240:243], v[164:167], a[188:191], a[240:243]\nds_write_addtid_b32 v130 offset:512\nG 7\nv_cvt_pk_bf16_f32 v135, v142, v143\nv_mfma_f32_16x16x32_bf16 a[244:247], v[168:171], a[172:175], a[244:247]\n"
"R 160,187,16384\nv_cvt_pk_bf16_f32 v136, v144, v145\nv_mfma_f32_16x16x32_bf16 a[244:247], v[172:175], a[188:191], a[244:247]\nds_write_addtid_b32 v131 offset:768\nv_cvt_pk_bf16_f32 v137, v146, v147\nv_mfma_f32_16x16x32_bf16 a[228:231], v[168:171], a[168:171], a[228:231]\n"
"R 162,187,20480\nv_cvt_pk_bf16_f32 v138, v148, v149\nv_mfma_f32_16x16x32_bf16 a[228:231], v[172:175], a[184:187], a[228:231]\nds_write_addtid_b32 v132 offset:1024\nv_cvt_pk_bf16_f32 v139, v150, v151\nM 20,168,164\n"
"R 164,187,24576\nv_cvt_pk_bf16_f32 v140, v152, v153\nM 20,172,180\nds_write_addtid_b32 v133 offset:1280\nv_cvt_pk_bf16_f32 v141, v154, v155\nM 4,168,160\n"
"R 166,187,28672\nv_cvt_pk_bf16_f32 v142, v156, v157\nM 4,172,176\nds_write_addtid_b32 v134 offset:1536\nG 9\nv_cvt_pk_bf16_f32 v143, v158, v159\n"
"M 8,176,160\nR 168,184,32768\nds_write_addtid_b32 v135 offset:1792\nM 8,180,176\nds_write_addtid_b32 v136 offset:2048\nds_write_addtid_b32 v137 offset:2304\n"
"M 24,176,164\nR 170,184,36864\nds_write_addtid_b32 v138 offset:2560\nM 24,180,180\nds_write_addtid_b32 v139 offset:2816\nds_write_addtid_b32 v140 offset:3072\n"
"v_mfma_f32_16x16x32_bf16 a[232:235], v[176:179], a[168:171], a[232:235]\nR 172,184,40960\nds_write_addtid_b32 v141 offset:3328\nv_mfma_f32_16x16x32_bf16 a[232:235], v[180:183], a[184:187], a[232:235]\nds_write_addtid_b32 v142 offset:3584\nds_write_addtid_b32 v143 offset:3840\n"
"v_mfma_f32_16x16x32_bf16 a[248:251], v[176:179], a[172:175], a[248:251]\nR 174,184,45056\ns_add_u32 m0, s50, 81920\nv_mfma_f32_16x16x32_bf16 a[248:251], v[180:183], a[188:191], a[248:251]\nG 14\nv_mfma_f32_16x16x32_bf16 a[252:255], v[160:163], a[172:175], a[252:255]\n"
"R 176,185,32768\ns_waitcnt vmcnt(11) lgkmcnt(2)\ns_barrier\nv_mfma_f32_16x16x32_bf16 a[252:255], v[164:167], a[188:191], a[252:255]\nbuffer_load_dwordx4 v221, s[16:19], s63 offen offset:1024 lds\nv_mfma_f32_16x16x32_bf16 a[236:239], v[160:163], a[168:171], a[236:239]\n"
"R 178,185,36864\ns_add_u32 s63, s59, 524288\nv_mfma_f32_16x16x32_bf16 a[236:239], v[164:167], a[184:187], a[236:239]\ns_add_u32 m0, s50, 90112\nds_read2st64_b32 v[128:129], v225 offset0:0 offset1:1\nM 28,160,164\n"
"R 180,185,40960\nM 28,164,180\nbuffer_load_dwordx4 v220, s[16:19], s63 offen lds\nM 12,160,160\nR 182,185,45056\nM 12,164,176\n"
"ds_read2st64_b32 v[130:131], v225 offset0:2 offset1:3\nG 6\nM 0,168,192\nR 160,186,32768\nM 0,172,208\nbuffer_load_dwordx4 v221, s[16:19], s63 offen offset:1024 lds\n"
"ds_read2st64_b32 v[132:133], v225 offset0:4 offset1:5\nM 16,168,196\nR 162,186,36864\ns_add_u32 s63, s59, 65536\nds_read2st64_b32 v[134:135], v225 offset0:6 offset1:7\nM 16,172,212\n"
"s_add_u32 m0, s50, 98304\nds_read2st64_b32 v[136:137], v225 offset0:8 offset1:9\nv_mfma_f32_16x16x32_bf16 a[224:227], v[168:171], a[200:203], a[224:227]\nR 164,186,40960\nv_mfma_f32_16x16x32_bf16 a[224:227], v[172:175], a[216:219], a[224:227]\nbuffer_load_dwordx4 v220, s[16:19], s63 offen lds\n"
"G 6\nv_mfma_f32_16x16x32_bf16 v[96:99], v[208:211], v[128:131], v[96:99]\nv_mfma_f32_16x16x32_bf16 v[100:103], v[210:213], v[128:131], v[100:103]\nds_read2st64_b32 v[138:139], v225 offset0:10 offset1:11\nv_mfma_f32_16x16x32_bf16 a[240:243], v[168:171], a[204:207], a[240:243]\nR 166,186,45056\n"
"v_mfma_f32_16x16x32_bf16 a[240:243], v[172:175], a[220:223], a[240:243]\nds_read2st64_b32 v[140:141], v225 offset0:12 offset1:13\nv_mfma_f32_16x16x32_bf16 a[244:247], v[176:179], a[204:207], a[244:247]\nR 168,187,32768\nG 6\nv_mfma_f32_16x16x32_bf16 v[104:107], v[208:211], v[132:135], v[104:107]\n"
"v_mfma_f32_16x16x32_bf16 v[108:111], v[210:213], v[132:135], v[108:111]\nds_read2st64_b32 v[142:143], v225 offset0:14 offset1:15\nv_mfma_f32_16x16x32_bf16 a[244:247], v[180:183], a[220:223], a[244:247]\nbuffer_load_dwordx4 v221, s[16:19], s63 offen offset:1024 lds\nv_mfma_f32_16x16x32_bf16 a[228:231], v[176:179], a[200:203], a[228:231]\nR 170,187,36864\n"
"v_mfma_f32_16x16x32_bf16 a[228:231], v[180:183], a[216:219], a[228:231]\ns_add_u32 m0, s50, 106496\nG 5\nv_mfma_f32_16x16x32_bf16 v[112:115], v[208:211], v[136:139], v[112:115]\nv_mfma_f32_16x16x32_bf16 v[116:119], v[210:213], v[136:139], v[116:119]\nM 20,176,196\n"
"R 172,187,40960\nM 20,180,212\ns_add_u32 s63, s59, 589824\nM 4,176,192\nR 174,187,45056\nG 3\n"
"v_mfma_f32_16x16x32_bf16 v[120:123], v[208:211], v[140:143], v[120:123]\nv_mfma_f32_16x16x32_bf16 v[124:127], v[210:213], v[140:143], v[124:127]\nM 4,180,208\nM 8,160,192\nR 176,184,49152\nM 8,164,208\n"
"buffer_load_dwordx4 v220, s[16:19], s63 offen lds\nM 24,160,196\nR 178,184,53248\nM 24,164,212\nv_mfma_f32_16x16x32_bf16 a[232:235], v[160:163], a[200:203], a[232:235]\nR 180,184,57344\n"
"v_cvt_pk_bf16_f32 v120, v120, v121\nv_cvt_pk_bf16_f32 v96, v96, v97\nv_cvt_pk_bf16_f32 v112, v112, v113\nv_cvt_pk_bf16_f32 v121, v122, v123\nv_mfma_f32_16x16x32_bf16 a[232:235], v[164:167], a[216:219], a[232:235]\nbuffer_load_dwordx4 v221, s[16:19], s63 offen offset:1024 lds\n"
"v_cvt_pk_bf16_f32 v97, v98, v99\nv_cvt_pk_bf16_f32 v113, v114, v115\nv_cvt_pk_bf16_f32 v122, v124, v125\nv_mfma_f32_16x16x32_bf16 a[248:251], v[160:163], a[204:207], a[248:251]\nR 182,184,61440\nv_cvt_pk_bf16_f32 v123, v126, v127\n"
"v_cvt_pk_bf16_f32 v98, v100, v101\nv_cvt_pk_bf16_f32 v114, v116, v117\nv_mfma_f32_16x16x32_bf16 a[248:251], v[164:167], a[220:223], a[248:251]\ns_bfe_u32 s100, s56, 0x20007\ns_mul_i32 s100, s100, 49152\ns_add_u32 s100, s100, s72\n"
"s_add_u32 s100, s100, 52428800\nG 4\nv_cvt_pk_bf16_f32 v99, v102, v103\nv_cvt_pk_bf16_f32 v115, v118, v119\nv_mfma_f32_16x16x32_bf16 a[252:255], v[168:171], a[204:207], a[252:255]\nR 160,185,49152\n"
"s_add_u32 s63, s59, 131072\nv_mfma_f32_16x16x32_bf16 a[252:255], v[172:175], a[220:223], a[252:255]\nbuffer_store_dwordx4 v[96:99], v252, s[20:23], s100 offen offset:0 sc1\nv_mfma_f32_16x16x32_bf16 a[236:239], v[168:171], a[200:203], a[236:239]\nR 162,185,53248\nv_cvt_pk_bf16_f32 v104, v104, v105\n"
"v_mfma_f32_16x16x32_bf16 a[236:239], v[172:175], a[216:219], a[236:239]\ns_add_u32 m0, s50, 114688\nv_cvt_pk_bf16_f32 v105, v106, v107\nv_cvt_pk_bf16_f32 v106, v108, v109\nM 28,168,196\nR 164,185,57344\n"
"v_cvt_pk_bf16_f32 v107, v110, v111\nM 28,172,212\nbuffer_load_dwordx4 v220, s[16:19], s63 offen lds\nM 12,168,192\nR 166,185,61440\nM 12,172,208\n"
"buffer_store_dwordx4 v[104:107], v252, s[20:23], s100 offen offset:1024 sc1\ns_waitcnt vmcnt(12)\ns_barrier\nG 4\nM 32,176,160\nR 168,186,49152\n"
"M 32,180,176\nv_xor_b32 v240, 0x2000, v225\nM 48,176,164\nR 170,186,53248\nds_write2st64_b32 v240, a224, a225 offset0:0 offset1:1\nM 48,180,180\n"
"buffer_store_dwordx4 v[112:115], v252, s[20:23], s100 offen offset:2048 sc1\nds_write2st64_b32 v240, a226, a227 offset0:2 offset1:3\nM 64,176,168\nR 172,186,57344\nds_write2st64_b32 v240, a228, a229 offset0:4 offset1:5\nM 64,180,184\n"
"ds_write2st64_b32 v240, a230, a231 offset0:6 offset1:7\nM 80,176,172\nR 174,186,61440\nds_write2st64_b32 v240, a232, a233 offset0:8 offset1:9\nM 80,180,188\nds_write2st64_b32 v240, a234, a235 offset0:10 offset1:11\n"
"G 10\nM 84,160,172\nR 176,187,49152\nds_write2st64_b32 v240, a236, a237 offset0:12 offset1:13\nM 84,164,188\nbuffer_store_dwordx4 v[120:123], v252, s[20:23], s100 offen offset:3072 sc1\n"
"ds_write2st64_b32 v240, a238, a239 offset0:14 offset1:15\nM 68,160,168\nR 178,187,53248\nds_write2st64_b32 v240, a240, a241 offset0:16 offset1:17\nM 68,164,184\nbuffer_load_dwordx4 v221, s[16:19], s63 offen offset:1024 lds\n"
"ds_write2st64_b32 v240, a242, a243 offset0:18 offset1:19\nM 52,160,164\nR 180,187,57344\ns_add_u32 m0, s50, 0\ns_add_u32 s63, s59, 655360\nds_write2st64_b32 v240, a244, a245 offset0:20 offset1:21\n"
"M 52,164,180\nbuffer_load_dwordx4 v220, s[16:19], s63 offen lds\nds_write2st64_b32 v240, a246, a247 offset0:22 offset1:23\nM 36,160,160\nR 182,187,61440\nds_write2st64_b32 v240, a248, a249 offset0:24 offset1:25\n"
"M 36,164,176\nds_write2st64_b32 v240, a250, a251 offset0:26 offset1:27\nG 14\nM 40,168,160\nR 160,188,0\nds_write2st64_b32 v240, a252, a253 offset0:28 offset1:29\n"
"M 40,172,176\nbuffer_load_dwordx4 v221, s[16:19], s63 offen offset:1024 lds\nM 56,168,164\nR 162,188,4096\ns_add_u32 s63, s59, 196608\nM 56,172,180\n"
"s_add_u32 m0, s50, 8192\nds_write2st64_b32 v240, a254, a255 offset0:30 offset1:31\nM 72,168,168\nR 164,188,8192\nM 72,172,184\nbuffer_load_dwordx4 v220, s[16:19], s63 offen lds\n"
"M 88,168,172\nR 166,188,12288\nM 88,172,188\nG 8\nM 92,176,172\nR 168,189,0\n"
"s_waitcnt vmcnt(13) lgkmcnt(3)\ns_barrier\nM 92,180,188\nbuffer_load_dwordx4 v221, s[16:19], s63 offen offset:1024 lds\nds_read2st64_b32 v[128:129], v225 offset0:0 offset1:1\nM 76,176,168\n"
"R 170,189,4096\ns_add_u32 s63, s59, 720896\nds_read2st64_b32 v[130:131], v225 offset0:2 offset1:3\nM 76,180,184\ns_add_u32 m0, s50, 16384\nds_read2st64_b32 v[132:133], v225 offset0:4 offset1:5\n"
"ds_read2st64_b32 v[134:135], v225 offset0:6 offset1:7\nM 60,176,164\nR 172,189,8192\nds_read2st64_b32 v[136:137], v225 offset0:8 offset1:9\nM 60,180,180\nbuffer_load_dwordx4 v220, s[16:19], s63 offen lds\n"
"ds_read2st64_b32 v[138:139], v225 offset0:10 offset1:11\nM 44,176,160\nR 174,189,12288\nds_read2st64_b32 v[140:141], v225 offset0:12 offset1:13\nM 44,180,176\nds_read2st64_b32 v[142:143], v225 offset0:14 offset1:15\n"
"ds_read2st64_b32 v[144:145], v225 offset0:16 offset1:17\nG 13\nM 32,160,192\nR 176,190,0\nds_read2st64_b32 v[146:147], v225 offset0:18 offset1:19\nG 13\n"
"v_add_f32 v0, v0, v128\nM 32,164,208\nbuffer_load_dwordx4 v221, s[16:19], s63 offen offset:1024 lds\nds_read2st64_b32 v[148:149], v225 offset0:20 offset1:21\nM 48,160,196\nR 178,190,4096\n"
"s_add_u32 s63, s59, 393216\nds_read2st64_b32 v[150:151], v225 offset0:22 offset1:23\nM 48,164,212\ns_add_u32 m0, s50, 24576\nds_read2st64_b32 v[152:153], v225 offset0:24 offset1:25\nds_read2st64_b32 v[154:155], v225 offset0:26 offset1:27\n"
"M 64,160,200\nR 180,190,8192\nds_read2st64_b32 v[156:157], v225 offset0:28 offset1:29\nv_add_f32 v1, v1, v129\nM 64,164,216\nbuffer_load_dwordx4 v220, s[16:19], s63 offen lds\n"
"ds_read2st64_b32 v[158:159], v225 offset0:30 offset1:31\nM 80,160,204\nR 182,190,12288\nG 15\nv_add_f32 v2, v2, v130\nv_add_f32 v3, v3, v131\n"
"M 80,164,220\nG 14\nv_add_f32 v4, v4, v132\nv_add_f32 v5, v5, v133\nv_add_f32 v6, v6, v134\nM 84,168,204\n"
"R 160,191,0\nv_add_f32 v7, v7, v135\nv_add_f32 v8, v8, v136\nM 84,172,220\nbuffer_load_dwordx4 v221, s[16:19], s63 offen offset:1024 lds\nv_add_f32 v9, v9, v137\n"
"M 68,168,200\nR 162,191,4096\ns_add_u32 s63, s59, 458752\nv_add_f32 v10, v10, v138\nM 68,172,216\ns_add_u32 m0, s50, 32768\n"
"v_add_f32 v11, v11, v139\nG 15\nv_add_f32 v12, v12, v140\nv_add_f32 v13, v13, v141\nM 52,168,196\nR 164,191,8192\n"
"G 15\nv_add_f32 v14, v14, v142\nv_add_f32 v15, v15, v143\nM 52,172,212\nbuffer_load_dwordx4 v220, s[16:19], s63 offen lds\nG 14\n"
"v_add_f32 v16, v16, v144\nM 36,168,192\nR 166,191,12288\nv_add_f32 v17, v17, v145\nG 13\nv_add_f32 v18, v18, v146\n"
"M 36,172,208\nG 4\nv_add_f32 v19, v19, v147\nv_add_f32 v20, v20, v148\nv_add_f32 v21, v21, v149\nM 40,176,192\n"
"R 168,188,16384\nv_add_f32 v22, v22, v150\nv_add_f32 v23, v23, v151\nM 40,180,208\nbuffer_load_dwordx4 v221, s[16:19], s63 offen offset:1024 lds\nv_add_f32 v24, v24, v152\n"
"M 56,176,196\nR 170,188,20480\ns_add_u32 s63, s59, 262144\nv_add_f32 v25, v25, v153\nM 56,180,212\ns_add_u32 m0, s50, 40960\n"
"v_add_f32 v26, v26, v154\nv_add_f32 v27, v27, v155\nv_add_f32 v28, v28, v156\nv_add_f32 v29, v29, v157\nv_add_f32 v30, v30, v158\nv_add_f32 v31, v31, v159\n"
"M 72,176,200\nR 172,188,24576\nv_cvt_pk_bf16_f32 v24, v24, v25\nv_cvt_pk_bf16_f32 v0, v0, v1\nv_cvt_pk_bf16_f32 v8, v8, v9\nv_cvt_pk_bf16_f32 v16, v16, v17\n"
"v_cvt_pk_bf16_f32 v25, v26, v27\nM 72,180,216\nbuffer_load_dwordx4 v220, s[16:19], s63 offen lds\nv_cvt_pk_bf16_f32 v9, v10, v11\nv_cvt_pk_bf16_f32 v17, v18, v19\nv_cvt_pk_bf16_f32 v26, v28, v29\n"
"M 88,176,204\nR 174,188,28672\nv_cvt_pk_bf16_f32 v27, v30, v31\nv_cvt_pk_bf16_f32 v1, v2, v3\nM 88,180,220\nG 4\n"
"v_cvt_pk_bf16_f32 v2, v4, v5\nv_cvt_pk_bf16_f32 v3, v6, v7\nM 92,160,204\nR 176,189,16384\ns_bfe_u32 s100, s57, 0x20007\ns_mul_i32 s100, s100, 49152\n"
"s_add_u32 s100, s100, s72\ns_add_u32 s100, s100, 52445184\nv_cvt_pk_bf16_f32 v10, v12, v13\nM 92,164,220\nbuffer_store_dwordx4 v[0:3], v252, s[20:23], s100 offen offset:0 sc1\nM 76,160,200\n"
"R 178,189,20480\nv_cvt_pk_bf16_f32 v11, v14, v15\nM 76,164,216\nbuffer_load_dwordx4 v221, s[16:19], s63 offen offset:1024 lds\nM 60,160,196\nR 180,189,24576\n"
"s_add_u32 m0, s50, 49152\ns_add_u32 s63, s59, 786432\nM 60,164,212\nbuffer_load_dwordx4 v220, s[16:19], s63 offen lds\nM 44,160,192\nR 182,189,28672\n"
"Lpg16:\ns_cbranch_execz LretW0z\nv_cvt_pk_bf16_f32 v18, v20, v21\nM 44,164,208\nbuffer_store_dwordx4 v[8:11], v252, s[20:23], s100 offen offset:1024 sc1\ns_waitcnt vmcnt(21)\n"
"s_barrier\nG 4\nv_mfma_f32_16x16x32_bf16 v[96:99], v[168:171], a[0:3], 0\nR 160,190,16384\nv_cvt_pk_bf16_f32 v19, v22, v23\nM 96,172,16\n"
"s_mov_b32 m0, s51\nv_mfma_f32_16x16x32_bf16 v[112:115], v[168:171], a[4:7], 0\nR 162,190,20480\nv_cvt_pk_bf16_f32 v64, v64, v65\nv_cvt_pk_bf16_f32 v65, v66, v67\nM 112,172,20\n"
"buffer_store_dwordx4 v[16:19], v252, s[20:23], s100 offen offset:2048 sc1\nv_cvt_pk_bf16_f32 v66, v68, v69\nv_mfma_f32_16x16x32_bf16 v[128:131], v[168:171], a[8:11], 0\nR 164,190,24576\nv_cvt_pk_bf16_f32 v67, v70, v71\nv_cvt_pk_bf16_f32 v68, v72, v73\n"
"M 128,172,24\nds_write_addtid_b32 v64 offset:0\nv_cvt_pk_bf16_f32 v69, v74, v75\nv_cvt_pk_bf16_f32 v70, v76, v77\nv_mfma_f32_16x16x32_bf16 v[144:147], v[168:171], a[12:15], 0\nR 166,190,28672\n"
"v_cvt_pk_bf16_f32 v71, v78, v79\nv_cvt_pk_bf16_f32 v72, v80, v81\nM 144,172,28\nds_write_addtid_b32 v65 offset:256\nG 6\nv_cvt_pk_bf16_f32 v73, v82, v83\n"
"v_mfma_f32_16x16x32_bf16 v[148:151], v[176:179], a[12:15], 0\nR 168,191,16384\nv_cvt_pk_bf16_f32 v74, v84, v85\nv_cvt_pk_bf16_f32 v75, v86, v87\nM 148,180,28\nbuffer_store_dwordx4 v[24:27], v252, s[20:23], s100 offen offset:3072 sc1\n"
"v_mfma_f32_16x16x32_bf16 v[132:135], v[176:179], a[8:11], 0\nR 170,191,20480\nv_cvt_pk_bf16_f32 v76, v88, v89\nM 132,180,24\nds_write_addtid_b32 v66 offset:512\nv_cvt_pk_bf16_f32 v77, v90, v91\n"
"v_mfma_f32_16x16x32_bf16 v[116:119], v[176:179], a[4:7], 0\nR 172,191,24576\nv_cvt_pk_bf16_f32 v78, v92, v93\nM 116,180,20\nds_write_addtid_b32 v67 offset:768\nv_cvt_pk_bf16_f32 v79, v94, v95\n"
"v_mfma_f32_16x16x32_bf16 v[100:103], v[176:179], a[0:3], 0\nR 174,191,28672\nds_write_addtid_b32 v68 offset:1024\nM 100,180,16\nds_write_addtid_b32 v69 offset:1280\nds_write_addtid_b32 v70 offset:1536\n"
"G 10\nv_mfma_f32_16x16x32_bf16 v[104:107], v[160:163], a[0:3], 0\nR 176,188,32768\nds_write_addtid_b32 v71 offset:1792\nM 104,164,16\nds_write_addtid_b32 v72 offset:2048\n"
"ds_write_addtid_b32 v73 offset:2304\nv_mfma_f32_16x16x32_bf16 v[120:123], v[160:163], a[4:7], 0\nR 178,188,36864\nds_write_addtid_b32 v74 offset:2560\nM 120,164,20\nds_write_addtid_b32 v75 offset:2816\n"
"ds_write_addtid_b32 v76 offset:3072\nv_mfma_f32_16x16x32_bf16 v[136:139], v[160:163], a[8:11], 0\nR 180,188,40960\nds_write_addtid_b32 v77 offset:3328\nM 136,164,24\nds_write_addtid_b32 v78 offset:3584\n"
"ds_write_addtid_b32 v79 offset:3840\nv_mfma_f32_16x16x32_bf16 v[152:155], v[160:163], a[12:15], 0\nR 182,188,45056\nM 152,164,28\nG 15\nv_mfma_f32_16x16x32_bf16 v[156:159], v[168:171], a[12:15], 0\n"
"R 160,189,32768\ns_waitcnt vmcnt(15) lgkmcnt(2)\ns_barrier\nM 156,172,28\nds_read2st64_b32 v[64:65], v225 offset0:0 offset1:1\nv_mfma_f32_16x16x32_bf16 v[140:143], v[168:171], a[8:11], 0\n"
"R 162,189,36864\nM 140,172,24\nds_read2st64_b32 v[66:67], v225 offset0:2 offset1:3\nv_mfma_f32_16x16x32_bf16 v[124:127], v[168:171], a[4:7], 0\nR 164,189,40960\nM 124,172,20\n"
"ds_read2st64_b32 v[68:69], v225 offset0:4 offset1:5\nv_mfma_f32_16x16x32_bf16 v[108:111], v[168:171], a[0:3], 0\nR 166,189,45056\nM 108,172,16\nds_read2st64_b32 v[70:71], v225 offset0:6 offset1:7\nG 8\n"
"M 96,176,32\nR 168,190,32768\nv_bfe_u32 v250, v252, 4, 2\nv_lshlrev_b32 v250, 3, v250\nM 96,180,48\ns_lshl_b32 s60, s43, 5\n"
"v_add_u32 v250, s60, v250\nG 5\nv_mfma_f32_16x16x32_bf16 v[32:35], v[208:211], v[64:67], v[32:35]\nv_mfma_f32_16x16x32_bf16 v[36:39], v[210:213], v[64:67], v[36:39]\nM 112,176,36\nR 170,190,36864\n"
"M 112,180,52\nds_read2st64_b32 v[72:73], v225 offset0:8 offset1:9\nM 128,176,40\nR 172,190,40960\nM 128,180,56\nds_read2st64_b32 v[74:75], v225 offset0:10 offset1:11\n"
"G 5\nv_mfma_f32_16x16x32_bf16 v[40:43], v[208:211], v[68:71], v[40:43]\nv_mfma_f32_16x16x32_bf16 v[44:47], v[210:213], v[68:71], v[44:47]\nM 144,176,44\nR 174,190,45056\nM 144,180,60\n"
"ds_read2st64_b32 v[76:77], v225 offset0:12 offset1:13\nM 148,160,44\nR 176,191,32768\nds_read2st64_b32 v[78:79], v225 offset0:14 offset1:15\nM 148,164,60\nM 132,160,40\n"
"R 178,191,36864\nM 132,164,56\nG 5\nv_mfma_f32_16x16x32_bf16 v[48:51], v[208:211], v[72:75], v[48:51]\nv_mfma_f32_16x16x32_bf16 v[52:55], v[210:213], v[72:75], v[52:55]\nM 116,160,36\n"
"R 180,191,40960\nM 116,164,52\nM 100,160,32\nR 182,191,45056\nG 3\nv_mfma_f32_16x16x32_bf16 v[56:59], v[208:211], v[76:79], v[56:59]\n"
"v_mfma_f32_16x16x32_bf16 v[60:63], v[210:213], v[76:79], v[60:63]\nM 100,164,48\nM 104,168,32\nR 160,188,49152\nM 104,172,48\nM 120,168,36\n"
"R 162,188,53248\nv_mov_b32 v226, s46\nM 120,172,52\ns_add_u32 m0, s50, 49152\nv_mov_b32 v227, s47\nM 136,168,40\n"
"R 164,184,0\ns_bfe_u32 s62, s56, 0x20007\ns_lshl_b32 s62, s62, 5\ns_add_u32 s61, s74, 548864\ns_add_u32 s61, s61, s62\ns_waitcnt vmcnt(0)\n"
"s_mov_b64 exec, 1\nbuffer_atomic_swap_x2 v[226:227], off, s[20:23], s61 sc1\ns_mov_b64 exec, -1\nv_cvt_pk_bf16_f32 v32, v32, v33\nv_cvt_pk_bf16_f32 v48, v48, v49\nv_cvt_pk_bf16_f32 v56, v56, v57\n"
"M 136,172,56\ns_bfe_u32 s62, s57, 0x20007\ns_lshl_b32 s62, s62, 5\ns_add_u32 s61, s74, 548872\ns_add_u32 s61, s61, s62\ns_mov_b64 exec, 1\n"
"buffer_atomic_swap_x2 v[226:227], off, s[20:23], s61 sc1\ns_mov_b64 exec, -1\nv_cvt_pk_bf16_f32 v49, v50, v51\nv_cvt_pk_bf16_f32 v57, v58, v59\nM 152,168,44\nR 166,184,4096\n"
"v_cvt_pk_bf16_f32 v50, v52, v53\nv_cvt_pk_bf16_f32 v33, v34, v35\nM 152,172,60\nG 4\nv_cvt_pk_bf16_f32 v34, v36, v37\nv_cvt_pk_bf16_f32 v35, v38, v39\n"
"M 156,176,44\nR 168,189,49152\ns_bfe_u32 s100, s58, 0x20007\ns_mul_i32 s100, s100, 49152\ns_add_u32 s100, s100, s72\ns_add_u32 s100, s100, 52461568\n"
"v_cvt_pk_bf16_f32 v40, v40, v41\nM 156,180,60\nbuffer_store_dwordx4 v[32:35], v252, s[20:23], s100 offen offset:0 sc1\nbuffer_load_dwordx4 v221, s[16:19], s63 offen offset:1024 lds\nM 140,176,40\nR 170,189,53248\n"
"s_add_u32 m0, s50, 57344\ns_add_u32 s63, s59, 327680\nM 140,180,56\nbuffer_load_dwordx4 v220, s[16:19], s63 offen lds\nM 124,176,36\nR 172,185,0\n"
"v_cvt_pk_bf16_f32 v41, v42, v43\nM 124,180,52\nv_cvt_pk_bf16_f32 v42, v44, v45\nv_cvt_pk_bf16_f32 v43, v46, v47\nM 108,176,32\nR 174,185,4096\n"
"v_cvt_pk_bf16_f32 v51, v54, v55\nM 108,180,48\nbuffer_store_dwordx4 v[40:43], v252, s[20:23], s100 offen offset:1024 sc1\ns_barrier\nG 4\nM 96,160,64\n"
"R 176,190,49152\nv_cvt_pk_bf16_f32 v58, v60, v61\nM 96,164,80\nbuffer_load_dwordx4 v221, s[16:19], s63 offen offset:1024 lds\nM 112,160,68\nR 178,190,53248\n"
"s_add_u32 m0, s50, 65536\nM 112,164,84\nbuffer_store_dwordx4 v[48:51], v252, s[20:23], s100 offen offset:2048 sc1\nM 128,160,72\nR 180,186,0\ns_add_u32 s63, s59, 851968\n"
"M 128,164,88\nbuffer_load_dwordx4 v220, s[16:19], s63 offen lds\nM 144,160,76\nR 182,186,4096\nv_cvt_pk_bf16_f32 v59, v62, v63\nM 144,164,92\n"
"G 4\nM 148,168,76\nR 160,191,49152\nLq581:\ns_lshr_b32 s60, s45, 3\ns_lshl2_add_u32 s60, s60, s40\n"
"s_lshl_b32 s60, s60, 8\nv_lshl_add_u32 v224, v252, 3, s60\nv_add_u32 v224, Lq581+4096-LqPC, v224\nv_min_u32 v224, LqE-LqPC-4, v224\ns_mov_b64 exec, 3\nglobal_load_dword v224, v224, s[98:99]\n"
"s_mov_b64 exec, -1\nM 148,172,92\nbuffer_store_dwordx4 v[56:59], v252, s[20:23], s100 offen offset:3072 sc1\nbuffer_load_dwordx4 v221, s[16:19], s63 offen offset:1024 lds\nM 132,168,72\nR 162,191,53248\n"
"M 132,172,88\nM 116,168,68\nR 164,187,0\nM 116,172,84\nM 100,168,64\nR 166,187,4096\n"
"v_mov_b32 v248, 0\nM 100,172,80\nG 4\nv_mov_b32 v249, 0\nM 104,176,64\nR 168,184,8192\n"
"s_add_u32 s61, s74, 548864\ns_mov_b64 exec, 3\nbuffer_atomic_or_x2 v[248:249], v250, s[20:23], s61 offen sc0 sc1\ns_mov_b64 exec, -1\nM 104,180,80\nM 120,176,68\n"
"R 170,184,12288\nv_mov_b32 v242, v208\nM 120,180,84\nv_mov_b32 v243, v209\nv_mov_b32 v244, v208\nM 136,176,72\n"
"R 172,184,16384\nv_mov_b32 v245, v209\nM 136,180,88\nv_mov_b32 v64, v192\nv_mov_b32 v65, v193\nM 152,176,76\n"
"R 174,184,20480\nv_mov_b32 v70, v194\nM 152,180,92\nG 4\nv_mov_b32 v71, v195\nv_mov_b32 v72, v196\n"
"M 156,160,76\nR 176,185,8192\nv_mov_b32 v73, v197\nM 156,164,92\nv_mov_b32 v78, v198\nv_mov_b32 v79, v199\n"
"M 140,160,72\nR 178,185,12288\nv_mov_b32 v80, v200\nM 140,164,88\nv_mov_b32 v81, v201\nv_mov_b32 v86, v202\n"
"M 124,160,68\nR 180,185,16384\nv_mov_b32 v87, v203\nM 124,164,84\nv_mov_b32 v88, v204\nv_mov_b32 v89, v205\n"
"M 108,160,64\nR 182,185,20480\nv_mov_b32 v94, v206\nM 108,164,80\nG 4\nv_mov_b32 v95, v207\n"
"v_mov_b32 v32, v232\nM 96,168,96\nR 160,186,8192\nv_mov_b32 v33, v233\nM 96,172,112\nv_mov_b32 v38, v234\n"
"v_mov_b32 v39, v235\nM 112,168,100\nR 162,186,12288\nv_mov_b32 v40, v236\nM 112,172,116\nv_mov_b32 v41, v237\n"
"v_mov_b32 v46, v238\nM 128,168,104\nR 164,186,16384\nv_mov_b32 v47, v239\nM 128,172,120\nv_mov_b32 v48, v232\n"
"v_mov_b32 v49, v233\nM 144,168,108\nR 166,186,20480\nv_mov_b32 v54, v234\nM 144,172,124\nG 4\n"
"v_mov_b32 v55, v235\nv_mov_b32 v56, v236\nM 148,176,108\nR 168,187,8192\nv_mov_b32 v57, v237\nM 148,180,124\n"
"v_mov_b32 v62, v238\nv_mov_b32 v63, v239\nM 132,176,104\nR 170,187,12288\nM 132,180,120\nM 116,176,100\n"
"R 172,187,16384\nM 116,180,116\nM 100,176,96\nR 174,187,20480\nM 100,180,112\nG 4\n"
"M 104,160,96\nR 176,184,24576\ns_waitcnt vmcnt(9)\ns_barrier\nM 104,164,112\nM 120,160,100\n"
"R 178,184,28672\nM 120,164,116\nM 136,160,104\nR 180,184,32768\nM 136,164,120\nM 152,160,108\n"
"R 182,184,36864\nM 152,164,124\nG 4\nM 156,168,108\nR 160,185,24576\nM 156,172,124\n"
"M 140,168,104\nR 162,185,28672\nM 140,172,120\nM 124,168,100\nR 164,185,32768\nM 124,172,116\n"
"M 108,168,96\nR 166,185,36864\nv_mov_b32 v226, s46\nM 108,172,112\nG 4\nv_mov_b32 v227, s47\n"
"M 96,176,128\nR 168,186,24576\ns_bfe_u32 s62, s58, 0x20007\ns_lshl_b32 s62, s62, 5\ns_add_u32 s61, s74, 548880\ns_add_u32 s61, s61, s62\n"
"s_waitcnt vmcnt(0)\ns_mov_b64 exec, 1\nbuffer_atomic_swap_x2 v[226:227], off, s[20:23], s61 sc1\ns_mov_b64 exec, -1\nM 96,180,144\ns_mul_i32 s64, s43, 49152\n"
"M 112,176,132\nR 170,186,28672\nM 112,180,148\ns_add_u32 s64, s64, s72\nM 128,176,136\nR 172,186,32768\n"
"v_cmp_eq_u64 s[86:87], s[46:47], v[248:249]\ns_lshr_b32 s60, s45, 3\ns_lshl2_add_u32 s60, s60, s40\ns_lshl_b32 s60, s60, 8\nv_lshl_add_u32 v224, v252, 3, s60\nv_add_u32 v224, LqM-LqPC, v224\n"
"v_min_u32 v224, LqE-LqPC-4, v224\ns_mov_b64 exec, 3\nglobal_load_dword v224, v224, s[98:99]\ns_mov_b64 exec, -1\nM 128,180,152\ns_add_u32 s64, s64, 52428800\n"
"M 144,176,140\nR 174,186,36864\ns_and_b32 s62, s86, 3\ns_cmp_eq_u32 s62, 3\ns_cbranch_scc0 Lq506\nLq507:\n"
"M 144,180,156\nG 4\nM 148,160,140\nR 176,187,24576\nM 148,164,156\nbuffer_load_dwordx4 v[2:5], v252, s[20:23], s64 offen offset:0 sc1\n"
"M 132,160,136\nR 178,187,28672\nM 132,164,152\nbuffer_load_dwordx4 v[10:13], v252, s[20:23], s64 offen offset:1024 sc1\nM 116,160,132\nR 180,187,32768\n"
"M 116,164,148\ns_mul_i32 s65, s43, 49152\nM 100,160,128\nR 182,187,36864\nM 100,164,144\nbuffer_load_dwordx4 v[18:21], v252, s[20:23], s64 offen offset:2048 sc1\n"
"G 4\nM 104,168,128\nR 160,184,40960\nM 104,172,144\nbuffer_load_dwordx4 v[26:29], v252, s[20:23], s64 offen offset:3072 sc1\nM 120,168,132\n"
"R 162,184,45056\nM 120,172,148\ns_add_u32 s65, s65, s72\nM 136,168,136\nR 164,184,49152\ns_add_u32 s65, s65, 52445184\n"
"M 136,172,152\nbuffer_load_dwordx4 v[66:69], v252, s[20:23], s65 offen offset:0 sc1\nM 152,168,140\nR 166,184,53248\nM 152,172,156\nG 4\n"
"M 156,176,140\nR 168,185,40960\ns_barrier\nM 156,180,156\nbuffer_load_dwordx4 v[74:77], v252, s[20:23], s65 offen offset:1024 sc1\nM 140,176,136\n"
"R 170,185,45056\nv_mov_b32 v246, 0\nM 140,180,152\nbuffer_load_dwordx4 v[82:85], v252, s[20:23], s65 offen offset:2048 sc1\nv_mov_b32 v247, 0\nM 124,176,132\n"
"R 172,185,49152\ns_add_u32 s61, s74, 548864\ns_mov_b64 exec, 4\nbuffer_atomic_or_x2 v[246:247], v250, s[20:23], s61 offen sc0 sc1\ns_mov_b64 exec, -1\nM 124,180,148\n"
"buffer_load_dwordx4 v[90:93], v252, s[20:23], s65 offen offset:3072 sc1\nM 108,176,128\nR 174,185,53248\nM 108,180,144\nG 4\nM 128,160,168\n"
"R 176,186,40960\nM 128,164,184\nR 178,186,45056\nM 144,160,172\nR 180,186,49152\nM 144,164,188\n"
"R 182,186,53248\nG 4\nM 148,168,172\nR 160,187,40960\ns_movk_i32 s63, 6144\nv_mad_u32_u24 v220, v228, s63, v229\n"
"v_add_u32 v221, 3144704, v220\ns_add_u32 m0, s50, 73728\ns_add_u32 s63, s52, 2048\nbuffer_load_dwordx4 v220, s[4:7], s63 offen lds\nM 148,172,188\nR 162,187,45056\n"
"M 132,168,168\nR 164,187,49152\nM 132,172,184\nR 166,187,53248\nG 4\nM 136,176,168\n"
"R 168,184,57344\nbuffer_load_dwordx4 v221, s[4:7], s63 offen offset:1024 lds\nM 136,180,184\nR 170,184,61440\ns_add_u32 m0, s50, 81920\nM 152,176,172\n"
"R 172,188,0\ns_add_u32 s63, s52, 198656\nM 152,180,188\nR 174,188,4096\nG 4\nM 156,160,172\n"
"R 176,185,57344\nbuffer_load_dwordx4 v220, s[4:7], s63 offen lds\nM 156,164,188\nR 178,185,61440\nM 140,160,168\nR 180,189,0\n"
"M 140,164,184\nR 182,189,4096\nG 4\nM 128,168,200\nR 160,186,57344\nbuffer_load_dwordx4 v221, s[4:7], s63 offen offset:1024 lds\n"
"M 128,172,216\nR 162,186,61440\ns_add_u32 m0, s50, 90112\nM 144,168,204\nR 164,190,0\ns_add_u32 s63, s52, 4096\n"
"M 144,172,220\nR 166,190,4096\nG 4\nM 148,176,204\nR 168,187,57344\nbuffer_load_dwordx4 v220, s[4:7], s63 offen lds\n"
"M 148,180,220\nR 170,187,61440\nM 132,176,200\nR 172,191,0\nM 132,180,216\nR 174,191,4096\n"
"G 4\nM 136,160,200\nR 176,184,40960\nbuffer_load_dwordx4 v221, s[4:7], s63 offen offset:1024 lds\nM 136,164,216\nR 178,184,45056\n"
"s_add_u32 m0, s50, 98304\nM 152,160,204\nR 180,184,49152\ns_add_u32 s63, s52, 200704\nM 152,164,220\nR 182,184,53248\n"
"G 4\nM 156,168,204\nR 160,185,40960\nbuffer_load_dwordx4 v220, s[4:7], s63 offen lds\nM 156,172,220\nR 162,185,45056\n"
"M 140,168,200\nR 164,185,49152\nM 140,172,216\nR 166,185,53248\nG 4\nM 96,176,160\n"
"R 168,186,40960\nbuffer_load_dwordx4 v221, s[4:7], s63 offen offset:1024 lds\nM 96,180,176\nR 170,186,45056\ns_mov_b32 m0, s51\nM 112,176,164\n"
"R 172,186,49152\nLpg17:\ns_cbranch_execz LretW0z\nv_cvt_pk_bf16_f32 v128, v128, v129\nv_cvt_pk_bf16_f32 v129, v130, v131\nM 112,180,180\n"
"R 174,186,53248\nG 4\nv_cvt_pk_bf16_f32 v130, v132, v133\nM 116,160,164\nR 176,187,40960\ns_mul_i32 s66, s43, 49152\n"
"v_cvt_pk_bf16_f32 v131, v134, v135\nM 116,164,180\nR 178,187,45056\ns_add_u32 s66, s66, s72\nv_cvt_pk_bf16_f32 v132, v136, v137\nM 100,160,160\n"
"R 180,187,49152\ns_add_u32 s66, s66, 52461568\nv_cvt_pk_bf16_f32 v133, v138, v139\nM 100,164,176\nR 182,187,53248\nG 4\n"
"v_cvt_pk_bf16_f32 v134, v140, v141\nM 104,168,160\nR 160,184,57344\ns_waitcnt vmcnt(9)\nv_cmp_eq_u64 s[86:87], s[46:47], v[246:247]\nv_cvt_pk_bf16_f32 v135, v142, v143\n"
"M 104,172,176\nR 162,184,61440\nv_cvt_pk_bf16_f32 v136, v144, v145\nv_cvt_pk_bf16_f32 v137, v146, v147\nM 120,168,164\nR 164,188,0\n"
"s_and_b32 s62, s86, 4\ns_cmp_eq_u32 s62, 4\ns_cbranch_scc0 Lq522\nLq523:\nv_cvt_pk_bf16_f32 v138, v148, v149\nM 120,172,180\n"
"R 166,188,4096\nG 4\nv_cvt_pk_bf16_f32 v139, v150, v151\nM 124,176,164\nR 168,185,57344\nbuffer_load_dwordx4 v[34:37], v252, s[20:23], s66 offen offset:0 sc1\n"
"v_cvt_pk_bf16_f32 v140, v152, v153\nM 124,180,180\nR 170,185,61440\nv_cvt_pk_bf16_f32 v141, v154, v155\nv_cvt_pk_bf16_f32 v142, v156, v157\nM 108,176,160\n"
"R 172,189,0\nbuffer_load_dwordx4 v[42:45], v252, s[20:23], s66 offen offset:1024 sc1\nds_write_addtid_b32 v128 offset:0\nds_write_addtid_b32 v129 offset:256\nds_write_addtid_b32 v130 offset:512\nds_write_addtid_b32 v131 offset:768\n"
"ds_write_addtid_b32 v132 offset:1024\nds_write_addtid_b32 v133 offset:1280\nds_write_addtid_b32 v134 offset:1536\nds_write_addtid_b32 v135 offset:1792\nds_write_addtid_b32 v136 offset:2048\nds_write_addtid_b32 v137 offset:2304\n"
"ds_write_addtid_b32 v138 offset:2560\nds_write_addtid_b32 v139 offset:2816\nds_write_addtid_b32 v140 offset:3072\nds_write_addtid_b32 v141 offset:3328\nds_write_addtid_b32 v142 offset:3584\nv_cvt_pk_bf16_f32 v143, v158, v159\n"
"ds_write_addtid_b32 v143 offset:3840\nM 108,180,176\nR 174,189,4096\ns_add_u32 m0, s50, 106496\ns_add_u32 s63, s52, 395264\nG 15\n"
"M 96,160,192\nR 176,186,57344\nbuffer_load_dwordx4 v220, s[4:7], s63 offen lds\nM 96,164,208\nR 178,186,61440\nbuffer_load_dwordx4 v[50:53], v252, s[20:23], s66 offen offset:2048 sc1\n"
"G 3\ns_barrier\nM 112,160,196\nR 180,190,0\nds_read2st64_b32 v[0:1], v225 offset0:0 offset1:1\nM 112,164,212\n"
"R 182,190,4096\nds_read2st64_b32 v[6:7], v225 offset0:2 offset1:3\nG 6\nM 116,168,196\nR 160,187,57344\nbuffer_load_dwordx4 v[58:61], v252, s[20:23], s66 offen offset:3072 sc1\n"
"ds_read2st64_b32 v[8:9], v225 offset0:4 offset1:5\nM 116,172,212\nR 162,187,61440\nbuffer_load_dwordx4 v221, s[4:7], s63 offen offset:1024 lds\nds_read2st64_b32 v[14:15], v225 offset0:6 offset1:7\nM 100,168,192\n"
"R 164,191,0\ns_add_u32 m0, s50, 114688\ns_add_u32 s63, s52, 591872\nds_read2st64_b32 v[16:17], v225 offset0:8 offset1:9\nM 100,172,208\nR 166,191,4096\n"
"ds_read2st64_b32 v[22:23], v225 offset0:10 offset1:11\nG 9\nM 104,176,192\nbuffer_load_dwordx4 v220, s[4:7], s63 offen lds\nds_read2st64_b32 v[24:25], v225 offset0:12 offset1:13\nM 104,180,208\n"
"ds_read2st64_b32 v[30:31], v225 offset0:14 offset1:15\nM 120,176,196\nbuffer_load_dwordx4 v221, s[4:7], s63 offen offset:1024 lds\nM 120,180,212\nG 3\nM 124,160,196\n"
"M 124,164,212\nM 108,160,192\nM 108,164,208\nG 0\ns_barrier\ns_lshl_b32 s84, s75, 11\n"
"s_add_u32 s84, s84, s49\ns_add_u32 s84, s84, 18874368\ns_add_u32 s85, s84, 32768\nv_mov_b32 v238, s46\nv_mov_b32 v239, s47\nv_mfma_f32_16x16x32_bf16 v[96:99], v[242:245], v[64:67], v[96:99]\n"
"v_mfma_f32_16x16x32_bf16 v[100:103], v[242:245], v[68:71], v[100:103]\nv_mfma_f32_16x16x32_bf16 v[104:107], v[242:245], v[72:75], v[104:107]\nv_mfma_f32_16x16x32_bf16 v[108:111], v[242:245], v[76:79], v[108:111]\nv_mfma_f32_16x16x32_bf16 v[112:115], v[242:245], v[80:83], v[112:115]\nv_mfma_f32_16x16x32_bf16 v[116:119], v[242:245], v[84:87], v[116:119]\ns_waitcnt vmcnt(16)\n"
"v_mfma_f32_16x16x32_bf16 v[120:123], v[242:245], v[88:91], v[120:123]\nv_mfma_f32_16x16x32_bf16 v[124:127], v[242:245], v[92:95], v[124:127]\nv_mfma_f32_16x16x32_bf16 v[96:99], v[242:245], v[0:3], v[96:99]\nv_mfma_f32_16x16x32_bf16 v[100:103], v[242:245], v[4:7], v[100:103]\nv_mfma_f32_16x16x32_bf16 v[104:107], v[242:245], v[8:11], v[104:107]\nv_mfma_f32_16x16x32_bf16 v[108:111], v[242:245], v[12:15], v[108:111]\n"
"v_mfma_f32_16x16x32_bf16 v[112:115], v[242:245], v[16:19], v[112:115]\nv_mfma_f32_16x16x32_bf16 v[116:119], v[242:245], v[20:23], v[116:119]\nv_mfma_f32_16x16x32_bf16 v[120:123], v[242:245], v[24:27], v[120:123]\nv_mfma_f32_16x16x32_bf16 v[124:127], v[242:245], v[28:31], v[124:127]\ns_waitcnt vmcnt(7)\nv_mfma_f32_16x16x32_bf16 v[96:99], v[242:245], v[32:35], v[96:99]\n"
"v_mfma_f32_16x16x32_bf16 v[100:103], v[242:245], v[36:39], v[100:103]\ns_waitcnt vmcnt(6)\nv_mfma_f32_16x16x32_bf16 v[104:107], v[242:245], v[40:43], v[104:107]\nv_mfma_f32_16x16x32_bf16 v[108:111], v[242:245], v[44:47], v[108:111]\ns_waitcnt vmcnt(4)\nv_mfma_f32_16x16x32_bf16 v[112:115], v[242:245], v[48:51], v[112:115]\n"
"v_mfma_f32_16x16x32_bf16 v[116:119], v[242:245], v[52:55], v[116:119]\ns_waitcnt vmcnt(3)\nv_mfma_f32_16x16x32_bf16 v[120:123], v[242:245], v[56:59], v[120:123]\nv_mfma_f32_16x16x32_bf16 v[124:127], v[242:245], v[60:63], v[124:127]\nv_cvt_pk_bf16_f32 v192, v96, v97\nv_cvt_pk_bf16_f32 v193, v98, v99\n"
"v_cvt_pk_bf16_f32 v194, v100, v101\nv_cvt_pk_bf16_f32 v195, v102, v103\nbuffer_store_dwordx4 v[192:195], v252, s[20:23], s84 offen offset:0 sc1\nv_cvt_pk_bf16_f32 v196, v104, v105\nv_cvt_pk_bf16_f32 v197, v106, v107\nv_cvt_pk_bf16_f32 v198, v108, v109\n"
"v_cvt_pk_bf16_f32 v199, v110, v111\nbuffer_store_dwordx4 v[196:199], v252, s[20:23], s84 offen offset:1024 sc1\nv_cvt_pk_bf16_f32 v200, v112, v113\nv_cvt_pk_bf16_f32 v201, v114, v115\nv_cvt_pk_bf16_f32 v202, v116, v117\nv_cvt_pk_bf16_f32 v203, v118, v119\n"
"buffer_store_dwordx4 v[200:203], v252, s[20:23], s85 offen offset:0 sc1\nv_cvt_pk_bf16_f32 v204, v120, v121\nv_cvt_pk_bf16_f32 v205, v122, v123\nv_cvt_pk_bf16_f32 v206, v124, v125\nv_cvt_pk_bf16_f32 v207, v126, v127\nbuffer_store_dwordx4 v[204:207], v252, s[20:23], s85 offen offset:1024 sc1\n"
"s_mov_b64 exec, 0\ns_cmp_eq_u32 s40, 1\ns_cbranch_scc1 Lpg19\ns_cmp_eq_u32 s40, 2\ns_cbranch_scc1 Lpg20\ns_cmp_eq_u32 s40, 3\n"
"s_cbranch_scc1 Lpg21\ns_branch Lpg18\nLretW0n:\ns_mov_b64 exec, -1\ns_branch LqM\nLq116:\n"
"s_lshl_b32 s61, s42, 13\ns_add_u32 s61, s61, 84017152\nbuffer_load_dwordx4 v[128:131], v230, s[20:23], s61 offen sc1\ns_lshl_b32 s61, s42, 13\ns_add_u32 s61, s61, 84021248\nbuffer_load_dwordx4 v[132:135], v230, s[20:23], s61 offen sc1\n"
"s_waitcnt vmcnt(0)\nv_min3_u32 v232, v129, v131, v133\nv_min3_u32 v232, v232, v135, v135\nv_max3_u32 v233, v129, v131, v133\nv_max3_u32 v233, v233, v135, v135\nv_cmp_ne_u32 s[86:87], s46, v232\n"
"v_cmp_ne_u32 s[88:89], s46, v233\ns_nop 0\ns_or_b64 s[86:87], s[86:87], s[88:89]\ns_cbranch_scc0 Lq117\ns_sub_u32 s48, s48, 1\ns_cmp_gt_i32 s48, 0\n"
"s_cbranch_scc1 Lq116\ns_branch Lq117\nLq124:\ns_lshl_b32 s61, s42, 13\ns_add_u32 s61, s61, 84148224\nbuffer_load_dwordx4 v[144:147], v234, s[20:23], s61 offen sc1\n"
"s_lshl_b32 s61, s42, 13\ns_add_u32 s61, s61, 84152320\nbuffer_load_dwordx4 v[148:151], v234, s[20:23], s61 offen sc1\ns_waitcnt vmcnt(0)\nv_min3_u32 v236, v145, v147, v149\nv_min3_u32 v236, v236, v151, v151\n"
"v_max3_u32 v237, v145, v147, v149\nv_max3_u32 v237, v237, v151, v151\nv_cmp_ne_u32 s[86:87], s46, v236\nv_cmp_ne_u32 s[88:89], s46, v237\ns_nop 0\ns_or_b64 s[86:87], s[86:87], s[88:89]\n"
"s_cbranch_scc0 Lq125\ns_sub_u32 s48, s48, 1\ns_cmp_gt_i32 s48, 0\ns_cbranch_scc1 Lq124\ns_branch Lq125\nLq132:\n"
"s_lshl_b32 s61, s42, 13\ns_add_u32 s61, s61, 83886080\nbuffer_load_dwordx4 v[128:131], v230, s[20:23], s61 offen sc1\ns_lshl_b32 s61, s42, 13\ns_add_u32 s61, s61, 83890176\nbuffer_load_dwordx4 v[132:135], v230, s[20:23], s61 offen sc1\n"
"s_waitcnt vmcnt(0)\nv_min3_u32 v232, v129, v131, v133\nv_min3_u32 v232, v232, v135, v135\nv_max3_u32 v233, v129, v131, v133\nv_max3_u32 v233, v233, v135, v135\nv_cmp_ne_u32 s[86:87], s46, v232\n"
"v_cmp_ne_u32 s[88:89], s46, v233\ns_nop 0\ns_or_b64 s[86:87], s[86:87], s[88:89]\ns_cbranch_scc0 Lq133\ns_sub_u32 s48, s48, 1\ns_cmp_gt_i32 s48, 0\n"
"s_cbranch_scc1 Lq132\ns_branch Lq133\nLq140:\ns_lshr_b32 s84, s49, 4\ns_add_u32 s84, s84, 81788928\nbuffer_load_dwordx4 v[128:131], v252, s[20:23], s84 offen offset:0 sc1\n"
"buffer_load_dwordx4 v[132:135], v252, s[20:23], s84 offen offset:1024 sc1\nbuffer_load_dwordx4 v[136:139], v252, s[20:23], s84 offen offset:2048 sc1\nbuffer_load_dwordx4 v[140:143], v252, s[20:23], s84 offen offset:3072 sc1\ns_mov_b32 s85, 0x0c0c0400\ns_waitcnt vmcnt(3)\nv_perm_b32 v144, v129, v128, s85\n"
"v_perm_b32 v148, v131, v130, s85\nv_lshl_or_b32 v144, v148, 16, v144\ns_waitcnt vmcnt(2)\nv_perm_b32 v145, v133, v132, s85\nv_perm_b32 v149, v135, v134, s85\nv_lshl_or_b32 v145, v149, 16, v145\n"
"s_waitcnt vmcnt(1)\nv_perm_b32 v146, v137, v136, s85\nv_perm_b32 v150, v139, v138, s85\nv_lshl_or_b32 v146, v150, 16, v146\ns_waitcnt vmcnt(0)\nv_perm_b32 v147, v141, v140, s85\n"
"v_perm_b32 v151, v143, v142, s85\nv_lshl_or_b32 v147, v151, 16, v147\nv_min3_u32 v230, v144, v145, v146\nv_min3_u32 v230, v230, v147, v147\nv_max3_u32 v231, v144, v145, v146\nv_max3_u32 v231, v231, v147, v147\n"
"v_cmp_ne_u32 s[86:87], s46, v230\nv_cmp_ne_u32 s[88:89], s46, v231\ns_nop 0\ns_or_b64 s[86:87], s[86:87], s[88:89]\ns_cbranch_scc0 Lq141\ns_sub_u32 s48, s48, 1\n"
"s_cmp_gt_i32 s48, 0\ns_cbranch_scc1 Lq140\ns_branch Lq141\nLq187:\ns_mov_b64 exec, s[66:67]\nbuffer_load_dwordx4 v[230:233], v252, s[20:23], s65 offen offset:0 sc1\n"
"buffer_load_dwordx2 v[64:65], v227, s[20:23], s65 offen offset:2048 sc1\nbuffer_load_dwordx2 v[70:71], v227, s[20:23], s65 offen offset:2560 sc1\ns_mov_b64 exec, -1\ns_waitcnt vmcnt(0)\nv_max3_u32 v248, v233, v65, v71\nv_cmp_eq_u32 vcc, -1, v248\n"
"s_nop 1\ns_cbranch_vccz Lq188\ns_sub_u32 s48, s48, 1\ns_cmp_gt_i32 s48, 0\ns_cbranch_scc1 Lq187\ns_branch Lq188\n"
"Lq189:\ns_mov_b64 exec, s[66:67]\nbuffer_load_dwordx4 v[234:237], v252, s[20:23], s65 offen offset:1024 sc1\nbuffer_load_dwordx2 v[76:77], v227, s[20:23], s65 offen offset:3072 sc1\nbuffer_load_dwordx2 v[82:83], v227, s[20:23], s65 offen offset:3584 sc1\ns_mov_b64 exec, -1\n"
"s_waitcnt vmcnt(0)\nv_max3_u32 v248, v237, v77, v83\nv_cmp_eq_u32 vcc, -1, v248\ns_nop 1\ns_cbranch_vccz Lq190\ns_sub_u32 s48, s48, 1\n"
"s_cmp_gt_i32 s48, 0\ns_cbranch_scc1 Lq189\ns_branch Lq190\nLq317:\ns_lshr_b32 s84, s49, 4\ns_add_u32 s84, s84, 82313216\n"
"buffer_load_dwordx4 v[0:3], v252, s[20:23], s84 offen offset:0 sc1\nbuffer_load_dwordx4 v[4:7], v252, s[20:23], s84 offen offset:1024 sc1\nbuffer_load_dwordx4 v[8:11], v252, s[20:23], s84 offen offset:2048 sc1\nbuffer_load_dwordx4 v[12:15], v252, s[20:23], s84 offen offset:3072 sc1\ns_mov_b32 s85, 0x0c0c0400\ns_waitcnt vmcnt(3)\n"
"v_perm_b32 v16, v1, v0, s85\nv_perm_b32 v20, v3, v2, s85\nv_lshl_or_b32 v16, v20, 16, v16\ns_waitcnt vmcnt(2)\nv_perm_b32 v17, v5, v4, s85\nv_perm_b32 v21, v7, v6, s85\n"
"v_lshl_or_b32 v17, v21, 16, v17\ns_waitcnt vmcnt(1)\nv_perm_b32 v18, v9, v8, s85\nv_perm_b32 v22, v11, v10, s85\nv_lshl_or_b32 v18, v22, 16, v18\ns_waitcnt vmcnt(0)\n"
"v_perm_b32 v19, v13, v12, s85\nv_perm_b32 v23, v15, v14, s85\nv_lshl_or_b32 v19, v23, 16, v19\nv_min3_u32 v230, v16, v17, v18\nv_min3_u32 v230, v230, v19, v19\nv_max3_u32 v231, v16, v17, v18\n"
"v_max3_u32 v231, v231, v19, v19\nv_cmp_ne_u32 s[86:87], s46, v230\nv_cmp_ne_u32 s[88:89], s46, v231\ns_nop 0\ns_or_b64 s[86:87], s[86:87], s[88:89]\ns_cbranch_scc0 Lq318\n"
"s_sub_u32 s48, s48, 1\ns_cmp_gt_i32 s48, 0\ns_cbranch_scc1 Lq317\ns_branch Lq318\nLq319:\ns_lshl_b32 s61, s42, 13\n"
"s_add_u32 s61, s61, 84279296\nbuffer_load_dwordx4 v[0:3], v238, s[20:23], s61 offen sc1\ns_lshl_b32 s61, s42, 13\ns_add_u32 s61, s61, 84283392\nbuffer_load_dwordx4 v[4:7], v238, s[20:23], s61 offen sc1\ns_waitcnt vmcnt(0)\n"
"v_min3_u32 v240, v1, v3, v5\nv_min3_u32 v240, v240, v7, v7\nv_max3_u32 v241, v1, v3, v5\nv_max3_u32 v241, v241, v7, v7\nv_cmp_ne_u32 s[86:87], s46, v240\nv_cmp_ne_u32 s[88:89], s46, v241\n"
"s_nop 0\ns_or_b64 s[86:87], s[86:87], s[88:89]\ns_cbranch_scc0 Lq320\ns_sub_u32 s48, s48, 1\ns_cmp_gt_i32 s48, 0\ns_cbranch_scc1 Lq319\n"
"s_branch Lq320\nLq329:\ns_lshl_b32 s61, s42, 13\ns_add_u32 s61, s61, 84410368\nbuffer_load_dwordx4 v[16:19], v242, s[20:23], s61 offen sc1\ns_lshl_b32 s61, s42, 13\n"
"s_add_u32 s61, s61, 84414464\nbuffer_load_dwordx4 v[20:23], v242, s[20:23], s61 offen sc1\ns_waitcnt vmcnt(0)\nv_min3_u32 v244, v17, v19, v21\nv_min3_u32 v244, v244, v23, v23\nv_max3_u32 v245, v17, v19, v21\n"
"v_max3_u32 v245, v245, v23, v23\nv_cmp_ne_u32 s[86:87], s46, v244\nv_cmp_ne_u32 s[88:89], s46, v245\ns_nop 0\ns_or_b64 s[86:87], s[86:87], s[88:89]\ns_cbranch_scc0 Lq330\n"
"s_sub_u32 s48, s48, 1\ns_cmp_gt_i32 s48, 0\ns_cbranch_scc1 Lq329\ns_branch Lq330\nLq339:\ns_lshl_b32 s61, s42, 13\n"
"s_add_u32 s61, s61, 84541440\nbuffer_load_dwordx4 v[0:3], v238, s[20:23], s61 offen sc1\ns_lshl_b32 s61, s42, 13\ns_add_u32 s61, s61, 84545536\nbuffer_load_dwordx4 v[4:7], v238, s[20:23], s61 offen sc1\ns_waitcnt vmcnt(0)\n"
"v_min3_u32 v240, v1, v3, v5\nv_min3_u32 v240, v240, v7, v7\nv_max3_u32 v241, v1, v3, v5\nv_max3_u32 v241, v241, v7, v7\nv_cmp_ne_u32 s[86:87], s46, v240\nv_cmp_ne_u32 s[88:89], s46, v241\n"
"s_nop 0\ns_or_b64 s[86:87], s[86:87], s[88:89]\ns_cbranch_scc0 Lq340\ns_sub_u32 s48, s48, 1\ns_cmp_gt_i32 s48, 0\ns_cbranch_scc1 Lq339\n"
"s_branch Lq340\nLq349:\ns_lshl_b32 s61, s42, 13\ns_add_u32 s61, s61, 84672512\nbuffer_load_dwordx4 v[16:19], v242, s[20:23], s61 offen sc1\ns_lshl_b32 s61, s42, 13\n"
"s_add_u32 s61, s61, 84676608\nbuffer_load_dwordx4 v[20:23], v242, s[20:23], s61 offen sc1\ns_waitcnt vmcnt(0)\nv_min3_u32 v244, v17, v19, v21\nv_min3_u32 v244, v244, v23, v23\nv_max3_u32 v245, v17, v19, v21\n"
"v_max3_u32 v245, v245, v23, v23\nv_cmp_ne_u32 s[86:87], s46, v244\nv_cmp_ne_u32 s[88:89], s46, v245\ns_nop 0\ns_or_b64 s[86:87], s[86:87], s[88:89]\ns_cbranch_scc0 Lq350\n"
"s_sub_u32 s48, s48, 1\ns_cmp_gt_i32 s48, 0\ns_cbranch_scc1 Lq349\ns_branch Lq350\nLq377:\nv_mov_b32 v248, 0\n"
"v_mov_b32 v249, 0\ns_add_u32 s61, s82, 385024\ns_mov_b64 exec, 0xf\nbuffer_atomic_or_x2 v[248:249], v250, s[20:23], s61 offen sc0 sc1\ns_mov_b64 exec, -1\ns_waitcnt vmcnt(0)\n"
"v_cmp_eq_u64 s[86:87], s[46:47], v[248:249]\ns_nop 1\ns_and_b32 s62, s86, 0xf\ns_cmp_eq_u32 s62, 0xf\ns_cbranch_scc1 Lq378\ns_sleep 1\n"
"s_sub_u32 s48, s48, 1\ns_cmp_gt_i32 s48, 0\ns_cbranch_scc1 Lq377\ns_branch Lq378\nLq412:\nv_mov_b32 v248, 0\n"
"v_mov_b32 v249, 0\ns_add_u32 s61, s82, 385032\ns_mov_b64 exec, 0xf\nbuffer_atomic_or_x2 v[248:249], v250, s[20:23], s61 offen sc0 sc1\ns_mov_b64 exec, -1\ns_waitcnt vmcnt(0)\n"
"v_cmp_eq_u64 s[86:87], s[46:47], v[248:249]\ns_nop 1\ns_and_b32 s62, s86, 0xf\ns_cmp_eq_u32 s62, 0xf\ns_cbranch_scc1 Lq413\ns_sleep 1\n"
"s_sub_u32 s48, s48, 1\ns_cmp_gt_i32 s48, 0\ns_cbranch_scc1 Lq412\ns_branch Lq413\nLq447:\nv_mov_b32 v248, 0\n"
"v_mov_b32 v249, 0\ns_add_u32 s61, s82, 385048\ns_mov_b64 exec, 0xf\nbuffer_atomic_or_x2 v[248:249], v250, s[20:23], s61 offen sc0 sc1\ns_mov_b64 exec, -1\ns_waitcnt vmcnt(0)\n"
"v_cmp_eq_u64 s[86:87], s[46:47], v[248:249]\ns_nop 1\ns_and_b32 s62, s86, 0xf\ns_cmp_eq_u32 s62, 0xf\ns_cbranch_scc1 Lq448\ns_sleep 1\n"
"s_sub_u32 s48, s48, 1\ns_cmp_gt_i32 s48, 0\ns_cbranch_scc1 Lq447\ns_branch Lq448\nLq466:\nv_mov_b32 v248, 0\n"
"v_mov_b32 v249, 0\ns_add_u32 s61, s82, 385040\ns_mov_b64 exec, 0xf\nbuffer_atomic_or_x2 v[248:249], v250, s[20:23], s61 offen sc0 sc1\ns_mov_b64 exec, -1\ns_waitcnt vmcnt(0)\n"
"v_cmp_eq_u64 s[86:87], s[46:47], v[248:249]\ns_nop 1\ns_and_b32 s62, s86, 0xf\ns_cmp_eq_u32 s62, 0xf\ns_cbranch_scc1 Lq467\ns_sleep 1\n"
"s_sub_u32 s48, s48, 1\ns_cmp_gt_i32 s48, 0\ns_cbranch_scc1 Lq466\ns_branch Lq467\nLq506:\nv_mov_b32 v248, 0\n"
"v_mov_b32 v249, 0\ns_add_u32 s61, s74, 548864\ns_mov_b64 exec, 3\nbuffer_atomic_or_x2 v[248:249], v250, s[20:23], s61 offen sc0 sc1\ns_mov_b64 exec, -1\ns_waitcnt vmcnt(0)\n"
"v_cmp_eq_u64 s[86:87], s[46:47], v[248:249]\ns_nop 1\ns_and_b32 s62, s86, 3\ns_cmp_eq_u32 s62, 3\ns_cbranch_scc1 Lq507\ns_sleep 1\n"
"s_sub_u32 s48, s48, 1\ns_cmp_gt_i32 s48, 0\ns_cbranch_scc1 Lq506\ns_branch Lq507\nLq522:\nv_mov_b32 v246, 0\n"
"v_mov_b32 v247, 0\ns_add_u32 s61, s74, 548864\ns_mov_b64 exec, 4\nbuffer_atomic_or_x2 v[246:247], v250, s[20:23], s61 offen sc0 sc1\ns_mov_b64 exec, -1\ns_waitcnt vmcnt(0)\n"
"v_cmp_eq_u64 s[86:87], s[46:47], v[246:247]\ns_nop 1\ns_and_b32 s62, s86, 4\ns_cmp_eq_u32 s62, 4\ns_cbranch_scc1 Lq523\ns_sleep 1\n"
"s_sub_u32 s48, s48, 1\ns_cmp_gt_i32 s48, 0\ns_cbranch_scc1 Lq522\ns_branch Lq523\nLqM:\ns_add_u32 s61, s79, 88358912\n"
"s_lshl_b32 s62, s75, 3\ns_add_u32 s61, s61, s62\ns_waitcnt vmcnt(0)\ns_mov_b64 exec, 1\nbuffer_atomic_swap_x2 v[238:239], off, s[20:23], s61 sc1\ns_mov_b64 exec, -1\n"
"Lqq1:\nv_bfe_u32 v250, v252, 7, 1\nv_mul_u32_u24 v250, 2176, v250\nv_bfe_u32 v251, v252, 4, 3\nv_lshl_add_u32 v250, v251, 3, v250\ns_lshl_b32 s60, s90, 6\n"
"v_add_u32 v250, s60, v250\ns_sleep 3\nv_mov_b32 v248, 0\nv_mov_b32 v249, 0\ns_add_u32 s61, s80, 88358912\ns_mov_b64 exec, 0xffff\n"
"buffer_atomic_or_x2 v[248:249], v250, s[20:23], s61 offen sc0 sc1\ns_mov_b64 exec, -1\ns_add_u32 m0, s50, 0\ns_add_u32 s63, s52, 397312\nbuffer_load_dwordx4 v220, s[4:7], s63 offen lds\nbuffer_load_dwordx4 v221, s[4:7], s63 offen offset:1024 lds\n"
"s_add_u32 m0, s50, 8192\ns_add_u32 s63, s52, 593920\nbuffer_load_dwordx4 v220, s[4:7], s63 offen lds\nbuffer_load_dwordx4 v221, s[4:7], s63 offen offset:1024 lds\ns_add_u32 m0, s50, 16384\ns_add_u32 s63, s52, 788480\n"
"buffer_load_dwordx4 v220, s[4:7], s63 offen lds\nbuffer_load_dwordx4 v221, s[4:7], s63 offen offset:1024 lds\ns_add_u32 m0, s50, 24576\ns_add_u32 s63, s52, 985088\nbuffer_load_dwordx4 v220, s[4:7], s63 offen lds\nbuffer_load_dwordx4 v221, s[4:7], s63 offen offset:1024 lds\n"
"s_add_u32 m0, s50, 32768\ns_add_u32 s63, s52, 790528\nbuffer_load_dwordx4 v220, s[4:7], s63 offen lds\nbuffer_load_dwordx4 v221, s[4:7], s63 offen offset:1024 lds\ns_waitcnt vmcnt(10)\nv_cmp_eq_u64 s[86:87], s[46:47], v[248:249]\n"
"s_nop 1\ns_and_b32 s88, s86, 0xffff\ns_cmp_eq_u32 s88, 0xffff\ns_cbranch_scc1 Lq595\nv_mov_b32 v246, 0\nv_mov_b32 v247, 0\n"
"s_add_u32 s61, s80, 88358912\ns_mov_b64 exec, 0xffff\nbuffer_atomic_or_x2 v[246:247], v250, s[20:23], s61 offen sc0 sc1\ns_mov_b64 exec, -1\nLq595:\ns_add_u32 m0, s50, 40960\n"
"s_add_u32 s63, s52, 987136\nbuffer_load_dwordx4 v220, s[4:7], s63 offen lds\nbuffer_load_dwordx4 v221, s[4:7], s63 offen offset:1024 lds\ns_mov_b64 exec, 0\ns_cmp_eq_u32 s40, 1\ns_cbranch_scc1 LretQ1z\n"
"s_cmp_eq_u32 s40, 2\ns_cbranch_scc1 LretQ1z\ns_cmp_eq_u32 s40, 3\ns_cbranch_scc1 LretQ1z\ns_branch LretQ1z\nLretQ1z:\n"
"s_mov_b64 exec, -1\nv_mov_b32 v230, 0\nv_mov_b32 v231, 0\nv_mov_b32 v232, 0\nv_mov_b32 v233, 0\nv_dot2c_f32_bf16 v230, s76, v192\n"
"v_dot2c_f32_bf16 v231, v192, v192\nv_dot2c_f32_bf16 v232, s76, v200\nv_dot2c_f32_bf16 v233, v200, v200\nv_dot2c_f32_bf16 v230, s76, v193\nv_dot2c_f32_bf16 v231, v193, v193\nv_dot2c_f32_bf16 v232, s76, v201\n"
"v_dot2c_f32_bf16 v233, v201, v201\nv_dot2c_f32_bf16 v230, s76, v194\nv_dot2c_f32_bf16 v231, v194, v194\nv_dot2c_f32_bf16 v232, s76, v202\nv_dot2c_f32_bf16 v233, v202, v202\nv_dot2c_f32_bf16 v230, s76, v195\n"
"v_dot2c_f32_bf16 v231, v195, v195\nv_dot2c_f32_bf16 v232, s76, v203\nv_dot2c_f32_bf16 v233, v203, v203\nv_dot2c_f32_bf16 v230, s76, v196\nv_dot2c_f32_bf16 v231, v196, v196\nv_dot2c_f32_bf16 v232, s76, v204\n"
"v_dot2c_f32_bf16 v233, v204, v204\nv_dot2c_f32_bf16 v230, s76, v197\nv_dot2c_f32_bf16 v231, v197, v197\nv_dot2c_f32_bf16 v232, s76, v205\nv_dot2c_f32_bf16 v233, v205, v205\nv_dot2c_f32_bf16 v230, s76, v198\n"
"v_dot2c_f32_bf16 v231, v198, v198\nv_dot2c_f32_bf16 v232, s76, v206\nv_dot2c_f32_bf16 v233, v206, v206\nv_dot2c_f32_bf16 v230, s76, v199\nv_dot2c_f32_bf16 v231, v199, v199\nv_dot2c_f32_bf16 v232, s76, v207\n"
"v_dot2c_f32_bf16 v233, v207, v207\nv_lshrrev_b32 v234, 5, v252\nv_and_b32 v234, 24, v234\nv_bfe_u32 v235, s46, v234, 8\nv_lshrrev_b32 v234, 1, v234\nv_and_b32 v236, 0xf0, v252\n"
"v_add_u32 v234, v234, v236\nv_permlane32_swap_b32 v230, v231\nv_permlane32_swap_b32 v232, v233\nv_add_f32 v230, v230, v231\nv_add_f32 v232, v232, v233\ns_nop 1\n"
"v_permlane16_swap_b32 v230, v232\nv_add_f32 v230, v230, v232\ns_movk_i32 s62, 0xff\nv_bfi_b32 v230, s62, v235, v230\ns_lshr_b32 s61, s49, 4\ns_lshl1_add_u32 s61, s53, s61\n"
"s_add_u32 s61, s61, 82837504\nbuffer_store_dword v230, v234, s[20:23], s61 offen sc1\ns_cmp_eq_u32 s88, 0xffff\ns_cbranch_scc1 Lq596\ns_waitcnt vmcnt(3)\nv_mov_b32 v248, v246\n"
"v_mov_b32 v249, v247\nv_cmp_eq_u64 s[86:87], s[46:47], v[248:249]\ns_nop 1\ns_and_b32 s62, s86, 0xffff\ns_cmp_eq_u32 s62, 0xffff\ns_cbranch_scc1 Lq587\n"
"Lq586:\nv_mov_b32 v248, 0\nv_mov_b32 v249, 0\ns_add_u32 s61, s80, 88358912\ns_mov_b64 exec, 0xffff\nbuffer_atomic_or_x2 v[248:249], v250, s[20:23], s61 offen sc0 sc1\n"
"s_mov_b64 exec, -1\ns_waitcnt vmcnt(0)\nv_cmp_eq_u64 s[86:87], s[46:47], v[248:249]\ns_nop 1\ns_and_b32 s62, s86, 0xffff\ns_cmp_eq_u32 s62, 0xffff\n"
"s_cbranch_scc1 Lq587\ns_sub_u32 s48, s48, 1\ns_cmp_gt_i32 s48, 0\ns_cbranch_scc1 Lq586\nLq587:\nLq596:\n"
"s_add_u32 s64, s70, 18874368\nLpg18:\ns_cbranch_execz Lpg22\nbuffer_load_dwordx4 a[0:3], v252, s[20:23], s64 offen offset:0 sc1\ns_add_u32 s65, s70, 18907136\nbuffer_load_dwordx4 a[4:7], v252, s[20:23], s65 offen offset:0 sc1\n"
"s_add_u32 s66, s71, 18874368\nbuffer_load_dwordx4 a[8:11], v252, s[20:23], s66 offen offset:0 sc1\ns_add_u32 s67, s71, 18907136\nbuffer_load_dwordx4 a[12:15], v252, s[20:23], s67 offen offset:0 sc1\nbuffer_load_dwordx4 a[16:19], v252, s[20:23], s64 offen offset:1024 sc1\nbuffer_load_dwordx4 a[20:23], v252, s[20:23], s65 offen offset:1024 sc1\n"
"buffer_load_dwordx4 a[24:27], v252, s[20:23], s66 offen offset:1024 sc1\nbuffer_load_dwordx4 a[28:31], v252, s[20:23], s67 offen offset:1024 sc1\ns_barrier\nR 160,188,8192\nR 162,188,12288\nR 164,188,16384\n"
"R 166,188,20480\nR 168,189,8192\nR 170,189,12288\nR 172,189,16384\nR 174,189,20480\nR 176,190,8192\n"
"R 178,190,12288\nR 180,190,16384\nR 182,190,20480\nbuffer_load_dwordx4 a[32:35], v252, s[20:23], s64 offen offset:2048 sc1\nbuffer_load_dwordx4 a[36:39], v252, s[20:23], s65 offen offset:2048 sc1\nbuffer_load_dwordx4 a[40:43], v252, s[20:23], s66 offen offset:2048 sc1\n"
"buffer_load_dwordx4 a[44:47], v252, s[20:23], s67 offen offset:2048 sc1\nbuffer_load_dwordx4 a[48:51], v252, s[20:23], s64 offen offset:3072 sc1\nbuffer_load_dwordx4 a[52:55], v252, s[20:23], s65 offen offset:3072 sc1\nbuffer_load_dwordx4 a[56:59], v252, s[20:23], s66 offen offset:3072 sc1\nbuffer_load_dwordx4 a[60:63], v252, s[20:23], s67 offen offset:3072 sc1\ns_waitcnt vmcnt(15) lgkmcnt(10)\n"
"v_mfma_f32_16x16x32_bf16 v[0:3], v[160:163], a[0:3], 0\ns_add_u32 s64, s70, 18878464\nbuffer_load_dwordx4 a[64:67], v252, s[20:23], s64 offen offset:0 sc1\ns_waitcnt vmcnt(12) lgkmcnt(8)\nM 0,164,16\nv_mfma_f32_16x16x32_bf16 v[16:19], v[160:163], a[4:7], 0\n"
"s_waitcnt vmcnt(11)\nM 16,164,20\nv_mfma_f32_16x16x32_bf16 v[32:35], v[160:163], a[8:11], 0\ns_waitcnt vmcnt(10)\nM 32,164,24\nv_mfma_f32_16x16x32_bf16 v[48:51], v[160:163], a[12:15], 0\n"
"s_waitcnt vmcnt(9)\nM 48,164,28\nG 4\nv_mfma_f32_16x16x32_bf16 v[52:55], v[168:171], a[12:15], 0\nR 160,191,8192\nM 52,172,28\n"
"s_add_u32 s65, s70, 18911232\nbuffer_load_dwordx4 a[68:71], v252, s[20:23], s65 offen offset:0 sc1\nv_mfma_f32_16x16x32_bf16 v[36:39], v[168:171], a[8:11], 0\nR 162,191,12288\nM 36,172,24\nv_mfma_f32_16x16x32_bf16 v[20:23], v[168:171], a[4:7], 0\n"
"R 164,191,16384\nM 20,172,20\nv_mfma_f32_16x16x32_bf16 v[4:7], v[168:171], a[0:3], 0\nR 166,191,20480\nM 4,172,16\nG 4\n"
"v_mfma_f32_16x16x32_bf16 v[8:11], v[176:179], a[0:3], 0\nR 168,188,24576\nM 8,180,16\ns_add_u32 s66, s71, 18878464\nbuffer_load_dwordx4 a[72:75], v252, s[20:23], s66 offen offset:0 sc1\nv_mfma_f32_16x16x32_bf16 v[24:27], v[176:179], a[4:7], 0\n"
"R 170,188,28672\nM 24,180,20\ns_add_u32 m0, s50, 49152\nv_mfma_f32_16x16x32_bf16 v[40:43], v[176:179], a[8:11], 0\nR 172,188,32768\ns_add_u32 s63, s52, 1181696\n"
"M 40,180,24\ns_add_u32 s67, s71, 18911232\nbuffer_load_dwordx4 a[76:79], v252, s[20:23], s67 offen offset:0 sc1\nv_mfma_f32_16x16x32_bf16 v[56:59], v[176:179], a[12:15], 0\nR 174,188,36864\nM 56,180,28\n"
"G 4\nv_mfma_f32_16x16x32_bf16 v[60:63], v[160:163], a[12:15], 0\nR 176,189,24576\nM 60,164,28\nbuffer_load_dwordx4 v220, s[4:7], s63 offen lds\nv_mfma_f32_16x16x32_bf16 v[44:47], v[160:163], a[8:11], 0\n"
"R 178,189,28672\nM 44,164,24\nv_mfma_f32_16x16x32_bf16 v[28:31], v[160:163], a[4:7], 0\nR 180,189,32768\nM 28,164,20\nbuffer_load_dwordx4 a[80:83], v252, s[20:23], s64 offen offset:1024 sc1\n"
"v_mfma_f32_16x16x32_bf16 v[12:15], v[160:163], a[0:3], 0\nR 182,189,36864\nM 12,164,16\nG 4\nv_mfma_f32_16x16x32_bf16 v[64:67], a[0:3], v[168:171], 0\nR 160,190,24576\n"
"N 64,16,172\nv_mfma_f32_16x16x32_bf16 v[68:71], a[4:7], v[168:171], 0\nR 162,190,28672\nN 68,20,172\nbuffer_load_dwordx4 a[84:87], v252, s[20:23], s65 offen offset:1024 sc1\nv_mfma_f32_16x16x32_bf16 v[96:99], a[8:11], v[168:171], 0\n"
"R 164,190,32768\nN 96,24,172\nv_mfma_f32_16x16x32_bf16 v[100:103], a[12:15], v[168:171], 0\nR 166,190,36864\nN 100,28,172\ns_barrier\n"
"G 4\nv_mfma_f32_16x16x32_bf16 v[108:111], a[12:15], v[176:179], 0\nR 168,191,24576\nN 108,28,180\nbuffer_load_dwordx4 v221, s[4:7], s63 offen offset:1024 lds\nv_mfma_f32_16x16x32_bf16 v[104:107], a[8:11], v[176:179], 0\n"
"R 170,191,28672\nN 104,24,180\nbuffer_load_dwordx4 a[88:91], v252, s[20:23], s66 offen offset:1024 sc1\nv_mfma_f32_16x16x32_bf16 v[76:79], a[4:7], v[176:179], 0\nR 172,191,32768\nN 76,20,180\n"
"v_mfma_f32_16x16x32_bf16 v[72:75], a[0:3], v[176:179], 0\nR 174,191,36864\nN 72,16,180\nG 4\nv_mfma_f32_16x16x32_bf16 v[80:83], a[0:3], v[160:163], 0\nR 176,188,40960\n"
"s_add_u32 s63, s52, 1378304\nN 80,16,164\nbuffer_load_dwordx4 a[92:95], v252, s[20:23], s67 offen offset:1024 sc1\nv_mfma_f32_16x16x32_bf16 v[84:87], a[4:7], v[160:163], 0\nR 178,188,45056\nN 84,20,164\n"
"s_add_u32 m0, s50, 57344\nv_mfma_f32_16x16x32_bf16 v[112:115], a[8:11], v[160:163], 0\nR 180,188,49152\nN 112,24,164\nbuffer_load_dwordx4 v220, s[4:7], s63 offen lds\nv_mfma_f32_16x16x32_bf16 v[116:119], a[12:15], v[160:163], 0\n"
"R 182,188,53248\nN 116,28,164\nG 4\nv_mfma_f32_16x16x32_bf16 v[124:127], a[12:15], v[168:171], 0\nR 160,189,40960\nN 124,28,172\n"
"buffer_load_dwordx4 a[96:99], v252, s[20:23], s64 offen offset:2048 sc1\nv_mfma_f32_16x16x32_bf16 v[120:123], a[8:11], v[168:171], 0\nR 162,189,45056\nN 120,24,172\nv_mfma_f32_16x16x32_bf16 v[92:95], a[4:7], v[168:171], 0\nR 164,189,49152\n"
"N 92,20,172\nv_mfma_f32_16x16x32_bf16 v[88:91], a[0:3], v[168:171], 0\nR 166,189,53248\nN 88,16,172\nG 4\ns_waitcnt vmcnt(19)\n"
"M 0,176,32\nR 168,190,40960\ns_waitcnt vmcnt(15)\nM 0,180,48\nbuffer_load_dwordx4 a[100:103], v252, s[20:23], s65 offen offset:2048 sc1\nM 16,176,36\n"
"R 170,190,45056\ns_waitcnt vmcnt(15)\nM 16,180,52\nM 32,176,40\nR 172,190,49152\ns_waitcnt vmcnt(14)\n"
"M 32,180,56\nbuffer_load_dwordx4 v221, s[4:7], s63 offen offset:1024 lds\nM 48,176,44\nR 174,190,53248\ns_waitcnt vmcnt(14)\nM 48,180,60\n"
"s_barrier\nG 4\nM 52,160,44\nR 176,191,40960\nM 52,164,60\nbuffer_load_dwordx4 a[104:107], v252, s[20:23], s66 offen offset:2048 sc1\n"
"M 36,160,40\nR 178,191,45056\nM 36,164,56\ns_add_u32 m0, s50, 65536\nM 20,160,36\nR 180,191,49152\n"
"s_add_u32 s63, s52, 1183744\nM 20,164,52\nbuffer_load_dwordx4 a[108:111], v252, s[20:23], s67 offen offset:2048 sc1\nM 4,160,32\nR 182,191,53248\nM 4,164,48\n"
"G 4\nM 8,168,32\nR 160,184,0\nM 8,172,48\nbuffer_load_dwordx4 v220, s[4:7], s63 offen lds\nM 24,168,36\n"
"R 162,184,4096\nM 24,172,52\nM 40,168,40\nR 164,184,8192\nM 40,172,56\nbuffer_load_dwordx4 a[112:115], v252, s[20:23], s64 offen offset:3072 sc1\n"
"M 56,168,44\nR 166,184,12288\nM 56,172,60\nG 4\nM 60,176,44\nR 168,185,0\n"
"M 60,180,60\nM 44,176,40\nR 170,185,4096\nM 44,180,56\nbuffer_load_dwordx4 a[116:119], v252, s[20:23], s65 offen offset:3072 sc1\nM 28,176,36\n"
"R 172,185,8192\nM 28,180,52\nM 12,176,32\nR 174,185,12288\nM 12,180,48\nG 4\n"
"N 64,32,160\nR 176,186,0\nN 64,48,164\nbuffer_load_dwordx4 v221, s[4:7], s63 offen offset:1024 lds\nN 68,36,160\nR 178,186,4096\n"
"N 68,52,164\nbuffer_load_dwordx4 a[120:123], v252, s[20:23], s66 offen offset:3072 sc1\nN 96,40,160\nR 180,186,8192\nN 96,56,164\ns_lshr_b32 s84, s49, 4\n"
"N 100,44,160\nR 182,186,12288\ns_add_u32 s84, s84, 82837504\nN 100,60,164\ns_barrier\nG 4\n"
"N 108,44,168\nR 160,187,0\nN 108,60,172\nbuffer_load_dwordx4 a[124:127], v252, s[20:23], s67 offen offset:3072 sc1\nN 104,40,168\nR 162,187,4096\n"
"N 104,56,172\nbuffer_load_dwordx4 v[128:131], v252, s[20:23], s84 offen offset:0 sc1\nN 76,36,168\nR 164,187,8192\nN 76,52,172\nN 72,32,168\n"
"R 166,187,12288\nN 72,48,172\nbuffer_load_dwordx4 v[132:135], v252, s[20:23], s84 offen offset:1024 sc1\nG 4\nN 80,32,176\nR 168,184,16384\n"
"N 80,48,180\ns_add_u32 s64, s70, 18882560\nbuffer_load_dwordx4 a[128:131], v252, s[20:23], s64 offen offset:0 sc1\nN 84,36,176\nR 170,184,20480\nN 84,52,180\n"
"buffer_load_dwordx4 v[136:139], v252, s[20:23], s84 offen offset:2048 sc1\nN 112,40,176\nR 172,184,24576\nN 112,56,180\nN 116,44,176\nR 174,184,28672\n"
"N 116,60,180\nbuffer_load_dwordx4 v[140:143], v252, s[20:23], s84 offen offset:3072 sc1\nG 4\nN 124,44,160\nR 176,185,16384\nN 124,60,164\n"
"s_add_u32 s65, s70, 18915328\nbuffer_load_dwordx4 a[132:135], v252, s[20:23], s65 offen offset:0 sc1\nN 120,40,160\nR 178,185,20480\nN 120,56,164\nN 92,36,160\n"
"R 180,185,24576\nN 92,52,164\nN 88,32,160\nR 182,185,28672\nN 88,48,164\nG 4\n"
"s_waitcnt vmcnt(27)\nM 0,168,64\nR 160,186,16384\ns_waitcnt vmcnt(22)\nM 0,172,80\ns_add_u32 s66, s71, 18882560\n"
"buffer_load_dwordx4 a[136:139], v252, s[20:23], s66 offen offset:0 sc1\nM 16,168,68\nR 162,186,20480\ns_waitcnt vmcnt(22)\nM 16,172,84\nM 32,168,72\n"
"R 164,186,24576\ns_waitcnt vmcnt(20)\nM 32,172,88\ns_add_u32 s67, s71, 18915328\nbuffer_load_dwordx4 a[140:143], v252, s[20:23], s67 offen offset:0 sc1\nM 48,168,76\n"
"R 166,186,28672\ns_waitcnt vmcnt(20)\nM 48,172,92\ns_barrier\nG 4\nM 52,176,76\n"
"R 168,187,16384\nM 52,180,92\nM 36,176,72\nR 170,187,20480\nM 36,180,88\ns_add_u32 m0, s50, 73728\n"
"M 20,176,68\nR 172,187,24576\ns_add_u32 s63, s52, 1380352\nM 20,180,84\nbuffer_load_dwordx4 a[144:147], v252, s[20:23], s64 offen offset:1024 sc1\nM 4,176,64\n"
"R 174,187,28672\nM 4,180,80\nG 4\nM 8,160,64\nR 176,184,32768\nM 8,164,80\n"
"buffer_load_dwordx4 v220, s[4:7], s63 offen lds\nM 24,160,68\nR 178,184,36864\nM 24,164,84\nbuffer_load_dwordx4 a[148:151], v252, s[20:23], s65 offen offset:1024 sc1\nM 40,160,72\n"
"R 180,184,40960\nM 40,164,88\nbuffer_load_dwordx4 v221, s[4:7], s63 offen offset:1024 lds\nM 56,160,76\nR 182,184,45056\nM 56,164,92\n"
"G 4\nM 60,168,76\nR 160,185,32768\nM 60,172,92\nM 44,168,72\nR 162,185,36864\n"
"M 44,172,88\nbuffer_load_dwordx4 a[152:155], v252, s[20:23], s66 offen offset:1024 sc1\nM 28,168,68\nR 164,185,40960\nM 28,172,84\nM 12,168,64\n"
"R 166,185,45056\nM 12,172,80\nG 4\nN 64,64,176\nR 168,186,32768\nN 64,80,180\n"
"buffer_load_dwordx4 a[156:159], v252, s[20:23], s67 offen offset:1024 sc1\nN 68,68,176\nR 170,186,36864\nN 68,84,180\nN 96,72,176\nR 172,186,40960\n"
"s_mov_b32 s85, 0x0c0c0400\nN 96,88,180\ns_add_u32 m0, s50, 81920\ns_waitcnt vmcnt(13)\nv_perm_b32 v144, v129, v128, s85\nv_perm_b32 v148, v131, v130, s85\n"
"N 100,76,176\nR 174,186,45056\ns_add_u32 s63, s52, 1574912\nN 100,92,180\ns_barrier\nG 4\n"
"N 108,76,160\nR 176,187,32768\nv_lshl_or_b32 v144, v148, 16, v144\nN 108,92,164\nbuffer_load_dwordx4 a[160:163], v252, s[20:23], s64 offen offset:2048 sc1\nN 104,72,160\n"
"R 178,187,36864\ns_waitcnt vmcnt(13)\nv_perm_b32 v145, v133, v132, s85\nN 104,88,164\nbuffer_load_dwordx4 v220, s[4:7], s63 offen lds\nN 76,68,160\n"
"R 180,187,40960\nv_perm_b32 v149, v135, v134, s85\nN 76,84,164\nbuffer_load_dwordx4 v221, s[4:7], s63 offen offset:1024 lds\nN 72,64,160\nR 182,187,45056\n"
"s_add_u32 m0, s50, 90112\nN 72,80,164\nG 4\nv_lshl_or_b32 v145, v149, 16, v145\ns_waitcnt vmcnt(13)\nv_perm_b32 v146, v137, v136, s85\n"
"N 80,64,168\nR 160,184,49152\ns_add_u32 s63, s52, 1771520\nN 80,80,172\nbuffer_load_dwordx4 a[164:167], v252, s[20:23], s65 offen offset:2048 sc1\nN 84,68,168\n"
"R 162,184,53248\nv_perm_b32 v150, v139, v138, s85\nN 84,84,172\nbuffer_load_dwordx4 v220, s[4:7], s63 offen lds\nN 112,72,168\nR 164,184,57344\n"
"v_lshl_or_b32 v146, v150, 16, v146\nN 112,88,172\nbuffer_load_dwordx4 v221, s[4:7], s63 offen offset:1024 lds\nN 116,76,168\nR 166,184,61440\ns_waitcnt vmcnt(15)\n"
"v_perm_b32 v147, v141, v140, s85\nN 116,92,172\nG 4\nv_perm_b32 v151, v143, v142, s85\nv_lshl_or_b32 v147, v151, 16, v147\nN 124,76,176\n"
"R 168,185,49152\nv_min3_u32 v230, v144, v145, v146\nN 124,92,180\nbuffer_load_dwordx4 a[168:171], v252, s[20:23], s66 offen offset:2048 sc1\nN 120,72,176\nR 170,185,53248\n"
"v_min3_u32 v230, v230, v147, v147\nN 120,88,180\nv_max3_u32 v231, v144, v145, v146\nv_max3_u32 v231, v231, v147, v147\nN 92,68,176\nR 172,185,57344\n"
"v_cmp_ne_u32 s[86:87], s46, v230\nN 92,84,180\nbuffer_load_dwordx4 a[172:175], v252, s[20:23], s67 offen offset:2048 sc1\nN 88,64,176\nR 174,185,61440\nv_cmp_ne_u32 s[88:89], s46, v231\n"
"N 88,80,180\ns_or_b64 s[86:87], s[86:87], s[88:89]\ns_cbranch_scc1 Lq645\nLq646:\nG 4\nM 0,160,96\n"
"R 176,186,49152\nv_and_b32 v128, 0xffffff00, v128\nM 0,164,112\nv_and_b32 v129, 0xffffff00, v129\nv_and_b32 v130, 0xffffff00, v130\nM 16,160,100\n"
"R 178,186,53248\nv_and_b32 v131, 0xffffff00, v131\nM 16,164,116\ns_add_u32 m0, s50, 98304\nv_and_b32 v132, 0xffffff00, v132\nv_and_b32 v133, 0xffffff00, v133\n"
"M 32,160,104\nR 180,186,57344\ns_add_u32 s63, s52, 1576960\nM 32,164,120\nbuffer_load_dwordx4 a[176:179], v252, s[20:23], s64 offen offset:3072 sc1\nM 48,160,108\n"
"R 182,186,61440\nv_and_b32 v134, 0xffffff00, v134\nM 48,164,124\ns_waitcnt vmcnt(11)\ns_barrier\nG 4\n"
"M 52,168,108\nR 160,187,49152\nv_and_b32 v135, 0xffffff00, v135\nM 52,172,124\nbuffer_load_dwordx4 v220, s[4:7], s63 offen lds\nM 36,168,104\n"
"R 162,187,53248\nv_and_b32 v136, 0xffffff00, v136\nM 36,172,120\nbuffer_load_dwordx4 a[180:183], v252, s[20:23], s65 offen offset:3072 sc1\nM 20,168,100\nR 164,187,57344\n"
"v_and_b32 v137, 0xffffff00, v137\nM 20,172,116\nbuffer_load_dwordx4 v221, s[4:7], s63 offen offset:1024 lds\nM 4,168,96\nR 166,187,61440\ns_add_u32 m0, s50, 106496\n"
"M 4,172,112\nG 4\nv_and_b32 v138, 0xffffff00, v138\nv_and_b32 v139, 0xffffff00, v139\nM 8,176,96\nR 168,188,0\n"
"s_add_u32 s63, s52, 1773568\nM 8,180,112\nbuffer_load_dwordx4 v220, s[4:7], s63 offen lds\nM 24,176,100\nR 170,188,4096\nv_and_b32 v140, 0xffffff00, v140\n"
"M 24,180,116\nbuffer_load_dwordx4 a[184:187], v252, s[20:23], s66 offen offset:3072 sc1\nM 40,176,104\nR 172,188,8192\nv_and_b32 v141, 0xffffff00, v141\nM 40,180,120\n"
"buffer_load_dwordx4 v221, s[4:7], s63 offen offset:1024 lds\nM 56,176,108\nR 174,188,12288\nv_and_b32 v142, 0xffffff00, v142\nM 56,180,124\nG 4\n"
"v_and_b32 v143, 0xffffff00, v143\nv_add_f32 v128, v128, v136\nM 60,160,108\nR 176,189,0\nv_add_f32 v129, v129, v137\nM 60,164,124\n"
"buffer_load_dwordx4 a[188:191], v252, s[20:23], s67 offen offset:3072 sc1\nM 44,160,104\nR 178,189,4096\nv_add_f32 v130, v130, v138\nM 44,164,120\nv_add_f32 v131, v131, v139\n"
"v_add_f32 v132, v132, v140\nM 28,160,100\nR 180,189,8192\nv_add_f32 v133, v133, v141\nM 28,164,116\nv_add_f32 v134, v134, v142\n"
"v_add_f32 v135, v135, v143\nM 12,160,96\nR 182,189,12288\nv_add_f32 v128, v128, v132\nM 12,164,112\nG 4\n"
"v_add_f32 v129, v129, v133\nv_add_f32 v130, v130, v134\nN 64,96,168\nR 160,190,0\nLpg19:\ns_cbranch_execz LretW0n\n"
"v_add_f32 v131, v131, v135\nN 64,112,172\ns_add_u32 s64, s70, 18886656\nbuffer_load_dwordx4 a[192:195], v252, s[20:23], s64 offen offset:0 sc1\nN 68,100,168\nR 162,190,4096\n"
"v_permlane32_swap_b32 v128, v130\nN 68,116,172\nv_permlane32_swap_b32 v129, v131\nv_add_f32 v128, v128, v130\nN 96,104,168\nR 164,190,8192\n"
"v_add_f32 v129, v129, v131\nN 96,120,172\ns_add_u32 m0, s50, 114688\nv_permlane16_swap_b32 v128, v129\nv_add_f32 v230, v128, v129\nN 100,108,168\n"
"R 166,190,12288\ns_add_u32 s63, s52, 1968128\nN 100,124,172\ns_waitcnt vmcnt(11)\ns_barrier\nG 4\n"
"N 108,108,176\nR 168,191,0\nv_mov_b32 v232, v230\nN 108,124,180\ns_add_u32 s65, s70, 18919424\nbuffer_load_dwordx4 a[196:199], v252, s[20:23], s65 offen offset:0 sc1\n"
"N 104,104,176\nR 170,191,4096\nv_permlane16_swap_b32 v230, v232\nN 104,120,180\nbuffer_load_dwordx4 v220, s[4:7], s63 offen lds\nN 76,100,176\n"
"R 172,191,8192\nv_mov_b32 v231, v230\nN 76,116,180\nbuffer_load_dwordx4 v221, s[4:7], s63 offen offset:1024 lds\nN 72,96,176\nR 174,191,12288\n"
"s_add_u32 m0, s50, 0\nN 72,112,180\nG 4\nv_mov_b32 v233, v232\nv_permlane32_swap_b32 v230, v231\nN 80,96,160\n"
"R 176,188,16384\ns_add_u32 s63, s52, 2164736\nN 80,112,164\ns_add_u32 s66, s71, 18886656\nbuffer_load_dwordx4 a[200:203], v252, s[20:23], s66 offen offset:0 sc1\nN 84,100,160\n"
"R 178,188,20480\nv_permlane32_swap_b32 v232, v233\nN 84,116,164\nbuffer_load_dwordx4 v220, s[4:7], s63 offen lds\nN 112,104,160\nR 180,188,24576\n"
"v_mul_f32 v230, 0xba800000, v230\nN 112,120,164\ns_add_u32 s67, s71, 18919424\nbuffer_load_dwordx4 a[204:207], v252, s[20:23], s67 offen offset:0 sc1\nN 116,108,160\nR 182,188,28672\n"
"v_mul_f32 v214, v230, v230\nN 116,124,164\nG 4\nv_mul_f32 v231, 0x3a800000, v231\nv_sub_f32 v231, v231, v214\nN 124,108,168\n"
"R 160,189,16384\nbuffer_load_dwordx4 v221, s[4:7], s63 offen offset:1024 lds\nN 124,124,172\nv_max_f32 v231, 0, v231\nv_add_f32 v231, 0x3727c5ac, v231\nN 120,104,168\n"
"R 162,189,20480\nv_and_b32 v208, s77, v230\nN 120,120,172\nv_rsq_f32 v234, v231\nN 92,100,168\nR 164,189,24576\n"
"v_mul_f32 v216, v231, v234\nN 92,116,172\nbuffer_load_dwordx4 a[208:211], v252, s[20:23], s64 offen offset:1024 sc1\nN 88,96,168\nR 166,189,28672\nv_sub_f32 v230, v230, v208\n"
"N 88,112,172\nG 4\nv_cvt_pk_bf16_f32 v209, v230, v216\nv_cvt_pk_bf16_f32 v208, v208, v208\nM 0,176,128\nR 168,190,16384\n"
"v_mov_b32 v214, v234\nM 0,180,144\ns_mov_b64 s[60:61], 0xffff\nv_cndmask_b32 v208, 0, v208, s[60:61]\nv_mul_f32 v215, 0x3e8293ee, v234\nM 16,176,132\n"
"R 170,190,20480\nv_mul_f32 v232, 0xba800000, v232\nM 16,180,148\nbuffer_load_dwordx4 a[212:215], v252, s[20:23], s65 offen offset:1024 sc1\nM 32,176,136\nR 172,190,24576\n"
"s_add_u32 s63, s52, 1970176\nM 32,180,152\ns_add_u32 m0, s50, 8192\ns_mov_b64 s[60:61], 0xffff\nv_cndmask_b32 v209, 0, v209, s[60:61]\nM 48,176,140\n"
"R 174,190,28672\nv_mul_f32 v217, v232, v232\nM 48,180,156\ns_waitcnt vmcnt(11)\ns_barrier\nG 4\n"
"M 52,160,140\nR 176,191,16384\nv_mul_f32 v233, 0x3a800000, v233\nM 52,164,156\nbuffer_load_dwordx4 v220, s[4:7], s63 offen lds\nM 36,160,136\n"
"R 178,191,20480\nv_sub_f32 v233, v233, v217\nM 36,164,152\nbuffer_load_dwordx4 a[216:219], v252, s[20:23], s66 offen offset:1024 sc1\nM 20,160,132\nR 180,191,24576\n"
"v_max_f32 v233, 0, v233\nM 20,164,148\nbuffer_load_dwordx4 v221, s[4:7], s63 offen offset:1024 lds\nM 4,160,128\nR 182,191,28672\ns_add_u32 m0, s50, 16384\n"
"M 4,164,144\nG 4\nv_add_f32 v233, 0x3727c5ac, v233\nv_and_b32 v212, s77, v232\nM 8,168,128\nR 160,188,32768\n"
"s_add_u32 s63, s52, 2166784\nM 8,172,144\nbuffer_load_dwordx4 a[220:223], v252, s[20:23], s67 offen offset:1024 sc1\nM 24,168,132\nR 162,188,36864\nv_sub_f32 v232, v232, v212\n"
"M 24,172,148\nbuffer_load_dwordx4 v220, s[4:7], s63 offen lds\nM 40,168,136\nR 164,188,40960\nv_cvt_pk_bf16_f32 v212, v212, v212\nM 40,172,152\n"
"buffer_load_dwordx4 v221, s[4:7], s63 offen offset:1024 lds\nM 56,168,140\nR 166,188,45056\nv_bfe_u32 v230, v252, 2, 6\nM 56,172,156\nG 4\n"
"v_rsq_f32 v235, v233\nM 60,176,140\nR 168,189,32768\nv_mul_f32 v219, v233, v235\nM 60,180,156\nbuffer_load_dwordx4 a[224:227], v252, s[20:23], s64 offen offset:2048 sc1\n"
"M 44,176,136\nR 170,189,36864\nv_cvt_pk_bf16_f32 v213, v232, v219\nM 44,180,152\ns_mov_b64 s[60:61], 0xffff\nv_cndmask_b32 v212, 0, v212, s[60:61]\n"
"v_mov_b32 v217, v235\nM 28,176,132\nR 172,189,40960\nv_mul_f32 v218, 0x3e8293ee, v235\nM 28,180,148\ns_mov_b64 s[60:61], 0xffff\n"
"v_cndmask_b32 v213, 0, v213, s[60:61]\nM 12,176,128\nR 174,189,45056\nM 12,180,144\nG 4\nN 64,128,160\n"
"R 176,190,32768\nN 64,144,164\nbuffer_load_dwordx4 a[228:231], v252, s[20:23], s65 offen offset:2048 sc1\nN 68,132,160\nR 178,190,36864\nN 68,148,164\n"
"s_lshl_b32 s60, s40, 7\ns_add_u32 s60, s60, 161280\nv_add_u32 v230, s60, v230\nN 96,136,160\nR 180,190,40960\nN 96,152,164\n"
"s_add_u32 m0, s50, 24576\nds_write_b32 v230, v214 offset:0\nN 100,140,160\nR 182,190,45056\ns_add_u32 s63, s52, 2361344\nN 100,156,164\n"
"s_waitcnt vmcnt(10)\ns_barrier\nG 5\nN 108,140,168\nR 160,191,32768\nN 108,156,172\n"
"buffer_load_dwordx4 a[232:235], v252, s[20:23], s66 offen offset:2048 sc1\nN 104,136,168\nR 162,191,36864\nN 104,152,172\nbuffer_load_dwordx4 v220, s[4:7], s63 offen lds\nN 76,132,168\n"
"R 164,191,40960\nLq688:\ns_lshr_b32 s60, s45, 3\ns_lshl2_add_u32 s60, s60, s40\ns_lshl_b32 s60, s60, 8\nv_lshl_add_u32 v224, v252, 3, s60\n"
"v_add_u32 v224, Lq688+4096-LqPC, v224\nv_min_u32 v224, LqE-LqPC-4, v224\ns_mov_b64 exec, 3\nglobal_load_dword v224, v224, s[98:99]\ns_mov_b64 exec, -1\nN 76,148,172\n"
"buffer_load_dwordx4 a[236:239], v252, s[20:23], s67 offen offset:2048 sc1\nN 72,128,168\nR 166,191,45056\nN 72,144,172\nds_write_b32 v230, v217 offset:64\nG 5\n"
"N 80,128,176\nR 168,188,49152\nbuffer_load_dwordx4 v221, s[4:7], s63 offen offset:1024 lds\nN 80,144,180\ns_add_u32 m0, s50, 32768\ns_add_u32 s63, s52, 2557952\n"
"buffer_load_dwordx4 v220, s[4:7], s63 offen lds\nN 84,132,176\nR 170,188,53248\nN 84,148,180\nN 112,136,176\nR 172,184,0\n"
"N 112,152,180\nbuffer_load_dwordx4 a[240:243], v252, s[20:23], s64 offen offset:3072 sc1\nN 116,140,176\nR 174,184,4096\nN 116,156,180\nG 5\n"
"N 124,140,160\nR 176,189,49152\nbuffer_load_dwordx4 v221, s[4:7], s63 offen offset:1024 lds\nN 124,156,164\nN 120,136,160\nR 178,189,53248\n"
"N 120,152,164\nbuffer_load_dwordx4 a[244:247], v252, s[20:23], s65 offen offset:3072 sc1\nN 92,132,160\nR 180,185,0\nN 92,148,164\nN 88,128,160\n"
"R 182,185,4096\nN 88,144,164\nG 4\nM 0,168,160\nR 160,190,49152\nM 0,172,176\n"
"M 16,168,164\nR 162,190,53248\nM 16,172,180\nbuffer_load_dwordx4 a[248:251], v252, s[20:23], s66 offen offset:3072 sc1\nM 32,168,168\nR 164,186,0\n"
"M 32,172,184\ns_add_u32 m0, s50, 40960\nM 48,168,172\nR 166,186,4096\ns_add_u32 s63, s52, 2754560\nM 48,172,188\n"
"s_waitcnt vmcnt(12)\ns_barrier\nG 4\nM 52,176,172\nR 168,191,49152\nM 52,180,188\n"
"buffer_load_dwordx4 a[252:255], v252, s[20:23], s67 offen offset:3072 sc1\nM 36,176,168\nR 170,191,53248\nM 36,180,184\nbuffer_load_dwordx4 v220, s[4:7], s63 offen lds\nM 20,176,164\n"
"R 172,187,0\nM 20,180,180\nbuffer_load_dwordx4 v221, s[4:7], s63 offen offset:1024 lds\nM 4,176,160\nR 174,187,4096\ns_add_u32 m0, s50, 49152\n"
"M 4,180,176\nG 4\nM 8,160,160\nR 176,184,8192\ns_add_u32 s63, s52, 2951168\nM 8,164,176\n"
"buffer_load_dwordx4 v220, s[4:7], s63 offen lds\nM 24,160,164\nR 178,184,12288\nM 24,164,180\nM 40,160,168\nR 180,184,16384\n"
"M 40,164,184\nbuffer_load_dwordx4 v221, s[4:7], s63 offen offset:1024 lds\nM 56,160,172\nR 182,184,20480\nM 56,164,188\nG 4\n"
"M 60,168,172\nR 160,185,8192\nM 60,172,188\nM 44,168,168\nR 162,185,12288\nM 44,172,184\n"
"s_add_u32 m0, s50, 57344\nM 28,168,164\nR 164,185,16384\nM 28,172,180\ns_add_u32 s63, s52, 2363392\nM 12,168,160\n"
"R 166,185,20480\nM 12,172,176\nG 4\nN 64,160,176\nR 168,186,8192\nN 64,176,180\n"
"buffer_load_dwordx4 v220, s[4:7], s63 offen lds\nN 68,164,176\nR 170,186,12288\nN 68,180,180\nN 96,168,176\nR 172,186,16384\n"
"N 96,184,180\nbuffer_load_dwordx4 v221, s[4:7], s63 offen offset:1024 lds\nN 100,172,176\nR 174,186,20480\ns_add_u32 m0, s50, 65536\nN 100,188,180\n"
"s_waitcnt vmcnt(9)\ns_barrier\nG 4\nN 108,172,160\nR 176,187,8192\ns_add_u32 s63, s52, 2560000\n"
"N 108,188,164\nbuffer_load_dwordx4 v220, s[4:7], s63 offen lds\nN 104,168,160\nR 178,187,12288\nN 104,184,164\nN 76,164,160\n"
"R 180,187,16384\nN 76,180,164\nN 72,160,160\nR 182,187,20480\nN 72,176,164\nG 4\n"
"N 80,160,168\nR 160,184,24576\nN 80,176,172\nbuffer_load_dwordx4 v221, s[4:7], s63 offen offset:1024 lds\nN 84,164,168\nR 162,184,28672\n"
"s_add_u32 s63, s52, 2756608\nN 84,180,172\ns_add_u32 m0, s50, 73728\nN 112,168,168\nR 164,184,32768\nN 112,184,172\n"
"buffer_load_dwordx4 v220, s[4:7], s63 offen lds\nN 116,172,168\nR 166,184,36864\nN 116,188,172\nG 4\nN 124,172,176\n"
"R 168,185,24576\nN 124,188,180\nbuffer_load_dwordx4 v221, s[4:7], s63 offen offset:1024 lds\nN 120,168,176\nR 170,185,28672\ns_add_u32 s63, s52, 2953216\n"
"N 120,184,180\ns_add_u32 m0, s50, 81920\nN 92,164,176\nR 172,185,32768\nN 92,180,180\nbuffer_load_dwordx4 v220, s[4:7], s63 offen lds\n"
"N 88,160,176\nR 174,185,36864\nN 88,176,180\nG 4\nM 0,160,192\nR 176,186,24576\n"
"s_mov_b32 s60, 156160\nv_bfe_u32 v246, v252, 4, 4\nv_lshl_add_u32 v246, v246, 3, s60\nM 0,164,208\nds_read_b64 v[238:239], v246 offset:0\nM 16,160,196\n"
"R 178,186,28672\nds_read_b64 v[240:241], v246 offset:128\nM 16,164,212\nds_read_b64 v[242:243], v246 offset:256\nM 32,160,200\nR 180,186,32768\n"
"ds_read_b64 v[244:245], v246 offset:384\nM 32,164,216\nds_read_b64 v[246:247], v246 offset:0\nM 48,160,204\nR 182,186,36864\nM 48,164,220\n"
"s_waitcnt vmcnt(7) lgkmcnt(1)\ns_barrier\nM 52,168,204\nR 160,187,24576\nM 52,172,220\nbuffer_load_dwordx4 v221, s[4:7], s63 offen offset:1024 lds\n"
"M 36,168,200\nR 162,187,28672\ns_add_u32 s63, s52, 0\nM 36,172,216\ns_add_u32 m0, s50, 90112\nM 20,168,196\n"
"R 164,187,32768\nM 20,172,212\nbuffer_load_dwordx4 v220, s[4:7], s63 offen lds\nM 4,168,192\nR 166,187,36864\nv_mfma_f32_16x16x32_bf16 v[0:3], v[238:241], v[208:211], v[0:3]\n"
"M 4,172,208\nG 4\nM 8,176,192\nR 168,184,40960\nv_mfma_f32_16x16x32_bf16 v[4:7], v[240:243], v[208:211], v[4:7]\nM 8,180,208\n"
"buffer_load_dwordx4 v221, s[4:7], s63 offen offset:1024 lds\nM 24,176,196\nR 170,184,45056\nv_mfma_f32_16x16x32_bf16 v[8:11], v[242:245], v[208:211], v[8:11]\ns_add_u32 m0, s50, 98304\ns_add_u32 s63, s52, 196608\n"
"M 24,180,212\nbuffer_load_dwordx4 v220, s[4:7], s63 offen lds\nM 40,176,200\nR 172,184,49152\nv_mfma_f32_16x16x32_bf16 v[12:15], v[244:247], v[208:211], v[12:15]\nM 40,180,216\n"
"M 56,176,204\nR 174,184,53248\nv_mfma_f32_16x16x32_bf16 v[16:19], v[244:247], v[210:213], v[16:19]\nM 56,180,220\nG 4\nM 60,160,204\n"
"R 176,185,40960\nv_mfma_f32_16x16x32_bf16 v[20:23], v[238:241], v[210:213], v[20:23]\nM 60,164,220\nbuffer_load_dwordx4 v221, s[4:7], s63 offen offset:1024 lds\nM 44,160,200\nR 178,185,45056\n"
"v_mfma_f32_16x16x32_bf16 v[24:27], v[240:243], v[210:213], v[24:27]\ns_add_u32 m0, s50, 106496\ns_add_u32 s63, s52, 393216\nM 44,164,216\nbuffer_load_dwordx4 v220, s[4:7], s63 offen lds\nM 28,160,196\n"
"R 180,185,49152\nv_mfma_f32_16x16x32_bf16 v[28:31], v[242:245], v[210:213], v[28:31]\nM 28,164,212\nM 12,160,192\nR 182,185,53248\nM 12,164,208\n"
"G 4\nM 0,168,224\nR 160,186,40960\nM 0,172,240\nbuffer_load_dwordx4 v221, s[4:7], s63 offen offset:1024 lds\nM 16,168,228\n"
"R 162,186,45056\ns_add_u32 s63, s52, 589824\nM 16,172,244\ns_add_u32 m0, s50, 114688\nM 32,168,232\nR 164,186,49152\n"
"M 32,172,248\nbuffer_load_dwordx4 v220, s[4:7], s63 offen lds\nM 48,168,236\nR 166,186,53248\nM 48,172,252\ns_waitcnt vmcnt(11)\n"
"s_barrier\nG 4\nM 52,176,236\nR 168,187,40960\nM 52,180,252\nbuffer_load_dwordx4 v221, s[4:7], s63 offen offset:1024 lds\n"
"M 36,176,232\nR 170,187,45056\ns_add_u32 s63, s52, 786432\nM 36,180,248\ns_add_u32 m0, s50, 0\nM 20,176,228\n"
"R 172,187,49152\nM 20,180,244\nbuffer_load_dwordx4 v220, s[4:7], s63 offen lds\nM 4,176,224\nR 174,187,53248\nM 4,180,240\n"
"G 4\nM 8,160,224\nR 176,184,57344\nM 8,164,240\nbuffer_load_dwordx4 v221, s[4:7], s63 offen offset:1024 lds\nM 24,160,228\n"
"R 178,184,61440\nM 24,164,244\ns_add_u32 m0, s50, 8192\nM 40,160,232\nR 180,188,0\nM 40,164,248\n"
"s_add_u32 s63, s52, 983040\nM 56,160,236\nR 182,188,4096\nM 56,164,252\nG 4\nM 60,168,236\n"
"R 160,185,57344\nM 60,172,252\nbuffer_load_dwordx4 v220, s[4:7], s63 offen lds\nM 44,168,232\nR 162,185,61440\nM 44,172,248\n"
"M 28,168,228\nR 164,189,0\nM 28,172,244\nbuffer_load_dwordx4 v221, s[4:7], s63 offen offset:1024 lds\nM 12,168,224\nR 166,189,4096\n"
"M 12,172,240\ns_waitcnt vmcnt(12)\ns_barrier\nG 4\nN 64,192,176\nR 168,186,57344\n"
"N 64,208,180\nLpg20:\ns_cbranch_execz LretW0n\ns_mov_b32 m0, s51\nN 68,196,176\nR 170,186,61440\n"
"N 68,212,180\nds_write_addtid_b32 v32 offset:0\nds_write_addtid_b32 v33 offset:256\nN 96,200,176\nR 172,190,0\nds_write_addtid_b32 v34 offset:512\n"
"N 96,216,180\nds_write_addtid_b32 v35 offset:768\nds_write_addtid_b32 v36 offset:1024\nN 100,204,176\nR 174,190,4096\nds_write_addtid_b32 v37 offset:1280\n"
"N 100,220,180\nds_write_addtid_b32 v38 offset:1536\nds_write_addtid_b32 v39 offset:1792\nG 12\nN 108,204,160\nR 176,187,57344\n"
"ds_write_addtid_b32 v40 offset:2048\nN 108,220,164\nds_write_addtid_b32 v41 offset:2304\nds_write_addtid_b32 v42 offset:2560\nN 104,200,160\nR 178,187,61440\n"
"ds_write_addtid_b32 v43 offset:2816\nN 104,216,164\nds_write_addtid_b32 v44 offset:3072\nds_write_addtid_b32 v45 offset:3328\nN 76,196,160\nR 180,191,0\n"
"ds_write_addtid_b32 v46 offset:3584\nN 76,212,164\nds_write_addtid_b32 v47 offset:3840\nds_write_addtid_b32 v48 offset:4096\nN 72,192,160\nR 182,191,4096\n"
"ds_write_addtid_b32 v49 offset:4352\nN 72,208,164\nds_write_addtid_b32 v50 offset:4608\nds_write_addtid_b32 v51 offset:4864\nG 15\nN 80,192,168\n"
"R 160,188,8192\nds_write_addtid_b32 v52 offset:5120\nN 80,208,172\nds_write_addtid_b32 v53 offset:5376\nds_write_addtid_b32 v54 offset:5632\nN 84,196,168\n"
"R 162,188,12288\nds_write_addtid_b32 v55 offset:5888\nN 84,212,172\nds_write_addtid_b32 v56 offset:6144\nds_write_addtid_b32 v57 offset:6400\nN 112,200,168\n"
"R 164,188,16384\nds_write_addtid_b32 v58 offset:6656\nN 112,216,172\nds_write_addtid_b32 v59 offset:6912\nds_write_addtid_b32 v60 offset:7168\nN 116,204,168\n"
"R 166,188,20480\nds_write_addtid_b32 v61 offset:7424\nN 116,220,172\nds_write_addtid_b32 v62 offset:7680\nds_write_addtid_b32 v63 offset:7936\nG 15\n"
"N 124,204,176\nR 168,189,8192\ns_add_u32 m0, s50, 16384\ns_add_u32 s63, s52, 1179648\nN 124,220,180\nbuffer_load_dwordx4 v220, s[4:7], s63 offen lds\n"
"N 120,200,176\nR 170,189,12288\ns_waitcnt vmcnt(9) lgkmcnt(2)\ns_barrier\nN 120,216,180\nds_read2st64_b32 v[32:33], v225 offset0:0 offset1:1\n"
"ds_read2st64_b32 v[34:35], v225 offset0:2 offset1:3\nN 92,196,176\nR 172,189,16384\nds_read2st64_b32 v[36:37], v225 offset0:4 offset1:5\nN 92,212,180\nds_read2st64_b32 v[38:39], v225 offset0:6 offset1:7\n"
"ds_read2st64_b32 v[40:41], v225 offset0:8 offset1:9\nN 88,192,176\nR 174,189,20480\nds_read2st64_b32 v[42:43], v225 offset0:10 offset1:11\nN 88,208,180\nds_read2st64_b32 v[44:45], v225 offset0:12 offset1:13\n"
"ds_read2st64_b32 v[46:47], v225 offset0:14 offset1:15\nN 64,224,160\nR 176,190,8192\ns_mov_b32 s60, 156672\nv_bfe_u32 v246, v252, 4, 4\nv_lshl_add_u32 v246, v246, 3, s60\n"
"ds_read2st64_b32 v[48:49], v225 offset0:16 offset1:17\nN 64,240,164\nds_read_b64 v[238:239], v246 offset:0\nds_read2st64_b32 v[50:51], v225 offset0:18 offset1:19\nG 13\nv_add_f32 v0, v0, v32\n"
"N 68,228,160\nR 178,190,12288\nds_read_b64 v[240:241], v246 offset:128\nds_read2st64_b32 v[52:53], v225 offset0:20 offset1:21\nN 68,244,164\nds_read_b64 v[242:243], v246 offset:256\n"
"ds_read2st64_b32 v[54:55], v225 offset0:22 offset1:23\nv_add_f32 v1, v1, v33\nN 96,232,160\nR 180,190,16384\nds_read_b64 v[244:245], v246 offset:384\nds_read2st64_b32 v[56:57], v225 offset0:24 offset1:25\n"
"N 96,248,164\nds_read_b64 v[246:247], v246 offset:0\nds_read2st64_b32 v[58:59], v225 offset0:26 offset1:27\nG 15\nv_add_f32 v2, v2, v34\nN 100,236,160\n"
"R 182,190,20480\nds_read2st64_b32 v[60:61], v225 offset0:28 offset1:29\nv_add_f32 v3, v3, v35\nN 100,252,164\nds_read2st64_b32 v[62:63], v225 offset0:30 offset1:31\nv_add_f32 v4, v4, v36\n"
"N 108,236,168\nR 160,191,8192\nv_add_f32 v5, v5, v37\nv_add_f32 v6, v6, v38\nN 108,252,172\nbuffer_load_dwordx4 v221, s[4:7], s63 offen offset:1024 lds\n"
"v_add_f32 v7, v7, v39\nN 104,232,168\nR 162,191,12288\ns_add_u32 s63, s52, 1376256\nv_add_f32 v8, v8, v40\nN 104,248,172\n"
"s_add_u32 m0, s50, 24576\nv_add_f32 v9, v9, v41\nv_add_f32 v10, v10, v42\nv_add_f32 v11, v11, v43\nN 76,228,168\nR 164,191,16384\n"
"v_add_f32 v12, v12, v44\nv_add_f32 v13, v13, v45\nN 76,244,172\nG 14\nv_mfma_f32_16x16x32_bf16 v[64:67], v[208:211], v[238:241], v[64:67]\nbuffer_load_dwordx4 v220, s[4:7], s63 offen lds\n"
"v_add_f32 v14, v14, v46\nN 72,224,168\nR 166,191,20480\nv_add_f32 v15, v15, v47\nv_add_f32 v16, v16, v48\nN 72,240,172\n"
"G 13\nv_mfma_f32_16x16x32_bf16 v[72:75], v[208:211], v[240:243], v[72:75]\nG 6\nv_add_f32 v17, v17, v49\nv_add_f32 v18, v18, v50\nv_add_f32 v19, v19, v51\n"
"N 80,224,176\nR 168,188,24576\nv_add_f32 v20, v20, v52\nv_add_f32 v21, v21, v53\nN 80,240,180\nv_mfma_f32_16x16x32_bf16 v[80:83], v[208:211], v[242:245], v[80:83]\n"
"buffer_load_dwordx4 v221, s[4:7], s63 offen offset:1024 lds\nv_add_f32 v22, v22, v54\nN 84,228,176\nR 170,188,28672\ns_add_u32 m0, s50, 49152\ns_add_u32 s63, s52, 1572864\n"
"v_add_f32 v23, v23, v55\nN 84,244,180\nv_mfma_f32_16x16x32_bf16 v[88:91], v[208:211], v[244:247], v[88:91]\nbuffer_load_dwordx4 v220, s[4:7], s63 offen lds\nv_add_f32 v24, v24, v56\nN 112,232,176\n"
"R 172,188,32768\nv_add_f32 v25, v25, v57\nN 112,248,180\nv_mfma_f32_16x16x32_bf16 v[68:71], v[210:213], v[244:247], v[68:71]\nv_add_f32 v26, v26, v58\nv_add_f32 v27, v27, v59\n"
"N 116,236,176\nR 174,188,36864\nG 9\nv_add_f32 v28, v28, v60\nN 116,252,180\nv_mfma_f32_16x16x32_bf16 v[76:79], v[210:213], v[238:241], v[76:79]\n"
"G 4\nv_add_f32 v29, v29, v61\nv_add_f32 v30, v30, v62\nN 124,236,160\nR 176,189,24576\nv_add_f32 v31, v31, v63\n"
"N 124,252,164\nv_mfma_f32_16x16x32_bf16 v[84:87], v[210:213], v[240:243], v[84:87]\nbuffer_load_dwordx4 v221, s[4:7], s63 offen offset:1024 lds\nN 120,232,160\nR 178,189,28672\ns_add_u32 m0, s50, 57344\n"
"s_add_u32 s63, s52, 1769472\nv_lshrrev_b32 v227, 1, v252\nN 120,248,164\nv_mfma_f32_16x16x32_bf16 v[92:95], v[210:213], v[242:245], v[92:95]\nbuffer_load_dwordx4 v220, s[4:7], s63 offen lds\nv_mul_f32 v16, v16, v217\n"
"N 92,228,160\nR 180,189,32768\ns_lshl4_add_u32 s64, s41, s42\nv_mul_f32 v17, v17, v217\nN 92,244,164\ns_cmp_eq_u32 s40, 3\n"
"s_cselect_b64 s[66:67], -1, 0\ns_lshl_b32 s64, s64, 12\nv_mul_f32 v18, v18, v217\nN 88,224,160\nR 182,189,36864\ns_add_u32 s64, s64, 78643200\n"
"v_mul_f32 v19, v19, v217\nN 88,240,164\ns_waitcnt vmcnt(11)\ns_barrier\nG 4\nv_mul_f32 v20, v20, v217\n"
"v_mfma_f32_16x16x32_bf16 v[128:131], v[168:171], a[0:3], 0\nR 160,190,24576\nv_mul_f32 v21, v21, v217\nv_mul_f32 v22, v22, v217\nM 128,172,16\ns_mov_b32 m0, s51\n"
"v_mul_f32 v23, v23, v217\nv_mul_f32 v24, v24, v217\nv_mul_f32 v25, v25, v217\nv_mfma_f32_16x16x32_bf16 v[144:147], v[168:171], a[4:7], 0\nR 162,190,28672\nv_mul_f32 v26, v26, v217\n"
"v_mul_f32 v27, v27, v217\nM 144,172,20\nv_mul_f32 v28, v28, v217\nv_mul_f32 v29, v29, v217\nv_mul_f32 v30, v30, v217\nv_mfma_f32_16x16x32_bf16 v[32:35], v[168:171], a[8:11], 0\n"
"R 164,190,32768\nv_mul_f32 v31, v31, v217\nv_cvt_pk_bf16_f32 v16, v16, v17\nM 32,172,24\nv_cvt_pk_bf16_f32 v17, v18, v19\nv_cvt_pk_bf16_f32 v18, v20, v21\n"
"v_cvt_pk_bf16_f32 v19, v22, v23\nv_mfma_f32_16x16x32_bf16 v[48:51], v[168:171], a[12:15], 0\nR 166,190,36864\nv_cvt_pk_bf16_f32 v20, v24, v25\nv_cvt_pk_bf16_f32 v21, v26, v27\nM 48,172,28\n"
"ds_write_addtid_b32 v96 offset:0\nG 5\nv_cvt_pk_bf16_f32 v22, v28, v29\nv_cvt_pk_bf16_f32 v23, v30, v31\nv_mul_f32 v0, v0, v214\nv_mfma_f32_16x16x32_bf16 v[52:55], v[176:179], a[12:15], 0\n"
"R 168,191,24576\nds_write_addtid_b32 v97 offset:256\nv_mul_f32 v1, v1, v214\nv_mul_f32 v2, v2, v214\nM 52,180,28\nds_write_addtid_b32 v98 offset:512\n"
"v_mul_f32 v3, v3, v214\nv_mul_f32 v4, v4, v214\nv_mul_f32 v5, v5, v214\nv_mfma_f32_16x16x32_bf16 v[36:39], v[176:179], a[8:11], 0\nR 170,191,28672\nds_write_addtid_b32 v99 offset:768\n"
"v_mul_f32 v6, v6, v214\nv_mul_f32 v7, v7, v214\nM 36,180,24\nds_write_addtid_b32 v100 offset:1024\nv_mul_f32 v8, v8, v214\nv_mul_f32 v9, v9, v214\n"
"v_mul_f32 v10, v10, v214\nv_mfma_f32_16x16x32_bf16 v[148:151], v[176:179], a[4:7], 0\nR 172,191,32768\nds_write_addtid_b32 v101 offset:1280\nv_mul_f32 v11, v11, v214\nv_mul_f32 v12, v12, v214\n"
"M 148,180,20\nds_write_addtid_b32 v102 offset:1536\nv_mul_f32 v13, v13, v214\nv_mul_f32 v14, v14, v214\nv_mul_f32 v15, v15, v214\nv_mfma_f32_16x16x32_bf16 v[132:135], v[176:179], a[0:3], 0\n"
"R 174,191,36864\nds_write_addtid_b32 v103 offset:1792\nv_cvt_pk_bf16_f32 v0, v0, v1\nv_cvt_pk_bf16_f32 v1, v2, v3\nM 132,180,16\nds_write_addtid_b32 v104 offset:2048\n"
"G 13\nv_cvt_pk_bf16_f32 v2, v4, v5\nv_cvt_pk_bf16_f32 v3, v6, v7\nv_cvt_pk_bf16_f32 v4, v8, v9\nv_mfma_f32_16x16x32_bf16 v[136:139], v[160:163], a[0:3], 0\nR 176,188,40960\n"
"ds_write_addtid_b32 v105 offset:2304\nv_cvt_pk_bf16_f32 v5, v10, v11\nv_cvt_pk_bf16_f32 v6, v12, v13\nM 136,164,16\nds_write_addtid_b32 v106 offset:2560\nds_write_addtid_b32 v107 offset:2816\n"
"s_mov_b64 exec, s[66:67]\nbuffer_store_dwordx4 v[16:19], v252, s[20:23], s64 offen offset:0 sc1\ns_mov_b64 exec, -1\nv_cvt_pk_bf16_f32 v7, v14, v15\nv_mfma_f32_16x16x32_bf16 v[152:155], v[160:163], a[4:7], 0\nR 178,188,45056\n"
"ds_write_addtid_b32 v108 offset:3072\nM 152,164,20\nds_write_addtid_b32 v109 offset:3328\nds_write_addtid_b32 v110 offset:3584\nv_mfma_f32_16x16x32_bf16 v[40:43], v[160:163], a[8:11], 0\nR 180,188,49152\n"
"ds_write_addtid_b32 v111 offset:3840\nM 40,164,24\nds_write_addtid_b32 v112 offset:4096\nds_write_addtid_b32 v113 offset:4352\ns_mov_b64 exec, s[66:67]\nbuffer_store_dwordx4 v[20:23], v252, s[20:23], s64 offen offset:1024 sc1\n"
"s_mov_b64 exec, -1\nv_mfma_f32_16x16x32_bf16 v[56:59], v[160:163], a[12:15], 0\nR 182,188,53248\nds_write_addtid_b32 v114 offset:4608\nM 56,164,28\nds_write_addtid_b32 v115 offset:4864\n"
"ds_write_addtid_b32 v116 offset:5120\nG 15\nv_mfma_f32_16x16x32_bf16 v[60:63], v[168:171], a[12:15], 0\nR 160,189,40960\nds_write_addtid_b32 v117 offset:5376\nM 60,172,28\n"
"ds_write_addtid_b32 v118 offset:5632\nds_write_addtid_b32 v119 offset:5888\nv_mfma_f32_16x16x32_bf16 v[44:47], v[168:171], a[8:11], 0\nR 162,189,45056\nds_write_addtid_b32 v120 offset:6144\nM 44,172,24\n"
"ds_write_addtid_b32 v121 offset:6400\nds_write_addtid_b32 v122 offset:6656\nds_write_addtid_b32 v123 offset:6912\nds_write_addtid_b32 v124 offset:7168\nds_write_addtid_b32 v125 offset:7424\nds_write_addtid_b32 v126 offset:7680\n"
"ds_write_addtid_b32 v127 offset:7936\nv_mfma_f32_16x16x32_bf16 v[156:159], v[168:171], a[4:7], 0\nR 164,189,49152\ns_add_u32 m0, s50, 57344\nM 156,172,20\nbuffer_load_dwordx4 v221, s[4:7], s63 offen offset:1024 lds\n"
"v_mfma_f32_16x16x32_bf16 v[140:143], v[168:171], a[0:3], 0\nR 166,189,53248\ns_waitcnt vmcnt(10) lgkmcnt(2)\ns_barrier\nM 140,172,16\nds_read2st64_b32 v[96:97], v225 offset0:0 offset1:1\n"
"ds_read2st64_b32 v[98:99], v225 offset0:2 offset1:3\nv_lshrrev_b32 v226, 8, v252\nM 128,176,32\nR 168,190,40960\ns_add_u32 s63, s52, 1966080\nds_read2st64_b32 v[100:101], v225 offset0:4 offset1:5\n"
"s_lshl_b32 s60, s40, 7\ns_add_u32 s60, s60, 161280\nv_lshl_add_u32 v226, v226, 4, s60\nM 128,180,48\ns_add_u32 m0, s50, 65536\nds_read2st64_b32 v[102:103], v225 offset0:6 offset1:7\n"
"ds_read2st64_b32 v[104:105], v225 offset0:8 offset1:9\nds_read_b128 v[230:233], v226 offset:0\nM 144,176,36\nR 170,190,45056\nds_read2st64_b32 v[106:107], v225 offset0:10 offset1:11\nds_read_b128 v[234:237], v226 offset:64\n"
"M 144,180,52\nbuffer_load_dwordx4 v220, s[4:7], s63 offen lds\nds_read2st64_b32 v[108:109], v225 offset0:12 offset1:13\nM 32,176,40\nR 172,190,49152\nds_read2st64_b32 v[110:111], v225 offset0:14 offset1:15\n"
"M 32,180,56\nds_read2st64_b32 v[112:113], v225 offset0:16 offset1:17\nds_read2st64_b32 v[114:115], v225 offset0:18 offset1:19\nG 14\nv_add_f32 v64, v64, v96\nM 48,176,44\n"
"R 174,190,53248\nds_read2st64_b32 v[116:117], v225 offset0:20 offset1:21\nv_add_f32 v65, v65, v97\nM 48,180,60\nds_read2st64_b32 v[118:119], v225 offset0:22 offset1:23\nds_read2st64_b32 v[120:121], v225 offset0:24 offset1:25\n"
"M 52,160,44\nR 176,191,40960\nds_read2st64_b32 v[122:123], v225 offset0:26 offset1:27\nG 15\nv_add_f32 v66, v66, v98\nM 52,164,60\n"
"buffer_load_dwordx4 v221, s[4:7], s63 offen offset:1024 lds\nds_read2st64_b32 v[124:125], v225 offset0:28 offset1:29\nM 36,160,40\nR 178,191,45056\nds_read2st64_b32 v[126:127], v225 offset0:30 offset1:31\nv_add_f32 v67, v67, v99\n"
"M 36,164,56\ns_add_u32 m0, s50, 73728\nv_add_f32 v68, v68, v100\nv_add_f32 v69, v69, v101\nv_add_f32 v70, v70, v102\nM 148,160,36\n"
"R 180,191,49152\nv_add_f32 v71, v71, v103\nv_add_f32 v72, v72, v104\nM 148,164,52\ns_add_u32 s63, s52, 2162688\nv_add_f32 v73, v73, v105\n"
"G 15\nv_add_f32 v74, v74, v106\nv_add_f32 v75, v75, v107\nM 132,160,32\nR 182,191,53248\nG 15\n"
"v_add_f32 v76, v76, v108\nv_add_f32 v77, v77, v109\nM 132,164,48\nG 10\nv_add_f32 v78, v78, v110\nv_add_f32 v79, v79, v111\n"
"v_add_f32 v80, v80, v112\nM 136,168,32\nR 160,184,0\nv_add_f32 v81, v81, v113\nv_add_f32 v82, v82, v114\nM 136,172,48\n"
"buffer_load_dwordx4 v220, s[4:7], s63 offen lds\nv_add_f32 v83, v83, v115\nM 152,168,36\nR 162,184,4096\nG 11\nv_add_f32 v84, v84, v116\n"
"M 152,172,52\nv_add_f32 v85, v85, v117\nG 10\nv_add_f32 v86, v86, v118\nM 40,168,40\nR 164,184,8192\n"
"v_add_f32 v87, v87, v119\nM 40,172,56\nbuffer_load_dwordx4 v221, s[4:7], s63 offen offset:1024 lds\nM 56,168,44\nR 166,184,12288\ns_add_u32 m0, s50, 81920\n"
"M 56,172,60\nG 4\nv_add_f32 v88, v88, v120\nv_add_f32 v89, v89, v121\nM 60,176,44\nR 168,185,0\n"
"s_add_u32 s63, s52, 2359296\nM 60,180,60\nbuffer_load_dwordx4 v220, s[4:7], s63 offen lds\nM 44,176,40\nR 170,185,4096\nv_add_f32 v90, v90, v122\n"
"M 44,180,56\nv_add_f32 v91, v91, v123\nv_add_f32 v92, v92, v124\nM 156,176,36\nR 172,185,8192\nv_add_f32 v93, v93, v125\n"
"M 156,180,52\nbuffer_load_dwordx4 v221, s[4:7], s63 offen offset:1024 lds\nM 140,176,32\nR 174,185,12288\ns_add_u32 m0, s50, 90112\nM 140,180,48\n"
"G 4\nv_add_f32 v94, v94, v126\nv_add_f32 v95, v95, v127\nM 128,160,64\nR 176,186,0\ns_add_u32 s63, s52, 2555904\n"
"v_mul_f32 v64, v64, v230\nM 128,164,80\nbuffer_load_dwordx4 v220, s[4:7], s63 offen lds\nv_mul_f32 v65, v65, v231\nv_add_u32 v108, 161792, v252\nM 144,160,68\n"
"R 178,186,4096\nds_read_b128 v[112:115], v108\nv_mul_f32 v66, v66, v232\nv_mul_f32 v67, v67, v233\nM 144,164,84\nds_read_b128 v[104:107], v108 offset:1024\n"
"v_mul_f32 v68, v68, v234\nv_mul_f32 v69, v69, v235\nv_mul_f32 v70, v70, v236\nM 32,160,72\nR 180,186,8192\nv_mul_f32 v71, v71, v237\n"
"v_cvt_pk_bf16_f32 v67, v66, v67\nM 32,164,88\nv_cvt_pk_bf16_f32 v66, v64, v65\nv_cvt_pk_bf16_f32 v68, v68, v69\nv_cvt_pk_bf16_f32 v69, v70, v71\nM 48,160,76\n"
"R 182,186,12288\nv_mul_f32 v72, v72, v230\nv_mul_f32 v73, v73, v231\nM 48,164,92\nv_mul_f32 v74, v74, v232\nv_mul_f32 v75, v75, v233\n"
"G 6\nv_mul_f32 v76, v76, v234\nM 52,168,76\nR 160,187,0\nv_mul_f32 v77, v77, v235\nv_mul_f32 v78, v78, v236\n"
"M 52,172,92\nbuffer_load_dwordx4 v221, s[4:7], s63 offen offset:1024 lds\nv_mul_f32 v79, v79, v237\nG 3\nv_lshlrev_b32 v100, 16, v104\nM 36,168,72\n"
"R 162,187,4096\ns_add_u32 s63, s52, 2752512\nv_cvt_pk_bf16_f32 v72, v72, v73\nv_and_b32 v101, s77, v104\nM 36,172,88\ns_add_u32 m0, s50, 98304\n"
"v_cvt_pk_bf16_f32 v73, v74, v75\nv_cvt_pk_bf16_f32 v74, v76, v77\nv_cvt_pk_bf16_f32 v75, v78, v79\nM 148,168,68\nR 164,187,8192\nv_mul_f32 v80, v80, v230\n"
"v_mul_f32 v81, v81, v231\nM 148,172,84\nbuffer_load_dwordx4 v220, s[4:7], s63 offen lds\nv_mul_f32 v82, v82, v232\nM 132,168,64\nR 166,187,12288\n"
"s_waitcnt vmcnt(9)\ns_barrier\nv_mul_f32 v83, v83, v233\nM 132,172,80\nG 4\nv_mul_f32 v84, v84, v234\n"
"v_mul_f32 v85, v85, v235\nv_mul_f32 v86, v86, v236\nM 136,176,64\nR 168,184,16384\nv_mul_f32 v87, v87, v237\nv_cvt_pk_bf16_f32 v78, v80, v81\n"
"M 136,180,80\ns_mov_b64 exec, s[66:67]\nbuffer_store_dwordx2 v[68:69], v227, s[20:23], s64 offen offset:2048 sc1\ns_mov_b64 exec, -1\nv_cvt_pk_bf16_f32 v79, v82, v83\nv_cvt_pk_bf16_f32 v80, v84, v85\n"
"M 152,176,68\nR 170,184,20480\nv_cvt_pk_bf16_f32 v81, v86, v87\nv_mul_f32 v88, v88, v230\nM 152,180,84\nv_mul_f32 v89, v89, v231\n"
"v_mul_f32 v90, v90, v232\nv_mul_f32 v91, v91, v233\nM 40,176,72\nR 172,184,24576\ns_mov_b32 s60, 155648\nv_bfe_u32 v246, v252, 4, 4\n"
"v_lshl_add_u32 v246, v246, 3, s60\nv_mul_f32 v92, v92, v234\nM 40,180,88\nbuffer_load_dwordx4 v221, s[4:7], s63 offen offset:1024 lds\nv_mul_f32 v93, v93, v235\nM 56,176,76\n"
"R 174,184,28672\nds_read_b64 v[238:239], v246 offset:0\nv_mul_f32 v94, v94, v236\nM 56,180,92\nG 5\nv_mul_f32 v95, v95, v237\n"
"v_cvt_pk_bf16_f32 v84, v88, v89\nv_cvt_pk_bf16_f32 v85, v90, v91\nM 60,160,76\nR 176,185,16384\nds_read_b64 v[240:241], v246 offset:128\nLpg21:\n"
"s_cbranch_execz LretW0n\nv_cvt_pk_bf16_f32 v86, v92, v93\nM 60,164,92\ns_add_u32 m0, s50, 106496\ns_mov_b64 exec, s[66:67]\nbuffer_store_dwordx2 v[74:75], v227, s[20:23], s64 offen offset:2560 sc1\n"
"s_mov_b64 exec, -1\nv_cvt_pk_bf16_f32 v87, v94, v95\nv_lshlrev_b32 v102, 16, v105\nM 44,160,72\nR 178,185,20480\nds_read_b64 v[242:243], v246 offset:256\n"
"s_lshl_b32 s60, s40, 12\ns_add_u32 s60, s60, 69632\nv_add_u32 v230, s60, v252\nv_add_u32 v231, s60, v227\nM 44,164,88\ns_add_u32 s63, s52, 2949120\n"
"v_and_b32 v103, s77, v105\nv_lshlrev_b32 v96, 16, v106\nM 156,160,68\nR 180,185,24576\nds_read_b64 v[244:245], v246 offset:384\nM 156,164,84\n"
"buffer_load_dwordx4 v220, s[4:7], s63 offen lds\nM 140,160,64\nR 182,185,28672\nds_read_b64 v[246:247], v246 offset:0\nM 140,164,80\ns_mov_b64 exec, s[66:67]\n"
"buffer_store_dwordx2 v[80:81], v227, s[20:23], s64 offen offset:3072 sc1\ns_mov_b64 exec, -1\nG 9\nv_and_b32 v97, s77, v106\nv_lshlrev_b32 v98, 16, v107\nM 128,168,96\n"
"R 160,186,16384\nv_and_b32 v99, s77, v107\nM 128,172,112\nv_add_f32 v104, s78, v100\nv_add_f32 v105, s78, v101\nM 144,168,100\n"
"R 162,186,20480\nv_add_f32 v106, s78, v102\nM 144,172,116\nv_add_f32 v107, s78, v103\nM 32,168,104\nR 164,186,24576\n"
"G 9\nv_mfma_f32_16x16x32_bf16 v[128:131], v[238:241], v[208:211], v[128:131]\nM 32,172,120\nbuffer_load_dwordx4 v221, s[4:7], s63 offen offset:1024 lds\nM 48,168,108\nR 166,186,28672\n"
"G 8\nv_mfma_f32_16x16x32_bf16 v[132:135], v[240:243], v[208:211], v[132:135]\nM 48,172,124\ns_mov_b64 exec, s[66:67]\nbuffer_store_dwordx2 v[86:87], v227, s[20:23], s64 offen offset:3584 sc1\ns_mov_b64 exec, -1\n"
"G 5\nM 52,176,108\nR 168,187,16384\nG 5\nv_mfma_f32_16x16x32_bf16 v[144:147], v[244:247], v[210:213], v[144:147]\ns_not_b64 exec, s[66:67]\n"
"ds_write_b128 v230, v[16:19] offset:0\nds_write_b128 v230, v[20:23] offset:1024\ns_mov_b64 exec, -1\nM 52,180,124\nM 36,176,104\nR 170,187,20480\n"
"v_mfma_f32_16x16x32_bf16 v[148:151], v[238:241], v[210:213], v[148:151]\nM 36,180,120\ns_movk_i32 s63, 2048\nv_mad_u32_u24 v220, v228, s63, v229\nv_add_u32 v221, 1047552, v220\ns_add_u32 m0, s50, 114688\n"
"s_add_u32 s63, s53, 0\nbuffer_load_dwordx4 v220, s[8:11], s63 offen lds\nM 148,176,100\nR 172,187,24576\nM 148,180,116\ns_not_b64 exec, s[66:67]\n"
"ds_write_b64 v231, v[68:69] offset:2048\nds_write_b64 v231, v[74:75] offset:2560\ns_mov_b64 exec, -1\nM 132,176,96\nR 174,187,28672\nM 132,180,112\n"
"G 8\nM 136,160,96\nR 176,184,49152\nM 136,164,112\nM 152,160,100\nR 178,184,53248\n"
"v_lshrrev_b32 v248, 5, v252\nM 152,164,116\nbuffer_load_dwordx4 v221, s[8:11], s63 offen offset:1024 lds\nM 40,160,104\nR 180,184,57344\nv_lshlrev_b32 v248, 5, v248\n"
"M 40,164,120\ns_and_b32 s60, s41, 7\ns_lshl2_add_u32 s60, s60, s40\nv_add_u32 v248, s60, v248\nM 56,160,108\nR 182,184,61440\n"
"v_mul_u32_u24 v248, 7168, v248\nM 56,164,124\nG 4\nv_bfe_u32 v249, v252, 4, 1\nM 60,168,108\nR 160,185,49152\n"
"s_not_b64 exec, s[66:67]\nds_write_b64 v231, v[80:81] offset:3072\nds_write_b64 v231, v[86:87] offset:3584\ns_mov_b64 exec, -1\nM 60,172,124\ns_sub_u32 s60, s54, s54\n"
"s_add_u32 s60, s60, 128\nv_mul_lo_u32 v249, v249, s60\nM 44,168,104\nR 162,185,53248\nv_add_u32 v248, v248, v249\nM 44,172,120\n"
"s_add_u32 m0, s50, 0\nM 156,168,100\nR 164,185,57344\nM 156,172,116\ns_add_u32 s63, s53, 65536\nM 140,168,96\n"
"R 166,185,61440\nM 140,172,112\nG 6\nM 128,176,128\nR 168,186,49152\ns_waitcnt vmcnt(12) lgkmcnt(4)\n"
"s_barrier\nM 128,180,144\nbuffer_load_dwordx4 v220, s[8:11], s63 offen lds\nM 144,176,132\nR 170,186,53248\nM 144,180,148\n"
"s_cmp_eq_u32 s78, 0\ns_cbranch_scc1 Lq656\nv_mov_b32 v230, 0\nv_mov_b32 v231, 0\nv_mov_b32 v232, 0\nv_mov_b32 v233, 0\n"
"v_mov_b32 v64, 0\nv_mov_b32 v65, 0\nv_mov_b32 v70, 0\nv_mov_b32 v71, 0\nv_mov_b32 v234, 0\nv_mov_b32 v235, 0\n"
"v_mov_b32 v236, 0\nv_mov_b32 v237, 0\nv_mov_b32 v76, 0\nv_mov_b32 v77, 0\nv_mov_b32 v82, 0\nv_mov_b32 v83, 0\n"
"Lq656:\ns_lshl4_add_u32 s65, s41, s42\nM 32,176,136\nR 172,186,57344\ns_lshl_b32 s65, s65, 12\nM 32,180,152\n"
"s_add_u32 s65, s65, 78577664\nM 48,176,140\nR 174,186,61440\ns_bfm_b64 exec, 16, 0\nbuffer_load_dword v249, v248, s[12:15], s54 offen\ns_mov_b64 exec, -1\n"
"M 48,180,156\nG 4\nM 52,160,140\nR 176,187,49152\nM 52,164,156\nbuffer_load_dwordx4 v221, s[8:11], s63 offen offset:1024 lds\n"
"M 36,160,136\nR 178,187,53248\nM 36,164,152\ns_add_u32 m0, s50, 8192\nM 148,160,132\nR 180,187,57344\n"
"M 148,164,148\ns_add_u32 s63, s53, 131072\nM 132,160,128\nR 182,187,61440\nM 132,164,144\nG 4\n"
"M 136,168,128\nR 160,188,0\nM 136,172,144\nbuffer_load_dwordx4 v220, s[8:11], s63 offen lds\nM 152,168,132\nR 162,188,4096\n"
"M 152,172,148\nM 40,168,136\nR 164,188,8192\nM 40,172,152\nM 56,168,140\nR 166,188,12288\n"
"v_mfma_f32_16x16x32_bf16 v[136:139], v[242:245], v[208:211], v[136:139]\ns_bfm_b64 exec, 16, 16\nbuffer_load_dword v249, v248, s[12:15], s54 offen\ns_mov_b64 exec, -1\nM 56,172,156\nG 4\n"
"M 60,176,140\nR 168,189,0\nv_mfma_f32_16x16x32_bf16 v[140:143], v[244:247], v[208:211], v[140:143]\nM 60,180,156\nbuffer_load_dwordx4 v221, s[8:11], s63 offen offset:1024 lds\nM 44,176,136\n"
"R 170,189,4096\nv_mfma_f32_16x16x32_bf16 v[152:155], v[240:243], v[210:213], v[152:155]\ns_add_u32 s63, s53, 196608\nM 44,180,152\ns_add_u32 m0, s50, 16384\nM 156,176,132\n"
"R 172,189,8192\nv_mfma_f32_16x16x32_bf16 v[156:159], v[242:245], v[210:213], v[156:159]\nM 156,180,148\nbuffer_load_dwordx4 v220, s[8:11], s63 offen lds\nM 140,176,128\nR 174,189,12288\n"
"M 140,180,144\nG 4\nM 128,160,160\nR 176,190,0\nM 128,164,176\nM 144,160,164\n"
"R 178,190,4096\nM 144,164,180\nbuffer_load_dwordx4 v221, s[8:11], s63 offen offset:1024 lds\nM 32,160,168\nR 180,190,8192\nM 32,164,184\n"
"s_add_u32 m0, s50, 24576\nM 48,160,172\nR 182,190,12288\ns_bfm_b64 exec, 16, 32\nbuffer_load_dword v249, v248, s[12:15], s54 offen\ns_mov_b64 exec, -1\n"
"M 48,164,188\nG 4\nM 52,168,172\nR 160,191,0\nM 52,172,188\ns_add_u32 s63, s53, 262144\n"
"M 36,168,168\nR 162,191,4096\nM 36,172,184\nbuffer_load_dwordx4 v220, s[8:11], s63 offen lds\nM 148,168,164\nR 164,191,8192\n"
"s_lshl_b32 s60, s40, 12\ns_add_u32 s60, s60, 65536\nv_add_u32 v226, s60, v252\nv_add_u32 v111, s60, v227\nM 148,172,180\nM 132,168,160\n"
"R 166,191,12288\ns_cmp_eq_u32 s40, 0\ns_cselect_b64 s[88:89], 0, -1\ns_cselect_b32 s60, s41, 0\ns_cmp_lg_u32 s60, 0\ns_cselect_b64 s[66:67], -1, 0\n"
"M 132,172,176\nG 4\nM 136,176,160\nR 168,188,16384\nM 136,180,176\ns_mov_b64 exec, s[88:89]\n"
"ds_read_b128 v[230:233], v226 offset:0\nds_read_b64 v[64:65], v111 offset:2048\ns_mov_b64 exec, -1\nM 152,176,164\nR 170,188,20480\ns_waitcnt vmcnt(13) lgkmcnt(1)\n"
"s_barrier\nM 152,180,180\nbuffer_load_dwordx4 v221, s[8:11], s63 offen offset:1024 lds\nM 40,176,168\nR 172,188,24576\nM 40,180,184\n"
"s_mov_b64 exec, s[88:89]\nds_read_b64 v[70:71], v111 offset:2560\nds_read_b128 v[234:237], v226 offset:1024\ns_mov_b64 exec, -1\nM 56,176,172\nR 174,188,28672\n"
"s_bfm_b64 exec, 16, 48\nbuffer_load_dword v249, v248, s[12:15], s54 offen\ns_mov_b64 exec, -1\nM 56,180,188\nM 60,160,172\nR 176,189,16384\n"
"M 60,164,188\ns_mov_b64 exec, s[88:89]\nds_read_b64 v[76:77], v111 offset:3072\nds_read_b64 v[82:83], v111 offset:3584\ns_mov_b64 exec, -1\nM 44,160,168\n"
"R 178,189,20480\nM 44,164,184\nM 156,160,164\nR 180,189,24576\nM 156,164,180\ns_mov_b64 exec, s[66:67]\n"
"buffer_load_dwordx4 v[230:233], v252, s[20:23], s65 offen offset:0 sc1\ns_mov_b64 exec, -1\nM 140,160,160\nR 182,189,28672\nM 140,164,176\nG 6\n"
"M 32,168,200\nR 160,190,16384\ns_mov_b64 exec, s[66:67]\nbuffer_load_dwordx2 v[64:65], v227, s[20:23], s65 offen offset:2048 sc1\ns_mov_b64 exec, -1\nM 32,172,216\n"
"R 162,190,20480\nM 48,168,204\nR 164,190,24576\nM 48,172,220\nR 166,190,28672\nG 4\n"
"M 52,176,204\nR 168,191,16384\ns_mov_b64 exec, s[66:67]\nbuffer_load_dwordx2 v[70:71], v227, s[20:23], s65 offen offset:2560 sc1\ns_mov_b64 exec, -1\nM 52,180,220\n"
"R 170,191,20480\nM 36,176,200\nR 172,191,24576\nM 36,180,216\nR 174,191,28672\nG 4\n"
"M 40,160,200\nR 176,188,32768\ns_mov_b64 exec, s[66:67]\nbuffer_load_dwordx4 v[234:237], v252, s[20:23], s65 offen offset:1024 sc1\ns_mov_b64 exec, -1\nM 40,164,216\n"
"R 178,188,36864\nM 56,160,204\nR 180,188,40960\nM 56,164,220\nR 182,188,45056\nG 4\n"
"M 60,168,204\nR 160,189,32768\ns_mov_b64 exec, s[66:67]\nbuffer_load_dwordx2 v[76:77], v227, s[20:23], s65 offen offset:3072 sc1\ns_mov_b64 exec, -1\nM 60,172,220\n"
"R 162,189,36864\nM 44,168,200\nR 164,189,40960\nM 44,172,216\nR 166,189,45056\nG 4\n"
"M 32,176,232\nR 168,190,32768\ns_mov_b64 exec, s[66:67]\nbuffer_load_dwordx2 v[82:83], v227, s[20:23], s65 offen offset:3584 sc1\ns_mov_b64 exec, -1\nLq690:\n"
"s_lshr_b32 s60, s45, 3\ns_lshl2_add_u32 s60, s60, s40\ns_lshl_b32 s60, s60, 8\nv_lshl_add_u32 v224, v252, 3, s60\nv_add_u32 v224, Lq690+4096-LqPC, v224\nv_min_u32 v224, LqE-LqPC-4, v224\n"
"s_mov_b64 exec, 3\nglobal_load_dword v224, v224, s[98:99]\ns_mov_b64 exec, -1\nM 32,180,248\nR 170,190,36864\nM 48,176,236\n"
"R 172,190,40960\nM 48,180,252\nR 174,190,45056\nG 4\nM 52,160,236\nR 176,191,32768\n"
"M 52,164,252\nR 178,191,36864\nM 36,160,232\nR 180,191,40960\nM 36,164,248\nR 182,191,45056\n"
"G 4\nM 40,168,232\nR 160,188,16384\nM 40,172,248\nR 162,188,20480\nM 56,168,236\n"
"R 164,188,24576\nM 56,172,252\nR 166,188,28672\nG 4\nM 60,176,236\nR 168,189,16384\n"
"M 60,180,252\nR 170,189,20480\nM 44,176,232\nR 172,189,24576\nM 44,180,248\nR 174,189,28672\n"
"G 4\nM 128,160,192\nR 176,190,16384\nM 128,164,208\nR 178,190,20480\ns_mov_b32 m0, s51\n"
"M 144,160,196\nR 180,190,24576\nds_write_addtid_b32 v32 offset:0\nM 144,164,212\nR 182,190,28672\nds_write_addtid_b32 v33 offset:256\n"
"G 6\nM 148,168,196\nR 160,191,16384\nds_write_addtid_b32 v34 offset:512\nM 148,172,212\nR 162,191,20480\n"
"ds_write_addtid_b32 v35 offset:768\nM 132,168,192\nR 164,191,24576\nds_write_addtid_b32 v36 offset:1024\nM 132,172,208\nR 166,191,28672\n"
"ds_write_addtid_b32 v37 offset:1280\nG 9\nM 136,176,192\nR 168,188,32768\nds_write_addtid_b32 v38 offset:1536\nM 136,180,208\n"
"R 170,188,36864\nds_write_addtid_b32 v39 offset:1792\nM 152,176,196\nR 172,188,40960\nds_write_addtid_b32 v40 offset:2048\nM 152,180,212\n"
"R 174,188,45056\nds_write_addtid_b32 v41 offset:2304\nG 9\nM 156,160,196\nR 176,189,32768\nds_write_addtid_b32 v42 offset:2560\n"
"M 156,164,212\nR 178,189,36864\nds_write_addtid_b32 v43 offset:2816\nM 140,160,192\nR 180,189,40960\nds_write_addtid_b32 v44 offset:3072\n"
"ds_write_addtid_b32 v45 offset:3328\nds_write_addtid_b32 v46 offset:3584\nds_write_addtid_b32 v47 offset:3840\nds_write_addtid_b32 v48 offset:4096\nds_write_addtid_b32 v49 offset:4352\nds_write_addtid_b32 v50 offset:4608\n"
"ds_write_addtid_b32 v51 offset:4864\nds_write_addtid_b32 v52 offset:5120\nds_write_addtid_b32 v53 offset:5376\nds_write_addtid_b32 v54 offset:5632\nds_write_addtid_b32 v55 offset:5888\nds_write_addtid_b32 v56 offset:6144\n"
"ds_write_addtid_b32 v57 offset:6400\nds_write_addtid_b32 v58 offset:6656\nds_write_addtid_b32 v59 offset:6912\nds_write_addtid_b32 v60 offset:7168\nds_write_addtid_b32 v61 offset:7424\nds_write_addtid_b32 v62 offset:7680\n"
"ds_write_addtid_b32 v63 offset:7936\nM 140,164,208\nR 182,189,45056\nG 15\nM 128,168,224\nR 160,190,32768\n"
"s_add_u32 m0, s50, 32768\nM 128,172,240\nR 162,190,36864\nG 3\ns_barrier\ns_add_u32 s63, s53, 327680\n"
"M 144,168,228\nR 164,190,40960\nbuffer_load_dwordx4 v220, s[8:11], s63 offen lds\nds_read2st64_b32 v[32:33], v225 offset0:0 offset1:1\nM 144,172,244\nR 166,190,45056\n"
"buffer_load_dwordx4 v221, s[8:11], s63 offen offset:1024 lds\nds_read2st64_b32 v[34:35], v225 offset0:2 offset1:3\nG 6\nM 148,176,228\nR 168,191,32768\ns_add_u32 m0, s50, 40960\n"
"ds_read2st64_b32 v[36:37], v225 offset0:4 offset1:5\nM 148,180,244\nR 170,191,36864\ns_add_u32 s63, s53, 393216\nds_read2st64_b32 v[38:39], v225 offset0:6 offset1:7\nM 132,176,224\n"
"R 172,191,40960\nds_read2st64_b32 v[40:41], v225 offset0:8 offset1:9\nM 132,180,240\nR 174,191,45056\nds_read2st64_b32 v[42:43], v225 offset0:10 offset1:11\nG 9\n"
"M 136,160,224\nbuffer_load_dwordx4 v220, s[8:11], s63 offen lds\nds_read2st64_b32 v[44:45], v225 offset0:12 offset1:13\nM 136,164,240\nds_read2st64_b32 v[46:47], v225 offset0:14 offset1:15\nds_read2st64_b32 v[48:49], v225 offset0:16 offset1:17\n"
"M 152,160,228\nbuffer_load_dwordx4 v221, s[8:11], s63 offen offset:1024 lds\nds_read2st64_b32 v[50:51], v225 offset0:18 offset1:19\nM 152,164,244\nds_read2st64_b32 v[52:53], v225 offset0:20 offset1:21\nds_read2st64_b32 v[54:55], v225 offset0:22 offset1:23\n"
"G 7\nM 156,168,228\nds_read2st64_b32 v[56:57], v225 offset0:24 offset1:25\nds_read2st64_b32 v[58:59], v225 offset0:26 offset1:27\nM 156,172,244\nds_read2st64_b32 v[60:61], v225 offset0:28 offset1:29\n"
"ds_read2st64_b32 v[62:63], v225 offset0:30 offset1:31\nM 140,168,224\nM 140,172,240\nG 7\nv_pk_add_f32 v[144:145], v[144:145], v[48:49]\nG 6\n"
"v_pk_add_f32 v[146:147], v[146:147], v[50:51]\nG 5\nv_pk_add_f32 v[148:149], v[148:149], v[52:53]\nG 4\nv_pk_add_f32 v[150:151], v[150:151], v[54:55]\nG 3\n"
"v_pk_add_f32 v[152:153], v[152:153], v[56:57]\nG 2\nv_pk_add_f32 v[154:155], v[154:155], v[58:59]\nG 1\nv_pk_add_f32 v[156:157], v[156:157], v[60:61]\nG 0\n"
"v_pk_add_f32 v[158:159], v[158:159], v[62:63]\nv_pk_add_f32 v[128:129], v[128:129], v[32:33]\nv_pk_add_f32 v[130:131], v[130:131], v[34:35]\nv_pk_add_f32 v[132:133], v[132:133], v[36:37]\nv_pk_add_f32 v[134:135], v[134:135], v[38:39]\nv_pk_add_f32 v[136:137], v[136:137], v[40:41]\n"
"v_pk_add_f32 v[138:139], v[138:139], v[42:43]\nv_pk_add_f32 v[140:141], v[140:141], v[44:45]\nv_pk_add_f32 v[142:143], v[142:143], v[46:47]\nv_cvt_pk_bf16_f32 v144, v144, v145\nv_cvt_pk_bf16_f32 v145, v146, v147\nv_cvt_pk_bf16_f32 v146, v148, v149\n"
"v_cvt_pk_bf16_f32 v147, v150, v151\nv_cvt_pk_bf16_f32 v152, v152, v153\nv_cvt_pk_bf16_f32 v153, v154, v155\nv_cvt_pk_bf16_f32 v154, v156, v157\nv_cvt_pk_bf16_f32 v155, v158, v159\nv_cvt_pk_bf16_f32 v128, v128, v129\n"
"v_cvt_pk_bf16_f32 v129, v130, v131\nv_cvt_pk_bf16_f32 v130, v132, v133\nv_cvt_pk_bf16_f32 v131, v134, v135\nv_cvt_pk_bf16_f32 v136, v136, v137\nv_cvt_pk_bf16_f32 v137, v138, v139\nv_cvt_pk_bf16_f32 v138, v140, v141\n"
"v_cvt_pk_bf16_f32 v139, v142, v143\nv_mfma_f32_16x16x32_bf16 v[52:55], v[16:19], v[144:147], v[96:99]\nv_mfma_f32_16x16x32_bf16 v[48:51], v[0:3], v[144:147], v[100:103]\nv_mfma_f32_16x16x32_bf16 v[60:63], v[20:23], v[152:155], v[96:99]\nv_mfma_f32_16x16x32_bf16 v[56:59], v[4:7], v[152:155], v[100:103]\ns_waitcnt vmcnt(8)\n"
"v_max3_u32 v248, v233, v65, v71\nv_cmp_eq_u32 vcc, -1, v248\ns_nop 0\nLpg22:\ns_cbranch_execz LretW0n\ns_cbranch_vccnz Lq692\n"
"Lq693:\ns_waitcnt vmcnt(5)\nv_max3_u32 v248, v237, v77, v83\nv_cmp_eq_u32 vcc, -1, v248\ns_nop 0\ns_cbranch_vccnz Lq694\n"
"Lq695:\nv_mfma_f32_16x16x32_bf16 v[36:39], v[0:3], v[128:131], v[96:99]\nv_mfma_f32_16x16x32_bf16 v[32:35], v[230:233], v[128:131], v[104:107]\nv_mfma_f32_16x16x32_bf16 v[44:47], v[4:7], v[136:139], v[96:99]\nv_mfma_f32_16x16x32_bf16 v[40:43], v[234:237], v[136:139], v[104:107]\ns_lshl_b32 s84, s42, 11\n"
"s_add_u32 s84, s84, s49\ns_add_u32 s84, s84, 6291456\ns_add_u32 s85, s84, 32768\nv_mov_b32 v238, s46\nv_mov_b32 v239, s47\nv_max_f32 v52, v52, v53\n"
"v_max_f32 v54, v54, v55\nv_max3_f32 v52, v52, v48, v49\nv_max3_f32 v54, v54, v50, v51\nv_max_f32 v118, v52, v54\nv_max_f32 v60, v60, v61\nv_max_f32 v62, v62, v63\n"
"v_max3_f32 v60, v60, v56, v57\nv_max3_f32 v62, v62, v58, v59\nv_max_f32 v119, v60, v62\nv_max_f32 v36, v36, v37\nv_max_f32 v38, v38, v39\nv_max3_f32 v36, v36, v32, v33\n"
"v_max3_f32 v38, v38, v34, v35\nv_max_f32 v116, v36, v38\nv_max_f32 v44, v44, v45\nv_max_f32 v46, v46, v47\nv_max3_f32 v44, v44, v40, v41\nv_max3_f32 v46, v46, v42, v43\n"
"v_max_f32 v117, v44, v46\nv_permlane32_swap_b32 v116, v118\ns_nop 0\nv_permlane32_swap_b32 v117, v119\nv_max_f32 v116, v116, v118\nv_max_f32 v117, v117, v119\n"
"s_nop 1\nv_permlane16_swap_b32 v116, v117\nv_max_f32 v116, v116, v117\nv_mov_b32 v117, v116\ns_nop 1\nv_permlane16_swap_b32 v116, v117\n"
"v_mov_b32 v118, v116\nv_mov_b32 v119, v117\ns_nop 0\nv_permlane32_swap_b32 v116, v118\nv_permlane32_swap_b32 v117, v119\nv_mul_f32 v116, v116, v215\n"
"v_mul_f32 v118, v118, v218\nv_mul_f32 v117, v117, v215\nv_mul_f32 v119, v119, v218\nv_fma_f32 v52, v52, v218, -v118\nv_fma_f32 v54, v54, v218, -v118\nv_fma_f32 v60, v60, v218, -v119\n"
"v_fma_f32 v62, v62, v218, -v119\nv_fma_f32 v36, v36, v215, -v116\nv_fma_f32 v38, v38, v215, -v116\nv_fma_f32 v44, v44, v215, -v117\nv_fma_f32 v46, v46, v215, -v117\nv_exp_f32 v52, v52\n"
"v_exp_f32 v54, v54\nv_exp_f32 v60, v60\nv_exp_f32 v62, v62\nv_exp_f32 v36, v36\nv_exp_f32 v38, v38\nv_exp_f32 v44, v44\n"
"v_exp_f32 v46, v46\nv_add_f32 v118, v52, v54\nv_add_f32 v119, v60, v62\nv_add_f32 v116, v36, v38\nv_add_f32 v117, v44, v46\ns_nop 0\n"
"v_permlane32_swap_b32 v116, v118\nv_permlane32_swap_b32 v117, v119\nv_add_f32 v116, v116, v118\nv_add_f32 v117, v117, v119\ns_nop 1\nv_permlane16_swap_b32 v116, v117\n"
"v_add_f32 v116, v116, v117\ns_barrier\nv_rcp_f32 v116, v116\ns_nop 0\nv_mov_b32 v117, v116\ns_nop 1\n"
"v_permlane16_swap_b32 v116, v117\nv_mov_b32 v118, v116\nv_mov_b32 v119, v117\ns_nop 0\nv_permlane32_swap_b32 v116, v118\nv_permlane32_swap_b32 v117, v119\n"
"v_mul_f32 v52, v52, v118\nv_mul_f32 v54, v54, v118\nv_mul_f32 v60, v60, v119\nv_mul_f32 v62, v62, v119\nv_mul_f32 v36, v36, v116\nv_mul_f32 v38, v38, v116\n"
"v_mul_f32 v44, v44, v117\nv_mul_f32 v46, v46, v117\nv_cvt_pk_bf16_f32 v52, v52, v52\nv_cvt_pk_bf16_f32 v54, v54, v54\nv_cvt_pk_bf16_f32 v60, v60, v60\nv_cvt_pk_bf16_f32 v62, v62, v62\n"
"v_cvt_pk_bf16_f32 v36, v36, v36\nv_cvt_pk_bf16_f32 v38, v38, v38\nv_cvt_pk_bf16_f32 v44, v44, v44\nv_cvt_pk_bf16_f32 v46, v46, v46\nv_and_b32 v48, v52, v112\nv_and_b32 v49, v54, v113\n"
"v_and_b32 v50, v52, v114\nv_and_b32 v51, v54, v115\nv_and_b32 v56, v60, v112\nv_and_b32 v57, v62, v113\nv_and_b32 v58, v60, v114\nv_and_b32 v59, v62, v115\n"
"v_mfma_f32_16x16x32_bf16 v[144:147], v[66:69], v[48:51], 0\nv_mfma_f32_16x16x32_bf16 v[148:151], v[72:75], v[48:51], 0\nv_and_b32 v32, v36, v112\nv_and_b32 v33, v38, v113\nv_and_b32 v34, v36, v114\nv_and_b32 v35, v38, v115\n"
"v_mfma_f32_16x16x32_bf16 v[152:155], v[78:81], v[56:59], 0\nv_mfma_f32_16x16x32_bf16 v[156:159], v[84:87], v[56:59], 0\nv_and_b32 v40, v44, v112\nv_and_b32 v41, v46, v113\nv_and_b32 v42, v44, v114\nv_and_b32 v43, v46, v115\n"
"v_mfma_f32_16x16x32_bf16 v[128:131], v[64:67], v[32:35], 0\nv_mfma_f32_16x16x32_bf16 v[132:135], v[70:73], v[32:35], 0\nv_mfma_f32_16x16x32_bf16 v[136:139], v[76:79], v[40:43], 0\nv_mfma_f32_16x16x32_bf16 v[140:143], v[82:85], v[40:43], 0\nv_cvt_pk_bf16_f32 v144, v144, v145\nv_cvt_pk_bf16_f32 v145, v146, v147\n"
"v_cvt_pk_bf16_f32 v146, v148, v149\nv_cvt_pk_bf16_f32 v147, v150, v151\nbuffer_store_dwordx4 v[144:147], v252, s[20:23], s85 offen offset:0 sc1\nv_cvt_pk_bf16_f32 v152, v152, v153\nv_cvt_pk_bf16_f32 v153, v154, v155\nv_cvt_pk_bf16_f32 v154, v156, v157\n"
"v_cvt_pk_bf16_f32 v155, v158, v159\nbuffer_store_dwordx4 v[152:155], v252, s[20:23], s85 offen offset:1024 sc1\nv_cvt_pk_bf16_f32 v128, v128, v129\nv_cvt_pk_bf16_f32 v129, v130, v131\nv_cvt_pk_bf16_f32 v130, v132, v133\nv_cvt_pk_bf16_f32 v131, v134, v135\n"
"buffer_store_dwordx4 v[128:131], v252, s[20:23], s84 offen offset:0 sc1\nv_cvt_pk_bf16_f32 v136, v136, v137\nv_cvt_pk_bf16_f32 v137, v138, v139\nv_cvt_pk_bf16_f32 v138, v140, v141\nv_cvt_pk_bf16_f32 v139, v142, v143\nbuffer_store_dwordx4 v[136:139], v252, s[20:23], s84 offen offset:1024 sc1\n"
"s_mov_b64 exec, 0\ns_cmp_eq_u32 s40, 1\ns_cbranch_scc1 LretO1\ns_cmp_eq_u32 s40, 2\ns_cbranch_scc1 LretO1\ns_cmp_eq_u32 s40, 3\n"
"s_cbranch_scc1 LretO1\ns_branch Lpg23\nLretO1:\ns_mov_b64 exec, -1\ns_add_u32 s61, s79, 88498176\ns_lshl_b32 s62, s42, 3\n"
"s_add_u32 s61, s61, s62\ns_waitcnt vmcnt(0)\ns_mov_b64 exec, 1\nbuffer_atomic_swap_x2 v[238:239], off, s[20:23], s61 sc1\ns_mov_b64 exec, -1\nv_bfe_u32 v250, v252, 7, 1\n"
"v_mul_u32_u24 v250, 2176, v250\nv_bfe_u32 v251, v252, 4, 3\nv_lshl_add_u32 v250, v251, 3, v250\ns_lshl_b32 s60, s90, 6\nv_add_u32 v250, s60, v250\ns_sleep 3\n"
"v_mov_b32 v248, 0\nv_mov_b32 v249, 0\ns_add_u32 s61, s80, 88498176\ns_mov_b64 exec, 0xffff\nbuffer_atomic_or_x2 v[248:249], v250, s[20:23], s61 offen sc0 sc1\ns_mov_b64 exec, -1\n"
"s_add_u32 m0, s50, 49152\ns_add_u32 s63, s53, 458752\nbuffer_load_dwordx4 v220, s[8:11], s63 offen lds\nbuffer_load_dwordx4 v221, s[8:11], s63 offen offset:1024 lds\ns_add_u32 m0, s50, 57344\ns_add_u32 s63, s53, 524288\n"
"buffer_load_dwordx4 v220, s[8:11], s63 offen lds\nbuffer_load_dwordx4 v221, s[8:11], s63 offen offset:1024 lds\ns_add_u32 m0, s50, 65536\ns_add_u32 s63, s53, 589824\nbuffer_load_dwordx4 v220, s[8:11], s63 offen lds\nbuffer_load_dwordx4 v221, s[8:11], s63 offen offset:1024 lds\n"
"s_add_u32 m0, s50, 73728\ns_add_u32 s63, s53, 655360\nbuffer_load_dwordx4 v220, s[8:11], s63 offen lds\nbuffer_load_dwordx4 v221, s[8:11], s63 offen offset:1024 lds\ns_add_u32 m0, s50, 81920\ns_add_u32 s63, s53, 720896\n"
"buffer_load_dwordx4 v220, s[8:11], s63 offen lds\nbuffer_load_dwordx4 v221, s[8:11], s63 offen offset:1024 lds\ns_waitcnt vmcnt(10)\nv_cmp_eq_u64 s[86:87], s[46:47], v[248:249]\ns_nop 1\ns_and_b32 s88, s86, 0xffff\n"
"s_cmp_eq_u32 s88, 0xffff\ns_cbranch_scc1 Lq706\nv_mov_b32 v246, 0\nv_mov_b32 v247, 0\ns_add_u32 s61, s80, 88498176\ns_mov_b64 exec, 0xffff\n"
"buffer_atomic_or_x2 v[246:247], v250, s[20:23], s61 offen sc0 sc1\ns_mov_b64 exec, -1\nLq706:\ns_add_u32 m0, s50, 90112\ns_add_u32 s63, s53, 786432\nbuffer_load_dwordx4 v220, s[8:11], s63 offen lds\n"
"buffer_load_dwordx4 v221, s[8:11], s63 offen offset:1024 lds\ns_add_u32 m0, s50, 98304\ns_add_u32 s63, s53, 851968\nbuffer_load_dwordx4 v220, s[8:11], s63 offen lds\nbuffer_load_dwordx4 v221, s[8:11], s63 offen offset:1024 lds\ns_add_u32 m0, s50, 106496\n"
"s_add_u32 s63, s53, 917504\nbuffer_load_dwordx4 v220, s[8:11], s63 offen lds\nbuffer_load_dwordx4 v221, s[8:11], s63 offen offset:1024 lds\nv_bfe_u32 v234, v252, 4, 4\nv_lshrrev_b32 v235, 8, v252\nv_lshlrev_b32 v235, 2, v235\n"
"v_sub_u32 v234, v234, v235\nv_lshlrev_b32 v235, 4, v234\ns_mov_b64 s[60:61], 0x3f80\nv_lshlrev_b64 v[208:209], v235, s[60:61]\nv_cmp_gt_u32 s[60:61], 4, v234\ns_nop 1\n"
"v_cndmask_b32 v212, 0, v208, s[60:61]\nv_cndmask_b32 v208, 0, v208, s[60:61]\nv_cndmask_b32 v213, 0, v209, s[60:61]\nv_cndmask_b32 v209, 0, v209, s[60:61]\ns_nop 1\nv_mfma_f32_16x16x32_bf16 v[0:3], v[208:211], v[192:195], 0\n"
"v_mfma_f32_16x16x32_bf16 v[4:7], v[210:213], v[192:195], 0\nv_mfma_f32_16x16x32_bf16 v[8:11], v[208:211], v[196:199], 0\nv_mfma_f32_16x16x32_bf16 v[12:15], v[210:213], v[196:199], 0\nv_mfma_f32_16x16x32_bf16 v[16:19], v[208:211], v[200:203], 0\nv_mfma_f32_16x16x32_bf16 v[20:23], v[210:213], v[200:203], 0\nv_mfma_f32_16x16x32_bf16 v[24:27], v[208:211], v[204:207], 0\n"
"v_mfma_f32_16x16x32_bf16 v[28:31], v[210:213], v[204:207], 0\ns_cmp_eq_u32 s88, 0xffff\ns_cbranch_scc1 Lq707\ns_waitcnt vmcnt(6)\nv_mov_b32 v248, v246\nv_mov_b32 v249, v247\n"
"v_cmp_eq_u64 s[86:87], s[46:47], v[248:249]\ns_nop 1\ns_and_b32 s62, s86, 0xffff\ns_cmp_eq_u32 s62, 0xffff\ns_cbranch_scc1 Lq698\nLq697:\n"
"v_mov_b32 v248, 0\nv_mov_b32 v249, 0\ns_add_u32 s61, s80, 88498176\ns_mov_b64 exec, 0xffff\nbuffer_atomic_or_x2 v[248:249], v250, s[20:23], s61 offen sc0 sc1\ns_mov_b64 exec, -1\n"
"s_waitcnt vmcnt(0)\nv_cmp_eq_u64 s[86:87], s[46:47], v[248:249]\ns_nop 1\ns_and_b32 s62, s86, 0xffff\ns_cmp_eq_u32 s62, 0xffff\ns_cbranch_scc1 Lq698\n"
"s_sub_u32 s48, s48, 1\ns_cmp_gt_i32 s48, 0\ns_cbranch_scc1 Lq697\nLq698:\nLq707:\ns_add_u32 s64, s70, 6291456\n"
"buffer_load_dwordx4 a[0:3], v252, s[20:23], s64 offen offset:0 sc1\ns_add_u32 s65, s70, 6324224\nbuffer_load_dwordx4 a[4:7], v252, s[20:23], s65 offen offset:0 sc1\ns_add_u32 s66, s71, 6291456\nbuffer_load_dwordx4 a[8:11], v252, s[20:23], s66 offen offset:0 sc1\ns_add_u32 s67, s71, 6324224\n"
"buffer_load_dwordx4 a[12:15], v252, s[20:23], s67 offen offset:0 sc1\nbuffer_load_dwordx4 a[16:19], v252, s[20:23], s64 offen offset:1024 sc1\nbuffer_load_dwordx4 a[20:23], v252, s[20:23], s65 offen offset:1024 sc1\nbuffer_load_dwordx4 a[24:27], v252, s[20:23], s66 offen offset:1024 sc1\nbuffer_load_dwordx4 a[28:31], v252, s[20:23], s67 offen offset:1024 sc1\ns_barrier\n"
"R 160,188,49152\nR 162,188,53248\nR 164,184,0\nR 166,184,4096\nR 168,189,49152\nR 170,189,53248\n"
"R 172,185,0\nR 174,185,4096\nR 176,190,49152\nR 178,190,53248\nR 180,186,0\nR 182,186,4096\n"
"buffer_load_dwordx4 a[32:35], v252, s[20:23], s64 offen offset:2048 sc1\nbuffer_load_dwordx4 a[36:39], v252, s[20:23], s65 offen offset:2048 sc1\nbuffer_load_dwordx4 a[40:43], v252, s[20:23], s66 offen offset:2048 sc1\nbuffer_load_dwordx4 a[44:47], v252, s[20:23], s67 offen offset:2048 sc1\nbuffer_load_dwordx4 a[48:51], v252, s[20:23], s64 offen offset:3072 sc1\nbuffer_load_dwordx4 a[52:55], v252, s[20:23], s65 offen offset:3072 sc1\n"
"buffer_load_dwordx4 a[56:59], v252, s[20:23], s66 offen offset:3072 sc1\nbuffer_load_dwordx4 a[60:63], v252, s[20:23], s67 offen offset:3072 sc1\ns_add_u32 s64, s70, 6295552\nbuffer_load_dwordx4 a[64:67], v252, s[20:23], s64 offen offset:0 sc1\ns_add_u32 s65, s70, 6328320\nbuffer_load_dwordx4 a[68:71], v252, s[20:23], s65 offen offset:0 sc1\n"
"s_add_u32 s66, s71, 6295552\nbuffer_load_dwordx4 a[72:75], v252, s[20:23], s66 offen offset:0 sc1\ns_add_u32 s67, s71, 6328320\nbuffer_load_dwordx4 a[76:79], v252, s[20:23], s67 offen offset:0 sc1\ns_waitcnt vmcnt(19) lgkmcnt(10)\nM 0,160,0\n"
"buffer_load_dwordx4 a[80:83], v252, s[20:23], s64 offen offset:1024 sc1\ns_waitcnt vmcnt(16) lgkmcnt(8)\nM 0,164,16\nM 16,160,4\ns_waitcnt vmcnt(15)\nM 16,164,20\n"
"v_mfma_f32_16x16x32_bf16 v[32:35], v[160:163], a[8:11], 0\nbuffer_load_dwordx4 a[84:87], v252, s[20:23], s65 offen offset:1024 sc1\ns_waitcnt vmcnt(15)\nM 32,164,24\nv_mfma_f32_16x16x32_bf16 v[48:51], v[160:163], a[12:15], 0\ns_waitcnt vmcnt(14)\n"
"M 48,164,28\nG 4\nv_mfma_f32_16x16x32_bf16 v[52:55], v[168:171], a[12:15], 0\nR 160,191,49152\nM 52,172,28\nbuffer_load_dwordx4 a[88:91], v252, s[20:23], s66 offen offset:1024 sc1\n"
"v_mfma_f32_16x16x32_bf16 v[36:39], v[168:171], a[8:11], 0\nR 162,191,53248\nM 36,172,24\nM 20,168,4\nR 164,187,0\nM 20,172,20\n"
"buffer_load_dwordx4 a[92:95], v252, s[20:23], s67 offen offset:1024 sc1\nM 4,168,0\nR 166,187,4096\nM 4,172,16\nG 4\nM 8,176,0\n"
"R 168,184,8192\nM 8,180,16\nbuffer_load_dwordx4 a[96:99], v252, s[20:23], s64 offen offset:2048 sc1\nM 24,176,4\nR 170,184,12288\nM 24,180,20\n"
"v_mfma_f32_16x16x32_bf16 v[40:43], v[176:179], a[8:11], 0\nR 172,184,16384\nM 40,180,24\nbuffer_load_dwordx4 a[100:103], v252, s[20:23], s65 offen offset:2048 sc1\nv_mfma_f32_16x16x32_bf16 v[56:59], v[176:179], a[12:15], 0\nR 174,184,20480\n"
"M 56,180,28\nG 4\nv_mfma_f32_16x16x32_bf16 v[60:63], v[160:163], a[12:15], 0\nR 176,185,8192\nM 60,164,28\nbuffer_load_dwordx4 a[104:107], v252, s[20:23], s66 offen offset:2048 sc1\n"
"v_mfma_f32_16x16x32_bf16 v[44:47], v[160:163], a[8:11], 0\nR 178,185,12288\nM 44,164,24\nM 28,160,4\nR 180,185,16384\nM 28,164,20\n"
"buffer_load_dwordx4 a[108:111], v252, s[20:23], s67 offen offset:2048 sc1\nM 12,160,0\nR 182,185,20480\nM 12,164,16\nG 4\ns_waitcnt vmcnt(19)\n"
"M 0,168,32\nR 160,186,8192\ns_waitcnt vmcnt(15)\nM 0,172,48\nbuffer_load_dwordx4 a[112:115], v252, s[20:23], s64 offen offset:3072 sc1\nM 16,168,36\n"
"R 162,186,12288\ns_waitcnt vmcnt(15)\nM 16,172,52\nM 32,168,40\nR 164,186,16384\ns_waitcnt vmcnt(14)\n"
"M 32,172,56\nbuffer_load_dwordx4 a[116:119], v252, s[20:23], s65 offen offset:3072 sc1\nM 48,168,44\nR 166,186,20480\ns_waitcnt vmcnt(14)\nM 48,172,60\n"
"G 4\nM 52,176,44\nR 168,187,8192\nM 52,180,60\nbuffer_load_dwordx4 a[120:123], v252, s[20:23], s66 offen offset:3072 sc1\nM 36,176,40\n"
"R 170,187,12288\nM 36,180,56\nM 20,176,36\nR 172,187,16384\nM 20,180,52\nbuffer_load_dwordx4 a[124:127], v252, s[20:23], s67 offen offset:3072 sc1\n"
"M 4,176,32\nR 174,187,20480\nM 4,180,48\nG 4\nM 8,160,32\nR 176,184,24576\n"
"M 8,164,48\ns_add_u32 s64, s70, 6299648\nbuffer_load_dwordx4 a[128:131], v252, s[20:23], s64 offen offset:0 sc1\nM 24,160,36\nR 178,184,28672\nM 24,164,52\n"
"M 40,160,40\nR 180,184,32768\nM 40,164,56\ns_add_u32 s65, s70, 6332416\nbuffer_load_dwordx4 a[132:135], v252, s[20:23], s65 offen offset:0 sc1\nM 56,160,44\n"
"R 182,184,36864\nM 56,164,60\nG 4\nM 60,168,44\nR 160,185,24576\nM 60,172,60\n"
"s_add_u32 s66, s71, 6299648\nbuffer_load_dwordx4 a[136:139], v252, s[20:23], s66 offen offset:0 sc1\nM 44,168,40\nR 162,185,28672\nM 44,172,56\nM 28,168,36\n"
"R 164,185,32768\nM 28,172,52\ns_add_u32 s67, s71, 6332416\nbuffer_load_dwordx4 a[140:143], v252, s[20:23], s67 offen offset:0 sc1\nM 12,168,32\nR 166,185,36864\n"
"M 12,172,48\ns_barrier\nG 4\ns_waitcnt vmcnt(19)\nM 0,176,64\nR 168,186,24576\n"
"s_waitcnt vmcnt(15)\nM 0,180,80\nbuffer_load_dwordx4 a[144:147], v252, s[20:23], s64 offen offset:1024 sc1\nM 16,176,68\nR 170,186,28672\ns_waitcnt vmcnt(15)\n"
"M 16,180,84\nM 32,176,72\nR 172,186,32768\ns_waitcnt vmcnt(14)\nM 32,180,88\nbuffer_load_dwordx4 a[148:151], v252, s[20:23], s65 offen offset:1024 sc1\n"
"M 48,176,76\nR 174,186,36864\ns_waitcnt vmcnt(14)\nM 48,180,92\nG 4\nM 52,160,76\n"
"R 176,187,24576\nM 52,164,92\nbuffer_load_dwordx4 a[152:155], v252, s[20:23], s66 offen offset:1024 sc1\nM 36,160,72\nR 178,187,28672\nM 36,164,88\n"
"M 20,160,68\nR 180,187,32768\nM 20,164,84\nbuffer_load_dwordx4 a[156:159], v252, s[20:23], s67 offen offset:1024 sc1\nM 4,160,64\nR 182,187,36864\n"
"M 4,164,80\nG 4\nM 8,168,64\nR 160,184,40960\nM 8,172,80\nbuffer_load_dwordx4 a[160:163], v252, s[20:23], s64 offen offset:2048 sc1\n"
"M 24,168,68\nR 162,184,45056\nM 24,172,84\nM 40,168,72\nR 164,184,49152\nM 40,172,88\n"
"buffer_load_dwordx4 a[164:167], v252, s[20:23], s65 offen offset:2048 sc1\nM 56,168,76\nR 166,184,53248\nM 56,172,92\nG 4\nM 60,176,76\n"
"R 168,185,40960\nM 60,180,92\nbuffer_load_dwordx4 a[168:171], v252, s[20:23], s66 offen offset:2048 sc1\nM 44,176,72\nR 170,185,45056\nM 44,180,88\n"
"M 28,176,68\nR 172,185,49152\nM 28,180,84\nbuffer_load_dwordx4 a[172:175], v252, s[20:23], s67 offen offset:2048 sc1\nM 12,176,64\nR 174,185,53248\n"
"M 12,180,80\nG 4\ns_waitcnt vmcnt(19)\nM 0,160,96\nR 176,186,40960\ns_waitcnt vmcnt(15)\n"
"M 0,164,112\nbuffer_load_dwordx4 a[176:179], v252, s[20:23], s64 offen offset:3072 sc1\nM 16,160,100\nR 178,186,45056\ns_waitcnt vmcnt(15)\nM 16,164,116\n"
"M 32,160,104\nR 180,186,49152\ns_lshr_b32 s60, s45, 3\ns_lshl2_add_u32 s60, s60, s40\ns_lshl_b32 s60, s60, 8\nv_lshl_add_u32 v224, v252, 3, s60\n"
"v_add_u32 v224, Lqw1+24576-LqPC, v224\nv_min_u32 v224, LqE-LqPC-4, v224\ns_mov_b64 exec, 3\nglobal_load_dword v224, v224, s[98:99]\ns_mov_b64 exec, -1\ns_waitcnt vmcnt(15)\n"
"M 32,164,120\nbuffer_load_dwordx4 a[180:183], v252, s[20:23], s65 offen offset:3072 sc1\nM 48,160,108\nR 182,186,53248\ns_waitcnt vmcnt(15)\nM 48,164,124\n"
"G 4\nM 52,168,108\nR 160,187,40960\nM 52,172,124\nbuffer_load_dwordx4 a[184:187], v252, s[20:23], s66 offen offset:3072 sc1\nM 36,168,104\n"
"R 162,187,45056\nM 36,172,120\nM 20,168,100\nR 164,187,49152\nM 20,172,116\nbuffer_load_dwordx4 a[188:191], v252, s[20:23], s67 offen offset:3072 sc1\n"
"M 4,168,96\nR 166,187,53248\nM 4,172,112\nG 4\nM 8,176,96\nR 168,184,57344\n"
"M 8,180,112\ns_add_u32 s64, s70, 6303744\nbuffer_load_dwordx4 a[192:195], v252, s[20:23], s64 offen offset:0 sc1\nM 24,176,100\nR 170,184,61440\ns_add_u32 s63, s53, 983040\n"
"M 24,180,116\ns_add_u32 m0, s50, 114688\nM 40,176,104\nR 172,188,0\nM 40,180,120\ns_add_u32 s65, s70, 6336512\n"
"buffer_load_dwordx4 a[196:199], v252, s[20:23], s65 offen offset:0 sc1\nM 56,176,108\nR 174,188,4096\nM 56,180,124\nbuffer_load_dwordx4 v220, s[8:11], s63 offen lds\nG 4\n"
"M 60,160,108\nR 176,185,57344\nM 60,164,124\ns_add_u32 s66, s71, 6303744\nbuffer_load_dwordx4 a[200:203], v252, s[20:23], s66 offen offset:0 sc1\nM 44,160,104\n"
"R 178,185,61440\nM 44,164,120\nbuffer_load_dwordx4 v221, s[8:11], s63 offen offset:1024 lds\nM 28,160,100\nR 180,189,0\nM 28,164,116\n"
"s_add_u32 s67, s71, 6336512\nbuffer_load_dwordx4 a[204:207], v252, s[20:23], s67 offen offset:0 sc1\nM 12,160,96\nR 182,189,4096\nM 12,164,112\ns_barrier\n"
"G 4\ns_waitcnt vmcnt(22)\nM 0,168,128\nR 160,186,57344\ns_waitcnt vmcnt(18)\nM 0,172,144\n"
"buffer_load_dwordx4 a[208:211], v252, s[20:23], s64 offen offset:1024 sc1\nM 16,168,132\nR 162,186,61440\ns_waitcnt vmcnt(18)\nM 16,172,148\nM 32,168,136\n"
"R 164,190,0\nLpg23:\ns_cbranch_execz LretO1\ns_waitcnt vmcnt(17)\nM 32,172,152\nbuffer_load_dwordx4 a[212:215], v252, s[20:23], s65 offen offset:1024 sc1\n"
"M 48,168,140\nR 166,190,4096\ns_waitcnt vmcnt(17)\nM 48,172,156\nG 4\nM 52,176,140\n"
"R 168,187,57344\nM 52,180,156\nbuffer_load_dwordx4 a[216:219], v252, s[20:23], s66 offen offset:1024 sc1\nM 36,176,136\nR 170,187,61440\nM 36,180,152\n"
"M 20,176,132\nR 172,191,0\nM 20,180,148\nbuffer_load_dwordx4 a[220:223], v252, s[20:23], s67 offen offset:1024 sc1\nM 4,176,128\nR 174,191,4096\n"
"M 4,180,144\nG 4\nM 8,160,128\nR 176,188,8192\nM 8,164,144\nbuffer_load_dwordx4 a[224:227], v252, s[20:23], s64 offen offset:2048 sc1\n"
"M 24,160,132\nR 178,188,12288\nM 24,164,148\nM 40,160,136\nR 180,188,16384\nM 40,164,152\n"
"buffer_load_dwordx4 a[228:231], v252, s[20:23], s65 offen offset:2048 sc1\nM 56,160,140\nR 182,188,20480\nM 56,164,156\nG 4\nM 60,168,140\n"
"R 160,189,8192\nM 60,172,156\nbuffer_load_dwordx4 a[232:235], v252, s[20:23], s66 offen offset:2048 sc1\nM 44,168,136\nR 162,189,12288\nM 44,172,152\n"
"M 28,168,132\nR 164,189,16384\nM 28,172,148\nbuffer_load_dwordx4 a[236:239], v252, s[20:23], s67 offen offset:2048 sc1\nM 12,168,128\nR 166,189,20480\n"
"M 12,172,144\nG 4\ns_waitcnt vmcnt(22)\nM 0,176,160\nR 168,190,8192\ns_waitcnt vmcnt(18)\n"
"M 0,180,176\nbuffer_load_dwordx4 a[240:243], v252, s[20:23], s64 offen offset:3072 sc1\nM 16,176,164\nR 170,190,12288\ns_waitcnt vmcnt(17)\nM 16,180,180\n"
"M 32,176,168\nR 172,190,16384\ns_waitcnt vmcnt(16)\nM 32,180,184\nbuffer_load_dwordx4 a[244:247], v252, s[20:23], s65 offen offset:3072 sc1\nM 48,176,172\n"
"R 174,190,20480\ns_waitcnt vmcnt(16)\nM 48,180,188\nG 4\nM 52,160,172\nR 176,191,8192\n"
"M 52,164,188\nbuffer_load_dwordx4 a[248:251], v252, s[20:23], s66 offen offset:3072 sc1\nM 36,160,168\nR 178,191,12288\ns_waitcnt vmcnt(12)\ns_barrier\n"
"M 36,164,184\nM 20,160,164\nR 180,191,16384\nM 20,164,180\nbuffer_load_dwordx4 a[252:255], v252, s[20:23], s67 offen offset:3072 sc1\nM 4,160,160\n"
"R 182,191,20480\nM 4,164,176\nG 4\nM 8,168,160\nR 160,188,24576\nM 8,172,176\n"
"M 24,168,164\nR 162,188,28672\nM 24,172,180\nM 40,168,168\nR 164,188,32768\nM 40,172,184\n"
"M 56,168,172\nR 166,188,36864\nM 56,172,188\nG 4\nM 60,176,172\nR 168,189,24576\n"
"M 60,180,188\ns_movk_i32 s63, 7168\nv_mad_u32_u24 v220, v228, s63, v229\nv_add_u32 v221, 3668992, v220\ns_add_u32 m0, s50, 0\ns_add_u32 s63, s54, 0\n"
"buffer_load_dwordx4 v220, s[12:15], s63 offen lds\nM 44,176,168\nR 170,189,28672\nM 44,180,184\nM 28,176,164\nR 172,189,32768\n"
"M 28,180,180\nbuffer_load_dwordx4 v221, s[12:15], s63 offen offset:1024 lds\nM 12,176,160\nR 174,189,36864\ns_add_u32 m0, s50, 8192\nM 12,180,176\n"
"G 4\nM 32,160,200\nR 176,190,24576\ns_add_u32 s63, s54, 229376\ns_waitcnt vmcnt(11)\nM 32,164,216\n"
"R 178,190,28672\nM 48,160,204\nR 180,190,32768\ns_waitcnt vmcnt(10)\nM 48,164,220\nR 182,190,36864\n"
"G 4\nM 52,168,204\nR 160,191,24576\nbuffer_load_dwordx4 v220, s[12:15], s63 offen lds\nM 52,172,220\nR 162,191,28672\n"
"M 36,168,200\nR 164,191,32768\nM 36,172,216\nR 166,191,36864\nG 4\nM 40,176,200\n"
"R 168,188,40960\nbuffer_load_dwordx4 v221, s[12:15], s63 offen offset:1024 lds\nM 40,180,216\nR 170,188,45056\ns_add_u32 m0, s50, 16384\nM 56,176,204\n"
"R 172,188,49152\ns_add_u32 s63, s54, 128\nM 56,180,220\nR 174,188,53248\nG 4\nM 60,160,204\n"
"R 176,189,40960\nbuffer_load_dwordx4 v220, s[12:15], s63 offen lds\nM 60,164,220\nR 178,189,45056\nM 44,160,200\nR 180,189,49152\n"
"M 44,164,216\nR 182,189,53248\nG 4\ns_waitcnt vmcnt(10)\nM 32,168,232\nR 160,190,40960\n"
"buffer_load_dwordx4 v221, s[12:15], s63 offen offset:1024 lds\ns_waitcnt vmcnt(7)\nM 32,172,248\nR 162,190,45056\ns_add_u32 m0, s50, 24576\nM 48,168,236\n"
"R 164,190,49152\ns_add_u32 s63, s54, 229504\ns_waitcnt vmcnt(6)\nM 48,172,252\nR 166,190,53248\nG 4\n"
"M 52,176,236\nR 168,191,40960\nbuffer_load_dwordx4 v220, s[12:15], s63 offen lds\nM 52,180,252\nR 170,191,45056\nM 36,176,232\n"
"R 172,191,49152\nM 36,180,248\nR 174,191,53248\nG 4\nM 40,160,232\nR 176,188,24576\n"
"buffer_load_dwordx4 v221, s[12:15], s63 offen offset:1024 lds\nM 40,164,248\nR 178,188,28672\nM 56,160,236\nR 180,188,32768\nM 56,164,252\n"
"R 182,188,36864\nG 4\nM 60,168,236\nR 160,189,24576\nM 60,172,252\nR 162,189,28672\n"
"M 44,168,232\nR 164,189,32768\nM 44,172,248\nR 166,189,36864\nG 4\nM 0,176,192\n"
"R 168,190,24576\nM 0,180,208\nR 170,190,28672\ns_mov_b32 m0, s51\nM 16,176,196\nR 172,190,32768\n"
"v_cvt_pk_bf16_f32 v32, v32, v33\nv_cvt_pk_bf16_f32 v33, v34, v35\nM 16,180,212\nR 174,190,36864\nG 4\nv_cvt_pk_bf16_f32 v34, v36, v37\n"
"M 20,160,196\nR 176,191,24576\nv_cvt_pk_bf16_f32 v35, v38, v39\nv_cvt_pk_bf16_f32 v36, v40, v41\nM 20,164,212\nR 178,191,28672\n"
"v_cvt_pk_bf16_f32 v37, v42, v43\nv_cvt_pk_bf16_f32 v38, v44, v45\nM 4,160,192\nR 180,191,32768\nv_cvt_pk_bf16_f32 v39, v46, v47\nv_cvt_pk_bf16_f32 v40, v48, v49\n"
"M 4,164,208\nR 182,191,36864\nG 4\nv_cvt_pk_bf16_f32 v41, v50, v51\nM 8,168,192\nR 160,188,40960\n"
"v_cvt_pk_bf16_f32 v42, v52, v53\nv_cvt_pk_bf16_f32 v43, v54, v55\nM 8,172,208\nR 162,188,45056\nv_cvt_pk_bf16_f32 v44, v56, v57\nv_cvt_pk_bf16_f32 v45, v58, v59\n"
"M 24,168,196\nR 164,188,49152\nv_cvt_pk_bf16_f32 v46, v60, v61\nv_cvt_pk_bf16_f32 v47, v62, v63\nM 24,172,212\nR 166,188,53248\n"
"ds_write_addtid_b32 v32 offset:0\nG 5\nM 28,176,196\nR 168,189,40960\nds_write_addtid_b32 v33 offset:256\nM 28,180,212\n"
"R 170,189,45056\nds_write_addtid_b32 v34 offset:512\nM 12,176,192\nR 172,189,49152\nds_write_addtid_b32 v35 offset:768\nds_write_addtid_b32 v36 offset:1024\n"
"ds_write_addtid_b32 v37 offset:1280\nds_write_addtid_b32 v38 offset:1536\nds_write_addtid_b32 v39 offset:1792\nds_write_addtid_b32 v40 offset:2048\nds_write_addtid_b32 v41 offset:2304\nds_write_addtid_b32 v42 offset:2560\n"
"ds_write_addtid_b32 v43 offset:2816\nds_write_addtid_b32 v44 offset:3072\nds_write_addtid_b32 v45 offset:3328\nds_write_addtid_b32 v46 offset:3584\nds_write_addtid_b32 v47 offset:3840\nM 12,180,208\n"
"R 174,189,53248\nG 15\nM 0,160,224\nR 176,190,40960\ns_add_u32 m0, s50, 32768\nM 0,164,240\n"
"R 178,190,45056\nG 3\ns_barrier\ns_add_u32 s63, s54, 458752\nM 16,160,228\nR 180,190,49152\n"
"buffer_load_dwordx4 v220, s[12:15], s63 offen lds\nds_read2st64_b32 v[32:33], v225 offset0:0 offset1:1\nM 16,164,244\nR 182,190,53248\nds_read2st64_b32 v[34:35], v225 offset0:2 offset1:3\nG 6\n"
"M 20,168,228\nR 160,191,40960\nds_read2st64_b32 v[36:37], v225 offset0:4 offset1:5\nM 20,172,244\nR 162,191,45056\nds_read2st64_b32 v[38:39], v225 offset0:6 offset1:7\n"
"M 4,168,224\nR 164,191,49152\nbuffer_load_dwordx4 v221, s[12:15], s63 offen offset:1024 lds\nds_read2st64_b32 v[40:41], v225 offset0:8 offset1:9\nM 4,172,240\nR 166,191,53248\n"
"s_add_u32 m0, s50, 40960\ns_add_u32 s63, s54, 688128\nds_read2st64_b32 v[42:43], v225 offset0:10 offset1:11\nG 9\nM 8,176,224\nbuffer_load_dwordx4 v220, s[12:15], s63 offen lds\n"
"ds_read2st64_b32 v[44:45], v225 offset0:12 offset1:13\nM 8,180,240\nds_read2st64_b32 v[46:47], v225 offset0:14 offset1:15\nM 24,176,228\nM 24,180,244\nG 3\n"
"M 28,160,228\nbuffer_load_dwordx4 v221, s[12:15], s63 offen offset:1024 lds\nM 28,164,244\nM 12,160,224\nM 12,164,240\nG 0\n"
"s_barrier\nv_mfma_f32_16x16x32_bf16 v[0:3], v[208:211], v[32:35], v[0:3]\nv_mfma_f32_16x16x32_bf16 v[4:7], v[210:213], v[32:35], v[4:7]\nv_mfma_f32_16x16x32_bf16 v[8:11], v[208:211], v[36:39], v[8:11]\nv_mfma_f32_16x16x32_bf16 v[12:15], v[210:213], v[36:39], v[12:15]\nv_mfma_f32_16x16x32_bf16 v[16:19], v[208:211], v[40:43], v[16:19]\n"
"v_mfma_f32_16x16x32_bf16 v[20:23], v[210:213], v[40:43], v[20:23]\nv_mfma_f32_16x16x32_bf16 v[24:27], v[208:211], v[44:47], v[24:27]\nv_mfma_f32_16x16x32_bf16 v[28:31], v[210:213], v[44:47], v[28:31]\ns_lshl_b32 s84, s75, 11\ns_add_u32 s84, s84, s49\ns_add_u32 s84, s84, 14680064\n"
"s_add_u32 s85, s84, 32768\nv_mov_b32 v238, s46\nv_mov_b32 v239, s47\nv_cvt_pk_bf16_f32 v192, v0, v1\nv_cvt_pk_bf16_f32 v193, v2, v3\nv_cvt_pk_bf16_f32 v194, v4, v5\n"
"v_cvt_pk_bf16_f32 v195, v6, v7\nbuffer_store_dwordx4 v[192:195], v252, s[20:23], s84 offen offset:0 sc1\nv_cvt_pk_bf16_f32 v196, v8, v9\nv_cvt_pk_bf16_f32 v197, v10, v11\nv_cvt_pk_bf16_f32 v198, v12, v13\nv_cvt_pk_bf16_f32 v199, v14, v15\n"
"buffer_store_dwordx4 v[196:199], v252, s[20:23], s84 offen offset:1024 sc1\nv_cvt_pk_bf16_f32 v200, v16, v17\nv_cvt_pk_bf16_f32 v201, v18, v19\nv_cvt_pk_bf16_f32 v202, v20, v21\nv_cvt_pk_bf16_f32 v203, v22, v23\nbuffer_store_dwordx4 v[200:203], v252, s[20:23], s85 offen offset:0 sc1\n"
"v_cvt_pk_bf16_f32 v204, v24, v25\nv_cvt_pk_bf16_f32 v205, v26, v27\nv_cvt_pk_bf16_f32 v206, v28, v29\nv_cvt_pk_bf16_f32 v207, v30, v31\nbuffer_store_dwordx4 v[204:207], v252, s[20:23], s85 offen offset:1024 sc1\ns_mov_b64 exec, 0\n"
"s_cmp_eq_u32 s40, 1\ns_cbranch_scc1 Lpg25\ns_cmp_eq_u32 s40, 2\ns_cbranch_scc1 Lpg26\ns_cmp_eq_u32 s40, 3\ns_cbranch_scc1 Lpg27\n"
"s_branch Lpg24\nLretO1n:\ns_mov_b64 exec, -1\ns_add_u32 s61, s79, 88637440\ns_lshl_b32 s62, s75, 3\ns_add_u32 s61, s61, s62\n"
"s_waitcnt vmcnt(0)\ns_mov_b64 exec, 1\nbuffer_atomic_swap_x2 v[238:239], off, s[20:23], s61 sc1\ns_mov_b64 exec, -1\nLqw1:\nv_bfe_u32 v250, v252, 7, 1\n"
"v_mul_u32_u24 v250, 2176, v250\nv_bfe_u32 v251, v252, 4, 3\nv_lshl_add_u32 v250, v251, 3, v250\ns_lshl_b32 s60, s90, 6\nv_add_u32 v250, s60, v250\ns_sleep 3\n"
"v_mov_b32 v248, 0\nv_mov_b32 v249, 0\ns_add_u32 s61, s80, 88637440\ns_mov_b64 exec, 0xffff\nbuffer_atomic_or_x2 v[248:249], v250, s[20:23], s61 offen sc0 sc1\ns_mov_b64 exec, -1\n"
"s_add_u32 m0, s50, 49152\ns_add_u32 s63, s54, 458880\nbuffer_load_dwordx4 v220, s[12:15], s63 offen lds\nbuffer_load_dwordx4 v221, s[12:15], s63 offen offset:1024 lds\ns_add_u32 m0, s50, 57344\ns_add_u32 s63, s54, 688256\n"
"buffer_load_dwordx4 v220, s[12:15], s63 offen lds\nbuffer_load_dwordx4 v221, s[12:15], s63 offen offset:1024 lds\ns_add_u32 m0, s50, 65536\ns_add_u32 s63, s54, 917504\nbuffer_load_dwordx4 v220, s[12:15], s63 offen lds\nbuffer_load_dwordx4 v221, s[12:15], s63 offen offset:1024 lds\n"
"s_add_u32 m0, s50, 73728\ns_add_u32 s63, s54, 1146880\nbuffer_load_dwordx4 v220, s[12:15], s63 offen lds\nbuffer_load_dwordx4 v221, s[12:15], s63 offen offset:1024 lds\ns_add_u32 m0, s50, 81920\ns_add_u32 s63, s54, 917632\n"
"buffer_load_dwordx4 v220, s[12:15], s63 offen lds\nbuffer_load_dwordx4 v221, s[12:15], s63 offen offset:1024 lds\ns_waitcnt vmcnt(10)\nv_cmp_eq_u64 s[86:87], s[46:47], v[248:249]\ns_nop 1\ns_and_b32 s88, s86, 0xffff\n"
"s_cmp_eq_u32 s88, 0xffff\ns_cbranch_scc1 Lq768\nv_mov_b32 v246, 0\nv_mov_b32 v247, 0\ns_add_u32 s61, s80, 88637440\ns_mov_b64 exec, 0xffff\n"
"buffer_atomic_or_x2 v[246:247], v250, s[20:23], s61 offen sc0 sc1\ns_mov_b64 exec, -1\nLq768:\ns_add_u32 m0, s50, 90112\ns_add_u32 s63, s54, 1147008\nbuffer_load_dwordx4 v220, s[12:15], s63 offen lds\n"
"buffer_load_dwordx4 v221, s[12:15], s63 offen offset:1024 lds\ns_mov_b64 exec, 0\ns_cmp_eq_u32 s40, 1\ns_cbranch_scc1 Lpg33\ns_cmp_eq_u32 s40, 2\ns_cbranch_scc1 LretW1z\n"
"s_cmp_eq_u32 s40, 3\ns_cbranch_scc1 LretW1z\ns_branch Lpg32\nLretW1z:\ns_mov_b64 exec, -1\nv_mov_b32 v230, 0\n"
"v_mov_b32 v231, 0\nv_mov_b32 v232, 0\nv_mov_b32 v233, 0\nv_dot2c_f32_bf16 v230, s76, v192\nv_dot2c_f32_bf16 v231, v192, v192\nv_dot2c_f32_bf16 v232, s76, v200\n"
"v_dot2c_f32_bf16 v233, v200, v200\nv_dot2c_f32_bf16 v230, s76, v193\nv_dot2c_f32_bf16 v231, v193, v193\nv_dot2c_f32_bf16 v232, s76, v201\nv_dot2c_f32_bf16 v233, v201, v201\nv_dot2c_f32_bf16 v230, s76, v194\n"
"v_dot2c_f32_bf16 v231, v194, v194\nv_dot2c_f32_bf16 v232, s76, v202\nv_dot2c_f32_bf16 v233, v202, v202\nv_dot2c_f32_bf16 v230, s76, v195\nv_dot2c_f32_bf16 v231, v195, v195\nv_dot2c_f32_bf16 v232, s76, v203\n"
"v_dot2c_f32_bf16 v233, v203, v203\nv_dot2c_f32_bf16 v230, s76, v196\nv_dot2c_f32_bf16 v231, v196, v196\nv_dot2c_f32_bf16 v232, s76, v204\nv_dot2c_f32_bf16 v233, v204, v204\nv_dot2c_f32_bf16 v230, s76, v197\n"
"v_dot2c_f32_bf16 v231, v197, v197\nv_dot2c_f32_bf16 v232, s76, v205\nv_dot2c_f32_bf16 v233, v205, v205\nv_dot2c_f32_bf16 v230, s76, v198\nv_dot2c_f32_bf16 v231, v198, v198\nv_dot2c_f32_bf16 v232, s76, v206\n"
"v_dot2c_f32_bf16 v233, v206, v206\nv_dot2c_f32_bf16 v230, s76, v199\nv_dot2c_f32_bf16 v231, v199, v199\nv_dot2c_f32_bf16 v232, s76, v207\nv_dot2c_f32_bf16 v233, v207, v207\nv_lshrrev_b32 v234, 5, v252\n"
"v_and_b32 v234, 24, v234\nv_bfe_u32 v235, s46, v234, 8\nv_lshrrev_b32 v234, 1, v234\nv_and_b32 v236, 0xf0, v252\nv_add_u32 v234, v234, v236\nv_permlane32_swap_b32 v230, v231\n"
"v_permlane32_swap_b32 v232, v233\nv_add_f32 v230, v230, v231\nv_add_f32 v232, v232, v233\ns_nop 1\nv_permlane16_swap_b32 v230, v232\nv_add_f32 v230, v230, v232\n"
"s_movk_i32 s62, 0xff\nv_bfi_b32 v230, s62, v235, v230\ns_lshr_b32 s61, s49, 4\ns_lshl1_add_u32 s61, s53, s61\ns_add_u32 s61, s61, 83361792\nbuffer_store_dword v230, v234, s[20:23], s61 offen sc1\n"
"s_cmp_eq_u32 s88, 0xffff\ns_cbranch_scc1 Lq769\ns_waitcnt vmcnt(3)\nv_mov_b32 v248, v246\nv_mov_b32 v249, v247\nv_cmp_eq_u64 s[86:87], s[46:47], v[248:249]\n"
"s_nop 1\ns_and_b32 s62, s86, 0xffff\ns_cmp_eq_u32 s62, 0xffff\ns_cbranch_scc1 Lq760\nLq759:\nv_mov_b32 v248, 0\n"
"v_mov_b32 v249, 0\ns_add_u32 s61, s80, 88637440\ns_mov_b64 exec, 0xffff\nbuffer_atomic_or_x2 v[248:249], v250, s[20:23], s61 offen sc0 sc1\ns_mov_b64 exec, -1\ns_waitcnt vmcnt(0)\n"
"v_cmp_eq_u64 s[86:87], s[46:47], v[248:249]\ns_nop 1\ns_and_b32 s62, s86, 0xffff\ns_cmp_eq_u32 s62, 0xffff\ns_cbranch_scc1 Lq760\ns_sub_u32 s48, s48, 1\n"
"s_cmp_gt_i32 s48, 0\ns_cbranch_scc1 Lq759\nLq760:\nLq769:\ns_add_u32 s64, s70, 14680064\nbuffer_load_dwordx4 a[0:3], v252, s[20:23], s64 offen offset:0 sc1\n"
"s_add_u32 s65, s70, 14712832\nbuffer_load_dwordx4 a[4:7], v252, s[20:23], s65 offen offset:0 sc1\ns_add_u32 s66, s71, 14680064\nbuffer_load_dwordx4 a[8:11], v252, s[20:23], s66 offen offset:0 sc1\ns_add_u32 s67, s71, 14712832\nbuffer_load_dwordx4 a[12:15], v252, s[20:23], s67 offen offset:0 sc1\n"
"buffer_load_dwordx4 a[16:19], v252, s[20:23], s64 offen offset:1024 sc1\nbuffer_load_dwordx4 a[20:23], v252, s[20:23], s65 offen offset:1024 sc1\nbuffer_load_dwordx4 a[24:27], v252, s[20:23], s66 offen offset:1024 sc1\nbuffer_load_dwordx4 a[28:31], v252, s[20:23], s67 offen offset:1024 sc1\ns_barrier\nR 160,184,0\n"
"R 162,184,4096\nR 164,184,8192\nR 166,184,12288\nR 168,185,0\nR 170,185,4096\nR 172,185,8192\n"
"R 174,185,12288\nR 176,186,0\nR 178,186,4096\nR 180,186,8192\nR 182,186,12288\nbuffer_load_dwordx4 a[32:35], v252, s[20:23], s64 offen offset:2048 sc1\n"
"buffer_load_dwordx4 a[36:39], v252, s[20:23], s65 offen offset:2048 sc1\nbuffer_load_dwordx4 a[40:43], v252, s[20:23], s66 offen offset:2048 sc1\nbuffer_load_dwordx4 a[44:47], v252, s[20:23], s67 offen offset:2048 sc1\nbuffer_load_dwordx4 a[48:51], v252, s[20:23], s64 offen offset:3072 sc1\nbuffer_load_dwordx4 a[52:55], v252, s[20:23], s65 offen offset:3072 sc1\nbuffer_load_dwordx4 a[56:59], v252, s[20:23], s66 offen offset:3072 sc1\n"
"buffer_load_dwordx4 a[60:63], v252, s[20:23], s67 offen offset:3072 sc1\ns_waitcnt vmcnt(15) lgkmcnt(10)\nv_mfma_f32_16x16x32_bf16 v[32:35], v[160:163], a[0:3], 0\ns_add_u32 s64, s70, 14684160\nbuffer_load_dwordx4 a[64:67], v252, s[20:23], s64 offen offset:0 sc1\ns_waitcnt vmcnt(12) lgkmcnt(8)\n"
"M 32,164,16\nv_mfma_f32_16x16x32_bf16 v[48:51], v[160:163], a[4:7], 0\ns_waitcnt vmcnt(11)\nM 48,164,20\nv_mfma_f32_16x16x32_bf16 v[64:67], v[160:163], a[8:11], 0\ns_waitcnt vmcnt(10)\n"
"M 64,164,24\nv_mfma_f32_16x16x32_bf16 v[80:83], v[160:163], a[12:15], 0\ns_add_u32 s65, s70, 14716928\nbuffer_load_dwordx4 a[68:71], v252, s[20:23], s65 offen offset:0 sc1\ns_waitcnt vmcnt(10)\nM 80,164,28\n"
"G 4\nv_mfma_f32_16x16x32_bf16 v[84:87], v[168:171], a[12:15], 0\nR 160,187,0\nM 84,172,28\nv_mfma_f32_16x16x32_bf16 v[68:71], v[168:171], a[8:11], 0\nR 162,187,4096\n"
"M 68,172,24\nv_mfma_f32_16x16x32_bf16 v[52:55], v[168:171], a[4:7], 0\nR 164,187,8192\nM 52,172,20\ns_add_u32 s66, s71, 14684160\nbuffer_load_dwordx4 a[72:75], v252, s[20:23], s66 offen offset:0 sc1\n"
"v_mfma_f32_16x16x32_bf16 v[36:39], v[168:171], a[0:3], 0\nR 166,187,12288\nM 36,172,16\nG 4\nv_mfma_f32_16x16x32_bf16 v[40:43], v[176:179], a[0:3], 0\nR 168,184,16384\n"
"M 40,180,16\nv_mfma_f32_16x16x32_bf16 v[56:59], v[176:179], a[4:7], 0\nR 170,184,20480\nM 56,180,20\ns_add_u32 s67, s71, 14716928\nbuffer_load_dwordx4 a[76:79], v252, s[20:23], s67 offen offset:0 sc1\n"
"Lpg24:\ns_cbranch_execz Lpg28\nv_mfma_f32_16x16x32_bf16 v[72:75], v[176:179], a[8:11], 0\nR 172,184,24576\nM 72,180,24\nv_mfma_f32_16x16x32_bf16 v[88:91], v[176:179], a[12:15], 0\n"
"R 174,184,28672\nM 88,180,28\nG 4\nv_mfma_f32_16x16x32_bf16 v[92:95], v[160:163], a[12:15], 0\nR 176,185,16384\ns_add_u32 s63, s54, 1376256\n"
"M 92,164,28\nbuffer_load_dwordx4 a[80:83], v252, s[20:23], s64 offen offset:1024 sc1\nv_mfma_f32_16x16x32_bf16 v[76:79], v[160:163], a[8:11], 0\nR 178,185,20480\nM 76,164,24\ns_add_u32 m0, s50, 98304\n"
"v_mfma_f32_16x16x32_bf16 v[60:63], v[160:163], a[4:7], 0\nR 180,185,24576\nM 60,164,20\nbuffer_load_dwordx4 v220, s[12:15], s63 offen lds\nv_mfma_f32_16x16x32_bf16 v[44:47], v[160:163], a[0:3], 0\nR 182,185,28672\n"
"M 44,164,16\nG 4\nv_mfma_f32_16x16x32_bf16 v[96:99], v[168:171], a[0:3], 0\nR 160,186,16384\nM 96,172,16\nbuffer_load_dwordx4 a[84:87], v252, s[20:23], s65 offen offset:1024 sc1\n"
"v_mfma_f32_16x16x32_bf16 v[112:115], v[168:171], a[4:7], 0\nR 162,186,20480\nM 112,172,20\nv_mfma_f32_16x16x32_bf16 v[128:131], v[168:171], a[8:11], 0\nR 164,186,24576\nM 128,172,24\n"
"buffer_load_dwordx4 a[88:91], v252, s[20:23], s66 offen offset:1024 sc1\nv_mfma_f32_16x16x32_bf16 v[144:147], v[168:171], a[12:15], 0\nR 166,186,28672\nM 144,172,28\ns_barrier\nG 4\n"
"v_mfma_f32_16x16x32_bf16 v[148:151], v[176:179], a[12:15], 0\nR 168,187,16384\nM 148,180,28\nv_mfma_f32_16x16x32_bf16 v[132:135], v[176:179], a[8:11], 0\nR 170,187,20480\nM 132,180,24\n"
"buffer_load_dwordx4 a[92:95], v252, s[20:23], s67 offen offset:1024 sc1\nv_mfma_f32_16x16x32_bf16 v[116:119], v[176:179], a[4:7], 0\nR 172,187,24576\nM 116,180,20\nv_mfma_f32_16x16x32_bf16 v[100:103], v[176:179], a[0:3], 0\nR 174,187,28672\n"
"M 100,180,16\nG 4\nv_mfma_f32_16x16x32_bf16 v[104:107], v[160:163], a[0:3], 0\nR 176,184,32768\nM 104,164,16\nbuffer_load_dwordx4 a[96:99], v252, s[20:23], s64 offen offset:2048 sc1\n"
"v_mfma_f32_16x16x32_bf16 v[120:123], v[160:163], a[4:7], 0\nR 178,184,36864\nM 120,164,20\nv_mfma_f32_16x16x32_bf16 v[136:139], v[160:163], a[8:11], 0\nR 180,184,40960\nM 136,164,24\n"
"buffer_load_dwordx4 v221, s[12:15], s63 offen offset:1024 lds\nv_mfma_f32_16x16x32_bf16 v[152:155], v[160:163], a[12:15], 0\nR 182,184,45056\nM 152,164,28\nG 4\nv_mfma_f32_16x16x32_bf16 v[156:159], v[168:171], a[12:15], 0\n"
"R 160,185,32768\nM 156,172,28\nbuffer_load_dwordx4 a[100:103], v252, s[20:23], s65 offen offset:2048 sc1\nv_mfma_f32_16x16x32_bf16 v[140:143], v[168:171], a[8:11], 0\nR 162,185,36864\nM 140,172,24\n"
"s_add_u32 m0, s50, 106496\nv_mfma_f32_16x16x32_bf16 v[124:127], v[168:171], a[4:7], 0\nR 164,185,40960\ns_add_u32 s63, s54, 1605632\nM 124,172,20\nbuffer_load_dwordx4 a[104:107], v252, s[20:23], s66 offen offset:2048 sc1\n"
"v_mfma_f32_16x16x32_bf16 v[108:111], v[168:171], a[0:3], 0\nR 166,185,45056\nM 108,172,16\nG 4\ns_waitcnt vmcnt(20)\nM 32,176,32\n"
"R 168,186,32768\ns_waitcnt vmcnt(16)\nM 32,180,48\nbuffer_load_dwordx4 v220, s[12:15], s63 offen lds\nM 48,176,36\nR 170,186,36864\n"
"s_waitcnt vmcnt(16)\nM 48,180,52\nbuffer_load_dwordx4 a[108:111], v252, s[20:23], s67 offen offset:2048 sc1\nM 64,176,40\nR 172,186,40960\ns_waitcnt vmcnt(16)\n"
"M 64,180,56\nM 80,176,44\nR 174,186,45056\ns_waitcnt vmcnt(15)\nM 80,180,60\ns_barrier\n"
"G 4\nM 84,160,44\nR 176,187,32768\nM 84,164,60\nbuffer_load_dwordx4 a[112:115], v252, s[20:23], s64 offen offset:3072 sc1\nM 68,160,40\n"
"R 178,187,36864\nM 68,164,56\nM 52,160,36\nR 180,187,40960\nM 52,164,52\nM 36,160,32\n"
"R 182,187,45056\nM 36,164,48\nG 4\nM 40,168,32\nR 160,184,49152\nM 40,172,48\n"
"buffer_load_dwordx4 a[116:119], v252, s[20:23], s65 offen offset:3072 sc1\nM 56,168,36\nR 162,184,53248\nM 56,172,52\nM 72,168,40\nR 164,184,57344\n"
"M 72,172,56\nbuffer_load_dwordx4 a[120:123], v252, s[20:23], s66 offen offset:3072 sc1\nM 88,168,44\nR 166,184,61440\nM 88,172,60\nG 4\n"
"M 92,176,44\nR 168,185,49152\nM 92,180,60\nbuffer_load_dwordx4 v221, s[12:15], s63 offen offset:1024 lds\nM 76,176,40\nR 170,185,53248\n"
"M 76,180,56\nbuffer_load_dwordx4 a[124:127], v252, s[20:23], s67 offen offset:3072 sc1\nM 60,176,36\nR 172,185,57344\nM 60,180,52\nM 44,176,32\n"
"R 174,185,61440\nM 44,180,48\nG 4\nM 96,160,32\nR 176,186,49152\nM 96,164,48\n"
"s_add_u32 s64, s70, 14688256\nbuffer_load_dwordx4 a[128:131], v252, s[20:23], s64 offen offset:0 sc1\nM 112,160,36\nR 178,186,53248\nM 112,164,52\ns_lshr_b32 s84, s49, 4\n"
"M 128,160,40\nR 180,186,57344\nM 128,164,56\ns_add_u32 s84, s84, 83361792\nM 144,160,44\nR 182,186,61440\n"
"M 144,164,60\ns_barrier\nG 4\nM 148,168,44\nR 160,187,49152\nM 148,172,60\n"
"s_add_u32 s65, s70, 14721024\nbuffer_load_dwordx4 a[132:135], v252, s[20:23], s65 offen offset:0 sc1\nM 132,168,40\nR 162,187,53248\nM 132,172,56\nbuffer_load_dwordx4 v[0:3], v252, s[20:23], s84 offen offset:0 sc1\n"
"M 116,168,36\nR 164,187,57344\nM 116,172,52\ns_add_u32 s66, s71, 14688256\nbuffer_load_dwordx4 a[136:139], v252, s[20:23], s66 offen offset:0 sc1\nM 100,168,32\n"
"R 166,187,61440\nM 100,172,48\nG 4\nM 104,176,32\nR 168,188,0\nM 104,180,48\n"
"buffer_load_dwordx4 v[4:7], v252, s[20:23], s84 offen offset:1024 sc1\nM 120,176,36\nR 170,188,4096\nM 120,180,52\ns_add_u32 s67, s71, 14721024\nbuffer_load_dwordx4 a[140:143], v252, s[20:23], s67 offen offset:0 sc1\n"
"M 136,176,40\nR 172,188,8192\nM 136,180,56\nbuffer_load_dwordx4 v[8:11], v252, s[20:23], s84 offen offset:2048 sc1\nM 152,176,44\nR 174,188,12288\n"
"M 152,180,60\nbuffer_load_dwordx4 v[12:15], v252, s[20:23], s84 offen offset:3072 sc1\nG 4\nM 156,160,44\nR 176,189,0\nM 156,164,60\n"
"buffer_load_dwordx4 a[144:147], v252, s[20:23], s64 offen offset:1024 sc1\nM 140,160,40\nR 178,189,4096\nM 140,164,56\nM 124,160,36\nR 180,189,8192\n"
"M 124,164,52\nM 108,160,32\nR 182,189,12288\nM 108,164,48\nG 4\ns_waitcnt vmcnt(28)\n"
"M 32,168,64\nR 160,190,0\ns_waitcnt vmcnt(24)\nM 32,172,80\nbuffer_load_dwordx4 a[148:151], v252, s[20:23], s65 offen offset:1024 sc1\nM 48,168,68\n"
"R 162,190,4096\ns_waitcnt vmcnt(23)\nM 48,172,84\ns_add_u32 m0, s50, 114688\nM 64,168,72\nR 164,190,8192\n"
"s_add_u32 s63, s54, 1376384\ns_waitcnt vmcnt(22)\nM 64,172,88\nbuffer_load_dwordx4 a[152:155], v252, s[20:23], s66 offen offset:1024 sc1\nM 80,168,76\nR 166,190,12288\n"
"s_waitcnt vmcnt(22)\nM 80,172,92\ns_barrier\nG 4\nM 84,176,76\nR 168,191,0\n"
"M 84,180,92\nbuffer_load_dwordx4 v220, s[12:15], s63 offen lds\nM 68,176,72\nR 170,191,4096\nM 68,180,88\nbuffer_load_dwordx4 a[156:159], v252, s[20:23], s67 offen offset:1024 sc1\n"
"M 52,176,68\nR 172,191,8192\nM 52,180,84\nbuffer_load_dwordx4 v221, s[12:15], s63 offen offset:1024 lds\nM 36,176,64\nR 174,191,12288\n"
"s_add_u32 m0, s50, 0\nM 36,180,80\nG 4\nM 40,160,64\nR 176,188,16384\ns_add_u32 s63, s54, 1605760\n"
"M 40,164,80\nbuffer_load_dwordx4 a[160:163], v252, s[20:23], s64 offen offset:2048 sc1\nM 56,160,68\nR 178,188,20480\nM 56,164,84\nbuffer_load_dwordx4 v220, s[12:15], s63 offen lds\n"
"M 72,160,72\nR 180,188,24576\nM 72,164,88\nbuffer_load_dwordx4 v221, s[12:15], s63 offen offset:1024 lds\nM 88,160,76\nR 182,188,28672\n"
"M 88,164,92\nG 4\nM 92,168,76\nR 160,189,16384\nM 92,172,92\nbuffer_load_dwordx4 a[164:167], v252, s[20:23], s65 offen offset:2048 sc1\n"
"M 76,168,72\nR 162,189,20480\nM 76,172,88\nM 60,168,68\nR 164,189,24576\nM 60,172,84\n"
"buffer_load_dwordx4 a[168:171], v252, s[20:23], s66 offen offset:2048 sc1\nM 44,168,64\nR 166,189,28672\nM 44,172,80\nG 4\nM 96,176,64\n"
"R 168,190,16384\ns_mov_b32 s85, 0x0c0c0400\nM 96,180,80\ns_waitcnt vmcnt(16)\nv_perm_b32 v16, v1, v0, s85\nv_perm_b32 v20, v3, v2, s85\n"
"M 112,176,68\nR 170,190,20480\nv_lshl_or_b32 v16, v20, 16, v16\nM 112,180,84\nbuffer_load_dwordx4 a[172:175], v252, s[20:23], s67 offen offset:2048 sc1\nM 128,176,72\n"
"R 172,190,24576\ns_waitcnt vmcnt(15)\nv_perm_b32 v17, v5, v4, s85\nM 128,180,88\ns_add_u32 m0, s50, 8192\nv_perm_b32 v21, v7, v6, s85\n"
"v_lshl_or_b32 v17, v21, 16, v17\nM 144,176,76\nR 174,190,28672\ns_add_u32 s63, s54, 1835008\nM 144,180,92\ns_barrier\n"
"G 4\nM 148,160,76\nR 176,191,16384\ns_waitcnt vmcnt(13)\nv_perm_b32 v18, v9, v8, s85\nM 148,164,92\n"
"buffer_load_dwordx4 a[176:179], v252, s[20:23], s64 offen offset:3072 sc1\nM 132,160,72\nR 178,191,20480\nv_perm_b32 v22, v11, v10, s85\nM 132,164,88\nbuffer_load_dwordx4 v220, s[12:15], s63 offen lds\n"
"M 116,160,68\nR 180,191,24576\nv_lshl_or_b32 v18, v22, 16, v18\nM 116,164,84\nbuffer_load_dwordx4 v221, s[12:15], s63 offen offset:1024 lds\nM 100,160,64\n"
"R 182,191,28672\ns_add_u32 m0, s50, 16384\nM 100,164,80\nG 4\ns_waitcnt vmcnt(15)\nv_perm_b32 v19, v13, v12, s85\n"
"v_perm_b32 v23, v15, v14, s85\nM 104,168,64\nR 160,188,32768\ns_add_u32 s63, s54, 2064384\nM 104,172,80\nbuffer_load_dwordx4 a[180:183], v252, s[20:23], s65 offen offset:3072 sc1\n"
"M 120,168,68\nR 162,188,36864\nv_lshl_or_b32 v19, v23, 16, v19\nM 120,172,84\nbuffer_load_dwordx4 v220, s[12:15], s63 offen lds\nM 136,168,72\n"
"R 164,188,40960\nv_min3_u32 v230, v16, v17, v18\nM 136,172,88\nbuffer_load_dwordx4 a[184:187], v252, s[20:23], s66 offen offset:3072 sc1\nM 152,168,76\nR 166,188,45056\n"
"v_min3_u32 v230, v230, v19, v19\nM 152,172,92\nG 4\nv_max3_u32 v231, v16, v17, v18\nv_max3_u32 v231, v231, v19, v19\nM 156,176,76\n"
"R 168,189,32768\nbuffer_load_dwordx4 v221, s[12:15], s63 offen offset:1024 lds\nM 156,180,92\nv_cmp_ne_u32 s[86:87], s46, v230\nv_cmp_ne_u32 s[88:89], s46, v231\nM 140,176,72\n"
"R 170,189,36864\nM 140,180,88\nbuffer_load_dwordx4 a[188:191], v252, s[20:23], s67 offen offset:3072 sc1\nM 124,176,68\nR 172,189,40960\nM 124,180,84\n"
"s_or_b64 s[86:87], s[86:87], s[88:89]\ns_cbranch_scc1 Lq818\nLq819:\nv_and_b32 v0, 0xffffff00, v0\nM 108,176,64\nR 174,189,45056\n"
"v_and_b32 v1, 0xffffff00, v1\nM 108,180,80\nG 4\nv_and_b32 v2, 0xffffff00, v2\nv_and_b32 v3, 0xffffff00, v3\nM 32,160,96\n"
"R 176,190,32768\nv_and_b32 v4, 0xffffff00, v4\nM 32,164,112\ns_add_u32 s64, s70, 14692352\nbuffer_load_dwordx4 a[192:195], v252, s[20:23], s64 offen offset:0 sc1\nM 48,160,100\n"
"R 178,190,36864\nv_and_b32 v5, 0xffffff00, v5\nM 48,164,116\nv_and_b32 v6, 0xffffff00, v6\nv_and_b32 v7, 0xffffff00, v7\nM 64,160,104\n"
"R 180,190,40960\nv_and_b32 v8, 0xffffff00, v8\nM 64,164,120\ns_add_u32 m0, s50, 24576\nv_and_b32 v9, 0xffffff00, v9\nv_and_b32 v10, 0xffffff00, v10\n"
"M 80,160,108\nR 182,190,45056\ns_add_u32 s63, s54, 2293760\nM 80,164,124\ns_waitcnt vmcnt(12)\ns_barrier\n"
"G 4\nM 84,168,108\nR 160,191,32768\nv_and_b32 v11, 0xffffff00, v11\nM 84,172,124\ns_add_u32 s65, s70, 14725120\n"
"buffer_load_dwordx4 a[196:199], v252, s[20:23], s65 offen offset:0 sc1\nM 68,168,104\nR 162,191,36864\nv_and_b32 v12, 0xffffff00, v12\nM 68,172,120\nbuffer_load_dwordx4 v220, s[12:15], s63 offen lds\n"
"M 52,168,100\nR 164,191,40960\nv_and_b32 v13, 0xffffff00, v13\nM 52,172,116\ns_add_u32 s66, s71, 14692352\nbuffer_load_dwordx4 a[200:203], v252, s[20:23], s66 offen offset:0 sc1\n"
"M 36,168,96\nR 166,191,45056\nv_and_b32 v14, 0xffffff00, v14\nM 36,172,112\nG 4\nv_and_b32 v15, 0xffffff00, v15\n"
"v_add_f32 v0, v0, v8\nM 40,176,96\nR 168,188,49152\nbuffer_load_dwordx4 v221, s[12:15], s63 offen offset:1024 lds\nM 40,180,112\ns_add_u32 m0, s50, 32768\n"
"s_add_u32 s63, s54, 2523136\nbuffer_load_dwordx4 v220, s[12:15], s63 offen lds\nM 56,176,100\nR 170,188,53248\nv_add_f32 v1, v1, v9\nM 56,180,116\n"
"s_add_u32 s67, s71, 14725120\nbuffer_load_dwordx4 a[204:207], v252, s[20:23], s67 offen offset:0 sc1\nM 72,176,104\nR 172,184,0\nv_add_f32 v2, v2, v10\nM 72,180,120\n"
"buffer_load_dwordx4 v221, s[12:15], s63 offen offset:1024 lds\nM 88,176,108\nR 174,184,4096\nv_add_f32 v3, v3, v11\nM 88,180,124\nG 4\n"
"v_add_f32 v4, v4, v12\nv_add_f32 v5, v5, v13\nM 92,160,108\nR 176,189,49152\nv_add_f32 v6, v6, v14\nM 92,164,124\n"
"buffer_load_dwordx4 a[208:211], v252, s[20:23], s64 offen offset:1024 sc1\nM 76,160,104\nR 178,189,53248\nv_add_f32 v7, v7, v15\nM 76,164,120\nv_add_f32 v0, v0, v4\n"
"v_add_f32 v1, v1, v5\nM 60,160,100\nR 180,185,0\nLq1034:\ns_lshr_b32 s60, s45, 3\ns_lshl2_add_u32 s60, s60, s40\n"
"s_lshl_b32 s60, s60, 8\nv_lshl_add_u32 v224, v252, 3, s60\nv_add_u32 v224, Lq1034+4096-LqPC, v224\nv_min_u32 v224, LqE-LqPC-4, v224\ns_mov_b64 exec, 3\nglobal_load_dword v224, v224, s[98:99]\n"
"s_mov_b64 exec, -1\nM 60,164,116\nv_add_f32 v2, v2, v6\nv_add_f32 v3, v3, v7\nM 44,160,96\nR 182,185,4096\n"
"v_permlane32_swap_b32 v0, v2\nM 44,164,112\nG 4\nv_permlane32_swap_b32 v1, v3\nv_add_f32 v0, v0, v2\nM 96,168,96\n"
"R 160,190,49152\nv_add_f32 v1, v1, v3\nM 96,172,112\nbuffer_load_dwordx4 a[212:215], v252, s[20:23], s65 offen offset:1024 sc1\nM 112,168,100\nR 162,190,53248\n"
"v_permlane16_swap_b32 v0, v1\nM 112,172,116\ns_add_u32 m0, s50, 40960\nv_add_f32 v230, v0, v1\nv_mov_b32 v232, v230\nM 128,168,104\n"
"R 164,186,0\ns_add_u32 s63, s54, 2752512\nM 128,172,120\nbuffer_load_dwordx4 a[216:219], v252, s[20:23], s66 offen offset:1024 sc1\nM 144,168,108\nR 166,186,4096\n"
"v_permlane16_swap_b32 v230, v232\nM 144,172,124\ns_waitcnt vmcnt(13)\ns_barrier\nG 4\nM 148,176,108\n"
"R 168,191,49152\nv_mov_b32 v231, v230\nM 148,180,124\nbuffer_load_dwordx4 v220, s[12:15], s63 offen lds\nM 132,176,104\nR 170,191,53248\n"
"v_mov_b32 v233, v232\nM 132,180,120\nbuffer_load_dwordx4 a[220:223], v252, s[20:23], s67 offen offset:1024 sc1\nM 116,176,100\nR 172,187,0\nv_permlane32_swap_b32 v230, v231\n"
"M 116,180,116\nbuffer_load_dwordx4 v221, s[12:15], s63 offen offset:1024 lds\nM 100,176,96\nR 174,187,4096\ns_add_u32 m0, s50, 49152\nM 100,180,112\n"
"G 4\nv_permlane32_swap_b32 v232, v233\nv_mul_f32 v230, 0xba800000, v230\nM 104,160,96\nR 176,184,8192\ns_add_u32 s63, s54, 2981888\n"
"M 104,164,112\nbuffer_load_dwordx4 a[224:227], v252, s[20:23], s64 offen offset:2048 sc1\nM 120,160,100\nR 178,184,12288\nv_mul_f32 v214, v230, v230\nM 120,164,116\n"
"buffer_load_dwordx4 v220, s[12:15], s63 offen lds\nM 136,160,104\nR 180,184,16384\nv_mul_f32 v231, 0x3a800000, v231\nM 136,164,120\nbuffer_load_dwordx4 v221, s[12:15], s63 offen offset:1024 lds\n"
"Lpg25:\ns_cbranch_execz Lpg29\nM 152,160,108\nR 182,184,20480\nv_sub_f32 v231, v231, v214\nM 152,164,124\n"
"G 4\nv_max_f32 v231, 0, v231\nv_add_f32 v231, 0x3727c5ac, v231\nM 156,168,108\nR 160,185,8192\nv_and_b32 v208, s77, v230\n"
"M 156,172,124\nbuffer_load_dwordx4 a[228:231], v252, s[20:23], s65 offen offset:2048 sc1\nM 140,168,104\nR 162,185,12288\nv_sub_f32 v230, v230, v208\nM 140,172,120\n"
"v_rsq_f32 v234, v231\nM 124,168,100\nR 164,185,16384\nv_mul_f32 v216, v231, v234\nM 124,172,116\nbuffer_load_dwordx4 a[232:235], v252, s[20:23], s66 offen offset:2048 sc1\n"
"M 108,168,96\nR 166,185,20480\nv_cvt_pk_bf16_f32 v209, v230, v216\nM 108,172,112\nG 4\nv_cvt_pk_bf16_f32 v208, v208, v208\n"
"v_mul_f32 v214, 0xc0135761, v234\nM 32,176,128\nR 168,186,8192\nv_mul_f32 v215, v234, v234\nM 32,180,144\ns_mov_b64 s[60:61], 0xffff\n"
"v_cndmask_b32 v208, 0, v208, s[60:61]\nv_mul_f32 v215, v215, v214\nM 48,176,132\nR 170,186,12288\nv_mul_f32 v215, 0x3d372713, v215\nM 48,180,148\n"
"buffer_load_dwordx4 a[236:239], v252, s[20:23], s67 offen offset:2048 sc1\nM 64,176,136\nR 172,186,16384\nv_mul_f32 v232, 0xba800000, v232\nM 64,180,152\ns_add_u32 m0, s50, 57344\n"
"s_mov_b64 s[60:61], 0xffff\nv_cndmask_b32 v209, 0, v209, s[60:61]\nM 80,176,140\nR 174,186,20480\ns_add_u32 s63, s54, 3211264\nM 80,180,156\n"
"s_waitcnt vmcnt(13)\ns_barrier\nG 4\nM 84,160,140\nR 176,187,8192\nv_mul_f32 v217, v232, v232\n"
"M 84,164,156\nbuffer_load_dwordx4 a[240:243], v252, s[20:23], s64 offen offset:3072 sc1\nM 68,160,136\nR 178,187,12288\nv_mul_f32 v233, 0x3a800000, v233\nM 68,164,152\n"
"buffer_load_dwordx4 v220, s[12:15], s63 offen lds\nM 52,160,132\nR 180,187,16384\nv_sub_f32 v233, v233, v217\nM 52,164,148\nbuffer_load_dwordx4 v221, s[12:15], s63 offen offset:1024 lds\n"
"M 36,160,128\nR 182,187,20480\ns_add_u32 m0, s50, 65536\nM 36,164,144\nG 4\nv_max_f32 v233, 0, v233\n"
"v_add_f32 v233, 0x3727c5ac, v233\nM 40,168,128\nR 160,184,24576\ns_add_u32 s63, s54, 3440640\nM 40,172,144\nbuffer_load_dwordx4 a[244:247], v252, s[20:23], s65 offen offset:3072 sc1\n"
"M 56,168,132\nR 162,184,28672\nv_and_b32 v212, s77, v232\nM 56,172,148\nbuffer_load_dwordx4 v220, s[12:15], s63 offen lds\nM 72,168,136\n"
"R 164,184,32768\nv_sub_f32 v232, v232, v212\nM 72,172,152\nbuffer_load_dwordx4 a[248:251], v252, s[20:23], s66 offen offset:3072 sc1\nM 88,168,140\nR 166,184,36864\n"
"v_cvt_pk_bf16_f32 v212, v212, v212\nM 88,172,156\nG 4\nv_rsq_f32 v235, v233\nM 92,176,140\nR 168,185,24576\n"
"buffer_load_dwordx4 v221, s[12:15], s63 offen offset:1024 lds\nM 92,180,156\ns_mov_b64 s[60:61], 0xffff\nv_cndmask_b32 v212, 0, v212, s[60:61]\nv_mul_f32 v219, v233, v235\nM 76,176,136\n"
"R 170,185,28672\nv_cvt_pk_bf16_f32 v213, v232, v219\nM 76,180,152\nbuffer_load_dwordx4 a[252:255], v252, s[20:23], s67 offen offset:3072 sc1\nM 60,176,132\nR 172,185,32768\n"
"v_mul_f32 v217, 0xc0135761, v235\nM 60,180,148\ns_mov_b64 s[60:61], 0xffff\nv_cndmask_b32 v213, 0, v213, s[60:61]\nv_mul_f32 v218, v235, v235\nM 44,176,128\n"
"R 174,185,36864\nv_mul_f32 v218, v218, v217\nM 44,180,144\nG 4\nv_mul_f32 v218, 0x3d372713, v218\nM 32,160,160\n"
"R 176,186,24576\nM 32,164,176\nM 48,160,164\nR 178,186,28672\nM 48,164,180\ns_add_u32 m0, s50, 73728\n"
"M 64,160,168\nR 180,186,32768\nM 64,164,184\ns_add_u32 s63, s54, 1835136\nM 80,160,172\nR 182,186,36864\n"
"M 80,164,188\ns_waitcnt vmcnt(11)\ns_barrier\nG 4\nM 84,168,172\nR 160,187,24576\n"
"M 84,172,188\nbuffer_load_dwordx4 v220, s[12:15], s63 offen lds\nM 68,168,168\nR 162,187,28672\nM 68,172,184\nM 52,168,164\n"
"R 164,187,32768\nv_bfe_u32 v250, v252, 5, 1\nv_mul_u32_u24 v250, 2560, v250\nM 52,172,180\nbuffer_load_dwordx4 v221, s[12:15], s63 offen offset:1024 lds\nv_and_b32 v251, 16, v252\n"
"M 36,168,160\nR 166,187,36864\ns_add_u32 m0, s50, 81920\nv_lshl_add_u32 v250, v251, 1, v250\nM 36,172,176\ns_lshl_b32 s60, s90, 6\n"
"v_add_u32 v250, s60, v250\nG 4\nM 40,176,160\nR 168,184,40960\ns_add_u32 s63, s54, 2064512\nM 40,180,176\n"
"buffer_load_dwordx4 v220, s[12:15], s63 offen lds\nM 56,176,164\nR 170,184,45056\nM 56,180,180\nM 72,176,168\nR 172,184,49152\n"
"M 72,180,184\nbuffer_load_dwordx4 v221, s[12:15], s63 offen offset:1024 lds\nM 88,176,172\nR 174,184,53248\ns_add_u32 m0, s50, 90112\nM 88,180,188\n"
"G 4\nM 92,160,172\nR 176,185,40960\ns_add_u32 s63, s54, 2293888\nM 92,164,188\nbuffer_load_dwordx4 v220, s[12:15], s63 offen lds\n"
"M 76,160,168\nR 178,185,45056\nM 76,164,184\nM 60,160,164\nR 180,185,49152\nM 60,164,180\n"
"buffer_load_dwordx4 v221, s[12:15], s63 offen offset:1024 lds\nM 44,160,160\nR 182,185,53248\ns_add_u32 s63, s54, 2523264\nM 44,164,176\nG 4\n"
"M 32,168,192\nR 160,186,40960\nM 32,172,208\ns_add_u32 m0, s50, 98304\nM 48,168,196\nR 162,186,45056\n"
"M 48,172,212\nbuffer_load_dwordx4 v220, s[12:15], s63 offen lds\nM 64,168,200\nR 164,186,49152\nM 64,172,216\nM 80,168,204\n"
"R 166,186,53248\nM 80,172,220\ns_waitcnt vmcnt(8)\ns_barrier\nG 4\nM 84,176,204\n"
"R 168,187,40960\nM 84,180,220\nbuffer_load_dwordx4 v221, s[12:15], s63 offen offset:1024 lds\nM 68,176,200\nR 170,187,45056\ns_add_u32 s63, s54, 2752640\n"
"M 68,180,216\ns_add_u32 m0, s50, 106496\nM 52,176,196\nR 172,187,49152\nM 52,180,212\nbuffer_load_dwordx4 v220, s[12:15], s63 offen lds\n"
"M 36,176,192\nR 174,187,53248\nM 36,180,208\nG 4\nM 40,160,192\nR 176,184,57344\n"
"M 40,164,208\nbuffer_load_dwordx4 v221, s[12:15], s63 offen offset:1024 lds\nM 56,160,196\nR 178,184,61440\ns_add_u32 s63, s54, 2982016\nM 56,164,212\n"
"s_add_u32 m0, s50, 114688\nM 72,160,200\nR 180,188,0\nM 72,164,216\nbuffer_load_dwordx4 v220, s[12:15], s63 offen lds\nM 88,160,204\n"
"R 182,188,4096\nM 88,164,220\nG 4\nM 92,168,204\nR 160,185,57344\nM 92,172,220\n"
"buffer_load_dwordx4 v221, s[12:15], s63 offen offset:1024 lds\nM 76,168,200\nR 162,185,61440\ns_add_u32 s63, s54, 3211392\nM 76,172,216\ns_add_u32 m0, s50, 0\n"
"M 60,168,196\nR 164,189,0\nM 60,172,212\nbuffer_load_dwordx4 v220, s[12:15], s63 offen lds\nM 44,168,192\nR 166,189,4096\n"
"s_mov_b32 s60, 157184\nv_bfe_u32 v246, v252, 4, 4\nv_lshl_add_u32 v246, v246, 3, s60\nM 44,172,208\nds_read_b64 v[238:239], v246 offset:0\nG 5\n"
"M 32,176,224\nR 168,186,57344\nds_read_b64 v[240:241], v246 offset:128\nM 32,180,240\nds_read_b64 v[242:243], v246 offset:256\nM 48,176,228\n"
"R 170,186,61440\nds_read_b64 v[244:245], v246 offset:384\nM 48,180,244\nds_read_b64 v[246:247], v246 offset:0\nM 64,176,232\nR 172,190,0\n"
"M 64,180,248\nbuffer_load_dwordx4 v221, s[12:15], s63 offen offset:1024 lds\nM 80,176,236\nR 174,190,4096\ns_add_u32 m0, s50, 8192\ns_waitcnt vmcnt(14)\n"
"M 80,180,252\ns_waitcnt vmcnt(10) lgkmcnt(2)\ns_barrier\nM 84,160,236\nR 176,187,57344\ns_add_u32 s63, s54, 3440768\n"
"M 84,164,252\nbuffer_load_dwordx4 v220, s[12:15], s63 offen lds\nM 68,160,232\nR 178,187,61440\nM 68,164,248\nM 52,160,228\n"
"R 180,191,0\nv_mfma_f32_16x16x32_bf16 v[32:35], v[238:241], v[208:211], v[32:35]\nM 52,164,244\nbuffer_load_dwordx4 v221, s[12:15], s63 offen offset:1024 lds\nM 36,160,224\nR 182,191,4096\n"
"v_mfma_f32_16x16x32_bf16 v[36:39], v[240:243], v[208:211], v[36:39]\ns_add_u32 m0, s50, 16384\nM 36,164,240\nG 4\nM 40,168,224\nR 160,188,8192\n"
"v_mfma_f32_16x16x32_bf16 v[40:43], v[242:245], v[208:211], v[40:43]\ns_add_u32 s63, s55, 0\nM 40,172,240\nbuffer_load_dwordx4 v222, s[12:15], s63 offen lds\nM 56,168,228\nR 162,188,12288\n"
"v_mfma_f32_16x16x32_bf16 v[44:47], v[244:247], v[208:211], v[44:47]\nM 56,172,244\nM 72,168,232\nR 164,188,16384\nv_mfma_f32_16x16x32_bf16 v[48:51], v[244:247], v[210:213], v[48:51]\nM 72,172,248\n"
"buffer_load_dwordx4 v223, s[12:15], s63 offen offset:1024 lds\nM 88,168,236\nR 166,188,20480\nv_mfma_f32_16x16x32_bf16 v[52:55], v[238:241], v[210:213], v[52:55]\ns_add_u32 s63, s55, 458752\nM 88,172,252\n"
"G 4\nM 92,176,236\nR 168,189,8192\nv_mfma_f32_16x16x32_bf16 v[56:59], v[240:243], v[210:213], v[56:59]\nM 92,180,252\ns_add_u32 m0, s50, 24576\n"
"M 76,176,232\nR 170,189,12288\nv_mfma_f32_16x16x32_bf16 v[60:63], v[242:245], v[210:213], v[60:63]\nM 76,180,248\nbuffer_load_dwordx4 v222, s[12:15], s63 offen lds\nM 60,176,228\n"
"R 172,189,16384\nM 60,180,244\nM 44,176,224\nR 174,189,20480\nM 44,180,240\ns_waitcnt vmcnt(11)\n"
"s_barrier\nG 4\nM 96,160,128\nR 176,190,8192\nM 96,164,144\ns_mov_b32 m0, s51\n"
"M 112,160,132\nR 178,190,12288\nM 112,164,148\nds_write_addtid_b32 v64 offset:0\nM 128,160,136\nR 180,190,16384\n"
"M 128,164,152\nds_write_addtid_b32 v65 offset:256\nM 144,160,140\nR 182,190,20480\nds_write_addtid_b32 v66 offset:512\nM 144,164,156\n"
"ds_write_addtid_b32 v67 offset:768\nds_write_addtid_b32 v68 offset:1024\nG 9\nM 148,168,140\nR 160,191,8192\nds_write_addtid_b32 v69 offset:1280\n"
"M 148,172,156\nds_write_addtid_b32 v70 offset:1536\nds_write_addtid_b32 v71 offset:1792\nM 132,168,136\nR 162,191,12288\nds_write_addtid_b32 v72 offset:2048\n"
"M 132,172,152\nds_write_addtid_b32 v73 offset:2304\nds_write_addtid_b32 v74 offset:2560\nM 116,168,132\nR 164,191,16384\nds_write_addtid_b32 v75 offset:2816\n"
"M 116,172,148\nds_write_addtid_b32 v76 offset:3072\nds_write_addtid_b32 v77 offset:3328\nM 100,168,128\nR 166,191,20480\nds_write_addtid_b32 v78 offset:3584\n"
"M 100,172,144\nds_write_addtid_b32 v79 offset:3840\nds_write_addtid_b32 v80 offset:4096\nG 15\nM 104,176,128\nR 168,188,24576\n"
"ds_write_addtid_b32 v81 offset:4352\nM 104,180,144\nds_write_addtid_b32 v82 offset:4608\nds_write_addtid_b32 v83 offset:4864\nM 120,176,132\nR 170,188,28672\n"
"ds_write_addtid_b32 v84 offset:5120\nM 120,180,148\nds_write_addtid_b32 v85 offset:5376\nds_write_addtid_b32 v86 offset:5632\nM 136,176,136\nR 172,188,32768\n"
"ds_write_addtid_b32 v87 offset:5888\nM 136,180,152\nds_write_addtid_b32 v88 offset:6144\nds_write_addtid_b32 v89 offset:6400\nM 152,176,140\nR 174,188,36864\n"
"ds_write_addtid_b32 v90 offset:6656\nM 152,180,156\nds_write_addtid_b32 v91 offset:6912\nds_write_addtid_b32 v92 offset:7168\nG 15\nM 156,160,140\n"
"R 176,189,24576\nds_write_addtid_b32 v93 offset:7424\nM 156,164,156\nds_write_addtid_b32 v94 offset:7680\nds_write_addtid_b32 v95 offset:7936\nM 140,160,136\n"
"R 178,189,28672\ns_add_u32 m0, s50, 24576\nM 140,164,152\nbuffer_load_dwordx4 v223, s[12:15], s63 offen offset:1024 lds\nM 124,160,132\nR 180,189,32768\n"
"s_waitcnt vmcnt(8) lgkmcnt(2)\ns_barrier\nM 124,164,148\ns_add_u32 m0, s50, 32768\nds_read2st64_b32 v[64:65], v225 offset0:0 offset1:1\nds_read2st64_b32 v[66:67], v225 offset0:2 offset1:3\n"
"M 108,160,128\nR 182,189,36864\ns_add_u32 s63, s55, 917504\nds_read2st64_b32 v[68:69], v225 offset0:4 offset1:5\nM 108,164,144\nds_read2st64_b32 v[70:71], v225 offset0:6 offset1:7\n"
"ds_read2st64_b32 v[72:73], v225 offset0:8 offset1:9\nM 96,168,160\nR 160,190,24576\nds_read2st64_b32 v[74:75], v225 offset0:10 offset1:11\nM 96,172,176\nbuffer_load_dwordx4 v222, s[12:15], s63 offen lds\n"
"ds_read2st64_b32 v[76:77], v225 offset0:12 offset1:13\nM 112,168,164\nR 162,190,28672\nds_read2st64_b32 v[78:79], v225 offset0:14 offset1:15\nM 112,172,180\nds_read2st64_b32 v[80:81], v225 offset0:16 offset1:17\n"
"ds_read2st64_b32 v[82:83], v225 offset0:18 offset1:19\nG 12\nv_add_f32 v32, v32, v64\nM 128,168,168\nR 164,190,32768\nds_read2st64_b32 v[84:85], v225 offset0:20 offset1:21\n"
"v_add_f32 v33, v33, v65\nM 128,172,184\nds_read2st64_b32 v[86:87], v225 offset0:22 offset1:23\nds_read2st64_b32 v[88:89], v225 offset0:24 offset1:25\nG 15\nv_add_f32 v34, v34, v66\n"
"M 144,168,172\nR 166,190,36864\nds_read2st64_b32 v[90:91], v225 offset0:26 offset1:27\nv_add_f32 v35, v35, v67\nM 144,172,188\nds_read2st64_b32 v[92:93], v225 offset0:28 offset1:29\n"
"ds_read2st64_b32 v[94:95], v225 offset0:30 offset1:31\nG 15\nM 148,176,172\nR 168,191,24576\nv_add_f32 v36, v36, v68\nv_add_f32 v37, v37, v69\n"
"M 148,180,188\nbuffer_load_dwordx4 v223, s[12:15], s63 offen offset:1024 lds\nM 132,176,168\nR 170,191,28672\nv_add_f32 v38, v38, v70\nM 132,180,184\n"
"s_add_u32 m0, s50, 40960\nv_add_f32 v39, v39, v71\nv_add_f32 v40, v40, v72\nM 116,176,164\nR 172,191,32768\nv_add_f32 v41, v41, v73\n"
"M 116,180,180\ns_add_u32 s63, s55, 1376256\nG 15\nv_add_f32 v42, v42, v74\nv_add_f32 v43, v43, v75\nM 100,176,160\n"
"R 174,191,36864\nv_add_f32 v44, v44, v76\nM 100,180,176\nG 7\nv_add_f32 v45, v45, v77\nv_add_f32 v46, v46, v78\n"
"M 104,160,160\nR 176,188,40960\nv_add_f32 v47, v47, v79\nM 104,164,176\nbuffer_load_dwordx4 v222, s[12:15], s63 offen lds\nM 120,160,164\n"
"R 178,188,45056\nv_add_f32 v48, v48, v80\nM 120,164,180\nv_add_f32 v49, v49, v81\nv_add_f32 v50, v50, v82\nM 136,160,168\n"
"R 180,188,49152\nv_add_f32 v51, v51, v83\nM 136,164,184\nv_add_f32 v52, v52, v84\nv_add_f32 v53, v53, v85\nM 152,160,172\n"
"R 182,188,53248\nv_add_f32 v54, v54, v86\nM 152,164,188\nG 4\nv_add_f32 v55, v55, v87\nv_add_f32 v56, v56, v88\n"
"M 156,168,172\nR 160,189,40960\nv_add_f32 v57, v57, v89\nM 156,172,188\nbuffer_load_dwordx4 v223, s[12:15], s63 offen offset:1024 lds\nM 140,168,168\n"
"R 162,189,45056\nv_add_f32 v58, v58, v90\nM 140,172,184\ns_add_u32 m0, s50, 49152\nv_add_f32 v59, v59, v91\nv_add_f32 v60, v60, v92\n"
"M 124,168,164\nR 164,189,49152\nv_add_f32 v61, v61, v93\nM 124,172,180\ns_add_u32 s63, s55, 1835008\nv_add_f32 v62, v62, v94\n"
"v_add_f32 v63, v63, v95\nM 108,168,160\nR 166,189,53248\nv_mul_f32 v230, v32, v32\nv_mul_f32 v231, v33, v33\nM 108,172,176\n"
"s_add_u32 s84, s73, 37748736\ns_add_u32 s85, s73, 37863424\nG 4\nv_mul_f32 v232, v34, v34\nM 96,176,192\nR 168,190,40960\n"
"v_mul_f32 v233, v35, v35\nv_fma_f32 v230, v230, v215, v214\nM 96,180,208\nbuffer_load_dwordx4 v222, s[12:15], s63 offen lds\nv_fma_f32 v231, v231, v215, v214\nLpg26:\n"
"s_cbranch_execz Lpg30\nM 112,176,196\nR 170,190,45056\ns_waitcnt vmcnt(7)\ns_barrier\nv_fma_f32 v232, v232, v215, v214\n"
"M 112,180,212\nv_fma_f32 v233, v233, v215, v214\nv_mul_f32 v230, v32, v230\nv_mul_f32 v231, v33, v231\nM 128,176,200\nR 172,190,49152\n"
"v_mul_f32 v232, v34, v232\nv_mul_f32 v233, v35, v233\nM 128,180,216\nv_exp_f32 v230, v230\nv_exp_f32 v231, v231\nM 144,176,204\n"
"R 174,190,53248\nv_fma_f32 v230, v230, v216, v216\nv_exp_f32 v232, v232\nM 144,180,220\nG 4\nv_exp_f32 v233, v233\n"
"v_fma_f32 v231, v231, v216, v216\nM 148,160,204\nR 176,191,40960\nv_fma_f32 v232, v232, v216, v216\nv_fma_f32 v233, v233, v216, v216\nM 148,164,220\n"
"buffer_load_dwordx4 v223, s[12:15], s63 offen offset:1024 lds\nv_rcp_f32 v230, v230\nM 132,160,200\nR 178,191,45056\nv_mul_f32 v230, v32, v230\nv_rcp_f32 v231, v231\n"
"M 132,164,216\ns_add_u32 m0, s50, 57344\nv_rcp_f32 v232, v232\nv_rcp_f32 v233, v233\nM 116,160,196\nR 180,191,49152\n"
"v_mul_f32 v231, v33, v231\nv_mul_f32 v232, v34, v232\nM 116,164,212\ns_add_u32 s63, s55, 2293760\nv_mul_f32 v233, v35, v233\nv_cvt_pk_bf16_f32 v32, v230, v231\n"
"v_cvt_pk_bf16_f32 v33, v232, v233\nM 100,160,192\nR 182,191,53248\nv_mul_f32 v234, v36, v36\nv_mul_f32 v235, v37, v37\nM 100,164,208\n"
"G 4\nv_mul_f32 v236, v38, v38\nv_mul_f32 v237, v39, v39\nv_fma_f32 v234, v234, v215, v214\nM 104,168,192\nR 160,184,0\n"
"v_fma_f32 v235, v235, v215, v214\nv_fma_f32 v236, v236, v215, v214\nM 104,172,208\nbuffer_load_dwordx4 v222, s[12:15], s63 offen lds\nv_fma_f32 v237, v237, v215, v214\nM 120,168,196\n"
"R 162,184,4096\nv_mul_f32 v234, v36, v234\nv_mul_f32 v235, v37, v235\nM 120,172,212\nv_exp_f32 v234, v234\nv_mul_f32 v236, v38, v236\n"
"M 136,168,200\nR 164,184,8192\nv_mul_f32 v237, v39, v237\nv_exp_f32 v235, v235\nM 136,172,216\nv_exp_f32 v236, v236\n"
"v_exp_f32 v237, v237\nM 152,168,204\nR 166,184,12288\nv_fma_f32 v234, v234, v216, v216\nv_fma_f32 v235, v235, v216, v216\nM 152,172,220\n"
"G 4\nv_rcp_f32 v234, v234\nv_fma_f32 v236, v236, v216, v216\nM 156,176,204\nR 168,185,0\nv_fma_f32 v237, v237, v216, v216\n"
"v_rcp_f32 v235, v235\nM 156,180,220\nbuffer_load_dwordx4 v223, s[12:15], s63 offen offset:1024 lds\nv_rcp_f32 v236, v236\nM 140,176,200\nR 170,185,4096\n"
"v_mul_f32 v234, v36, v234\nv_rcp_f32 v237, v237\nM 140,180,216\nv_mul_f32 v235, v37, v235\nv_mul_f32 v236, v38, v236\nv_mul_f32 v237, v39, v237\n"
"M 124,176,196\nR 172,185,8192\nv_cvt_pk_bf16_f32 v34, v234, v235\nv_cvt_pk_bf16_f32 v35, v236, v237\nM 124,180,212\nbuffer_store_dwordx4 v[32:35], v252, s[20:23], s84 offen offset:0 sc1\n"
"v_mul_f32 v230, v40, v40\nM 108,176,192\nR 174,185,12288\ns_mov_b32 s60, 157696\nv_bfe_u32 v246, v252, 4, 4\nv_lshl_add_u32 v246, v246, 3, s60\n"
"v_mul_f32 v231, v41, v41\nM 108,180,208\nds_read_b64 v[238:239], v246 offset:0\nG 5\nv_mul_f32 v232, v42, v42\nM 96,160,224\n"
"R 176,186,0\nds_read_b64 v[240:241], v246 offset:128\nv_mul_f32 v233, v43, v43\nM 96,164,240\nds_read_b64 v[242:243], v246 offset:256\ns_add_u32 m0, s50, 65536\n"
"v_fma_f32 v230, v230, v215, v214\nM 112,160,228\nR 178,186,4096\nds_read_b64 v[244:245], v246 offset:384\nv_fma_f32 v231, v231, v215, v214\nM 112,164,244\n"
"ds_read_b64 v[246:247], v246 offset:0\ns_add_u32 s63, s55, 2752512\nv_fma_f32 v232, v232, v215, v214\nM 128,160,232\nR 180,186,8192\nv_fma_f32 v233, v233, v215, v214\n"
"v_mul_f32 v230, v40, v230\nM 128,164,248\nbuffer_load_dwordx4 v222, s[12:15], s63 offen lds\nv_mul_f32 v231, v41, v231\nM 144,160,236\nR 182,186,12288\n"
"v_mul_f32 v232, v42, v232\nv_mul_f32 v233, v43, v233\nM 144,164,252\nG 9\nv_exp_f32 v230, v230\nv_exp_f32 v231, v231\n"
"M 148,168,236\nR 160,187,0\nv_fma_f32 v230, v230, v216, v216\nv_exp_f32 v232, v232\nM 148,172,252\nv_exp_f32 v233, v233\n"
"v_fma_f32 v231, v231, v216, v216\nM 132,168,232\nR 162,187,4096\nv_fma_f32 v232, v232, v216, v216\nv_fma_f32 v233, v233, v216, v216\nM 132,172,248\n"
"buffer_load_dwordx4 v223, s[12:15], s63 offen offset:1024 lds\nv_rcp_f32 v230, v230\nM 116,168,228\nR 164,187,8192\nG 9\nv_mfma_f32_16x16x32_bf16 v[96:99], v[238:241], v[208:211], v[96:99]\n"
"s_add_u32 s63, s55, 3211264\nv_rcp_f32 v231, v231\nM 116,172,244\ns_add_u32 m0, s50, 73728\nv_rcp_f32 v232, v232\nv_rcp_f32 v233, v233\n"
"M 100,168,224\nR 166,187,12288\nG 9\nv_mfma_f32_16x16x32_bf16 v[100:103], v[240:243], v[208:211], v[100:103]\nv_mul_f32 v230, v40, v230\nv_mul_f32 v231, v41, v231\n"
"M 100,172,240\nG 4\nv_mul_f32 v232, v42, v232\nv_mul_f32 v233, v43, v233\nv_cvt_pk_bf16_f32 v40, v230, v231\nM 104,176,224\n"
"R 168,184,16384\nv_mfma_f32_16x16x32_bf16 v[104:107], v[242:245], v[208:211], v[104:107]\nv_cvt_pk_bf16_f32 v41, v232, v233\nv_mul_f32 v234, v44, v44\nM 104,180,240\nbuffer_load_dwordx4 v222, s[12:15], s63 offen lds\n"
"v_mul_f32 v235, v45, v45\nM 120,176,228\nR 170,184,20480\nv_mfma_f32_16x16x32_bf16 v[108:111], v[244:247], v[208:211], v[108:111]\nv_mul_f32 v236, v46, v46\nv_mul_f32 v237, v47, v47\n"
"M 120,180,244\nv_fma_f32 v234, v234, v215, v214\nv_fma_f32 v235, v235, v215, v214\nv_fma_f32 v236, v236, v215, v214\nM 136,176,232\nR 172,186,16384\n"
"v_mfma_f32_16x16x32_bf16 v[112:115], v[244:247], v[210:213], v[112:115]\nv_fma_f32 v237, v237, v215, v214\nv_mul_f32 v234, v44, v234\nM 136,180,248\nv_exp_f32 v234, v234\nv_mul_f32 v235, v45, v235\n"
"M 152,176,236\nR 174,186,20480\nv_mfma_f32_16x16x32_bf16 v[116:119], v[238:241], v[210:213], v[116:119]\nv_mul_f32 v236, v46, v236\nv_mul_f32 v237, v47, v237\nM 152,180,252\n"
"G 4\nv_exp_f32 v235, v235\nv_exp_f32 v236, v236\nM 156,160,236\nR 176,185,16384\nv_mfma_f32_16x16x32_bf16 v[120:123], v[240:243], v[210:213], v[120:123]\n"
"v_fma_f32 v234, v234, v216, v216\nv_exp_f32 v237, v237\nM 156,164,252\nbuffer_load_dwordx4 v223, s[12:15], s63 offen offset:1024 lds\nv_fma_f32 v235, v235, v216, v216\nM 140,160,232\n"
"R 178,185,20480\nv_mfma_f32_16x16x32_bf16 v[124:127], v[242:245], v[210:213], v[124:127]\nv_fma_f32 v236, v236, v216, v216\nv_fma_f32 v237, v237, v216, v216\nM 140,164,248\nv_rcp_f32 v234, v234\n"
"v_rcp_f32 v235, v235\nM 124,160,228\nR 180,187,16384\nv_mul_f32 v234, v44, v234\nv_rcp_f32 v236, v236\nM 124,164,244\n"
"v_rcp_f32 v237, v237\nv_mul_f32 v235, v45, v235\nM 108,160,224\nR 182,187,20480\nv_mul_f32 v236, v46, v236\nv_mul_f32 v237, v47, v237\n"
"M 108,164,240\ns_waitcnt vmcnt(11)\ns_barrier\nG 4\nv_cvt_pk_bf16_f32 v42, v234, v235\nv_mfma_f32_16x16x32_bf16 v[0:3], v[168:171], a[0:3], 0\n"
"R 160,184,24576\nv_cvt_pk_bf16_f32 v43, v236, v237\nv_mul_f32 v230, v48, v48\nM 0,172,16\ns_mov_b32 m0, s51\nv_mul_f32 v231, v49, v49\n"
"v_mul_f32 v232, v50, v50\nv_mul_f32 v233, v51, v51\nv_mfma_f32_16x16x32_bf16 v[16:19], v[168:171], a[4:7], 0\nR 162,184,28672\nv_fma_f32 v230, v230, v218, v217\nv_fma_f32 v231, v231, v218, v217\n"
"M 16,172,20\nbuffer_store_dwordx4 v[40:43], v252, s[20:23], s84 offen offset:1024 sc1\nv_fma_f32 v232, v232, v218, v217\nv_mfma_f32_16x16x32_bf16 v[8:11], v[168:171], a[8:11], 0\nR 164,186,24576\nv_fma_f32 v233, v233, v218, v217\n"
"v_mul_f32 v230, v48, v230\nM 8,172,24\nds_write_addtid_b32 v128 offset:0\nv_exp_f32 v230, v230\nv_mul_f32 v231, v49, v231\nv_mfma_f32_16x16x32_bf16 v[24:27], v[168:171], a[12:15], 0\n"
"R 166,186,28672\nds_write_addtid_b32 v129 offset:256\nv_mul_f32 v232, v50, v232\nv_mul_f32 v233, v51, v233\nM 24,172,28\nds_write_addtid_b32 v130 offset:512\n"
"G 7\nv_exp_f32 v231, v231\nv_exp_f32 v232, v232\nv_mfma_f32_16x16x32_bf16 v[28:31], v[176:179], a[12:15], 0\nR 168,185,24576\nds_write_addtid_b32 v131 offset:768\n"
"v_fma_f32 v230, v230, v219, v219\nv_exp_f32 v233, v233\nM 28,180,28\nds_write_addtid_b32 v132 offset:1024\nv_rcp_f32 v230, v230\nv_fma_f32 v231, v231, v219, v219\n"
"v_mfma_f32_16x16x32_bf16 v[12:15], v[176:179], a[8:11], 0\nR 170,185,28672\nds_write_addtid_b32 v133 offset:1280\nv_fma_f32 v232, v232, v219, v219\nv_fma_f32 v233, v233, v219, v219\nM 12,180,24\n"
"ds_write_addtid_b32 v134 offset:1536\nv_rcp_f32 v231, v231\nv_rcp_f32 v232, v232\nv_mfma_f32_16x16x32_bf16 v[20:23], v[176:179], a[4:7], 0\nR 172,187,24576\nds_write_addtid_b32 v135 offset:1792\n"
"v_mul_f32 v230, v48, v230\nv_rcp_f32 v233, v233\nM 20,180,20\nds_write_addtid_b32 v136 offset:2048\nv_mul_f32 v231, v49, v231\nv_mul_f32 v232, v50, v232\n"
"v_mul_f32 v233, v51, v233\nv_mfma_f32_16x16x32_bf16 v[4:7], v[176:179], a[0:3], 0\nR 174,187,28672\nds_write_addtid_b32 v137 offset:2304\nv_cvt_pk_bf16_f32 v48, v230, v231\nv_cvt_pk_bf16_f32 v49, v232, v233\n"
"M 4,180,16\nds_write_addtid_b32 v138 offset:2560\nG 14\nv_mul_f32 v234, v52, v52\nv_mul_f32 v235, v53, v53\nv_mul_f32 v236, v54, v54\n"
"M 0,160,32\nR 176,184,32768\nds_write_addtid_b32 v139 offset:2816\nv_mul_f32 v237, v55, v55\nv_fma_f32 v234, v234, v218, v217\nM 0,164,48\n"
"ds_write_addtid_b32 v140 offset:3072\nv_fma_f32 v235, v235, v218, v217\nv_fma_f32 v236, v236, v218, v217\nv_fma_f32 v237, v237, v218, v217\nM 16,160,36\nR 178,184,36864\n"
"ds_write_addtid_b32 v141 offset:3328\nv_mul_f32 v234, v52, v234\nv_mul_f32 v235, v53, v235\nM 16,164,52\nds_write_addtid_b32 v142 offset:3584\nv_exp_f32 v234, v234\n"
"v_mul_f32 v236, v54, v236\nM 8,160,40\nR 180,186,32768\nds_write_addtid_b32 v143 offset:3840\nv_mul_f32 v237, v55, v237\nv_exp_f32 v235, v235\n"
"M 8,164,56\nds_write_addtid_b32 v144 offset:4096\nv_exp_f32 v236, v236\nv_exp_f32 v237, v237\nM 24,160,44\nR 182,186,36864\n"
"ds_write_addtid_b32 v145 offset:4352\nv_fma_f32 v234, v234, v219, v219\nv_fma_f32 v235, v235, v219, v219\nM 24,164,60\nds_write_addtid_b32 v146 offset:4608\nG 14\n"
"v_rcp_f32 v234, v234\nv_fma_f32 v236, v236, v219, v219\nM 28,168,44\nR 160,185,32768\nds_write_addtid_b32 v147 offset:4864\nv_fma_f32 v237, v237, v219, v219\n"
"v_rcp_f32 v235, v235\nM 28,172,60\nds_write_addtid_b32 v148 offset:5120\nds_write_addtid_b32 v149 offset:5376\nds_write_addtid_b32 v150 offset:5632\nds_write_addtid_b32 v151 offset:5888\n"
"ds_write_addtid_b32 v152 offset:6144\nds_write_addtid_b32 v153 offset:6400\nds_write_addtid_b32 v154 offset:6656\nds_write_addtid_b32 v155 offset:6912\nds_write_addtid_b32 v156 offset:7168\nds_write_addtid_b32 v157 offset:7424\n"
"ds_write_addtid_b32 v158 offset:7680\nds_write_addtid_b32 v159 offset:7936\nv_rcp_f32 v236, v236\nv_rcp_f32 v237, v237\nM 12,168,40\nR 162,185,36864\n"
"s_add_u32 m0, s50, 81920\ns_add_u32 s63, s54, 256\nv_mul_f32 v234, v52, v234\nM 12,172,56\nbuffer_load_dwordx4 v220, s[12:15], s63 offen lds\nv_mul_f32 v235, v53, v235\n"
"M 20,168,36\nR 164,187,32768\ns_waitcnt vmcnt(4) lgkmcnt(2)\ns_barrier\nv_mul_f32 v236, v54, v236\nM 20,172,52\n"
"ds_read2st64_b32 v[128:129], v225 offset0:0 offset1:1\nv_mul_f32 v237, v55, v237\nv_cvt_pk_bf16_f32 v50, v234, v235\nv_cvt_pk_bf16_f32 v51, v236, v237\nM 4,168,32\nR 166,187,36864\n"
"ds_read2st64_b32 v[130:131], v225 offset0:2 offset1:3\nv_mul_f32 v230, v56, v56\nv_mul_f32 v231, v57, v57\nM 4,172,48\nds_read2st64_b32 v[132:133], v225 offset0:4 offset1:5\nv_mul_f32 v232, v58, v58\n"
"v_mul_f32 v233, v59, v59\nv_fma_f32 v230, v230, v218, v217\nM 0,176,64\nR 168,184,40960\nds_read2st64_b32 v[134:135], v225 offset0:6 offset1:7\nv_fma_f32 v231, v231, v218, v217\n"
"v_fma_f32 v232, v232, v218, v217\nM 0,180,80\nbuffer_load_dwordx4 v221, s[12:15], s63 offen offset:1024 lds\nds_read2st64_b32 v[136:137], v225 offset0:8 offset1:9\nv_fma_f32 v233, v233, v218, v217\nM 16,176,68\n"
"R 170,184,45056\ns_add_u32 m0, s50, 90112\ns_add_u32 s63, s54, 229632\nds_read2st64_b32 v[138:139], v225 offset0:10 offset1:11\nv_mul_f32 v230, v56, v230\nM 16,180,84\n"
"buffer_load_dwordx4 v220, s[12:15], s63 offen lds\nds_read2st64_b32 v[140:141], v225 offset0:12 offset1:13\nv_mul_f32 v231, v57, v231\nM 8,176,72\nR 172,186,40960\nds_read2st64_b32 v[142:143], v225 offset0:14 offset1:15\n"
"v_mul_f32 v232, v58, v232\nv_mul_f32 v233, v59, v233\nM 8,180,88\nbuffer_store_dwordx4 v[48:51], v252, s[20:23], s85 offen offset:0 sc1\nds_read2st64_b32 v[144:145], v225 offset0:16 offset1:17\nv_exp_f32 v230, v230\n"
"M 24,176,76\nR 174,186,45056\nds_read2st64_b32 v[146:147], v225 offset0:18 offset1:19\nv_fma_f32 v230, v230, v219, v219\nv_exp_f32 v231, v231\nM 24,180,92\n"
"ds_read2st64_b32 v[148:149], v225 offset0:20 offset1:21\nG 14\nv_exp_f32 v232, v232\nv_exp_f32 v233, v233\nM 28,160,76\nR 176,185,40960\n"
"ds_read2st64_b32 v[150:151], v225 offset0:22 offset1:23\nv_fma_f32 v231, v231, v219, v219\nv_fma_f32 v232, v232, v219, v219\nM 28,164,92\nbuffer_load_dwordx4 v221, s[12:15], s63 offen offset:1024 lds\nds_read2st64_b32 v[152:153], v225 offset0:24 offset1:25\n"
"v_fma_f32 v233, v233, v219, v219\nM 12,160,72\nR 178,185,45056\ns_add_u32 s63, s54, 459008\nds_read2st64_b32 v[154:155], v225 offset0:26 offset1:27\nv_rcp_f32 v230, v230\n"
"M 12,164,88\ns_add_u32 m0, s50, 98304\nds_read2st64_b32 v[156:157], v225 offset0:28 offset1:29\nv_rcp_f32 v231, v231\nv_rcp_f32 v232, v232\nM 20,160,68\n"
"R 180,187,40960\nds_read2st64_b32 v[158:159], v225 offset0:30 offset1:31\nv_mul_f32 v230, v56, v230\nv_rcp_f32 v233, v233\nM 20,164,84\nbuffer_load_dwordx4 v220, s[12:15], s63 offen lds\n"
"v_add_f32 v96, v96, v128\nv_mul_f32 v231, v57, v231\nM 4,160,64\nR 182,187,45056\nv_mul_f32 v232, v58, v232\nv_add_f32 v97, v97, v129\n"
"v_mul_f32 v233, v59, v233\nM 4,164,80\nG 11\nv_cvt_pk_bf16_f32 v56, v230, v231\nv_cvt_pk_bf16_f32 v57, v232, v233\nv_add_f32 v98, v98, v130\n"
"v_mul_f32 v234, v60, v60\nM 0,168,96\nR 160,184,49152\nv_mul_f32 v235, v61, v61\nv_add_f32 v99, v99, v131\nv_mul_f32 v236, v62, v62\n"
"M 0,172,112\nbuffer_load_dwordx4 v221, s[12:15], s63 offen offset:1024 lds\nv_add_f32 v100, v100, v132\nv_mul_f32 v237, v63, v63\nM 16,168,100\nR 162,184,53248\n"
"s_add_u32 s63, s54, 688384\nv_add_f32 v101, v101, v133\nv_fma_f32 v234, v234, v218, v217\nM 16,172,116\ns_add_u32 m0, s50, 106496\nv_fma_f32 v235, v235, v218, v217\n"
"v_fma_f32 v236, v236, v218, v217\nv_add_f32 v102, v102, v134\nv_fma_f32 v237, v237, v218, v217\nM 8,168,104\nR 164,186,49152\nv_mul_f32 v234, v60, v234\n"
"v_add_f32 v103, v103, v135\nv_mul_f32 v235, v61, v235\nM 8,172,120\nbuffer_load_dwordx4 v220, s[12:15], s63 offen lds\nv_add_f32 v104, v104, v136\nv_mul_f32 v236, v62, v236\n"
"M 24,168,108\nR 166,186,53248\nv_mul_f32 v237, v63, v237\nv_add_f32 v105, v105, v137\nv_exp_f32 v234, v234\nM 24,172,124\n"
"G 4\nv_add_f32 v106, v106, v138\nv_add_f32 v107, v107, v139\nv_add_f32 v108, v108, v140\nv_exp_f32 v235, v235\nM 28,176,108\n"
"R 168,185,49152\nv_add_f32 v109, v109, v141\nv_add_f32 v110, v110, v142\nv_exp_f32 v236, v236\nM 28,180,124\nv_add_f32 v111, v111, v143\n"
"v_add_f32 v112, v112, v144\nv_add_f32 v113, v113, v145\nv_exp_f32 v237, v237\nM 12,176,104\nR 170,185,53248\nv_add_f32 v114, v114, v146\n"
"v_add_f32 v115, v115, v147\nv_fma_f32 v234, v234, v219, v219\nM 12,180,120\nbuffer_load_dwordx4 v221, s[12:15], s63 offen offset:1024 lds\nv_add_f32 v116, v116, v148\nv_fma_f32 v235, v235, v219, v219\n"
"M 20,176,100\nR 172,187,49152\ns_add_u32 s63, s54, 917760\nv_add_f32 v117, v117, v149\nM 20,180,116\ns_add_u32 m0, s50, 114688\n"
"v_add_f32 v118, v118, v150\nv_add_f32 v119, v119, v151\nv_add_f32 v120, v120, v152\nv_add_f32 v121, v121, v153\nv_add_f32 v122, v122, v154\nv_add_f32 v123, v123, v155\n"
"v_add_f32 v124, v124, v156\nv_add_f32 v125, v125, v157\nv_add_f32 v126, v126, v158\nv_add_f32 v127, v127, v159\nM 4,176,96\nR 174,187,53248\n"
"v_fma_f32 v236, v236, v219, v219\nM 4,180,112\nG 4\nv_rcp_f32 v234, v234\nM 0,160,128\nR 176,184,57344\n"
"v_fma_f32 v237, v237, v219, v219\nM 0,164,144\nbuffer_load_dwordx4 v220, s[12:15], s63 offen lds\nM 16,160,132\nR 178,184,61440\nv_mul_f32 v234, v60, v234\n"
"M 16,164,148\nv_rcp_f32 v235, v235\nM 8,160,136\nR 180,186,57344\nLq1036:\ns_lshr_b32 s60, s45, 3\n"
"s_lshl2_add_u32 s60, s60, s40\ns_lshl_b32 s60, s60, 8\nv_lshl_add_u32 v224, v252, 3, s60\nv_add_u32 v224, Lq1036+4096-LqPC, v224\nv_min_u32 v224, LqE-LqPC-4, v224\ns_mov_b64 exec, 3\n"
"global_load_dword v224, v224, s[98:99]\ns_mov_b64 exec, -1\nM 8,164,152\nv_rcp_f32 v236, v236\nM 24,160,140\nR 182,186,61440\n"
"v_mul_f32 v235, v61, v235\nM 24,164,156\nG 4\nv_rcp_f32 v237, v237\nM 28,168,140\nR 160,185,57344\n"
"v_mul_f32 v236, v62, v236\nM 28,172,156\nbuffer_load_dwordx4 v221, s[12:15], s63 offen offset:1024 lds\nM 12,168,136\nR 162,185,61440\ns_add_u32 m0, s50, 0\n"
"s_add_u32 s63, s54, 1147136\nM 12,172,152\nbuffer_load_dwordx4 v220, s[12:15], s63 offen lds\nM 20,168,132\nR 164,187,57344\nv_mul_f32 v237, v63, v237\n"
"M 20,172,148\nv_cvt_pk_bf16_f32 v58, v234, v235\nv_cvt_pk_bf16_f32 v59, v236, v237\nM 4,168,128\nR 166,187,61440\nM 4,172,144\n"
"G 4\nM 0,176,160\nR 168,188,0\nM 0,180,176\nbuffer_load_dwordx4 v221, s[12:15], s63 offen offset:1024 lds\nLpg27:\n"
"s_cbranch_execz Lpg31\nM 16,176,164\nR 170,188,4096\ns_add_u32 m0, s50, 8192\ns_add_u32 s63, s54, 1376512\nM 16,180,180\n"
"buffer_load_dwordx4 v220, s[12:15], s63 offen lds\nM 8,176,168\nR 172,190,0\nM 8,180,184\nbuffer_store_dwordx4 v[56:59], v252, s[20:23], s85 offen offset:1024 sc1\nM 24,176,172\n"
"R 174,190,4096\nM 24,180,188\nG 4\nM 28,160,172\nR 176,189,0\ns_waitcnt vmcnt(11)\n"
"s_barrier\nv_mul_f32 v230, v96, v96\nM 28,164,188\nbuffer_load_dwordx4 v221, s[12:15], s63 offen offset:1024 lds\nv_mul_f32 v231, v97, v97\nM 12,160,168\n"
"R 178,189,4096\ns_add_u32 m0, s50, 16384\ns_add_u32 s63, s54, 1605888\nv_mul_f32 v232, v98, v98\nM 12,164,184\nbuffer_load_dwordx4 v220, s[12:15], s63 offen lds\n"
"v_mul_f32 v233, v99, v99\nM 20,160,164\nR 180,191,0\nv_fma_f32 v230, v230, v215, v214\nv_fma_f32 v231, v231, v215, v214\nM 20,164,180\n"
"s_add_u32 s84, s73, 37750784\ns_add_u32 s85, s73, 37865472\nv_fma_f32 v232, v232, v215, v214\nv_fma_f32 v233, v233, v215, v214\nM 4,160,160\nR 182,191,4096\n"
"s_mov_b32 s60, 158720\nv_bfe_u32 v242, v252, 4, 4\nv_lshl_add_u32 v242, v242, 3, s60\nv_mul_f32 v230, v96, v230\nM 4,164,176\nds_read_b64 v[238:239], v242 offset:0\n"
"G 5\nv_mul_f32 v231, v97, v231\nM 0,168,192\nR 160,188,8192\nds_read_b64 v[240:241], v242 offset:128\nv_mul_f32 v232, v98, v232\n"
"M 0,172,208\nds_read_b64 v[242:243], v242 offset:0\nv_mul_f32 v233, v99, v233\nv_exp_f32 v230, v230\nM 16,168,196\nR 162,188,12288\n"
"v_fma_f32 v230, v230, v216, v216\nv_exp_f32 v231, v231\nM 16,172,212\nv_exp_f32 v232, v232\nv_exp_f32 v233, v233\nM 8,168,200\n"
"R 164,190,8192\nv_fma_f32 v231, v231, v216, v216\nv_fma_f32 v232, v232, v216, v216\nM 8,172,216\nv_rcp_f32 v230, v230\nv_fma_f32 v233, v233, v216, v216\n"
"M 24,168,204\nR 166,190,12288\nv_mul_f32 v230, v96, v230\nv_rcp_f32 v231, v231\nM 24,172,220\nG 7\n"
"v_rcp_f32 v232, v232\nv_rcp_f32 v233, v233\nM 28,176,204\nR 168,189,8192\nv_mul_f32 v231, v97, v231\nv_mul_f32 v232, v98, v232\n"
"M 28,180,220\nv_mul_f32 v233, v99, v233\nv_cvt_pk_bf16_f32 v96, v230, v231\nv_cvt_pk_bf16_f32 v97, v232, v233\nM 12,176,200\nR 170,189,12288\n"
"v_mul_f32 v234, v100, v100\nv_mul_f32 v235, v101, v101\nM 12,180,216\nv_mul_f32 v236, v102, v102\nv_mul_f32 v237, v103, v103\nv_fma_f32 v234, v234, v215, v214\n"
"M 20,176,196\nR 172,191,8192\nG 7\nv_mfma_f32_16x16x32_bf16 v[0:3], v[238:241], v[208:211], v[0:3]\nv_fma_f32 v235, v235, v215, v214\nv_fma_f32 v236, v236, v215, v214\n"
"M 20,180,212\nv_fma_f32 v237, v237, v215, v214\nv_mul_f32 v234, v100, v234\nv_mul_f32 v235, v101, v235\nM 4,176,192\nR 174,191,12288\n"
"G 7\nv_mfma_f32_16x16x32_bf16 v[4:7], v[240:243], v[208:211], v[4:7]\nv_mul_f32 v236, v102, v236\nv_mul_f32 v237, v103, v237\nM 4,180,208\nG 4\n"
"v_exp_f32 v234, v234\nv_exp_f32 v235, v235\nM 0,160,224\nR 176,188,16384\nv_mfma_f32_16x16x32_bf16 v[16:19], v[240:243], v[210:213], v[16:19]\nv_fma_f32 v234, v234, v216, v216\n"
"v_exp_f32 v236, v236\nM 0,164,240\nv_exp_f32 v237, v237\nv_fma_f32 v235, v235, v216, v216\nM 16,160,228\nR 178,188,20480\n"
"v_mfma_f32_16x16x32_bf16 v[20:23], v[238:241], v[210:213], v[20:23]\nv_fma_f32 v236, v236, v216, v216\nv_fma_f32 v237, v237, v216, v216\nM 16,164,244\nv_rcp_f32 v234, v234\nv_rcp_f32 v235, v235\n"
"M 8,160,232\nR 180,188,24576\nv_mul_f32 v234, v100, v234\nv_rcp_f32 v236, v236\nM 8,164,248\nv_rcp_f32 v237, v237\n"
"v_mul_f32 v235, v101, v235\nM 24,160,236\nR 182,188,28672\nv_mul_f32 v236, v102, v236\nv_mul_f32 v237, v103, v237\nM 24,164,252\n"
"G 4\nv_cvt_pk_bf16_f32 v98, v234, v235\nv_cvt_pk_bf16_f32 v99, v236, v237\nv_mul_f32 v230, v104, v104\nM 28,168,236\nR 160,189,16384\n"
"v_mul_f32 v231, v105, v105\nv_mul_f32 v232, v106, v106\nM 28,172,252\nv_mul_f32 v233, v107, v107\nv_fma_f32 v230, v230, v215, v214\nv_fma_f32 v231, v231, v215, v214\n"
"M 12,168,232\nR 162,189,20480\nv_fma_f32 v232, v232, v215, v214\nv_fma_f32 v233, v233, v215, v214\nM 12,172,248\nv_mul_f32 v230, v104, v230\n"
"v_mul_f32 v231, v105, v231\nv_mul_f32 v232, v106, v232\nM 20,168,228\nR 164,189,24576\nv_mul_f32 v233, v107, v233\nv_exp_f32 v230, v230\n"
"M 20,172,244\nv_exp_f32 v231, v231\nv_exp_f32 v232, v232\nM 4,168,224\nR 166,189,28672\nv_fma_f32 v230, v230, v216, v216\n"
"v_exp_f32 v233, v233\nM 4,172,240\ns_waitcnt vmcnt(11)\ns_barrier\nG 4\nv_fma_f32 v231, v231, v216, v216\n"
"v_mfma_f32_16x16x32_bf16 v[32:35], v[176:179], a[0:3], 0\nR 168,190,16384\nv_fma_f32 v232, v232, v216, v216\nv_fma_f32 v233, v233, v216, v216\nM 32,180,16\ns_mov_b32 m0, s51\n"
"v_rcp_f32 v230, v230\nv_rcp_f32 v231, v231\nv_mfma_f32_16x16x32_bf16 v[48:51], v[176:179], a[4:7], 0\nR 170,190,20480\nds_write_addtid_b32 v8 offset:0\nv_mul_f32 v230, v104, v230\n"
"v_rcp_f32 v232, v232\nM 48,180,20\nds_write_addtid_b32 v9 offset:256\nv_rcp_f32 v233, v233\nv_mul_f32 v231, v105, v231\nv_mfma_f32_16x16x32_bf16 v[64:67], v[176:179], a[8:11], 0\n"
"R 172,190,24576\nds_write_addtid_b32 v10 offset:512\nv_mov_b32 v226, s46\nv_mul_f32 v232, v106, v232\nM 64,180,24\nds_write_addtid_b32 v11 offset:768\n"
"v_mov_b32 v227, s47\nv_mul_f32 v233, v107, v233\nv_cvt_pk_bf16_f32 v104, v230, v231\nv_mfma_f32_16x16x32_bf16 v[80:83], v[176:179], a[12:15], 0\nR 174,190,28672\ns_add_u32 s61, s81, 845824\n"
"s_lshl_b32 s62, s44, 5\ns_add_u32 s61, s61, s62\ns_waitcnt vmcnt(0)\ns_mov_b64 exec, 1\nbuffer_atomic_swap_x2 v[226:227], off, s[20:23], s61 sc1\ns_mov_b64 exec, -1\n"
"ds_write_addtid_b32 v12 offset:1024\nv_cvt_pk_bf16_f32 v105, v232, v233\nM 80,180,28\nds_write_addtid_b32 v13 offset:1280\nG 10\nv_mul_f32 v234, v108, v108\n"
"v_mul_f32 v235, v109, v109\nv_mul_f32 v236, v110, v110\nv_mfma_f32_16x16x32_bf16 v[84:87], v[160:163], a[12:15], 0\nR 176,191,16384\nds_write_addtid_b32 v14 offset:1536\nv_mul_f32 v237, v111, v111\n"
"v_fma_f32 v234, v234, v215, v214\nM 84,164,28\nds_write_addtid_b32 v15 offset:1792\nv_fma_f32 v235, v235, v215, v214\nv_fma_f32 v236, v236, v215, v214\nv_fma_f32 v237, v237, v215, v214\n"
"v_mfma_f32_16x16x32_bf16 v[68:71], v[160:163], a[8:11], 0\nR 178,191,20480\nds_write_addtid_b32 v24 offset:4096\nv_mul_f32 v234, v108, v234\nv_mul_f32 v235, v109, v235\nM 68,164,24\n"
"buffer_store_dwordx4 v[96:99], v252, s[20:23], s84 offen offset:0 sc1\nds_write_addtid_b32 v25 offset:4352\nv_mul_f32 v236, v110, v236\nv_mfma_f32_16x16x32_bf16 v[52:55], v[160:163], a[4:7], 0\nR 180,191,24576\nds_write_addtid_b32 v26 offset:4608\n"
"v_mul_f32 v237, v111, v237\nv_exp_f32 v234, v234\nM 52,164,20\nds_write_addtid_b32 v27 offset:4864\nds_write_addtid_b32 v28 offset:5120\nds_write_addtid_b32 v29 offset:5376\n"
"ds_write_addtid_b32 v30 offset:5632\nds_write_addtid_b32 v31 offset:5888\nv_exp_f32 v235, v235\nv_exp_f32 v236, v236\nv_mfma_f32_16x16x32_bf16 v[36:39], v[160:163], a[0:3], 0\nR 182,191,28672\n"
"s_add_u32 m0, s50, 16384\nv_exp_f32 v237, v237\nM 36,164,16\nG 15\nv_fma_f32 v234, v234, v216, v216\nv_fma_f32 v235, v235, v216, v216\n"
"v_fma_f32 v236, v236, v216, v216\nv_mfma_f32_16x16x32_bf16 v[40:43], v[168:171], a[0:3], 0\nR 160,188,32768\nG 2\ns_barrier\nv_fma_f32 v237, v237, v216, v216\n"
"M 40,172,16\nbuffer_load_dwordx4 v221, s[12:15], s63 offen offset:1024 lds\nds_read2st64_b32 v[8:9], v225 offset0:0 offset1:1\nv_rcp_f32 v234, v234\nv_mfma_f32_16x16x32_bf16 v[56:59], v[168:171], a[4:7], 0\nR 162,188,36864\n"
"s_add_u32 m0, s50, 24576\ns_add_u32 s63, s54, 1835264\nds_read2st64_b32 v[10:11], v225 offset0:2 offset1:3\nv_rcp_f32 v235, v235\nM 56,172,20\nbuffer_load_dwordx4 v220, s[12:15], s63 offen lds\n"
"ds_read2st64_b32 v[12:13], v225 offset0:4 offset1:5\nv_rcp_f32 v236, v236\nv_mfma_f32_16x16x32_bf16 v[72:75], v[168:171], a[8:11], 0\nR 164,188,40960\nds_read2st64_b32 v[14:15], v225 offset0:6 offset1:7\nv_mul_f32 v234, v108, v234\n"
"v_rcp_f32 v237, v237\nM 72,172,24\nds_read2st64_b32 v[24:25], v225 offset0:16 offset1:17\nv_mul_f32 v235, v109, v235\nv_mul_f32 v236, v110, v236\nv_mul_f32 v237, v111, v237\n"
"v_mfma_f32_16x16x32_bf16 v[88:91], v[168:171], a[12:15], 0\nR 166,188,45056\nds_read2st64_b32 v[26:27], v225 offset0:18 offset1:19\nv_cvt_pk_bf16_f32 v106, v234, v235\nv_cvt_pk_bf16_f32 v107, v236, v237\nM 88,172,28\n"
"ds_read2st64_b32 v[28:29], v225 offset0:20 offset1:21\nG 11\nv_mul_f32 v230, v112, v112\nv_mul_f32 v231, v113, v113\nv_mul_f32 v232, v114, v114\nv_mfma_f32_16x16x32_bf16 v[92:95], v[176:179], a[12:15], 0\n"
"R 168,189,32768\nds_read2st64_b32 v[30:31], v225 offset0:22 offset1:23\nv_mul_f32 v233, v115, v115\nv_fma_f32 v230, v230, v218, v217\nM 92,180,28\nbuffer_load_dwordx4 v221, s[12:15], s63 offen offset:1024 lds\n"
"G 11\nv_add_f32 v0, v0, v8\nv_fma_f32 v231, v231, v218, v217\nv_mfma_f32_16x16x32_bf16 v[76:79], v[176:179], a[8:11], 0\nR 170,189,36864\ns_add_u32 m0, s50, 32768\n"
"s_add_u32 s63, s54, 2064640\nv_add_f32 v1, v1, v9\nv_fma_f32 v232, v232, v218, v217\nM 76,180,24\nbuffer_load_dwordx4 v220, s[12:15], s63 offen lds\nG 10\n"
"v_add_f32 v2, v2, v10\nv_fma_f32 v233, v233, v218, v217\nv_mfma_f32_16x16x32_bf16 v[60:63], v[176:179], a[4:7], 0\nR 172,189,40960\nv_mul_f32 v230, v112, v230\nv_add_f32 v3, v3, v11\n"
"v_mul_f32 v231, v113, v231\nM 60,180,20\nbuffer_store_dwordx4 v[104:107], v252, s[20:23], s84 offen offset:1024 sc1\nG 10\nv_add_f32 v4, v4, v12\nv_mul_f32 v232, v114, v232\n"
"v_mfma_f32_16x16x32_bf16 v[44:47], v[176:179], a[0:3], 0\nR 174,189,45056\nv_mul_f32 v233, v115, v233\nv_add_f32 v5, v5, v13\nv_exp_f32 v230, v230\nM 44,180,16\n"
"G 7\nv_exp_f32 v231, v231\nv_add_f32 v6, v6, v14\nv_exp_f32 v232, v232\nM 32,160,32\nR 176,190,32768\n"
"v_mov_b32 v248, 0\nv_add_f32 v7, v7, v15\nv_exp_f32 v233, v233\nM 32,164,48\nbuffer_load_dwordx4 v221, s[12:15], s63 offen offset:1024 lds\nv_mov_b32 v249, 0\n"
"v_add_f32 v16, v16, v24\nv_fma_f32 v230, v230, v219, v219\nM 48,160,36\nR 178,190,36864\ns_add_u32 s61, s82, 845824\ns_mov_b64 exec, 0xf\n"
"buffer_atomic_or_x2 v[248:249], v250, s[20:23], s61 offen sc0 sc1\ns_mov_b64 exec, -1\nv_add_f32 v17, v17, v25\nv_fma_f32 v231, v231, v219, v219\nM 48,164,52\nv_rcp_f32 v230, v230\n"
"G 8\nv_add_f32 v18, v18, v26\nv_fma_f32 v232, v232, v219, v219\nM 64,160,40\nR 180,190,40960\nv_fma_f32 v233, v233, v219, v219\n"
"v_add_f32 v19, v19, v27\nG 8\nv_add_f32 v20, v20, v28\nv_add_f32 v21, v21, v29\nG 6\nv_add_f32 v22, v22, v30\n"
"v_add_f32 v23, v23, v31\nv_rcp_f32 v231, v231\nM 64,164,56\ns_add_u32 m0, s50, 40960\nv_rcp_f32 v232, v232\nv_rcp_f32 v233, v233\n"
"M 80,160,44\nR 182,190,45056\nv_mul_f32 v230, v112, v230\nv_mul_f32 v231, v113, v231\nM 80,164,60\nG 4\n"
"v_mul_f32 v232, v114, v232\nv_mul_f32 v233, v115, v233\nv_cvt_pk_bf16_f32 v112, v230, v231\nM 84,168,44\nR 160,191,32768\nv_cvt_pk_bf16_f32 v113, v232, v233\n"
"v_mul_f32 v234, v116, v116\nM 84,172,60\ns_add_u32 s63, s54, 2294016\nv_mul_f32 v235, v117, v117\nv_mul_f32 v236, v118, v118\nM 68,168,40\n"
"R 162,191,36864\nv_mul_f32 v237, v119, v119\nM 68,172,56\nbuffer_load_dwordx4 v220, s[12:15], s63 offen lds\nM 52,168,36\nR 164,191,40960\n"
"v_fma_f32 v234, v234, v218, v217\nM 52,172,52\nv_fma_f32 v235, v235, v218, v217\nv_fma_f32 v236, v236, v218, v217\nM 36,168,32\nR 166,191,45056\n"
"v_fma_f32 v237, v237, v218, v217\nM 36,172,48\nG 4\nv_mul_f32 v234, v116, v234\nv_mul_f32 v235, v117, v235\nM 40,176,32\n"
"R 168,188,49152\nv_mul_f32 v236, v118, v236\nM 40,180,48\nbuffer_load_dwordx4 v221, s[12:15], s63 offen offset:1024 lds\nM 56,176,36\nR 170,188,53248\n"
"s_add_u32 s63, s54, 2523392\nM 56,180,52\ns_add_u32 m0, s50, 49152\nv_exp_f32 v234, v234\nM 72,176,40\nR 172,184,0\n"
"v_mul_f32 v237, v119, v237\nM 72,180,56\nbuffer_load_dwordx4 v220, s[12:15], s63 offen lds\nM 88,176,44\nR 174,184,4096\nv_fma_f32 v234, v234, v219, v219\n"
"M 88,180,60\nG 4\nv_exp_f32 v235, v235\nM 92,160,44\nR 176,189,49152\nv_fma_f32 v235, v235, v219, v219\n"
"M 92,164,60\nbuffer_load_dwordx4 v221, s[12:15], s63 offen offset:1024 lds\nM 76,160,40\nR 178,189,53248\ns_add_u32 s63, s54, 2752768\nM 76,164,56\n"
"s_add_u32 m0, s50, 57344\nv_exp_f32 v236, v236\nM 60,160,36\nR 180,185,0\nv_fma_f32 v236, v236, v219, v219\nM 60,164,52\n"
"buffer_load_dwordx4 v220, s[12:15], s63 offen lds\nM 44,160,32\nR 182,185,4096\nv_mul_f32 v230, v120, v120\nM 44,164,48\nG 4\n"
"v_exp_f32 v237, v237\nM 32,168,64\nR 160,190,49152\nv_fma_f32 v237, v237, v219, v219\nM 32,172,80\nbuffer_load_dwordx4 v221, s[12:15], s63 offen offset:1024 lds\n"
"M 48,168,68\nR 162,190,53248\ns_add_u32 s63, s54, 2982144\nM 48,172,84\ns_add_u32 m0, s50, 65536\nv_rcp_f32 v234, v234\n"
"M 64,168,72\nR 164,186,0\nv_mul_f32 v234, v116, v234\nM 64,172,88\nbuffer_load_dwordx4 v220, s[12:15], s63 offen lds\nM 80,168,76\n"
"R 166,186,4096\nv_mul_f32 v231, v121, v121\nM 80,172,92\nG 4\nv_rcp_f32 v235, v235\nM 84,176,76\n"
"R 168,191,49152\nv_mul_f32 v235, v117, v235\nM 84,180,92\nv_rcp_f32 v236, v236\nM 68,176,72\nR 170,191,53248\n"
"v_mul_f32 v236, v118, v236\nM 68,180,88\nbuffer_load_dwordx4 v221, s[12:15], s63 offen offset:1024 lds\nM 52,176,68\nR 172,187,0\nv_cvt_pk_bf16_f32 v114, v234, v235\n"
"M 52,180,84\ns_add_u32 s64, s68, 37748736\ns_add_u32 s63, s54, 3211520\nv_mul_f32 v232, v122, v122\nM 36,176,64\nR 174,187,4096\n"
"s_add_u32 m0, s50, 73728\nM 36,180,80\nG 4\nv_rcp_f32 v237, v237\nM 40,160,64\nR 176,184,8192\n"
"s_waitcnt vmcnt(9)\ns_barrier\ns_waitcnt vmcnt(8)\nv_cmp_eq_u64 s[86:87], s[46:47], v[248:249]\nM 40,164,80\ns_add_u32 s65, s68, 37863424\n"
"v_mul_f32 v237, v119, v237\nv_cvt_pk_bf16_f32 v115, v236, v237\nM 56,160,68\nR 178,184,12288\ns_and_b32 s62, s86, 0xf\ns_cmp_eq_u32 s62, 0xf\n"
"s_cbranch_scc0 Lq838\nLq839:\nM 56,164,84\nbuffer_load_dwordx4 v220, s[12:15], s63 offen lds\nM 72,160,72\nR 180,184,16384\n"
"v_mul_f32 v233, v123, v123\nM 72,164,88\nbuffer_load_dwordx4 a[0:3], v252, s[20:23], s64 offen sc1\nM 88,160,76\nR 182,184,20480\nv_fma_f32 v230, v230, v218, v217\n"
"M 88,164,92\nG 4\nv_fma_f32 v231, v231, v218, v217\nv_fma_f32 v232, v232, v218, v217\nM 92,168,76\nR 160,185,8192\n"
"v_fma_f32 v233, v233, v218, v217\nM 92,172,92\nbuffer_load_dwordx4 a[4:7], v252, s[20:23], s65 offen sc1\nM 76,168,72\nR 162,185,12288\nv_mul_f32 v230, v120, v230\n"
"M 76,172,88\ns_add_u32 s66, s69, 37748736\nv_exp_f32 v230, v230\nM 60,168,68\nR 164,185,16384\nv_mul_f32 v231, v121, v231\n"
"M 60,172,84\ns_add_u32 s67, s69, 37863424\nv_exp_f32 v231, v231\nM 44,168,64\nR 166,185,20480\nv_mul_f32 v232, v122, v232\n"
"M 44,172,80\nG 4\nv_exp_f32 v232, v232\nM 32,176,96\nR 168,186,8192\nv_mul_f32 v233, v123, v233\n"
"M 32,180,112\nbuffer_load_dwordx4 a[8:11], v252, s[20:23], s66 offen sc1\nM 48,176,100\nR 170,186,12288\nv_fma_f32 v230, v230, v219, v219\nM 48,180,116\n"
"buffer_load_dwordx4 a[12:15], v252, s[20:23], s67 offen sc1\nM 64,176,104\nR 172,186,16384\nv_fma_f32 v231, v231, v219, v219\nM 64,180,120\ns_add_u32 s64, s68, 37755904\n"
"v_exp_f32 v233, v233\nM 80,176,108\nR 174,186,20480\nv_fma_f32 v232, v232, v219, v219\nM 80,180,124\nG 4\n"
"v_rcp_f32 v230, v230\nM 84,160,108\nR 176,187,8192\nv_fma_f32 v233, v233, v219, v219\nM 84,164,124\nbuffer_load_dwordx4 a[16:19], v252, s[20:23], s64 offen sc1\n"
"M 68,160,104\nR 178,187,12288\nv_mul_f32 v230, v120, v230\nM 68,164,120\ns_add_u32 s65, s68, 37870592\nv_rcp_f32 v231, v231\n"
"M 52,160,100\nR 180,187,16384\nv_mul_f32 v231, v121, v231\nM 52,164,116\ns_add_u32 s66, s69, 37755904\nv_rcp_f32 v232, v232\n"
"M 36,160,96\nR 182,187,20480\nv_mul_f32 v232, v122, v232\nM 36,164,112\nG 4\nv_rcp_f32 v233, v233\n"
"M 40,168,96\nR 160,184,24576\nv_mul_f32 v233, v123, v233\nM 40,172,112\nbuffer_load_dwordx4 a[20:23], v252, s[20:23], s65 offen sc1\nM 56,168,100\n"
"R 162,184,28672\nv_cvt_pk_bf16_f32 v120, v230, v231\nM 56,172,116\nbuffer_load_dwordx4 a[24:27], v252, s[20:23], s66 offen sc1\nM 72,168,104\nR 164,184,32768\n"
"Lpg28:\ns_cbranch_execz LretO1n\nv_cvt_pk_bf16_f32 v121, v232, v233\nM 72,172,120\ns_add_u32 s67, s69, 37870592\nv_mul_f32 v234, v124, v124\n"
"v_mul_f32 v235, v125, v125\nM 88,168,108\nR 166,184,36864\nv_mul_f32 v236, v126, v126\nM 88,172,124\nG 4\n"
"v_mul_f32 v237, v127, v127\nv_fma_f32 v234, v234, v218, v217\nM 92,176,108\nR 168,185,24576\nv_fma_f32 v235, v235, v218, v217\nM 92,180,124\n"
"buffer_load_dwordx4 a[28:31], v252, s[20:23], s67 offen sc1\nM 76,176,104\nR 170,185,28672\nv_fma_f32 v236, v236, v218, v217\nM 76,180,120\ns_add_u32 s64, s68, 37749760\n"
"v_fma_f32 v237, v237, v218, v217\nv_mul_f32 v234, v124, v234\nM 60,176,100\nR 172,185,32768\nv_mul_f32 v235, v125, v235\nM 60,180,116\n"
"s_add_u32 s65, s68, 37864448\nv_exp_f32 v234, v234\nM 44,176,96\nR 174,185,36864\nv_mul_f32 v236, v126, v236\nM 44,180,112\n"
"G 4\nv_exp_f32 v235, v235\nM 32,160,128\nR 176,186,24576\nv_mul_f32 v237, v127, v237\nM 32,164,144\n"
"buffer_load_dwordx4 a[32:35], v252, s[20:23], s64 offen sc1\nbuffer_load_dwordx4 v221, s[12:15], s63 offen offset:1024 lds\nM 48,160,132\nR 178,186,28672\ns_add_u32 m0, s50, 81920\nM 48,164,148\n"
"buffer_load_dwordx4 a[36:39], v252, s[20:23], s65 offen sc1\nM 64,160,136\nR 180,186,32768\ns_add_u32 s63, s54, 3440896\nM 64,164,152\ns_add_u32 s66, s69, 37749760\n"
"buffer_load_dwordx4 v220, s[12:15], s63 offen lds\nM 80,160,140\nR 182,186,36864\nv_fma_f32 v234, v234, v219, v219\nM 80,164,156\nG 4\n"
"v_exp_f32 v236, v236\nM 84,168,140\nR 160,187,24576\nv_fma_f32 v235, v235, v219, v219\nM 84,172,156\nbuffer_load_dwordx4 a[40:43], v252, s[20:23], s66 offen sc1\n"
"buffer_load_dwordx4 v221, s[12:15], s63 offen offset:1024 lds\nM 68,168,136\nR 162,187,28672\nv_fma_f32 v236, v236, v219, v219\nM 68,172,152\ns_add_u32 s67, s69, 37864448\n"
"v_exp_f32 v237, v237\nM 52,168,132\nR 164,187,32768\ns_waitcnt vmcnt(15)\ns_barrier\nM 52,172,148\n"
"buffer_load_dwordx4 a[44:47], v252, s[20:23], s67 offen sc1\ns_movk_i32 s63, 2048\nv_mad_u32_u24 v220, v228, s63, v229\nv_add_u32 v221, 916480, v220\ns_add_u32 m0, s50, 90112\ns_add_u32 s63, s56, 0\n"
"buffer_load_dwordx4 v220, s[16:19], s63 offen lds\nM 36,168,128\nR 166,187,36864\nv_fma_f32 v237, v237, v219, v219\nM 36,172,144\nG 4\n"
"v_rcp_f32 v234, v234\nM 40,176,128\nR 168,184,40960\nv_mul_f32 v234, v124, v234\nM 40,180,144\ns_add_u32 s64, s68, 37756928\n"
"buffer_load_dwordx4 v221, s[16:19], s63 offen offset:1024 lds\nM 56,176,132\nR 170,184,45056\ns_add_u32 m0, s50, 98304\nM 56,180,148\nbuffer_load_dwordx4 a[48:51], v252, s[20:23], s64 offen sc1\n"
"M 72,176,136\nR 172,184,49152\ns_add_u32 s63, s56, 524288\nM 72,180,152\ns_add_u32 s65, s68, 37871616\nbuffer_load_dwordx4 v220, s[16:19], s63 offen lds\n"
"M 88,176,140\nR 174,184,53248\nv_rcp_f32 v235, v235\nM 88,180,156\nG 4\nv_rcp_f32 v236, v236\n"
"M 92,160,140\nR 176,185,40960\nv_mul_f32 v235, v125, v235\nM 92,164,156\nbuffer_load_dwordx4 a[52:55], v252, s[20:23], s65 offen sc1\nbuffer_load_dwordx4 v221, s[16:19], s63 offen offset:1024 lds\n"
"M 76,160,136\nR 178,185,45056\ns_add_u32 m0, s50, 106496\nM 76,164,152\ns_add_u32 s66, s69, 37756928\nv_rcp_f32 v237, v237\n"
"M 60,160,132\nR 180,185,49152\ns_add_u32 s63, s56, 65536\nM 60,164,148\nbuffer_load_dwordx4 a[56:59], v252, s[20:23], s66 offen sc1\nM 44,160,128\n"
"R 182,185,53248\nv_mul_f32 v236, v126, v236\nM 44,164,144\nbuffer_load_dwordx4 v220, s[16:19], s63 offen lds\nG 4\nM 32,168,160\n"
"R 160,186,40960\nv_mul_f32 v237, v127, v237\nM 32,172,176\ns_add_u32 s67, s69, 37871616\nv_cvt_pk_bf16_f32 v122, v234, v235\nv_cvt_pk_bf16_f32 v123, v236, v237\n"
"M 48,168,164\nR 162,186,45056\nM 48,172,180\nbuffer_load_dwordx4 a[60:63], v252, s[20:23], s67 offen sc1\nbuffer_load_dwordx4 v221, s[16:19], s63 offen offset:1024 lds\nM 64,168,168\n"
"R 164,186,49152\ns_add_u32 m0, s50, 114688\ns_add_u32 s63, s56, 589824\nM 64,172,184\nbuffer_load_dwordx4 v220, s[16:19], s63 offen lds\nM 80,168,172\n"
"R 166,186,53248\nbuffer_store_dwordx4 v[112:115], v252, s[20:23], s85 offen offset:0 sc1\nM 80,172,188\nbuffer_store_dwordx4 v[120:123], v252, s[20:23], s85 offen offset:1024 sc1\nG 4\nM 84,176,172\n"
"R 168,187,40960\nM 84,180,188\nbuffer_load_dwordx4 v221, s[16:19], s63 offen offset:1024 lds\nM 68,176,168\nR 170,187,45056\ns_add_u32 s63, s57, 0\n"
"v_mul_f32 v230, v0, v0\nM 68,180,184\ns_add_u32 m0, s50, 0\ns_add_u32 s84, s73, 37754880\ns_add_u32 s85, s73, 37869568\nv_mul_f32 v231, v1, v1\n"
"M 52,176,164\nR 172,187,49152\nv_mul_f32 v232, v2, v2\nv_mul_f32 v233, v3, v3\nM 52,180,180\nbuffer_load_dwordx4 v220, s[16:19], s63 offen lds\n"
"v_fma_f32 v230, v230, v215, v214\nM 36,176,160\nR 174,187,53248\nv_fma_f32 v231, v231, v215, v214\nv_fma_f32 v232, v232, v215, v214\nM 36,180,176\n"
"G 4\nv_fma_f32 v233, v233, v215, v214\nv_mul_f32 v230, v0, v230\nv_mul_f32 v231, v1, v231\nM 40,160,160\nR 176,184,57344\n"
"v_mul_f32 v232, v2, v232\nv_mul_f32 v233, v3, v233\nM 40,164,176\nv_exp_f32 v230, v230\nv_exp_f32 v231, v231\nM 56,160,164\n"
"R 178,184,61440\nv_fma_f32 v230, v230, v216, v216\nv_exp_f32 v232, v232\nM 56,164,180\nv_exp_f32 v233, v233\nv_fma_f32 v231, v231, v216, v216\n"
"M 72,160,168\nR 180,188,0\nv_fma_f32 v232, v232, v216, v216\nv_fma_f32 v233, v233, v216, v216\nM 72,164,184\nv_rcp_f32 v230, v230\n"
"v_rcp_f32 v231, v231\nM 88,160,172\nR 182,188,4096\nv_mul_f32 v230, v0, v230\nv_rcp_f32 v232, v232\nM 88,164,188\n"
"G 4\nv_rcp_f32 v233, v233\nv_mul_f32 v231, v1, v231\nM 92,168,172\nR 160,185,57344\nv_mul_f32 v232, v2, v232\n"
"v_mul_f32 v233, v3, v233\nM 92,172,188\nv_cvt_pk_bf16_f32 v0, v230, v231\nv_cvt_pk_bf16_f32 v1, v232, v233\nv_mul_f32 v234, v4, v4\nM 76,168,168\n"
"R 162,185,61440\nv_mul_f32 v235, v5, v5\nv_mul_f32 v236, v6, v6\nM 76,172,184\nv_mul_f32 v237, v7, v7\nv_fma_f32 v234, v234, v215, v214\n"
"v_fma_f32 v235, v235, v215, v214\nM 60,168,164\nR 164,189,0\nv_fma_f32 v236, v236, v215, v214\nv_fma_f32 v237, v237, v215, v214\nM 60,172,180\n"
"v_mul_f32 v234, v4, v234\nv_mul_f32 v235, v5, v235\nv_mul_f32 v236, v6, v236\nM 44,168,160\nR 166,189,4096\nv_mul_f32 v237, v7, v237\n"
"v_exp_f32 v234, v234\nM 44,172,176\nG 4\nv_exp_f32 v235, v235\nv_exp_f32 v236, v236\nM 32,176,192\n"
"R 168,186,57344\nv_fma_f32 v234, v234, v216, v216\nv_exp_f32 v237, v237\nM 32,180,208\nv_rcp_f32 v234, v234\nv_fma_f32 v235, v235, v216, v216\n"
"M 48,176,196\nR 170,186,61440\nv_fma_f32 v236, v236, v216, v216\nv_fma_f32 v237, v237, v216, v216\nM 48,180,212\nv_rcp_f32 v235, v235\n"
"v_rcp_f32 v236, v236\nM 64,176,200\nR 172,190,0\nv_mul_f32 v234, v4, v234\nv_rcp_f32 v237, v237\nM 64,180,216\n"
"v_mul_f32 v235, v5, v235\nv_mul_f32 v236, v6, v236\nv_mul_f32 v237, v7, v237\nM 80,176,204\nR 174,190,4096\nv_cvt_pk_bf16_f32 v2, v234, v235\n"
"v_cvt_pk_bf16_f32 v3, v236, v237\nM 80,180,220\nG 4\nv_mul_f32 v230, v16, v16\nv_mul_f32 v231, v17, v17\nv_mul_f32 v232, v18, v18\n"
"M 84,160,204\nR 176,187,57344\ns_waitcnt vmcnt(9)\ns_barrier\nv_mul_f32 v233, v19, v19\nM 84,164,220\n"
"v_fma_f32 v230, v230, v218, v217\nv_fma_f32 v231, v231, v218, v217\nv_fma_f32 v232, v232, v218, v217\nM 68,160,200\nR 178,187,61440\nv_fma_f32 v233, v233, v218, v217\n"
"v_mul_f32 v230, v16, v230\nM 68,164,216\nv_exp_f32 v230, v230\nv_mul_f32 v231, v17, v231\nM 52,160,196\nR 180,191,0\n"
"v_mul_f32 v232, v18, v232\nv_mul_f32 v233, v19, v233\nM 52,164,212\nv_exp_f32 v231, v231\nv_exp_f32 v232, v232\nM 36,160,192\n"
"R 182,191,4096\nv_fma_f32 v230, v230, v219, v219\nv_exp_f32 v233, v233\nM 36,164,208\nG 4\nv_rcp_f32 v230, v230\n"
"v_fma_f32 v231, v231, v219, v219\nM 40,168,192\nR 160,188,8192\nv_fma_f32 v232, v232, v219, v219\nv_fma_f32 v233, v233, v219, v219\nM 40,172,208\n"
"v_rcp_f32 v231, v231\nv_rcp_f32 v232, v232\nM 56,168,196\nR 162,188,12288\nv_mul_f32 v230, v16, v230\nv_rcp_f32 v233, v233\n"
"M 56,172,212\nv_mul_f32 v231, v17, v231\nv_mul_f32 v232, v18, v232\nv_mul_f32 v233, v19, v233\nM 72,168,200\nR 164,188,16384\n"
"v_cvt_pk_bf16_f32 v16, v230, v231\nv_cvt_pk_bf16_f32 v17, v232, v233\nM 72,172,216\nv_mul_f32 v234, v20, v20\nv_mul_f32 v235, v21, v21\nv_mul_f32 v236, v22, v22\n"
"M 88,168,204\nR 166,188,20480\nv_mov_b32 v226, s46\nv_mul_f32 v237, v23, v23\nM 88,172,220\nG 4\n"
"v_mov_b32 v227, s47\nv_fma_f32 v234, v234, v218, v217\nv_fma_f32 v235, v235, v218, v217\nM 92,176,204\nR 168,189,8192\ns_add_u32 s61, s81, 845832\n"
"s_lshl_b32 s62, s44, 5\ns_add_u32 s61, s61, s62\ns_waitcnt vmcnt(0)\ns_mov_b64 exec, 1\nbuffer_atomic_swap_x2 v[226:227], off, s[20:23], s61 sc1\ns_mov_b64 exec, -1\n"
"v_fma_f32 v236, v236, v218, v217\nM 92,180,220\nbuffer_load_dwordx4 v221, s[16:19], s63 offen offset:1024 lds\nv_fma_f32 v237, v237, v218, v217\nM 76,176,200\nR 170,189,12288\n"
"s_add_u32 m0, s50, 8192\ns_add_u32 s63, s57, 524288\nv_mul_f32 v234, v20, v234\nM 76,180,216\nbuffer_load_dwordx4 v220, s[16:19], s63 offen lds\nv_mul_f32 v235, v21, v235\n"
"M 60,176,196\nR 172,189,16384\nv_mul_f32 v236, v22, v236\nv_mul_f32 v237, v23, v237\nM 60,180,212\nv_exp_f32 v234, v234\n"
"v_exp_f32 v235, v235\nM 44,176,192\nR 174,189,20480\ns_mov_b32 s60, 158208\nv_bfe_u32 v246, v252, 4, 4\nv_lshl_add_u32 v246, v246, 3, s60\n"
"v_exp_f32 v236, v236\nM 44,180,208\nds_read_b64 v[238:239], v246 offset:0\nG 5\nv_exp_f32 v237, v237\nM 32,160,224\n"
"R 176,190,8192\nds_read_b64 v[240:241], v246 offset:128\nv_fma_f32 v234, v234, v219, v219\nM 32,164,240\nds_read_b64 v[242:243], v246 offset:256\nbuffer_load_dwordx4 v221, s[16:19], s63 offen offset:1024 lds\n"
"v_fma_f32 v235, v235, v219, v219\nM 48,160,228\nR 178,190,12288\nds_read_b64 v[244:245], v246 offset:384\nv_fma_f32 v236, v236, v219, v219\nM 48,164,244\n"
"ds_read_b64 v[246:247], v246 offset:0\ns_add_u32 m0, s50, 16384\nv_fma_f32 v237, v237, v219, v219\nM 64,160,232\nR 180,190,16384\ns_add_u32 s63, s57, 65536\n"
"v_rcp_f32 v234, v234\nM 64,164,248\nbuffer_load_dwordx4 v220, s[16:19], s63 offen lds\nv_rcp_f32 v235, v235\nM 80,160,236\nR 182,190,20480\n"
"v_mul_f32 v234, v20, v234\nv_rcp_f32 v236, v236\nM 80,164,252\nG 9\nv_rcp_f32 v237, v237\nv_mul_f32 v235, v21, v235\n"
"M 84,168,236\nR 160,191,8192\nv_mul_f32 v236, v22, v236\nM 84,172,252\nbuffer_load_dwordx4 v221, s[16:19], s63 offen offset:1024 lds\nM 68,168,232\n"
"R 162,191,12288\ns_add_u32 m0, s50, 24576\ns_add_u32 s63, s57, 589824\nM 68,172,248\nbuffer_load_dwordx4 v220, s[16:19], s63 offen lds\nM 52,168,228\n"
"R 164,191,16384\nG 9\nv_mfma_f32_16x16x32_bf16 v[32:35], v[238:241], v[208:211], v[32:35]\nv_mul_f32 v237, v23, v237\nM 52,172,244\nbuffer_store_dwordx4 v[0:3], v252, s[20:23], s84 offen offset:0 sc1\n"
"M 36,168,224\nR 166,191,20480\nG 9\nv_mfma_f32_16x16x32_bf16 v[36:39], v[240:243], v[208:211], v[36:39]\nv_cvt_pk_bf16_f32 v18, v234, v235\nM 36,172,240\n"
"G 4\nv_cvt_pk_bf16_f32 v19, v236, v237\nM 40,176,224\nR 168,188,24576\nv_mfma_f32_16x16x32_bf16 v[40:43], v[242:245], v[208:211], v[40:43]\nbuffer_load_dwordx4 v221, s[16:19], s63 offen offset:1024 lds\n"
"buffer_store_dwordx4 v[16:19], v252, s[20:23], s85 offen offset:0 sc1\nM 40,180,240\ns_add_u32 m0, s50, 32768\ns_add_u32 s63, s56, 131072\nbuffer_load_dwordx4 v220, s[16:19], s63 offen lds\nM 56,176,228\n"
"R 170,188,28672\nv_mfma_f32_16x16x32_bf16 v[44:47], v[244:247], v[208:211], v[44:47]\nv_mov_b32 v248, 0\nM 56,180,244\nv_mov_b32 v249, 0\nM 72,176,232\n"
"R 172,188,32768\nv_mfma_f32_16x16x32_bf16 v[48:51], v[244:247], v[210:213], v[48:51]\ns_add_u32 s61, s82, 845832\ns_mov_b64 exec, 0xf\nbuffer_atomic_or_x2 v[248:249], v250, s[20:23], s61 offen sc0 sc1\ns_mov_b64 exec, -1\n"
"M 72,180,248\nbuffer_load_dwordx4 v221, s[16:19], s63 offen offset:1024 lds\nM 88,176,236\nR 174,188,36864\nv_mfma_f32_16x16x32_bf16 v[52:55], v[238:241], v[210:213], v[52:55]\ns_add_u32 m0, s50, 40960\n"
"M 88,180,252\nG 4\nM 92,160,236\nR 176,189,24576\nv_mfma_f32_16x16x32_bf16 v[56:59], v[240:243], v[210:213], v[56:59]\ns_add_u32 s63, s56, 655360\n"
"M 92,164,252\nbuffer_load_dwordx4 v220, s[16:19], s63 offen lds\nM 76,160,232\nR 178,189,28672\nv_mfma_f32_16x16x32_bf16 v[60:63], v[242:245], v[210:213], v[60:63]\nM 76,164,248\n"
"M 60,160,228\nR 180,189,32768\nM 60,164,244\nM 44,160,224\nR 182,189,36864\nM 44,164,240\n"
"s_barrier\nG 4\nv_mfma_f32_16x16x32_bf16 v[96:99], v[168:171], a[0:3], 0\nR 160,190,24576\nM 96,172,16\ns_mov_b32 m0, s51\n"
"v_mfma_f32_16x16x32_bf16 v[112:115], v[168:171], a[4:7], 0\nR 162,190,28672\nM 112,172,20\nds_write_addtid_b32 v64 offset:0\nv_mfma_f32_16x16x32_bf16 v[128:131], v[168:171], a[8:11], 0\nR 164,190,32768\n"
"M 128,172,24\nds_write_addtid_b32 v65 offset:256\nv_mfma_f32_16x16x32_bf16 v[144:147], v[168:171], a[12:15], 0\nR 166,190,36864\nds_write_addtid_b32 v66 offset:512\nM 144,172,28\n"
"ds_write_addtid_b32 v67 offset:768\nds_write_addtid_b32 v68 offset:1024\nG 9\nv_mfma_f32_16x16x32_bf16 v[148:151], v[176:179], a[12:15], 0\nR 168,191,24576\nds_write_addtid_b32 v69 offset:1280\n"
"M 148,180,28\nds_write_addtid_b32 v70 offset:1536\nds_write_addtid_b32 v71 offset:1792\nv_mfma_f32_16x16x32_bf16 v[132:135], v[176:179], a[8:11], 0\nR 170,191,28672\nds_write_addtid_b32 v72 offset:2048\n"
"M 132,180,24\nds_write_addtid_b32 v73 offset:2304\nds_write_addtid_b32 v74 offset:2560\nv_mfma_f32_16x16x32_bf16 v[116:119], v[176:179], a[4:7], 0\nR 172,191,32768\nds_write_addtid_b32 v75 offset:2816\n"
"M 116,180,20\nds_write_addtid_b32 v76 offset:3072\nds_write_addtid_b32 v77 offset:3328\nv_mfma_f32_16x16x32_bf16 v[100:103], v[176:179], a[0:3], 0\nR 174,191,36864\nds_write_addtid_b32 v78 offset:3584\n"
"M 100,180,16\nds_write_addtid_b32 v79 offset:3840\nds_write_addtid_b32 v80 offset:4096\nG 15\nv_mfma_f32_16x16x32_bf16 v[104:107], v[160:163], a[0:3], 0\nR 176,188,40960\n"
"ds_write_addtid_b32 v81 offset:4352\nM 104,164,16\nds_write_addtid_b32 v82 offset:4608\nds_write_addtid_b32 v83 offset:4864\nv_mfma_f32_16x16x32_bf16 v[120:123], v[160:163], a[4:7], 0\nR 178,188,45056\n"
"ds_write_addtid_b32 v84 offset:5120\nM 120,164,20\nds_write_addtid_b32 v85 offset:5376\nds_write_addtid_b32 v86 offset:5632\nv_mfma_f32_16x16x32_bf16 v[136:139], v[160:163], a[8:11], 0\nR 180,188,49152\n"
"ds_write_addtid_b32 v87 offset:5888\nM 136,164,24\nds_write_addtid_b32 v88 offset:6144\nds_write_addtid_b32 v89 offset:6400\nv_mfma_f32_16x16x32_bf16 v[152:155], v[160:163], a[12:15], 0\nR 182,188,53248\n"
"ds_write_addtid_b32 v90 offset:6656\nM 152,164,28\nds_write_addtid_b32 v91 offset:6912\nds_write_addtid_b32 v92 offset:7168\nG 15\nv_mfma_f32_16x16x32_bf16 v[156:159], v[168:171], a[12:15], 0\n"
"R 160,189,40960\nds_write_addtid_b32 v93 offset:7424\nM 156,172,28\nds_write_addtid_b32 v94 offset:7680\nds_write_addtid_b32 v95 offset:7936\nv_mfma_f32_16x16x32_bf16 v[140:143], v[168:171], a[8:11], 0\n"
"R 162,189,45056\nM 140,172,24\nv_mfma_f32_16x16x32_bf16 v[124:127], v[168:171], a[4:7], 0\nR 164,189,49152\ns_waitcnt vmcnt(5) lgkmcnt(2)\ns_barrier\n"
"M 124,172,20\nds_read2st64_b32 v[64:65], v225 offset0:0 offset1:1\nds_read2st64_b32 v[66:67], v225 offset0:2 offset1:3\nv_mfma_f32_16x16x32_bf16 v[108:111], v[168:171], a[0:3], 0\nR 166,189,53248\nds_read2st64_b32 v[68:69], v225 offset0:4 offset1:5\n"
"M 108,172,16\nds_read2st64_b32 v[70:71], v225 offset0:6 offset1:7\nds_read2st64_b32 v[72:73], v225 offset0:8 offset1:9\nM 96,176,32\nR 168,190,40960\nds_read2st64_b32 v[74:75], v225 offset0:10 offset1:11\n"
"M 96,180,48\ns_add_u32 s64, s68, 37750784\nds_read2st64_b32 v[76:77], v225 offset0:12 offset1:13\nv_mov_b32 v226, s46\nM 112,176,36\nLpg29:\n"
"s_cbranch_execz LretO1n\nR 170,190,45056\nds_read2st64_b32 v[78:79], v225 offset0:14 offset1:15\nv_mov_b32 v227, s47\ns_waitcnt vmcnt(2)\nv_cmp_eq_u64 s[86:87], s[46:47], v[248:249]\n"
"M 112,180,52\ns_add_u32 s61, s81, 845848\ns_lshl_b32 s62, s44, 5\ns_add_u32 s61, s61, s62\ns_waitcnt vmcnt(0)\ns_mov_b64 exec, 1\n"
"buffer_atomic_swap_x2 v[226:227], off, s[20:23], s61 sc1\ns_mov_b64 exec, -1\nds_read2st64_b32 v[80:81], v225 offset0:16 offset1:17\nM 128,176,40\nR 172,190,49152\ns_and_b32 s62, s86, 0xf\n"
"s_cmp_eq_u32 s62, 0xf\ns_cbranch_scc0 Lq873\nLq874:\nds_read2st64_b32 v[82:83], v225 offset0:18 offset1:19\nM 128,180,56\ns_add_u32 s65, s68, 37865472\n"
"ds_read2st64_b32 v[84:85], v225 offset0:20 offset1:21\nds_read2st64_b32 v[86:87], v225 offset0:22 offset1:23\nM 144,176,44\nR 174,190,53248\ns_add_u32 m0, s50, 40960\nds_read2st64_b32 v[88:89], v225 offset0:24 offset1:25\n"
"M 144,180,60\nds_read2st64_b32 v[90:91], v225 offset0:26 offset1:27\nds_read2st64_b32 v[92:93], v225 offset0:28 offset1:29\nG 15\nM 148,160,44\nR 176,191,40960\n"
"buffer_load_dwordx4 v221, s[16:19], s63 offen offset:1024 lds\nds_read2st64_b32 v[94:95], v225 offset0:30 offset1:31\nM 148,164,60\nbuffer_load_dwordx4 a[64:67], v252, s[20:23], s64 offen sc1\nv_add_f32 v32, v32, v64\nM 132,160,40\n"
"R 178,191,45056\ns_add_u32 m0, s50, 49152\nv_add_f32 v33, v33, v65\nM 132,164,56\ns_add_u32 s66, s69, 37750784\ns_add_u32 s63, s56, 196608\n"
"v_add_f32 v34, v34, v66\nv_add_f32 v35, v35, v67\nM 116,160,36\nR 180,191,49152\nbuffer_load_dwordx4 v220, s[16:19], s63 offen lds\nv_add_f32 v36, v36, v68\n"
"M 116,164,52\nbuffer_load_dwordx4 a[68:71], v252, s[20:23], s65 offen sc1\nv_add_f32 v37, v37, v69\nM 100,160,32\nR 182,191,53248\nv_add_f32 v38, v38, v70\n"
"v_add_f32 v39, v39, v71\nM 100,164,48\nG 8\nv_add_f32 v40, v40, v72\nv_add_f32 v41, v41, v73\nv_add_f32 v42, v42, v74\n"
"M 104,168,32\nR 160,184,0\nbuffer_load_dwordx4 v221, s[16:19], s63 offen offset:1024 lds\nv_add_f32 v43, v43, v75\nM 104,172,48\ns_add_u32 s67, s69, 37865472\n"
"v_add_f32 v44, v44, v76\nv_add_f32 v45, v45, v77\nv_add_f32 v46, v46, v78\nM 120,168,36\nR 162,184,4096\nv_add_f32 v47, v47, v79\n"
"v_add_f32 v48, v48, v80\nM 120,172,52\nbuffer_load_dwordx4 a[72:75], v252, s[20:23], s66 offen sc1\nv_add_f32 v49, v49, v81\nM 136,168,40\nR 164,184,8192\n"
"v_add_f32 v50, v50, v82\nv_add_f32 v51, v51, v83\nM 136,172,56\nv_add_f32 v52, v52, v84\nv_add_f32 v53, v53, v85\nM 152,168,44\n"
"R 166,184,12288\nv_add_f32 v54, v54, v86\nM 152,172,60\nG 4\nv_add_f32 v55, v55, v87\nv_add_f32 v56, v56, v88\n"
"M 156,176,44\nR 168,185,0\nv_add_f32 v57, v57, v89\nM 156,180,60\nbuffer_load_dwordx4 a[76:79], v252, s[20:23], s67 offen sc1\nM 140,176,40\n"
"R 170,185,4096\nv_add_f32 v58, v58, v90\nM 140,180,56\ns_add_u32 s64, s68, 37757952\nv_add_f32 v59, v59, v91\nv_add_f32 v60, v60, v92\n"
"M 124,176,36\nR 172,185,8192\nv_add_f32 v61, v61, v93\nM 124,180,52\ns_add_u32 s65, s68, 37872640\nv_add_f32 v62, v62, v94\n"
"v_add_f32 v63, v63, v95\nM 108,176,32\nR 174,185,12288\nv_mul_f32 v230, v32, v32\nv_mul_f32 v231, v33, v33\nM 108,180,48\n"
"s_add_u32 s84, s73, 37752832\ns_add_u32 s85, s73, 37867520\nG 4\nv_mul_f32 v232, v34, v34\nv_mfma_f32_16x16x32_bf16 v[0:3], v[160:163], a[0:3], 0\nR 176,186,0\n"
"s_lshr_b32 s60, s45, 3\ns_lshl2_add_u32 s60, s60, s40\ns_lshl_b32 s60, s60, 8\nv_lshl_add_u32 v224, v252, 3, s60\nv_add_u32 v224, LqF-32768-LqPC, v224\nv_min_u32 v224, LqE-LqPC-4, v224\n"
"s_mov_b64 exec, 3\nglobal_load_dword v224, v224, s[98:99]\ns_mov_b64 exec, -1\nv_mul_f32 v233, v35, v35\nM 0,164,16\nbuffer_load_dwordx4 a[80:83], v252, s[20:23], s64 offen sc1\n"
"v_fma_f32 v230, v230, v215, v214\nv_mfma_f32_16x16x32_bf16 v[16:19], v[160:163], a[4:7], 0\nR 178,186,4096\nv_fma_f32 v231, v231, v215, v214\nv_fma_f32 v232, v232, v215, v214\nM 16,164,20\n"
"s_add_u32 s66, s69, 37757952\nv_fma_f32 v233, v233, v215, v214\nv_mul_f32 v230, v32, v230\nv_mul_f32 v231, v33, v231\nv_mfma_f32_16x16x32_bf16 a[224:227], v[160:163], a[8:11], 0\nR 180,186,8192\n"
"v_mul_f32 v232, v34, v232\nv_mul_f32 v233, v35, v233\nv_mfma_f32_16x16x32_bf16 a[224:227], v[164:167], a[24:27], a[224:227]\nbuffer_load_dwordx4 a[84:87], v252, s[20:23], s65 offen sc1\nv_exp_f32 v230, v230\nv_mfma_f32_16x16x32_bf16 a[240:243], v[160:163], a[12:15], 0\n"
"R 182,186,12288\nv_fma_f32 v230, v230, v216, v216\nv_exp_f32 v231, v231\nv_mfma_f32_16x16x32_bf16 a[240:243], v[164:167], a[28:31], a[240:243]\nG 4\nv_exp_f32 v232, v232\n"
"v_exp_f32 v233, v233\nv_mfma_f32_16x16x32_bf16 a[244:247], v[168:171], a[12:15], 0\nR 160,187,0\ns_add_u32 m0, s50, 57344\nv_fma_f32 v231, v231, v216, v216\nv_mfma_f32_16x16x32_bf16 a[244:247], v[172:175], a[28:31], a[244:247]\n"
"s_add_u32 s67, s69, 37872640\nv_rcp_f32 v230, v230\nv_fma_f32 v232, v232, v216, v216\nv_mfma_f32_16x16x32_bf16 a[228:231], v[168:171], a[8:11], 0\nR 162,187,4096\ns_add_u32 s63, s56, 720896\n"
"v_fma_f32 v233, v233, v216, v216\nv_mfma_f32_16x16x32_bf16 a[228:231], v[172:175], a[24:27], a[228:231]\nbuffer_load_dwordx4 a[88:91], v252, s[20:23], s66 offen sc1\nv_rcp_f32 v231, v231\nv_mfma_f32_16x16x32_bf16 v[20:23], v[168:171], a[4:7], 0\nR 164,187,8192\n"
"v_mul_f32 v230, v32, v230\nv_rcp_f32 v232, v232\nM 20,172,20\nbuffer_load_dwordx4 v220, s[16:19], s63 offen lds\nv_rcp_f32 v233, v233\nv_mfma_f32_16x16x32_bf16 v[4:7], v[168:171], a[0:3], 0\n"
"R 166,187,12288\nv_mul_f32 v231, v33, v231\nv_mul_f32 v232, v34, v232\nM 4,172,16\nG 4\nv_mul_f32 v233, v35, v233\n"
"v_cvt_pk_bf16_f32 v32, v230, v231\nv_cvt_pk_bf16_f32 v33, v232, v233\nv_mfma_f32_16x16x32_bf16 v[8:11], v[176:179], a[0:3], 0\nR 168,184,16384\nv_mul_f32 v234, v36, v36\nv_mul_f32 v235, v37, v37\n"
"M 8,180,16\nbuffer_load_dwordx4 a[92:95], v252, s[20:23], s67 offen sc1\nbuffer_load_dwordx4 v221, s[16:19], s63 offen offset:1024 lds\nv_mul_f32 v236, v38, v38\nv_mfma_f32_16x16x32_bf16 v[24:27], v[176:179], a[4:7], 0\nR 170,184,20480\n"
"s_add_u32 m0, s50, 65536\nv_mul_f32 v237, v39, v39\nM 24,180,20\ns_add_u32 s64, s68, 37751808\nv_fma_f32 v234, v234, v215, v214\nv_fma_f32 v235, v235, v215, v214\n"
"v_fma_f32 v236, v236, v215, v214\nv_mfma_f32_16x16x32_bf16 a[232:235], v[176:179], a[8:11], 0\nR 172,184,24576\ns_add_u32 s63, s57, 131072\nv_fma_f32 v237, v237, v215, v214\nv_mfma_f32_16x16x32_bf16 a[232:235], v[180:183], a[24:27], a[232:235]\n"
"s_add_u32 s65, s68, 37866496\nbuffer_load_dwordx4 v220, s[16:19], s63 offen lds\nv_mul_f32 v234, v36, v234\nv_mfma_f32_16x16x32_bf16 a[248:251], v[176:179], a[12:15], 0\nR 174,184,28672\nv_mul_f32 v235, v37, v235\n"
"v_mul_f32 v236, v38, v236\nv_mfma_f32_16x16x32_bf16 a[248:251], v[180:183], a[28:31], a[248:251]\nG 4\nv_exp_f32 v234, v234\nv_mul_f32 v237, v39, v237\nv_mfma_f32_16x16x32_bf16 a[252:255], v[160:163], a[12:15], 0\n"
"R 176,185,16384\nv_fma_f32 v234, v234, v216, v216\nv_exp_f32 v235, v235\nv_mfma_f32_16x16x32_bf16 a[252:255], v[164:167], a[28:31], a[252:255]\nbuffer_load_dwordx4 a[96:99], v252, s[20:23], s64 offen sc1\nbuffer_load_dwordx4 v221, s[16:19], s63 offen offset:1024 lds\n"
"v_exp_f32 v236, v236\nv_mfma_f32_16x16x32_bf16 a[236:239], v[160:163], a[8:11], 0\nR 178,185,20480\ns_add_u32 m0, s50, 73728\nv_exp_f32 v237, v237\nv_mfma_f32_16x16x32_bf16 a[236:239], v[164:167], a[24:27], a[236:239]\n"
"s_add_u32 s66, s69, 37751808\nv_rcp_f32 v234, v234\nv_fma_f32 v235, v235, v216, v216\nv_mfma_f32_16x16x32_bf16 v[28:31], v[160:163], a[4:7], 0\nR 180,185,24576\ns_add_u32 s63, s57, 655360\n"
"v_fma_f32 v236, v236, v216, v216\nM 28,164,20\nbuffer_load_dwordx4 a[100:103], v252, s[20:23], s65 offen sc1\nbuffer_load_dwordx4 v220, s[16:19], s63 offen lds\nv_fma_f32 v237, v237, v216, v216\nv_mfma_f32_16x16x32_bf16 v[12:15], v[160:163], a[0:3], 0\n"
"R 182,185,28672\nv_mul_f32 v234, v36, v234\nv_rcp_f32 v235, v235\nM 12,164,16\ns_waitcnt vmcnt(14)\ns_barrier\n"
"G 4\nv_rcp_f32 v236, v236\nM 0,168,32\nR 160,186,16384\nv_mul_f32 v235, v37, v235\nv_rcp_f32 v237, v237\n"
"M 0,172,48\ns_add_u32 s67, s69, 37866496\nbuffer_load_dwordx4 v221, s[16:19], s63 offen offset:1024 lds\nv_mul_f32 v236, v38, v236\nM 16,168,36\nR 162,186,20480\n"
"v_mul_f32 v237, v39, v237\nv_cvt_pk_bf16_f32 v34, v234, v235\nM 16,172,52\nbuffer_load_dwordx4 a[104:107], v252, s[20:23], s66 offen sc1\nv_cvt_pk_bf16_f32 v35, v236, v237\nv_mfma_f32_16x16x32_bf16 a[224:227], v[168:171], a[40:43], a[224:227]\n"
"R 164,186,24576\nv_mul_f32 v230, v40, v40\nv_mul_f32 v231, v41, v41\nv_mfma_f32_16x16x32_bf16 a[224:227], v[172:175], a[56:59], a[224:227]\nv_mul_f32 v232, v42, v42\nv_mul_f32 v233, v43, v43\n"
"v_fma_f32 v230, v230, v215, v214\nv_mfma_f32_16x16x32_bf16 a[240:243], v[168:171], a[44:47], a[240:243]\nR 166,186,28672\nv_fma_f32 v231, v231, v215, v214\nv_fma_f32 v232, v232, v215, v214\nv_mfma_f32_16x16x32_bf16 a[240:243], v[172:175], a[60:63], a[240:243]\n"
"G 4\nv_fma_f32 v233, v233, v215, v214\nv_mul_f32 v230, v40, v230\nv_mul_f32 v231, v41, v231\nv_mfma_f32_16x16x32_bf16 a[244:247], v[176:179], a[44:47], a[244:247]\nR 168,187,16384\n"
"v_mul_f32 v232, v42, v232\nv_mul_f32 v233, v43, v233\nv_mfma_f32_16x16x32_bf16 a[244:247], v[180:183], a[60:63], a[244:247]\nbuffer_load_dwordx4 a[108:111], v252, s[20:23], s67 offen sc1\nv_exp_f32 v230, v230\nv_mfma_f32_16x16x32_bf16 a[228:231], v[176:179], a[40:43], a[228:231]\n"
"R 170,187,20480\nv_fma_f32 v230, v230, v216, v216\nv_exp_f32 v231, v231\nv_mfma_f32_16x16x32_bf16 a[228:231], v[180:183], a[56:59], a[228:231]\ns_add_u32 s64, s68, 37758976\nv_exp_f32 v232, v232\n"
"v_exp_f32 v233, v233\nM 20,176,36\nR 172,187,24576\nv_fma_f32 v231, v231, v216, v216\nv_fma_f32 v232, v232, v216, v216\nM 20,180,52\n"
"s_add_u32 s65, s68, 37873664\nv_rcp_f32 v230, v230\nv_fma_f32 v233, v233, v216, v216\nM 4,176,32\nR 174,187,28672\ns_add_u32 m0, s50, 81920\n"
"v_rcp_f32 v231, v231\nM 4,180,48\nG 4\nv_rcp_f32 v232, v232\nv_rcp_f32 v233, v233\nM 8,160,32\n"
"R 176,184,32768\ns_add_u32 s63, s57, 196608\nv_mul_f32 v230, v40, v230\nM 8,164,48\nbuffer_load_dwordx4 a[112:115], v252, s[20:23], s64 offen sc1\nv_mul_f32 v231, v41, v231\n"
"M 24,160,36\nR 178,184,36864\nv_mul_f32 v232, v42, v232\nv_mul_f32 v233, v43, v233\nM 24,164,52\ns_add_u32 s66, s69, 37758976\n"
"buffer_load_dwordx4 v220, s[16:19], s63 offen lds\nv_cvt_pk_bf16_f32 v40, v230, v231\nv_mfma_f32_16x16x32_bf16 a[232:235], v[160:163], a[40:43], a[232:235]\nR 180,184,40960\nv_cvt_pk_bf16_f32 v41, v232, v233\nv_mul_f32 v234, v44, v44\n"
"v_mfma_f32_16x16x32_bf16 a[232:235], v[164:167], a[56:59], a[232:235]\nbuffer_load_dwordx4 a[116:119], v252, s[20:23], s65 offen sc1\nv_mul_f32 v235, v45, v45\nv_mfma_f32_16x16x32_bf16 a[248:251], v[160:163], a[44:47], a[248:251]\nR 182,184,45056\nv_mul_f32 v236, v46, v46\n"
"v_mul_f32 v237, v47, v47\nv_mfma_f32_16x16x32_bf16 a[248:251], v[164:167], a[60:63], a[248:251]\nbuffer_load_dwordx4 v221, s[16:19], s63 offen offset:1024 lds\nG 4\nv_fma_f32 v234, v234, v215, v214\nv_mfma_f32_16x16x32_bf16 a[252:255], v[168:171], a[44:47], a[252:255]\n"
"R 160,185,32768\ns_add_u32 m0, s50, 90112\nv_fma_f32 v235, v235, v215, v214\nv_mfma_f32_16x16x32_bf16 a[252:255], v[172:175], a[60:63], a[252:255]\ns_add_u32 s67, s69, 37873664\ns_add_u32 s63, s57, 720896\n"
"v_fma_f32 v236, v236, v215, v214\nv_fma_f32 v237, v237, v215, v214\nv_mfma_f32_16x16x32_bf16 a[236:239], v[168:171], a[40:43], a[236:239]\nR 162,185,36864\nv_mov_b32 v248, 0\nv_mul_f32 v234, v44, v234\n"
"v_mfma_f32_16x16x32_bf16 a[236:239], v[172:175], a[56:59], a[236:239]\nbuffer_load_dwordx4 a[120:123], v252, s[20:23], s66 offen sc1\nbuffer_load_dwordx4 v220, s[16:19], s63 offen lds\nv_mov_b32 v249, 0\nv_mul_f32 v235, v45, v235\nM 28,168,36\n"
"R 164,185,40960\ns_add_u32 s61, s82, 845848\ns_mov_b64 exec, 0xf\nbuffer_atomic_or_x2 v[248:249], v250, s[20:23], s61 offen sc0 sc1\ns_mov_b64 exec, -1\nv_mul_f32 v236, v46, v236\n"
"M 28,172,52\nbuffer_load_dwordx4 v221, s[16:19], s63 offen offset:1024 lds\nv_mul_f32 v237, v47, v237\nM 12,168,32\nR 166,185,45056\ns_add_u32 m0, s50, 98304\n"
"v_exp_f32 v234, v234\nM 12,172,48\nG 4\nv_exp_f32 v235, v235\nv_exp_f32 v236, v236\nM 96,176,64\n"
"R 168,186,32768\ns_add_u32 s63, s58, 0\nv_exp_f32 v237, v237\ns_waitcnt vmcnt(21)\nM 96,180,80\nbuffer_load_dwordx4 a[124:127], v252, s[20:23], s67 offen sc1\n"
"v_fma_f32 v234, v234, v216, v216\nM 112,176,68\nR 170,186,36864\nv_fma_f32 v235, v235, v216, v216\nv_fma_f32 v236, v236, v216, v216\ns_waitcnt vmcnt(21)\n"
"M 112,180,84\nbuffer_load_dwordx4 v220, s[16:19], s63 offen lds\nv_fma_f32 v237, v237, v216, v216\nM 128,176,72\nR 172,186,40960\nv_mul_f32 v230, v48, v48\n"
"v_rcp_f32 v234, v234\ns_waitcnt vmcnt(21)\nM 128,180,88\nbuffer_load_dwordx4 v221, s[16:19], s63 offen offset:1024 lds\nv_rcp_f32 v235, v235\nM 144,176,76\n"
"R 174,186,45056\ns_add_u32 m0, s50, 106496\nv_rcp_f32 v236, v236\ns_waitcnt vmcnt(20)\nM 144,180,92\nG 4\n"
"v_rcp_f32 v237, v237\nv_mul_f32 v234, v44, v234\nM 148,160,76\nR 176,187,32768\ns_add_u32 s63, s58, 524288\nv_mul_f32 v235, v45, v235\n"
"M 148,164,92\nbuffer_load_dwordx4 v220, s[16:19], s63 offen lds\nv_mul_f32 v236, v46, v236\nM 132,160,72\nR 178,187,36864\nv_mul_f32 v237, v47, v237\n"
"v_cvt_pk_bf16_f32 v42, v234, v235\nM 132,164,88\nbuffer_store_dwordx4 v[32:35], v252, s[20:23], s84 offen offset:0 sc1\nv_cvt_pk_bf16_f32 v43, v236, v237\nM 116,160,68\nR 180,187,40960\n"
"v_mul_f32 v231, v49, v49\nv_mul_f32 v232, v50, v50\nM 116,164,84\nbuffer_load_dwordx4 v221, s[16:19], s63 offen offset:1024 lds\nv_mul_f32 v233, v51, v51\nM 100,160,64\n"
"R 182,187,45056\nv_fma_f32 v230, v230, v218, v217\nv_fma_f32 v231, v231, v218, v217\nM 100,164,80\nG 4\nv_fma_f32 v232, v232, v218, v217\n"
"v_fma_f32 v233, v233, v218, v217\nv_mul_f32 v230, v48, v230\nM 104,168,64\nR 160,184,49152\ns_waitcnt vmcnt(16)\ns_barrier\n"
"v_mul_f32 v231, v49, v231\nM 104,172,80\nbuffer_store_dwordx4 v[40:43], v252, s[20:23], s84 offen offset:1024 sc1\nv_mul_f32 v232, v50, v232\nM 120,168,68\nR 162,184,53248\n"
"v_mul_f32 v233, v51, v233\nv_exp_f32 v230, v230\nM 120,172,84\ns_add_u32 m0, s50, 114688\nv_exp_f32 v231, v231\nv_exp_f32 v232, v232\n"
"M 136,168,72\nR 164,184,57344\nv_fma_f32 v230, v230, v219, v219\nv_exp_f32 v233, v233\nM 136,172,88\ns_add_u32 s63, s58, 65536\n"
"v_rcp_f32 v230, v230\nv_fma_f32 v231, v231, v219, v219\nM 152,168,76\nR 166,184,61440\nv_fma_f32 v232, v232, v219, v219\nv_fma_f32 v233, v233, v219, v219\n"
"M 152,172,92\nG 4\nv_rcp_f32 v231, v231\nv_rcp_f32 v232, v232\nM 156,176,76\nR 168,185,49152\n"
"v_mul_f32 v230, v48, v230\nv_rcp_f32 v233, v233\nM 156,180,92\nbuffer_load_dwordx4 v220, s[16:19], s63 offen lds\nv_mul_f32 v231, v49, v231\nM 140,176,72\n"
"R 170,185,53248\nv_mul_f32 v232, v50, v232\nv_mul_f32 v233, v51, v233\nM 140,180,88\nv_cvt_pk_bf16_f32 v48, v230, v231\nv_cvt_pk_bf16_f32 v49, v232, v233\n"
"v_mul_f32 v234, v52, v52\nM 124,176,68\nR 172,185,57344\nv_mul_f32 v235, v53, v53\nv_mul_f32 v236, v54, v54\nM 124,180,84\n"
"buffer_load_dwordx4 v221, s[16:19], s63 offen offset:1024 lds\nv_mul_f32 v237, v55, v55\nM 108,176,64\nR 174,185,61440\ns_add_u32 m0, s50, 0\nv_fma_f32 v234, v234, v218, v217\n"
"M 108,180,80\nG 4\nv_fma_f32 v235, v235, v218, v217\nv_fma_f32 v236, v236, v218, v217\nv_fma_f32 v237, v237, v218, v217\nM 96,160,96\n"
"R 176,186,49152\ns_add_u32 s63, s58, 589824\nv_mul_f32 v234, v52, v234\ns_waitcnt vmcnt(16)\nM 96,164,112\nbuffer_load_dwordx4 v220, s[16:19], s63 offen lds\n"
"v_mul_f32 v235, v53, v235\nM 112,160,100\nR 178,186,53248\nv_mul_f32 v236, v54, v236\nv_mul_f32 v237, v55, v237\ns_waitcnt vmcnt(15)\n"
"M 112,164,116\nv_exp_f32 v234, v234\nv_exp_f32 v235, v235\nM 128,160,104\nR 180,186,57344\nv_fma_f32 v234, v234, v219, v219\n"
"v_exp_f32 v236, v236\ns_waitcnt vmcnt(13)\nM 128,164,120\nv_exp_f32 v237, v237\nv_fma_f32 v235, v235, v219, v219\nM 144,160,108\n"
"R 182,186,61440\nv_fma_f32 v236, v236, v219, v219\nv_fma_f32 v237, v237, v219, v219\ns_waitcnt vmcnt(9)\nM 144,164,124\nG 4\n"
"v_rcp_f32 v234, v234\nv_rcp_f32 v235, v235\nM 148,168,108\nR 160,187,49152\nv_mul_f32 v234, v52, v234\nv_rcp_f32 v236, v236\n"
"M 148,172,124\ns_add_u32 s64, s68, 37754880\nv_rcp_f32 v237, v237\nv_mul_f32 v235, v53, v235\nM 132,168,104\nR 162,187,53248\n"
"v_cmp_eq_u64 s[86:87], s[46:47], v[248:249]\nv_mul_f32 v236, v54, v236\nM 132,172,120\nbuffer_load_dwordx4 v221, s[16:19], s63 offen offset:1024 lds\nv_mul_f32 v237, v55, v237\nM 116,168,100\n"
"R 164,187,57344\ns_and_b32 s62, s86, 0xf\ns_cmp_eq_u32 s62, 0xf\ns_cbranch_scc0 Lq908\nLq909:\nv_cvt_pk_bf16_f32 v50, v234, v235\n"
"M 116,172,116\ns_add_u32 s65, s68, 37869568\nv_cvt_pk_bf16_f32 v51, v236, v237\nv_mul_f32 v230, v56, v56\nv_mul_f32 v231, v57, v57\nM 100,168,96\n"
"R 166,187,61440\nv_mul_f32 v232, v58, v58\nv_mul_f32 v233, v59, v59\nM 100,172,112\nG 4\nv_fma_f32 v230, v230, v218, v217\n"
"v_fma_f32 v231, v231, v218, v217\nM 104,176,96\nR 168,188,0\nv_fma_f32 v232, v232, v218, v217\nM 104,180,112\nbuffer_load_dwordx4 a[128:131], v252, s[20:23], s64 offen sc1\n"
"M 120,176,100\nR 170,188,4096\nv_fma_f32 v233, v233, v218, v217\nM 120,180,116\ns_add_u32 s66, s69, 37754880\nv_mul_f32 v230, v56, v230\n"
"v_mul_f32 v231, v57, v231\nM 136,176,104\nR 172,188,8192\nv_mul_f32 v232, v58, v232\nM 136,180,120\nbuffer_load_dwordx4 a[132:135], v252, s[20:23], s65 offen sc1\n"
"M 152,176,108\nR 174,188,12288\nv_mul_f32 v233, v59, v233\nM 152,180,124\nG 4\nv_exp_f32 v230, v230\n"
"M 156,160,108\nR 176,189,0\nv_fma_f32 v230, v230, v219, v219\nM 156,164,124\ns_add_u32 s67, s69, 37869568\nv_exp_f32 v231, v231\n"
"M 140,160,104\nR 178,189,4096\nv_fma_f32 v231, v231, v219, v219\nM 140,164,120\nbuffer_load_dwordx4 a[136:139], v252, s[20:23], s66 offen sc1\nM 124,160,100\n"
"R 180,189,8192\nv_mul_f32 v234, v60, v60\nM 124,164,116\nbuffer_store_dwordx4 v[48:51], v252, s[20:23], s85 offen offset:0 sc1\nM 108,160,96\nLpg30:\n"
"s_cbranch_execz LretO1n\nR 182,189,12288\ns_add_u32 m0, s50, 8192\nM 108,164,112\ns_waitcnt vmcnt(9)\ns_barrier\n"
"G 4\nM 0,168,64\nR 160,190,0\ns_add_u32 s63, s58, 131072\nM 0,172,80\nbuffer_load_dwordx4 a[140:143], v252, s[20:23], s67 offen sc1\n"
"M 16,168,68\nR 162,190,4096\nv_mul_f32 v235, v61, v61\nM 16,172,84\ns_add_u32 s64, s68, 37762048\nv_exp_f32 v232, v232\n"
"v_mfma_f32_16x16x32_bf16 a[224:227], v[168:171], a[72:75], a[224:227]\nR 164,190,8192\nbuffer_load_dwordx4 v220, s[16:19], s63 offen lds\nv_mfma_f32_16x16x32_bf16 a[224:227], v[172:175], a[88:91], a[224:227]\ns_add_u32 s65, s68, 37876736\nv_exp_f32 v233, v233\n"
"v_mfma_f32_16x16x32_bf16 a[240:243], v[168:171], a[76:79], a[240:243]\nR 166,190,12288\nv_fma_f32 v232, v232, v219, v219\nv_mfma_f32_16x16x32_bf16 a[240:243], v[172:175], a[92:95], a[240:243]\nG 4\nv_rcp_f32 v230, v230\n"
"v_mfma_f32_16x16x32_bf16 a[244:247], v[176:179], a[76:79], a[244:247]\nR 168,191,0\nbuffer_load_dwordx4 v221, s[16:19], s63 offen offset:1024 lds\nv_mfma_f32_16x16x32_bf16 a[244:247], v[180:183], a[92:95], a[244:247]\nbuffer_load_dwordx4 a[144:147], v252, s[20:23], s64 offen sc1\nv_mfma_f32_16x16x32_bf16 a[228:231], v[176:179], a[72:75], a[228:231]\n"
"R 170,191,4096\ns_add_u32 m0, s50, 16384\nv_mfma_f32_16x16x32_bf16 a[228:231], v[180:183], a[88:91], a[228:231]\ns_add_u32 s66, s69, 37762048\ns_add_u32 s63, s58, 655360\nv_fma_f32 v233, v233, v219, v219\n"
"M 20,176,68\nR 172,191,8192\nbuffer_load_dwordx4 v220, s[16:19], s63 offen lds\nM 20,180,84\nbuffer_load_dwordx4 a[148:151], v252, s[20:23], s65 offen sc1\nM 4,176,64\n"
"R 174,191,12288\nv_mul_f32 v230, v56, v230\nM 4,180,80\nG 4\nv_rcp_f32 v231, v231\nM 8,160,64\n"
"R 176,188,16384\nbuffer_load_dwordx4 v221, s[16:19], s63 offen offset:1024 lds\nM 8,164,80\ns_add_u32 s67, s69, 37876736\nv_rcp_f32 v232, v232\nM 24,160,68\n"
"R 178,188,20480\nv_mul_f32 v231, v57, v231\nM 24,164,84\nbuffer_load_dwordx4 a[152:155], v252, s[20:23], s66 offen sc1\nv_mfma_f32_16x16x32_bf16 a[232:235], v[160:163], a[72:75], a[232:235]\nR 180,188,24576\n"
"v_mul_f32 v232, v58, v232\nv_mfma_f32_16x16x32_bf16 a[232:235], v[164:167], a[88:91], a[232:235]\nv_rcp_f32 v233, v233\nv_mfma_f32_16x16x32_bf16 a[248:251], v[160:163], a[76:79], a[248:251]\nR 182,188,28672\nv_mul_f32 v233, v59, v233\n"
"v_mfma_f32_16x16x32_bf16 a[248:251], v[164:167], a[92:95], a[248:251]\nG 4\nv_cvt_pk_bf16_f32 v56, v230, v231\nv_cvt_pk_bf16_f32 v57, v232, v233\nv_mfma_f32_16x16x32_bf16 a[252:255], v[168:171], a[76:79], a[252:255]\nR 160,189,16384\n"
"v_mul_f32 v236, v62, v62\nv_mfma_f32_16x16x32_bf16 a[252:255], v[172:175], a[92:95], a[252:255]\nbuffer_load_dwordx4 a[156:159], v252, s[20:23], s67 offen sc1\nv_mfma_f32_16x16x32_bf16 a[236:239], v[168:171], a[72:75], a[236:239]\nR 162,189,20480\nv_mul_f32 v237, v63, v63\n"
"v_mfma_f32_16x16x32_bf16 a[236:239], v[172:175], a[88:91], a[236:239]\nv_fma_f32 v234, v234, v218, v217\nv_fma_f32 v235, v235, v218, v217\nM 28,168,68\nR 164,189,24576\nv_fma_f32 v236, v236, v218, v217\n"
"M 28,172,84\ns_add_u32 m0, s50, 24576\nv_fma_f32 v237, v237, v218, v217\nv_mul_f32 v234, v60, v234\nM 12,168,64\nR 166,189,28672\n"
"v_mul_f32 v235, v61, v235\nM 12,172,80\nG 4\nv_exp_f32 v234, v234\nM 0,176,96\nR 168,190,16384\n"
"v_mul_f32 v236, v62, v236\nM 0,180,112\ns_add_u32 s63, s58, 196608\nv_exp_f32 v235, v235\nM 16,176,100\nR 170,190,20480\n"
"v_mul_f32 v237, v63, v237\nM 16,180,116\nbuffer_load_dwordx4 v220, s[16:19], s63 offen lds\nv_mfma_f32_16x16x32_bf16 a[224:227], v[176:179], a[104:107], a[224:227]\nR 172,190,24576\nv_fma_f32 v234, v234, v219, v219\n"
"v_mfma_f32_16x16x32_bf16 a[224:227], v[180:183], a[120:123], a[224:227]\nv_exp_f32 v236, v236\nv_mfma_f32_16x16x32_bf16 a[240:243], v[176:179], a[108:111], a[240:243]\nR 174,190,28672\nv_fma_f32 v235, v235, v219, v219\nv_mfma_f32_16x16x32_bf16 a[240:243], v[180:183], a[124:127], a[240:243]\n"
"G 4\nv_exp_f32 v237, v237\nv_mfma_f32_16x16x32_bf16 a[244:247], v[160:163], a[108:111], a[244:247]\nR 176,191,16384\nv_fma_f32 v236, v236, v219, v219\nv_mfma_f32_16x16x32_bf16 a[244:247], v[164:167], a[124:127], a[244:247]\n"
"buffer_load_dwordx4 v221, s[16:19], s63 offen offset:1024 lds\nv_mfma_f32_16x16x32_bf16 a[228:231], v[160:163], a[104:107], a[228:231]\nR 178,191,20480\ns_add_u32 s63, s58, 720896\nv_mfma_f32_16x16x32_bf16 a[228:231], v[164:167], a[120:123], a[228:231]\ns_add_u32 m0, s50, 32768\n"
"v_rcp_f32 v234, v234\nM 20,160,100\nR 180,191,24576\nv_fma_f32 v237, v237, v219, v219\nM 20,164,116\nbuffer_load_dwordx4 v220, s[16:19], s63 offen lds\n"
"M 4,160,96\nR 182,191,28672\nv_mul_f32 v234, v60, v234\nM 4,164,112\nG 4\nv_rcp_f32 v235, v235\n"
"M 8,168,96\nR 160,188,32768\nv_mul_f32 v235, v61, v235\nM 8,172,112\nbuffer_load_dwordx4 v221, s[16:19], s63 offen offset:1024 lds\nM 24,168,100\n"
"R 162,188,36864\ns_add_u32 s63, s58, 393216\nM 24,172,116\ns_add_u32 m0, s50, 40960\nv_rcp_f32 v236, v236\nv_mfma_f32_16x16x32_bf16 a[232:235], v[168:171], a[104:107], a[232:235]\n"
"R 164,188,40960\nv_mul_f32 v236, v62, v236\nv_mfma_f32_16x16x32_bf16 a[232:235], v[172:175], a[120:123], a[232:235]\nbuffer_load_dwordx4 v220, s[16:19], s63 offen lds\nv_mfma_f32_16x16x32_bf16 a[248:251], v[168:171], a[108:111], a[248:251]\nR 166,188,45056\n"
"v_cvt_pk_bf16_f32 v58, v234, v235\nv_mfma_f32_16x16x32_bf16 a[248:251], v[172:175], a[124:127], a[248:251]\nG 4\nv_rcp_f32 v237, v237\nv_mfma_f32_16x16x32_bf16 a[252:255], v[176:179], a[108:111], a[252:255]\nR 168,189,32768\n"
"v_mul_f32 v237, v63, v237\nv_mfma_f32_16x16x32_bf16 a[252:255], v[180:183], a[124:127], a[252:255]\nbuffer_load_dwordx4 v221, s[16:19], s63 offen offset:1024 lds\nv_mfma_f32_16x16x32_bf16 a[236:239], v[176:179], a[104:107], a[236:239]\nR 170,189,36864\ns_add_u32 s63, s58, 458752\n"
"v_mfma_f32_16x16x32_bf16 a[236:239], v[180:183], a[120:123], a[236:239]\ns_add_u32 m0, s50, 49152\nv_cvt_pk_bf16_f32 v59, v236, v237\nbuffer_store_dwordx4 v[56:59], v252, s[20:23], s85 offen offset:1024 sc1\nM 28,176,100\nR 172,189,40960\n"
"M 28,180,116\nbuffer_load_dwordx4 v220, s[16:19], s63 offen lds\nM 12,176,96\nR 174,189,45056\nM 12,180,112\ns_waitcnt vmcnt(10)\n"
"s_barrier\nG 4\nv_mfma_f32_16x16x32_bf16 v[32:35], v[160:163], a[0:3], 0\nR 176,190,32768\nM 32,164,16\nbuffer_load_dwordx4 v221, s[16:19], s63 offen offset:1024 lds\n"
"v_mfma_f32_16x16x32_bf16 v[48:51], v[160:163], a[4:7], 0\nR 178,190,36864\nM 48,164,20\nv_mfma_f32_16x16x32_bf16 v[64:67], v[160:163], a[8:11], 0\nR 180,190,40960\nM 64,164,24\n"
"v_mfma_f32_16x16x32_bf16 v[80:83], v[160:163], a[12:15], 0\nR 182,190,45056\nM 80,164,28\nG 4\nv_mfma_f32_16x16x32_bf16 v[84:87], v[168:171], a[12:15], 0\nR 160,191,32768\n"
"M 84,172,28\nv_mfma_f32_16x16x32_bf16 v[68:71], v[168:171], a[8:11], 0\nR 162,191,36864\nM 68,172,24\nv_mfma_f32_16x16x32_bf16 v[52:55], v[168:171], a[4:7], 0\nR 164,191,40960\n"
"M 52,172,20\nv_mfma_f32_16x16x32_bf16 v[36:39], v[168:171], a[0:3], 0\nR 166,191,45056\nM 36,172,16\nG 4\nv_mfma_f32_16x16x32_bf16 v[40:43], v[176:179], a[0:3], 0\n"
"R 168,188,49152\nM 40,180,16\nv_mfma_f32_16x16x32_bf16 v[56:59], v[176:179], a[4:7], 0\nR 170,188,53248\nM 56,180,20\nv_mfma_f32_16x16x32_bf16 v[72:75], v[176:179], a[8:11], 0\n"
"R 172,184,0\nM 72,180,24\nv_mfma_f32_16x16x32_bf16 v[88:91], v[176:179], a[12:15], 0\nR 174,184,4096\nM 88,180,28\nG 4\n"
"v_mfma_f32_16x16x32_bf16 v[92:95], v[160:163], a[12:15], 0\nR 176,189,49152\nM 92,164,28\nv_mfma_f32_16x16x32_bf16 v[76:79], v[160:163], a[8:11], 0\nR 178,189,53248\nM 76,164,24\n"
"v_mfma_f32_16x16x32_bf16 v[60:63], v[160:163], a[4:7], 0\nR 180,185,0\nM 60,164,20\nv_mfma_f32_16x16x32_bf16 v[44:47], v[160:163], a[0:3], 0\nR 182,185,4096\nM 44,164,16\n"
"G 4\nM 32,168,32\nR 160,190,49152\nM 32,172,48\nM 48,168,36\nR 162,190,53248\n"
"M 48,172,52\nM 64,168,40\nR 164,186,0\nM 64,172,56\nM 80,168,44\nR 166,186,4096\n"
"M 80,172,60\nG 4\nM 84,176,44\nR 168,191,49152\nM 84,180,60\nM 68,176,40\n"
"R 170,191,53248\nv_mov_b32 v226, s46\nM 68,180,56\ns_add_u32 m0, s50, 57344\nv_mov_b32 v227, s47\nM 52,176,36\n"
"R 172,187,0\ns_add_u32 s61, s81, 845840\ns_lshl_b32 s62, s44, 5\ns_add_u32 s61, s61, s62\ns_waitcnt vmcnt(0)\ns_mov_b64 exec, 1\n"
"buffer_atomic_swap_x2 v[226:227], off, s[20:23], s61 sc1\ns_mov_b64 exec, -1\nM 52,180,52\ns_add_u32 s63, s56, 393216\nM 36,176,32\nR 174,187,4096\n"
"M 36,180,48\nG 4\nM 40,160,32\nR 176,184,8192\nM 40,164,48\nbuffer_load_dwordx4 v220, s[16:19], s63 offen lds\n"
"M 56,160,36\nR 178,184,12288\nM 56,164,52\nM 72,160,40\nR 180,184,16384\nM 72,164,56\n"
"buffer_load_dwordx4 v221, s[16:19], s63 offen offset:1024 lds\nM 88,160,44\nR 182,184,20480\ns_add_u32 m0, s50, 65536\nM 88,164,60\nG 4\n"
"M 92,168,44\nR 160,185,8192\ns_add_u32 s63, s56, 458752\nM 92,172,60\nbuffer_load_dwordx4 v220, s[16:19], s63 offen lds\nM 76,168,40\n"
"R 162,185,12288\nM 76,172,56\nM 60,168,36\nR 164,185,16384\nM 60,172,52\nbuffer_load_dwordx4 v221, s[16:19], s63 offen offset:1024 lds\n"
"M 44,168,32\nR 166,185,20480\ns_add_u32 m0, s50, 73728\nM 44,172,48\ns_barrier\nG 4\n"
"M 32,176,64\nR 168,186,8192\ns_add_u32 s63, s57, 393216\nM 32,180,80\nbuffer_load_dwordx4 v220, s[16:19], s63 offen lds\nM 48,176,68\n"
"R 170,186,12288\nM 48,180,84\nM 64,176,72\nR 172,186,16384\nM 64,180,88\nbuffer_load_dwordx4 v221, s[16:19], s63 offen offset:1024 lds\n"
"M 80,176,76\nR 174,186,20480\ns_add_u32 s63, s57, 458752\nM 80,180,92\nG 4\nM 84,160,76\n"
"R 176,187,8192\nM 84,164,92\ns_add_u32 m0, s50, 81920\nM 68,160,72\nR 178,187,12288\nM 68,164,88\n"
"buffer_load_dwordx4 v220, s[16:19], s63 offen lds\nM 52,160,68\nR 180,187,16384\nv_mov_b32 v248, 0\nM 52,164,84\nv_mov_b32 v249, 0\n"
"M 36,160,64\nR 182,187,20480\ns_add_u32 s61, s82, 845840\ns_mov_b64 exec, 0xf\nbuffer_atomic_or_x2 v[248:249], v250, s[20:23], s61 offen sc0 sc1\ns_mov_b64 exec, -1\n"
"M 36,164,80\nG 4\nM 40,168,64\nR 160,184,24576\nM 40,172,80\nM 56,168,68\n"
"R 162,184,28672\nM 56,172,84\nbuffer_load_dwordx4 v221, s[16:19], s63 offen offset:1024 lds\nM 72,168,72\nR 164,184,32768\ns_add_u32 s63, s56, 262144\n"
"M 72,172,88\ns_add_u32 m0, s50, 90112\nM 88,168,76\nR 166,184,36864\nM 88,172,92\nG 4\n"
"M 92,176,76\nR 168,185,24576\nM 92,180,92\nbuffer_load_dwordx4 v220, s[16:19], s63 offen lds\nM 76,176,72\nR 170,185,28672\n"
"M 76,180,88\nM 60,176,68\nR 172,185,32768\nM 60,180,84\nbuffer_load_dwordx4 v221, s[16:19], s63 offen offset:1024 lds\nM 44,176,64\n"
"R 174,185,36864\ns_add_u32 m0, s50, 98304\nM 44,180,80\nG 4\nM 32,160,96\nR 176,186,24576\n"
"s_add_u32 s63, s56, 786432\nM 32,164,112\nbuffer_load_dwordx4 v220, s[16:19], s63 offen lds\nM 48,160,100\nR 178,186,28672\nM 48,164,116\n"
"M 64,160,104\nR 180,186,32768\nM 64,164,120\nbuffer_load_dwordx4 v221, s[16:19], s63 offen offset:1024 lds\nM 80,160,108\nR 182,186,36864\n"
"s_add_u32 m0, s50, 106496\nM 80,164,124\nG 4\nM 84,168,108\nR 160,187,24576\ns_add_u32 s63, s56, 327680\n"
"M 84,172,124\nbuffer_load_dwordx4 v220, s[16:19], s63 offen lds\nM 68,168,104\nR 162,187,28672\nM 68,172,120\nM 52,168,100\n"
"R 164,187,32768\nM 52,172,116\nbuffer_load_dwordx4 v221, s[16:19], s63 offen offset:1024 lds\nM 36,168,96\nR 166,187,36864\ns_add_u32 s63, s56, 851968\n"
"M 36,172,112\nG 4\nM 40,176,96\nR 168,184,40960\nM 40,180,112\ns_add_u32 m0, s50, 114688\n"
"M 56,176,100\nR 170,184,45056\nM 56,180,116\nbuffer_load_dwordx4 v220, s[16:19], s63 offen lds\nM 72,176,104\nR 172,184,49152\n"
"M 72,180,120\nM 88,176,108\nR 174,184,53248\ns_waitcnt vmcnt(12)\ns_barrier\nM 88,180,124\n"
"G 4\nM 92,160,108\nR 176,185,40960\nM 92,164,124\ns_add_u32 s64, s68, 37752832\nM 76,160,104\n"
"R 178,185,45056\nM 76,164,120\nbuffer_load_dwordx4 v221, s[16:19], s63 offen offset:1024 lds\nM 60,160,100\nR 180,185,49152\ns_waitcnt vmcnt(9)\n"
"v_cmp_eq_u64 s[86:87], s[46:47], v[248:249]\nM 60,164,116\ns_add_u32 s65, s68, 37867520\nM 44,160,96\nR 182,185,53248\ns_and_b32 s62, s86, 0xf\n"
"s_cmp_eq_u32 s62, 0xf\ns_cbranch_scc0 Lq927\nLq928:\nM 44,164,112\nG 4\nM 32,168,128\n"
"R 160,186,40960\nM 32,172,144\nbuffer_load_dwordx4 a[160:163], v252, s[20:23], s64 offen sc1\nM 48,168,132\nR 162,186,45056\nM 48,172,148\n"
"s_add_u32 s66, s69, 37752832\nM 64,168,136\nR 164,186,49152\nM 64,172,152\nbuffer_load_dwordx4 a[164:167], v252, s[20:23], s65 offen sc1\nM 80,168,140\n"
"R 166,186,53248\nM 80,172,156\nG 4\nM 84,176,140\nR 168,187,40960\nM 84,180,156\n"
"s_add_u32 s67, s69, 37867520\nM 68,176,136\nR 170,187,45056\nM 68,180,152\nbuffer_load_dwordx4 a[168:171], v252, s[20:23], s66 offen sc1\nM 52,176,132\n"
"R 172,187,49152\nM 52,180,148\ns_add_u32 s64, s68, 37760000\nM 36,176,128\nR 174,187,53248\ns_add_u32 m0, s50, 0\n"
"M 36,180,144\nG 4\nM 40,160,128\nR 176,184,57344\ns_add_u32 s63, s57, 262144\nM 40,164,144\n"
"buffer_load_dwordx4 a[172:175], v252, s[20:23], s67 offen sc1\nM 56,160,132\nR 178,184,61440\nM 56,164,148\ns_add_u32 s65, s68, 37874688\nbuffer_load_dwordx4 v220, s[16:19], s63 offen lds\n"
"M 72,160,136\nR 180,188,0\nM 72,164,152\nbuffer_load_dwordx4 a[176:179], v252, s[20:23], s64 offen sc1\nM 88,160,140\nR 182,188,4096\n"
"M 88,164,156\nbuffer_load_dwordx4 v221, s[16:19], s63 offen offset:1024 lds\nG 4\nM 92,168,140\nR 160,185,57344\ns_add_u32 m0, s50, 8192\n"
"M 92,172,156\ns_add_u32 s66, s69, 37760000\nM 76,168,136\nR 162,185,61440\ns_add_u32 s63, s57, 786432\nM 76,172,152\n"
"buffer_load_dwordx4 a[180:183], v252, s[20:23], s65 offen sc1\nbuffer_load_dwordx4 v220, s[16:19], s63 offen lds\nM 60,168,132\nR 164,189,0\nM 60,172,148\ns_add_u32 s67, s69, 37874688\n"
"M 44,168,128\nR 166,189,4096\nM 44,172,144\nbuffer_load_dwordx4 v221, s[16:19], s63 offen offset:1024 lds\nG 4\nM 96,176,128\n"
"R 168,186,57344\ns_add_u32 m0, s50, 16384\nM 96,180,144\nbuffer_load_dwordx4 a[184:187], v252, s[20:23], s66 offen sc1\nM 112,176,132\nR 170,186,61440\n"
"s_add_u32 s63, s57, 327680\nM 112,180,148\ns_add_u32 s64, s68, 37753856\nbuffer_load_dwordx4 v220, s[16:19], s63 offen lds\nM 128,176,136\nR 172,190,0\n"
"M 128,180,152\nbuffer_load_dwordx4 a[188:191], v252, s[20:23], s67 offen sc1\nM 144,176,140\nR 174,190,4096\nM 144,180,156\nbuffer_load_dwordx4 v221, s[16:19], s63 offen offset:1024 lds\n"
"G 4\nM 148,160,140\nR 176,187,57344\ns_waitcnt vmcnt(18)\ns_barrier\nM 148,164,156\n"
"s_add_u32 s65, s68, 37868544\ns_add_u32 s63, s57, 851968\nM 132,160,136\nR 178,187,61440\nLpg31:\ns_cbranch_execz LretO1n\n"
"s_add_u32 m0, s50, 24576\nM 132,164,152\nbuffer_load_dwordx4 a[192:195], v252, s[20:23], s64 offen sc1\nM 116,160,132\nR 180,191,0\nbuffer_load_dwordx4 v220, s[16:19], s63 offen lds\n"
"M 116,164,148\ns_add_u32 s66, s69, 37753856\nM 100,160,128\nR 182,191,4096\nM 100,164,144\nG 4\n"
"M 104,168,128\nR 160,188,8192\nbuffer_load_dwordx4 v221, s[16:19], s63 offen offset:1024 lds\nM 104,172,144\nbuffer_load_dwordx4 a[196:199], v252, s[20:23], s65 offen sc1\nM 120,168,132\n"
"R 162,188,12288\nM 120,172,148\ns_add_u32 s67, s69, 37868544\nM 136,168,136\nR 164,188,16384\nM 136,172,152\n"
"buffer_load_dwordx4 a[200:203], v252, s[20:23], s66 offen sc1\nM 152,168,140\nR 166,188,20480\nM 152,172,156\nG 4\nM 156,176,140\n"
"R 168,189,8192\nM 156,180,156\nbuffer_load_dwordx4 a[204:207], v252, s[20:23], s67 offen sc1\nM 140,176,136\nR 170,189,12288\nM 140,180,152\n"
"s_add_u32 s64, s68, 37761024\nM 124,176,132\nR 172,189,16384\nM 124,180,148\ns_add_u32 s65, s68, 37875712\nM 108,176,128\n"
"R 174,189,20480\nM 108,180,144\nG 4\nM 0,160,128\nR 176,190,8192\nM 0,164,144\n"
"buffer_load_dwordx4 a[208:211], v252, s[20:23], s64 offen sc1\nM 16,160,132\nR 178,190,12288\nM 16,164,148\ns_add_u32 s66, s69, 37761024\nv_mfma_f32_16x16x32_bf16 a[224:227], v[160:163], a[136:139], a[224:227]\n"
"R 180,190,16384\nv_mfma_f32_16x16x32_bf16 a[224:227], v[164:167], a[152:155], a[224:227]\nbuffer_load_dwordx4 a[212:215], v252, s[20:23], s65 offen sc1\nv_mfma_f32_16x16x32_bf16 a[240:243], v[160:163], a[140:143], a[240:243]\nR 182,190,20480\nv_mfma_f32_16x16x32_bf16 a[240:243], v[164:167], a[156:159], a[240:243]\n"
"G 4\nv_mfma_f32_16x16x32_bf16 a[244:247], v[168:171], a[140:143], a[244:247]\nR 160,191,8192\nv_mfma_f32_16x16x32_bf16 a[244:247], v[172:175], a[156:159], a[244:247]\nbuffer_load_dwordx4 a[216:219], v252, s[20:23], s66 offen sc1\nv_mfma_f32_16x16x32_bf16 a[228:231], v[168:171], a[136:139], a[228:231]\n"
"R 162,191,12288\nv_mfma_f32_16x16x32_bf16 a[228:231], v[172:175], a[152:155], a[228:231]\ns_add_u32 s67, s69, 37875712\nM 20,168,132\nR 164,191,16384\nM 20,172,148\n"
"M 4,168,128\nR 166,191,20480\nM 4,172,144\nG 4\nM 8,176,128\nR 168,188,24576\n"
"M 8,180,144\nbuffer_load_dwordx4 a[220:223], v252, s[20:23], s67 offen sc1\nM 24,176,132\nR 170,188,28672\nM 24,180,148\ns_add_u32 m0, s50, 32768\n"
"v_mfma_f32_16x16x32_bf16 a[232:235], v[176:179], a[136:139], a[232:235]\nR 172,188,32768\nv_mfma_f32_16x16x32_bf16 a[232:235], v[180:183], a[152:155], a[232:235]\ns_add_u32 s63, s58, 262144\nv_mfma_f32_16x16x32_bf16 a[248:251], v[176:179], a[140:143], a[248:251]\nR 174,188,36864\n"
"v_mfma_f32_16x16x32_bf16 a[248:251], v[180:183], a[156:159], a[248:251]\nG 4\nv_mfma_f32_16x16x32_bf16 a[252:255], v[160:163], a[140:143], a[252:255]\nR 176,189,24576\nv_mfma_f32_16x16x32_bf16 a[252:255], v[164:167], a[156:159], a[252:255]\nbuffer_load_dwordx4 v220, s[16:19], s63 offen lds\n"
"v_mfma_f32_16x16x32_bf16 a[236:239], v[160:163], a[136:139], a[236:239]\nR 178,189,28672\nv_mfma_f32_16x16x32_bf16 a[236:239], v[164:167], a[152:155], a[236:239]\nM 28,160,132\nR 180,189,32768\nM 28,164,148\n"
"buffer_load_dwordx4 v221, s[16:19], s63 offen offset:1024 lds\nM 12,160,128\nR 182,189,36864\nM 12,164,144\ns_waitcnt vmcnt(9)\ns_barrier\n"
"G 4\nM 96,168,160\nR 160,190,24576\nM 96,172,176\ns_add_u32 m0, s50, 40960\nM 112,168,164\n"
"R 162,190,28672\nM 112,172,180\ns_add_u32 s63, s58, 786432\nM 128,168,168\nR 164,190,32768\nv_lshrrev_b32 v230, 8, v252\n"
"M 128,172,184\nbuffer_load_dwordx4 v220, s[16:19], s63 offen lds\nM 144,168,172\nR 166,190,36864\nv_lshlrev_b32 v230, 3, v230\nM 144,172,188\n"
"G 4\nM 148,176,172\nR 168,191,24576\nM 148,180,188\nbuffer_load_dwordx2 v[232:233], v230, s[36:39], s53 offen offset:0\nM 132,176,168\n"
"R 170,191,28672\nM 132,180,184\nbuffer_load_dwordx2 v[234:235], v230, s[36:39], s53 offen offset:32\nM 116,176,164\nR 172,191,32768\nM 116,180,180\n"
"buffer_load_dwordx4 v221, s[16:19], s63 offen offset:1024 lds\nM 100,176,160\nR 174,191,36864\nM 100,180,176\nG 4\nM 104,160,160\n"
"R 176,188,40960\nM 104,164,176\ns_add_u32 m0, s50, 49152\nM 120,160,164\nR 178,188,45056\nM 120,164,180\n"
"s_add_u32 s63, s58, 327680\nM 136,160,168\nR 180,188,49152\nbuffer_load_dwordx2 v[236:237], v230, s[36:39], s53 offen offset:64\nM 136,164,184\nbuffer_load_dwordx4 v220, s[16:19], s63 offen lds\n"
"M 152,160,172\nR 182,188,53248\nM 152,164,188\nbuffer_load_dwordx2 v[238:239], v230, s[36:39], s53 offen offset:96\nG 4\nM 156,168,172\n"
"R 160,189,40960\nv_bfe_u32 v242, v252, 4, 4\nM 156,172,188\nv_lshrrev_b32 v243, 8, v252\nv_lshlrev_b32 v243, 2, v243\nM 140,168,168\n"
"R 162,189,45056\nv_sub_u32 v242, v242, v243\nM 140,172,184\nv_lshlrev_b32 v243, 4, v242\ns_mov_b64 s[60:61], 0x3f80\nv_lshlrev_b64 v[208:209], v243, s[60:61]\n"
"M 124,168,164\nR 164,189,49152\nM 124,172,180\nbuffer_load_dwordx4 v221, s[16:19], s63 offen offset:1024 lds\nM 108,168,160\nR 166,189,53248\n"
"M 108,172,176\nG 4\nM 96,176,192\nR 168,190,40960\ns_waitcnt vmcnt(13)\nM 96,180,208\n"
"s_add_u32 m0, s50, 57344\nM 112,176,196\nR 170,190,45056\ns_waitcnt vmcnt(12)\nM 112,180,212\ns_add_u32 s63, s58, 851968\n"
"M 128,176,200\nR 172,190,49152\ns_waitcnt vmcnt(11)\nM 128,180,216\nbuffer_load_dwordx4 v220, s[16:19], s63 offen lds\nM 144,176,204\n"
"R 174,190,53248\ns_waitcnt vmcnt(11)\nM 144,180,220\nG 4\nM 148,160,204\nR 176,191,40960\n"
"M 148,164,220\nv_cmp_gt_u32 s[60:61], 4, v242\ns_nop 1\nv_cndmask_b32 v212, 0, v208, s[60:61]\nv_cndmask_b32 v208, 0, v208, s[60:61]\nv_cndmask_b32 v213, 0, v209, s[60:61]\n"
"v_cndmask_b32 v209, 0, v209, s[60:61]\nM 132,160,200\nR 178,191,45056\nM 132,164,216\nM 116,160,196\nR 180,191,49152\n"
"M 116,164,212\nbuffer_load_dwordx4 v221, s[16:19], s63 offen offset:1024 lds\nM 100,160,192\nR 182,191,53248\nM 100,164,208\nG 4\n"
"M 104,168,192\nR 160,184,0\nM 104,172,208\ns_add_u32 m0, s50, 65536\nM 120,168,196\nR 162,184,4096\n"
"M 120,172,212\ns_add_u32 s63, s59, 0\nM 136,168,200\nR 164,184,8192\nM 136,172,216\nbuffer_load_dwordx4 v220, s[16:19], s63 offen lds\n"
"M 152,168,204\nR 166,184,12288\nM 152,172,220\nG 4\nM 156,176,204\nR 168,185,0\n"
"M 156,180,220\nM 140,176,200\nR 170,185,4096\nM 140,180,216\nbuffer_load_dwordx4 v221, s[16:19], s63 offen offset:1024 lds\nM 124,176,196\n"
"R 172,185,8192\nM 124,180,212\nM 108,176,192\nR 174,185,12288\nM 108,180,208\nG 4\n"
"M 0,160,160\nR 176,186,0\nM 0,164,176\ns_mov_b32 m0, s51\nM 16,160,164\nR 178,186,4096\n"
"v_cvt_pk_bf16_f32 v128, v128, v129\nv_cvt_pk_bf16_f32 v129, v130, v131\nM 16,164,180\nds_write_addtid_b32 v128 offset:0\nv_cvt_pk_bf16_f32 v130, v132, v133\nv_cvt_pk_bf16_f32 v131, v134, v135\n"
"v_mfma_f32_16x16x32_bf16 a[224:227], v[160:163], a[168:171], a[224:227]\nR 180,186,8192\nv_cvt_pk_bf16_f32 v132, v136, v137\nv_mfma_f32_16x16x32_bf16 a[224:227], v[164:167], a[184:187], a[224:227]\nds_write_addtid_b32 v129 offset:256\nv_cvt_pk_bf16_f32 v133, v138, v139\n"
"v_mfma_f32_16x16x32_bf16 a[240:243], v[160:163], a[172:175], a[240:243]\nR 182,186,12288\nv_cvt_pk_bf16_f32 v134, v140, v141\nv_mfma_f32_16x16x32_bf16 a[240:243], v[164:167], a[188:191], a[240:243]\nds_write_addtid_b32 v130 offset:512\nG 7\n"
"v_cvt_pk_bf16_f32 v135, v142, v143\nv_mfma_f32_16x16x32_bf16 a[244:247], v[168:171], a[172:175], a[244:247]\nR 160,187,0\nv_cvt_pk_bf16_f32 v136, v144, v145\nv_mfma_f32_16x16x32_bf16 a[244:247], v[172:175], a[188:191], a[244:247]\nds_write_addtid_b32 v131 offset:768\n"
"v_cvt_pk_bf16_f32 v137, v146, v147\nv_mfma_f32_16x16x32_bf16 a[228:231], v[168:171], a[168:171], a[228:231]\nR 162,187,4096\nv_cvt_pk_bf16_f32 v138, v148, v149\nv_mfma_f32_16x16x32_bf16 a[228:231], v[172:175], a[184:187], a[228:231]\nds_write_addtid_b32 v132 offset:1024\n"
"v_cvt_pk_bf16_f32 v139, v150, v151\nM 20,168,164\nR 164,187,8192\nv_cvt_pk_bf16_f32 v140, v152, v153\nM 20,172,180\nds_write_addtid_b32 v133 offset:1280\n"
"v_cvt_pk_bf16_f32 v141, v154, v155\nM 4,168,160\nR 166,187,12288\nv_cvt_pk_bf16_f32 v142, v156, v157\nM 4,172,176\nds_write_addtid_b32 v134 offset:1536\n"
"G 9\nv_cvt_pk_bf16_f32 v143, v158, v159\nM 8,176,160\nR 168,184,16384\nds_write_addtid_b32 v135 offset:1792\nM 8,180,176\n"
"ds_write_addtid_b32 v136 offset:2048\nds_write_addtid_b32 v137 offset:2304\nM 24,176,164\nR 170,184,20480\nds_write_addtid_b32 v138 offset:2560\nM 24,180,180\n"
"ds_write_addtid_b32 v139 offset:2816\nds_write_addtid_b32 v140 offset:3072\nv_mfma_f32_16x16x32_bf16 a[232:235], v[176:179], a[168:171], a[232:235]\nR 172,184,24576\nds_write_addtid_b32 v141 offset:3328\nv_mfma_f32_16x16x32_bf16 a[232:235], v[180:183], a[184:187], a[232:235]\n"
"ds_write_addtid_b32 v142 offset:3584\nds_write_addtid_b32 v143 offset:3840\nv_mfma_f32_16x16x32_bf16 a[248:251], v[176:179], a[172:175], a[248:251]\nR 174,184,28672\ns_add_u32 m0, s50, 73728\nv_mfma_f32_16x16x32_bf16 a[248:251], v[180:183], a[188:191], a[248:251]\n"
"s_add_u32 s63, s59, 524288\nG 14\nv_mfma_f32_16x16x32_bf16 a[252:255], v[160:163], a[172:175], a[252:255]\nR 176,185,16384\ns_waitcnt vmcnt(8) lgkmcnt(2)\ns_barrier\n"
"v_mfma_f32_16x16x32_bf16 a[252:255], v[164:167], a[188:191], a[252:255]\nbuffer_load_dwordx4 v220, s[16:19], s63 offen lds\nv_mfma_f32_16x16x32_bf16 a[236:239], v[160:163], a[168:171], a[236:239]\nR 178,185,20480\nv_mfma_f32_16x16x32_bf16 a[236:239], v[164:167], a[184:187], a[236:239]\nds_read2st64_b32 v[128:129], v225 offset0:0 offset1:1\n"
"M 28,160,164\nR 180,185,24576\nM 28,164,180\nbuffer_load_dwordx4 v221, s[16:19], s63 offen offset:1024 lds\nM 12,160,160\nR 182,185,28672\n"
"s_add_u32 m0, s50, 81920\nM 12,164,176\nds_read2st64_b32 v[130:131], v225 offset0:2 offset1:3\nG 6\nM 0,168,192\nR 160,186,16384\n"
"s_add_u32 s63, s59, 65536\nM 0,172,208\nbuffer_load_dwordx4 v220, s[16:19], s63 offen lds\nds_read2st64_b32 v[132:133], v225 offset0:4 offset1:5\nM 16,168,196\nR 162,186,20480\n"
"ds_read2st64_b32 v[134:135], v225 offset0:6 offset1:7\nM 16,172,212\nds_read2st64_b32 v[136:137], v225 offset0:8 offset1:9\nv_mfma_f32_16x16x32_bf16 a[224:227], v[168:171], a[200:203], a[224:227]\nR 164,186,24576\nv_mfma_f32_16x16x32_bf16 a[224:227], v[172:175], a[216:219], a[224:227]\n"
"buffer_load_dwordx4 v221, s[16:19], s63 offen offset:1024 lds\nG 6\nv_mfma_f32_16x16x32_bf16 v[96:99], v[208:211], v[128:131], v[96:99]\nv_mfma_f32_16x16x32_bf16 v[100:103], v[210:213], v[128:131], v[100:103]\nds_read2st64_b32 v[138:139], v225 offset0:10 offset1:11\nv_mfma_f32_16x16x32_bf16 a[240:243], v[168:171], a[204:207], a[240:243]\n"
"R 166,186,28672\ns_add_u32 m0, s50, 90112\nv_mfma_f32_16x16x32_bf16 a[240:243], v[172:175], a[220:223], a[240:243]\nds_read2st64_b32 v[140:141], v225 offset0:12 offset1:13\nv_mfma_f32_16x16x32_bf16 a[244:247], v[176:179], a[204:207], a[244:247]\nR 168,187,16384\n"
"s_add_u32 s63, s59, 589824\nG 6\nv_mfma_f32_16x16x32_bf16 v[104:107], v[208:211], v[132:135], v[104:107]\nv_mfma_f32_16x16x32_bf16 v[108:111], v[210:213], v[132:135], v[108:111]\nds_read2st64_b32 v[142:143], v225 offset0:14 offset1:15\nv_mfma_f32_16x16x32_bf16 a[244:247], v[180:183], a[220:223], a[244:247]\n"
"buffer_load_dwordx4 v220, s[16:19], s63 offen lds\nv_mfma_f32_16x16x32_bf16 a[228:231], v[176:179], a[200:203], a[228:231]\nR 170,187,20480\nv_mfma_f32_16x16x32_bf16 a[228:231], v[180:183], a[216:219], a[228:231]\nG 5\nv_mfma_f32_16x16x32_bf16 v[112:115], v[208:211], v[136:139], v[112:115]\n"
"v_mfma_f32_16x16x32_bf16 v[116:119], v[210:213], v[136:139], v[116:119]\nM 20,176,196\nR 172,187,24576\nM 20,180,212\nbuffer_load_dwordx4 v221, s[16:19], s63 offen offset:1024 lds\nM 4,176,192\n"
"R 174,187,28672\ns_add_u32 s63, s59, 131072\nG 3\nv_mfma_f32_16x16x32_bf16 v[120:123], v[208:211], v[140:143], v[120:123]\nv_mfma_f32_16x16x32_bf16 v[124:127], v[210:213], v[140:143], v[124:127]\nM 4,180,208\n"
"M 8,160,192\nR 176,184,32768\nM 8,164,208\ns_add_u32 m0, s50, 98304\nM 24,160,196\nR 178,184,36864\n"
"M 24,164,212\nbuffer_load_dwordx4 v220, s[16:19], s63 offen lds\nv_mfma_f32_16x16x32_bf16 a[232:235], v[160:163], a[200:203], a[232:235]\nR 180,184,40960\nv_cvt_pk_bf16_f32 v120, v120, v121\nv_cvt_pk_bf16_f32 v112, v112, v113\n"
"v_cvt_pk_bf16_f32 v121, v122, v123\nv_mfma_f32_16x16x32_bf16 a[232:235], v[164:167], a[216:219], a[232:235]\nv_cvt_pk_bf16_f32 v122, v124, v125\nv_cvt_pk_bf16_f32 v123, v126, v127\nv_cvt_pk_bf16_f32 v96, v96, v97\nv_cvt_pk_bf16_f32 v113, v114, v115\n"
"v_mfma_f32_16x16x32_bf16 a[248:251], v[160:163], a[204:207], a[248:251]\nR 182,184,45056\nv_cvt_pk_bf16_f32 v114, v116, v117\nv_cvt_pk_bf16_f32 v97, v98, v99\nv_mfma_f32_16x16x32_bf16 a[248:251], v[164:167], a[220:223], a[248:251]\nG 4\n"
"v_cvt_pk_bf16_f32 v98, v100, v101\nv_cvt_pk_bf16_f32 v99, v102, v103\nv_mfma_f32_16x16x32_bf16 a[252:255], v[168:171], a[204:207], a[252:255]\nR 160,185,32768\ns_bfe_u32 s100, s56, 0x20007\ns_mul_i32 s100, s100, 49152\n"
"s_add_u32 s100, s100, s72\ns_add_u32 s100, s100, 65011712\nv_cvt_pk_bf16_f32 v115, v118, v119\nv_mfma_f32_16x16x32_bf16 a[252:255], v[172:175], a[220:223], a[252:255]\nbuffer_store_dwordx4 v[96:99], v252, s[20:23], s100 offen offset:0 sc1\nv_mfma_f32_16x16x32_bf16 a[236:239], v[168:171], a[200:203], a[236:239]\n"
"R 162,185,36864\nv_cvt_pk_bf16_f32 v104, v104, v105\nv_mfma_f32_16x16x32_bf16 a[236:239], v[172:175], a[216:219], a[236:239]\nv_cvt_pk_bf16_f32 v105, v106, v107\nv_cvt_pk_bf16_f32 v106, v108, v109\nM 28,168,196\n"
"R 164,185,40960\nv_cvt_pk_bf16_f32 v107, v110, v111\nM 28,172,212\nbuffer_load_dwordx4 v221, s[16:19], s63 offen offset:1024 lds\nM 12,168,192\nR 166,185,45056\n"
"M 12,172,208\nbuffer_store_dwordx4 v[104:107], v252, s[20:23], s100 offen offset:1024 sc1\ns_waitcnt vmcnt(12)\ns_barrier\nG 4\nM 32,176,160\n"
"R 168,186,32768\nM 32,180,176\nv_xor_b32 v240, 0x2000, v225\nM 48,176,164\nR 170,186,36864\nds_write2st64_b32 v240, a224, a225 offset0:0 offset1:1\n"
"M 48,180,180\nbuffer_store_dwordx4 v[112:115], v252, s[20:23], s100 offen offset:2048 sc1\nds_write2st64_b32 v240, a226, a227 offset0:2 offset1:3\nM 64,176,168\nR 172,186,40960\nds_write2st64_b32 v240, a228, a229 offset0:4 offset1:5\n"
"M 64,180,184\nds_write2st64_b32 v240, a230, a231 offset0:6 offset1:7\nM 80,176,172\nR 174,186,45056\nds_write2st64_b32 v240, a232, a233 offset0:8 offset1:9\nM 80,180,188\n"
"ds_write2st64_b32 v240, a234, a235 offset0:10 offset1:11\nG 10\nM 84,160,172\nR 176,187,32768\ns_add_u32 s63, s59, 655360\nds_write2st64_b32 v240, a236, a237 offset0:12 offset1:13\n"
"M 84,164,188\nbuffer_store_dwordx4 v[120:123], v252, s[20:23], s100 offen offset:3072 sc1\nds_write2st64_b32 v240, a238, a239 offset0:14 offset1:15\nM 68,160,168\nR 178,187,36864\nds_write2st64_b32 v240, a240, a241 offset0:16 offset1:17\n"
"M 68,164,184\ns_add_u32 m0, s50, 106496\nds_write2st64_b32 v240, a242, a243 offset0:18 offset1:19\nM 52,160,164\nR 180,187,40960\nds_write2st64_b32 v240, a244, a245 offset0:20 offset1:21\n"
"M 52,164,180\nbuffer_load_dwordx4 v220, s[16:19], s63 offen lds\nds_write2st64_b32 v240, a246, a247 offset0:22 offset1:23\nM 36,160,160\nR 182,187,45056\nds_write2st64_b32 v240, a248, a249 offset0:24 offset1:25\n"
"M 36,164,176\nds_write2st64_b32 v240, a250, a251 offset0:26 offset1:27\nG 14\nM 40,168,160\nR 160,184,49152\nds_write2st64_b32 v240, a252, a253 offset0:28 offset1:29\n"
"M 40,172,176\nbuffer_load_dwordx4 v221, s[16:19], s63 offen offset:1024 lds\nM 56,168,164\nR 162,184,53248\ns_add_u32 s63, s59, 196608\nM 56,172,180\n"
"s_add_u32 m0, s50, 114688\nds_write2st64_b32 v240, a254, a255 offset0:30 offset1:31\nM 72,168,168\nR 164,184,57344\nM 72,172,184\nbuffer_load_dwordx4 v220, s[16:19], s63 offen lds\n"
"M 88,168,172\nR 166,184,61440\nM 88,172,188\nG 8\nM 92,176,172\nR 168,185,49152\n"
"s_waitcnt vmcnt(13) lgkmcnt(3)\ns_barrier\nM 92,180,188\nbuffer_load_dwordx4 v221, s[16:19], s63 offen offset:1024 lds\nds_read2st64_b32 v[128:129], v225 offset0:0 offset1:1\nM 76,176,168\n"
"R 170,185,53248\ns_add_u32 s63, s59, 720896\nds_read2st64_b32 v[130:131], v225 offset0:2 offset1:3\nM 76,180,184\ns_add_u32 m0, s50, 0\nds_read2st64_b32 v[132:133], v225 offset0:4 offset1:5\n"
"ds_read2st64_b32 v[134:135], v225 offset0:6 offset1:7\nM 60,176,164\nR 172,185,57344\nds_read2st64_b32 v[136:137], v225 offset0:8 offset1:9\nM 60,180,180\nbuffer_load_dwordx4 v220, s[16:19], s63 offen lds\n"
"ds_read2st64_b32 v[138:139], v225 offset0:10 offset1:11\nM 44,176,160\nR 174,185,61440\nLpg32:\ns_cbranch_execz LretW1z\nds_read2st64_b32 v[140:141], v225 offset0:12 offset1:13\n"
"M 44,180,176\nds_read2st64_b32 v[142:143], v225 offset0:14 offset1:15\nds_read2st64_b32 v[144:145], v225 offset0:16 offset1:17\nG 13\nM 32,160,192\nR 176,186,49152\n"
"ds_read2st64_b32 v[146:147], v225 offset0:18 offset1:19\nG 13\nv_add_f32 v0, v0, v128\nM 32,164,208\nbuffer_load_dwordx4 v221, s[16:19], s63 offen offset:1024 lds\nds_read2st64_b32 v[148:149], v225 offset0:20 offset1:21\n"
"M 48,160,196\nR 178,186,53248\ns_add_u32 s63, s59, 393216\nds_read2st64_b32 v[150:151], v225 offset0:22 offset1:23\nM 48,164,212\ns_add_u32 m0, s50, 8192\n"
"ds_read2st64_b32 v[152:153], v225 offset0:24 offset1:25\nds_read2st64_b32 v[154:155], v225 offset0:26 offset1:27\nM 64,160,200\nR 180,186,57344\nds_read2st64_b32 v[156:157], v225 offset0:28 offset1:29\nv_add_f32 v1, v1, v129\n"
"M 64,164,216\nbuffer_load_dwordx4 v220, s[16:19], s63 offen lds\nds_read2st64_b32 v[158:159], v225 offset0:30 offset1:31\nM 80,160,204\nR 182,186,61440\nG 15\n"
"v_add_f32 v2, v2, v130\nv_add_f32 v3, v3, v131\nM 80,164,220\nG 14\nv_add_f32 v4, v4, v132\nv_add_f32 v5, v5, v133\n"
"v_add_f32 v6, v6, v134\nM 84,168,204\nR 160,187,49152\nv_add_f32 v7, v7, v135\nv_add_f32 v8, v8, v136\nM 84,172,220\n"
"buffer_load_dwordx4 v221, s[16:19], s63 offen offset:1024 lds\nv_add_f32 v9, v9, v137\nM 68,168,200\nR 162,187,53248\ns_add_u32 s63, s59, 458752\nv_add_f32 v10, v10, v138\n"
"M 68,172,216\ns_add_u32 m0, s50, 16384\nv_add_f32 v11, v11, v139\nG 15\nv_add_f32 v12, v12, v140\nv_add_f32 v13, v13, v141\n"
"M 52,168,196\nR 164,187,57344\nG 15\nv_add_f32 v14, v14, v142\nv_add_f32 v15, v15, v143\nM 52,172,212\n"
"buffer_load_dwordx4 v220, s[16:19], s63 offen lds\nG 14\nv_add_f32 v16, v16, v144\nM 36,168,192\nR 166,187,61440\nv_add_f32 v17, v17, v145\n"
"G 13\nv_add_f32 v18, v18, v146\nM 36,172,208\nG 4\nv_add_f32 v19, v19, v147\nv_add_f32 v20, v20, v148\n"
"v_add_f32 v21, v21, v149\nM 40,176,192\nR 168,188,0\nv_add_f32 v22, v22, v150\nv_add_f32 v23, v23, v151\nM 40,180,208\n"
"buffer_load_dwordx4 v221, s[16:19], s63 offen offset:1024 lds\nv_add_f32 v24, v24, v152\nM 56,176,196\nR 170,188,4096\ns_add_u32 s63, s59, 262144\nv_add_f32 v25, v25, v153\n"
"M 56,180,212\ns_add_u32 m0, s50, 24576\nv_add_f32 v26, v26, v154\nv_add_f32 v27, v27, v155\nv_add_f32 v28, v28, v156\nv_add_f32 v29, v29, v157\n"
"v_add_f32 v30, v30, v158\nv_add_f32 v31, v31, v159\nM 72,176,200\nR 172,188,8192\nv_cvt_pk_bf16_f32 v24, v24, v25\nv_cvt_pk_bf16_f32 v0, v0, v1\n"
"v_cvt_pk_bf16_f32 v8, v8, v9\nv_cvt_pk_bf16_f32 v16, v16, v17\nv_cvt_pk_bf16_f32 v25, v26, v27\nM 72,180,216\nbuffer_load_dwordx4 v220, s[16:19], s63 offen lds\nv_cvt_pk_bf16_f32 v9, v10, v11\n"
"v_cvt_pk_bf16_f32 v17, v18, v19\nv_cvt_pk_bf16_f32 v26, v28, v29\nM 88,176,204\nR 174,188,12288\nv_cvt_pk_bf16_f32 v27, v30, v31\nv_cvt_pk_bf16_f32 v1, v2, v3\n"
"M 88,180,220\nG 4\nv_cvt_pk_bf16_f32 v2, v4, v5\nv_cvt_pk_bf16_f32 v3, v6, v7\nM 92,160,204\nR 176,189,0\n"
"s_bfe_u32 s100, s57, 0x20007\ns_mul_i32 s100, s100, 49152\ns_add_u32 s100, s100, s72\ns_add_u32 s100, s100, 65028096\nv_cvt_pk_bf16_f32 v10, v12, v13\nM 92,164,220\n"
"buffer_store_dwordx4 v[0:3], v252, s[20:23], s100 offen offset:0 sc1\nM 76,160,200\nR 178,189,4096\nv_cvt_pk_bf16_f32 v11, v14, v15\nM 76,164,216\nbuffer_load_dwordx4 v221, s[16:19], s63 offen offset:1024 lds\n"
"M 60,160,196\nR 180,189,8192\ns_add_u32 m0, s50, 32768\ns_add_u32 s63, s59, 786432\nM 60,164,212\nbuffer_load_dwordx4 v220, s[16:19], s63 offen lds\n"
"M 44,160,192\nR 182,189,12288\nv_cvt_pk_bf16_f32 v18, v20, v21\nM 44,164,208\nbuffer_store_dwordx4 v[8:11], v252, s[20:23], s100 offen offset:1024 sc1\ns_waitcnt vmcnt(21)\n"
"s_barrier\nG 4\nv_mfma_f32_16x16x32_bf16 v[96:99], v[168:171], a[0:3], 0\nR 160,190,0\nv_cvt_pk_bf16_f32 v19, v22, v23\nM 96,172,16\n"
"s_mov_b32 m0, s51\nv_mfma_f32_16x16x32_bf16 v[112:115], v[168:171], a[4:7], 0\nR 162,190,4096\nv_cvt_pk_bf16_f32 v64, v64, v65\nv_cvt_pk_bf16_f32 v65, v66, v67\nM 112,172,20\n"
"buffer_store_dwordx4 v[16:19], v252, s[20:23], s100 offen offset:2048 sc1\nv_cvt_pk_bf16_f32 v66, v68, v69\nv_mfma_f32_16x16x32_bf16 v[128:131], v[168:171], a[8:11], 0\nR 164,190,8192\nv_cvt_pk_bf16_f32 v67, v70, v71\nv_cvt_pk_bf16_f32 v68, v72, v73\n"
"M 128,172,24\nds_write_addtid_b32 v64 offset:0\nv_cvt_pk_bf16_f32 v69, v74, v75\nv_cvt_pk_bf16_f32 v70, v76, v77\nv_mfma_f32_16x16x32_bf16 v[144:147], v[168:171], a[12:15], 0\nR 166,190,12288\n"
"v_cvt_pk_bf16_f32 v71, v78, v79\nv_cvt_pk_bf16_f32 v72, v80, v81\nM 144,172,28\nds_write_addtid_b32 v65 offset:256\nG 6\nv_cvt_pk_bf16_f32 v73, v82, v83\n"
"v_mfma_f32_16x16x32_bf16 v[148:151], v[176:179], a[12:15], 0\nR 168,191,0\nv_cvt_pk_bf16_f32 v74, v84, v85\nv_cvt_pk_bf16_f32 v75, v86, v87\nM 148,180,28\nbuffer_store_dwordx4 v[24:27], v252, s[20:23], s100 offen offset:3072 sc1\n"
"v_mfma_f32_16x16x32_bf16 v[132:135], v[176:179], a[8:11], 0\nR 170,191,4096\nv_cvt_pk_bf16_f32 v76, v88, v89\nM 132,180,24\nds_write_addtid_b32 v66 offset:512\nv_cvt_pk_bf16_f32 v77, v90, v91\n"
"v_mfma_f32_16x16x32_bf16 v[116:119], v[176:179], a[4:7], 0\nR 172,191,8192\nv_cvt_pk_bf16_f32 v78, v92, v93\nM 116,180,20\nds_write_addtid_b32 v67 offset:768\nv_cvt_pk_bf16_f32 v79, v94, v95\n"
"v_mfma_f32_16x16x32_bf16 v[100:103], v[176:179], a[0:3], 0\nR 174,191,12288\nds_write_addtid_b32 v68 offset:1024\nM 100,180,16\nds_write_addtid_b32 v69 offset:1280\nds_write_addtid_b32 v70 offset:1536\n"
"G 10\nv_mfma_f32_16x16x32_bf16 v[104:107], v[160:163], a[0:3], 0\nR 176,188,16384\nds_write_addtid_b32 v71 offset:1792\nM 104,164,16\nds_write_addtid_b32 v72 offset:2048\n"
"ds_write_addtid_b32 v73 offset:2304\nv_mfma_f32_16x16x32_bf16 v[120:123], v[160:163], a[4:7], 0\nR 178,188,20480\nds_write_addtid_b32 v74 offset:2560\nM 120,164,20\nds_write_addtid_b32 v75 offset:2816\n"
"ds_write_addtid_b32 v76 offset:3072\nv_mfma_f32_16x16x32_bf16 v[136:139], v[160:163], a[8:11], 0\nR 180,188,24576\nds_write_addtid_b32 v77 offset:3328\nM 136,164,24\nds_write_addtid_b32 v78 offset:3584\n"
"ds_write_addtid_b32 v79 offset:3840\nv_mfma_f32_16x16x32_bf16 v[152:155], v[160:163], a[12:15], 0\nR 182,188,28672\nM 152,164,28\nG 15\nv_mfma_f32_16x16x32_bf16 v[156:159], v[168:171], a[12:15], 0\n"
"R 160,189,16384\ns_waitcnt vmcnt(15) lgkmcnt(2)\ns_barrier\nM 156,172,28\nds_read2st64_b32 v[64:65], v225 offset0:0 offset1:1\nv_mfma_f32_16x16x32_bf16 v[140:143], v[168:171], a[8:11], 0\n"
"R 162,189,20480\nM 140,172,24\nds_read2st64_b32 v[66:67], v225 offset0:2 offset1:3\nv_mfma_f32_16x16x32_bf16 v[124:127], v[168:171], a[4:7], 0\nR 164,189,24576\nM 124,172,20\n"
"ds_read2st64_b32 v[68:69], v225 offset0:4 offset1:5\nv_mfma_f32_16x16x32_bf16 v[108:111], v[168:171], a[0:3], 0\nR 166,189,28672\nM 108,172,16\nds_read2st64_b32 v[70:71], v225 offset0:6 offset1:7\nG 8\n"
"M 96,176,32\nR 168,190,16384\nv_bfe_u32 v250, v252, 4, 2\nv_lshlrev_b32 v250, 3, v250\nM 96,180,48\ns_lshl_b32 s60, s43, 5\n"
"v_add_u32 v250, s60, v250\nG 5\nv_mfma_f32_16x16x32_bf16 v[32:35], v[208:211], v[64:67], v[32:35]\nv_mfma_f32_16x16x32_bf16 v[36:39], v[210:213], v[64:67], v[36:39]\nM 112,176,36\nR 170,190,20480\n"
"M 112,180,52\nds_read2st64_b32 v[72:73], v225 offset0:8 offset1:9\nM 128,176,40\nR 172,190,24576\nM 128,180,56\nds_read2st64_b32 v[74:75], v225 offset0:10 offset1:11\n"
"G 5\nv_mfma_f32_16x16x32_bf16 v[40:43], v[208:211], v[68:71], v[40:43]\nv_mfma_f32_16x16x32_bf16 v[44:47], v[210:213], v[68:71], v[44:47]\nM 144,176,44\nR 174,190,28672\nM 144,180,60\n"
"ds_read2st64_b32 v[76:77], v225 offset0:12 offset1:13\nM 148,160,44\nR 176,191,16384\nds_read2st64_b32 v[78:79], v225 offset0:14 offset1:15\nM 148,164,60\nM 132,160,40\n"
"R 178,191,20480\nM 132,164,56\nG 5\nv_mfma_f32_16x16x32_bf16 v[48:51], v[208:211], v[72:75], v[48:51]\nv_mfma_f32_16x16x32_bf16 v[52:55], v[210:213], v[72:75], v[52:55]\nM 116,160,36\n"
"R 180,191,24576\nM 116,164,52\nM 100,160,32\nR 182,191,28672\nG 3\nv_mfma_f32_16x16x32_bf16 v[56:59], v[208:211], v[76:79], v[56:59]\n"
"v_mfma_f32_16x16x32_bf16 v[60:63], v[210:213], v[76:79], v[60:63]\nM 100,164,48\nM 104,168,32\nR 160,188,32768\nM 104,172,48\nM 120,168,36\n"
"R 162,188,36864\nv_mov_b32 v226, s46\nM 120,172,52\ns_add_u32 m0, s50, 32768\nv_mov_b32 v227, s47\nM 136,168,40\n"
"R 164,188,40960\ns_bfe_u32 s62, s56, 0x20007\ns_lshl_b32 s62, s62, 5\ns_add_u32 s61, s74, 1009664\ns_add_u32 s61, s61, s62\ns_waitcnt vmcnt(0)\n"
"s_mov_b64 exec, 1\nbuffer_atomic_swap_x2 v[226:227], off, s[20:23], s61 sc1\ns_mov_b64 exec, -1\nv_cvt_pk_bf16_f32 v32, v32, v33\nv_cvt_pk_bf16_f32 v48, v48, v49\nv_cvt_pk_bf16_f32 v56, v56, v57\n"
"M 136,172,56\ns_bfe_u32 s62, s57, 0x20007\ns_lshl_b32 s62, s62, 5\ns_add_u32 s61, s74, 1009672\ns_add_u32 s61, s61, s62\ns_mov_b64 exec, 1\n"
"buffer_atomic_swap_x2 v[226:227], off, s[20:23], s61 sc1\ns_mov_b64 exec, -1\nv_cvt_pk_bf16_f32 v49, v50, v51\nM 152,168,44\nR 166,188,45056\nv_cvt_pk_bf16_f32 v50, v52, v53\n"
"v_cvt_pk_bf16_f32 v33, v34, v35\nM 152,172,60\nG 4\nv_cvt_pk_bf16_f32 v34, v36, v37\nv_cvt_pk_bf16_f32 v35, v38, v39\nM 156,176,44\n"
"R 168,189,32768\ns_bfe_u32 s100, s58, 0x20007\ns_mul_i32 s100, s100, 49152\ns_add_u32 s100, s100, s72\ns_add_u32 s100, s100, 65044480\nv_cvt_pk_bf16_f32 v40, v40, v41\n"
"M 156,180,60\nbuffer_store_dwordx4 v[32:35], v252, s[20:23], s100 offen offset:0 sc1\nbuffer_load_dwordx4 v221, s[16:19], s63 offen offset:1024 lds\nM 140,176,40\nR 170,189,36864\ns_add_u32 m0, s50, 40960\n"
"s_add_u32 s63, s59, 327680\nM 140,180,56\nbuffer_load_dwordx4 v220, s[16:19], s63 offen lds\nM 124,176,36\nR 172,189,40960\nv_cvt_pk_bf16_f32 v41, v42, v43\n"
"M 124,180,52\nv_cvt_pk_bf16_f32 v42, v44, v45\nv_cvt_pk_bf16_f32 v43, v46, v47\nM 108,176,32\nR 174,189,45056\nv_cvt_pk_bf16_f32 v51, v54, v55\n"
"M 108,180,48\nbuffer_store_dwordx4 v[40:43], v252, s[20:23], s100 offen offset:1024 sc1\ns_barrier\nG 4\nM 96,160,64\nR 176,190,32768\n"
"v_cvt_pk_bf16_f32 v57, v58, v59\nM 96,164,80\nbuffer_load_dwordx4 v221, s[16:19], s63 offen offset:1024 lds\nM 112,160,68\nR 178,190,36864\ns_add_u32 m0, s50, 49152\n"
"M 112,164,84\nbuffer_store_dwordx4 v[48:51], v252, s[20:23], s100 offen offset:2048 sc1\nM 128,160,72\nR 180,190,40960\ns_add_u32 s63, s59, 851968\nM 128,164,88\n"
"buffer_load_dwordx4 v220, s[16:19], s63 offen lds\nM 144,160,76\nR 182,190,45056\nv_cvt_pk_bf16_f32 v58, v60, v61\nM 144,164,92\nG 4\n"
"v_cvt_pk_bf16_f32 v59, v62, v63\nM 148,168,76\nR 160,191,32768\nM 148,172,92\nbuffer_store_dwordx4 v[56:59], v252, s[20:23], s100 offen offset:3072 sc1\nbuffer_load_dwordx4 v221, s[16:19], s63 offen offset:1024 lds\n"
"M 132,168,72\nR 162,191,36864\nM 132,172,88\nM 116,168,68\nR 164,191,40960\nM 116,172,84\n"
"M 100,168,64\nR 166,191,45056\nv_mov_b32 v248, 0\nM 100,172,80\nG 4\nv_mov_b32 v249, 0\n"
"M 104,176,64\nR 168,188,49152\ns_add_u32 s61, s74, 1009664\ns_mov_b64 exec, 3\nbuffer_atomic_or_x2 v[248:249], v250, s[20:23], s61 offen sc0 sc1\ns_mov_b64 exec, -1\n"
"M 104,180,80\nM 120,176,68\nR 170,188,53248\nv_mov_b32 v242, v208\nM 120,180,84\nv_mov_b32 v243, v209\n"
"v_mov_b32 v244, v208\nM 136,176,72\nR 172,184,0\nv_mov_b32 v245, v209\nM 136,180,88\nv_mov_b32 v64, v192\n"
"v_mov_b32 v65, v193\nM 152,176,76\nR 174,184,4096\nv_mov_b32 v70, v194\nM 152,180,92\nG 4\n"
"v_mov_b32 v71, v195\nv_mov_b32 v72, v196\nM 156,160,76\nR 176,189,49152\nv_mov_b32 v73, v197\nM 156,164,92\n"
"v_mov_b32 v78, v198\nv_mov_b32 v79, v199\nM 140,160,72\nR 178,189,53248\nv_mov_b32 v80, v200\nM 140,164,88\n"
"v_mov_b32 v81, v201\nv_mov_b32 v86, v202\nM 124,160,68\nR 180,185,0\nv_mov_b32 v87, v203\nM 124,164,84\n"
"v_mov_b32 v88, v204\nv_mov_b32 v89, v205\nM 108,160,64\nR 182,185,4096\nv_mov_b32 v94, v206\nM 108,164,80\n"
"G 4\nv_mov_b32 v95, v207\nv_mov_b32 v32, v232\nM 96,168,96\nR 160,190,49152\nv_mov_b32 v33, v233\n"
"M 96,172,112\nv_mov_b32 v38, v234\nv_mov_b32 v39, v235\nM 112,168,100\nR 162,190,53248\nv_mov_b32 v40, v236\n"
"M 112,172,116\nv_mov_b32 v41, v237\nv_mov_b32 v46, v238\nM 128,168,104\nR 164,186,0\nv_mov_b32 v47, v239\n"
"M 128,172,120\nv_mov_b32 v48, v232\nv_mov_b32 v49, v233\nM 144,168,108\nR 166,186,4096\nv_mov_b32 v54, v234\n"
"M 144,172,124\nG 4\nv_mov_b32 v55, v235\nv_mov_b32 v56, v236\nM 148,176,108\nR 168,191,49152\n"
"v_mov_b32 v57, v237\nM 148,180,124\nv_mov_b32 v62, v238\nv_mov_b32 v63, v239\nM 132,176,104\nR 170,191,53248\n"
"M 132,180,120\nM 116,176,100\nR 172,187,0\nM 116,180,116\nM 100,176,96\nR 174,187,4096\n"
"M 100,180,112\nG 4\nM 104,160,96\nR 176,184,8192\ns_waitcnt vmcnt(8)\ns_barrier\n"
"M 104,164,112\nM 120,160,100\nR 178,184,12288\nM 120,164,116\nM 136,160,104\nR 180,184,16384\n"
"M 136,164,120\nM 152,160,108\nR 182,184,20480\nM 152,164,124\nG 4\nM 156,168,108\n"
"R 160,185,8192\nM 156,172,124\nM 140,168,104\nR 162,185,12288\nM 140,172,120\nM 124,168,100\n"
"R 164,185,16384\nM 124,172,116\nM 108,168,96\nR 166,185,20480\nv_mov_b32 v226, s46\nM 108,172,112\n"
"G 4\nv_mov_b32 v227, s47\nM 96,176,128\nR 168,186,8192\ns_bfe_u32 s62, s58, 0x20007\ns_lshl_b32 s62, s62, 5\n"
"s_add_u32 s61, s74, 1009680\ns_add_u32 s61, s61, s62\ns_waitcnt vmcnt(0)\ns_mov_b64 exec, 1\nbuffer_atomic_swap_x2 v[226:227], off, s[20:23], s61 sc1\ns_mov_b64 exec, -1\n"
"M 96,180,144\ns_mul_i32 s64, s43, 49152\nM 112,176,132\nR 170,186,12288\nM 112,180,148\ns_add_u32 s64, s64, s72\n"
"M 128,176,136\nR 172,186,16384\nv_cmp_eq_u64 s[86:87], s[46:47], v[248:249]\nM 128,180,152\ns_add_u32 s64, s64, 65011712\nM 144,176,140\n"
"R 174,186,20480\ns_and_b32 s62, s86, 3\ns_cmp_eq_u32 s62, 3\ns_cbranch_scc0 Lq967\nLq968:\nM 144,180,156\n"
"G 4\nM 148,160,140\nR 176,187,8192\nM 148,164,156\nbuffer_load_dwordx4 v[2:5], v252, s[20:23], s64 offen offset:0 sc1\nM 132,160,136\n"
"R 178,187,12288\nM 132,164,152\nbuffer_load_dwordx4 v[10:13], v252, s[20:23], s64 offen offset:1024 sc1\nM 116,160,132\nR 180,187,16384\nM 116,164,148\n"
"s_mul_i32 s65, s43, 49152\nM 100,160,128\nR 182,187,20480\nM 100,164,144\nbuffer_load_dwordx4 v[18:21], v252, s[20:23], s64 offen offset:2048 sc1\nG 4\n"
"M 104,168,128\nR 160,184,24576\nM 104,172,144\nbuffer_load_dwordx4 v[26:29], v252, s[20:23], s64 offen offset:3072 sc1\nM 120,168,132\nR 162,184,28672\n"
"M 120,172,148\ns_add_u32 s65, s65, s72\nM 136,168,136\nR 164,184,32768\ns_add_u32 s65, s65, 65028096\nM 136,172,152\n"
"buffer_load_dwordx4 v[66:69], v252, s[20:23], s65 offen offset:0 sc1\nM 152,168,140\nR 166,184,36864\nM 152,172,156\nG 4\nM 156,176,140\n"
"R 168,185,24576\ns_barrier\nM 156,180,156\nbuffer_load_dwordx4 v[74:77], v252, s[20:23], s65 offen offset:1024 sc1\nM 140,176,136\nR 170,185,28672\n"
"v_mov_b32 v246, 0\nM 140,180,152\nbuffer_load_dwordx4 v[82:85], v252, s[20:23], s65 offen offset:2048 sc1\nv_mov_b32 v247, 0\nM 124,176,132\nR 172,185,32768\n"
"s_add_u32 s61, s74, 1009664\nLpg33:\ns_cbranch_execz LretW1z\ns_mov_b64 exec, 4\nbuffer_atomic_or_x2 v[246:247], v250, s[20:23], s61 offen sc0 sc1\ns_mov_b64 exec, -1\n"
"M 124,180,148\nbuffer_load_dwordx4 v[90:93], v252, s[20:23], s65 offen offset:3072 sc1\nM 108,176,128\nR 174,185,36864\nM 108,180,144\nG 4\n"
"M 128,160,168\nR 176,186,24576\nM 128,164,184\nR 178,186,28672\nM 144,160,172\nR 180,186,32768\n"
"M 144,164,188\nR 182,186,36864\nG 4\nM 148,168,172\nR 160,187,24576\nM 148,172,188\n"
"R 162,187,28672\nM 132,168,168\nR 164,187,32768\nM 132,172,184\nR 166,187,36864\nG 4\n"
"M 136,176,168\nR 168,184,40960\nM 136,180,184\nR 170,184,45056\nM 152,176,172\nR 172,184,49152\n"
"M 152,180,188\nR 174,184,53248\nG 4\nM 156,160,172\nR 176,185,40960\nM 156,164,188\n"
"R 178,185,45056\nM 140,160,168\nR 180,185,49152\nM 140,164,184\nR 182,185,53248\nG 4\n"
"M 128,168,200\nR 160,186,40960\nM 128,172,216\nR 162,186,45056\nM 144,168,204\nR 164,186,49152\n"
"M 144,172,220\nR 166,186,53248\nG 4\nM 148,176,204\nR 168,187,40960\nM 148,180,220\n"
"R 170,187,45056\nM 132,176,200\nR 172,187,49152\nM 132,180,216\nR 174,187,53248\nG 4\n"
"M 136,160,200\nR 176,184,24576\nM 136,164,216\nR 178,184,28672\nM 152,160,204\nR 180,184,32768\n"
"M 152,164,220\nR 182,184,36864\nG 4\nM 156,168,204\nR 160,185,24576\nM 156,172,220\n"
"R 162,185,28672\nM 140,168,200\nR 164,185,32768\nM 140,172,216\nR 166,185,36864\nG 4\n"
"M 96,176,160\nR 168,186,24576\nM 96,180,176\nR 170,186,28672\ns_mov_b32 m0, s51\nM 112,176,164\n"
"R 172,186,32768\nv_cvt_pk_bf16_f32 v128, v128, v129\nv_cvt_pk_bf16_f32 v129, v130, v131\nM 112,180,180\nR 174,186,36864\nG 4\n"
"v_cvt_pk_bf16_f32 v130, v132, v133\nM 116,160,164\nR 176,187,24576\ns_mul_i32 s66, s43, 49152\nv_cvt_pk_bf16_f32 v131, v134, v135\nM 116,164,180\n"
"R 178,187,28672\ns_add_u32 s66, s66, s72\nv_cvt_pk_bf16_f32 v132, v136, v137\nM 100,160,160\nR 180,187,32768\ns_add_u32 s66, s66, 65044480\n"
"v_cvt_pk_bf16_f32 v133, v138, v139\nM 100,164,176\nR 182,187,36864\nG 4\nv_cvt_pk_bf16_f32 v134, v140, v141\nM 104,168,160\n"
"R 160,184,40960\ns_waitcnt vmcnt(1)\nv_cmp_eq_u64 s[86:87], s[46:47], v[246:247]\nv_cvt_pk_bf16_f32 v135, v142, v143\nM 104,172,176\nR 162,184,45056\n"
"v_cvt_pk_bf16_f32 v136, v144, v145\nv_cvt_pk_bf16_f32 v137, v146, v147\nM 120,168,164\nR 164,184,49152\ns_and_b32 s62, s86, 4\ns_cmp_eq_u32 s62, 4\n"
"s_cbranch_scc0 Lq983\nLq984:\nv_cvt_pk_bf16_f32 v138, v148, v149\nM 120,172,180\nR 166,184,53248\nG 4\n"
"v_cvt_pk_bf16_f32 v139, v150, v151\nM 124,176,164\nR 168,185,40960\nbuffer_load_dwordx4 v[34:37], v252, s[20:23], s66 offen offset:0 sc1\nv_cvt_pk_bf16_f32 v140, v152, v153\nM 124,180,180\n"
"R 170,185,45056\nv_cvt_pk_bf16_f32 v141, v154, v155\nv_cvt_pk_bf16_f32 v142, v156, v157\nM 108,176,160\nR 172,185,49152\nbuffer_load_dwordx4 v[42:45], v252, s[20:23], s66 offen offset:1024 sc1\n"
"ds_write_addtid_b32 v128 offset:0\nds_write_addtid_b32 v129 offset:256\nds_write_addtid_b32 v130 offset:512\nds_write_addtid_b32 v131 offset:768\nds_write_addtid_b32 v132 offset:1024\nds_write_addtid_b32 v133 offset:1280\n"
"ds_write_addtid_b32 v134 offset:1536\nds_write_addtid_b32 v135 offset:1792\nds_write_addtid_b32 v136 offset:2048\nds_write_addtid_b32 v137 offset:2304\nds_write_addtid_b32 v138 offset:2560\nds_write_addtid_b32 v139 offset:2816\n"
"ds_write_addtid_b32 v140 offset:3072\nds_write_addtid_b32 v141 offset:3328\nds_write_addtid_b32 v142 offset:3584\nv_cvt_pk_bf16_f32 v143, v158, v159\nds_write_addtid_b32 v143 offset:3840\nM 108,180,176\n"
"R 174,185,53248\nG 15\nM 96,160,192\nR 176,186,40960\nM 96,164,208\nR 178,186,45056\n"
"buffer_load_dwordx4 v[50:53], v252, s[20:23], s66 offen offset:2048 sc1\nG 3\ns_barrier\nM 112,160,196\nR 180,186,49152\nds_read2st64_b32 v[0:1], v225 offset0:0 offset1:1\n"
"M 112,164,212\nR 182,186,53248\nds_read2st64_b32 v[6:7], v225 offset0:2 offset1:3\nG 6\nM 116,168,196\nR 160,187,40960\n"
"buffer_load_dwordx4 v[58:61], v252, s[20:23], s66 offen offset:3072 sc1\nds_read2st64_b32 v[8:9], v225 offset0:4 offset1:5\nM 116,172,212\nR 162,187,45056\nds_read2st64_b32 v[14:15], v225 offset0:6 offset1:7\nM 100,168,192\n"
"R 164,187,49152\nds_read2st64_b32 v[16:17], v225 offset0:8 offset1:9\nM 100,172,208\nR 166,187,53248\nds_read2st64_b32 v[22:23], v225 offset0:10 offset1:11\nG 9\n"
"M 104,176,192\nds_read2st64_b32 v[24:25], v225 offset0:12 offset1:13\nds_read2st64_b32 v[30:31], v225 offset0:14 offset1:15\nM 104,180,208\nM 120,176,196\nM 120,180,212\n"
"G 3\nM 124,160,196\nM 124,164,212\nM 108,160,192\nM 108,164,208\nv_mfma_f32_16x16x32_bf16 v[96:99], v[242:245], v[64:67], v[96:99]\n"
"v_mfma_f32_16x16x32_bf16 v[100:103], v[242:245], v[68:71], v[100:103]\nv_mfma_f32_16x16x32_bf16 v[104:107], v[242:245], v[72:75], v[104:107]\nv_mfma_f32_16x16x32_bf16 v[108:111], v[242:245], v[76:79], v[108:111]\nv_mfma_f32_16x16x32_bf16 v[112:115], v[242:245], v[80:83], v[112:115]\nv_mfma_f32_16x16x32_bf16 v[116:119], v[242:245], v[84:87], v[116:119]\ns_waitcnt vmcnt(4)\n"
"v_mfma_f32_16x16x32_bf16 v[120:123], v[242:245], v[88:91], v[120:123]\nv_mfma_f32_16x16x32_bf16 v[124:127], v[242:245], v[92:95], v[124:127]\nv_mfma_f32_16x16x32_bf16 v[96:99], v[242:245], v[0:3], v[96:99]\nv_mfma_f32_16x16x32_bf16 v[100:103], v[242:245], v[4:7], v[100:103]\nv_mfma_f32_16x16x32_bf16 v[104:107], v[242:245], v[8:11], v[104:107]\nv_mfma_f32_16x16x32_bf16 v[108:111], v[242:245], v[12:15], v[108:111]\n"
"v_mfma_f32_16x16x32_bf16 v[112:115], v[242:245], v[16:19], v[112:115]\nG 2\nv_mfma_f32_16x16x32_bf16 v[116:119], v[242:245], v[20:23], v[116:119]\nG 1\nv_mfma_f32_16x16x32_bf16 v[120:123], v[242:245], v[24:27], v[120:123]\nG 0\n"
"v_mfma_f32_16x16x32_bf16 v[124:127], v[242:245], v[28:31], v[124:127]\ns_waitcnt vmcnt(3)\nv_mfma_f32_16x16x32_bf16 v[96:99], v[242:245], v[32:35], v[96:99]\nv_mfma_f32_16x16x32_bf16 v[100:103], v[242:245], v[36:39], v[100:103]\ns_waitcnt vmcnt(2)\nv_mfma_f32_16x16x32_bf16 v[104:107], v[242:245], v[40:43], v[104:107]\n"
"v_mfma_f32_16x16x32_bf16 v[108:111], v[242:245], v[44:47], v[108:111]\ns_waitcnt vmcnt(1)\nv_mfma_f32_16x16x32_bf16 v[112:115], v[242:245], v[48:51], v[112:115]\nv_mfma_f32_16x16x32_bf16 v[116:119], v[242:245], v[52:55], v[116:119]\ns_waitcnt vmcnt(0)\nv_mfma_f32_16x16x32_bf16 v[120:123], v[242:245], v[56:59], v[120:123]\n"
"v_mfma_f32_16x16x32_bf16 v[124:127], v[242:245], v[60:63], v[124:127]\nv_cvt_pk_bf16_f32 v192, v96, v97\nv_cvt_pk_bf16_f32 v193, v98, v99\nv_cvt_pk_bf16_f32 v194, v100, v101\nv_cvt_pk_bf16_f32 v195, v102, v103\nv_cvt_pk_bf16_f32 v196, v104, v105\n"
"v_cvt_pk_bf16_f32 v197, v106, v107\nv_cvt_pk_bf16_f32 v198, v108, v109\nv_cvt_pk_bf16_f32 v199, v110, v111\nv_cvt_pk_bf16_f32 v200, v112, v113\nv_cvt_pk_bf16_f32 v201, v114, v115\nv_cvt_pk_bf16_f32 v202, v116, v117\n"
"v_cvt_pk_bf16_f32 v203, v118, v119\nv_cvt_pk_bf16_f32 v204, v120, v121\nv_cvt_pk_bf16_f32 v205, v122, v123\nv_cvt_pk_bf16_f32 v206, v124, v125\nv_cvt_pk_bf16_f32 v207, v126, v127\nv_and_b32 v230, 0xf0, v252\n"
"v_lshlrev_b32 v230, 7, v230\nv_bfe_u32 v231, v252, 8, 1\nv_lshl_add_u32 v230, v231, 5, v230\nv_lshrrev_b32 v231, 9, v252\nv_lshl_add_u32 v230, v231, 4, v230\ns_add_u32 s61, s49, s53\n"
"s_add_u32 s62, s61, 32768\nv_permlane16_swap_b32 v192, v194\nv_permlane16_swap_b32 v193, v195\nbuffer_store_dwordx4 v[192:195], v230, s[28:31], s61 offen offset:0\nv_permlane16_swap_b32 v196, v198\nv_permlane16_swap_b32 v197, v199\n"
"buffer_store_dwordx4 v[196:199], v230, s[28:31], s61 offen offset:64\nv_permlane16_swap_b32 v200, v202\nv_permlane16_swap_b32 v201, v203\nbuffer_store_dwordx4 v[200:203], v230, s[28:31], s62 offen offset:0\nv_permlane16_swap_b32 v204, v206\nv_permlane16_swap_b32 v205, v207\n"
"buffer_store_dwordx4 v[204:207], v230, s[28:31], s62 offen offset:64\ns_add_u32 s60, s83, 1\nv_mov_b32 v234, s60\ns_or_b32 s60, s45, s40\ns_cmp_eq_u32 s60, 0\ns_cselect_b64 exec, 1, 0\n"
"buffer_store_dword v234, off, s[20:23], 0 sc1\ns_mov_b64 exec, -1\ns_endpgm\nLqF:\nLq645:\ns_lshr_b32 s84, s49, 4\n"
"s_add_u32 s84, s84, 82837504\nbuffer_load_dwordx4 v[128:131], v252, s[20:23], s84 offen offset:0 sc1\nbuffer_load_dwordx4 v[132:135], v252, s[20:23], s84 offen offset:1024 sc1\nbuffer_load_dwordx4 v[136:139], v252, s[20:23], s84 offen offset:2048 sc1\nbuffer_load_dwordx4 v[140:143], v252, s[20:23], s84 offen offset:3072 sc1\ns_mov_b32 s85, 0x0c0c0400\n"
"s_waitcnt vmcnt(3)\nv_perm_b32 v144, v129, v128, s85\nv_perm_b32 v148, v131, v130, s85\nv_lshl_or_b32 v144, v148, 16, v144\ns_waitcnt vmcnt(2)\nv_perm_b32 v145, v133, v132, s85\n"
"v_perm_b32 v149, v135, v134, s85\nv_lshl_or_b32 v145, v149, 16, v145\ns_waitcnt vmcnt(1)\nv_perm_b32 v146, v137, v136, s85\nv_perm_b32 v150, v139, v138, s85\nv_lshl_or_b32 v146, v150, 16, v146\n"
"s_waitcnt vmcnt(0)\nv_perm_b32 v147, v141, v140, s85\nv_perm_b32 v151, v143, v142, s85\nv_lshl_or_b32 v147, v151, 16, v147\nv_min3_u32 v230, v144, v145, v146\nv_min3_u32 v230, v230, v147, v147\n"
"v_max3_u32 v231, v144, v145, v146\nv_max3_u32 v231, v231, v147, v147\nv_cmp_ne_u32 s[86:87], s46, v230\nv_cmp_ne_u32 s[88:89], s46, v231\ns_nop 0\ns_or_b64 s[86:87], s[86:87], s[88:89]\n"
"s_cbranch_scc0 Lq646\ns_sub_u32 s48, s48, 1\ns_cmp_gt_i32 s48, 0\ns_cbranch_scc1 Lq645\ns_branch Lq646\nLq692:\n"
"s_mov_b64 exec, s[66:67]\nbuffer_load_dwordx4 v[230:233], v252, s[20:23], s65 offen offset:0 sc1\nbuffer_load_dwordx2 v[64:65], v227, s[20:23], s65 offen offset:2048 sc1\nbuffer_load_dwordx2 v[70:71], v227, s[20:23], s65 offen offset:2560 sc1\ns_mov_b64 exec, -1\ns_waitcnt vmcnt(0)\n"
"v_max3_u32 v248, v233, v65, v71\nv_cmp_eq_u32 vcc, -1, v248\ns_nop 1\ns_cbranch_vccz Lq693\ns_sub_u32 s48, s48, 1\ns_cmp_gt_i32 s48, 0\n"
"s_cbranch_scc1 Lq692\ns_branch Lq693\nLq694:\ns_mov_b64 exec, s[66:67]\nbuffer_load_dwordx4 v[234:237], v252, s[20:23], s65 offen offset:1024 sc1\nbuffer_load_dwordx2 v[76:77], v227, s[20:23], s65 offen offset:3072 sc1\n"
"buffer_load_dwordx2 v[82:83], v227, s[20:23], s65 offen offset:3584 sc1\ns_mov_b64 exec, -1\ns_waitcnt vmcnt(0)\nv_max3_u32 v248, v237, v77, v83\nv_cmp_eq_u32 vcc, -1, v248\ns_nop 1\n"
"s_cbranch_vccz Lq695\ns_sub_u32 s48, s48, 1\ns_cmp_gt_i32 s48, 0\ns_cbranch_scc1 Lq694\ns_branch Lq695\nLq818:\n"
"s_lshr_b32 s84, s49, 4\ns_add_u32 s84, s84, 83361792\nbuffer_load_dwordx4 v[0:3], v252, s[20:23], s84 offen offset:0 sc1\nbuffer_load_dwordx4 v[4:7], v252, s[20:23], s84 offen offset:1024 sc1\nbuffer_load_dwordx4 v[8:11], v252, s[20:23], s84 offen offset:2048 sc1\nbuffer_load_dwordx4 v[12:15], v252, s[20:23], s84 offen offset:3072 sc1\n"
"s_mov_b32 s85, 0x0c0c0400\ns_waitcnt vmcnt(3)\nv_perm_b32 v16, v1, v0, s85\nv_perm_b32 v20, v3, v2, s85\nv_lshl_or_b32 v16, v20, 16, v16\ns_waitcnt vmcnt(2)\n"
"v_perm_b32 v17, v5, v4, s85\nv_perm_b32 v21, v7, v6, s85\nv_lshl_or_b32 v17, v21, 16, v17\ns_waitcnt vmcnt(1)\nv_perm_b32 v18, v9, v8, s85\nv_perm_b32 v22, v11, v10, s85\n"
"v_lshl_or_b32 v18, v22, 16, v18\ns_waitcnt vmcnt(0)\nv_perm_b32 v19, v13, v12, s85\nv_perm_b32 v23, v15, v14, s85\nv_lshl_or_b32 v19, v23, 16, v19\nv_min3_u32 v230, v16, v17, v18\n"
"v_min3_u32 v230, v230, v19, v19\nv_max3_u32 v231, v16, v17, v18\nv_max3_u32 v231, v231, v19, v19\nv_cmp_ne_u32 s[86:87], s46, v230\nv_cmp_ne_u32 s[88:89], s46, v231\ns_nop 0\n"
"s_or_b64 s[86:87], s[86:87], s[88:89]\ns_cbranch_scc0 Lq819\ns_sub_u32 s48, s48, 1\ns_cmp_gt_i32 s48, 0\ns_cbranch_scc1 Lq818\ns_branch Lq819\n"
"Lq838:\nv_mov_b32 v248, 0\nv_mov_b32 v249, 0\ns_add_u32 s61, s82, 845824\ns_mov_b64 exec, 0xf\nbuffer_atomic_or_x2 v[248:249], v250, s[20:23], s61 offen sc0 sc1\n"
"s_mov_b64 exec, -1\ns_waitcnt vmcnt(0)\nv_cmp_eq_u64 s[86:87], s[46:47], v[248:249]\ns_nop 1\ns_and_b32 s62, s86, 0xf\ns_cmp_eq_u32 s62, 0xf\n"
"s_cbranch_scc1 Lq839\ns_sleep 1\ns_sub_u32 s48, s48, 1\ns_cmp_gt_i32 s48, 0\ns_cbranch_scc1 Lq838\ns_branch Lq839\n"
"Lq873:\nv_mov_b32 v248, 0\nv_mov_b32 v249, 0\ns_add_u32 s61, s82, 845832\ns_mov_b64 exec, 0xf\nbuffer_atomic_or_x2 v[248:249], v250, s[20:23], s61 offen sc0 sc1\n"
"s_mov_b64 exec, -1\ns_waitcnt vmcnt(0)\nv_cmp_eq_u64 s[86:87], s[46:47], v[248:249]\ns_nop 1\ns_and_b32 s62, s86, 0xf\ns_cmp_eq_u32 s62, 0xf\n"
"s_cbranch_scc1 Lq874\ns_sleep 1\ns_sub_u32 s48, s48, 1\ns_cmp_gt_i32 s48, 0\ns_cbranch_scc1 Lq873\ns_branch Lq874\n"
"Lq908:\nv_mov_b32 v248, 0\nv_mov_b32 v249, 0\ns_add_u32 s61, s82, 845848\ns_mov_b64 exec, 0xf\nbuffer_atomic_or_x2 v[248:249], v250, s[20:23], s61 offen sc0 sc1\n"
"s_mov_b64 exec, -1\ns_waitcnt vmcnt(0)\nv_cmp_eq_u64 s[86:87], s[46:47], v[248:249]\ns_nop 1\ns_and_b32 s62, s86, 0xf\ns_cmp_eq_u32 s62, 0xf\n"
"s_cbranch_scc1 Lq909\ns_sleep 1\ns_sub_u32 s48, s48, 1\ns_cmp_gt_i32 s48, 0\ns_cbranch_scc1 Lq908\ns_branch Lq909\n"
"Lq927:\nv_mov_b32 v248, 0\nv_mov_b32 v249, 0\ns_add_u32 s61, s82, 845840\ns_mov_b64 exec, 0xf\nbuffer_atomic_or_x2 v[248:249], v250, s[20:23], s61 offen sc0 sc1\n"
"s_mov_b64 exec, -1\ns_waitcnt vmcnt(0)\nv_cmp_eq_u64 s[86:87], s[46:47], v[248:249]\ns_nop 1\ns_and_b32 s62, s86, 0xf\ns_cmp_eq_u32 s62, 0xf\n"
"s_cbranch_scc1 Lq928\ns_sleep 1\ns_sub_u32 s48, s48, 1\ns_cmp_gt_i32 s48, 0\ns_cbranch_scc1 Lq927\ns_branch Lq928\n"
"Lq967:\nv_mov_b32 v248, 0\nv_mov_b32 v249, 0\ns_add_u32 s61, s74, 1009664\ns_mov_b64 exec, 3\nbuffer_atomic_or_x2 v[248:249], v250, s[20:23], s61 offen sc0 sc1\n"
"s_mov_b64 exec, -1\ns_waitcnt vmcnt(0)\nv_cmp_eq_u64 s[86:87], s[46:47], v[248:249]\ns_nop 1\ns_and_b32 s62, s86, 3\ns_cmp_eq_u32 s62, 3\n"
"s_cbranch_scc1 Lq968\ns_sleep 1\ns_sub_u32 s48, s48, 1\ns_cmp_gt_i32 s48, 0\ns_cbranch_scc1 Lq967\ns_branch Lq968\n"
"Lq983:\nv_mov_b32 v246, 0\nv_mov_b32 v247, 0\ns_add_u32 s61, s74, 1009664\ns_mov_b64 exec, 4\nbuffer_atomic_or_x2 v[246:247], v250, s[20:23], s61 offen sc0 sc1\n"
"s_mov_b64 exec, -1\ns_waitcnt vmcnt(0)\nv_cmp_eq_u64 s[86:87], s[46:47], v[246:247]\ns_nop 1\ns_and_b32 s62, s86, 4\ns_cmp_eq_u32 s62, 4\n"
"s_cbranch_scc1 Lq984\ns_sleep 1\ns_sub_u32 s48, s48, 1\ns_cmp_gt_i32 s48, 0\ns_cbranch_scc1 Lq983\ns_branch Lq984\n"
"LqE:\n"
  : "+{v0}"(tid), "+{s[92:93]}"(ka), "+{s45}"(bid) :
  : "a0", "a1", "a2", "a3", "a4", "a5", "a6", "a7", "a8", "a9", "a10", "a11", "a12", "a13", "a14", "a15", "a16", "a17", "a18", "a19", "a20", "a21", "a22", "a23", "a24", "a25", "a26", "a27", "a28", "a29", "a30", "a31", "a32", "a33", "a34", "a35", "a36", "a37", "a38", "a39", "a40", "a41", "a42", "a43", "a44", "a45", "a46", "a47", "a48", "a49", "a50", "a51", "a52", "a53", "a54", "a55", "a56", "a57", "a58", "a59", "a60", "a61", "a62", "a63", "a64", "a65", "a66", "a67", "a68", "a69", "a70", "a71", "a72", "a73", "a74", "a75", "a76", "a77", "a78", "a79", "a80", "a81", "a82", "a83", "a84", "a85", "a86", "a87", "a88", "a89", "a90", "a91", "a92", "a93", "a94", "a95", "a96", "a97", "a98", "a99", "a100", "a101", "a102", "a103", "a104", "a105", "a106", "a107", "a108", "a109", "a110", "a111", "a112", "a113", "a114", "a115", "a116", "a117", "a118", "a119", "a120", "a121", "a122", "a123", "a124", "a125", "a126", "a127", "a128", "a129", "a130", "a131", "a132", "a133", "a134", "a135", "a136", "a137", "a138", "a139", "a140", "a141", "a142", "a143", "a144", "a145", "a146", "a147", "a148", "a149", "a150", "a151", "a152", "a153", "a154", "a155", "a156", "a157", "a158", "a159", "a160", "a161", "a162", "a163", "a164", "a165", "a166", "a167", "a168", "a169", "a170", "a171", "a172", "a173", "a174", "a175", "a176", "a177", "a178", "a179", "a180", "a181", "a182", "a183", "a184", "a185", "a186", "a187", "a188", "a189", "a190", "a191", "a192", "a193", "a194", "a195", "a196", "a197", "a198", "a199", "a200", "a201", "a202", "a203", "a204", "a205", "a206", "a207", "a208", "a209", "a210", "a211", "a212", "a213", "a214", "a215", "a216", "a217", "a218", "a219", "a220", "a221", "a222", "a223", "a224", "a225", "a226", "a227", "a228", "a229", "a230", "a231", "a232", "a233", "a234", "a235", "a236", "a237", "a238", "a239", "a240", "a241", "a242", "a243", "a244", "a245", "a246", "a247", "a248", "a249", "a250", "a251", "a252", "a253", "a254", "a255", "v1", "v2", "v3", "v4", "v5", "v6", "v7", "v8", "v9", "v10", "v11", "v12", "v13", "v14", "v15", "v16", "v17", "v18", "v19", "v20", "v21", "v22", "v23", "v24", "v25", "v26", "v27", "v28", "v29", "v30", "v31", "v32", "v33", "v34", "v35", "v36", "v37", "v38", "v39", "v40", "v41", "v42", "v43", "v44", "v45", "v46", "v47", "v48", "v49", "v50", "v51", "v52", "v53", "v54", "v55", "v56", "v57", "v58", "v59", "v60", "v61", "v62", "v63", "v64", "v65", "v66", "v67", "v68", "v69", "v70", "v71", "v72", "v73", "v74", "v75", "v76", "v77", "v78", "v79", "v80", "v81", "v82", "v83", "v84", "v85", "v86", "v87", "v88", "v89", "v90", "v91", "v92", "v93", "v94", "v95", "v96", "v97", "v98", "v99", "v100", "v101", "v102", "v103", "v104", "v105", "v106", "v107", "v108", "v109", "v110", "v111", "v112", "v113", "v114", "v115", "v116", "v117", "v118", "v119", "v120", "v121", "v122", "v123", "v124", "v125", "v126", "v127", "v128", "v129", "v130", "v131", "v132", "v133", "v134", "v135", "v136", "v137", "v138", "v139", "v140", "v141", "v142", "v143", "v144", "v145", "v146", "v147", "v148", "v149", "v150", "v151", "v152", "v153", "v154", "v155", "v156", "v157", "v158", "v159", "v160", "v161", "v162", "v163", "v164", "v165", "v166", "v167", "v168", "v169", "v170", "v171", "v172", "v173", "v174", "v175", "v176", "v177", "v178", "v179", "v180", "v181", "v182", "v183", "v184", "v185", "v186", "v187", "v188", "v189", "v190", "v191", "v192", "v193", "v194", "v195", "v196", "v197", "v198", "v199", "v200", "v201", "v202", "v203", "v204", "v205", "v206", "v207", "v208", "v209", "v210", "v211", "v212", "v213", "v214", "v215", "v216", "v217", "v218", "v219", "v220", "v221", "v222", "v223", "v224", "v225", "v226", "v227", "v228", "v229", "v230", "v231", "v232", "v233", "v234", "v235", "v236", "v237", "v238", "v239", "v240", "v241", "v242", "v243", "v244", "v245", "v246", "v247", "v248", "v249", "v250", "v251", "v252", "v253", "v254", "v255", "s4", "s5", "s6", "s7", "s8", "s9", "s10", "s11", "s12", "s13", "s14", "s15", "s16", "s17", "s18", "s19", "s20", "s21", "s22", "s23", "s24", "s25", "s26", "s27", "s28", "s29", "s30", "s31", "s32", "s33", "s34", "s35", "s36", "s37", "s38", "s39", "s40", "s41", "s42", "s43", "s44", "s46", "s47", "s48", "s49", "s50", "s51", "s52", "s53", "s54", "s55", "s56", "s57", "s58", "s59", "s60", "s61", "s62", "s63", "s64", "s65", "s66", "s67", "s68", "s69", "s70", "s71", "s72", "s73", "s74", "s75", "s76", "s77", "s78", "s79", "s80", "s81", "s82", "s83", "s84", "s85", "s86", "s87", "s88", "s89", "s90", "s91", "s94", "s95", "s96", "s97", "s98", "s99", "s100", "s101", "vcc", "m0", "scc", "memory");
#endif
}
extern "C" void kernel_launch(void* const* d_in, const int* in_sizes, int n_in, void* d_out, int out_size, void* d_ws, size_t ws_size, hipStream_t stream) {
  (void)hipFuncSetAttribute((const void*)layers, hipFuncAttributeMaxDynamicSharedMemorySize, 160 * 1024);
  hipLaunchKernelGGL(layers, dim3(256), dim3(256), 160 * 1024, stream, d_in[0], d_in[1], d_in[2], d_in[4], d_in[5], d_in[6], d_in[7], d_out, d_ws);
}
```

```cpp
#include <hip/hip_runtime.h>
__global__ __launch_bounds__(256, 1) __attribute__((aligned(4096))) void layers(const void* x, const void* wqkv, const void* wo, const void* w1, const void* b1, const void* w2, const void* b2, void* out, void* ws) {
#if defined(__HIP_DEVICE_COMPILE__)
  int tid = threadIdx.x, bid = blockIdx.x;
  unsigned long long ka = (unsigned long long)__builtin_amdgcn_kernarg_segment_ptr();
  asm volatile(
".macro M d,w,a\nv_mfma_f32_16x16x32_bf16 v[\\d:\\d+3], v[\\w:\\w+3], a[\\a:\\a+3], v[\\d:\\d+3]\n.endm\n.macro N d,a,w\nv_mfma_f32_16x16x32_bf16 v[\\d:\\d+3], a[\\a:\\a+3], v[\\w:\\w+3], v[\\d:\\d+3]\n.endm\n"
".macro R d,b,o\nds_read_b64_tr_b16 v[\\d:\\d+1], v\\b offset:\\o\n.endm\n.macro G n\ns_waitcnt lgkmcnt(\\n)\n.endm\n"
"s_load_dwordx2 s[24:25], s[92:93], 0\ns_load_dwordx2 s[4:5], s[92:93], 8\ns_load_dwordx2 s[8:9], s[92:93], 16\ns_load_dwordx2 s[12:13], s[92:93], 24\ns_load_dwordx2 s[32:33], s[92:93], 32\ns_load_dwordx2 s[16:17], s[92:93], 40\n"
"s_load_dwordx2 s[36:37], s[92:93], 48\ns_load_dwordx2 s[28:29], s[92:93], 56\ns_load_dwordx2 s[20:21], s[92:93], 64\nv_lshrrev_b32 v1, 6, v0\nv_and_b32 v0, 63, v0\nv_readfirstlane_b32 s40, v1\n"
"v_lshlrev_b32 v252, 4, v0\ns_getpc_b64 s[98:99]\nLqPC:\ns_lshr_b32 s60, s45, 3\ns_lshl2_add_u32 s60, s60, s40\ns_lshl_b32 s60, s60, 8\n"
"v_lshl_add_u32 v224, v252, 3, s60\nv_add_u32 v224, LqPC-LqPC, v224\nv_min_u32 v224, LqE-LqPC-4, v224\ns_mov_b64 exec, 3\nglobal_load_dword v224, v224, s[98:99]\ns_mov_b64 exec, -1\n"
"s_mov_b32 s6, 6291456\ns_mov_b32 s7, 0x20000\ns_mov_b32 s10, 2097152\ns_mov_b32 s11, 0x20000\ns_mov_b32 s14, 7340032\ns_mov_b32 s15, 0x20000\n"
"s_mov_b32 s18, 7340032\ns_mov_b32 s19, 0x20000\ns_mov_b32 s22, 89128960\ns_mov_b32 s23, 0x20000\ns_mov_b32 s26, 4194304\ns_mov_b32 s27, 0x20000\n"
"s_mov_b32 s30, 4194304\ns_mov_b32 s31, 0x20000\ns_mov_b32 s34, 7168\ns_mov_b32 s35, 0x20000\ns_mov_b32 s38, 2048\ns_mov_b32 s39, 0x20000\n"
"s_bfe_u32 s41, s45, 0x30003\ns_bfe_u32 s60, s45, 65536\ns_lshl3_add_u32 s41, s60, s41\ns_bfe_u32 s43, s45, 131073\ns_lshr_b32 s44, s45, 6\ns_lshl2_add_u32 s42, s43, s44\n"
"s_lshl2_add_u32 s75, s44, s43\ns_and_b32 s90, s40, 1\ns_lshl2_add_u32 s60, s41, s40\ns_lshl_b32 s49, s60, 16\ns_lshl_b32 s50, s40, 11\ns_add_u32 s50, s50, 32768\n"
"s_lshl_b32 s52, s42, 7\ns_lshl_b32 s51, s40, 13\ns_lshl_b32 s60, s90, 10\ns_add_u32 s70, s49, s60\ns_xor_b32 s71, s70, 0x10000\ns_mul_i32 s101, s41, 196608\n"
"s_add_u32 s101, s101, s52\ns_lshl_b32 s53, s75, 7\nLqq0:\nv_lshrrev_b32 v96, 7, v252\nv_lshlrev_b32 v96, 11, v96\nv_lshrrev_b32 v97, 8, v252\n"
"v_lshlrev_b32 v97, 4, v97\nv_and_b32 v98, 0x70, v252\nv_xor_b32 v98, v98, v97\nv_add_u32 v226, v96, v98\nv_xor_b32 v227, 64, v226\nG 0\n"
"s_add_u32 m0, s51, 0\ns_add_u32 s60, s70, 0\nbuffer_load_dwordx4 v226, s[24:27], s60 offen lds\ns_add_u32 m0, s51, 1024\ns_add_u32 s60, s70, 16384\nbuffer_load_dwordx4 v227, s[24:27], s60 offen lds\n"
"s_add_u32 m0, s51, 2048\ns_add_u32 s60, s70, 32768\nbuffer_load_dwordx4 v226, s[24:27], s60 offen lds\ns_add_u32 m0, s51, 3072\ns_add_u32 s60, s70, 49152\nbuffer_load_dwordx4 v227, s[24:27], s60 offen lds\n"
"s_add_u32 m0, s51, 4096\ns_add_u32 s60, s71, 0\nbuffer_load_dwordx4 v226, s[24:27], s60 offen lds\ns_add_u32 m0, s51, 5120\ns_add_u32 s60, s71, 16384\nbuffer_load_dwordx4 v227, s[24:27], s60 offen lds\n"
"s_add_u32 m0, s51, 6144\ns_add_u32 s60, s71, 32768\nbuffer_load_dwordx4 v226, s[24:27], s60 offen lds\ns_add_u32 m0, s51, 7168\ns_add_u32 s60, s71, 49152\nbuffer_load_dwordx4 v227, s[24:27], s60 offen lds\n"
"buffer_load_dwordx2 v[230:231], off, s[20:23], 0 sc1\nv_bfe_u32 v96, v252, 5, 2\nv_bfe_u32 v97, v252, 8, 2\nv_xor_b32 v96, v96, v97\ns_and_b32 s60, s40, 1\ns_lshl_b32 s60, s60, 4\n"
"s_and_b32 s61, s40, 2\ns_lshl1_add_u32 s60, s61, s60\nv_bfe_u32 v97, v252, 7, 2\nv_lshrrev_b32 v98, 9, v252\nv_lshl_add_u32 v97, v98, 3, v97\nv_add_u32 v97, s60, v97\n"
"v_mul_u32_u24 v97, 6144, v97\nv_lshl_add_u32 v97, v96, 5, v97\nv_and_b32 v96, 16, v252\nv_add_u32 v220, v96, v97\nv_add_u32 v221, 3144704, v220\ns_add_u32 m0, s50, 0\n"
"s_add_u32 s63, s52, 2048\nbuffer_load_dwordx4 v220, s[4:7], s63 offen lds\nbuffer_load_dwordx4 v221, s[4:7], s63 offen offset:1024 lds\ns_add_u32 m0, s50, 8192\ns_add_u32 s63, s52, 198656\nbuffer_load_dwordx4 v220, s[4:7], s63 offen lds\n"
"buffer_load_dwordx4 v221, s[4:7], s63 offen offset:1024 lds\ns_lshl2_add_u32 s60, s41, s40\ns_mul_i32 s79, s60, 2176\ns_and_b32 s61, s60, -2\ns_mul_i32 s80, s61, 2176\ns_lshl2_add_u32 s61, s61, s43\n"
"s_mul_i32 s82, s61, 640\ns_mul_i32 s61, s43, 28672\ns_mul_i32 s73, s60, 229376\ns_add_u32 s73, s73, s61\ns_mul_i32 s62, s90, 14336\ns_add_u32 s68, s73, s62\n"
"s_add_u32 s61, s61, s62\ns_xor_b32 s62, s60, 1\ns_mul_i32 s69, s62, 229376\ns_add_u32 s69, s69, s61\ns_mul_i32 s61, s44, 7168\ns_add_u32 s73, s73, s61\n"
"s_lshl2_add_u32 s61, s60, s43\ns_mul_i32 s81, s61, 640\ns_lshl2_add_u32 s61, s60, s44\ns_mul_i32 s74, s61, 640\ns_lshl2_add_u32 s61, s41, s44\ns_mul_i32 s61, s61, 48\n"
"s_add_u32 s61, s61, s40\ns_lshl_b32 s72, s61, 12\ns_mul_i32 s55, s42, 448\ns_add_u32 s54, s55, 64\ns_and_b32 s54, s54, 0xffffff80\ns_bitcmp0_b32 s42, 0\n"
"s_cselect_b32 s61, 384, 0\ns_add_u32 s55, s55, s61\ns_lshl_b32 s61, s41, 9\ns_lshl_b32 s91, s42, 13\ns_add_u32 s91, s91, s61\ns_add_u32 s91, s91, 83886080\n"
"s_mul_i32 s61, s43, 1835008\ns_lshl_b32 s62, s44, 9\ns_add_u32 s61, s61, s62\ns_add_u32 s62, s43, 1\ns_and_b32 s62, s62, 3\ns_lshl_b32 s62, s62, 7\n"
"s_add_u32 s56, s61, s62\ns_add_u32 s62, s43, 2\ns_and_b32 s62, s62, 3\ns_lshl_b32 s62, s62, 7\ns_add_u32 s57, s61, s62\ns_add_u32 s62, s43, 3\n"
"s_and_b32 s62, s62, 3\ns_lshl_b32 s62, s62, 7\ns_add_u32 s58, s61, s62\ns_add_u32 s62, s43, 4\ns_and_b32 s62, s62, 3\ns_lshl_b32 s62, s62, 7\n"
"s_add_u32 s59, s61, s62\ns_mov_b32 s76, 0x3f803f80\ns_mov_b32 s77, 0xffff0000\ns_mov_b32 s48, 131072\ns_cmp_eq_u32 s60, 0\ns_cselect_b32 s78, 0xf149f2ca, 0\n"
"v_bfe_u32 v96, v252, 6, 3\nv_lshlrev_b32 v96, 7, v96\nv_lshrrev_b32 v97, 9, v252\nv_lshl_add_u32 v96, v97, 11, v96\nv_bfe_u32 v97, v252, 4, 2\nv_lshl_add_u32 v96, v97, 3, v96\n"
"s_lshl_b32 s60, s90, 10\ns_add_u32 s60, s60, 32768\nv_add_u32 v96, s60, v96\nv_bfe_u32 v97, v252, 7, 2\nv_xor_b32 v98, 0, v97\nv_lshl_add_u32 v184, v98, 5, v96\n"
"v_add_u32 v188, 0x10000, v184\nv_xor_b32 v98, 1, v97\nv_lshl_add_u32 v185, v98, 5, v96\nv_add_u32 v189, 0x10000, v185\nv_xor_b32 v98, 2, v97\nv_lshl_add_u32 v186, v98, 5, v96\n"
"v_add_u32 v190, 0x10000, v186\nv_xor_b32 v98, 3, v97\nv_lshl_add_u32 v187, v98, 5, v96\nv_add_u32 v191, 0x10000, v187\nv_lshrrev_b32 v96, 2, v252\ns_xor_b32 s60, s51, 0x2000\n"
"v_add_u32 v225, s60, v96\nv_mov_b32 v210, 0\nv_mov_b32 v211, 0\nv_bfe_u32 v96, v252, 5, 2\nv_bfe_u32 v97, v252, 8, 2\nv_xor_b32 v96, v96, v97\n"
"v_lshrrev_b32 v97, 7, v252\ns_lshl_b32 s60, s40, 3\nv_add_u32 v97, s60, v97\nv_mov_b32 v228, v97\nv_and_b32 v97, 16, v252\nv_lshl_add_u32 v229, v96, 5, v97\n"
"v_bfe_u32 v96, v252, 5, 2\nv_bfe_u32 v97, v252, 8, 2\nv_xor_b32 v96, v96, v97\nv_lshrrev_b32 v97, 7, v252\ns_lshl_b32 s60, s40, 3\nv_add_u32 v97, s60, v97\n"
"v_lshrrev_b32 v98, 1, v96\nv_lshl_add_u32 v97, v98, 5, v97\nv_and_b32 v96, 1, v96\nv_mul_u32_u24 v97, 7168, v97\nv_lshl_add_u32 v97, v96, 5, v97\nv_and_b32 v96, 16, v252\n"
"v_add_u32 v222, v96, v97\nv_add_u32 v223, 3668992, v222\nv_bfe_u32 v96, v252, 4, 4\nv_lshrrev_b32 v97, 8, v252\nv_lshrrev_b32 v98, 1, v96\nv_xor_b32 v97, v97, v98\n"
"v_lshlrev_b32 v96, 7, v96\nv_lshl_add_u32 v96, v97, 4, v96\nv_add_u32 v248, s51, v96\nv_xor_b32 v249, 64, v248\nv_add_u32 v250, 122880, v248\nv_add_u32 v251, 122880, v249\n"
"s_add_u32 m0, s51, 122880\ns_add_u32 s60, s70, 128\nbuffer_load_dwordx4 v226, s[24:27], s60 offen lds\ns_add_u32 m0, s51, 123904\ns_add_u32 s60, s70, 16512\nbuffer_load_dwordx4 v227, s[24:27], s60 offen lds\n"
"s_add_u32 m0, s51, 124928\ns_add_u32 s60, s70, 32896\nbuffer_load_dwordx4 v226, s[24:27], s60 offen lds\ns_add_u32 m0, s51, 125952\ns_add_u32 s60, s70, 49280\nbuffer_load_dwordx4 v227, s[24:27], s60 offen lds\n"
"s_add_u32 m0, s51, 126976\ns_add_u32 s60, s71, 128\nbuffer_load_dwordx4 v226, s[24:27], s60 offen lds\ns_add_u32 m0, s51, 128000\ns_add_u32 s60, s71, 16512\nbuffer_load_dwordx4 v227, s[24:27], s60 offen lds\n"
"s_add_u32 m0, s51, 129024\ns_add_u32 s60, s71, 32896\nbuffer_load_dwordx4 v226, s[24:27], s60 offen lds\ns_add_u32 m0, s51, 130048\ns_add_u32 s60, s71, 49280\nbuffer_load_dwordx4 v227, s[24:27], s60 offen lds\n"
"s_add_u32 m0, s50, 16384\ns_add_u32 s63, s52, 4096\nbuffer_load_dwordx4 v220, s[4:7], s63 offen lds\nbuffer_load_dwordx4 v221, s[4:7], s63 offen offset:1024 lds\ns_add_u32 m0, s50, 24576\ns_add_u32 s63, s52, 200704\n"
"buffer_load_dwordx4 v220, s[4:7], s63 offen lds\nbuffer_load_dwordx4 v221, s[4:7], s63 offen offset:1024 lds\ns_add_u32 m0, s50, 32768\ns_add_u32 s63, s101, 2048\nbuffer_load_dwordx4 v220, s[4:7], s63 offen lds\nbuffer_load_dwordx4 v221, s[4:7], s63 offen offset:1024 lds\n"
"s_add_u32 m0, s50, 40960\ns_add_u32 s63, s101, 4096\nbuffer_load_dwordx4 v220, s[4:7], s63 offen lds\nbuffer_load_dwordx4 v221, s[4:7], s63 offen offset:1024 lds\nv_bfe_u32 v246, v252, 4, 4\nv_lshlrev_b32 v246, 11, v246\n"
"v_lshrrev_b32 v247, 8, v252\nv_lshl_add_u32 v246, v247, 3, v246\ns_add_u32 s84, s49, s53\ns_add_u32 s85, s84, 32768\nbuffer_load_dwordx2 v[192:193], v246, s[24:27], s84 offen offset:0\nbuffer_load_dwordx2 v[194:195], v246, s[24:27], s84 offen offset:32\n"
"buffer_load_dwordx2 v[196:197], v246, s[24:27], s84 offen offset:64\nbuffer_load_dwordx2 v[198:199], v246, s[24:27], s84 offen offset:96\nbuffer_load_dwordx2 v[200:201], v246, s[24:27], s85 offen offset:0\nbuffer_load_dwordx2 v[202:203], v246, s[24:27], s85 offen offset:32\nbuffer_load_dwordx2 v[204:205], v246, s[24:27], s85 offen offset:64\nbuffer_load_dwordx2 v[206:207], v246, s[24:27], s85 offen offset:96\n"
"v_mov_b32 v106, 0xf149f149\nv_mov_b32 v104, 0xffff\nv_mov_b32 v105, 0xffff0000\ns_mov_b32 s60, 0x50000\ns_mov_b32 s61, 0x5550055\nv_cndmask_b32 v96, 0, v104, s[60:61]\n"
"s_mov_b32 s60, 0xa0000\ns_mov_b32 s61, 0xaaa00aa\nv_cndmask_b32 v96, v96, v105, s[60:61]\nv_bfi_b32 v100, v96, 0, v106\ns_mov_b32 s60, 0x150001\ns_mov_b32 s61, 0x15550155\n"
"v_cndmask_b32 v97, 0, v104, s[60:61]\ns_mov_b32 s60, 0x2a0002\ns_mov_b32 s61, 0x2aaa02aa\nv_cndmask_b32 v97, v97, v105, s[60:61]\nv_bfi_b32 v101, v97, 0, v106\ns_mov_b32 s60, 0x55505555\n"
"s_mov_b32 s61, 0x50005500\nv_cndmask_b32 v98, 0, v104, s[60:61]\ns_mov_b32 s60, 0xaaa0aaaa\ns_mov_b32 s61, 0xa000aa00\nv_cndmask_b32 v98, v98, v105, s[60:61]\nv_bfi_b32 v102, v98, 0, v106\n"
"s_mov_b32 s60, 0x55405554\ns_mov_b32 s61, 0x40005400\nv_cndmask_b32 v99, 0, v104, s[60:61]\ns_mov_b32 s60, 0xaa80aaa8\ns_mov_b32 s61, 0x8000a800\nv_cndmask_b32 v99, v99, v105, s[60:61]\n"
"v_bfi_b32 v103, v99, 0, v106\nv_add_u32 v104, 161792, v252\nds_write_b128 v104, v[96:99] offset:0\nds_write_b128 v104, v[100:103] offset:1024\ns_waitcnt vmcnt(28)\nv_readfirstlane_b32 s83, v230\n"
"v_readfirstlane_b32 s60, v231\ns_mul_i32 s46, s83, 0x9e3779b1\ns_add_u32 s46, s46, 0x7f4a7c15\ns_xor_b32 s47, s83, s60\ns_mul_i32 s47, s47, 0x85ebca6b\ns_add_u32 s47, s47, 0xc2b2ae35\n"
"v_mov_b32 v144, -1\nv_mov_b32 v145, -1\nv_mov_b32 v146, -1\nv_mov_b32 v147, -1\ns_mov_b64 exec, 0\ns_cmp_eq_u32 s40, 1\n"
"s_cbranch_scc1 Lpg2\ns_cmp_eq_u32 s40, 2\ns_cbranch_scc1 Lpg3\ns_cmp_eq_u32 s40, 3\ns_cbranch_scc1 Lpg4\ns_branch Lpg1\n"
"LretQ0:\ns_mov_b64 exec, -1\nds_read_b128 a[0:3], v248 offset:0\nds_read_b128 a[4:7], v248 offset:2048\nds_read_b128 a[8:11], v248 offset:4096\nds_read_b128 a[12:15], v248 offset:6144\n"
"ds_read_b128 a[16:19], v249 offset:0\nds_read_b128 a[20:23], v249 offset:2048\nds_read_b128 a[24:27], v249 offset:4096\nds_read_b128 a[28:31], v249 offset:6144\ns_waitcnt vmcnt(12) lgkmcnt(8)\ns_barrier\n"
"R 160,184,0\nR 162,184,4096\nR 164,184,8192\nR 166,184,12288\nR 168,185,0\nR 170,185,4096\n"
"R 172,185,8192\nR 174,185,12288\nR 176,186,0\nR 178,186,4096\nR 180,186,8192\nR 182,186,12288\n"
"G 10\nv_mfma_f32_16x16x32_bf16 v[0:3], v[160:163], a[0:3], 0\ns_add_u32 m0, s50, 49152\ns_add_u32 s63, s52, 395264\nbuffer_load_dwordx4 v220, s[4:7], s63 offen lds\nG 8\n"
"M 0,164,16\nbuffer_load_dwordx4 v221, s[4:7], s63 offen offset:1024 lds\nv_mfma_f32_16x16x32_bf16 v[16:19], v[160:163], a[4:7], 0\ns_add_u32 m0, s51, 0\ns_add_u32 s60, s70, 256\nbuffer_load_dwordx4 v226, s[24:27], s60 offen lds\n"
"M 16,164,20\ns_lshl4_add_u32 s62, s41, s42\ns_lshl_b32 s62, s62, 12\ns_add_u32 s62, s62, 77594624\ns_lshl_b32 s60, s40, 10\ns_add_u32 s62, s62, s60\n"
"buffer_store_dwordx4 v[144:147], v252, s[20:23], s62 offen sc1\ns_add_u32 m0, s50, 57344\ns_add_u32 s63, s52, 591872\nv_mfma_f32_16x16x32_bf16 v[32:35], v[160:163], a[8:11], 0\nbuffer_load_dwordx4 v220, s[4:7], s63 offen lds\nM 32,164,24\n"
"buffer_load_dwordx4 v221, s[4:7], s63 offen offset:1024 lds\nv_mfma_f32_16x16x32_bf16 v[48:51], v[160:163], a[12:15], 0\ns_add_u32 m0, s51, 1024\ns_add_u32 s60, s70, 16640\nbuffer_load_dwordx4 v227, s[24:27], s60 offen lds\nM 48,164,28\n"
"s_add_u32 m0, s50, 65536\ns_add_u32 s63, s52, 397312\nbuffer_load_dwordx4 v220, s[4:7], s63 offen lds\nG 4\nv_mfma_f32_16x16x32_bf16 v[52:55], v[168:171], a[12:15], 0\nR 160,187,0\n"
"M 52,172,28\nbuffer_load_dwordx4 v221, s[4:7], s63 offen offset:1024 lds\nv_mfma_f32_16x16x32_bf16 v[36:39], v[168:171], a[8:11], 0\nR 162,187,4096\nM 36,172,24\ns_add_u32 m0, s51, 2048\n"
"s_add_u32 s60, s70, 33024\nbuffer_load_dwordx4 v226, s[24:27], s60 offen lds\nv_mfma_f32_16x16x32_bf16 v[20:23], v[168:171], a[4:7], 0\nR 164,187,8192\ns_add_u32 m0, s50, 73728\nM 20,172,20\n"
"s_lshl4_add_u32 s62, s41, s42\ns_lshl_b32 s62, s62, 12\ns_add_u32 s62, s62, 78643200\ns_lshl_b32 s60, s40, 10\ns_add_u32 s62, s62, s60\nbuffer_store_dwordx4 v[144:147], v252, s[20:23], s62 offen sc1\n"
"s_add_u32 s63, s52, 593920\nv_mfma_f32_16x16x32_bf16 v[4:7], v[168:171], a[0:3], 0\nR 166,187,12288\nbuffer_load_dwordx4 v220, s[4:7], s63 offen lds\nM 4,172,16\nG 4\n"
"v_mfma_f32_16x16x32_bf16 v[8:11], v[176:179], a[0:3], 0\nR 168,184,16384\nM 8,180,16\ns_add_u32 m0, s51, 3072\ns_add_u32 s60, s70, 49408\nbuffer_load_dwordx4 v227, s[24:27], s60 offen lds\n"
"v_mfma_f32_16x16x32_bf16 v[24:27], v[176:179], a[4:7], 0\nR 170,184,20480\ns_add_u32 m0, s50, 73728\ns_nop 0\nbuffer_load_dwordx4 v221, s[4:7], s63 offen offset:1024 lds\nM 24,180,20\n"
"s_add_u32 m0, s51, 4096\ns_add_u32 s60, s71, 256\nbuffer_load_dwordx4 v226, s[24:27], s60 offen lds\nv_mfma_f32_16x16x32_bf16 v[40:43], v[176:179], a[8:11], 0\nR 172,184,24576\nM 40,180,24\n"
"v_mfma_f32_16x16x32_bf16 v[56:59], v[176:179], a[12:15], 0\nR 174,184,28672\nM 56,180,28\nG 4\nv_mfma_f32_16x16x32_bf16 v[60:63], v[160:163], a[12:15], 0\nR 176,185,16384\n"
"M 60,164,28\ns_add_u32 m0, s51, 5120\ns_add_u32 s60, s71, 16640\nbuffer_load_dwordx4 v227, s[24:27], s60 offen lds\nv_mfma_f32_16x16x32_bf16 v[44:47], v[160:163], a[8:11], 0\nR 178,185,20480\n"
"M 44,164,24\ns_add_u32 m0, s51, 6144\ns_add_u32 s60, s71, 33024\nbuffer_load_dwordx4 v226, s[24:27], s60 offen lds\nv_mfma_f32_16x16x32_bf16 v[28:31], v[160:163], a[4:7], 0\nR 180,185,24576\n"
"s_add_u32 m0, s50, 81920\ns_add_u32 s63, s101, 0\nM 28,164,20\nbuffer_load_dwordx4 v220, s[4:7], s63 offen lds\nv_mfma_f32_16x16x32_bf16 v[12:15], v[160:163], a[0:3], 0\nR 182,185,28672\n"
"M 12,164,16\ns_waitcnt vmcnt(12)\ns_barrier\nG 4\nv_mfma_f32_16x16x32_bf16 v[64:67], a[0:3], v[168:171], 0\nR 160,186,16384\n"
"N 64,16,172\ns_add_u32 m0, s51, 7168\ns_add_u32 s60, s71, 49408\nbuffer_load_dwordx4 v227, s[24:27], s60 offen lds\nv_mfma_f32_16x16x32_bf16 v[68:71], a[4:7], v[168:171], 0\nR 162,186,20480\n"
"s_add_u32 m0, s50, 81920\nN 68,20,172\nbuffer_load_dwordx4 v221, s[4:7], s63 offen offset:1024 lds\nv_mfma_f32_16x16x32_bf16 v[96:99], a[8:11], v[168:171], 0\nR 164,186,24576\ns_add_u32 m0, s50, 0\n"
"s_add_u32 s63, s52, 788480\nN 96,24,172\nbuffer_load_dwordx4 v220, s[4:7], s63 offen lds\nv_mfma_f32_16x16x32_bf16 v[100:103], a[12:15], v[168:171], 0\nR 166,186,28672\nN 100,28,172\n"
"G 4\nv_mfma_f32_16x16x32_bf16 v[108:111], a[12:15], v[176:179], 0\nR 168,187,16384\nv_mov_b32 v242, s76\nv_mov_b32 v243, s76\nN 108,28,180\n"
"buffer_load_dwordx4 v221, s[4:7], s63 offen offset:1024 lds\nv_mov_b32 v244, s76\nv_mfma_f32_16x16x32_bf16 v[104:107], a[8:11], v[176:179], 0\nR 170,187,20480\ns_add_u32 s63, s52, 985088\nN 104,24,180\n"
"s_add_u32 m0, s50, 8192\ns_lshl_b32 s60, s40, 5\nv_xor_b32 v238, s60, v184\nv_mfma_f32_16x16x32_bf16 v[76:79], a[4:7], v[176:179], 0\nR 172,187,24576\nv_and_b32 v238, 0xfffffbff, v238\n"
"N 76,20,180\nbuffer_load_dwordx4 v220, s[4:7], s63 offen lds\nv_mfma_f32_16x16x32_bf16 v[72:75], a[0:3], v[176:179], 0\nR 174,187,28672\nv_mov_b32 v245, s76\nN 72,16,180\n"
"G 4\nv_mfma_f32_16x16x32_bf16 v[80:83], a[0:3], v[160:163], 0\nR 176,184,49152\nds_read_b128 a[32:35], v250 offset:0\nR 128,238,32768\nR 130,238,36864\n"
"N 80,16,164\nds_read_b128 a[36:39], v250 offset:2048\nv_mfma_f32_16x16x32_bf16 v[84:87], a[4:7], v[160:163], 0\nR 178,184,53248\nds_read_b128 a[40:43], v250 offset:4096\nR 132,238,33792\n"
"R 134,238,37888\nN 84,20,164\nds_read_b128 a[44:47], v250 offset:6144\nv_mfma_f32_16x16x32_bf16 v[112:115], a[8:11], v[160:163], 0\nR 180,184,57344\nbuffer_load_dwordx4 v221, s[4:7], s63 offen offset:1024 lds\n"
"N 112,24,164\nds_read_b128 a[48:51], v251 offset:0\nv_mfma_f32_16x16x32_bf16 v[116:119], a[12:15], v[160:163], 0\nR 182,184,61440\nds_read_b128 a[52:55], v251 offset:2048\nN 116,28,164\n"
"ds_read_b128 a[56:59], v251 offset:4096\nG 15\nv_mfma_f32_16x16x32_bf16 v[124:127], a[12:15], v[168:171], 0\nR 160,185,49152\nG 12\nv_mfma_f32_16x16x32_bf16 v[136:139], v[242:245], v[128:131], 0\n"
"N 124,28,172\nds_read_b128 a[60:63], v251 offset:6144\nG 8\nv_mfma_f32_16x16x32_bf16 v[136:139], v[242:245], v[132:135], v[136:139]\nv_mfma_f32_16x16x32_bf16 v[120:123], a[8:11], v[168:171], 0\nR 162,185,53248\n"
"N 120,24,172\nv_mfma_f32_16x16x32_bf16 v[92:95], a[4:7], v[168:171], 0\nR 164,185,57344\nN 92,20,172\nG 2\ns_add_u32 m0, s51, 122880\n"
"s_add_u32 s60, s70, 384\nbuffer_load_dwordx4 v226, s[24:27], s60 offen lds\nv_mfma_f32_16x16x32_bf16 v[88:91], a[0:3], v[168:171], 0\nR 166,185,61440\nv_lshrrev_b32 v128, 1, v252\nv_mov_b32 v137, s46\n"
"N 88,16,172\ns_waitcnt vmcnt(11)\ns_barrier\nM 0,176,32\nR 168,186,49152\ns_lshl_b32 s60, s40, 5\n"
"v_xor_b32 v238, s60, v184\nM 0,180,48\ns_add_u32 m0, s51, 123904\ns_add_u32 s60, s70, 16768\nbuffer_load_dwordx4 v227, s[24:27], s60 offen lds\ns_and_b32 s61, s40, 3\n"
"s_lshl_b32 s61, s61, 7\ns_add_u32 s61, s61, s91\ns_add_u32 s61, s61, 131072\ns_mov_b64 exec, 0xffff\nbuffer_store_dwordx2 v[136:137], v128, s[20:23], s61 offen sc1\ns_mov_b64 exec, -1\n"
"M 16,176,36\nR 170,186,53248\ns_add_u32 m0, s50, 16384\ns_add_u32 s63, s52, 790528\nM 16,180,52\nbuffer_load_dwordx4 v220, s[4:7], s63 offen lds\n"
"M 32,176,40\nR 172,186,57344\nv_and_b32 v238, 0xfffffbff, v238\nM 32,180,56\ns_add_u32 m0, s51, 124928\ns_add_u32 s60, s70, 33152\n"
"buffer_load_dwordx4 v226, s[24:27], s60 offen lds\nM 48,176,44\nR 174,186,61440\ns_add_u32 m0, s50, 16384\nv_mov_b32 v230, 0\nLpg1:\n"
"s_cbranch_execz Lpg5\nM 48,180,60\nbuffer_load_dwordx4 v221, s[4:7], s63 offen offset:1024 lds\nG 4\nv_mov_b32 v231, 0\nM 52,160,44\n"
"R 176,187,49152\nR 128,238,40960\nR 130,238,45056\nv_mov_b32 v232, 0\nM 52,164,60\ns_add_u32 m0, s51, 125952\n"
"s_add_u32 s60, s70, 49536\nbuffer_load_dwordx4 v227, s[24:27], s60 offen lds\nv_mov_b32 v233, 0\nM 36,160,40\nR 178,187,53248\nR 132,238,41984\n"
"R 134,238,46080\ns_add_u32 m0, s50, 24576\ns_add_u32 s63, s52, 987136\nv_dot2c_f32_bf16 v230, s76, v192\nM 36,164,56\nbuffer_load_dwordx4 v220, s[4:7], s63 offen lds\n"
"v_dot2c_f32_bf16 v231, v192, v192\nM 20,160,36\nR 180,187,57344\nv_lshrrev_b32 v234, 5, v252\nv_dot2c_f32_bf16 v232, s76, v200\nM 20,164,52\n"
"s_add_u32 m0, s51, 126976\ns_add_u32 s60, s71, 384\nbuffer_load_dwordx4 v226, s[24:27], s60 offen lds\nv_dot2c_f32_bf16 v233, v200, v200\nM 4,160,32\nR 182,187,61440\n"
"s_add_u32 m0, s50, 24576\nv_dot2c_f32_bf16 v230, s76, v193\nM 4,164,48\nG 8\nv_and_b32 v234, 24, v234\nv_bfe_u32 v235, s46, v234, 8\n"
"v_dot2c_f32_bf16 v231, v193, v193\nM 8,168,32\nR 160,188,0\nG 6\nv_mfma_f32_16x16x32_bf16 v[136:139], v[242:245], v[128:131], 0\nbuffer_load_dwordx4 v221, s[4:7], s63 offen offset:1024 lds\n"
"v_dot2c_f32_bf16 v232, s76, v201\nM 8,172,48\ns_add_u32 m0, s51, 128000\ns_add_u32 s60, s71, 16768\nbuffer_load_dwordx4 v227, s[24:27], s60 offen lds\nG 3\n"
"v_mfma_f32_16x16x32_bf16 v[136:139], v[242:245], v[132:135], v[136:139]\nv_dot2c_f32_bf16 v233, v201, v201\nM 24,168,36\nR 162,188,4096\nv_lshrrev_b32 v234, 1, v234\nv_dot2c_f32_bf16 v230, s76, v194\n"
"M 24,172,52\nv_and_b32 v236, 0xf0, v252\nv_add_u32 v234, v234, v236\nv_dot2c_f32_bf16 v231, v194, v194\nM 40,168,40\nR 164,188,8192\n"
"v_dot2c_f32_bf16 v232, s76, v202\nM 40,172,56\ns_add_u32 m0, s51, 129024\ns_add_u32 s60, s71, 33152\nbuffer_load_dwordx4 v226, s[24:27], s60 offen lds\nv_dot2c_f32_bf16 v233, v202, v202\n"
"M 56,168,44\nR 166,188,12288\nv_lshrrev_b32 v128, 1, v252\nv_dot2c_f32_bf16 v230, s76, v195\nM 56,172,60\nG 4\n"
"v_mov_b32 v137, s46\nv_dot2c_f32_bf16 v231, v195, v195\nM 60,176,44\nR 168,189,0\nv_dot2c_f32_bf16 v232, s76, v203\nM 60,180,60\n"
"s_add_u32 m0, s51, 130048\ns_add_u32 s60, s71, 49536\nbuffer_load_dwordx4 v227, s[24:27], s60 offen lds\ns_and_b32 s61, s40, 3\ns_lshl_b32 s61, s61, 7\ns_add_u32 s61, s61, s91\n"
"s_add_u32 s61, s61, 262144\ns_mov_b64 exec, 0xffff\nbuffer_store_dwordx2 v[136:137], v128, s[20:23], s61 offen sc1\ns_mov_b64 exec, -1\nv_dot2c_f32_bf16 v233, v203, v203\nM 44,176,40\n"
"R 170,189,4096\nv_dot2c_f32_bf16 v230, s76, v196\nM 44,180,56\nv_dot2c_f32_bf16 v231, v196, v196\nM 28,176,36\nR 172,189,8192\n"
"v_dot2c_f32_bf16 v232, s76, v204\nM 28,180,52\ns_add_u32 m0, s50, 32768\nv_dot2c_f32_bf16 v233, v204, v204\nM 12,176,32\nR 174,189,12288\n"
"v_dot2c_f32_bf16 v230, s76, v197\nM 12,180,48\ns_waitcnt vmcnt(14)\ns_barrier\nG 4\nv_dot2c_f32_bf16 v231, v197, v197\n"
"N 64,32,160\nR 176,190,0\nv_dot2c_f32_bf16 v232, s76, v205\nN 64,48,164\ns_add_u32 s63, s52, 1181696\nv_dot2c_f32_bf16 v233, v205, v205\n"
"N 68,36,160\nR 178,190,4096\nv_dot2c_f32_bf16 v230, s76, v198\nN 68,52,164\nbuffer_load_dwordx4 v220, s[4:7], s63 offen lds\nv_dot2c_f32_bf16 v231, v198, v198\n"
"N 96,40,160\nR 180,190,8192\nds_read_b128 a[64:67], v248 offset:0\nv_dot2c_f32_bf16 v232, s76, v206\nN 96,56,164\nds_read_b128 a[68:71], v248 offset:2048\n"
"v_dot2c_f32_bf16 v233, v206, v206\nN 100,44,160\nR 182,190,12288\nds_read_b128 a[72:75], v248 offset:4096\nv_dot2c_f32_bf16 v230, s76, v199\nN 100,60,164\n"
"ds_read_b128 a[76:79], v248 offset:6144\nG 8\nv_dot2c_f32_bf16 v231, v199, v199\nN 108,44,168\nR 160,191,0\ns_nop 0\n"
"v_permlane32_swap_b32 v230, v231\nv_dot2c_f32_bf16 v232, s76, v207\nN 108,60,172\nds_read_b128 a[80:83], v249 offset:0\nbuffer_load_dwordx4 v221, s[4:7], s63 offen offset:1024 lds\nv_dot2c_f32_bf16 v233, v207, v207\n"
"N 104,40,168\nR 162,191,4096\nds_read_b128 a[84:87], v249 offset:2048\nN 104,56,172\nds_read_b128 a[88:91], v249 offset:4096\ns_add_u32 m0, s50, 40960\n"
"N 76,36,168\nR 164,191,8192\ns_add_u32 s63, s52, 1378304\nN 76,52,172\nds_read_b128 a[92:95], v249 offset:6144\nv_permlane32_swap_b32 v232, v233\n"
"N 72,32,168\nR 166,191,12288\nbuffer_load_dwordx4 v220, s[4:7], s63 offen lds\nN 72,48,172\nG 10\nv_add_f32 v230, v230, v231\n"
"v_add_f32 v232, v232, v233\nN 80,32,176\nR 168,184,0\nv_permlane16_swap_b32 v230, v232\nN 80,48,180\nG 2\n"
"s_add_u32 m0, s51, 0\ns_add_u32 s60, s70, 512\nbuffer_load_dwordx4 v226, s[24:27], s60 offen lds\nN 84,36,176\nR 170,184,4096\ns_add_u32 m0, s50, 40960\n"
"s_nop 0\nbuffer_load_dwordx4 v221, s[4:7], s63 offen offset:1024 lds\nN 84,52,180\nv_add_f32 v230, v230, v232\ns_movk_i32 s62, 0xff\nv_bfi_b32 v230, s62, v235, v230\n"
"N 112,40,176\nR 172,184,8192\nN 112,56,180\ns_add_u32 m0, s51, 1024\ns_add_u32 s60, s70, 16896\nbuffer_load_dwordx4 v227, s[24:27], s60 offen lds\n"
"N 116,44,176\nR 174,184,12288\nN 116,60,180\nG 4\nN 124,44,160\nR 176,185,0\n"
"N 124,60,164\ns_add_u32 m0, s51, 2048\ns_add_u32 s60, s70, 33280\nbuffer_load_dwordx4 v226, s[24:27], s60 offen lds\nN 120,40,160\nR 178,185,4096\n"
"N 120,56,164\ns_lshl_b32 s60, s40, 5\nv_xor_b32 v238, s60, v188\ns_lshr_b32 s61, s49, 4\ns_lshl1_add_u32 s61, s53, s61\ns_add_u32 s61, s61, 81788928\n"
"buffer_store_dword v230, v234, s[20:23], s61 offen sc1\nv_and_b32 v238, 0xfffffbff, v238\nN 92,36,160\nR 180,185,8192\nN 92,52,164\ns_add_u32 m0, s51, 3072\n"
"s_add_u32 s60, s70, 49664\nbuffer_load_dwordx4 v227, s[24:27], s60 offen lds\nN 88,32,160\nR 182,185,12288\nN 88,48,164\ns_waitcnt vmcnt(13)\n"
"s_barrier\nG 4\nM 0,168,64\nR 160,186,0\nM 0,172,80\ns_add_u32 m0, s51, 4096\n"
"s_add_u32 s60, s71, 512\nbuffer_load_dwordx4 v226, s[24:27], s60 offen lds\nM 16,168,68\nR 162,186,4096\nR 128,238,16384\nR 130,238,20480\n"
"s_add_u32 m0, s50, 49152\ns_add_u32 s63, s52, 1183744\nM 16,172,84\nbuffer_load_dwordx4 v220, s[4:7], s63 offen lds\nM 32,168,72\nR 164,186,8192\n"
"R 132,238,17408\nR 134,238,21504\nM 32,172,88\ns_add_u32 m0, s51, 5120\ns_add_u32 s60, s71, 16896\nbuffer_load_dwordx4 v227, s[24:27], s60 offen lds\n"
"M 48,168,76\nR 166,186,12288\ns_add_u32 m0, s50, 49152\nM 48,172,92\nG 8\nM 52,176,76\n"
"R 168,187,0\nbuffer_load_dwordx4 v221, s[4:7], s63 offen offset:1024 lds\nM 52,180,92\ns_add_u32 m0, s51, 6144\ns_add_u32 s60, s71, 33280\nbuffer_load_dwordx4 v226, s[24:27], s60 offen lds\n"
"M 36,176,72\nR 170,187,4096\nG 6\nv_mfma_f32_16x16x32_bf16 v[136:139], v[242:245], v[128:131], 0\ns_add_u32 m0, s50, 57344\ns_add_u32 s63, s52, 1380352\n"
"M 36,180,88\nG 3\nv_mfma_f32_16x16x32_bf16 v[136:139], v[242:245], v[132:135], v[136:139]\nbuffer_load_dwordx4 v220, s[4:7], s63 offen lds\nM 20,176,68\nR 172,187,8192\n"
"M 20,180,84\ns_add_u32 m0, s51, 7168\ns_add_u32 s60, s71, 49664\nbuffer_load_dwordx4 v227, s[24:27], s60 offen lds\nM 4,176,64\nR 174,187,12288\n"
"s_add_u32 m0, s50, 57344\nM 4,180,80\nG 4\nM 8,160,64\nR 176,184,16384\nbuffer_load_dwordx4 v221, s[4:7], s63 offen offset:1024 lds\n"
"M 8,164,80\nv_lshrrev_b32 v128, 1, v252\nv_mov_b32 v137, s46\nM 24,160,68\nR 178,184,20480\nM 24,164,84\n"
"s_and_b32 s61, s40, 3\ns_lshl_b32 s61, s61, 7\ns_add_u32 s61, s61, s91\ns_add_u32 s61, s61, 0\ns_mov_b64 exec, 0xffff\nbuffer_store_dwordx2 v[136:137], v128, s[20:23], s61 offen sc1\n"
"s_mov_b64 exec, -1\nM 40,160,72\nR 180,184,24576\nM 40,164,88\nM 56,160,76\nR 182,184,28672\n"
"M 56,164,92\nG 4\nM 60,168,76\nR 160,185,16384\nM 60,172,92\ns_add_u32 m0, s50, 65536\n"
"M 44,168,72\nR 162,185,20480\nM 44,172,88\ns_add_u32 s63, s52, 1574912\nM 28,168,68\nR 164,185,24576\n"
"M 28,172,84\nbuffer_load_dwordx4 v220, s[4:7], s63 offen lds\nM 12,168,64\nR 166,185,28672\nM 12,172,80\ns_waitcnt vmcnt(14)\n"
"s_barrier\nG 4\nN 64,64,176\nR 168,186,16384\nN 64,80,180\nv_lshrrev_b32 v230, 7, v252\n"
"N 68,68,176\nR 170,186,20480\nN 68,84,180\nbuffer_load_dwordx4 v221, s[4:7], s63 offen offset:1024 lds\nv_lshlrev_b32 v230, 9, v230\nN 96,72,176\n"
"R 172,186,24576\ns_add_u32 m0, s50, 73728\nN 96,88,180\nds_read_b128 a[96:99], v250 offset:0\nN 100,76,176\nR 174,186,28672\n"
"ds_read_b128 a[100:103], v250 offset:2048\ns_add_u32 s63, s52, 1771520\nN 100,92,180\nds_read_b128 a[104:107], v250 offset:4096\nG 7\nN 108,76,160\n"
"R 176,187,16384\nv_and_b32 v231, 0x70, v252\nN 108,92,164\nds_read_b128 a[108:111], v250 offset:6144\nbuffer_load_dwordx4 v220, s[4:7], s63 offen lds\nN 104,72,160\n"
"R 178,187,20480\nds_read_b128 a[112:115], v251 offset:0\nN 104,88,164\nds_read_b128 a[116:119], v251 offset:2048\ns_and_b32 s60, s40, 3\ns_lshl_b32 s60, s60, 7\n"
"v_add3_u32 v230, v230, v231, s60\nN 76,68,160\nR 180,187,24576\nds_read_b128 a[120:123], v251 offset:4096\nN 76,84,164\nds_read_b128 a[124:127], v251 offset:6144\n"
"N 72,64,160\nR 182,187,28672\nN 72,80,164\ns_lshl_b32 s61, s42, 13\ns_add_u32 s61, s61, 84017152\nbuffer_load_dwordx4 v[128:131], v230, s[20:23], s61 offen sc1\n"
"G 11\nN 80,64,168\nR 160,184,32768\nbuffer_load_dwordx4 v221, s[4:7], s63 offen offset:1024 lds\nN 80,80,172\ns_lshl_b32 s61, s42, 13\n"
"s_add_u32 s61, s61, 84021248\nbuffer_load_dwordx4 v[132:135], v230, s[20:23], s61 offen sc1\nN 84,68,168\nR 162,184,36864\nN 84,84,172\nN 112,72,168\n"
"R 164,184,40960\ns_waitcnt vmcnt(8)\nds_read_b128 a[128:131], v248 offset:0\nN 112,88,172\nds_read_b128 a[132:135], v248 offset:2048\nN 116,76,168\n"
"R 166,184,45056\nds_read_b128 a[136:139], v248 offset:4096\nN 116,92,172\nds_read_b128 a[140:143], v248 offset:6144\nG 8\nN 124,76,176\n"
"R 168,185,32768\nN 124,92,180\nds_read_b128 a[144:147], v249 offset:0\nN 120,72,176\nR 170,185,36864\nds_read_b128 a[148:151], v249 offset:2048\n"
"N 120,88,180\nds_read_b128 a[152:155], v249 offset:4096\nN 92,68,176\nR 172,185,40960\nN 92,84,180\nds_read_b128 a[156:159], v249 offset:6144\n"
"N 88,64,176\nR 174,185,45056\nN 88,80,180\ns_waitcnt vmcnt(7)\ns_barrier\nG 10\n"
"M 0,160,96\nR 176,186,32768\nM 0,164,112\nG 2\ns_add_u32 m0, s51, 0\ns_add_u32 s60, s70, 640\n"
"buffer_load_dwordx4 v226, s[24:27], s60 offen lds\nM 16,160,100\nR 178,186,36864\nM 16,164,116\ns_add_u32 m0, s51, 1024\ns_add_u32 s60, s70, 17024\n"
"buffer_load_dwordx4 v227, s[24:27], s60 offen lds\nM 32,160,104\nR 180,186,40960\ns_add_u32 m0, s50, 81920\ns_add_u32 s63, s52, 1576960\nbuffer_load_dwordx4 v220, s[4:7], s63 offen lds\n"
"v_lshrrev_b32 v234, 7, v252\nM 32,164,120\ns_add_u32 m0, s51, 2048\ns_add_u32 s60, s70, 33408\nbuffer_load_dwordx4 v226, s[24:27], s60 offen lds\nM 48,160,108\n"
"R 182,186,45056\nM 48,164,124\ns_add_u32 m0, s51, 3072\ns_add_u32 s60, s70, 49792\nbuffer_load_dwordx4 v227, s[24:27], s60 offen lds\nG 4\n"
"M 52,168,108\nR 160,187,32768\ns_add_u32 m0, s50, 81920\ns_nop 0\nbuffer_load_dwordx4 v221, s[4:7], s63 offen offset:1024 lds\nv_lshlrev_b32 v234, 9, v234\n"
"M 52,172,124\ns_add_u32 m0, s51, 4096\ns_add_u32 s60, s71, 640\nbuffer_load_dwordx4 v226, s[24:27], s60 offen lds\nM 36,168,104\nR 162,187,36864\n"
"v_and_b32 v235, 0x70, v252\nM 36,172,120\ns_add_u32 m0, s51, 5120\ns_add_u32 s60, s71, 17024\nbuffer_load_dwordx4 v227, s[24:27], s60 offen lds\nM 20,168,100\n"
"R 164,187,40960\ns_add_u32 m0, s50, 90112\ns_add_u32 s63, s52, 1773568\nbuffer_load_dwordx4 v220, s[4:7], s63 offen lds\nM 20,172,116\ns_add_u32 m0, s51, 6144\n"
"s_add_u32 s60, s71, 33408\nbuffer_load_dwordx4 v226, s[24:27], s60 offen lds\nM 4,168,96\nR 166,187,45056\ns_and_b32 s60, s40, 3\ns_lshl_b32 s60, s60, 7\n"
"v_add3_u32 v234, v234, v235, s60\nM 4,172,112\ns_add_u32 m0, s51, 7168\ns_add_u32 s60, s71, 49792\nbuffer_load_dwordx4 v227, s[24:27], s60 offen lds\nG 4\n"
"M 8,176,96\nR 168,184,49152\ns_add_u32 m0, s50, 90112\ns_nop 0\nbuffer_load_dwordx4 v221, s[4:7], s63 offen offset:1024 lds\nM 8,180,112\n"
"s_lshl_b32 s61, s42, 13\ns_add_u32 s61, s61, 84148224\nbuffer_load_dwordx4 v[144:147], v234, s[20:23], s61 offen sc1\nM 24,176,100\nR 170,184,53248\nM 24,180,116\n"
"M 40,176,104\nR 172,184,57344\nM 40,180,120\ns_lshl_b32 s61, s42, 13\ns_add_u32 s61, s61, 84152320\nbuffer_load_dwordx4 v[148:151], v234, s[20:23], s61 offen sc1\n"
"M 56,176,108\nR 174,184,61440\nM 56,180,124\nG 4\nM 60,160,108\nR 176,185,49152\n"
"M 60,164,124\nM 44,160,104\nR 178,185,53248\nM 44,164,120\ns_add_u32 m0, s50, 98304\nM 28,160,100\n"
"R 180,185,57344\nM 28,164,116\ns_add_u32 s63, s52, 1968128\nM 12,160,96\nR 182,185,61440\nM 12,164,112\n"
"s_waitcnt vmcnt(15)\ns_barrier\nG 4\nN 64,96,168\nR 160,186,49152\ns_waitcnt vmcnt(14)\n"
"v_min3_u32 v232, v129, v131, v133\nv_min3_u32 v232, v232, v135, v135\nN 64,112,172\nbuffer_load_dwordx4 v220, s[4:7], s63 offen lds\nv_max3_u32 v233, v129, v131, v133\nN 68,100,168\n"
"R 162,186,53248\nv_max3_u32 v233, v233, v135, v135\nv_cmp_ne_u32 s[86:87], s46, v232\nN 68,116,172\nv_cmp_ne_u32 s[88:89], s46, v233\nN 96,104,168\n"
"R 164,186,57344\ns_or_b64 s[86:87], s[86:87], s[88:89]\ns_cbranch_scc1 Lq116\nLq117:\nN 96,120,172\nbuffer_load_dwordx4 v221, s[4:7], s63 offen offset:1024 lds\n"
"v_add_f32 v128, v128, v132\nN 100,108,168\nR 166,186,61440\ns_add_u32 m0, s50, 106496\nv_add_f32 v130, v130, v134\nN 100,124,172\n"
"G 4\nv_add_f32_dpp v128, v128, v128 row_ror:8 row_mask:0xf bank_mask:0xf\nv_add_f32_dpp v130, v130, v130 row_ror:8 row_mask:0xf bank_mask:0xf\nv_mov_b32 v132, v128\nN 108,108,176\nR 168,187,49152\n"
"s_add_u32 s63, s52, 2164736\nv_mov_b32 v133, v130\nN 108,124,180\nbuffer_load_dwordx4 v220, s[4:7], s63 offen lds\nv_permlane32_swap_b32 v132, v128\nN 104,104,176\n"
"R 170,187,53248\nv_permlane32_swap_b32 v133, v130\nv_add_f32 v128, v128, v132\nN 104,120,180\nv_add_f32 v130, v130, v133\nv_mov_b32 v132, v128\n"
"v_mov_b32 v133, v130\nN 76,100,176\nR 172,187,57344\nv_permlane16_swap_b32 v132, v128\nv_permlane16_swap_b32 v133, v130\nN 76,116,180\n"
"buffer_load_dwordx4 v221, s[4:7], s63 offen offset:1024 lds\nv_add_f32 v128, v128, v132\nN 72,96,176\nR 174,187,61440\ns_add_u32 s63, s52, 1970176\nN 72,112,180\n"
"G 4\nv_add_f32 v130, v130, v133\nv_and_b32 v136, s77, v128\nN 80,96,160\nR 176,188,0\nv_sub_f32 v137, v128, v136\n"
"N 80,112,164\ns_add_u32 m0, s50, 114688\nv_cvt_pk_bf16_f32 v132, v136, v137\nv_cvt_pk_bf16_f32 v133, v136, 0\nN 84,100,160\nR 178,188,4096\n"
"v_and_b32 v136, s77, v130\nN 84,116,164\nLpg2:\ns_cbranch_execz LretQ0\nbuffer_load_dwordx4 v220, s[4:7], s63 offen lds\nN 112,104,160\n"
"R 180,188,8192\nv_sub_f32 v137, v130, v136\nN 112,120,164\nv_cvt_pk_bf16_f32 v134, v136, v137\nv_cvt_pk_bf16_f32 v135, v136, 0\nN 116,108,160\n"
"R 182,188,12288\nv_and_b32 v230, 0x1ff, v230\nN 116,124,164\nv_add_u32 v230, 156160, v230\nds_write_b128 v230, v[132:135]\nG 5\n"
"N 124,108,168\nR 160,189,0\nN 124,124,172\nbuffer_load_dwordx4 v221, s[4:7], s63 offen offset:1024 lds\nN 120,104,168\nR 162,189,4096\n"
"s_add_u32 s63, s52, 2166784\nN 120,120,172\ns_add_u32 m0, s50, 0\nN 92,100,168\nR 164,189,8192\nN 92,116,172\n"
"buffer_load_dwordx4 v220, s[4:7], s63 offen lds\nN 88,96,168\nR 166,189,12288\nN 88,112,172\ns_waitcnt vmcnt(10)\nds_read_b128 a[160:163], v248 offset:0\n"
"s_waitcnt vmcnt(9) lgkmcnt(5)\ns_barrier\nM 0,176,128\nR 168,190,0\nds_read_b128 a[164:167], v248 offset:2048\nM 0,180,144\n"
"ds_read_b128 a[168:171], v248 offset:4096\nM 16,176,132\nR 170,190,4096\ns_waitcnt vmcnt(7)\nv_min3_u32 v236, v145, v147, v149\nv_min3_u32 v236, v236, v151, v151\n"
"M 16,180,148\nds_read_b128 a[172:175], v248 offset:6144\nv_max3_u32 v237, v145, v147, v149\nv_max3_u32 v237, v237, v151, v151\nM 32,176,136\nR 172,190,8192\n"
"ds_read_b128 a[176:179], v249 offset:0\nv_cmp_ne_u32 s[86:87], s46, v236\nM 32,180,152\nds_read_b128 a[180:183], v249 offset:2048\nv_lshrrev_b32 v230, 7, v252\nv_cmp_ne_u32 s[88:89], s46, v237\n"
"M 48,176,140\nR 174,190,12288\nds_read_b128 a[184:187], v249 offset:4096\ns_or_b64 s[86:87], s[86:87], s[88:89]\ns_cbranch_scc1 Lq124\nLq125:\n"
"M 48,180,156\nds_read_b128 a[188:191], v249 offset:6144\nG 12\nv_add_f32 v144, v144, v148\nM 52,160,140\nR 176,191,0\n"
"v_add_f32 v146, v146, v150\nv_add_f32_dpp v144, v144, v144 row_ror:8 row_mask:0xf bank_mask:0xf\nM 52,164,156\nbuffer_load_dwordx4 v221, s[4:7], s63 offen offset:1024 lds\nv_lshlrev_b32 v230, 9, v230\nv_add_f32_dpp v146, v146, v146 row_ror:8 row_mask:0xf bank_mask:0xf\n"
"M 36,160,136\nR 178,191,4096\nv_and_b32 v231, 0x70, v252\nv_mov_b32 v148, v144\nM 36,164,152\nG 2\n"
"s_add_u32 m0, s51, 0\ns_add_u32 s60, s70, 768\nbuffer_load_dwordx4 v226, s[24:27], s60 offen lds\nv_mov_b32 v149, v146\nM 20,160,132\nR 180,191,8192\n"
"v_permlane32_swap_b32 v148, v144\nv_permlane32_swap_b32 v149, v146\nM 20,164,148\ns_add_u32 m0, s51, 1024\ns_add_u32 s60, s70, 17152\nbuffer_load_dwordx4 v227, s[24:27], s60 offen lds\n"
"v_add_f32 v144, v144, v148\nM 4,160,128\nR 182,191,12288\ns_and_b32 s60, s40, 3\ns_lshl_b32 s60, s60, 7\nv_add3_u32 v230, v230, v231, s60\n"
"v_add_f32 v146, v146, v149\nM 4,164,144\ns_add_u32 m0, s51, 2048\ns_add_u32 s60, s70, 33536\nbuffer_load_dwordx4 v226, s[24:27], s60 offen lds\nv_mov_b32 v148, v144\n"
"M 8,168,128\nR 160,188,16384\nv_mov_b32 v149, v146\nv_permlane16_swap_b32 v148, v144\nM 8,172,144\ns_add_u32 m0, s51, 3072\n"
"s_add_u32 s60, s70, 49920\nbuffer_load_dwordx4 v227, s[24:27], s60 offen lds\nv_permlane16_swap_b32 v149, v146\nM 24,168,132\nR 162,188,20480\ns_lshl_b32 s61, s42, 13\n"
"s_add_u32 s61, s61, 83886080\nbuffer_load_dwordx4 v[128:131], v230, s[20:23], s61 offen sc1\nv_add_f32 v144, v144, v148\nM 24,172,148\ns_add_u32 m0, s51, 4096\ns_add_u32 s60, s71, 768\n"
"buffer_load_dwordx4 v226, s[24:27], s60 offen lds\nv_add_f32 v146, v146, v149\nM 40,168,136\nR 164,188,24576\ns_lshl_b32 s61, s42, 13\ns_add_u32 s61, s61, 83890176\n"
"buffer_load_dwordx4 v[132:135], v230, s[20:23], s61 offen sc1\nv_and_b32 v152, s77, v144\nM 40,172,152\ns_add_u32 m0, s51, 5120\ns_add_u32 s60, s71, 17152\nbuffer_load_dwordx4 v227, s[24:27], s60 offen lds\n"
"M 56,168,140\nR 166,188,28672\nv_sub_f32 v153, v144, v152\nM 56,172,156\ns_add_u32 m0, s51, 6144\ns_add_u32 s60, s71, 33536\n"
"buffer_load_dwordx4 v226, s[24:27], s60 offen lds\nG 4\nM 60,176,140\nR 168,189,16384\nv_cvt_pk_bf16_f32 v148, v152, v153\nM 60,180,156\n"
"s_add_u32 m0, s51, 7168\ns_add_u32 s60, s71, 49920\nbuffer_load_dwordx4 v227, s[24:27], s60 offen lds\nM 44,176,136\nR 170,189,20480\nv_cvt_pk_bf16_f32 v149, v152, 0\n"
"M 44,180,152\ns_add_u32 m0, s50, 8192\nv_and_b32 v152, s77, v146\nv_sub_f32 v153, v146, v152\nM 28,176,132\nR 172,189,24576\n"
"v_cvt_pk_bf16_f32 v150, v152, v153\nM 28,180,148\ns_add_u32 s63, s52, 2361344\nv_cvt_pk_bf16_f32 v151, v152, 0\nv_and_b32 v234, 0x1ff, v234\nM 12,176,128\n"
"R 174,189,28672\nv_add_u32 v234, 156672, v234\nM 12,180,144\ns_waitcnt vmcnt(14)\ns_barrier\nG 4\n"
"N 64,128,160\nR 176,190,16384\nds_write_b128 v234, v[148:151]\nN 64,144,164\nbuffer_load_dwordx4 v220, s[4:7], s63 offen lds\nN 68,132,160\n"
"R 178,190,20480\nN 68,148,164\nN 96,136,160\nR 180,190,24576\nN 96,152,164\nbuffer_load_dwordx4 v221, s[4:7], s63 offen offset:1024 lds\n"
"N 100,140,160\nR 182,190,28672\ns_add_u32 m0, s50, 16384\nN 100,156,164\nG 5\nN 108,140,168\n"
"R 160,191,16384\ns_add_u32 s63, s52, 2557952\nN 108,156,172\nbuffer_load_dwordx4 v220, s[4:7], s63 offen lds\nN 104,136,168\nR 162,191,20480\n"
"N 104,152,172\nN 76,132,168\nR 164,191,24576\nLq183:\ns_lshr_b32 s60, s45, 3\ns_lshl2_add_u32 s60, s60, s40\n"
"s_lshl_b32 s60, s60, 8\nv_lshl_add_u32 v224, v252, 3, s60\nv_add_u32 v224, Lq183+4096-LqPC, v224\nv_min_u32 v224, LqE-LqPC-4, v224\ns_mov_b64 exec, 3\nglobal_load_dword v224, v224, s[98:99]\n"
"s_mov_b64 exec, -1\nN 76,148,172\nbuffer_load_dwordx4 v221, s[4:7], s63 offen offset:1024 lds\nN 72,128,168\nR 166,191,28672\nN 72,144,172\n"
"G 4\nN 80,128,176\nR 168,188,32768\nN 80,144,180\nN 84,132,176\nR 170,188,36864\n"
"N 84,148,180\ns_add_u32 m0, s50, 24576\nN 112,136,176\nR 172,188,40960\nN 112,152,180\ns_add_u32 s63, s52, 2754560\n"
"N 116,140,176\nR 174,188,45056\nN 116,156,180\nG 4\nN 124,140,160\nR 176,189,32768\n"
"N 124,156,164\nbuffer_load_dwordx4 v220, s[4:7], s63 offen lds\nN 120,136,160\nR 178,189,36864\nN 120,152,164\nN 92,132,160\n"
"R 180,189,40960\ns_waitcnt vmcnt(9)\nv_min3_u32 v232, v129, v131, v133\nv_min3_u32 v232, v232, v135, v135\nN 92,148,164\nbuffer_load_dwordx4 v221, s[4:7], s63 offen offset:1024 lds\n"
"v_max3_u32 v233, v129, v131, v133\nN 88,128,160\nR 182,189,45056\ns_add_u32 m0, s50, 32768\nv_max3_u32 v233, v233, v135, v135\nN 88,144,164\n"
"s_barrier\nG 4\nv_cmp_ne_u32 s[86:87], s46, v232\nM 0,168,160\nR 160,190,32768\ns_add_u32 s63, s52, 2951168\n"
"v_cmp_ne_u32 s[88:89], s46, v233\nM 0,172,176\nbuffer_load_dwordx4 v220, s[4:7], s63 offen lds\ns_or_b64 s[86:87], s[86:87], s[88:89]\ns_cbranch_scc1 Lq132\nLq133:\n"
"M 16,168,164\nR 162,190,36864\nv_add_f32 v128, v128, v132\nv_add_f32 v130, v130, v134\nM 16,172,180\nv_add_f32_dpp v128, v128, v128 row_ror:8 row_mask:0xf bank_mask:0xf\n"
"v_add_f32_dpp v130, v130, v130 row_ror:8 row_mask:0xf bank_mask:0xf\nv_mov_b32 v132, v128\nM 32,168,168\nR 164,190,40960\nv_mov_b32 v133, v130\nv_permlane32_swap_b32 v132, v128\n"
"M 32,172,184\nv_permlane32_swap_b32 v133, v130\nv_add_f32 v128, v128, v132\nv_add_f32 v130, v130, v133\nM 48,168,172\nR 166,190,45056\n"
"v_mov_b32 v132, v128\nv_mov_b32 v133, v130\nM 48,172,188\nG 4\nv_permlane16_swap_b32 v132, v128\nv_permlane16_swap_b32 v133, v130\n"
"v_add_f32 v128, v128, v132\nM 52,176,172\nR 168,191,32768\nv_add_f32 v130, v130, v133\nv_and_b32 v136, s77, v128\nM 52,180,188\n"
"buffer_load_dwordx4 v221, s[4:7], s63 offen offset:1024 lds\nv_sub_f32 v137, v128, v136\nM 36,176,168\nR 170,191,36864\ns_waitcnt vmcnt(9)\nds_read_b128 a[192:195], v248 offset:0\n"
"v_cvt_pk_bf16_f32 v132, v136, v137\nM 36,180,184\nds_read_b128 a[196:199], v248 offset:2048\nv_cvt_pk_bf16_f32 v133, v136, 0\nv_and_b32 v136, s77, v130\nM 20,176,164\n"
"R 172,191,40960\nds_read_b128 a[200:203], v248 offset:4096\nv_sub_f32 v137, v130, v136\nM 20,180,180\nds_read_b128 a[204:207], v248 offset:6144\nv_cvt_pk_bf16_f32 v134, v136, v137\n"
"v_cvt_pk_bf16_f32 v135, v136, 0\nM 4,176,160\nR 174,191,45056\nv_and_b32 v230, 0x1ff, v230\nv_add_u32 v230, 155648, v230\nM 4,180,176\n"
"ds_read_b128 a[208:211], v249 offset:0\nds_write_b128 v230, v[132:135]\nG 10\nM 8,160,160\nR 176,188,49152\nds_read_b128 a[212:215], v249 offset:2048\n"
"M 8,164,176\nds_read_b128 a[216:219], v249 offset:4096\ns_add_u32 m0, s50, 40960\nM 24,160,164\nR 178,188,53248\ns_add_u32 s63, s52, 2363392\n"
"M 24,164,180\nds_read_b128 a[220:223], v249 offset:6144\nM 40,160,168\nR 180,184,0\nM 40,164,184\nbuffer_load_dwordx4 v220, s[4:7], s63 offen lds\n"
"M 56,160,172\nR 182,184,4096\nM 56,164,188\nG 2\ns_add_u32 m0, s51, 0\ns_add_u32 s60, s70, 896\n"
"buffer_load_dwordx4 v226, s[24:27], s60 offen lds\nM 60,168,172\nR 160,189,49152\nM 60,172,188\ns_add_u32 m0, s51, 1024\ns_add_u32 s60, s70, 17280\n"
"buffer_load_dwordx4 v227, s[24:27], s60 offen lds\nM 44,168,168\nR 162,189,53248\nM 44,172,184\ns_add_u32 m0, s51, 2048\ns_add_u32 s60, s70, 33664\n"
"buffer_load_dwordx4 v226, s[24:27], s60 offen lds\nM 28,168,164\nR 164,185,0\nM 28,172,180\ns_add_u32 m0, s51, 3072\ns_add_u32 s60, s70, 50048\n"
"buffer_load_dwordx4 v227, s[24:27], s60 offen lds\nM 12,168,160\nR 166,185,4096\nM 12,172,176\ns_add_u32 m0, s51, 4096\ns_add_u32 s60, s71, 896\n"
"buffer_load_dwordx4 v226, s[24:27], s60 offen lds\ns_waitcnt vmcnt(10)\ns_barrier\nG 4\nN 64,160,176\nR 168,190,49152\n"
"N 64,176,180\ns_add_u32 m0, s51, 5120\ns_add_u32 s60, s71, 17280\nbuffer_load_dwordx4 v227, s[24:27], s60 offen lds\nN 68,164,176\nR 170,190,53248\n"
"s_lshr_b32 s84, s49, 4\nN 68,180,180\ns_add_u32 m0, s51, 6144\ns_add_u32 s60, s71, 33664\nbuffer_load_dwordx4 v226, s[24:27], s60 offen lds\nN 96,168,176\n"
"R 172,186,0\ns_add_u32 s84, s84, 81788928\nN 96,184,180\ns_add_u32 m0, s51, 7168\ns_add_u32 s60, s71, 50048\nbuffer_load_dwordx4 v227, s[24:27], s60 offen lds\n"
"N 100,172,176\nR 174,186,4096\ns_add_u32 m0, s50, 40960\nN 100,188,180\nG 4\nN 108,172,160\n"
"R 176,191,49152\nbuffer_load_dwordx4 v221, s[4:7], s63 offen offset:1024 lds\nN 108,188,164\nbuffer_load_dwordx4 v[128:131], v252, s[20:23], s84 offen offset:0 sc1\ns_add_u32 s63, s52, 2560000\nN 104,168,160\n"
"R 178,191,53248\ns_add_u32 m0, s50, 49152\nN 104,184,164\nbuffer_load_dwordx4 v[132:135], v252, s[20:23], s84 offen offset:1024 sc1\nN 76,164,160\nR 180,187,0\n"
"buffer_load_dwordx4 v220, s[4:7], s63 offen lds\nN 76,180,164\nbuffer_load_dwordx4 v221, s[4:7], s63 offen offset:1024 lds\nN 72,160,160\nR 182,187,4096\nN 72,176,164\n"
"G 4\nN 80,160,168\nR 160,184,8192\nN 80,176,172\nbuffer_load_dwordx4 v[136:139], v252, s[20:23], s84 offen offset:2048 sc1\nN 84,164,168\n"
"R 162,184,12288\nN 84,180,172\nbuffer_load_dwordx4 v[140:143], v252, s[20:23], s84 offen offset:3072 sc1\nN 112,168,168\nR 164,184,16384\nN 112,184,172\n"
"N 116,172,168\nR 166,184,20480\nN 116,188,172\nG 4\nN 124,172,176\nR 168,185,8192\n"
"N 124,188,180\nN 120,168,176\nR 170,185,12288\nN 120,184,180\ns_add_u32 m0, s50, 57344\nN 92,164,176\n"
"R 172,185,16384\nN 92,180,180\ns_add_u32 s63, s52, 2756608\nN 88,160,176\nR 174,185,20480\nN 88,176,180\n"
"s_waitcnt vmcnt(16)\ns_barrier\nG 4\nM 0,160,192\nR 176,186,8192\nM 0,164,208\n"
"buffer_load_dwordx4 v220, s[4:7], s63 offen lds\nM 16,160,196\nR 178,186,12288\nM 16,164,212\nM 32,160,200\nR 180,186,16384\n"
"M 32,164,216\nbuffer_load_dwordx4 v221, s[4:7], s63 offen offset:1024 lds\nM 48,160,204\nR 182,186,20480\ns_lshl2_add_u32 s60, s45, s40\nv_lshlrev_b32 v224, 3, v252\n"
"s_mul_i32 s60, s60, 2048\ns_bfm_b64 exec, 16, 0\nbuffer_load_dword v224, v224, s[8:11], s60 offen\ns_mov_b64 exec, -1\nM 48,164,220\ns_add_u32 s63, s52, 2953216\n"
"G 4\nM 52,168,204\nR 160,187,8192\ns_add_u32 m0, s50, 65536\nM 52,172,220\nbuffer_load_dwordx4 v220, s[4:7], s63 offen lds\n"
"M 36,168,200\nR 162,187,12288\nM 36,172,216\nM 20,168,196\nR 164,187,16384\nM 20,172,212\n"
"buffer_load_dwordx4 v221, s[4:7], s63 offen offset:1024 lds\nM 4,168,192\nR 166,187,20480\nM 4,172,208\nG 4\nM 8,176,192\n"
"R 168,184,24576\nM 8,180,208\nM 24,176,196\nR 170,184,28672\ns_waitcnt vmcnt(12)\nds_read_b128 a[224:227], v248 offset:0\n"
"M 24,180,212\nds_read_b128 a[228:231], v248 offset:2048\nM 40,176,200\nR 172,184,32768\nds_read_b128 a[232:235], v248 offset:4096\nM 40,180,216\n"
"ds_read_b128 a[236:239], v248 offset:6144\nM 56,176,204\nR 174,184,36864\nM 56,180,220\nds_read_b128 a[240:243], v249 offset:0\nG 9\n"
"M 60,160,204\nR 176,185,24576\nds_read_b128 a[244:247], v249 offset:2048\nM 60,164,220\nds_read_b128 a[248:251], v249 offset:4096\ns_add_u32 m0, s50, 73728\n"
"M 44,160,200\nR 178,185,28672\ns_add_u32 s63, s52, 0\nM 44,164,216\nds_read_b128 a[252:255], v249 offset:6144\nM 28,160,196\n"
"R 180,185,32768\nM 28,164,212\nbuffer_load_dwordx4 v220, s[4:7], s63 offen lds\nM 12,160,192\nR 182,185,36864\nM 12,164,208\n"
"s_waitcnt vmcnt(8)\ns_barrier\nG 8\nM 0,168,224\nR 160,186,24576\ns_mov_b32 s85, 0x0c0c0400\n"
"v_perm_b32 v144, v129, v128, s85\nG 8\nM 0,172,240\nbuffer_load_dwordx4 v221, s[4:7], s63 offen offset:1024 lds\nv_perm_b32 v148, v131, v130, s85\nM 16,168,228\n"
"R 162,186,28672\ns_add_u32 s63, s52, 196608\nv_lshl_or_b32 v144, v148, 16, v144\nG 7\nM 16,172,244\ns_add_u32 m0, s50, 81920\n"
"v_perm_b32 v145, v133, v132, s85\nv_perm_b32 v149, v135, v134, s85\nv_lshl_or_b32 v145, v149, 16, v145\nM 32,168,232\nR 164,186,32768\ns_waitcnt vmcnt(8)\n"
"v_perm_b32 v146, v137, v136, s85\nv_perm_b32 v150, v139, v138, s85\nG 7\nM 32,172,248\nbuffer_load_dwordx4 v220, s[4:7], s63 offen lds\nv_lshl_or_b32 v146, v150, 16, v146\n"
"M 48,168,236\nR 166,186,36864\ns_lshl_b32 s60, s90, 14\ns_add_u32 s70, s49, s60\ns_xor_b32 s71, s70, 0x10000\ns_waitcnt vmcnt(8)\n"
"v_perm_b32 v147, v141, v140, s85\nG 6\nM 48,172,252\nG 4\nv_perm_b32 v151, v143, v142, s85\nv_lshl_or_b32 v147, v151, 16, v147\n"
"v_min3_u32 v230, v144, v145, v146\nM 52,176,236\nR 168,187,24576\nv_min3_u32 v230, v230, v147, v147\nv_max3_u32 v231, v144, v145, v146\nM 52,180,252\n"
"buffer_load_dwordx4 v221, s[4:7], s63 offen offset:1024 lds\nv_max3_u32 v231, v231, v147, v147\nLpg3:\ns_cbranch_execz LretQ0\nM 36,176,232\nR 170,187,28672\n"
"s_add_u32 s63, s52, 393216\nv_cmp_ne_u32 s[86:87], s46, v230\nM 36,180,248\ns_add_u32 m0, s50, 90112\nv_cmp_ne_u32 s[88:89], s46, v231\nM 20,176,228\n"
"R 172,187,32768\ns_or_b64 s[86:87], s[86:87], s[88:89]\ns_cbranch_scc1 Lq140\nLq141:\nM 20,180,244\nbuffer_load_dwordx4 v220, s[4:7], s63 offen lds\n"
"v_and_b32 v128, 0xffffff00, v128\nM 4,176,224\nR 174,187,36864\nv_and_b32 v129, 0xffffff00, v129\nv_and_b32 v130, 0xffffff00, v130\nM 4,180,240\n"
"G 4\nv_and_b32 v131, 0xffffff00, v131\nv_and_b32 v132, 0xffffff00, v132\nv_and_b32 v133, 0xffffff00, v133\nM 8,160,224\nR 176,184,40960\n"
"v_and_b32 v134, 0xffffff00, v134\nv_and_b32 v135, 0xffffff00, v135\nM 8,164,240\nbuffer_load_dwordx4 v221, s[4:7], s63 offen offset:1024 lds\nv_and_b32 v136, 0xffffff00, v136\nM 24,160,228\n"
"R 178,184,45056\nv_and_b32 v137, 0xffffff00, v137\nv_and_b32 v138, 0xffffff00, v138\nM 24,164,244\ns_add_u32 m0, s50, 98304\nv_and_b32 v139, 0xffffff00, v139\n"
"v_and_b32 v140, 0xffffff00, v140\nv_and_b32 v141, 0xffffff00, v141\nM 40,160,232\nR 180,184,49152\nv_and_b32 v142, 0xffffff00, v142\nv_and_b32 v143, 0xffffff00, v143\n"
"M 40,164,248\ns_add_u32 s63, s52, 589824\nv_add_f32 v128, v128, v136\nv_add_f32 v129, v129, v137\nv_add_f32 v130, v130, v138\nM 56,160,236\n"
"R 182,184,53248\nv_add_f32 v131, v131, v139\nv_add_f32 v132, v132, v140\nM 56,164,252\nG 4\nv_add_f32 v133, v133, v141\n"
"v_add_f32 v134, v134, v142\nv_add_f32 v135, v135, v143\nM 60,168,236\nR 160,185,40960\nv_add_f32 v128, v128, v132\nv_add_f32 v129, v129, v133\n"
"M 60,172,252\nbuffer_load_dwordx4 v220, s[4:7], s63 offen lds\nv_add_f32 v130, v130, v134\nM 44,168,232\nR 162,185,45056\nv_add_f32 v131, v131, v135\n"
"v_permlane32_swap_b32 v128, v130\nM 44,172,248\nv_permlane32_swap_b32 v129, v131\nv_add_f32 v128, v128, v130\nv_add_f32 v129, v129, v131\nM 28,168,228\n"
"R 164,185,49152\nv_permlane16_swap_b32 v128, v129\nv_add_f32 v230, v128, v129\nM 28,172,244\nbuffer_load_dwordx4 v221, s[4:7], s63 offen offset:1024 lds\nv_mov_b32 v232, v230\n"
"M 12,168,224\nR 166,185,53248\nv_permlane16_swap_b32 v230, v232\nv_mov_b32 v231, v230\nM 12,172,240\ns_waitcnt vmcnt(8)\n"
"s_barrier\nG 4\nv_mov_b32 v233, v232\nN 64,192,176\nR 168,186,40960\nv_permlane32_swap_b32 v230, v231\n"
"v_permlane32_swap_b32 v232, v233\nN 64,208,180\ns_mov_b32 m0, s51\nv_mul_f32 v230, 0xba800000, v230\nv_mul_f32 v214, v230, v230\nv_mul_f32 v231, 0x3a800000, v231\n"
"N 68,196,176\nR 170,186,45056\ns_lshl2_add_u32 s60, s45, s40\nv_lshlrev_b32 v224, 3, v252\ns_mul_i32 s60, s60, 7168\ns_bfm_b64 exec, 14, 0\n"
"buffer_load_dword v224, v224, s[12:15], s60 offen\ns_mov_b64 exec, -1\nds_write_addtid_b32 v32 offset:0\nv_sub_f32 v231, v231, v214\nN 68,212,180\nds_write_addtid_b32 v33 offset:256\n"
"ds_write_addtid_b32 v34 offset:512\nv_max_f32 v231, 0, v231\nv_add_f32 v231, 0x3727c5ac, v231\nN 96,200,176\nR 172,186,49152\ns_mov_b32 s60, 156160\n"
"v_bfe_u32 v246, v252, 4, 4\nv_lshl_add_u32 v246, v246, 3, s60\nds_write_addtid_b32 v35 offset:768\nv_rsq_f32 v234, v231\nN 96,216,180\nds_read_b64 v[238:239], v246 offset:0\n"
"ds_write_addtid_b32 v36 offset:1024\nv_and_b32 v208, s77, v230\nv_mul_f32 v216, v231, v234\nN 100,204,176\nR 174,186,53248\nds_read_b64 v[240:241], v246 offset:128\n"
"ds_write_addtid_b32 v37 offset:1280\nv_sub_f32 v230, v230, v208\nN 100,220,180\nds_read_b64 v[242:243], v246 offset:256\nds_write_addtid_b32 v38 offset:1536\nG 14\n"
"v_cvt_pk_bf16_f32 v209, v230, v216\nN 108,204,160\nR 176,187,40960\nds_read_b64 v[244:245], v246 offset:384\nds_write_addtid_b32 v39 offset:1792\nv_cvt_pk_bf16_f32 v208, v208, v208\n"
"N 108,220,164\nds_read_b64 v[246:247], v246 offset:0\nds_write_addtid_b32 v40 offset:2048\nv_mov_b32 v214, v234\nv_mul_f32 v215, 0x3e8293ee, v234\nN 104,200,160\n"
"R 178,187,45056\nds_write_addtid_b32 v41 offset:2304\nv_mul_f32 v232, 0xba800000, v232\nv_mul_f32 v217, v232, v232\nN 104,216,164\nds_write_addtid_b32 v42 offset:2560\n"
"ds_write_addtid_b32 v43 offset:2816\nv_mul_f32 v233, 0x3a800000, v233\nv_sub_f32 v233, v233, v217\nN 76,196,160\nR 180,187,49152\nds_write_addtid_b32 v44 offset:3072\n"
"v_max_f32 v233, 0, v233\nv_add_f32 v233, 0x3727c5ac, v233\nN 76,212,164\nds_write_addtid_b32 v45 offset:3328\nv_and_b32 v212, s77, v232\nv_rsq_f32 v235, v233\n"
"s_nop 0\nv_mul_f32 v219, v233, v235\nv_sub_f32 v232, v232, v212\nv_cvt_pk_bf16_f32 v213, v232, v219\nv_cvt_pk_bf16_f32 v212, v212, v212\nv_mov_b32 v217, v235\n"
"v_mul_f32 v218, 0x3e8293ee, v235\ns_mov_b64 s[60:61], 0xffff\nv_cndmask_b32 v208, 0, v208, s[60:61]\ns_mov_b64 s[60:61], 0xffff\nv_cndmask_b32 v209, 0, v209, s[60:61]\ns_mov_b64 s[60:61], 0xffff\n"
"v_cndmask_b32 v212, 0, v212, s[60:61]\ns_mov_b64 s[60:61], 0xffff\nv_cndmask_b32 v213, 0, v213, s[60:61]\nv_bfe_u32 v230, v252, 2, 6\ns_lshl_b32 s60, s40, 7\ns_add_u32 s60, s60, 161280\n"
"v_add_u32 v230, s60, v230\nds_write_b32 v230, v214 offset:0\nds_write_b32 v230, v217 offset:64\nds_write_addtid_b32 v46 offset:3584\nN 72,192,160\nR 182,187,53248\n"
"ds_write_addtid_b32 v47 offset:3840\nN 72,208,164\nds_write_addtid_b32 v48 offset:4096\nds_write_addtid_b32 v49 offset:4352\nG 15\nN 80,192,168\n"
"R 160,184,57344\nds_write_addtid_b32 v50 offset:4608\nN 80,208,172\nds_write_addtid_b32 v51 offset:4864\nds_write_addtid_b32 v52 offset:5120\nN 84,196,168\n"
"R 162,184,61440\nv_mfma_f32_16x16x32_bf16 v[0:3], v[238:241], v[208:211], v[0:3]\nds_write_addtid_b32 v53 offset:5376\nN 84,212,172\nv_mfma_f32_16x16x32_bf16 v[4:7], v[240:243], v[208:211], v[4:7]\nds_write_addtid_b32 v54 offset:5632\n"
"ds_write_addtid_b32 v55 offset:5888\nN 112,200,168\nR 164,188,0\nv_mfma_f32_16x16x32_bf16 v[8:11], v[242:245], v[208:211], v[8:11]\nds_write_addtid_b32 v56 offset:6144\nN 112,216,172\n"
"v_mfma_f32_16x16x32_bf16 v[12:15], v[244:247], v[208:211], v[12:15]\nds_write_addtid_b32 v57 offset:6400\nds_write_addtid_b32 v58 offset:6656\nN 116,204,168\nR 166,188,4096\nv_mfma_f32_16x16x32_bf16 v[16:19], v[244:247], v[210:213], v[16:19]\n"
"ds_write_addtid_b32 v59 offset:6912\nN 116,220,172\nv_mfma_f32_16x16x32_bf16 v[20:23], v[238:241], v[210:213], v[20:23]\nds_write_addtid_b32 v60 offset:7168\nds_write_addtid_b32 v61 offset:7424\nds_write_addtid_b32 v62 offset:7680\n"
"ds_write_addtid_b32 v63 offset:7936\nG 15\nN 124,204,176\nR 168,185,57344\nv_mfma_f32_16x16x32_bf16 v[24:27], v[240:243], v[210:213], v[24:27]\ns_add_u32 m0, s50, 106496\n"
"s_add_u32 s63, s52, 786432\nN 124,220,180\nv_mfma_f32_16x16x32_bf16 v[28:31], v[242:245], v[210:213], v[28:31]\nbuffer_load_dwordx4 v220, s[4:7], s63 offen lds\nN 120,200,176\nR 170,185,61440\n"
"s_waitcnt vmcnt(6) lgkmcnt(2)\ns_barrier\nN 120,216,180\nds_read2st64_b32 v[32:33], v225 offset0:0 offset1:1\nds_read2st64_b32 v[34:35], v225 offset0:2 offset1:3\nN 92,196,176\n"
"R 172,189,0\nds_read2st64_b32 v[36:37], v225 offset0:4 offset1:5\nN 92,212,180\nbuffer_load_dwordx4 v221, s[4:7], s63 offen offset:1024 lds\nds_read2st64_b32 v[38:39], v225 offset0:6 offset1:7\nN 88,192,176\n"
"R 174,189,4096\nds_read2st64_b32 v[40:41], v225 offset0:8 offset1:9\nN 88,208,180\nds_read2st64_b32 v[42:43], v225 offset0:10 offset1:11\nds_read2st64_b32 v[44:45], v225 offset0:12 offset1:13\nN 64,224,160\n"
"R 176,186,57344\ns_mov_b32 s60, 156672\nv_bfe_u32 v246, v252, 4, 4\nv_lshl_add_u32 v246, v246, 3, s60\nds_read2st64_b32 v[46:47], v225 offset0:14 offset1:15\nN 64,240,164\n"
"ds_read_b64 v[238:239], v246 offset:0\ns_add_u32 m0, s50, 114688\nds_read2st64_b32 v[48:49], v225 offset0:16 offset1:17\nN 68,228,160\nR 178,186,61440\nds_read_b64 v[240:241], v246 offset:128\n"
"ds_read2st64_b32 v[50:51], v225 offset0:18 offset1:19\nN 68,244,164\nds_read_b64 v[242:243], v246 offset:256\ns_add_u32 s63, s52, 983040\nds_read2st64_b32 v[52:53], v225 offset0:20 offset1:21\nN 96,232,160\n"
"R 180,190,0\nds_read_b64 v[244:245], v246 offset:384\nds_read2st64_b32 v[54:55], v225 offset0:22 offset1:23\nN 96,248,164\nds_read_b64 v[246:247], v246 offset:0\nds_read2st64_b32 v[56:57], v225 offset0:24 offset1:25\n"
"G 15\nv_add_f32 v0, v0, v32\nN 100,236,160\nR 182,190,4096\nbuffer_load_dwordx4 v220, s[4:7], s63 offen lds\nds_read2st64_b32 v[58:59], v225 offset0:26 offset1:27\n"
"N 100,252,164\nds_read2st64_b32 v[60:61], v225 offset0:28 offset1:29\nds_read2st64_b32 v[62:63], v225 offset0:30 offset1:31\nN 108,236,168\nR 160,187,57344\nv_add_f32 v1, v1, v33\n"
"v_add_f32 v2, v2, v34\nN 108,252,172\nbuffer_load_dwordx4 v221, s[4:7], s63 offen offset:1024 lds\nv_add_f32 v3, v3, v35\nN 104,232,168\nR 162,187,61440\n"
"s_add_u32 m0, s50, 0\ns_add_u32 s63, s52, 1179648\nv_add_f32 v4, v4, v36\nN 104,248,172\nbuffer_load_dwordx4 v220, s[4:7], s63 offen lds\nv_add_f32 v5, v5, v37\n"
"N 76,228,168\nR 164,191,0\nv_add_f32 v6, v6, v38\nv_add_f32 v7, v7, v39\nN 76,244,172\nG 15\n"
"v_mfma_f32_16x16x32_bf16 v[64:67], v[208:211], v[238:241], v[64:67]\nv_add_f32 v8, v8, v40\nv_add_f32 v9, v9, v41\nv_add_f32 v10, v10, v42\nN 72,224,168\nR 166,191,4096\n"
"v_add_f32 v11, v11, v43\nv_add_f32 v12, v12, v44\nN 72,240,172\nG 14\nv_mfma_f32_16x16x32_bf16 v[72:75], v[208:211], v[240:243], v[72:75]\nG 7\n"
"v_add_f32 v13, v13, v45\nv_add_f32 v14, v14, v46\nv_add_f32 v15, v15, v47\nN 80,224,176\nR 168,188,8192\nbuffer_load_dwordx4 v221, s[4:7], s63 offen offset:1024 lds\n"
"v_add_f32 v16, v16, v48\nN 80,240,180\nv_mfma_f32_16x16x32_bf16 v[80:83], v[208:211], v[242:245], v[80:83]\ns_add_u32 m0, s50, 8192\ns_add_u32 s63, s52, 1376256\nbuffer_load_dwordx4 v220, s[4:7], s63 offen lds\n"
"v_add_f32 v17, v17, v49\nN 84,228,176\nR 170,188,12288\nv_add_f32 v18, v18, v50\nv_add_f32 v19, v19, v51\nN 84,244,180\n"
"v_mfma_f32_16x16x32_bf16 v[88:91], v[208:211], v[244:247], v[88:91]\nv_add_f32 v20, v20, v52\nv_add_f32 v21, v21, v53\nv_add_f32 v22, v22, v54\nN 112,232,176\nR 172,188,16384\n"
"v_add_f32 v23, v23, v55\nv_add_f32 v24, v24, v56\nN 112,248,180\nv_mfma_f32_16x16x32_bf16 v[68:71], v[210:213], v[244:247], v[68:71]\nbuffer_load_dwordx4 v221, s[4:7], s63 offen offset:1024 lds\nv_add_f32 v25, v25, v57\n"
"N 116,236,176\nR 174,188,20480\ns_add_u32 m0, s50, 32768\nG 10\nv_add_f32 v26, v26, v58\nN 116,252,180\n"
"v_mfma_f32_16x16x32_bf16 v[76:79], v[210:213], v[238:241], v[76:79]\nG 4\nv_add_f32 v27, v27, v59\nv_add_f32 v28, v28, v60\nv_add_f32 v29, v29, v61\nN 124,236,160\n"
"R 176,189,8192\ns_add_u32 s63, s52, 1572864\nv_add_f32 v30, v30, v62\nN 124,252,164\nv_mfma_f32_16x16x32_bf16 v[84:87], v[210:213], v[240:243], v[84:87]\nbuffer_load_dwordx4 v220, s[4:7], s63 offen lds\n"
"v_add_f32 v31, v31, v63\nN 120,232,160\nR 178,189,12288\ns_lshl4_add_u32 s64, s41, s42\nv_lshrrev_b32 v227, 1, v252\nN 120,248,164\n"
"v_mfma_f32_16x16x32_bf16 v[92:95], v[210:213], v[242:245], v[92:95]\nbuffer_load_dwordx4 v221, s[4:7], s63 offen offset:1024 lds\nv_mul_f32 v16, v16, v217\nN 92,228,160\nR 180,189,16384\ns_add_u32 m0, s50, 40960\n"
"s_add_u32 s63, s52, 1769472\nv_mul_f32 v17, v17, v217\nN 92,244,164\nbuffer_load_dwordx4 v220, s[4:7], s63 offen lds\nv_mul_f32 v18, v18, v217\nN 88,224,160\n"
"R 182,189,20480\ns_lshl_b32 s64, s64, 12\nv_mul_f32 v19, v19, v217\nN 88,240,164\ns_waitcnt vmcnt(12)\ns_barrier\n"
"G 4\nv_mul_f32 v20, v20, v217\nv_mfma_f32_16x16x32_bf16 v[128:131], v[168:171], a[0:3], 0\nR 160,190,8192\ns_add_u32 s64, s64, 77594624\nv_mul_f32 v21, v21, v217\n"
"M 128,172,16\ns_mov_b32 m0, s51\ns_cmp_eq_u32 s40, 3\ns_cselect_b64 s[66:67], -1, 0\nv_mul_f32 v22, v22, v217\nv_mfma_f32_16x16x32_bf16 v[144:147], v[168:171], a[4:7], 0\n"
"R 162,190,12288\ns_lshl2_add_u32 s60, s45, s40\ns_waitcnt vmcnt(11)\nv_lshlrev_b32 v224, 3, v252\ns_mul_i32 s60, s60, 7168\ns_bfm_b64 exec, 14, 14\n"
"buffer_load_dword v224, v224, s[12:15], s60 offen\ns_mov_b64 exec, -1\nv_mul_f32 v23, v23, v217\nM 144,172,20\nv_mul_f32 v24, v24, v217\nv_mul_f32 v25, v25, v217\n"
"v_mul_f32 v26, v26, v217\nv_mfma_f32_16x16x32_bf16 v[32:35], v[168:171], a[8:11], 0\nR 164,190,16384\nv_mul_f32 v27, v27, v217\nv_mul_f32 v28, v28, v217\nM 32,172,24\n"
"v_mul_f32 v29, v29, v217\nv_mul_f32 v30, v30, v217\nv_mul_f32 v31, v31, v217\nv_mfma_f32_16x16x32_bf16 v[48:51], v[168:171], a[12:15], 0\nR 166,190,20480\nds_write_addtid_b32 v96 offset:0\n"
"v_cvt_pk_bf16_f32 v16, v16, v17\nv_cvt_pk_bf16_f32 v17, v18, v19\nM 48,172,28\nds_write_addtid_b32 v97 offset:256\nG 6\nv_cvt_pk_bf16_f32 v18, v20, v21\n"
"v_cvt_pk_bf16_f32 v19, v22, v23\nv_cvt_pk_bf16_f32 v20, v24, v25\nv_mfma_f32_16x16x32_bf16 v[52:55], v[176:179], a[12:15], 0\nR 168,191,8192\nds_write_addtid_b32 v98 offset:512\ns_lshr_b32 s100, s41, 1\n"
"s_mul_i32 s100, s100, 458752\ns_add_u32 s100, s100, s55\nv_cvt_pk_bf16_f32 v21, v26, v27\nv_cvt_pk_bf16_f32 v22, v28, v29\nM 52,180,28\nds_write_addtid_b32 v99 offset:768\n"
"s_mul_i32 s101, s41, 229376\ns_add_u32 s101, s101, s54\nv_cvt_pk_bf16_f32 v23, v30, v31\nv_mul_f32 v0, v0, v214\nv_mul_f32 v1, v1, v214\nv_mfma_f32_16x16x32_bf16 v[36:39], v[176:179], a[8:11], 0\n"
"R 170,191,12288\nds_write_addtid_b32 v100 offset:1024\nv_mul_f32 v2, v2, v214\nv_mul_f32 v3, v3, v214\nM 36,180,24\nds_write_addtid_b32 v101 offset:1280\n"
"v_mul_f32 v4, v4, v214\nv_mul_f32 v5, v5, v214\nv_mul_f32 v6, v6, v214\nv_mfma_f32_16x16x32_bf16 v[148:151], v[176:179], a[4:7], 0\nR 172,191,16384\nds_write_addtid_b32 v102 offset:1536\n"
"v_mul_f32 v7, v7, v214\nv_mul_f32 v8, v8, v214\nM 148,180,20\nds_write_addtid_b32 v103 offset:1792\nv_mul_f32 v9, v9, v214\nv_mul_f32 v10, v10, v214\n"
"v_mul_f32 v11, v11, v214\nv_mfma_f32_16x16x32_bf16 v[132:135], v[176:179], a[0:3], 0\nR 174,191,20480\nds_write_addtid_b32 v104 offset:2048\nv_mul_f32 v12, v12, v214\nv_mul_f32 v13, v13, v214\n"
"M 132,180,16\nds_write_addtid_b32 v105 offset:2304\nG 14\nv_mul_f32 v14, v14, v214\nv_mul_f32 v15, v15, v214\nv_cvt_pk_bf16_f32 v0, v0, v1\n"
"v_mfma_f32_16x16x32_bf16 v[136:139], v[160:163], a[0:3], 0\nR 176,188,24576\nds_write_addtid_b32 v106 offset:2560\nv_cvt_pk_bf16_f32 v1, v2, v3\nv_cvt_pk_bf16_f32 v2, v4, v5\nM 136,164,16\n"
"ds_write_addtid_b32 v107 offset:2816\nv_cvt_pk_bf16_f32 v3, v6, v7\nv_cvt_pk_bf16_f32 v4, v8, v9\nv_cvt_pk_bf16_f32 v5, v10, v11\nv_mfma_f32_16x16x32_bf16 v[152:155], v[160:163], a[4:7], 0\nR 178,188,28672\n"
"ds_write_addtid_b32 v108 offset:3072\nv_cvt_pk_bf16_f32 v6, v12, v13\nv_cvt_pk_bf16_f32 v7, v14, v15\nM 152,164,20\nds_write_addtid_b32 v109 offset:3328\nds_write_addtid_b32 v110 offset:3584\n"
"s_mov_b64 exec, s[66:67]\nbuffer_store_dwordx4 v[16:19], v252, s[20:23], s64 offen offset:0 sc1\ns_mov_b64 exec, -1\nv_mfma_f32_16x16x32_bf16 v[40:43], v[160:163], a[8:11], 0\nR 180,188,32768\nds_write_addtid_b32 v111 offset:3840\n"
"M 40,164,24\nds_write_addtid_b32 v112 offset:4096\nds_write_addtid_b32 v113 offset:4352\nv_mfma_f32_16x16x32_bf16 v[56:59], v[160:163], a[12:15], 0\nR 182,188,36864\nds_write_addtid_b32 v114 offset:4608\n"
"M 56,164,28\nds_write_addtid_b32 v115 offset:4864\nds_write_addtid_b32 v116 offset:5120\ns_mov_b64 exec, s[66:67]\nbuffer_store_dwordx4 v[20:23], v252, s[20:23], s64 offen offset:1024 sc1\ns_mov_b64 exec, -1\n"
"G 15\nv_mfma_f32_16x16x32_bf16 v[60:63], v[168:171], a[12:15], 0\nR 160,189,24576\nds_write_addtid_b32 v117 offset:5376\nM 60,172,28\nds_write_addtid_b32 v118 offset:5632\n"
"ds_write_addtid_b32 v119 offset:5888\nv_mfma_f32_16x16x32_bf16 v[44:47], v[168:171], a[8:11], 0\nR 162,189,28672\nds_write_addtid_b32 v120 offset:6144\nM 44,172,24\nds_write_addtid_b32 v121 offset:6400\n"
"ds_write_addtid_b32 v122 offset:6656\nds_write_addtid_b32 v123 offset:6912\nds_write_addtid_b32 v124 offset:7168\nds_write_addtid_b32 v125 offset:7424\nds_write_addtid_b32 v126 offset:7680\nds_write_addtid_b32 v127 offset:7936\n"
"v_mfma_f32_16x16x32_bf16 v[156:159], v[168:171], a[4:7], 0\nR 164,189,32768\ns_add_u32 m0, s50, 40960\nM 156,172,20\nbuffer_load_dwordx4 v221, s[4:7], s63 offen offset:1024 lds\nv_mfma_f32_16x16x32_bf16 v[140:143], v[168:171], a[0:3], 0\n"
"R 166,189,36864\ns_waitcnt vmcnt(11) lgkmcnt(2)\ns_barrier\nM 140,172,16\nds_read2st64_b32 v[96:97], v225 offset0:0 offset1:1\nds_read2st64_b32 v[98:99], v225 offset0:2 offset1:3\n"
"v_lshrrev_b32 v226, 8, v252\nM 128,176,32\nR 168,190,24576\ns_add_u32 s63, s52, 1966080\nds_read2st64_b32 v[100:101], v225 offset0:4 offset1:5\ns_lshl_b32 s60, s40, 7\n"
"s_add_u32 s60, s60, 161280\nv_lshl_add_u32 v226, v226, 4, s60\nM 128,180,48\ns_add_u32 m0, s50, 49152\nds_read2st64_b32 v[102:103], v225 offset0:6 offset1:7\nds_read2st64_b32 v[104:105], v225 offset0:8 offset1:9\n"
"ds_read_b128 v[230:233], v226 offset:0\nM 144,176,36\nR 170,190,28672\nds_read2st64_b32 v[106:107], v225 offset0:10 offset1:11\nds_read_b128 v[234:237], v226 offset:64\nM 144,180,52\n"
"buffer_load_dwordx4 v220, s[4:7], s63 offen lds\nds_read2st64_b32 v[108:109], v225 offset0:12 offset1:13\nM 32,176,40\nR 172,190,32768\nds_read2st64_b32 v[110:111], v225 offset0:14 offset1:15\nM 32,180,56\n"
"ds_read2st64_b32 v[112:113], v225 offset0:16 offset1:17\nds_read2st64_b32 v[114:115], v225 offset0:18 offset1:19\nG 14\nv_add_f32 v64, v64, v96\nM 48,176,44\nR 174,190,36864\n"
"ds_read2st64_b32 v[116:117], v225 offset0:20 offset1:21\nv_add_f32 v65, v65, v97\nM 48,180,60\nds_read2st64_b32 v[118:119], v225 offset0:22 offset1:23\nds_read2st64_b32 v[120:121], v225 offset0:24 offset1:25\nM 52,160,44\n"
"R 176,191,24576\nds_read2st64_b32 v[122:123], v225 offset0:26 offset1:27\nG 15\nv_add_f32 v66, v66, v98\nM 52,164,60\nbuffer_load_dwordx4 v221, s[4:7], s63 offen offset:1024 lds\n"
"ds_read2st64_b32 v[124:125], v225 offset0:28 offset1:29\nM 36,160,40\nR 178,191,28672\nds_read2st64_b32 v[126:127], v225 offset0:30 offset1:31\nLpg4:\ns_cbranch_execz LretQ0\n"
"v_add_f32 v67, v67, v99\nM 36,164,56\ns_add_u32 m0, s50, 57344\nv_add_f32 v68, v68, v100\nv_add_f32 v69, v69, v101\nv_add_f32 v70, v70, v102\n"
"M 148,160,36\nR 180,191,32768\nv_add_f32 v71, v71, v103\nv_add_f32 v72, v72, v104\nM 148,164,52\ns_add_u32 s63, s52, 2162688\n"
"v_add_f32 v73, v73, v105\nG 15\nv_add_f32 v74, v74, v106\nv_add_f32 v75, v75, v107\nM 132,160,32\nR 182,191,36864\n"
"G 15\nv_add_f32 v76, v76, v108\nv_add_f32 v77, v77, v109\nM 132,164,48\nG 10\nv_add_f32 v78, v78, v110\n"
"v_add_f32 v79, v79, v111\nv_add_f32 v80, v80, v112\nM 136,168,32\nR 160,188,40960\nv_add_f32 v81, v81, v113\nv_add_f32 v82, v82, v114\n"
"M 136,172,48\nbuffer_load_dwordx4 v220, s[4:7], s63 offen lds\nv_add_f32 v83, v83, v115\nM 152,168,36\nR 162,188,45056\nG 11\n"
"v_add_f32 v84, v84, v116\nM 152,172,52\nv_add_f32 v85, v85, v117\nG 10\nv_add_f32 v86, v86, v118\nM 40,168,40\n"
"R 164,188,49152\nv_add_f32 v87, v87, v119\nM 40,172,56\nbuffer_load_dwordx4 v221, s[4:7], s63 offen offset:1024 lds\nM 56,168,44\nR 166,188,53248\n"
"s_add_u32 m0, s50, 65536\nM 56,172,60\nG 4\nv_add_f32 v88, v88, v120\nv_add_f32 v89, v89, v121\nM 60,176,44\n"
"R 168,189,40960\ns_add_u32 s63, s52, 2359296\nM 60,180,60\nbuffer_load_dwordx4 v220, s[4:7], s63 offen lds\nM 44,176,40\nR 170,189,45056\n"
"v_add_f32 v90, v90, v122\nM 44,180,56\nv_add_f32 v91, v91, v123\nv_add_f32 v92, v92, v124\nM 156,176,36\nR 172,189,49152\n"
"v_add_f32 v93, v93, v125\nM 156,180,52\nbuffer_load_dwordx4 v221, s[4:7], s63 offen offset:1024 lds\nM 140,176,32\nR 174,189,53248\ns_add_u32 m0, s50, 73728\n"
"M 140,180,48\nG 4\nv_add_f32 v94, v94, v126\nv_add_f32 v95, v95, v127\nM 128,160,64\nR 176,190,40960\n"
"s_add_u32 s63, s52, 2555904\nv_mul_f32 v64, v64, v230\nM 128,164,80\nbuffer_load_dwordx4 v220, s[4:7], s63 offen lds\nv_mul_f32 v65, v65, v231\nM 144,160,68\n"
"R 178,190,45056\nv_mul_f32 v66, v66, v232\nv_mul_f32 v67, v67, v233\nM 144,164,84\nv_mul_f32 v68, v68, v234\nv_mul_f32 v69, v69, v235\n"
"v_mul_f32 v70, v70, v236\nM 32,160,72\nR 180,190,49152\nv_mul_f32 v71, v71, v237\nv_cvt_pk_bf16_f32 v67, v66, v67\nM 32,164,88\n"
"buffer_load_dwordx4 v221, s[4:7], s63 offen offset:1024 lds\nv_cvt_pk_bf16_f32 v66, v64, v65\nM 48,160,76\nR 182,190,53248\ns_add_u32 s63, s52, 2752512\nv_cvt_pk_bf16_f32 v68, v68, v69\n"
"M 48,164,92\nG 4\nv_cvt_pk_bf16_f32 v69, v70, v71\nv_mul_f32 v72, v72, v230\nv_mul_f32 v73, v73, v231\nM 52,168,76\n"
"R 160,191,40960\nv_mul_f32 v74, v74, v232\nv_mul_f32 v75, v75, v233\nM 52,172,92\ns_add_u32 m0, s50, 81920\nv_mul_f32 v76, v76, v234\n"
"v_mul_f32 v77, v77, v235\nv_mul_f32 v78, v78, v236\nM 36,168,72\nR 162,191,45056\nv_mul_f32 v79, v79, v237\nv_cvt_pk_bf16_f32 v72, v72, v73\n"
"M 36,172,88\nbuffer_load_dwordx4 v220, s[4:7], s63 offen lds\nv_cvt_pk_bf16_f32 v73, v74, v75\nM 148,168,68\nR 164,191,49152\nv_cvt_pk_bf16_f32 v74, v76, v77\n"
"v_cvt_pk_bf16_f32 v75, v78, v79\nM 148,172,84\ns_mov_b64 exec, s[66:67]\nbuffer_store_dwordx2 v[68:69], v227, s[20:23], s64 offen offset:2048 sc1\ns_mov_b64 exec, -1\nv_mul_f32 v80, v80, v230\n"
"v_mul_f32 v81, v81, v231\nM 132,168,64\nR 166,191,53248\ns_waitcnt vmcnt(10)\ns_barrier\nv_mul_f32 v82, v82, v232\n"
"M 132,172,80\nG 4\nv_mul_f32 v83, v83, v233\nv_mul_f32 v84, v84, v234\nv_mul_f32 v85, v85, v235\nM 136,176,64\n"
"R 168,184,0\ns_lshl2_add_u32 s60, s45, s40\nv_lshlrev_b32 v224, 3, v252\ns_mul_i32 s60, s60, 7168\ns_bfm_b64 exec, 14, 28\nbuffer_load_dword v224, v224, s[12:15], s60 offen\n"
"s_mov_b64 exec, -1\nv_mul_f32 v86, v86, v236\nM 136,180,80\nv_mul_f32 v87, v87, v237\nv_cvt_pk_bf16_f32 v78, v80, v81\nv_cvt_pk_bf16_f32 v79, v82, v83\n"
"M 152,176,68\nR 170,184,4096\nv_cvt_pk_bf16_f32 v80, v84, v85\nv_cvt_pk_bf16_f32 v81, v86, v87\nM 152,180,84\nbuffer_load_dwordx4 v221, s[4:7], s63 offen offset:1024 lds\n"
"v_mul_f32 v88, v88, v230\nM 40,176,72\nR 172,184,8192\ns_mov_b32 s60, 155648\nv_bfe_u32 v246, v252, 4, 4\nv_lshl_add_u32 v246, v246, 3, s60\n"
"v_mul_f32 v89, v89, v231\nM 40,180,88\ns_add_u32 m0, s50, 90112\nv_mul_f32 v90, v90, v232\nv_mul_f32 v91, v91, v233\nv_mul_f32 v92, v92, v234\n"
"M 56,176,76\nR 174,184,12288\nds_read_b64 v[238:239], v246 offset:0\nv_mul_f32 v93, v93, v235\nM 56,180,92\nG 5\n"
"v_mul_f32 v94, v94, v236\nv_mul_f32 v95, v95, v237\nv_cvt_pk_bf16_f32 v84, v88, v89\nM 60,160,76\nR 176,185,0\nds_read_b64 v[240:241], v246 offset:128\n"
"v_cvt_pk_bf16_f32 v85, v90, v91\nM 60,164,92\ns_add_u32 s63, s52, 2949120\ns_mov_b64 exec, s[66:67]\nbuffer_store_dwordx2 v[74:75], v227, s[20:23], s64 offen offset:2560 sc1\ns_mov_b64 exec, -1\n"
"v_cvt_pk_bf16_f32 v86, v92, v93\nv_cvt_pk_bf16_f32 v87, v94, v95\nM 44,160,72\nR 178,185,4096\nds_read_b64 v[242:243], v246 offset:256\ns_lshl_b32 s60, s40, 12\n"
"s_add_u32 s60, s60, 53248\nv_add_u32 v230, s60, v252\nv_add_u32 v231, s60, v227\nM 44,164,88\nbuffer_load_dwordx4 v220, s[4:7], s63 offen lds\nM 156,160,68\n"
"R 180,185,8192\nds_read_b64 v[244:245], v246 offset:384\nM 156,164,84\nv_add_u32 v108, 161792, v252\nds_read_b128 v[112:115], v108\nM 140,160,64\n"
"R 182,185,12288\nds_read_b64 v[246:247], v246 offset:0\nM 140,164,80\nds_read_b128 v[104:107], v108 offset:1024\ns_mov_b64 exec, s[66:67]\nbuffer_store_dwordx2 v[80:81], v227, s[20:23], s64 offen offset:3072 sc1\n"
"s_mov_b64 exec, -1\nG 11\nM 128,168,96\nR 160,186,0\nM 128,172,112\nM 144,168,100\n"
"R 162,186,4096\nM 144,172,116\nbuffer_load_dwordx4 v221, s[4:7], s63 offen offset:1024 lds\nM 32,168,104\nR 164,186,8192\nG 11\n"
"v_mfma_f32_16x16x32_bf16 v[128:131], v[238:241], v[208:211], v[128:131]\nM 32,172,120\ns_mov_b64 exec, s[66:67]\nbuffer_store_dwordx2 v[86:87], v227, s[20:23], s64 offen offset:3584 sc1\ns_mov_b64 exec, -1\nG 3\n"
"v_lshlrev_b32 v100, 16, v104\nv_and_b32 v101, s77, v104\nM 48,168,108\nR 166,186,12288\nv_mfma_f32_16x16x32_bf16 v[132:135], v[240:243], v[208:211], v[132:135]\ns_not_b64 exec, s[66:67]\n"
"ds_write_b128 v230, v[16:19] offset:0\nds_write_b128 v230, v[20:23] offset:1024\ns_mov_b64 exec, -1\nv_lshlrev_b32 v102, 16, v105\nM 48,172,124\nv_and_b32 v103, s77, v105\n"
"v_lshlrev_b32 v96, 16, v106\nM 52,176,108\nR 168,187,0\nv_mfma_f32_16x16x32_bf16 v[144:147], v[244:247], v[210:213], v[144:147]\nv_and_b32 v97, s77, v106\nM 52,180,124\n"
"s_movk_i32 s63, 7168\nv_mad_u32_u24 v220, v228, s63, v229\nv_add_u32 v221, 3668992, v220\ns_add_u32 m0, s50, 98304\ns_add_u32 s63, s101, 0\nbuffer_load_dwordx4 v220, s[12:15], s63 offen lds\n"
"M 36,176,104\nR 170,187,4096\nv_mfma_f32_16x16x32_bf16 v[148:151], v[238:241], v[210:213], v[148:151]\nv_lshlrev_b32 v98, 16, v107\nM 36,180,120\nv_and_b32 v99, s77, v107\n"
"v_add_f32 v104, s78, v100\nM 148,176,100\nR 172,187,8192\nv_add_f32 v105, s78, v101\nM 148,180,116\nv_add_f32 v106, s78, v102\n"
"v_add_f32 v107, s78, v103\nM 132,176,96\nR 174,187,12288\nM 132,180,112\nG 6\nM 136,160,96\n"
"R 176,184,32768\nM 136,164,112\nbuffer_load_dwordx4 v221, s[12:15], s63 offen offset:1024 lds\nM 152,160,100\nR 178,184,36864\ns_not_b64 exec, s[66:67]\n"
"ds_write_b64 v231, v[68:69] offset:2048\nds_write_b64 v231, v[74:75] offset:2560\ns_mov_b64 exec, -1\nM 152,164,116\nM 40,160,104\nR 180,184,40960\n"
"M 40,164,120\nM 56,160,108\nR 182,184,45056\nM 56,164,124\nG 6\nM 60,168,108\n"
"R 160,185,32768\ns_not_b64 exec, s[66:67]\nds_write_b64 v231, v[80:81] offset:3072\nds_write_b64 v231, v[86:87] offset:3584\ns_mov_b64 exec, -1\nM 60,172,124\n"
"M 44,168,104\nR 162,185,36864\nM 44,172,120\ns_add_u32 m0, s50, 106496\nM 156,168,100\nR 164,185,40960\n"
"M 156,172,116\ns_add_u32 s63, s101, 128\nM 140,168,96\nR 166,185,45056\nM 140,172,112\nG 6\n"
"M 128,176,128\nR 168,186,32768\ns_waitcnt vmcnt(13) lgkmcnt(4)\ns_barrier\nM 128,180,144\nbuffer_load_dwordx4 v220, s[12:15], s63 offen lds\n"
"M 144,176,132\nR 170,186,36864\nM 144,180,148\ns_cmp_eq_u32 s78, 0\ns_cbranch_scc1 Lq151\nv_mov_b32 v230, 0\n"
"v_mov_b32 v231, 0\nv_mov_b32 v232, 0\nv_mov_b32 v233, 0\nv_mov_b32 v64, 0\nv_mov_b32 v65, 0\nv_mov_b32 v70, 0\n"
"v_mov_b32 v71, 0\nv_mov_b32 v234, 0\nv_mov_b32 v235, 0\nv_mov_b32 v236, 0\nv_mov_b32 v237, 0\nv_mov_b32 v76, 0\n"
"v_mov_b32 v77, 0\nv_mov_b32 v82, 0\nv_mov_b32 v83, 0\nLq151:\ns_lshl4_add_u32 s65, s41, s42\nM 32,176,136\n"
"R 172,186,40960\ns_lshl_b32 s65, s65, 12\nM 32,180,152\ns_add_u32 s65, s65, 77529088\nM 48,176,140\nR 174,186,45056\n"
"M 48,180,156\nG 4\nM 52,160,140\nR 176,187,32768\nM 52,164,156\nbuffer_load_dwordx4 v221, s[12:15], s63 offen offset:1024 lds\n"
"M 36,160,136\nR 178,187,36864\nM 36,164,152\ns_add_u32 m0, s50, 114688\nM 148,160,132\nR 180,187,40960\n"
"M 148,164,148\ns_add_u32 s63, s101, 256\nM 132,160,128\nR 182,187,45056\nM 132,164,144\nG 4\n"
"M 136,168,128\nR 160,184,49152\nM 136,172,144\nbuffer_load_dwordx4 v220, s[12:15], s63 offen lds\nM 152,168,132\nR 162,184,53248\n"
"M 152,172,148\nM 40,168,136\nR 164,184,57344\ns_lshl2_add_u32 s60, s45, s40\ns_waitcnt vmcnt(11)\nv_lshlrev_b32 v224, 3, v252\n"
"s_mul_i32 s60, s60, 7168\ns_bfm_b64 exec, 14, 42\nbuffer_load_dword v224, v224, s[12:15], s60 offen\ns_mov_b64 exec, -1\nM 40,172,152\nM 56,168,140\n"
"R 166,184,61440\nv_mfma_f32_16x16x32_bf16 v[136:139], v[242:245], v[208:211], v[136:139]\nM 56,172,156\nG 4\nM 60,176,140\nR 168,185,49152\n"
"v_mfma_f32_16x16x32_bf16 v[140:143], v[244:247], v[208:211], v[140:143]\nM 60,180,156\nbuffer_load_dwordx4 v221, s[12:15], s63 offen offset:1024 lds\nM 44,176,136\nR 170,185,53248\nv_mfma_f32_16x16x32_bf16 v[152:155], v[240:243], v[210:213], v[152:155]\n"
"s_add_u32 s63, s100, 0\nM 44,180,152\ns_add_u32 m0, s50, 0\nM 156,176,132\nR 172,185,57344\nv_mfma_f32_16x16x32_bf16 v[156:159], v[242:245], v[210:213], v[156:159]\n"
"M 156,180,148\nbuffer_load_dwordx4 v222, s[12:15], s63 offen lds\nM 140,176,128\nR 174,185,61440\nM 140,180,144\nG 4\n"
"M 128,160,160\nR 176,186,49152\nM 128,164,176\nM 144,160,164\nR 178,186,53248\nM 144,164,180\n"
"buffer_load_dwordx4 v223, s[12:15], s63 offen offset:1024 lds\nM 32,160,168\nR 180,186,57344\nM 32,164,184\nM 48,160,172\nR 182,186,61440\n"
"M 48,164,188\nG 4\nM 52,168,172\nR 160,187,49152\nM 52,172,188\nM 36,168,168\n"
"R 162,187,53248\nM 36,172,184\ns_movk_i32 s63, 2048\nv_mad_u32_u24 v220, v228, s63, v229\nv_add_u32 v221, 1047552, v220\ns_add_u32 m0, s50, 8192\n"
"s_add_u32 s63, s53, 0\nbuffer_load_dwordx4 v220, s[8:11], s63 offen lds\nM 148,168,164\nR 164,187,57344\ns_lshl_b32 s60, s40, 12\ns_add_u32 s60, s60, 49152\n"
"v_add_u32 v226, s60, v252\nv_add_u32 v111, s60, v227\nM 148,172,180\nM 132,168,160\nR 166,187,61440\ns_cmp_eq_u32 s40, 0\n"
"s_cselect_b64 s[88:89], 0, -1\ns_cselect_b32 s60, s41, 0\ns_cmp_lg_u32 s60, 0\ns_cselect_b64 s[66:67], -1, 0\nM 132,172,176\nG 4\n"
"M 136,176,160\nR 168,188,0\nM 136,180,176\ns_mov_b64 exec, s[88:89]\nds_read_b128 v[230:233], v226 offset:0\nds_read_b64 v[64:65], v111 offset:2048\n"
"s_mov_b64 exec, -1\nM 152,176,164\nR 170,188,4096\ns_waitcnt vmcnt(11) lgkmcnt(1)\ns_barrier\nM 152,180,180\n"
"buffer_load_dwordx4 v221, s[8:11], s63 offen offset:1024 lds\nM 40,176,168\nR 172,188,8192\nM 40,180,184\ns_mov_b64 exec, s[88:89]\nds_read_b64 v[70:71], v111 offset:2560\n"
"ds_read_b128 v[234:237], v226 offset:1024\ns_mov_b64 exec, -1\nM 56,176,172\nR 174,188,12288\nM 56,180,188\nM 60,160,172\n"
"R 176,189,0\nM 60,164,188\ns_mov_b64 exec, s[88:89]\nds_read_b64 v[76:77], v111 offset:3072\nds_read_b64 v[82:83], v111 offset:3584\ns_mov_b64 exec, -1\n"
"M 44,160,168\nR 178,189,4096\nM 44,164,184\nM 156,160,164\nR 180,189,8192\nM 156,164,180\n"
"s_mov_b64 exec, s[66:67]\nbuffer_load_dwordx4 v[230:233], v252, s[20:23], s65 offen offset:0 sc1\ns_mov_b64 exec, -1\nM 140,160,160\nR 182,189,12288\nM 140,164,176\n"
"G 6\nM 32,168,200\nR 160,190,0\ns_mov_b64 exec, s[66:67]\nbuffer_load_dwordx2 v[64:65], v227, s[20:23], s65 offen offset:2048 sc1\ns_mov_b64 exec, -1\n"
"M 32,172,216\nR 162,190,4096\nM 48,168,204\nR 164,190,8192\nM 48,172,220\nR 166,190,12288\n"
"G 4\nM 52,176,204\nR 168,191,0\ns_mov_b64 exec, s[66:67]\nbuffer_load_dwordx2 v[70:71], v227, s[20:23], s65 offen offset:2560 sc1\ns_mov_b64 exec, -1\n"
"M 52,180,220\nR 170,191,4096\nM 36,176,200\nR 172,191,8192\nM 36,180,216\nR 174,191,12288\n"
"G 4\nM 40,160,200\nR 176,188,16384\ns_mov_b64 exec, s[66:67]\nbuffer_load_dwordx4 v[234:237], v252, s[20:23], s65 offen offset:1024 sc1\ns_mov_b64 exec, -1\n"
"M 40,164,216\nR 178,188,20480\nM 56,160,204\nR 180,188,24576\nM 56,164,220\nR 182,188,28672\n"
"G 4\nM 60,168,204\nR 160,189,16384\ns_mov_b64 exec, s[66:67]\nbuffer_load_dwordx2 v[76:77], v227, s[20:23], s65 offen offset:3072 sc1\ns_mov_b64 exec, -1\n"
"M 60,172,220\nR 162,189,20480\nM 44,168,200\nR 164,189,24576\nM 44,172,216\nR 166,189,28672\n"
"G 4\nM 32,176,232\nR 168,190,16384\ns_mov_b64 exec, s[66:67]\nbuffer_load_dwordx2 v[82:83], v227, s[20:23], s65 offen offset:3584 sc1\ns_mov_b64 exec, -1\n"
"Lq185:\ns_lshr_b32 s60, s45, 3\ns_lshl2_add_u32 s60, s60, s40\ns_lshl_b32 s60, s60, 8\ns_waitcnt vmcnt(11)\nv_lshl_add_u32 v224, v252, 3, s60\n"
"v_add_u32 v224, Lq185+4096-LqPC, v224\nv_min_u32 v224, LqE-LqPC-4, v224\ns_mov_b64 exec, 3\nglobal_load_dword v224, v224, s[98:99]\ns_mov_b64 exec, -1\nM 32,180,248\n"
"R 170,190,20480\nM 48,176,236\nR 172,190,24576\nM 48,180,252\nR 174,190,28672\nG 4\n"
"M 52,160,236\nR 176,191,16384\nM 52,164,252\nR 178,191,20480\nM 36,160,232\nR 180,191,24576\n"
"M 36,164,248\nR 182,191,28672\nG 4\nM 40,168,232\nR 160,188,0\nM 40,172,248\n"
"R 162,188,4096\nM 56,168,236\nR 164,188,8192\nM 56,172,252\nR 166,188,12288\nG 4\n"
"M 60,176,236\nR 168,189,0\nM 60,180,252\nR 170,189,4096\nM 44,176,232\nR 172,189,8192\n"
"M 44,180,248\nR 174,189,12288\nG 4\nM 128,160,192\nR 176,190,0\nM 128,164,208\n"
"R 178,190,4096\ns_mov_b32 m0, s51\nM 144,160,196\nR 180,190,8192\nds_write_addtid_b32 v32 offset:0\nM 144,164,212\n"
"R 182,190,12288\nds_write_addtid_b32 v33 offset:256\nG 6\nM 148,168,196\nR 160,191,0\nds_write_addtid_b32 v34 offset:512\n"
"M 148,172,212\nR 162,191,4096\nds_write_addtid_b32 v35 offset:768\nM 132,168,192\nR 164,191,8192\nds_write_addtid_b32 v36 offset:1024\n"
"Lpg5:\ns_cbranch_execz LretQ0\nM 132,172,208\nR 166,191,12288\nds_write_addtid_b32 v37 offset:1280\nG 9\n"
"M 136,176,192\nR 168,188,16384\nds_write_addtid_b32 v38 offset:1536\nM 136,180,208\nR 170,188,20480\nds_write_addtid_b32 v39 offset:1792\n"
"M 152,176,196\nR 172,188,24576\nds_write_addtid_b32 v40 offset:2048\nM 152,180,212\nR 174,188,28672\nds_write_addtid_b32 v41 offset:2304\n"
"G 9\nM 156,160,196\nR 176,189,16384\nds_write_addtid_b32 v42 offset:2560\nM 156,164,212\nR 178,189,20480\n"
"ds_write_addtid_b32 v43 offset:2816\nM 140,160,192\nR 180,189,24576\nds_write_addtid_b32 v44 offset:3072\nds_write_addtid_b32 v45 offset:3328\nds_write_addtid_b32 v46 offset:3584\n"
"ds_write_addtid_b32 v47 offset:3840\nds_write_addtid_b32 v48 offset:4096\nds_write_addtid_b32 v49 offset:4352\nds_write_addtid_b32 v50 offset:4608\nds_write_addtid_b32 v51 offset:4864\nds_write_addtid_b32 v52 offset:5120\n"
"ds_write_addtid_b32 v53 offset:5376\nds_write_addtid_b32 v54 offset:5632\nds_write_addtid_b32 v55 offset:5888\nds_write_addtid_b32 v56 offset:6144\nds_write_addtid_b32 v57 offset:6400\nds_write_addtid_b32 v58 offset:6656\n"
"ds_write_addtid_b32 v59 offset:6912\nds_write_addtid_b32 v60 offset:7168\nds_write_addtid_b32 v61 offset:7424\nds_write_addtid_b32 v62 offset:7680\nds_write_addtid_b32 v63 offset:7936\nM 140,164,208\n"
"R 182,189,28672\nG 15\nM 128,168,224\nR 160,190,16384\ns_add_u32 m0, s50, 16384\nM 128,172,240\n"
"R 162,190,20480\nG 3\ns_barrier\ns_add_u32 s63, s53, 65536\nM 144,168,228\nR 164,190,24576\n"
"buffer_load_dwordx4 v220, s[8:11], s63 offen lds\nds_read2st64_b32 v[32:33], v225 offset0:0 offset1:1\nM 144,172,244\nR 166,190,28672\nbuffer_load_dwordx4 v221, s[8:11], s63 offen offset:1024 lds\nds_read2st64_b32 v[34:35], v225 offset0:2 offset1:3\n"
"G 6\nM 148,176,228\nR 168,191,16384\ns_add_u32 m0, s50, 24576\ns_add_u32 s63, s53, 131072\nbuffer_load_dwordx4 v220, s[8:11], s63 offen lds\n"
"ds_read2st64_b32 v[36:37], v225 offset0:4 offset1:5\nM 148,180,244\nR 170,191,20480\nbuffer_load_dwordx4 v221, s[8:11], s63 offen offset:1024 lds\nds_read2st64_b32 v[38:39], v225 offset0:6 offset1:7\nM 132,176,224\n"
"R 172,191,24576\ns_add_u32 m0, s50, 32768\ns_add_u32 s63, s53, 196608\nbuffer_load_dwordx4 v220, s[8:11], s63 offen lds\nds_read2st64_b32 v[40:41], v225 offset0:8 offset1:9\nM 132,180,240\n"
"R 174,191,28672\nbuffer_load_dwordx4 v221, s[8:11], s63 offen offset:1024 lds\nds_read2st64_b32 v[42:43], v225 offset0:10 offset1:11\nG 9\nM 136,160,224\ns_add_u32 m0, s50, 40960\n"
"s_add_u32 s63, s53, 262144\nbuffer_load_dwordx4 v220, s[8:11], s63 offen lds\nds_read2st64_b32 v[44:45], v225 offset0:12 offset1:13\nM 136,164,240\nbuffer_load_dwordx4 v221, s[8:11], s63 offen offset:1024 lds\nds_read2st64_b32 v[46:47], v225 offset0:14 offset1:15\n"
"M 152,160,228\ns_add_u32 m0, s50, 49152\ns_add_u32 s63, s53, 327680\nbuffer_load_dwordx4 v220, s[8:11], s63 offen lds\nds_read2st64_b32 v[48:49], v225 offset0:16 offset1:17\nM 152,164,244\n"
"ds_read2st64_b32 v[50:51], v225 offset0:18 offset1:19\nds_read2st64_b32 v[52:53], v225 offset0:20 offset1:21\nG 6\nM 156,168,228\nds_read2st64_b32 v[54:55], v225 offset0:22 offset1:23\nds_read2st64_b32 v[56:57], v225 offset0:24 offset1:25\n"
"M 156,172,244\nds_read2st64_b32 v[58:59], v225 offset0:26 offset1:27\nds_read2st64_b32 v[60:61], v225 offset0:28 offset1:29\nM 140,168,224\nds_read2st64_b32 v[62:63], v225 offset0:30 offset1:31\nM 140,172,240\n"
"G 7\nv_pk_add_f32 v[144:145], v[144:145], v[48:49]\nG 6\nv_pk_add_f32 v[146:147], v[146:147], v[50:51]\nG 5\nv_pk_add_f32 v[148:149], v[148:149], v[52:53]\n"
"G 4\nv_pk_add_f32 v[150:151], v[150:151], v[54:55]\nG 3\nv_pk_add_f32 v[152:153], v[152:153], v[56:57]\nG 2\nv_pk_add_f32 v[154:155], v[154:155], v[58:59]\n"
"G 1\nv_pk_add_f32 v[156:157], v[156:157], v[60:61]\nG 0\nv_pk_add_f32 v[158:159], v[158:159], v[62:63]\nv_pk_add_f32 v[128:129], v[128:129], v[32:33]\nv_pk_add_f32 v[130:131], v[130:131], v[34:35]\n"
"v_pk_add_f32 v[132:133], v[132:133], v[36:37]\nv_pk_add_f32 v[134:135], v[134:135], v[38:39]\nv_pk_add_f32 v[136:137], v[136:137], v[40:41]\nv_pk_add_f32 v[138:139], v[138:139], v[42:43]\nv_pk_add_f32 v[140:141], v[140:141], v[44:45]\nv_pk_add_f32 v[142:143], v[142:143], v[46:47]\n"
"v_cvt_pk_bf16_f32 v144, v144, v145\nv_cvt_pk_bf16_f32 v145, v146, v147\nv_cvt_pk_bf16_f32 v146, v148, v149\nv_cvt_pk_bf16_f32 v147, v150, v151\nv_cvt_pk_bf16_f32 v152, v152, v153\nv_cvt_pk_bf16_f32 v153, v154, v155\n"
"v_cvt_pk_bf16_f32 v154, v156, v157\nv_cvt_pk_bf16_f32 v155, v158, v159\nv_cvt_pk_bf16_f32 v128, v128, v129\nv_cvt_pk_bf16_f32 v129, v130, v131\nv_cvt_pk_bf16_f32 v130, v132, v133\nv_cvt_pk_bf16_f32 v131, v134, v135\n"
"v_cvt_pk_bf16_f32 v136, v136, v137\nv_cvt_pk_bf16_f32 v137, v138, v139\nv_cvt_pk_bf16_f32 v138, v140, v141\nv_cvt_pk_bf16_f32 v139, v142, v143\nv_mfma_f32_16x16x32_bf16 v[52:55], v[16:19], v[144:147], v[96:99]\nv_mfma_f32_16x16x32_bf16 v[48:51], v[0:3], v[144:147], v[100:103]\n"
"v_mfma_f32_16x16x32_bf16 v[60:63], v[20:23], v[152:155], v[96:99]\nv_mfma_f32_16x16x32_bf16 v[56:59], v[4:7], v[152:155], v[100:103]\ns_waitcnt vmcnt(13)\nv_max3_u32 v248, v233, v65, v71\nv_cmp_eq_u32 vcc, -1, v248\ns_nop 0\n"
"s_cbranch_vccnz Lq187\nLq188:\ns_waitcnt vmcnt(10)\nv_max3_u32 v248, v237, v77, v83\nv_cmp_eq_u32 vcc, -1, v248\ns_nop 0\n"
"s_cbranch_vccnz Lq189\nLq190:\nv_mfma_f32_16x16x32_bf16 v[36:39], v[0:3], v[128:131], v[96:99]\nv_mfma_f32_16x16x32_bf16 v[32:35], v[230:233], v[128:131], v[104:107]\nv_mfma_f32_16x16x32_bf16 v[44:47], v[4:7], v[136:139], v[96:99]\nv_mfma_f32_16x16x32_bf16 v[40:43], v[234:237], v[136:139], v[104:107]\n"
"s_lshl_b32 s84, s42, 11\ns_add_u32 s84, s84, s49\ns_add_u32 s84, s84, 2097152\ns_add_u32 s85, s84, 32768\ns_lshl2_add_u32 s60, s45, s40\ns_waitcnt vmcnt(9)\n"
"v_lshlrev_b32 v224, 3, v252\ns_mul_i32 s60, s60, 7168\ns_bfm_b64 exec, 28, 28\nbuffer_load_dword v224, v224, s[16:19], s60 offen\ns_mov_b64 exec, -1\nv_mov_b32 v238, s46\n"
"v_mov_b32 v239, s47\nv_max_f32 v52, v52, v53\nv_max_f32 v54, v54, v55\nv_max3_f32 v52, v52, v48, v49\nv_max3_f32 v54, v54, v50, v51\nv_max_f32 v118, v52, v54\n"
"v_max_f32 v60, v60, v61\nv_max_f32 v62, v62, v63\nv_max3_f32 v60, v60, v56, v57\nv_max3_f32 v62, v62, v58, v59\nv_max_f32 v119, v60, v62\nv_max_f32 v36, v36, v37\n"
"v_max_f32 v38, v38, v39\nv_max3_f32 v36, v36, v32, v33\nv_max3_f32 v38, v38, v34, v35\nv_max_f32 v116, v36, v38\nv_max_f32 v44, v44, v45\nv_max_f32 v46, v46, v47\n"
"v_max3_f32 v44, v44, v40, v41\nv_max3_f32 v46, v46, v42, v43\nv_max_f32 v117, v44, v46\nv_permlane32_swap_b32 v116, v118\ns_nop 0\nv_permlane32_swap_b32 v117, v119\n"
"v_max_f32 v116, v116, v118\nv_max_f32 v117, v117, v119\ns_nop 1\nv_permlane16_swap_b32 v116, v117\nv_max_f32 v116, v116, v117\nv_mov_b32 v117, v116\n"
"s_nop 1\nv_permlane16_swap_b32 v116, v117\nv_mov_b32 v118, v116\nv_mov_b32 v119, v117\ns_nop 0\nv_permlane32_swap_b32 v116, v118\n"
"v_permlane32_swap_b32 v117, v119\nv_mul_f32 v116, v116, v215\nv_mul_f32 v118, v118, v218\nv_mul_f32 v117, v117, v215\nv_mul_f32 v119, v119, v218\nv_fma_f32 v52, v52, v218, -v118\n"
"v_fma_f32 v54, v54, v218, -v118\nv_fma_f32 v60, v60, v218, -v119\nv_fma_f32 v62, v62, v218, -v119\nv_fma_f32 v36, v36, v215, -v116\nv_fma_f32 v38, v38, v215, -v116\nv_fma_f32 v44, v44, v215, -v117\n"
"v_fma_f32 v46, v46, v215, -v117\nv_exp_f32 v52, v52\nv_exp_f32 v54, v54\nv_exp_f32 v60, v60\nv_exp_f32 v62, v62\nv_exp_f32 v36, v36\n"
"v_exp_f32 v38, v38\nv_exp_f32 v44, v44\nv_exp_f32 v46, v46\nv_add_f32 v118, v52, v54\nv_add_f32 v119, v60, v62\nv_add_f32 v116, v36, v38\n"
"v_add_f32 v117, v44, v46\ns_nop 0\nv_permlane32_swap_b32 v116, v118\nv_permlane32_swap_b32 v117, v119\nv_add_f32 v116, v116, v118\nv_add_f32 v117, v117, v119\n"
"s_nop 1\nv_permlane16_swap_b32 v116, v117\nv_add_f32 v116, v116, v117\ns_barrier\nv_rcp_f32 v116, v116\ns_nop 0\n"
"v_mov_b32 v117, v116\ns_nop 1\nv_permlane16_swap_b32 v116, v117\nv_mov_b32 v118, v116\nv_mov_b32 v119, v117\ns_nop 0\n"
"v_permlane32_swap_b32 v116, v118\nv_permlane32_swap_b32 v117, v119\nv_mul_f32 v52, v52, v118\nv_mul_f32 v54, v54, v118\nv_mul_f32 v60, v60, v119\nv_mul_f32 v62, v62, v119\n"
"v_mul_f32 v36, v36, v116\nv_mul_f32 v38, v38, v116\nv_mul_f32 v44, v44, v117\nv_mul_f32 v46, v46, v117\nv_cvt_pk_bf16_f32 v52, v52, v52\nv_cvt_pk_bf16_f32 v54, v54, v54\n"
"v_cvt_pk_bf16_f32 v60, v60, v60\nv_cvt_pk_bf16_f32 v62, v62, v62\nv_cvt_pk_bf16_f32 v36, v36, v36\nv_cvt_pk_bf16_f32 v38, v38, v38\nv_cvt_pk_bf16_f32 v44, v44, v44\nv_cvt_pk_bf16_f32 v46, v46, v46\n"
"v_and_b32 v48, v52, v112\nv_and_b32 v49, v54, v113\nv_and_b32 v50, v52, v114\nv_and_b32 v51, v54, v115\nv_and_b32 v56, v60, v112\nv_and_b32 v57, v62, v113\n"
"v_and_b32 v58, v60, v114\nv_and_b32 v59, v62, v115\nv_mfma_f32_16x16x32_bf16 v[144:147], v[66:69], v[48:51], 0\nv_mfma_f32_16x16x32_bf16 v[148:151], v[72:75], v[48:51], 0\nv_and_b32 v32, v36, v112\nv_and_b32 v33, v38, v113\n"
"v_and_b32 v34, v36, v114\nv_and_b32 v35, v38, v115\nv_mfma_f32_16x16x32_bf16 v[152:155], v[78:81], v[56:59], 0\nv_mfma_f32_16x16x32_bf16 v[156:159], v[84:87], v[56:59], 0\nv_and_b32 v40, v44, v112\nv_and_b32 v41, v46, v113\n"
"v_and_b32 v42, v44, v114\nv_and_b32 v43, v46, v115\nv_mfma_f32_16x16x32_bf16 v[128:131], v[64:67], v[32:35], 0\nv_mfma_f32_16x16x32_bf16 v[132:135], v[70:73], v[32:35], 0\nv_mfma_f32_16x16x32_bf16 v[136:139], v[76:79], v[40:43], 0\nv_mfma_f32_16x16x32_bf16 v[140:143], v[82:85], v[40:43], 0\n"
"v_cvt_pk_bf16_f32 v144, v144, v145\nv_cvt_pk_bf16_f32 v145, v146, v147\nv_cvt_pk_bf16_f32 v146, v148, v149\nv_cvt_pk_bf16_f32 v147, v150, v151\nbuffer_store_dwordx4 v[144:147], v252, s[20:23], s85 offen offset:0 sc1\nv_cvt_pk_bf16_f32 v152, v152, v153\n"
"v_cvt_pk_bf16_f32 v153, v154, v155\nv_cvt_pk_bf16_f32 v154, v156, v157\nv_cvt_pk_bf16_f32 v155, v158, v159\nbuffer_store_dwordx4 v[152:155], v252, s[20:23], s85 offen offset:1024 sc1\nv_cvt_pk_bf16_f32 v128, v128, v129\nv_cvt_pk_bf16_f32 v129, v130, v131\n"
"v_cvt_pk_bf16_f32 v130, v132, v133\nv_cvt_pk_bf16_f32 v131, v134, v135\nbuffer_store_dwordx4 v[128:131], v252, s[20:23], s84 offen offset:0 sc1\nv_cvt_pk_bf16_f32 v136, v136, v137\nv_cvt_pk_bf16_f32 v137, v138, v139\nv_cvt_pk_bf16_f32 v138, v140, v141\n"
"v_cvt_pk_bf16_f32 v139, v142, v143\nbuffer_store_dwordx4 v[136:139], v252, s[20:23], s84 offen offset:1024 sc1\ns_mov_b64 exec, 0\ns_cmp_eq_u32 s40, 1\ns_cbranch_scc1 Lpg7\ns_cmp_eq_u32 s40, 2\n"
"s_cbranch_scc1 LretO0\ns_cmp_eq_u32 s40, 3\ns_cbranch_scc1 LretO0\ns_branch Lpg6\nLretO0:\ns_mov_b64 exec, -1\n"
"s_add_u32 s61, s79, 88080384\ns_lshl_b32 s62, s42, 3\ns_add_u32 s61, s61, s62\ns_waitcnt vmcnt(0)\ns_mov_b64 exec, 1\nbuffer_atomic_swap_x2 v[238:239], off, s[20:23], s61 sc1\n"
"s_mov_b64 exec, -1\nv_bfe_u32 v250, v252, 7, 1\nv_mul_u32_u24 v250, 2176, v250\nv_bfe_u32 v251, v252, 4, 3\nv_lshl_add_u32 v250, v251, 3, v250\ns_lshl_b32 s60, s90, 6\n"
"v_add_u32 v250, s60, v250\ns_sleep 3\nv_mov_b32 v248, 0\nv_mov_b32 v249, 0\ns_add_u32 s61, s80, 88080384\ns_mov_b64 exec, 0xffff\n"
"buffer_atomic_or_x2 v[248:249], v250, s[20:23], s61 offen sc0 sc1\ns_mov_b64 exec, -1\nbuffer_load_dwordx4 v221, s[8:11], s63 offen offset:1024 lds\ns_add_u32 m0, s50, 57344\ns_add_u32 s63, s53, 393216\nbuffer_load_dwordx4 v220, s[8:11], s63 offen lds\n"
"buffer_load_dwordx4 v221, s[8:11], s63 offen offset:1024 lds\ns_add_u32 m0, s50, 65536\ns_add_u32 s63, s53, 458752\nbuffer_load_dwordx4 v220, s[8:11], s63 offen lds\nbuffer_load_dwordx4 v221, s[8:11], s63 offen offset:1024 lds\ns_add_u32 m0, s50, 73728\n"
"s_add_u32 s63, s53, 524288\nbuffer_load_dwordx4 v220, s[8:11], s63 offen lds\nbuffer_load_dwordx4 v221, s[8:11], s63 offen offset:1024 lds\ns_add_u32 m0, s50, 81920\ns_add_u32 s63, s53, 589824\nbuffer_load_dwordx4 v220, s[8:11], s63 offen lds\n"
"buffer_load_dwordx4 v221, s[8:11], s63 offen offset:1024 lds\ns_add_u32 m0, s50, 90112\ns_add_u32 s63, s53, 655360\nbuffer_load_dwordx4 v220, s[8:11], s63 offen lds\ns_waitcnt vmcnt(10)\nv_cmp_eq_u64 s[86:87], s[46:47], v[248:249]\n"
"s_nop 1\ns_and_b32 s88, s86, 0xffff\ns_cmp_eq_u32 s88, 0xffff\ns_cbranch_scc1 Lq201\nv_mov_b32 v246, 0\nv_mov_b32 v247, 0\n"
"s_add_u32 s61, s80, 88080384\ns_mov_b64 exec, 0xffff\nbuffer_atomic_or_x2 v[246:247], v250, s[20:23], s61 offen sc0 sc1\ns_mov_b64 exec, -1\nLq201:\nbuffer_load_dwordx4 v221, s[8:11], s63 offen offset:1024 lds\n"
"v_bfe_u32 v234, v252, 4, 4\nv_lshrrev_b32 v235, 8, v252\nv_lshlrev_b32 v235, 2, v235\nv_sub_u32 v234, v234, v235\nv_lshlrev_b32 v235, 4, v234\ns_mov_b64 s[60:61], 0x3f80\n"
"v_lshlrev_b64 v[208:209], v235, s[60:61]\nv_cmp_gt_u32 s[60:61], 4, v234\ns_nop 1\nv_cndmask_b32 v212, 0, v208, s[60:61]\nv_cndmask_b32 v208, 0, v208, s[60:61]\nv_cndmask_b32 v213, 0, v209, s[60:61]\n"
"v_cndmask_b32 v209, 0, v209, s[60:61]\ns_nop 1\nv_mfma_f32_16x16x32_bf16 v[0:3], v[208:211], v[192:195], 0\nv_mfma_f32_16x16x32_bf16 v[4:7], v[210:213], v[192:195], 0\nv_mfma_f32_16x16x32_bf16 v[8:11], v[208:211], v[196:199], 0\nv_mfma_f32_16x16x32_bf16 v[12:15], v[210:213], v[196:199], 0\n"
"v_mfma_f32_16x16x32_bf16 v[16:19], v[208:211], v[200:203], 0\nv_mfma_f32_16x16x32_bf16 v[20:23], v[210:213], v[200:203], 0\nv_mfma_f32_16x16x32_bf16 v[24:27], v[208:211], v[204:207], 0\nv_mfma_f32_16x16x32_bf16 v[28:31], v[210:213], v[204:207], 0\ns_cmp_eq_u32 s88, 0xffff\ns_cbranch_scc1 Lq202\n"
"s_waitcnt vmcnt(1)\nv_mov_b32 v248, v246\nv_mov_b32 v249, v247\nv_cmp_eq_u64 s[86:87], s[46:47], v[248:249]\ns_nop 1\ns_and_b32 s62, s86, 0xffff\n"
"s_cmp_eq_u32 s62, 0xffff\ns_cbranch_scc1 Lq193\nLq192:\nv_mov_b32 v248, 0\nv_mov_b32 v249, 0\ns_add_u32 s61, s80, 88080384\n"
"s_mov_b64 exec, 0xffff\nbuffer_atomic_or_x2 v[248:249], v250, s[20:23], s61 offen sc0 sc1\ns_mov_b64 exec, -1\ns_waitcnt vmcnt(0)\nv_cmp_eq_u64 s[86:87], s[46:47], v[248:249]\ns_nop 1\n"
"s_and_b32 s62, s86, 0xffff\ns_cmp_eq_u32 s62, 0xffff\ns_cbranch_scc1 Lq193\ns_sub_u32 s48, s48, 1\ns_cmp_gt_i32 s48, 0\ns_cbranch_scc1 Lq192\n"
"Lq193:\nLq202:\ns_add_u32 s64, s70, 2097152\nbuffer_load_dwordx4 a[0:3], v252, s[20:23], s64 offen offset:0 sc1\ns_add_u32 s65, s70, 2129920\nbuffer_load_dwordx4 a[4:7], v252, s[20:23], s65 offen offset:0 sc1\n"
"s_add_u32 s66, s71, 2097152\nbuffer_load_dwordx4 a[8:11], v252, s[20:23], s66 offen offset:0 sc1\ns_add_u32 s67, s71, 2129920\nbuffer_load_dwordx4 a[12:15], v252, s[20:23], s67 offen offset:0 sc1\nbuffer_load_dwordx4 a[16:19], v252, s[20:23], s64 offen offset:1024 sc1\nbuffer_load_dwordx4 a[20:23], v252, s[20:23], s65 offen offset:1024 sc1\n"
"buffer_load_dwordx4 a[24:27], v252, s[20:23], s66 offen offset:1024 sc1\nbuffer_load_dwordx4 a[28:31], v252, s[20:23], s67 offen offset:1024 sc1\ns_barrier\nR 160,184,8192\nR 162,184,12288\nR 164,184,16384\n"
"R 166,184,20480\nR 168,185,8192\nR 170,185,12288\nR 172,185,16384\nR 174,185,20480\nR 176,186,8192\n"
"R 178,186,12288\nR 180,186,16384\nR 182,186,20480\nbuffer_load_dwordx4 a[32:35], v252, s[20:23], s64 offen offset:2048 sc1\nbuffer_load_dwordx4 a[36:39], v252, s[20:23], s65 offen offset:2048 sc1\nbuffer_load_dwordx4 a[40:43], v252, s[20:23], s66 offen offset:2048 sc1\n"
"buffer_load_dwordx4 a[44:47], v252, s[20:23], s67 offen offset:2048 sc1\nbuffer_load_dwordx4 a[48:51], v252, s[20:23], s64 offen offset:3072 sc1\nbuffer_load_dwordx4 a[52:55], v252, s[20:23], s65 offen offset:3072 sc1\nbuffer_load_dwordx4 a[56:59], v252, s[20:23], s66 offen offset:3072 sc1\nbuffer_load_dwordx4 a[60:63], v252, s[20:23], s67 offen offset:3072 sc1\ns_add_u32 s64, s70, 2101248\n"
"buffer_load_dwordx4 a[64:67], v252, s[20:23], s64 offen offset:0 sc1\ns_add_u32 s65, s70, 2134016\nbuffer_load_dwordx4 a[68:71], v252, s[20:23], s65 offen offset:0 sc1\ns_add_u32 s66, s71, 2101248\nbuffer_load_dwordx4 a[72:75], v252, s[20:23], s66 offen offset:0 sc1\ns_add_u32 s67, s71, 2134016\n"
"buffer_load_dwordx4 a[76:79], v252, s[20:23], s67 offen offset:0 sc1\ns_waitcnt vmcnt(19) lgkmcnt(10)\nM 0,160,0\nbuffer_load_dwordx4 a[80:83], v252, s[20:23], s64 offen offset:1024 sc1\ns_waitcnt vmcnt(16) lgkmcnt(8)\nM 0,164,16\n"
"M 16,160,4\ns_lshl_b32 s60, s40, 5\nv_xor_b32 v238, s60, v188\nv_and_b32 v238, 0xfffffbff, v238\ns_waitcnt vmcnt(15)\nM 16,164,20\n"
"v_mov_b32 v242, s76\nv_mov_b32 v243, s76\nv_mfma_f32_16x16x32_bf16 v[32:35], v[160:163], a[8:11], 0\nbuffer_load_dwordx4 a[84:87], v252, s[20:23], s65 offen offset:1024 sc1\ns_waitcnt vmcnt(15)\nM 32,164,24\n"
"v_mov_b32 v244, s76\nv_mov_b32 v245, s76\nv_mfma_f32_16x16x32_bf16 v[48:51], v[160:163], a[12:15], 0\ns_waitcnt vmcnt(14)\nM 48,164,28\nG 4\n"
"v_mfma_f32_16x16x32_bf16 v[52:55], v[168:171], a[12:15], 0\nR 160,187,8192\nM 52,172,28\nbuffer_load_dwordx4 a[88:91], v252, s[20:23], s66 offen offset:1024 sc1\nv_mfma_f32_16x16x32_bf16 v[36:39], v[168:171], a[8:11], 0\nR 162,187,12288\n"
"M 36,172,24\nM 20,168,4\nR 164,187,16384\nR 64,238,32768\nR 66,238,36864\nM 20,172,20\n"
"buffer_load_dwordx4 a[92:95], v252, s[20:23], s67 offen offset:1024 sc1\nM 4,168,0\nR 166,187,20480\nR 68,238,33792\nR 70,238,37888\nM 4,172,16\n"
"G 8\nM 8,176,0\nR 168,184,24576\nM 8,180,16\nbuffer_load_dwordx4 a[96:99], v252, s[20:23], s64 offen offset:2048 sc1\nM 24,176,4\n"
"R 170,184,28672\nM 24,180,20\nv_mfma_f32_16x16x32_bf16 v[40:43], v[176:179], a[8:11], 0\nR 172,184,32768\nG 6\nv_mfma_f32_16x16x32_bf16 v[72:75], v[242:245], v[64:67], 0\n"
"M 40,180,24\nG 3\nv_mfma_f32_16x16x32_bf16 v[72:75], v[242:245], v[68:71], v[72:75]\nbuffer_load_dwordx4 a[100:103], v252, s[20:23], s65 offen offset:2048 sc1\nv_mfma_f32_16x16x32_bf16 v[56:59], v[176:179], a[12:15], 0\nR 174,184,36864\n"
"M 56,180,28\nv_mfma_f32_16x16x32_bf16 v[60:63], v[160:163], a[12:15], 0\nR 176,185,24576\nM 60,164,28\nbuffer_load_dwordx4 a[104:107], v252, s[20:23], s66 offen offset:2048 sc1\nv_lshrrev_b32 v64, 1, v252\n"
"v_mfma_f32_16x16x32_bf16 v[44:47], v[160:163], a[8:11], 0\nR 178,185,28672\nv_mov_b32 v73, s46\ns_and_b32 s61, s40, 3\ns_lshl_b32 s61, s61, 7\ns_add_u32 s61, s61, s91\n"
"s_add_u32 s61, s61, 393216\ns_mov_b64 exec, 0xffff\nbuffer_store_dwordx2 v[72:73], v64, s[20:23], s61 offen sc1\ns_mov_b64 exec, -1\nM 44,164,24\nM 28,160,4\n"
"R 180,185,32768\nM 28,164,20\nbuffer_load_dwordx4 a[108:111], v252, s[20:23], s67 offen offset:2048 sc1\nM 12,160,0\nR 182,185,36864\nM 12,164,16\n"
"s_lshl_b32 s60, s40, 5\nv_xor_b32 v238, s60, v188\nG 4\ns_waitcnt vmcnt(20)\nM 0,168,32\nR 160,186,24576\n"
"v_and_b32 v238, 0xfffffbff, v238\ns_waitcnt vmcnt(16)\nM 0,172,48\nbuffer_load_dwordx4 a[112:115], v252, s[20:23], s64 offen offset:3072 sc1\nM 16,168,36\nR 162,186,28672\n"
"s_waitcnt vmcnt(16)\nM 16,172,52\nM 32,168,40\nR 164,186,32768\ns_waitcnt vmcnt(15)\nM 32,172,56\n"
"buffer_load_dwordx4 a[116:119], v252, s[20:23], s65 offen offset:3072 sc1\ns_barrier\nM 48,168,44\nR 166,186,36864\nR 64,238,40960\nR 66,238,45056\n"
"s_waitcnt vmcnt(15)\nM 48,172,60\nG 6\nM 52,176,44\nR 168,187,24576\nR 68,238,41984\n"
"R 70,238,46080\nM 52,180,60\nbuffer_load_dwordx4 a[120:123], v252, s[20:23], s66 offen offset:3072 sc1\nM 36,176,40\nR 170,187,28672\nM 36,180,56\n"
"M 20,176,36\nR 172,187,32768\nM 20,180,52\nbuffer_load_dwordx4 a[124:127], v252, s[20:23], s67 offen offset:3072 sc1\nM 4,176,32\nR 174,187,36864\n"
"G 6\nv_mfma_f32_16x16x32_bf16 v[72:75], v[242:245], v[64:67], 0\nM 4,180,48\nG 3\nv_mfma_f32_16x16x32_bf16 v[72:75], v[242:245], v[68:71], v[72:75]\nM 8,160,32\n"
"R 176,184,40960\nM 8,164,48\ns_add_u32 s64, s70, 2105344\nbuffer_load_dwordx4 a[128:131], v252, s[20:23], s64 offen offset:0 sc1\nM 24,160,36\nR 178,184,45056\n"
"M 24,164,52\nv_lshrrev_b32 v64, 1, v252\nv_mov_b32 v73, s46\ns_and_b32 s61, s40, 3\nLpg6:\ns_cbranch_execz LretO0\n"
"s_lshl_b32 s61, s61, 7\ns_add_u32 s61, s61, s91\ns_add_u32 s61, s61, 524288\ns_mov_b64 exec, 0xffff\nbuffer_store_dwordx2 v[72:73], v64, s[20:23], s61 offen sc1\ns_mov_b64 exec, -1\n"
"M 40,160,40\nR 180,184,49152\nM 40,164,56\ns_add_u32 s65, s70, 2138112\nbuffer_load_dwordx4 a[132:135], v252, s[20:23], s65 offen offset:0 sc1\nM 56,160,44\n"
"R 182,184,53248\nM 56,164,60\ns_lshl_b32 s60, s40, 5\nv_xor_b32 v238, s60, v188\nG 4\nM 60,168,44\n"
"R 160,185,40960\nv_and_b32 v238, 0xfffffbff, v238\nM 60,172,60\ns_add_u32 s66, s71, 2105344\nbuffer_load_dwordx4 a[136:139], v252, s[20:23], s66 offen offset:0 sc1\nM 44,168,40\n"
"R 162,185,45056\nM 44,172,56\nM 28,168,36\nR 164,185,49152\nM 28,172,52\ns_add_u32 s67, s71, 2138112\n"
"buffer_load_dwordx4 a[140:143], v252, s[20:23], s67 offen offset:0 sc1\nM 12,168,32\nR 166,185,53248\nM 12,172,48\ns_barrier\nG 4\n"
"s_waitcnt vmcnt(21)\nM 0,176,64\nR 168,186,40960\nR 64,238,49152\nR 66,238,53248\ns_waitcnt vmcnt(17)\n"
"M 0,180,80\nbuffer_load_dwordx4 a[144:147], v252, s[20:23], s64 offen offset:1024 sc1\nM 16,176,68\nR 170,186,45056\nR 68,238,50176\nR 70,238,54272\n"
"s_waitcnt vmcnt(17)\nM 16,180,84\nM 32,176,72\nR 172,186,49152\ns_waitcnt vmcnt(16)\nM 32,180,88\n"
"buffer_load_dwordx4 a[148:151], v252, s[20:23], s65 offen offset:1024 sc1\nM 48,176,76\nR 174,186,53248\ns_add_u32 m0, s50, 98304\ns_waitcnt vmcnt(16)\nM 48,180,92\n"
"G 8\nM 52,160,76\nR 176,187,40960\nG 6\nv_mfma_f32_16x16x32_bf16 v[72:75], v[242:245], v[64:67], 0\ns_add_u32 s63, s53, 720896\n"
"M 52,164,92\nG 3\nv_mfma_f32_16x16x32_bf16 v[72:75], v[242:245], v[68:71], v[72:75]\nbuffer_load_dwordx4 a[152:155], v252, s[20:23], s66 offen offset:1024 sc1\nM 36,160,72\nR 178,187,45056\n"
"M 36,164,88\nbuffer_load_dwordx4 v220, s[8:11], s63 offen lds\nM 20,160,68\nR 180,187,49152\nM 20,164,84\nbuffer_load_dwordx4 a[156:159], v252, s[20:23], s67 offen offset:1024 sc1\n"
"v_lshrrev_b32 v64, 1, v252\nM 4,160,64\nR 182,187,53248\nv_mov_b32 v73, s46\ns_and_b32 s61, s40, 3\ns_lshl_b32 s61, s61, 7\n"
"s_add_u32 s61, s61, s91\ns_add_u32 s61, s61, 655360\ns_mov_b64 exec, 0xffff\nbuffer_store_dwordx2 v[72:73], v64, s[20:23], s61 offen sc1\ns_mov_b64 exec, -1\nM 4,164,80\n"
"G 4\nM 8,168,64\nR 160,184,57344\nM 8,172,80\nbuffer_load_dwordx4 a[160:163], v252, s[20:23], s64 offen offset:2048 sc1\nM 24,168,68\n"
"R 162,184,61440\nbuffer_load_dwordx4 v221, s[8:11], s63 offen offset:1024 lds\nM 24,172,84\nM 40,168,72\nR 164,188,0\nM 40,172,88\n"
"buffer_load_dwordx4 a[164:167], v252, s[20:23], s65 offen offset:2048 sc1\nM 56,168,76\nR 166,188,4096\nM 56,172,92\ns_and_b32 s60, s41, 1\ns_lshl1_add_u32 s60, s60, s90\n"
"s_lshl_b32 s60, s60, 5\nv_xor_b32 v238, s60, v184\nG 4\nM 60,176,76\nR 168,185,57344\nv_and_b32 v238, 0xfffffbff, v238\n"
"M 60,180,92\nbuffer_load_dwordx4 a[168:171], v252, s[20:23], s66 offen offset:2048 sc1\nM 44,176,72\nR 170,185,61440\nR 64,238,0\nR 66,238,4096\n"
"M 44,180,88\ns_add_u32 m0, s50, 106496\nM 28,176,68\nR 172,189,0\nR 68,238,1024\nR 70,238,5120\n"
"M 28,180,84\nbuffer_load_dwordx4 a[172:175], v252, s[20:23], s67 offen offset:2048 sc1\nM 12,176,64\nR 174,189,4096\ns_add_u32 s63, s53, 786432\nM 12,180,80\n"
"s_barrier\nG 8\ns_waitcnt vmcnt(24)\nM 0,160,96\nR 176,186,57344\ns_waitcnt vmcnt(19)\n"
"M 0,164,112\nbuffer_load_dwordx4 a[176:179], v252, s[20:23], s64 offen offset:3072 sc1\nbuffer_load_dwordx4 v220, s[8:11], s63 offen lds\nM 16,160,100\nR 178,186,61440\nG 6\n"
"v_mfma_f32_16x16x32_bf16 v[72:75], v[242:245], v[64:67], 0\ns_waitcnt vmcnt(20)\nM 16,164,116\nG 3\nv_mfma_f32_16x16x32_bf16 v[72:75], v[242:245], v[68:71], v[72:75]\nbuffer_load_dwordx4 v221, s[8:11], s63 offen offset:1024 lds\n"
"M 32,160,104\nR 180,190,0\ns_lshr_b32 s60, s45, 3\ns_lshl2_add_u32 s60, s60, s40\ns_lshl_b32 s60, s60, 8\nv_lshl_add_u32 v224, v252, 3, s60\n"
"v_add_u32 v224, Lqw0+24576-LqPC, v224\nv_min_u32 v224, LqE-LqPC-4, v224\ns_mov_b64 exec, 3\nglobal_load_dword v224, v224, s[98:99]\ns_mov_b64 exec, -1\ns_add_u32 s63, s53, 851968\n"
"s_waitcnt vmcnt(21)\nM 32,164,120\nbuffer_load_dwordx4 a[180:183], v252, s[20:23], s65 offen offset:3072 sc1\nM 48,160,108\nR 182,190,4096\ns_add_u32 m0, s50, 114688\n"
"s_waitcnt vmcnt(21)\nM 48,164,124\nbuffer_load_dwordx4 v220, s[8:11], s63 offen lds\nG 4\nv_lshrrev_b32 v64, 1, v252\nM 52,168,108\n"
"R 160,187,57344\nv_mov_b32 v73, s46\ns_and_b32 s61, s40, 1\ns_lshl_b32 s61, s61, 7\ns_add_u32 s61, s61, s91\ns_add_u32 s61, s61, 786432\n"
"s_mov_b64 exec, 0xffff\nbuffer_store_dwordx2 v[72:73], v64, s[20:23], s61 offen sc1\ns_mov_b64 exec, -1\nM 52,172,124\nbuffer_load_dwordx4 a[184:187], v252, s[20:23], s66 offen offset:3072 sc1\nM 36,168,104\n"
"R 162,187,61440\nM 36,172,120\nbuffer_load_dwordx4 v221, s[8:11], s63 offen offset:1024 lds\nM 20,168,100\nR 164,191,0\nM 20,172,116\n"
"buffer_load_dwordx4 a[188:191], v252, s[20:23], s67 offen offset:3072 sc1\nM 4,168,96\nR 166,191,4096\nM 4,172,112\nG 4\nM 8,176,96\n"
"R 168,188,8192\nM 8,180,112\ns_add_u32 s64, s70, 2109440\nbuffer_load_dwordx4 a[192:195], v252, s[20:23], s64 offen offset:0 sc1\nM 24,176,100\nR 170,188,12288\n"
"M 24,180,116\nM 40,176,104\nR 172,188,16384\nM 40,180,120\ns_add_u32 s65, s70, 2142208\nbuffer_load_dwordx4 a[196:199], v252, s[20:23], s65 offen offset:0 sc1\n"
"M 56,176,108\nR 174,188,20480\nM 56,180,124\nG 4\nM 60,160,108\nR 176,189,8192\n"
"M 60,164,124\ns_add_u32 s66, s71, 2109440\nbuffer_load_dwordx4 a[200:203], v252, s[20:23], s66 offen offset:0 sc1\nM 44,160,104\nR 178,189,12288\ns_add_u32 s63, s53, 917504\n"
"M 44,164,120\ns_add_u32 m0, s50, 0\nM 28,160,100\nR 180,189,16384\nM 28,164,116\ns_add_u32 s67, s71, 2142208\n"
"buffer_load_dwordx4 a[204:207], v252, s[20:23], s67 offen offset:0 sc1\nM 12,160,96\nR 182,189,20480\nM 12,164,112\ns_waitcnt vmcnt(17)\ns_barrier\n"
"buffer_load_dwordx4 v220, s[8:11], s63 offen lds\nG 4\nM 0,168,128\nR 160,190,8192\nbuffer_load_dwordx4 v221, s[8:11], s63 offen offset:1024 lds\nM 0,172,144\n"
"buffer_load_dwordx4 a[208:211], v252, s[20:23], s64 offen offset:1024 sc1\ns_add_u32 m0, s50, 8192\ns_add_u32 s63, s53, 983040\nbuffer_load_dwordx4 v220, s[8:11], s63 offen lds\nM 16,168,132\nR 162,190,12288\n"
"buffer_load_dwordx4 v221, s[8:11], s63 offen offset:1024 lds\nM 16,172,148\nM 32,168,136\nR 164,190,16384\nM 32,172,152\nbuffer_load_dwordx4 a[212:215], v252, s[20:23], s65 offen offset:1024 sc1\n"
"M 48,168,140\nR 166,190,20480\nM 48,172,156\nG 4\nM 52,176,140\nR 168,191,8192\n"
"M 52,180,156\nbuffer_load_dwordx4 a[216:219], v252, s[20:23], s66 offen offset:1024 sc1\nM 36,176,136\nR 170,191,12288\nM 36,180,152\nM 20,176,132\n"
"R 172,191,16384\nM 20,180,148\nbuffer_load_dwordx4 a[220:223], v252, s[20:23], s67 offen offset:1024 sc1\nM 4,176,128\nR 174,191,20480\nM 4,180,144\n"
"G 4\nM 8,160,128\nR 176,188,24576\nM 8,164,144\nbuffer_load_dwordx4 a[224:227], v252, s[20:23], s64 offen offset:2048 sc1\nM 24,160,132\n"
"R 178,188,28672\nM 24,164,148\nM 40,160,136\nR 180,188,32768\nM 40,164,152\nbuffer_load_dwordx4 a[228:231], v252, s[20:23], s65 offen offset:2048 sc1\n"
"M 56,160,140\nR 182,188,36864\nM 56,164,156\nG 4\nM 60,168,140\nR 160,189,24576\n"
"M 60,172,156\nbuffer_load_dwordx4 a[232:235], v252, s[20:23], s66 offen offset:2048 sc1\nM 44,168,136\nR 162,189,28672\nM 44,172,152\nM 28,168,132\n"
"R 164,189,32768\nM 28,172,148\nbuffer_load_dwordx4 a[236:239], v252, s[20:23], s67 offen offset:2048 sc1\nM 12,168,128\nR 166,189,36864\nM 12,172,144\n"
"G 4\nM 0,176,160\nR 168,190,24576\ns_waitcnt vmcnt(25)\nM 0,180,176\nbuffer_load_dwordx4 a[240:243], v252, s[20:23], s64 offen offset:3072 sc1\n"
"M 16,176,164\nR 170,190,28672\ns_waitcnt vmcnt(22)\nM 16,180,180\nM 32,176,168\nR 172,190,32768\n"
"s_waitcnt vmcnt(19)\nM 32,180,184\nbuffer_load_dwordx4 a[244:247], v252, s[20:23], s65 offen offset:3072 sc1\nM 48,176,172\nR 174,190,36864\ns_waitcnt vmcnt(18)\n"
"M 48,180,188\nG 4\nM 52,160,172\nR 176,191,24576\nM 52,164,188\nbuffer_load_dwordx4 a[248:251], v252, s[20:23], s66 offen offset:3072 sc1\n"
"M 36,160,168\nR 178,191,28672\ns_waitcnt vmcnt(10)\ns_barrier\nM 36,164,184\nM 20,160,164\n"
"R 180,191,32768\nM 20,164,180\nbuffer_load_dwordx4 a[252:255], v252, s[20:23], s67 offen offset:3072 sc1\nM 4,160,160\nR 182,191,36864\nM 4,164,176\n"
"G 4\nM 8,168,160\nR 160,188,40960\nM 8,172,176\nM 24,168,164\nR 162,188,45056\n"
"M 24,172,180\nM 40,168,168\nR 164,188,49152\nM 40,172,184\nM 56,168,172\nR 166,188,53248\n"
"M 56,172,188\nG 4\nM 60,176,172\nR 168,189,40960\nM 60,180,188\ns_movk_i32 s63, 7168\n"
"v_mad_u32_u24 v220, v228, s63, v229\nv_add_u32 v221, 3668992, v220\ns_add_u32 m0, s50, 16384\ns_add_u32 s63, s54, 0\nbuffer_load_dwordx4 v220, s[12:15], s63 offen lds\nM 44,176,168\n"
"R 170,189,45056\nM 44,180,184\nM 28,176,164\nR 172,189,49152\nM 28,180,180\nbuffer_load_dwordx4 v221, s[12:15], s63 offen offset:1024 lds\n"
"M 12,176,160\nR 174,189,53248\ns_add_u32 m0, s50, 24576\nM 12,180,176\nG 4\nM 32,160,200\n"
"R 176,190,40960\ns_add_u32 s63, s54, 229376\ns_waitcnt vmcnt(11)\nM 32,164,216\nR 178,190,45056\nM 48,160,204\n"
"R 180,190,49152\ns_waitcnt vmcnt(10)\nM 48,164,220\nR 182,190,53248\nG 4\nM 52,168,204\n"
"R 160,191,40960\nbuffer_load_dwordx4 v220, s[12:15], s63 offen lds\nM 52,172,220\nR 162,191,45056\nM 36,168,200\nR 164,191,49152\n"
"M 36,172,216\nR 166,191,53248\nG 4\nM 40,176,200\nR 168,184,0\nbuffer_load_dwordx4 v221, s[12:15], s63 offen offset:1024 lds\n"
"M 40,180,216\nR 170,184,4096\ns_add_u32 m0, s50, 32768\nM 56,176,204\nR 172,184,8192\ns_add_u32 s63, s54, 128\n"
"M 56,180,220\nR 174,184,12288\nG 4\nM 60,160,204\nR 176,185,0\nbuffer_load_dwordx4 v220, s[12:15], s63 offen lds\n"
"M 60,164,220\nR 178,185,4096\nM 44,160,200\nR 180,185,8192\nM 44,164,216\nR 182,185,12288\n"
"G 4\ns_waitcnt vmcnt(10)\nM 32,168,232\nR 160,186,0\nbuffer_load_dwordx4 v221, s[12:15], s63 offen offset:1024 lds\ns_waitcnt vmcnt(7)\n"
"M 32,172,248\nR 162,186,4096\ns_add_u32 m0, s50, 40960\nM 48,168,236\nR 164,186,8192\ns_add_u32 s63, s54, 229504\n"
"s_waitcnt vmcnt(6)\nM 48,172,252\nR 166,186,12288\nG 4\nM 52,176,236\nR 168,187,0\n"
"buffer_load_dwordx4 v220, s[12:15], s63 offen lds\nM 52,180,252\nR 170,187,4096\nM 36,176,232\nR 172,187,8192\nM 36,180,248\n"
"R 174,187,12288\nG 4\nM 40,160,232\nR 176,188,40960\nbuffer_load_dwordx4 v221, s[12:15], s63 offen offset:1024 lds\nM 40,164,248\n"
"R 178,188,45056\nM 56,160,236\nR 180,188,49152\nM 56,164,252\nR 182,188,53248\nG 4\n"
"M 60,168,236\nR 160,189,40960\nM 60,172,252\nR 162,189,45056\nM 44,168,232\nR 164,189,49152\n"
"M 44,172,248\nR 166,189,53248\nG 4\nM 0,176,192\nR 168,190,40960\nM 0,180,208\n"
"R 170,190,45056\ns_mov_b32 m0, s51\nM 16,176,196\nR 172,190,49152\nv_cvt_pk_bf16_f32 v32, v32, v33\nv_cvt_pk_bf16_f32 v33, v34, v35\n"
"M 16,180,212\nR 174,190,53248\nG 4\nv_cvt_pk_bf16_f32 v34, v36, v37\nM 20,160,196\nR 176,191,40960\n"
"v_cvt_pk_bf16_f32 v35, v38, v39\nv_cvt_pk_bf16_f32 v36, v40, v41\nM 20,164,212\nR 178,191,45056\nv_cvt_pk_bf16_f32 v37, v42, v43\nv_cvt_pk_bf16_f32 v38, v44, v45\n"
"M 4,160,192\nR 180,191,49152\nv_cvt_pk_bf16_f32 v39, v46, v47\nv_cvt_pk_bf16_f32 v40, v48, v49\nM 4,164,208\nR 182,191,53248\n"
"G 4\nv_cvt_pk_bf16_f32 v41, v50, v51\nM 8,168,192\nR 160,184,0\nv_cvt_pk_bf16_f32 v42, v52, v53\nv_cvt_pk_bf16_f32 v43, v54, v55\n"
"M 8,172,208\nR 162,184,4096\nv_cvt_pk_bf16_f32 v44, v56, v57\nv_cvt_pk_bf16_f32 v45, v58, v59\nM 24,168,196\nR 164,184,8192\n"
"v_cvt_pk_bf16_f32 v46, v60, v61\nv_cvt_pk_bf16_f32 v47, v62, v63\nM 24,172,212\nR 166,184,12288\nds_write_addtid_b32 v32 offset:0\nG 5\n"
"M 28,176,196\nR 168,185,0\nds_write_addtid_b32 v33 offset:256\nM 28,180,212\nR 170,185,4096\nds_write_addtid_b32 v34 offset:512\n"
"M 12,176,192\nR 172,185,8192\nds_write_addtid_b32 v35 offset:768\nds_write_addtid_b32 v36 offset:1024\nds_write_addtid_b32 v37 offset:1280\nds_write_addtid_b32 v38 offset:1536\n"
"ds_write_addtid_b32 v39 offset:1792\nds_write_addtid_b32 v40 offset:2048\nds_write_addtid_b32 v41 offset:2304\nds_write_addtid_b32 v42 offset:2560\nds_write_addtid_b32 v43 offset:2816\nds_write_addtid_b32 v44 offset:3072\n"
"ds_write_addtid_b32 v45 offset:3328\nds_write_addtid_b32 v46 offset:3584\nds_write_addtid_b32 v47 offset:3840\nM 12,180,208\nR 174,185,12288\nG 15\n"
"M 0,160,224\nR 176,186,0\ns_add_u32 m0, s50, 49152\nM 0,164,240\nR 178,186,4096\nG 3\n"
"s_barrier\ns_add_u32 s63, s54, 458752\nM 16,160,228\nR 180,186,8192\nbuffer_load_dwordx4 v220, s[12:15], s63 offen lds\nds_read2st64_b32 v[32:33], v225 offset0:0 offset1:1\n"
"M 16,164,244\nR 182,186,12288\nds_read2st64_b32 v[34:35], v225 offset0:2 offset1:3\nG 6\nM 20,168,228\nR 160,187,0\n"
"ds_read2st64_b32 v[36:37], v225 offset0:4 offset1:5\nM 20,172,244\nR 162,187,4096\nds_read2st64_b32 v[38:39], v225 offset0:6 offset1:7\nM 4,168,224\nR 164,187,8192\n"
"buffer_load_dwordx4 v221, s[12:15], s63 offen offset:1024 lds\nds_read2st64_b32 v[40:41], v225 offset0:8 offset1:9\nM 4,172,240\nR 166,187,12288\ns_add_u32 m0, s50, 57344\ns_add_u32 s63, s54, 688128\n"
"Lpg7:\ns_cbranch_execz LretO0\nds_read2st64_b32 v[42:43], v225 offset0:10 offset1:11\nG 9\nM 8,176,224\nbuffer_load_dwordx4 v220, s[12:15], s63 offen lds\n"
"ds_read2st64_b32 v[44:45], v225 offset0:12 offset1:13\nM 8,180,240\nds_read2st64_b32 v[46:47], v225 offset0:14 offset1:15\nM 24,176,228\nM 24,180,244\nG 3\n"
"M 28,160,228\nbuffer_load_dwordx4 v221, s[12:15], s63 offen offset:1024 lds\nM 28,164,244\nM 12,160,224\nM 12,164,240\nG 0\n"
"s_barrier\nv_mfma_f32_16x16x32_bf16 v[0:3], v[208:211], v[32:35], v[0:3]\nv_mfma_f32_16x16x32_bf16 v[4:7], v[210:213], v[32:35], v[4:7]\nv_mfma_f32_16x16x32_bf16 v[8:11], v[208:211], v[36:39], v[8:11]\nv_mfma_f32_16x16x32_bf16 v[12:15], v[210:213], v[36:39], v[12:15]\nv_mfma_f32_16x16x32_bf16 v[16:19], v[208:211], v[40:43], v[16:19]\n"
"v_mfma_f32_16x16x32_bf16 v[20:23], v[210:213], v[40:43], v[20:23]\nv_mfma_f32_16x16x32_bf16 v[24:27], v[208:211], v[44:47], v[24:27]\nv_mfma_f32_16x16x32_bf16 v[28:31], v[210:213], v[44:47], v[28:31]\ns_lshl_b32 s84, s75, 11\ns_add_u32 s84, s84, s49\ns_add_u32 s84, s84, 10485760\n"
"s_add_u32 s85, s84, 32768\nv_mov_b32 v238, s46\nv_mov_b32 v239, s47\nv_cvt_pk_bf16_f32 v192, v0, v1\nv_cvt_pk_bf16_f32 v193, v2, v3\nv_cvt_pk_bf16_f32 v194, v4, v5\n"
"v_cvt_pk_bf16_f32 v195, v6, v7\nbuffer_store_dwordx4 v[192:195], v252, s[20:23], s84 offen offset:0 sc1\nv_cvt_pk_bf16_f32 v196, v8, v9\nv_cvt_pk_bf16_f32 v197, v10, v11\nv_cvt_pk_bf16_f32 v198, v12, v13\nv_cvt_pk_bf16_f32 v199, v14, v15\n"
"buffer_store_dwordx4 v[196:199], v252, s[20:23], s84 offen offset:1024 sc1\nv_cvt_pk_bf16_f32 v200, v16, v17\nv_cvt_pk_bf16_f32 v201, v18, v19\nv_cvt_pk_bf16_f32 v202, v20, v21\nv_cvt_pk_bf16_f32 v203, v22, v23\nbuffer_store_dwordx4 v[200:203], v252, s[20:23], s85 offen offset:0 sc1\n"
"v_cvt_pk_bf16_f32 v204, v24, v25\nv_cvt_pk_bf16_f32 v205, v26, v27\nv_cvt_pk_bf16_f32 v206, v28, v29\nv_cvt_pk_bf16_f32 v207, v30, v31\nbuffer_store_dwordx4 v[204:207], v252, s[20:23], s85 offen offset:1024 sc1\ns_mov_b64 exec, 0\n"
"s_cmp_eq_u32 s40, 1\ns_cbranch_scc1 Lpg9\ns_cmp_eq_u32 s40, 2\ns_cbranch_scc1 Lpg10\ns_cmp_eq_u32 s40, 3\ns_cbranch_scc1 Lpg11\n"
"s_branch Lpg8\nLretO0n:\ns_mov_b64 exec, -1\ns_add_u32 s61, s79, 88219648\ns_lshl_b32 s62, s75, 3\ns_add_u32 s61, s61, s62\n"
"s_waitcnt vmcnt(0)\ns_mov_b64 exec, 1\nbuffer_atomic_swap_x2 v[238:239], off, s[20:23], s61 sc1\ns_mov_b64 exec, -1\nLqw0:\nv_bfe_u32 v250, v252, 7, 1\n"
"v_mul_u32_u24 v250, 2176, v250\nv_bfe_u32 v251, v252, 4, 3\nv_lshl_add_u32 v250, v251, 3, v250\ns_lshl_b32 s60, s90, 6\nv_add_u32 v250, s60, v250\ns_sleep 3\n"
"v_mov_b32 v248, 0\nv_mov_b32 v249, 0\ns_add_u32 s61, s80, 88219648\ns_mov_b64 exec, 0xffff\nbuffer_atomic_or_x2 v[248:249], v250, s[20:23], s61 offen sc0 sc1\ns_mov_b64 exec, -1\n"
"s_add_u32 m0, s50, 65536\ns_add_u32 s63, s54, 458880\nbuffer_load_dwordx4 v220, s[12:15], s63 offen lds\nbuffer_load_dwordx4 v221, s[12:15], s63 offen offset:1024 lds\ns_add_u32 m0, s50, 73728\ns_add_u32 s63, s54, 688256\n"
"buffer_load_dwordx4 v220, s[12:15], s63 offen lds\nbuffer_load_dwordx4 v221, s[12:15], s63 offen offset:1024 lds\ns_add_u32 m0, s50, 81920\ns_add_u32 s63, s54, 917504\nbuffer_load_dwordx4 v220, s[12:15], s63 offen lds\nbuffer_load_dwordx4 v221, s[12:15], s63 offen offset:1024 lds\n"
"s_add_u32 m0, s50, 90112\ns_add_u32 s63, s54, 1146880\nbuffer_load_dwordx4 v220, s[12:15], s63 offen lds\nbuffer_load_dwordx4 v221, s[12:15], s63 offen offset:1024 lds\ns_add_u32 m0, s50, 98304\ns_add_u32 s63, s54, 917632\n"
"buffer_load_dwordx4 v220, s[12:15], s63 offen lds\nbuffer_load_dwordx4 v221, s[12:15], s63 offen offset:1024 lds\ns_waitcnt vmcnt(10)\nv_cmp_eq_u64 s[86:87], s[46:47], v[248:249]\ns_nop 1\ns_and_b32 s88, s86, 0xffff\n"
"s_cmp_eq_u32 s88, 0xffff\ns_cbranch_scc1 Lq265\nv_mov_b32 v246, 0\nv_mov_b32 v247, 0\ns_add_u32 s61, s80, 88219648\ns_mov_b64 exec, 0xffff\n"
"buffer_atomic_or_x2 v[246:247], v250, s[20:23], s61 offen sc0 sc1\ns_mov_b64 exec, -1\nLq265:\ns_add_u32 m0, s50, 106496\ns_add_u32 s63, s54, 1147008\nbuffer_load_dwordx4 v220, s[12:15], s63 offen lds\n"
"buffer_load_dwordx4 v221, s[12:15], s63 offen offset:1024 lds\ns_mov_b64 exec, 0\ns_cmp_eq_u32 s40, 1\ns_cbranch_scc1 Lpg17\ns_cmp_eq_u32 s40, 2\ns_cbranch_scc1 LretW0z\n"
"s_cmp_eq_u32 s40, 3\ns_cbranch_scc1 LretW0z\ns_branch Lpg16\nLretW0z:\ns_mov_b64 exec, -1\nv_mov_b32 v230, 0\n"
"v_mov_b32 v231, 0\nv_mov_b32 v232, 0\nv_mov_b32 v233, 0\nv_dot2c_f32_bf16 v230, s76, v192\nv_dot2c_f32_bf16 v231, v192, v192\nv_dot2c_f32_bf16 v232, s76, v200\n"
"v_dot2c_f32_bf16 v233, v200, v200\nv_dot2c_f32_bf16 v230, s76, v193\nv_dot2c_f32_bf16 v231, v193, v193\nv_dot2c_f32_bf16 v232, s76, v201\nv_dot2c_f32_bf16 v233, v201, v201\nv_dot2c_f32_bf16 v230, s76, v194\n"
"v_dot2c_f32_bf16 v231, v194, v194\nv_dot2c_f32_bf16 v232, s76, v202\nv_dot2c_f32_bf16 v233, v202, v202\nv_dot2c_f32_bf16 v230, s76, v195\nv_dot2c_f32_bf16 v231, v195, v195\nv_dot2c_f32_bf16 v232, s76, v203\n"
"v_dot2c_f32_bf16 v233, v203, v203\nv_dot2c_f32_bf16 v230, s76, v196\nv_dot2c_f32_bf16 v231, v196, v196\nv_dot2c_f32_bf16 v232, s76, v204\nv_dot2c_f32_bf16 v233, v204, v204\nv_dot2c_f32_bf16 v230, s76, v197\n"
"v_dot2c_f32_bf16 v231, v197, v197\nv_dot2c_f32_bf16 v232, s76, v205\nv_dot2c_f32_bf16 v233, v205, v205\nv_dot2c_f32_bf16 v230, s76, v198\nv_dot2c_f32_bf16 v231, v198, v198\nv_dot2c_f32_bf16 v232, s76, v206\n"
"v_dot2c_f32_bf16 v233, v206, v206\nv_dot2c_f32_bf16 v230, s76, v199\nv_dot2c_f32_bf16 v231, v199, v199\nv_dot2c_f32_bf16 v232, s76, v207\nv_dot2c_f32_bf16 v233, v207, v207\nv_lshrrev_b32 v234, 5, v252\n"
"v_and_b32 v234, 24, v234\nv_bfe_u32 v235, s46, v234, 8\nv_lshrrev_b32 v234, 1, v234\nv_and_b32 v236, 0xf0, v252\nv_add_u32 v234, v234, v236\nv_permlane32_swap_b32 v230, v231\n"
"v_permlane32_swap_b32 v232, v233\nv_add_f32 v230, v230, v231\nv_add_f32 v232, v232, v233\ns_nop 1\nv_permlane16_swap_b32 v230, v232\nv_add_f32 v230, v230, v232\n"
"s_movk_i32 s62, 0xff\nv_bfi_b32 v230, s62, v235, v230\ns_lshr_b32 s61, s49, 4\ns_lshl1_add_u32 s61, s53, s61\ns_add_u32 s61, s61, 82313216\nbuffer_store_dword v230, v234, s[20:23], s61 offen sc1\n"
"s_cmp_eq_u32 s88, 0xffff\ns_cbranch_scc1 Lq266\ns_waitcnt vmcnt(3)\nv_mov_b32 v248, v246\nv_mov_b32 v249, v247\nv_cmp_eq_u64 s[86:87], s[46:47], v[248:249]\n"
"s_nop 1\ns_and_b32 s62, s86, 0xffff\ns_cmp_eq_u32 s62, 0xffff\ns_cbranch_scc1 Lq257\nLq256:\nv_mov_b32 v248, 0\n"
"v_mov_b32 v249, 0\ns_add_u32 s61, s80, 88219648\ns_mov_b64 exec, 0xffff\nbuffer_atomic_or_x2 v[248:249], v250, s[20:23], s61 offen sc0 sc1\ns_mov_b64 exec, -1\ns_waitcnt vmcnt(0)\n"
"v_cmp_eq_u64 s[86:87], s[46:47], v[248:249]\ns_nop 1\ns_and_b32 s62, s86, 0xffff\ns_cmp_eq_u32 s62, 0xffff\ns_cbranch_scc1 Lq257\ns_sub_u32 s48, s48, 1\n"
"s_cmp_gt_i32 s48, 0\ns_cbranch_scc1 Lq256\nLq257:\nLq266:\ns_add_u32 s64, s70, 10485760\nbuffer_load_dwordx4 a[0:3], v252, s[20:23], s64 offen offset:0 sc1\n"
"s_add_u32 s65, s70, 10518528\nbuffer_load_dwordx4 a[4:7], v252, s[20:23], s65 offen offset:0 sc1\ns_add_u32 s66, s71, 10485760\nbuffer_load_dwordx4 a[8:11], v252, s[20:23], s66 offen offset:0 sc1\ns_add_u32 s67, s71, 10518528\nbuffer_load_dwordx4 a[12:15], v252, s[20:23], s67 offen offset:0 sc1\n"
"buffer_load_dwordx4 a[16:19], v252, s[20:23], s64 offen offset:1024 sc1\nbuffer_load_dwordx4 a[20:23], v252, s[20:23], s65 offen offset:1024 sc1\nbuffer_load_dwordx4 a[24:27], v252, s[20:23], s66 offen offset:1024 sc1\nbuffer_load_dwordx4 a[28:31], v252, s[20:23], s67 offen offset:1024 sc1\ns_barrier\nR 160,184,16384\n"
"R 162,184,20480\nR 164,184,24576\nR 166,184,28672\nR 168,185,16384\nR 170,185,20480\nR 172,185,24576\n"
"R 174,185,28672\nR 176,186,16384\nR 178,186,20480\nR 180,186,24576\nR 182,186,28672\nbuffer_load_dwordx4 a[32:35], v252, s[20:23], s64 offen offset:2048 sc1\n"
"buffer_load_dwordx4 a[36:39], v252, s[20:23], s65 offen offset:2048 sc1\nbuffer_load_dwordx4 a[40:43], v252, s[20:23], s66 offen offset:2048 sc1\nbuffer_load_dwordx4 a[44:47], v252, s[20:23], s67 offen offset:2048 sc1\nbuffer_load_dwordx4 a[48:51], v252, s[20:23], s64 offen offset:3072 sc1\nbuffer_load_dwordx4 a[52:55], v252, s[20:23], s65 offen offset:3072 sc1\nbuffer_load_dwordx4 a[56:59], v252, s[20:23], s66 offen offset:3072 sc1\n"
"buffer_load_dwordx4 a[60:63], v252, s[20:23], s67 offen offset:3072 sc1\ns_waitcnt vmcnt(15) lgkmcnt(10)\nv_mfma_f32_16x16x32_bf16 v[32:35], v[160:163], a[0:3], 0\ns_add_u32 s64, s70, 10489856\nbuffer_load_dwordx4 a[64:67], v252, s[20:23], s64 offen offset:0 sc1\ns_waitcnt vmcnt(12) lgkmcnt(8)\n"
"M 32,164,16\nv_mfma_f32_16x16x32_bf16 v[48:51], v[160:163], a[4:7], 0\ns_waitcnt vmcnt(11)\nM 48,164,20\nv_mfma_f32_16x16x32_bf16 v[64:67], v[160:163], a[8:11], 0\nv_lshrrev_b32 v238, 7, v252\n"
"s_waitcnt vmcnt(10)\nM 64,164,24\nv_lshlrev_b32 v238, 9, v238\nv_mfma_f32_16x16x32_bf16 v[80:83], v[160:163], a[12:15], 0\ns_add_u32 s65, s70, 10522624\nbuffer_load_dwordx4 a[68:71], v252, s[20:23], s65 offen offset:0 sc1\n"
"s_waitcnt vmcnt(10)\nM 80,164,28\nG 4\nv_and_b32 v239, 0x70, v252\nv_mfma_f32_16x16x32_bf16 v[84:87], v[168:171], a[12:15], 0\nR 160,187,16384\n"
"M 84,172,28\ns_and_b32 s60, s40, 3\ns_lshl_b32 s60, s60, 7\nv_add3_u32 v238, v238, v239, s60\nv_mfma_f32_16x16x32_bf16 v[68:71], v[168:171], a[8:11], 0\nR 162,187,20480\n"
"s_lshl_b32 s61, s42, 13\ns_add_u32 s61, s61, 84279296\nbuffer_load_dwordx4 v[0:3], v238, s[20:23], s61 offen sc1\nM 68,172,24\ns_lshl_b32 s61, s42, 13\ns_add_u32 s61, s61, 84283392\n"
"buffer_load_dwordx4 v[4:7], v238, s[20:23], s61 offen sc1\nv_mfma_f32_16x16x32_bf16 v[52:55], v[168:171], a[4:7], 0\nR 164,187,24576\nM 52,172,20\ns_add_u32 s66, s71, 10489856\nbuffer_load_dwordx4 a[72:75], v252, s[20:23], s66 offen offset:0 sc1\n"
"v_bfe_u32 v239, v238, 2, 7\nv_mfma_f32_16x16x32_bf16 v[36:39], v[168:171], a[0:3], 0\nR 166,187,28672\nM 36,172,16\nbuffer_load_dword v239, v239, s[32:35], s54 offen offset:0\nG 4\n"
"v_mfma_f32_16x16x32_bf16 v[40:43], v[176:179], a[0:3], 0\nR 168,184,32768\nM 40,180,16\nv_mfma_f32_16x16x32_bf16 v[56:59], v[176:179], a[4:7], 0\nR 170,184,36864\nM 56,180,20\n"
"s_add_u32 s67, s71, 10522624\nbuffer_load_dwordx4 a[76:79], v252, s[20:23], s67 offen offset:0 sc1\nv_mfma_f32_16x16x32_bf16 v[72:75], v[176:179], a[8:11], 0\nR 172,184,40960\nM 72,180,24\nv_mfma_f32_16x16x32_bf16 v[88:91], v[176:179], a[12:15], 0\n"
"R 174,184,45056\nM 88,180,28\nG 4\nv_mfma_f32_16x16x32_bf16 v[92:95], v[160:163], a[12:15], 0\nR 176,185,32768\nM 92,164,28\n"
"buffer_load_dwordx4 a[80:83], v252, s[20:23], s64 offen offset:1024 sc1\nv_mfma_f32_16x16x32_bf16 v[76:79], v[160:163], a[8:11], 0\nR 178,185,36864\nM 76,164,24\nv_mfma_f32_16x16x32_bf16 v[60:63], v[160:163], a[4:7], 0\nR 180,185,40960\n"
"M 60,164,20\nv_mfma_f32_16x16x32_bf16 v[44:47], v[160:163], a[0:3], 0\nR 182,185,45056\nM 44,164,16\nG 4\nv_mfma_f32_16x16x32_bf16 v[96:99], v[168:171], a[0:3], 0\n"
"R 160,186,32768\nM 96,172,16\nbuffer_load_dwordx4 a[84:87], v252, s[20:23], s65 offen offset:1024 sc1\nv_mfma_f32_16x16x32_bf16 v[112:115], v[168:171], a[4:7], 0\nR 162,186,36864\nM 112,172,20\n"
"v_mfma_f32_16x16x32_bf16 v[128:131], v[168:171], a[8:11], 0\nR 164,186,40960\nM 128,172,24\nbuffer_load_dwordx4 a[88:91], v252, s[20:23], s66 offen offset:1024 sc1\nv_mfma_f32_16x16x32_bf16 v[144:147], v[168:171], a[12:15], 0\nR 166,186,45056\n"
"M 144,172,28\ns_barrier\nG 4\nv_mfma_f32_16x16x32_bf16 v[148:151], v[176:179], a[12:15], 0\nR 168,187,32768\nM 148,180,28\n"
"v_mfma_f32_16x16x32_bf16 v[132:135], v[176:179], a[8:11], 0\nR 170,187,36864\nM 132,180,24\nbuffer_load_dwordx4 a[92:95], v252, s[20:23], s67 offen offset:1024 sc1\nv_mfma_f32_16x16x32_bf16 v[116:119], v[176:179], a[4:7], 0\nR 172,187,40960\n"
"M 116,180,20\nv_mfma_f32_16x16x32_bf16 v[100:103], v[176:179], a[0:3], 0\nR 174,187,45056\nM 100,180,16\nG 4\nv_mfma_f32_16x16x32_bf16 v[104:107], v[160:163], a[0:3], 0\n"
"R 176,184,49152\nv_lshrrev_b32 v242, 7, v252\nM 104,164,16\nbuffer_load_dwordx4 a[96:99], v252, s[20:23], s64 offen offset:2048 sc1\nv_mfma_f32_16x16x32_bf16 v[120:123], v[160:163], a[4:7], 0\nR 178,184,53248\n"
"v_lshlrev_b32 v242, 9, v242\ns_waitcnt vmcnt(8)\nv_min3_u32 v240, v1, v3, v5\nM 120,164,20\nv_and_b32 v243, 0x70, v252\nv_min3_u32 v240, v240, v7, v7\n"
"v_max3_u32 v241, v1, v3, v5\nv_mfma_f32_16x16x32_bf16 v[136:139], v[160:163], a[8:11], 0\nR 180,184,57344\nv_max3_u32 v241, v241, v7, v7\nv_cmp_ne_u32 s[86:87], s46, v240\nM 136,164,24\n"
"s_and_b32 s60, s40, 3\ns_lshl_b32 s60, s60, 7\nv_add3_u32 v242, v242, v243, s60\nv_cmp_ne_u32 s[88:89], s46, v241\nv_mfma_f32_16x16x32_bf16 v[152:155], v[160:163], a[12:15], 0\nR 182,184,61440\n"
"s_lshl_b32 s61, s42, 13\ns_add_u32 s61, s61, 84410368\nbuffer_load_dwordx4 v[16:19], v242, s[20:23], s61 offen sc1\ns_or_b64 s[86:87], s[86:87], s[88:89]\ns_cbranch_scc1 Lq319\nLq320:\n"
"M 152,164,28\ns_lshl_b32 s61, s42, 13\ns_add_u32 s61, s61, 84414464\nbuffer_load_dwordx4 v[20:23], v242, s[20:23], s61 offen sc1\nG 4\nv_mfma_f32_16x16x32_bf16 v[156:159], v[168:171], a[12:15], 0\n"
"R 160,185,49152\nv_add_f32 v0, v0, v4\nM 156,172,28\nbuffer_load_dwordx4 a[100:103], v252, s[20:23], s65 offen offset:2048 sc1\nv_bfe_u32 v243, v242, 2, 7\nv_mfma_f32_16x16x32_bf16 v[140:143], v[168:171], a[8:11], 0\n"
"R 162,185,53248\nv_add_f32 v2, v2, v6\nM 140,172,24\nbuffer_load_dword v243, v243, s[32:35], s54 offen offset:128\nv_mfma_f32_16x16x32_bf16 v[124:127], v[168:171], a[4:7], 0\nR 164,185,57344\n"
"v_add_f32_dpp v0, v0, v0 row_ror:8 row_mask:0xf bank_mask:0xf\nM 124,172,20\nbuffer_load_dwordx4 a[104:107], v252, s[20:23], s66 offen offset:2048 sc1\nv_mfma_f32_16x16x32_bf16 v[108:111], v[168:171], a[0:3], 0\nR 166,185,61440\nv_add_f32_dpp v2, v2, v2 row_ror:8 row_mask:0xf bank_mask:0xf\n"
"M 108,172,16\nG 4\nv_mov_b32 v4, v0\nv_mov_b32 v5, v2\nM 32,176,32\nR 168,186,49152\n"
"v_permlane32_swap_b32 v4, v0\nM 32,180,48\nv_permlane32_swap_b32 v5, v2\nv_add_f32 v0, v0, v4\nM 48,176,36\nR 170,186,53248\n"
"v_add_f32 v2, v2, v5\nM 48,180,52\nbuffer_load_dwordx4 a[108:111], v252, s[20:23], s67 offen offset:2048 sc1\nM 64,176,40\nR 172,186,57344\nv_mov_b32 v4, v0\n"
"M 64,180,56\nv_mov_b32 v5, v2\nv_permlane16_swap_b32 v4, v0\nM 80,176,44\nR 174,186,61440\nv_permlane16_swap_b32 v5, v2\n"
"M 80,180,60\ns_barrier\nG 4\nM 84,160,44\nR 176,187,49152\nv_add_f32 v0, v0, v4\n"
"M 84,164,60\nbuffer_load_dwordx4 a[112:115], v252, s[20:23], s64 offen offset:3072 sc1\nM 68,160,40\nR 178,187,53248\nv_add_f32 v2, v2, v5\nM 68,164,56\n"
"v_and_b32 v8, s77, v0\nv_sub_f32 v9, v0, v8\nM 52,160,36\nR 180,187,57344\ns_waitcnt vmcnt(13)\nv_lshlrev_b32 v10, 16, v239\n"
"M 52,164,52\nv_cvt_pk_bf16_f32 v4, v8, v9\nv_cvt_pk_bf16_f32 v5, v8, v10\nM 36,160,32\nR 182,187,61440\nv_and_b32 v8, s77, v2\n"
"M 36,164,48\nG 4\nv_sub_f32 v9, v2, v8\nv_and_b32 v10, s77, v239\nM 40,168,32\nR 160,188,0\n"
"v_cvt_pk_bf16_f32 v6, v8, v9\nM 40,172,48\nbuffer_load_dwordx4 a[116:119], v252, s[20:23], s65 offen offset:3072 sc1\nM 56,168,36\nR 162,188,4096\nv_cvt_pk_bf16_f32 v7, v8, v10\n"
"M 56,172,52\nv_and_b32 v238, 0x1ff, v238\nv_add_u32 v238, 157184, v238\nM 72,168,40\nR 164,188,8192\nds_write_b128 v238, v[4:7]\n"
"M 72,172,56\nbuffer_load_dwordx4 a[120:123], v252, s[20:23], s66 offen offset:3072 sc1\nM 88,168,44\nR 166,188,12288\nM 88,172,60\nG 5\n"
"M 92,176,44\nR 168,189,0\nM 92,180,60\nM 76,176,40\nR 170,189,4096\nM 76,180,56\n"
"buffer_load_dwordx4 a[124:127], v252, s[20:23], s67 offen offset:3072 sc1\nM 60,176,36\nR 172,189,8192\nv_lshrrev_b32 v238, 7, v252\nM 60,180,52\nv_lshlrev_b32 v238, 9, v238\n"
"M 44,176,32\nR 174,189,12288\ns_waitcnt vmcnt(8)\nv_min3_u32 v244, v17, v19, v21\nv_min3_u32 v244, v244, v23, v23\nM 44,180,48\n"
"G 4\nv_and_b32 v239, 0x70, v252\nv_max3_u32 v245, v17, v19, v21\nv_max3_u32 v245, v245, v23, v23\nM 96,160,32\nR 176,190,0\n"
"s_and_b32 s60, s40, 3\ns_lshl_b32 s60, s60, 7\nv_add3_u32 v238, v238, v239, s60\nv_cmp_ne_u32 s[86:87], s46, v244\nM 96,164,48\ns_add_u32 s64, s70, 10493952\n"
"buffer_load_dwordx4 a[128:131], v252, s[20:23], s64 offen offset:0 sc1\nv_cmp_ne_u32 s[88:89], s46, v245\nM 112,160,36\nR 178,190,4096\ns_lshl_b32 s61, s42, 13\ns_add_u32 s61, s61, 84541440\n"
"buffer_load_dwordx4 v[0:3], v238, s[20:23], s61 offen sc1\ns_or_b64 s[86:87], s[86:87], s[88:89]\ns_cbranch_scc1 Lq329\nLq330:\nM 112,164,52\ns_lshl_b32 s61, s42, 13\n"
"s_add_u32 s61, s61, 84545536\nbuffer_load_dwordx4 v[4:7], v238, s[20:23], s61 offen sc1\nv_add_f32 v16, v16, v20\nM 128,160,40\nR 180,190,8192\nv_add_f32 v18, v18, v22\n"
"v_add_f32_dpp v16, v16, v16 row_ror:8 row_mask:0xf bank_mask:0xf\nM 128,164,56\ns_add_u32 m0, s50, 114688\nv_bfe_u32 v239, v238, 2, 7\nv_add_f32_dpp v18, v18, v18 row_ror:8 row_mask:0xf bank_mask:0xf\nv_mov_b32 v20, v16\n"
"M 144,160,44\nR 182,190,12288\ns_add_u32 s63, s54, 1376256\nv_mov_b32 v21, v18\nM 144,164,60\nbuffer_load_dword v239, v239, s[32:35], s54 offen offset:256\n"
"s_barrier\nG 4\nv_permlane32_swap_b32 v20, v16\nM 148,168,44\nR 160,191,0\nv_permlane32_swap_b32 v21, v18\n"
"v_add_f32 v16, v16, v20\nM 148,172,60\ns_add_u32 s65, s70, 10526720\nbuffer_load_dwordx4 a[132:135], v252, s[20:23], s65 offen offset:0 sc1\nv_add_f32 v18, v18, v21\nM 132,168,40\n"
"R 162,191,4096\nv_mov_b32 v20, v16\nv_mov_b32 v21, v18\nM 132,172,56\nbuffer_load_dwordx4 v220, s[12:15], s63 offen lds\nM 116,168,36\n"
"R 164,191,8192\nv_permlane16_swap_b32 v20, v16\nM 116,172,52\ns_add_u32 s66, s71, 10493952\nbuffer_load_dwordx4 a[136:139], v252, s[20:23], s66 offen offset:0 sc1\nM 100,168,32\n"
"R 166,191,12288\nv_permlane16_swap_b32 v21, v18\nM 100,172,48\nG 4\nv_add_f32 v16, v16, v20\nv_add_f32 v18, v18, v21\n"
"M 104,176,32\nR 168,188,16384\nbuffer_load_dwordx4 v221, s[12:15], s63 offen offset:1024 lds\nM 104,180,48\ns_add_u32 m0, s50, 0\ns_add_u32 s63, s54, 1605632\n"
"buffer_load_dwordx4 v220, s[12:15], s63 offen lds\nM 120,176,36\nR 170,188,20480\nv_and_b32 v24, s77, v16\nM 120,180,52\ns_add_u32 s67, s71, 10526720\n"
"buffer_load_dwordx4 a[140:143], v252, s[20:23], s67 offen offset:0 sc1\nM 136,176,40\nR 172,188,24576\nv_sub_f32 v25, v16, v24\nM 136,180,56\nbuffer_load_dwordx4 v221, s[12:15], s63 offen offset:1024 lds\n"
"M 152,176,44\nR 174,188,28672\ns_waitcnt vmcnt(17)\nv_lshlrev_b32 v26, 16, v243\nM 152,180,60\nG 4\n"
"v_cvt_pk_bf16_f32 v20, v24, v25\nv_cvt_pk_bf16_f32 v21, v24, v26\nM 156,160,44\nR 176,189,16384\nv_and_b32 v24, s77, v18\nM 156,164,60\n"
"buffer_load_dwordx4 a[144:147], v252, s[20:23], s64 offen offset:1024 sc1\nM 140,160,40\nR 178,189,20480\nv_sub_f32 v25, v18, v24\nM 140,164,56\nv_and_b32 v26, s77, v243\n"
"v_cvt_pk_bf16_f32 v22, v24, v25\nM 124,160,36\nR 180,189,24576\nv_cvt_pk_bf16_f32 v23, v24, v26\nM 124,164,52\nv_and_b32 v242, 0x1ff, v242\n"
"v_add_u32 v242, 157696, v242\nM 108,160,32\nR 182,189,28672\nds_write_b128 v242, v[20:23]\nM 108,164,48\nG 5\n"
"M 32,168,64\nR 160,190,16384\nM 32,172,80\nbuffer_load_dwordx4 a[148:151], v252, s[20:23], s65 offen offset:1024 sc1\nLpg8:\ns_cbranch_execz Lpg12\n"
"M 48,168,68\nR 162,190,20480\nM 48,172,84\ns_add_u32 m0, s50, 8192\nM 64,168,72\nR 164,190,24576\n"
"s_add_u32 s63, s54, 1376384\nM 64,172,88\nbuffer_load_dwordx4 a[152:155], v252, s[20:23], s66 offen offset:1024 sc1\nM 80,168,76\nR 166,190,28672\nM 80,172,92\n"
"G 4\ns_barrier\nM 84,176,76\nR 168,191,16384\nv_lshrrev_b32 v242, 7, v252\nM 84,180,92\n"
"buffer_load_dwordx4 v220, s[12:15], s63 offen lds\nv_lshlrev_b32 v242, 9, v242\nM 68,176,72\nR 170,191,20480\ns_waitcnt vmcnt(12)\nv_min3_u32 v240, v1, v3, v5\n"
"v_min3_u32 v240, v240, v7, v7\nM 68,180,88\nbuffer_load_dwordx4 a[156:159], v252, s[20:23], s67 offen offset:1024 sc1\nv_and_b32 v243, 0x70, v252\nv_max3_u32 v241, v1, v3, v5\nM 52,176,68\n"
"R 172,191,24576\nv_max3_u32 v241, v241, v7, v7\nv_cmp_ne_u32 s[86:87], s46, v240\nM 52,180,84\ns_and_b32 s60, s40, 1\ns_lshl_b32 s60, s60, 7\n"
"v_add3_u32 v242, v242, v243, s60\nv_cmp_ne_u32 s[88:89], s46, v241\nM 36,176,64\nR 174,191,28672\ns_lshl_b32 s61, s42, 13\ns_add_u32 s61, s61, 84672512\n"
"buffer_load_dwordx4 v[16:19], v242, s[20:23], s61 offen sc1\ns_or_b64 s[86:87], s[86:87], s[88:89]\ns_cbranch_scc1 Lq339\nLq340:\nM 36,180,80\ns_lshl_b32 s61, s42, 13\n"
"s_add_u32 s61, s61, 84676608\nbuffer_load_dwordx4 v[20:23], v242, s[20:23], s61 offen sc1\nG 4\nv_add_f32 v0, v0, v4\nM 40,160,64\nR 176,188,32768\n"
"buffer_load_dwordx4 v221, s[12:15], s63 offen offset:1024 lds\nv_add_f32 v2, v2, v6\nM 40,164,80\nbuffer_load_dwordx4 a[160:163], v252, s[20:23], s64 offen offset:2048 sc1\ns_add_u32 s63, s54, 1605760\nv_bfe_u32 v243, v242, 2, 7\n"
"v_add_f32_dpp v0, v0, v0 row_ror:8 row_mask:0xf bank_mask:0xf\nM 56,160,68\nR 178,188,36864\ns_add_u32 m0, s50, 16384\nv_add_f32_dpp v2, v2, v2 row_ror:8 row_mask:0xf bank_mask:0xf\nM 56,164,84\n"
"buffer_load_dword v243, v243, s[32:35], s55 offen offset:0\nv_mov_b32 v4, v0\nM 72,160,72\nR 180,188,40960\nbuffer_load_dwordx4 v220, s[12:15], s63 offen lds\nv_mov_b32 v5, v2\n"
"M 72,164,88\nbuffer_load_dwordx4 v221, s[12:15], s63 offen offset:1024 lds\nv_permlane32_swap_b32 v4, v0\nM 88,160,76\nR 182,188,45056\nv_permlane32_swap_b32 v5, v2\n"
"v_add_f32 v0, v0, v4\nM 88,164,92\nG 4\nv_add_f32 v2, v2, v5\nv_mov_b32 v4, v0\nv_mov_b32 v5, v2\n"
"M 92,168,76\nR 160,189,32768\nv_permlane16_swap_b32 v4, v0\nv_permlane16_swap_b32 v5, v2\nM 92,172,92\nbuffer_load_dwordx4 a[164:167], v252, s[20:23], s65 offen offset:2048 sc1\n"
"v_add_f32 v0, v0, v4\nM 76,168,72\nR 162,189,36864\nv_add_f32 v2, v2, v5\nv_and_b32 v8, s77, v0\nM 76,172,88\n"
"v_sub_f32 v9, v0, v8\ns_waitcnt vmcnt(20)\nv_lshlrev_b32 v10, 16, v239\nM 60,168,68\nR 164,189,40960\nv_cvt_pk_bf16_f32 v4, v8, v9\n"
"M 60,172,84\nbuffer_load_dwordx4 a[168:171], v252, s[20:23], s66 offen offset:2048 sc1\nM 44,168,64\nR 166,189,45056\nv_cvt_pk_bf16_f32 v5, v8, v10\nM 44,172,80\n"
"G 4\nv_and_b32 v8, s77, v2\nv_sub_f32 v9, v2, v8\nM 96,176,64\nR 168,190,32768\nv_and_b32 v10, s77, v239\n"
"M 96,180,80\nv_cvt_pk_bf16_f32 v6, v8, v9\nv_cvt_pk_bf16_f32 v7, v8, v10\nM 112,176,68\nR 170,190,36864\nv_and_b32 v238, 0x1ff, v238\n"
"M 112,180,84\nbuffer_load_dwordx4 a[172:175], v252, s[20:23], s67 offen offset:2048 sc1\nM 128,176,72\nR 172,190,40960\nv_add_u32 v238, 158208, v238\nM 128,180,88\n"
"s_add_u32 m0, s50, 24576\nds_write_b128 v238, v[4:7]\nM 144,176,76\nR 174,190,45056\ns_add_u32 s63, s54, 1835008\nM 144,180,92\n"
"s_waitcnt vmcnt(15) lgkmcnt(1)\ns_barrier\nM 148,160,76\nR 176,191,32768\nM 148,164,92\nbuffer_load_dwordx4 a[176:179], v252, s[20:23], s64 offen offset:3072 sc1\n"
"M 132,160,72\nR 178,191,36864\nM 132,164,88\nbuffer_load_dwordx4 v220, s[12:15], s63 offen lds\nM 116,160,68\nR 180,191,40960\n"
"M 116,164,84\nbuffer_load_dwordx4 v221, s[12:15], s63 offen offset:1024 lds\nM 100,160,64\nR 182,191,45056\ns_add_u32 m0, s50, 32768\nM 100,164,80\n"
"G 4\nM 104,168,64\nR 160,188,49152\ns_add_u32 s63, s54, 2064384\nM 104,172,80\nbuffer_load_dwordx4 a[180:183], v252, s[20:23], s65 offen offset:3072 sc1\n"
"M 120,168,68\nR 162,188,53248\nM 120,172,84\nbuffer_load_dwordx4 v220, s[12:15], s63 offen lds\nM 136,168,72\nR 164,184,0\n"
"M 136,172,88\nbuffer_load_dwordx4 a[184:187], v252, s[20:23], s66 offen offset:3072 sc1\nM 152,168,76\nR 166,184,4096\ns_waitcnt vmcnt(14)\nv_min3_u32 v244, v17, v19, v21\n"
"v_min3_u32 v244, v244, v23, v23\nM 152,172,92\nG 4\nv_max3_u32 v245, v17, v19, v21\nv_max3_u32 v245, v245, v23, v23\nv_cmp_ne_u32 s[86:87], s46, v244\n"
"M 156,176,76\nR 168,189,49152\nbuffer_load_dwordx4 v221, s[12:15], s63 offen offset:1024 lds\nv_cmp_ne_u32 s[88:89], s46, v245\nM 156,180,92\ns_or_b64 s[86:87], s[86:87], s[88:89]\n"
"s_cbranch_scc1 Lq349\nLq350:\nv_add_f32 v16, v16, v20\nv_add_f32 v18, v18, v22\nM 140,176,72\nR 170,189,53248\n"
"v_add_f32_dpp v16, v16, v16 row_ror:8 row_mask:0xf bank_mask:0xf\nv_add_f32_dpp v18, v18, v18 row_ror:8 row_mask:0xf bank_mask:0xf\nM 140,180,88\nbuffer_load_dwordx4 a[188:191], v252, s[20:23], s67 offen offset:3072 sc1\nv_mov_b32 v20, v16\nM 124,176,68\n"
"R 172,185,0\nv_mov_b32 v21, v18\nM 124,180,84\nv_permlane32_swap_b32 v20, v16\nv_permlane32_swap_b32 v21, v18\nM 108,176,64\n"
"R 174,185,4096\nv_add_f32 v16, v16, v20\nM 108,180,80\nG 4\nv_add_f32 v18, v18, v21\nv_mov_b32 v20, v16\n"
"M 32,160,96\nR 176,190,49152\nv_mov_b32 v21, v18\nM 32,164,112\ns_add_u32 s64, s70, 10498048\nbuffer_load_dwordx4 a[192:195], v252, s[20:23], s64 offen offset:0 sc1\n"
"M 48,160,100\nR 178,190,53248\nv_permlane16_swap_b32 v20, v16\nM 48,164,116\nv_permlane16_swap_b32 v21, v18\nv_add_f32 v16, v16, v20\n"
"M 64,160,104\nR 180,186,0\nv_add_f32 v18, v18, v21\nM 64,164,120\ns_add_u32 m0, s50, 40960\nv_and_b32 v24, s77, v16\n"
"v_sub_f32 v25, v16, v24\nM 80,160,108\nR 182,186,4096\ns_add_u32 s63, s54, 2293760\nM 80,164,124\ns_waitcnt vmcnt(12)\n"
"s_barrier\nG 4\nM 84,168,108\nR 160,191,49152\nv_lshlrev_b32 v26, 16, v243\nM 84,172,124\n"
"s_add_u32 s65, s70, 10530816\nbuffer_load_dwordx4 a[196:199], v252, s[20:23], s65 offen offset:0 sc1\nM 68,168,104\nR 162,191,53248\nv_cvt_pk_bf16_f32 v20, v24, v25\nM 68,172,120\n"
"buffer_load_dwordx4 v220, s[12:15], s63 offen lds\nM 52,168,100\nR 164,187,0\nv_cvt_pk_bf16_f32 v21, v24, v26\nM 52,172,116\ns_add_u32 s66, s71, 10498048\n"
"buffer_load_dwordx4 a[200:203], v252, s[20:23], s66 offen offset:0 sc1\nM 36,168,96\nR 166,187,4096\nv_and_b32 v24, s77, v18\nM 36,172,112\nG 4\n"
"v_sub_f32 v25, v18, v24\nv_and_b32 v26, s77, v243\nM 40,176,96\nR 168,184,8192\nbuffer_load_dwordx4 v221, s[12:15], s63 offen offset:1024 lds\nM 40,180,112\n"
"s_add_u32 m0, s50, 49152\ns_add_u32 s63, s54, 2523136\nbuffer_load_dwordx4 v220, s[12:15], s63 offen lds\nM 56,176,100\nR 170,184,12288\nv_cvt_pk_bf16_f32 v22, v24, v25\n"
"M 56,180,116\ns_add_u32 s67, s71, 10530816\nbuffer_load_dwordx4 a[204:207], v252, s[20:23], s67 offen offset:0 sc1\nM 72,176,104\nR 172,184,16384\nv_cvt_pk_bf16_f32 v23, v24, v26\n"
"M 72,180,120\nbuffer_load_dwordx4 v221, s[12:15], s63 offen offset:1024 lds\nM 88,176,108\nR 174,184,20480\nv_and_b32 v242, 0x1ff, v242\nM 88,180,124\n"
"v_add_u32 v242, 158720, v242\nds_write_b128 v242, v[20:23]\nG 5\nM 92,160,108\nR 176,185,8192\nM 92,164,124\n"
"buffer_load_dwordx4 a[208:211], v252, s[20:23], s64 offen offset:1024 sc1\nM 76,160,104\nR 178,185,12288\nM 76,164,120\ns_lshr_b32 s84, s49, 4\nM 60,160,100\n"
"R 180,185,16384\nLq573:\ns_lshr_b32 s60, s45, 3\ns_lshl2_add_u32 s60, s60, s40\ns_lshl_b32 s60, s60, 8\nv_lshl_add_u32 v224, v252, 3, s60\n"
"v_add_u32 v224, Lq573+4096-LqPC, v224\nv_min_u32 v224, LqE-LqPC-4, v224\ns_mov_b64 exec, 3\nglobal_load_dword v224, v224, s[98:99]\ns_mov_b64 exec, -1\nM 60,164,116\n"
"s_add_u32 s84, s84, 82313216\nM 44,160,96\nR 182,185,20480\nM 44,164,112\nG 5\nM 96,168,96\n"
"R 160,186,8192\nM 96,172,112\nbuffer_load_dwordx4 a[212:215], v252, s[20:23], s65 offen offset:1024 sc1\nM 112,168,100\nR 162,186,12288\nM 112,172,116\n"
"buffer_load_dwordx4 v[0:3], v252, s[20:23], s84 offen offset:0 sc1\nM 128,168,104\nR 164,186,16384\nM 128,172,120\nbuffer_load_dwordx4 a[216:219], v252, s[20:23], s66 offen offset:1024 sc1\nM 144,168,108\n"
"R 166,186,20480\ns_add_u32 m0, s50, 57344\nM 144,172,124\nbuffer_load_dwordx4 v[4:7], v252, s[20:23], s84 offen offset:1024 sc1\ns_waitcnt vmcnt(15) lgkmcnt(8)\ns_barrier\n"
"G 4\nM 148,176,108\nR 168,187,8192\ns_add_u32 s63, s54, 2752512\nM 148,180,124\nbuffer_load_dwordx4 v[8:11], v252, s[20:23], s84 offen offset:2048 sc1\n"
"M 132,176,104\nR 170,187,12288\nM 132,180,120\nbuffer_load_dwordx4 a[220:223], v252, s[20:23], s67 offen offset:1024 sc1\nM 116,176,100\nR 172,187,16384\n"
"buffer_load_dwordx4 v220, s[12:15], s63 offen lds\nM 116,180,116\nbuffer_load_dwordx4 v[12:15], v252, s[20:23], s84 offen offset:3072 sc1\nM 100,176,96\nR 174,187,20480\nM 100,180,112\n"
"G 4\nM 104,160,96\nR 176,184,24576\nbuffer_load_dwordx4 v221, s[12:15], s63 offen offset:1024 lds\nM 104,164,112\nbuffer_load_dwordx4 a[224:227], v252, s[20:23], s64 offen offset:2048 sc1\n"
"s_add_u32 s63, s54, 2981888\nM 120,160,100\nR 178,184,28672\ns_add_u32 m0, s50, 65536\nM 120,164,116\nbuffer_load_dwordx4 v220, s[12:15], s63 offen lds\n"
"M 136,160,104\nR 180,184,32768\nM 136,164,120\nbuffer_load_dwordx4 v221, s[12:15], s63 offen offset:1024 lds\nM 152,160,108\nR 182,184,36864\n"
"M 152,164,124\nG 4\nM 156,168,108\nR 160,185,24576\nM 156,172,124\nbuffer_load_dwordx4 a[228:231], v252, s[20:23], s65 offen offset:2048 sc1\n"
"M 140,168,104\nR 162,185,28672\nM 140,172,120\nM 124,168,100\nR 164,185,32768\nM 124,172,116\n"
"buffer_load_dwordx4 a[232:235], v252, s[20:23], s66 offen offset:2048 sc1\nM 108,168,96\nR 166,185,36864\nM 108,172,112\nG 4\nM 32,176,128\n"
"R 168,186,24576\nM 32,180,144\nM 48,176,132\nR 170,186,28672\nM 48,180,148\nbuffer_load_dwordx4 a[236:239], v252, s[20:23], s67 offen offset:2048 sc1\n"
"M 64,176,136\nR 172,186,32768\nM 64,180,152\ns_add_u32 m0, s50, 73728\nM 80,176,140\nR 174,186,36864\n"
"s_add_u32 s63, s54, 3211264\nM 80,180,156\ns_waitcnt vmcnt(17)\ns_barrier\nG 4\nM 84,160,140\n"
"R 176,187,24576\nM 84,164,156\nbuffer_load_dwordx4 a[240:243], v252, s[20:23], s64 offen offset:3072 sc1\nM 68,160,136\nR 178,187,28672\nM 68,164,152\n"
"buffer_load_dwordx4 v220, s[12:15], s63 offen lds\nM 52,160,132\nR 180,187,32768\nM 52,164,148\nbuffer_load_dwordx4 v221, s[12:15], s63 offen offset:1024 lds\nM 36,160,128\n"
"R 182,187,36864\ns_add_u32 m0, s50, 81920\nM 36,164,144\nG 4\nM 40,168,128\nR 160,184,40960\n"
"s_add_u32 s63, s54, 3440640\nM 40,172,144\nbuffer_load_dwordx4 a[244:247], v252, s[20:23], s65 offen offset:3072 sc1\nM 56,168,132\nR 162,184,45056\nM 56,172,148\n"
"buffer_load_dwordx4 v220, s[12:15], s63 offen lds\nM 72,168,136\nR 164,184,49152\nM 72,172,152\nbuffer_load_dwordx4 a[248:251], v252, s[20:23], s66 offen offset:3072 sc1\nM 88,168,140\n"
"R 166,184,53248\ns_mov_b32 s85, 0x0c0c0400\ns_waitcnt vmcnt(19)\nv_perm_b32 v16, v1, v0, s85\nM 88,172,156\nG 4\n"
"v_perm_b32 v20, v3, v2, s85\nv_lshl_or_b32 v16, v20, 16, v16\ns_waitcnt vmcnt(17)\nv_perm_b32 v17, v5, v4, s85\nM 92,176,140\nR 168,185,40960\n"
"buffer_load_dwordx4 v221, s[12:15], s63 offen offset:1024 lds\nv_perm_b32 v21, v7, v6, s85\nM 92,180,156\nv_lshl_or_b32 v17, v21, 16, v17\ns_waitcnt vmcnt(17)\nv_perm_b32 v18, v9, v8, s85\n"
"v_perm_b32 v22, v11, v10, s85\nM 76,176,136\nR 170,185,45056\nv_lshl_or_b32 v18, v22, 16, v18\ns_waitcnt vmcnt(14)\nv_perm_b32 v19, v13, v12, s85\n"
"M 76,180,152\nbuffer_load_dwordx4 a[252:255], v252, s[20:23], s67 offen offset:3072 sc1\nv_perm_b32 v23, v15, v14, s85\nM 60,176,132\nR 172,185,49152\nv_lshl_or_b32 v19, v23, 16, v19\n"
"v_min3_u32 v230, v16, v17, v18\nM 60,180,148\nv_min3_u32 v230, v230, v19, v19\nv_max3_u32 v231, v16, v17, v18\nv_max3_u32 v231, v231, v19, v19\nM 44,176,128\n"
"R 174,185,53248\nv_cmp_ne_u32 s[86:87], s46, v230\nv_cmp_ne_u32 s[88:89], s46, v231\nM 44,180,144\ns_or_b64 s[86:87], s[86:87], s[88:89]\ns_cbranch_scc1 Lq317\n"
"Lq318:\nG 4\nv_and_b32 v0, 0xffffff00, v0\nM 32,160,160\nR 176,186,40960\nv_and_b32 v1, 0xffffff00, v1\n"
"v_and_b32 v2, 0xffffff00, v2\nM 32,164,176\nv_and_b32 v3, 0xffffff00, v3\nv_and_b32 v4, 0xffffff00, v4\nv_and_b32 v5, 0xffffff00, v5\nM 48,160,164\n"
"R 178,186,45056\nv_and_b32 v6, 0xffffff00, v6\nv_and_b32 v7, 0xffffff00, v7\nM 48,164,180\ns_add_u32 m0, s50, 90112\nv_and_b32 v8, 0xffffff00, v8\n"
"v_and_b32 v9, 0xffffff00, v9\nv_and_b32 v10, 0xffffff00, v10\nM 64,160,168\nR 180,186,49152\nv_and_b32 v11, 0xffffff00, v11\nv_and_b32 v12, 0xffffff00, v12\n"
"M 64,164,184\ns_add_u32 s63, s54, 1835136\nv_and_b32 v13, 0xffffff00, v13\nv_and_b32 v14, 0xffffff00, v14\nv_and_b32 v15, 0xffffff00, v15\nM 80,160,172\n"
"R 182,186,53248\nv_add_f32 v0, v0, v8\nv_add_f32 v1, v1, v9\nM 80,164,188\ns_waitcnt vmcnt(11)\ns_barrier\n"
"G 4\nv_add_f32 v2, v2, v10\nM 84,168,172\nR 160,187,40960\nv_add_f32 v3, v3, v11\nv_add_f32 v4, v4, v12\n"
"M 84,172,188\nbuffer_load_dwordx4 v220, s[12:15], s63 offen lds\nv_add_f32 v5, v5, v13\nM 68,168,168\nR 162,187,45056\nv_add_f32 v6, v6, v14\n"
"v_add_f32 v7, v7, v15\nM 68,172,184\nv_add_f32 v0, v0, v4\nv_add_f32 v1, v1, v5\nv_add_f32 v2, v2, v6\nM 52,168,164\n"
"R 164,187,49152\nv_bfe_u32 v250, v252, 5, 1\nv_add_f32 v3, v3, v7\nv_mul_u32_u24 v250, 2560, v250\nM 52,172,180\nbuffer_load_dwordx4 v221, s[12:15], s63 offen offset:1024 lds\n"
"v_permlane32_swap_b32 v0, v2\nv_and_b32 v251, 16, v252\nM 36,168,160\nR 166,187,53248\ns_add_u32 m0, s50, 98304\nv_permlane32_swap_b32 v1, v3\n"
"v_lshl_add_u32 v250, v251, 1, v250\nM 36,172,176\ns_lshl_b32 s60, s90, 6\nv_add_u32 v250, s60, v250\nG 4\nv_add_f32 v0, v0, v2\n"
"M 40,176,160\nR 168,184,57344\ns_add_u32 s63, s54, 2064512\nv_add_f32 v1, v1, v3\nM 40,180,176\nbuffer_load_dwordx4 v220, s[12:15], s63 offen lds\n"
"v_permlane16_swap_b32 v0, v1\nM 56,176,164\nR 170,184,61440\nv_add_f32 v230, v0, v1\nv_mov_b32 v232, v230\nM 56,180,180\n"
"s_nop 0\nv_permlane16_swap_b32 v230, v232\nv_mov_b32 v231, v230\nv_mov_b32 v233, v232\nM 72,176,168\nR 172,188,0\n"
"v_permlane32_swap_b32 v230, v231\nv_permlane32_swap_b32 v232, v233\nM 72,180,184\nbuffer_load_dwordx4 v221, s[12:15], s63 offen offset:1024 lds\nv_mul_f32 v230, 0xba800000, v230\nM 88,176,172\n"
"R 174,188,4096\ns_add_u32 m0, s50, 106496\nM 88,180,188\nG 4\nv_mul_f32 v214, v230, v230\nv_mul_f32 v231, 0x3a800000, v231\n"
"M 92,160,172\nR 176,185,57344\ns_add_u32 s63, s54, 2293888\nM 92,164,188\nbuffer_load_dwordx4 v220, s[12:15], s63 offen lds\nM 76,160,168\n"
"R 178,185,61440\nv_sub_f32 v231, v231, v214\nM 76,164,184\nv_max_f32 v231, 0, v231\nLpg9:\ns_cbranch_execz Lpg13\n"
"v_add_f32 v231, 0x3727c5ac, v231\nM 60,160,164\nR 180,189,0\nv_and_b32 v208, s77, v230\nM 60,164,180\nv_rsq_f32 v234, v231\n"
"M 44,160,160\nR 182,189,4096\nv_mul_f32 v216, v231, v234\nM 44,164,176\nG 4\nv_sub_f32 v230, v230, v208\n"
"v_cvt_pk_bf16_f32 v209, v230, v216\nM 32,168,192\nR 160,186,57344\nv_cvt_pk_bf16_f32 v208, v208, v208\nM 32,172,208\nbuffer_load_dwordx4 v221, s[12:15], s63 offen offset:1024 lds\n"
"M 48,168,196\nR 162,186,61440\ns_add_u32 s63, s54, 2523264\nM 48,172,212\ns_add_u32 m0, s50, 114688\ns_mov_b64 s[60:61], 0xffff\n"
"v_cndmask_b32 v208, 0, v208, s[60:61]\nv_mul_f32 v214, 0xc0135761, v234\nM 64,168,200\nR 164,190,0\nv_mul_f32 v215, v234, v234\nM 64,172,216\n"
"buffer_load_dwordx4 v220, s[12:15], s63 offen lds\nM 80,168,204\nR 166,190,4096\nv_mul_f32 v215, v215, v214\nM 80,172,220\ns_waitcnt vmcnt(8)\n"
"s_barrier\nG 4\nM 84,176,204\nR 168,187,57344\nv_mul_f32 v215, 0x3d372713, v215\nM 84,180,220\n"
"buffer_load_dwordx4 v221, s[12:15], s63 offen offset:1024 lds\nM 68,176,200\nR 170,187,61440\ns_add_u32 s63, s54, 2752640\nM 68,180,216\ns_add_u32 m0, s50, 0\n"
"s_mov_b64 s[60:61], 0xffff\nv_cndmask_b32 v209, 0, v209, s[60:61]\nv_mul_f32 v232, 0xba800000, v232\nM 52,176,196\nR 172,191,0\nv_mul_f32 v217, v232, v232\n"
"M 52,180,212\nbuffer_load_dwordx4 v220, s[12:15], s63 offen lds\nM 36,176,192\nR 174,191,4096\nv_mul_f32 v233, 0x3a800000, v233\nM 36,180,208\n"
"G 4\nv_sub_f32 v233, v233, v217\nv_max_f32 v233, 0, v233\nM 40,160,192\nR 176,188,8192\nv_add_f32 v233, 0x3727c5ac, v233\n"
"M 40,164,208\nbuffer_load_dwordx4 v221, s[12:15], s63 offen offset:1024 lds\nM 56,160,196\nR 178,188,12288\nv_and_b32 v212, s77, v232\nM 56,164,212\n"
"s_add_u32 m0, s50, 8192\nv_rsq_f32 v235, v233\nM 72,160,200\nR 180,188,16384\nv_mul_f32 v219, v233, v235\nM 72,164,216\n"
"s_add_u32 s63, s54, 2982016\nv_sub_f32 v232, v232, v212\nv_cvt_pk_bf16_f32 v213, v232, v219\nM 88,160,204\nR 182,188,20480\nv_cvt_pk_bf16_f32 v212, v212, v212\n"
"M 88,164,220\ns_mov_b64 s[60:61], 0xffff\nv_cndmask_b32 v212, 0, v212, s[60:61]\nG 4\nv_mul_f32 v217, 0xc0135761, v235\nM 92,168,204\n"
"R 160,189,8192\nv_mul_f32 v218, v235, v235\nM 92,172,220\nbuffer_load_dwordx4 v220, s[12:15], s63 offen lds\nM 76,168,200\nR 162,189,12288\n"
"v_mul_f32 v218, v218, v217\nM 76,172,216\ns_mov_b64 s[60:61], 0xffff\nv_cndmask_b32 v213, 0, v213, s[60:61]\nv_mul_f32 v218, 0x3d372713, v218\nM 60,168,196\n"
"R 164,189,16384\nM 60,172,212\nbuffer_load_dwordx4 v221, s[12:15], s63 offen offset:1024 lds\nM 44,168,192\nR 166,189,20480\ns_mov_b32 s60, 157184\n"
"v_bfe_u32 v246, v252, 4, 4\nv_lshl_add_u32 v246, v246, 3, s60\nM 44,172,208\nds_read_b64 v[238:239], v246 offset:0\nG 5\nM 32,176,224\n"
"R 168,190,8192\nds_read_b64 v[240:241], v246 offset:128\nM 32,180,240\nds_read_b64 v[242:243], v246 offset:256\ns_add_u32 m0, s50, 16384\nM 48,176,228\n"
"R 170,190,12288\nds_read_b64 v[244:245], v246 offset:384\nM 48,180,244\nds_read_b64 v[246:247], v246 offset:0\ns_add_u32 s63, s54, 3211392\nM 64,176,232\n"
"R 172,190,16384\nM 64,180,248\nbuffer_load_dwordx4 v220, s[12:15], s63 offen lds\nM 80,176,236\nR 174,190,20480\ns_waitcnt vmcnt(13)\n"
"M 80,180,252\ns_waitcnt vmcnt(9) lgkmcnt(2)\ns_barrier\nM 84,160,236\nR 176,191,8192\nM 84,164,252\n"
"buffer_load_dwordx4 v221, s[12:15], s63 offen offset:1024 lds\nM 68,160,232\nR 178,191,12288\ns_add_u32 s63, s54, 3440768\nM 68,164,248\ns_add_u32 m0, s50, 24576\n"
"M 52,160,228\nR 180,191,16384\nv_mfma_f32_16x16x32_bf16 v[32:35], v[238:241], v[208:211], v[32:35]\nM 52,164,244\nbuffer_load_dwordx4 v220, s[12:15], s63 offen lds\nM 36,160,224\n"
"R 182,191,20480\nv_mfma_f32_16x16x32_bf16 v[36:39], v[240:243], v[208:211], v[36:39]\nM 36,164,240\nG 4\nM 40,168,224\nR 160,188,24576\n"
"v_mfma_f32_16x16x32_bf16 v[40:43], v[242:245], v[208:211], v[40:43]\nM 40,172,240\nbuffer_load_dwordx4 v221, s[12:15], s63 offen offset:1024 lds\nM 56,168,228\nR 162,188,28672\nv_mfma_f32_16x16x32_bf16 v[44:47], v[244:247], v[208:211], v[44:47]\n"
"s_add_u32 m0, s50, 32768\ns_add_u32 s63, s55, 0\nM 56,172,244\nbuffer_load_dwordx4 v222, s[12:15], s63 offen lds\nM 72,168,232\nR 164,188,32768\n"
"v_mfma_f32_16x16x32_bf16 v[48:51], v[244:247], v[210:213], v[48:51]\nM 72,172,248\nM 88,168,236\nR 166,188,36864\nv_mfma_f32_16x16x32_bf16 v[52:55], v[238:241], v[210:213], v[52:55]\nM 88,172,252\n"
"G 4\nM 92,176,236\nR 168,189,24576\nv_mfma_f32_16x16x32_bf16 v[56:59], v[240:243], v[210:213], v[56:59]\nM 92,180,252\nbuffer_load_dwordx4 v223, s[12:15], s63 offen offset:1024 lds\n"
"M 76,176,232\nR 170,189,28672\nv_mfma_f32_16x16x32_bf16 v[60:63], v[242:245], v[210:213], v[60:63]\nM 76,180,248\nM 60,176,228\nR 172,189,32768\n"
"M 60,180,244\nM 44,176,224\nR 174,189,36864\nM 44,180,240\ns_waitcnt vmcnt(10)\ns_barrier\n"
"G 4\nM 96,160,128\nR 176,190,24576\nM 96,164,144\ns_mov_b32 m0, s51\nM 112,160,132\n"
"R 178,190,28672\ns_lshl2_add_u32 s60, s45, s40\nv_lshlrev_b32 v224, 3, v252\ns_mul_i32 s60, s60, 7168\ns_bfm_b64 exec, 14, 0\nbuffer_load_dword v224, v224, s[16:19], s60 offen\n"
"s_mov_b64 exec, -1\nM 112,164,148\nds_write_addtid_b32 v64 offset:0\nM 128,160,136\nR 180,190,32768\nM 128,164,152\n"
"ds_write_addtid_b32 v65 offset:256\nM 144,160,140\nR 182,190,36864\nds_write_addtid_b32 v66 offset:512\nM 144,164,156\nds_write_addtid_b32 v67 offset:768\n"
"ds_write_addtid_b32 v68 offset:1024\nG 9\nM 148,168,140\nR 160,191,24576\nds_write_addtid_b32 v69 offset:1280\nM 148,172,156\n"
"ds_write_addtid_b32 v70 offset:1536\nds_write_addtid_b32 v71 offset:1792\nM 132,168,136\nR 162,191,28672\nds_write_addtid_b32 v72 offset:2048\nM 132,172,152\n"
"ds_write_addtid_b32 v73 offset:2304\nds_write_addtid_b32 v74 offset:2560\nM 116,168,132\nR 164,191,32768\nds_write_addtid_b32 v75 offset:2816\nM 116,172,148\n"
"ds_write_addtid_b32 v76 offset:3072\nds_write_addtid_b32 v77 offset:3328\nM 100,168,128\nR 166,191,36864\nds_write_addtid_b32 v78 offset:3584\nM 100,172,144\n"
"ds_write_addtid_b32 v79 offset:3840\nds_write_addtid_b32 v80 offset:4096\nG 15\nM 104,176,128\nR 168,188,40960\nds_write_addtid_b32 v81 offset:4352\n"
"M 104,180,144\nds_write_addtid_b32 v82 offset:4608\nds_write_addtid_b32 v83 offset:4864\nM 120,176,132\nR 170,188,45056\nds_write_addtid_b32 v84 offset:5120\n"
"M 120,180,148\nds_write_addtid_b32 v85 offset:5376\nds_write_addtid_b32 v86 offset:5632\nM 136,176,136\nR 172,188,49152\nds_write_addtid_b32 v87 offset:5888\n"
"M 136,180,152\nds_write_addtid_b32 v88 offset:6144\nds_write_addtid_b32 v89 offset:6400\nM 152,176,140\nR 174,188,53248\nds_write_addtid_b32 v90 offset:6656\n"
"M 152,180,156\nds_write_addtid_b32 v91 offset:6912\nds_write_addtid_b32 v92 offset:7168\nG 15\nM 156,160,140\nR 176,189,40960\n"
"ds_write_addtid_b32 v93 offset:7424\nM 156,164,156\nds_write_addtid_b32 v94 offset:7680\nds_write_addtid_b32 v95 offset:7936\nM 140,160,136\nR 178,189,45056\n"
"s_add_u32 m0, s50, 40960\ns_add_u32 s63, s55, 458752\nM 140,164,152\nbuffer_load_dwordx4 v222, s[12:15], s63 offen lds\nM 124,160,132\nR 180,189,49152\n"
"s_waitcnt vmcnt(8) lgkmcnt(2)\ns_barrier\nM 124,164,148\nds_read2st64_b32 v[64:65], v225 offset0:0 offset1:1\nds_read2st64_b32 v[66:67], v225 offset0:2 offset1:3\nM 108,160,128\n"
"R 182,189,53248\nds_read2st64_b32 v[68:69], v225 offset0:4 offset1:5\nM 108,164,144\nds_read2st64_b32 v[70:71], v225 offset0:6 offset1:7\nds_read2st64_b32 v[72:73], v225 offset0:8 offset1:9\nM 96,168,160\n"
"R 160,190,40960\nds_read2st64_b32 v[74:75], v225 offset0:10 offset1:11\nM 96,172,176\nbuffer_load_dwordx4 v223, s[12:15], s63 offen offset:1024 lds\nds_read2st64_b32 v[76:77], v225 offset0:12 offset1:13\nM 112,168,164\n"
"R 162,190,45056\nds_read2st64_b32 v[78:79], v225 offset0:14 offset1:15\nM 112,172,180\ns_add_u32 m0, s50, 49152\nds_read2st64_b32 v[80:81], v225 offset0:16 offset1:17\nds_read2st64_b32 v[82:83], v225 offset0:18 offset1:19\n"
"M 128,168,168\nR 164,190,49152\nds_read2st64_b32 v[84:85], v225 offset0:20 offset1:21\nG 14\nv_add_f32 v32, v32, v64\nM 128,172,184\n"
"s_add_u32 s63, s55, 917504\nds_read2st64_b32 v[86:87], v225 offset0:22 offset1:23\nds_read2st64_b32 v[88:89], v225 offset0:24 offset1:25\nM 144,168,172\nR 166,190,53248\nds_read2st64_b32 v[90:91], v225 offset0:26 offset1:27\n"
"v_add_f32 v33, v33, v65\nM 144,172,188\nds_read2st64_b32 v[92:93], v225 offset0:28 offset1:29\nds_read2st64_b32 v[94:95], v225 offset0:30 offset1:31\nG 15\nM 148,176,172\n"
"R 168,191,40960\nv_add_f32 v34, v34, v66\nv_add_f32 v35, v35, v67\nM 148,180,188\nbuffer_load_dwordx4 v222, s[12:15], s63 offen lds\nv_add_f32 v36, v36, v68\n"
"M 132,176,168\nR 170,191,45056\nv_add_f32 v37, v37, v69\nv_add_f32 v38, v38, v70\nM 132,180,184\nv_add_f32 v39, v39, v71\n"
"v_add_f32 v40, v40, v72\nM 116,176,164\nR 172,191,49152\nv_add_f32 v41, v41, v73\nM 116,180,180\nG 15\n"
"v_add_f32 v42, v42, v74\nv_add_f32 v43, v43, v75\nM 100,176,160\nR 174,191,53248\nv_add_f32 v44, v44, v76\nM 100,180,176\n"
"G 7\nv_add_f32 v45, v45, v77\nv_add_f32 v46, v46, v78\nM 104,160,160\nR 176,184,0\nv_add_f32 v47, v47, v79\n"
"M 104,164,176\nbuffer_load_dwordx4 v223, s[12:15], s63 offen offset:1024 lds\nM 120,160,164\nR 178,184,4096\nv_add_f32 v48, v48, v80\nM 120,164,180\n"
"s_add_u32 m0, s50, 57344\nv_add_f32 v49, v49, v81\nv_add_f32 v50, v50, v82\nM 136,160,168\nR 180,184,8192\nv_add_f32 v51, v51, v83\n"
"M 136,164,184\ns_add_u32 s63, s55, 1376256\nv_add_f32 v52, v52, v84\nv_add_f32 v53, v53, v85\nM 152,160,172\nR 182,184,12288\n"
"v_add_f32 v54, v54, v86\nM 152,164,188\nG 4\nv_add_f32 v55, v55, v87\nv_add_f32 v56, v56, v88\nM 156,168,172\n"
"R 160,185,0\nv_add_f32 v57, v57, v89\nM 156,172,188\nbuffer_load_dwordx4 v222, s[12:15], s63 offen lds\nM 140,168,168\nR 162,185,4096\n"
"v_add_f32 v58, v58, v90\nM 140,172,184\nv_add_f32 v59, v59, v91\nv_add_f32 v60, v60, v92\nM 124,168,164\nR 164,185,8192\n"
"v_add_f32 v61, v61, v93\nM 124,172,180\nv_add_f32 v62, v62, v94\nv_add_f32 v63, v63, v95\nM 108,168,160\nR 166,185,12288\n"
"v_mul_f32 v230, v32, v32\nv_mul_f32 v231, v33, v33\nM 108,172,176\ns_add_u32 s84, s73, 23068672\ns_add_u32 s85, s73, 23183360\nG 4\n"
"v_mul_f32 v232, v34, v34\nM 96,176,192\nR 168,186,0\nv_mul_f32 v233, v35, v35\nv_fma_f32 v230, v230, v215, v214\nM 96,180,208\n"
"v_fma_f32 v231, v231, v215, v214\nv_fma_f32 v232, v232, v215, v214\nv_fma_f32 v233, v233, v215, v214\nM 112,176,196\nR 170,186,4096\ns_lshl2_add_u32 s60, s45, s40\n"
"s_waitcnt vmcnt(5)\nv_lshlrev_b32 v224, 3, v252\ns_mul_i32 s60, s60, 7168\ns_bfm_b64 exec, 14, 14\nbuffer_load_dword v224, v224, s[16:19], s60 offen\ns_mov_b64 exec, -1\n"
"s_barrier\nv_mul_f32 v230, v32, v230\nM 112,180,212\nv_exp_f32 v230, v230\nv_mul_f32 v231, v33, v231\nM 128,176,200\n"
"R 172,186,8192\nv_mul_f32 v232, v34, v232\nv_mul_f32 v233, v35, v233\nM 128,180,216\nv_exp_f32 v231, v231\nv_exp_f32 v232, v232\n"
"M 144,176,204\nR 174,186,12288\nv_fma_f32 v230, v230, v216, v216\nv_exp_f32 v233, v233\nM 144,180,220\nG 4\n"
"v_rcp_f32 v230, v230\nv_fma_f32 v231, v231, v216, v216\nM 148,160,204\nR 176,187,0\nv_fma_f32 v232, v232, v216, v216\nv_fma_f32 v233, v233, v216, v216\n"
"M 148,164,220\nbuffer_load_dwordx4 v223, s[12:15], s63 offen offset:1024 lds\nv_rcp_f32 v231, v231\nM 132,160,200\nR 178,187,4096\nv_mul_f32 v230, v32, v230\n"
"v_rcp_f32 v232, v232\nM 132,164,216\ns_add_u32 m0, s50, 65536\nv_rcp_f32 v233, v233\nv_mul_f32 v231, v33, v231\nM 116,160,196\n"
"R 180,187,8192\nv_mul_f32 v232, v34, v232\nv_mul_f32 v233, v35, v233\nM 116,164,212\ns_add_u32 s63, s55, 1835008\nv_cvt_pk_bf16_f32 v32, v230, v231\n"
"v_cvt_pk_bf16_f32 v33, v232, v233\nv_mul_f32 v234, v36, v36\nM 100,160,192\nR 182,187,12288\nv_mul_f32 v235, v37, v37\nv_mul_f32 v236, v38, v38\n"
"M 100,164,208\nG 4\nv_mul_f32 v237, v39, v39\nv_fma_f32 v234, v234, v215, v214\nv_fma_f32 v235, v235, v215, v214\nM 104,168,192\n"
"R 160,184,16384\nv_fma_f32 v236, v236, v215, v214\nv_fma_f32 v237, v237, v215, v214\nM 104,172,208\nbuffer_load_dwordx4 v222, s[12:15], s63 offen lds\nv_mul_f32 v234, v36, v234\n"
"M 120,168,196\nR 162,184,20480\nv_mul_f32 v235, v37, v235\nv_mul_f32 v236, v38, v236\nM 120,172,212\nv_exp_f32 v234, v234\n"
"v_mul_f32 v237, v39, v237\nM 136,168,200\nR 164,184,24576\nv_fma_f32 v234, v234, v216, v216\nv_exp_f32 v235, v235\nM 136,172,216\n"
"v_exp_f32 v236, v236\nv_exp_f32 v237, v237\nM 152,168,204\nR 166,184,28672\nv_fma_f32 v235, v235, v216, v216\nv_fma_f32 v236, v236, v216, v216\n"
"M 152,172,220\nG 4\nv_rcp_f32 v234, v234\nv_fma_f32 v237, v237, v216, v216\nM 156,176,204\nR 168,185,16384\n"
"v_mul_f32 v234, v36, v234\nv_rcp_f32 v235, v235\nM 156,180,220\nbuffer_load_dwordx4 v223, s[12:15], s63 offen offset:1024 lds\nv_rcp_f32 v236, v236\nM 140,176,200\n"
"R 170,185,20480\nv_mul_f32 v235, v37, v235\nv_rcp_f32 v237, v237\nM 140,180,216\nv_mul_f32 v236, v38, v236\nv_mul_f32 v237, v39, v237\n"
"v_cvt_pk_bf16_f32 v34, v234, v235\nM 124,176,196\nR 172,185,24576\nv_cvt_pk_bf16_f32 v35, v236, v237\nv_mul_f32 v230, v40, v40\nM 124,180,212\n"
"buffer_store_dwordx4 v[32:35], v252, s[20:23], s84 offen offset:0 sc1\nv_mul_f32 v231, v41, v41\nM 108,176,192\nR 174,185,28672\ns_mov_b32 s60, 157696\nv_bfe_u32 v246, v252, 4, 4\n"
"v_lshl_add_u32 v246, v246, 3, s60\nv_mul_f32 v232, v42, v42\nM 108,180,208\nds_read_b64 v[238:239], v246 offset:0\nG 5\nv_mul_f32 v233, v43, v43\n"
"M 96,160,224\nR 176,186,16384\nds_read_b64 v[240:241], v246 offset:128\nv_fma_f32 v230, v230, v215, v214\nM 96,164,240\nds_read_b64 v[242:243], v246 offset:256\n"
"s_add_u32 m0, s50, 73728\nv_fma_f32 v231, v231, v215, v214\nM 112,160,228\nR 178,186,20480\nds_read_b64 v[244:245], v246 offset:384\nv_fma_f32 v232, v232, v215, v214\n"
"M 112,164,244\nds_read_b64 v[246:247], v246 offset:0\ns_add_u32 s63, s55, 2293760\nv_fma_f32 v233, v233, v215, v214\nM 128,160,232\nR 180,186,24576\n"
"v_mul_f32 v230, v40, v230\nv_mul_f32 v231, v41, v231\nM 128,164,248\nbuffer_load_dwordx4 v222, s[12:15], s63 offen lds\nv_mul_f32 v232, v42, v232\nM 144,160,236\n"
"R 182,186,28672\nv_mul_f32 v233, v43, v233\nv_exp_f32 v230, v230\nM 144,164,252\nG 9\nv_exp_f32 v231, v231\n"
"v_exp_f32 v232, v232\nM 148,168,236\nR 160,187,16384\nv_fma_f32 v230, v230, v216, v216\nv_exp_f32 v233, v233\nM 148,172,252\n"
"buffer_load_dwordx4 v223, s[12:15], s63 offen offset:1024 lds\nv_fma_f32 v231, v231, v216, v216\nM 132,168,232\nR 162,187,20480\ns_add_u32 s63, s55, 2752512\nv_fma_f32 v232, v232, v216, v216\n"
"M 132,172,248\ns_add_u32 m0, s50, 81920\nv_rcp_f32 v230, v230\nv_fma_f32 v233, v233, v216, v216\nM 116,168,228\nR 164,187,24576\n"
"G 9\nv_mfma_f32_16x16x32_bf16 v[96:99], v[238:241], v[208:211], v[96:99]\nLpg10:\ns_cbranch_execz Lpg14\nv_mul_f32 v230, v40, v230\nv_rcp_f32 v231, v231\n"
"M 116,172,244\nbuffer_load_dwordx4 v222, s[12:15], s63 offen lds\nv_rcp_f32 v232, v232\nM 100,168,224\nR 166,187,28672\nG 9\n"
"v_mfma_f32_16x16x32_bf16 v[100:103], v[240:243], v[208:211], v[100:103]\nv_mul_f32 v231, v41, v231\nv_rcp_f32 v233, v233\nM 100,172,240\nG 4\nv_mul_f32 v232, v42, v232\n"
"v_mul_f32 v233, v43, v233\nv_cvt_pk_bf16_f32 v40, v230, v231\nM 104,176,224\nR 168,184,32768\nv_mfma_f32_16x16x32_bf16 v[104:107], v[242:245], v[208:211], v[104:107]\nv_cvt_pk_bf16_f32 v41, v232, v233\n"
"v_mul_f32 v234, v44, v44\nM 104,180,240\nbuffer_load_dwordx4 v223, s[12:15], s63 offen offset:1024 lds\nv_mul_f32 v235, v45, v45\nM 120,176,228\nR 170,184,36864\n"
"v_mfma_f32_16x16x32_bf16 v[108:111], v[244:247], v[208:211], v[108:111]\nv_mul_f32 v236, v46, v46\nv_mul_f32 v237, v47, v47\nM 120,180,244\ns_add_u32 m0, s50, 90112\nv_fma_f32 v234, v234, v215, v214\n"
"v_fma_f32 v235, v235, v215, v214\nv_fma_f32 v236, v236, v215, v214\nM 136,176,232\nR 172,186,32768\nv_mfma_f32_16x16x32_bf16 v[112:115], v[244:247], v[210:213], v[112:115]\nv_fma_f32 v237, v237, v215, v214\n"
"v_mul_f32 v234, v44, v234\nM 136,180,248\ns_add_u32 s63, s55, 3211264\nv_exp_f32 v234, v234\nv_mul_f32 v235, v45, v235\nM 152,176,236\n"
"R 174,186,36864\nv_mfma_f32_16x16x32_bf16 v[116:119], v[238:241], v[210:213], v[116:119]\nv_mul_f32 v236, v46, v236\nv_mul_f32 v237, v47, v237\nM 152,180,252\nG 4\n"
"v_exp_f32 v235, v235\nv_exp_f32 v236, v236\nM 156,160,236\nR 176,185,32768\nv_mfma_f32_16x16x32_bf16 v[120:123], v[240:243], v[210:213], v[120:123]\nv_fma_f32 v234, v234, v216, v216\n"
"v_exp_f32 v237, v237\nM 156,164,252\nbuffer_load_dwordx4 v222, s[12:15], s63 offen lds\nv_fma_f32 v235, v235, v216, v216\nM 140,160,232\nR 178,185,36864\n"
"v_mfma_f32_16x16x32_bf16 v[124:127], v[242:245], v[210:213], v[124:127]\nv_fma_f32 v236, v236, v216, v216\nv_fma_f32 v237, v237, v216, v216\nM 140,164,248\nv_rcp_f32 v234, v234\nv_rcp_f32 v235, v235\n"
"M 124,160,228\nR 180,187,32768\nv_mul_f32 v234, v44, v234\nv_rcp_f32 v236, v236\nM 124,164,244\nbuffer_load_dwordx4 v223, s[12:15], s63 offen offset:1024 lds\n"
"v_rcp_f32 v237, v237\nM 108,160,224\nR 182,187,36864\nv_mul_f32 v235, v45, v235\nv_mul_f32 v236, v46, v236\nM 108,164,240\n"
"s_waitcnt vmcnt(12)\ns_barrier\nG 4\nv_mul_f32 v237, v47, v237\nv_mfma_f32_16x16x32_bf16 v[0:3], v[168:171], a[0:3], 0\nR 160,184,40960\n"
"v_cvt_pk_bf16_f32 v42, v234, v235\nv_cvt_pk_bf16_f32 v43, v236, v237\nM 0,172,16\ns_mov_b32 m0, s51\nv_mul_f32 v230, v48, v48\nv_mul_f32 v231, v49, v49\n"
"v_mul_f32 v232, v50, v50\nv_mfma_f32_16x16x32_bf16 v[16:19], v[168:171], a[4:7], 0\nR 162,184,45056\nv_mul_f32 v233, v51, v51\nv_fma_f32 v230, v230, v218, v217\nM 16,172,20\n"
"buffer_store_dwordx4 v[40:43], v252, s[20:23], s84 offen offset:1024 sc1\nv_fma_f32 v231, v231, v218, v217\nv_mfma_f32_16x16x32_bf16 v[8:11], v[168:171], a[8:11], 0\nR 164,186,40960\nv_fma_f32 v232, v232, v218, v217\nv_fma_f32 v233, v233, v218, v217\n"
"M 8,172,24\nds_write_addtid_b32 v128 offset:0\nv_mul_f32 v230, v48, v230\nv_mul_f32 v231, v49, v231\nv_mul_f32 v232, v50, v232\nv_mfma_f32_16x16x32_bf16 v[24:27], v[168:171], a[12:15], 0\n"
"R 166,186,45056\nds_write_addtid_b32 v129 offset:256\nv_mul_f32 v233, v51, v233\nv_exp_f32 v230, v230\nM 24,172,28\nds_write_addtid_b32 v130 offset:512\n"
"G 7\nv_exp_f32 v231, v231\nv_exp_f32 v232, v232\nv_mfma_f32_16x16x32_bf16 v[28:31], v[176:179], a[12:15], 0\nR 168,185,40960\nds_write_addtid_b32 v131 offset:768\n"
"v_fma_f32 v230, v230, v219, v219\nv_exp_f32 v233, v233\nM 28,180,28\nds_write_addtid_b32 v132 offset:1024\nv_rcp_f32 v230, v230\nv_fma_f32 v231, v231, v219, v219\n"
"v_mfma_f32_16x16x32_bf16 v[12:15], v[176:179], a[8:11], 0\nR 170,185,45056\nds_write_addtid_b32 v133 offset:1280\nv_fma_f32 v232, v232, v219, v219\nv_fma_f32 v233, v233, v219, v219\nM 12,180,24\n"
"ds_write_addtid_b32 v134 offset:1536\nv_rcp_f32 v231, v231\nv_rcp_f32 v232, v232\nv_mfma_f32_16x16x32_bf16 v[20:23], v[176:179], a[4:7], 0\nR 172,187,40960\nds_write_addtid_b32 v135 offset:1792\n"
"v_mul_f32 v230, v48, v230\nv_rcp_f32 v233, v233\nM 20,180,20\nds_write_addtid_b32 v136 offset:2048\nv_mul_f32 v231, v49, v231\nv_mul_f32 v232, v50, v232\n"
"v_mul_f32 v233, v51, v233\nv_mfma_f32_16x16x32_bf16 v[4:7], v[176:179], a[0:3], 0\nR 174,187,45056\nds_write_addtid_b32 v137 offset:2304\nv_cvt_pk_bf16_f32 v48, v230, v231\nv_cvt_pk_bf16_f32 v49, v232, v233\n"
"M 4,180,16\nds_write_addtid_b32 v138 offset:2560\nG 14\nv_mul_f32 v234, v52, v52\nv_mul_f32 v235, v53, v53\nv_mul_f32 v236, v54, v54\n"
"M 0,160,32\nR 176,184,49152\nds_write_addtid_b32 v139 offset:2816\nv_mul_f32 v237, v55, v55\nv_fma_f32 v234, v234, v218, v217\nM 0,164,48\n"
"ds_write_addtid_b32 v140 offset:3072\nv_fma_f32 v235, v235, v218, v217\nv_fma_f32 v236, v236, v218, v217\nv_fma_f32 v237, v237, v218, v217\nM 16,160,36\nR 178,184,53248\n"
"ds_write_addtid_b32 v141 offset:3328\nv_mul_f32 v234, v52, v234\nv_mul_f32 v235, v53, v235\nM 16,164,52\nds_write_addtid_b32 v142 offset:3584\nv_exp_f32 v234, v234\n"
"v_mul_f32 v236, v54, v236\nM 8,160,40\nR 180,186,49152\nds_write_addtid_b32 v143 offset:3840\nv_mul_f32 v237, v55, v237\nv_exp_f32 v235, v235\n"
"M 8,164,56\nds_write_addtid_b32 v144 offset:4096\nv_exp_f32 v236, v236\nv_exp_f32 v237, v237\nM 24,160,44\nR 182,186,53248\n"
"ds_write_addtid_b32 v145 offset:4352\nv_fma_f32 v234, v234, v219, v219\nv_fma_f32 v235, v235, v219, v219\nM 24,164,60\nds_write_addtid_b32 v146 offset:4608\nG 14\n"
"v_rcp_f32 v234, v234\nv_fma_f32 v236, v236, v219, v219\nM 28,168,44\nR 160,185,49152\nds_write_addtid_b32 v147 offset:4864\nv_fma_f32 v237, v237, v219, v219\n"
"v_rcp_f32 v235, v235\nM 28,172,60\nds_write_addtid_b32 v148 offset:5120\nds_write_addtid_b32 v149 offset:5376\nds_write_addtid_b32 v150 offset:5632\nds_write_addtid_b32 v151 offset:5888\n"
"ds_write_addtid_b32 v152 offset:6144\nds_write_addtid_b32 v153 offset:6400\nds_write_addtid_b32 v154 offset:6656\nds_write_addtid_b32 v155 offset:6912\nds_write_addtid_b32 v156 offset:7168\nds_write_addtid_b32 v157 offset:7424\n"
"ds_write_addtid_b32 v158 offset:7680\nds_write_addtid_b32 v159 offset:7936\nv_rcp_f32 v236, v236\nv_rcp_f32 v237, v237\nM 12,168,40\nR 162,185,53248\n"
"s_add_u32 m0, s50, 98304\ns_add_u32 s63, s54, 256\nv_mul_f32 v234, v52, v234\nM 12,172,56\nbuffer_load_dwordx4 v220, s[12:15], s63 offen lds\nv_mul_f32 v235, v53, v235\n"
"M 20,168,36\nR 164,187,49152\ns_waitcnt vmcnt(4) lgkmcnt(2)\ns_barrier\nv_mul_f32 v236, v54, v236\nM 20,172,52\n"
"ds_read2st64_b32 v[128:129], v225 offset0:0 offset1:1\nv_mul_f32 v237, v55, v237\nv_cvt_pk_bf16_f32 v50, v234, v235\nv_cvt_pk_bf16_f32 v51, v236, v237\nM 4,168,32\nR 166,187,53248\n"
"ds_read2st64_b32 v[130:131], v225 offset0:2 offset1:3\nv_mul_f32 v230, v56, v56\nv_mul_f32 v231, v57, v57\nM 4,172,48\nds_read2st64_b32 v[132:133], v225 offset0:4 offset1:5\nv_mul_f32 v232, v58, v58\n"
"v_mul_f32 v233, v59, v59\nv_fma_f32 v230, v230, v218, v217\nM 0,176,64\nR 168,184,57344\nds_read2st64_b32 v[134:135], v225 offset0:6 offset1:7\nv_fma_f32 v231, v231, v218, v217\n"
"v_fma_f32 v232, v232, v218, v217\nM 0,180,80\nbuffer_load_dwordx4 v221, s[12:15], s63 offen offset:1024 lds\nds_read2st64_b32 v[136:137], v225 offset0:8 offset1:9\nv_fma_f32 v233, v233, v218, v217\nM 16,176,68\n"
"R 170,184,61440\ns_add_u32 m0, s50, 106496\ns_add_u32 s63, s54, 229632\nds_read2st64_b32 v[138:139], v225 offset0:10 offset1:11\nv_mul_f32 v230, v56, v230\nM 16,180,84\n"
"buffer_load_dwordx4 v220, s[12:15], s63 offen lds\nds_read2st64_b32 v[140:141], v225 offset0:12 offset1:13\nv_mul_f32 v231, v57, v231\nM 8,176,72\nR 172,186,57344\nds_read2st64_b32 v[142:143], v225 offset0:14 offset1:15\n"
"v_mul_f32 v232, v58, v232\nv_mul_f32 v233, v59, v233\nM 8,180,88\nbuffer_store_dwordx4 v[48:51], v252, s[20:23], s85 offen offset:0 sc1\nds_read2st64_b32 v[144:145], v225 offset0:16 offset1:17\nv_exp_f32 v230, v230\n"
"M 24,176,76\nR 174,186,61440\nds_read2st64_b32 v[146:147], v225 offset0:18 offset1:19\nv_fma_f32 v230, v230, v219, v219\nv_exp_f32 v231, v231\nM 24,180,92\n"
"ds_read2st64_b32 v[148:149], v225 offset0:20 offset1:21\nG 14\nv_exp_f32 v232, v232\nv_exp_f32 v233, v233\nM 28,160,76\nR 176,185,57344\n"
"ds_read2st64_b32 v[150:151], v225 offset0:22 offset1:23\nv_fma_f32 v231, v231, v219, v219\nv_fma_f32 v232, v232, v219, v219\nM 28,164,92\nbuffer_load_dwordx4 v221, s[12:15], s63 offen offset:1024 lds\nds_read2st64_b32 v[152:153], v225 offset0:24 offset1:25\n"
"v_fma_f32 v233, v233, v219, v219\nM 12,160,72\nR 178,185,61440\ns_add_u32 s63, s54, 459008\nds_read2st64_b32 v[154:155], v225 offset0:26 offset1:27\nv_rcp_f32 v230, v230\n"
"M 12,164,88\ns_add_u32 m0, s50, 114688\nds_read2st64_b32 v[156:157], v225 offset0:28 offset1:29\nv_rcp_f32 v231, v231\nv_rcp_f32 v232, v232\nM 20,160,68\n"
"R 180,187,57344\nds_read2st64_b32 v[158:159], v225 offset0:30 offset1:31\nv_mul_f32 v230, v56, v230\nv_rcp_f32 v233, v233\nM 20,164,84\nbuffer_load_dwordx4 v220, s[12:15], s63 offen lds\n"
"v_add_f32 v96, v96, v128\nv_mul_f32 v231, v57, v231\nM 4,160,64\nR 182,187,61440\nv_mul_f32 v232, v58, v232\nv_add_f32 v97, v97, v129\n"
"v_mul_f32 v233, v59, v233\nM 4,164,80\nG 11\nv_cvt_pk_bf16_f32 v56, v230, v231\nv_cvt_pk_bf16_f32 v57, v232, v233\nv_add_f32 v98, v98, v130\n"
"v_mul_f32 v234, v60, v60\nM 0,168,96\nR 160,188,0\nv_mul_f32 v235, v61, v61\nv_add_f32 v99, v99, v131\nv_mul_f32 v236, v62, v62\n"
"M 0,172,112\nbuffer_load_dwordx4 v221, s[12:15], s63 offen offset:1024 lds\nv_add_f32 v100, v100, v132\nv_mul_f32 v237, v63, v63\nM 16,168,100\nR 162,188,4096\n"
"v_fma_f32 v234, v234, v218, v217\nv_add_f32 v101, v101, v133\nv_fma_f32 v235, v235, v218, v217\nM 16,172,116\ns_add_u32 m0, s50, 0\nv_fma_f32 v236, v236, v218, v217\n"
"v_fma_f32 v237, v237, v218, v217\nv_add_f32 v102, v102, v134\nv_mul_f32 v234, v60, v234\nM 8,168,104\nR 164,190,0\nv_mul_f32 v235, v61, v235\n"
"v_add_f32 v103, v103, v135\nv_mul_f32 v236, v62, v236\nM 8,172,120\ns_add_u32 s63, s54, 688384\nv_exp_f32 v234, v234\nv_add_f32 v104, v104, v136\n"
"v_mul_f32 v237, v63, v237\nM 24,168,108\nR 166,190,4096\nv_add_f32 v105, v105, v137\nv_add_f32 v106, v106, v138\nv_exp_f32 v235, v235\n"
"M 24,172,124\nG 4\nv_add_f32 v107, v107, v139\nv_add_f32 v108, v108, v140\nv_add_f32 v109, v109, v141\nv_exp_f32 v236, v236\n"
"M 28,176,108\nR 168,189,0\nv_add_f32 v110, v110, v142\nv_add_f32 v111, v111, v143\nv_exp_f32 v237, v237\nM 28,180,124\n"
"buffer_load_dwordx4 v220, s[12:15], s63 offen lds\nv_add_f32 v112, v112, v144\nv_fma_f32 v234, v234, v219, v219\nM 12,176,104\nR 170,189,4096\nv_add_f32 v113, v113, v145\n"
"v_add_f32 v114, v114, v146\nv_fma_f32 v235, v235, v219, v219\nM 12,180,120\nv_add_f32 v115, v115, v147\nv_add_f32 v116, v116, v148\nv_add_f32 v117, v117, v149\n"
"v_fma_f32 v236, v236, v219, v219\nM 20,176,100\nR 172,191,0\nv_add_f32 v118, v118, v150\nv_add_f32 v119, v119, v151\nM 20,180,116\n"
"buffer_load_dwordx4 v221, s[12:15], s63 offen offset:1024 lds\nv_add_f32 v120, v120, v152\nv_add_f32 v121, v121, v153\nv_add_f32 v122, v122, v154\nv_add_f32 v123, v123, v155\nv_add_f32 v124, v124, v156\n"
"v_add_f32 v125, v125, v157\nv_add_f32 v126, v126, v158\nv_add_f32 v127, v127, v159\nM 4,176,96\nR 174,191,4096\ns_add_u32 m0, s50, 8192\n"
"M 4,180,112\nG 4\nv_rcp_f32 v234, v234\nM 0,160,128\nR 176,188,8192\ns_add_u32 s63, s54, 917760\n"
"M 0,164,144\nbuffer_load_dwordx4 v220, s[12:15], s63 offen lds\nM 16,160,132\nR 178,188,12288\nv_fma_f32 v237, v237, v219, v219\nM 16,164,148\n"
"v_rcp_f32 v235, v235\nM 8,160,136\nR 180,190,8192\nLq575:\ns_lshr_b32 s60, s45, 3\ns_lshl2_add_u32 s60, s60, s40\n"
"s_lshl_b32 s60, s60, 8\nv_lshl_add_u32 v224, v252, 3, s60\nv_add_u32 v224, Lq575+4096-LqPC, v224\nv_min_u32 v224, LqE-LqPC-4, v224\ns_mov_b64 exec, 3\nglobal_load_dword v224, v224, s[98:99]\n"
"s_mov_b64 exec, -1\nM 8,164,152\nv_rcp_f32 v236, v236\nM 24,160,140\nR 182,190,12288\nv_mul_f32 v234, v60, v234\n"
"M 24,164,156\nG 4\nv_rcp_f32 v237, v237\nM 28,168,140\nR 160,189,8192\nv_mul_f32 v235, v61, v235\n"
"M 28,172,156\nbuffer_load_dwordx4 v221, s[12:15], s63 offen offset:1024 lds\nM 12,168,136\nR 162,189,12288\nv_mul_f32 v236, v62, v236\nM 12,172,152\n"
"s_add_u32 m0, s50, 16384\nv_mul_f32 v237, v63, v237\nv_cvt_pk_bf16_f32 v58, v234, v235\nM 20,168,132\nR 164,191,8192\nv_cvt_pk_bf16_f32 v59, v236, v237\n"
"M 20,172,148\ns_add_u32 s63, s54, 1147136\nM 4,168,128\nR 166,191,12288\nM 4,172,144\nG 4\n"
"M 0,176,160\nR 168,188,16384\nM 0,180,176\nbuffer_load_dwordx4 v220, s[12:15], s63 offen lds\nM 16,176,164\nR 170,188,20480\n"
"M 16,180,180\nbuffer_store_dwordx4 v[56:59], v252, s[20:23], s85 offen offset:1024 sc1\nM 8,176,168\nR 172,190,16384\nM 8,180,184\nbuffer_load_dwordx4 v221, s[12:15], s63 offen offset:1024 lds\n"
"M 24,176,172\nR 174,190,20480\ns_add_u32 m0, s50, 24576\nM 24,180,188\ns_add_u32 s63, s54, 1376512\nG 4\n"
"M 28,160,172\nR 176,189,16384\ns_waitcnt vmcnt(10)\ns_barrier\nv_mul_f32 v230, v96, v96\nM 28,164,188\n"
"buffer_load_dwordx4 v220, s[12:15], s63 offen lds\nv_mul_f32 v231, v97, v97\nM 12,160,168\nR 178,189,20480\nv_mul_f32 v232, v98, v98\nv_mul_f32 v233, v99, v99\n"
"M 12,164,184\ns_add_u32 s84, s73, 23070720\ns_add_u32 s85, s73, 23185408\nv_fma_f32 v230, v230, v215, v214\nv_fma_f32 v231, v231, v215, v214\nM 20,160,164\n"
"R 180,191,16384\nv_fma_f32 v232, v232, v215, v214\nv_fma_f32 v233, v233, v215, v214\nM 20,164,180\nbuffer_load_dwordx4 v221, s[12:15], s63 offen offset:1024 lds\nv_mul_f32 v230, v96, v230\n"
"M 4,160,160\nR 182,191,20480\ns_mov_b32 s60, 158720\nv_bfe_u32 v242, v252, 4, 4\nv_lshl_add_u32 v242, v242, 3, s60\nv_mul_f32 v231, v97, v231\n"
"M 4,164,176\nds_read_b64 v[238:239], v242 offset:0\nG 5\nv_mul_f32 v232, v98, v232\nM 0,168,192\nR 160,188,24576\n"
"ds_read_b64 v[240:241], v242 offset:128\nv_mul_f32 v233, v99, v233\nM 0,172,208\nds_read_b64 v[242:243], v242 offset:0\ns_add_u32 m0, s50, 32768\nv_exp_f32 v230, v230\n"
"M 16,168,196\nR 162,188,28672\ns_add_u32 s63, s54, 1605888\nv_exp_f32 v231, v231\nM 16,172,212\nbuffer_load_dwordx4 v220, s[12:15], s63 offen lds\n"
"v_exp_f32 v232, v232\nM 8,168,200\nR 164,190,24576\nv_fma_f32 v230, v230, v216, v216\nv_exp_f32 v233, v233\nM 8,172,216\n"
"v_rcp_f32 v230, v230\nv_fma_f32 v231, v231, v216, v216\nM 24,168,204\nR 166,190,28672\nv_fma_f32 v232, v232, v216, v216\nv_fma_f32 v233, v233, v216, v216\n"
"M 24,172,220\nG 7\nv_rcp_f32 v231, v231\nv_rcp_f32 v232, v232\nM 28,176,204\nR 168,189,24576\n"
"v_mul_f32 v230, v96, v230\nv_rcp_f32 v233, v233\nM 28,180,220\nbuffer_load_dwordx4 v221, s[12:15], s63 offen offset:1024 lds\nv_mul_f32 v231, v97, v231\nM 12,176,200\n"
"R 170,189,28672\ns_add_u32 s63, s54, 1835264\nv_mul_f32 v232, v98, v232\nM 12,180,216\ns_add_u32 m0, s50, 40960\nv_mul_f32 v233, v99, v233\n"
"v_cvt_pk_bf16_f32 v96, v230, v231\nv_cvt_pk_bf16_f32 v97, v232, v233\nM 20,176,196\nR 172,191,24576\nG 7\nv_mfma_f32_16x16x32_bf16 v[0:3], v[238:241], v[208:211], v[0:3]\n"
"v_mul_f32 v234, v100, v100\nv_mul_f32 v235, v101, v101\nM 20,180,212\nbuffer_load_dwordx4 v220, s[12:15], s63 offen lds\nv_mul_f32 v236, v102, v102\nM 4,176,192\n"
"R 174,191,28672\nG 7\nv_mfma_f32_16x16x32_bf16 v[4:7], v[240:243], v[208:211], v[4:7]\nv_mul_f32 v237, v103, v103\nv_fma_f32 v234, v234, v215, v214\nM 4,180,208\n"
"G 4\nv_fma_f32 v235, v235, v215, v214\nv_fma_f32 v236, v236, v215, v214\nv_fma_f32 v237, v237, v215, v214\nM 0,160,224\nR 176,188,32768\n"
"v_mfma_f32_16x16x32_bf16 v[16:19], v[240:243], v[210:213], v[16:19]\nv_mul_f32 v234, v100, v234\nv_mul_f32 v235, v101, v235\nM 0,164,240\nbuffer_load_dwordx4 v221, s[12:15], s63 offen offset:1024 lds\nv_mul_f32 v236, v102, v236\n"
"M 16,160,228\nR 178,188,36864\nv_mfma_f32_16x16x32_bf16 v[20:23], v[238:241], v[210:213], v[20:23]\ns_add_u32 m0, s50, 49152\ns_add_u32 s63, s54, 2064640\nv_mul_f32 v237, v103, v237\n"
"M 16,164,244\nbuffer_load_dwordx4 v220, s[12:15], s63 offen lds\nv_exp_f32 v234, v234\nM 8,160,232\nR 180,188,40960\nv_fma_f32 v234, v234, v216, v216\n"
"v_exp_f32 v235, v235\nM 8,164,248\nv_exp_f32 v236, v236\nv_exp_f32 v237, v237\nM 24,160,236\nR 182,188,45056\n"
"v_fma_f32 v235, v235, v216, v216\nv_fma_f32 v236, v236, v216, v216\nM 24,164,252\nG 4\nv_rcp_f32 v234, v234\nv_fma_f32 v237, v237, v216, v216\n"
"M 28,168,236\nR 160,189,32768\nv_mul_f32 v234, v100, v234\nv_rcp_f32 v235, v235\nM 28,172,252\nbuffer_load_dwordx4 v221, s[12:15], s63 offen offset:1024 lds\n"
"v_rcp_f32 v236, v236\nM 12,168,232\nR 162,189,36864\ns_add_u32 s63, s54, 2294016\nv_rcp_f32 v237, v237\nM 12,172,248\n"
"s_add_u32 m0, s50, 57344\nv_mul_f32 v235, v101, v235\nv_mul_f32 v236, v102, v236\nv_mul_f32 v237, v103, v237\nM 20,168,228\nLpg11:\n"
"s_cbranch_execz Lpg15\nR 164,189,40960\nv_cvt_pk_bf16_f32 v98, v234, v235\nv_cvt_pk_bf16_f32 v99, v236, v237\nM 20,172,244\nbuffer_load_dwordx4 v220, s[12:15], s63 offen lds\n"
"v_mul_f32 v230, v104, v104\nM 4,168,224\nR 166,189,45056\nv_mul_f32 v231, v105, v105\nv_mul_f32 v232, v106, v106\nM 4,172,240\n"
"s_waitcnt vmcnt(17)\ns_barrier\nG 4\nv_mul_f32 v233, v107, v107\nv_mfma_f32_16x16x32_bf16 v[32:35], v[176:179], a[0:3], 0\nR 168,190,32768\n"
"v_fma_f32 v230, v230, v215, v214\nv_fma_f32 v231, v231, v215, v214\nM 32,180,16\ns_mov_b32 m0, s51\nv_fma_f32 v232, v232, v215, v214\nv_fma_f32 v233, v233, v215, v214\n"
"v_mul_f32 v230, v104, v230\nv_mfma_f32_16x16x32_bf16 v[48:51], v[176:179], a[4:7], 0\nR 170,190,36864\nds_write_addtid_b32 v8 offset:0\nv_mul_f32 v231, v105, v231\nv_mul_f32 v232, v106, v232\n"
"M 48,180,20\nbuffer_store_dwordx4 v[96:99], v252, s[20:23], s84 offen offset:0 sc1\nds_write_addtid_b32 v9 offset:256\nv_mul_f32 v233, v107, v233\nv_mfma_f32_16x16x32_bf16 v[64:67], v[176:179], a[8:11], 0\nR 172,190,40960\n"
"ds_write_addtid_b32 v10 offset:512\nv_mov_b32 v226, s46\nv_exp_f32 v230, v230\nM 64,180,24\nds_write_addtid_b32 v11 offset:768\nv_mov_b32 v227, s47\n"
"v_fma_f32 v230, v230, v216, v216\nv_exp_f32 v231, v231\nv_mfma_f32_16x16x32_bf16 v[80:83], v[176:179], a[12:15], 0\nR 174,190,45056\ns_add_u32 s61, s81, 385024\ns_lshl_b32 s62, s44, 5\n"
"s_add_u32 s61, s61, s62\ns_waitcnt vmcnt(11)\ns_mov_b64 exec, 1\nbuffer_atomic_swap_x2 v[226:227], off, s[20:23], s61 sc1\ns_mov_b64 exec, -1\nds_write_addtid_b32 v12 offset:1024\n"
"v_exp_f32 v232, v232\nM 80,180,28\nds_write_addtid_b32 v13 offset:1280\nG 10\nv_exp_f32 v233, v233\nv_fma_f32 v231, v231, v216, v216\n"
"v_mfma_f32_16x16x32_bf16 v[84:87], v[160:163], a[12:15], 0\nR 176,191,32768\nds_write_addtid_b32 v14 offset:1536\nv_fma_f32 v232, v232, v216, v216\nv_fma_f32 v233, v233, v216, v216\nM 84,164,28\n"
"ds_write_addtid_b32 v15 offset:1792\nv_rcp_f32 v230, v230\nv_rcp_f32 v231, v231\nv_mfma_f32_16x16x32_bf16 v[68:71], v[160:163], a[8:11], 0\nR 178,191,36864\nds_write_addtid_b32 v24 offset:4096\n"
"v_mul_f32 v230, v104, v230\nv_rcp_f32 v232, v232\nM 68,164,24\nds_write_addtid_b32 v25 offset:4352\nv_rcp_f32 v233, v233\nv_mul_f32 v231, v105, v231\n"
"v_mfma_f32_16x16x32_bf16 v[52:55], v[160:163], a[4:7], 0\nR 180,191,40960\nds_write_addtid_b32 v26 offset:4608\nv_mul_f32 v232, v106, v232\nv_mul_f32 v233, v107, v233\nM 52,164,20\n"
"ds_write_addtid_b32 v27 offset:4864\nds_write_addtid_b32 v28 offset:5120\nds_write_addtid_b32 v29 offset:5376\nds_write_addtid_b32 v30 offset:5632\nds_write_addtid_b32 v31 offset:5888\nv_cvt_pk_bf16_f32 v104, v230, v231\n"
"v_cvt_pk_bf16_f32 v105, v232, v233\nv_mul_f32 v234, v108, v108\nv_mfma_f32_16x16x32_bf16 v[36:39], v[160:163], a[0:3], 0\nR 182,191,45056\ns_add_u32 m0, s50, 57344\nv_mul_f32 v235, v109, v109\n"
"M 36,164,16\nG 15\nv_mul_f32 v236, v110, v110\nv_mul_f32 v237, v111, v111\nv_fma_f32 v234, v234, v215, v214\nv_mfma_f32_16x16x32_bf16 v[40:43], v[168:171], a[0:3], 0\n"
"R 160,188,49152\ns_waitcnt vmcnt(9) lgkmcnt(2)\ns_barrier\nv_fma_f32 v235, v235, v215, v214\nM 40,172,16\nbuffer_load_dwordx4 v221, s[12:15], s63 offen offset:1024 lds\n"
"ds_read2st64_b32 v[8:9], v225 offset0:0 offset1:1\nv_fma_f32 v236, v236, v215, v214\nv_mfma_f32_16x16x32_bf16 v[56:59], v[168:171], a[4:7], 0\nR 162,188,53248\nds_read2st64_b32 v[10:11], v225 offset0:2 offset1:3\nv_fma_f32 v237, v237, v215, v214\n"
"v_mul_f32 v234, v108, v234\nM 56,172,20\ns_add_u32 m0, s50, 65536\nds_read2st64_b32 v[12:13], v225 offset0:4 offset1:5\nv_exp_f32 v234, v234\nv_mul_f32 v235, v109, v235\n"
"v_mfma_f32_16x16x32_bf16 v[72:75], v[168:171], a[8:11], 0\nR 164,184,0\nds_read2st64_b32 v[14:15], v225 offset0:6 offset1:7\nv_mul_f32 v236, v110, v236\nv_mul_f32 v237, v111, v237\nM 72,172,24\n"
"s_add_u32 s63, s54, 2523392\nds_read2st64_b32 v[24:25], v225 offset0:16 offset1:17\nv_exp_f32 v235, v235\nv_exp_f32 v236, v236\nv_mfma_f32_16x16x32_bf16 v[88:91], v[168:171], a[12:15], 0\nR 166,184,4096\n"
"ds_read2st64_b32 v[26:27], v225 offset0:18 offset1:19\nv_fma_f32 v234, v234, v216, v216\nv_exp_f32 v237, v237\nM 88,172,28\nds_read2st64_b32 v[28:29], v225 offset0:20 offset1:21\nG 11\n"
"v_rcp_f32 v234, v234\nv_fma_f32 v235, v235, v216, v216\nv_mfma_f32_16x16x32_bf16 v[92:95], v[176:179], a[12:15], 0\nR 168,189,49152\nds_read2st64_b32 v[30:31], v225 offset0:22 offset1:23\nv_fma_f32 v236, v236, v216, v216\n"
"v_fma_f32 v237, v237, v216, v216\nM 92,180,28\nbuffer_load_dwordx4 v220, s[12:15], s63 offen lds\nG 11\nv_add_f32 v0, v0, v8\nv_rcp_f32 v235, v235\n"
"v_mfma_f32_16x16x32_bf16 v[76:79], v[176:179], a[8:11], 0\nR 170,189,53248\nv_mul_f32 v234, v108, v234\nv_add_f32 v1, v1, v9\nv_rcp_f32 v236, v236\nM 76,180,24\n"
"v_rcp_f32 v237, v237\nG 10\nv_add_f32 v2, v2, v10\nv_mul_f32 v235, v109, v235\nv_mfma_f32_16x16x32_bf16 v[60:63], v[176:179], a[4:7], 0\nR 172,185,0\n"
"v_mul_f32 v236, v110, v236\nv_add_f32 v3, v3, v11\nv_mul_f32 v237, v111, v237\nM 60,180,20\nv_cvt_pk_bf16_f32 v106, v234, v235\nv_cvt_pk_bf16_f32 v107, v236, v237\n"
"buffer_store_dwordx4 v[104:107], v252, s[20:23], s84 offen offset:1024 sc1\nG 10\nv_add_f32 v4, v4, v12\nv_mfma_f32_16x16x32_bf16 v[44:47], v[176:179], a[0:3], 0\nR 174,185,4096\nv_mul_f32 v230, v112, v112\n"
"v_add_f32 v5, v5, v13\nv_mul_f32 v231, v113, v113\nM 44,180,16\nG 7\nv_mul_f32 v232, v114, v114\nv_mul_f32 v233, v115, v115\n"
"v_add_f32 v6, v6, v14\nv_fma_f32 v230, v230, v218, v217\nM 32,160,32\nR 176,190,49152\nv_mov_b32 v248, 0\nv_add_f32 v7, v7, v15\n"
"v_fma_f32 v231, v231, v218, v217\nM 32,164,48\nv_mov_b32 v249, 0\nv_fma_f32 v232, v232, v218, v217\nv_add_f32 v16, v16, v24\nv_fma_f32 v233, v233, v218, v217\n"
"M 48,160,36\nR 178,190,53248\ns_add_u32 s61, s82, 385024\ns_mov_b64 exec, 0xf\nbuffer_atomic_or_x2 v[248:249], v250, s[20:23], s61 offen sc0 sc1\ns_mov_b64 exec, -1\n"
"v_add_f32 v17, v17, v25\nv_mul_f32 v230, v112, v230\nM 48,164,52\nv_exp_f32 v230, v230\nG 8\nv_add_f32 v18, v18, v26\n"
"v_mul_f32 v231, v113, v231\nM 64,160,40\nR 180,186,0\nv_mul_f32 v232, v114, v232\nv_add_f32 v19, v19, v27\nG 8\n"
"v_add_f32 v20, v20, v28\nv_add_f32 v21, v21, v29\nG 6\nv_add_f32 v22, v22, v30\nv_add_f32 v23, v23, v31\nv_mul_f32 v233, v115, v233\n"
"M 64,164,56\nbuffer_load_dwordx4 v221, s[12:15], s63 offen offset:1024 lds\nv_exp_f32 v231, v231\nM 80,160,44\nR 182,186,4096\nv_fma_f32 v230, v230, v219, v219\n"
"M 80,164,60\nG 4\nv_exp_f32 v232, v232\nM 84,168,44\nR 160,191,49152\nv_fma_f32 v231, v231, v219, v219\n"
"M 84,172,60\ns_add_u32 m0, s50, 73728\nv_exp_f32 v233, v233\nM 68,168,40\nR 162,191,53248\nv_fma_f32 v232, v232, v219, v219\n"
"M 68,172,56\ns_add_u32 s63, s54, 2752768\nv_rcp_f32 v230, v230\nM 52,168,36\nR 164,187,0\nv_fma_f32 v233, v233, v219, v219\n"
"M 52,172,52\nbuffer_load_dwordx4 v220, s[12:15], s63 offen lds\nM 36,168,32\nR 166,187,4096\nv_mul_f32 v230, v112, v230\nM 36,172,48\n"
"G 4\nv_rcp_f32 v231, v231\nM 40,176,32\nR 168,184,8192\nv_mul_f32 v231, v113, v231\nM 40,180,48\n"
"v_rcp_f32 v232, v232\nM 56,176,36\nR 170,184,12288\nv_mul_f32 v232, v114, v232\nM 56,180,52\nbuffer_load_dwordx4 v221, s[12:15], s63 offen offset:1024 lds\n"
"M 72,176,40\nR 172,184,16384\ns_add_u32 s63, s54, 2982144\nM 72,180,56\ns_add_u32 m0, s50, 81920\nv_rcp_f32 v233, v233\n"
"M 88,176,44\nR 174,184,20480\nv_mul_f32 v233, v115, v233\nM 88,180,60\nG 4\nv_cvt_pk_bf16_f32 v112, v230, v231\n"
"v_cvt_pk_bf16_f32 v113, v232, v233\nM 92,160,44\nR 176,185,8192\nv_mul_f32 v234, v116, v116\nM 92,164,60\nbuffer_load_dwordx4 v220, s[12:15], s63 offen lds\n"
"M 76,160,40\nR 178,185,12288\nv_mul_f32 v235, v117, v117\nM 76,164,56\nv_mul_f32 v236, v118, v118\nv_mul_f32 v237, v119, v119\n"
"M 60,160,36\nR 180,185,16384\nv_fma_f32 v234, v234, v218, v217\nM 60,164,52\nv_fma_f32 v235, v235, v218, v217\nv_fma_f32 v236, v236, v218, v217\n"
"M 44,160,32\nR 182,185,20480\nv_fma_f32 v237, v237, v218, v217\nM 44,164,48\nG 4\nv_mul_f32 v234, v116, v234\n"
"v_mul_f32 v235, v117, v235\nM 32,168,64\nR 160,186,8192\nv_mul_f32 v236, v118, v236\nM 32,172,80\nbuffer_load_dwordx4 v221, s[12:15], s63 offen offset:1024 lds\n"
"M 48,168,68\nR 162,186,12288\nv_mul_f32 v237, v119, v237\nM 48,172,84\ns_add_u32 m0, s50, 90112\nv_exp_f32 v234, v234\n"
"M 64,168,72\nR 164,186,16384\nv_fma_f32 v234, v234, v219, v219\nM 64,172,88\ns_add_u32 s63, s54, 3211520\nv_exp_f32 v235, v235\n"
"M 80,168,76\nR 166,186,20480\nv_fma_f32 v235, v235, v219, v219\nM 80,172,92\nG 4\nv_exp_f32 v236, v236\n"
"M 84,176,76\nR 168,187,8192\nv_fma_f32 v236, v236, v219, v219\nM 84,180,92\nbuffer_load_dwordx4 v220, s[12:15], s63 offen lds\nM 68,176,72\n"
"R 170,187,12288\nv_mul_f32 v230, v120, v120\nM 68,180,88\nv_exp_f32 v237, v237\nM 52,176,68\nR 172,187,16384\n"
"v_fma_f32 v237, v237, v219, v219\nM 52,180,84\ns_add_u32 s64, s68, 23068672\nv_rcp_f32 v234, v234\nM 36,176,64\nR 174,187,20480\n"
"v_mul_f32 v234, v116, v234\nM 36,180,80\nG 4\nv_rcp_f32 v235, v235\nM 40,160,64\nR 176,184,24576\n"
"s_waitcnt vmcnt(13)\ns_barrier\ns_waitcnt vmcnt(6)\nv_cmp_eq_u64 s[86:87], s[46:47], v[248:249]\nM 40,164,80\ns_add_u32 s65, s68, 23183360\n"
"v_rcp_f32 v236, v236\nM 56,160,68\nR 178,184,28672\ns_and_b32 s62, s86, 0xf\ns_cmp_eq_u32 s62, 0xf\ns_cbranch_scc0 Lq377\n"
"Lq378:\nM 56,164,84\nbuffer_load_dwordx4 v221, s[12:15], s63 offen offset:1024 lds\nM 72,160,72\nR 180,184,32768\nv_mul_f32 v235, v117, v235\n"
"M 72,164,88\nbuffer_load_dwordx4 a[0:3], v252, s[20:23], s64 offen sc1\nM 88,160,76\nR 182,184,36864\nv_mul_f32 v236, v118, v236\nM 88,164,92\n"
"G 4\nv_rcp_f32 v237, v237\nM 92,168,76\nR 160,185,24576\nv_mul_f32 v237, v119, v237\nM 92,172,92\n"
"buffer_load_dwordx4 a[4:7], v252, s[20:23], s65 offen sc1\nM 76,168,72\nR 162,185,28672\nv_cvt_pk_bf16_f32 v114, v234, v235\nM 76,172,88\ns_add_u32 s66, s69, 23068672\n"
"v_cvt_pk_bf16_f32 v115, v236, v237\nv_mul_f32 v231, v121, v121\nM 60,168,68\nR 164,185,32768\nv_mul_f32 v232, v122, v122\nM 60,172,84\n"
"s_add_u32 s67, s69, 23183360\nv_mul_f32 v233, v123, v123\nv_fma_f32 v230, v230, v218, v217\nM 44,168,64\nR 166,185,36864\nv_fma_f32 v231, v231, v218, v217\n"
"M 44,172,80\nG 4\nv_fma_f32 v232, v232, v218, v217\nv_fma_f32 v233, v233, v218, v217\nM 32,176,96\nR 168,186,24576\n"
"v_mul_f32 v230, v120, v230\nM 32,180,112\nbuffer_load_dwordx4 a[8:11], v252, s[20:23], s66 offen sc1\nM 48,176,100\nR 170,186,28672\nv_mul_f32 v231, v121, v231\n"
"M 48,180,116\nbuffer_load_dwordx4 a[12:15], v252, s[20:23], s67 offen sc1\nM 64,176,104\nR 172,186,32768\nv_mul_f32 v232, v122, v232\nM 64,180,120\n"
"s_add_u32 s64, s68, 23075840\nv_exp_f32 v230, v230\nM 80,176,108\nR 174,186,36864\nv_mul_f32 v233, v123, v233\nM 80,180,124\n"
"G 4\nv_exp_f32 v231, v231\nM 84,160,108\nR 176,187,24576\nv_fma_f32 v230, v230, v219, v219\nM 84,164,124\n"
"buffer_load_dwordx4 a[16:19], v252, s[20:23], s64 offen sc1\nM 68,160,104\nR 178,187,28672\nv_fma_f32 v231, v231, v219, v219\nM 68,164,120\ns_add_u32 s65, s68, 23190528\n"
"v_exp_f32 v232, v232\nM 52,160,100\nR 180,187,32768\nv_fma_f32 v232, v232, v219, v219\nM 52,164,116\ns_add_u32 s66, s69, 23075840\n"
"v_exp_f32 v233, v233\nM 36,160,96\nR 182,187,36864\nv_fma_f32 v233, v233, v219, v219\nM 36,164,112\nG 4\n"
"v_rcp_f32 v230, v230\nM 40,168,96\nR 160,184,40960\nv_mul_f32 v230, v120, v230\nM 40,172,112\nbuffer_load_dwordx4 a[20:23], v252, s[20:23], s65 offen sc1\n"
"M 56,168,100\nR 162,184,45056\nv_mul_f32 v234, v124, v124\nM 56,172,116\nbuffer_load_dwordx4 a[24:27], v252, s[20:23], s66 offen sc1\nM 72,168,104\n"
"R 164,184,49152\nv_mul_f32 v235, v125, v125\nM 72,172,120\ns_add_u32 s67, s69, 23190528\nv_rcp_f32 v231, v231\nM 88,168,108\n"
"R 166,184,53248\nv_mul_f32 v231, v121, v231\nM 88,172,124\nG 4\nv_rcp_f32 v232, v232\nM 92,176,108\n"
"R 168,185,40960\nv_mul_f32 v232, v122, v232\nM 92,180,124\nbuffer_load_dwordx4 a[28:31], v252, s[20:23], s67 offen sc1\nM 76,176,104\nR 170,185,45056\n"
"v_cvt_pk_bf16_f32 v120, v230, v231\nM 76,180,120\ns_add_u32 s64, s68, 23069696\nv_rcp_f32 v233, v233\nM 60,176,100\nR 172,185,49152\n"
"v_mul_f32 v233, v123, v233\nM 60,180,116\ns_add_u32 s65, s68, 23184384\nv_cvt_pk_bf16_f32 v121, v232, v233\nv_mul_f32 v236, v126, v126\nM 44,176,96\n"
"R 174,185,53248\nv_mul_f32 v237, v127, v127\nM 44,180,112\nG 4\nv_fma_f32 v234, v234, v218, v217\nv_fma_f32 v235, v235, v218, v217\n"
"M 32,160,128\nR 176,186,40960\ns_add_u32 m0, s50, 98304\nM 32,164,144\nbuffer_load_dwordx4 a[32:35], v252, s[20:23], s64 offen sc1\nM 48,160,132\n"
"R 178,186,45056\ns_add_u32 s63, s54, 3440896\nM 48,164,148\nbuffer_load_dwordx4 a[36:39], v252, s[20:23], s65 offen sc1\nM 64,160,136\nR 180,186,49152\n"
"v_fma_f32 v236, v236, v218, v217\nM 64,164,152\ns_add_u32 s66, s69, 23069696\nbuffer_load_dwordx4 v220, s[12:15], s63 offen lds\nM 80,160,140\nR 182,186,53248\n"
"v_fma_f32 v237, v237, v218, v217\nM 80,164,156\nG 4\nv_mul_f32 v234, v124, v234\nv_mul_f32 v235, v125, v235\nM 84,168,140\n"
"R 160,187,40960\nv_mul_f32 v236, v126, v236\nM 84,172,156\nbuffer_load_dwordx4 a[40:43], v252, s[20:23], s66 offen sc1\nbuffer_load_dwordx4 v221, s[12:15], s63 offen offset:1024 lds\nM 68,168,136\n"
"R 162,187,45056\nv_mul_f32 v237, v127, v237\nM 68,172,152\ns_add_u32 s67, s69, 23184384\nv_exp_f32 v234, v234\nM 52,168,132\n"
"R 164,187,49152\ns_waitcnt vmcnt(15)\ns_barrier\nM 52,172,148\nbuffer_load_dwordx4 a[44:47], v252, s[20:23], s67 offen sc1\ns_movk_i32 s63, 2048\n"
"v_mad_u32_u24 v220, v228, s63, v229\nv_add_u32 v221, 916480, v220\ns_add_u32 m0, s50, 106496\ns_add_u32 s63, s56, 0\nbuffer_load_dwordx4 v220, s[16:19], s63 offen lds\nM 36,168,128\n"
"R 166,187,53248\nv_fma_f32 v234, v234, v219, v219\nM 36,172,144\nG 4\nv_exp_f32 v235, v235\nM 40,176,128\n"
"R 168,184,57344\nv_fma_f32 v235, v235, v219, v219\nM 40,180,144\ns_add_u32 s64, s68, 23076864\nbuffer_load_dwordx4 v221, s[16:19], s63 offen offset:1024 lds\nM 56,176,132\n"
"R 170,184,61440\ns_add_u32 m0, s50, 114688\nM 56,180,148\nbuffer_load_dwordx4 a[48:51], v252, s[20:23], s64 offen sc1\nM 72,176,136\nR 172,188,0\n"
"s_add_u32 s63, s56, 524288\nM 72,180,152\ns_add_u32 s65, s68, 23191552\nbuffer_load_dwordx4 v220, s[16:19], s63 offen lds\nM 88,176,140\nR 174,188,4096\n"
"v_exp_f32 v236, v236\nM 88,180,156\nG 4\nv_exp_f32 v237, v237\nM 92,160,140\nR 176,185,57344\n"
"v_fma_f32 v236, v236, v219, v219\nM 92,164,156\nbuffer_load_dwordx4 a[52:55], v252, s[20:23], s65 offen sc1\nbuffer_load_dwordx4 v221, s[16:19], s63 offen offset:1024 lds\nM 76,160,136\nR 178,185,61440\n"
"v_fma_f32 v237, v237, v219, v219\nM 76,164,152\ns_add_u32 s66, s69, 23076864\nv_rcp_f32 v234, v234\nM 60,160,132\nR 180,189,0\n"
"v_mul_f32 v234, v124, v234\nM 60,164,148\nbuffer_load_dwordx4 a[56:59], v252, s[20:23], s66 offen sc1\nM 44,160,128\nR 182,189,4096\nM 44,164,144\n"
"G 4\nv_rcp_f32 v235, v235\nM 32,168,160\nR 160,186,57344\nv_mul_f32 v235, v125, v235\nM 32,172,176\n"
"s_add_u32 s67, s69, 23191552\nv_rcp_f32 v236, v236\nM 48,168,164\nR 162,186,61440\nv_mul_f32 v236, v126, v236\nM 48,172,180\n"
"buffer_load_dwordx4 a[60:63], v252, s[20:23], s67 offen sc1\nM 64,168,168\nR 164,190,0\nv_cvt_pk_bf16_f32 v122, v234, v235\nM 64,172,184\nbuffer_store_dwordx4 v[112:115], v252, s[20:23], s85 offen offset:0 sc1\n"
"M 80,168,172\nR 166,190,4096\nv_rcp_f32 v237, v237\nM 80,172,188\nG 4\nv_mul_f32 v237, v127, v237\n"
"v_cvt_pk_bf16_f32 v123, v236, v237\nM 84,176,172\nLpg12:\ns_cbranch_execz LretO0n\nR 168,187,57344\nM 84,180,188\n"
"s_add_u32 m0, s50, 0\nbuffer_store_dwordx4 v[120:123], v252, s[20:23], s85 offen offset:1024 sc1\nM 68,176,168\nR 170,187,61440\nv_mul_f32 v230, v0, v0\nv_mul_f32 v231, v1, v1\n"
"M 68,180,184\ns_add_u32 s63, s56, 65536\ns_add_u32 s84, s73, 23074816\ns_add_u32 s85, s73, 23189504\nv_mul_f32 v232, v2, v2\nM 52,176,164\n"
"R 172,191,0\nv_mul_f32 v233, v3, v3\nv_fma_f32 v230, v230, v215, v214\nM 52,180,180\nbuffer_load_dwordx4 v220, s[16:19], s63 offen lds\nv_fma_f32 v231, v231, v215, v214\n"
"M 36,176,160\nR 174,191,4096\nv_fma_f32 v232, v232, v215, v214\nv_fma_f32 v233, v233, v215, v214\nM 36,180,176\nG 4\n"
"v_mul_f32 v230, v0, v230\nv_mul_f32 v231, v1, v231\nv_mul_f32 v232, v2, v232\nM 40,160,160\nR 176,188,8192\nv_mul_f32 v233, v3, v233\n"
"v_exp_f32 v230, v230\nM 40,164,176\nv_exp_f32 v231, v231\nv_exp_f32 v232, v232\nM 56,160,164\nR 178,188,12288\n"
"v_fma_f32 v230, v230, v216, v216\nv_exp_f32 v233, v233\nM 56,164,180\nbuffer_load_dwordx4 v221, s[16:19], s63 offen offset:1024 lds\nv_fma_f32 v231, v231, v216, v216\nM 72,160,168\n"
"R 180,188,16384\ns_add_u32 s63, s56, 589824\nv_fma_f32 v232, v232, v216, v216\nM 72,164,184\ns_add_u32 m0, s50, 8192\nv_rcp_f32 v230, v230\n"
"v_fma_f32 v233, v233, v216, v216\nM 88,160,172\nR 182,188,20480\nv_mul_f32 v230, v0, v230\nv_rcp_f32 v231, v231\nM 88,164,188\n"
"G 4\nv_rcp_f32 v232, v232\nv_rcp_f32 v233, v233\nM 92,168,172\nR 160,189,8192\nv_mul_f32 v231, v1, v231\n"
"v_mul_f32 v232, v2, v232\nM 92,172,188\nbuffer_load_dwordx4 v220, s[16:19], s63 offen lds\nv_mul_f32 v233, v3, v233\nM 76,168,168\nR 162,189,12288\n"
"v_cvt_pk_bf16_f32 v0, v230, v231\nv_cvt_pk_bf16_f32 v1, v232, v233\nM 76,172,184\nv_mul_f32 v234, v4, v4\nv_mul_f32 v235, v5, v5\nv_mul_f32 v236, v6, v6\n"
"M 60,168,164\nR 164,189,16384\nv_mul_f32 v237, v7, v7\nv_fma_f32 v234, v234, v215, v214\nM 60,172,180\nbuffer_load_dwordx4 v221, s[16:19], s63 offen offset:1024 lds\n"
"v_fma_f32 v235, v235, v215, v214\nM 44,168,160\nR 166,189,20480\ns_add_u32 m0, s50, 16384\nv_fma_f32 v236, v236, v215, v214\nM 44,172,176\n"
"G 4\nv_fma_f32 v237, v237, v215, v214\nv_mul_f32 v234, v4, v234\nv_mul_f32 v235, v5, v235\nM 32,176,192\nR 168,190,8192\n"
"s_add_u32 s63, s57, 0\nv_mul_f32 v236, v6, v236\nM 32,180,208\nbuffer_load_dwordx4 v220, s[16:19], s63 offen lds\nv_mul_f32 v237, v7, v237\nM 48,176,196\n"
"R 170,190,12288\nv_mul_f32 v230, v16, v16\nv_exp_f32 v234, v234\nM 48,180,212\nv_exp_f32 v235, v235\nv_exp_f32 v236, v236\n"
"M 64,176,200\nR 172,190,16384\nv_fma_f32 v234, v234, v216, v216\nv_exp_f32 v237, v237\nM 64,180,216\nbuffer_load_dwordx4 v221, s[16:19], s63 offen offset:1024 lds\n"
"v_fma_f32 v235, v235, v216, v216\nM 80,176,204\nR 174,190,20480\ns_add_u32 s63, s57, 524288\nv_fma_f32 v236, v236, v216, v216\nM 80,180,220\n"
"G 4\nv_rcp_f32 v234, v234\nv_fma_f32 v237, v237, v216, v216\nM 84,160,204\nR 176,191,8192\ns_waitcnt vmcnt(10)\n"
"s_barrier\nv_rcp_f32 v235, v235\nM 84,164,220\ns_add_u32 m0, s50, 24576\nv_rcp_f32 v236, v236\nv_rcp_f32 v237, v237\n"
"M 68,160,200\nR 178,191,12288\nv_mul_f32 v234, v4, v234\nv_mul_f32 v235, v5, v235\nM 68,164,216\nbuffer_load_dwordx4 v220, s[16:19], s63 offen lds\n"
"v_mul_f32 v236, v6, v236\nM 52,160,196\nR 180,191,16384\nv_mul_f32 v237, v7, v237\nv_cvt_pk_bf16_f32 v2, v234, v235\nM 52,164,212\n"
"v_cvt_pk_bf16_f32 v3, v236, v237\nv_mul_f32 v231, v17, v17\nv_mul_f32 v232, v18, v18\nM 36,160,192\nR 182,191,20480\nv_mul_f32 v233, v19, v19\n"
"v_fma_f32 v230, v230, v218, v217\nM 36,164,208\nG 4\nv_fma_f32 v231, v231, v218, v217\nv_fma_f32 v232, v232, v218, v217\nv_fma_f32 v233, v233, v218, v217\n"
"M 40,168,192\nR 160,188,24576\nv_mul_f32 v230, v16, v230\nv_mul_f32 v231, v17, v231\nM 40,172,208\nbuffer_load_dwordx4 v221, s[16:19], s63 offen offset:1024 lds\n"
"v_mul_f32 v232, v18, v232\nM 56,168,196\nR 162,188,28672\nv_mul_f32 v233, v19, v233\nv_exp_f32 v230, v230\nM 56,172,212\n"
"buffer_store_dwordx4 v[0:3], v252, s[20:23], s84 offen offset:0 sc1\nv_exp_f32 v231, v231\nM 72,168,200\nR 164,188,32768\nv_fma_f32 v230, v230, v219, v219\nv_exp_f32 v232, v232\n"
"M 72,172,216\ns_add_u32 m0, s50, 32768\nv_exp_f32 v233, v233\nv_fma_f32 v231, v231, v219, v219\nM 88,168,204\nR 166,188,36864\n"
"v_mov_b32 v226, s46\nv_fma_f32 v232, v232, v219, v219\nM 88,172,220\nG 4\nv_mov_b32 v227, s47\nv_fma_f32 v233, v233, v219, v219\n"
"v_rcp_f32 v230, v230\nM 92,176,204\nR 168,189,24576\ns_add_u32 s61, s81, 385032\ns_lshl_b32 s62, s44, 5\ns_add_u32 s61, s61, s62\n"
"s_waitcnt vmcnt(9)\ns_mov_b64 exec, 1\nbuffer_atomic_swap_x2 v[226:227], off, s[20:23], s61 sc1\ns_mov_b64 exec, -1\nv_rcp_f32 v231, v231\nM 92,180,220\n"
"s_add_u32 s63, s57, 65536\nv_rcp_f32 v232, v232\nv_rcp_f32 v233, v233\nM 76,176,200\nR 170,189,28672\nv_mul_f32 v230, v16, v230\n"
"v_mul_f32 v231, v17, v231\nM 76,180,216\nbuffer_load_dwordx4 v220, s[16:19], s63 offen lds\nv_mul_f32 v232, v18, v232\nM 60,176,196\nR 172,189,32768\n"
"v_mul_f32 v233, v19, v233\nv_cvt_pk_bf16_f32 v16, v230, v231\nM 60,180,212\nv_cvt_pk_bf16_f32 v17, v232, v233\nv_mul_f32 v234, v20, v20\nv_mul_f32 v235, v21, v21\n"
"M 44,176,192\nR 174,189,36864\ns_mov_b32 s60, 158208\nv_bfe_u32 v246, v252, 4, 4\nv_lshl_add_u32 v246, v246, 3, s60\nv_mul_f32 v236, v22, v22\n"
"M 44,180,208\nds_read_b64 v[238:239], v246 offset:0\nG 5\nv_mul_f32 v237, v23, v23\nM 32,160,224\nR 176,190,24576\n"
"ds_read_b64 v[240:241], v246 offset:128\nv_fma_f32 v234, v234, v218, v217\nM 32,164,240\nds_read_b64 v[242:243], v246 offset:256\nv_fma_f32 v235, v235, v218, v217\nv_fma_f32 v236, v236, v218, v217\n"
"M 48,160,228\nR 178,190,28672\nds_read_b64 v[244:245], v246 offset:384\nv_fma_f32 v237, v237, v218, v217\nM 48,164,244\nds_read_b64 v[246:247], v246 offset:0\n"
"v_mul_f32 v234, v20, v234\nv_mul_f32 v235, v21, v235\nM 64,160,232\nR 180,190,32768\nv_mul_f32 v236, v22, v236\nv_mul_f32 v237, v23, v237\n"
"M 64,164,248\nbuffer_load_dwordx4 v221, s[16:19], s63 offen offset:1024 lds\nv_exp_f32 v234, v234\nM 80,160,236\nR 182,190,36864\ns_add_u32 m0, s50, 40960\n"
"v_exp_f32 v235, v235\nM 80,164,252\nG 9\nv_exp_f32 v236, v236\nv_exp_f32 v237, v237\nM 84,168,236\n"
"R 160,191,24576\ns_add_u32 s63, s57, 589824\nv_fma_f32 v234, v234, v219, v219\nM 84,172,252\nbuffer_load_dwordx4 v220, s[16:19], s63 offen lds\nv_fma_f32 v235, v235, v219, v219\n"
"M 68,168,232\nR 162,191,28672\nv_fma_f32 v236, v236, v219, v219\nv_fma_f32 v237, v237, v219, v219\nM 68,172,248\nv_rcp_f32 v234, v234\n"
"v_rcp_f32 v235, v235\nM 52,168,228\nR 164,191,32768\nG 9\nv_mfma_f32_16x16x32_bf16 v[32:35], v[238:241], v[208:211], v[32:35]\nv_mul_f32 v234, v20, v234\n"
"v_rcp_f32 v236, v236\nM 52,172,244\nbuffer_load_dwordx4 v221, s[16:19], s63 offen offset:1024 lds\nv_rcp_f32 v237, v237\nM 36,168,224\nR 166,191,36864\n"
"G 9\nv_mfma_f32_16x16x32_bf16 v[36:39], v[240:243], v[208:211], v[36:39]\ns_add_u32 m0, s50, 49152\nv_mul_f32 v235, v21, v235\nM 36,172,240\nG 4\n"
"v_mul_f32 v236, v22, v236\nv_mul_f32 v237, v23, v237\nv_cvt_pk_bf16_f32 v18, v234, v235\nM 40,176,224\nR 168,188,40960\nv_mfma_f32_16x16x32_bf16 v[40:43], v[242:245], v[208:211], v[40:43]\n"
"s_add_u32 s63, s56, 131072\nv_cvt_pk_bf16_f32 v19, v236, v237\nM 40,180,240\nbuffer_load_dwordx4 v220, s[16:19], s63 offen lds\nbuffer_store_dwordx4 v[16:19], v252, s[20:23], s85 offen offset:0 sc1\nM 56,176,228\n"
"R 170,188,45056\nv_mfma_f32_16x16x32_bf16 v[44:47], v[244:247], v[208:211], v[44:47]\nv_mov_b32 v248, 0\nM 56,180,244\nv_mov_b32 v249, 0\nM 72,176,232\n"
"R 172,188,49152\nv_mfma_f32_16x16x32_bf16 v[48:51], v[244:247], v[210:213], v[48:51]\ns_add_u32 s61, s82, 385032\ns_mov_b64 exec, 0xf\nbuffer_atomic_or_x2 v[248:249], v250, s[20:23], s61 offen sc0 sc1\ns_mov_b64 exec, -1\n"
"M 72,180,248\nM 88,176,236\nR 174,188,53248\nv_mfma_f32_16x16x32_bf16 v[52:55], v[238:241], v[210:213], v[52:55]\nM 88,180,252\nG 4\n"
"M 92,160,236\nR 176,189,40960\nv_mfma_f32_16x16x32_bf16 v[56:59], v[240:243], v[210:213], v[56:59]\nM 92,164,252\nbuffer_load_dwordx4 v221, s[16:19], s63 offen offset:1024 lds\nM 76,160,232\n"
"R 178,189,45056\nv_mfma_f32_16x16x32_bf16 v[60:63], v[242:245], v[210:213], v[60:63]\ns_add_u32 m0, s50, 57344\ns_add_u32 s63, s56, 655360\nM 76,164,248\nbuffer_load_dwordx4 v220, s[16:19], s63 offen lds\n"
"M 60,160,228\nR 180,189,49152\nM 60,164,244\nM 44,160,224\nR 182,189,53248\nM 44,164,240\n"
"s_waitcnt vmcnt(15)\ns_barrier\nG 4\nv_mfma_f32_16x16x32_bf16 v[96:99], v[168:171], a[0:3], 0\nR 160,190,40960\nM 96,172,16\n"
"s_mov_b32 m0, s51\nv_mfma_f32_16x16x32_bf16 v[112:115], v[168:171], a[4:7], 0\nR 162,190,45056\nM 112,172,20\nds_write_addtid_b32 v64 offset:0\nv_mfma_f32_16x16x32_bf16 v[128:131], v[168:171], a[8:11], 0\n"
"R 164,190,49152\nM 128,172,24\nds_write_addtid_b32 v65 offset:256\nv_mfma_f32_16x16x32_bf16 v[144:147], v[168:171], a[12:15], 0\nR 166,190,53248\nds_write_addtid_b32 v66 offset:512\n"
"M 144,172,28\nds_write_addtid_b32 v67 offset:768\nds_write_addtid_b32 v68 offset:1024\nG 9\nv_mfma_f32_16x16x32_bf16 v[148:151], v[176:179], a[12:15], 0\nR 168,191,40960\n"
"ds_write_addtid_b32 v69 offset:1280\nM 148,180,28\nds_write_addtid_b32 v70 offset:1536\nds_write_addtid_b32 v71 offset:1792\nv_mfma_f32_16x16x32_bf16 v[132:135], v[176:179], a[8:11], 0\nR 170,191,45056\n"
"ds_write_addtid_b32 v72 offset:2048\nM 132,180,24\nds_write_addtid_b32 v73 offset:2304\nds_write_addtid_b32 v74 offset:2560\nv_mfma_f32_16x16x32_bf16 v[116:119], v[176:179], a[4:7], 0\nR 172,191,49152\n"
"ds_write_addtid_b32 v75 offset:2816\nM 116,180,20\nds_write_addtid_b32 v76 offset:3072\nds_write_addtid_b32 v77 offset:3328\nv_mfma_f32_16x16x32_bf16 v[100:103], v[176:179], a[0:3], 0\nR 174,191,53248\n"
"ds_write_addtid_b32 v78 offset:3584\nM 100,180,16\nds_write_addtid_b32 v79 offset:3840\nds_write_addtid_b32 v80 offset:4096\nG 15\nv_mfma_f32_16x16x32_bf16 v[104:107], v[160:163], a[0:3], 0\n"
"R 176,184,0\nds_write_addtid_b32 v81 offset:4352\nM 104,164,16\nds_write_addtid_b32 v82 offset:4608\nds_write_addtid_b32 v83 offset:4864\nv_mfma_f32_16x16x32_bf16 v[120:123], v[160:163], a[4:7], 0\n"
"R 178,184,4096\nds_write_addtid_b32 v84 offset:5120\nM 120,164,20\nds_write_addtid_b32 v85 offset:5376\nds_write_addtid_b32 v86 offset:5632\nv_mfma_f32_16x16x32_bf16 v[136:139], v[160:163], a[8:11], 0\n"
"R 180,184,8192\nds_write_addtid_b32 v87 offset:5888\nM 136,164,24\nds_write_addtid_b32 v88 offset:6144\nds_write_addtid_b32 v89 offset:6400\nv_mfma_f32_16x16x32_bf16 v[152:155], v[160:163], a[12:15], 0\n"
"R 182,184,12288\nds_write_addtid_b32 v90 offset:6656\nM 152,164,28\nds_write_addtid_b32 v91 offset:6912\nds_write_addtid_b32 v92 offset:7168\nG 15\n"
"v_mfma_f32_16x16x32_bf16 v[156:159], v[168:171], a[12:15], 0\nR 160,185,0\nds_write_addtid_b32 v93 offset:7424\nM 156,172,28\nds_write_addtid_b32 v94 offset:7680\nds_write_addtid_b32 v95 offset:7936\n"
"v_mfma_f32_16x16x32_bf16 v[140:143], v[168:171], a[8:11], 0\nR 162,185,4096\ns_add_u32 m0, s50, 57344\nM 140,172,24\nbuffer_load_dwordx4 v221, s[16:19], s63 offen offset:1024 lds\nv_mfma_f32_16x16x32_bf16 v[124:127], v[168:171], a[4:7], 0\n"
"R 164,185,8192\ns_waitcnt vmcnt(6) lgkmcnt(2)\ns_barrier\nM 124,172,20\nds_read2st64_b32 v[64:65], v225 offset0:0 offset1:1\nds_read2st64_b32 v[66:67], v225 offset0:2 offset1:3\n"
"v_mfma_f32_16x16x32_bf16 v[108:111], v[168:171], a[0:3], 0\nR 166,185,12288\nds_read2st64_b32 v[68:69], v225 offset0:4 offset1:5\nM 108,172,16\nds_read2st64_b32 v[70:71], v225 offset0:6 offset1:7\nds_read2st64_b32 v[72:73], v225 offset0:8 offset1:9\n"
"M 96,176,32\nR 168,186,0\nds_read2st64_b32 v[74:75], v225 offset0:10 offset1:11\nM 96,180,48\ns_add_u32 s64, s68, 23070720\nds_read2st64_b32 v[76:77], v225 offset0:12 offset1:13\n"
"v_mov_b32 v226, s46\nM 112,176,36\nR 170,186,4096\nds_read2st64_b32 v[78:79], v225 offset0:14 offset1:15\nv_mov_b32 v227, s47\ns_waitcnt vmcnt(3)\n"
"v_cmp_eq_u64 s[86:87], s[46:47], v[248:249]\nM 112,180,52\ns_add_u32 s61, s81, 385048\ns_lshl_b32 s62, s44, 5\ns_add_u32 s61, s61, s62\ns_mov_b64 exec, 1\n"
"buffer_atomic_swap_x2 v[226:227], off, s[20:23], s61 sc1\ns_mov_b64 exec, -1\nds_read2st64_b32 v[80:81], v225 offset0:16 offset1:17\nM 128,176,40\nR 172,186,8192\ns_and_b32 s62, s86, 0xf\n"
"s_cmp_eq_u32 s62, 0xf\ns_cbranch_scc0 Lq412\nLq413:\nds_read2st64_b32 v[82:83], v225 offset0:18 offset1:19\nM 128,180,56\ns_add_u32 s65, s68, 23185408\n"
"ds_read2st64_b32 v[84:85], v225 offset0:20 offset1:21\nds_read2st64_b32 v[86:87], v225 offset0:22 offset1:23\nM 144,176,44\nR 174,186,12288\ns_add_u32 m0, s50, 65536\nds_read2st64_b32 v[88:89], v225 offset0:24 offset1:25\n"
"M 144,180,60\nds_read2st64_b32 v[90:91], v225 offset0:26 offset1:27\nds_read2st64_b32 v[92:93], v225 offset0:28 offset1:29\nG 15\nM 148,160,44\nR 176,187,0\n"
"s_add_u32 s63, s56, 196608\nds_read2st64_b32 v[94:95], v225 offset0:30 offset1:31\nM 148,164,60\nbuffer_load_dwordx4 a[64:67], v252, s[20:23], s64 offen sc1\nv_add_f32 v32, v32, v64\nM 132,160,40\n"
"R 178,187,4096\nv_add_f32 v33, v33, v65\nv_add_f32 v34, v34, v66\nM 132,164,56\ns_add_u32 s66, s69, 23070720\nv_add_f32 v35, v35, v67\n"
"v_add_f32 v36, v36, v68\nv_add_f32 v37, v37, v69\nM 116,160,36\nR 180,187,8192\nbuffer_load_dwordx4 v220, s[16:19], s63 offen lds\nv_add_f32 v38, v38, v70\n"
"M 116,164,52\nbuffer_load_dwordx4 a[68:71], v252, s[20:23], s65 offen sc1\nv_add_f32 v39, v39, v71\nM 100,160,32\nR 182,187,12288\nG 15\n"
"v_add_f32 v40, v40, v72\nv_add_f32 v41, v41, v73\nM 100,164,48\nG 8\nv_add_f32 v42, v42, v74\nv_add_f32 v43, v43, v75\n"
"v_add_f32 v44, v44, v76\nM 104,168,32\nR 160,184,16384\nbuffer_load_dwordx4 v221, s[16:19], s63 offen offset:1024 lds\nv_add_f32 v45, v45, v77\nM 104,172,48\n"
"s_add_u32 s67, s69, 23185408\nv_add_f32 v46, v46, v78\nv_add_f32 v47, v47, v79\nv_add_f32 v48, v48, v80\nM 120,168,36\nR 162,184,20480\n"
"v_add_f32 v49, v49, v81\nv_add_f32 v50, v50, v82\nM 120,172,52\nbuffer_load_dwordx4 a[72:75], v252, s[20:23], s66 offen sc1\nM 136,168,40\nR 164,184,24576\n"
"v_add_f32 v51, v51, v83\nM 136,172,56\nv_add_f32 v52, v52, v84\nv_add_f32 v53, v53, v85\nM 152,168,44\nR 166,184,28672\n"
"v_add_f32 v54, v54, v86\nM 152,172,60\nG 4\nv_add_f32 v55, v55, v87\nv_add_f32 v56, v56, v88\nM 156,176,44\n"
"R 168,185,16384\nv_add_f32 v57, v57, v89\nM 156,180,60\nbuffer_load_dwordx4 a[76:79], v252, s[20:23], s67 offen sc1\nM 140,176,40\nR 170,185,20480\n"
"v_add_f32 v58, v58, v90\nM 140,180,56\ns_add_u32 s64, s68, 23077888\nv_add_f32 v59, v59, v91\nv_add_f32 v60, v60, v92\nM 124,176,36\n"
"R 172,185,24576\nv_add_f32 v61, v61, v93\nM 124,180,52\ns_add_u32 s65, s68, 23192576\nv_add_f32 v62, v62, v94\nv_add_f32 v63, v63, v95\n"
"M 108,176,32\nR 174,185,28672\nv_mul_f32 v230, v32, v32\nv_mul_f32 v231, v33, v33\nM 108,180,48\ns_add_u32 s84, s73, 23072768\n"
"s_add_u32 s85, s73, 23187456\nG 4\nv_mul_f32 v232, v34, v34\nv_mfma_f32_16x16x32_bf16 v[0:3], v[160:163], a[0:3], 0\nR 176,186,16384\nLq577:\n"
"s_lshr_b32 s60, s45, 3\ns_lshl2_add_u32 s60, s60, s40\ns_lshl_b32 s60, s60, 8\nv_lshl_add_u32 v224, v252, 3, s60\nv_add_u32 v224, Lq577+4096-LqPC, v224\nv_min_u32 v224, LqE-LqPC-4, v224\n"
"s_mov_b64 exec, 3\nglobal_load_dword v224, v224, s[98:99]\ns_mov_b64 exec, -1\nv_mul_f32 v233, v35, v35\nM 0,164,16\nbuffer_load_dwordx4 a[80:83], v252, s[20:23], s64 offen sc1\n"
"v_fma_f32 v230, v230, v215, v214\nv_mfma_f32_16x16x32_bf16 v[16:19], v[160:163], a[4:7], 0\nR 178,186,20480\nv_fma_f32 v231, v231, v215, v214\nv_fma_f32 v232, v232, v215, v214\nM 16,164,20\n"
"s_add_u32 s66, s69, 23077888\nv_fma_f32 v233, v233, v215, v214\nv_mul_f32 v230, v32, v230\nv_mul_f32 v231, v33, v231\nv_mfma_f32_16x16x32_bf16 a[224:227], v[160:163], a[8:11], 0\nR 180,186,24576\n"
"v_mul_f32 v232, v34, v232\nv_mul_f32 v233, v35, v233\nv_mfma_f32_16x16x32_bf16 a[224:227], v[164:167], a[24:27], a[224:227]\nbuffer_load_dwordx4 a[84:87], v252, s[20:23], s65 offen sc1\nv_exp_f32 v230, v230\nv_mfma_f32_16x16x32_bf16 a[240:243], v[160:163], a[12:15], 0\n"
"R 182,186,28672\nv_fma_f32 v230, v230, v216, v216\nv_exp_f32 v231, v231\nv_mfma_f32_16x16x32_bf16 a[240:243], v[164:167], a[28:31], a[240:243]\nG 4\nv_exp_f32 v232, v232\n"
"v_exp_f32 v233, v233\nv_mfma_f32_16x16x32_bf16 a[244:247], v[168:171], a[12:15], 0\nR 160,187,16384\ns_add_u32 m0, s50, 73728\nv_fma_f32 v231, v231, v216, v216\nv_mfma_f32_16x16x32_bf16 a[244:247], v[172:175], a[28:31], a[244:247]\n"
"s_add_u32 s67, s69, 23192576\nv_rcp_f32 v230, v230\nv_fma_f32 v232, v232, v216, v216\nLpg13:\ns_cbranch_execz LretO0n\nv_mfma_f32_16x16x32_bf16 a[228:231], v[168:171], a[8:11], 0\n"
"R 162,187,20480\ns_add_u32 s63, s56, 720896\nv_fma_f32 v233, v233, v216, v216\nv_mfma_f32_16x16x32_bf16 a[228:231], v[172:175], a[24:27], a[228:231]\nbuffer_load_dwordx4 a[88:91], v252, s[20:23], s66 offen sc1\nv_rcp_f32 v231, v231\n"
"v_mfma_f32_16x16x32_bf16 v[20:23], v[168:171], a[4:7], 0\nR 164,187,24576\nv_mul_f32 v230, v32, v230\nv_rcp_f32 v232, v232\nM 20,172,20\nbuffer_load_dwordx4 v220, s[16:19], s63 offen lds\n"
"v_rcp_f32 v233, v233\nv_mfma_f32_16x16x32_bf16 v[4:7], v[168:171], a[0:3], 0\nR 166,187,28672\nv_mul_f32 v231, v33, v231\nv_mul_f32 v232, v34, v232\nM 4,172,16\n"
"G 4\nv_mul_f32 v233, v35, v233\nv_cvt_pk_bf16_f32 v32, v230, v231\nv_cvt_pk_bf16_f32 v33, v232, v233\nv_mfma_f32_16x16x32_bf16 v[8:11], v[176:179], a[0:3], 0\nR 168,184,32768\n"
"v_mul_f32 v234, v36, v36\nv_mul_f32 v235, v37, v37\nM 8,180,16\nbuffer_load_dwordx4 a[92:95], v252, s[20:23], s67 offen sc1\nbuffer_load_dwordx4 v221, s[16:19], s63 offen offset:1024 lds\nv_mul_f32 v236, v38, v38\n"
"v_mfma_f32_16x16x32_bf16 v[24:27], v[176:179], a[4:7], 0\nR 170,184,36864\ns_add_u32 m0, s50, 81920\nv_mul_f32 v237, v39, v39\nM 24,180,20\ns_add_u32 s64, s68, 23071744\n"
"v_fma_f32 v234, v234, v215, v214\nv_fma_f32 v235, v235, v215, v214\nv_fma_f32 v236, v236, v215, v214\nv_mfma_f32_16x16x32_bf16 a[232:235], v[176:179], a[8:11], 0\nR 172,184,40960\ns_add_u32 s63, s57, 131072\n"
"v_fma_f32 v237, v237, v215, v214\nv_mfma_f32_16x16x32_bf16 a[232:235], v[180:183], a[24:27], a[232:235]\ns_add_u32 s65, s68, 23186432\nbuffer_load_dwordx4 v220, s[16:19], s63 offen lds\nv_mul_f32 v234, v36, v234\nv_mfma_f32_16x16x32_bf16 a[248:251], v[176:179], a[12:15], 0\n"
"R 174,184,45056\nv_mul_f32 v235, v37, v235\nv_mul_f32 v236, v38, v236\nv_mfma_f32_16x16x32_bf16 a[248:251], v[180:183], a[28:31], a[248:251]\nG 4\nv_exp_f32 v234, v234\n"
"v_mul_f32 v237, v39, v237\nv_mfma_f32_16x16x32_bf16 a[252:255], v[160:163], a[12:15], 0\nR 176,185,32768\nv_fma_f32 v234, v234, v216, v216\nv_exp_f32 v235, v235\nv_mfma_f32_16x16x32_bf16 a[252:255], v[164:167], a[28:31], a[252:255]\n"
"buffer_load_dwordx4 a[96:99], v252, s[20:23], s64 offen sc1\nbuffer_load_dwordx4 v221, s[16:19], s63 offen offset:1024 lds\nv_exp_f32 v236, v236\nv_mfma_f32_16x16x32_bf16 a[236:239], v[160:163], a[8:11], 0\nR 178,185,36864\ns_add_u32 m0, s50, 90112\n"
"v_exp_f32 v237, v237\nv_mfma_f32_16x16x32_bf16 a[236:239], v[164:167], a[24:27], a[236:239]\ns_add_u32 s66, s69, 23071744\nv_rcp_f32 v234, v234\nv_fma_f32 v235, v235, v216, v216\nv_mfma_f32_16x16x32_bf16 v[28:31], v[160:163], a[4:7], 0\n"
"R 180,185,40960\ns_add_u32 s63, s57, 655360\nv_fma_f32 v236, v236, v216, v216\nM 28,164,20\nbuffer_load_dwordx4 a[100:103], v252, s[20:23], s65 offen sc1\nbuffer_load_dwordx4 v220, s[16:19], s63 offen lds\n"
"v_fma_f32 v237, v237, v216, v216\nv_mfma_f32_16x16x32_bf16 v[12:15], v[160:163], a[0:3], 0\nR 182,185,45056\nv_mul_f32 v234, v36, v234\nv_rcp_f32 v235, v235\nM 12,164,16\n"
"s_waitcnt vmcnt(14)\ns_barrier\nG 4\nv_rcp_f32 v236, v236\nM 0,168,32\nR 160,186,32768\n"
"v_mul_f32 v235, v37, v235\nv_rcp_f32 v237, v237\nM 0,172,48\ns_add_u32 s67, s69, 23186432\nbuffer_load_dwordx4 v221, s[16:19], s63 offen offset:1024 lds\nv_mul_f32 v236, v38, v236\n"
"M 16,168,36\nR 162,186,36864\nv_mul_f32 v237, v39, v237\nv_cvt_pk_bf16_f32 v34, v234, v235\nM 16,172,52\nbuffer_load_dwordx4 a[104:107], v252, s[20:23], s66 offen sc1\n"
"v_cvt_pk_bf16_f32 v35, v236, v237\nv_mfma_f32_16x16x32_bf16 a[224:227], v[168:171], a[40:43], a[224:227]\nR 164,186,40960\nv_mul_f32 v230, v40, v40\nv_mul_f32 v231, v41, v41\nv_mfma_f32_16x16x32_bf16 a[224:227], v[172:175], a[56:59], a[224:227]\n"
"v_mul_f32 v232, v42, v42\nv_mul_f32 v233, v43, v43\nv_fma_f32 v230, v230, v215, v214\nv_mfma_f32_16x16x32_bf16 a[240:243], v[168:171], a[44:47], a[240:243]\nR 166,186,45056\nv_fma_f32 v231, v231, v215, v214\n"
"v_fma_f32 v232, v232, v215, v214\nv_mfma_f32_16x16x32_bf16 a[240:243], v[172:175], a[60:63], a[240:243]\nG 4\nv_fma_f32 v233, v233, v215, v214\nv_mul_f32 v230, v40, v230\nv_mul_f32 v231, v41, v231\n"
"v_mfma_f32_16x16x32_bf16 a[244:247], v[176:179], a[44:47], a[244:247]\nR 168,187,32768\nv_mul_f32 v232, v42, v232\nv_mul_f32 v233, v43, v233\nv_mfma_f32_16x16x32_bf16 a[244:247], v[180:183], a[60:63], a[244:247]\nbuffer_load_dwordx4 a[108:111], v252, s[20:23], s67 offen sc1\n"
"v_exp_f32 v230, v230\nv_mfma_f32_16x16x32_bf16 a[228:231], v[176:179], a[40:43], a[228:231]\nR 170,187,36864\nv_fma_f32 v230, v230, v216, v216\nv_exp_f32 v231, v231\nv_mfma_f32_16x16x32_bf16 a[228:231], v[180:183], a[56:59], a[228:231]\n"
"s_add_u32 s64, s68, 23078912\nv_exp_f32 v232, v232\nv_exp_f32 v233, v233\nM 20,176,36\nR 172,187,40960\nv_fma_f32 v231, v231, v216, v216\n"
"v_fma_f32 v232, v232, v216, v216\nM 20,180,52\ns_add_u32 s65, s68, 23193600\nv_rcp_f32 v230, v230\nv_fma_f32 v233, v233, v216, v216\nM 4,176,32\n"
"R 174,187,45056\ns_add_u32 m0, s50, 98304\nv_rcp_f32 v231, v231\nM 4,180,48\nG 4\nv_rcp_f32 v232, v232\n"
"v_rcp_f32 v233, v233\nM 8,160,32\nR 176,184,49152\ns_add_u32 s63, s57, 196608\nv_mul_f32 v230, v40, v230\nM 8,164,48\n"
"buffer_load_dwordx4 a[112:115], v252, s[20:23], s64 offen sc1\nv_mul_f32 v231, v41, v231\nM 24,160,36\nR 178,184,53248\nv_mul_f32 v232, v42, v232\nv_mul_f32 v233, v43, v233\n"
"M 24,164,52\ns_add_u32 s66, s69, 23078912\nbuffer_load_dwordx4 v220, s[16:19], s63 offen lds\nv_cvt_pk_bf16_f32 v40, v230, v231\nv_mfma_f32_16x16x32_bf16 a[232:235], v[160:163], a[40:43], a[232:235]\nR 180,184,57344\n"
"v_cvt_pk_bf16_f32 v41, v232, v233\nv_mul_f32 v234, v44, v44\nv_mfma_f32_16x16x32_bf16 a[232:235], v[164:167], a[56:59], a[232:235]\nbuffer_load_dwordx4 a[116:119], v252, s[20:23], s65 offen sc1\nv_mul_f32 v235, v45, v45\nv_mfma_f32_16x16x32_bf16 a[248:251], v[160:163], a[44:47], a[248:251]\n"
"R 182,184,61440\nv_mul_f32 v236, v46, v46\nv_mul_f32 v237, v47, v47\nv_mfma_f32_16x16x32_bf16 a[248:251], v[164:167], a[60:63], a[248:251]\nbuffer_load_dwordx4 v221, s[16:19], s63 offen offset:1024 lds\nG 4\n"
"v_fma_f32 v234, v234, v215, v214\nv_mfma_f32_16x16x32_bf16 a[252:255], v[168:171], a[44:47], a[252:255]\nR 160,185,49152\ns_add_u32 m0, s50, 106496\nv_fma_f32 v235, v235, v215, v214\nv_mfma_f32_16x16x32_bf16 a[252:255], v[172:175], a[60:63], a[252:255]\n"
"s_add_u32 s67, s69, 23193600\ns_add_u32 s63, s57, 720896\nv_fma_f32 v236, v236, v215, v214\nv_fma_f32 v237, v237, v215, v214\nv_mfma_f32_16x16x32_bf16 a[236:239], v[168:171], a[40:43], a[236:239]\nR 162,185,53248\n"
"v_mov_b32 v248, 0\nv_mul_f32 v234, v44, v234\nv_mfma_f32_16x16x32_bf16 a[236:239], v[172:175], a[56:59], a[236:239]\nbuffer_load_dwordx4 a[120:123], v252, s[20:23], s66 offen sc1\nbuffer_load_dwordx4 v220, s[16:19], s63 offen lds\nv_mov_b32 v249, 0\n"
"v_mul_f32 v235, v45, v235\nM 28,168,36\nR 164,185,57344\ns_add_u32 s61, s82, 385048\ns_mov_b64 exec, 0xf\nbuffer_atomic_or_x2 v[248:249], v250, s[20:23], s61 offen sc0 sc1\n"
"s_mov_b64 exec, -1\nv_mul_f32 v236, v46, v236\nM 28,172,52\nbuffer_load_dwordx4 v221, s[16:19], s63 offen offset:1024 lds\nv_mul_f32 v237, v47, v237\nM 12,168,32\n"
"R 166,185,61440\ns_add_u32 m0, s50, 114688\nv_exp_f32 v234, v234\nM 12,172,48\nG 4\nv_exp_f32 v235, v235\n"
"v_exp_f32 v236, v236\nM 96,176,64\nR 168,186,49152\ns_add_u32 s63, s58, 0\nv_exp_f32 v237, v237\ns_waitcnt vmcnt(21)\n"
"M 96,180,80\nbuffer_load_dwordx4 a[124:127], v252, s[20:23], s67 offen sc1\nv_fma_f32 v234, v234, v216, v216\nM 112,176,68\nR 170,186,53248\nv_fma_f32 v235, v235, v216, v216\n"
"v_fma_f32 v236, v236, v216, v216\ns_waitcnt vmcnt(21)\nM 112,180,84\nbuffer_load_dwordx4 v220, s[16:19], s63 offen lds\nv_fma_f32 v237, v237, v216, v216\nM 128,176,72\n"
"R 172,186,57344\nv_mul_f32 v230, v48, v48\nv_rcp_f32 v234, v234\ns_waitcnt vmcnt(21)\nM 128,180,88\nbuffer_load_dwordx4 v221, s[16:19], s63 offen offset:1024 lds\n"
"v_rcp_f32 v235, v235\nM 144,176,76\nR 174,186,61440\ns_add_u32 m0, s50, 0\nv_rcp_f32 v236, v236\ns_waitcnt vmcnt(20)\n"
"M 144,180,92\nG 4\nv_rcp_f32 v237, v237\nv_mul_f32 v234, v44, v234\nM 148,160,76\nR 176,187,49152\n"
"s_add_u32 s63, s58, 524288\nv_mul_f32 v235, v45, v235\nM 148,164,92\nbuffer_load_dwordx4 v220, s[16:19], s63 offen lds\nv_mul_f32 v236, v46, v236\nM 132,160,72\n"
"R 178,187,53248\nv_mul_f32 v237, v47, v237\nv_cvt_pk_bf16_f32 v42, v234, v235\nM 132,164,88\nbuffer_store_dwordx4 v[32:35], v252, s[20:23], s84 offen offset:0 sc1\nv_cvt_pk_bf16_f32 v43, v236, v237\n"
"M 116,160,68\nR 180,187,57344\nv_mul_f32 v231, v49, v49\nv_mul_f32 v232, v50, v50\nM 116,164,84\nbuffer_load_dwordx4 v221, s[16:19], s63 offen offset:1024 lds\n"
"v_mul_f32 v233, v51, v51\nM 100,160,64\nR 182,187,61440\nv_fma_f32 v230, v230, v218, v217\nv_fma_f32 v231, v231, v218, v217\nM 100,164,80\n"
"G 4\nv_fma_f32 v232, v232, v218, v217\nv_fma_f32 v233, v233, v218, v217\nv_mul_f32 v230, v48, v230\nM 104,168,64\nR 160,188,0\n"
"s_waitcnt vmcnt(16)\ns_barrier\nv_mul_f32 v231, v49, v231\nM 104,172,80\nbuffer_store_dwordx4 v[40:43], v252, s[20:23], s84 offen offset:1024 sc1\nv_mul_f32 v232, v50, v232\n"
"M 120,168,68\nR 162,188,4096\nv_mul_f32 v233, v51, v233\nv_exp_f32 v230, v230\nM 120,172,84\ns_add_u32 m0, s50, 8192\n"
"v_exp_f32 v231, v231\nv_exp_f32 v232, v232\nM 136,168,72\nR 164,188,8192\nv_fma_f32 v230, v230, v219, v219\nv_exp_f32 v233, v233\n"
"M 136,172,88\ns_add_u32 s63, s58, 65536\nv_rcp_f32 v230, v230\nv_fma_f32 v231, v231, v219, v219\nM 152,168,76\nR 166,188,12288\n"
"v_fma_f32 v232, v232, v219, v219\nv_fma_f32 v233, v233, v219, v219\nM 152,172,92\nG 4\nv_rcp_f32 v231, v231\nv_rcp_f32 v232, v232\n"
"M 156,176,76\nR 168,189,0\nv_mul_f32 v230, v48, v230\nv_rcp_f32 v233, v233\nM 156,180,92\nbuffer_load_dwordx4 v220, s[16:19], s63 offen lds\n"
"v_mul_f32 v231, v49, v231\nM 140,176,72\nR 170,189,4096\nv_mul_f32 v232, v50, v232\nv_mul_f32 v233, v51, v233\nM 140,180,88\n"
"v_cvt_pk_bf16_f32 v48, v230, v231\nv_cvt_pk_bf16_f32 v49, v232, v233\nv_mul_f32 v234, v52, v52\nM 124,176,68\nR 172,189,8192\nv_mul_f32 v235, v53, v53\n"
"v_mul_f32 v236, v54, v54\nM 124,180,84\nbuffer_load_dwordx4 v221, s[16:19], s63 offen offset:1024 lds\nv_mul_f32 v237, v55, v55\nM 108,176,64\nR 174,189,12288\n"
"s_add_u32 m0, s50, 16384\nv_fma_f32 v234, v234, v218, v217\nM 108,180,80\nG 4\nv_fma_f32 v235, v235, v218, v217\nv_fma_f32 v236, v236, v218, v217\n"
"v_fma_f32 v237, v237, v218, v217\nM 96,160,96\nR 176,190,0\ns_add_u32 s63, s58, 589824\nv_mul_f32 v234, v52, v234\ns_waitcnt vmcnt(16)\n"
"M 96,164,112\nbuffer_load_dwordx4 v220, s[16:19], s63 offen lds\nv_mul_f32 v235, v53, v235\nM 112,160,100\nR 178,190,4096\nv_mul_f32 v236, v54, v236\n"
"v_mul_f32 v237, v55, v237\ns_waitcnt vmcnt(15)\nM 112,164,116\nv_exp_f32 v234, v234\nv_exp_f32 v235, v235\nM 128,160,104\n"
"R 180,190,8192\nv_fma_f32 v234, v234, v219, v219\nv_exp_f32 v236, v236\ns_waitcnt vmcnt(13)\nM 128,164,120\nv_exp_f32 v237, v237\n"
"v_fma_f32 v235, v235, v219, v219\nM 144,160,108\nR 182,190,12288\nv_fma_f32 v236, v236, v219, v219\nv_fma_f32 v237, v237, v219, v219\ns_waitcnt vmcnt(9)\n"
"M 144,164,124\nG 4\nv_rcp_f32 v234, v234\nv_rcp_f32 v235, v235\nM 148,168,108\nR 160,191,0\n"
"v_mul_f32 v234, v52, v234\nv_rcp_f32 v236, v236\nM 148,172,124\ns_add_u32 s64, s68, 23074816\nv_rcp_f32 v237, v237\nv_mul_f32 v235, v53, v235\n"
"M 132,168,104\nR 162,191,4096\nv_cmp_eq_u64 s[86:87], s[46:47], v[248:249]\nv_mul_f32 v236, v54, v236\nM 132,172,120\nbuffer_load_dwordx4 v221, s[16:19], s63 offen offset:1024 lds\n"
"v_mul_f32 v237, v55, v237\nM 116,168,100\nR 164,191,8192\ns_and_b32 s62, s86, 0xf\ns_cmp_eq_u32 s62, 0xf\ns_cbranch_scc0 Lq447\n"
"Lq448:\nM 116,172,116\ns_add_u32 s65, s68, 23189504\nv_cvt_pk_bf16_f32 v50, v234, v235\nv_cvt_pk_bf16_f32 v51, v236, v237\nM 100,168,96\n"
"R 166,191,12288\nv_mul_f32 v230, v56, v56\nM 100,172,112\nG 4\nv_mul_f32 v231, v57, v57\nv_mul_f32 v232, v58, v58\n"
"M 104,176,96\nR 168,188,16384\nv_mul_f32 v233, v59, v59\nM 104,180,112\nbuffer_load_dwordx4 a[128:131], v252, s[20:23], s64 offen sc1\nM 120,176,100\n"
"R 170,188,20480\nv_fma_f32 v230, v230, v218, v217\nM 120,180,116\ns_add_u32 s66, s69, 23074816\nv_fma_f32 v231, v231, v218, v217\nv_fma_f32 v232, v232, v218, v217\n"
"M 136,176,104\nR 172,188,24576\nv_fma_f32 v233, v233, v218, v217\nM 136,180,120\nbuffer_load_dwordx4 a[132:135], v252, s[20:23], s65 offen sc1\nM 152,176,108\n"
"R 174,188,28672\nv_mul_f32 v230, v56, v230\nM 152,180,124\nG 4\nv_exp_f32 v230, v230\nM 156,160,108\n"
"R 176,189,16384\nv_mul_f32 v231, v57, v231\nM 156,164,124\ns_add_u32 s67, s69, 23189504\nv_exp_f32 v231, v231\nM 140,160,104\n"
"R 178,189,20480\nv_mul_f32 v232, v58, v232\nM 140,164,120\nbuffer_load_dwordx4 a[136:139], v252, s[20:23], s66 offen sc1\nM 124,160,100\nR 180,189,24576\n"
"v_mul_f32 v233, v59, v233\nM 124,164,116\nbuffer_store_dwordx4 v[48:51], v252, s[20:23], s85 offen offset:0 sc1\nM 108,160,96\nR 182,189,28672\ns_add_u32 m0, s50, 24576\n"
"M 108,164,112\ns_waitcnt vmcnt(9)\ns_barrier\nG 4\nM 0,168,64\nR 160,190,16384\n"
"s_add_u32 s63, s58, 131072\nM 0,172,80\nbuffer_load_dwordx4 a[140:143], v252, s[20:23], s67 offen sc1\nM 16,168,68\nR 162,190,20480\nv_fma_f32 v230, v230, v219, v219\n"
"M 16,172,84\ns_add_u32 s64, s68, 23081984\nv_exp_f32 v232, v232\nv_mfma_f32_16x16x32_bf16 a[224:227], v[168:171], a[72:75], a[224:227]\nR 164,190,24576\nbuffer_load_dwordx4 v220, s[16:19], s63 offen lds\n"
"v_mfma_f32_16x16x32_bf16 a[224:227], v[172:175], a[88:91], a[224:227]\ns_add_u32 s65, s68, 23196672\nv_exp_f32 v233, v233\nv_mfma_f32_16x16x32_bf16 a[240:243], v[168:171], a[76:79], a[240:243]\nR 166,190,28672\nv_fma_f32 v231, v231, v219, v219\n"
"v_mfma_f32_16x16x32_bf16 a[240:243], v[172:175], a[92:95], a[240:243]\nG 4\nv_rcp_f32 v230, v230\nv_mfma_f32_16x16x32_bf16 a[244:247], v[176:179], a[76:79], a[244:247]\nR 168,191,16384\nbuffer_load_dwordx4 v221, s[16:19], s63 offen offset:1024 lds\n"
"v_mfma_f32_16x16x32_bf16 a[244:247], v[180:183], a[92:95], a[244:247]\nbuffer_load_dwordx4 a[144:147], v252, s[20:23], s64 offen sc1\nv_mfma_f32_16x16x32_bf16 a[228:231], v[176:179], a[72:75], a[228:231]\nR 170,191,20480\ns_add_u32 m0, s50, 32768\nv_mfma_f32_16x16x32_bf16 a[228:231], v[180:183], a[88:91], a[228:231]\n"
"s_add_u32 s66, s69, 23081984\ns_add_u32 s63, s58, 655360\nv_fma_f32 v232, v232, v219, v219\nM 20,176,68\nR 172,191,24576\nbuffer_load_dwordx4 v220, s[16:19], s63 offen lds\n"
"M 20,180,84\nbuffer_load_dwordx4 a[148:151], v252, s[20:23], s65 offen sc1\nM 4,176,64\nR 174,191,28672\nv_fma_f32 v233, v233, v219, v219\nM 4,180,80\n"
"G 4\nv_rcp_f32 v231, v231\nM 8,160,64\nR 176,188,32768\nbuffer_load_dwordx4 v221, s[16:19], s63 offen offset:1024 lds\nM 8,164,80\n"
"s_add_u32 s67, s69, 23196672\nv_rcp_f32 v232, v232\nM 24,160,68\nR 178,188,36864\nv_mul_f32 v230, v56, v230\nM 24,164,84\n"
"buffer_load_dwordx4 a[152:155], v252, s[20:23], s66 offen sc1\nv_mfma_f32_16x16x32_bf16 a[232:235], v[160:163], a[72:75], a[232:235]\nR 180,188,40960\nv_mul_f32 v231, v57, v231\nv_mfma_f32_16x16x32_bf16 a[232:235], v[164:167], a[88:91], a[232:235]\nv_rcp_f32 v233, v233\n"
"v_mfma_f32_16x16x32_bf16 a[248:251], v[160:163], a[76:79], a[248:251]\nR 182,188,45056\nv_mul_f32 v232, v58, v232\nv_mfma_f32_16x16x32_bf16 a[248:251], v[164:167], a[92:95], a[248:251]\nG 4\nv_mul_f32 v233, v59, v233\n"
"v_cvt_pk_bf16_f32 v56, v230, v231\nv_mfma_f32_16x16x32_bf16 a[252:255], v[168:171], a[76:79], a[252:255]\nR 160,189,32768\nv_cvt_pk_bf16_f32 v57, v232, v233\nv_mfma_f32_16x16x32_bf16 a[252:255], v[172:175], a[92:95], a[252:255]\nbuffer_load_dwordx4 a[156:159], v252, s[20:23], s67 offen sc1\n"
"v_mfma_f32_16x16x32_bf16 a[236:239], v[168:171], a[72:75], a[236:239]\nR 162,189,36864\nv_mul_f32 v234, v60, v60\nv_mfma_f32_16x16x32_bf16 a[236:239], v[172:175], a[88:91], a[236:239]\nv_mul_f32 v235, v61, v61\nv_mul_f32 v236, v62, v62\n"
"M 28,168,68\nR 164,189,40960\nv_mul_f32 v237, v63, v63\nM 28,172,84\ns_add_u32 m0, s50, 40960\nv_fma_f32 v234, v234, v218, v217\n"
"v_fma_f32 v235, v235, v218, v217\nM 12,168,64\nR 166,189,45056\nv_fma_f32 v236, v236, v218, v217\nM 12,172,80\nG 4\n"
"v_fma_f32 v237, v237, v218, v217\nv_mul_f32 v234, v60, v234\nM 0,176,96\nR 168,190,32768\nv_mul_f32 v235, v61, v235\nM 0,180,112\n"
"s_add_u32 s63, s58, 196608\nv_exp_f32 v234, v234\nM 16,176,100\nR 170,190,36864\nv_mul_f32 v236, v62, v236\nM 16,180,116\n"
"buffer_load_dwordx4 v220, s[16:19], s63 offen lds\nv_mfma_f32_16x16x32_bf16 a[224:227], v[176:179], a[104:107], a[224:227]\nR 172,190,40960\nv_mul_f32 v237, v63, v237\nv_mfma_f32_16x16x32_bf16 a[224:227], v[180:183], a[120:123], a[224:227]\nv_exp_f32 v235, v235\n"
"v_mfma_f32_16x16x32_bf16 a[240:243], v[176:179], a[108:111], a[240:243]\nR 174,190,45056\nv_fma_f32 v234, v234, v219, v219\nv_mfma_f32_16x16x32_bf16 a[240:243], v[180:183], a[124:127], a[240:243]\nG 4\nv_exp_f32 v236, v236\n"
"v_mfma_f32_16x16x32_bf16 a[244:247], v[160:163], a[108:111], a[244:247]\nR 176,191,32768\nv_fma_f32 v235, v235, v219, v219\nv_mfma_f32_16x16x32_bf16 a[244:247], v[164:167], a[124:127], a[244:247]\nbuffer_load_dwordx4 v221, s[16:19], s63 offen offset:1024 lds\nv_mfma_f32_16x16x32_bf16 a[228:231], v[160:163], a[104:107], a[228:231]\n"
"R 178,191,36864\ns_add_u32 s63, s58, 720896\nv_mfma_f32_16x16x32_bf16 a[228:231], v[164:167], a[120:123], a[228:231]\ns_add_u32 m0, s50, 49152\nv_exp_f32 v237, v237\nM 20,160,100\n"
"R 180,191,40960\nv_fma_f32 v236, v236, v219, v219\nM 20,164,116\nbuffer_load_dwordx4 v220, s[16:19], s63 offen lds\nM 4,160,96\nR 182,191,45056\n"
"v_fma_f32 v237, v237, v219, v219\nM 4,164,112\nG 4\nv_rcp_f32 v234, v234\nM 8,168,96\nR 160,188,49152\n"
"v_mul_f32 v234, v60, v234\nM 8,172,112\nbuffer_load_dwordx4 v221, s[16:19], s63 offen offset:1024 lds\nM 24,168,100\nR 162,188,53248\ns_add_u32 s63, s58, 393216\n"
"M 24,172,116\ns_add_u32 m0, s50, 57344\nv_rcp_f32 v235, v235\nv_mfma_f32_16x16x32_bf16 a[232:235], v[168:171], a[104:107], a[232:235]\nLpg14:\ns_cbranch_execz LretO0n\n"
"R 164,184,0\nv_mul_f32 v235, v61, v235\nv_mfma_f32_16x16x32_bf16 a[232:235], v[172:175], a[120:123], a[232:235]\nbuffer_load_dwordx4 v220, s[16:19], s63 offen lds\nv_mfma_f32_16x16x32_bf16 a[248:251], v[168:171], a[108:111], a[248:251]\nR 166,184,4096\n"
"v_cvt_pk_bf16_f32 v58, v234, v235\nv_mfma_f32_16x16x32_bf16 a[248:251], v[172:175], a[124:127], a[248:251]\nG 4\nv_rcp_f32 v236, v236\nv_mfma_f32_16x16x32_bf16 a[252:255], v[176:179], a[108:111], a[252:255]\nR 168,189,49152\n"
"v_mul_f32 v236, v62, v236\nv_mfma_f32_16x16x32_bf16 a[252:255], v[180:183], a[124:127], a[252:255]\nbuffer_load_dwordx4 v221, s[16:19], s63 offen offset:1024 lds\nv_mfma_f32_16x16x32_bf16 a[236:239], v[176:179], a[104:107], a[236:239]\nR 170,189,53248\nv_rcp_f32 v237, v237\n"
"v_mfma_f32_16x16x32_bf16 a[236:239], v[180:183], a[120:123], a[236:239]\ns_add_u32 m0, s50, 65536\nv_mul_f32 v237, v63, v237\nv_cvt_pk_bf16_f32 v59, v236, v237\nM 28,176,100\nR 172,185,0\n"
"M 28,180,116\ns_add_u32 s63, s58, 458752\nbuffer_store_dwordx4 v[56:59], v252, s[20:23], s85 offen offset:1024 sc1\nM 12,176,96\nR 174,185,4096\nM 12,180,112\n"
"s_waitcnt vmcnt(9)\ns_barrier\nG 4\nv_mfma_f32_16x16x32_bf16 v[32:35], v[160:163], a[0:3], 0\nR 176,190,49152\nM 32,164,16\n"
"buffer_load_dwordx4 v220, s[16:19], s63 offen lds\nv_mfma_f32_16x16x32_bf16 v[48:51], v[160:163], a[4:7], 0\nR 178,190,53248\nM 48,164,20\nv_mfma_f32_16x16x32_bf16 v[64:67], v[160:163], a[8:11], 0\nR 180,186,0\n"
"M 64,164,24\nv_mfma_f32_16x16x32_bf16 v[80:83], v[160:163], a[12:15], 0\nR 182,186,4096\nM 80,164,28\nG 4\nv_mfma_f32_16x16x32_bf16 v[84:87], v[168:171], a[12:15], 0\n"
"R 160,191,49152\nM 84,172,28\nbuffer_load_dwordx4 v221, s[16:19], s63 offen offset:1024 lds\nv_mfma_f32_16x16x32_bf16 v[68:71], v[168:171], a[8:11], 0\nR 162,191,53248\nM 68,172,24\n"
"s_add_u32 m0, s50, 73728\nv_mfma_f32_16x16x32_bf16 v[52:55], v[168:171], a[4:7], 0\nR 164,187,0\nM 52,172,20\ns_add_u32 s63, s56, 393216\nv_mfma_f32_16x16x32_bf16 v[36:39], v[168:171], a[0:3], 0\n"
"R 166,187,4096\nM 36,172,16\nG 4\nv_mfma_f32_16x16x32_bf16 v[40:43], v[176:179], a[0:3], 0\nR 168,184,8192\nM 40,180,16\n"
"buffer_load_dwordx4 v220, s[16:19], s63 offen lds\nv_mfma_f32_16x16x32_bf16 v[56:59], v[176:179], a[4:7], 0\nR 170,184,12288\nM 56,180,20\nv_mfma_f32_16x16x32_bf16 v[72:75], v[176:179], a[8:11], 0\nR 172,184,16384\n"
"M 72,180,24\nv_mfma_f32_16x16x32_bf16 v[88:91], v[176:179], a[12:15], 0\nR 174,184,20480\nM 88,180,28\nG 4\nv_mfma_f32_16x16x32_bf16 v[92:95], v[160:163], a[12:15], 0\n"
"R 176,185,8192\nM 92,164,28\nbuffer_load_dwordx4 v221, s[16:19], s63 offen offset:1024 lds\nv_mfma_f32_16x16x32_bf16 v[76:79], v[160:163], a[8:11], 0\nR 178,185,12288\nM 76,164,24\n"
"s_add_u32 m0, s50, 81920\nv_mfma_f32_16x16x32_bf16 v[60:63], v[160:163], a[4:7], 0\nR 180,185,16384\nM 60,164,20\ns_add_u32 s63, s56, 458752\nv_mfma_f32_16x16x32_bf16 v[44:47], v[160:163], a[0:3], 0\n"
"R 182,185,20480\nM 44,164,16\nG 4\nM 32,168,32\nR 160,186,8192\nM 32,172,48\n"
"buffer_load_dwordx4 v220, s[16:19], s63 offen lds\nM 48,168,36\nR 162,186,12288\nM 48,172,52\nM 64,168,40\nR 164,186,16384\n"
"M 64,172,56\nbuffer_load_dwordx4 v221, s[16:19], s63 offen offset:1024 lds\nM 80,168,44\nR 166,186,20480\ns_add_u32 m0, s50, 90112\nM 80,172,60\n"
"G 4\nM 84,176,44\nR 168,187,8192\ns_add_u32 s63, s57, 393216\nM 84,180,60\nbuffer_load_dwordx4 v220, s[16:19], s63 offen lds\n"
"M 68,176,40\nR 170,187,12288\nv_mov_b32 v226, s46\nM 68,180,56\nv_mov_b32 v227, s47\nM 52,176,36\n"
"R 172,187,16384\ns_add_u32 s61, s81, 385040\ns_lshl_b32 s62, s44, 5\ns_add_u32 s61, s61, s62\ns_waitcnt vmcnt(7)\ns_mov_b64 exec, 1\n"
"buffer_atomic_swap_x2 v[226:227], off, s[20:23], s61 sc1\ns_mov_b64 exec, -1\nM 52,180,52\nM 36,176,32\nR 174,187,20480\nM 36,180,48\n"
"G 4\nM 40,160,32\nR 176,184,24576\nM 40,164,48\nbuffer_load_dwordx4 v221, s[16:19], s63 offen offset:1024 lds\nM 56,160,36\n"
"R 178,184,28672\nM 56,164,52\ns_add_u32 m0, s50, 98304\nM 72,160,40\nR 180,184,32768\nM 72,164,56\n"
"s_add_u32 s63, s57, 458752\nM 88,160,44\nR 182,184,36864\nM 88,164,60\nG 4\nM 92,168,44\n"
"R 160,185,24576\nM 92,172,60\nbuffer_load_dwordx4 v220, s[16:19], s63 offen lds\nM 76,168,40\nR 162,185,28672\nM 76,172,56\n"
"M 60,168,36\nR 164,185,32768\nM 60,172,52\nbuffer_load_dwordx4 v221, s[16:19], s63 offen offset:1024 lds\nM 44,168,32\nR 166,185,36864\n"
"s_add_u32 s63, s56, 262144\nM 44,172,48\ns_waitcnt vmcnt(9)\ns_barrier\nG 4\nM 32,176,64\n"
"R 168,186,24576\nM 32,180,80\ns_add_u32 m0, s50, 106496\nM 48,176,68\nR 170,186,28672\nM 48,180,84\n"
"buffer_load_dwordx4 v220, s[16:19], s63 offen lds\nM 64,176,72\nR 172,186,32768\nM 64,180,88\nM 80,176,76\nR 174,186,36864\n"
"M 80,180,92\nG 4\nM 84,160,76\nR 176,187,24576\nM 84,164,92\nbuffer_load_dwordx4 v221, s[16:19], s63 offen offset:1024 lds\n"
"M 68,160,72\nR 178,187,28672\nM 68,164,88\ns_add_u32 m0, s50, 114688\nM 52,160,68\nR 180,187,32768\n"
"v_mov_b32 v248, 0\nM 52,164,84\ns_add_u32 s63, s56, 786432\nv_mov_b32 v249, 0\nM 36,160,64\nR 182,187,36864\n"
"s_add_u32 s61, s82, 385040\ns_mov_b64 exec, 0xf\nbuffer_atomic_or_x2 v[248:249], v250, s[20:23], s61 offen sc0 sc1\ns_mov_b64 exec, -1\nM 36,164,80\nG 4\n"
"M 40,168,64\nR 160,184,40960\nM 40,172,80\nbuffer_load_dwordx4 v220, s[16:19], s63 offen lds\nM 56,168,68\nR 162,184,45056\n"
"M 56,172,84\nM 72,168,72\nR 164,184,49152\nM 72,172,88\nM 88,168,76\nR 166,184,53248\n"
"M 88,172,92\nG 4\nM 92,176,76\nR 168,185,40960\nM 92,180,92\nbuffer_load_dwordx4 v221, s[16:19], s63 offen offset:1024 lds\n"
"M 76,176,72\nR 170,185,45056\nM 76,180,88\ns_add_u32 m0, s50, 0\nM 60,176,68\nR 172,185,49152\n"
"M 60,180,84\ns_add_u32 s63, s56, 327680\nM 44,176,64\nR 174,185,53248\nM 44,180,80\nG 4\n"
"M 32,160,96\nR 176,186,40960\nM 32,164,112\nbuffer_load_dwordx4 v220, s[16:19], s63 offen lds\nM 48,160,100\nR 178,186,45056\n"
"M 48,164,116\nM 64,160,104\nR 180,186,49152\nM 64,164,120\nbuffer_load_dwordx4 v221, s[16:19], s63 offen offset:1024 lds\nM 80,160,108\n"
"R 182,186,53248\ns_add_u32 m0, s50, 8192\nM 80,164,124\nG 4\nM 84,168,108\nR 160,187,40960\n"
"s_add_u32 s63, s56, 851968\nM 84,172,124\nbuffer_load_dwordx4 v220, s[16:19], s63 offen lds\nM 68,168,104\nR 162,187,45056\nM 68,172,120\n"
"M 52,168,100\nR 164,187,49152\nM 52,172,116\nbuffer_load_dwordx4 v221, s[16:19], s63 offen offset:1024 lds\nM 36,168,96\nR 166,187,53248\n"
"s_add_u32 m0, s50, 16384\nM 36,172,112\nG 4\nM 40,176,96\nR 168,184,57344\ns_add_u32 s63, s57, 262144\n"
"M 40,180,112\nbuffer_load_dwordx4 v220, s[16:19], s63 offen lds\nM 56,176,100\nR 170,184,61440\nM 56,180,116\nM 72,176,104\n"
"R 172,188,0\nM 72,180,120\nM 88,176,108\nR 174,188,4096\ns_waitcnt vmcnt(15)\ns_barrier\n"
"M 88,180,124\nG 4\nM 92,160,108\nR 176,185,57344\nM 92,164,124\ns_add_u32 s64, s68, 23072768\n"
"M 76,160,104\nR 178,185,61440\nM 76,164,120\nbuffer_load_dwordx4 v221, s[16:19], s63 offen offset:1024 lds\nM 60,160,100\nR 180,189,0\n"
"s_waitcnt vmcnt(8)\nv_cmp_eq_u64 s[86:87], s[46:47], v[248:249]\nM 60,164,116\ns_add_u32 s65, s68, 23187456\nM 44,160,96\nR 182,189,4096\n"
"s_and_b32 s62, s86, 0xf\ns_cmp_eq_u32 s62, 0xf\ns_cbranch_scc0 Lq466\nLq467:\nM 44,164,112\nG 4\n"
"M 32,168,128\nR 160,186,57344\nM 32,172,144\nbuffer_load_dwordx4 a[160:163], v252, s[20:23], s64 offen sc1\nM 48,168,132\nR 162,186,61440\n"
"M 48,172,148\ns_add_u32 s66, s69, 23072768\nM 64,168,136\nR 164,190,0\nM 64,172,152\nbuffer_load_dwordx4 a[164:167], v252, s[20:23], s65 offen sc1\n"
"M 80,168,140\nR 166,190,4096\nM 80,172,156\nG 4\nM 84,176,140\nR 168,187,57344\n"
"M 84,180,156\ns_add_u32 s67, s69, 23187456\nM 68,176,136\nR 170,187,61440\nM 68,180,152\nbuffer_load_dwordx4 a[168:171], v252, s[20:23], s66 offen sc1\n"
"M 52,176,132\nR 172,191,0\nM 52,180,148\ns_add_u32 s64, s68, 23079936\nM 36,176,128\nR 174,191,4096\n"
"M 36,180,144\nG 4\nM 40,160,128\nR 176,188,8192\nM 40,164,144\nbuffer_load_dwordx4 a[172:175], v252, s[20:23], s67 offen sc1\n"
"M 56,160,132\nR 178,188,12288\nM 56,164,148\ns_add_u32 s65, s68, 23194624\nM 72,160,136\nR 180,188,16384\n"
"Lq579:\ns_lshr_b32 s60, s45, 3\ns_lshl2_add_u32 s60, s60, s40\ns_lshl_b32 s60, s60, 8\nv_lshl_add_u32 v224, v252, 3, s60\nv_add_u32 v224, Lq579+4096-LqPC, v224\n"
"v_min_u32 v224, LqE-LqPC-4, v224\ns_mov_b64 exec, 3\nglobal_load_dword v224, v224, s[98:99]\ns_mov_b64 exec, -1\nM 72,164,152\nbuffer_load_dwordx4 a[176:179], v252, s[20:23], s64 offen sc1\n"
"M 88,160,140\nR 182,188,20480\ns_add_u32 m0, s50, 24576\nM 88,164,156\nG 4\nM 92,168,140\n"
"R 160,189,8192\ns_add_u32 s63, s57, 786432\nM 92,172,156\ns_add_u32 s66, s69, 23079936\nM 76,168,136\nR 162,189,12288\n"
"M 76,172,152\nbuffer_load_dwordx4 a[180:183], v252, s[20:23], s65 offen sc1\nbuffer_load_dwordx4 v220, s[16:19], s63 offen lds\nM 60,168,132\nR 164,189,16384\nM 60,172,148\n"
"s_add_u32 s67, s69, 23194624\nM 44,168,128\nR 166,189,20480\nM 44,172,144\nbuffer_load_dwordx4 v221, s[16:19], s63 offen offset:1024 lds\nG 4\n"
"M 96,176,128\nR 168,190,8192\ns_add_u32 m0, s50, 32768\nM 96,180,144\nbuffer_load_dwordx4 a[184:187], v252, s[20:23], s66 offen sc1\nM 112,176,132\n"
"R 170,190,12288\ns_add_u32 s63, s57, 327680\nM 112,180,148\ns_add_u32 s64, s68, 23073792\nbuffer_load_dwordx4 v220, s[16:19], s63 offen lds\nM 128,176,136\n"
"R 172,190,16384\nM 128,180,152\nbuffer_load_dwordx4 a[188:191], v252, s[20:23], s67 offen sc1\nM 144,176,140\nR 174,190,20480\nM 144,180,156\n"
"buffer_load_dwordx4 v221, s[16:19], s63 offen offset:1024 lds\nG 4\nM 148,160,140\nR 176,191,8192\ns_waitcnt vmcnt(19)\ns_barrier\n"
"M 148,164,156\ns_add_u32 s65, s68, 23188480\ns_add_u32 s63, s57, 851968\nM 132,160,136\nR 178,191,12288\ns_add_u32 m0, s50, 40960\n"
"M 132,164,152\nbuffer_load_dwordx4 a[192:195], v252, s[20:23], s64 offen sc1\nM 116,160,132\nR 180,191,16384\nbuffer_load_dwordx4 v220, s[16:19], s63 offen lds\nM 116,164,148\n"
"s_add_u32 s66, s69, 23073792\nM 100,160,128\nR 182,191,20480\nM 100,164,144\nG 4\nM 104,168,128\n"
"R 160,188,24576\nbuffer_load_dwordx4 v221, s[16:19], s63 offen offset:1024 lds\nM 104,172,144\nbuffer_load_dwordx4 a[196:199], v252, s[20:23], s65 offen sc1\nM 120,168,132\nR 162,188,28672\n"
"M 120,172,148\ns_add_u32 s67, s69, 23188480\nM 136,168,136\nR 164,188,32768\nM 136,172,152\nbuffer_load_dwordx4 a[200:203], v252, s[20:23], s66 offen sc1\n"
"M 152,168,140\nR 166,188,36864\nM 152,172,156\nG 4\nM 156,176,140\nR 168,189,24576\n"
"M 156,180,156\nbuffer_load_dwordx4 a[204:207], v252, s[20:23], s67 offen sc1\nM 140,176,136\nR 170,189,28672\nM 140,180,152\ns_add_u32 s64, s68, 23080960\n"
"M 124,176,132\nR 172,189,32768\nM 124,180,148\ns_add_u32 s65, s68, 23195648\nM 108,176,128\nR 174,189,36864\n"
"M 108,180,144\nG 4\nM 0,160,128\nR 176,190,24576\nM 0,164,144\nbuffer_load_dwordx4 a[208:211], v252, s[20:23], s64 offen sc1\n"
"M 16,160,132\nR 178,190,28672\nM 16,164,148\ns_add_u32 s66, s69, 23080960\nv_mfma_f32_16x16x32_bf16 a[224:227], v[160:163], a[136:139], a[224:227]\nR 180,190,32768\n"
"v_mfma_f32_16x16x32_bf16 a[224:227], v[164:167], a[152:155], a[224:227]\nbuffer_load_dwordx4 a[212:215], v252, s[20:23], s65 offen sc1\nv_mfma_f32_16x16x32_bf16 a[240:243], v[160:163], a[140:143], a[240:243]\nR 182,190,36864\nv_mfma_f32_16x16x32_bf16 a[240:243], v[164:167], a[156:159], a[240:243]\nG 4\n"
"v_mfma_f32_16x16x32_bf16 a[244:247], v[168:171], a[140:143], a[244:247]\nR 160,191,24576\nv_mfma_f32_16x16x32_bf16 a[244:247], v[172:175], a[156:159], a[244:247]\nbuffer_load_dwordx4 a[216:219], v252, s[20:23], s66 offen sc1\nv_mfma_f32_16x16x32_bf16 a[228:231], v[168:171], a[136:139], a[228:231]\nR 162,191,28672\n"
"v_mfma_f32_16x16x32_bf16 a[228:231], v[172:175], a[152:155], a[228:231]\ns_add_u32 s67, s69, 23195648\nM 20,168,132\nR 164,191,32768\nM 20,172,148\nM 4,168,128\n"
"R 166,191,36864\nM 4,172,144\nG 4\nM 8,176,128\nR 168,188,40960\nM 8,180,144\n"
"buffer_load_dwordx4 a[220:223], v252, s[20:23], s67 offen sc1\nM 24,176,132\nR 170,188,45056\nM 24,180,148\ns_add_u32 m0, s50, 49152\nv_mfma_f32_16x16x32_bf16 a[232:235], v[176:179], a[136:139], a[232:235]\n"
"R 172,188,49152\nv_mfma_f32_16x16x32_bf16 a[232:235], v[180:183], a[152:155], a[232:235]\ns_add_u32 s63, s58, 262144\nv_mfma_f32_16x16x32_bf16 a[248:251], v[176:179], a[140:143], a[248:251]\nR 174,188,53248\nv_mfma_f32_16x16x32_bf16 a[248:251], v[180:183], a[156:159], a[248:251]\n"
"G 4\nv_mfma_f32_16x16x32_bf16 a[252:255], v[160:163], a[140:143], a[252:255]\nR 176,189,40960\nv_mfma_f32_16x16x32_bf16 a[252:255], v[164:167], a[156:159], a[252:255]\nbuffer_load_dwordx4 v220, s[16:19], s63 offen lds\nv_mfma_f32_16x16x32_bf16 a[236:239], v[160:163], a[136:139], a[236:239]\n"
"R 178,189,45056\ns_add_u32 s84, s52, 2048\nv_mfma_f32_16x16x32_bf16 a[236:239], v[164:167], a[152:155], a[236:239]\nv_lshrrev_b32 v244, 5, v252\nv_lshlrev_b32 v244, 5, v244\nM 28,160,132\n"
"R 180,189,49152\nv_bfe_u32 v245, v252, 4, 1\nM 28,164,148\nbuffer_load_dwordx4 v221, s[16:19], s63 offen offset:1024 lds\nM 12,160,128\nR 182,189,53248\n"
"M 12,164,144\ns_waitcnt vmcnt(9)\ns_barrier\nG 4\nM 96,168,160\nR 160,190,40960\n"
"M 96,172,176\ns_add_u32 m0, s50, 57344\nM 112,168,164\nR 162,190,45056\nM 112,172,180\ns_add_u32 s63, s58, 786432\n"
"M 128,168,168\nR 164,190,49152\nv_lshrrev_b32 v230, 8, v252\nM 128,172,184\nbuffer_load_dwordx4 v220, s[16:19], s63 offen lds\nM 144,168,172\n"
"R 166,190,53248\nv_lshlrev_b32 v230, 3, v230\nM 144,172,188\nG 4\nM 148,176,172\nR 168,191,40960\n"
"M 148,180,188\ns_and_b32 s60, s41, 7\ns_lshl2_add_u32 s60, s60, s40\nv_add_u32 v244, s60, v244\nM 132,176,168\nLpg15:\n"
"s_cbranch_execz LretO0n\nR 170,191,45056\nv_mul_u32_u24 v244, 6144, v244\nM 132,180,184\ns_sub_u32 s60, s84, s84\ns_add_u32 s60, s60, 2048\n"
"v_mul_lo_u32 v245, v245, s60\nM 116,176,164\nR 172,191,49152\nv_add_u32 v244, v244, v245\nM 116,180,180\nbuffer_load_dwordx2 v[232:233], v230, s[36:39], s53 offen offset:0\n"
"M 100,176,160\nR 174,191,53248\ns_bfm_b64 exec, 16, 0\nbuffer_load_dword v245, v244, s[4:7], s84 offen\ns_mov_b64 exec, -1\nM 100,180,176\n"
"G 4\nM 104,160,160\nR 176,184,0\nM 104,164,176\nbuffer_load_dwordx4 v221, s[16:19], s63 offen offset:1024 lds\nM 120,160,164\n"
"R 178,184,4096\nM 120,164,180\ns_add_u32 m0, s50, 65536\nM 136,160,168\nR 180,184,8192\nbuffer_load_dwordx2 v[234:235], v230, s[36:39], s53 offen offset:32\n"
"M 136,164,184\ns_add_u32 s63, s58, 327680\nbuffer_load_dwordx2 v[236:237], v230, s[36:39], s53 offen offset:64\nM 152,160,172\nR 182,184,12288\nM 152,164,188\n"
"buffer_load_dwordx2 v[238:239], v230, s[36:39], s53 offen offset:96\nG 4\nM 156,168,172\nR 160,185,0\nv_bfe_u32 v242, v252, 4, 4\nM 156,172,188\n"
"buffer_load_dwordx4 v220, s[16:19], s63 offen lds\nM 140,168,168\nR 162,185,4096\nv_lshrrev_b32 v243, 8, v252\nM 140,172,184\nv_lshlrev_b32 v243, 2, v243\n"
"v_sub_u32 v242, v242, v243\nM 124,168,164\nR 164,185,8192\nv_lshlrev_b32 v243, 4, v242\nM 124,172,180\ns_mov_b64 s[60:61], 0x3f80\n"
"v_lshlrev_b64 v[208:209], v243, s[60:61]\nM 108,168,160\nR 166,185,12288\ns_bfm_b64 exec, 16, 16\nbuffer_load_dword v245, v244, s[4:7], s84 offen\ns_mov_b64 exec, -1\n"
"M 108,172,176\nG 4\nM 96,176,192\nR 168,186,0\ns_waitcnt vmcnt(14)\nM 96,180,208\n"
"buffer_load_dwordx4 v221, s[16:19], s63 offen offset:1024 lds\nM 112,176,196\nR 170,186,4096\ns_waitcnt vmcnt(14)\nM 112,180,212\ns_add_u32 m0, s50, 73728\n"
"M 128,176,200\nR 172,186,8192\ns_waitcnt vmcnt(13)\nM 128,180,216\ns_add_u32 s63, s58, 851968\nM 144,176,204\n"
"R 174,186,12288\ns_waitcnt vmcnt(12)\nM 144,180,220\nG 4\nv_cmp_gt_u32 s[60:61], 4, v242\ns_nop 1\n"
"v_cndmask_b32 v212, 0, v208, s[60:61]\nv_cndmask_b32 v208, 0, v208, s[60:61]\nv_cndmask_b32 v213, 0, v209, s[60:61]\nv_cndmask_b32 v209, 0, v209, s[60:61]\nM 148,160,204\nR 176,187,0\n"
"M 148,164,220\nbuffer_load_dwordx4 v220, s[16:19], s63 offen lds\nM 132,160,200\nR 178,187,4096\nM 132,164,216\nM 116,160,196\n"
"R 180,187,8192\nM 116,164,212\nM 100,160,192\nR 182,187,12288\ns_bfm_b64 exec, 16, 32\nbuffer_load_dword v245, v244, s[4:7], s84 offen\n"
"s_mov_b64 exec, -1\nM 100,164,208\nG 4\nM 104,168,192\nR 160,184,16384\nM 104,172,208\n"
"buffer_load_dwordx4 v221, s[16:19], s63 offen offset:1024 lds\nM 120,168,196\nR 162,184,20480\nM 120,172,212\ns_add_u32 m0, s50, 81920\nM 136,168,200\n"
"R 164,184,24576\nM 136,172,216\ns_add_u32 s63, s59, 0\nM 152,168,204\nR 166,184,28672\nM 152,172,220\n"
"G 4\nM 156,176,204\nR 168,185,16384\nM 156,180,220\nbuffer_load_dwordx4 v220, s[16:19], s63 offen lds\nM 140,176,200\n"
"R 170,185,20480\nM 140,180,216\nM 124,176,196\nR 172,185,24576\nM 124,180,212\nM 108,176,192\n"
"R 174,185,28672\ns_bfm_b64 exec, 16, 48\nbuffer_load_dword v245, v244, s[4:7], s84 offen\ns_mov_b64 exec, -1\nM 108,180,208\nG 4\n"
"M 0,160,160\nR 176,186,16384\nM 0,164,176\ns_mov_b32 m0, s51\nM 16,160,164\nR 178,186,20480\n"
"v_cvt_pk_bf16_f32 v128, v128, v129\nv_cvt_pk_bf16_f32 v129, v130, v131\nM 16,164,180\nds_write_addtid_b32 v128 offset:0\nv_cvt_pk_bf16_f32 v130, v132, v133\nv_cvt_pk_bf16_f32 v131, v134, v135\n"
"v_mfma_f32_16x16x32_bf16 a[224:227], v[160:163], a[168:171], a[224:227]\nR 180,186,24576\nv_cvt_pk_bf16_f32 v132, v136, v137\nv_mfma_f32_16x16x32_bf16 a[224:227], v[164:167], a[184:187], a[224:227]\nds_write_addtid_b32 v129 offset:256\nv_cvt_pk_bf16_f32 v133, v138, v139\n"
"v_mfma_f32_16x16x32_bf16 a[240:243], v[160:163], a[172:175], a[240:243]\nR 182,186,28672\nv_cvt_pk_bf16_f32 v134, v140, v141\nv_mfma_f32_16x16x32_bf16 a[240:243], v[164:167], a[188:191], a[240:243]\nds_write_addtid_b32 v130 offset:512\nG 7\n"
"v_cvt_pk_bf16_f32 v135, v142, v143\nv_mfma_f32_16x16x32_bf16 a[244:247], v[168:171], a[172:175], a[244:247]\nR 160,187,16384\nv_cvt_pk_bf16_f32 v136, v144, v145\nv_mfma_f32_16x16x32_bf16 a[244:247], v[172:175], a[188:191], a[244:247]\nds_write_addtid_b32 v131 offset:768\n"
"v_cvt_pk_bf16_f32 v137, v146, v147\nv_mfma_f32_16x16x32_bf16 a[228:231], v[168:171], a[168:171], a[228:231]\nR 162,187,20480\nv_cvt_pk_bf16_f32 v138, v148, v149\nv_mfma_f32_16x16x32_bf16 a[228:231], v[172:175], a[184:187], a[228:231]\nds_write_addtid_b32 v132 offset:1024\n"
"v_cvt_pk_bf16_f32 v139, v150, v151\nM 20,168,164\nR 164,187,24576\nv_cvt_pk_bf16_f32 v140, v152, v153\nM 20,172,180\nds_write_addtid_b32 v133 offset:1280\n"
"v_cvt_pk_bf16_f32 v141, v154, v155\nM 4,168,160\nR 166,187,28672\nv_cvt_pk_bf16_f32 v142, v156, v157\nM 4,172,176\nds_write_addtid_b32 v134 offset:1536\n"
"G 9\nv_cvt_pk_bf16_f32 v143, v158, v159\nM 8,176,160\nR 168,184,32768\nds_write_addtid_b32 v135 offset:1792\nM 8,180,176\n"
"ds_write_addtid_b32 v136 offset:2048\nds_write_addtid_b32 v137 offset:2304\nM 24,176,164\nR 170,184,36864\nds_write_addtid_b32 v138 offset:2560\nM 24,180,180\n"
"ds_write_addtid_b32 v139 offset:2816\nds_write_addtid_b32 v140 offset:3072\nv_mfma_f32_16x16x32_bf16 a[232:235], v[176:179], a[168:171], a[232:235]\nR 172,184,40960\nds_write_addtid_b32 v141 offset:3328\nv_mfma_f32_16x16x32_bf16 a[232:235], v[180:183], a[184:187], a[232:235]\n"
"ds_write_addtid_b32 v142 offset:3584\nds_write_addtid_b32 v143 offset:3840\nv_mfma_f32_16x16x32_bf16 a[248:251], v[176:179], a[172:175], a[248:251]\nR 174,184,45056\ns_add_u32 m0, s50, 81920\nv_mfma_f32_16x16x32_bf16 a[248:251], v[180:183], a[188:191], a[248:251]\n"
"G 14\nv_mfma_f32_16x16x32_bf16 a[252:255], v[160:163], a[172:175], a[252:255]\nR 176,185,32768\ns_waitcnt vmcnt(11) lgkmcnt(2)\ns_barrier\nv_mfma_f32_16x16x32_bf16 a[252:255], v[164:167], a[188:191], a[252:255]\n"
"buffer_load_dwordx4 v221, s[16:19], s63 offen offset:1024 lds\nv_mfma_f32_16x16x32_bf16 a[236:239], v[160:163], a[168:171], a[236:239]\nR 178,185,36864\ns_add_u32 s63, s59, 524288\nv_mfma_f32_16x16x32_bf16 a[236:239], v[164:167], a[184:187], a[236:239]\ns_add_u32 m0, s50, 90112\n"
"ds_read2st64_b32 v[128:129], v225 offset0:0 offset1:1\nM 28,160,164\nR 180,185,40960\nM 28,164,180\nbuffer_load_dwordx4 v220, s[16:19], s63 offen lds\nM 12,160,160\n"
"R 182,185,45056\nM 12,164,176\nds_read2st64_b32 v[130:131], v225 offset0:2 offset1:3\nG 6\nM 0,168,192\nR 160,186,32768\n"
"M 0,172,208\nbuffer_load_dwordx4 v221, s[16:19], s63 offen offset:1024 lds\nds_read2st64_b32 v[132:133], v225 offset0:4 offset1:5\nM 16,168,196\nR 162,186,36864\ns_add_u32 s63, s59, 65536\n"
"ds_read2st64_b32 v[134:135], v225 offset0:6 offset1:7\nM 16,172,212\ns_add_u32 m0, s50, 98304\nds_read2st64_b32 v[136:137], v225 offset0:8 offset1:9\nv_mfma_f32_16x16x32_bf16 a[224:227], v[168:171], a[200:203], a[224:227]\nR 164,186,40960\n"
"v_mfma_f32_16x16x32_bf16 a[224:227], v[172:175], a[216:219], a[224:227]\nbuffer_load_dwordx4 v220, s[16:19], s63 offen lds\nG 6\nv_mfma_f32_16x16x32_bf16 v[96:99], v[208:211], v[128:131], v[96:99]\nv_mfma_f32_16x16x32_bf16 v[100:103], v[210:213], v[128:131], v[100:103]\nds_read2st64_b32 v[138:139], v225 offset0:10 offset1:11\n"
"v_mfma_f32_16x16x32_bf16 a[240:243], v[168:171], a[204:207], a[240:243]\nR 166,186,45056\nv_mfma_f32_16x16x32_bf16 a[240:243], v[172:175], a[220:223], a[240:243]\nds_read2st64_b32 v[140:141], v225 offset0:12 offset1:13\nv_mfma_f32_16x16x32_bf16 a[244:247], v[176:179], a[204:207], a[244:247]\nR 168,187,32768\n"
"G 6\nv_mfma_f32_16x16x32_bf16 v[104:107], v[208:211], v[132:135], v[104:107]\nv_mfma_f32_16x16x32_bf16 v[108:111], v[210:213], v[132:135], v[108:111]\nds_read2st64_b32 v[142:143], v225 offset0:14 offset1:15\nv_mfma_f32_16x16x32_bf16 a[244:247], v[180:183], a[220:223], a[244:247]\nbuffer_load_dwordx4 v221, s[16:19], s63 offen offset:1024 lds\n"
"v_mfma_f32_16x16x32_bf16 a[228:231], v[176:179], a[200:203], a[228:231]\nR 170,187,36864\nv_mfma_f32_16x16x32_bf16 a[228:231], v[180:183], a[216:219], a[228:231]\ns_add_u32 m0, s50, 106496\nG 5\nv_mfma_f32_16x16x32_bf16 v[112:115], v[208:211], v[136:139], v[112:115]\n"
"v_mfma_f32_16x16x32_bf16 v[116:119], v[210:213], v[136:139], v[116:119]\nM 20,176,196\nR 172,187,40960\nM 20,180,212\ns_add_u32 s63, s59, 589824\nM 4,176,192\n"
"R 174,187,45056\nG 3\nv_mfma_f32_16x16x32_bf16 v[120:123], v[208:211], v[140:143], v[120:123]\nv_mfma_f32_16x16x32_bf16 v[124:127], v[210:213], v[140:143], v[124:127]\nM 4,180,208\nM 8,160,192\n"
"R 176,184,49152\nM 8,164,208\nbuffer_load_dwordx4 v220, s[16:19], s63 offen lds\nM 24,160,196\nR 178,184,53248\nM 24,164,212\n"
"v_mfma_f32_16x16x32_bf16 a[232:235], v[160:163], a[200:203], a[232:235]\nR 180,184,57344\nv_cvt_pk_bf16_f32 v120, v120, v121\nv_cvt_pk_bf16_f32 v96, v96, v97\nv_cvt_pk_bf16_f32 v112, v112, v113\nv_cvt_pk_bf16_f32 v121, v122, v123\n"
"v_mfma_f32_16x16x32_bf16 a[232:235], v[164:167], a[216:219], a[232:235]\nbuffer_load_dwordx4 v221, s[16:19], s63 offen offset:1024 lds\nv_cvt_pk_bf16_f32 v97, v98, v99\nv_cvt_pk_bf16_f32 v113, v114, v115\nv_cvt_pk_bf16_f32 v122, v124, v125\nv_mfma_f32_16x16x32_bf16 a[248:251], v[160:163], a[204:207], a[248:251]\n"
"R 182,184,61440\nv_cvt_pk_bf16_f32 v123, v126, v127\nv_cvt_pk_bf16_f32 v98, v100, v101\nv_cvt_pk_bf16_f32 v114, v116, v117\nv_mfma_f32_16x16x32_bf16 a[248:251], v[164:167], a[220:223], a[248:251]\ns_bfe_u32 s100, s56, 0x20007\n"
"s_mul_i32 s100, s100, 49152\ns_add_u32 s100, s100, s72\ns_add_u32 s100, s100, 52428800\nG 4\nv_cvt_pk_bf16_f32 v99, v102, v103\nv_cvt_pk_bf16_f32 v115, v118, v119\n"
"v_mfma_f32_16x16x32_bf16 a[252:255], v[168:171], a[204:207], a[252:255]\nR 160,185,49152\ns_add_u32 s63, s59, 131072\nv_mfma_f32_16x16x32_bf16 a[252:255], v[172:175], a[220:223], a[252:255]\nbuffer_store_dwordx4 v[96:99], v252, s[20:23], s100 offen offset:0 sc1\nv_mfma_f32_16x16x32_bf16 a[236:239], v[168:171], a[200:203], a[236:239]\n"
"R 162,185,53248\nv_cvt_pk_bf16_f32 v104, v104, v105\nv_mfma_f32_16x16x32_bf16 a[236:239], v[172:175], a[216:219], a[236:239]\ns_add_u32 m0, s50, 114688\nv_cvt_pk_bf16_f32 v105, v106, v107\nv_cvt_pk_bf16_f32 v106, v108, v109\n"
"M 28,168,196\nR 164,185,57344\nv_cvt_pk_bf16_f32 v107, v110, v111\nM 28,172,212\nbuffer_load_dwordx4 v220, s[16:19], s63 offen lds\nM 12,168,192\n"
"R 166,185,61440\nM 12,172,208\nbuffer_store_dwordx4 v[104:107], v252, s[20:23], s100 offen offset:1024 sc1\ns_waitcnt vmcnt(12)\ns_barrier\nG 4\n"
"M 32,176,160\nR 168,186,49152\nM 32,180,176\nv_xor_b32 v240, 0x2000, v225\nM 48,176,164\nR 170,186,53248\n"
"ds_write2st64_b32 v240, a224, a225 offset0:0 offset1:1\nM 48,180,180\nbuffer_store_dwordx4 v[112:115], v252, s[20:23], s100 offen offset:2048 sc1\nds_write2st64_b32 v240, a226, a227 offset0:2 offset1:3\nM 64,176,168\nR 172,186,57344\n"
"ds_write2st64_b32 v240, a228, a229 offset0:4 offset1:5\nM 64,180,184\nds_write2st64_b32 v240, a230, a231 offset0:6 offset1:7\nM 80,176,172\nR 174,186,61440\nds_write2st64_b32 v240, a232, a233 offset0:8 offset1:9\n"
"M 80,180,188\nds_write2st64_b32 v240, a234, a235 offset0:10 offset1:11\nG 10\nM 84,160,172\nR 176,187,49152\nds_write2st64_b32 v240, a236, a237 offset0:12 offset1:13\n"
"M 84,164,188\nbuffer_store_dwordx4 v[120:123], v252, s[20:23], s100 offen offset:3072 sc1\nds_write2st64_b32 v240, a238, a239 offset0:14 offset1:15\nM 68,160,168\nR 178,187,53248\nds_write2st64_b32 v240, a240, a241 offset0:16 offset1:17\n"
"M 68,164,184\nds_write2st64_b32 v240, a242, a243 offset0:18 offset1:19\nM 52,160,164\nR 180,187,57344\nds_write2st64_b32 v240, a244, a245 offset0:20 offset1:21\nM 52,164,180\n"
"ds_write2st64_b32 v240, a246, a247 offset0:22 offset1:23\nM 36,160,160\nR 182,187,61440\nds_write2st64_b32 v240, a248, a249 offset0:24 offset1:25\nM 36,164,176\nds_write2st64_b32 v240, a250, a251 offset0:26 offset1:27\n"
"G 14\nM 40,168,160\nR 160,188,0\nds_write2st64_b32 v240, a252, a253 offset0:28 offset1:29\nM 40,172,176\nbuffer_load_dwordx4 v221, s[16:19], s63 offen offset:1024 lds\n"
"M 56,168,164\nR 162,188,4096\ns_add_u32 s63, s59, 655360\nM 56,172,180\ns_add_u32 m0, s50, 0\nds_write2st64_b32 v240, a254, a255 offset0:30 offset1:31\n"
"M 72,168,168\nR 164,188,8192\nM 72,172,184\nbuffer_load_dwordx4 v220, s[16:19], s63 offen lds\nM 88,168,172\nR 166,188,12288\n"
"M 88,172,188\nG 8\nM 92,176,172\nR 168,189,0\ns_waitcnt vmcnt(11) lgkmcnt(3)\ns_barrier\n"
"M 92,180,188\nbuffer_load_dwordx4 v221, s[16:19], s63 offen offset:1024 lds\nds_read2st64_b32 v[128:129], v225 offset0:0 offset1:1\nM 76,176,168\nR 170,189,4096\ns_add_u32 s63, s59, 196608\n"
"ds_read2st64_b32 v[130:131], v225 offset0:2 offset1:3\nM 76,180,184\ns_add_u32 m0, s50, 8192\nds_read2st64_b32 v[132:133], v225 offset0:4 offset1:5\nds_read2st64_b32 v[134:135], v225 offset0:6 offset1:7\nM 60,176,164\n"
"R 172,189,8192\nds_read2st64_b32 v[136:137], v225 offset0:8 offset1:9\nM 60,180,180\nbuffer_load_dwordx4 v220, s[16:19], s63 offen lds\nds_read2st64_b32 v[138:139], v225 offset0:10 offset1:11\nM 44,176,160\n"
"R 174,189,12288\nds_read2st64_b32 v[140:141], v225 offset0:12 offset1:13\nM 44,180,176\nds_read2st64_b32 v[142:143], v225 offset0:14 offset1:15\nds_read2st64_b32 v[144:145], v225 offset0:16 offset1:17\nG 13\n"
"M 32,160,192\nR 176,190,0\nds_read2st64_b32 v[146:147], v225 offset0:18 offset1:19\nG 13\nv_add_f32 v0, v0, v128\nM 32,164,208\n"
"buffer_load_dwordx4 v221, s[16:19], s63 offen offset:1024 lds\nds_read2st64_b32 v[148:149], v225 offset0:20 offset1:21\nM 48,160,196\nR 178,190,4096\nds_read2st64_b32 v[150:151], v225 offset0:22 offset1:23\nv_add_f32 v1, v1, v129\n"
"M 48,164,212\ns_add_u32 m0, s50, 16384\nds_read2st64_b32 v[152:153], v225 offset0:24 offset1:25\nds_read2st64_b32 v[154:155], v225 offset0:26 offset1:27\nM 64,160,200\nR 180,190,8192\n"
"ds_read2st64_b32 v[156:157], v225 offset0:28 offset1:29\nG 15\nv_add_f32 v2, v2, v130\nM 64,164,216\ns_add_u32 s63, s59, 720896\nds_read2st64_b32 v[158:159], v225 offset0:30 offset1:31\n"
"v_add_f32 v3, v3, v131\nM 80,160,204\nR 182,190,12288\nv_add_f32 v4, v4, v132\nv_add_f32 v5, v5, v133\nM 80,164,220\n"
"G 14\nv_add_f32 v6, v6, v134\nv_add_f32 v7, v7, v135\nv_add_f32 v8, v8, v136\nM 84,168,204\nR 160,191,0\n"
"v_add_f32 v9, v9, v137\nv_add_f32 v10, v10, v138\nM 84,172,220\nbuffer_load_dwordx4 v220, s[16:19], s63 offen lds\nv_add_f32 v11, v11, v139\nM 68,168,200\n"
"R 162,191,4096\nG 15\nv_add_f32 v12, v12, v140\nv_add_f32 v13, v13, v141\nM 68,172,216\nG 14\n"
"v_add_f32 v14, v14, v142\nv_add_f32 v15, v15, v143\nG 13\nv_add_f32 v16, v16, v144\nM 52,168,196\nR 164,191,8192\n"
"v_add_f32 v17, v17, v145\nG 12\nv_add_f32 v18, v18, v146\nM 52,172,212\nbuffer_load_dwordx4 v221, s[16:19], s63 offen offset:1024 lds\nv_add_f32 v19, v19, v147\n"
"M 36,168,192\nR 166,191,12288\ns_add_u32 m0, s50, 24576\nG 12\nv_add_f32 v20, v20, v148\nM 36,172,208\n"
"G 4\nv_add_f32 v21, v21, v149\nv_add_f32 v22, v22, v150\nv_add_f32 v23, v23, v151\nM 40,176,192\nR 168,188,16384\n"
"s_add_u32 s63, s59, 393216\nv_add_f32 v24, v24, v152\nM 40,180,208\nbuffer_load_dwordx4 v220, s[16:19], s63 offen lds\nv_add_f32 v25, v25, v153\nM 56,176,196\n"
"R 170,188,20480\nv_add_f32 v26, v26, v154\nv_add_f32 v27, v27, v155\nM 56,180,212\nv_add_f32 v28, v28, v156\nv_add_f32 v29, v29, v157\n"
"v_add_f32 v30, v30, v158\nv_add_f32 v31, v31, v159\nM 72,176,200\nR 172,188,24576\nv_cvt_pk_bf16_f32 v24, v24, v25\nv_cvt_pk_bf16_f32 v0, v0, v1\n"
"v_cvt_pk_bf16_f32 v25, v26, v27\nM 72,180,216\nbuffer_load_dwordx4 v221, s[16:19], s63 offen offset:1024 lds\nv_cvt_pk_bf16_f32 v26, v28, v29\nM 88,176,204\nR 174,188,28672\n"
"v_cvt_pk_bf16_f32 v27, v30, v31\nv_cvt_pk_bf16_f32 v1, v2, v3\nM 88,180,220\nG 4\nv_cvt_pk_bf16_f32 v2, v4, v5\nv_cvt_pk_bf16_f32 v3, v6, v7\n"
"M 92,160,204\nR 176,189,16384\ns_bfe_u32 s100, s57, 0x20007\ns_mul_i32 s100, s100, 49152\ns_add_u32 s100, s100, s72\ns_add_u32 s100, s100, 52445184\n"
"v_cvt_pk_bf16_f32 v16, v16, v17\nM 92,164,220\nbuffer_store_dwordx4 v[0:3], v252, s[20:23], s100 offen offset:0 sc1\nM 76,160,200\nR 178,189,20480\nv_cvt_pk_bf16_f32 v17, v18, v19\n"
"M 76,164,216\nv_cvt_pk_bf16_f32 v8, v8, v9\nv_cvt_pk_bf16_f32 v9, v10, v11\nM 60,160,196\nR 180,189,24576\nv_cvt_pk_bf16_f32 v10, v12, v13\n"
"M 60,164,212\nv_cvt_pk_bf16_f32 v11, v14, v15\nv_cvt_pk_bf16_f32 v18, v20, v21\nM 44,160,192\nR 182,189,28672\nv_cvt_pk_bf16_f32 v19, v22, v23\n"
"M 44,164,208\nbuffer_store_dwordx4 v[8:11], v252, s[20:23], s100 offen offset:1024 sc1\ns_waitcnt vmcnt(16)\ns_barrier\nG 4\nv_mfma_f32_16x16x32_bf16 v[96:99], v[168:171], a[0:3], 0\n"
"R 160,190,16384\nM 96,172,16\nLpg16:\ns_cbranch_execz LretW0z\ns_mov_b32 m0, s51\nv_mfma_f32_16x16x32_bf16 v[112:115], v[168:171], a[4:7], 0\n"
"R 162,190,20480\nv_cvt_pk_bf16_f32 v64, v64, v65\nv_cvt_pk_bf16_f32 v65, v66, v67\nM 112,172,20\nbuffer_store_dwordx4 v[16:19], v252, s[20:23], s100 offen offset:2048 sc1\nv_cvt_pk_bf16_f32 v66, v68, v69\n"
"v_mfma_f32_16x16x32_bf16 v[128:131], v[168:171], a[8:11], 0\nR 164,190,24576\nv_cvt_pk_bf16_f32 v67, v70, v71\nv_cvt_pk_bf16_f32 v68, v72, v73\nM 128,172,24\nds_write_addtid_b32 v64 offset:0\n"
"v_cvt_pk_bf16_f32 v69, v74, v75\nv_cvt_pk_bf16_f32 v70, v76, v77\nv_mfma_f32_16x16x32_bf16 v[144:147], v[168:171], a[12:15], 0\nR 166,190,28672\nv_cvt_pk_bf16_f32 v71, v78, v79\nv_cvt_pk_bf16_f32 v72, v80, v81\n"
"M 144,172,28\nds_write_addtid_b32 v65 offset:256\nG 6\nv_cvt_pk_bf16_f32 v73, v82, v83\nv_mfma_f32_16x16x32_bf16 v[148:151], v[176:179], a[12:15], 0\nR 168,191,16384\n"
"v_cvt_pk_bf16_f32 v74, v84, v85\nv_cvt_pk_bf16_f32 v75, v86, v87\nM 148,180,28\nbuffer_store_dwordx4 v[24:27], v252, s[20:23], s100 offen offset:3072 sc1\nv_mfma_f32_16x16x32_bf16 v[132:135], v[176:179], a[8:11], 0\nR 170,191,20480\n"
"v_cvt_pk_bf16_f32 v76, v88, v89\nM 132,180,24\nds_write_addtid_b32 v66 offset:512\nv_cvt_pk_bf16_f32 v77, v90, v91\nv_mfma_f32_16x16x32_bf16 v[116:119], v[176:179], a[4:7], 0\nR 172,191,24576\n"
"v_cvt_pk_bf16_f32 v78, v92, v93\nM 116,180,20\nds_write_addtid_b32 v67 offset:768\nv_cvt_pk_bf16_f32 v79, v94, v95\nv_mfma_f32_16x16x32_bf16 v[100:103], v[176:179], a[0:3], 0\nR 174,191,28672\n"
"ds_write_addtid_b32 v68 offset:1024\nM 100,180,16\nds_write_addtid_b32 v69 offset:1280\nds_write_addtid_b32 v70 offset:1536\nG 10\nv_mfma_f32_16x16x32_bf16 v[104:107], v[160:163], a[0:3], 0\n"
"R 176,188,32768\nds_write_addtid_b32 v71 offset:1792\nM 104,164,16\nds_write_addtid_b32 v72 offset:2048\nds_write_addtid_b32 v73 offset:2304\nv_mfma_f32_16x16x32_bf16 v[120:123], v[160:163], a[4:7], 0\n"
"R 178,188,36864\nds_write_addtid_b32 v74 offset:2560\nM 120,164,20\nds_write_addtid_b32 v75 offset:2816\nds_write_addtid_b32 v76 offset:3072\nv_mfma_f32_16x16x32_bf16 v[136:139], v[160:163], a[8:11], 0\n"
"R 180,188,40960\nds_write_addtid_b32 v77 offset:3328\nM 136,164,24\nds_write_addtid_b32 v78 offset:3584\nds_write_addtid_b32 v79 offset:3840\nv_mfma_f32_16x16x32_bf16 v[152:155], v[160:163], a[12:15], 0\n"
"R 182,188,45056\ns_add_u32 m0, s50, 32768\nM 152,164,28\ns_add_u32 s63, s59, 458752\nG 15\nv_mfma_f32_16x16x32_bf16 v[156:159], v[168:171], a[12:15], 0\n"
"R 160,189,32768\ns_waitcnt vmcnt(10) lgkmcnt(2)\ns_barrier\nM 156,172,28\nbuffer_load_dwordx4 v220, s[16:19], s63 offen lds\nv_mfma_f32_16x16x32_bf16 v[140:143], v[168:171], a[8:11], 0\n"
"R 162,189,36864\nM 140,172,24\nds_read2st64_b32 v[64:65], v225 offset0:0 offset1:1\nv_mfma_f32_16x16x32_bf16 v[124:127], v[168:171], a[4:7], 0\nR 164,189,40960\nM 124,172,20\n"
"buffer_load_dwordx4 v221, s[16:19], s63 offen offset:1024 lds\nv_mfma_f32_16x16x32_bf16 v[108:111], v[168:171], a[0:3], 0\nR 166,189,45056\ns_add_u32 m0, s50, 40960\nM 108,172,16\nds_read2st64_b32 v[66:67], v225 offset0:2 offset1:3\n"
"G 6\nM 96,176,32\nR 168,190,32768\ns_add_u32 s63, s59, 262144\nv_bfe_u32 v250, v252, 4, 2\nM 96,180,48\n"
"buffer_load_dwordx4 v220, s[16:19], s63 offen lds\nds_read2st64_b32 v[68:69], v225 offset0:4 offset1:5\nv_lshlrev_b32 v250, 3, v250\nM 112,176,36\nR 170,190,36864\nds_read2st64_b32 v[70:71], v225 offset0:6 offset1:7\n"
"s_lshl_b32 s60, s43, 5\nv_add_u32 v250, s60, v250\nM 112,180,52\nds_read2st64_b32 v[72:73], v225 offset0:8 offset1:9\nM 128,176,40\nR 172,190,40960\n"
"M 128,180,56\nbuffer_load_dwordx4 v221, s[16:19], s63 offen offset:1024 lds\nG 6\nv_mfma_f32_16x16x32_bf16 v[32:35], v[208:211], v[64:67], v[32:35]\nv_mfma_f32_16x16x32_bf16 v[36:39], v[210:213], v[64:67], v[36:39]\nds_read2st64_b32 v[74:75], v225 offset0:10 offset1:11\n"
"M 144,176,44\nR 174,190,45056\ns_add_u32 m0, s50, 49152\nM 144,180,60\nds_read2st64_b32 v[76:77], v225 offset0:12 offset1:13\nM 148,160,44\n"
"R 176,191,32768\ns_add_u32 s63, s59, 786432\nG 6\nv_mfma_f32_16x16x32_bf16 v[40:43], v[208:211], v[68:71], v[40:43]\nv_mfma_f32_16x16x32_bf16 v[44:47], v[210:213], v[68:71], v[44:47]\nds_read2st64_b32 v[78:79], v225 offset0:14 offset1:15\n"
"M 148,164,60\nbuffer_load_dwordx4 v220, s[16:19], s63 offen lds\nM 132,160,40\nR 178,191,36864\nM 132,164,56\nG 5\n"
"v_mfma_f32_16x16x32_bf16 v[48:51], v[208:211], v[72:75], v[48:51]\nv_mfma_f32_16x16x32_bf16 v[52:55], v[210:213], v[72:75], v[52:55]\nM 116,160,36\nR 180,191,40960\nM 116,164,52\nM 100,160,32\n"
"R 182,191,45056\nG 3\nv_mfma_f32_16x16x32_bf16 v[56:59], v[208:211], v[76:79], v[56:59]\nv_mfma_f32_16x16x32_bf16 v[60:63], v[210:213], v[76:79], v[60:63]\nM 100,164,48\nM 104,168,32\n"
"R 160,188,49152\nM 104,172,48\nbuffer_load_dwordx4 v221, s[16:19], s63 offen offset:1024 lds\nM 120,168,36\nR 162,188,53248\nv_mov_b32 v226, s46\n"
"M 120,172,52\nv_mov_b32 v227, s47\nM 136,168,40\nR 164,184,0\ns_bfe_u32 s62, s56, 0x20007\ns_lshl_b32 s62, s62, 5\n"
"s_add_u32 s61, s74, 548864\ns_add_u32 s61, s61, s62\ns_waitcnt vmcnt(6)\ns_mov_b64 exec, 1\nbuffer_atomic_swap_x2 v[226:227], off, s[20:23], s61 sc1\ns_mov_b64 exec, -1\n"
"v_cvt_pk_bf16_f32 v32, v32, v33\nM 136,172,56\ns_bfe_u32 s62, s57, 0x20007\ns_lshl_b32 s62, s62, 5\ns_add_u32 s61, s74, 548872\ns_add_u32 s61, s61, s62\n"
"s_mov_b64 exec, 1\nbuffer_atomic_swap_x2 v[226:227], off, s[20:23], s61 sc1\ns_mov_b64 exec, -1\nM 152,168,44\nR 166,184,4096\nv_cvt_pk_bf16_f32 v33, v34, v35\n"
"M 152,172,60\nG 4\nv_cvt_pk_bf16_f32 v34, v36, v37\nv_cvt_pk_bf16_f32 v35, v38, v39\nM 156,176,44\nR 168,189,49152\n"
"s_bfe_u32 s100, s58, 0x20007\ns_mul_i32 s100, s100, 49152\ns_add_u32 s100, s100, s72\ns_add_u32 s100, s100, 52461568\nv_cvt_pk_bf16_f32 v48, v48, v49\nM 156,180,60\n"
"buffer_store_dwordx4 v[32:35], v252, s[20:23], s100 offen offset:0 sc1\nM 140,176,40\nR 170,189,53248\nv_cvt_pk_bf16_f32 v40, v40, v41\nM 140,180,56\nv_cvt_pk_bf16_f32 v41, v42, v43\n"
"v_cvt_pk_bf16_f32 v42, v44, v45\nM 124,176,36\nR 172,185,0\nv_cvt_pk_bf16_f32 v43, v46, v47\nM 124,180,52\nv_cvt_pk_bf16_f32 v49, v50, v51\n"
"v_cvt_pk_bf16_f32 v50, v52, v53\nM 108,176,32\nR 174,185,4096\nv_cvt_pk_bf16_f32 v51, v54, v55\nM 108,180,48\nbuffer_store_dwordx4 v[40:43], v252, s[20:23], s100 offen offset:1024 sc1\n"
"s_barrier\nG 4\nM 96,160,64\nR 176,190,49152\nv_cvt_pk_bf16_f32 v56, v56, v57\nM 96,164,80\n"
"v_cvt_pk_bf16_f32 v57, v58, v59\nv_cvt_pk_bf16_f32 v58, v60, v61\nM 112,160,68\nR 178,190,53248\nv_cvt_pk_bf16_f32 v59, v62, v63\nM 112,164,84\n"
"buffer_store_dwordx4 v[48:51], v252, s[20:23], s100 offen offset:2048 sc1\nM 128,160,72\nR 180,186,0\nM 128,164,88\nM 144,160,76\nR 182,186,4096\n"
"M 144,164,92\nG 4\nM 148,168,76\nR 160,191,49152\nLq581:\ns_lshr_b32 s60, s45, 3\n"
"s_lshl2_add_u32 s60, s60, s40\ns_lshl_b32 s60, s60, 8\nv_lshl_add_u32 v224, v252, 3, s60\nv_add_u32 v224, Lq581+4096-LqPC, v224\nv_min_u32 v224, LqE-LqPC-4, v224\ns_mov_b64 exec, 3\n"
"global_load_dword v224, v224, s[98:99]\ns_mov_b64 exec, -1\nM 148,172,92\nbuffer_store_dwordx4 v[56:59], v252, s[20:23], s100 offen offset:3072 sc1\nM 132,168,72\nR 162,191,53248\n"
"M 132,172,88\nM 116,168,68\nR 164,187,0\nM 116,172,84\nM 100,168,64\nR 166,187,4096\n"
"v_mov_b32 v248, 0\nM 100,172,80\nG 4\nv_mov_b32 v249, 0\nM 104,176,64\nR 168,184,8192\n"
"s_add_u32 s61, s74, 548864\ns_mov_b64 exec, 3\nbuffer_atomic_or_x2 v[248:249], v250, s[20:23], s61 offen sc0 sc1\ns_mov_b64 exec, -1\nM 104,180,80\nM 120,176,68\n"
"R 170,184,12288\nv_mov_b32 v242, v208\nM 120,180,84\ns_add_u32 m0, s50, 57344\nv_mov_b32 v243, v209\nv_mov_b32 v244, v208\n"
"M 136,176,72\nR 172,184,16384\nv_mov_b32 v245, v209\nM 136,180,88\ns_add_u32 s63, s59, 327680\nv_mov_b32 v64, v192\n"
"v_mov_b32 v65, v193\nM 152,176,76\nR 174,184,20480\nv_mov_b32 v70, v194\nM 152,180,92\nG 4\n"
"v_mov_b32 v71, v195\nv_mov_b32 v72, v196\nM 156,160,76\nR 176,185,8192\nv_mov_b32 v73, v197\nM 156,164,92\n"
"buffer_load_dwordx4 v220, s[16:19], s63 offen lds\nM 140,160,72\nR 178,185,12288\nv_mov_b32 v78, v198\nM 140,164,88\nv_mov_b32 v79, v199\n"
"v_mov_b32 v80, v200\nM 124,160,68\nR 180,185,16384\nv_mov_b32 v81, v201\nM 124,164,84\nbuffer_load_dwordx4 v221, s[16:19], s63 offen offset:1024 lds\n"
"M 108,160,64\nR 182,185,20480\ns_add_u32 m0, s50, 65536\nM 108,164,80\nG 4\nv_mov_b32 v86, v202\n"
"v_mov_b32 v87, v203\nM 96,168,96\nR 160,186,8192\ns_add_u32 s63, s59, 851968\nM 96,172,112\nbuffer_load_dwordx4 v220, s[16:19], s63 offen lds\n"
"M 112,168,100\nR 162,186,12288\nv_mov_b32 v88, v204\nM 112,172,116\nv_mov_b32 v89, v205\nv_mov_b32 v94, v206\n"
"M 128,168,104\nR 164,186,16384\nv_mov_b32 v95, v207\nM 128,172,120\nbuffer_load_dwordx4 v221, s[16:19], s63 offen offset:1024 lds\nM 144,168,108\n"
"R 166,186,20480\nv_mov_b32 v32, v232\nM 144,172,124\nG 4\nv_mov_b32 v33, v233\nv_mov_b32 v38, v234\n"
"M 148,176,108\nR 168,187,8192\nv_mov_b32 v39, v235\nM 148,180,124\ns_movk_i32 s63, 6144\nv_mad_u32_u24 v220, v228, s63, v229\n"
"v_add_u32 v221, 3144704, v220\ns_add_u32 m0, s50, 73728\ns_add_u32 s63, s52, 2048\nbuffer_load_dwordx4 v220, s[4:7], s63 offen lds\nM 132,176,104\nR 170,187,12288\n"
"v_mov_b32 v40, v236\nM 132,180,120\nv_mov_b32 v41, v237\nv_mov_b32 v46, v238\nM 116,176,100\nR 172,187,16384\n"
"v_mov_b32 v47, v239\nM 116,180,116\nv_mov_b32 v48, v232\nv_mov_b32 v49, v233\nM 100,176,96\nR 174,187,20480\n"
"v_mov_b32 v54, v234\nM 100,180,112\nG 4\nv_mov_b32 v55, v235\nv_mov_b32 v56, v236\nM 104,160,96\n"
"R 176,184,24576\ns_waitcnt vmcnt(13)\ns_barrier\nM 104,164,112\nbuffer_load_dwordx4 v221, s[4:7], s63 offen offset:1024 lds\nM 120,160,100\n"
"R 178,184,28672\ns_add_u32 s63, s52, 198656\nM 120,164,116\ns_add_u32 m0, s50, 81920\nv_mov_b32 v57, v237\nv_mov_b32 v62, v238\n"
"M 136,160,104\nR 180,184,32768\nv_mov_b32 v63, v239\nM 136,164,120\nbuffer_load_dwordx4 v220, s[4:7], s63 offen lds\nM 152,160,108\n"
"R 182,184,36864\nM 152,164,124\nG 4\nM 156,168,108\nR 160,185,24576\nM 156,172,124\n"
"buffer_load_dwordx4 v221, s[4:7], s63 offen offset:1024 lds\nM 140,168,104\nR 162,185,28672\ns_add_u32 s63, s52, 4096\nM 140,172,120\ns_add_u32 m0, s50, 90112\n"
"M 124,168,100\nR 164,185,32768\nM 124,172,116\nbuffer_load_dwordx4 v220, s[4:7], s63 offen lds\nM 108,168,96\nR 166,185,36864\n"
"v_mov_b32 v226, s46\nM 108,172,112\nG 4\nv_mov_b32 v227, s47\nM 96,176,128\nR 168,186,24576\n"
"s_bfe_u32 s62, s58, 0x20007\ns_lshl_b32 s62, s62, 5\ns_add_u32 s61, s74, 548880\ns_add_u32 s61, s61, s62\ns_waitcnt vmcnt(10)\ns_mov_b64 exec, 1\n"
"buffer_atomic_swap_x2 v[226:227], off, s[20:23], s61 sc1\ns_mov_b64 exec, -1\nM 96,180,144\ns_mul_i32 s64, s43, 49152\nM 112,176,132\nR 170,186,28672\n"
"M 112,180,148\ns_add_u32 s64, s64, s72\nM 128,176,136\nR 172,186,32768\ns_waitcnt vmcnt(10)\nv_cmp_eq_u64 s[86:87], s[46:47], v[248:249]\n"
"s_lshr_b32 s60, s45, 3\ns_lshl2_add_u32 s60, s60, s40\ns_lshl_b32 s60, s60, 8\nv_lshl_add_u32 v224, v252, 3, s60\nv_add_u32 v224, LqM-LqPC, v224\nv_min_u32 v224, LqE-LqPC-4, v224\n"
"s_mov_b64 exec, 3\nglobal_load_dword v224, v224, s[98:99]\ns_mov_b64 exec, -1\nM 128,180,152\ns_add_u32 s64, s64, 52428800\nM 144,176,140\n"
"R 174,186,36864\ns_and_b32 s62, s86, 3\ns_cmp_eq_u32 s62, 3\ns_cbranch_scc0 Lq506\nLq507:\nM 144,180,156\n"
"G 4\nM 148,160,140\nR 176,187,24576\nM 148,164,156\nbuffer_load_dwordx4 v[2:5], v252, s[20:23], s64 offen offset:0 sc1\nM 132,160,136\n"
"R 178,187,28672\nM 132,164,152\nbuffer_load_dwordx4 v[10:13], v252, s[20:23], s64 offen offset:1024 sc1\nM 116,160,132\nR 180,187,32768\nM 116,164,148\n"
"s_mul_i32 s65, s43, 49152\nM 100,160,128\nR 182,187,36864\nM 100,164,144\nbuffer_load_dwordx4 v[18:21], v252, s[20:23], s64 offen offset:2048 sc1\nG 4\n"
"M 104,168,128\nR 160,184,40960\nM 104,172,144\nbuffer_load_dwordx4 v[26:29], v252, s[20:23], s64 offen offset:3072 sc1\nM 120,168,132\nR 162,184,45056\n"
"M 120,172,148\ns_add_u32 s65, s65, s72\nM 136,168,136\nR 164,184,49152\ns_add_u32 s65, s65, 52445184\nM 136,172,152\n"
"buffer_load_dwordx4 v[66:69], v252, s[20:23], s65 offen offset:0 sc1\nM 152,168,140\nR 166,184,53248\nM 152,172,156\nG 4\nM 156,176,140\n"
"R 168,185,40960\ns_waitcnt vmcnt(12)\ns_barrier\nM 156,180,156\nbuffer_load_dwordx4 v[74:77], v252, s[20:23], s65 offen offset:1024 sc1\nM 140,176,136\n"
"R 170,185,45056\nv_mov_b32 v246, 0\nM 140,180,152\nbuffer_load_dwordx4 v[82:85], v252, s[20:23], s65 offen offset:2048 sc1\nv_mov_b32 v247, 0\nM 124,176,132\n"
"R 172,185,49152\ns_add_u32 s61, s74, 548864\ns_mov_b64 exec, 4\nbuffer_atomic_or_x2 v[246:247], v250, s[20:23], s61 offen sc0 sc1\ns_mov_b64 exec, -1\nM 124,180,148\n"
"buffer_load_dwordx4 v[90:93], v252, s[20:23], s65 offen offset:3072 sc1\nM 108,176,128\nR 174,185,53248\nM 108,180,144\nG 4\nM 128,160,168\n"
"R 176,186,40960\nM 128,164,184\nR 178,186,45056\nM 144,160,172\nR 180,186,49152\nM 144,164,188\n"
"R 182,186,53248\nG 4\nM 148,168,172\nR 160,187,40960\nM 148,172,188\nR 162,187,45056\n"
"M 132,168,168\nR 164,187,49152\nM 132,172,184\nR 166,187,53248\nG 4\nM 136,176,168\n"
"R 168,184,57344\nM 136,180,184\nR 170,184,61440\nM 152,176,172\nR 172,188,0\nM 152,180,188\n"
"R 174,188,4096\nG 4\nM 156,160,172\nR 176,185,57344\nM 156,164,188\nR 178,185,61440\n"
"M 140,160,168\nR 180,189,0\nM 140,164,184\nR 182,189,4096\nG 4\nM 128,168,200\n"
"R 160,186,57344\nM 128,172,216\nR 162,186,61440\nM 144,168,204\nR 164,190,0\nM 144,172,220\n"
"R 166,190,4096\nG 4\nM 148,176,204\nR 168,187,57344\nM 148,180,220\nR 170,187,61440\n"
"M 132,176,200\nR 172,191,0\nM 132,180,216\nR 174,191,4096\nG 4\nM 136,160,200\n"
"R 176,184,40960\nbuffer_load_dwordx4 v221, s[4:7], s63 offen offset:1024 lds\nM 136,164,216\nR 178,184,45056\ns_add_u32 m0, s50, 98304\nM 152,160,204\n"
"R 180,184,49152\ns_add_u32 s63, s52, 200704\nM 152,164,220\nR 182,184,53248\nG 4\nM 156,168,204\n"
"R 160,185,40960\nbuffer_load_dwordx4 v220, s[4:7], s63 offen lds\nM 156,172,220\nR 162,185,45056\nM 140,168,200\nR 164,185,49152\n"
"M 140,172,216\nR 166,185,53248\nG 4\nM 96,176,160\nR 168,186,40960\nbuffer_load_dwordx4 v221, s[4:7], s63 offen offset:1024 lds\n"
"M 96,180,176\nR 170,186,45056\nLpg17:\ns_cbranch_execz LretW0z\ns_mov_b32 m0, s51\nM 112,176,164\n"
"R 172,186,49152\nv_cvt_pk_bf16_f32 v128, v128, v129\nv_cvt_pk_bf16_f32 v129, v130, v131\nM 112,180,180\nR 174,186,53248\nG 4\n"
"v_cvt_pk_bf16_f32 v130, v132, v133\nM 116,160,164\nR 176,187,40960\ns_mul_i32 s66, s43, 49152\nv_cvt_pk_bf16_f32 v131, v134, v135\nM 116,164,180\n"
"R 178,187,45056\ns_add_u32 s66, s66, s72\nv_cvt_pk_bf16_f32 v132, v136, v137\nM 100,160,160\nR 180,187,49152\ns_add_u32 s66, s66, 52461568\n"
"v_cvt_pk_bf16_f32 v133, v138, v139\nM 100,164,176\nR 182,187,53248\nG 4\nv_cvt_pk_bf16_f32 v134, v140, v141\nM 104,168,160\n"
"R 160,184,57344\ns_waitcnt vmcnt(4)\nv_cmp_eq_u64 s[86:87], s[46:47], v[246:247]\nv_cvt_pk_bf16_f32 v135, v142, v143\nM 104,172,176\nR 162,184,61440\n"
"v_cvt_pk_bf16_f32 v136, v144, v145\nv_cvt_pk_bf16_f32 v137, v146, v147\nM 120,168,164\nR 164,188,0\ns_and_b32 s62, s86, 4\ns_cmp_eq_u32 s62, 4\n"
"s_cbranch_scc0 Lq522\nLq523:\nv_cvt_pk_bf16_f32 v138, v148, v149\nM 120,172,180\nR 166,188,4096\nG 4\n"
"v_cvt_pk_bf16_f32 v139, v150, v151\nM 124,176,164\nR 168,185,57344\nbuffer_load_dwordx4 v[34:37], v252, s[20:23], s66 offen offset:0 sc1\nv_cvt_pk_bf16_f32 v140, v152, v153\nM 124,180,180\n"
"R 170,185,61440\nv_cvt_pk_bf16_f32 v141, v154, v155\nv_cvt_pk_bf16_f32 v142, v156, v157\nM 108,176,160\nR 172,189,0\nbuffer_load_dwordx4 v[42:45], v252, s[20:23], s66 offen offset:1024 sc1\n"
"ds_write_addtid_b32 v128 offset:0\nds_write_addtid_b32 v129 offset:256\nds_write_addtid_b32 v130 offset:512\nds_write_addtid_b32 v131 offset:768\nds_write_addtid_b32 v132 offset:1024\nds_write_addtid_b32 v133 offset:1280\n"
"ds_write_addtid_b32 v134 offset:1536\nds_write_addtid_b32 v135 offset:1792\nds_write_addtid_b32 v136 offset:2048\nds_write_addtid_b32 v137 offset:2304\nds_write_addtid_b32 v138 offset:2560\nds_write_addtid_b32 v139 offset:2816\n"
"ds_write_addtid_b32 v140 offset:3072\nds_write_addtid_b32 v141 offset:3328\nds_write_addtid_b32 v142 offset:3584\nv_cvt_pk_bf16_f32 v143, v158, v159\nds_write_addtid_b32 v143 offset:3840\nM 108,180,176\n"
"R 174,189,4096\ns_add_u32 m0, s50, 106496\ns_add_u32 s63, s52, 395264\nG 15\nM 96,160,192\nR 176,186,57344\n"
"buffer_load_dwordx4 v220, s[4:7], s63 offen lds\nM 96,164,208\nR 178,186,61440\nbuffer_load_dwordx4 v[50:53], v252, s[20:23], s66 offen offset:2048 sc1\nG 3\ns_barrier\n"
"M 112,160,196\nR 180,190,0\nds_read2st64_b32 v[0:1], v225 offset0:0 offset1:1\nM 112,164,212\nR 182,190,4096\nds_read2st64_b32 v[6:7], v225 offset0:2 offset1:3\n"
"G 6\nM 116,168,196\nR 160,187,57344\nbuffer_load_dwordx4 v[58:61], v252, s[20:23], s66 offen offset:3072 sc1\nds_read2st64_b32 v[8:9], v225 offset0:4 offset1:5\nM 116,172,212\n"
"R 162,187,61440\nbuffer_load_dwordx4 v221, s[4:7], s63 offen offset:1024 lds\nds_read2st64_b32 v[14:15], v225 offset0:6 offset1:7\nM 100,168,192\nR 164,191,0\ns_add_u32 m0, s50, 114688\n"
"s_add_u32 s63, s52, 591872\nds_read2st64_b32 v[16:17], v225 offset0:8 offset1:9\nM 100,172,208\nR 166,191,4096\nds_read2st64_b32 v[22:23], v225 offset0:10 offset1:11\nG 9\n"
"M 104,176,192\nbuffer_load_dwordx4 v220, s[4:7], s63 offen lds\nds_read2st64_b32 v[24:25], v225 offset0:12 offset1:13\nM 104,180,208\nds_read2st64_b32 v[30:31], v225 offset0:14 offset1:15\nM 120,176,196\n"
"buffer_load_dwordx4 v221, s[4:7], s63 offen offset:1024 lds\nM 120,180,212\nG 3\nM 124,160,196\nM 124,164,212\nM 108,160,192\n"
"M 108,164,208\nG 0\ns_barrier\ns_lshl_b32 s84, s75, 11\ns_add_u32 s84, s84, s49\ns_add_u32 s84, s84, 18874368\n"
"s_add_u32 s85, s84, 32768\nv_mov_b32 v238, s46\nv_mov_b32 v239, s47\nv_mfma_f32_16x16x32_bf16 v[96:99], v[242:245], v[64:67], v[96:99]\nv_mfma_f32_16x16x32_bf16 v[100:103], v[242:245], v[68:71], v[100:103]\nv_mfma_f32_16x16x32_bf16 v[104:107], v[242:245], v[72:75], v[104:107]\n"
"v_mfma_f32_16x16x32_bf16 v[108:111], v[242:245], v[76:79], v[108:111]\nv_mfma_f32_16x16x32_bf16 v[112:115], v[242:245], v[80:83], v[112:115]\nv_mfma_f32_16x16x32_bf16 v[116:119], v[242:245], v[84:87], v[116:119]\ns_waitcnt vmcnt(11)\nv_mfma_f32_16x16x32_bf16 v[120:123], v[242:245], v[88:91], v[120:123]\nv_mfma_f32_16x16x32_bf16 v[124:127], v[242:245], v[92:95], v[124:127]\n"
"v_mfma_f32_16x16x32_bf16 v[96:99], v[242:245], v[0:3], v[96:99]\nv_mfma_f32_16x16x32_bf16 v[100:103], v[242:245], v[4:7], v[100:103]\nv_mfma_f32_16x16x32_bf16 v[104:107], v[242:245], v[8:11], v[104:107]\nv_mfma_f32_16x16x32_bf16 v[108:111], v[242:245], v[12:15], v[108:111]\nv_mfma_f32_16x16x32_bf16 v[112:115], v[242:245], v[16:19], v[112:115]\nv_mfma_f32_16x16x32_bf16 v[116:119], v[242:245], v[20:23], v[116:119]\n"
"v_mfma_f32_16x16x32_bf16 v[120:123], v[242:245], v[24:27], v[120:123]\nv_mfma_f32_16x16x32_bf16 v[124:127], v[242:245], v[28:31], v[124:127]\ns_waitcnt vmcnt(7)\nv_mfma_f32_16x16x32_bf16 v[96:99], v[242:245], v[32:35], v[96:99]\nv_mfma_f32_16x16x32_bf16 v[100:103], v[242:245], v[36:39], v[100:103]\ns_waitcnt vmcnt(6)\n"
"v_mfma_f32_16x16x32_bf16 v[104:107], v[242:245], v[40:43], v[104:107]\nv_mfma_f32_16x16x32_bf16 v[108:111], v[242:245], v[44:47], v[108:111]\ns_waitcnt vmcnt(4)\nv_mfma_f32_16x16x32_bf16 v[112:115], v[242:245], v[48:51], v[112:115]\nv_mfma_f32_16x16x32_bf16 v[116:119], v[242:245], v[52:55], v[116:119]\ns_waitcnt vmcnt(3)\n"
"v_mfma_f32_16x16x32_bf16 v[120:123], v[242:245], v[56:59], v[120:123]\nv_mfma_f32_16x16x32_bf16 v[124:127], v[242:245], v[60:63], v[124:127]\nv_cvt_pk_bf16_f32 v192, v96, v97\nv_cvt_pk_bf16_f32 v193, v98, v99\nv_cvt_pk_bf16_f32 v194, v100, v101\nv_cvt_pk_bf16_f32 v195, v102, v103\n"
"buffer_store_dwordx4 v[192:195], v252, s[20:23], s84 offen offset:0 sc1\nv_cvt_pk_bf16_f32 v196, v104, v105\nv_cvt_pk_bf16_f32 v197, v106, v107\nv_cvt_pk_bf16_f32 v198, v108, v109\nv_cvt_pk_bf16_f32 v199, v110, v111\nbuffer_store_dwordx4 v[196:199], v252, s[20:23], s84 offen offset:1024 sc1\n"
"v_cvt_pk_bf16_f32 v200, v112, v113\nv_cvt_pk_bf16_f32 v201, v114, v115\nv_cvt_pk_bf16_f32 v202, v116, v117\nv_cvt_pk_bf16_f32 v203, v118, v119\nbuffer_store_dwordx4 v[200:203], v252, s[20:23], s85 offen offset:0 sc1\nv_cvt_pk_bf16_f32 v204, v120, v121\n"
"v_cvt_pk_bf16_f32 v205, v122, v123\nv_cvt_pk_bf16_f32 v206, v124, v125\nv_cvt_pk_bf16_f32 v207, v126, v127\nbuffer_store_dwordx4 v[204:207], v252, s[20:23], s85 offen offset:1024 sc1\ns_mov_b64 exec, 0\ns_cmp_eq_u32 s40, 1\n"
"s_cbranch_scc1 Lpg19\ns_cmp_eq_u32 s40, 2\ns_cbranch_scc1 Lpg20\ns_cmp_eq_u32 s40, 3\ns_cbranch_scc1 Lpg21\ns_branch Lpg18\n"
"LretW0n:\ns_mov_b64 exec, -1\ns_branch LqM\nLq116:\ns_lshl_b32 s61, s42, 13\ns_add_u32 s61, s61, 84017152\n"
"buffer_load_dwordx4 v[128:131], v230, s[20:23], s61 offen sc1\ns_lshl_b32 s61, s42, 13\ns_add_u32 s61, s61, 84021248\nbuffer_load_dwordx4 v[132:135], v230, s[20:23], s61 offen sc1\ns_waitcnt vmcnt(0)\nv_min3_u32 v232, v129, v131, v133\n"
"v_min3_u32 v232, v232, v135, v135\nv_max3_u32 v233, v129, v131, v133\nv_max3_u32 v233, v233, v135, v135\nv_cmp_ne_u32 s[86:87], s46, v232\nv_cmp_ne_u32 s[88:89], s46, v233\ns_nop 0\n"
"s_or_b64 s[86:87], s[86:87], s[88:89]\ns_cbranch_scc0 Lq117\ns_sub_u32 s48, s48, 1\ns_cmp_gt_i32 s48, 0\ns_cbranch_scc1 Lq116\ns_branch Lq117\n"
"Lq124:\ns_lshl_b32 s61, s42, 13\ns_add_u32 s61, s61, 84148224\nbuffer_load_dwordx4 v[144:147], v234, s[20:23], s61 offen sc1\ns_lshl_b32 s61, s42, 13\ns_add_u32 s61, s61, 84152320\n"
"buffer_load_dwordx4 v[148:151], v234, s[20:23], s61 offen sc1\ns_waitcnt vmcnt(0)\nv_min3_u32 v236, v145, v147, v149\nv_min3_u32 v236, v236, v151, v151\nv_max3_u32 v237, v145, v147, v149\nv_max3_u32 v237, v237, v151, v151\n"
"v_cmp_ne_u32 s[86:87], s46, v236\nv_cmp_ne_u32 s[88:89], s46, v237\ns_nop 0\ns_or_b64 s[86:87], s[86:87], s[88:89]\ns_cbranch_scc0 Lq125\ns_sub_u32 s48, s48, 1\n"
"s_cmp_gt_i32 s48, 0\ns_cbranch_scc1 Lq124\ns_branch Lq125\nLq132:\ns_lshl_b32 s61, s42, 13\ns_add_u32 s61, s61, 83886080\n"
"buffer_load_dwordx4 v[128:131], v230, s[20:23], s61 offen sc1\ns_lshl_b32 s61, s42, 13\ns_add_u32 s61, s61, 83890176\nbuffer_load_dwordx4 v[132:135], v230, s[20:23], s61 offen sc1\ns_waitcnt vmcnt(0)\nv_min3_u32 v232, v129, v131, v133\n"
"v_min3_u32 v232, v232, v135, v135\nv_max3_u32 v233, v129, v131, v133\nv_max3_u32 v233, v233, v135, v135\nv_cmp_ne_u32 s[86:87], s46, v232\nv_cmp_ne_u32 s[88:89], s46, v233\ns_nop 0\n"
"s_or_b64 s[86:87], s[86:87], s[88:89]\ns_cbranch_scc0 Lq133\ns_sub_u32 s48, s48, 1\ns_cmp_gt_i32 s48, 0\ns_cbranch_scc1 Lq132\ns_branch Lq133\n"
"Lq140:\ns_lshr_b32 s84, s49, 4\ns_add_u32 s84, s84, 81788928\nbuffer_load_dwordx4 v[128:131], v252, s[20:23], s84 offen offset:0 sc1\nbuffer_load_dwordx4 v[132:135], v252, s[20:23], s84 offen offset:1024 sc1\nbuffer_load_dwordx4 v[136:139], v252, s[20:23], s84 offen offset:2048 sc1\n"
"buffer_load_dwordx4 v[140:143], v252, s[20:23], s84 offen offset:3072 sc1\ns_mov_b32 s85, 0x0c0c0400\ns_waitcnt vmcnt(3)\nv_perm_b32 v144, v129, v128, s85\nv_perm_b32 v148, v131, v130, s85\nv_lshl_or_b32 v144, v148, 16, v144\n"
"s_waitcnt vmcnt(2)\nv_perm_b32 v145, v133, v132, s85\nv_perm_b32 v149, v135, v134, s85\nv_lshl_or_b32 v145, v149, 16, v145\ns_waitcnt vmcnt(1)\nv_perm_b32 v146, v137, v136, s85\n"
"v_perm_b32 v150, v139, v138, s85\nv_lshl_or_b32 v146, v150, 16, v146\ns_waitcnt vmcnt(0)\nv_perm_b32 v147, v141, v140, s85\nv_perm_b32 v151, v143, v142, s85\nv_lshl_or_b32 v147, v151, 16, v147\n"
"v_min3_u32 v230, v144, v145, v146\nv_min3_u32 v230, v230, v147, v147\nv_max3_u32 v231, v144, v145, v146\nv_max3_u32 v231, v231, v147, v147\nv_cmp_ne_u32 s[86:87], s46, v230\nv_cmp_ne_u32 s[88:89], s46, v231\n"
"s_nop 0\ns_or_b64 s[86:87], s[86:87], s[88:89]\ns_cbranch_scc0 Lq141\ns_sub_u32 s48, s48, 1\ns_cmp_gt_i32 s48, 0\ns_cbranch_scc1 Lq140\n"
"s_branch Lq141\nLq187:\ns_mov_b64 exec, s[66:67]\nbuffer_load_dwordx4 v[230:233], v252, s[20:23], s65 offen offset:0 sc1\nbuffer_load_dwordx2 v[64:65], v227, s[20:23], s65 offen offset:2048 sc1\nbuffer_load_dwordx2 v[70:71], v227, s[20:23], s65 offen offset:2560 sc1\n"
"s_mov_b64 exec, -1\ns_waitcnt vmcnt(0)\nv_max3_u32 v248, v233, v65, v71\nv_cmp_eq_u32 vcc, -1, v248\ns_nop 1\ns_cbranch_vccz Lq188\n"
"s_sub_u32 s48, s48, 1\ns_cmp_gt_i32 s48, 0\ns_cbranch_scc1 Lq187\ns_branch Lq188\nLq189:\ns_mov_b64 exec, s[66:67]\n"
"buffer_load_dwordx4 v[234:237], v252, s[20:23], s65 offen offset:1024 sc1\nbuffer_load_dwordx2 v[76:77], v227, s[20:23], s65 offen offset:3072 sc1\nbuffer_load_dwordx2 v[82:83], v227, s[20:23], s65 offen offset:3584 sc1\ns_mov_b64 exec, -1\ns_waitcnt vmcnt(0)\nv_max3_u32 v248, v237, v77, v83\n"
"v_cmp_eq_u32 vcc, -1, v248\ns_nop 1\ns_cbranch_vccz Lq190\ns_sub_u32 s48, s48, 1\ns_cmp_gt_i32 s48, 0\ns_cbranch_scc1 Lq189\n"
"s_branch Lq190\nLq317:\ns_lshr_b32 s84, s49, 4\ns_add_u32 s84, s84, 82313216\nbuffer_load_dwordx4 v[0:3], v252, s[20:23], s84 offen offset:0 sc1\nbuffer_load_dwordx4 v[4:7], v252, s[20:23], s84 offen offset:1024 sc1\n"
"buffer_load_dwordx4 v[8:11], v252, s[20:23], s84 offen offset:2048 sc1\nbuffer_load_dwordx4 v[12:15], v252, s[20:23], s84 offen offset:3072 sc1\ns_mov_b32 s85, 0x0c0c0400\ns_waitcnt vmcnt(3)\nv_perm_b32 v16, v1, v0, s85\nv_perm_b32 v20, v3, v2, s85\n"
"v_lshl_or_b32 v16, v20, 16, v16\ns_waitcnt vmcnt(2)\nv_perm_b32 v17, v5, v4, s85\nv_perm_b32 v21, v7, v6, s85\nv_lshl_or_b32 v17, v21, 16, v17\ns_waitcnt vmcnt(1)\n"
"v_perm_b32 v18, v9, v8, s85\nv_perm_b32 v22, v11, v10, s85\nv_lshl_or_b32 v18, v22, 16, v18\ns_waitcnt vmcnt(0)\nv_perm_b32 v19, v13, v12, s85\nv_perm_b32 v23, v15, v14, s85\n"
"v_lshl_or_b32 v19, v23, 16, v19\nv_min3_u32 v230, v16, v17, v18\nv_min3_u32 v230, v230, v19, v19\nv_max3_u32 v231, v16, v17, v18\nv_max3_u32 v231, v231, v19, v19\nv_cmp_ne_u32 s[86:87], s46, v230\n"
"v_cmp_ne_u32 s[88:89], s46, v231\ns_nop 0\ns_or_b64 s[86:87], s[86:87], s[88:89]\ns_cbranch_scc0 Lq318\ns_sub_u32 s48, s48, 1\ns_cmp_gt_i32 s48, 0\n"
"s_cbranch_scc1 Lq317\ns_branch Lq318\nLq319:\ns_lshl_b32 s61, s42, 13\ns_add_u32 s61, s61, 84279296\nbuffer_load_dwordx4 v[0:3], v238, s[20:23], s61 offen sc1\n"
"s_lshl_b32 s61, s42, 13\ns_add_u32 s61, s61, 84283392\nbuffer_load_dwordx4 v[4:7], v238, s[20:23], s61 offen sc1\ns_waitcnt vmcnt(0)\nv_min3_u32 v240, v1, v3, v5\nv_min3_u32 v240, v240, v7, v7\n"
"v_max3_u32 v241, v1, v3, v5\nv_max3_u32 v241, v241, v7, v7\nv_cmp_ne_u32 s[86:87], s46, v240\nv_cmp_ne_u32 s[88:89], s46, v241\ns_nop 0\ns_or_b64 s[86:87], s[86:87], s[88:89]\n"
"s_cbranch_scc0 Lq320\ns_sub_u32 s48, s48, 1\ns_cmp_gt_i32 s48, 0\ns_cbranch_scc1 Lq319\ns_branch Lq320\nLq329:\n"
"s_lshl_b32 s61, s42, 13\ns_add_u32 s61, s61, 84410368\nbuffer_load_dwordx4 v[16:19], v242, s[20:23], s61 offen sc1\ns_lshl_b32 s61, s42, 13\ns_add_u32 s61, s61, 84414464\nbuffer_load_dwordx4 v[20:23], v242, s[20:23], s61 offen sc1\n"
"s_waitcnt vmcnt(0)\nv_min3_u32 v244, v17, v19, v21\nv_min3_u32 v244, v244, v23, v23\nv_max3_u32 v245, v17, v19, v21\nv_max3_u32 v245, v245, v23, v23\nv_cmp_ne_u32 s[86:87], s46, v244\n"
"v_cmp_ne_u32 s[88:89], s46, v245\ns_nop 0\ns_or_b64 s[86:87], s[86:87], s[88:89]\ns_cbranch_scc0 Lq330\ns_sub_u32 s48, s48, 1\ns_cmp_gt_i32 s48, 0\n"
"s_cbranch_scc1 Lq329\ns_branch Lq330\nLq339:\ns_lshl_b32 s61, s42, 13\ns_add_u32 s61, s61, 84541440\nbuffer_load_dwordx4 v[0:3], v238, s[20:23], s61 offen sc1\n"
"s_lshl_b32 s61, s42, 13\ns_add_u32 s61, s61, 84545536\nbuffer_load_dwordx4 v[4:7], v238, s[20:23], s61 offen sc1\ns_waitcnt vmcnt(0)\nv_min3_u32 v240, v1, v3, v5\nv_min3_u32 v240, v240, v7, v7\n"
"v_max3_u32 v241, v1, v3, v5\nv_max3_u32 v241, v241, v7, v7\nv_cmp_ne_u32 s[86:87], s46, v240\nv_cmp_ne_u32 s[88:89], s46, v241\ns_nop 0\ns_or_b64 s[86:87], s[86:87], s[88:89]\n"
"s_cbranch_scc0 Lq340\ns_sub_u32 s48, s48, 1\ns_cmp_gt_i32 s48, 0\ns_cbranch_scc1 Lq339\ns_branch Lq340\nLq349:\n"
"s_lshl_b32 s61, s42, 13\ns_add_u32 s61, s61, 84672512\nbuffer_load_dwordx4 v[16:19], v242, s[20:23], s61 offen sc1\ns_lshl_b32 s61, s42, 13\ns_add_u32 s61, s61, 84676608\nbuffer_load_dwordx4 v[20:23], v242, s[20:23], s61 offen sc1\n"
"s_waitcnt vmcnt(0)\nv_min3_u32 v244, v17, v19, v21\nv_min3_u32 v244, v244, v23, v23\nv_max3_u32 v245, v17, v19, v21\nv_max3_u32 v245, v245, v23, v23\nv_cmp_ne_u32 s[86:87], s46, v244\n"
"v_cmp_ne_u32 s[88:89], s46, v245\ns_nop 0\ns_or_b64 s[86:87], s[86:87], s[88:89]\ns_cbranch_scc0 Lq350\ns_sub_u32 s48, s48, 1\ns_cmp_gt_i32 s48, 0\n"
"s_cbranch_scc1 Lq349\ns_branch Lq350\nLq377:\nv_mov_b32 v248, 0\nv_mov_b32 v249, 0\ns_add_u32 s61, s82, 385024\n"
"s_mov_b64 exec, 0xf\nbuffer_atomic_or_x2 v[248:249], v250, s[20:23], s61 offen sc0 sc1\ns_mov_b64 exec, -1\ns_waitcnt vmcnt(0)\nv_cmp_eq_u64 s[86:87], s[46:47], v[248:249]\ns_nop 1\n"
"s_and_b32 s62, s86, 0xf\ns_cmp_eq_u32 s62, 0xf\ns_cbranch_scc1 Lq378\ns_sleep 1\ns_sub_u32 s48, s48, 1\ns_cmp_gt_i32 s48, 0\n"
"s_cbranch_scc1 Lq377\ns_branch Lq378\nLq412:\nv_mov_b32 v248, 0\nv_mov_b32 v249, 0\ns_add_u32 s61, s82, 385032\n"
"s_mov_b64 exec, 0xf\nbuffer_atomic_or_x2 v[248:249], v250, s[20:23], s61 offen sc0 sc1\ns_mov_b64 exec, -1\ns_waitcnt vmcnt(0)\nv_cmp_eq_u64 s[86:87], s[46:47], v[248:249]\ns_nop 1\n"
"s_and_b32 s62, s86, 0xf\ns_cmp_eq_u32 s62, 0xf\ns_cbranch_scc1 Lq413\ns_sleep 1\ns_sub_u32 s48, s48, 1\ns_cmp_gt_i32 s48, 0\n"
"s_cbranch_scc1 Lq412\ns_branch Lq413\nLq447:\nv_mov_b32 v248, 0\nv_mov_b32 v249, 0\ns_add_u32 s61, s82, 385048\n"
"s_mov_b64 exec, 0xf\nbuffer_atomic_or_x2 v[248:249], v250, s[20:23], s61 offen sc0 sc1\ns_mov_b64 exec, -1\ns_waitcnt vmcnt(0)\nv_cmp_eq_u64 s[86:87], s[46:47], v[248:249]\ns_nop 1\n"
"s_and_b32 s62, s86, 0xf\ns_cmp_eq_u32 s62, 0xf\ns_cbranch_scc1 Lq448\ns_sleep 1\ns_sub_u32 s48, s48, 1\ns_cmp_gt_i32 s48, 0\n"
"s_cbranch_scc1 Lq447\ns_branch Lq448\nLq466:\nv_mov_b32 v248, 0\nv_mov_b32 v249, 0\ns_add_u32 s61, s82, 385040\n"
"s_mov_b64 exec, 0xf\nbuffer_atomic_or_x2 v[248:249], v250, s[20:23], s61 offen sc0 sc1\ns_mov_b64 exec, -1\ns_waitcnt vmcnt(0)\nv_cmp_eq_u64 s[86:87], s[46:47], v[248:249]\ns_nop 1\n"
"s_and_b32 s62, s86, 0xf\ns_cmp_eq_u32 s62, 0xf\ns_cbranch_scc1 Lq467\ns_sleep 1\ns_sub_u32 s48, s48, 1\ns_cmp_gt_i32 s48, 0\n"
"s_cbranch_scc1 Lq466\ns_branch Lq467\nLq506:\nv_mov_b32 v248, 0\nv_mov_b32 v249, 0\ns_add_u32 s61, s74, 548864\n"
"s_mov_b64 exec, 3\nbuffer_atomic_or_x2 v[248:249], v250, s[20:23], s61 offen sc0 sc1\ns_mov_b64 exec, -1\ns_waitcnt vmcnt(0)\nv_cmp_eq_u64 s[86:87], s[46:47], v[248:249]\ns_nop 1\n"
"s_and_b32 s62, s86, 3\ns_cmp_eq_u32 s62, 3\ns_cbranch_scc1 Lq507\ns_sleep 1\ns_sub_u32 s48, s48, 1\ns_cmp_gt_i32 s48, 0\n"
"s_cbranch_scc1 Lq506\ns_branch Lq507\nLq522:\nv_mov_b32 v246, 0\nv_mov_b32 v247, 0\ns_add_u32 s61, s74, 548864\n"
"s_mov_b64 exec, 4\nbuffer_atomic_or_x2 v[246:247], v250, s[20:23], s61 offen sc0 sc1\ns_mov_b64 exec, -1\ns_waitcnt vmcnt(0)\nv_cmp_eq_u64 s[86:87], s[46:47], v[246:247]\ns_nop 1\n"
"s_and_b32 s62, s86, 4\ns_cmp_eq_u32 s62, 4\ns_cbranch_scc1 Lq523\ns_sleep 1\ns_sub_u32 s48, s48, 1\ns_cmp_gt_i32 s48, 0\n"
"s_cbranch_scc1 Lq522\ns_branch Lq523\nLqM:\ns_add_u32 s61, s79, 88358912\ns_lshl_b32 s62, s75, 3\ns_add_u32 s61, s61, s62\n"
"s_waitcnt vmcnt(0)\ns_mov_b64 exec, 1\nbuffer_atomic_swap_x2 v[238:239], off, s[20:23], s61 sc1\ns_mov_b64 exec, -1\nLqq1:\nv_bfe_u32 v250, v252, 7, 1\n"
"v_mul_u32_u24 v250, 2176, v250\nv_bfe_u32 v251, v252, 4, 3\nv_lshl_add_u32 v250, v251, 3, v250\ns_lshl_b32 s60, s90, 6\nv_add_u32 v250, s60, v250\ns_sleep 3\n"
"v_mov_b32 v248, 0\nv_mov_b32 v249, 0\ns_add_u32 s61, s80, 88358912\ns_mov_b64 exec, 0xffff\nbuffer_atomic_or_x2 v[248:249], v250, s[20:23], s61 offen sc0 sc1\ns_mov_b64 exec, -1\n"
"s_add_u32 m0, s50, 0\ns_add_u32 s63, s52, 397312\nbuffer_load_dwordx4 v220, s[4:7], s63 offen lds\nbuffer_load_dwordx4 v221, s[4:7], s63 offen offset:1024 lds\ns_add_u32 m0, s50, 8192\ns_add_u32 s63, s52, 593920\n"
"buffer_load_dwordx4 v220, s[4:7], s63 offen lds\nbuffer_load_dwordx4 v221, s[4:7], s63 offen offset:1024 lds\ns_add_u32 m0, s50, 16384\ns_add_u32 s63, s52, 788480\nbuffer_load_dwordx4 v220, s[4:7], s63 offen lds\nbuffer_load_dwordx4 v221, s[4:7], s63 offen offset:1024 lds\n"
"s_add_u32 m0, s50, 24576\ns_add_u32 s63, s52, 985088\nbuffer_load_dwordx4 v220, s[4:7], s63 offen lds\nbuffer_load_dwordx4 v221, s[4:7], s63 offen offset:1024 lds\ns_add_u32 m0, s50, 32768\ns_add_u32 s63, s52, 790528\n"
"buffer_load_dwordx4 v220, s[4:7], s63 offen lds\nbuffer_load_dwordx4 v221, s[4:7], s63 offen offset:1024 lds\ns_waitcnt vmcnt(10)\nv_cmp_eq_u64 s[86:87], s[46:47], v[248:249]\ns_nop 1\ns_and_b32 s88, s86, 0xffff\n"
"s_cmp_eq_u32 s88, 0xffff\ns_cbranch_scc1 Lq595\nv_mov_b32 v246, 0\nv_mov_b32 v247, 0\ns_add_u32 s61, s80, 88358912\ns_mov_b64 exec, 0xffff\n"
"buffer_atomic_or_x2 v[246:247], v250, s[20:23], s61 offen sc0 sc1\ns_mov_b64 exec, -1\nLq595:\ns_add_u32 m0, s50, 40960\ns_add_u32 s63, s52, 987136\nbuffer_load_dwordx4 v220, s[4:7], s63 offen lds\n"
"buffer_load_dwordx4 v221, s[4:7], s63 offen offset:1024 lds\ns_mov_b64 exec, 0\ns_cmp_eq_u32 s40, 1\ns_cbranch_scc1 LretQ1z\ns_cmp_eq_u32 s40, 2\ns_cbranch_scc1 LretQ1z\n"
"s_cmp_eq_u32 s40, 3\ns_cbranch_scc1 LretQ1z\ns_branch LretQ1z\nLretQ1z:\ns_mov_b64 exec, -1\nv_mov_b32 v230, 0\n"
"v_mov_b32 v231, 0\nv_mov_b32 v232, 0\nv_mov_b32 v233, 0\nv_dot2c_f32_bf16 v230, s76, v192\nv_dot2c_f32_bf16 v231, v192, v192\nv_dot2c_f32_bf16 v232, s76, v200\n"
"v_dot2c_f32_bf16 v233, v200, v200\nv_dot2c_f32_bf16 v230, s76, v193\nv_dot2c_f32_bf16 v231, v193, v193\nv_dot2c_f32_bf16 v232, s76, v201\nv_dot2c_f32_bf16 v233, v201, v201\nv_dot2c_f32_bf16 v230, s76, v194\n"
"v_dot2c_f32_bf16 v231, v194, v194\nv_dot2c_f32_bf16 v232, s76, v202\nv_dot2c_f32_bf16 v233, v202, v202\nv_dot2c_f32_bf16 v230, s76, v195\nv_dot2c_f32_bf16 v231, v195, v195\nv_dot2c_f32_bf16 v232, s76, v203\n"
"v_dot2c_f32_bf16 v233, v203, v203\nv_dot2c_f32_bf16 v230, s76, v196\nv_dot2c_f32_bf16 v231, v196, v196\nv_dot2c_f32_bf16 v232, s76, v204\nv_dot2c_f32_bf16 v233, v204, v204\nv_dot2c_f32_bf16 v230, s76, v197\n"
"v_dot2c_f32_bf16 v231, v197, v197\nv_dot2c_f32_bf16 v232, s76, v205\nv_dot2c_f32_bf16 v233, v205, v205\nv_dot2c_f32_bf16 v230, s76, v198\nv_dot2c_f32_bf16 v231, v198, v198\nv_dot2c_f32_bf16 v232, s76, v206\n"
"v_dot2c_f32_bf16 v233, v206, v206\nv_dot2c_f32_bf16 v230, s76, v199\nv_dot2c_f32_bf16 v231, v199, v199\nv_dot2c_f32_bf16 v232, s76, v207\nv_dot2c_f32_bf16 v233, v207, v207\nv_lshrrev_b32 v234, 5, v252\n"
"v_and_b32 v234, 24, v234\nv_bfe_u32 v235, s46, v234, 8\nv_lshrrev_b32 v234, 1, v234\nv_and_b32 v236, 0xf0, v252\nv_add_u32 v234, v234, v236\nv_permlane32_swap_b32 v230, v231\n"
"v_permlane32_swap_b32 v232, v233\nv_add_f32 v230, v230, v231\nv_add_f32 v232, v232, v233\ns_nop 1\nv_permlane16_swap_b32 v230, v232\nv_add_f32 v230, v230, v232\n"
"s_movk_i32 s62, 0xff\nv_bfi_b32 v230, s62, v235, v230\ns_lshr_b32 s61, s49, 4\ns_lshl1_add_u32 s61, s53, s61\ns_add_u32 s61, s61, 82837504\nbuffer_store_dword v230, v234, s[20:23], s61 offen sc1\n"
"s_cmp_eq_u32 s88, 0xffff\ns_cbranch_scc1 Lq596\ns_waitcnt vmcnt(3)\nv_mov_b32 v248, v246\nv_mov_b32 v249, v247\nv_cmp_eq_u64 s[86:87], s[46:47], v[248:249]\n"
"s_nop 1\ns_and_b32 s62, s86, 0xffff\ns_cmp_eq_u32 s62, 0xffff\ns_cbranch_scc1 Lq587\nLq586:\nv_mov_b32 v248, 0\n"
"v_mov_b32 v249, 0\ns_add_u32 s61, s80, 88358912\ns_mov_b64 exec, 0xffff\nbuffer_atomic_or_x2 v[248:249], v250, s[20:23], s61 offen sc0 sc1\ns_mov_b64 exec, -1\ns_waitcnt vmcnt(0)\n"
"v_cmp_eq_u64 s[86:87], s[46:47], v[248:249]\ns_nop 1\ns_and_b32 s62, s86, 0xffff\ns_cmp_eq_u32 s62, 0xffff\ns_cbranch_scc1 Lq587\nLpg18:\n"
"s_cbranch_execz Lpg22\ns_sub_u32 s48, s48, 1\ns_cmp_gt_i32 s48, 0\ns_cbranch_scc1 Lq586\nLq587:\nLq596:\n"
"s_add_u32 s64, s70, 18874368\nbuffer_load_dwordx4 a[0:3], v252, s[20:23], s64 offen offset:0 sc1\ns_add_u32 s65, s70, 18907136\nbuffer_load_dwordx4 a[4:7], v252, s[20:23], s65 offen offset:0 sc1\ns_add_u32 s66, s71, 18874368\nbuffer_load_dwordx4 a[8:11], v252, s[20:23], s66 offen offset:0 sc1\n"
"s_add_u32 s67, s71, 18907136\nbuffer_load_dwordx4 a[12:15], v252, s[20:23], s67 offen offset:0 sc1\nbuffer_load_dwordx4 a[16:19], v252, s[20:23], s64 offen offset:1024 sc1\nbuffer_load_dwordx4 a[20:23], v252, s[20:23], s65 offen offset:1024 sc1\nbuffer_load_dwordx4 a[24:27], v252, s[20:23], s66 offen offset:1024 sc1\nbuffer_load_dwordx4 a[28:31], v252, s[20:23], s67 offen offset:1024 sc1\n"
"s_barrier\nR 160,188,8192\nR 162,188,12288\nR 164,188,16384\nR 166,188,20480\nR 168,189,8192\n"
"R 170,189,12288\nR 172,189,16384\nR 174,189,20480\nR 176,190,8192\nR 178,190,12288\nR 180,190,16384\n"
"R 182,190,20480\nbuffer_load_dwordx4 a[32:35], v252, s[20:23], s64 offen offset:2048 sc1\nbuffer_load_dwordx4 a[36:39], v252, s[20:23], s65 offen offset:2048 sc1\nbuffer_load_dwordx4 a[40:43], v252, s[20:23], s66 offen offset:2048 sc1\nbuffer_load_dwordx4 a[44:47], v252, s[20:23], s67 offen offset:2048 sc1\nbuffer_load_dwordx4 a[48:51], v252, s[20:23], s64 offen offset:3072 sc1\n"
"buffer_load_dwordx4 a[52:55], v252, s[20:23], s65 offen offset:3072 sc1\nbuffer_load_dwordx4 a[56:59], v252, s[20:23], s66 offen offset:3072 sc1\nbuffer_load_dwordx4 a[60:63], v252, s[20:23], s67 offen offset:3072 sc1\ns_waitcnt vmcnt(15) lgkmcnt(10)\nv_mfma_f32_16x16x32_bf16 v[0:3], v[160:163], a[0:3], 0\ns_add_u32 s64, s70, 18878464\n"
"buffer_load_dwordx4 a[64:67], v252, s[20:23], s64 offen offset:0 sc1\ns_waitcnt vmcnt(12) lgkmcnt(8)\nM 0,164,16\nv_mfma_f32_16x16x32_bf16 v[16:19], v[160:163], a[4:7], 0\ns_waitcnt vmcnt(11)\nM 16,164,20\n"
"v_mfma_f32_16x16x32_bf16 v[32:35], v[160:163], a[8:11], 0\ns_waitcnt vmcnt(10)\nM 32,164,24\nv_mfma_f32_16x16x32_bf16 v[48:51], v[160:163], a[12:15], 0\ns_waitcnt vmcnt(9)\nM 48,164,28\n"
"G 4\nv_mfma_f32_16x16x32_bf16 v[52:55], v[168:171], a[12:15], 0\nR 160,191,8192\nM 52,172,28\ns_add_u32 s65, s70, 18911232\nbuffer_load_dwordx4 a[68:71], v252, s[20:23], s65 offen offset:0 sc1\n"
"v_mfma_f32_16x16x32_bf16 v[36:39], v[168:171], a[8:11], 0\nR 162,191,12288\nM 36,172,24\nv_mfma_f32_16x16x32_bf16 v[20:23], v[168:171], a[4:7], 0\nR 164,191,16384\nM 20,172,20\n"
"v_mfma_f32_16x16x32_bf16 v[4:7], v[168:171], a[0:3], 0\nR 166,191,20480\nM 4,172,16\nG 4\nv_mfma_f32_16x16x32_bf16 v[8:11], v[176:179], a[0:3], 0\nR 168,188,24576\n"
"M 8,180,16\ns_add_u32 s66, s71, 18878464\nbuffer_load_dwordx4 a[72:75], v252, s[20:23], s66 offen offset:0 sc1\nv_mfma_f32_16x16x32_bf16 v[24:27], v[176:179], a[4:7], 0\nR 170,188,28672\nM 24,180,20\n"
"s_add_u32 m0, s50, 49152\nv_mfma_f32_16x16x32_bf16 v[40:43], v[176:179], a[8:11], 0\nR 172,188,32768\ns_add_u32 s63, s52, 1181696\nM 40,180,24\ns_add_u32 s67, s71, 18911232\n"
"buffer_load_dwordx4 a[76:79], v252, s[20:23], s67 offen offset:0 sc1\nv_mfma_f32_16x16x32_bf16 v[56:59], v[176:179], a[12:15], 0\nR 174,188,36864\nM 56,180,28\nG 4\nv_mfma_f32_16x16x32_bf16 v[60:63], v[160:163], a[12:15], 0\n"
"R 176,189,24576\nM 60,164,28\nbuffer_load_dwordx4 v220, s[4:7], s63 offen lds\nv_mfma_f32_16x16x32_bf16 v[44:47], v[160:163], a[8:11], 0\nR 178,189,28672\nM 44,164,24\n"
"v_mfma_f32_16x16x32_bf16 v[28:31], v[160:163], a[4:7], 0\nR 180,189,32768\nM 28,164,20\nbuffer_load_dwordx4 a[80:83], v252, s[20:23], s64 offen offset:1024 sc1\nv_mfma_f32_16x16x32_bf16 v[12:15], v[160:163], a[0:3], 0\nR 182,189,36864\n"
"M 12,164,16\nG 4\nv_mfma_f32_16x16x32_bf16 v[64:67], a[0:3], v[168:171], 0\nR 160,190,24576\nN 64,16,172\nv_mfma_f32_16x16x32_bf16 v[68:71], a[4:7], v[168:171], 0\n"
"R 162,190,28672\nN 68,20,172\nbuffer_load_dwordx4 a[84:87], v252, s[20:23], s65 offen offset:1024 sc1\nv_mfma_f32_16x16x32_bf16 v[96:99], a[8:11], v[168:171], 0\nR 164,190,32768\nN 96,24,172\n"
"v_mfma_f32_16x16x32_bf16 v[100:103], a[12:15], v[168:171], 0\nR 166,190,36864\nN 100,28,172\ns_barrier\nG 4\nv_mfma_f32_16x16x32_bf16 v[108:111], a[12:15], v[176:179], 0\n"
"R 168,191,24576\nN 108,28,180\nbuffer_load_dwordx4 v221, s[4:7], s63 offen offset:1024 lds\nv_mfma_f32_16x16x32_bf16 v[104:107], a[8:11], v[176:179], 0\nR 170,191,28672\nN 104,24,180\n"
"buffer_load_dwordx4 a[88:91], v252, s[20:23], s66 offen offset:1024 sc1\nv_mfma_f32_16x16x32_bf16 v[76:79], a[4:7], v[176:179], 0\nR 172,191,32768\nN 76,20,180\nv_mfma_f32_16x16x32_bf16 v[72:75], a[0:3], v[176:179], 0\nR 174,191,36864\n"
"N 72,16,180\nG 4\nv_mfma_f32_16x16x32_bf16 v[80:83], a[0:3], v[160:163], 0\nR 176,188,40960\ns_add_u32 s63, s52, 1378304\nN 80,16,164\n"
"buffer_load_dwordx4 a[92:95], v252, s[20:23], s67 offen offset:1024 sc1\nv_mfma_f32_16x16x32_bf16 v[84:87], a[4:7], v[160:163], 0\nR 178,188,45056\nN 84,20,164\ns_add_u32 m0, s50, 57344\nv_mfma_f32_16x16x32_bf16 v[112:115], a[8:11], v[160:163], 0\n"
"R 180,188,49152\nN 112,24,164\nbuffer_load_dwordx4 v220, s[4:7], s63 offen lds\nv_mfma_f32_16x16x32_bf16 v[116:119], a[12:15], v[160:163], 0\nR 182,188,53248\nN 116,28,164\n"
"G 4\nv_mfma_f32_16x16x32_bf16 v[124:127], a[12:15], v[168:171], 0\nR 160,189,40960\nN 124,28,172\nbuffer_load_dwordx4 a[96:99], v252, s[20:23], s64 offen offset:2048 sc1\nv_mfma_f32_16x16x32_bf16 v[120:123], a[8:11], v[168:171], 0\n"
"R 162,189,45056\nN 120,24,172\nv_mfma_f32_16x16x32_bf16 v[92:95], a[4:7], v[168:171], 0\nR 164,189,49152\nN 92,20,172\nv_mfma_f32_16x16x32_bf16 v[88:91], a[0:3], v[168:171], 0\n"
"R 166,189,53248\nN 88,16,172\nG 4\ns_waitcnt vmcnt(19)\nM 0,176,32\nR 168,190,40960\n"
"s_waitcnt vmcnt(15)\nM 0,180,48\nbuffer_load_dwordx4 a[100:103], v252, s[20:23], s65 offen offset:2048 sc1\nM 16,176,36\nR 170,190,45056\ns_waitcnt vmcnt(15)\n"
"M 16,180,52\nM 32,176,40\nR 172,190,49152\ns_waitcnt vmcnt(14)\nM 32,180,56\nbuffer_load_dwordx4 v221, s[4:7], s63 offen offset:1024 lds\n"
"M 48,176,44\nR 174,190,53248\ns_waitcnt vmcnt(14)\nM 48,180,60\ns_barrier\nG 4\n"
"M 52,160,44\nR 176,191,40960\nM 52,164,60\nbuffer_load_dwordx4 a[104:107], v252, s[20:23], s66 offen offset:2048 sc1\nM 36,160,40\nR 178,191,45056\n"
"M 36,164,56\ns_add_u32 m0, s50, 65536\nM 20,160,36\nR 180,191,49152\ns_add_u32 s63, s52, 1183744\nM 20,164,52\n"
"buffer_load_dwordx4 a[108:111], v252, s[20:23], s67 offen offset:2048 sc1\nM 4,160,32\nR 182,191,53248\nM 4,164,48\nG 4\nM 8,168,32\n"
"R 160,184,0\nM 8,172,48\nbuffer_load_dwordx4 v220, s[4:7], s63 offen lds\nM 24,168,36\nR 162,184,4096\nM 24,172,52\n"
"M 40,168,40\nR 164,184,8192\nM 40,172,56\nbuffer_load_dwordx4 a[112:115], v252, s[20:23], s64 offen offset:3072 sc1\nM 56,168,44\nR 166,184,12288\n"
"M 56,172,60\nG 4\nM 60,176,44\nR 168,185,0\nM 60,180,60\nM 44,176,40\n"
"R 170,185,4096\nM 44,180,56\nbuffer_load_dwordx4 a[116:119], v252, s[20:23], s65 offen offset:3072 sc1\nM 28,176,36\nR 172,185,8192\nM 28,180,52\n"
"M 12,176,32\nR 174,185,12288\nM 12,180,48\nG 4\nN 64,32,160\nR 176,186,0\n"
"N 64,48,164\nbuffer_load_dwordx4 v221, s[4:7], s63 offen offset:1024 lds\nN 68,36,160\nR 178,186,4096\nN 68,52,164\nbuffer_load_dwordx4 a[120:123], v252, s[20:23], s66 offen offset:3072 sc1\n"
"N 96,40,160\nR 180,186,8192\nN 96,56,164\ns_lshr_b32 s84, s49, 4\nN 100,44,160\nR 182,186,12288\n"
"s_add_u32 s84, s84, 82837504\nN 100,60,164\ns_barrier\nG 4\nN 108,44,168\nR 160,187,0\n"
"N 108,60,172\nbuffer_load_dwordx4 a[124:127], v252, s[20:23], s67 offen offset:3072 sc1\nN 104,40,168\nR 162,187,4096\nN 104,56,172\nbuffer_load_dwordx4 v[128:131], v252, s[20:23], s84 offen offset:0 sc1\n"
"N 76,36,168\nR 164,187,8192\nN 76,52,172\nN 72,32,168\nR 166,187,12288\nN 72,48,172\n"
"buffer_load_dwordx4 v[132:135], v252, s[20:23], s84 offen offset:1024 sc1\nG 4\nN 80,32,176\nR 168,184,16384\nN 80,48,180\ns_add_u32 s64, s70, 18882560\n"
"buffer_load_dwordx4 a[128:131], v252, s[20:23], s64 offen offset:0 sc1\nN 84,36,176\nR 170,184,20480\nN 84,52,180\nbuffer_load_dwordx4 v[136:139], v252, s[20:23], s84 offen offset:2048 sc1\nN 112,40,176\n"
"R 172,184,24576\nN 112,56,180\nN 116,44,176\nR 174,184,28672\nN 116,60,180\nbuffer_load_dwordx4 v[140:143], v252, s[20:23], s84 offen offset:3072 sc1\n"
"G 4\nN 124,44,160\nR 176,185,16384\nN 124,60,164\ns_add_u32 s65, s70, 18915328\nbuffer_load_dwordx4 a[132:135], v252, s[20:23], s65 offen offset:0 sc1\n"
"N 120,40,160\nR 178,185,20480\nN 120,56,164\nN 92,36,160\nR 180,185,24576\nN 92,52,164\n"
"N 88,32,160\nR 182,185,28672\nN 88,48,164\nG 4\ns_waitcnt vmcnt(27)\nM 0,168,64\n"
"R 160,186,16384\ns_waitcnt vmcnt(22)\nM 0,172,80\ns_add_u32 s66, s71, 18882560\nbuffer_load_dwordx4 a[136:139], v252, s[20:23], s66 offen offset:0 sc1\nM 16,168,68\n"
"R 162,186,20480\ns_waitcnt vmcnt(22)\nM 16,172,84\nM 32,168,72\nR 164,186,24576\ns_waitcnt vmcnt(20)\n"
"M 32,172,88\ns_add_u32 s67, s71, 18915328\nbuffer_load_dwordx4 a[140:143], v252, s[20:23], s67 offen offset:0 sc1\nM 48,168,76\nR 166,186,28672\ns_waitcnt vmcnt(20)\n"
"M 48,172,92\ns_barrier\nG 4\nM 52,176,76\nR 168,187,16384\nM 52,180,92\n"
"M 36,176,72\nR 170,187,20480\nM 36,180,88\ns_add_u32 m0, s50, 73728\nM 20,176,68\nR 172,187,24576\n"
"s_add_u32 s63, s52, 1380352\nM 20,180,84\nbuffer_load_dwordx4 a[144:147], v252, s[20:23], s64 offen offset:1024 sc1\nM 4,176,64\nR 174,187,28672\nM 4,180,80\n"
"G 4\nM 8,160,64\nR 176,184,32768\nM 8,164,80\nbuffer_load_dwordx4 v220, s[4:7], s63 offen lds\nM 24,160,68\n"
"R 178,184,36864\nM 24,164,84\nbuffer_load_dwordx4 a[148:151], v252, s[20:23], s65 offen offset:1024 sc1\nM 40,160,72\nR 180,184,40960\nM 40,164,88\n"
"buffer_load_dwordx4 v221, s[4:7], s63 offen offset:1024 lds\nM 56,160,76\nR 182,184,45056\nM 56,164,92\nG 4\nM 60,168,76\n"
"R 160,185,32768\nM 60,172,92\nM 44,168,72\nR 162,185,36864\nM 44,172,88\nbuffer_load_dwordx4 a[152:155], v252, s[20:23], s66 offen offset:1024 sc1\n"
"M 28,168,68\nR 164,185,40960\nM 28,172,84\nM 12,168,64\nR 166,185,45056\nM 12,172,80\n"
"G 4\nN 64,64,176\nR 168,186,32768\nN 64,80,180\nbuffer_load_dwordx4 a[156:159], v252, s[20:23], s67 offen offset:1024 sc1\nN 68,68,176\n"
"R 170,186,36864\nN 68,84,180\nN 96,72,176\nR 172,186,40960\ns_mov_b32 s85, 0x0c0c0400\nN 96,88,180\n"
"s_add_u32 m0, s50, 81920\ns_waitcnt vmcnt(13)\nv_perm_b32 v144, v129, v128, s85\nv_perm_b32 v148, v131, v130, s85\nN 100,76,176\nR 174,186,45056\n"
"s_add_u32 s63, s52, 1574912\nN 100,92,180\ns_barrier\nG 4\nN 108,76,160\nR 176,187,32768\n"
"v_lshl_or_b32 v144, v148, 16, v144\nN 108,92,164\nbuffer_load_dwordx4 a[160:163], v252, s[20:23], s64 offen offset:2048 sc1\nN 104,72,160\nR 178,187,36864\ns_waitcnt vmcnt(13)\n"
"v_perm_b32 v145, v133, v132, s85\nN 104,88,164\nbuffer_load_dwordx4 v220, s[4:7], s63 offen lds\nN 76,68,160\nR 180,187,40960\nv_perm_b32 v149, v135, v134, s85\n"
"N 76,84,164\nbuffer_load_dwordx4 v221, s[4:7], s63 offen offset:1024 lds\nN 72,64,160\nR 182,187,45056\ns_add_u32 m0, s50, 90112\nN 72,80,164\n"
"G 4\nv_lshl_or_b32 v145, v149, 16, v145\ns_waitcnt vmcnt(13)\nv_perm_b32 v146, v137, v136, s85\nN 80,64,168\nR 160,184,49152\n"
"s_add_u32 s63, s52, 1771520\nN 80,80,172\nbuffer_load_dwordx4 a[164:167], v252, s[20:23], s65 offen offset:2048 sc1\nN 84,68,168\nR 162,184,53248\nv_perm_b32 v150, v139, v138, s85\n"
"N 84,84,172\nbuffer_load_dwordx4 v220, s[4:7], s63 offen lds\nN 112,72,168\nR 164,184,57344\nv_lshl_or_b32 v146, v150, 16, v146\nN 112,88,172\n"
"buffer_load_dwordx4 v221, s[4:7], s63 offen offset:1024 lds\nN 116,76,168\nR 166,184,61440\ns_waitcnt vmcnt(15)\nv_perm_b32 v147, v141, v140, s85\nN 116,92,172\n"
"G 4\nv_perm_b32 v151, v143, v142, s85\nv_lshl_or_b32 v147, v151, 16, v147\nN 124,76,176\nR 168,185,49152\nv_min3_u32 v230, v144, v145, v146\n"
"N 124,92,180\nbuffer_load_dwordx4 a[168:171], v252, s[20:23], s66 offen offset:2048 sc1\nN 120,72,176\nR 170,185,53248\nv_min3_u32 v230, v230, v147, v147\nN 120,88,180\n"
"v_max3_u32 v231, v144, v145, v146\nv_max3_u32 v231, v231, v147, v147\nN 92,68,176\nR 172,185,57344\nv_cmp_ne_u32 s[86:87], s46, v230\nN 92,84,180\n"
"buffer_load_dwordx4 a[172:175], v252, s[20:23], s67 offen offset:2048 sc1\nN 88,64,176\nR 174,185,61440\nv_cmp_ne_u32 s[88:89], s46, v231\nN 88,80,180\ns_or_b64 s[86:87], s[86:87], s[88:89]\n"
"s_cbranch_scc1 Lq645\nLq646:\nG 4\nM 0,160,96\nR 176,186,49152\nv_and_b32 v128, 0xffffff00, v128\n"
"M 0,164,112\nv_and_b32 v129, 0xffffff00, v129\nv_and_b32 v130, 0xffffff00, v130\nM 16,160,100\nR 178,186,53248\nv_and_b32 v131, 0xffffff00, v131\n"
"M 16,164,116\ns_add_u32 m0, s50, 98304\nv_and_b32 v132, 0xffffff00, v132\nv_and_b32 v133, 0xffffff00, v133\nM 32,160,104\nR 180,186,57344\n"
"s_add_u32 s63, s52, 1576960\nM 32,164,120\nbuffer_load_dwordx4 a[176:179], v252, s[20:23], s64 offen offset:3072 sc1\nM 48,160,108\nR 182,186,61440\nv_and_b32 v134, 0xffffff00, v134\n"
"M 48,164,124\ns_waitcnt vmcnt(11)\ns_barrier\nG 4\nM 52,168,108\nR 160,187,49152\n"
"v_and_b32 v135, 0xffffff00, v135\nM 52,172,124\nbuffer_load_dwordx4 v220, s[4:7], s63 offen lds\nM 36,168,104\nR 162,187,53248\nv_and_b32 v136, 0xffffff00, v136\n"
"M 36,172,120\nbuffer_load_dwordx4 a[180:183], v252, s[20:23], s65 offen offset:3072 sc1\nM 20,168,100\nR 164,187,57344\nv_and_b32 v137, 0xffffff00, v137\nM 20,172,116\n"
"buffer_load_dwordx4 v221, s[4:7], s63 offen offset:1024 lds\nM 4,168,96\nR 166,187,61440\ns_add_u32 m0, s50, 106496\nM 4,172,112\nG 4\n"
"v_and_b32 v138, 0xffffff00, v138\nv_and_b32 v139, 0xffffff00, v139\nM 8,176,96\nR 168,188,0\ns_add_u32 s63, s52, 1773568\nM 8,180,112\n"
"buffer_load_dwordx4 v220, s[4:7], s63 offen lds\nM 24,176,100\nR 170,188,4096\nv_and_b32 v140, 0xffffff00, v140\nM 24,180,116\nbuffer_load_dwordx4 a[184:187], v252, s[20:23], s66 offen offset:3072 sc1\n"
"M 40,176,104\nR 172,188,8192\nv_and_b32 v141, 0xffffff00, v141\nM 40,180,120\nbuffer_load_dwordx4 v221, s[4:7], s63 offen offset:1024 lds\nM 56,176,108\n"
"R 174,188,12288\nv_and_b32 v142, 0xffffff00, v142\nM 56,180,124\nG 4\nv_and_b32 v143, 0xffffff00, v143\nv_add_f32 v128, v128, v136\n"
"M 60,160,108\nR 176,189,0\nv_add_f32 v129, v129, v137\nM 60,164,124\nbuffer_load_dwordx4 a[188:191], v252, s[20:23], s67 offen offset:3072 sc1\nM 44,160,104\n"
"R 178,189,4096\nv_add_f32 v130, v130, v138\nM 44,164,120\nv_add_f32 v131, v131, v139\nv_add_f32 v132, v132, v140\nM 28,160,100\n"
"R 180,189,8192\nv_add_f32 v133, v133, v141\nM 28,164,116\nv_add_f32 v134, v134, v142\nv_add_f32 v135, v135, v143\nM 12,160,96\n"
"R 182,189,12288\nv_add_f32 v128, v128, v132\nM 12,164,112\nG 4\nv_add_f32 v129, v129, v133\nLpg19:\n"
"s_cbranch_execz LretW0n\nv_add_f32 v130, v130, v134\nN 64,96,168\nR 160,190,0\nv_add_f32 v131, v131, v135\nN 64,112,172\n"
"s_add_u32 s64, s70, 18886656\nbuffer_load_dwordx4 a[192:195], v252, s[20:23], s64 offen offset:0 sc1\nN 68,100,168\nR 162,190,4096\nv_permlane32_swap_b32 v128, v130\nN 68,116,172\n"
"v_permlane32_swap_b32 v129, v131\nv_add_f32 v128, v128, v130\nN 96,104,168\nR 164,190,8192\nv_add_f32 v129, v129, v131\nN 96,120,172\n"
"s_add_u32 m0, s50, 114688\nv_permlane16_swap_b32 v128, v129\nv_add_f32 v230, v128, v129\nN 100,108,168\nR 166,190,12288\ns_add_u32 s63, s52, 1968128\n"
"N 100,124,172\ns_waitcnt vmcnt(11)\ns_barrier\nG 4\nN 108,108,176\nR 168,191,0\n"
"v_mov_b32 v232, v230\nN 108,124,180\ns_add_u32 s65, s70, 18919424\nbuffer_load_dwordx4 a[196:199], v252, s[20:23], s65 offen offset:0 sc1\nN 104,104,176\nR 170,191,4096\n"
"v_permlane16_swap_b32 v230, v232\nN 104,120,180\nbuffer_load_dwordx4 v220, s[4:7], s63 offen lds\nN 76,100,176\nR 172,191,8192\nv_mov_b32 v231, v230\n"
"N 76,116,180\nbuffer_load_dwordx4 v221, s[4:7], s63 offen offset:1024 lds\nN 72,96,176\nR 174,191,12288\ns_add_u32 m0, s50, 0\nN 72,112,180\n"
"G 4\nv_mov_b32 v233, v232\nv_permlane32_swap_b32 v230, v231\nN 80,96,160\nR 176,188,16384\ns_add_u32 s63, s52, 2164736\n"
"N 80,112,164\ns_add_u32 s66, s71, 18886656\nbuffer_load_dwordx4 a[200:203], v252, s[20:23], s66 offen offset:0 sc1\nN 84,100,160\nR 178,188,20480\nv_permlane32_swap_b32 v232, v233\n"
"N 84,116,164\nbuffer_load_dwordx4 v220, s[4:7], s63 offen lds\nN 112,104,160\nR 180,188,24576\nv_mul_f32 v230, 0xba800000, v230\nN 112,120,164\n"
"s_add_u32 s67, s71, 18919424\nbuffer_load_dwordx4 a[204:207], v252, s[20:23], s67 offen offset:0 sc1\nN 116,108,160\nR 182,188,28672\nv_mul_f32 v214, v230, v230\nN 116,124,164\n"
"G 4\nv_mul_f32 v231, 0x3a800000, v231\nv_sub_f32 v231, v231, v214\nN 124,108,168\nR 160,189,16384\nbuffer_load_dwordx4 v221, s[4:7], s63 offen offset:1024 lds\n"
"N 124,124,172\nv_max_f32 v231, 0, v231\nv_add_f32 v231, 0x3727c5ac, v231\nN 120,104,168\nR 162,189,20480\nv_and_b32 v208, s77, v230\n"
"N 120,120,172\nv_rsq_f32 v234, v231\nN 92,100,168\nR 164,189,24576\nv_mul_f32 v216, v231, v234\nN 92,116,172\n"
"buffer_load_dwordx4 a[208:211], v252, s[20:23], s64 offen offset:1024 sc1\nN 88,96,168\nR 166,189,28672\nv_sub_f32 v230, v230, v208\nN 88,112,172\nG 4\n"
"v_cvt_pk_bf16_f32 v209, v230, v216\nv_cvt_pk_bf16_f32 v208, v208, v208\nM 0,176,128\nR 168,190,16384\nv_mov_b32 v214, v234\nM 0,180,144\n"
"s_mov_b64 s[60:61], 0xffff\nv_cndmask_b32 v208, 0, v208, s[60:61]\nv_mul_f32 v215, 0x3e8293ee, v234\nM 16,176,132\nR 170,190,20480\nv_mul_f32 v232, 0xba800000, v232\n"
"M 16,180,148\nbuffer_load_dwordx4 a[212:215], v252, s[20:23], s65 offen offset:1024 sc1\nM 32,176,136\nR 172,190,24576\ns_add_u32 s63, s52, 1970176\nM 32,180,152\n"
"s_add_u32 m0, s50, 8192\ns_mov_b64 s[60:61], 0xffff\nv_cndmask_b32 v209, 0, v209, s[60:61]\nM 48,176,140\nR 174,190,28672\nv_mul_f32 v217, v232, v232\n"
"M 48,180,156\ns_waitcnt vmcnt(11)\ns_barrier\nG 4\nM 52,160,140\nR 176,191,16384\n"
"v_mul_f32 v233, 0x3a800000, v233\nM 52,164,156\nbuffer_load_dwordx4 v220, s[4:7], s63 offen lds\nM 36,160,136\nR 178,191,20480\nv_sub_f32 v233, v233, v217\n"
"M 36,164,152\nbuffer_load_dwordx4 a[216:219], v252, s[20:23], s66 offen offset:1024 sc1\nM 20,160,132\nR 180,191,24576\nv_max_f32 v233, 0, v233\nM 20,164,148\n"
"buffer_load_dwordx4 v221, s[4:7], s63 offen offset:1024 lds\nM 4,160,128\nR 182,191,28672\ns_add_u32 m0, s50, 16384\nM 4,164,144\nG 4\n"
"v_add_f32 v233, 0x3727c5ac, v233\nv_and_b32 v212, s77, v232\nM 8,168,128\nR 160,188,32768\ns_add_u32 s63, s52, 2166784\nM 8,172,144\n"
"buffer_load_dwordx4 a[220:223], v252, s[20:23], s67 offen offset:1024 sc1\nM 24,168,132\nR 162,188,36864\nv_sub_f32 v232, v232, v212\nM 24,172,148\nbuffer_load_dwordx4 v220, s[4:7], s63 offen lds\n"
"M 40,168,136\nR 164,188,40960\nv_cvt_pk_bf16_f32 v212, v212, v212\nM 40,172,152\nbuffer_load_dwordx4 v221, s[4:7], s63 offen offset:1024 lds\nM 56,168,140\n"
"R 166,188,45056\nv_bfe_u32 v230, v252, 2, 6\nM 56,172,156\nG 4\nv_rsq_f32 v235, v233\nM 60,176,140\n"
"R 168,189,32768\nv_mul_f32 v219, v233, v235\nM 60,180,156\nbuffer_load_dwordx4 a[224:227], v252, s[20:23], s64 offen offset:2048 sc1\nM 44,176,136\nR 170,189,36864\n"
"v_cvt_pk_bf16_f32 v213, v232, v219\nM 44,180,152\ns_mov_b64 s[60:61], 0xffff\nv_cndmask_b32 v212, 0, v212, s[60:61]\nv_mov_b32 v217, v235\nM 28,176,132\n"
"R 172,189,40960\nv_mul_f32 v218, 0x3e8293ee, v235\nM 28,180,148\ns_mov_b64 s[60:61], 0xffff\nv_cndmask_b32 v213, 0, v213, s[60:61]\nM 12,176,128\n"
"R 174,189,45056\nM 12,180,144\nG 4\nN 64,128,160\nR 176,190,32768\nN 64,144,164\n"
"buffer_load_dwordx4 a[228:231], v252, s[20:23], s65 offen offset:2048 sc1\nN 68,132,160\nR 178,190,36864\nN 68,148,164\ns_lshl_b32 s60, s40, 7\ns_add_u32 s60, s60, 161280\n"
"v_add_u32 v230, s60, v230\nN 96,136,160\nR 180,190,40960\nN 96,152,164\ns_add_u32 m0, s50, 24576\nds_write_b32 v230, v214 offset:0\n"
"N 100,140,160\nR 182,190,45056\ns_add_u32 s63, s52, 2361344\nN 100,156,164\ns_waitcnt vmcnt(10)\ns_barrier\n"
"G 5\nN 108,140,168\nR 160,191,32768\nN 108,156,172\nbuffer_load_dwordx4 a[232:235], v252, s[20:23], s66 offen offset:2048 sc1\nN 104,136,168\n"
"R 162,191,36864\nN 104,152,172\nbuffer_load_dwordx4 v220, s[4:7], s63 offen lds\nN 76,132,168\nR 164,191,40960\nLq688:\n"
"s_lshr_b32 s60, s45, 3\ns_lshl2_add_u32 s60, s60, s40\ns_lshl_b32 s60, s60, 8\nv_lshl_add_u32 v224, v252, 3, s60\nv_add_u32 v224, Lq688+4096-LqPC, v224\nv_min_u32 v224, LqE-LqPC-4, v224\n"
"s_mov_b64 exec, 3\nglobal_load_dword v224, v224, s[98:99]\ns_mov_b64 exec, -1\nN 76,148,172\nbuffer_load_dwordx4 a[236:239], v252, s[20:23], s67 offen offset:2048 sc1\nN 72,128,168\n"
"R 166,191,45056\nN 72,144,172\nds_write_b32 v230, v217 offset:64\nG 5\nN 80,128,176\nR 168,188,49152\n"
"buffer_load_dwordx4 v221, s[4:7], s63 offen offset:1024 lds\nN 80,144,180\ns_add_u32 m0, s50, 32768\ns_add_u32 s63, s52, 2557952\nbuffer_load_dwordx4 v220, s[4:7], s63 offen lds\nN 84,132,176\n"
"R 170,188,53248\nN 84,148,180\nN 112,136,176\nR 172,184,0\nN 112,152,180\nbuffer_load_dwordx4 a[240:243], v252, s[20:23], s64 offen offset:3072 sc1\n"
"N 116,140,176\nR 174,184,4096\nN 116,156,180\nG 5\nN 124,140,160\nR 176,189,49152\n"
"buffer_load_dwordx4 v221, s[4:7], s63 offen offset:1024 lds\nN 124,156,164\nN 120,136,160\nR 178,189,53248\nN 120,152,164\nbuffer_load_dwordx4 a[244:247], v252, s[20:23], s65 offen offset:3072 sc1\n"
"N 92,132,160\nR 180,185,0\nN 92,148,164\nN 88,128,160\nR 182,185,4096\nN 88,144,164\n"
"G 4\nM 0,168,160\nR 160,190,49152\nM 0,172,176\nM 16,168,164\nR 162,190,53248\n"
"M 16,172,180\nbuffer_load_dwordx4 a[248:251], v252, s[20:23], s66 offen offset:3072 sc1\nM 32,168,168\nR 164,186,0\nM 32,172,184\ns_add_u32 m0, s50, 40960\n"
"M 48,168,172\nR 166,186,4096\ns_add_u32 s63, s52, 2754560\nM 48,172,188\ns_waitcnt vmcnt(12)\ns_barrier\n"
"G 4\nM 52,176,172\nR 168,191,49152\nM 52,180,188\nbuffer_load_dwordx4 a[252:255], v252, s[20:23], s67 offen offset:3072 sc1\nM 36,176,168\n"
"R 170,191,53248\nM 36,180,184\nbuffer_load_dwordx4 v220, s[4:7], s63 offen lds\nM 20,176,164\nR 172,187,0\nM 20,180,180\n"
"buffer_load_dwordx4 v221, s[4:7], s63 offen offset:1024 lds\nM 4,176,160\nR 174,187,4096\ns_add_u32 m0, s50, 49152\nM 4,180,176\nG 4\n"
"M 8,160,160\nR 176,184,8192\ns_add_u32 s63, s52, 2951168\nM 8,164,176\nbuffer_load_dwordx4 v220, s[4:7], s63 offen lds\nM 24,160,164\n"
"R 178,184,12288\nM 24,164,180\nM 40,160,168\nR 180,184,16384\nM 40,164,184\nbuffer_load_dwordx4 v221, s[4:7], s63 offen offset:1024 lds\n"
"M 56,160,172\nR 182,184,20480\nM 56,164,188\nG 4\nM 60,168,172\nR 160,185,8192\n"
"M 60,172,188\nM 44,168,168\nR 162,185,12288\nM 44,172,184\ns_add_u32 m0, s50, 57344\nM 28,168,164\n"
"R 164,185,16384\nM 28,172,180\ns_add_u32 s63, s52, 2363392\nM 12,168,160\nR 166,185,20480\nM 12,172,176\n"
"G 4\nN 64,160,176\nR 168,186,8192\nN 64,176,180\nbuffer_load_dwordx4 v220, s[4:7], s63 offen lds\nN 68,164,176\n"
"R 170,186,12288\nN 68,180,180\nN 96,168,176\nR 172,186,16384\nN 96,184,180\nbuffer_load_dwordx4 v221, s[4:7], s63 offen offset:1024 lds\n"
"N 100,172,176\nR 174,186,20480\ns_add_u32 m0, s50, 65536\nN 100,188,180\ns_waitcnt vmcnt(9)\ns_barrier\n"
"G 4\nN 108,172,160\nR 176,187,8192\ns_add_u32 s63, s52, 2560000\nN 108,188,164\nbuffer_load_dwordx4 v220, s[4:7], s63 offen lds\n"
"N 104,168,160\nR 178,187,12288\nN 104,184,164\nN 76,164,160\nR 180,187,16384\nN 76,180,164\n"
"N 72,160,160\nR 182,187,20480\nN 72,176,164\nG 4\nN 80,160,168\nR 160,184,24576\n"
"N 80,176,172\nbuffer_load_dwordx4 v221, s[4:7], s63 offen offset:1024 lds\nN 84,164,168\nR 162,184,28672\ns_add_u32 s63, s52, 2756608\nN 84,180,172\n"
"s_add_u32 m0, s50, 73728\nN 112,168,168\nR 164,184,32768\nN 112,184,172\nbuffer_load_dwordx4 v220, s[4:7], s63 offen lds\nN 116,172,168\n"
"R 166,184,36864\nN 116,188,172\nG 4\nN 124,172,176\nR 168,185,24576\nN 124,188,180\n"
"buffer_load_dwordx4 v221, s[4:7], s63 offen offset:1024 lds\nN 120,168,176\nR 170,185,28672\ns_add_u32 s63, s52, 2953216\nN 120,184,180\ns_add_u32 m0, s50, 81920\n"
"N 92,164,176\nR 172,185,32768\nN 92,180,180\nbuffer_load_dwordx4 v220, s[4:7], s63 offen lds\nN 88,160,176\nR 174,185,36864\n"
"N 88,176,180\nG 4\nM 0,160,192\nR 176,186,24576\ns_mov_b32 s60, 156160\nv_bfe_u32 v246, v252, 4, 4\n"
"v_lshl_add_u32 v246, v246, 3, s60\nM 0,164,208\nds_read_b64 v[238:239], v246 offset:0\nM 16,160,196\nR 178,186,28672\nds_read_b64 v[240:241], v246 offset:128\n"
"M 16,164,212\nds_read_b64 v[242:243], v246 offset:256\nM 32,160,200\nR 180,186,32768\nds_read_b64 v[244:245], v246 offset:384\nM 32,164,216\n"
"ds_read_b64 v[246:247], v246 offset:0\nM 48,160,204\nR 182,186,36864\nM 48,164,220\ns_waitcnt vmcnt(7) lgkmcnt(1)\ns_barrier\n"
"M 52,168,204\nR 160,187,24576\nM 52,172,220\nbuffer_load_dwordx4 v221, s[4:7], s63 offen offset:1024 lds\nM 36,168,200\nR 162,187,28672\n"
"s_add_u32 s63, s52, 0\nM 36,172,216\ns_add_u32 m0, s50, 90112\nM 20,168,196\nR 164,187,32768\nM 20,172,212\n"
"buffer_load_dwordx4 v220, s[4:7], s63 offen lds\nM 4,168,192\nR 166,187,36864\nv_mfma_f32_16x16x32_bf16 v[0:3], v[238:241], v[208:211], v[0:3]\nM 4,172,208\nG 4\n"
"M 8,176,192\nR 168,184,40960\nv_mfma_f32_16x16x32_bf16 v[4:7], v[240:243], v[208:211], v[4:7]\nM 8,180,208\nbuffer_load_dwordx4 v221, s[4:7], s63 offen offset:1024 lds\nM 24,176,196\n"
"R 170,184,45056\nv_mfma_f32_16x16x32_bf16 v[8:11], v[242:245], v[208:211], v[8:11]\ns_add_u32 m0, s50, 98304\ns_add_u32 s63, s52, 196608\nM 24,180,212\nbuffer_load_dwordx4 v220, s[4:7], s63 offen lds\n"
"M 40,176,200\nR 172,184,49152\nv_mfma_f32_16x16x32_bf16 v[12:15], v[244:247], v[208:211], v[12:15]\nM 40,180,216\nM 56,176,204\nR 174,184,53248\n"
"v_mfma_f32_16x16x32_bf16 v[16:19], v[244:247], v[210:213], v[16:19]\nM 56,180,220\nG 4\nM 60,160,204\nR 176,185,40960\nv_mfma_f32_16x16x32_bf16 v[20:23], v[238:241], v[210:213], v[20:23]\n"
"M 60,164,220\nbuffer_load_dwordx4 v221, s[4:7], s63 offen offset:1024 lds\nM 44,160,200\nR 178,185,45056\nv_mfma_f32_16x16x32_bf16 v[24:27], v[240:243], v[210:213], v[24:27]\ns_add_u32 m0, s50, 106496\n"
"s_add_u32 s63, s52, 393216\nM 44,164,216\nbuffer_load_dwordx4 v220, s[4:7], s63 offen lds\nM 28,160,196\nR 180,185,49152\nv_mfma_f32_16x16x32_bf16 v[28:31], v[242:245], v[210:213], v[28:31]\n"
"M 28,164,212\nM 12,160,192\nR 182,185,53248\nM 12,164,208\nG 4\nM 0,168,224\n"
"R 160,186,40960\nM 0,172,240\nbuffer_load_dwordx4 v221, s[4:7], s63 offen offset:1024 lds\nM 16,168,228\nR 162,186,45056\ns_add_u32 s63, s52, 589824\n"
"M 16,172,244\ns_add_u32 m0, s50, 114688\nM 32,168,232\nR 164,186,49152\nM 32,172,248\nbuffer_load_dwordx4 v220, s[4:7], s63 offen lds\n"
"M 48,168,236\nR 166,186,53248\nM 48,172,252\ns_waitcnt vmcnt(11)\ns_barrier\nG 4\n"
"M 52,176,236\nR 168,187,40960\nM 52,180,252\nbuffer_load_dwordx4 v221, s[4:7], s63 offen offset:1024 lds\nM 36,176,232\nR 170,187,45056\n"
"s_add_u32 s63, s52, 786432\nM 36,180,248\ns_add_u32 m0, s50, 0\nM 20,176,228\nR 172,187,49152\nM 20,180,244\n"
"buffer_load_dwordx4 v220, s[4:7], s63 offen lds\nM 4,176,224\nR 174,187,53248\nM 4,180,240\nG 4\nM 8,160,224\n"
"R 176,184,57344\nM 8,164,240\nbuffer_load_dwordx4 v221, s[4:7], s63 offen offset:1024 lds\nM 24,160,228\nR 178,184,61440\nM 24,164,244\n"
"s_add_u32 m0, s50, 8192\nM 40,160,232\nR 180,188,0\nM 40,164,248\ns_add_u32 s63, s52, 983040\nM 56,160,236\n"
"R 182,188,4096\nM 56,164,252\nG 4\nM 60,168,236\nR 160,185,57344\nM 60,172,252\n"
"buffer_load_dwordx4 v220, s[4:7], s63 offen lds\nM 44,168,232\nR 162,185,61440\nM 44,172,248\nM 28,168,228\nR 164,189,0\n"
"M 28,172,244\nbuffer_load_dwordx4 v221, s[4:7], s63 offen offset:1024 lds\nM 12,168,224\nR 166,189,4096\nM 12,172,240\ns_waitcnt vmcnt(12)\n"
"s_barrier\nG 4\nN 64,192,176\nLpg20:\ns_cbranch_execz LretW0n\nR 168,186,57344\n"
"N 64,208,180\ns_mov_b32 m0, s51\nN 68,196,176\nR 170,186,61440\nN 68,212,180\nds_write_addtid_b32 v32 offset:0\n"
"ds_write_addtid_b32 v33 offset:256\nN 96,200,176\nR 172,190,0\nds_write_addtid_b32 v34 offset:512\nN 96,216,180\nds_write_addtid_b32 v35 offset:768\n"
"ds_write_addtid_b32 v36 offset:1024\nN 100,204,176\nR 174,190,4096\nds_write_addtid_b32 v37 offset:1280\nN 100,220,180\nds_write_addtid_b32 v38 offset:1536\n"
"ds_write_addtid_b32 v39 offset:1792\nG 12\nN 108,204,160\nR 176,187,57344\nds_write_addtid_b32 v40 offset:2048\nN 108,220,164\n"
"ds_write_addtid_b32 v41 offset:2304\nds_write_addtid_b32 v42 offset:2560\nN 104,200,160\nR 178,187,61440\nds_write_addtid_b32 v43 offset:2816\nN 104,216,164\n"
"ds_write_addtid_b32 v44 offset:3072\nds_write_addtid_b32 v45 offset:3328\nN 76,196,160\nR 180,191,0\nds_write_addtid_b32 v46 offset:3584\nN 76,212,164\n"
"ds_write_addtid_b32 v47 offset:3840\nds_write_addtid_b32 v48 offset:4096\nN 72,192,160\nR 182,191,4096\nds_write_addtid_b32 v49 offset:4352\nN 72,208,164\n"
"ds_write_addtid_b32 v50 offset:4608\nds_write_addtid_b32 v51 offset:4864\nG 15\nN 80,192,168\nR 160,188,8192\nds_write_addtid_b32 v52 offset:5120\n"
"N 80,208,172\nds_write_addtid_b32 v53 offset:5376\nds_write_addtid_b32 v54 offset:5632\nN 84,196,168\nR 162,188,12288\nds_write_addtid_b32 v55 offset:5888\n"
"N 84,212,172\nds_write_addtid_b32 v56 offset:6144\nds_write_addtid_b32 v57 offset:6400\nN 112,200,168\nR 164,188,16384\nds_write_addtid_b32 v58 offset:6656\n"
"N 112,216,172\nds_write_addtid_b32 v59 offset:6912\nds_write_addtid_b32 v60 offset:7168\nN 116,204,168\nR 166,188,20480\nds_write_addtid_b32 v61 offset:7424\n"
"N 116,220,172\nds_write_addtid_b32 v62 offset:7680\nds_write_addtid_b32 v63 offset:7936\nG 15\nN 124,204,176\nR 168,189,8192\n"
"s_add_u32 m0, s50, 16384\ns_add_u32 s63, s52, 1179648\nN 124,220,180\nbuffer_load_dwordx4 v220, s[4:7], s63 offen lds\nN 120,200,176\nR 170,189,12288\n"
"s_waitcnt vmcnt(9) lgkmcnt(2)\ns_barrier\nN 120,216,180\nds_read2st64_b32 v[32:33], v225 offset0:0 offset1:1\nds_read2st64_b32 v[34:35], v225 offset0:2 offset1:3\nN 92,196,176\n"
"R 172,189,16384\nds_read2st64_b32 v[36:37], v225 offset0:4 offset1:5\nN 92,212,180\nds_read2st64_b32 v[38:39], v225 offset0:6 offset1:7\nds_read2st64_b32 v[40:41], v225 offset0:8 offset1:9\nN 88,192,176\n"
"R 174,189,20480\nds_read2st64_b32 v[42:43], v225 offset0:10 offset1:11\nN 88,208,180\nds_read2st64_b32 v[44:45], v225 offset0:12 offset1:13\nds_read2st64_b32 v[46:47], v225 offset0:14 offset1:15\nN 64,224,160\n"
"R 176,190,8192\ns_mov_b32 s60, 156672\nv_bfe_u32 v246, v252, 4, 4\nv_lshl_add_u32 v246, v246, 3, s60\nds_read2st64_b32 v[48:49], v225 offset0:16 offset1:17\nN 64,240,164\n"
"ds_read_b64 v[238:239], v246 offset:0\nds_read2st64_b32 v[50:51], v225 offset0:18 offset1:19\nG 13\nv_add_f32 v0, v0, v32\nN 68,228,160\nR 178,190,12288\n"
"ds_read_b64 v[240:241], v246 offset:128\nds_read2st64_b32 v[52:53], v225 offset0:20 offset1:21\nN 68,244,164\nds_read_b64 v[242:243], v246 offset:256\nds_read2st64_b32 v[54:55], v225 offset0:22 offset1:23\nv_add_f32 v1, v1, v33\n"
"N 96,232,160\nR 180,190,16384\nds_read_b64 v[244:245], v246 offset:384\nds_read2st64_b32 v[56:57], v225 offset0:24 offset1:25\nN 96,248,164\nds_read_b64 v[246:247], v246 offset:0\n"
"ds_read2st64_b32 v[58:59], v225 offset0:26 offset1:27\nG 15\nv_add_f32 v2, v2, v34\nN 100,236,160\nR 182,190,20480\nds_read2st64_b32 v[60:61], v225 offset0:28 offset1:29\n"
"v_add_f32 v3, v3, v35\nN 100,252,164\nds_read2st64_b32 v[62:63], v225 offset0:30 offset1:31\nv_add_f32 v4, v4, v36\nN 108,236,168\nR 160,191,8192\n"
"v_add_f32 v5, v5, v37\nv_add_f32 v6, v6, v38\nN 108,252,172\nbuffer_load_dwordx4 v221, s[4:7], s63 offen offset:1024 lds\nv_add_f32 v7, v7, v39\nN 104,232,168\n"
"R 162,191,12288\ns_add_u32 s63, s52, 1376256\nv_add_f32 v8, v8, v40\nN 104,248,172\ns_add_u32 m0, s50, 24576\nv_add_f32 v9, v9, v41\n"
"v_add_f32 v10, v10, v42\nv_add_f32 v11, v11, v43\nN 76,228,168\nR 164,191,16384\nv_add_f32 v12, v12, v44\nv_add_f32 v13, v13, v45\n"
"N 76,244,172\nG 14\nv_mfma_f32_16x16x32_bf16 v[64:67], v[208:211], v[238:241], v[64:67]\nbuffer_load_dwordx4 v220, s[4:7], s63 offen lds\nv_add_f32 v14, v14, v46\nN 72,224,168\n"
"R 166,191,20480\nv_add_f32 v15, v15, v47\nv_add_f32 v16, v16, v48\nN 72,240,172\nG 13\nv_mfma_f32_16x16x32_bf16 v[72:75], v[208:211], v[240:243], v[72:75]\n"
"G 6\nv_add_f32 v17, v17, v49\nv_add_f32 v18, v18, v50\nv_add_f32 v19, v19, v51\nN 80,224,176\nR 168,188,24576\n"
"v_add_f32 v20, v20, v52\nv_add_f32 v21, v21, v53\nN 80,240,180\nv_mfma_f32_16x16x32_bf16 v[80:83], v[208:211], v[242:245], v[80:83]\nbuffer_load_dwordx4 v221, s[4:7], s63 offen offset:1024 lds\nv_add_f32 v22, v22, v54\n"
"N 84,228,176\nR 170,188,28672\ns_add_u32 m0, s50, 49152\ns_add_u32 s63, s52, 1572864\nv_add_f32 v23, v23, v55\nN 84,244,180\n"
"v_mfma_f32_16x16x32_bf16 v[88:91], v[208:211], v[244:247], v[88:91]\nbuffer_load_dwordx4 v220, s[4:7], s63 offen lds\nv_add_f32 v24, v24, v56\nN 112,232,176\nR 172,188,32768\nv_add_f32 v25, v25, v57\n"
"N 112,248,180\nv_mfma_f32_16x16x32_bf16 v[68:71], v[210:213], v[244:247], v[68:71]\nv_add_f32 v26, v26, v58\nv_add_f32 v27, v27, v59\nN 116,236,176\nR 174,188,36864\n"
"G 9\nv_add_f32 v28, v28, v60\nN 116,252,180\nv_mfma_f32_16x16x32_bf16 v[76:79], v[210:213], v[238:241], v[76:79]\nG 4\nv_add_f32 v29, v29, v61\n"
"v_add_f32 v30, v30, v62\nN 124,236,160\nR 176,189,24576\nv_add_f32 v31, v31, v63\nN 124,252,164\nv_mfma_f32_16x16x32_bf16 v[84:87], v[210:213], v[240:243], v[84:87]\n"
"buffer_load_dwordx4 v221, s[4:7], s63 offen offset:1024 lds\nN 120,232,160\nR 178,189,28672\ns_add_u32 m0, s50, 57344\ns_add_u32 s63, s52, 1769472\nv_lshrrev_b32 v227, 1, v252\n"
"N 120,248,164\nv_mfma_f32_16x16x32_bf16 v[92:95], v[210:213], v[242:245], v[92:95]\nbuffer_load_dwordx4 v220, s[4:7], s63 offen lds\nv_mul_f32 v16, v16, v217\nN 92,228,160\nR 180,189,32768\n"
"s_lshl4_add_u32 s64, s41, s42\nv_mul_f32 v17, v17, v217\nN 92,244,164\ns_cmp_eq_u32 s40, 3\ns_cselect_b64 s[66:67], -1, 0\ns_lshl_b32 s64, s64, 12\n"
"v_mul_f32 v18, v18, v217\nN 88,224,160\nR 182,189,36864\ns_add_u32 s64, s64, 78643200\nv_mul_f32 v19, v19, v217\nN 88,240,164\n"
"s_waitcnt vmcnt(11)\ns_barrier\nG 4\nv_mul_f32 v20, v20, v217\nv_mfma_f32_16x16x32_bf16 v[128:131], v[168:171], a[0:3], 0\nR 160,190,24576\n"
"v_mul_f32 v21, v21, v217\nv_mul_f32 v22, v22, v217\nM 128,172,16\ns_mov_b32 m0, s51\nv_mul_f32 v23, v23, v217\nv_mul_f32 v24, v24, v217\n"
"v_mul_f32 v25, v25, v217\nv_mfma_f32_16x16x32_bf16 v[144:147], v[168:171], a[4:7], 0\nR 162,190,28672\nv_mul_f32 v26, v26, v217\nv_mul_f32 v27, v27, v217\nM 144,172,20\n"
"v_mul_f32 v28, v28, v217\nv_mul_f32 v29, v29, v217\nv_mul_f32 v30, v30, v217\nv_mfma_f32_16x16x32_bf16 v[32:35], v[168:171], a[8:11], 0\nR 164,190,32768\nv_mul_f32 v31, v31, v217\n"
"v_cvt_pk_bf16_f32 v16, v16, v17\nM 32,172,24\nv_cvt_pk_bf16_f32 v17, v18, v19\nv_cvt_pk_bf16_f32 v18, v20, v21\nv_cvt_pk_bf16_f32 v19, v22, v23\nv_mfma_f32_16x16x32_bf16 v[48:51], v[168:171], a[12:15], 0\n"
"R 166,190,36864\nv_cvt_pk_bf16_f32 v20, v24, v25\nv_cvt_pk_bf16_f32 v21, v26, v27\nM 48,172,28\nds_write_addtid_b32 v96 offset:0\nG 5\n"
"v_cvt_pk_bf16_f32 v22, v28, v29\nv_cvt_pk_bf16_f32 v23, v30, v31\nv_mul_f32 v0, v0, v214\nv_mfma_f32_16x16x32_bf16 v[52:55], v[176:179], a[12:15], 0\nR 168,191,24576\nds_write_addtid_b32 v97 offset:256\n"
"v_mul_f32 v1, v1, v214\nv_mul_f32 v2, v2, v214\nM 52,180,28\nds_write_addtid_b32 v98 offset:512\nv_mul_f32 v3, v3, v214\nv_mul_f32 v4, v4, v214\n"
"v_mul_f32 v5, v5, v214\nv_mfma_f32_16x16x32_bf16 v[36:39], v[176:179], a[8:11], 0\nR 170,191,28672\nds_write_addtid_b32 v99 offset:768\nv_mul_f32 v6, v6, v214\nv_mul_f32 v7, v7, v214\n"
"M 36,180,24\nds_write_addtid_b32 v100 offset:1024\nv_mul_f32 v8, v8, v214\nv_mul_f32 v9, v9, v214\nv_mul_f32 v10, v10, v214\nv_mfma_f32_16x16x32_bf16 v[148:151], v[176:179], a[4:7], 0\n"
"R 172,191,32768\nds_write_addtid_b32 v101 offset:1280\nv_mul_f32 v11, v11, v214\nv_mul_f32 v12, v12, v214\nM 148,180,20\nds_write_addtid_b32 v102 offset:1536\n"
"v_mul_f32 v13, v13, v214\nv_mul_f32 v14, v14, v214\nv_mul_f32 v15, v15, v214\nv_mfma_f32_16x16x32_bf16 v[132:135], v[176:179], a[0:3], 0\nR 174,191,36864\nds_write_addtid_b32 v103 offset:1792\n"
"v_cvt_pk_bf16_f32 v0, v0, v1\nv_cvt_pk_bf16_f32 v1, v2, v3\nM 132,180,16\nds_write_addtid_b32 v104 offset:2048\nG 13\nv_cvt_pk_bf16_f32 v2, v4, v5\n"
"v_cvt_pk_bf16_f32 v3, v6, v7\nv_cvt_pk_bf16_f32 v4, v8, v9\nv_mfma_f32_16x16x32_bf16 v[136:139], v[160:163], a[0:3], 0\nR 176,188,40960\nds_write_addtid_b32 v105 offset:2304\nv_cvt_pk_bf16_f32 v5, v10, v11\n"
"v_cvt_pk_bf16_f32 v6, v12, v13\nM 136,164,16\nds_write_addtid_b32 v106 offset:2560\nds_write_addtid_b32 v107 offset:2816\ns_mov_b64 exec, s[66:67]\nbuffer_store_dwordx4 v[16:19], v252, s[20:23], s64 offen offset:0 sc1\n"
"s_mov_b64 exec, -1\nv_cvt_pk_bf16_f32 v7, v14, v15\nv_mfma_f32_16x16x32_bf16 v[152:155], v[160:163], a[4:7], 0\nR 178,188,45056\nds_write_addtid_b32 v108 offset:3072\nM 152,164,20\n"
"ds_write_addtid_b32 v109 offset:3328\nds_write_addtid_b32 v110 offset:3584\nv_mfma_f32_16x16x32_bf16 v[40:43], v[160:163], a[8:11], 0\nR 180,188,49152\nds_write_addtid_b32 v111 offset:3840\nM 40,164,24\n"
"ds_write_addtid_b32 v112 offset:4096\nds_write_addtid_b32 v113 offset:4352\ns_mov_b64 exec, s[66:67]\nbuffer_store_dwordx4 v[20:23], v252, s[20:23], s64 offen offset:1024 sc1\ns_mov_b64 exec, -1\nv_mfma_f32_16x16x32_bf16 v[56:59], v[160:163], a[12:15], 0\n"
"R 182,188,53248\nds_write_addtid_b32 v114 offset:4608\nM 56,164,28\nds_write_addtid_b32 v115 offset:4864\nds_write_addtid_b32 v116 offset:5120\nG 15\n"
"v_mfma_f32_16x16x32_bf16 v[60:63], v[168:171], a[12:15], 0\nR 160,189,40960\nds_write_addtid_b32 v117 offset:5376\nM 60,172,28\nds_write_addtid_b32 v118 offset:5632\nds_write_addtid_b32 v119 offset:5888\n"
"v_mfma_f32_16x16x32_bf16 v[44:47], v[168:171], a[8:11], 0\nR 162,189,45056\nds_write_addtid_b32 v120 offset:6144\nM 44,172,24\nds_write_addtid_b32 v121 offset:6400\nds_write_addtid_b32 v122 offset:6656\n"
"ds_write_addtid_b32 v123 offset:6912\nds_write_addtid_b32 v124 offset:7168\nds_write_addtid_b32 v125 offset:7424\nds_write_addtid_b32 v126 offset:7680\nds_write_addtid_b32 v127 offset:7936\nv_mfma_f32_16x16x32_bf16 v[156:159], v[168:171], a[4:7], 0\n"
"R 164,189,49152\ns_add_u32 m0, s50, 57344\nM 156,172,20\nbuffer_load_dwordx4 v221, s[4:7], s63 offen offset:1024 lds\nv_mfma_f32_16x16x32_bf16 v[140:143], v[168:171], a[0:3], 0\nR 166,189,53248\n"
"s_waitcnt vmcnt(10) lgkmcnt(2)\ns_barrier\nM 140,172,16\nds_read2st64_b32 v[96:97], v225 offset0:0 offset1:1\nds_read2st64_b32 v[98:99], v225 offset0:2 offset1:3\nv_lshrrev_b32 v226, 8, v252\n"
"M 128,176,32\nR 168,190,40960\ns_add_u32 s63, s52, 1966080\nds_read2st64_b32 v[100:101], v225 offset0:4 offset1:5\ns_lshl_b32 s60, s40, 7\ns_add_u32 s60, s60, 161280\n"
"v_lshl_add_u32 v226, v226, 4, s60\nM 128,180,48\ns_add_u32 m0, s50, 65536\nds_read2st64_b32 v[102:103], v225 offset0:6 offset1:7\nds_read2st64_b32 v[104:105], v225 offset0:8 offset1:9\nds_read_b128 v[230:233], v226 offset:0\n"
"M 144,176,36\nR 170,190,45056\nds_read2st64_b32 v[106:107], v225 offset0:10 offset1:11\nds_read_b128 v[234:237], v226 offset:64\nM 144,180,52\nbuffer_load_dwordx4 v220, s[4:7], s63 offen lds\n"
"ds_read2st64_b32 v[108:109], v225 offset0:12 offset1:13\nM 32,176,40\nR 172,190,49152\nds_read2st64_b32 v[110:111], v225 offset0:14 offset1:15\nM 32,180,56\nds_read2st64_b32 v[112:113], v225 offset0:16 offset1:17\n"
"ds_read2st64_b32 v[114:115], v225 offset0:18 offset1:19\nG 14\nv_add_f32 v64, v64, v96\nM 48,176,44\nR 174,190,53248\nds_read2st64_b32 v[116:117], v225 offset0:20 offset1:21\n"
"v_add_f32 v65, v65, v97\nM 48,180,60\nds_read2st64_b32 v[118:119], v225 offset0:22 offset1:23\nds_read2st64_b32 v[120:121], v225 offset0:24 offset1:25\nM 52,160,44\nR 176,191,40960\n"
"ds_read2st64_b32 v[122:123], v225 offset0:26 offset1:27\nG 15\nv_add_f32 v66, v66, v98\nM 52,164,60\nbuffer_load_dwordx4 v221, s[4:7], s63 offen offset:1024 lds\nds_read2st64_b32 v[124:125], v225 offset0:28 offset1:29\n"
"M 36,160,40\nR 178,191,45056\nds_read2st64_b32 v[126:127], v225 offset0:30 offset1:31\nv_add_f32 v67, v67, v99\nM 36,164,56\ns_add_u32 m0, s50, 73728\n"
"v_add_f32 v68, v68, v100\nv_add_f32 v69, v69, v101\nv_add_f32 v70, v70, v102\nM 148,160,36\nR 180,191,49152\nv_add_f32 v71, v71, v103\n"
"v_add_f32 v72, v72, v104\nM 148,164,52\ns_add_u32 s63, s52, 2162688\nv_add_f32 v73, v73, v105\nG 15\nv_add_f32 v74, v74, v106\n"
"v_add_f32 v75, v75, v107\nM 132,160,32\nR 182,191,53248\nG 15\nv_add_f32 v76, v76, v108\nv_add_f32 v77, v77, v109\n"
"M 132,164,48\nG 10\nv_add_f32 v78, v78, v110\nv_add_f32 v79, v79, v111\nv_add_f32 v80, v80, v112\nM 136,168,32\n"
"R 160,184,0\nv_add_f32 v81, v81, v113\nv_add_f32 v82, v82, v114\nM 136,172,48\nbuffer_load_dwordx4 v220, s[4:7], s63 offen lds\nv_add_f32 v83, v83, v115\n"
"M 152,168,36\nR 162,184,4096\nG 11\nv_add_f32 v84, v84, v116\nM 152,172,52\nv_add_f32 v85, v85, v117\n"
"G 10\nv_add_f32 v86, v86, v118\nM 40,168,40\nR 164,184,8192\nv_add_f32 v87, v87, v119\nM 40,172,56\n"
"buffer_load_dwordx4 v221, s[4:7], s63 offen offset:1024 lds\nM 56,168,44\nR 166,184,12288\ns_add_u32 m0, s50, 81920\nM 56,172,60\nG 4\n"
"v_add_f32 v88, v88, v120\nv_add_f32 v89, v89, v121\nM 60,176,44\nR 168,185,0\ns_add_u32 s63, s52, 2359296\nM 60,180,60\n"
"buffer_load_dwordx4 v220, s[4:7], s63 offen lds\nM 44,176,40\nR 170,185,4096\nv_add_f32 v90, v90, v122\nM 44,180,56\nv_add_f32 v91, v91, v123\n"
"v_add_f32 v92, v92, v124\nM 156,176,36\nR 172,185,8192\nv_add_f32 v93, v93, v125\nM 156,180,52\nbuffer_load_dwordx4 v221, s[4:7], s63 offen offset:1024 lds\n"
"M 140,176,32\nR 174,185,12288\ns_add_u32 m0, s50, 90112\nM 140,180,48\nG 4\nv_add_f32 v94, v94, v126\n"
"v_add_f32 v95, v95, v127\nM 128,160,64\nR 176,186,0\ns_add_u32 s63, s52, 2555904\nv_mul_f32 v64, v64, v230\nM 128,164,80\n"
"buffer_load_dwordx4 v220, s[4:7], s63 offen lds\nv_mul_f32 v65, v65, v231\nv_add_u32 v108, 161792, v252\nM 144,160,68\nR 178,186,4096\nds_read_b128 v[112:115], v108\n"
"v_mul_f32 v66, v66, v232\nv_mul_f32 v67, v67, v233\nM 144,164,84\nds_read_b128 v[104:107], v108 offset:1024\nv_mul_f32 v68, v68, v234\nv_mul_f32 v69, v69, v235\n"
"v_mul_f32 v70, v70, v236\nM 32,160,72\nR 180,186,8192\nv_mul_f32 v71, v71, v237\nv_cvt_pk_bf16_f32 v67, v66, v67\nM 32,164,88\n"
"v_cvt_pk_bf16_f32 v66, v64, v65\nv_cvt_pk_bf16_f32 v68, v68, v69\nv_cvt_pk_bf16_f32 v69, v70, v71\nM 48,160,76\nR 182,186,12288\nv_mul_f32 v72, v72, v230\n"
"v_mul_f32 v73, v73, v231\nM 48,164,92\nv_mul_f32 v74, v74, v232\nv_mul_f32 v75, v75, v233\nG 6\nv_mul_f32 v76, v76, v234\n"
"M 52,168,76\nR 160,187,0\nv_mul_f32 v77, v77, v235\nv_mul_f32 v78, v78, v236\nM 52,172,92\nbuffer_load_dwordx4 v221, s[4:7], s63 offen offset:1024 lds\n"
"v_mul_f32 v79, v79, v237\nG 3\nv_lshlrev_b32 v100, 16, v104\nM 36,168,72\nR 162,187,4096\ns_add_u32 s63, s52, 2752512\n"
"v_cvt_pk_bf16_f32 v72, v72, v73\nv_and_b32 v101, s77, v104\nM 36,172,88\ns_add_u32 m0, s50, 98304\nv_cvt_pk_bf16_f32 v73, v74, v75\nv_cvt_pk_bf16_f32 v74, v76, v77\n"
"v_cvt_pk_bf16_f32 v75, v78, v79\nM 148,168,68\nR 164,187,8192\nv_mul_f32 v80, v80, v230\nv_mul_f32 v81, v81, v231\nM 148,172,84\n"
"buffer_load_dwordx4 v220, s[4:7], s63 offen lds\nv_mul_f32 v82, v82, v232\nM 132,168,64\nR 166,187,12288\ns_waitcnt vmcnt(9)\ns_barrier\n"
"v_mul_f32 v83, v83, v233\nM 132,172,80\nG 4\nv_mul_f32 v84, v84, v234\nv_mul_f32 v85, v85, v235\nv_mul_f32 v86, v86, v236\n"
"M 136,176,64\nR 168,184,16384\nv_mul_f32 v87, v87, v237\nv_cvt_pk_bf16_f32 v78, v80, v81\nM 136,180,80\ns_mov_b64 exec, s[66:67]\n"
"buffer_store_dwordx2 v[68:69], v227, s[20:23], s64 offen offset:2048 sc1\ns_mov_b64 exec, -1\nv_cvt_pk_bf16_f32 v79, v82, v83\nv_cvt_pk_bf16_f32 v80, v84, v85\nM 152,176,68\nR 170,184,20480\n"
"v_cvt_pk_bf16_f32 v81, v86, v87\nv_mul_f32 v88, v88, v230\nM 152,180,84\nv_mul_f32 v89, v89, v231\nv_mul_f32 v90, v90, v232\nv_mul_f32 v91, v91, v233\n"
"M 40,176,72\nR 172,184,24576\ns_mov_b32 s60, 155648\nv_bfe_u32 v246, v252, 4, 4\nv_lshl_add_u32 v246, v246, 3, s60\nv_mul_f32 v92, v92, v234\n"
"M 40,180,88\nbuffer_load_dwordx4 v221, s[4:7], s63 offen offset:1024 lds\nv_mul_f32 v93, v93, v235\nM 56,176,76\nR 174,184,28672\nds_read_b64 v[238:239], v246 offset:0\n"
"v_mul_f32 v94, v94, v236\nM 56,180,92\nG 5\nv_mul_f32 v95, v95, v237\nv_cvt_pk_bf16_f32 v84, v88, v89\nv_cvt_pk_bf16_f32 v85, v90, v91\n"
"M 60,160,76\nLpg21:\ns_cbranch_execz LretW0n\nR 176,185,16384\nds_read_b64 v[240:241], v246 offset:128\nv_cvt_pk_bf16_f32 v86, v92, v93\n"
"M 60,164,92\ns_add_u32 m0, s50, 106496\ns_mov_b64 exec, s[66:67]\nbuffer_store_dwordx2 v[74:75], v227, s[20:23], s64 offen offset:2560 sc1\ns_mov_b64 exec, -1\nv_cvt_pk_bf16_f32 v87, v94, v95\n"
"v_lshlrev_b32 v102, 16, v105\nM 44,160,72\nR 178,185,20480\nds_read_b64 v[242:243], v246 offset:256\ns_lshl_b32 s60, s40, 12\ns_add_u32 s60, s60, 69632\n"
"v_add_u32 v230, s60, v252\nv_add_u32 v231, s60, v227\nM 44,164,88\ns_add_u32 s63, s52, 2949120\nv_and_b32 v103, s77, v105\nv_lshlrev_b32 v96, 16, v106\n"
"M 156,160,68\nR 180,185,24576\nds_read_b64 v[244:245], v246 offset:384\nM 156,164,84\nbuffer_load_dwordx4 v220, s[4:7], s63 offen lds\nM 140,160,64\n"
"R 182,185,28672\nds_read_b64 v[246:247], v246 offset:0\nM 140,164,80\ns_mov_b64 exec, s[66:67]\nbuffer_store_dwordx2 v[80:81], v227, s[20:23], s64 offen offset:3072 sc1\ns_mov_b64 exec, -1\n"
"G 9\nv_and_b32 v97, s77, v106\nv_lshlrev_b32 v98, 16, v107\nM 128,168,96\nR 160,186,16384\nv_and_b32 v99, s77, v107\n"
"M 128,172,112\nv_add_f32 v104, s78, v100\nv_add_f32 v105, s78, v101\nM 144,168,100\nR 162,186,20480\nv_add_f32 v106, s78, v102\n"
"M 144,172,116\nv_add_f32 v107, s78, v103\nM 32,168,104\nR 164,186,24576\nG 9\nv_mfma_f32_16x16x32_bf16 v[128:131], v[238:241], v[208:211], v[128:131]\n"
"M 32,172,120\nbuffer_load_dwordx4 v221, s[4:7], s63 offen offset:1024 lds\nM 48,168,108\nR 166,186,28672\nG 8\nv_mfma_f32_16x16x32_bf16 v[132:135], v[240:243], v[208:211], v[132:135]\n"
"M 48,172,124\ns_mov_b64 exec, s[66:67]\nbuffer_store_dwordx2 v[86:87], v227, s[20:23], s64 offen offset:3584 sc1\ns_mov_b64 exec, -1\nG 5\nM 52,176,108\n"
"R 168,187,16384\nG 5\nv_mfma_f32_16x16x32_bf16 v[144:147], v[244:247], v[210:213], v[144:147]\ns_not_b64 exec, s[66:67]\nds_write_b128 v230, v[16:19] offset:0\nds_write_b128 v230, v[20:23] offset:1024\n"
"s_mov_b64 exec, -1\nM 52,180,124\nM 36,176,104\nR 170,187,20480\nv_mfma_f32_16x16x32_bf16 v[148:151], v[238:241], v[210:213], v[148:151]\nM 36,180,120\n"
"s_movk_i32 s63, 2048\nv_mad_u32_u24 v220, v228, s63, v229\nv_add_u32 v221, 1047552, v220\ns_add_u32 m0, s50, 114688\ns_add_u32 s63, s53, 0\nbuffer_load_dwordx4 v220, s[8:11], s63 offen lds\n"
"M 148,176,100\nR 172,187,24576\nM 148,180,116\ns_not_b64 exec, s[66:67]\nds_write_b64 v231, v[68:69] offset:2048\nds_write_b64 v231, v[74:75] offset:2560\n"
"s_mov_b64 exec, -1\nM 132,176,96\nR 174,187,28672\nM 132,180,112\nG 8\nM 136,160,96\n"
"R 176,184,49152\nM 136,164,112\nM 152,160,100\nR 178,184,53248\nv_lshrrev_b32 v248, 5, v252\nM 152,164,116\n"
"buffer_load_dwordx4 v221, s[8:11], s63 offen offset:1024 lds\nM 40,160,104\nR 180,184,57344\nv_lshlrev_b32 v248, 5, v248\nM 40,164,120\ns_and_b32 s60, s41, 7\n"
"s_lshl2_add_u32 s60, s60, s40\nv_add_u32 v248, s60, v248\nM 56,160,108\nR 182,184,61440\nv_mul_u32_u24 v248, 7168, v248\nM 56,164,124\n"
"G 4\nv_bfe_u32 v249, v252, 4, 1\nM 60,168,108\nR 160,185,49152\ns_not_b64 exec, s[66:67]\nds_write_b64 v231, v[80:81] offset:3072\n"
"ds_write_b64 v231, v[86:87] offset:3584\ns_mov_b64 exec, -1\nM 60,172,124\ns_sub_u32 s60, s54, s54\ns_add_u32 s60, s60, 128\nv_mul_lo_u32 v249, v249, s60\n"
"M 44,168,104\nR 162,185,53248\nv_add_u32 v248, v248, v249\nM 44,172,120\ns_add_u32 m0, s50, 0\nM 156,168,100\n"
"R 164,185,57344\nM 156,172,116\ns_add_u32 s63, s53, 65536\nM 140,168,96\nR 166,185,61440\nM 140,172,112\n"
"G 6\nM 128,176,128\nR 168,186,49152\ns_waitcnt vmcnt(12) lgkmcnt(4)\ns_barrier\nM 128,180,144\n"
"buffer_load_dwordx4 v220, s[8:11], s63 offen lds\nM 144,176,132\nR 170,186,53248\nM 144,180,148\ns_cmp_eq_u32 s78, 0\ns_cbranch_scc1 Lq656\n"
"v_mov_b32 v230, 0\nv_mov_b32 v231, 0\nv_mov_b32 v232, 0\nv_mov_b32 v233, 0\nv_mov_b32 v64, 0\nv_mov_b32 v65, 0\n"
"v_mov_b32 v70, 0\nv_mov_b32 v71, 0\nv_mov_b32 v234, 0\nv_mov_b32 v235, 0\nv_mov_b32 v236, 0\nv_mov_b32 v237, 0\n"
"v_mov_b32 v76, 0\nv_mov_b32 v77, 0\nv_mov_b32 v82, 0\nv_mov_b32 v83, 0\nLq656:\ns_lshl4_add_u32 s65, s41, s42\n"
"M 32,176,136\nR 172,186,57344\ns_lshl_b32 s65, s65, 12\nM 32,180,152\ns_add_u32 s65, s65, 78577664\nM 48,176,140\n"
"R 174,186,61440\ns_bfm_b64 exec, 16, 0\nbuffer_load_dword v249, v248, s[12:15], s54 offen\ns_mov_b64 exec, -1\nM 48,180,156\nG 4\n"
"M 52,160,140\nR 176,187,49152\nM 52,164,156\nbuffer_load_dwordx4 v221, s[8:11], s63 offen offset:1024 lds\nM 36,160,136\nR 178,187,53248\n"
"M 36,164,152\ns_add_u32 m0, s50, 8192\nM 148,160,132\nR 180,187,57344\nM 148,164,148\ns_add_u32 s63, s53, 131072\n"
"M 132,160,128\nR 182,187,61440\nM 132,164,144\nG 4\nM 136,168,128\nR 160,188,0\n"
"M 136,172,144\nbuffer_load_dwordx4 v220, s[8:11], s63 offen lds\nM 152,168,132\nR 162,188,4096\nM 152,172,148\nM 40,168,136\n"
"R 164,188,8192\nM 40,172,152\nM 56,168,140\nR 166,188,12288\nv_mfma_f32_16x16x32_bf16 v[136:139], v[242:245], v[208:211], v[136:139]\ns_bfm_b64 exec, 16, 16\n"
"buffer_load_dword v249, v248, s[12:15], s54 offen\ns_mov_b64 exec, -1\nM 56,172,156\nG 4\nM 60,176,140\nR 168,189,0\n"
"v_mfma_f32_16x16x32_bf16 v[140:143], v[244:247], v[208:211], v[140:143]\nM 60,180,156\nbuffer_load_dwordx4 v221, s[8:11], s63 offen offset:1024 lds\nM 44,176,136\nR 170,189,4096\nv_mfma_f32_16x16x32_bf16 v[152:155], v[240:243], v[210:213], v[152:155]\n"
"s_add_u32 s63, s53, 196608\nM 44,180,152\ns_add_u32 m0, s50, 16384\nM 156,176,132\nR 172,189,8192\nv_mfma_f32_16x16x32_bf16 v[156:159], v[242:245], v[210:213], v[156:159]\n"
"M 156,180,148\nbuffer_load_dwordx4 v220, s[8:11], s63 offen lds\nM 140,176,128\nR 174,189,12288\nM 140,180,144\nG 4\n"
"M 128,160,160\nR 176,190,0\nM 128,164,176\nM 144,160,164\nR 178,190,4096\nM 144,164,180\n"
"buffer_load_dwordx4 v221, s[8:11], s63 offen offset:1024 lds\nM 32,160,168\nR 180,190,8192\nM 32,164,184\ns_add_u32 m0, s50, 24576\nM 48,160,172\n"
"R 182,190,12288\ns_bfm_b64 exec, 16, 32\nbuffer_load_dword v249, v248, s[12:15], s54 offen\ns_mov_b64 exec, -1\nM 48,164,188\nG 4\n"
"M 52,168,172\nR 160,191,0\nM 52,172,188\ns_add_u32 s63, s53, 262144\nM 36,168,168\nR 162,191,4096\n"
"M 36,172,184\nbuffer_load_dwordx4 v220, s[8:11], s63 offen lds\nM 148,168,164\nR 164,191,8192\ns_lshl_b32 s60, s40, 12\ns_add_u32 s60, s60, 65536\n"
"v_add_u32 v226, s60, v252\nv_add_u32 v111, s60, v227\nM 148,172,180\nM 132,168,160\nR 166,191,12288\ns_cmp_eq_u32 s40, 0\n"
"s_cselect_b64 s[88:89], 0, -1\ns_cselect_b32 s60, s41, 0\ns_cmp_lg_u32 s60, 0\ns_cselect_b64 s[66:67], -1, 0\nM 132,172,176\nG 4\n"
"M 136,176,160\nR 168,188,16384\nM 136,180,176\ns_mov_b64 exec, s[88:89]\nds_read_b128 v[230:233], v226 offset:0\nds_read_b64 v[64:65], v111 offset:2048\n"
"s_mov_b64 exec, -1\nM 152,176,164\nR 170,188,20480\ns_waitcnt vmcnt(13) lgkmcnt(1)\ns_barrier\nM 152,180,180\n"
"buffer_load_dwordx4 v221, s[8:11], s63 offen offset:1024 lds\nM 40,176,168\nR 172,188,24576\nM 40,180,184\ns_mov_b64 exec, s[88:89]\nds_read_b64 v[70:71], v111 offset:2560\n"
"ds_read_b128 v[234:237], v226 offset:1024\ns_mov_b64 exec, -1\nM 56,176,172\nR 174,188,28672\ns_bfm_b64 exec, 16, 48\nbuffer_load_dword v249, v248, s[12:15], s54 offen\n"
"s_mov_b64 exec, -1\nM 56,180,188\nM 60,160,172\nR 176,189,16384\nM 60,164,188\ns_mov_b64 exec, s[88:89]\n"
"ds_read_b64 v[76:77], v111 offset:3072\nds_read_b64 v[82:83], v111 offset:3584\ns_mov_b64 exec, -1\nM 44,160,168\nR 178,189,20480\nM 44,164,184\n"
"M 156,160,164\nR 180,189,24576\nM 156,164,180\ns_mov_b64 exec, s[66:67]\nbuffer_load_dwordx4 v[230:233], v252, s[20:23], s65 offen offset:0 sc1\ns_mov_b64 exec, -1\n"
"M 140,160,160\nR 182,189,28672\nM 140,164,176\nG 6\nM 32,168,200\nR 160,190,16384\n"
"s_mov_b64 exec, s[66:67]\nbuffer_load_dwordx2 v[64:65], v227, s[20:23], s65 offen offset:2048 sc1\ns_mov_b64 exec, -1\nM 32,172,216\nR 162,190,20480\nM 48,168,204\n"
"R 164,190,24576\nM 48,172,220\nR 166,190,28672\nG 4\nM 52,176,204\nR 168,191,16384\n"
"s_mov_b64 exec, s[66:67]\nbuffer_load_dwordx2 v[70:71], v227, s[20:23], s65 offen offset:2560 sc1\ns_mov_b64 exec, -1\nM 52,180,220\nR 170,191,20480\nM 36,176,200\n"
"R 172,191,24576\nM 36,180,216\nR 174,191,28672\nG 4\nM 40,160,200\nR 176,188,32768\n"
"s_mov_b64 exec, s[66:67]\nbuffer_load_dwordx4 v[234:237], v252, s[20:23], s65 offen offset:1024 sc1\ns_mov_b64 exec, -1\nM 40,164,216\nR 178,188,36864\nM 56,160,204\n"
"R 180,188,40960\nM 56,164,220\nR 182,188,45056\nG 4\nM 60,168,204\nR 160,189,32768\n"
"s_mov_b64 exec, s[66:67]\nbuffer_load_dwordx2 v[76:77], v227, s[20:23], s65 offen offset:3072 sc1\ns_mov_b64 exec, -1\nM 60,172,220\nR 162,189,36864\nM 44,168,200\n"
"R 164,189,40960\nM 44,172,216\nR 166,189,45056\nG 4\nM 32,176,232\nR 168,190,32768\n"
"s_mov_b64 exec, s[66:67]\nbuffer_load_dwordx2 v[82:83], v227, s[20:23], s65 offen offset:3584 sc1\ns_mov_b64 exec, -1\nLq690:\ns_lshr_b32 s60, s45, 3\ns_lshl2_add_u32 s60, s60, s40\n"
"s_lshl_b32 s60, s60, 8\nv_lshl_add_u32 v224, v252, 3, s60\nv_add_u32 v224, Lq690+4096-LqPC, v224\nv_min_u32 v224, LqE-LqPC-4, v224\ns_mov_b64 exec, 3\nglobal_load_dword v224, v224, s[98:99]\n"
"s_mov_b64 exec, -1\nM 32,180,248\nR 170,190,36864\nM 48,176,236\nR 172,190,40960\nM 48,180,252\n"
"R 174,190,45056\nG 4\nM 52,160,236\nR 176,191,32768\nM 52,164,252\nR 178,191,36864\n"
"M 36,160,232\nR 180,191,40960\nM 36,164,248\nR 182,191,45056\nG 4\nM 40,168,232\n"
"R 160,188,16384\nM 40,172,248\nR 162,188,20480\nM 56,168,236\nR 164,188,24576\nM 56,172,252\n"
"R 166,188,28672\nG 4\nM 60,176,236\nR 168,189,16384\nM 60,180,252\nR 170,189,20480\n"
"M 44,176,232\nR 172,189,24576\nM 44,180,248\nR 174,189,28672\nG 4\nM 128,160,192\n"
"R 176,190,16384\nM 128,164,208\nR 178,190,20480\ns_mov_b32 m0, s51\nM 144,160,196\nR 180,190,24576\n"
"ds_write_addtid_b32 v32 offset:0\nM 144,164,212\nR 182,190,28672\nds_write_addtid_b32 v33 offset:256\nG 6\nM 148,168,196\n"
"R 160,191,16384\nds_write_addtid_b32 v34 offset:512\nM 148,172,212\nR 162,191,20480\nds_write_addtid_b32 v35 offset:768\nM 132,168,192\n"
"R 164,191,24576\nds_write_addtid_b32 v36 offset:1024\nM 132,172,208\nR 166,191,28672\nds_write_addtid_b32 v37 offset:1280\nG 9\n"
"M 136,176,192\nR 168,188,32768\nds_write_addtid_b32 v38 offset:1536\nM 136,180,208\nR 170,188,36864\nds_write_addtid_b32 v39 offset:1792\n"
"M 152,176,196\nR 172,188,40960\nds_write_addtid_b32 v40 offset:2048\nM 152,180,212\nR 174,188,45056\nds_write_addtid_b32 v41 offset:2304\n"
"G 9\nM 156,160,196\nR 176,189,32768\nds_write_addtid_b32 v42 offset:2560\nM 156,164,212\nR 178,189,36864\n"
"ds_write_addtid_b32 v43 offset:2816\nM 140,160,192\nR 180,189,40960\nds_write_addtid_b32 v44 offset:3072\nds_write_addtid_b32 v45 offset:3328\nds_write_addtid_b32 v46 offset:3584\n"
"ds_write_addtid_b32 v47 offset:3840\nds_write_addtid_b32 v48 offset:4096\nds_write_addtid_b32 v49 offset:4352\nds_write_addtid_b32 v50 offset:4608\nds_write_addtid_b32 v51 offset:4864\nds_write_addtid_b32 v52 offset:5120\n"
"ds_write_addtid_b32 v53 offset:5376\nds_write_addtid_b32 v54 offset:5632\nds_write_addtid_b32 v55 offset:5888\nds_write_addtid_b32 v56 offset:6144\nds_write_addtid_b32 v57 offset:6400\nds_write_addtid_b32 v58 offset:6656\n"
"ds_write_addtid_b32 v59 offset:6912\nds_write_addtid_b32 v60 offset:7168\nds_write_addtid_b32 v61 offset:7424\nds_write_addtid_b32 v62 offset:7680\nds_write_addtid_b32 v63 offset:7936\nM 140,164,208\n"
"R 182,189,45056\nG 15\nM 128,168,224\nR 160,190,32768\ns_add_u32 m0, s50, 32768\nM 128,172,240\n"
"R 162,190,36864\nG 3\ns_barrier\ns_add_u32 s63, s53, 327680\nM 144,168,228\nR 164,190,40960\n"
"buffer_load_dwordx4 v220, s[8:11], s63 offen lds\nds_read2st64_b32 v[32:33], v225 offset0:0 offset1:1\nM 144,172,244\nR 166,190,45056\nbuffer_load_dwordx4 v221, s[8:11], s63 offen offset:1024 lds\nds_read2st64_b32 v[34:35], v225 offset0:2 offset1:3\n"
"G 6\nM 148,176,228\nR 168,191,32768\ns_add_u32 m0, s50, 40960\nds_read2st64_b32 v[36:37], v225 offset0:4 offset1:5\nM 148,180,244\n"
"R 170,191,36864\ns_add_u32 s63, s53, 393216\nds_read2st64_b32 v[38:39], v225 offset0:6 offset1:7\nM 132,176,224\nR 172,191,40960\nds_read2st64_b32 v[40:41], v225 offset0:8 offset1:9\n"
"M 132,180,240\nR 174,191,45056\nds_read2st64_b32 v[42:43], v225 offset0:10 offset1:11\nG 9\nM 136,160,224\nbuffer_load_dwordx4 v220, s[8:11], s63 offen lds\n"
"ds_read2st64_b32 v[44:45], v225 offset0:12 offset1:13\nM 136,164,240\nds_read2st64_b32 v[46:47], v225 offset0:14 offset1:15\nds_read2st64_b32 v[48:49], v225 offset0:16 offset1:17\nM 152,160,228\nbuffer_load_dwordx4 v221, s[8:11], s63 offen offset:1024 lds\n"
"ds_read2st64_b32 v[50:51], v225 offset0:18 offset1:19\nM 152,164,244\nds_read2st64_b32 v[52:53], v225 offset0:20 offset1:21\nds_read2st64_b32 v[54:55], v225 offset0:22 offset1:23\nG 7\nM 156,168,228\n"
"ds_read2st64_b32 v[56:57], v225 offset0:24 offset1:25\nds_read2st64_b32 v[58:59], v225 offset0:26 offset1:27\nM 156,172,244\nds_read2st64_b32 v[60:61], v225 offset0:28 offset1:29\nds_read2st64_b32 v[62:63], v225 offset0:30 offset1:31\nM 140,168,224\n"
"M 140,172,240\nG 7\nv_pk_add_f32 v[144:145], v[144:145], v[48:49]\nG 6\nv_pk_add_f32 v[146:147], v[146:147], v[50:51]\nG 5\n"
"v_pk_add_f32 v[148:149], v[148:149], v[52:53]\nG 4\nv_pk_add_f32 v[150:151], v[150:151], v[54:55]\nG 3\nv_pk_add_f32 v[152:153], v[152:153], v[56:57]\nG 2\n"
"v_pk_add_f32 v[154:155], v[154:155], v[58:59]\nG 1\nv_pk_add_f32 v[156:157], v[156:157], v[60:61]\nG 0\nv_pk_add_f32 v[158:159], v[158:159], v[62:63]\nv_pk_add_f32 v[128:129], v[128:129], v[32:33]\n"
"v_pk_add_f32 v[130:131], v[130:131], v[34:35]\nv_pk_add_f32 v[132:133], v[132:133], v[36:37]\nv_pk_add_f32 v[134:135], v[134:135], v[38:39]\nv_pk_add_f32 v[136:137], v[136:137], v[40:41]\nv_pk_add_f32 v[138:139], v[138:139], v[42:43]\nv_pk_add_f32 v[140:141], v[140:141], v[44:45]\n"
"v_pk_add_f32 v[142:143], v[142:143], v[46:47]\nv_cvt_pk_bf16_f32 v144, v144, v145\nv_cvt_pk_bf16_f32 v145, v146, v147\nv_cvt_pk_bf16_f32 v146, v148, v149\nv_cvt_pk_bf16_f32 v147, v150, v151\nv_cvt_pk_bf16_f32 v152, v152, v153\n"
"v_cvt_pk_bf16_f32 v153, v154, v155\nv_cvt_pk_bf16_f32 v154, v156, v157\nv_cvt_pk_bf16_f32 v155, v158, v159\nv_cvt_pk_bf16_f32 v128, v128, v129\nv_cvt_pk_bf16_f32 v129, v130, v131\nv_cvt_pk_bf16_f32 v130, v132, v133\n"
"v_cvt_pk_bf16_f32 v131, v134, v135\nv_cvt_pk_bf16_f32 v136, v136, v137\nv_cvt_pk_bf16_f32 v137, v138, v139\nv_cvt_pk_bf16_f32 v138, v140, v141\nv_cvt_pk_bf16_f32 v139, v142, v143\nv_mfma_f32_16x16x32_bf16 v[52:55], v[16:19], v[144:147], v[96:99]\n"
"v_mfma_f32_16x16x32_bf16 v[48:51], v[0:3], v[144:147], v[100:103]\nv_mfma_f32_16x16x32_bf16 v[60:63], v[20:23], v[152:155], v[96:99]\nv_mfma_f32_16x16x32_bf16 v[56:59], v[4:7], v[152:155], v[100:103]\ns_waitcnt vmcnt(8)\nLpg22:\ns_cbranch_execz LretW0n\n"
"v_max3_u32 v248, v233, v65, v71\nv_cmp_eq_u32 vcc, -1, v248\ns_nop 0\ns_cbranch_vccnz Lq692\nLq693:\ns_waitcnt vmcnt(5)\n"
"v_max3_u32 v248, v237, v77, v83\nv_cmp_eq_u32 vcc, -1, v248\ns_nop 0\ns_cbranch_vccnz Lq694\nLq695:\nv_mfma_f32_16x16x32_bf16 v[36:39], v[0:3], v[128:131], v[96:99]\n"
"v_mfma_f32_16x16x32_bf16 v[32:35], v[230:233], v[128:131], v[104:107]\nv_mfma_f32_16x16x32_bf16 v[44:47], v[4:7], v[136:139], v[96:99]\nv_mfma_f32_16x16x32_bf16 v[40:43], v[234:237], v[136:139], v[104:107]\ns_lshl_b32 s84, s42, 11\ns_add_u32 s84, s84, s49\ns_add_u32 s84, s84, 6291456\n"
"s_add_u32 s85, s84, 32768\nv_mov_b32 v238, s46\nv_mov_b32 v239, s47\nv_max_f32 v52, v52, v53\nv_max_f32 v54, v54, v55\nv_max3_f32 v52, v52, v48, v49\n"
"v_max3_f32 v54, v54, v50, v51\nv_max_f32 v118, v52, v54\nv_max_f32 v60, v60, v61\nv_max_f32 v62, v62, v63\nv_max3_f32 v60, v60, v56, v57\nv_max3_f32 v62, v62, v58, v59\n"
"v_max_f32 v119, v60, v62\nv_max_f32 v36, v36, v37\nv_max_f32 v38, v38, v39\nv_max3_f32 v36, v36, v32, v33\nv_max3_f32 v38, v38, v34, v35\nv_max_f32 v116, v36, v38\n"
"v_max_f32 v44, v44, v45\nv_max_f32 v46, v46, v47\nv_max3_f32 v44, v44, v40, v41\nv_max3_f32 v46, v46, v42, v43\nv_max_f32 v117, v44, v46\nv_permlane32_swap_b32 v116, v118\n"
"s_nop 0\nv_permlane32_swap_b32 v117, v119\nv_max_f32 v116, v116, v118\nv_max_f32 v117, v117, v119\ns_nop 1\nv_permlane16_swap_b32 v116, v117\n"
"v_max_f32 v116, v116, v117\nv_mov_b32 v117, v116\ns_nop 1\nv_permlane16_swap_b32 v116, v117\nv_mov_b32 v118, v116\nv_mov_b32 v119, v117\n"
"s_nop 0\nv_permlane32_swap_b32 v116, v118\nv_permlane32_swap_b32 v117, v119\nv_mul_f32 v116, v116, v215\nv_mul_f32 v118, v118, v218\nv_mul_f32 v117, v117, v215\n"
"v_mul_f32 v119, v119, v218\nv_fma_f32 v52, v52, v218, -v118\nv_fma_f32 v54, v54, v218, -v118\nv_fma_f32 v60, v60, v218, -v119\nv_fma_f32 v62, v62, v218, -v119\nv_fma_f32 v36, v36, v215, -v116\n"
"v_fma_f32 v38, v38, v215, -v116\nv_fma_f32 v44, v44, v215, -v117\nv_fma_f32 v46, v46, v215, -v117\nv_exp_f32 v52, v52\nv_exp_f32 v54, v54\nv_exp_f32 v60, v60\n"
"v_exp_f32 v62, v62\nv_exp_f32 v36, v36\nv_exp_f32 v38, v38\nv_exp_f32 v44, v44\nv_exp_f32 v46, v46\nv_add_f32 v118, v52, v54\n"
"v_add_f32 v119, v60, v62\nv_add_f32 v116, v36, v38\nv_add_f32 v117, v44, v46\ns_nop 0\nv_permlane32_swap_b32 v116, v118\nv_permlane32_swap_b32 v117, v119\n"
"v_add_f32 v116, v116, v118\nv_add_f32 v117, v117, v119\ns_nop 1\nv_permlane16_swap_b32 v116, v117\nv_add_f32 v116, v116, v117\ns_barrier\n"
"v_rcp_f32 v116, v116\ns_nop 0\nv_mov_b32 v117, v116\ns_nop 1\nv_permlane16_swap_b32 v116, v117\nv_mov_b32 v118, v116\n"
"v_mov_b32 v119, v117\ns_nop 0\nv_permlane32_swap_b32 v116, v118\nv_permlane32_swap_b32 v117, v119\nv_mul_f32 v52, v52, v118\nv_mul_f32 v54, v54, v118\n"
"v_mul_f32 v60, v60, v119\nv_mul_f32 v62, v62, v119\nv_mul_f32 v36, v36, v116\nv_mul_f32 v38, v38, v116\nv_mul_f32 v44, v44, v117\nv_mul_f32 v46, v46, v117\n"
"v_cvt_pk_bf16_f32 v52, v52, v52\nv_cvt_pk_bf16_f32 v54, v54, v54\nv_cvt_pk_bf16_f32 v60, v60, v60\nv_cvt_pk_bf16_f32 v62, v62, v62\nv_cvt_pk_bf16_f32 v36, v36, v36\nv_cvt_pk_bf16_f32 v38, v38, v38\n"
"v_cvt_pk_bf16_f32 v44, v44, v44\nv_cvt_pk_bf16_f32 v46, v46, v46\nv_and_b32 v48, v52, v112\nv_and_b32 v49, v54, v113\nv_and_b32 v50, v52, v114\nv_and_b32 v51, v54, v115\n"
"v_and_b32 v56, v60, v112\nv_and_b32 v57, v62, v113\nv_and_b32 v58, v60, v114\nv_and_b32 v59, v62, v115\nv_mfma_f32_16x16x32_bf16 v[144:147], v[66:69], v[48:51], 0\nv_mfma_f32_16x16x32_bf16 v[148:151], v[72:75], v[48:51], 0\n"
"v_and_b32 v32, v36, v112\nv_and_b32 v33, v38, v113\nv_and_b32 v34, v36, v114\nv_and_b32 v35, v38, v115\nv_mfma_f32_16x16x32_bf16 v[152:155], v[78:81], v[56:59], 0\nv_mfma_f32_16x16x32_bf16 v[156:159], v[84:87], v[56:59], 0\n"
"v_and_b32 v40, v44, v112\nv_and_b32 v41, v46, v113\nv_and_b32 v42, v44, v114\nv_and_b32 v43, v46, v115\nv_mfma_f32_16x16x32_bf16 v[128:131], v[64:67], v[32:35], 0\nv_mfma_f32_16x16x32_bf16 v[132:135], v[70:73], v[32:35], 0\n"
"v_mfma_f32_16x16x32_bf16 v[136:139], v[76:79], v[40:43], 0\nv_mfma_f32_16x16x32_bf16 v[140:143], v[82:85], v[40:43], 0\nv_cvt_pk_bf16_f32 v144, v144, v145\nv_cvt_pk_bf16_f32 v145, v146, v147\nv_cvt_pk_bf16_f32 v146, v148, v149\nv_cvt_pk_bf16_f32 v147, v150, v151\n"
"buffer_store_dwordx4 v[144:147], v252, s[20:23], s85 offen offset:0 sc1\nv_cvt_pk_bf16_f32 v152, v152, v153\nv_cvt_pk_bf16_f32 v153, v154, v155\nv_cvt_pk_bf16_f32 v154, v156, v157\nv_cvt_pk_bf16_f32 v155, v158, v159\nbuffer_store_dwordx4 v[152:155], v252, s[20:23], s85 offen offset:1024 sc1\n"
"v_cvt_pk_bf16_f32 v128, v128, v129\nv_cvt_pk_bf16_f32 v129, v130, v131\nv_cvt_pk_bf16_f32 v130, v132, v133\nv_cvt_pk_bf16_f32 v131, v134, v135\nbuffer_store_dwordx4 v[128:131], v252, s[20:23], s84 offen offset:0 sc1\nv_cvt_pk_bf16_f32 v136, v136, v137\n"
"v_cvt_pk_bf16_f32 v137, v138, v139\nv_cvt_pk_bf16_f32 v138, v140, v141\nv_cvt_pk_bf16_f32 v139, v142, v143\nbuffer_store_dwordx4 v[136:139], v252, s[20:23], s84 offen offset:1024 sc1\ns_mov_b64 exec, 0\ns_cmp_eq_u32 s40, 1\n"
"s_cbranch_scc1 LretO1\ns_cmp_eq_u32 s40, 2\ns_cbranch_scc1 LretO1\ns_cmp_eq_u32 s40, 3\ns_cbranch_scc1 LretO1\ns_branch Lpg23\n"
"LretO1:\ns_mov_b64 exec, -1\ns_add_u32 s61, s79, 88498176\ns_lshl_b32 s62, s42, 3\ns_add_u32 s61, s61, s62\ns_waitcnt vmcnt(0)\n"
"s_mov_b64 exec, 1\nbuffer_atomic_swap_x2 v[238:239], off, s[20:23], s61 sc1\ns_mov_b64 exec, -1\nv_bfe_u32 v250, v252, 7, 1\nv_mul_u32_u24 v250, 2176, v250\nv_bfe_u32 v251, v252, 4, 3\n"
"v_lshl_add_u32 v250, v251, 3, v250\ns_lshl_b32 s60, s90, 6\nv_add_u32 v250, s60, v250\ns_sleep 3\nv_mov_b32 v248, 0\nv_mov_b32 v249, 0\n"
"s_add_u32 s61, s80, 88498176\ns_mov_b64 exec, 0xffff\nbuffer_atomic_or_x2 v[248:249], v250, s[20:23], s61 offen sc0 sc1\ns_mov_b64 exec, -1\ns_add_u32 m0, s50, 49152\ns_add_u32 s63, s53, 458752\n"
"buffer_load_dwordx4 v220, s[8:11], s63 offen lds\nbuffer_load_dwordx4 v221, s[8:11], s63 offen offset:1024 lds\ns_add_u32 m0, s50, 57344\ns_add_u32 s63, s53, 524288\nbuffer_load_dwordx4 v220, s[8:11], s63 offen lds\nbuffer_load_dwordx4 v221, s[8:11], s63 offen offset:1024 lds\n"
"s_add_u32 m0, s50, 65536\ns_add_u32 s63, s53, 589824\nbuffer_load_dwordx4 v220, s[8:11], s63 offen lds\nbuffer_load_dwordx4 v221, s[8:11], s63 offen offset:1024 lds\ns_add_u32 m0, s50, 73728\ns_add_u32 s63, s53, 655360\n"
"buffer_load_dwordx4 v220, s[8:11], s63 offen lds\nbuffer_load_dwordx4 v221, s[8:11], s63 offen offset:1024 lds\ns_add_u32 m0, s50, 81920\ns_add_u32 s63, s53, 720896\nbuffer_load_dwordx4 v220, s[8:11], s63 offen lds\nbuffer_load_dwordx4 v221, s[8:11], s63 offen offset:1024 lds\n"
"s_waitcnt vmcnt(10)\nv_cmp_eq_u64 s[86:87], s[46:47], v[248:249]\ns_nop 1\ns_and_b32 s88, s86, 0xffff\ns_cmp_eq_u32 s88, 0xffff\ns_cbranch_scc1 Lq706\n"
"v_mov_b32 v246, 0\nv_mov_b32 v247, 0\ns_add_u32 s61, s80, 88498176\ns_mov_b64 exec, 0xffff\nbuffer_atomic_or_x2 v[246:247], v250, s[20:23], s61 offen sc0 sc1\ns_mov_b64 exec, -1\n"
"Lq706:\ns_add_u32 m0, s50, 90112\ns_add_u32 s63, s53, 786432\nbuffer_load_dwordx4 v220, s[8:11], s63 offen lds\nbuffer_load_dwordx4 v221, s[8:11], s63 offen offset:1024 lds\ns_add_u32 m0, s50, 98304\n"
"s_add_u32 s63, s53, 851968\nbuffer_load_dwordx4 v220, s[8:11], s63 offen lds\nbuffer_load_dwordx4 v221, s[8:11], s63 offen offset:1024 lds\ns_add_u32 m0, s50, 106496\ns_add_u32 s63, s53, 917504\nbuffer_load_dwordx4 v220, s[8:11], s63 offen lds\n"
"buffer_load_dwordx4 v221, s[8:11], s63 offen offset:1024 lds\nv_bfe_u32 v234, v252, 4, 4\nv_lshrrev_b32 v235, 8, v252\nv_lshlrev_b32 v235, 2, v235\nv_sub_u32 v234, v234, v235\nv_lshlrev_b32 v235, 4, v234\n"
"s_mov_b64 s[60:61], 0x3f80\nv_lshlrev_b64 v[208:209], v235, s[60:61]\nv_cmp_gt_u32 s[60:61], 4, v234\ns_nop 1\nv_cndmask_b32 v212, 0, v208, s[60:61]\nv_cndmask_b32 v208, 0, v208, s[60:61]\n"
"v_cndmask_b32 v213, 0, v209, s[60:61]\nv_cndmask_b32 v209, 0, v209, s[60:61]\ns_nop 1\nv_mfma_f32_16x16x32_bf16 v[0:3], v[208:211], v[192:195], 0\nv_mfma_f32_16x16x32_bf16 v[4:7], v[210:213], v[192:195], 0\nv_mfma_f32_16x16x32_bf16 v[8:11], v[208:211], v[196:199], 0\n"
"v_mfma_f32_16x16x32_bf16 v[12:15], v[210:213], v[196:199], 0\nv_mfma_f32_16x16x32_bf16 v[16:19], v[208:211], v[200:203], 0\nv_mfma_f32_16x16x32_bf16 v[20:23], v[210:213], v[200:203], 0\nv_mfma_f32_16x16x32_bf16 v[24:27], v[208:211], v[204:207], 0\nv_mfma_f32_16x16x32_bf16 v[28:31], v[210:213], v[204:207], 0\ns_cmp_eq_u32 s88, 0xffff\n"
"s_cbranch_scc1 Lq707\ns_waitcnt vmcnt(6)\nv_mov_b32 v248, v246\nv_mov_b32 v249, v247\nv_cmp_eq_u64 s[86:87], s[46:47], v[248:249]\ns_nop 1\n"
"s_and_b32 s62, s86, 0xffff\ns_cmp_eq_u32 s62, 0xffff\ns_cbranch_scc1 Lq698\nLq697:\nv_mov_b32 v248, 0\nv_mov_b32 v249, 0\n"
"s_add_u32 s61, s80, 88498176\ns_mov_b64 exec, 0xffff\nbuffer_atomic_or_x2 v[248:249], v250, s[20:23], s61 offen sc0 sc1\ns_mov_b64 exec, -1\ns_waitcnt vmcnt(0)\nv_cmp_eq_u64 s[86:87], s[46:47], v[248:249]\n"
"s_nop 1\ns_and_b32 s62, s86, 0xffff\ns_cmp_eq_u32 s62, 0xffff\ns_cbranch_scc1 Lq698\ns_sub_u32 s48, s48, 1\ns_cmp_gt_i32 s48, 0\n"
"s_cbranch_scc1 Lq697\nLq698:\nLq707:\ns_add_u32 s64, s70, 6291456\nbuffer_load_dwordx4 a[0:3], v252, s[20:23], s64 offen offset:0 sc1\ns_add_u32 s65, s70, 6324224\n"
"buffer_load_dwordx4 a[4:7], v252, s[20:23], s65 offen offset:0 sc1\ns_add_u32 s66, s71, 6291456\nbuffer_load_dwordx4 a[8:11], v252, s[20:23], s66 offen offset:0 sc1\ns_add_u32 s67, s71, 6324224\nbuffer_load_dwordx4 a[12:15], v252, s[20:23], s67 offen offset:0 sc1\nbuffer_load_dwordx4 a[16:19], v252, s[20:23], s64 offen offset:1024 sc1\n"
"buffer_load_dwordx4 a[20:23], v252, s[20:23], s65 offen offset:1024 sc1\nbuffer_load_dwordx4 a[24:27], v252, s[20:23], s66 offen offset:1024 sc1\nbuffer_load_dwordx4 a[28:31], v252, s[20:23], s67 offen offset:1024 sc1\ns_barrier\nR 160,188,49152\nR 162,188,53248\n"
"R 164,184,0\nR 166,184,4096\nR 168,189,49152\nR 170,189,53248\nR 172,185,0\nR 174,185,4096\n"
"R 176,190,49152\nR 178,190,53248\nR 180,186,0\nR 182,186,4096\nbuffer_load_dwordx4 a[32:35], v252, s[20:23], s64 offen offset:2048 sc1\nbuffer_load_dwordx4 a[36:39], v252, s[20:23], s65 offen offset:2048 sc1\n"
"buffer_load_dwordx4 a[40:43], v252, s[20:23], s66 offen offset:2048 sc1\nbuffer_load_dwordx4 a[44:47], v252, s[20:23], s67 offen offset:2048 sc1\nbuffer_load_dwordx4 a[48:51], v252, s[20:23], s64 offen offset:3072 sc1\nbuffer_load_dwordx4 a[52:55], v252, s[20:23], s65 offen offset:3072 sc1\nbuffer_load_dwordx4 a[56:59], v252, s[20:23], s66 offen offset:3072 sc1\nbuffer_load_dwordx4 a[60:63], v252, s[20:23], s67 offen offset:3072 sc1\n"
"s_add_u32 s64, s70, 6295552\nbuffer_load_dwordx4 a[64:67], v252, s[20:23], s64 offen offset:0 sc1\ns_add_u32 s65, s70, 6328320\nbuffer_load_dwordx4 a[68:71], v252, s[20:23], s65 offen offset:0 sc1\ns_add_u32 s66, s71, 6295552\nbuffer_load_dwordx4 a[72:75], v252, s[20:23], s66 offen offset:0 sc1\n"
"s_add_u32 s67, s71, 6328320\nbuffer_load_dwordx4 a[76:79], v252, s[20:23], s67 offen offset:0 sc1\ns_waitcnt vmcnt(19) lgkmcnt(10)\nM 0,160,0\nbuffer_load_dwordx4 a[80:83], v252, s[20:23], s64 offen offset:1024 sc1\ns_waitcnt vmcnt(16) lgkmcnt(8)\n"
"M 0,164,16\nM 16,160,4\ns_waitcnt vmcnt(15)\nM 16,164,20\nv_mfma_f32_16x16x32_bf16 v[32:35], v[160:163], a[8:11], 0\nbuffer_load_dwordx4 a[84:87], v252, s[20:23], s65 offen offset:1024 sc1\n"
"s_waitcnt vmcnt(15)\nM 32,164,24\nv_mfma_f32_16x16x32_bf16 v[48:51], v[160:163], a[12:15], 0\ns_waitcnt vmcnt(14)\nM 48,164,28\nG 4\n"
"v_mfma_f32_16x16x32_bf16 v[52:55], v[168:171], a[12:15], 0\nR 160,191,49152\nM 52,172,28\nbuffer_load_dwordx4 a[88:91], v252, s[20:23], s66 offen offset:1024 sc1\nv_mfma_f32_16x16x32_bf16 v[36:39], v[168:171], a[8:11], 0\nR 162,191,53248\n"
"M 36,172,24\nM 20,168,4\nR 164,187,0\nM 20,172,20\nbuffer_load_dwordx4 a[92:95], v252, s[20:23], s67 offen offset:1024 sc1\nM 4,168,0\n"
"R 166,187,4096\nM 4,172,16\nG 4\nM 8,176,0\nR 168,184,8192\nM 8,180,16\n"
"buffer_load_dwordx4 a[96:99], v252, s[20:23], s64 offen offset:2048 sc1\nM 24,176,4\nR 170,184,12288\nM 24,180,20\nv_mfma_f32_16x16x32_bf16 v[40:43], v[176:179], a[8:11], 0\nR 172,184,16384\n"
"M 40,180,24\nbuffer_load_dwordx4 a[100:103], v252, s[20:23], s65 offen offset:2048 sc1\nv_mfma_f32_16x16x32_bf16 v[56:59], v[176:179], a[12:15], 0\nR 174,184,20480\nM 56,180,28\nG 4\n"
"v_mfma_f32_16x16x32_bf16 v[60:63], v[160:163], a[12:15], 0\nR 176,185,8192\nM 60,164,28\nbuffer_load_dwordx4 a[104:107], v252, s[20:23], s66 offen offset:2048 sc1\nv_mfma_f32_16x16x32_bf16 v[44:47], v[160:163], a[8:11], 0\nR 178,185,12288\n"
"M 44,164,24\nM 28,160,4\nR 180,185,16384\nM 28,164,20\nbuffer_load_dwordx4 a[108:111], v252, s[20:23], s67 offen offset:2048 sc1\nM 12,160,0\n"
"R 182,185,20480\nM 12,164,16\nG 4\ns_waitcnt vmcnt(19)\nM 0,168,32\nR 160,186,8192\n"
"s_waitcnt vmcnt(15)\nM 0,172,48\nbuffer_load_dwordx4 a[112:115], v252, s[20:23], s64 offen offset:3072 sc1\nM 16,168,36\nR 162,186,12288\ns_waitcnt vmcnt(15)\n"
"M 16,172,52\nM 32,168,40\nR 164,186,16384\ns_waitcnt vmcnt(14)\nM 32,172,56\nbuffer_load_dwordx4 a[116:119], v252, s[20:23], s65 offen offset:3072 sc1\n"
"M 48,168,44\nR 166,186,20480\ns_waitcnt vmcnt(14)\nM 48,172,60\nG 4\nM 52,176,44\n"
"R 168,187,8192\nM 52,180,60\nbuffer_load_dwordx4 a[120:123], v252, s[20:23], s66 offen offset:3072 sc1\nM 36,176,40\nR 170,187,12288\nM 36,180,56\n"
"M 20,176,36\nR 172,187,16384\nM 20,180,52\nbuffer_load_dwordx4 a[124:127], v252, s[20:23], s67 offen offset:3072 sc1\nM 4,176,32\nR 174,187,20480\n"
"M 4,180,48\nG 4\nM 8,160,32\nR 176,184,24576\nM 8,164,48\ns_add_u32 s64, s70, 6299648\n"
"buffer_load_dwordx4 a[128:131], v252, s[20:23], s64 offen offset:0 sc1\nM 24,160,36\nR 178,184,28672\nM 24,164,52\nM 40,160,40\nR 180,184,32768\n"
"M 40,164,56\ns_add_u32 s65, s70, 6332416\nbuffer_load_dwordx4 a[132:135], v252, s[20:23], s65 offen offset:0 sc1\nM 56,160,44\nR 182,184,36864\nM 56,164,60\n"
"G 4\nM 60,168,44\nR 160,185,24576\nM 60,172,60\ns_add_u32 s66, s71, 6299648\nbuffer_load_dwordx4 a[136:139], v252, s[20:23], s66 offen offset:0 sc1\n"
"M 44,168,40\nR 162,185,28672\nM 44,172,56\nM 28,168,36\nR 164,185,32768\nM 28,172,52\n"
"s_add_u32 s67, s71, 6332416\nbuffer_load_dwordx4 a[140:143], v252, s[20:23], s67 offen offset:0 sc1\nM 12,168,32\nR 166,185,36864\nM 12,172,48\ns_barrier\n"
"G 4\ns_waitcnt vmcnt(19)\nM 0,176,64\nR 168,186,24576\ns_waitcnt vmcnt(15)\nM 0,180,80\n"
"buffer_load_dwordx4 a[144:147], v252, s[20:23], s64 offen offset:1024 sc1\nM 16,176,68\nR 170,186,28672\ns_waitcnt vmcnt(15)\nM 16,180,84\nM 32,176,72\n"
"R 172,186,32768\ns_waitcnt vmcnt(14)\nM 32,180,88\nbuffer_load_dwordx4 a[148:151], v252, s[20:23], s65 offen offset:1024 sc1\nM 48,176,76\nR 174,186,36864\n"
"s_waitcnt vmcnt(14)\nM 48,180,92\nG 4\nM 52,160,76\nR 176,187,24576\nM 52,164,92\n"
"buffer_load_dwordx4 a[152:155], v252, s[20:23], s66 offen offset:1024 sc1\nM 36,160,72\nR 178,187,28672\nM 36,164,88\nM 20,160,68\nR 180,187,32768\n"
"M 20,164,84\nbuffer_load_dwordx4 a[156:159], v252, s[20:23], s67 offen offset:1024 sc1\nM 4,160,64\nR 182,187,36864\nM 4,164,80\nG 4\n"
"M 8,168,64\nR 160,184,40960\nM 8,172,80\nbuffer_load_dwordx4 a[160:163], v252, s[20:23], s64 offen offset:2048 sc1\nM 24,168,68\nR 162,184,45056\n"
"M 24,172,84\nM 40,168,72\nR 164,184,49152\nM 40,172,88\nbuffer_load_dwordx4 a[164:167], v252, s[20:23], s65 offen offset:2048 sc1\nM 56,168,76\n"
"R 166,184,53248\nM 56,172,92\nG 4\nM 60,176,76\nR 168,185,40960\nM 60,180,92\n"
"buffer_load_dwordx4 a[168:171], v252, s[20:23], s66 offen offset:2048 sc1\nM 44,176,72\nR 170,185,45056\nM 44,180,88\nM 28,176,68\nR 172,185,49152\n"
"M 28,180,84\nbuffer_load_dwordx4 a[172:175], v252, s[20:23], s67 offen offset:2048 sc1\nM 12,176,64\nR 174,185,53248\nM 12,180,80\nG 4\n"
"s_waitcnt vmcnt(19)\nM 0,160,96\nR 176,186,40960\ns_waitcnt vmcnt(15)\nM 0,164,112\nbuffer_load_dwordx4 a[176:179], v252, s[20:23], s64 offen offset:3072 sc1\n"
"M 16,160,100\nR 178,186,45056\ns_waitcnt vmcnt(15)\nM 16,164,116\nM 32,160,104\nR 180,186,49152\n"
"s_lshr_b32 s60, s45, 3\ns_lshl2_add_u32 s60, s60, s40\ns_lshl_b32 s60, s60, 8\nv_lshl_add_u32 v224, v252, 3, s60\nv_add_u32 v224, Lqw1+24576-LqPC, v224\nv_min_u32 v224, LqE-LqPC-4, v224\n"
"s_mov_b64 exec, 3\nglobal_load_dword v224, v224, s[98:99]\ns_mov_b64 exec, -1\ns_waitcnt vmcnt(15)\nM 32,164,120\nbuffer_load_dwordx4 a[180:183], v252, s[20:23], s65 offen offset:3072 sc1\n"
"M 48,160,108\nR 182,186,53248\ns_waitcnt vmcnt(15)\nM 48,164,124\nG 4\nM 52,168,108\n"
"R 160,187,40960\nM 52,172,124\nbuffer_load_dwordx4 a[184:187], v252, s[20:23], s66 offen offset:3072 sc1\nM 36,168,104\nR 162,187,45056\nM 36,172,120\n"
"M 20,168,100\nR 164,187,49152\nM 20,172,116\nbuffer_load_dwordx4 a[188:191], v252, s[20:23], s67 offen offset:3072 sc1\nM 4,168,96\nR 166,187,53248\n"
"M 4,172,112\nG 4\nM 8,176,96\nR 168,184,57344\nM 8,180,112\ns_add_u32 s64, s70, 6303744\n"
"buffer_load_dwordx4 a[192:195], v252, s[20:23], s64 offen offset:0 sc1\nM 24,176,100\nR 170,184,61440\ns_add_u32 s63, s53, 983040\nM 24,180,116\ns_add_u32 m0, s50, 114688\n"
"M 40,176,104\nR 172,188,0\nM 40,180,120\ns_add_u32 s65, s70, 6336512\nbuffer_load_dwordx4 a[196:199], v252, s[20:23], s65 offen offset:0 sc1\nM 56,176,108\n"
"R 174,188,4096\nM 56,180,124\nbuffer_load_dwordx4 v220, s[8:11], s63 offen lds\nG 4\nM 60,160,108\nR 176,185,57344\n"
"M 60,164,124\ns_add_u32 s66, s71, 6303744\nbuffer_load_dwordx4 a[200:203], v252, s[20:23], s66 offen offset:0 sc1\nM 44,160,104\nR 178,185,61440\nM 44,164,120\n"
"buffer_load_dwordx4 v221, s[8:11], s63 offen offset:1024 lds\nM 28,160,100\nR 180,189,0\nM 28,164,116\ns_add_u32 s67, s71, 6336512\nbuffer_load_dwordx4 a[204:207], v252, s[20:23], s67 offen offset:0 sc1\n"
"M 12,160,96\nR 182,189,4096\nM 12,164,112\ns_barrier\nG 4\ns_waitcnt vmcnt(22)\n"
"M 0,168,128\nR 160,186,57344\ns_waitcnt vmcnt(18)\nM 0,172,144\nbuffer_load_dwordx4 a[208:211], v252, s[20:23], s64 offen offset:1024 sc1\nM 16,168,132\n"
"R 162,186,61440\ns_waitcnt vmcnt(18)\nM 16,172,148\nLpg23:\ns_cbranch_execz LretO1\nM 32,168,136\n"
"R 164,190,0\ns_waitcnt vmcnt(17)\nM 32,172,152\nbuffer_load_dwordx4 a[212:215], v252, s[20:23], s65 offen offset:1024 sc1\nM 48,168,140\nR 166,190,4096\n"
"s_waitcnt vmcnt(17)\nM 48,172,156\nG 4\nM 52,176,140\nR 168,187,57344\nM 52,180,156\n"
"buffer_load_dwordx4 a[216:219], v252, s[20:23], s66 offen offset:1024 sc1\nM 36,176,136\nR 170,187,61440\nM 36,180,152\nM 20,176,132\nR 172,191,0\n"
"M 20,180,148\nbuffer_load_dwordx4 a[220:223], v252, s[20:23], s67 offen offset:1024 sc1\nM 4,176,128\nR 174,191,4096\nM 4,180,144\nG 4\n"
"M 8,160,128\nR 176,188,8192\nM 8,164,144\nbuffer_load_dwordx4 a[224:227], v252, s[20:23], s64 offen offset:2048 sc1\nM 24,160,132\nR 178,188,12288\n"
"M 24,164,148\nM 40,160,136\nR 180,188,16384\nM 40,164,152\nbuffer_load_dwordx4 a[228:231], v252, s[20:23], s65 offen offset:2048 sc1\nM 56,160,140\n"
"R 182,188,20480\nM 56,164,156\nG 4\nM 60,168,140\nR 160,189,8192\nM 60,172,156\n"
"buffer_load_dwordx4 a[232:235], v252, s[20:23], s66 offen offset:2048 sc1\nM 44,168,136\nR 162,189,12288\nM 44,172,152\nM 28,168,132\nR 164,189,16384\n"
"M 28,172,148\nbuffer_load_dwordx4 a[236:239], v252, s[20:23], s67 offen offset:2048 sc1\nM 12,168,128\nR 166,189,20480\nM 12,172,144\nG 4\n"
"s_waitcnt vmcnt(22)\nM 0,176,160\nR 168,190,8192\ns_waitcnt vmcnt(18)\nM 0,180,176\nbuffer_load_dwordx4 a[240:243], v252, s[20:23], s64 offen offset:3072 sc1\n"
"M 16,176,164\nR 170,190,12288\ns_waitcnt vmcnt(17)\nM 16,180,180\nM 32,176,168\nR 172,190,16384\n"
"s_waitcnt vmcnt(16)\nM 32,180,184\nbuffer_load_dwordx4 a[244:247], v252, s[20:23], s65 offen offset:3072 sc1\nM 48,176,172\nR 174,190,20480\ns_waitcnt vmcnt(16)\n"
"M 48,180,188\nG 4\nM 52,160,172\nR 176,191,8192\nM 52,164,188\nbuffer_load_dwordx4 a[248:251], v252, s[20:23], s66 offen offset:3072 sc1\n"
"M 36,160,168\nR 178,191,12288\ns_waitcnt vmcnt(12)\ns_barrier\nM 36,164,184\nM 20,160,164\n"
"R 180,191,16384\nM 20,164,180\nbuffer_load_dwordx4 a[252:255], v252, s[20:23], s67 offen offset:3072 sc1\nM 4,160,160\nR 182,191,20480\nM 4,164,176\n"
"G 4\nM 8,168,160\nR 160,188,24576\nM 8,172,176\nM 24,168,164\nR 162,188,28672\n"
"M 24,172,180\nM 40,168,168\nR 164,188,32768\nM 40,172,184\nM 56,168,172\nR 166,188,36864\n"
"M 56,172,188\nG 4\nM 60,176,172\nR 168,189,24576\nM 60,180,188\ns_movk_i32 s63, 7168\n"
"v_mad_u32_u24 v220, v228, s63, v229\nv_add_u32 v221, 3668992, v220\ns_add_u32 m0, s50, 0\ns_add_u32 s63, s54, 0\nbuffer_load_dwordx4 v220, s[12:15], s63 offen lds\nM 44,176,168\n"
"R 170,189,28672\nM 44,180,184\nM 28,176,164\nR 172,189,32768\nM 28,180,180\nbuffer_load_dwordx4 v221, s[12:15], s63 offen offset:1024 lds\n"
"M 12,176,160\nR 174,189,36864\ns_add_u32 m0, s50, 8192\nM 12,180,176\nG 4\nM 32,160,200\n"
"R 176,190,24576\ns_add_u32 s63, s54, 229376\ns_waitcnt vmcnt(11)\nM 32,164,216\nR 178,190,28672\nM 48,160,204\n"
"R 180,190,32768\ns_waitcnt vmcnt(10)\nM 48,164,220\nR 182,190,36864\nG 4\nM 52,168,204\n"
"R 160,191,24576\nbuffer_load_dwordx4 v220, s[12:15], s63 offen lds\nM 52,172,220\nR 162,191,28672\nM 36,168,200\nR 164,191,32768\n"
"M 36,172,216\nR 166,191,36864\nG 4\nM 40,176,200\nR 168,188,40960\nbuffer_load_dwordx4 v221, s[12:15], s63 offen offset:1024 lds\n"
"M 40,180,216\nR 170,188,45056\ns_add_u32 m0, s50, 16384\nM 56,176,204\nR 172,188,49152\ns_add_u32 s63, s54, 128\n"
"M 56,180,220\nR 174,188,53248\nG 4\nM 60,160,204\nR 176,189,40960\nbuffer_load_dwordx4 v220, s[12:15], s63 offen lds\n"
"M 60,164,220\nR 178,189,45056\nM 44,160,200\nR 180,189,49152\nM 44,164,216\nR 182,189,53248\n"
"G 4\ns_waitcnt vmcnt(10)\nM 32,168,232\nR 160,190,40960\nbuffer_load_dwordx4 v221, s[12:15], s63 offen offset:1024 lds\ns_waitcnt vmcnt(7)\n"
"M 32,172,248\nR 162,190,45056\ns_add_u32 m0, s50, 24576\nM 48,168,236\nR 164,190,49152\ns_add_u32 s63, s54, 229504\n"
"s_waitcnt vmcnt(6)\nM 48,172,252\nR 166,190,53248\nG 4\nM 52,176,236\nR 168,191,40960\n"
"buffer_load_dwordx4 v220, s[12:15], s63 offen lds\nM 52,180,252\nR 170,191,45056\nM 36,176,232\nR 172,191,49152\nM 36,180,248\n"
"R 174,191,53248\nG 4\nM 40,160,232\nR 176,188,24576\nbuffer_load_dwordx4 v221, s[12:15], s63 offen offset:1024 lds\nM 40,164,248\n"
"R 178,188,28672\nM 56,160,236\nR 180,188,32768\nM 56,164,252\nR 182,188,36864\nG 4\n"
"M 60,168,236\nR 160,189,24576\nM 60,172,252\nR 162,189,28672\nM 44,168,232\nR 164,189,32768\n"
"M 44,172,248\nR 166,189,36864\nG 4\nM 0,176,192\nR 168,190,24576\nM 0,180,208\n"
"R 170,190,28672\ns_mov_b32 m0, s51\nM 16,176,196\nR 172,190,32768\nv_cvt_pk_bf16_f32 v32, v32, v33\nv_cvt_pk_bf16_f32 v33, v34, v35\n"
"M 16,180,212\nR 174,190,36864\nG 4\nv_cvt_pk_bf16_f32 v34, v36, v37\nM 20,160,196\nR 176,191,24576\n"
"v_cvt_pk_bf16_f32 v35, v38, v39\nv_cvt_pk_bf16_f32 v36, v40, v41\nM 20,164,212\nR 178,191,28672\nv_cvt_pk_bf16_f32 v37, v42, v43\nv_cvt_pk_bf16_f32 v38, v44, v45\n"
"M 4,160,192\nR 180,191,32768\nv_cvt_pk_bf16_f32 v39, v46, v47\nv_cvt_pk_bf16_f32 v40, v48, v49\nM 4,164,208\nR 182,191,36864\n"
"G 4\nv_cvt_pk_bf16_f32 v41, v50, v51\nM 8,168,192\nR 160,188,40960\nv_cvt_pk_bf16_f32 v42, v52, v53\nv_cvt_pk_bf16_f32 v43, v54, v55\n"
"M 8,172,208\nR 162,188,45056\nv_cvt_pk_bf16_f32 v44, v56, v57\nv_cvt_pk_bf16_f32 v45, v58, v59\nM 24,168,196\nR 164,188,49152\n"
"v_cvt_pk_bf16_f32 v46, v60, v61\nv_cvt_pk_bf16_f32 v47, v62, v63\nM 24,172,212\nR 166,188,53248\nds_write_addtid_b32 v32 offset:0\nG 5\n"
"M 28,176,196\nR 168,189,40960\nds_write_addtid_b32 v33 offset:256\nM 28,180,212\nR 170,189,45056\nds_write_addtid_b32 v34 offset:512\n"
"M 12,176,192\nR 172,189,49152\nds_write_addtid_b32 v35 offset:768\nds_write_addtid_b32 v36 offset:1024\nds_write_addtid_b32 v37 offset:1280\nds_write_addtid_b32 v38 offset:1536\n"
"ds_write_addtid_b32 v39 offset:1792\nds_write_addtid_b32 v40 offset:2048\nds_write_addtid_b32 v41 offset:2304\nds_write_addtid_b32 v42 offset:2560\nds_write_addtid_b32 v43 offset:2816\nds_write_addtid_b32 v44 offset:3072\n"
"ds_write_addtid_b32 v45 offset:3328\nds_write_addtid_b32 v46 offset:3584\nds_write_addtid_b32 v47 offset:3840\nM 12,180,208\nR 174,189,53248\nG 15\n"
"M 0,160,224\nR 176,190,40960\ns_add_u32 m0, s50, 32768\nM 0,164,240\nR 178,190,45056\nG 3\n"
"s_barrier\ns_add_u32 s63, s54, 458752\nM 16,160,228\nR 180,190,49152\nbuffer_load_dwordx4 v220, s[12:15], s63 offen lds\nds_read2st64_b32 v[32:33], v225 offset0:0 offset1:1\n"
"M 16,164,244\nR 182,190,53248\nds_read2st64_b32 v[34:35], v225 offset0:2 offset1:3\nG 6\nM 20,168,228\nR 160,191,40960\n"
"ds_read2st64_b32 v[36:37], v225 offset0:4 offset1:5\nM 20,172,244\nR 162,191,45056\nds_read2st64_b32 v[38:39], v225 offset0:6 offset1:7\nM 4,168,224\nR 164,191,49152\n"
"buffer_load_dwordx4 v221, s[12:15], s63 offen offset:1024 lds\nds_read2st64_b32 v[40:41], v225 offset0:8 offset1:9\nM 4,172,240\nR 166,191,53248\ns_add_u32 m0, s50, 40960\ns_add_u32 s63, s54, 688128\n"
"ds_read2st64_b32 v[42:43], v225 offset0:10 offset1:11\nG 9\nM 8,176,224\nbuffer_load_dwordx4 v220, s[12:15], s63 offen lds\nds_read2st64_b32 v[44:45], v225 offset0:12 offset1:13\nM 8,180,240\n"
"ds_read2st64_b32 v[46:47], v225 offset0:14 offset1:15\nM 24,176,228\nM 24,180,244\nG 3\nM 28,160,228\nbuffer_load_dwordx4 v221, s[12:15], s63 offen offset:1024 lds\n"
"M 28,164,244\nM 12,160,224\nM 12,164,240\nG 0\ns_barrier\nv_mfma_f32_16x16x32_bf16 v[0:3], v[208:211], v[32:35], v[0:3]\n"
"v_mfma_f32_16x16x32_bf16 v[4:7], v[210:213], v[32:35], v[4:7]\nv_mfma_f32_16x16x32_bf16 v[8:11], v[208:211], v[36:39], v[8:11]\nv_mfma_f32_16x16x32_bf16 v[12:15], v[210:213], v[36:39], v[12:15]\nv_mfma_f32_16x16x32_bf16 v[16:19], v[208:211], v[40:43], v[16:19]\nv_mfma_f32_16x16x32_bf16 v[20:23], v[210:213], v[40:43], v[20:23]\nv_mfma_f32_16x16x32_bf16 v[24:27], v[208:211], v[44:47], v[24:27]\n"
"v_mfma_f32_16x16x32_bf16 v[28:31], v[210:213], v[44:47], v[28:31]\ns_lshl_b32 s84, s75, 11\ns_add_u32 s84, s84, s49\ns_add_u32 s84, s84, 14680064\ns_add_u32 s85, s84, 32768\nv_mov_b32 v238, s46\n"
"v_mov_b32 v239, s47\nv_cvt_pk_bf16_f32 v192, v0, v1\nv_cvt_pk_bf16_f32 v193, v2, v3\nv_cvt_pk_bf16_f32 v194, v4, v5\nv_cvt_pk_bf16_f32 v195, v6, v7\nbuffer_store_dwordx4 v[192:195], v252, s[20:23], s84 offen offset:0 sc1\n"
"v_cvt_pk_bf16_f32 v196, v8, v9\nv_cvt_pk_bf16_f32 v197, v10, v11\nv_cvt_pk_bf16_f32 v198, v12, v13\nv_cvt_pk_bf16_f32 v199, v14, v15\nbuffer_store_dwordx4 v[196:199], v252, s[20:23], s84 offen offset:1024 sc1\nv_cvt_pk_bf16_f32 v200, v16, v17\n"
"v_cvt_pk_bf16_f32 v201, v18, v19\nv_cvt_pk_bf16_f32 v202, v20, v21\nv_cvt_pk_bf16_f32 v203, v22, v23\nbuffer_store_dwordx4 v[200:203], v252, s[20:23], s85 offen offset:0 sc1\nv_cvt_pk_bf16_f32 v204, v24, v25\nv_cvt_pk_bf16_f32 v205, v26, v27\n"
"v_cvt_pk_bf16_f32 v206, v28, v29\nv_cvt_pk_bf16_f32 v207, v30, v31\nbuffer_store_dwordx4 v[204:207], v252, s[20:23], s85 offen offset:1024 sc1\ns_mov_b64 exec, 0\ns_cmp_eq_u32 s40, 1\ns_cbranch_scc1 Lpg25\n"
"s_cmp_eq_u32 s40, 2\ns_cbranch_scc1 Lpg26\ns_cmp_eq_u32 s40, 3\ns_cbranch_scc1 Lpg27\ns_branch Lpg24\nLretO1n:\n"
"s_mov_b64 exec, -1\ns_add_u32 s61, s79, 88637440\ns_lshl_b32 s62, s75, 3\ns_add_u32 s61, s61, s62\ns_waitcnt vmcnt(0)\ns_mov_b64 exec, 1\n"
"buffer_atomic_swap_x2 v[238:239], off, s[20:23], s61 sc1\ns_mov_b64 exec, -1\nLqw1:\nv_bfe_u32 v250, v252, 7, 1\nv_mul_u32_u24 v250, 2176, v250\nv_bfe_u32 v251, v252, 4, 3\n"
"v_lshl_add_u32 v250, v251, 3, v250\ns_lshl_b32 s60, s90, 6\nv_add_u32 v250, s60, v250\ns_sleep 3\nv_mov_b32 v248, 0\nv_mov_b32 v249, 0\n"
"s_add_u32 s61, s80, 88637440\ns_mov_b64 exec, 0xffff\nbuffer_atomic_or_x2 v[248:249], v250, s[20:23], s61 offen sc0 sc1\ns_mov_b64 exec, -1\ns_add_u32 m0, s50, 49152\ns_add_u32 s63, s54, 458880\n"
"buffer_load_dwordx4 v220, s[12:15], s63 offen lds\nbuffer_load_dwordx4 v221, s[12:15], s63 offen offset:1024 lds\ns_add_u32 m0, s50, 57344\ns_add_u32 s63, s54, 688256\nbuffer_load_dwordx4 v220, s[12:15], s63 offen lds\nbuffer_load_dwordx4 v221, s[12:15], s63 offen offset:1024 lds\n"
"s_add_u32 m0, s50, 65536\ns_add_u32 s63, s54, 917504\nbuffer_load_dwordx4 v220, s[12:15], s63 offen lds\nbuffer_load_dwordx4 v221, s[12:15], s63 offen offset:1024 lds\ns_add_u32 m0, s50, 73728\ns_add_u32 s63, s54, 1146880\n"
"buffer_load_dwordx4 v220, s[12:15], s63 offen lds\nbuffer_load_dwordx4 v221, s[12:15], s63 offen offset:1024 lds\ns_add_u32 m0, s50, 81920\ns_add_u32 s63, s54, 917632\nbuffer_load_dwordx4 v220, s[12:15], s63 offen lds\nbuffer_load_dwordx4 v221, s[12:15], s63 offen offset:1024 lds\n"
"s_waitcnt vmcnt(10)\nv_cmp_eq_u64 s[86:87], s[46:47], v[248:249]\ns_nop 1\ns_and_b32 s88, s86, 0xffff\ns_cmp_eq_u32 s88, 0xffff\ns_cbranch_scc1 Lq768\n"
"v_mov_b32 v246, 0\nv_mov_b32 v247, 0\ns_add_u32 s61, s80, 88637440\ns_mov_b64 exec, 0xffff\nbuffer_atomic_or_x2 v[246:247], v250, s[20:23], s61 offen sc0 sc1\ns_mov_b64 exec, -1\n"
"Lq768:\ns_add_u32 m0, s50, 90112\ns_add_u32 s63, s54, 1147008\nbuffer_load_dwordx4 v220, s[12:15], s63 offen lds\nbuffer_load_dwordx4 v221, s[12:15], s63 offen offset:1024 lds\ns_mov_b64 exec, 0\n"
"s_cmp_eq_u32 s40, 1\ns_cbranch_scc1 Lpg33\ns_cmp_eq_u32 s40, 2\ns_cbranch_scc1 LretW1z\ns_cmp_eq_u32 s40, 3\ns_cbranch_scc1 LretW1z\n"
"s_branch Lpg32\nLretW1z:\ns_mov_b64 exec, -1\nv_mov_b32 v230, 0\nv_mov_b32 v231, 0\nv_mov_b32 v232, 0\n"
"v_mov_b32 v233, 0\nv_dot2c_f32_bf16 v230, s76, v192\nv_dot2c_f32_bf16 v231, v192, v192\nv_dot2c_f32_bf16 v232, s76, v200\nv_dot2c_f32_bf16 v233, v200, v200\nv_dot2c_f32_bf16 v230, s76, v193\n"
"v_dot2c_f32_bf16 v231, v193, v193\nv_dot2c_f32_bf16 v232, s76, v201\nv_dot2c_f32_bf16 v233, v201, v201\nv_dot2c_f32_bf16 v230, s76, v194\nv_dot2c_f32_bf16 v231, v194, v194\nv_dot2c_f32_bf16 v232, s76, v202\n"
"v_dot2c_f32_bf16 v233, v202, v202\nv_dot2c_f32_bf16 v230, s76, v195\nv_dot2c_f32_bf16 v231, v195, v195\nv_dot2c_f32_bf16 v232, s76, v203\nv_dot2c_f32_bf16 v233, v203, v203\nv_dot2c_f32_bf16 v230, s76, v196\n"
"v_dot2c_f32_bf16 v231, v196, v196\nv_dot2c_f32_bf16 v232, s76, v204\nv_dot2c_f32_bf16 v233, v204, v204\nv_dot2c_f32_bf16 v230, s76, v197\nv_dot2c_f32_bf16 v231, v197, v197\nv_dot2c_f32_bf16 v232, s76, v205\n"
"v_dot2c_f32_bf16 v233, v205, v205\nv_dot2c_f32_bf16 v230, s76, v198\nv_dot2c_f32_bf16 v231, v198, v198\nv_dot2c_f32_bf16 v232, s76, v206\nv_dot2c_f32_bf16 v233, v206, v206\nv_dot2c_f32_bf16 v230, s76, v199\n"
"v_dot2c_f32_bf16 v231, v199, v199\nv_dot2c_f32_bf16 v232, s76, v207\nv_dot2c_f32_bf16 v233, v207, v207\nv_lshrrev_b32 v234, 5, v252\nv_and_b32 v234, 24, v234\nv_bfe_u32 v235, s46, v234, 8\n"
"v_lshrrev_b32 v234, 1, v234\nv_and_b32 v236, 0xf0, v252\nv_add_u32 v234, v234, v236\nv_permlane32_swap_b32 v230, v231\nv_permlane32_swap_b32 v232, v233\nv_add_f32 v230, v230, v231\n"
"v_add_f32 v232, v232, v233\ns_nop 1\nv_permlane16_swap_b32 v230, v232\nv_add_f32 v230, v230, v232\ns_movk_i32 s62, 0xff\nv_bfi_b32 v230, s62, v235, v230\n"
"s_lshr_b32 s61, s49, 4\ns_lshl1_add_u32 s61, s53, s61\ns_add_u32 s61, s61, 83361792\nbuffer_store_dword v230, v234, s[20:23], s61 offen sc1\ns_cmp_eq_u32 s88, 0xffff\ns_cbranch_scc1 Lq769\n"
"s_waitcnt vmcnt(3)\nv_mov_b32 v248, v246\nv_mov_b32 v249, v247\nv_cmp_eq_u64 s[86:87], s[46:47], v[248:249]\ns_nop 1\ns_and_b32 s62, s86, 0xffff\n"
"s_cmp_eq_u32 s62, 0xffff\ns_cbranch_scc1 Lq760\nLq759:\nv_mov_b32 v248, 0\nv_mov_b32 v249, 0\ns_add_u32 s61, s80, 88637440\n"
"s_mov_b64 exec, 0xffff\nbuffer_atomic_or_x2 v[248:249], v250, s[20:23], s61 offen sc0 sc1\ns_mov_b64 exec, -1\ns_waitcnt vmcnt(0)\nv_cmp_eq_u64 s[86:87], s[46:47], v[248:249]\ns_nop 1\n"
"s_and_b32 s62, s86, 0xffff\ns_cmp_eq_u32 s62, 0xffff\ns_cbranch_scc1 Lq760\ns_sub_u32 s48, s48, 1\ns_cmp_gt_i32 s48, 0\ns_cbranch_scc1 Lq759\n"
"Lq760:\nLq769:\ns_add_u32 s64, s70, 14680064\nbuffer_load_dwordx4 a[0:3], v252, s[20:23], s64 offen offset:0 sc1\ns_add_u32 s65, s70, 14712832\nbuffer_load_dwordx4 a[4:7], v252, s[20:23], s65 offen offset:0 sc1\n"
"s_add_u32 s66, s71, 14680064\nbuffer_load_dwordx4 a[8:11], v252, s[20:23], s66 offen offset:0 sc1\ns_add_u32 s67, s71, 14712832\nbuffer_load_dwordx4 a[12:15], v252, s[20:23], s67 offen offset:0 sc1\nbuffer_load_dwordx4 a[16:19], v252, s[20:23], s64 offen offset:1024 sc1\nbuffer_load_dwordx4 a[20:23], v252, s[20:23], s65 offen offset:1024 sc1\n"
"buffer_load_dwordx4 a[24:27], v252, s[20:23], s66 offen offset:1024 sc1\nbuffer_load_dwordx4 a[28:31], v252, s[20:23], s67 offen offset:1024 sc1\ns_barrier\nR 160,184,0\nR 162,184,4096\nR 164,184,8192\n"
"R 166,184,12288\nR 168,185,0\nR 170,185,4096\nR 172,185,8192\nR 174,185,12288\nR 176,186,0\n"
"R 178,186,4096\nR 180,186,8192\nR 182,186,12288\nbuffer_load_dwordx4 a[32:35], v252, s[20:23], s64 offen offset:2048 sc1\nbuffer_load_dwordx4 a[36:39], v252, s[20:23], s65 offen offset:2048 sc1\nbuffer_load_dwordx4 a[40:43], v252, s[20:23], s66 offen offset:2048 sc1\n"
"buffer_load_dwordx4 a[44:47], v252, s[20:23], s67 offen offset:2048 sc1\nbuffer_load_dwordx4 a[48:51], v252, s[20:23], s64 offen offset:3072 sc1\nbuffer_load_dwordx4 a[52:55], v252, s[20:23], s65 offen offset:3072 sc1\nbuffer_load_dwordx4 a[56:59], v252, s[20:23], s66 offen offset:3072 sc1\nbuffer_load_dwordx4 a[60:63], v252, s[20:23], s67 offen offset:3072 sc1\ns_waitcnt vmcnt(15) lgkmcnt(10)\n"
"v_mfma_f32_16x16x32_bf16 v[32:35], v[160:163], a[0:3], 0\ns_add_u32 s64, s70, 14684160\nbuffer_load_dwordx4 a[64:67], v252, s[20:23], s64 offen offset:0 sc1\ns_waitcnt vmcnt(12) lgkmcnt(8)\nM 32,164,16\nv_mfma_f32_16x16x32_bf16 v[48:51], v[160:163], a[4:7], 0\n"
"s_waitcnt vmcnt(11)\nM 48,164,20\nv_mfma_f32_16x16x32_bf16 v[64:67], v[160:163], a[8:11], 0\ns_waitcnt vmcnt(10)\nM 64,164,24\nv_mfma_f32_16x16x32_bf16 v[80:83], v[160:163], a[12:15], 0\n"
"s_add_u32 s65, s70, 14716928\nbuffer_load_dwordx4 a[68:71], v252, s[20:23], s65 offen offset:0 sc1\ns_waitcnt vmcnt(10)\nM 80,164,28\nG 4\nv_mfma_f32_16x16x32_bf16 v[84:87], v[168:171], a[12:15], 0\n"
"R 160,187,0\nM 84,172,28\nv_mfma_f32_16x16x32_bf16 v[68:71], v[168:171], a[8:11], 0\nR 162,187,4096\nM 68,172,24\nv_mfma_f32_16x16x32_bf16 v[52:55], v[168:171], a[4:7], 0\n"
"R 164,187,8192\nM 52,172,20\ns_add_u32 s66, s71, 14684160\nbuffer_load_dwordx4 a[72:75], v252, s[20:23], s66 offen offset:0 sc1\nv_mfma_f32_16x16x32_bf16 v[36:39], v[168:171], a[0:3], 0\nR 166,187,12288\n"
"M 36,172,16\nG 4\nv_mfma_f32_16x16x32_bf16 v[40:43], v[176:179], a[0:3], 0\nR 168,184,16384\nM 40,180,16\nv_mfma_f32_16x16x32_bf16 v[56:59], v[176:179], a[4:7], 0\n"
"R 170,184,20480\nM 56,180,20\nLpg24:\ns_cbranch_execz Lpg28\ns_add_u32 s67, s71, 14716928\nbuffer_load_dwordx4 a[76:79], v252, s[20:23], s67 offen offset:0 sc1\n"
"v_mfma_f32_16x16x32_bf16 v[72:75], v[176:179], a[8:11], 0\nR 172,184,24576\nM 72,180,24\nv_mfma_f32_16x16x32_bf16 v[88:91], v[176:179], a[12:15], 0\nR 174,184,28672\nM 88,180,28\n"
"G 4\nv_mfma_f32_16x16x32_bf16 v[92:95], v[160:163], a[12:15], 0\nR 176,185,16384\ns_add_u32 s63, s54, 1376256\nM 92,164,28\nbuffer_load_dwordx4 a[80:83], v252, s[20:23], s64 offen offset:1024 sc1\n"
"v_mfma_f32_16x16x32_bf16 v[76:79], v[160:163], a[8:11], 0\nR 178,185,20480\nM 76,164,24\ns_add_u32 m0, s50, 98304\nv_mfma_f32_16x16x32_bf16 v[60:63], v[160:163], a[4:7], 0\nR 180,185,24576\n"
"M 60,164,20\nbuffer_load_dwordx4 v220, s[12:15], s63 offen lds\nv_mfma_f32_16x16x32_bf16 v[44:47], v[160:163], a[0:3], 0\nR 182,185,28672\nM 44,164,16\nG 4\n"
"v_mfma_f32_16x16x32_bf16 v[96:99], v[168:171], a[0:3], 0\nR 160,186,16384\nM 96,172,16\nbuffer_load_dwordx4 a[84:87], v252, s[20:23], s65 offen offset:1024 sc1\nv_mfma_f32_16x16x32_bf16 v[112:115], v[168:171], a[4:7], 0\nR 162,186,20480\n"
"M 112,172,20\nv_mfma_f32_16x16x32_bf16 v[128:131], v[168:171], a[8:11], 0\nR 164,186,24576\nM 128,172,24\nbuffer_load_dwordx4 a[88:91], v252, s[20:23], s66 offen offset:1024 sc1\nv_mfma_f32_16x16x32_bf16 v[144:147], v[168:171], a[12:15], 0\n"
"R 166,186,28672\nM 144,172,28\ns_barrier\nG 4\nv_mfma_f32_16x16x32_bf16 v[148:151], v[176:179], a[12:15], 0\nR 168,187,16384\n"
"M 148,180,28\nv_mfma_f32_16x16x32_bf16 v[132:135], v[176:179], a[8:11], 0\nR 170,187,20480\nM 132,180,24\nbuffer_load_dwordx4 a[92:95], v252, s[20:23], s67 offen offset:1024 sc1\nv_mfma_f32_16x16x32_bf16 v[116:119], v[176:179], a[4:7], 0\n"
"R 172,187,24576\nM 116,180,20\nv_mfma_f32_16x16x32_bf16 v[100:103], v[176:179], a[0:3], 0\nR 174,187,28672\nM 100,180,16\nG 4\n"
"v_mfma_f32_16x16x32_bf16 v[104:107], v[160:163], a[0:3], 0\nR 176,184,32768\nM 104,164,16\nbuffer_load_dwordx4 a[96:99], v252, s[20:23], s64 offen offset:2048 sc1\nv_mfma_f32_16x16x32_bf16 v[120:123], v[160:163], a[4:7], 0\nR 178,184,36864\n"
"M 120,164,20\nv_mfma_f32_16x16x32_bf16 v[136:139], v[160:163], a[8:11], 0\nR 180,184,40960\nM 136,164,24\nbuffer_load_dwordx4 v221, s[12:15], s63 offen offset:1024 lds\nv_mfma_f32_16x16x32_bf16 v[152:155], v[160:163], a[12:15], 0\n"
"R 182,184,45056\nM 152,164,28\nG 4\nv_mfma_f32_16x16x32_bf16 v[156:159], v[168:171], a[12:15], 0\nR 160,185,32768\nM 156,172,28\n"
"buffer_load_dwordx4 a[100:103], v252, s[20:23], s65 offen offset:2048 sc1\nv_mfma_f32_16x16x32_bf16 v[140:143], v[168:171], a[8:11], 0\nR 162,185,36864\nM 140,172,24\ns_add_u32 m0, s50, 106496\nv_mfma_f32_16x16x32_bf16 v[124:127], v[168:171], a[4:7], 0\n"
"R 164,185,40960\ns_add_u32 s63, s54, 1605632\nM 124,172,20\nbuffer_load_dwordx4 a[104:107], v252, s[20:23], s66 offen offset:2048 sc1\nv_mfma_f32_16x16x32_bf16 v[108:111], v[168:171], a[0:3], 0\nR 166,185,45056\n"
"M 108,172,16\nG 4\ns_waitcnt vmcnt(20)\nM 32,176,32\nR 168,186,32768\ns_waitcnt vmcnt(16)\n"
"M 32,180,48\nbuffer_load_dwordx4 v220, s[12:15], s63 offen lds\nM 48,176,36\nR 170,186,36864\ns_waitcnt vmcnt(16)\nM 48,180,52\n"
"buffer_load_dwordx4 a[108:111], v252, s[20:23], s67 offen offset:2048 sc1\nM 64,176,40\nR 172,186,40960\ns_waitcnt vmcnt(16)\nM 64,180,56\nM 80,176,44\n"
"R 174,186,45056\ns_waitcnt vmcnt(15)\nM 80,180,60\ns_barrier\nG 4\nM 84,160,44\n"
"R 176,187,32768\nM 84,164,60\nbuffer_load_dwordx4 a[112:115], v252, s[20:23], s64 offen offset:3072 sc1\nM 68,160,40\nR 178,187,36864\nM 68,164,56\n"
"M 52,160,36\nR 180,187,40960\nM 52,164,52\nM 36,160,32\nR 182,187,45056\nM 36,164,48\n"
"G 4\nM 40,168,32\nR 160,184,49152\nM 40,172,48\nbuffer_load_dwordx4 a[116:119], v252, s[20:23], s65 offen offset:3072 sc1\nM 56,168,36\n"
"R 162,184,53248\nM 56,172,52\nM 72,168,40\nR 164,184,57344\nM 72,172,56\nbuffer_load_dwordx4 a[120:123], v252, s[20:23], s66 offen offset:3072 sc1\n"
"M 88,168,44\nR 166,184,61440\nM 88,172,60\nG 4\nM 92,176,44\nR 168,185,49152\n"
"M 92,180,60\nbuffer_load_dwordx4 v221, s[12:15], s63 offen offset:1024 lds\nM 76,176,40\nR 170,185,53248\nM 76,180,56\nbuffer_load_dwordx4 a[124:127], v252, s[20:23], s67 offen offset:3072 sc1\n"
"M 60,176,36\nR 172,185,57344\nM 60,180,52\nM 44,176,32\nR 174,185,61440\nM 44,180,48\n"
"G 4\nM 96,160,32\nR 176,186,49152\nM 96,164,48\ns_add_u32 s64, s70, 14688256\nbuffer_load_dwordx4 a[128:131], v252, s[20:23], s64 offen offset:0 sc1\n"
"M 112,160,36\nR 178,186,53248\nM 112,164,52\ns_lshr_b32 s84, s49, 4\nM 128,160,40\nR 180,186,57344\n"
"M 128,164,56\ns_add_u32 s84, s84, 83361792\nM 144,160,44\nR 182,186,61440\nM 144,164,60\ns_barrier\n"
"G 4\nM 148,168,44\nR 160,187,49152\nM 148,172,60\ns_add_u32 s65, s70, 14721024\nbuffer_load_dwordx4 a[132:135], v252, s[20:23], s65 offen offset:0 sc1\n"
"M 132,168,40\nR 162,187,53248\nM 132,172,56\nbuffer_load_dwordx4 v[0:3], v252, s[20:23], s84 offen offset:0 sc1\nM 116,168,36\nR 164,187,57344\n"
"M 116,172,52\ns_add_u32 s66, s71, 14688256\nbuffer_load_dwordx4 a[136:139], v252, s[20:23], s66 offen offset:0 sc1\nM 100,168,32\nR 166,187,61440\nM 100,172,48\n"
"G 4\nM 104,176,32\nR 168,188,0\nM 104,180,48\nbuffer_load_dwordx4 v[4:7], v252, s[20:23], s84 offen offset:1024 sc1\nM 120,176,36\n"
"R 170,188,4096\nM 120,180,52\ns_add_u32 s67, s71, 14721024\nbuffer_load_dwordx4 a[140:143], v252, s[20:23], s67 offen offset:0 sc1\nM 136,176,40\nR 172,188,8192\n"
"M 136,180,56\nbuffer_load_dwordx4 v[8:11], v252, s[20:23], s84 offen offset:2048 sc1\nM 152,176,44\nR 174,188,12288\nM 152,180,60\nbuffer_load_dwordx4 v[12:15], v252, s[20:23], s84 offen offset:3072 sc1\n"
"G 4\nM 156,160,44\nR 176,189,0\nM 156,164,60\nbuffer_load_dwordx4 a[144:147], v252, s[20:23], s64 offen offset:1024 sc1\nM 140,160,40\n"
"R 178,189,4096\nM 140,164,56\nM 124,160,36\nR 180,189,8192\nM 124,164,52\nM 108,160,32\n"
"R 182,189,12288\nM 108,164,48\nG 4\ns_waitcnt vmcnt(28)\nM 32,168,64\nR 160,190,0\n"
"s_waitcnt vmcnt(24)\nM 32,172,80\nbuffer_load_dwordx4 a[148:151], v252, s[20:23], s65 offen offset:1024 sc1\nM 48,168,68\nR 162,190,4096\ns_waitcnt vmcnt(23)\n"
"M 48,172,84\ns_add_u32 m0, s50, 114688\nM 64,168,72\nR 164,190,8192\ns_add_u32 s63, s54, 1376384\ns_waitcnt vmcnt(22)\n"
"M 64,172,88\nbuffer_load_dwordx4 a[152:155], v252, s[20:23], s66 offen offset:1024 sc1\nM 80,168,76\nR 166,190,12288\ns_waitcnt vmcnt(22)\nM 80,172,92\n"
"s_barrier\nG 4\nM 84,176,76\nR 168,191,0\nM 84,180,92\nbuffer_load_dwordx4 v220, s[12:15], s63 offen lds\n"
"M 68,176,72\nR 170,191,4096\nM 68,180,88\nbuffer_load_dwordx4 a[156:159], v252, s[20:23], s67 offen offset:1024 sc1\nM 52,176,68\nR 172,191,8192\n"
"M 52,180,84\nbuffer_load_dwordx4 v221, s[12:15], s63 offen offset:1024 lds\nM 36,176,64\nR 174,191,12288\ns_add_u32 m0, s50, 0\nM 36,180,80\n"
"G 4\nM 40,160,64\nR 176,188,16384\ns_add_u32 s63, s54, 1605760\nM 40,164,80\nbuffer_load_dwordx4 a[160:163], v252, s[20:23], s64 offen offset:2048 sc1\n"
"M 56,160,68\nR 178,188,20480\nM 56,164,84\nbuffer_load_dwordx4 v220, s[12:15], s63 offen lds\nM 72,160,72\nR 180,188,24576\n"
"M 72,164,88\nbuffer_load_dwordx4 v221, s[12:15], s63 offen offset:1024 lds\nM 88,160,76\nR 182,188,28672\nM 88,164,92\nG 4\n"
"M 92,168,76\nR 160,189,16384\nM 92,172,92\nbuffer_load_dwordx4 a[164:167], v252, s[20:23], s65 offen offset:2048 sc1\nM 76,168,72\nR 162,189,20480\n"
"M 76,172,88\nM 60,168,68\nR 164,189,24576\nM 60,172,84\nbuffer_load_dwordx4 a[168:171], v252, s[20:23], s66 offen offset:2048 sc1\nM 44,168,64\n"
"R 166,189,28672\nM 44,172,80\nG 4\nM 96,176,64\nR 168,190,16384\ns_mov_b32 s85, 0x0c0c0400\n"
"M 96,180,80\ns_waitcnt vmcnt(16)\nv_perm_b32 v16, v1, v0, s85\nv_perm_b32 v20, v3, v2, s85\nM 112,176,68\nR 170,190,20480\n"
"v_lshl_or_b32 v16, v20, 16, v16\nM 112,180,84\nbuffer_load_dwordx4 a[172:175], v252, s[20:23], s67 offen offset:2048 sc1\nM 128,176,72\nR 172,190,24576\ns_waitcnt vmcnt(15)\n"
"v_perm_b32 v17, v5, v4, s85\nM 128,180,88\ns_add_u32 m0, s50, 8192\nv_perm_b32 v21, v7, v6, s85\nv_lshl_or_b32 v17, v21, 16, v17\nM 144,176,76\n"
"R 174,190,28672\ns_add_u32 s63, s54, 1835008\nM 144,180,92\ns_barrier\nG 4\nM 148,160,76\n"
"R 176,191,16384\ns_waitcnt vmcnt(13)\nv_perm_b32 v18, v9, v8, s85\nM 148,164,92\nbuffer_load_dwordx4 a[176:179], v252, s[20:23], s64 offen offset:3072 sc1\nM 132,160,72\n"
"R 178,191,20480\nv_perm_b32 v22, v11, v10, s85\nM 132,164,88\nbuffer_load_dwordx4 v220, s[12:15], s63 offen lds\nM 116,160,68\nR 180,191,24576\n"
"v_lshl_or_b32 v18, v22, 16, v18\nM 116,164,84\nbuffer_load_dwordx4 v221, s[12:15], s63 offen offset:1024 lds\nM 100,160,64\nR 182,191,28672\ns_add_u32 m0, s50, 16384\n"
"M 100,164,80\nG 4\ns_waitcnt vmcnt(15)\nv_perm_b32 v19, v13, v12, s85\nv_perm_b32 v23, v15, v14, s85\nM 104,168,64\n"
"R 160,188,32768\ns_add_u32 s63, s54, 2064384\nM 104,172,80\nbuffer_load_dwordx4 a[180:183], v252, s[20:23], s65 offen offset:3072 sc1\nM 120,168,68\nR 162,188,36864\n"
"v_lshl_or_b32 v19, v23, 16, v19\nM 120,172,84\nbuffer_load_dwordx4 v220, s[12:15], s63 offen lds\nM 136,168,72\nR 164,188,40960\nv_min3_u32 v230, v16, v17, v18\n"
"M 136,172,88\nbuffer_load_dwordx4 a[184:187], v252, s[20:23], s66 offen offset:3072 sc1\nM 152,168,76\nR 166,188,45056\nv_min3_u32 v230, v230, v19, v19\nM 152,172,92\n"
"G 4\nv_max3_u32 v231, v16, v17, v18\nv_max3_u32 v231, v231, v19, v19\nM 156,176,76\nR 168,189,32768\nbuffer_load_dwordx4 v221, s[12:15], s63 offen offset:1024 lds\n"
"M 156,180,92\nv_cmp_ne_u32 s[86:87], s46, v230\nv_cmp_ne_u32 s[88:89], s46, v231\nM 140,176,72\nR 170,189,36864\nM 140,180,88\n"
"buffer_load_dwordx4 a[188:191], v252, s[20:23], s67 offen offset:3072 sc1\nM 124,176,68\nR 172,189,40960\nM 124,180,84\ns_or_b64 s[86:87], s[86:87], s[88:89]\ns_cbranch_scc1 Lq818\n"
"Lq819:\nv_and_b32 v0, 0xffffff00, v0\nM 108,176,64\nR 174,189,45056\nv_and_b32 v1, 0xffffff00, v1\nM 108,180,80\n"
"G 4\nv_and_b32 v2, 0xffffff00, v2\nv_and_b32 v3, 0xffffff00, v3\nM 32,160,96\nR 176,190,32768\nv_and_b32 v4, 0xffffff00, v4\n"
"M 32,164,112\ns_add_u32 s64, s70, 14692352\nbuffer_load_dwordx4 a[192:195], v252, s[20:23], s64 offen offset:0 sc1\nM 48,160,100\nR 178,190,36864\nv_and_b32 v5, 0xffffff00, v5\n"
"M 48,164,116\nv_and_b32 v6, 0xffffff00, v6\nv_and_b32 v7, 0xffffff00, v7\nM 64,160,104\nR 180,190,40960\nv_and_b32 v8, 0xffffff00, v8\n"
"M 64,164,120\ns_add_u32 m0, s50, 24576\nv_and_b32 v9, 0xffffff00, v9\nv_and_b32 v10, 0xffffff00, v10\nM 80,160,108\nR 182,190,45056\n"
"s_add_u32 s63, s54, 2293760\nM 80,164,124\ns_waitcnt vmcnt(12)\ns_barrier\nG 4\nM 84,168,108\n"
"R 160,191,32768\nv_and_b32 v11, 0xffffff00, v11\nM 84,172,124\ns_add_u32 s65, s70, 14725120\nbuffer_load_dwordx4 a[196:199], v252, s[20:23], s65 offen offset:0 sc1\nM 68,168,104\n"
"R 162,191,36864\nv_and_b32 v12, 0xffffff00, v12\nM 68,172,120\nbuffer_load_dwordx4 v220, s[12:15], s63 offen lds\nM 52,168,100\nR 164,191,40960\n"
"v_and_b32 v13, 0xffffff00, v13\nM 52,172,116\ns_add_u32 s66, s71, 14692352\nbuffer_load_dwordx4 a[200:203], v252, s[20:23], s66 offen offset:0 sc1\nM 36,168,96\nR 166,191,45056\n"
"v_and_b32 v14, 0xffffff00, v14\nM 36,172,112\nG 4\nv_and_b32 v15, 0xffffff00, v15\nv_add_f32 v0, v0, v8\nM 40,176,96\n"
"R 168,188,49152\nbuffer_load_dwordx4 v221, s[12:15], s63 offen offset:1024 lds\nM 40,180,112\ns_add_u32 m0, s50, 32768\ns_add_u32 s63, s54, 2523136\nbuffer_load_dwordx4 v220, s[12:15], s63 offen lds\n"
"M 56,176,100\nR 170,188,53248\nv_add_f32 v1, v1, v9\nM 56,180,116\ns_add_u32 s67, s71, 14725120\nbuffer_load_dwordx4 a[204:207], v252, s[20:23], s67 offen offset:0 sc1\n"
"M 72,176,104\nR 172,184,0\nv_add_f32 v2, v2, v10\nM 72,180,120\nbuffer_load_dwordx4 v221, s[12:15], s63 offen offset:1024 lds\nM 88,176,108\n"
"R 174,184,4096\nv_add_f32 v3, v3, v11\nM 88,180,124\nG 4\nv_add_f32 v4, v4, v12\nv_add_f32 v5, v5, v13\n"
"M 92,160,108\nR 176,189,49152\nv_add_f32 v6, v6, v14\nM 92,164,124\nbuffer_load_dwordx4 a[208:211], v252, s[20:23], s64 offen offset:1024 sc1\nM 76,160,104\n"
"R 178,189,53248\nv_add_f32 v7, v7, v15\nM 76,164,120\nv_add_f32 v0, v0, v4\nv_add_f32 v1, v1, v5\nM 60,160,100\n"
"R 180,185,0\nLq1034:\ns_lshr_b32 s60, s45, 3\ns_lshl2_add_u32 s60, s60, s40\ns_lshl_b32 s60, s60, 8\nv_lshl_add_u32 v224, v252, 3, s60\n"
"v_add_u32 v224, Lq1034+4096-LqPC, v224\nv_min_u32 v224, LqE-LqPC-4, v224\ns_mov_b64 exec, 3\nglobal_load_dword v224, v224, s[98:99]\ns_mov_b64 exec, -1\nM 60,164,116\n"
"v_add_f32 v2, v2, v6\nv_add_f32 v3, v3, v7\nM 44,160,96\nR 182,185,4096\nv_permlane32_swap_b32 v0, v2\nM 44,164,112\n"
"G 4\nv_permlane32_swap_b32 v1, v3\nv_add_f32 v0, v0, v2\nM 96,168,96\nR 160,190,49152\nv_add_f32 v1, v1, v3\n"
"M 96,172,112\nbuffer_load_dwordx4 a[212:215], v252, s[20:23], s65 offen offset:1024 sc1\nM 112,168,100\nR 162,190,53248\nv_permlane16_swap_b32 v0, v1\nM 112,172,116\n"
"s_add_u32 m0, s50, 40960\nv_add_f32 v230, v0, v1\nv_mov_b32 v232, v230\nM 128,168,104\nR 164,186,0\ns_add_u32 s63, s54, 2752512\n"
"M 128,172,120\nbuffer_load_dwordx4 a[216:219], v252, s[20:23], s66 offen offset:1024 sc1\nM 144,168,108\nR 166,186,4096\nv_permlane16_swap_b32 v230, v232\nM 144,172,124\n"
"s_waitcnt vmcnt(13)\ns_barrier\nG 4\nM 148,176,108\nR 168,191,49152\nv_mov_b32 v231, v230\n"
"M 148,180,124\nbuffer_load_dwordx4 v220, s[12:15], s63 offen lds\nM 132,176,104\nR 170,191,53248\nv_mov_b32 v233, v232\nM 132,180,120\n"
"buffer_load_dwordx4 a[220:223], v252, s[20:23], s67 offen offset:1024 sc1\nM 116,176,100\nR 172,187,0\nv_permlane32_swap_b32 v230, v231\nM 116,180,116\nbuffer_load_dwordx4 v221, s[12:15], s63 offen offset:1024 lds\n"
"M 100,176,96\nR 174,187,4096\ns_add_u32 m0, s50, 49152\nM 100,180,112\nG 4\nv_permlane32_swap_b32 v232, v233\n"
"v_mul_f32 v230, 0xba800000, v230\nM 104,160,96\nR 176,184,8192\ns_add_u32 s63, s54, 2981888\nM 104,164,112\nbuffer_load_dwordx4 a[224:227], v252, s[20:23], s64 offen offset:2048 sc1\n"
"M 120,160,100\nR 178,184,12288\nv_mul_f32 v214, v230, v230\nM 120,164,116\nbuffer_load_dwordx4 v220, s[12:15], s63 offen lds\nM 136,160,104\n"
"R 180,184,16384\nv_mul_f32 v231, 0x3a800000, v231\nLpg25:\ns_cbranch_execz Lpg29\nM 136,164,120\nbuffer_load_dwordx4 v221, s[12:15], s63 offen offset:1024 lds\n"
"M 152,160,108\nR 182,184,20480\nv_sub_f32 v231, v231, v214\nM 152,164,124\nG 4\nv_max_f32 v231, 0, v231\n"
"v_add_f32 v231, 0x3727c5ac, v231\nM 156,168,108\nR 160,185,8192\nv_and_b32 v208, s77, v230\nM 156,172,124\nbuffer_load_dwordx4 a[228:231], v252, s[20:23], s65 offen offset:2048 sc1\n"
"M 140,168,104\nR 162,185,12288\nv_sub_f32 v230, v230, v208\nM 140,172,120\nv_rsq_f32 v234, v231\nM 124,168,100\n"
"R 164,185,16384\nv_mul_f32 v216, v231, v234\nM 124,172,116\nbuffer_load_dwordx4 a[232:235], v252, s[20:23], s66 offen offset:2048 sc1\nM 108,168,96\nR 166,185,20480\n"
"v_cvt_pk_bf16_f32 v209, v230, v216\nM 108,172,112\nG 4\nv_cvt_pk_bf16_f32 v208, v208, v208\nv_mul_f32 v214, 0xc0135761, v234\nM 32,176,128\n"
"R 168,186,8192\nv_mul_f32 v215, v234, v234\nM 32,180,144\ns_mov_b64 s[60:61], 0xffff\nv_cndmask_b32 v208, 0, v208, s[60:61]\nv_mul_f32 v215, v215, v214\n"
"M 48,176,132\nR 170,186,12288\nv_mul_f32 v215, 0x3d372713, v215\nM 48,180,148\nbuffer_load_dwordx4 a[236:239], v252, s[20:23], s67 offen offset:2048 sc1\nM 64,176,136\n"
"R 172,186,16384\nv_mul_f32 v232, 0xba800000, v232\nM 64,180,152\ns_add_u32 m0, s50, 57344\ns_mov_b64 s[60:61], 0xffff\nv_cndmask_b32 v209, 0, v209, s[60:61]\n"
"M 80,176,140\nR 174,186,20480\ns_add_u32 s63, s54, 3211264\nM 80,180,156\ns_waitcnt vmcnt(13)\ns_barrier\n"
"G 4\nM 84,160,140\nR 176,187,8192\nv_mul_f32 v217, v232, v232\nM 84,164,156\nbuffer_load_dwordx4 a[240:243], v252, s[20:23], s64 offen offset:3072 sc1\n"
"M 68,160,136\nR 178,187,12288\nv_mul_f32 v233, 0x3a800000, v233\nM 68,164,152\nbuffer_load_dwordx4 v220, s[12:15], s63 offen lds\nM 52,160,132\n"
"R 180,187,16384\nv_sub_f32 v233, v233, v217\nM 52,164,148\nbuffer_load_dwordx4 v221, s[12:15], s63 offen offset:1024 lds\nM 36,160,128\nR 182,187,20480\n"
"s_add_u32 m0, s50, 65536\nM 36,164,144\nG 4\nv_max_f32 v233, 0, v233\nv_add_f32 v233, 0x3727c5ac, v233\nM 40,168,128\n"
"R 160,184,24576\ns_add_u32 s63, s54, 3440640\nM 40,172,144\nbuffer_load_dwordx4 a[244:247], v252, s[20:23], s65 offen offset:3072 sc1\nM 56,168,132\nR 162,184,28672\n"
"v_and_b32 v212, s77, v232\nM 56,172,148\nbuffer_load_dwordx4 v220, s[12:15], s63 offen lds\nM 72,168,136\nR 164,184,32768\nv_sub_f32 v232, v232, v212\n"
"M 72,172,152\nbuffer_load_dwordx4 a[248:251], v252, s[20:23], s66 offen offset:3072 sc1\nM 88,168,140\nR 166,184,36864\nv_cvt_pk_bf16_f32 v212, v212, v212\nM 88,172,156\n"
"G 4\nv_rsq_f32 v235, v233\nM 92,176,140\nR 168,185,24576\nbuffer_load_dwordx4 v221, s[12:15], s63 offen offset:1024 lds\nM 92,180,156\n"
"s_mov_b64 s[60:61], 0xffff\nv_cndmask_b32 v212, 0, v212, s[60:61]\nv_mul_f32 v219, v233, v235\nM 76,176,136\nR 170,185,28672\nv_cvt_pk_bf16_f32 v213, v232, v219\n"
"M 76,180,152\nbuffer_load_dwordx4 a[252:255], v252, s[20:23], s67 offen offset:3072 sc1\nM 60,176,132\nR 172,185,32768\nv_mul_f32 v217, 0xc0135761, v235\nM 60,180,148\n"
"s_mov_b64 s[60:61], 0xffff\nv_cndmask_b32 v213, 0, v213, s[60:61]\nv_mul_f32 v218, v235, v235\nM 44,176,128\nR 174,185,36864\nv_mul_f32 v218, v218, v217\n"
"M 44,180,144\nG 4\nv_mul_f32 v218, 0x3d372713, v218\nM 32,160,160\nR 176,186,24576\nM 32,164,176\n"
"M 48,160,164\nR 178,186,28672\nM 48,164,180\ns_add_u32 m0, s50, 73728\nM 64,160,168\nR 180,186,32768\n"
"M 64,164,184\ns_add_u32 s63, s54, 1835136\nM 80,160,172\nR 182,186,36864\nM 80,164,188\ns_waitcnt vmcnt(11)\n"
"s_barrier\nG 4\nM 84,168,172\nR 160,187,24576\nM 84,172,188\nbuffer_load_dwordx4 v220, s[12:15], s63 offen lds\n"
"M 68,168,168\nR 162,187,28672\nM 68,172,184\nM 52,168,164\nR 164,187,32768\nv_bfe_u32 v250, v252, 5, 1\n"
"v_mul_u32_u24 v250, 2560, v250\nM 52,172,180\nbuffer_load_dwordx4 v221, s[12:15], s63 offen offset:1024 lds\nv_and_b32 v251, 16, v252\nM 36,168,160\nR 166,187,36864\n"
"s_add_u32 m0, s50, 81920\nv_lshl_add_u32 v250, v251, 1, v250\nM 36,172,176\ns_lshl_b32 s60, s90, 6\nv_add_u32 v250, s60, v250\nG 4\n"
"M 40,176,160\nR 168,184,40960\ns_add_u32 s63, s54, 2064512\nM 40,180,176\nbuffer_load_dwordx4 v220, s[12:15], s63 offen lds\nM 56,176,164\n"
"R 170,184,45056\nM 56,180,180\nM 72,176,168\nR 172,184,49152\nM 72,180,184\nbuffer_load_dwordx4 v221, s[12:15], s63 offen offset:1024 lds\n"
"M 88,176,172\nR 174,184,53248\ns_add_u32 m0, s50, 90112\nM 88,180,188\nG 4\nM 92,160,172\n"
"R 176,185,40960\ns_add_u32 s63, s54, 2293888\nM 92,164,188\nbuffer_load_dwordx4 v220, s[12:15], s63 offen lds\nM 76,160,168\nR 178,185,45056\n"
"M 76,164,184\nM 60,160,164\nR 180,185,49152\nM 60,164,180\nbuffer_load_dwordx4 v221, s[12:15], s63 offen offset:1024 lds\nM 44,160,160\n"
"R 182,185,53248\ns_add_u32 s63, s54, 2523264\nM 44,164,176\nG 4\nM 32,168,192\nR 160,186,40960\n"
"M 32,172,208\ns_add_u32 m0, s50, 98304\nM 48,168,196\nR 162,186,45056\nM 48,172,212\nbuffer_load_dwordx4 v220, s[12:15], s63 offen lds\n"
"M 64,168,200\nR 164,186,49152\nM 64,172,216\nM 80,168,204\nR 166,186,53248\nM 80,172,220\n"
"s_waitcnt vmcnt(8)\ns_barrier\nG 4\nM 84,176,204\nR 168,187,40960\nM 84,180,220\n"
"buffer_load_dwordx4 v221, s[12:15], s63 offen offset:1024 lds\nM 68,176,200\nR 170,187,45056\ns_add_u32 s63, s54, 2752640\nM 68,180,216\ns_add_u32 m0, s50, 106496\n"
"M 52,176,196\nR 172,187,49152\nM 52,180,212\nbuffer_load_dwordx4 v220, s[12:15], s63 offen lds\nM 36,176,192\nR 174,187,53248\n"
"M 36,180,208\nG 4\nM 40,160,192\nR 176,184,57344\nM 40,164,208\nbuffer_load_dwordx4 v221, s[12:15], s63 offen offset:1024 lds\n"
"M 56,160,196\nR 178,184,61440\ns_add_u32 s63, s54, 2982016\nM 56,164,212\ns_add_u32 m0, s50, 114688\nM 72,160,200\n"
"R 180,188,0\nM 72,164,216\nbuffer_load_dwordx4 v220, s[12:15], s63 offen lds\nM 88,160,204\nR 182,188,4096\nM 88,164,220\n"
"G 4\nM 92,168,204\nR 160,185,57344\nM 92,172,220\nbuffer_load_dwordx4 v221, s[12:15], s63 offen offset:1024 lds\nM 76,168,200\n"
"R 162,185,61440\ns_add_u32 s63, s54, 3211392\nM 76,172,216\ns_add_u32 m0, s50, 0\nM 60,168,196\nR 164,189,0\n"
"M 60,172,212\nbuffer_load_dwordx4 v220, s[12:15], s63 offen lds\nM 44,168,192\nR 166,189,4096\ns_mov_b32 s60, 157184\nv_bfe_u32 v246, v252, 4, 4\n"
"v_lshl_add_u32 v246, v246, 3, s60\nM 44,172,208\nds_read_b64 v[238:239], v246 offset:0\nG 5\nM 32,176,224\nR 168,186,57344\n"
"ds_read_b64 v[240:241], v246 offset:128\nM 32,180,240\nds_read_b64 v[242:243], v246 offset:256\nM 48,176,228\nR 170,186,61440\nds_read_b64 v[244:245], v246 offset:384\n"
"M 48,180,244\nds_read_b64 v[246:247], v246 offset:0\nM 64,176,232\nR 172,190,0\nM 64,180,248\nbuffer_load_dwordx4 v221, s[12:15], s63 offen offset:1024 lds\n"
"M 80,176,236\nR 174,190,4096\ns_add_u32 m0, s50, 8192\ns_waitcnt vmcnt(14)\nM 80,180,252\ns_waitcnt vmcnt(10) lgkmcnt(2)\n"
"s_barrier\nM 84,160,236\nR 176,187,57344\ns_add_u32 s63, s54, 3440768\nM 84,164,252\nbuffer_load_dwordx4 v220, s[12:15], s63 offen lds\n"
"M 68,160,232\nR 178,187,61440\nM 68,164,248\nM 52,160,228\nR 180,191,0\nv_mfma_f32_16x16x32_bf16 v[32:35], v[238:241], v[208:211], v[32:35]\n"
"M 52,164,244\nbuffer_load_dwordx4 v221, s[12:15], s63 offen offset:1024 lds\nM 36,160,224\nR 182,191,4096\nv_mfma_f32_16x16x32_bf16 v[36:39], v[240:243], v[208:211], v[36:39]\ns_add_u32 m0, s50, 16384\n"
"M 36,164,240\nG 4\nM 40,168,224\nR 160,188,8192\nv_mfma_f32_16x16x32_bf16 v[40:43], v[242:245], v[208:211], v[40:43]\ns_add_u32 s63, s55, 0\n"
"M 40,172,240\nbuffer_load_dwordx4 v222, s[12:15], s63 offen lds\nM 56,168,228\nR 162,188,12288\nv_mfma_f32_16x16x32_bf16 v[44:47], v[244:247], v[208:211], v[44:47]\nM 56,172,244\n"
"M 72,168,232\nR 164,188,16384\nv_mfma_f32_16x16x32_bf16 v[48:51], v[244:247], v[210:213], v[48:51]\nM 72,172,248\nbuffer_load_dwordx4 v223, s[12:15], s63 offen offset:1024 lds\nM 88,168,236\n"
"R 166,188,20480\nv_mfma_f32_16x16x32_bf16 v[52:55], v[238:241], v[210:213], v[52:55]\ns_add_u32 s63, s55, 458752\nM 88,172,252\nG 4\nM 92,176,236\n"
"R 168,189,8192\nv_mfma_f32_16x16x32_bf16 v[56:59], v[240:243], v[210:213], v[56:59]\nM 92,180,252\ns_add_u32 m0, s50, 24576\nM 76,176,232\nR 170,189,12288\n"
"v_mfma_f32_16x16x32_bf16 v[60:63], v[242:245], v[210:213], v[60:63]\nM 76,180,248\nbuffer_load_dwordx4 v222, s[12:15], s63 offen lds\nM 60,176,228\nR 172,189,16384\nM 60,180,244\n"
"M 44,176,224\nR 174,189,20480\nM 44,180,240\ns_waitcnt vmcnt(11)\ns_barrier\nG 4\n"
"M 96,160,128\nR 176,190,8192\nM 96,164,144\ns_mov_b32 m0, s51\nM 112,160,132\nR 178,190,12288\n"
"M 112,164,148\nds_write_addtid_b32 v64 offset:0\nM 128,160,136\nR 180,190,16384\nM 128,164,152\nds_write_addtid_b32 v65 offset:256\n"
"M 144,160,140\nR 182,190,20480\nds_write_addtid_b32 v66 offset:512\nM 144,164,156\nds_write_addtid_b32 v67 offset:768\nds_write_addtid_b32 v68 offset:1024\n"
"G 9\nM 148,168,140\nR 160,191,8192\nds_write_addtid_b32 v69 offset:1280\nM 148,172,156\nds_write_addtid_b32 v70 offset:1536\n"
"ds_write_addtid_b32 v71 offset:1792\nM 132,168,136\nR 162,191,12288\nds_write_addtid_b32 v72 offset:2048\nM 132,172,152\nds_write_addtid_b32 v73 offset:2304\n"
"ds_write_addtid_b32 v74 offset:2560\nM 116,168,132\nR 164,191,16384\nds_write_addtid_b32 v75 offset:2816\nM 116,172,148\nds_write_addtid_b32 v76 offset:3072\n"
"ds_write_addtid_b32 v77 offset:3328\nM 100,168,128\nR 166,191,20480\nds_write_addtid_b32 v78 offset:3584\nM 100,172,144\nds_write_addtid_b32 v79 offset:3840\n"
"ds_write_addtid_b32 v80 offset:4096\nG 15\nM 104,176,128\nR 168,188,24576\nds_write_addtid_b32 v81 offset:4352\nM 104,180,144\n"
"ds_write_addtid_b32 v82 offset:4608\nds_write_addtid_b32 v83 offset:4864\nM 120,176,132\nR 170,188,28672\nds_write_addtid_b32 v84 offset:5120\nM 120,180,148\n"
"ds_write_addtid_b32 v85 offset:5376\nds_write_addtid_b32 v86 offset:5632\nM 136,176,136\nR 172,188,32768\nds_write_addtid_b32 v87 offset:5888\nM 136,180,152\n"
"ds_write_addtid_b32 v88 offset:6144\nds_write_addtid_b32 v89 offset:6400\nM 152,176,140\nR 174,188,36864\nds_write_addtid_b32 v90 offset:6656\nM 152,180,156\n"
"ds_write_addtid_b32 v91 offset:6912\nds_write_addtid_b32 v92 offset:7168\nG 15\nM 156,160,140\nR 176,189,24576\nds_write_addtid_b32 v93 offset:7424\n"
"M 156,164,156\nds_write_addtid_b32 v94 offset:7680\nds_write_addtid_b32 v95 offset:7936\nM 140,160,136\nR 178,189,28672\ns_add_u32 m0, s50, 24576\n"
"M 140,164,152\nbuffer_load_dwordx4 v223, s[12:15], s63 offen offset:1024 lds\nM 124,160,132\nR 180,189,32768\ns_waitcnt vmcnt(8) lgkmcnt(2)\ns_barrier\n"
"M 124,164,148\ns_add_u32 m0, s50, 32768\nds_read2st64_b32 v[64:65], v225 offset0:0 offset1:1\nds_read2st64_b32 v[66:67], v225 offset0:2 offset1:3\nM 108,160,128\nR 182,189,36864\n"
"s_add_u32 s63, s55, 917504\nds_read2st64_b32 v[68:69], v225 offset0:4 offset1:5\nM 108,164,144\nds_read2st64_b32 v[70:71], v225 offset0:6 offset1:7\nds_read2st64_b32 v[72:73], v225 offset0:8 offset1:9\nM 96,168,160\n"
"R 160,190,24576\nds_read2st64_b32 v[74:75], v225 offset0:10 offset1:11\nM 96,172,176\nbuffer_load_dwordx4 v222, s[12:15], s63 offen lds\nds_read2st64_b32 v[76:77], v225 offset0:12 offset1:13\nM 112,168,164\n"
"R 162,190,28672\nds_read2st64_b32 v[78:79], v225 offset0:14 offset1:15\nM 112,172,180\nds_read2st64_b32 v[80:81], v225 offset0:16 offset1:17\nds_read2st64_b32 v[82:83], v225 offset0:18 offset1:19\nG 12\n"
"v_add_f32 v32, v32, v64\nM 128,168,168\nR 164,190,32768\nds_read2st64_b32 v[84:85], v225 offset0:20 offset1:21\nv_add_f32 v33, v33, v65\nM 128,172,184\n"
"ds_read2st64_b32 v[86:87], v225 offset0:22 offset1:23\nds_read2st64_b32 v[88:89], v225 offset0:24 offset1:25\nG 15\nv_add_f32 v34, v34, v66\nM 144,168,172\nR 166,190,36864\n"
"ds_read2st64_b32 v[90:91], v225 offset0:26 offset1:27\nv_add_f32 v35, v35, v67\nM 144,172,188\nds_read2st64_b32 v[92:93], v225 offset0:28 offset1:29\nds_read2st64_b32 v[94:95], v225 offset0:30 offset1:31\nG 15\n"
"M 148,176,172\nR 168,191,24576\nv_add_f32 v36, v36, v68\nv_add_f32 v37, v37, v69\nM 148,180,188\nbuffer_load_dwordx4 v223, s[12:15], s63 offen offset:1024 lds\n"
"M 132,176,168\nR 170,191,28672\nv_add_f32 v38, v38, v70\nM 132,180,184\ns_add_u32 m0, s50, 40960\nv_add_f32 v39, v39, v71\n"
"v_add_f32 v40, v40, v72\nM 116,176,164\nR 172,191,32768\nv_add_f32 v41, v41, v73\nM 116,180,180\ns_add_u32 s63, s55, 1376256\n"
"G 15\nv_add_f32 v42, v42, v74\nv_add_f32 v43, v43, v75\nM 100,176,160\nR 174,191,36864\nv_add_f32 v44, v44, v76\n"
"M 100,180,176\nG 7\nv_add_f32 v45, v45, v77\nv_add_f32 v46, v46, v78\nM 104,160,160\nR 176,188,40960\n"
"v_add_f32 v47, v47, v79\nM 104,164,176\nbuffer_load_dwordx4 v222, s[12:15], s63 offen lds\nM 120,160,164\nR 178,188,45056\nv_add_f32 v48, v48, v80\n"
"M 120,164,180\nv_add_f32 v49, v49, v81\nv_add_f32 v50, v50, v82\nM 136,160,168\nR 180,188,49152\nv_add_f32 v51, v51, v83\n"
"M 136,164,184\nv_add_f32 v52, v52, v84\nv_add_f32 v53, v53, v85\nM 152,160,172\nR 182,188,53248\nv_add_f32 v54, v54, v86\n"
"M 152,164,188\nG 4\nv_add_f32 v55, v55, v87\nv_add_f32 v56, v56, v88\nM 156,168,172\nR 160,189,40960\n"
"v_add_f32 v57, v57, v89\nM 156,172,188\nbuffer_load_dwordx4 v223, s[12:15], s63 offen offset:1024 lds\nM 140,168,168\nR 162,189,45056\nv_add_f32 v58, v58, v90\n"
"M 140,172,184\ns_add_u32 m0, s50, 49152\nv_add_f32 v59, v59, v91\nv_add_f32 v60, v60, v92\nM 124,168,164\nR 164,189,49152\n"
"v_add_f32 v61, v61, v93\nM 124,172,180\ns_add_u32 s63, s55, 1835008\nv_add_f32 v62, v62, v94\nv_add_f32 v63, v63, v95\nM 108,168,160\n"
"R 166,189,53248\nv_mul_f32 v230, v32, v32\nv_mul_f32 v231, v33, v33\nM 108,172,176\ns_add_u32 s84, s73, 37748736\ns_add_u32 s85, s73, 37863424\n"
"G 4\nv_mul_f32 v232, v34, v34\nM 96,176,192\nR 168,190,40960\nv_mul_f32 v233, v35, v35\nv_fma_f32 v230, v230, v215, v214\n"
"M 96,180,208\nLpg26:\ns_cbranch_execz Lpg30\nbuffer_load_dwordx4 v222, s[12:15], s63 offen lds\nv_fma_f32 v231, v231, v215, v214\nM 112,176,196\n"
"R 170,190,45056\ns_waitcnt vmcnt(7)\ns_barrier\nv_fma_f32 v232, v232, v215, v214\nM 112,180,212\nv_fma_f32 v233, v233, v215, v214\n"
"v_mul_f32 v230, v32, v230\nv_mul_f32 v231, v33, v231\nM 128,176,200\nR 172,190,49152\nv_mul_f32 v232, v34, v232\nv_mul_f32 v233, v35, v233\n"
"M 128,180,216\nv_exp_f32 v230, v230\nv_exp_f32 v231, v231\nM 144,176,204\nR 174,190,53248\nv_fma_f32 v230, v230, v216, v216\n"
"v_exp_f32 v232, v232\nM 144,180,220\nG 4\nv_exp_f32 v233, v233\nv_fma_f32 v231, v231, v216, v216\nM 148,160,204\n"
"R 176,191,40960\nv_fma_f32 v232, v232, v216, v216\nv_fma_f32 v233, v233, v216, v216\nM 148,164,220\nbuffer_load_dwordx4 v223, s[12:15], s63 offen offset:1024 lds\nv_rcp_f32 v230, v230\n"
"M 132,160,200\nR 178,191,45056\nv_mul_f32 v230, v32, v230\nv_rcp_f32 v231, v231\nM 132,164,216\ns_add_u32 m0, s50, 57344\n"
"v_rcp_f32 v232, v232\nv_rcp_f32 v233, v233\nM 116,160,196\nR 180,191,49152\nv_mul_f32 v231, v33, v231\nv_mul_f32 v232, v34, v232\n"
"M 116,164,212\ns_add_u32 s63, s55, 2293760\nv_mul_f32 v233, v35, v233\nv_cvt_pk_bf16_f32 v32, v230, v231\nv_cvt_pk_bf16_f32 v33, v232, v233\nM 100,160,192\n"
"R 182,191,53248\nv_mul_f32 v234, v36, v36\nv_mul_f32 v235, v37, v37\nM 100,164,208\nG 4\nv_mul_f32 v236, v38, v38\n"
"v_mul_f32 v237, v39, v39\nv_fma_f32 v234, v234, v215, v214\nM 104,168,192\nR 160,184,0\nv_fma_f32 v235, v235, v215, v214\nv_fma_f32 v236, v236, v215, v214\n"
"M 104,172,208\nbuffer_load_dwordx4 v222, s[12:15], s63 offen lds\nv_fma_f32 v237, v237, v215, v214\nM 120,168,196\nR 162,184,4096\nv_mul_f32 v234, v36, v234\n"
"v_mul_f32 v235, v37, v235\nM 120,172,212\nv_exp_f32 v234, v234\nv_mul_f32 v236, v38, v236\nM 136,168,200\nR 164,184,8192\n"
"v_mul_f32 v237, v39, v237\nv_exp_f32 v235, v235\nM 136,172,216\nv_exp_f32 v236, v236\nv_exp_f32 v237, v237\nM 152,168,204\n"
"R 166,184,12288\nv_fma_f32 v234, v234, v216, v216\nv_fma_f32 v235, v235, v216, v216\nM 152,172,220\nG 4\nv_rcp_f32 v234, v234\n"
"v_fma_f32 v236, v236, v216, v216\nM 156,176,204\nR 168,185,0\nv_fma_f32 v237, v237, v216, v216\nv_rcp_f32 v235, v235\nM 156,180,220\n"
"buffer_load_dwordx4 v223, s[12:15], s63 offen offset:1024 lds\nv_rcp_f32 v236, v236\nM 140,176,200\nR 170,185,4096\nv_mul_f32 v234, v36, v234\nv_rcp_f32 v237, v237\n"
"M 140,180,216\nv_mul_f32 v235, v37, v235\nv_mul_f32 v236, v38, v236\nv_mul_f32 v237, v39, v237\nM 124,176,196\nR 172,185,8192\n"
"v_cvt_pk_bf16_f32 v34, v234, v235\nv_cvt_pk_bf16_f32 v35, v236, v237\nM 124,180,212\nbuffer_store_dwordx4 v[32:35], v252, s[20:23], s84 offen offset:0 sc1\nv_mul_f32 v230, v40, v40\nM 108,176,192\n"
"R 174,185,12288\ns_mov_b32 s60, 157696\nv_bfe_u32 v246, v252, 4, 4\nv_lshl_add_u32 v246, v246, 3, s60\nv_mul_f32 v231, v41, v41\nM 108,180,208\n"
"ds_read_b64 v[238:239], v246 offset:0\nG 5\nv_mul_f32 v232, v42, v42\nM 96,160,224\nR 176,186,0\nds_read_b64 v[240:241], v246 offset:128\n"
"v_mul_f32 v233, v43, v43\nM 96,164,240\nds_read_b64 v[242:243], v246 offset:256\ns_add_u32 m0, s50, 65536\nv_fma_f32 v230, v230, v215, v214\nM 112,160,228\n"
"R 178,186,4096\nds_read_b64 v[244:245], v246 offset:384\nv_fma_f32 v231, v231, v215, v214\nM 112,164,244\nds_read_b64 v[246:247], v246 offset:0\ns_add_u32 s63, s55, 2752512\n"
"v_fma_f32 v232, v232, v215, v214\nM 128,160,232\nR 180,186,8192\nv_fma_f32 v233, v233, v215, v214\nv_mul_f32 v230, v40, v230\nM 128,164,248\n"
"buffer_load_dwordx4 v222, s[12:15], s63 offen lds\nv_mul_f32 v231, v41, v231\nM 144,160,236\nR 182,186,12288\nv_mul_f32 v232, v42, v232\nv_mul_f32 v233, v43, v233\n"
"M 144,164,252\nG 9\nv_exp_f32 v230, v230\nv_exp_f32 v231, v231\nM 148,168,236\nR 160,187,0\n"
"v_fma_f32 v230, v230, v216, v216\nv_exp_f32 v232, v232\nM 148,172,252\nv_exp_f32 v233, v233\nv_fma_f32 v231, v231, v216, v216\nM 132,168,232\n"
"R 162,187,4096\nv_fma_f32 v232, v232, v216, v216\nv_fma_f32 v233, v233, v216, v216\nM 132,172,248\nbuffer_load_dwordx4 v223, s[12:15], s63 offen offset:1024 lds\nv_rcp_f32 v230, v230\n"
"M 116,168,228\nR 164,187,8192\nG 9\nv_mfma_f32_16x16x32_bf16 v[96:99], v[238:241], v[208:211], v[96:99]\ns_add_u32 s63, s55, 3211264\nv_rcp_f32 v231, v231\n"
"M 116,172,244\ns_add_u32 m0, s50, 73728\nv_rcp_f32 v232, v232\nv_rcp_f32 v233, v233\nM 100,168,224\nR 166,187,12288\n"
"G 9\nv_mfma_f32_16x16x32_bf16 v[100:103], v[240:243], v[208:211], v[100:103]\nv_mul_f32 v230, v40, v230\nv_mul_f32 v231, v41, v231\nM 100,172,240\nG 4\n"
"v_mul_f32 v232, v42, v232\nv_mul_f32 v233, v43, v233\nv_cvt_pk_bf16_f32 v40, v230, v231\nM 104,176,224\nR 168,184,16384\nv_mfma_f32_16x16x32_bf16 v[104:107], v[242:245], v[208:211], v[104:107]\n"
"v_cvt_pk_bf16_f32 v41, v232, v233\nv_mul_f32 v234, v44, v44\nM 104,180,240\nbuffer_load_dwordx4 v222, s[12:15], s63 offen lds\nv_mul_f32 v235, v45, v45\nM 120,176,228\n"
"R 170,184,20480\nv_mfma_f32_16x16x32_bf16 v[108:111], v[244:247], v[208:211], v[108:111]\nv_mul_f32 v236, v46, v46\nv_mul_f32 v237, v47, v47\nM 120,180,244\nv_fma_f32 v234, v234, v215, v214\n"
"v_fma_f32 v235, v235, v215, v214\nv_fma_f32 v236, v236, v215, v214\nM 136,176,232\nR 172,186,16384\nv_mfma_f32_16x16x32_bf16 v[112:115], v[244:247], v[210:213], v[112:115]\nv_fma_f32 v237, v237, v215, v214\n"
"v_mul_f32 v234, v44, v234\nM 136,180,248\nv_exp_f32 v234, v234\nv_mul_f32 v235, v45, v235\nM 152,176,236\nR 174,186,20480\n"
"v_mfma_f32_16x16x32_bf16 v[116:119], v[238:241], v[210:213], v[116:119]\nv_mul_f32 v236, v46, v236\nv_mul_f32 v237, v47, v237\nM 152,180,252\nG 4\nv_exp_f32 v235, v235\n"
"v_exp_f32 v236, v236\nM 156,160,236\nR 176,185,16384\nv_mfma_f32_16x16x32_bf16 v[120:123], v[240:243], v[210:213], v[120:123]\nv_fma_f32 v234, v234, v216, v216\nv_exp_f32 v237, v237\n"
"M 156,164,252\nbuffer_load_dwordx4 v223, s[12:15], s63 offen offset:1024 lds\nv_fma_f32 v235, v235, v216, v216\nM 140,160,232\nR 178,185,20480\nv_mfma_f32_16x16x32_bf16 v[124:127], v[242:245], v[210:213], v[124:127]\n"
"v_fma_f32 v236, v236, v216, v216\nv_fma_f32 v237, v237, v216, v216\nM 140,164,248\nv_rcp_f32 v234, v234\nv_rcp_f32 v235, v235\nM 124,160,228\n"
"R 180,187,16384\nv_mul_f32 v234, v44, v234\nv_rcp_f32 v236, v236\nM 124,164,244\nv_rcp_f32 v237, v237\nv_mul_f32 v235, v45, v235\n"
"M 108,160,224\nR 182,187,20480\nv_mul_f32 v236, v46, v236\nv_mul_f32 v237, v47, v237\nM 108,164,240\ns_waitcnt vmcnt(11)\n"
"s_barrier\nG 4\nv_cvt_pk_bf16_f32 v42, v234, v235\nv_mfma_f32_16x16x32_bf16 v[0:3], v[168:171], a[0:3], 0\nR 160,184,24576\nv_cvt_pk_bf16_f32 v43, v236, v237\n"
"v_mul_f32 v230, v48, v48\nM 0,172,16\ns_mov_b32 m0, s51\nv_mul_f32 v231, v49, v49\nv_mul_f32 v232, v50, v50\nv_mul_f32 v233, v51, v51\n"
"v_mfma_f32_16x16x32_bf16 v[16:19], v[168:171], a[4:7], 0\nR 162,184,28672\nv_fma_f32 v230, v230, v218, v217\nv_fma_f32 v231, v231, v218, v217\nM 16,172,20\nbuffer_store_dwordx4 v[40:43], v252, s[20:23], s84 offen offset:1024 sc1\n"
"v_fma_f32 v232, v232, v218, v217\nv_mfma_f32_16x16x32_bf16 v[8:11], v[168:171], a[8:11], 0\nR 164,186,24576\nv_fma_f32 v233, v233, v218, v217\nv_mul_f32 v230, v48, v230\nM 8,172,24\n"
"ds_write_addtid_b32 v128 offset:0\nv_exp_f32 v230, v230\nv_mul_f32 v231, v49, v231\nv_mfma_f32_16x16x32_bf16 v[24:27], v[168:171], a[12:15], 0\nR 166,186,28672\nds_write_addtid_b32 v129 offset:256\n"
"v_mul_f32 v232, v50, v232\nv_mul_f32 v233, v51, v233\nM 24,172,28\nds_write_addtid_b32 v130 offset:512\nG 7\nv_exp_f32 v231, v231\n"
"v_exp_f32 v232, v232\nv_mfma_f32_16x16x32_bf16 v[28:31], v[176:179], a[12:15], 0\nR 168,185,24576\nds_write_addtid_b32 v131 offset:768\nv_fma_f32 v230, v230, v219, v219\nv_exp_f32 v233, v233\n"
"M 28,180,28\nds_write_addtid_b32 v132 offset:1024\nv_rcp_f32 v230, v230\nv_fma_f32 v231, v231, v219, v219\nv_mfma_f32_16x16x32_bf16 v[12:15], v[176:179], a[8:11], 0\nR 170,185,28672\n"
"ds_write_addtid_b32 v133 offset:1280\nv_fma_f32 v232, v232, v219, v219\nv_fma_f32 v233, v233, v219, v219\nM 12,180,24\nds_write_addtid_b32 v134 offset:1536\nv_rcp_f32 v231, v231\n"
"v_rcp_f32 v232, v232\nv_mfma_f32_16x16x32_bf16 v[20:23], v[176:179], a[4:7], 0\nR 172,187,24576\nds_write_addtid_b32 v135 offset:1792\nv_mul_f32 v230, v48, v230\nv_rcp_f32 v233, v233\n"
"M 20,180,20\nds_write_addtid_b32 v136 offset:2048\nv_mul_f32 v231, v49, v231\nv_mul_f32 v232, v50, v232\nv_mul_f32 v233, v51, v233\nv_mfma_f32_16x16x32_bf16 v[4:7], v[176:179], a[0:3], 0\n"
"R 174,187,28672\nds_write_addtid_b32 v137 offset:2304\nv_cvt_pk_bf16_f32 v48, v230, v231\nv_cvt_pk_bf16_f32 v49, v232, v233\nM 4,180,16\nds_write_addtid_b32 v138 offset:2560\n"
"G 14\nv_mul_f32 v234, v52, v52\nv_mul_f32 v235, v53, v53\nv_mul_f32 v236, v54, v54\nM 0,160,32\nR 176,184,32768\n"
"ds_write_addtid_b32 v139 offset:2816\nv_mul_f32 v237, v55, v55\nv_fma_f32 v234, v234, v218, v217\nM 0,164,48\nds_write_addtid_b32 v140 offset:3072\nv_fma_f32 v235, v235, v218, v217\n"
"v_fma_f32 v236, v236, v218, v217\nv_fma_f32 v237, v237, v218, v217\nM 16,160,36\nR 178,184,36864\nds_write_addtid_b32 v141 offset:3328\nv_mul_f32 v234, v52, v234\n"
"v_mul_f32 v235, v53, v235\nM 16,164,52\nds_write_addtid_b32 v142 offset:3584\nv_exp_f32 v234, v234\nv_mul_f32 v236, v54, v236\nM 8,160,40\n"
"R 180,186,32768\nds_write_addtid_b32 v143 offset:3840\nv_mul_f32 v237, v55, v237\nv_exp_f32 v235, v235\nM 8,164,56\nds_write_addtid_b32 v144 offset:4096\n"
"v_exp_f32 v236, v236\nv_exp_f32 v237, v237\nM 24,160,44\nR 182,186,36864\nds_write_addtid_b32 v145 offset:4352\nv_fma_f32 v234, v234, v219, v219\n"
"v_fma_f32 v235, v235, v219, v219\nM 24,164,60\nds_write_addtid_b32 v146 offset:4608\nG 14\nv_rcp_f32 v234, v234\nv_fma_f32 v236, v236, v219, v219\n"
"M 28,168,44\nR 160,185,32768\nds_write_addtid_b32 v147 offset:4864\nv_fma_f32 v237, v237, v219, v219\nv_rcp_f32 v235, v235\nM 28,172,60\n"
"ds_write_addtid_b32 v148 offset:5120\nds_write_addtid_b32 v149 offset:5376\nds_write_addtid_b32 v150 offset:5632\nds_write_addtid_b32 v151 offset:5888\nds_write_addtid_b32 v152 offset:6144\nds_write_addtid_b32 v153 offset:6400\n"
"ds_write_addtid_b32 v154 offset:6656\nds_write_addtid_b32 v155 offset:6912\nds_write_addtid_b32 v156 offset:7168\nds_write_addtid_b32 v157 offset:7424\nds_write_addtid_b32 v158 offset:7680\nds_write_addtid_b32 v159 offset:7936\n"
"v_rcp_f32 v236, v236\nv_rcp_f32 v237, v237\nM 12,168,40\nR 162,185,36864\ns_add_u32 m0, s50, 81920\ns_add_u32 s63, s54, 256\n"
"v_mul_f32 v234, v52, v234\nM 12,172,56\nbuffer_load_dwordx4 v220, s[12:15], s63 offen lds\nv_mul_f32 v235, v53, v235\nM 20,168,36\nR 164,187,32768\n"
"s_waitcnt vmcnt(4) lgkmcnt(2)\ns_barrier\nv_mul_f32 v236, v54, v236\nM 20,172,52\nds_read2st64_b32 v[128:129], v225 offset0:0 offset1:1\nv_mul_f32 v237, v55, v237\n"
"v_cvt_pk_bf16_f32 v50, v234, v235\nv_cvt_pk_bf16_f32 v51, v236, v237\nM 4,168,32\nR 166,187,36864\nds_read2st64_b32 v[130:131], v225 offset0:2 offset1:3\nv_mul_f32 v230, v56, v56\n"
"v_mul_f32 v231, v57, v57\nM 4,172,48\nds_read2st64_b32 v[132:133], v225 offset0:4 offset1:5\nv_mul_f32 v232, v58, v58\nv_mul_f32 v233, v59, v59\nv_fma_f32 v230, v230, v218, v217\n"
"M 0,176,64\nR 168,184,40960\nds_read2st64_b32 v[134:135], v225 offset0:6 offset1:7\nv_fma_f32 v231, v231, v218, v217\nv_fma_f32 v232, v232, v218, v217\nM 0,180,80\n"
"buffer_load_dwordx4 v221, s[12:15], s63 offen offset:1024 lds\nds_read2st64_b32 v[136:137], v225 offset0:8 offset1:9\nv_fma_f32 v233, v233, v218, v217\nM 16,176,68\nR 170,184,45056\ns_add_u32 m0, s50, 90112\n"
"s_add_u32 s63, s54, 229632\nds_read2st64_b32 v[138:139], v225 offset0:10 offset1:11\nv_mul_f32 v230, v56, v230\nM 16,180,84\nbuffer_load_dwordx4 v220, s[12:15], s63 offen lds\nds_read2st64_b32 v[140:141], v225 offset0:12 offset1:13\n"
"v_mul_f32 v231, v57, v231\nM 8,176,72\nR 172,186,40960\nds_read2st64_b32 v[142:143], v225 offset0:14 offset1:15\nv_mul_f32 v232, v58, v232\nv_mul_f32 v233, v59, v233\n"
"M 8,180,88\nbuffer_store_dwordx4 v[48:51], v252, s[20:23], s85 offen offset:0 sc1\nds_read2st64_b32 v[144:145], v225 offset0:16 offset1:17\nv_exp_f32 v230, v230\nM 24,176,76\nR 174,186,45056\n"
"ds_read2st64_b32 v[146:147], v225 offset0:18 offset1:19\nv_fma_f32 v230, v230, v219, v219\nv_exp_f32 v231, v231\nM 24,180,92\nds_read2st64_b32 v[148:149], v225 offset0:20 offset1:21\nG 14\n"
"v_exp_f32 v232, v232\nv_exp_f32 v233, v233\nM 28,160,76\nR 176,185,40960\nds_read2st64_b32 v[150:151], v225 offset0:22 offset1:23\nv_fma_f32 v231, v231, v219, v219\n"
"v_fma_f32 v232, v232, v219, v219\nM 28,164,92\nbuffer_load_dwordx4 v221, s[12:15], s63 offen offset:1024 lds\nds_read2st64_b32 v[152:153], v225 offset0:24 offset1:25\nv_fma_f32 v233, v233, v219, v219\nM 12,160,72\n"
"R 178,185,45056\ns_add_u32 s63, s54, 459008\nds_read2st64_b32 v[154:155], v225 offset0:26 offset1:27\nv_rcp_f32 v230, v230\nM 12,164,88\ns_add_u32 m0, s50, 98304\n"
"ds_read2st64_b32 v[156:157], v225 offset0:28 offset1:29\nv_rcp_f32 v231, v231\nv_rcp_f32 v232, v232\nM 20,160,68\nR 180,187,40960\nds_read2st64_b32 v[158:159], v225 offset0:30 offset1:31\n"
"v_mul_f32 v230, v56, v230\nv_rcp_f32 v233, v233\nM 20,164,84\nbuffer_load_dwordx4 v220, s[12:15], s63 offen lds\nv_add_f32 v96, v96, v128\nv_mul_f32 v231, v57, v231\n"
"M 4,160,64\nR 182,187,45056\nv_mul_f32 v232, v58, v232\nv_add_f32 v97, v97, v129\nv_mul_f32 v233, v59, v233\nM 4,164,80\n"
"G 11\nv_cvt_pk_bf16_f32 v56, v230, v231\nv_cvt_pk_bf16_f32 v57, v232, v233\nv_add_f32 v98, v98, v130\nv_mul_f32 v234, v60, v60\nM 0,168,96\n"
"R 160,184,49152\nv_mul_f32 v235, v61, v61\nv_add_f32 v99, v99, v131\nv_mul_f32 v236, v62, v62\nM 0,172,112\nbuffer_load_dwordx4 v221, s[12:15], s63 offen offset:1024 lds\n"
"v_add_f32 v100, v100, v132\nv_mul_f32 v237, v63, v63\nM 16,168,100\nR 162,184,53248\ns_add_u32 s63, s54, 688384\nv_add_f32 v101, v101, v133\n"
"v_fma_f32 v234, v234, v218, v217\nM 16,172,116\ns_add_u32 m0, s50, 106496\nv_fma_f32 v235, v235, v218, v217\nv_fma_f32 v236, v236, v218, v217\nv_add_f32 v102, v102, v134\n"
"v_fma_f32 v237, v237, v218, v217\nM 8,168,104\nR 164,186,49152\nv_mul_f32 v234, v60, v234\nv_add_f32 v103, v103, v135\nv_mul_f32 v235, v61, v235\n"
"M 8,172,120\nbuffer_load_dwordx4 v220, s[12:15], s63 offen lds\nv_add_f32 v104, v104, v136\nv_mul_f32 v236, v62, v236\nM 24,168,108\nR 166,186,53248\n"
"v_mul_f32 v237, v63, v237\nv_add_f32 v105, v105, v137\nv_exp_f32 v234, v234\nM 24,172,124\nG 4\nv_add_f32 v106, v106, v138\n"
"v_add_f32 v107, v107, v139\nv_add_f32 v108, v108, v140\nv_exp_f32 v235, v235\nM 28,176,108\nR 168,185,49152\nv_add_f32 v109, v109, v141\n"
"v_add_f32 v110, v110, v142\nv_exp_f32 v236, v236\nM 28,180,124\nv_add_f32 v111, v111, v143\nv_add_f32 v112, v112, v144\nv_add_f32 v113, v113, v145\n"
"v_exp_f32 v237, v237\nM 12,176,104\nR 170,185,53248\nv_add_f32 v114, v114, v146\nv_add_f32 v115, v115, v147\nv_fma_f32 v234, v234, v219, v219\n"
"M 12,180,120\nbuffer_load_dwordx4 v221, s[12:15], s63 offen offset:1024 lds\nv_add_f32 v116, v116, v148\nv_fma_f32 v235, v235, v219, v219\nM 20,176,100\nR 172,187,49152\n"
"s_add_u32 s63, s54, 917760\nv_add_f32 v117, v117, v149\nM 20,180,116\ns_add_u32 m0, s50, 114688\nv_add_f32 v118, v118, v150\nv_add_f32 v119, v119, v151\n"
"v_add_f32 v120, v120, v152\nv_add_f32 v121, v121, v153\nv_add_f32 v122, v122, v154\nv_add_f32 v123, v123, v155\nv_add_f32 v124, v124, v156\nv_add_f32 v125, v125, v157\n"
"v_add_f32 v126, v126, v158\nv_add_f32 v127, v127, v159\nM 4,176,96\nR 174,187,53248\nv_fma_f32 v236, v236, v219, v219\nM 4,180,112\n"
"G 4\nv_rcp_f32 v234, v234\nM 0,160,128\nR 176,184,57344\nv_fma_f32 v237, v237, v219, v219\nM 0,164,144\n"
"buffer_load_dwordx4 v220, s[12:15], s63 offen lds\nM 16,160,132\nR 178,184,61440\nv_mul_f32 v234, v60, v234\nM 16,164,148\nv_rcp_f32 v235, v235\n"
"M 8,160,136\nR 180,186,57344\nLq1036:\ns_lshr_b32 s60, s45, 3\ns_lshl2_add_u32 s60, s60, s40\ns_lshl_b32 s60, s60, 8\n"
"v_lshl_add_u32 v224, v252, 3, s60\nv_add_u32 v224, Lq1036+4096-LqPC, v224\nv_min_u32 v224, LqE-LqPC-4, v224\ns_mov_b64 exec, 3\nglobal_load_dword v224, v224, s[98:99]\ns_mov_b64 exec, -1\n"
"M 8,164,152\nv_rcp_f32 v236, v236\nM 24,160,140\nR 182,186,61440\nv_mul_f32 v235, v61, v235\nM 24,164,156\n"
"G 4\nv_rcp_f32 v237, v237\nM 28,168,140\nR 160,185,57344\nv_mul_f32 v236, v62, v236\nM 28,172,156\n"
"buffer_load_dwordx4 v221, s[12:15], s63 offen offset:1024 lds\nM 12,168,136\nR 162,185,61440\ns_add_u32 s63, s54, 1147136\nM 12,172,152\ns_add_u32 m0, s50, 0\n"
"v_mul_f32 v237, v63, v237\nv_cvt_pk_bf16_f32 v58, v234, v235\nM 20,168,132\nR 164,187,57344\nv_cvt_pk_bf16_f32 v59, v236, v237\nM 20,172,148\n"
"buffer_load_dwordx4 v220, s[12:15], s63 offen lds\nM 4,168,128\nR 166,187,61440\nM 4,172,144\nG 4\nM 0,176,160\n"
"R 168,188,0\nLpg27:\ns_cbranch_execz Lpg31\nM 0,180,176\nbuffer_load_dwordx4 v221, s[12:15], s63 offen offset:1024 lds\nM 16,176,164\n"
"R 170,188,4096\ns_add_u32 s63, s54, 1376512\nM 16,180,180\ns_add_u32 m0, s50, 8192\nM 8,176,168\nR 172,190,0\n"
"M 8,180,184\nbuffer_load_dwordx4 v220, s[12:15], s63 offen lds\nM 24,176,172\nR 174,190,4096\nM 24,180,188\nbuffer_store_dwordx4 v[56:59], v252, s[20:23], s85 offen offset:1024 sc1\n"
"G 4\nM 28,160,172\nR 176,189,0\ns_waitcnt vmcnt(11)\ns_barrier\nv_mul_f32 v230, v96, v96\n"
"M 28,164,188\ns_add_u32 s84, s73, 37750784\ns_add_u32 s85, s73, 37865472\nv_mul_f32 v231, v97, v97\nv_mul_f32 v232, v98, v98\nM 12,160,168\n"
"R 178,189,4096\nv_mul_f32 v233, v99, v99\nv_fma_f32 v230, v230, v215, v214\nM 12,164,184\nbuffer_load_dwordx4 v221, s[12:15], s63 offen offset:1024 lds\nv_fma_f32 v231, v231, v215, v214\n"
"M 20,160,164\nR 180,191,0\nv_fma_f32 v232, v232, v215, v214\nv_fma_f32 v233, v233, v215, v214\nM 20,164,180\ns_add_u32 m0, s50, 16384\n"
"v_mul_f32 v230, v96, v230\nv_mul_f32 v231, v97, v231\nv_mul_f32 v232, v98, v232\nM 4,160,160\nR 182,191,4096\ns_mov_b32 s60, 158720\n"
"v_bfe_u32 v242, v252, 4, 4\nv_lshl_add_u32 v242, v242, 3, s60\nv_mul_f32 v233, v99, v233\nM 4,164,176\nds_read_b64 v[238:239], v242 offset:0\nG 5\n"
"v_exp_f32 v230, v230\nM 0,168,192\nR 160,188,8192\nds_read_b64 v[240:241], v242 offset:128\nv_exp_f32 v231, v231\nM 0,172,208\n"
"ds_read_b64 v[242:243], v242 offset:0\ns_add_u32 s63, s54, 1605888\nv_exp_f32 v232, v232\nM 16,168,196\nR 162,188,12288\nv_fma_f32 v230, v230, v216, v216\n"
"v_exp_f32 v233, v233\nM 16,172,212\nbuffer_load_dwordx4 v220, s[12:15], s63 offen lds\nv_fma_f32 v231, v231, v216, v216\nM 8,168,200\nR 164,190,8192\n"
"v_fma_f32 v232, v232, v216, v216\nv_fma_f32 v233, v233, v216, v216\nM 8,172,216\nv_rcp_f32 v230, v230\nv_rcp_f32 v231, v231\nM 24,168,204\n"
"R 166,190,12288\nv_mul_f32 v230, v96, v230\nv_rcp_f32 v232, v232\nM 24,172,220\nG 7\nv_rcp_f32 v233, v233\n"
"v_mul_f32 v231, v97, v231\nM 28,176,204\nR 168,189,8192\nv_mul_f32 v232, v98, v232\nv_mul_f32 v233, v99, v233\nM 28,180,220\n"
"buffer_load_dwordx4 v221, s[12:15], s63 offen offset:1024 lds\nv_cvt_pk_bf16_f32 v96, v230, v231\nM 12,176,200\nR 170,189,12288\ns_add_u32 s63, s54, 1835264\nv_cvt_pk_bf16_f32 v97, v232, v233\n"
"M 12,180,216\ns_add_u32 m0, s50, 24576\nv_mul_f32 v234, v100, v100\nv_mul_f32 v235, v101, v101\nv_mul_f32 v236, v102, v102\nM 20,176,196\n"
"R 172,191,8192\nG 7\nv_mfma_f32_16x16x32_bf16 v[0:3], v[238:241], v[208:211], v[0:3]\nv_mul_f32 v237, v103, v103\nv_fma_f32 v234, v234, v215, v214\nM 20,180,212\n"
"buffer_load_dwordx4 v220, s[12:15], s63 offen lds\nv_fma_f32 v235, v235, v215, v214\nM 4,176,192\nR 174,191,12288\nG 7\nv_mfma_f32_16x16x32_bf16 v[4:7], v[240:243], v[208:211], v[4:7]\n"
"v_fma_f32 v236, v236, v215, v214\nv_fma_f32 v237, v237, v215, v214\nM 4,180,208\nG 4\nv_mul_f32 v234, v100, v234\nv_mul_f32 v235, v101, v235\n"
"v_mul_f32 v236, v102, v236\nM 0,160,224\nR 176,188,16384\nv_mfma_f32_16x16x32_bf16 v[16:19], v[240:243], v[210:213], v[16:19]\nv_mul_f32 v237, v103, v237\nv_exp_f32 v234, v234\n"
"M 0,164,240\nbuffer_load_dwordx4 v221, s[12:15], s63 offen offset:1024 lds\nv_exp_f32 v235, v235\nM 16,160,228\nR 178,188,20480\nv_mfma_f32_16x16x32_bf16 v[20:23], v[238:241], v[210:213], v[20:23]\n"
"s_add_u32 m0, s50, 32768\ns_add_u32 s63, s54, 2064640\nv_exp_f32 v236, v236\nM 16,164,244\nbuffer_load_dwordx4 v220, s[12:15], s63 offen lds\nv_exp_f32 v237, v237\n"
"M 8,160,232\nR 180,188,24576\nv_fma_f32 v234, v234, v216, v216\nv_fma_f32 v235, v235, v216, v216\nM 8,164,248\nv_rcp_f32 v234, v234\n"
"v_fma_f32 v236, v236, v216, v216\nM 24,160,236\nR 182,188,28672\nv_fma_f32 v237, v237, v216, v216\nv_rcp_f32 v235, v235\nM 24,164,252\n"
"G 4\nv_rcp_f32 v236, v236\nv_rcp_f32 v237, v237\nM 28,168,236\nR 160,189,16384\nv_mul_f32 v234, v100, v234\n"
"v_mul_f32 v235, v101, v235\nM 28,172,252\nbuffer_load_dwordx4 v221, s[12:15], s63 offen offset:1024 lds\nv_mul_f32 v236, v102, v236\nM 12,168,232\nR 162,189,20480\n"
"s_add_u32 s63, s54, 2294016\nv_mul_f32 v237, v103, v237\nM 12,172,248\ns_add_u32 m0, s50, 40960\nv_cvt_pk_bf16_f32 v98, v234, v235\nv_cvt_pk_bf16_f32 v99, v236, v237\n"
"v_mul_f32 v230, v104, v104\nM 20,168,228\nR 164,189,24576\nv_mul_f32 v231, v105, v105\nv_mul_f32 v232, v106, v106\nM 20,172,244\n"
"buffer_load_dwordx4 v220, s[12:15], s63 offen lds\nv_mul_f32 v233, v107, v107\nM 4,168,224\nR 166,189,28672\nv_fma_f32 v230, v230, v215, v214\nv_fma_f32 v231, v231, v215, v214\n"
"M 4,172,240\ns_waitcnt vmcnt(17)\ns_barrier\nG 4\nv_fma_f32 v232, v232, v215, v214\nv_mfma_f32_16x16x32_bf16 v[32:35], v[176:179], a[0:3], 0\n"
"R 168,190,16384\nv_fma_f32 v233, v233, v215, v214\nv_mul_f32 v230, v104, v230\nM 32,180,16\ns_mov_b32 m0, s51\nv_exp_f32 v230, v230\n"
"v_mul_f32 v231, v105, v231\nv_mfma_f32_16x16x32_bf16 v[48:51], v[176:179], a[4:7], 0\nR 170,190,20480\nds_write_addtid_b32 v8 offset:0\nv_mul_f32 v232, v106, v232\nv_mul_f32 v233, v107, v233\n"
"M 48,180,20\nbuffer_store_dwordx4 v[96:99], v252, s[20:23], s84 offen offset:0 sc1\nds_write_addtid_b32 v9 offset:256\nv_exp_f32 v231, v231\nv_mfma_f32_16x16x32_bf16 v[64:67], v[176:179], a[8:11], 0\nR 172,190,24576\n"
"ds_write_addtid_b32 v10 offset:512\nv_mov_b32 v226, s46\nv_exp_f32 v232, v232\nM 64,180,24\nds_write_addtid_b32 v11 offset:768\nv_mov_b32 v227, s47\n"
"v_fma_f32 v230, v230, v216, v216\nv_exp_f32 v233, v233\nv_mfma_f32_16x16x32_bf16 v[80:83], v[176:179], a[12:15], 0\nR 174,190,28672\ns_add_u32 s61, s81, 845824\ns_lshl_b32 s62, s44, 5\n"
"s_add_u32 s61, s61, s62\ns_waitcnt vmcnt(9)\ns_mov_b64 exec, 1\nbuffer_atomic_swap_x2 v[226:227], off, s[20:23], s61 sc1\ns_mov_b64 exec, -1\nds_write_addtid_b32 v12 offset:1024\n"
"v_fma_f32 v231, v231, v216, v216\nM 80,180,28\nds_write_addtid_b32 v13 offset:1280\nG 10\nv_rcp_f32 v230, v230\nv_fma_f32 v232, v232, v216, v216\n"
"v_mfma_f32_16x16x32_bf16 v[84:87], v[160:163], a[12:15], 0\nR 176,191,16384\nds_write_addtid_b32 v14 offset:1536\nv_fma_f32 v233, v233, v216, v216\nv_rcp_f32 v231, v231\nM 84,164,28\n"
"ds_write_addtid_b32 v15 offset:1792\nv_rcp_f32 v232, v232\nv_rcp_f32 v233, v233\nv_mfma_f32_16x16x32_bf16 v[68:71], v[160:163], a[8:11], 0\nR 178,191,20480\nds_write_addtid_b32 v24 offset:4096\n"
"v_mul_f32 v230, v104, v230\nv_mul_f32 v231, v105, v231\nM 68,164,24\nds_write_addtid_b32 v25 offset:4352\nv_mul_f32 v232, v106, v232\nv_mul_f32 v233, v107, v233\n"
"v_cvt_pk_bf16_f32 v104, v230, v231\nv_mfma_f32_16x16x32_bf16 v[52:55], v[160:163], a[4:7], 0\nR 180,191,24576\nds_write_addtid_b32 v26 offset:4608\nv_cvt_pk_bf16_f32 v105, v232, v233\nv_mul_f32 v234, v108, v108\n"
"M 52,164,20\nds_write_addtid_b32 v27 offset:4864\nds_write_addtid_b32 v28 offset:5120\nds_write_addtid_b32 v29 offset:5376\nds_write_addtid_b32 v30 offset:5632\nds_write_addtid_b32 v31 offset:5888\n"
"v_mul_f32 v235, v109, v109\nv_mul_f32 v236, v110, v110\nv_mul_f32 v237, v111, v111\nv_mfma_f32_16x16x32_bf16 v[36:39], v[160:163], a[0:3], 0\nR 182,191,28672\ns_add_u32 m0, s50, 40960\n"
"v_fma_f32 v234, v234, v215, v214\nM 36,164,16\nG 15\nv_fma_f32 v235, v235, v215, v214\nv_fma_f32 v236, v236, v215, v214\nv_fma_f32 v237, v237, v215, v214\n"
"v_mfma_f32_16x16x32_bf16 v[40:43], v[168:171], a[0:3], 0\nR 160,188,32768\ns_waitcnt vmcnt(9) lgkmcnt(2)\ns_barrier\nv_mul_f32 v234, v108, v234\nM 40,172,16\n"
"buffer_load_dwordx4 v221, s[12:15], s63 offen offset:1024 lds\nds_read2st64_b32 v[8:9], v225 offset0:0 offset1:1\nv_mul_f32 v235, v109, v235\nv_mfma_f32_16x16x32_bf16 v[56:59], v[168:171], a[4:7], 0\nR 162,188,36864\nds_read2st64_b32 v[10:11], v225 offset0:2 offset1:3\n"
"v_mul_f32 v236, v110, v236\nv_mul_f32 v237, v111, v237\nM 56,172,20\ns_add_u32 m0, s50, 49152\nds_read2st64_b32 v[12:13], v225 offset0:4 offset1:5\nv_exp_f32 v234, v234\n"
"v_exp_f32 v235, v235\nv_mfma_f32_16x16x32_bf16 v[72:75], v[168:171], a[8:11], 0\nR 164,188,40960\nds_read2st64_b32 v[14:15], v225 offset0:6 offset1:7\nv_fma_f32 v234, v234, v216, v216\nv_exp_f32 v236, v236\n"
"M 72,172,24\ns_add_u32 s63, s54, 2523392\nds_read2st64_b32 v[24:25], v225 offset0:16 offset1:17\nv_exp_f32 v237, v237\nv_fma_f32 v235, v235, v216, v216\nv_mfma_f32_16x16x32_bf16 v[88:91], v[168:171], a[12:15], 0\n"
"R 166,188,45056\nds_read2st64_b32 v[26:27], v225 offset0:18 offset1:19\nv_fma_f32 v236, v236, v216, v216\nv_fma_f32 v237, v237, v216, v216\nM 88,172,28\nds_read2st64_b32 v[28:29], v225 offset0:20 offset1:21\n"
"G 11\nv_rcp_f32 v234, v234\nv_rcp_f32 v235, v235\nv_mfma_f32_16x16x32_bf16 v[92:95], v[176:179], a[12:15], 0\nR 168,189,32768\nds_read2st64_b32 v[30:31], v225 offset0:22 offset1:23\n"
"v_mul_f32 v234, v108, v234\nv_rcp_f32 v236, v236\nM 92,180,28\nbuffer_load_dwordx4 v220, s[12:15], s63 offen lds\nG 11\nv_add_f32 v0, v0, v8\n"
"v_rcp_f32 v237, v237\nv_mfma_f32_16x16x32_bf16 v[76:79], v[176:179], a[8:11], 0\nR 170,189,36864\nv_mul_f32 v235, v109, v235\nv_add_f32 v1, v1, v9\nv_mul_f32 v236, v110, v236\n"
"M 76,180,24\nv_mul_f32 v237, v111, v237\nv_cvt_pk_bf16_f32 v106, v234, v235\nG 10\nv_add_f32 v2, v2, v10\nv_cvt_pk_bf16_f32 v107, v236, v237\n"
"v_mfma_f32_16x16x32_bf16 v[60:63], v[176:179], a[4:7], 0\nR 172,189,40960\nv_mul_f32 v230, v112, v112\nv_add_f32 v3, v3, v11\nv_mul_f32 v231, v113, v113\nM 60,180,20\n"
"buffer_store_dwordx4 v[104:107], v252, s[20:23], s84 offen offset:1024 sc1\nG 10\nv_add_f32 v4, v4, v12\nv_mul_f32 v232, v114, v114\nv_mfma_f32_16x16x32_bf16 v[44:47], v[176:179], a[0:3], 0\nR 174,189,45056\n"
"v_mul_f32 v233, v115, v115\nv_add_f32 v5, v5, v13\nv_fma_f32 v230, v230, v218, v217\nM 44,180,16\nG 7\nv_fma_f32 v231, v231, v218, v217\n"
"v_fma_f32 v232, v232, v218, v217\nv_add_f32 v6, v6, v14\nv_fma_f32 v233, v233, v218, v217\nM 32,160,32\nR 176,190,32768\nv_mov_b32 v248, 0\n"
"v_add_f32 v7, v7, v15\nv_mul_f32 v230, v112, v230\nM 32,164,48\nv_mov_b32 v249, 0\nv_mul_f32 v231, v113, v231\nv_add_f32 v16, v16, v24\n"
"v_mul_f32 v232, v114, v232\nM 48,160,36\nR 178,190,36864\ns_add_u32 s61, s82, 845824\ns_mov_b64 exec, 0xf\nbuffer_atomic_or_x2 v[248:249], v250, s[20:23], s61 offen sc0 sc1\n"
"s_mov_b64 exec, -1\nv_add_f32 v17, v17, v25\nv_mul_f32 v233, v115, v233\nM 48,164,52\nv_exp_f32 v230, v230\nG 8\n"
"v_add_f32 v18, v18, v26\nv_exp_f32 v231, v231\nM 64,160,40\nR 180,190,40960\nv_add_f32 v19, v19, v27\nG 8\n"
"v_add_f32 v20, v20, v28\nv_add_f32 v21, v21, v29\nG 6\nv_add_f32 v22, v22, v30\nv_add_f32 v23, v23, v31\nM 64,164,56\n"
"buffer_load_dwordx4 v221, s[12:15], s63 offen offset:1024 lds\nM 80,160,44\nR 182,190,45056\nv_fma_f32 v230, v230, v219, v219\nM 80,164,60\nG 4\n"
"v_exp_f32 v232, v232\nM 84,168,44\nR 160,191,32768\nv_fma_f32 v231, v231, v219, v219\nM 84,172,60\ns_add_u32 m0, s50, 57344\n"
"v_exp_f32 v233, v233\nM 68,168,40\nR 162,191,36864\nv_fma_f32 v232, v232, v219, v219\nM 68,172,56\ns_add_u32 s63, s54, 2752768\n"
"v_rcp_f32 v230, v230\nM 52,168,36\nR 164,191,40960\nv_fma_f32 v233, v233, v219, v219\nM 52,172,52\nbuffer_load_dwordx4 v220, s[12:15], s63 offen lds\n"
"M 36,168,32\nR 166,191,45056\nv_mul_f32 v230, v112, v230\nM 36,172,48\nG 4\nv_rcp_f32 v231, v231\n"
"M 40,176,32\nR 168,188,49152\nv_mul_f32 v231, v113, v231\nM 40,180,48\nv_rcp_f32 v232, v232\nM 56,176,36\n"
"R 170,188,53248\nv_mul_f32 v232, v114, v232\nM 56,180,52\nbuffer_load_dwordx4 v221, s[12:15], s63 offen offset:1024 lds\nM 72,176,40\nR 172,184,0\n"
"s_add_u32 s63, s54, 2982144\nM 72,180,56\ns_add_u32 m0, s50, 65536\nv_rcp_f32 v233, v233\nM 88,176,44\nR 174,184,4096\n"
"v_mul_f32 v233, v115, v233\nM 88,180,60\nG 4\nv_cvt_pk_bf16_f32 v112, v230, v231\nv_cvt_pk_bf16_f32 v113, v232, v233\nM 92,160,44\n"
"R 176,189,49152\nv_mul_f32 v234, v116, v116\nM 92,164,60\nbuffer_load_dwordx4 v220, s[12:15], s63 offen lds\nM 76,160,40\nR 178,189,53248\n"
"v_mul_f32 v235, v117, v117\nM 76,164,56\nv_mul_f32 v236, v118, v118\nv_mul_f32 v237, v119, v119\nM 60,160,36\nR 180,185,0\n"
"v_fma_f32 v234, v234, v218, v217\nM 60,164,52\nv_fma_f32 v235, v235, v218, v217\nv_fma_f32 v236, v236, v218, v217\nM 44,160,32\nR 182,185,4096\n"
"v_fma_f32 v237, v237, v218, v217\nM 44,164,48\nG 4\nv_mul_f32 v234, v116, v234\nv_mul_f32 v235, v117, v235\nM 32,168,64\n"
"R 160,190,49152\nv_mul_f32 v236, v118, v236\nM 32,172,80\nbuffer_load_dwordx4 v221, s[12:15], s63 offen offset:1024 lds\nM 48,168,68\nR 162,190,53248\n"
"v_mul_f32 v237, v119, v237\nM 48,172,84\ns_add_u32 m0, s50, 73728\nv_exp_f32 v234, v234\nM 64,168,72\nR 164,186,0\n"
"v_fma_f32 v234, v234, v219, v219\nM 64,172,88\ns_add_u32 s63, s54, 3211520\nv_exp_f32 v235, v235\nM 80,168,76\nR 166,186,4096\n"
"v_fma_f32 v235, v235, v219, v219\nM 80,172,92\nG 4\nv_exp_f32 v236, v236\nM 84,176,76\nR 168,191,49152\n"
"v_fma_f32 v236, v236, v219, v219\nM 84,180,92\nbuffer_load_dwordx4 v220, s[12:15], s63 offen lds\nM 68,176,72\nR 170,191,53248\nv_mul_f32 v230, v120, v120\n"
"M 68,180,88\nv_exp_f32 v237, v237\nM 52,176,68\nR 172,187,0\nv_fma_f32 v237, v237, v219, v219\nM 52,180,84\n"
"s_add_u32 s64, s68, 37748736\nv_rcp_f32 v234, v234\nM 36,176,64\nR 174,187,4096\nv_mul_f32 v234, v116, v234\nM 36,180,80\n"
"G 4\nv_rcp_f32 v235, v235\nM 40,160,64\nR 176,184,8192\ns_waitcnt vmcnt(13)\ns_barrier\n"
"s_waitcnt vmcnt(6)\nv_cmp_eq_u64 s[86:87], s[46:47], v[248:249]\nM 40,164,80\ns_add_u32 s65, s68, 37863424\nv_rcp_f32 v236, v236\nM 56,160,68\n"
"R 178,184,12288\ns_and_b32 s62, s86, 0xf\ns_cmp_eq_u32 s62, 0xf\ns_cbranch_scc0 Lq838\nLq839:\nM 56,164,84\n"
"buffer_load_dwordx4 v221, s[12:15], s63 offen offset:1024 lds\nM 72,160,72\nR 180,184,16384\nv_mul_f32 v235, v117, v235\nM 72,164,88\nbuffer_load_dwordx4 a[0:3], v252, s[20:23], s64 offen sc1\n"
"M 88,160,76\nR 182,184,20480\nv_mul_f32 v236, v118, v236\nM 88,164,92\nG 4\nv_rcp_f32 v237, v237\n"
"M 92,168,76\nR 160,185,8192\nv_mul_f32 v237, v119, v237\nM 92,172,92\nbuffer_load_dwordx4 a[4:7], v252, s[20:23], s65 offen sc1\nM 76,168,72\n"
"R 162,185,12288\nv_cvt_pk_bf16_f32 v114, v234, v235\nM 76,172,88\ns_add_u32 s66, s69, 37748736\nv_cvt_pk_bf16_f32 v115, v236, v237\nv_mul_f32 v231, v121, v121\n"
"M 60,168,68\nR 164,185,16384\nv_mul_f32 v232, v122, v122\nM 60,172,84\ns_add_u32 s67, s69, 37863424\nv_mul_f32 v233, v123, v123\n"
"v_fma_f32 v230, v230, v218, v217\nM 44,168,64\nR 166,185,20480\nv_fma_f32 v231, v231, v218, v217\nM 44,172,80\nG 4\n"
"v_fma_f32 v232, v232, v218, v217\nv_fma_f32 v233, v233, v218, v217\nM 32,176,96\nR 168,186,8192\nv_mul_f32 v230, v120, v230\nM 32,180,112\n"
"buffer_load_dwordx4 a[8:11], v252, s[20:23], s66 offen sc1\nM 48,176,100\nR 170,186,12288\nv_mul_f32 v231, v121, v231\nM 48,180,116\nbuffer_load_dwordx4 a[12:15], v252, s[20:23], s67 offen sc1\n"
"M 64,176,104\nR 172,186,16384\nv_mul_f32 v232, v122, v232\nM 64,180,120\ns_add_u32 s64, s68, 37755904\nv_exp_f32 v230, v230\n"
"M 80,176,108\nR 174,186,20480\nv_mul_f32 v233, v123, v233\nM 80,180,124\nG 4\nv_exp_f32 v231, v231\n"
"M 84,160,108\nR 176,187,8192\nv_fma_f32 v230, v230, v219, v219\nM 84,164,124\nbuffer_load_dwordx4 a[16:19], v252, s[20:23], s64 offen sc1\nM 68,160,104\n"
"R 178,187,12288\nv_fma_f32 v231, v231, v219, v219\nM 68,164,120\ns_add_u32 s65, s68, 37870592\nv_exp_f32 v232, v232\nM 52,160,100\n"
"R 180,187,16384\nv_fma_f32 v232, v232, v219, v219\nM 52,164,116\ns_add_u32 s66, s69, 37755904\nv_exp_f32 v233, v233\nM 36,160,96\n"
"R 182,187,20480\nv_fma_f32 v233, v233, v219, v219\nM 36,164,112\nG 4\nv_rcp_f32 v230, v230\nM 40,168,96\n"
"R 160,184,24576\nv_mul_f32 v230, v120, v230\nM 40,172,112\nbuffer_load_dwordx4 a[20:23], v252, s[20:23], s65 offen sc1\nM 56,168,100\nR 162,184,28672\n"
"v_mul_f32 v234, v124, v124\nM 56,172,116\nbuffer_load_dwordx4 a[24:27], v252, s[20:23], s66 offen sc1\nM 72,168,104\nR 164,184,32768\nLpg28:\n"
"s_cbranch_execz LretO1n\nv_mul_f32 v235, v125, v125\nM 72,172,120\ns_add_u32 s67, s69, 37870592\nv_rcp_f32 v231, v231\nM 88,168,108\n"
"R 166,184,36864\nv_mul_f32 v231, v121, v231\nM 88,172,124\nG 4\nv_rcp_f32 v232, v232\nM 92,176,108\n"
"R 168,185,24576\nv_mul_f32 v232, v122, v232\nM 92,180,124\nbuffer_load_dwordx4 a[28:31], v252, s[20:23], s67 offen sc1\nM 76,176,104\nR 170,185,28672\n"
"v_cvt_pk_bf16_f32 v120, v230, v231\nM 76,180,120\ns_add_u32 s64, s68, 37749760\nv_rcp_f32 v233, v233\nM 60,176,100\nR 172,185,32768\n"
"v_mul_f32 v233, v123, v233\nM 60,180,116\ns_add_u32 s65, s68, 37864448\nv_cvt_pk_bf16_f32 v121, v232, v233\nv_mul_f32 v236, v126, v126\nM 44,176,96\n"
"R 174,185,36864\nv_mul_f32 v237, v127, v127\nM 44,180,112\nG 4\nv_fma_f32 v234, v234, v218, v217\nv_fma_f32 v235, v235, v218, v217\n"
"M 32,160,128\nR 176,186,24576\ns_add_u32 m0, s50, 81920\nM 32,164,144\nbuffer_load_dwordx4 a[32:35], v252, s[20:23], s64 offen sc1\nM 48,160,132\n"
"R 178,186,28672\ns_add_u32 s63, s54, 3440896\nM 48,164,148\nbuffer_load_dwordx4 a[36:39], v252, s[20:23], s65 offen sc1\nM 64,160,136\nR 180,186,32768\n"
"v_fma_f32 v236, v236, v218, v217\nM 64,164,152\ns_add_u32 s66, s69, 37749760\nbuffer_load_dwordx4 v220, s[12:15], s63 offen lds\nM 80,160,140\nR 182,186,36864\n"
"v_fma_f32 v237, v237, v218, v217\nM 80,164,156\nG 4\nv_mul_f32 v234, v124, v234\nv_mul_f32 v235, v125, v235\nM 84,168,140\n"
"R 160,187,24576\nv_mul_f32 v236, v126, v236\nM 84,172,156\nbuffer_load_dwordx4 a[40:43], v252, s[20:23], s66 offen sc1\nbuffer_load_dwordx4 v221, s[12:15], s63 offen offset:1024 lds\nM 68,168,136\n"
"R 162,187,28672\nv_mul_f32 v237, v127, v237\nM 68,172,152\ns_add_u32 s67, s69, 37864448\nv_exp_f32 v234, v234\nM 52,168,132\n"
"R 164,187,32768\ns_waitcnt vmcnt(15)\ns_barrier\nM 52,172,148\nbuffer_load_dwordx4 a[44:47], v252, s[20:23], s67 offen sc1\ns_movk_i32 s63, 2048\n"
"v_mad_u32_u24 v220, v228, s63, v229\nv_add_u32 v221, 916480, v220\ns_add_u32 m0, s50, 90112\ns_add_u32 s63, s56, 0\nbuffer_load_dwordx4 v220, s[16:19], s63 offen lds\nM 36,168,128\n"
"R 166,187,36864\nv_fma_f32 v234, v234, v219, v219\nM 36,172,144\nG 4\nv_exp_f32 v235, v235\nM 40,176,128\n"
"R 168,184,40960\nv_fma_f32 v235, v235, v219, v219\nM 40,180,144\ns_add_u32 s64, s68, 37756928\nbuffer_load_dwordx4 v221, s[16:19], s63 offen offset:1024 lds\nM 56,176,132\n"
"R 170,184,45056\ns_add_u32 m0, s50, 98304\nM 56,180,148\nbuffer_load_dwordx4 a[48:51], v252, s[20:23], s64 offen sc1\nM 72,176,136\nR 172,184,49152\n"
"s_add_u32 s63, s56, 524288\nM 72,180,152\ns_add_u32 s65, s68, 37871616\nbuffer_load_dwordx4 v220, s[16:19], s63 offen lds\nM 88,176,140\nR 174,184,53248\n"
"v_exp_f32 v236, v236\nM 88,180,156\nG 4\nv_exp_f32 v237, v237\nM 92,160,140\nR 176,185,40960\n"
"v_fma_f32 v236, v236, v219, v219\nM 92,164,156\nbuffer_load_dwordx4 a[52:55], v252, s[20:23], s65 offen sc1\nbuffer_load_dwordx4 v221, s[16:19], s63 offen offset:1024 lds\nM 76,160,136\nR 178,185,45056\n"
"v_fma_f32 v237, v237, v219, v219\nM 76,164,152\ns_add_u32 s66, s69, 37756928\nv_rcp_f32 v234, v234\nM 60,160,132\nR 180,185,49152\n"
"v_mul_f32 v234, v124, v234\nM 60,164,148\nbuffer_load_dwordx4 a[56:59], v252, s[20:23], s66 offen sc1\nM 44,160,128\nR 182,185,53248\nM 44,164,144\n"
"G 4\nv_rcp_f32 v235, v235\nM 32,168,160\nR 160,186,40960\nv_mul_f32 v235, v125, v235\nM 32,172,176\n"
"s_add_u32 s67, s69, 37871616\nv_rcp_f32 v236, v236\nM 48,168,164\nR 162,186,45056\nv_mul_f32 v236, v126, v236\nM 48,172,180\n"
"buffer_load_dwordx4 a[60:63], v252, s[20:23], s67 offen sc1\nM 64,168,168\nR 164,186,49152\nv_cvt_pk_bf16_f32 v122, v234, v235\nM 64,172,184\nbuffer_store_dwordx4 v[112:115], v252, s[20:23], s85 offen offset:0 sc1\n"
"M 80,168,172\nR 166,186,53248\nv_rcp_f32 v237, v237\nM 80,172,188\nG 4\nv_mul_f32 v237, v127, v237\n"
"v_cvt_pk_bf16_f32 v123, v236, v237\nM 84,176,172\nR 168,187,40960\nM 84,180,188\ns_add_u32 m0, s50, 106496\nbuffer_store_dwordx4 v[120:123], v252, s[20:23], s85 offen offset:1024 sc1\n"
"M 68,176,168\nR 170,187,45056\nv_mul_f32 v230, v0, v0\nv_mul_f32 v231, v1, v1\nM 68,180,184\ns_add_u32 s63, s56, 65536\n"
"s_add_u32 s84, s73, 37754880\ns_add_u32 s85, s73, 37869568\nv_mul_f32 v232, v2, v2\nM 52,176,164\nR 172,187,49152\nv_mul_f32 v233, v3, v3\n"
"v_fma_f32 v230, v230, v215, v214\nM 52,180,180\nbuffer_load_dwordx4 v220, s[16:19], s63 offen lds\nv_fma_f32 v231, v231, v215, v214\nM 36,176,160\nR 174,187,53248\n"
"v_fma_f32 v232, v232, v215, v214\nv_fma_f32 v233, v233, v215, v214\nM 36,180,176\nG 4\nv_mul_f32 v230, v0, v230\nv_mul_f32 v231, v1, v231\n"
"v_mul_f32 v232, v2, v232\nM 40,160,160\nR 176,184,57344\nv_mul_f32 v233, v3, v233\nv_exp_f32 v230, v230\nM 40,164,176\n"
"buffer_load_dwordx4 v221, s[16:19], s63 offen offset:1024 lds\nv_exp_f32 v231, v231\nM 56,160,164\nR 178,184,61440\nv_fma_f32 v230, v230, v216, v216\nv_exp_f32 v232, v232\n"
"M 56,164,180\ns_add_u32 m0, s50, 114688\nv_exp_f32 v233, v233\nv_fma_f32 v231, v231, v216, v216\nM 72,160,168\nR 180,188,0\n"
"v_fma_f32 v232, v232, v216, v216\nv_fma_f32 v233, v233, v216, v216\nM 72,164,184\ns_add_u32 s63, s56, 589824\nv_rcp_f32 v230, v230\nv_rcp_f32 v231, v231\n"
"M 88,160,172\nR 182,188,4096\nv_mul_f32 v230, v0, v230\nv_rcp_f32 v232, v232\nM 88,164,188\nG 4\n"
"v_rcp_f32 v233, v233\nv_mul_f32 v231, v1, v231\nM 92,168,172\nR 160,185,57344\nv_mul_f32 v232, v2, v232\nv_mul_f32 v233, v3, v233\n"
"M 92,172,188\nbuffer_load_dwordx4 v220, s[16:19], s63 offen lds\nv_cvt_pk_bf16_f32 v0, v230, v231\nM 76,168,168\nR 162,185,61440\nv_cvt_pk_bf16_f32 v1, v232, v233\n"
"v_mul_f32 v234, v4, v4\nM 76,172,184\nv_mul_f32 v235, v5, v5\nv_mul_f32 v236, v6, v6\nv_mul_f32 v237, v7, v7\nM 60,168,164\n"
"R 164,189,0\nv_fma_f32 v234, v234, v215, v214\nv_fma_f32 v235, v235, v215, v214\nM 60,172,180\nbuffer_load_dwordx4 v221, s[16:19], s63 offen offset:1024 lds\nv_fma_f32 v236, v236, v215, v214\n"
"M 44,168,160\nR 166,189,4096\ns_add_u32 m0, s50, 0\nv_fma_f32 v237, v237, v215, v214\nM 44,172,176\nG 4\n"
"v_mul_f32 v234, v4, v234\nv_mul_f32 v235, v5, v235\nv_mul_f32 v236, v6, v236\nM 32,176,192\nR 168,186,57344\ns_add_u32 s63, s57, 0\n"
"v_mul_f32 v237, v7, v237\nM 32,180,208\nbuffer_load_dwordx4 v220, s[16:19], s63 offen lds\nv_exp_f32 v234, v234\nM 48,176,196\nR 170,186,61440\n"
"v_fma_f32 v234, v234, v216, v216\nv_exp_f32 v235, v235\nM 48,180,212\nv_exp_f32 v236, v236\nv_exp_f32 v237, v237\nM 64,176,200\n"
"R 172,190,0\nv_fma_f32 v235, v235, v216, v216\nv_fma_f32 v236, v236, v216, v216\nM 64,180,216\nbuffer_load_dwordx4 v221, s[16:19], s63 offen offset:1024 lds\nv_fma_f32 v237, v237, v216, v216\n"
"M 80,176,204\nR 174,190,4096\ns_add_u32 s63, s57, 524288\nv_rcp_f32 v234, v234\nM 80,180,220\nG 4\n"
"v_rcp_f32 v235, v235\nv_rcp_f32 v236, v236\nM 84,160,204\nR 176,187,57344\ns_waitcnt vmcnt(10)\ns_barrier\n"
"v_rcp_f32 v237, v237\nM 84,164,220\ns_add_u32 m0, s50, 8192\nv_mul_f32 v234, v4, v234\nv_mul_f32 v235, v5, v235\nv_mul_f32 v236, v6, v236\n"
"M 68,160,200\nR 178,187,61440\nv_mul_f32 v237, v7, v237\nv_cvt_pk_bf16_f32 v2, v234, v235\nM 68,164,216\nbuffer_load_dwordx4 v220, s[16:19], s63 offen lds\n"
"v_cvt_pk_bf16_f32 v3, v236, v237\nM 52,160,196\nR 180,191,0\nv_mul_f32 v230, v16, v16\nv_mul_f32 v231, v17, v17\nM 52,164,212\n"
"v_mul_f32 v232, v18, v18\nv_mul_f32 v233, v19, v19\nv_fma_f32 v230, v230, v218, v217\nM 36,160,192\nR 182,191,4096\nv_fma_f32 v231, v231, v218, v217\n"
"v_fma_f32 v232, v232, v218, v217\nM 36,164,208\nG 4\nv_fma_f32 v233, v233, v218, v217\nv_mul_f32 v230, v16, v230\nv_mul_f32 v231, v17, v231\n"
"M 40,168,192\nR 160,188,8192\nv_mul_f32 v232, v18, v232\nv_mul_f32 v233, v19, v233\nM 40,172,208\nbuffer_load_dwordx4 v221, s[16:19], s63 offen offset:1024 lds\n"
"v_exp_f32 v230, v230\nM 56,168,196\nR 162,188,12288\nv_fma_f32 v230, v230, v219, v219\nv_exp_f32 v231, v231\nM 56,172,212\n"
"buffer_store_dwordx4 v[0:3], v252, s[20:23], s84 offen offset:0 sc1\nv_exp_f32 v232, v232\nM 72,168,200\nR 164,188,16384\nv_fma_f32 v231, v231, v219, v219\nv_exp_f32 v233, v233\n"
"M 72,172,216\ns_add_u32 m0, s50, 16384\nv_rcp_f32 v230, v230\nv_fma_f32 v232, v232, v219, v219\nM 88,168,204\nR 166,188,20480\n"
"v_mov_b32 v226, s46\nv_fma_f32 v233, v233, v219, v219\nM 88,172,220\nG 4\nv_mov_b32 v227, s47\nv_mul_f32 v230, v16, v230\n"
"v_rcp_f32 v231, v231\nM 92,176,204\nR 168,189,8192\ns_add_u32 s61, s81, 845832\ns_lshl_b32 s62, s44, 5\ns_add_u32 s61, s61, s62\n"
"s_waitcnt vmcnt(9)\ns_mov_b64 exec, 1\nbuffer_atomic_swap_x2 v[226:227], off, s[20:23], s61 sc1\ns_mov_b64 exec, -1\nv_rcp_f32 v232, v232\nM 92,180,220\n"
"s_add_u32 s63, s57, 65536\nv_rcp_f32 v233, v233\nv_mul_f32 v231, v17, v231\nM 76,176,200\nR 170,189,12288\nv_mul_f32 v232, v18, v232\n"
"v_mul_f32 v233, v19, v233\nM 76,180,216\nbuffer_load_dwordx4 v220, s[16:19], s63 offen lds\nv_cvt_pk_bf16_f32 v16, v230, v231\nM 60,176,196\nR 172,189,16384\n"
"v_cvt_pk_bf16_f32 v17, v232, v233\nv_mul_f32 v234, v20, v20\nM 60,180,212\nv_mul_f32 v235, v21, v21\nv_mul_f32 v236, v22, v22\nv_mul_f32 v237, v23, v23\n"
"M 44,176,192\nR 174,189,20480\ns_mov_b32 s60, 158208\nv_bfe_u32 v246, v252, 4, 4\nv_lshl_add_u32 v246, v246, 3, s60\nv_fma_f32 v234, v234, v218, v217\n"
"M 44,180,208\nds_read_b64 v[238:239], v246 offset:0\nG 5\nv_fma_f32 v235, v235, v218, v217\nM 32,160,224\nR 176,190,8192\n"
"ds_read_b64 v[240:241], v246 offset:128\nv_fma_f32 v236, v236, v218, v217\nM 32,164,240\nds_read_b64 v[242:243], v246 offset:256\nv_fma_f32 v237, v237, v218, v217\nv_mul_f32 v234, v20, v234\n"
"M 48,160,228\nR 178,190,12288\nds_read_b64 v[244:245], v246 offset:384\nv_mul_f32 v235, v21, v235\nM 48,164,244\nds_read_b64 v[246:247], v246 offset:0\n"
"v_mul_f32 v236, v22, v236\nv_mul_f32 v237, v23, v237\nM 64,160,232\nR 180,190,16384\nv_exp_f32 v234, v234\nM 64,164,248\n"
"buffer_load_dwordx4 v221, s[16:19], s63 offen offset:1024 lds\nv_exp_f32 v235, v235\nM 80,160,236\nR 182,190,20480\ns_add_u32 m0, s50, 24576\nv_exp_f32 v236, v236\n"
"M 80,164,252\nG 9\nv_exp_f32 v237, v237\nv_fma_f32 v234, v234, v219, v219\nM 84,168,236\nR 160,191,8192\n"
"s_add_u32 s63, s57, 589824\nv_fma_f32 v235, v235, v219, v219\nM 84,172,252\nbuffer_load_dwordx4 v220, s[16:19], s63 offen lds\nv_fma_f32 v236, v236, v219, v219\nM 68,168,232\n"
"R 162,191,12288\nv_fma_f32 v237, v237, v219, v219\nv_rcp_f32 v234, v234\nM 68,172,248\nv_rcp_f32 v235, v235\nv_rcp_f32 v236, v236\n"
"M 52,168,228\nR 164,191,16384\nG 9\nv_mfma_f32_16x16x32_bf16 v[32:35], v[238:241], v[208:211], v[32:35]\nv_mul_f32 v234, v20, v234\nv_rcp_f32 v237, v237\n"
"M 52,172,244\nbuffer_load_dwordx4 v221, s[16:19], s63 offen offset:1024 lds\nv_mul_f32 v235, v21, v235\nM 36,168,224\nR 166,191,20480\nG 9\n"
"v_mfma_f32_16x16x32_bf16 v[36:39], v[240:243], v[208:211], v[36:39]\ns_add_u32 m0, s50, 32768\nv_mul_f32 v236, v22, v236\nM 36,172,240\nG 4\nv_mul_f32 v237, v23, v237\n"
"v_cvt_pk_bf16_f32 v18, v234, v235\nv_cvt_pk_bf16_f32 v19, v236, v237\nM 40,176,224\nR 168,188,24576\nv_mfma_f32_16x16x32_bf16 v[40:43], v[242:245], v[208:211], v[40:43]\ns_add_u32 s63, s56, 131072\n"
"buffer_store_dwordx4 v[16:19], v252, s[20:23], s85 offen offset:0 sc1\nM 40,180,240\nbuffer_load_dwordx4 v220, s[16:19], s63 offen lds\nM 56,176,228\nR 170,188,28672\nv_mfma_f32_16x16x32_bf16 v[44:47], v[244:247], v[208:211], v[44:47]\n"
"v_mov_b32 v248, 0\nM 56,180,244\nv_mov_b32 v249, 0\nM 72,176,232\nR 172,188,32768\nv_mfma_f32_16x16x32_bf16 v[48:51], v[244:247], v[210:213], v[48:51]\n"
"s_add_u32 s61, s82, 845832\ns_mov_b64 exec, 0xf\nbuffer_atomic_or_x2 v[248:249], v250, s[20:23], s61 offen sc0 sc1\ns_mov_b64 exec, -1\nM 72,180,248\nM 88,176,236\n"
"R 174,188,36864\nv_mfma_f32_16x16x32_bf16 v[52:55], v[238:241], v[210:213], v[52:55]\nM 88,180,252\nG 4\nM 92,160,236\nR 176,189,24576\n"
"v_mfma_f32_16x16x32_bf16 v[56:59], v[240:243], v[210:213], v[56:59]\nM 92,164,252\nbuffer_load_dwordx4 v221, s[16:19], s63 offen offset:1024 lds\nM 76,160,232\nR 178,189,28672\nv_mfma_f32_16x16x32_bf16 v[60:63], v[242:245], v[210:213], v[60:63]\n"
"s_add_u32 m0, s50, 40960\ns_add_u32 s63, s56, 655360\nM 76,164,248\nbuffer_load_dwordx4 v220, s[16:19], s63 offen lds\nM 60,160,228\nR 180,189,32768\n"
"M 60,164,244\nM 44,160,224\nR 182,189,36864\nM 44,164,240\ns_waitcnt vmcnt(15)\ns_barrier\n"
"G 4\nv_mfma_f32_16x16x32_bf16 v[96:99], v[168:171], a[0:3], 0\nR 160,190,24576\nM 96,172,16\ns_mov_b32 m0, s51\nv_mfma_f32_16x16x32_bf16 v[112:115], v[168:171], a[4:7], 0\n"
"R 162,190,28672\nM 112,172,20\nds_write_addtid_b32 v64 offset:0\nv_mfma_f32_16x16x32_bf16 v[128:131], v[168:171], a[8:11], 0\nR 164,190,32768\nM 128,172,24\n"
"ds_write_addtid_b32 v65 offset:256\nv_mfma_f32_16x16x32_bf16 v[144:147], v[168:171], a[12:15], 0\nR 166,190,36864\nds_write_addtid_b32 v66 offset:512\nM 144,172,28\nds_write_addtid_b32 v67 offset:768\n"
"ds_write_addtid_b32 v68 offset:1024\nG 9\nv_mfma_f32_16x16x32_bf16 v[148:151], v[176:179], a[12:15], 0\nR 168,191,24576\nds_write_addtid_b32 v69 offset:1280\nM 148,180,28\n"
"ds_write_addtid_b32 v70 offset:1536\nds_write_addtid_b32 v71 offset:1792\nv_mfma_f32_16x16x32_bf16 v[132:135], v[176:179], a[8:11], 0\nR 170,191,28672\nds_write_addtid_b32 v72 offset:2048\nM 132,180,24\n"
"ds_write_addtid_b32 v73 offset:2304\nds_write_addtid_b32 v74 offset:2560\nv_mfma_f32_16x16x32_bf16 v[116:119], v[176:179], a[4:7], 0\nR 172,191,32768\nds_write_addtid_b32 v75 offset:2816\nM 116,180,20\n"
"ds_write_addtid_b32 v76 offset:3072\nds_write_addtid_b32 v77 offset:3328\nv_mfma_f32_16x16x32_bf16 v[100:103], v[176:179], a[0:3], 0\nR 174,191,36864\nds_write_addtid_b32 v78 offset:3584\nM 100,180,16\n"
"ds_write_addtid_b32 v79 offset:3840\nds_write_addtid_b32 v80 offset:4096\nG 15\nv_mfma_f32_16x16x32_bf16 v[104:107], v[160:163], a[0:3], 0\nR 176,188,40960\nds_write_addtid_b32 v81 offset:4352\n"
"M 104,164,16\nds_write_addtid_b32 v82 offset:4608\nds_write_addtid_b32 v83 offset:4864\nv_mfma_f32_16x16x32_bf16 v[120:123], v[160:163], a[4:7], 0\nR 178,188,45056\nds_write_addtid_b32 v84 offset:5120\n"
"M 120,164,20\nds_write_addtid_b32 v85 offset:5376\nds_write_addtid_b32 v86 offset:5632\nv_mfma_f32_16x16x32_bf16 v[136:139], v[160:163], a[8:11], 0\nR 180,188,49152\nds_write_addtid_b32 v87 offset:5888\n"
"M 136,164,24\nds_write_addtid_b32 v88 offset:6144\nds_write_addtid_b32 v89 offset:6400\nv_mfma_f32_16x16x32_bf16 v[152:155], v[160:163], a[12:15], 0\nR 182,188,53248\nds_write_addtid_b32 v90 offset:6656\n"
"M 152,164,28\nds_write_addtid_b32 v91 offset:6912\nds_write_addtid_b32 v92 offset:7168\nG 15\nv_mfma_f32_16x16x32_bf16 v[156:159], v[168:171], a[12:15], 0\nR 160,189,40960\n"
"ds_write_addtid_b32 v93 offset:7424\nM 156,172,28\nds_write_addtid_b32 v94 offset:7680\nds_write_addtid_b32 v95 offset:7936\nv_mfma_f32_16x16x32_bf16 v[140:143], v[168:171], a[8:11], 0\nR 162,189,45056\n"
"s_add_u32 m0, s50, 40960\nM 140,172,24\nbuffer_load_dwordx4 v221, s[16:19], s63 offen offset:1024 lds\nv_mfma_f32_16x16x32_bf16 v[124:127], v[168:171], a[4:7], 0\nR 164,189,49152\ns_waitcnt vmcnt(6) lgkmcnt(2)\n"
"s_barrier\nM 124,172,20\nds_read2st64_b32 v[64:65], v225 offset0:0 offset1:1\nds_read2st64_b32 v[66:67], v225 offset0:2 offset1:3\nv_mfma_f32_16x16x32_bf16 v[108:111], v[168:171], a[0:3], 0\nR 166,189,53248\n"
"ds_read2st64_b32 v[68:69], v225 offset0:4 offset1:5\nM 108,172,16\nds_read2st64_b32 v[70:71], v225 offset0:6 offset1:7\nds_read2st64_b32 v[72:73], v225 offset0:8 offset1:9\nM 96,176,32\nR 168,190,40960\n"
"ds_read2st64_b32 v[74:75], v225 offset0:10 offset1:11\nLpg29:\ns_cbranch_execz LretO1n\nM 96,180,48\ns_add_u32 s64, s68, 37750784\nds_read2st64_b32 v[76:77], v225 offset0:12 offset1:13\n"
"v_mov_b32 v226, s46\nM 112,176,36\nR 170,190,45056\nds_read2st64_b32 v[78:79], v225 offset0:14 offset1:15\nv_mov_b32 v227, s47\ns_waitcnt vmcnt(3)\n"
"v_cmp_eq_u64 s[86:87], s[46:47], v[248:249]\nM 112,180,52\ns_add_u32 s61, s81, 845848\ns_lshl_b32 s62, s44, 5\ns_add_u32 s61, s61, s62\ns_mov_b64 exec, 1\n"
"buffer_atomic_swap_x2 v[226:227], off, s[20:23], s61 sc1\ns_mov_b64 exec, -1\nds_read2st64_b32 v[80:81], v225 offset0:16 offset1:17\nM 128,176,40\nR 172,190,49152\ns_and_b32 s62, s86, 0xf\n"
"s_cmp_eq_u32 s62, 0xf\ns_cbranch_scc0 Lq873\nLq874:\nds_read2st64_b32 v[82:83], v225 offset0:18 offset1:19\nM 128,180,56\ns_add_u32 s65, s68, 37865472\n"
"ds_read2st64_b32 v[84:85], v225 offset0:20 offset1:21\nds_read2st64_b32 v[86:87], v225 offset0:22 offset1:23\nM 144,176,44\nR 174,190,53248\ns_add_u32 m0, s50, 49152\nds_read2st64_b32 v[88:89], v225 offset0:24 offset1:25\n"
"M 144,180,60\nds_read2st64_b32 v[90:91], v225 offset0:26 offset1:27\nds_read2st64_b32 v[92:93], v225 offset0:28 offset1:29\nG 15\nM 148,160,44\nR 176,191,40960\n"
"s_add_u32 s63, s56, 196608\nds_read2st64_b32 v[94:95], v225 offset0:30 offset1:31\nM 148,164,60\nbuffer_load_dwordx4 a[64:67], v252, s[20:23], s64 offen sc1\nv_add_f32 v32, v32, v64\nM 132,160,40\n"
"R 178,191,45056\nv_add_f32 v33, v33, v65\nv_add_f32 v34, v34, v66\nM 132,164,56\ns_add_u32 s66, s69, 37750784\nv_add_f32 v35, v35, v67\n"
"v_add_f32 v36, v36, v68\nv_add_f32 v37, v37, v69\nM 116,160,36\nR 180,191,49152\nbuffer_load_dwordx4 v220, s[16:19], s63 offen lds\nv_add_f32 v38, v38, v70\n"
"M 116,164,52\nbuffer_load_dwordx4 a[68:71], v252, s[20:23], s65 offen sc1\nv_add_f32 v39, v39, v71\nM 100,160,32\nR 182,191,53248\nG 15\n"
"v_add_f32 v40, v40, v72\nv_add_f32 v41, v41, v73\nM 100,164,48\nG 8\nv_add_f32 v42, v42, v74\nv_add_f32 v43, v43, v75\n"
"v_add_f32 v44, v44, v76\nM 104,168,32\nR 160,184,0\nbuffer_load_dwordx4 v221, s[16:19], s63 offen offset:1024 lds\nv_add_f32 v45, v45, v77\nM 104,172,48\n"
"s_add_u32 s67, s69, 37865472\nv_add_f32 v46, v46, v78\nv_add_f32 v47, v47, v79\nv_add_f32 v48, v48, v80\nM 120,168,36\nR 162,184,4096\n"
"v_add_f32 v49, v49, v81\nv_add_f32 v50, v50, v82\nM 120,172,52\nbuffer_load_dwordx4 a[72:75], v252, s[20:23], s66 offen sc1\nM 136,168,40\nR 164,184,8192\n"
"v_add_f32 v51, v51, v83\nM 136,172,56\nv_add_f32 v52, v52, v84\nv_add_f32 v53, v53, v85\nM 152,168,44\nR 166,184,12288\n"
"v_add_f32 v54, v54, v86\nM 152,172,60\nG 4\nv_add_f32 v55, v55, v87\nv_add_f32 v56, v56, v88\nM 156,176,44\n"
"R 168,185,0\nv_add_f32 v57, v57, v89\nM 156,180,60\nbuffer_load_dwordx4 a[76:79], v252, s[20:23], s67 offen sc1\nM 140,176,40\nR 170,185,4096\n"
"v_add_f32 v58, v58, v90\nM 140,180,56\ns_add_u32 s64, s68, 37757952\nv_add_f32 v59, v59, v91\nv_add_f32 v60, v60, v92\nM 124,176,36\n"
"R 172,185,8192\nv_add_f32 v61, v61, v93\nM 124,180,52\ns_add_u32 s65, s68, 37872640\nv_add_f32 v62, v62, v94\nv_add_f32 v63, v63, v95\n"
"M 108,176,32\nR 174,185,12288\nv_mul_f32 v230, v32, v32\nv_mul_f32 v231, v33, v33\nM 108,180,48\ns_add_u32 s84, s73, 37752832\n"
"s_add_u32 s85, s73, 37867520\nG 4\nv_mul_f32 v232, v34, v34\nv_mfma_f32_16x16x32_bf16 v[0:3], v[160:163], a[0:3], 0\nR 176,186,0\ns_lshr_b32 s60, s45, 3\n"
"s_lshl2_add_u32 s60, s60, s40\ns_lshl_b32 s60, s60, 8\nv_lshl_add_u32 v224, v252, 3, s60\nv_add_u32 v224, LqF-32768-LqPC, v224\nv_min_u32 v224, LqE-LqPC-4, v224\ns_mov_b64 exec, 3\n"
"global_load_dword v224, v224, s[98:99]\ns_mov_b64 exec, -1\nv_mul_f32 v233, v35, v35\nM 0,164,16\nbuffer_load_dwordx4 a[80:83], v252, s[20:23], s64 offen sc1\nv_fma_f32 v230, v230, v215, v214\n"
"v_mfma_f32_16x16x32_bf16 v[16:19], v[160:163], a[4:7], 0\nR 178,186,4096\nv_fma_f32 v231, v231, v215, v214\nv_fma_f32 v232, v232, v215, v214\nM 16,164,20\ns_add_u32 s66, s69, 37757952\n"
"v_fma_f32 v233, v233, v215, v214\nv_mul_f32 v230, v32, v230\nv_mul_f32 v231, v33, v231\nv_mfma_f32_16x16x32_bf16 a[224:227], v[160:163], a[8:11], 0\nR 180,186,8192\nv_mul_f32 v232, v34, v232\n"
"v_mul_f32 v233, v35, v233\nv_mfma_f32_16x16x32_bf16 a[224:227], v[164:167], a[24:27], a[224:227]\nbuffer_load_dwordx4 a[84:87], v252, s[20:23], s65 offen sc1\nv_exp_f32 v230, v230\nv_mfma_f32_16x16x32_bf16 a[240:243], v[160:163], a[12:15], 0\nR 182,186,12288\n"
"v_fma_f32 v230, v230, v216, v216\nv_exp_f32 v231, v231\nv_mfma_f32_16x16x32_bf16 a[240:243], v[164:167], a[28:31], a[240:243]\nG 4\nv_exp_f32 v232, v232\nv_exp_f32 v233, v233\n"
"v_mfma_f32_16x16x32_bf16 a[244:247], v[168:171], a[12:15], 0\nR 160,187,0\ns_add_u32 m0, s50, 57344\nv_fma_f32 v231, v231, v216, v216\nv_mfma_f32_16x16x32_bf16 a[244:247], v[172:175], a[28:31], a[244:247]\ns_add_u32 s67, s69, 37872640\n"
"v_rcp_f32 v230, v230\nv_fma_f32 v232, v232, v216, v216\nv_mfma_f32_16x16x32_bf16 a[228:231], v[168:171], a[8:11], 0\nR 162,187,4096\ns_add_u32 s63, s56, 720896\nv_fma_f32 v233, v233, v216, v216\n"
"v_mfma_f32_16x16x32_bf16 a[228:231], v[172:175], a[24:27], a[228:231]\nbuffer_load_dwordx4 a[88:91], v252, s[20:23], s66 offen sc1\nv_rcp_f32 v231, v231\nv_mfma_f32_16x16x32_bf16 v[20:23], v[168:171], a[4:7], 0\nR 164,187,8192\nv_mul_f32 v230, v32, v230\n"
"v_rcp_f32 v232, v232\nM 20,172,20\nbuffer_load_dwordx4 v220, s[16:19], s63 offen lds\nv_rcp_f32 v233, v233\nv_mfma_f32_16x16x32_bf16 v[4:7], v[168:171], a[0:3], 0\nR 166,187,12288\n"
"v_mul_f32 v231, v33, v231\nv_mul_f32 v232, v34, v232\nM 4,172,16\nG 4\nv_mul_f32 v233, v35, v233\nv_cvt_pk_bf16_f32 v32, v230, v231\n"
"v_cvt_pk_bf16_f32 v33, v232, v233\nv_mfma_f32_16x16x32_bf16 v[8:11], v[176:179], a[0:3], 0\nR 168,184,16384\nv_mul_f32 v234, v36, v36\nv_mul_f32 v235, v37, v37\nM 8,180,16\n"
"buffer_load_dwordx4 a[92:95], v252, s[20:23], s67 offen sc1\nbuffer_load_dwordx4 v221, s[16:19], s63 offen offset:1024 lds\nv_mul_f32 v236, v38, v38\nv_mfma_f32_16x16x32_bf16 v[24:27], v[176:179], a[4:7], 0\nR 170,184,20480\ns_add_u32 m0, s50, 65536\n"
"v_mul_f32 v237, v39, v39\nM 24,180,20\ns_add_u32 s64, s68, 37751808\nv_fma_f32 v234, v234, v215, v214\nv_fma_f32 v235, v235, v215, v214\nv_fma_f32 v236, v236, v215, v214\n"
"v_mfma_f32_16x16x32_bf16 a[232:235], v[176:179], a[8:11], 0\nR 172,184,24576\ns_add_u32 s63, s57, 131072\nv_fma_f32 v237, v237, v215, v214\nv_mfma_f32_16x16x32_bf16 a[232:235], v[180:183], a[24:27], a[232:235]\ns_add_u32 s65, s68, 37866496\n"
"buffer_load_dwordx4 v220, s[16:19], s63 offen lds\nv_mul_f32 v234, v36, v234\nv_mfma_f32_16x16x32_bf16 a[248:251], v[176:179], a[12:15], 0\nR 174,184,28672\nv_mul_f32 v235, v37, v235\nv_mul_f32 v236, v38, v236\n"
"v_mfma_f32_16x16x32_bf16 a[248:251], v[180:183], a[28:31], a[248:251]\nG 4\nv_exp_f32 v234, v234\nv_mul_f32 v237, v39, v237\nv_mfma_f32_16x16x32_bf16 a[252:255], v[160:163], a[12:15], 0\nR 176,185,16384\n"
"v_fma_f32 v234, v234, v216, v216\nv_exp_f32 v235, v235\nv_mfma_f32_16x16x32_bf16 a[252:255], v[164:167], a[28:31], a[252:255]\nbuffer_load_dwordx4 a[96:99], v252, s[20:23], s64 offen sc1\nbuffer_load_dwordx4 v221, s[16:19], s63 offen offset:1024 lds\nv_exp_f32 v236, v236\n"
"v_mfma_f32_16x16x32_bf16 a[236:239], v[160:163], a[8:11], 0\nR 178,185,20480\ns_add_u32 m0, s50, 73728\nv_exp_f32 v237, v237\nv_mfma_f32_16x16x32_bf16 a[236:239], v[164:167], a[24:27], a[236:239]\ns_add_u32 s66, s69, 37751808\n"
"v_rcp_f32 v234, v234\nv_fma_f32 v235, v235, v216, v216\nv_mfma_f32_16x16x32_bf16 v[28:31], v[160:163], a[4:7], 0\nR 180,185,24576\ns_add_u32 s63, s57, 655360\nv_fma_f32 v236, v236, v216, v216\n"
"M 28,164,20\nbuffer_load_dwordx4 a[100:103], v252, s[20:23], s65 offen sc1\nbuffer_load_dwordx4 v220, s[16:19], s63 offen lds\nv_fma_f32 v237, v237, v216, v216\nv_mfma_f32_16x16x32_bf16 v[12:15], v[160:163], a[0:3], 0\nR 182,185,28672\n"
"v_mul_f32 v234, v36, v234\nv_rcp_f32 v235, v235\nM 12,164,16\ns_waitcnt vmcnt(14)\ns_barrier\nG 4\n"
"v_rcp_f32 v236, v236\nM 0,168,32\nR 160,186,16384\nv_mul_f32 v235, v37, v235\nv_rcp_f32 v237, v237\nM 0,172,48\n"
"s_add_u32 s67, s69, 37866496\nbuffer_load_dwordx4 v221, s[16:19], s63 offen offset:1024 lds\nv_mul_f32 v236, v38, v236\nM 16,168,36\nR 162,186,20480\nv_mul_f32 v237, v39, v237\n"
"v_cvt_pk_bf16_f32 v34, v234, v235\nM 16,172,52\nbuffer_load_dwordx4 a[104:107], v252, s[20:23], s66 offen sc1\nv_cvt_pk_bf16_f32 v35, v236, v237\nv_mfma_f32_16x16x32_bf16 a[224:227], v[168:171], a[40:43], a[224:227]\nR 164,186,24576\n"
"v_mul_f32 v230, v40, v40\nv_mul_f32 v231, v41, v41\nv_mfma_f32_16x16x32_bf16 a[224:227], v[172:175], a[56:59], a[224:227]\nv_mul_f32 v232, v42, v42\nv_mul_f32 v233, v43, v43\nv_fma_f32 v230, v230, v215, v214\n"
"v_mfma_f32_16x16x32_bf16 a[240:243], v[168:171], a[44:47], a[240:243]\nR 166,186,28672\nv_fma_f32 v231, v231, v215, v214\nv_fma_f32 v232, v232, v215, v214\nv_mfma_f32_16x16x32_bf16 a[240:243], v[172:175], a[60:63], a[240:243]\nG 4\n"
"v_fma_f32 v233, v233, v215, v214\nv_mul_f32 v230, v40, v230\nv_mul_f32 v231, v41, v231\nv_mfma_f32_16x16x32_bf16 a[244:247], v[176:179], a[44:47], a[244:247]\nR 168,187,16384\nv_mul_f32 v232, v42, v232\n"
"v_mul_f32 v233, v43, v233\nv_mfma_f32_16x16x32_bf16 a[244:247], v[180:183], a[60:63], a[244:247]\nbuffer_load_dwordx4 a[108:111], v252, s[20:23], s67 offen sc1\nv_exp_f32 v230, v230\nv_mfma_f32_16x16x32_bf16 a[228:231], v[176:179], a[40:43], a[228:231]\nR 170,187,20480\n"
"v_fma_f32 v230, v230, v216, v216\nv_exp_f32 v231, v231\nv_mfma_f32_16x16x32_bf16 a[228:231], v[180:183], a[56:59], a[228:231]\ns_add_u32 s64, s68, 37758976\nv_exp_f32 v232, v232\nv_exp_f32 v233, v233\n"
"M 20,176,36\nR 172,187,24576\nv_fma_f32 v231, v231, v216, v216\nv_fma_f32 v232, v232, v216, v216\nM 20,180,52\ns_add_u32 s65, s68, 37873664\n"
"v_rcp_f32 v230, v230\nv_fma_f32 v233, v233, v216, v216\nM 4,176,32\nR 174,187,28672\ns_add_u32 m0, s50, 81920\nv_rcp_f32 v231, v231\n"
"M 4,180,48\nG 4\nv_rcp_f32 v232, v232\nv_rcp_f32 v233, v233\nM 8,160,32\nR 176,184,32768\n"
"s_add_u32 s63, s57, 196608\nv_mul_f32 v230, v40, v230\nM 8,164,48\nbuffer_load_dwordx4 a[112:115], v252, s[20:23], s64 offen sc1\nv_mul_f32 v231, v41, v231\nM 24,160,36\n"
"R 178,184,36864\nv_mul_f32 v232, v42, v232\nv_mul_f32 v233, v43, v233\nM 24,164,52\ns_add_u32 s66, s69, 37758976\nbuffer_load_dwordx4 v220, s[16:19], s63 offen lds\n"
"v_cvt_pk_bf16_f32 v40, v230, v231\nv_mfma_f32_16x16x32_bf16 a[232:235], v[160:163], a[40:43], a[232:235]\nR 180,184,40960\nv_cvt_pk_bf16_f32 v41, v232, v233\nv_mul_f32 v234, v44, v44\nv_mfma_f32_16x16x32_bf16 a[232:235], v[164:167], a[56:59], a[232:235]\n"
"buffer_load_dwordx4 a[116:119], v252, s[20:23], s65 offen sc1\nv_mul_f32 v235, v45, v45\nv_mfma_f32_16x16x32_bf16 a[248:251], v[160:163], a[44:47], a[248:251]\nR 182,184,45056\nv_mul_f32 v236, v46, v46\nv_mul_f32 v237, v47, v47\n"
"v_mfma_f32_16x16x32_bf16 a[248:251], v[164:167], a[60:63], a[248:251]\nbuffer_load_dwordx4 v221, s[16:19], s63 offen offset:1024 lds\nG 4\nv_fma_f32 v234, v234, v215, v214\nv_mfma_f32_16x16x32_bf16 a[252:255], v[168:171], a[44:47], a[252:255]\nR 160,185,32768\n"
"s_add_u32 m0, s50, 90112\nv_fma_f32 v235, v235, v215, v214\nv_mfma_f32_16x16x32_bf16 a[252:255], v[172:175], a[60:63], a[252:255]\ns_add_u32 s67, s69, 37873664\ns_add_u32 s63, s57, 720896\nv_fma_f32 v236, v236, v215, v214\n"
"v_fma_f32 v237, v237, v215, v214\nv_mfma_f32_16x16x32_bf16 a[236:239], v[168:171], a[40:43], a[236:239]\nR 162,185,36864\nv_mov_b32 v248, 0\nv_mul_f32 v234, v44, v234\nv_mfma_f32_16x16x32_bf16 a[236:239], v[172:175], a[56:59], a[236:239]\n"
"buffer_load_dwordx4 a[120:123], v252, s[20:23], s66 offen sc1\nbuffer_load_dwordx4 v220, s[16:19], s63 offen lds\nv_mov_b32 v249, 0\nv_mul_f32 v235, v45, v235\nM 28,168,36\nR 164,185,40960\n"
"s_add_u32 s61, s82, 845848\ns_mov_b64 exec, 0xf\nbuffer_atomic_or_x2 v[248:249], v250, s[20:23], s61 offen sc0 sc1\ns_mov_b64 exec, -1\nv_mul_f32 v236, v46, v236\nM 28,172,52\n"
"buffer_load_dwordx4 v221, s[16:19], s63 offen offset:1024 lds\nv_mul_f32 v237, v47, v237\nM 12,168,32\nR 166,185,45056\ns_add_u32 m0, s50, 98304\nv_exp_f32 v234, v234\n"
"M 12,172,48\nG 4\nv_exp_f32 v235, v235\nv_exp_f32 v236, v236\nM 96,176,64\nR 168,186,32768\n"
"s_add_u32 s63, s58, 0\nv_exp_f32 v237, v237\ns_waitcnt vmcnt(21)\nM 96,180,80\nbuffer_load_dwordx4 a[124:127], v252, s[20:23], s67 offen sc1\nv_fma_f32 v234, v234, v216, v216\n"
"M 112,176,68\nR 170,186,36864\nv_fma_f32 v235, v235, v216, v216\nv_fma_f32 v236, v236, v216, v216\ns_waitcnt vmcnt(21)\nM 112,180,84\n"
"buffer_load_dwordx4 v220, s[16:19], s63 offen lds\nv_fma_f32 v237, v237, v216, v216\nM 128,176,72\nR 172,186,40960\nv_mul_f32 v230, v48, v48\nv_rcp_f32 v234, v234\n"
"s_waitcnt vmcnt(21)\nM 128,180,88\nbuffer_load_dwordx4 v221, s[16:19], s63 offen offset:1024 lds\nv_rcp_f32 v235, v235\nM 144,176,76\nR 174,186,45056\n"
"s_add_u32 m0, s50, 106496\nv_rcp_f32 v236, v236\ns_waitcnt vmcnt(20)\nM 144,180,92\nG 4\nv_rcp_f32 v237, v237\n"
"v_mul_f32 v234, v44, v234\nM 148,160,76\nR 176,187,32768\ns_add_u32 s63, s58, 524288\nv_mul_f32 v235, v45, v235\nM 148,164,92\n"
"buffer_load_dwordx4 v220, s[16:19], s63 offen lds\nv_mul_f32 v236, v46, v236\nM 132,160,72\nR 178,187,36864\nv_mul_f32 v237, v47, v237\nv_cvt_pk_bf16_f32 v42, v234, v235\n"
"M 132,164,88\nbuffer_store_dwordx4 v[32:35], v252, s[20:23], s84 offen offset:0 sc1\nv_cvt_pk_bf16_f32 v43, v236, v237\nM 116,160,68\nR 180,187,40960\nv_mul_f32 v231, v49, v49\n"
"v_mul_f32 v232, v50, v50\nM 116,164,84\nbuffer_load_dwordx4 v221, s[16:19], s63 offen offset:1024 lds\nv_mul_f32 v233, v51, v51\nM 100,160,64\nR 182,187,45056\n"
"v_fma_f32 v230, v230, v218, v217\nv_fma_f32 v231, v231, v218, v217\nM 100,164,80\nG 4\nv_fma_f32 v232, v232, v218, v217\nv_fma_f32 v233, v233, v218, v217\n"
"v_mul_f32 v230, v48, v230\nM 104,168,64\nR 160,184,49152\ns_waitcnt vmcnt(16)\ns_barrier\nv_mul_f32 v231, v49, v231\n"
"M 104,172,80\nbuffer_store_dwordx4 v[40:43], v252, s[20:23], s84 offen offset:1024 sc1\nv_mul_f32 v232, v50, v232\nM 120,168,68\nR 162,184,53248\nv_mul_f32 v233, v51, v233\n"
"v_exp_f32 v230, v230\nM 120,172,84\ns_add_u32 m0, s50, 114688\nv_exp_f32 v231, v231\nv_exp_f32 v232, v232\nM 136,168,72\n"
"R 164,184,57344\nv_fma_f32 v230, v230, v219, v219\nv_exp_f32 v233, v233\nM 136,172,88\ns_add_u32 s63, s58, 65536\nv_rcp_f32 v230, v230\n"
"v_fma_f32 v231, v231, v219, v219\nM 152,168,76\nR 166,184,61440\nv_fma_f32 v232, v232, v219, v219\nv_fma_f32 v233, v233, v219, v219\nM 152,172,92\n"
"G 4\nv_rcp_f32 v231, v231\nv_rcp_f32 v232, v232\nM 156,176,76\nR 168,185,49152\nv_mul_f32 v230, v48, v230\n"
"v_rcp_f32 v233, v233\nM 156,180,92\nbuffer_load_dwordx4 v220, s[16:19], s63 offen lds\nv_mul_f32 v231, v49, v231\nM 140,176,72\nR 170,185,53248\n"
"v_mul_f32 v232, v50, v232\nv_mul_f32 v233, v51, v233\nM 140,180,88\nv_cvt_pk_bf16_f32 v48, v230, v231\nv_cvt_pk_bf16_f32 v49, v232, v233\nv_mul_f32 v234, v52, v52\n"
"M 124,176,68\nR 172,185,57344\nv_mul_f32 v235, v53, v53\nv_mul_f32 v236, v54, v54\nM 124,180,84\nbuffer_load_dwordx4 v221, s[16:19], s63 offen offset:1024 lds\n"
"v_mul_f32 v237, v55, v55\nM 108,176,64\nR 174,185,61440\ns_add_u32 m0, s50, 0\nv_fma_f32 v234, v234, v218, v217\nM 108,180,80\n"
"G 4\nv_fma_f32 v235, v235, v218, v217\nv_fma_f32 v236, v236, v218, v217\nv_fma_f32 v237, v237, v218, v217\nM 96,160,96\nR 176,186,49152\n"
"s_add_u32 s63, s58, 589824\nv_mul_f32 v234, v52, v234\ns_waitcnt vmcnt(16)\nM 96,164,112\nbuffer_load_dwordx4 v220, s[16:19], s63 offen lds\nv_mul_f32 v235, v53, v235\n"
"M 112,160,100\nR 178,186,53248\nv_mul_f32 v236, v54, v236\nv_mul_f32 v237, v55, v237\ns_waitcnt vmcnt(15)\nM 112,164,116\n"
"v_exp_f32 v234, v234\nv_exp_f32 v235, v235\nM 128,160,104\nR 180,186,57344\nv_fma_f32 v234, v234, v219, v219\nv_exp_f32 v236, v236\n"
"s_waitcnt vmcnt(13)\nM 128,164,120\nv_exp_f32 v237, v237\nv_fma_f32 v235, v235, v219, v219\nM 144,160,108\nR 182,186,61440\n"
"v_fma_f32 v236, v236, v219, v219\nv_fma_f32 v237, v237, v219, v219\ns_waitcnt vmcnt(9)\nM 144,164,124\nG 4\nv_rcp_f32 v234, v234\n"
"v_rcp_f32 v235, v235\nM 148,168,108\nR 160,187,49152\nv_mul_f32 v234, v52, v234\nv_rcp_f32 v236, v236\nM 148,172,124\n"
"s_add_u32 s64, s68, 37754880\nv_rcp_f32 v237, v237\nv_mul_f32 v235, v53, v235\nM 132,168,104\nR 162,187,53248\nv_cmp_eq_u64 s[86:87], s[46:47], v[248:249]\n"
"v_mul_f32 v236, v54, v236\nM 132,172,120\nbuffer_load_dwordx4 v221, s[16:19], s63 offen offset:1024 lds\nv_mul_f32 v237, v55, v237\nM 116,168,100\nR 164,187,57344\n"
"s_and_b32 s62, s86, 0xf\ns_cmp_eq_u32 s62, 0xf\ns_cbranch_scc0 Lq908\nLq909:\nv_cvt_pk_bf16_f32 v50, v234, v235\nM 116,172,116\n"
"s_add_u32 s65, s68, 37869568\nv_cvt_pk_bf16_f32 v51, v236, v237\nv_mul_f32 v230, v56, v56\nv_mul_f32 v231, v57, v57\nM 100,168,96\nR 166,187,61440\n"
"v_mul_f32 v232, v58, v58\nv_mul_f32 v233, v59, v59\nM 100,172,112\nG 4\nv_fma_f32 v230, v230, v218, v217\nv_fma_f32 v231, v231, v218, v217\n"
"M 104,176,96\nR 168,188,0\nv_fma_f32 v232, v232, v218, v217\nM 104,180,112\nbuffer_load_dwordx4 a[128:131], v252, s[20:23], s64 offen sc1\nM 120,176,100\n"
"R 170,188,4096\nv_fma_f32 v233, v233, v218, v217\nM 120,180,116\ns_add_u32 s66, s69, 37754880\nv_mul_f32 v230, v56, v230\nv_mul_f32 v231, v57, v231\n"
"M 136,176,104\nR 172,188,8192\nv_mul_f32 v232, v58, v232\nM 136,180,120\nbuffer_load_dwordx4 a[132:135], v252, s[20:23], s65 offen sc1\nM 152,176,108\n"
"R 174,188,12288\nv_mul_f32 v233, v59, v233\nM 152,180,124\nG 4\nv_exp_f32 v230, v230\nM 156,160,108\n"
"R 176,189,0\nv_fma_f32 v230, v230, v219, v219\nM 156,164,124\ns_add_u32 s67, s69, 37869568\nv_exp_f32 v231, v231\nM 140,160,104\n"
"R 178,189,4096\nv_fma_f32 v231, v231, v219, v219\nM 140,164,120\nbuffer_load_dwordx4 a[136:139], v252, s[20:23], s66 offen sc1\nM 124,160,100\nR 180,189,8192\n"
"v_mul_f32 v234, v60, v60\nM 124,164,116\nLpg30:\ns_cbranch_execz LretO1n\nbuffer_store_dwordx4 v[48:51], v252, s[20:23], s85 offen offset:0 sc1\nM 108,160,96\n"
"R 182,189,12288\ns_add_u32 m0, s50, 8192\nM 108,164,112\ns_waitcnt vmcnt(9)\ns_barrier\nG 4\n"
"M 0,168,64\nR 160,190,0\ns_add_u32 s63, s58, 131072\nM 0,172,80\nbuffer_load_dwordx4 a[140:143], v252, s[20:23], s67 offen sc1\nM 16,168,68\n"
"R 162,190,4096\nv_mul_f32 v235, v61, v61\nM 16,172,84\ns_add_u32 s64, s68, 37762048\nv_exp_f32 v232, v232\nv_mfma_f32_16x16x32_bf16 a[224:227], v[168:171], a[72:75], a[224:227]\n"
"R 164,190,8192\nbuffer_load_dwordx4 v220, s[16:19], s63 offen lds\nv_mfma_f32_16x16x32_bf16 a[224:227], v[172:175], a[88:91], a[224:227]\ns_add_u32 s65, s68, 37876736\nv_exp_f32 v233, v233\nv_mfma_f32_16x16x32_bf16 a[240:243], v[168:171], a[76:79], a[240:243]\n"
"R 166,190,12288\nv_fma_f32 v232, v232, v219, v219\nv_mfma_f32_16x16x32_bf16 a[240:243], v[172:175], a[92:95], a[240:243]\nG 4\nv_rcp_f32 v230, v230\nv_mfma_f32_16x16x32_bf16 a[244:247], v[176:179], a[76:79], a[244:247]\n"
"R 168,191,0\nbuffer_load_dwordx4 v221, s[16:19], s63 offen offset:1024 lds\nv_mfma_f32_16x16x32_bf16 a[244:247], v[180:183], a[92:95], a[244:247]\nbuffer_load_dwordx4 a[144:147], v252, s[20:23], s64 offen sc1\nv_mfma_f32_16x16x32_bf16 a[228:231], v[176:179], a[72:75], a[228:231]\nR 170,191,4096\n"
"s_add_u32 m0, s50, 16384\nv_mfma_f32_16x16x32_bf16 a[228:231], v[180:183], a[88:91], a[228:231]\ns_add_u32 s66, s69, 37762048\ns_add_u32 s63, s58, 655360\nv_fma_f32 v233, v233, v219, v219\nM 20,176,68\n"
"R 172,191,8192\nbuffer_load_dwordx4 v220, s[16:19], s63 offen lds\nM 20,180,84\nbuffer_load_dwordx4 a[148:151], v252, s[20:23], s65 offen sc1\nM 4,176,64\nR 174,191,12288\n"
"v_mul_f32 v230, v56, v230\nM 4,180,80\nG 4\nv_rcp_f32 v231, v231\nM 8,160,64\nR 176,188,16384\n"
"buffer_load_dwordx4 v221, s[16:19], s63 offen offset:1024 lds\nM 8,164,80\ns_add_u32 s67, s69, 37876736\nv_rcp_f32 v232, v232\nM 24,160,68\nR 178,188,20480\n"
"v_mul_f32 v231, v57, v231\nM 24,164,84\nbuffer_load_dwordx4 a[152:155], v252, s[20:23], s66 offen sc1\nv_mfma_f32_16x16x32_bf16 a[232:235], v[160:163], a[72:75], a[232:235]\nR 180,188,24576\nv_mul_f32 v232, v58, v232\n"
"v_mfma_f32_16x16x32_bf16 a[232:235], v[164:167], a[88:91], a[232:235]\nv_rcp_f32 v233, v233\nv_mfma_f32_16x16x32_bf16 a[248:251], v[160:163], a[76:79], a[248:251]\nR 182,188,28672\nv_mul_f32 v233, v59, v233\nv_mfma_f32_16x16x32_bf16 a[248:251], v[164:167], a[92:95], a[248:251]\n"
"G 4\nv_cvt_pk_bf16_f32 v56, v230, v231\nv_cvt_pk_bf16_f32 v57, v232, v233\nv_mfma_f32_16x16x32_bf16 a[252:255], v[168:171], a[76:79], a[252:255]\nR 160,189,16384\nv_mul_f32 v236, v62, v62\n"
"v_mfma_f32_16x16x32_bf16 a[252:255], v[172:175], a[92:95], a[252:255]\nbuffer_load_dwordx4 a[156:159], v252, s[20:23], s67 offen sc1\nv_mfma_f32_16x16x32_bf16 a[236:239], v[168:171], a[72:75], a[236:239]\nR 162,189,20480\nv_mul_f32 v237, v63, v63\nv_mfma_f32_16x16x32_bf16 a[236:239], v[172:175], a[88:91], a[236:239]\n"
"v_fma_f32 v234, v234, v218, v217\nv_fma_f32 v235, v235, v218, v217\nM 28,168,68\nR 164,189,24576\nv_fma_f32 v236, v236, v218, v217\nM 28,172,84\n"
"s_add_u32 m0, s50, 24576\nv_fma_f32 v237, v237, v218, v217\nv_mul_f32 v234, v60, v234\nM 12,168,64\nR 166,189,28672\nv_mul_f32 v235, v61, v235\n"
"M 12,172,80\nG 4\nv_exp_f32 v234, v234\nM 0,176,96\nR 168,190,16384\nv_mul_f32 v236, v62, v236\n"
"M 0,180,112\ns_add_u32 s63, s58, 196608\nv_exp_f32 v235, v235\nM 16,176,100\nR 170,190,20480\nv_mul_f32 v237, v63, v237\n"
"M 16,180,116\nbuffer_load_dwordx4 v220, s[16:19], s63 offen lds\nv_mfma_f32_16x16x32_bf16 a[224:227], v[176:179], a[104:107], a[224:227]\nR 172,190,24576\nv_fma_f32 v234, v234, v219, v219\nv_mfma_f32_16x16x32_bf16 a[224:227], v[180:183], a[120:123], a[224:227]\n"
"v_exp_f32 v236, v236\nv_mfma_f32_16x16x32_bf16 a[240:243], v[176:179], a[108:111], a[240:243]\nR 174,190,28672\nv_fma_f32 v235, v235, v219, v219\nv_mfma_f32_16x16x32_bf16 a[240:243], v[180:183], a[124:127], a[240:243]\nG 4\n"
"v_exp_f32 v237, v237\nv_mfma_f32_16x16x32_bf16 a[244:247], v[160:163], a[108:111], a[244:247]\nR 176,191,16384\nv_fma_f32 v236, v236, v219, v219\nv_mfma_f32_16x16x32_bf16 a[244:247], v[164:167], a[124:127], a[244:247]\nbuffer_load_dwordx4 v221, s[16:19], s63 offen offset:1024 lds\n"
"v_mfma_f32_16x16x32_bf16 a[228:231], v[160:163], a[104:107], a[228:231]\nR 178,191,20480\ns_add_u32 s63, s58, 720896\nv_mfma_f32_16x16x32_bf16 a[228:231], v[164:167], a[120:123], a[228:231]\ns_add_u32 m0, s50, 32768\nv_rcp_f32 v234, v234\n"
"M 20,160,100\nR 180,191,24576\nv_fma_f32 v237, v237, v219, v219\nM 20,164,116\nbuffer_load_dwordx4 v220, s[16:19], s63 offen lds\nM 4,160,96\n"
"R 182,191,28672\nv_mul_f32 v234, v60, v234\nM 4,164,112\nG 4\nv_rcp_f32 v235, v235\nM 8,168,96\n"
"R 160,188,32768\nv_mul_f32 v235, v61, v235\nM 8,172,112\nbuffer_load_dwordx4 v221, s[16:19], s63 offen offset:1024 lds\nM 24,168,100\nR 162,188,36864\n"
"s_add_u32 s63, s58, 393216\nM 24,172,116\ns_add_u32 m0, s50, 40960\nv_rcp_f32 v236, v236\nv_mfma_f32_16x16x32_bf16 a[232:235], v[168:171], a[104:107], a[232:235]\nR 164,188,40960\n"
"v_mul_f32 v236, v62, v236\nv_mfma_f32_16x16x32_bf16 a[232:235], v[172:175], a[120:123], a[232:235]\nbuffer_load_dwordx4 v220, s[16:19], s63 offen lds\nv_mfma_f32_16x16x32_bf16 a[248:251], v[168:171], a[108:111], a[248:251]\nR 166,188,45056\nv_cvt_pk_bf16_f32 v58, v234, v235\n"
"v_mfma_f32_16x16x32_bf16 a[248:251], v[172:175], a[124:127], a[248:251]\nG 4\nv_rcp_f32 v237, v237\nv_mfma_f32_16x16x32_bf16 a[252:255], v[176:179], a[108:111], a[252:255]\nR 168,189,32768\nv_mul_f32 v237, v63, v237\n"
"v_mfma_f32_16x16x32_bf16 a[252:255], v[180:183], a[124:127], a[252:255]\nbuffer_load_dwordx4 v221, s[16:19], s63 offen offset:1024 lds\nv_mfma_f32_16x16x32_bf16 a[236:239], v[176:179], a[104:107], a[236:239]\nR 170,189,36864\ns_add_u32 s63, s58, 458752\nv_mfma_f32_16x16x32_bf16 a[236:239], v[180:183], a[120:123], a[236:239]\n"
"s_add_u32 m0, s50, 49152\nv_cvt_pk_bf16_f32 v59, v236, v237\nbuffer_store_dwordx4 v[56:59], v252, s[20:23], s85 offen offset:1024 sc1\nM 28,176,100\nR 172,189,40960\nM 28,180,116\n"
"buffer_load_dwordx4 v220, s[16:19], s63 offen lds\nM 12,176,96\nR 174,189,45056\nM 12,180,112\ns_waitcnt vmcnt(10)\ns_barrier\n"
"G 4\nv_mfma_f32_16x16x32_bf16 v[32:35], v[160:163], a[0:3], 0\nR 176,190,32768\nM 32,164,16\nv_mfma_f32_16x16x32_bf16 v[48:51], v[160:163], a[4:7], 0\nR 178,190,36864\n"
"M 48,164,20\nbuffer_load_dwordx4 v221, s[16:19], s63 offen offset:1024 lds\nv_mfma_f32_16x16x32_bf16 v[64:67], v[160:163], a[8:11], 0\nR 180,190,40960\ns_add_u32 s63, s56, 393216\nM 64,164,24\n"
"s_add_u32 m0, s50, 57344\nv_mfma_f32_16x16x32_bf16 v[80:83], v[160:163], a[12:15], 0\nR 182,190,45056\nM 80,164,28\nG 4\nv_mfma_f32_16x16x32_bf16 v[84:87], v[168:171], a[12:15], 0\n"
"R 160,191,32768\nM 84,172,28\nbuffer_load_dwordx4 v220, s[16:19], s63 offen lds\nv_mfma_f32_16x16x32_bf16 v[68:71], v[168:171], a[8:11], 0\nR 162,191,36864\nM 68,172,24\n"
"v_mfma_f32_16x16x32_bf16 v[52:55], v[168:171], a[4:7], 0\nR 164,191,40960\nM 52,172,20\nv_mfma_f32_16x16x32_bf16 v[36:39], v[168:171], a[0:3], 0\nR 166,191,45056\nM 36,172,16\n"
"G 4\nv_mfma_f32_16x16x32_bf16 v[40:43], v[176:179], a[0:3], 0\nR 168,188,49152\nM 40,180,16\nbuffer_load_dwordx4 v221, s[16:19], s63 offen offset:1024 lds\nv_mfma_f32_16x16x32_bf16 v[56:59], v[176:179], a[4:7], 0\n"
"R 170,188,53248\nM 56,180,20\ns_add_u32 m0, s50, 65536\nv_mfma_f32_16x16x32_bf16 v[72:75], v[176:179], a[8:11], 0\nR 172,184,0\nM 72,180,24\n"
"s_add_u32 s63, s56, 458752\nv_mfma_f32_16x16x32_bf16 v[88:91], v[176:179], a[12:15], 0\nR 174,184,4096\nM 88,180,28\nG 4\nv_mfma_f32_16x16x32_bf16 v[92:95], v[160:163], a[12:15], 0\n"
"R 176,189,49152\nM 92,164,28\nbuffer_load_dwordx4 v220, s[16:19], s63 offen lds\nv_mfma_f32_16x16x32_bf16 v[76:79], v[160:163], a[8:11], 0\nR 178,189,53248\nM 76,164,24\n"
"v_mfma_f32_16x16x32_bf16 v[60:63], v[160:163], a[4:7], 0\nR 180,185,0\nM 60,164,20\nv_mfma_f32_16x16x32_bf16 v[44:47], v[160:163], a[0:3], 0\nR 182,185,4096\nM 44,164,16\n"
"G 4\nM 32,168,32\nR 160,190,49152\nM 32,172,48\nbuffer_load_dwordx4 v221, s[16:19], s63 offen offset:1024 lds\nM 48,168,36\n"
"R 162,190,53248\nM 48,172,52\ns_add_u32 m0, s50, 73728\nM 64,168,40\nR 164,186,0\nM 64,172,56\n"
"s_add_u32 s63, s57, 393216\nM 80,168,44\nR 166,186,4096\nM 80,172,60\nG 4\nM 84,176,44\n"
"R 168,191,49152\nM 84,180,60\nbuffer_load_dwordx4 v220, s[16:19], s63 offen lds\nM 68,176,40\nR 170,191,53248\nv_mov_b32 v226, s46\n"
"M 68,180,56\nv_mov_b32 v227, s47\nM 52,176,36\nR 172,187,0\ns_add_u32 s61, s81, 845840\ns_lshl_b32 s62, s44, 5\n"
"s_add_u32 s61, s61, s62\ns_waitcnt vmcnt(7)\ns_mov_b64 exec, 1\nbuffer_atomic_swap_x2 v[226:227], off, s[20:23], s61 sc1\ns_mov_b64 exec, -1\nM 52,180,52\n"
"M 36,176,32\nR 174,187,4096\nM 36,180,48\nG 4\nM 40,160,32\nR 176,184,8192\n"
"M 40,164,48\nbuffer_load_dwordx4 v221, s[16:19], s63 offen offset:1024 lds\nM 56,160,36\nR 178,184,12288\ns_add_u32 s63, s57, 458752\nM 56,164,52\n"
"s_add_u32 m0, s50, 81920\nM 72,160,40\nR 180,184,16384\nM 72,164,56\nbuffer_load_dwordx4 v220, s[16:19], s63 offen lds\nM 88,160,44\n"
"R 182,184,20480\nM 88,164,60\nG 4\nM 92,168,44\nR 160,185,8192\nM 92,172,60\n"
"buffer_load_dwordx4 v221, s[16:19], s63 offen offset:1024 lds\nM 76,168,40\nR 162,185,12288\ns_add_u32 s63, s56, 262144\nM 76,172,56\ns_add_u32 m0, s50, 90112\n"
"M 60,168,36\nR 164,185,16384\nM 60,172,52\nbuffer_load_dwordx4 v220, s[16:19], s63 offen lds\nM 44,168,32\nR 166,185,20480\n"
"M 44,172,48\ns_waitcnt vmcnt(10)\ns_barrier\nG 4\nM 32,176,64\nR 168,186,8192\n"
"M 32,180,80\nM 48,176,68\nR 170,186,12288\nM 48,180,84\nbuffer_load_dwordx4 v221, s[16:19], s63 offen offset:1024 lds\nM 64,176,72\n"
"R 172,186,16384\ns_add_u32 s63, s56, 786432\nM 64,180,88\ns_add_u32 m0, s50, 98304\nM 80,176,76\nR 174,186,20480\n"
"M 80,180,92\nG 4\nM 84,160,76\nR 176,187,8192\nM 84,164,92\nbuffer_load_dwordx4 v220, s[16:19], s63 offen lds\n"
"M 68,160,72\nR 178,187,12288\nM 68,164,88\nM 52,160,68\nR 180,187,16384\nv_mov_b32 v248, 0\n"
"M 52,164,84\nv_mov_b32 v249, 0\nM 36,160,64\nR 182,187,20480\ns_add_u32 s61, s82, 845840\ns_mov_b64 exec, 0xf\n"
"buffer_atomic_or_x2 v[248:249], v250, s[20:23], s61 offen sc0 sc1\ns_mov_b64 exec, -1\nM 36,164,80\nG 4\nM 40,168,64\nR 160,184,24576\n"
"M 40,172,80\nbuffer_load_dwordx4 v221, s[16:19], s63 offen offset:1024 lds\nM 56,168,68\nR 162,184,28672\nM 56,172,84\ns_add_u32 m0, s50, 106496\n"
"M 72,168,72\nR 164,184,32768\nM 72,172,88\ns_add_u32 s63, s56, 327680\nM 88,168,76\nR 166,184,36864\n"
"M 88,172,92\nG 4\nM 92,176,76\nR 168,185,24576\nM 92,180,92\nbuffer_load_dwordx4 v220, s[16:19], s63 offen lds\n"
"M 76,176,72\nR 170,185,28672\nM 76,180,88\nM 60,176,68\nR 172,185,32768\nM 60,180,84\n"
"M 44,176,64\nR 174,185,36864\nM 44,180,80\nG 4\nM 32,160,96\nR 176,186,24576\n"
"M 32,164,112\nbuffer_load_dwordx4 v221, s[16:19], s63 offen offset:1024 lds\nM 48,160,100\nR 178,186,28672\nM 48,164,116\ns_add_u32 m0, s50, 114688\n"
"M 64,160,104\nR 180,186,32768\nM 64,164,120\ns_add_u32 s63, s56, 851968\nM 80,160,108\nR 182,186,36864\n"
"M 80,164,124\nG 4\nM 84,168,108\nR 160,187,24576\nM 84,172,124\nbuffer_load_dwordx4 v220, s[16:19], s63 offen lds\n"
"M 68,168,104\nR 162,187,28672\nM 68,172,120\nM 52,168,100\nR 164,187,32768\nM 52,172,116\n"
"buffer_load_dwordx4 v221, s[16:19], s63 offen offset:1024 lds\nM 36,168,96\nR 166,187,36864\ns_add_u32 m0, s50, 0\nM 36,172,112\nG 4\n"
"M 40,176,96\nR 168,184,40960\ns_add_u32 s63, s57, 262144\nM 40,180,112\nbuffer_load_dwordx4 v220, s[16:19], s63 offen lds\nM 56,176,100\n"
"R 170,184,45056\nM 56,180,116\nM 72,176,104\nR 172,184,49152\nM 72,180,120\nbuffer_load_dwordx4 v221, s[16:19], s63 offen offset:1024 lds\n"
"M 88,176,108\nR 174,184,53248\ns_waitcnt vmcnt(16)\ns_barrier\nM 88,180,124\nG 4\n"
"M 92,160,108\nR 176,185,40960\nM 92,164,124\ns_add_u32 s64, s68, 37752832\nM 76,160,104\nR 178,185,45056\n"
"M 76,164,120\nM 60,160,100\nR 180,185,49152\ns_waitcnt vmcnt(7)\nv_cmp_eq_u64 s[86:87], s[46:47], v[248:249]\nM 60,164,116\n"
"s_add_u32 s65, s68, 37867520\nM 44,160,96\nR 182,185,53248\ns_and_b32 s62, s86, 0xf\ns_cmp_eq_u32 s62, 0xf\ns_cbranch_scc0 Lq927\n"
"Lq928:\nM 44,164,112\nG 4\nM 32,168,128\nR 160,186,40960\nM 32,172,144\n"
"buffer_load_dwordx4 a[160:163], v252, s[20:23], s64 offen sc1\nM 48,168,132\nR 162,186,45056\nM 48,172,148\ns_add_u32 s66, s69, 37752832\nM 64,168,136\n"
"R 164,186,49152\nM 64,172,152\nbuffer_load_dwordx4 a[164:167], v252, s[20:23], s65 offen sc1\nM 80,168,140\nR 166,186,53248\nM 80,172,156\n"
"G 4\nM 84,176,140\nR 168,187,40960\nM 84,180,156\ns_add_u32 s67, s69, 37867520\nM 68,176,136\n"
"R 170,187,45056\nM 68,180,152\nbuffer_load_dwordx4 a[168:171], v252, s[20:23], s66 offen sc1\nM 52,176,132\nR 172,187,49152\nM 52,180,148\n"
"s_add_u32 s64, s68, 37760000\nM 36,176,128\nR 174,187,53248\nM 36,180,144\nG 4\nM 40,160,128\n"
"R 176,184,57344\nM 40,164,144\nbuffer_load_dwordx4 a[172:175], v252, s[20:23], s67 offen sc1\nM 56,160,132\nR 178,184,61440\nM 56,164,148\n"
"s_add_u32 s65, s68, 37874688\nM 72,160,136\nR 180,188,0\nM 72,164,152\nbuffer_load_dwordx4 a[176:179], v252, s[20:23], s64 offen sc1\nM 88,160,140\n"
"R 182,188,4096\ns_add_u32 m0, s50, 8192\nM 88,164,156\nG 4\nM 92,168,140\nR 160,185,57344\n"
"s_add_u32 s63, s57, 786432\nM 92,172,156\ns_add_u32 s66, s69, 37760000\nM 76,168,136\nR 162,185,61440\nM 76,172,152\n"
"buffer_load_dwordx4 a[180:183], v252, s[20:23], s65 offen sc1\nbuffer_load_dwordx4 v220, s[16:19], s63 offen lds\nM 60,168,132\nR 164,189,0\nM 60,172,148\ns_add_u32 s67, s69, 37874688\n"
"M 44,168,128\nR 166,189,4096\nM 44,172,144\nbuffer_load_dwordx4 v221, s[16:19], s63 offen offset:1024 lds\nG 4\nM 96,176,128\n"
"R 168,186,57344\ns_add_u32 m0, s50, 16384\nM 96,180,144\nbuffer_load_dwordx4 a[184:187], v252, s[20:23], s66 offen sc1\nM 112,176,132\nR 170,186,61440\n"
"s_add_u32 s63, s57, 327680\nM 112,180,148\ns_add_u32 s64, s68, 37753856\nbuffer_load_dwordx4 v220, s[16:19], s63 offen lds\nM 128,176,136\nR 172,190,0\n"
"M 128,180,152\nbuffer_load_dwordx4 a[188:191], v252, s[20:23], s67 offen sc1\nM 144,176,140\nR 174,190,4096\nM 144,180,156\nbuffer_load_dwordx4 v221, s[16:19], s63 offen offset:1024 lds\n"
"G 4\nM 148,160,140\nR 176,187,57344\ns_waitcnt vmcnt(18)\ns_barrier\nM 148,164,156\n"
"s_add_u32 s65, s68, 37868544\nLpg31:\ns_cbranch_execz LretO1n\ns_add_u32 s63, s57, 851968\nM 132,160,136\nR 178,187,61440\n"
"s_add_u32 m0, s50, 24576\nM 132,164,152\nbuffer_load_dwordx4 a[192:195], v252, s[20:23], s64 offen sc1\nM 116,160,132\nR 180,191,0\nbuffer_load_dwordx4 v220, s[16:19], s63 offen lds\n"
"M 116,164,148\ns_add_u32 s66, s69, 37753856\nM 100,160,128\nR 182,191,4096\nM 100,164,144\nG 4\n"
"M 104,168,128\nR 160,188,8192\nbuffer_load_dwordx4 v221, s[16:19], s63 offen offset:1024 lds\nM 104,172,144\nbuffer_load_dwordx4 a[196:199], v252, s[20:23], s65 offen sc1\nM 120,168,132\n"
"R 162,188,12288\nM 120,172,148\ns_add_u32 s67, s69, 37868544\nM 136,168,136\nR 164,188,16384\nM 136,172,152\n"
"buffer_load_dwordx4 a[200:203], v252, s[20:23], s66 offen sc1\nM 152,168,140\nR 166,188,20480\nM 152,172,156\nG 4\nM 156,176,140\n"
"R 168,189,8192\nM 156,180,156\nbuffer_load_dwordx4 a[204:207], v252, s[20:23], s67 offen sc1\nM 140,176,136\nR 170,189,12288\nM 140,180,152\n"
"s_add_u32 s64, s68, 37761024\nM 124,176,132\nR 172,189,16384\nM 124,180,148\ns_add_u32 s65, s68, 37875712\nM 108,176,128\n"
"R 174,189,20480\nM 108,180,144\nG 4\nM 0,160,128\nR 176,190,8192\nM 0,164,144\n"
"buffer_load_dwordx4 a[208:211], v252, s[20:23], s64 offen sc1\nM 16,160,132\nR 178,190,12288\nM 16,164,148\ns_add_u32 s66, s69, 37761024\nv_mfma_f32_16x16x32_bf16 a[224:227], v[160:163], a[136:139], a[224:227]\n"
"R 180,190,16384\nv_mfma_f32_16x16x32_bf16 a[224:227], v[164:167], a[152:155], a[224:227]\nbuffer_load_dwordx4 a[212:215], v252, s[20:23], s65 offen sc1\nv_mfma_f32_16x16x32_bf16 a[240:243], v[160:163], a[140:143], a[240:243]\nR 182,190,20480\nv_mfma_f32_16x16x32_bf16 a[240:243], v[164:167], a[156:159], a[240:243]\n"
"G 4\nv_mfma_f32_16x16x32_bf16 a[244:247], v[168:171], a[140:143], a[244:247]\nR 160,191,8192\nv_mfma_f32_16x16x32_bf16 a[244:247], v[172:175], a[156:159], a[244:247]\nbuffer_load_dwordx4 a[216:219], v252, s[20:23], s66 offen sc1\nv_mfma_f32_16x16x32_bf16 a[228:231], v[168:171], a[136:139], a[228:231]\n"
"R 162,191,12288\nv_mfma_f32_16x16x32_bf16 a[228:231], v[172:175], a[152:155], a[228:231]\ns_add_u32 s67, s69, 37875712\nM 20,168,132\nR 164,191,16384\nM 20,172,148\n"
"M 4,168,128\nR 166,191,20480\nM 4,172,144\nG 4\nM 8,176,128\nR 168,188,24576\n"
"M 8,180,144\nbuffer_load_dwordx4 a[220:223], v252, s[20:23], s67 offen sc1\nM 24,176,132\nR 170,188,28672\nM 24,180,148\ns_add_u32 m0, s50, 32768\n"
"v_mfma_f32_16x16x32_bf16 a[232:235], v[176:179], a[136:139], a[232:235]\nR 172,188,32768\nv_mfma_f32_16x16x32_bf16 a[232:235], v[180:183], a[152:155], a[232:235]\ns_add_u32 s63, s58, 262144\nv_mfma_f32_16x16x32_bf16 a[248:251], v[176:179], a[140:143], a[248:251]\nR 174,188,36864\n"
"v_mfma_f32_16x16x32_bf16 a[248:251], v[180:183], a[156:159], a[248:251]\nG 4\nv_mfma_f32_16x16x32_bf16 a[252:255], v[160:163], a[140:143], a[252:255]\nR 176,189,24576\nv_mfma_f32_16x16x32_bf16 a[252:255], v[164:167], a[156:159], a[252:255]\nbuffer_load_dwordx4 v220, s[16:19], s63 offen lds\n"
"v_mfma_f32_16x16x32_bf16 a[236:239], v[160:163], a[136:139], a[236:239]\nR 178,189,28672\nv_mfma_f32_16x16x32_bf16 a[236:239], v[164:167], a[152:155], a[236:239]\nM 28,160,132\nR 180,189,32768\nM 28,164,148\n"
"buffer_load_dwordx4 v221, s[16:19], s63 offen offset:1024 lds\nM 12,160,128\nR 182,189,36864\nM 12,164,144\ns_waitcnt vmcnt(9)\ns_barrier\n"
"G 4\nM 96,168,160\nR 160,190,24576\nM 96,172,176\ns_add_u32 m0, s50, 40960\nM 112,168,164\n"
"R 162,190,28672\nM 112,172,180\ns_add_u32 s63, s58, 786432\nM 128,168,168\nR 164,190,32768\nv_lshrrev_b32 v230, 8, v252\n"
"M 128,172,184\nbuffer_load_dwordx4 v220, s[16:19], s63 offen lds\nM 144,168,172\nR 166,190,36864\nv_lshlrev_b32 v230, 3, v230\nM 144,172,188\n"
"G 4\nM 148,176,172\nR 168,191,24576\nM 148,180,188\nbuffer_load_dwordx2 v[232:233], v230, s[36:39], s53 offen offset:0\nM 132,176,168\n"
"R 170,191,28672\nM 132,180,184\nbuffer_load_dwordx2 v[234:235], v230, s[36:39], s53 offen offset:32\nM 116,176,164\nR 172,191,32768\nM 116,180,180\n"
"buffer_load_dwordx4 v221, s[16:19], s63 offen offset:1024 lds\nM 100,176,160\nR 174,191,36864\nM 100,180,176\nG 4\nM 104,160,160\n"
"R 176,188,40960\nM 104,164,176\ns_add_u32 m0, s50, 49152\nM 120,160,164\nR 178,188,45056\nM 120,164,180\n"
"s_add_u32 s63, s58, 327680\nM 136,160,168\nR 180,188,49152\nbuffer_load_dwordx2 v[236:237], v230, s[36:39], s53 offen offset:64\nM 136,164,184\nbuffer_load_dwordx4 v220, s[16:19], s63 offen lds\n"
"M 152,160,172\nR 182,188,53248\nM 152,164,188\nbuffer_load_dwordx2 v[238:239], v230, s[36:39], s53 offen offset:96\nG 4\nM 156,168,172\n"
"R 160,189,40960\nv_bfe_u32 v242, v252, 4, 4\nM 156,172,188\nv_lshrrev_b32 v243, 8, v252\nv_lshlrev_b32 v243, 2, v243\nM 140,168,168\n"
"R 162,189,45056\nv_sub_u32 v242, v242, v243\nM 140,172,184\nv_lshlrev_b32 v243, 4, v242\ns_mov_b64 s[60:61], 0x3f80\nv_lshlrev_b64 v[208:209], v243, s[60:61]\n"
"M 124,168,164\nR 164,189,49152\nM 124,172,180\nbuffer_load_dwordx4 v221, s[16:19], s63 offen offset:1024 lds\nM 108,168,160\nR 166,189,53248\n"
"M 108,172,176\nG 4\nM 96,176,192\nR 168,190,40960\ns_waitcnt vmcnt(13)\nM 96,180,208\n"
"s_add_u32 m0, s50, 57344\nM 112,176,196\nR 170,190,45056\ns_waitcnt vmcnt(12)\nM 112,180,212\ns_add_u32 s63, s58, 851968\n"
"M 128,176,200\nR 172,190,49152\ns_waitcnt vmcnt(11)\nM 128,180,216\nbuffer_load_dwordx4 v220, s[16:19], s63 offen lds\nM 144,176,204\n"
"R 174,190,53248\ns_waitcnt vmcnt(11)\nM 144,180,220\nG 4\nM 148,160,204\nR 176,191,40960\n"
"M 148,164,220\nv_cmp_gt_u32 s[60:61], 4, v242\ns_nop 1\nv_cndmask_b32 v212, 0, v208, s[60:61]\nv_cndmask_b32 v208, 0, v208, s[60:61]\nv_cndmask_b32 v213, 0, v209, s[60:61]\n"
"v_cndmask_b32 v209, 0, v209, s[60:61]\nM 132,160,200\nR 178,191,45056\nM 132,164,216\nM 116,160,196\nR 180,191,49152\n"
"M 116,164,212\nbuffer_load_dwordx4 v221, s[16:19], s63 offen offset:1024 lds\nM 100,160,192\nR 182,191,53248\nM 100,164,208\nG 4\n"
"M 104,168,192\nR 160,184,0\nM 104,172,208\ns_add_u32 m0, s50, 65536\nM 120,168,196\nR 162,184,4096\n"
"M 120,172,212\ns_add_u32 s63, s59, 0\nM 136,168,200\nR 164,184,8192\nM 136,172,216\nbuffer_load_dwordx4 v220, s[16:19], s63 offen lds\n"
"M 152,168,204\nR 166,184,12288\nM 152,172,220\nG 4\nM 156,176,204\nR 168,185,0\n"
"M 156,180,220\nM 140,176,200\nR 170,185,4096\nM 140,180,216\nbuffer_load_dwordx4 v221, s[16:19], s63 offen offset:1024 lds\nM 124,176,196\n"
"R 172,185,8192\nM 124,180,212\nM 108,176,192\nR 174,185,12288\nM 108,180,208\nG 4\n"
"M 0,160,160\nR 176,186,0\nM 0,164,176\ns_mov_b32 m0, s51\nM 16,160,164\nR 178,186,4096\n"
"v_cvt_pk_bf16_f32 v128, v128, v129\nv_cvt_pk_bf16_f32 v129, v130, v131\nM 16,164,180\nds_write_addtid_b32 v128 offset:0\nv_cvt_pk_bf16_f32 v130, v132, v133\nv_cvt_pk_bf16_f32 v131, v134, v135\n"
"v_mfma_f32_16x16x32_bf16 a[224:227], v[160:163], a[168:171], a[224:227]\nR 180,186,8192\nv_cvt_pk_bf16_f32 v132, v136, v137\nv_mfma_f32_16x16x32_bf16 a[224:227], v[164:167], a[184:187], a[224:227]\nds_write_addtid_b32 v129 offset:256\nv_cvt_pk_bf16_f32 v133, v138, v139\n"
"v_mfma_f32_16x16x32_bf16 a[240:243], v[160:163], a[172:175], a[240:243]\nR 182,186,12288\nv_cvt_pk_bf16_f32 v134, v140, v141\nv_mfma_f32_16x16x32_bf16 a[240:243], v[164:167], a[188:191], a[240:243]\nds_write_addtid_b32 v130 offset:512\nG 7\n"
"v_cvt_pk_bf16_f32 v135, v142, v143\nv_mfma_f32_16x16x32_bf16 a[244:247], v[168:171], a[172:175], a[244:247]\nR 160,187,0\nv_cvt_pk_bf16_f32 v136, v144, v145\nv_mfma_f32_16x16x32_bf16 a[244:247], v[172:175], a[188:191], a[244:247]\nds_write_addtid_b32 v131 offset:768\n"
"v_cvt_pk_bf16_f32 v137, v146, v147\nv_mfma_f32_16x16x32_bf16 a[228:231], v[168:171], a[168:171], a[228:231]\nR 162,187,4096\nv_cvt_pk_bf16_f32 v138, v148, v149\nv_mfma_f32_16x16x32_bf16 a[228:231], v[172:175], a[184:187], a[228:231]\nds_write_addtid_b32 v132 offset:1024\n"
"v_cvt_pk_bf16_f32 v139, v150, v151\nM 20,168,164\nR 164,187,8192\nv_cvt_pk_bf16_f32 v140, v152, v153\nM 20,172,180\nds_write_addtid_b32 v133 offset:1280\n"
"v_cvt_pk_bf16_f32 v141, v154, v155\nM 4,168,160\nR 166,187,12288\nv_cvt_pk_bf16_f32 v142, v156, v157\nM 4,172,176\nds_write_addtid_b32 v134 offset:1536\n"
"G 9\nv_cvt_pk_bf16_f32 v143, v158, v159\nM 8,176,160\nR 168,184,16384\nds_write_addtid_b32 v135 offset:1792\nM 8,180,176\n"
"ds_write_addtid_b32 v136 offset:2048\nds_write_addtid_b32 v137 offset:2304\nM 24,176,164\nR 170,184,20480\nds_write_addtid_b32 v138 offset:2560\nM 24,180,180\n"
"ds_write_addtid_b32 v139 offset:2816\nds_write_addtid_b32 v140 offset:3072\nv_mfma_f32_16x16x32_bf16 a[232:235], v[176:179], a[168:171], a[232:235]\nR 172,184,24576\nds_write_addtid_b32 v141 offset:3328\nv_mfma_f32_16x16x32_bf16 a[232:235], v[180:183], a[184:187], a[232:235]\n"
"ds_write_addtid_b32 v142 offset:3584\nds_write_addtid_b32 v143 offset:3840\nv_mfma_f32_16x16x32_bf16 a[248:251], v[176:179], a[172:175], a[248:251]\nR 174,184,28672\ns_add_u32 m0, s50, 73728\nv_mfma_f32_16x16x32_bf16 a[248:251], v[180:183], a[188:191], a[248:251]\n"
"s_add_u32 s63, s59, 524288\nG 14\nv_mfma_f32_16x16x32_bf16 a[252:255], v[160:163], a[172:175], a[252:255]\nR 176,185,16384\ns_waitcnt vmcnt(8) lgkmcnt(2)\ns_barrier\n"
"v_mfma_f32_16x16x32_bf16 a[252:255], v[164:167], a[188:191], a[252:255]\nbuffer_load_dwordx4 v220, s[16:19], s63 offen lds\nv_mfma_f32_16x16x32_bf16 a[236:239], v[160:163], a[168:171], a[236:239]\nR 178,185,20480\nv_mfma_f32_16x16x32_bf16 a[236:239], v[164:167], a[184:187], a[236:239]\nds_read2st64_b32 v[128:129], v225 offset0:0 offset1:1\n"
"M 28,160,164\nR 180,185,24576\nM 28,164,180\nbuffer_load_dwordx4 v221, s[16:19], s63 offen offset:1024 lds\nM 12,160,160\nR 182,185,28672\n"
"s_add_u32 m0, s50, 81920\nM 12,164,176\nds_read2st64_b32 v[130:131], v225 offset0:2 offset1:3\nG 6\nM 0,168,192\nR 160,186,16384\n"
"s_add_u32 s63, s59, 65536\nM 0,172,208\nbuffer_load_dwordx4 v220, s[16:19], s63 offen lds\nds_read2st64_b32 v[132:133], v225 offset0:4 offset1:5\nM 16,168,196\nR 162,186,20480\n"
"ds_read2st64_b32 v[134:135], v225 offset0:6 offset1:7\nM 16,172,212\nds_read2st64_b32 v[136:137], v225 offset0:8 offset1:9\nv_mfma_f32_16x16x32_bf16 a[224:227], v[168:171], a[200:203], a[224:227]\nR 164,186,24576\nv_mfma_f32_16x16x32_bf16 a[224:227], v[172:175], a[216:219], a[224:227]\n"
"buffer_load_dwordx4 v221, s[16:19], s63 offen offset:1024 lds\nG 6\nv_mfma_f32_16x16x32_bf16 v[96:99], v[208:211], v[128:131], v[96:99]\nv_mfma_f32_16x16x32_bf16 v[100:103], v[210:213], v[128:131], v[100:103]\nds_read2st64_b32 v[138:139], v225 offset0:10 offset1:11\nv_mfma_f32_16x16x32_bf16 a[240:243], v[168:171], a[204:207], a[240:243]\n"
"R 166,186,28672\ns_add_u32 m0, s50, 90112\nv_mfma_f32_16x16x32_bf16 a[240:243], v[172:175], a[220:223], a[240:243]\nds_read2st64_b32 v[140:141], v225 offset0:12 offset1:13\nv_mfma_f32_16x16x32_bf16 a[244:247], v[176:179], a[204:207], a[244:247]\nR 168,187,16384\n"
"s_add_u32 s63, s59, 589824\nG 6\nv_mfma_f32_16x16x32_bf16 v[104:107], v[208:211], v[132:135], v[104:107]\nv_mfma_f32_16x16x32_bf16 v[108:111], v[210:213], v[132:135], v[108:111]\nds_read2st64_b32 v[142:143], v225 offset0:14 offset1:15\nv_mfma_f32_16x16x32_bf16 a[244:247], v[180:183], a[220:223], a[244:247]\n"
"buffer_load_dwordx4 v220, s[16:19], s63 offen lds\nv_mfma_f32_16x16x32_bf16 a[228:231], v[176:179], a[200:203], a[228:231]\nR 170,187,20480\nv_mfma_f32_16x16x32_bf16 a[228:231], v[180:183], a[216:219], a[228:231]\nG 5\nv_mfma_f32_16x16x32_bf16 v[112:115], v[208:211], v[136:139], v[112:115]\n"
"v_mfma_f32_16x16x32_bf16 v[116:119], v[210:213], v[136:139], v[116:119]\nM 20,176,196\nR 172,187,24576\nM 20,180,212\nbuffer_load_dwordx4 v221, s[16:19], s63 offen offset:1024 lds\nM 4,176,192\n"
"R 174,187,28672\ns_add_u32 s63, s59, 131072\nG 3\nv_mfma_f32_16x16x32_bf16 v[120:123], v[208:211], v[140:143], v[120:123]\nv_mfma_f32_16x16x32_bf16 v[124:127], v[210:213], v[140:143], v[124:127]\nM 4,180,208\n"
"M 8,160,192\nR 176,184,32768\nM 8,164,208\ns_add_u32 m0, s50, 98304\nM 24,160,196\nR 178,184,36864\n"
"M 24,164,212\nbuffer_load_dwordx4 v220, s[16:19], s63 offen lds\nv_mfma_f32_16x16x32_bf16 a[232:235], v[160:163], a[200:203], a[232:235]\nR 180,184,40960\nv_cvt_pk_bf16_f32 v120, v120, v121\nv_cvt_pk_bf16_f32 v112, v112, v113\n"
"v_cvt_pk_bf16_f32 v121, v122, v123\nv_mfma_f32_16x16x32_bf16 a[232:235], v[164:167], a[216:219], a[232:235]\nv_cvt_pk_bf16_f32 v122, v124, v125\nv_cvt_pk_bf16_f32 v123, v126, v127\nv_cvt_pk_bf16_f32 v96, v96, v97\nv_cvt_pk_bf16_f32 v113, v114, v115\n"
"v_mfma_f32_16x16x32_bf16 a[248:251], v[160:163], a[204:207], a[248:251]\nR 182,184,45056\nv_cvt_pk_bf16_f32 v114, v116, v117\nv_cvt_pk_bf16_f32 v97, v98, v99\nv_mfma_f32_16x16x32_bf16 a[248:251], v[164:167], a[220:223], a[248:251]\nG 4\n"
"v_cvt_pk_bf16_f32 v98, v100, v101\nv_cvt_pk_bf16_f32 v99, v102, v103\nv_mfma_f32_16x16x32_bf16 a[252:255], v[168:171], a[204:207], a[252:255]\nR 160,185,32768\ns_bfe_u32 s100, s56, 0x20007\ns_mul_i32 s100, s100, 49152\n"
"s_add_u32 s100, s100, s72\ns_add_u32 s100, s100, 65011712\nv_cvt_pk_bf16_f32 v115, v118, v119\nv_mfma_f32_16x16x32_bf16 a[252:255], v[172:175], a[220:223], a[252:255]\nbuffer_store_dwordx4 v[96:99], v252, s[20:23], s100 offen offset:0 sc1\nv_mfma_f32_16x16x32_bf16 a[236:239], v[168:171], a[200:203], a[236:239]\n"
"R 162,185,36864\nv_cvt_pk_bf16_f32 v104, v104, v105\nv_mfma_f32_16x16x32_bf16 a[236:239], v[172:175], a[216:219], a[236:239]\nv_cvt_pk_bf16_f32 v105, v106, v107\nv_cvt_pk_bf16_f32 v106, v108, v109\nM 28,168,196\n"
"R 164,185,40960\nv_cvt_pk_bf16_f32 v107, v110, v111\nM 28,172,212\nbuffer_load_dwordx4 v221, s[16:19], s63 offen offset:1024 lds\nM 12,168,192\nR 166,185,45056\n"
"M 12,172,208\nbuffer_store_dwordx4 v[104:107], v252, s[20:23], s100 offen offset:1024 sc1\ns_waitcnt vmcnt(12)\ns_barrier\nG 4\nM 32,176,160\n"
"R 168,186,32768\nM 32,180,176\nv_xor_b32 v240, 0x2000, v225\nM 48,176,164\nR 170,186,36864\nds_write2st64_b32 v240, a224, a225 offset0:0 offset1:1\n"
"M 48,180,180\nbuffer_store_dwordx4 v[112:115], v252, s[20:23], s100 offen offset:2048 sc1\nds_write2st64_b32 v240, a226, a227 offset0:2 offset1:3\nM 64,176,168\nR 172,186,40960\nds_write2st64_b32 v240, a228, a229 offset0:4 offset1:5\n"
"M 64,180,184\nds_write2st64_b32 v240, a230, a231 offset0:6 offset1:7\nM 80,176,172\nR 174,186,45056\nds_write2st64_b32 v240, a232, a233 offset0:8 offset1:9\nM 80,180,188\n"
"ds_write2st64_b32 v240, a234, a235 offset0:10 offset1:11\nG 10\nM 84,160,172\nR 176,187,32768\ns_add_u32 s63, s59, 655360\nds_write2st64_b32 v240, a236, a237 offset0:12 offset1:13\n"
"M 84,164,188\nbuffer_store_dwordx4 v[120:123], v252, s[20:23], s100 offen offset:3072 sc1\nds_write2st64_b32 v240, a238, a239 offset0:14 offset1:15\nM 68,160,168\nR 178,187,36864\nds_write2st64_b32 v240, a240, a241 offset0:16 offset1:17\n"
"M 68,164,184\ns_add_u32 m0, s50, 106496\nds_write2st64_b32 v240, a242, a243 offset0:18 offset1:19\nM 52,160,164\nR 180,187,40960\nds_write2st64_b32 v240, a244, a245 offset0:20 offset1:21\n"
"M 52,164,180\nbuffer_load_dwordx4 v220, s[16:19], s63 offen lds\nds_write2st64_b32 v240, a246, a247 offset0:22 offset1:23\nM 36,160,160\nR 182,187,45056\nds_write2st64_b32 v240, a248, a249 offset0:24 offset1:25\n"
"M 36,164,176\nds_write2st64_b32 v240, a250, a251 offset0:26 offset1:27\nG 14\nM 40,168,160\nR 160,184,49152\nds_write2st64_b32 v240, a252, a253 offset0:28 offset1:29\n"
"M 40,172,176\nM 56,168,164\nR 162,184,53248\nds_write2st64_b32 v240, a254, a255 offset0:30 offset1:31\nM 56,172,180\nbuffer_load_dwordx4 v221, s[16:19], s63 offen offset:1024 lds\n"
"M 72,168,168\nR 164,184,57344\ns_add_u32 s63, s59, 196608\nM 72,172,184\ns_add_u32 m0, s50, 114688\nM 88,168,172\n"
"R 166,184,61440\nM 88,172,188\nG 8\nM 92,176,172\nR 168,185,49152\ns_waitcnt vmcnt(12) lgkmcnt(3)\n"
"s_barrier\nM 92,180,188\nbuffer_load_dwordx4 v220, s[16:19], s63 offen lds\nds_read2st64_b32 v[128:129], v225 offset0:0 offset1:1\nM 76,176,168\nR 170,185,53248\n"
"ds_read2st64_b32 v[130:131], v225 offset0:2 offset1:3\nM 76,180,184\nds_read2st64_b32 v[132:133], v225 offset0:4 offset1:5\nds_read2st64_b32 v[134:135], v225 offset0:6 offset1:7\nM 60,176,164\nR 172,185,57344\n"
"ds_read2st64_b32 v[136:137], v225 offset0:8 offset1:9\nM 60,180,180\nbuffer_load_dwordx4 v221, s[16:19], s63 offen offset:1024 lds\nds_read2st64_b32 v[138:139], v225 offset0:10 offset1:11\nM 44,176,160\nLpg32:\n"
"s_cbranch_execz LretW1z\nR 174,185,61440\ns_add_u32 m0, s50, 0\nds_read2st64_b32 v[140:141], v225 offset0:12 offset1:13\nM 44,180,176\nds_read2st64_b32 v[142:143], v225 offset0:14 offset1:15\n"
"ds_read2st64_b32 v[144:145], v225 offset0:16 offset1:17\nG 13\nM 32,160,192\nR 176,186,49152\ns_add_u32 s63, s59, 720896\nds_read2st64_b32 v[146:147], v225 offset0:18 offset1:19\n"
"M 32,164,208\nbuffer_load_dwordx4 v220, s[16:19], s63 offen lds\nds_read2st64_b32 v[148:149], v225 offset0:20 offset1:21\nM 48,160,196\nR 178,186,53248\nds_read2st64_b32 v[150:151], v225 offset0:22 offset1:23\n"
"G 15\nv_add_f32 v0, v0, v128\nM 48,164,212\nds_read2st64_b32 v[152:153], v225 offset0:24 offset1:25\nds_read2st64_b32 v[154:155], v225 offset0:26 offset1:27\nv_add_f32 v1, v1, v129\n"
"M 64,160,200\nR 180,186,57344\nds_read2st64_b32 v[156:157], v225 offset0:28 offset1:29\nG 15\nv_add_f32 v2, v2, v130\nM 64,164,216\n"
"buffer_load_dwordx4 v221, s[16:19], s63 offen offset:1024 lds\nds_read2st64_b32 v[158:159], v225 offset0:30 offset1:31\nM 80,160,204\nR 182,186,61440\ns_add_u32 m0, s50, 8192\nv_add_f32 v3, v3, v131\n"
"M 80,164,220\nG 14\nv_add_f32 v4, v4, v132\nv_add_f32 v5, v5, v133\nv_add_f32 v6, v6, v134\nM 84,168,204\n"
"R 160,187,49152\ns_add_u32 s63, s59, 393216\nv_add_f32 v7, v7, v135\nM 84,172,220\nbuffer_load_dwordx4 v220, s[16:19], s63 offen lds\nv_add_f32 v8, v8, v136\n"
"M 68,168,200\nR 162,187,53248\nv_add_f32 v9, v9, v137\nv_add_f32 v10, v10, v138\nM 68,172,216\nv_add_f32 v11, v11, v139\n"
"G 15\nv_add_f32 v12, v12, v140\nv_add_f32 v13, v13, v141\nM 52,168,196\nR 164,187,57344\nG 15\n"
"v_add_f32 v14, v14, v142\nv_add_f32 v15, v15, v143\nM 52,172,212\nbuffer_load_dwordx4 v221, s[16:19], s63 offen offset:1024 lds\nG 14\nv_add_f32 v16, v16, v144\n"
"M 36,168,192\nR 166,187,61440\ns_add_u32 s63, s59, 458752\nv_add_f32 v17, v17, v145\nM 36,172,208\nG 4\n"
"v_add_f32 v18, v18, v146\nv_add_f32 v19, v19, v147\nv_add_f32 v20, v20, v148\nM 40,176,192\nR 168,188,0\nv_add_f32 v21, v21, v149\n"
"v_add_f32 v22, v22, v150\nM 40,180,208\ns_add_u32 m0, s50, 16384\nv_add_f32 v23, v23, v151\nv_add_f32 v24, v24, v152\nv_add_f32 v25, v25, v153\n"
"M 56,176,196\nR 170,188,4096\nv_add_f32 v26, v26, v154\nv_add_f32 v27, v27, v155\nM 56,180,212\nbuffer_load_dwordx4 v220, s[16:19], s63 offen lds\n"
"v_add_f32 v28, v28, v156\nv_add_f32 v29, v29, v157\nv_add_f32 v30, v30, v158\nv_add_f32 v31, v31, v159\nM 72,176,200\nR 172,188,8192\n"
"v_cvt_pk_bf16_f32 v24, v24, v25\nv_cvt_pk_bf16_f32 v25, v26, v27\nM 72,180,216\nv_cvt_pk_bf16_f32 v26, v28, v29\nv_cvt_pk_bf16_f32 v27, v30, v31\nv_cvt_pk_bf16_f32 v0, v0, v1\n"
"v_cvt_pk_bf16_f32 v16, v16, v17\nM 88,176,204\nR 174,188,12288\nv_cvt_pk_bf16_f32 v1, v2, v3\nM 88,180,220\nG 4\n"
"v_cvt_pk_bf16_f32 v2, v4, v5\nv_cvt_pk_bf16_f32 v3, v6, v7\nM 92,160,204\nR 176,189,0\ns_bfe_u32 s100, s57, 0x20007\ns_mul_i32 s100, s100, 49152\n"
"s_add_u32 s100, s100, s72\ns_add_u32 s100, s100, 65028096\nv_cvt_pk_bf16_f32 v17, v18, v19\nM 92,164,220\nbuffer_store_dwordx4 v[0:3], v252, s[20:23], s100 offen offset:0 sc1\nM 76,160,200\n"
"R 178,189,4096\nv_cvt_pk_bf16_f32 v8, v8, v9\nM 76,164,216\nv_cvt_pk_bf16_f32 v9, v10, v11\nv_cvt_pk_bf16_f32 v10, v12, v13\nM 60,160,196\n"
"R 180,189,8192\nv_cvt_pk_bf16_f32 v11, v14, v15\nM 60,164,212\nbuffer_load_dwordx4 v221, s[16:19], s63 offen offset:1024 lds\nM 44,160,192\nR 182,189,12288\n"
"v_cvt_pk_bf16_f32 v18, v20, v21\nM 44,164,208\nbuffer_store_dwordx4 v[8:11], v252, s[20:23], s100 offen offset:1024 sc1\ns_waitcnt vmcnt(18)\ns_barrier\nG 4\n"
"v_mfma_f32_16x16x32_bf16 v[96:99], v[168:171], a[0:3], 0\nR 160,190,0\nv_cvt_pk_bf16_f32 v19, v22, v23\nM 96,172,16\ns_mov_b32 m0, s51\nv_mfma_f32_16x16x32_bf16 v[112:115], v[168:171], a[4:7], 0\n"
"R 162,190,4096\nv_cvt_pk_bf16_f32 v64, v64, v65\nv_cvt_pk_bf16_f32 v65, v66, v67\nM 112,172,20\nbuffer_store_dwordx4 v[16:19], v252, s[20:23], s100 offen offset:2048 sc1\nv_cvt_pk_bf16_f32 v66, v68, v69\n"
"v_mfma_f32_16x16x32_bf16 v[128:131], v[168:171], a[8:11], 0\nR 164,190,8192\nv_cvt_pk_bf16_f32 v67, v70, v71\nv_cvt_pk_bf16_f32 v68, v72, v73\nM 128,172,24\nds_write_addtid_b32 v64 offset:0\n"
"v_cvt_pk_bf16_f32 v69, v74, v75\nv_cvt_pk_bf16_f32 v70, v76, v77\nv_mfma_f32_16x16x32_bf16 v[144:147], v[168:171], a[12:15], 0\nR 166,190,12288\nv_cvt_pk_bf16_f32 v71, v78, v79\nv_cvt_pk_bf16_f32 v72, v80, v81\n"
"M 144,172,28\nds_write_addtid_b32 v65 offset:256\nG 6\nv_cvt_pk_bf16_f32 v73, v82, v83\nv_mfma_f32_16x16x32_bf16 v[148:151], v[176:179], a[12:15], 0\nR 168,191,0\n"
"v_cvt_pk_bf16_f32 v74, v84, v85\nv_cvt_pk_bf16_f32 v75, v86, v87\nM 148,180,28\nbuffer_store_dwordx4 v[24:27], v252, s[20:23], s100 offen offset:3072 sc1\nv_mfma_f32_16x16x32_bf16 v[132:135], v[176:179], a[8:11], 0\nR 170,191,4096\n"
"v_cvt_pk_bf16_f32 v76, v88, v89\nM 132,180,24\nds_write_addtid_b32 v66 offset:512\nv_cvt_pk_bf16_f32 v77, v90, v91\nv_mfma_f32_16x16x32_bf16 v[116:119], v[176:179], a[4:7], 0\nR 172,191,8192\n"
"v_cvt_pk_bf16_f32 v78, v92, v93\nM 116,180,20\nds_write_addtid_b32 v67 offset:768\nv_cvt_pk_bf16_f32 v79, v94, v95\nv_mfma_f32_16x16x32_bf16 v[100:103], v[176:179], a[0:3], 0\nR 174,191,12288\n"
"ds_write_addtid_b32 v68 offset:1024\nM 100,180,16\nds_write_addtid_b32 v69 offset:1280\nds_write_addtid_b32 v70 offset:1536\nG 10\nv_mfma_f32_16x16x32_bf16 v[104:107], v[160:163], a[0:3], 0\n"
"R 176,188,16384\nds_write_addtid_b32 v71 offset:1792\nM 104,164,16\nds_write_addtid_b32 v72 offset:2048\nds_write_addtid_b32 v73 offset:2304\nv_mfma_f32_16x16x32_bf16 v[120:123], v[160:163], a[4:7], 0\n"
"R 178,188,20480\nds_write_addtid_b32 v74 offset:2560\nM 120,164,20\nds_write_addtid_b32 v75 offset:2816\nds_write_addtid_b32 v76 offset:3072\nv_mfma_f32_16x16x32_bf16 v[136:139], v[160:163], a[8:11], 0\n"
"R 180,188,24576\nds_write_addtid_b32 v77 offset:3328\nM 136,164,24\nds_write_addtid_b32 v78 offset:3584\nds_write_addtid_b32 v79 offset:3840\nv_mfma_f32_16x16x32_bf16 v[152:155], v[160:163], a[12:15], 0\n"
"R 182,188,28672\ns_add_u32 m0, s50, 24576\nM 152,164,28\ns_add_u32 s63, s59, 262144\nG 15\nv_mfma_f32_16x16x32_bf16 v[156:159], v[168:171], a[12:15], 0\n"
"R 160,189,16384\ns_waitcnt vmcnt(12) lgkmcnt(2)\ns_barrier\nM 156,172,28\nbuffer_load_dwordx4 v220, s[16:19], s63 offen lds\nv_mfma_f32_16x16x32_bf16 v[140:143], v[168:171], a[8:11], 0\n"
"R 162,189,20480\nM 140,172,24\nds_read2st64_b32 v[64:65], v225 offset0:0 offset1:1\nv_mfma_f32_16x16x32_bf16 v[124:127], v[168:171], a[4:7], 0\nR 164,189,24576\nM 124,172,20\n"
"ds_read2st64_b32 v[66:67], v225 offset0:2 offset1:3\nv_mfma_f32_16x16x32_bf16 v[108:111], v[168:171], a[0:3], 0\nR 166,189,28672\nM 108,172,16\nds_read2st64_b32 v[68:69], v225 offset0:4 offset1:5\nG 7\n"
"M 96,176,32\nR 168,190,16384\nv_bfe_u32 v250, v252, 4, 2\nv_lshlrev_b32 v250, 3, v250\nM 96,180,48\nbuffer_load_dwordx4 v221, s[16:19], s63 offen offset:1024 lds\n"
"s_lshl_b32 s60, s43, 5\nv_add_u32 v250, s60, v250\nM 112,176,36\nR 170,190,20480\nds_read2st64_b32 v[70:71], v225 offset0:6 offset1:7\nM 112,180,52\n"
"s_add_u32 m0, s50, 32768\nds_read2st64_b32 v[72:73], v225 offset0:8 offset1:9\nG 6\nv_mfma_f32_16x16x32_bf16 v[32:35], v[208:211], v[64:67], v[32:35]\nv_mfma_f32_16x16x32_bf16 v[36:39], v[210:213], v[64:67], v[36:39]\nM 128,176,40\n"
"R 172,190,24576\nM 128,180,56\ns_add_u32 s63, s59, 786432\nds_read2st64_b32 v[74:75], v225 offset0:10 offset1:11\nM 144,176,44\nR 174,190,28672\n"
"M 144,180,60\nds_read2st64_b32 v[76:77], v225 offset0:12 offset1:13\nG 9\nM 148,160,44\nR 176,191,16384\nG 6\n"
"v_mfma_f32_16x16x32_bf16 v[40:43], v[208:211], v[68:71], v[40:43]\nv_mfma_f32_16x16x32_bf16 v[44:47], v[210:213], v[68:71], v[44:47]\nds_read2st64_b32 v[78:79], v225 offset0:14 offset1:15\nM 148,164,60\nbuffer_load_dwordx4 v220, s[16:19], s63 offen lds\nM 132,160,40\n"
"R 178,191,20480\nM 132,164,56\nG 5\nv_mfma_f32_16x16x32_bf16 v[48:51], v[208:211], v[72:75], v[48:51]\nv_mfma_f32_16x16x32_bf16 v[52:55], v[210:213], v[72:75], v[52:55]\nM 116,160,36\n"
"R 180,191,24576\nM 116,164,52\nbuffer_load_dwordx4 v221, s[16:19], s63 offen offset:1024 lds\nM 100,160,32\nR 182,191,28672\ns_add_u32 m0, s50, 40960\n"
"G 3\nv_mfma_f32_16x16x32_bf16 v[56:59], v[208:211], v[76:79], v[56:59]\nv_mfma_f32_16x16x32_bf16 v[60:63], v[210:213], v[76:79], v[60:63]\nM 100,164,48\nM 104,168,32\nR 160,188,32768\n"
"s_add_u32 s63, s59, 327680\nM 104,172,48\nbuffer_load_dwordx4 v220, s[16:19], s63 offen lds\nM 120,168,36\nR 162,188,36864\nv_mov_b32 v226, s46\n"
"M 120,172,52\nv_mov_b32 v227, s47\nM 136,168,40\nR 164,188,40960\ns_bfe_u32 s62, s56, 0x20007\ns_lshl_b32 s62, s62, 5\n"
"s_add_u32 s61, s74, 1009664\ns_add_u32 s61, s61, s62\ns_waitcnt vmcnt(5)\ns_mov_b64 exec, 1\nbuffer_atomic_swap_x2 v[226:227], off, s[20:23], s61 sc1\ns_mov_b64 exec, -1\n"
"v_cvt_pk_bf16_f32 v32, v32, v33\nM 136,172,56\ns_bfe_u32 s62, s57, 0x20007\ns_lshl_b32 s62, s62, 5\ns_add_u32 s61, s74, 1009672\ns_add_u32 s61, s61, s62\n"
"s_mov_b64 exec, 1\nbuffer_atomic_swap_x2 v[226:227], off, s[20:23], s61 sc1\ns_mov_b64 exec, -1\nM 152,168,44\nR 166,188,45056\nv_cvt_pk_bf16_f32 v33, v34, v35\n"
"M 152,172,60\nG 4\nv_cvt_pk_bf16_f32 v34, v36, v37\nv_cvt_pk_bf16_f32 v35, v38, v39\nM 156,176,44\nR 168,189,32768\n"
"s_bfe_u32 s100, s58, 0x20007\ns_mul_i32 s100, s100, 49152\ns_add_u32 s100, s100, s72\ns_add_u32 s100, s100, 65044480\nv_cvt_pk_bf16_f32 v48, v48, v49\nM 156,180,60\n"
"buffer_store_dwordx4 v[32:35], v252, s[20:23], s100 offen offset:0 sc1\nM 140,176,40\nR 170,189,36864\nv_cvt_pk_bf16_f32 v40, v40, v41\nM 140,180,56\nv_cvt_pk_bf16_f32 v41, v42, v43\n"
"v_cvt_pk_bf16_f32 v42, v44, v45\nM 124,176,36\nR 172,189,40960\nv_cvt_pk_bf16_f32 v43, v46, v47\nM 124,180,52\nv_cvt_pk_bf16_f32 v49, v50, v51\n"
"v_cvt_pk_bf16_f32 v50, v52, v53\nM 108,176,32\nR 174,189,45056\nv_cvt_pk_bf16_f32 v51, v54, v55\nM 108,180,48\nbuffer_store_dwordx4 v[40:43], v252, s[20:23], s100 offen offset:1024 sc1\n"
"s_barrier\nG 4\nM 96,160,64\nR 176,190,32768\nv_cvt_pk_bf16_f32 v56, v56, v57\nM 96,164,80\n"
"v_cvt_pk_bf16_f32 v57, v58, v59\nv_cvt_pk_bf16_f32 v58, v60, v61\nM 112,160,68\nR 178,190,36864\nv_cvt_pk_bf16_f32 v59, v62, v63\nM 112,164,84\n"
"buffer_store_dwordx4 v[48:51], v252, s[20:23], s100 offen offset:2048 sc1\nM 128,160,72\nR 180,190,40960\nM 128,164,88\nM 144,160,76\nR 182,190,45056\n"
"M 144,164,92\nG 4\nM 148,168,76\nR 160,191,32768\nM 148,172,92\nbuffer_store_dwordx4 v[56:59], v252, s[20:23], s100 offen offset:3072 sc1\n"
"M 132,168,72\nR 162,191,36864\nM 132,172,88\nM 116,168,68\nR 164,191,40960\nM 116,172,84\n"
"M 100,168,64\nR 166,191,45056\nv_mov_b32 v248, 0\nM 100,172,80\nG 4\nv_mov_b32 v249, 0\n"
"M 104,176,64\nR 168,188,49152\ns_add_u32 s61, s74, 1009664\ns_mov_b64 exec, 3\nbuffer_atomic_or_x2 v[248:249], v250, s[20:23], s61 offen sc0 sc1\ns_mov_b64 exec, -1\n"
"M 104,180,80\nM 120,176,68\nR 170,188,53248\nv_mov_b32 v242, v208\nM 120,180,84\nbuffer_load_dwordx4 v221, s[16:19], s63 offen offset:1024 lds\n"
"M 136,176,72\nR 172,184,0\nv_mov_b32 v243, v209\nM 136,180,88\ns_add_u32 m0, s50, 49152\nv_mov_b32 v244, v208\n"
"v_mov_b32 v245, v209\nM 152,176,76\nR 174,184,4096\nv_mov_b32 v64, v192\nM 152,180,92\nG 4\n"
"v_mov_b32 v65, v193\nv_mov_b32 v70, v194\nM 156,160,76\nR 176,189,49152\nv_mov_b32 v71, v195\nM 156,164,92\n"
"s_add_u32 s63, s59, 851968\nv_mov_b32 v72, v196\nv_mov_b32 v73, v197\nM 140,160,72\nR 178,189,53248\nv_mov_b32 v78, v198\n"
"M 140,164,88\nbuffer_load_dwordx4 v220, s[16:19], s63 offen lds\nM 124,160,68\nR 180,185,0\nv_mov_b32 v79, v199\nM 124,164,84\n"
"v_mov_b32 v80, v200\nv_mov_b32 v81, v201\nM 108,160,64\nR 182,185,4096\nv_mov_b32 v86, v202\nM 108,164,80\n"
"G 4\nv_mov_b32 v87, v203\nv_mov_b32 v88, v204\nM 96,168,96\nR 160,190,49152\nv_mov_b32 v89, v205\n"
"M 96,172,112\nv_mov_b32 v94, v206\nv_mov_b32 v95, v207\nM 112,168,100\nR 162,190,53248\nv_mov_b32 v32, v232\n"
"M 112,172,116\nbuffer_load_dwordx4 v221, s[16:19], s63 offen offset:1024 lds\nM 128,168,104\nR 164,186,0\nv_mov_b32 v33, v233\nM 128,172,120\n"
"v_mov_b32 v38, v234\nv_mov_b32 v39, v235\nM 144,168,108\nR 166,186,4096\nv_mov_b32 v40, v236\nM 144,172,124\n"
"G 4\nv_mov_b32 v41, v237\nv_mov_b32 v46, v238\nM 148,176,108\nR 168,191,49152\nv_mov_b32 v47, v239\n"
"M 148,180,124\nv_mov_b32 v48, v232\nv_mov_b32 v49, v233\nM 132,176,104\nR 170,191,53248\nv_mov_b32 v54, v234\n"
"M 132,180,120\nv_mov_b32 v55, v235\nv_mov_b32 v56, v236\nM 116,176,100\nR 172,187,0\nv_mov_b32 v57, v237\n"
"M 116,180,116\nv_mov_b32 v62, v238\nv_mov_b32 v63, v239\nM 100,176,96\nR 174,187,4096\nM 100,180,112\n"
"G 4\nM 104,160,96\nR 176,184,8192\ns_waitcnt vmcnt(11)\ns_barrier\nM 104,164,112\n"
"M 120,160,100\nR 178,184,12288\nM 120,164,116\nM 136,160,104\nR 180,184,16384\nM 136,164,120\n"
"M 152,160,108\nR 182,184,20480\nM 152,164,124\nG 4\nM 156,168,108\nR 160,185,8192\n"
"M 156,172,124\nM 140,168,104\nR 162,185,12288\nM 140,172,120\nM 124,168,100\nR 164,185,16384\n"
"M 124,172,116\nM 108,168,96\nR 166,185,20480\nv_mov_b32 v226, s46\nM 108,172,112\nG 4\n"
"v_mov_b32 v227, s47\nM 96,176,128\nR 168,186,8192\ns_bfe_u32 s62, s58, 0x20007\ns_lshl_b32 s62, s62, 5\ns_add_u32 s61, s74, 1009680\n"
"s_add_u32 s61, s61, s62\ns_waitcnt vmcnt(4)\ns_mov_b64 exec, 1\nbuffer_atomic_swap_x2 v[226:227], off, s[20:23], s61 sc1\ns_mov_b64 exec, -1\nM 96,180,144\n"
"s_mul_i32 s64, s43, 49152\nM 112,176,132\nR 170,186,12288\nM 112,180,148\ns_add_u32 s64, s64, s72\nM 128,176,136\n"
"R 172,186,16384\ns_waitcnt vmcnt(4)\nv_cmp_eq_u64 s[86:87], s[46:47], v[248:249]\nM 128,180,152\ns_add_u32 s64, s64, 65011712\nM 144,176,140\n"
"R 174,186,20480\ns_and_b32 s62, s86, 3\ns_cmp_eq_u32 s62, 3\ns_cbranch_scc0 Lq967\nLq968:\nM 144,180,156\n"
"G 4\nM 148,160,140\nR 176,187,8192\nM 148,164,156\nbuffer_load_dwordx4 v[2:5], v252, s[20:23], s64 offen offset:0 sc1\nM 132,160,136\n"
"R 178,187,12288\nM 132,164,152\nbuffer_load_dwordx4 v[10:13], v252, s[20:23], s64 offen offset:1024 sc1\nM 116,160,132\nR 180,187,16384\nM 116,164,148\n"
"s_mul_i32 s65, s43, 49152\nM 100,160,128\nR 182,187,20480\nM 100,164,144\nbuffer_load_dwordx4 v[18:21], v252, s[20:23], s64 offen offset:2048 sc1\nG 4\n"
"M 104,168,128\nR 160,184,24576\nM 104,172,144\nbuffer_load_dwordx4 v[26:29], v252, s[20:23], s64 offen offset:3072 sc1\nM 120,168,132\nR 162,184,28672\n"
"M 120,172,148\ns_add_u32 s65, s65, s72\nM 136,168,136\nR 164,184,32768\ns_add_u32 s65, s65, 65028096\nM 136,172,152\n"
"buffer_load_dwordx4 v[66:69], v252, s[20:23], s65 offen offset:0 sc1\nM 152,168,140\nR 166,184,36864\nM 152,172,156\nG 4\nM 156,176,140\n"
"R 168,185,24576\ns_waitcnt vmcnt(6)\ns_barrier\nM 156,180,156\nbuffer_load_dwordx4 v[74:77], v252, s[20:23], s65 offen offset:1024 sc1\nM 140,176,136\n"
"R 170,185,28672\nv_mov_b32 v246, 0\nM 140,180,152\nbuffer_load_dwordx4 v[82:85], v252, s[20:23], s65 offen offset:2048 sc1\nLpg33:\ns_cbranch_execz LretW1z\n"
"v_mov_b32 v247, 0\nM 124,176,132\nR 172,185,32768\ns_add_u32 s61, s74, 1009664\ns_mov_b64 exec, 4\nbuffer_atomic_or_x2 v[246:247], v250, s[20:23], s61 offen sc0 sc1\n"
"s_mov_b64 exec, -1\nM 124,180,148\nbuffer_load_dwordx4 v[90:93], v252, s[20:23], s65 offen offset:3072 sc1\nM 108,176,128\nR 174,185,36864\nM 108,180,144\n"
"G 4\nM 128,160,168\nR 176,186,24576\nM 128,164,184\nR 178,186,28672\nM 144,160,172\n"
"R 180,186,32768\nM 144,164,188\nR 182,186,36864\nG 4\nM 148,168,172\nR 160,187,24576\n"
"M 148,172,188\nR 162,187,28672\nM 132,168,168\nR 164,187,32768\nM 132,172,184\nR 166,187,36864\n"
"G 4\nM 136,176,168\nR 168,184,40960\nM 136,180,184\nR 170,184,45056\nM 152,176,172\n"
"R 172,184,49152\nM 152,180,188\nR 174,184,53248\nG 4\nM 156,160,172\nR 176,185,40960\n"
"M 156,164,188\nR 178,185,45056\nM 140,160,168\nR 180,185,49152\nM 140,164,184\nR 182,185,53248\n"
"G 4\nM 128,168,200\nR 160,186,40960\nM 128,172,216\nR 162,186,45056\nM 144,168,204\n"
"R 164,186,49152\nM 144,172,220\nR 166,186,53248\nG 4\nM 148,176,204\nR 168,187,40960\n"
"M 148,180,220\nR 170,187,45056\nM 132,176,200\nR 172,187,49152\nM 132,180,216\nR 174,187,53248\n"
"G 4\nM 136,160,200\nR 176,184,24576\nM 136,164,216\nR 178,184,28672\nM 152,160,204\n"
"R 180,184,32768\nM 152,164,220\nR 182,184,36864\nG 4\nM 156,168,204\nR 160,185,24576\n"
"M 156,172,220\nR 162,185,28672\nM 140,168,200\nR 164,185,32768\nM 140,172,216\nR 166,185,36864\n"
"G 4\nM 96,176,160\nR 168,186,24576\nM 96,180,176\nR 170,186,28672\ns_mov_b32 m0, s51\n"
"M 112,176,164\nR 172,186,32768\nv_cvt_pk_bf16_f32 v128, v128, v129\nv_cvt_pk_bf16_f32 v129, v130, v131\nM 112,180,180\nR 174,186,36864\n"
"G 4\nv_cvt_pk_bf16_f32 v130, v132, v133\nM 116,160,164\nR 176,187,24576\ns_mul_i32 s66, s43, 49152\nv_cvt_pk_bf16_f32 v131, v134, v135\n"
"M 116,164,180\nR 178,187,28672\ns_add_u32 s66, s66, s72\nv_cvt_pk_bf16_f32 v132, v136, v137\nM 100,160,160\nR 180,187,32768\n"
"s_add_u32 s66, s66, 65044480\nv_cvt_pk_bf16_f32 v133, v138, v139\nM 100,164,176\nR 182,187,36864\nG 4\nv_cvt_pk_bf16_f32 v134, v140, v141\n"
"M 104,168,160\nR 160,184,40960\ns_waitcnt vmcnt(1)\nv_cmp_eq_u64 s[86:87], s[46:47], v[246:247]\nv_cvt_pk_bf16_f32 v135, v142, v143\nM 104,172,176\n"
"R 162,184,45056\nv_cvt_pk_bf16_f32 v136, v144, v145\nv_cvt_pk_bf16_f32 v137, v146, v147\nM 120,168,164\nR 164,184,49152\ns_and_b32 s62, s86, 4\n"
"s_cmp_eq_u32 s62, 4\ns_cbranch_scc0 Lq983\nLq984:\nv_cvt_pk_bf16_f32 v138, v148, v149\nM 120,172,180\nR 166,184,53248\n"
"G 4\nv_cvt_pk_bf16_f32 v139, v150, v151\nM 124,176,164\nR 168,185,40960\nbuffer_load_dwordx4 v[34:37], v252, s[20:23], s66 offen offset:0 sc1\nv_cvt_pk_bf16_f32 v140, v152, v153\n"
"M 124,180,180\nR 170,185,45056\nv_cvt_pk_bf16_f32 v141, v154, v155\nv_cvt_pk_bf16_f32 v142, v156, v157\nM 108,176,160\nR 172,185,49152\n"
"buffer_load_dwordx4 v[42:45], v252, s[20:23], s66 offen offset:1024 sc1\nds_write_addtid_b32 v128 offset:0\nds_write_addtid_b32 v129 offset:256\nds_write_addtid_b32 v130 offset:512\nds_write_addtid_b32 v131 offset:768\nds_write_addtid_b32 v132 offset:1024\n"
"ds_write_addtid_b32 v133 offset:1280\nds_write_addtid_b32 v134 offset:1536\nds_write_addtid_b32 v135 offset:1792\nds_write_addtid_b32 v136 offset:2048\nds_write_addtid_b32 v137 offset:2304\nds_write_addtid_b32 v138 offset:2560\n"
"ds_write_addtid_b32 v139 offset:2816\nds_write_addtid_b32 v140 offset:3072\nds_write_addtid_b32 v141 offset:3328\nds_write_addtid_b32 v142 offset:3584\nv_cvt_pk_bf16_f32 v143, v158, v159\nds_write_addtid_b32 v143 offset:3840\n"
"M 108,180,176\nR 174,185,53248\nG 15\nM 96,160,192\nR 176,186,40960\nM 96,164,208\n"
"R 178,186,45056\nbuffer_load_dwordx4 v[50:53], v252, s[20:23], s66 offen offset:2048 sc1\nG 3\ns_barrier\nM 112,160,196\nR 180,186,49152\n"
"ds_read2st64_b32 v[0:1], v225 offset0:0 offset1:1\nM 112,164,212\nR 182,186,53248\nds_read2st64_b32 v[6:7], v225 offset0:2 offset1:3\nG 6\nM 116,168,196\n"
"R 160,187,40960\nbuffer_load_dwordx4 v[58:61], v252, s[20:23], s66 offen offset:3072 sc1\nds_read2st64_b32 v[8:9], v225 offset0:4 offset1:5\nM 116,172,212\nR 162,187,45056\nds_read2st64_b32 v[14:15], v225 offset0:6 offset1:7\n"
"M 100,168,192\nR 164,187,49152\nds_read2st64_b32 v[16:17], v225 offset0:8 offset1:9\nM 100,172,208\nR 166,187,53248\nds_read2st64_b32 v[22:23], v225 offset0:10 offset1:11\n"
"G 9\nM 104,176,192\nds_read2st64_b32 v[24:25], v225 offset0:12 offset1:13\nds_read2st64_b32 v[30:31], v225 offset0:14 offset1:15\nM 104,180,208\nM 120,176,196\n"
"M 120,180,212\nG 3\nM 124,160,196\nM 124,164,212\nM 108,160,192\nM 108,164,208\n"
"v_mfma_f32_16x16x32_bf16 v[96:99], v[242:245], v[64:67], v[96:99]\nv_mfma_f32_16x16x32_bf16 v[100:103], v[242:245], v[68:71], v[100:103]\nv_mfma_f32_16x16x32_bf16 v[104:107], v[242:245], v[72:75], v[104:107]\nv_mfma_f32_16x16x32_bf16 v[108:111], v[242:245], v[76:79], v[108:111]\nv_mfma_f32_16x16x32_bf16 v[112:115], v[242:245], v[80:83], v[112:115]\nv_mfma_f32_16x16x32_bf16 v[116:119], v[242:245], v[84:87], v[116:119]\n"
"s_waitcnt vmcnt(4)\nv_mfma_f32_16x16x32_bf16 v[120:123], v[242:245], v[88:91], v[120:123]\nv_mfma_f32_16x16x32_bf16 v[124:127], v[242:245], v[92:95], v[124:127]\nv_mfma_f32_16x16x32_bf16 v[96:99], v[242:245], v[0:3], v[96:99]\nv_mfma_f32_16x16x32_bf16 v[100:103], v[242:245], v[4:7], v[100:103]\nv_mfma_f32_16x16x32_bf16 v[104:107], v[242:245], v[8:11], v[104:107]\n"
"v_mfma_f32_16x16x32_bf16 v[108:111], v[242:245], v[12:15], v[108:111]\nv_mfma_f32_16x16x32_bf16 v[112:115], v[242:245], v[16:19], v[112:115]\nG 2\nv_mfma_f32_16x16x32_bf16 v[116:119], v[242:245], v[20:23], v[116:119]\nG 1\nv_mfma_f32_16x16x32_bf16 v[120:123], v[242:245], v[24:27], v[120:123]\n"
"G 0\nv_mfma_f32_16x16x32_bf16 v[124:127], v[242:245], v[28:31], v[124:127]\ns_waitcnt vmcnt(3)\nv_mfma_f32_16x16x32_bf16 v[96:99], v[242:245], v[32:35], v[96:99]\nv_mfma_f32_16x16x32_bf16 v[100:103], v[242:245], v[36:39], v[100:103]\ns_waitcnt vmcnt(2)\n"
"v_mfma_f32_16x16x32_bf16 v[104:107], v[242:245], v[40:43], v[104:107]\nv_mfma_f32_16x16x32_bf16 v[108:111], v[242:245], v[44:47], v[108:111]\ns_waitcnt vmcnt(1)\nv_mfma_f32_16x16x32_bf16 v[112:115], v[242:245], v[48:51], v[112:115]\nv_mfma_f32_16x16x32_bf16 v[116:119], v[242:245], v[52:55], v[116:119]\ns_waitcnt vmcnt(0)\n"
"v_mfma_f32_16x16x32_bf16 v[120:123], v[242:245], v[56:59], v[120:123]\nv_mfma_f32_16x16x32_bf16 v[124:127], v[242:245], v[60:63], v[124:127]\nv_cvt_pk_bf16_f32 v192, v96, v97\nv_cvt_pk_bf16_f32 v193, v98, v99\nv_cvt_pk_bf16_f32 v194, v100, v101\nv_cvt_pk_bf16_f32 v195, v102, v103\n"
"v_cvt_pk_bf16_f32 v196, v104, v105\nv_cvt_pk_bf16_f32 v197, v106, v107\nv_cvt_pk_bf16_f32 v198, v108, v109\nv_cvt_pk_bf16_f32 v199, v110, v111\nv_cvt_pk_bf16_f32 v200, v112, v113\nv_cvt_pk_bf16_f32 v201, v114, v115\n"
"v_cvt_pk_bf16_f32 v202, v116, v117\nv_cvt_pk_bf16_f32 v203, v118, v119\nv_cvt_pk_bf16_f32 v204, v120, v121\nv_cvt_pk_bf16_f32 v205, v122, v123\nv_cvt_pk_bf16_f32 v206, v124, v125\nv_cvt_pk_bf16_f32 v207, v126, v127\n"
"v_and_b32 v230, 0xf0, v252\nv_lshlrev_b32 v230, 7, v230\nv_bfe_u32 v231, v252, 8, 1\nv_lshl_add_u32 v230, v231, 5, v230\nv_lshrrev_b32 v231, 9, v252\nv_lshl_add_u32 v230, v231, 4, v230\n"
"s_add_u32 s61, s49, s53\ns_add_u32 s62, s61, 32768\nv_permlane16_swap_b32 v192, v194\nv_permlane16_swap_b32 v193, v195\nbuffer_store_dwordx4 v[192:195], v230, s[28:31], s61 offen offset:0\nv_permlane16_swap_b32 v196, v198\n"
"v_permlane16_swap_b32 v197, v199\nbuffer_store_dwordx4 v[196:199], v230, s[28:31], s61 offen offset:64\nv_permlane16_swap_b32 v200, v202\nv_permlane16_swap_b32 v201, v203\nbuffer_store_dwordx4 v[200:203], v230, s[28:31], s62 offen offset:0\nv_permlane16_swap_b32 v204, v206\n"
"v_permlane16_swap_b32 v205, v207\nbuffer_store_dwordx4 v[204:207], v230, s[28:31], s62 offen offset:64\ns_add_u32 s60, s83, 1\nv_mov_b32 v234, s60\ns_or_b32 s60, s45, s40\ns_cmp_eq_u32 s60, 0\n"
"s_cselect_b64 exec, 1, 0\nbuffer_store_dword v234, off, s[20:23], 0 sc1\ns_mov_b64 exec, -1\ns_endpgm\nLqF:\nLq645:\n"
"s_lshr_b32 s84, s49, 4\ns_add_u32 s84, s84, 82837504\nbuffer_load_dwordx4 v[128:131], v252, s[20:23], s84 offen offset:0 sc1\nbuffer_load_dwordx4 v[132:135], v252, s[20:23], s84 offen offset:1024 sc1\nbuffer_load_dwordx4 v[136:139], v252, s[20:23], s84 offen offset:2048 sc1\nbuffer_load_dwordx4 v[140:143], v252, s[20:23], s84 offen offset:3072 sc1\n"
"s_mov_b32 s85, 0x0c0c0400\ns_waitcnt vmcnt(3)\nv_perm_b32 v144, v129, v128, s85\nv_perm_b32 v148, v131, v130, s85\nv_lshl_or_b32 v144, v148, 16, v144\ns_waitcnt vmcnt(2)\n"
"v_perm_b32 v145, v133, v132, s85\nv_perm_b32 v149, v135, v134, s85\nv_lshl_or_b32 v145, v149, 16, v145\ns_waitcnt vmcnt(1)\nv_perm_b32 v146, v137, v136, s85\nv_perm_b32 v150, v139, v138, s85\n"
"v_lshl_or_b32 v146, v150, 16, v146\ns_waitcnt vmcnt(0)\nv_perm_b32 v147, v141, v140, s85\nv_perm_b32 v151, v143, v142, s85\nv_lshl_or_b32 v147, v151, 16, v147\nv_min3_u32 v230, v144, v145, v146\n"
"v_min3_u32 v230, v230, v147, v147\nv_max3_u32 v231, v144, v145, v146\nv_max3_u32 v231, v231, v147, v147\nv_cmp_ne_u32 s[86:87], s46, v230\nv_cmp_ne_u32 s[88:89], s46, v231\ns_nop 0\n"
"s_or_b64 s[86:87], s[86:87], s[88:89]\ns_cbranch_scc0 Lq646\ns_sub_u32 s48, s48, 1\ns_cmp_gt_i32 s48, 0\ns_cbranch_scc1 Lq645\ns_branch Lq646\n"
"Lq692:\ns_mov_b64 exec, s[66:67]\nbuffer_load_dwordx4 v[230:233], v252, s[20:23], s65 offen offset:0 sc1\nbuffer_load_dwordx2 v[64:65], v227, s[20:23], s65 offen offset:2048 sc1\nbuffer_load_dwordx2 v[70:71], v227, s[20:23], s65 offen offset:2560 sc1\ns_mov_b64 exec, -1\n"
"s_waitcnt vmcnt(0)\nv_max3_u32 v248, v233, v65, v71\nv_cmp_eq_u32 vcc, -1, v248\ns_nop 1\ns_cbranch_vccz Lq693\ns_sub_u32 s48, s48, 1\n"
"s_cmp_gt_i32 s48, 0\ns_cbranch_scc1 Lq692\ns_branch Lq693\nLq694:\ns_mov_b64 exec, s[66:67]\nbuffer_load_dwordx4 v[234:237], v252, s[20:23], s65 offen offset:1024 sc1\n"
"buffer_load_dwordx2 v[76:77], v227, s[20:23], s65 offen offset:3072 sc1\nbuffer_load_dwordx2 v[82:83], v227, s[20:23], s65 offen offset:3584 sc1\ns_mov_b64 exec, -1\ns_waitcnt vmcnt(0)\nv_max3_u32 v248, v237, v77, v83\nv_cmp_eq_u32 vcc, -1, v248\n"
"s_nop 1\ns_cbranch_vccz Lq695\ns_sub_u32 s48, s48, 1\ns_cmp_gt_i32 s48, 0\ns_cbranch_scc1 Lq694\ns_branch Lq695\n"
"Lq818:\ns_lshr_b32 s84, s49, 4\ns_add_u32 s84, s84, 83361792\nbuffer_load_dwordx4 v[0:3], v252, s[20:23], s84 offen offset:0 sc1\nbuffer_load_dwordx4 v[4:7], v252, s[20:23], s84 offen offset:1024 sc1\nbuffer_load_dwordx4 v[8:11], v252, s[20:23], s84 offen offset:2048 sc1\n"
"buffer_load_dwordx4 v[12:15], v252, s[20:23], s84 offen offset:3072 sc1\ns_mov_b32 s85, 0x0c0c0400\ns_waitcnt vmcnt(3)\nv_perm_b32 v16, v1, v0, s85\nv_perm_b32 v20, v3, v2, s85\nv_lshl_or_b32 v16, v20, 16, v16\n"
"s_waitcnt vmcnt(2)\nv_perm_b32 v17, v5, v4, s85\nv_perm_b32 v21, v7, v6, s85\nv_lshl_or_b32 v17, v21, 16, v17\ns_waitcnt vmcnt(1)\nv_perm_b32 v18, v9, v8, s85\n"
"v_perm_b32 v22, v11, v10, s85\nv_lshl_or_b32 v18, v22, 16, v18\ns_waitcnt vmcnt(0)\nv_perm_b32 v19, v13, v12, s85\nv_perm_b32 v23, v15, v14, s85\nv_lshl_or_b32 v19, v23, 16, v19\n"
"v_min3_u32 v230, v16, v17, v18\nv_min3_u32 v230, v230, v19, v19\nv_max3_u32 v231, v16, v17, v18\nv_max3_u32 v231, v231, v19, v19\nv_cmp_ne_u32 s[86:87], s46, v230\nv_cmp_ne_u32 s[88:89], s46, v231\n"
"s_nop 0\ns_or_b64 s[86:87], s[86:87], s[88:89]\ns_cbranch_scc0 Lq819\ns_sub_u32 s48, s48, 1\ns_cmp_gt_i32 s48, 0\ns_cbranch_scc1 Lq818\n"
"s_branch Lq819\nLq838:\nv_mov_b32 v248, 0\nv_mov_b32 v249, 0\ns_add_u32 s61, s82, 845824\ns_mov_b64 exec, 0xf\n"
"buffer_atomic_or_x2 v[248:249], v250, s[20:23], s61 offen sc0 sc1\ns_mov_b64 exec, -1\ns_waitcnt vmcnt(0)\nv_cmp_eq_u64 s[86:87], s[46:47], v[248:249]\ns_nop 1\ns_and_b32 s62, s86, 0xf\n"
"s_cmp_eq_u32 s62, 0xf\ns_cbranch_scc1 Lq839\ns_sleep 1\ns_sub_u32 s48, s48, 1\ns_cmp_gt_i32 s48, 0\ns_cbranch_scc1 Lq838\n"
"s_branch Lq839\nLq873:\nv_mov_b32 v248, 0\nv_mov_b32 v249, 0\ns_add_u32 s61, s82, 845832\ns_mov_b64 exec, 0xf\n"
"buffer_atomic_or_x2 v[248:249], v250, s[20:23], s61 offen sc0 sc1\ns_mov_b64 exec, -1\ns_waitcnt vmcnt(0)\nv_cmp_eq_u64 s[86:87], s[46:47], v[248:249]\ns_nop 1\ns_and_b32 s62, s86, 0xf\n"
"s_cmp_eq_u32 s62, 0xf\ns_cbranch_scc1 Lq874\ns_sleep 1\ns_sub_u32 s48, s48, 1\ns_cmp_gt_i32 s48, 0\ns_cbranch_scc1 Lq873\n"
"s_branch Lq874\nLq908:\nv_mov_b32 v248, 0\nv_mov_b32 v249, 0\ns_add_u32 s61, s82, 845848\ns_mov_b64 exec, 0xf\n"
"buffer_atomic_or_x2 v[248:249], v250, s[20:23], s61 offen sc0 sc1\ns_mov_b64 exec, -1\ns_waitcnt vmcnt(0)\nv_cmp_eq_u64 s[86:87], s[46:47], v[248:249]\ns_nop 1\ns_and_b32 s62, s86, 0xf\n"
"s_cmp_eq_u32 s62, 0xf\ns_cbranch_scc1 Lq909\ns_sleep 1\ns_sub_u32 s48, s48, 1\ns_cmp_gt_i32 s48, 0\ns_cbranch_scc1 Lq908\n"
"s_branch Lq909\nLq927:\nv_mov_b32 v248, 0\nv_mov_b32 v249, 0\ns_add_u32 s61, s82, 845840\ns_mov_b64 exec, 0xf\n"
"buffer_atomic_or_x2 v[248:249], v250, s[20:23], s61 offen sc0 sc1\ns_mov_b64 exec, -1\ns_waitcnt vmcnt(0)\nv_cmp_eq_u64 s[86:87], s[46:47], v[248:249]\ns_nop 1\ns_and_b32 s62, s86, 0xf\n"
"s_cmp_eq_u32 s62, 0xf\ns_cbranch_scc1 Lq928\ns_sleep 1\ns_sub_u32 s48, s48, 1\ns_cmp_gt_i32 s48, 0\ns_cbranch_scc1 Lq927\n"
"s_branch Lq928\nLq967:\nv_mov_b32 v248, 0\nv_mov_b32 v249, 0\ns_add_u32 s61, s74, 1009664\ns_mov_b64 exec, 3\n"
"buffer_atomic_or_x2 v[248:249], v250, s[20:23], s61 offen sc0 sc1\ns_mov_b64 exec, -1\ns_waitcnt vmcnt(0)\nv_cmp_eq_u64 s[86:87], s[46:47], v[248:249]\ns_nop 1\ns_and_b32 s62, s86, 3\n"
"s_cmp_eq_u32 s62, 3\ns_cbranch_scc1 Lq968\ns_sleep 1\ns_sub_u32 s48, s48, 1\ns_cmp_gt_i32 s48, 0\ns_cbranch_scc1 Lq967\n"
"s_branch Lq968\nLq983:\nv_mov_b32 v246, 0\nv_mov_b32 v247, 0\ns_add_u32 s61, s74, 1009664\ns_mov_b64 exec, 4\n"
"buffer_atomic_or_x2 v[246:247], v250, s[20:23], s61 offen sc0 sc1\ns_mov_b64 exec, -1\ns_waitcnt vmcnt(0)\nv_cmp_eq_u64 s[86:87], s[46:47], v[246:247]\ns_nop 1\ns_and_b32 s62, s86, 4\n"
"s_cmp_eq_u32 s62, 4\ns_cbranch_scc1 Lq984\ns_sleep 1\ns_sub_u32 s48, s48, 1\ns_cmp_gt_i32 s48, 0\ns_cbranch_scc1 Lq983\n"
"s_branch Lq984\nLqE:\n"
  : "+{v0}"(tid), "+{s[92:93]}"(ka), "+{s45}"(bid) :
  : "a0", "a1", "a2", "a3", "a4", "a5", "a6", "a7", "a8", "a9", "a10", "a11", "a12", "a13", "a14", "a15", "a16", "a17", "a18", "a19", "a20", "a21", "a22", "a23", "a24", "a25", "a26", "a27", "a28", "a29", "a30", "a31", "a32", "a33", "a34", "a35", "a36", "a37", "a38", "a39", "a40", "a41", "a42", "a43", "a44", "a45", "a46", "a47", "a48", "a49", "a50", "a51", "a52", "a53", "a54", "a55", "a56", "a57", "a58", "a59", "a60", "a61", "a62", "a63", "a64", "a65", "a66", "a67", "a68", "a69", "a70", "a71", "a72", "a73", "a74", "a75", "a76", "a77", "a78", "a79", "a80", "a81", "a82", "a83", "a84", "a85", "a86", "a87", "a88", "a89", "a90", "a91", "a92", "a93", "a94", "a95", "a96", "a97", "a98", "a99", "a100", "a101", "a102", "a103", "a104", "a105", "a106", "a107", "a108", "a109", "a110", "a111", "a112", "a113", "a114", "a115", "a116", "a117", "a118", "a119", "a120", "a121", "a122", "a123", "a124", "a125", "a126", "a127", "a128", "a129", "a130", "a131", "a132", "a133", "a134", "a135", "a136", "a137", "a138", "a139", "a140", "a141", "a142", "a143", "a144", "a145", "a146", "a147", "a148", "a149", "a150", "a151", "a152", "a153", "a154", "a155", "a156", "a157", "a158", "a159", "a160", "a161", "a162", "a163", "a164", "a165", "a166", "a167", "a168", "a169", "a170", "a171", "a172", "a173", "a174", "a175", "a176", "a177", "a178", "a179", "a180", "a181", "a182", "a183", "a184", "a185", "a186", "a187", "a188", "a189", "a190", "a191", "a192", "a193", "a194", "a195", "a196", "a197", "a198", "a199", "a200", "a201", "a202", "a203", "a204", "a205", "a206", "a207", "a208", "a209", "a210", "a211", "a212", "a213", "a214", "a215", "a216", "a217", "a218", "a219", "a220", "a221", "a222", "a223", "a224", "a225", "a226", "a227", "a228", "a229", "a230", "a231", "a232", "a233", "a234", "a235", "a236", "a237", "a238", "a239", "a240", "a241", "a242", "a243", "a244", "a245", "a246", "a247", "a248", "a249", "a250", "a251", "a252", "a253", "a254", "a255", "v1", "v2", "v3", "v4", "v5", "v6", "v7", "v8", "v9", "v10", "v11", "v12", "v13", "v14", "v15", "v16", "v17", "v18", "v19", "v20", "v21", "v22", "v23", "v24", "v25", "v26", "v27", "v28", "v29", "v30", "v31", "v32", "v33", "v34", "v35", "v36", "v37", "v38", "v39", "v40", "v41", "v42", "v43", "v44", "v45", "v46", "v47", "v48", "v49", "v50", "v51", "v52", "v53", "v54", "v55", "v56", "v57", "v58", "v59", "v60", "v61", "v62", "v63", "v64", "v65", "v66", "v67", "v68", "v69", "v70", "v71", "v72", "v73", "v74", "v75", "v76", "v77", "v78", "v79", "v80", "v81", "v82", "v83", "v84", "v85", "v86", "v87", "v88", "v89", "v90", "v91", "v92", "v93", "v94", "v95", "v96", "v97", "v98", "v99", "v100", "v101", "v102", "v103", "v104", "v105", "v106", "v107", "v108", "v109", "v110", "v111", "v112", "v113", "v114", "v115", "v116", "v117", "v118", "v119", "v120", "v121", "v122", "v123", "v124", "v125", "v126", "v127", "v128", "v129", "v130", "v131", "v132", "v133", "v134", "v135", "v136", "v137", "v138", "v139", "v140", "v141", "v142", "v143", "v144", "v145", "v146", "v147", "v148", "v149", "v150", "v151", "v152", "v153", "v154", "v155", "v156", "v157", "v158", "v159", "v160", "v161", "v162", "v163", "v164", "v165", "v166", "v167", "v168", "v169", "v170", "v171", "v172", "v173", "v174", "v175", "v176", "v177", "v178", "v179", "v180", "v181", "v182", "v183", "v184", "v185", "v186", "v187", "v188", "v189", "v190", "v191", "v192", "v193", "v194", "v195", "v196", "v197", "v198", "v199", "v200", "v201", "v202", "v203", "v204", "v205", "v206", "v207", "v208", "v209", "v210", "v211", "v212", "v213", "v214", "v215", "v216", "v217", "v218", "v219", "v220", "v221", "v222", "v223", "v224", "v225", "v226", "v227", "v228", "v229", "v230", "v231", "v232", "v233", "v234", "v235", "v236", "v237", "v238", "v239", "v240", "v241", "v242", "v243", "v244", "v245", "v246", "v247", "v248", "v249", "v250", "v251", "v252", "v253", "v254", "v255", "s4", "s5", "s6", "s7", "s8", "s9", "s10", "s11", "s12", "s13", "s14", "s15", "s16", "s17", "s18", "s19", "s20", "s21", "s22", "s23", "s24", "s25", "s26", "s27", "s28", "s29", "s30", "s31", "s32", "s33", "s34", "s35", "s36", "s37", "s38", "s39", "s40", "s41", "s42", "s43", "s44", "s46", "s47", "s48", "s49", "s50", "s51", "s52", "s53", "s54", "s55", "s56", "s57", "s58", "s59", "s60", "s61", "s62", "s63", "s64", "s65", "s66", "s67", "s68", "s69", "s70", "s71", "s72", "s73", "s74", "s75", "s76", "s77", "s78", "s79", "s80", "s81", "s82", "s83", "s84", "s85", "s86", "s87", "s88", "s89", "s90", "s91", "s94", "s95", "s96", "s97", "s98", "s99", "s100", "s101", "vcc", "m0", "scc", "memory");
#endif
}
extern "C" void kernel_launch(void* const* d_in, const int* in_sizes, int n_in, void* d_out, int out_size, void* d_ws, size_t ws_size, hipStream_t stream) {
  (void)hipFuncSetAttribute((const void*)layers, hipFuncAttributeMaxDynamicSharedMemorySize, 160 * 1024);
  hipLaunchKernelGGL(layers, dim3(256), dim3(256), 160 * 1024, stream, d_in[0], d_in[1], d_in[2], d_in[4], d_in[5], d_in[6], d_in[7], d_out, d_ws);
}
```

```cpp
#include <hip/hip_runtime.h>
__global__ __launch_bounds__(256, 1) __attribute__((aligned(4096))) void layers(const void* x, const void* wqkv, const void* wo, const void* w1, const void* b1, const void* w2, const void* b2, void* out, void* ws) {
#if defined(__HIP_DEVICE_COMPILE__)
  int tid = threadIdx.x, bid = blockIdx.x;
  unsigned long long ka = (unsigned long long)__builtin_amdgcn_kernarg_segment_ptr();
  asm volatile(
".macro M d,w,a\nv_mfma_f32_16x16x32_bf16 v[\\d:\\d+3], v[\\w:\\w+3], a[\\a:\\a+3], v[\\d:\\d+3]\n.endm\n.macro N d,a,w\nv_mfma_f32_16x16x32_bf16 v[\\d:\\d+3], a[\\a:\\a+3], v[\\w:\\w+3], v[\\d:\\d+3]\n.endm\n"
".macro R d,b,o\nds_read_b64_tr_b16 v[\\d:\\d+1], v\\b offset:\\o\n.endm\n.macro G n\ns_waitcnt lgkmcnt(\\n)\n.endm\n"
"s_load_dwordx2 s[24:25], s[92:93], 0\ns_load_dwordx2 s[4:5], s[92:93], 8\ns_load_dwordx2 s[8:9], s[92:93], 16\ns_load_dwordx2 s[12:13], s[92:93], 24\ns_load_dwordx2 s[32:33], s[92:93], 32\ns_load_dwordx2 s[16:17], s[92:93], 40\n"
"s_load_dwordx2 s[36:37], s[92:93], 48\ns_load_dwordx2 s[28:29], s[92:93], 56\ns_load_dwordx2 s[20:21], s[92:93], 64\nv_lshrrev_b32 v1, 6, v0\nv_and_b32 v0, 63, v0\nv_readfirstlane_b32 s40, v1\n"
"v_lshlrev_b32 v252, 4, v0\ns_getpc_b64 s[98:99]\nLqPC:\ns_lshr_b32 s60, s45, 3\ns_lshl2_add_u32 s60, s60, s40\ns_lshl_b32 s60, s60, 8\n"
"v_lshl_add_u32 v224, v252, 3, s60\nv_add_u32 v224, LqPC-LqPC, v224\nv_min_u32 v224, LqE-LqPC-4, v224\ns_mov_b64 exec, 3\nglobal_load_dword v224, v224, s[98:99]\ns_mov_b64 exec, -1\n"
"s_mov_b32 s6, 6291456\ns_mov_b32 s7, 0x20000\ns_mov_b32 s10, 2097152\ns_mov_b32 s11, 0x20000\ns_mov_b32 s14, 7340032\ns_mov_b32 s15, 0x20000\n"
"s_mov_b32 s18, 7340032\ns_mov_b32 s19, 0x20000\ns_mov_b32 s22, 89128960\ns_mov_b32 s23, 0x20000\ns_mov_b32 s26, 4194304\ns_mov_b32 s27, 0x20000\n"
"s_mov_b32 s30, 4194304\ns_mov_b32 s31, 0x20000\ns_mov_b32 s34, 7168\ns_mov_b32 s35, 0x20000\ns_mov_b32 s38, 2048\ns_mov_b32 s39, 0x20000\n"
"s_bfe_u32 s41, s45, 0x30003\ns_bfe_u32 s60, s45, 65536\ns_lshl3_add_u32 s41, s60, s41\ns_bfe_u32 s43, s45, 131073\ns_lshr_b32 s44, s45, 6\ns_lshl2_add_u32 s42, s43, s44\n"
"s_lshl2_add_u32 s75, s44, s43\ns_and_b32 s90, s40, 1\ns_lshl2_add_u32 s60, s41, s40\ns_lshl_b32 s49, s60, 16\ns_lshl_b32 s50, s40, 11\ns_add_u32 s50, s50, 32768\n"
"s_lshl_b32 s52, s42, 7\ns_lshl_b32 s51, s40, 13\ns_lshl_b32 s60, s90, 10\ns_add_u32 s70, s49, s60\ns_xor_b32 s71, s70, 0x10000\ns_mul_i32 s101, s41, 196608\n"
"s_add_u32 s101, s101, s52\ns_lshl_b32 s53, s75, 7\ns_and_b32 s60, s44, 2\ns_lshl2_add_u32 s89, s43, s60\ns_lshr_b32 s60, s40, 1\ns_add_u32 s89, s89, s60\n"
"Lqq0:\nv_lshrrev_b32 v96, 7, v252\nv_lshlrev_b32 v96, 11, v96\nv_lshrrev_b32 v97, 8, v252\nv_lshlrev_b32 v97, 4, v97\nv_and_b32 v98, 0x70, v252\n"
"v_xor_b32 v98, v98, v97\nv_add_u32 v226, v96, v98\nv_xor_b32 v227, 64, v226\nG 0\ns_add_u32 m0, s51, 0\ns_add_u32 s60, s70, 0\n"
"buffer_load_dwordx4 v226, s[24:27], s60 offen lds\ns_add_u32 m0, s51, 1024\ns_add_u32 s60, s70, 16384\nbuffer_load_dwordx4 v227, s[24:27], s60 offen lds\ns_add_u32 m0, s51, 2048\ns_add_u32 s60, s70, 32768\n"
"buffer_load_dwordx4 v226, s[24:27], s60 offen lds\ns_add_u32 m0, s51, 3072\ns_add_u32 s60, s70, 49152\nbuffer_load_dwordx4 v227, s[24:27], s60 offen lds\ns_add_u32 m0, s51, 4096\ns_add_u32 s60, s71, 0\n"
"buffer_load_dwordx4 v226, s[24:27], s60 offen lds\ns_add_u32 m0, s51, 5120\ns_add_u32 s60, s71, 16384\nbuffer_load_dwordx4 v227, s[24:27], s60 offen lds\ns_add_u32 m0, s51, 6144\ns_add_u32 s60, s71, 32768\n"
"buffer_load_dwordx4 v226, s[24:27], s60 offen lds\ns_add_u32 m0, s51, 7168\ns_add_u32 s60, s71, 49152\nbuffer_load_dwordx4 v227, s[24:27], s60 offen lds\nbuffer_load_dwordx2 v[230:231], off, s[20:23], 0 sc1\nv_bfe_u32 v96, v252, 5, 2\n"
"v_bfe_u32 v97, v252, 8, 2\nv_xor_b32 v96, v96, v97\ns_and_b32 s60, s40, 1\ns_lshl_b32 s60, s60, 4\ns_and_b32 s61, s40, 2\ns_lshl1_add_u32 s60, s61, s60\n"
"v_bfe_u32 v97, v252, 7, 2\nv_lshrrev_b32 v98, 9, v252\nv_lshl_add_u32 v97, v98, 3, v97\nv_add_u32 v97, s60, v97\nv_mul_u32_u24 v97, 6144, v97\nv_lshl_add_u32 v97, v96, 5, v97\n"
"v_and_b32 v96, 16, v252\nv_add_u32 v220, v96, v97\nv_add_u32 v221, 3144704, v220\ns_add_u32 m0, s50, 0\ns_add_u32 s63, s52, 2048\nbuffer_load_dwordx4 v220, s[4:7], s63 offen lds\n"
"buffer_load_dwordx4 v221, s[4:7], s63 offen offset:1024 lds\ns_add_u32 m0, s50, 8192\ns_add_u32 s63, s52, 198656\nbuffer_load_dwordx4 v220, s[4:7], s63 offen lds\nbuffer_load_dwordx4 v221, s[4:7], s63 offen offset:1024 lds\ns_lshl2_add_u32 s60, s41, s40\n"
"s_mul_i32 s79, s60, 2176\ns_and_b32 s61, s60, -2\ns_mul_i32 s80, s61, 2176\ns_lshl2_add_u32 s61, s61, s43\ns_mul_i32 s82, s61, 640\ns_mul_i32 s61, s43, 28672\n"
"s_mul_i32 s73, s60, 229376\ns_add_u32 s73, s73, s61\ns_mul_i32 s62, s90, 14336\ns_add_u32 s68, s73, s62\ns_add_u32 s61, s61, s62\ns_xor_b32 s62, s60, 1\n"
"s_mul_i32 s69, s62, 229376\ns_add_u32 s69, s69, s61\ns_mul_i32 s61, s44, 7168\ns_add_u32 s73, s73, s61\ns_lshl2_add_u32 s61, s60, s43\ns_mul_i32 s81, s61, 640\n"
"s_lshl2_add_u32 s61, s60, s44\ns_mul_i32 s74, s61, 640\ns_lshl2_add_u32 s61, s41, s44\ns_mul_i32 s61, s61, 48\ns_add_u32 s61, s61, s40\ns_lshl_b32 s72, s61, 12\n"
"s_mul_i32 s55, s42, 448\ns_add_u32 s54, s55, 64\ns_and_b32 s54, s54, 0xffffff80\ns_bitcmp0_b32 s42, 0\ns_cselect_b32 s61, 384, 0\ns_add_u32 s55, s55, s61\n"
"s_lshl_b32 s61, s41, 9\ns_lshl_b32 s91, s42, 13\ns_add_u32 s91, s91, s61\ns_add_u32 s91, s91, 83886080\ns_mul_i32 s61, s43, 1835008\ns_lshl_b32 s62, s44, 9\n"
"s_add_u32 s61, s61, s62\ns_add_u32 s62, s43, 1\ns_and_b32 s62, s62, 3\ns_lshl_b32 s62, s62, 7\ns_add_u32 s56, s61, s62\ns_add_u32 s62, s43, 2\n"
"s_and_b32 s62, s62, 3\ns_lshl_b32 s62, s62, 7\ns_add_u32 s57, s61, s62\ns_add_u32 s62, s43, 3\ns_and_b32 s62, s62, 3\ns_lshl_b32 s62, s62, 7\n"
"s_add_u32 s58, s61, s62\ns_add_u32 s62, s43, 4\ns_and_b32 s62, s62, 3\ns_lshl_b32 s62, s62, 7\ns_add_u32 s59, s61, s62\ns_mov_b32 s76, 0x3f803f80\n"
"s_mov_b32 s77, 0xffff0000\ns_mov_b32 s48, 131072\ns_cmp_eq_u32 s60, 0\ns_cselect_b32 s78, 0xf149f2ca, 0\nv_bfe_u32 v96, v252, 6, 3\nv_lshlrev_b32 v96, 7, v96\n"
"v_lshrrev_b32 v97, 9, v252\nv_lshl_add_u32 v96, v97, 11, v96\nv_bfe_u32 v97, v252, 4, 2\nv_lshl_add_u32 v96, v97, 3, v96\ns_lshl_b32 s60, s90, 10\ns_add_u32 s60, s60, 32768\n"
"v_add_u32 v96, s60, v96\nv_bfe_u32 v97, v252, 7, 2\nv_xor_b32 v98, 0, v97\nv_lshl_add_u32 v184, v98, 5, v96\nv_add_u32 v188, 0x10000, v184\nv_xor_b32 v98, 1, v97\n"
"v_lshl_add_u32 v185, v98, 5, v96\nv_add_u32 v189, 0x10000, v185\nv_xor_b32 v98, 2, v97\nv_lshl_add_u32 v186, v98, 5, v96\nv_add_u32 v190, 0x10000, v186\nv_xor_b32 v98, 3, v97\n"
"v_lshl_add_u32 v187, v98, 5, v96\nv_add_u32 v191, 0x10000, v187\nv_lshrrev_b32 v96, 2, v252\ns_xor_b32 s60, s51, 0x2000\nv_add_u32 v225, s60, v96\nv_mov_b32 v210, 0\n"
"v_mov_b32 v211, 0\nv_bfe_u32 v96, v252, 5, 2\nv_bfe_u32 v97, v252, 8, 2\nv_xor_b32 v96, v96, v97\nv_lshrrev_b32 v97, 7, v252\ns_lshl_b32 s60, s40, 3\n"
"v_add_u32 v97, s60, v97\nv_mov_b32 v228, v97\nv_and_b32 v97, 16, v252\nv_lshl_add_u32 v229, v96, 5, v97\nv_bfe_u32 v96, v252, 5, 2\nv_bfe_u32 v97, v252, 8, 2\n"
"v_xor_b32 v96, v96, v97\nv_lshrrev_b32 v97, 7, v252\ns_lshl_b32 s60, s40, 3\nv_add_u32 v97, s60, v97\nv_lshrrev_b32 v98, 1, v96\nv_lshl_add_u32 v97, v98, 5, v97\n"
"v_and_b32 v96, 1, v96\nv_mul_u32_u24 v97, 7168, v97\nv_lshl_add_u32 v97, v96, 5, v97\nv_and_b32 v96, 16, v252\nv_add_u32 v222, v96, v97\nv_add_u32 v223, 3668992, v222\n"
"v_bfe_u32 v96, v252, 4, 4\nv_lshrrev_b32 v97, 8, v252\nv_lshrrev_b32 v98, 1, v96\nv_xor_b32 v97, v97, v98\nv_lshlrev_b32 v96, 7, v96\nv_lshl_add_u32 v96, v97, 4, v96\n"
"v_add_u32 v248, s51, v96\nv_xor_b32 v249, 64, v248\nv_add_u32 v250, 122880, v248\nv_add_u32 v251, 122880, v249\ns_add_u32 m0, s51, 122880\ns_add_u32 s60, s70, 128\n"
"buffer_load_dwordx4 v226, s[24:27], s60 offen lds\ns_add_u32 m0, s51, 123904\ns_add_u32 s60, s70, 16512\nbuffer_load_dwordx4 v227, s[24:27], s60 offen lds\ns_add_u32 m0, s51, 124928\ns_add_u32 s60, s70, 32896\n"
"buffer_load_dwordx4 v226, s[24:27], s60 offen lds\ns_add_u32 m0, s51, 125952\ns_add_u32 s60, s70, 49280\nbuffer_load_dwordx4 v227, s[24:27], s60 offen lds\ns_add_u32 m0, s51, 126976\ns_add_u32 s60, s71, 128\n"
"buffer_load_dwordx4 v226, s[24:27], s60 offen lds\ns_add_u32 m0, s51, 128000\ns_add_u32 s60, s71, 16512\nbuffer_load_dwordx4 v227, s[24:27], s60 offen lds\ns_add_u32 m0, s51, 129024\ns_add_u32 s60, s71, 32896\n"
"buffer_load_dwordx4 v226, s[24:27], s60 offen lds\ns_add_u32 m0, s51, 130048\ns_add_u32 s60, s71, 49280\nbuffer_load_dwordx4 v227, s[24:27], s60 offen lds\ns_add_u32 m0, s50, 16384\ns_add_u32 s63, s52, 4096\n"
"buffer_load_dwordx4 v220, s[4:7], s63 offen lds\nbuffer_load_dwordx4 v221, s[4:7], s63 offen offset:1024 lds\ns_add_u32 m0, s50, 24576\ns_add_u32 s63, s52, 200704\nbuffer_load_dwordx4 v220, s[4:7], s63 offen lds\nbuffer_load_dwordx4 v221, s[4:7], s63 offen offset:1024 lds\n"
"s_add_u32 m0, s50, 32768\ns_add_u32 s63, s101, 2048\nbuffer_load_dwordx4 v220, s[4:7], s63 offen lds\nbuffer_load_dwordx4 v221, s[4:7], s63 offen offset:1024 lds\ns_add_u32 m0, s50, 40960\ns_add_u32 s63, s101, 4096\n"
"buffer_load_dwordx4 v220, s[4:7], s63 offen lds\nbuffer_load_dwordx4 v221, s[4:7], s63 offen offset:1024 lds\nv_bfe_u32 v246, v252, 4, 4\nv_lshlrev_b32 v246, 11, v246\nv_lshrrev_b32 v247, 8, v252\nv_lshl_add_u32 v246, v247, 3, v246\n"
"s_add_u32 s84, s49, s53\ns_add_u32 s85, s84, 32768\nbuffer_load_dwordx2 v[192:193], v246, s[24:27], s84 offen offset:0\nbuffer_load_dwordx2 v[194:195], v246, s[24:27], s84 offen offset:32\nbuffer_load_dwordx2 v[196:197], v246, s[24:27], s84 offen offset:64\nbuffer_load_dwordx2 v[198:199], v246, s[24:27], s84 offen offset:96\n"
"buffer_load_dwordx2 v[200:201], v246, s[24:27], s85 offen offset:0\nbuffer_load_dwordx2 v[202:203], v246, s[24:27], s85 offen offset:32\nbuffer_load_dwordx2 v[204:205], v246, s[24:27], s85 offen offset:64\nbuffer_load_dwordx2 v[206:207], v246, s[24:27], s85 offen offset:96\nv_mov_b32 v106, 0xf149f149\nv_mov_b32 v104, 0xffff\n"
"v_mov_b32 v105, 0xffff0000\ns_mov_b32 s60, 0x50000\ns_mov_b32 s61, 0x5550055\nv_cndmask_b32 v96, 0, v104, s[60:61]\ns_mov_b32 s60, 0xa0000\ns_mov_b32 s61, 0xaaa00aa\n"
"v_cndmask_b32 v96, v96, v105, s[60:61]\nv_bfi_b32 v100, v96, 0, v106\ns_mov_b32 s60, 0x150001\ns_mov_b32 s61, 0x15550155\nv_cndmask_b32 v97, 0, v104, s[60:61]\ns_mov_b32 s60, 0x2a0002\n"
"s_mov_b32 s61, 0x2aaa02aa\nv_cndmask_b32 v97, v97, v105, s[60:61]\nv_bfi_b32 v101, v97, 0, v106\ns_mov_b32 s60, 0x55505555\ns_mov_b32 s61, 0x50005500\nv_cndmask_b32 v98, 0, v104, s[60:61]\n"
"s_mov_b32 s60, 0xaaa0aaaa\ns_mov_b32 s61, 0xa000aa00\nv_cndmask_b32 v98, v98, v105, s[60:61]\nv_bfi_b32 v102, v98, 0, v106\ns_mov_b32 s60, 0x55405554\ns_mov_b32 s61, 0x40005400\n"
"v_cndmask_b32 v99, 0, v104, s[60:61]\ns_mov_b32 s60, 0xaa80aaa8\ns_mov_b32 s61, 0x8000a800\nv_cndmask_b32 v99, v99, v105, s[60:61]\nv_bfi_b32 v103, v99, 0, v106\nv_add_u32 v104, 161792, v252\n"
"ds_write_b128 v104, v[96:99] offset:0\nds_write_b128 v104, v[100:103] offset:1024\ns_waitcnt vmcnt(28)\nv_readfirstlane_b32 s83, v230\nv_readfirstlane_b32 s60, v231\ns_mul_i32 s46, s83, 0x9e3779b1\n"
"s_add_u32 s46, s46, 0x7f4a7c15\ns_xor_b32 s47, s83, s60\ns_mul_i32 s47, s47, 0x85ebca6b\ns_add_u32 s47, s47, 0xc2b2ae35\nv_mov_b32 v144, -1\nv_mov_b32 v145, -1\n"
"v_mov_b32 v146, -1\nv_mov_b32 v147, -1\ns_mov_b64 exec, 0\ns_cmp_eq_u32 s40, 1\ns_cbranch_scc1 Lpg2\ns_cmp_eq_u32 s40, 2\n"
"s_cbranch_scc1 Lpg3\ns_cmp_eq_u32 s40, 3\ns_cbranch_scc1 Lpg4\ns_branch Lpg1\nLretQ0:\ns_mov_b64 exec, -1\n"
"ds_read_b128 a[0:3], v248 offset:0\nds_read_b128 a[4:7], v248 offset:2048\nds_read_b128 a[8:11], v248 offset:4096\nds_read_b128 a[12:15], v248 offset:6144\nds_read_b128 a[16:19], v249 offset:0\nds_read_b128 a[20:23], v249 offset:2048\n"
"ds_read_b128 a[24:27], v249 offset:4096\nds_read_b128 a[28:31], v249 offset:6144\ns_waitcnt vmcnt(12) lgkmcnt(8)\ns_barrier\nR 160,184,0\nR 162,184,4096\n"
"R 164,184,8192\nR 166,184,12288\nR 168,185,0\nR 170,185,4096\nR 172,185,8192\nR 174,185,12288\n"
"R 176,186,0\nR 178,186,4096\nR 180,186,8192\nR 182,186,12288\nG 10\nv_mfma_f32_16x16x32_bf16 v[0:3], v[160:163], a[0:3], 0\n"
"s_add_u32 m0, s50, 49152\ns_add_u32 s63, s52, 395264\nbuffer_load_dwordx4 v220, s[4:7], s63 offen lds\nG 8\nM 0,164,16\nbuffer_load_dwordx4 v221, s[4:7], s63 offen offset:1024 lds\n"
"v_mfma_f32_16x16x32_bf16 v[16:19], v[160:163], a[4:7], 0\ns_add_u32 m0, s51, 0\ns_add_u32 s60, s70, 256\nbuffer_load_dwordx4 v226, s[24:27], s60 offen lds\nM 16,164,20\ns_lshl4_add_u32 s62, s41, s42\n"
"s_lshl_b32 s62, s62, 12\ns_add_u32 s62, s62, 77594624\ns_lshl_b32 s60, s40, 10\ns_add_u32 s62, s62, s60\nbuffer_store_dwordx4 v[144:147], v252, s[20:23], s62 offen sc1\ns_add_u32 m0, s50, 57344\n"
"s_add_u32 s63, s52, 591872\nv_mfma_f32_16x16x32_bf16 v[32:35], v[160:163], a[8:11], 0\nbuffer_load_dwordx4 v220, s[4:7], s63 offen lds\nM 32,164,24\nbuffer_load_dwordx4 v221, s[4:7], s63 offen offset:1024 lds\nv_mfma_f32_16x16x32_bf16 v[48:51], v[160:163], a[12:15], 0\n"
"s_add_u32 m0, s51, 1024\ns_add_u32 s60, s70, 16640\nbuffer_load_dwordx4 v227, s[24:27], s60 offen lds\nM 48,164,28\ns_add_u32 m0, s50, 65536\ns_add_u32 s63, s52, 397312\n"
"buffer_load_dwordx4 v220, s[4:7], s63 offen lds\nG 4\nv_mfma_f32_16x16x32_bf16 v[52:55], v[168:171], a[12:15], 0\nR 160,187,0\nM 52,172,28\nbuffer_load_dwordx4 v221, s[4:7], s63 offen offset:1024 lds\n"
"v_mfma_f32_16x16x32_bf16 v[36:39], v[168:171], a[8:11], 0\nR 162,187,4096\nM 36,172,24\ns_add_u32 m0, s51, 2048\ns_add_u32 s60, s70, 33024\nbuffer_load_dwordx4 v226, s[24:27], s60 offen lds\n"
"v_mfma_f32_16x16x32_bf16 v[20:23], v[168:171], a[4:7], 0\nR 164,187,8192\ns_add_u32 m0, s50, 73728\nM 20,172,20\ns_lshl4_add_u32 s62, s41, s42\ns_lshl_b32 s62, s62, 12\n"
"s_add_u32 s62, s62, 78643200\ns_lshl_b32 s60, s40, 10\ns_add_u32 s62, s62, s60\nbuffer_store_dwordx4 v[144:147], v252, s[20:23], s62 offen sc1\ns_add_u32 s63, s52, 593920\nv_mfma_f32_16x16x32_bf16 v[4:7], v[168:171], a[0:3], 0\n"
"R 166,187,12288\nbuffer_load_dwordx4 v220, s[4:7], s63 offen lds\nM 4,172,16\nG 4\nv_mfma_f32_16x16x32_bf16 v[8:11], v[176:179], a[0:3], 0\nR 168,184,16384\n"
"M 8,180,16\ns_add_u32 m0, s51, 3072\ns_add_u32 s60, s70, 49408\nbuffer_load_dwordx4 v227, s[24:27], s60 offen lds\nv_mfma_f32_16x16x32_bf16 v[24:27], v[176:179], a[4:7], 0\nR 170,184,20480\n"
"s_add_u32 m0, s50, 73728\ns_nop 0\nbuffer_load_dwordx4 v221, s[4:7], s63 offen offset:1024 lds\nM 24,180,20\ns_add_u32 m0, s51, 4096\ns_add_u32 s60, s71, 256\n"
"buffer_load_dwordx4 v226, s[24:27], s60 offen lds\nv_mfma_f32_16x16x32_bf16 v[40:43], v[176:179], a[8:11], 0\nR 172,184,24576\nM 40,180,24\nv_mfma_f32_16x16x32_bf16 v[56:59], v[176:179], a[12:15], 0\nR 174,184,28672\n"
"M 56,180,28\nG 4\nv_mfma_f32_16x16x32_bf16 v[60:63], v[160:163], a[12:15], 0\nR 176,185,16384\nM 60,164,28\ns_add_u32 m0, s51, 5120\n"
"s_add_u32 s60, s71, 16640\nbuffer_load_dwordx4 v227, s[24:27], s60 offen lds\nv_mfma_f32_16x16x32_bf16 v[44:47], v[160:163], a[8:11], 0\nR 178,185,20480\nM 44,164,24\ns_add_u32 m0, s51, 6144\n"
"s_add_u32 s60, s71, 33024\nbuffer_load_dwordx4 v226, s[24:27], s60 offen lds\nv_mfma_f32_16x16x32_bf16 v[28:31], v[160:163], a[4:7], 0\nR 180,185,24576\ns_add_u32 m0, s50, 81920\ns_add_u32 s63, s101, 0\n"
"M 28,164,20\nbuffer_load_dwordx4 v220, s[4:7], s63 offen lds\nv_mfma_f32_16x16x32_bf16 v[12:15], v[160:163], a[0:3], 0\nR 182,185,28672\nM 12,164,16\ns_waitcnt vmcnt(12)\n"
"s_barrier\nG 4\nv_mfma_f32_16x16x32_bf16 v[64:67], a[0:3], v[168:171], 0\nR 160,186,16384\nN 64,16,172\ns_add_u32 m0, s51, 7168\n"
"s_add_u32 s60, s71, 49408\nbuffer_load_dwordx4 v227, s[24:27], s60 offen lds\nv_mfma_f32_16x16x32_bf16 v[68:71], a[4:7], v[168:171], 0\nR 162,186,20480\ns_add_u32 m0, s50, 81920\nN 68,20,172\n"
"buffer_load_dwordx4 v221, s[4:7], s63 offen offset:1024 lds\nv_mfma_f32_16x16x32_bf16 v[96:99], a[8:11], v[168:171], 0\nR 164,186,24576\ns_add_u32 m0, s50, 0\ns_add_u32 s63, s52, 788480\nN 96,24,172\n"
"buffer_load_dwordx4 v220, s[4:7], s63 offen lds\nv_mfma_f32_16x16x32_bf16 v[100:103], a[12:15], v[168:171], 0\nR 166,186,28672\nN 100,28,172\nG 4\nv_mfma_f32_16x16x32_bf16 v[108:111], a[12:15], v[176:179], 0\n"
"R 168,187,16384\nv_mov_b32 v242, s76\nv_mov_b32 v243, s76\nN 108,28,180\nbuffer_load_dwordx4 v221, s[4:7], s63 offen offset:1024 lds\nv_mov_b32 v244, s76\n"
"v_mfma_f32_16x16x32_bf16 v[104:107], a[8:11], v[176:179], 0\nR 170,187,20480\ns_add_u32 s63, s52, 985088\nN 104,24,180\ns_add_u32 m0, s50, 8192\ns_lshl_b32 s60, s40, 5\n"
"v_xor_b32 v238, s60, v184\nv_mfma_f32_16x16x32_bf16 v[76:79], a[4:7], v[176:179], 0\nR 172,187,24576\nv_and_b32 v238, 0xfffffbff, v238\nN 76,20,180\nbuffer_load_dwordx4 v220, s[4:7], s63 offen lds\n"
"v_mfma_f32_16x16x32_bf16 v[72:75], a[0:3], v[176:179], 0\nR 174,187,28672\nv_mov_b32 v245, s76\nN 72,16,180\nG 4\nv_mfma_f32_16x16x32_bf16 v[80:83], a[0:3], v[160:163], 0\n"
"R 176,184,49152\nds_read_b128 a[32:35], v250 offset:0\nR 128,238,32768\nR 130,238,36864\nN 80,16,164\nds_read_b128 a[36:39], v250 offset:2048\n"
"v_mfma_f32_16x16x32_bf16 v[84:87], a[4:7], v[160:163], 0\nR 178,184,53248\nds_read_b128 a[40:43], v250 offset:4096\nR 132,238,33792\nR 134,238,37888\nN 84,20,164\n"
"ds_read_b128 a[44:47], v250 offset:6144\nv_mfma_f32_16x16x32_bf16 v[112:115], a[8:11], v[160:163], 0\nR 180,184,57344\nbuffer_load_dwordx4 v221, s[4:7], s63 offen offset:1024 lds\nN 112,24,164\nds_read_b128 a[48:51], v251 offset:0\n"
"v_mfma_f32_16x16x32_bf16 v[116:119], a[12:15], v[160:163], 0\nR 182,184,61440\nds_read_b128 a[52:55], v251 offset:2048\nN 116,28,164\nds_read_b128 a[56:59], v251 offset:4096\nG 15\n"
"v_mfma_f32_16x16x32_bf16 v[124:127], a[12:15], v[168:171], 0\nR 160,185,49152\nG 12\nv_mfma_f32_16x16x32_bf16 v[136:139], v[242:245], v[128:131], 0\nN 124,28,172\nds_read_b128 a[60:63], v251 offset:6144\n"
"G 8\nv_mfma_f32_16x16x32_bf16 v[136:139], v[242:245], v[132:135], v[136:139]\nv_mfma_f32_16x16x32_bf16 v[120:123], a[8:11], v[168:171], 0\nR 162,185,53248\nN 120,24,172\nv_mfma_f32_16x16x32_bf16 v[92:95], a[4:7], v[168:171], 0\n"
"R 164,185,57344\nN 92,20,172\nG 2\ns_add_u32 m0, s51, 122880\ns_add_u32 s60, s70, 384\nbuffer_load_dwordx4 v226, s[24:27], s60 offen lds\n"
"v_mfma_f32_16x16x32_bf16 v[88:91], a[0:3], v[168:171], 0\nR 166,185,61440\nv_lshrrev_b32 v128, 1, v252\nv_mov_b32 v137, s46\nN 88,16,172\ns_waitcnt vmcnt(11)\n"
"s_barrier\nM 0,176,32\nR 168,186,49152\ns_lshl_b32 s60, s40, 5\nv_xor_b32 v238, s60, v184\nM 0,180,48\n"
"s_add_u32 m0, s51, 123904\ns_add_u32 s60, s70, 16768\nbuffer_load_dwordx4 v227, s[24:27], s60 offen lds\ns_and_b32 s61, s40, 3\ns_lshl_b32 s61, s61, 7\ns_add_u32 s61, s61, s91\n"
"s_add_u32 s61, s61, 131072\ns_mov_b64 exec, 0xffff\nbuffer_store_dwordx2 v[136:137], v128, s[20:23], s61 offen sc1\ns_mov_b64 exec, -1\nM 16,176,36\nR 170,186,53248\n"
"s_add_u32 m0, s50, 16384\ns_add_u32 s63, s52, 790528\nM 16,180,52\nbuffer_load_dwordx4 v220, s[4:7], s63 offen lds\nM 32,176,40\nR 172,186,57344\n"
"v_and_b32 v238, 0xfffffbff, v238\nM 32,180,56\ns_add_u32 m0, s51, 124928\ns_add_u32 s60, s70, 33152\nbuffer_load_dwordx4 v226, s[24:27], s60 offen lds\nM 48,176,44\n"
"R 174,186,61440\nLpg1:\ns_cbranch_execz Lpg5\ns_add_u32 m0, s50, 16384\nv_mov_b32 v230, 0\nM 48,180,60\n"
"buffer_load_dwordx4 v221, s[4:7], s63 offen offset:1024 lds\nG 4\nv_mov_b32 v231, 0\nM 52,160,44\nR 176,187,49152\nR 128,238,40960\n"
"R 130,238,45056\nv_mov_b32 v232, 0\nM 52,164,60\ns_add_u32 m0, s51, 125952\ns_add_u32 s60, s70, 49536\nbuffer_load_dwordx4 v227, s[24:27], s60 offen lds\n"
"v_mov_b32 v233, 0\nM 36,160,40\nR 178,187,53248\nR 132,238,41984\nR 134,238,46080\ns_add_u32 m0, s50, 24576\n"
"s_add_u32 s63, s52, 987136\nv_dot2c_f32_bf16 v230, s76, v192\nM 36,164,56\nbuffer_load_dwordx4 v220, s[4:7], s63 offen lds\nv_dot2c_f32_bf16 v231, v192, v192\nM 20,160,36\n"
"R 180,187,57344\nv_lshrrev_b32 v234, 5, v252\nv_dot2c_f32_bf16 v232, s76, v200\nM 20,164,52\ns_add_u32 m0, s51, 126976\ns_add_u32 s60, s71, 384\n"
"buffer_load_dwordx4 v226, s[24:27], s60 offen lds\nv_dot2c_f32_bf16 v233, v200, v200\nM 4,160,32\nR 182,187,61440\ns_add_u32 m0, s50, 24576\nv_dot2c_f32_bf16 v230, s76, v193\n"
"M 4,164,48\nG 8\nv_and_b32 v234, 24, v234\nv_bfe_u32 v235, s46, v234, 8\nv_dot2c_f32_bf16 v231, v193, v193\nM 8,168,32\n"
"R 160,188,0\nG 6\nv_mfma_f32_16x16x32_bf16 v[136:139], v[242:245], v[128:131], 0\nbuffer_load_dwordx4 v221, s[4:7], s63 offen offset:1024 lds\nv_dot2c_f32_bf16 v232, s76, v201\nM 8,172,48\n"
"s_add_u32 m0, s51, 128000\ns_add_u32 s60, s71, 16768\nbuffer_load_dwordx4 v227, s[24:27], s60 offen lds\nG 3\nv_mfma_f32_16x16x32_bf16 v[136:139], v[242:245], v[132:135], v[136:139]\nv_dot2c_f32_bf16 v233, v201, v201\n"
"M 24,168,36\nR 162,188,4096\nv_lshrrev_b32 v234, 1, v234\nv_dot2c_f32_bf16 v230, s76, v194\nM 24,172,52\nv_and_b32 v236, 0xf0, v252\n"
"v_add_u32 v234, v234, v236\nv_dot2c_f32_bf16 v231, v194, v194\nM 40,168,40\nR 164,188,8192\nv_dot2c_f32_bf16 v232, s76, v202\nM 40,172,56\n"
"s_add_u32 m0, s51, 129024\ns_add_u32 s60, s71, 33152\nbuffer_load_dwordx4 v226, s[24:27], s60 offen lds\nv_dot2c_f32_bf16 v233, v202, v202\nM 56,168,44\nR 166,188,12288\n"
"v_lshrrev_b32 v128, 1, v252\nv_dot2c_f32_bf16 v230, s76, v195\nM 56,172,60\nG 4\nv_mov_b32 v137, s46\nv_dot2c_f32_bf16 v231, v195, v195\n"
"M 60,176,44\nR 168,189,0\nv_dot2c_f32_bf16 v232, s76, v203\nM 60,180,60\ns_add_u32 m0, s51, 130048\ns_add_u32 s60, s71, 49536\n"
"buffer_load_dwordx4 v227, s[24:27], s60 offen lds\ns_and_b32 s61, s40, 3\ns_lshl_b32 s61, s61, 7\ns_add_u32 s61, s61, s91\ns_add_u32 s61, s61, 262144\ns_mov_b64 exec, 0xffff\n"
"buffer_store_dwordx2 v[136:137], v128, s[20:23], s61 offen sc1\ns_mov_b64 exec, -1\nv_dot2c_f32_bf16 v233, v203, v203\nM 44,176,40\nR 170,189,4096\nv_dot2c_f32_bf16 v230, s76, v196\n"
"M 44,180,56\nv_dot2c_f32_bf16 v231, v196, v196\nM 28,176,36\nR 172,189,8192\nv_dot2c_f32_bf16 v232, s76, v204\nM 28,180,52\n"
"s_add_u32 m0, s50, 32768\nv_dot2c_f32_bf16 v233, v204, v204\nM 12,176,32\nR 174,189,12288\nv_dot2c_f32_bf16 v230, s76, v197\nM 12,180,48\n"
"s_waitcnt vmcnt(14)\ns_barrier\nG 4\nv_dot2c_f32_bf16 v231, v197, v197\nN 64,32,160\nR 176,190,0\n"
"v_dot2c_f32_bf16 v232, s76, v205\nN 64,48,164\ns_add_u32 s63, s52, 1181696\nv_dot2c_f32_bf16 v233, v205, v205\nN 68,36,160\nR 178,190,4096\n"
"v_dot2c_f32_bf16 v230, s76, v198\nN 68,52,164\nbuffer_load_dwordx4 v220, s[4:7], s63 offen lds\nv_dot2c_f32_bf16 v231, v198, v198\nN 96,40,160\nR 180,190,8192\n"
"ds_read_b128 a[64:67], v248 offset:0\nv_dot2c_f32_bf16 v232, s76, v206\nN 96,56,164\nds_read_b128 a[68:71], v248 offset:2048\nv_dot2c_f32_bf16 v233, v206, v206\nN 100,44,160\n"
"R 182,190,12288\nds_read_b128 a[72:75], v248 offset:4096\nv_dot2c_f32_bf16 v230, s76, v199\nN 100,60,164\nds_read_b128 a[76:79], v248 offset:6144\nG 8\n"
"v_dot2c_f32_bf16 v231, v199, v199\nN 108,44,168\nR 160,191,0\ns_nop 0\nv_permlane32_swap_b32 v230, v231\nv_dot2c_f32_bf16 v232, s76, v207\n"
"N 108,60,172\nds_read_b128 a[80:83], v249 offset:0\nbuffer_load_dwordx4 v221, s[4:7], s63 offen offset:1024 lds\nv_dot2c_f32_bf16 v233, v207, v207\nN 104,40,168\nR 162,191,4096\n"
"ds_read_b128 a[84:87], v249 offset:2048\nN 104,56,172\nds_read_b128 a[88:91], v249 offset:4096\ns_add_u32 m0, s50, 40960\nN 76,36,168\nR 164,191,8192\n"
"s_add_u32 s63, s52, 1378304\nN 76,52,172\nds_read_b128 a[92:95], v249 offset:6144\nv_permlane32_swap_b32 v232, v233\nN 72,32,168\nR 166,191,12288\n"
"buffer_load_dwordx4 v220, s[4:7], s63 offen lds\nN 72,48,172\nG 10\nv_add_f32 v230, v230, v231\nv_add_f32 v232, v232, v233\nN 80,32,176\n"
"R 168,184,0\nv_permlane16_swap_b32 v230, v232\nN 80,48,180\nG 2\ns_add_u32 m0, s51, 0\ns_add_u32 s60, s70, 512\n"
"buffer_load_dwordx4 v226, s[24:27], s60 offen lds\nN 84,36,176\nR 170,184,4096\ns_add_u32 m0, s50, 40960\ns_nop 0\nbuffer_load_dwordx4 v221, s[4:7], s63 offen offset:1024 lds\n"
"N 84,52,180\nv_add_f32 v230, v230, v232\ns_movk_i32 s62, 0xff\nv_bfi_b32 v230, s62, v235, v230\nN 112,40,176\nR 172,184,8192\n"
"N 112,56,180\ns_add_u32 m0, s51, 1024\ns_add_u32 s60, s70, 16896\nbuffer_load_dwordx4 v227, s[24:27], s60 offen lds\nN 116,44,176\nR 174,184,12288\n"
"N 116,60,180\nG 4\nN 124,44,160\nR 176,185,0\nN 124,60,164\ns_add_u32 m0, s51, 2048\n"
"s_add_u32 s60, s70, 33280\nbuffer_load_dwordx4 v226, s[24:27], s60 offen lds\nN 120,40,160\nR 178,185,4096\nN 120,56,164\ns_lshl_b32 s60, s40, 5\n"
"v_xor_b32 v238, s60, v188\ns_lshr_b32 s61, s49, 4\ns_lshl1_add_u32 s61, s53, s61\ns_add_u32 s61, s61, 81788928\nbuffer_store_dword v230, v234, s[20:23], s61 offen sc1\nv_and_b32 v238, 0xfffffbff, v238\n"
"N 92,36,160\nR 180,185,8192\nN 92,52,164\ns_add_u32 m0, s51, 3072\ns_add_u32 s60, s70, 49664\nbuffer_load_dwordx4 v227, s[24:27], s60 offen lds\n"
"N 88,32,160\nR 182,185,12288\nN 88,48,164\ns_waitcnt vmcnt(13)\ns_barrier\nG 4\n"
"M 0,168,64\nR 160,186,0\nM 0,172,80\ns_add_u32 m0, s51, 4096\ns_add_u32 s60, s71, 512\nbuffer_load_dwordx4 v226, s[24:27], s60 offen lds\n"
"M 16,168,68\nR 162,186,4096\nR 128,238,16384\nR 130,238,20480\ns_add_u32 m0, s50, 49152\ns_add_u32 s63, s52, 1183744\n"
"M 16,172,84\nbuffer_load_dwordx4 v220, s[4:7], s63 offen lds\nM 32,168,72\nR 164,186,8192\nR 132,238,17408\nR 134,238,21504\n"
"M 32,172,88\ns_add_u32 m0, s51, 5120\ns_add_u32 s60, s71, 16896\nbuffer_load_dwordx4 v227, s[24:27], s60 offen lds\nM 48,168,76\nR 166,186,12288\n"
"s_add_u32 m0, s50, 49152\nM 48,172,92\nG 8\nM 52,176,76\nR 168,187,0\nbuffer_load_dwordx4 v221, s[4:7], s63 offen offset:1024 lds\n"
"M 52,180,92\ns_add_u32 m0, s51, 6144\ns_add_u32 s60, s71, 33280\nbuffer_load_dwordx4 v226, s[24:27], s60 offen lds\nM 36,176,72\nR 170,187,4096\n"
"G 6\nv_mfma_f32_16x16x32_bf16 v[136:139], v[242:245], v[128:131], 0\ns_add_u32 m0, s50, 57344\ns_add_u32 s63, s52, 1380352\nM 36,180,88\nG 3\n"
"v_mfma_f32_16x16x32_bf16 v[136:139], v[242:245], v[132:135], v[136:139]\nbuffer_load_dwordx4 v220, s[4:7], s63 offen lds\nM 20,176,68\nR 172,187,8192\nM 20,180,84\ns_add_u32 m0, s51, 7168\n"
"s_add_u32 s60, s71, 49664\nbuffer_load_dwordx4 v227, s[24:27], s60 offen lds\nM 4,176,64\nR 174,187,12288\ns_add_u32 m0, s50, 57344\nM 4,180,80\n"
"G 4\nM 8,160,64\nR 176,184,16384\nbuffer_load_dwordx4 v221, s[4:7], s63 offen offset:1024 lds\nM 8,164,80\nv_lshrrev_b32 v128, 1, v252\n"
"v_mov_b32 v137, s46\nM 24,160,68\nR 178,184,20480\nM 24,164,84\ns_and_b32 s61, s40, 3\ns_lshl_b32 s61, s61, 7\n"
"s_add_u32 s61, s61, s91\ns_add_u32 s61, s61, 0\ns_mov_b64 exec, 0xffff\nbuffer_store_dwordx2 v[136:137], v128, s[20:23], s61 offen sc1\ns_mov_b64 exec, -1\nM 40,160,72\n"
"R 180,184,24576\nM 40,164,88\nM 56,160,76\nR 182,184,28672\nM 56,164,92\nG 4\n"
"M 60,168,76\nR 160,185,16384\nM 60,172,92\ns_add_u32 m0, s50, 65536\nM 44,168,72\nR 162,185,20480\n"
"M 44,172,88\ns_add_u32 s63, s52, 1574912\nM 28,168,68\nR 164,185,24576\nM 28,172,84\nbuffer_load_dwordx4 v220, s[4:7], s63 offen lds\n"
"M 12,168,64\nR 166,185,28672\nM 12,172,80\ns_waitcnt vmcnt(14)\ns_barrier\nG 4\n"
"N 64,64,176\nR 168,186,16384\nN 64,80,180\nv_lshrrev_b32 v230, 7, v252\nN 68,68,176\nR 170,186,20480\n"
"N 68,84,180\nbuffer_load_dwordx4 v221, s[4:7], s63 offen offset:1024 lds\nv_lshlrev_b32 v230, 9, v230\nN 96,72,176\nR 172,186,24576\ns_add_u32 m0, s50, 73728\n"
"N 96,88,180\nds_read_b128 a[96:99], v250 offset:0\nN 100,76,176\nR 174,186,28672\nds_read_b128 a[100:103], v250 offset:2048\ns_add_u32 s63, s52, 1771520\n"
"N 100,92,180\nds_read_b128 a[104:107], v250 offset:4096\nG 7\nN 108,76,160\nR 176,187,16384\nv_and_b32 v231, 0x70, v252\n"
"N 108,92,164\nds_read_b128 a[108:111], v250 offset:6144\nbuffer_load_dwordx4 v220, s[4:7], s63 offen lds\nN 104,72,160\nR 178,187,20480\nds_read_b128 a[112:115], v251 offset:0\n"
"N 104,88,164\nds_read_b128 a[116:119], v251 offset:2048\ns_and_b32 s60, s40, 3\ns_lshl_b32 s60, s60, 7\nv_add3_u32 v230, v230, v231, s60\nN 76,68,160\n"
"R 180,187,24576\nds_read_b128 a[120:123], v251 offset:4096\nN 76,84,164\nds_read_b128 a[124:127], v251 offset:6144\nN 72,64,160\nR 182,187,28672\n"
"N 72,80,164\ns_lshl_b32 s61, s42, 13\ns_add_u32 s61, s61, 84017152\nbuffer_load_dwordx4 v[128:131], v230, s[20:23], s61 offen sc1\nG 11\nN 80,64,168\n"
"R 160,184,32768\nbuffer_load_dwordx4 v221, s[4:7], s63 offen offset:1024 lds\nN 80,80,172\ns_lshl_b32 s61, s42, 13\ns_add_u32 s61, s61, 84021248\nbuffer_load_dwordx4 v[132:135], v230, s[20:23], s61 offen sc1\n"
"N 84,68,168\nR 162,184,36864\nN 84,84,172\nN 112,72,168\nR 164,184,40960\ns_waitcnt vmcnt(8)\n"
"ds_read_b128 a[128:131], v248 offset:0\nN 112,88,172\nds_read_b128 a[132:135], v248 offset:2048\nN 116,76,168\nR 166,184,45056\nds_read_b128 a[136:139], v248 offset:4096\n"
"N 116,92,172\nds_read_b128 a[140:143], v248 offset:6144\nG 8\nN 124,76,176\nR 168,185,32768\nN 124,92,180\n"
"ds_read_b128 a[144:147], v249 offset:0\nN 120,72,176\nR 170,185,36864\nds_read_b128 a[148:151], v249 offset:2048\nN 120,88,180\nds_read_b128 a[152:155], v249 offset:4096\n"
"N 92,68,176\nR 172,185,40960\nN 92,84,180\nds_read_b128 a[156:159], v249 offset:6144\nN 88,64,176\nR 174,185,45056\n"
"N 88,80,180\ns_waitcnt vmcnt(7)\ns_barrier\nG 10\nM 0,160,96\nR 176,186,32768\n"
"M 0,164,112\nG 2\ns_add_u32 m0, s51, 0\ns_add_u32 s60, s70, 640\nbuffer_load_dwordx4 v226, s[24:27], s60 offen lds\nM 16,160,100\n"
"R 178,186,36864\nM 16,164,116\ns_add_u32 m0, s51, 1024\ns_add_u32 s60, s70, 17024\nbuffer_load_dwordx4 v227, s[24:27], s60 offen lds\nM 32,160,104\n"
"R 180,186,40960\ns_add_u32 m0, s50, 81920\ns_add_u32 s63, s52, 1576960\nbuffer_load_dwordx4 v220, s[4:7], s63 offen lds\nv_lshrrev_b32 v234, 7, v252\nM 32,164,120\n"
"s_add_u32 m0, s51, 2048\ns_add_u32 s60, s70, 33408\nbuffer_load_dwordx4 v226, s[24:27], s60 offen lds\nM 48,160,108\nR 182,186,45056\nM 48,164,124\n"
"s_add_u32 m0, s51, 3072\ns_add_u32 s60, s70, 49792\nbuffer_load_dwordx4 v227, s[24:27], s60 offen lds\nG 4\nM 52,168,108\nR 160,187,32768\n"
"s_add_u32 m0, s50, 81920\ns_nop 0\nbuffer_load_dwordx4 v221, s[4:7], s63 offen offset:1024 lds\nv_lshlrev_b32 v234, 9, v234\nM 52,172,124\ns_add_u32 m0, s51, 4096\n"
"s_add_u32 s60, s71, 640\nbuffer_load_dwordx4 v226, s[24:27], s60 offen lds\nM 36,168,104\nR 162,187,36864\nv_and_b32 v235, 0x70, v252\nM 36,172,120\n"
"s_add_u32 m0, s51, 5120\ns_add_u32 s60, s71, 17024\nbuffer_load_dwordx4 v227, s[24:27], s60 offen lds\nM 20,168,100\nR 164,187,40960\ns_add_u32 m0, s50, 90112\n"
"s_add_u32 s63, s52, 1773568\nbuffer_load_dwordx4 v220, s[4:7], s63 offen lds\nM 20,172,116\ns_add_u32 m0, s51, 6144\ns_add_u32 s60, s71, 33408\nbuffer_load_dwordx4 v226, s[24:27], s60 offen lds\n"
"M 4,168,96\nR 166,187,45056\ns_and_b32 s60, s40, 3\ns_lshl_b32 s60, s60, 7\nv_add3_u32 v234, v234, v235, s60\nM 4,172,112\n"
"s_add_u32 m0, s51, 7168\ns_add_u32 s60, s71, 49792\nbuffer_load_dwordx4 v227, s[24:27], s60 offen lds\nG 4\nM 8,176,96\nR 168,184,49152\n"
"s_add_u32 m0, s50, 90112\ns_nop 0\nbuffer_load_dwordx4 v221, s[4:7], s63 offen offset:1024 lds\nM 8,180,112\ns_lshl_b32 s61, s42, 13\ns_add_u32 s61, s61, 84148224\n"
"buffer_load_dwordx4 v[144:147], v234, s[20:23], s61 offen sc1\nM 24,176,100\nR 170,184,53248\nM 24,180,116\nM 40,176,104\nR 172,184,57344\n"
"M 40,180,120\ns_lshl_b32 s61, s42, 13\ns_add_u32 s61, s61, 84152320\nbuffer_load_dwordx4 v[148:151], v234, s[20:23], s61 offen sc1\nM 56,176,108\nR 174,184,61440\n"
"M 56,180,124\nG 4\nM 60,160,108\nR 176,185,49152\nM 60,164,124\nM 44,160,104\n"
"R 178,185,53248\nM 44,164,120\ns_add_u32 m0, s50, 98304\nM 28,160,100\nR 180,185,57344\nM 28,164,116\n"
"s_add_u32 s63, s52, 1968128\nM 12,160,96\nR 182,185,61440\nM 12,164,112\ns_waitcnt vmcnt(15)\ns_barrier\n"
"G 4\nN 64,96,168\nR 160,186,49152\ns_waitcnt vmcnt(14)\nv_min3_u32 v232, v129, v131, v133\nv_min3_u32 v232, v232, v135, v135\n"
"N 64,112,172\nbuffer_load_dwordx4 v220, s[4:7], s63 offen lds\nv_max3_u32 v233, v129, v131, v133\nN 68,100,168\nR 162,186,53248\nv_max3_u32 v233, v233, v135, v135\n"
"v_cmp_ne_u32 s[86:87], s46, v232\nN 68,116,172\nv_cmp_ne_u32 vcc, s46, v233\nN 96,104,168\nR 164,186,57344\ns_or_b64 s[86:87], s[86:87], vcc\n"
"s_cbranch_scc1 Lq116\nLq117:\nN 96,120,172\nbuffer_load_dwordx4 v221, s[4:7], s63 offen offset:1024 lds\nv_add_f32 v128, v128, v132\nN 100,108,168\n"
"R 166,186,61440\ns_add_u32 m0, s50, 106496\nv_add_f32 v130, v130, v134\nN 100,124,172\nG 4\nv_add_f32_dpp v128, v128, v128 row_ror:8 row_mask:0xf bank_mask:0xf\n"
"v_add_f32_dpp v130, v130, v130 row_ror:8 row_mask:0xf bank_mask:0xf\nv_mov_b32 v132, v128\nN 108,108,176\nR 168,187,49152\ns_add_u32 s63, s52, 2164736\nv_mov_b32 v133, v130\n"
"N 108,124,180\nbuffer_load_dwordx4 v220, s[4:7], s63 offen lds\nv_permlane32_swap_b32 v132, v128\nN 104,104,176\nR 170,187,53248\nv_permlane32_swap_b32 v133, v130\n"
"v_add_f32 v128, v128, v132\nN 104,120,180\nv_add_f32 v130, v130, v133\nv_mov_b32 v132, v128\nv_mov_b32 v133, v130\nN 76,100,176\n"
"R 172,187,57344\nv_permlane16_swap_b32 v132, v128\nv_permlane16_swap_b32 v133, v130\nN 76,116,180\nbuffer_load_dwordx4 v221, s[4:7], s63 offen offset:1024 lds\nv_add_f32 v128, v128, v132\n"
"N 72,96,176\nR 174,187,61440\ns_add_u32 s63, s52, 1970176\nN 72,112,180\nG 4\nv_add_f32 v130, v130, v133\n"
"v_and_b32 v136, s77, v128\nN 80,96,160\nR 176,188,0\nv_sub_f32 v137, v128, v136\nN 80,112,164\ns_add_u32 m0, s50, 114688\n"
"v_cvt_pk_bf16_f32 v132, v136, v137\nv_cvt_pk_bf16_f32 v133, v136, 0\nN 84,100,160\nR 178,188,4096\nLpg2:\ns_cbranch_execz LretQ0\n"
"v_and_b32 v136, s77, v130\nN 84,116,164\nbuffer_load_dwordx4 v220, s[4:7], s63 offen lds\nN 112,104,160\nR 180,188,8192\nv_sub_f32 v137, v130, v136\n"
"N 112,120,164\nv_cvt_pk_bf16_f32 v134, v136, v137\nv_cvt_pk_bf16_f32 v135, v136, 0\nN 116,108,160\nR 182,188,12288\nv_and_b32 v230, 0x1ff, v230\n"
"N 116,124,164\nv_add_u32 v230, 156160, v230\nds_write_b128 v230, v[132:135]\nG 5\nN 124,108,168\nR 160,189,0\n"
"N 124,124,172\nbuffer_load_dwordx4 v221, s[4:7], s63 offen offset:1024 lds\nN 120,104,168\nR 162,189,4096\ns_add_u32 s63, s52, 2166784\nN 120,120,172\n"
"s_add_u32 m0, s50, 0\nN 92,100,168\nR 164,189,8192\nN 92,116,172\nbuffer_load_dwordx4 v220, s[4:7], s63 offen lds\nN 88,96,168\n"
"R 166,189,12288\nN 88,112,172\ns_waitcnt vmcnt(10)\nds_read_b128 a[160:163], v248 offset:0\ns_waitcnt vmcnt(9) lgkmcnt(5)\ns_barrier\n"
"M 0,176,128\nR 168,190,0\nds_read_b128 a[164:167], v248 offset:2048\nM 0,180,144\nds_read_b128 a[168:171], v248 offset:4096\nM 16,176,132\n"
"R 170,190,4096\ns_waitcnt vmcnt(7)\nv_min3_u32 v236, v145, v147, v149\nv_min3_u32 v236, v236, v151, v151\nM 16,180,148\nds_read_b128 a[172:175], v248 offset:6144\n"
"v_max3_u32 v237, v145, v147, v149\nv_max3_u32 v237, v237, v151, v151\nM 32,176,136\nR 172,190,8192\nds_read_b128 a[176:179], v249 offset:0\nv_cmp_ne_u32 s[86:87], s46, v236\n"
"M 32,180,152\nds_read_b128 a[180:183], v249 offset:2048\nv_lshrrev_b32 v230, 7, v252\nv_cmp_ne_u32 vcc, s46, v237\nM 48,176,140\nR 174,190,12288\n"
"ds_read_b128 a[184:187], v249 offset:4096\ns_or_b64 s[86:87], s[86:87], vcc\ns_cbranch_scc1 Lq124\nLq125:\nM 48,180,156\nds_read_b128 a[188:191], v249 offset:6144\n"
"G 12\nv_add_f32 v144, v144, v148\nM 52,160,140\nR 176,191,0\nv_add_f32 v146, v146, v150\nv_add_f32_dpp v144, v144, v144 row_ror:8 row_mask:0xf bank_mask:0xf\n"
"M 52,164,156\nbuffer_load_dwordx4 v221, s[4:7], s63 offen offset:1024 lds\nv_lshlrev_b32 v230, 9, v230\nv_add_f32_dpp v146, v146, v146 row_ror:8 row_mask:0xf bank_mask:0xf\nM 36,160,136\nR 178,191,4096\n"
"v_and_b32 v231, 0x70, v252\nv_mov_b32 v148, v144\nM 36,164,152\nG 2\ns_add_u32 m0, s51, 0\ns_add_u32 s60, s70, 768\n"
"buffer_load_dwordx4 v226, s[24:27], s60 offen lds\nv_mov_b32 v149, v146\nM 20,160,132\nR 180,191,8192\nv_permlane32_swap_b32 v148, v144\nv_permlane32_swap_b32 v149, v146\n"
"M 20,164,148\ns_add_u32 m0, s51, 1024\ns_add_u32 s60, s70, 17152\nbuffer_load_dwordx4 v227, s[24:27], s60 offen lds\nv_add_f32 v144, v144, v148\nM 4,160,128\n"
"R 182,191,12288\ns_and_b32 s60, s40, 3\ns_lshl_b32 s60, s60, 7\nv_add3_u32 v230, v230, v231, s60\nv_add_f32 v146, v146, v149\nM 4,164,144\n"
"s_add_u32 m0, s51, 2048\ns_add_u32 s60, s70, 33536\nbuffer_load_dwordx4 v226, s[24:27], s60 offen lds\nv_mov_b32 v148, v144\nM 8,168,128\nR 160,188,16384\n"
"v_mov_b32 v149, v146\nv_permlane16_swap_b32 v148, v144\nM 8,172,144\ns_add_u32 m0, s51, 3072\ns_add_u32 s60, s70, 49920\nbuffer_load_dwordx4 v227, s[24:27], s60 offen lds\n"
"v_permlane16_swap_b32 v149, v146\nM 24,168,132\nR 162,188,20480\ns_lshl_b32 s61, s42, 13\ns_add_u32 s61, s61, 83886080\nbuffer_load_dwordx4 v[128:131], v230, s[20:23], s61 offen sc1\n"
"v_add_f32 v144, v144, v148\nM 24,172,148\ns_add_u32 m0, s51, 4096\ns_add_u32 s60, s71, 768\nbuffer_load_dwordx4 v226, s[24:27], s60 offen lds\nv_add_f32 v146, v146, v149\n"
"M 40,168,136\nR 164,188,24576\ns_lshl_b32 s61, s42, 13\ns_add_u32 s61, s61, 83890176\nbuffer_load_dwordx4 v[132:135], v230, s[20:23], s61 offen sc1\nv_and_b32 v152, s77, v144\n"
"M 40,172,152\ns_add_u32 m0, s51, 5120\ns_add_u32 s60, s71, 17152\nbuffer_load_dwordx4 v227, s[24:27], s60 offen lds\nM 56,168,140\nR 166,188,28672\n"
"v_sub_f32 v153, v144, v152\nM 56,172,156\ns_add_u32 m0, s51, 6144\ns_add_u32 s60, s71, 33536\nbuffer_load_dwordx4 v226, s[24:27], s60 offen lds\nG 4\n"
"M 60,176,140\nR 168,189,16384\nv_cvt_pk_bf16_f32 v148, v152, v153\nM 60,180,156\ns_add_u32 m0, s51, 7168\ns_add_u32 s60, s71, 49920\n"
"buffer_load_dwordx4 v227, s[24:27], s60 offen lds\nM 44,176,136\nR 170,189,20480\nv_cvt_pk_bf16_f32 v149, v152, 0\nM 44,180,152\ns_add_u32 m0, s50, 8192\n"
"v_and_b32 v152, s77, v146\nv_sub_f32 v153, v146, v152\nM 28,176,132\nR 172,189,24576\nv_cvt_pk_bf16_f32 v150, v152, v153\nM 28,180,148\n"
"s_add_u32 s63, s52, 2361344\nv_cvt_pk_bf16_f32 v151, v152, 0\nv_and_b32 v234, 0x1ff, v234\nM 12,176,128\nR 174,189,28672\nv_add_u32 v234, 156672, v234\n"
"M 12,180,144\ns_waitcnt vmcnt(14)\ns_barrier\nG 4\nN 64,128,160\nR 176,190,16384\n"
"ds_write_b128 v234, v[148:151]\nN 64,144,164\nbuffer_load_dwordx4 v220, s[4:7], s63 offen lds\nN 68,132,160\nR 178,190,20480\nN 68,148,164\n"
"N 96,136,160\nR 180,190,24576\nN 96,152,164\nbuffer_load_dwordx4 v221, s[4:7], s63 offen offset:1024 lds\nN 100,140,160\nR 182,190,28672\n"
"s_add_u32 m0, s50, 16384\nN 100,156,164\nG 5\nN 108,140,168\nR 160,191,16384\ns_add_u32 s63, s52, 2557952\n"
"N 108,156,172\nbuffer_load_dwordx4 v220, s[4:7], s63 offen lds\nN 104,136,168\nR 162,191,20480\nN 104,152,172\nN 76,132,168\n"
"R 164,191,24576\nLq183:\ns_lshr_b32 s60, s45, 3\ns_lshl2_add_u32 s60, s60, s40\ns_lshl_b32 s60, s60, 8\nv_lshl_add_u32 v224, v252, 3, s60\n"
"v_add_u32 v224, Lq183+4096-LqPC, v224\nv_min_u32 v224, LqE-LqPC-4, v224\ns_mov_b64 exec, 3\nglobal_load_dword v224, v224, s[98:99]\ns_mov_b64 exec, -1\nN 76,148,172\n"
"buffer_load_dwordx4 v221, s[4:7], s63 offen offset:1024 lds\nN 72,128,168\nR 166,191,28672\nN 72,144,172\nG 4\nN 80,128,176\n"
"R 168,188,32768\nN 80,144,180\nN 84,132,176\nR 170,188,36864\nN 84,148,180\ns_add_u32 m0, s50, 24576\n"
"N 112,136,176\nR 172,188,40960\nN 112,152,180\ns_add_u32 s63, s52, 2754560\nN 116,140,176\nR 174,188,45056\n"
"N 116,156,180\nG 4\nN 124,140,160\nR 176,189,32768\nN 124,156,164\nbuffer_load_dwordx4 v220, s[4:7], s63 offen lds\n"
"N 120,136,160\nR 178,189,36864\nN 120,152,164\nN 92,132,160\nR 180,189,40960\ns_waitcnt vmcnt(9)\n"
"v_min3_u32 v232, v129, v131, v133\nv_min3_u32 v232, v232, v135, v135\nN 92,148,164\nbuffer_load_dwordx4 v221, s[4:7], s63 offen offset:1024 lds\nv_max3_u32 v233, v129, v131, v133\nN 88,128,160\n"
"R 182,189,45056\ns_add_u32 m0, s50, 32768\nv_max3_u32 v233, v233, v135, v135\nN 88,144,164\ns_barrier\nG 4\n"
"v_cmp_ne_u32 s[86:87], s46, v232\nM 0,168,160\nR 160,190,32768\ns_add_u32 s63, s52, 2951168\nv_cmp_ne_u32 vcc, s46, v233\nM 0,172,176\n"
"buffer_load_dwordx4 v220, s[4:7], s63 offen lds\ns_or_b64 s[86:87], s[86:87], vcc\ns_cbranch_scc1 Lq132\nLq133:\nM 16,168,164\nR 162,190,36864\n"
"v_add_f32 v128, v128, v132\nv_add_f32 v130, v130, v134\nM 16,172,180\nv_add_f32_dpp v128, v128, v128 row_ror:8 row_mask:0xf bank_mask:0xf\nv_add_f32_dpp v130, v130, v130 row_ror:8 row_mask:0xf bank_mask:0xf\nv_mov_b32 v132, v128\n"
"M 32,168,168\nR 164,190,40960\nv_mov_b32 v133, v130\nv_permlane32_swap_b32 v132, v128\nM 32,172,184\nv_permlane32_swap_b32 v133, v130\n"
"v_add_f32 v128, v128, v132\nv_add_f32 v130, v130, v133\nM 48,168,172\nR 166,190,45056\nv_mov_b32 v132, v128\nv_mov_b32 v133, v130\n"
"M 48,172,188\nG 4\nv_permlane16_swap_b32 v132, v128\nv_permlane16_swap_b32 v133, v130\nv_add_f32 v128, v128, v132\nM 52,176,172\n"
"R 168,191,32768\nv_add_f32 v130, v130, v133\nv_and_b32 v136, s77, v128\nM 52,180,188\nbuffer_load_dwordx4 v221, s[4:7], s63 offen offset:1024 lds\nv_sub_f32 v137, v128, v136\n"
"M 36,176,168\nR 170,191,36864\ns_waitcnt vmcnt(9)\nds_read_b128 a[192:195], v248 offset:0\nv_cvt_pk_bf16_f32 v132, v136, v137\nM 36,180,184\n"
"ds_read_b128 a[196:199], v248 offset:2048\nv_cvt_pk_bf16_f32 v133, v136, 0\nv_and_b32 v136, s77, v130\nM 20,176,164\nR 172,191,40960\nds_read_b128 a[200:203], v248 offset:4096\n"
"v_sub_f32 v137, v130, v136\nM 20,180,180\nds_read_b128 a[204:207], v248 offset:6144\nv_cvt_pk_bf16_f32 v134, v136, v137\nv_cvt_pk_bf16_f32 v135, v136, 0\nM 4,176,160\n"
"R 174,191,45056\nv_and_b32 v230, 0x1ff, v230\nv_add_u32 v230, 155648, v230\nM 4,180,176\nds_read_b128 a[208:211], v249 offset:0\nds_write_b128 v230, v[132:135]\n"
"G 10\nM 8,160,160\nR 176,188,49152\nds_read_b128 a[212:215], v249 offset:2048\nM 8,164,176\nds_read_b128 a[216:219], v249 offset:4096\n"
"s_add_u32 m0, s50, 40960\nM 24,160,164\nR 178,188,53248\ns_add_u32 s63, s52, 2363392\nM 24,164,180\nds_read_b128 a[220:223], v249 offset:6144\n"
"M 40,160,168\nR 180,184,0\nM 40,164,184\nbuffer_load_dwordx4 v220, s[4:7], s63 offen lds\nM 56,160,172\nR 182,184,4096\n"
"M 56,164,188\nG 2\ns_add_u32 m0, s51, 0\ns_add_u32 s60, s70, 896\nbuffer_load_dwordx4 v226, s[24:27], s60 offen lds\nM 60,168,172\n"
"R 160,189,49152\nM 60,172,188\ns_add_u32 m0, s51, 1024\ns_add_u32 s60, s70, 17280\nbuffer_load_dwordx4 v227, s[24:27], s60 offen lds\nM 44,168,168\n"
"R 162,189,53248\nM 44,172,184\ns_add_u32 m0, s51, 2048\ns_add_u32 s60, s70, 33664\nbuffer_load_dwordx4 v226, s[24:27], s60 offen lds\nM 28,168,164\n"
"R 164,185,0\nM 28,172,180\ns_add_u32 m0, s51, 3072\ns_add_u32 s60, s70, 50048\nbuffer_load_dwordx4 v227, s[24:27], s60 offen lds\nM 12,168,160\n"
"R 166,185,4096\nM 12,172,176\ns_add_u32 m0, s51, 4096\ns_add_u32 s60, s71, 896\nbuffer_load_dwordx4 v226, s[24:27], s60 offen lds\ns_waitcnt vmcnt(10)\n"
"s_barrier\nG 4\nN 64,160,176\nR 168,190,49152\nN 64,176,180\ns_add_u32 m0, s51, 5120\n"
"s_add_u32 s60, s71, 17280\nbuffer_load_dwordx4 v227, s[24:27], s60 offen lds\nN 68,164,176\nR 170,190,53248\ns_lshr_b32 s84, s49, 4\nN 68,180,180\n"
"s_add_u32 m0, s51, 6144\ns_add_u32 s60, s71, 33664\nbuffer_load_dwordx4 v226, s[24:27], s60 offen lds\nN 96,168,176\nR 172,186,0\ns_add_u32 s84, s84, 81788928\n"
"N 96,184,180\ns_add_u32 m0, s51, 7168\ns_add_u32 s60, s71, 50048\nbuffer_load_dwordx4 v227, s[24:27], s60 offen lds\nN 100,172,176\nR 174,186,4096\n"
"s_add_u32 m0, s50, 40960\nN 100,188,180\nG 4\nN 108,172,160\nR 176,191,49152\nbuffer_load_dwordx4 v221, s[4:7], s63 offen offset:1024 lds\n"
"N 108,188,164\nbuffer_load_dwordx4 v[128:131], v252, s[20:23], s84 offen offset:0 sc1\ns_add_u32 s63, s52, 2560000\nN 104,168,160\nR 178,191,53248\ns_add_u32 m0, s50, 49152\n"
"N 104,184,164\nbuffer_load_dwordx4 v[132:135], v252, s[20:23], s84 offen offset:1024 sc1\nN 76,164,160\nR 180,187,0\nbuffer_load_dwordx4 v220, s[4:7], s63 offen lds\nN 76,180,164\n"
"buffer_load_dwordx4 v221, s[4:7], s63 offen offset:1024 lds\nN 72,160,160\nR 182,187,4096\nN 72,176,164\nG 4\nN 80,160,168\n"
"R 160,184,8192\nN 80,176,172\nbuffer_load_dwordx4 v[136:139], v252, s[20:23], s84 offen offset:2048 sc1\nN 84,164,168\nR 162,184,12288\nN 84,180,172\n"
"buffer_load_dwordx4 v[140:143], v252, s[20:23], s84 offen offset:3072 sc1\nN 112,168,168\nR 164,184,16384\nN 112,184,172\nN 116,172,168\nR 166,184,20480\n"
"N 116,188,172\nG 4\nN 124,172,176\nR 168,185,8192\nN 124,188,180\nN 120,168,176\n"
"R 170,185,12288\nN 120,184,180\ns_add_u32 m0, s50, 57344\nN 92,164,176\nR 172,185,16384\nN 92,180,180\n"
"s_add_u32 s63, s52, 2756608\nN 88,160,176\nR 174,185,20480\nN 88,176,180\ns_waitcnt vmcnt(16)\ns_barrier\n"
"G 4\nM 0,160,192\nR 176,186,8192\nM 0,164,208\nbuffer_load_dwordx4 v220, s[4:7], s63 offen lds\nM 16,160,196\n"
"R 178,186,12288\nM 16,164,212\nM 32,160,200\nR 180,186,16384\nM 32,164,216\nbuffer_load_dwordx4 v221, s[4:7], s63 offen offset:1024 lds\n"
"M 48,160,204\nR 182,186,20480\ns_lshl2_add_u32 s60, s45, s40\nv_lshlrev_b32 v224, 3, v252\ns_mul_i32 s60, s60, 2048\ns_bfm_b64 exec, 16, 0\n"
"buffer_load_dword v224, v224, s[8:11], s60 offen\ns_mov_b64 exec, -1\nM 48,164,220\ns_add_u32 s63, s52, 2953216\nG 4\nM 52,168,204\n"
"R 160,187,8192\ns_add_u32 m0, s50, 65536\nM 52,172,220\nbuffer_load_dwordx4 v220, s[4:7], s63 offen lds\nM 36,168,200\nR 162,187,12288\n"
"M 36,172,216\nM 20,168,196\nR 164,187,16384\nM 20,172,212\nbuffer_load_dwordx4 v221, s[4:7], s63 offen offset:1024 lds\nM 4,168,192\n"
"R 166,187,20480\nM 4,172,208\nG 4\nM 8,176,192\nR 168,184,24576\nM 8,180,208\n"
"M 24,176,196\nR 170,184,28672\ns_waitcnt vmcnt(12)\nds_read_b128 a[224:227], v248 offset:0\nM 24,180,212\nds_read_b128 a[228:231], v248 offset:2048\n"
"M 40,176,200\nR 172,184,32768\nds_read_b128 a[232:235], v248 offset:4096\nM 40,180,216\nds_read_b128 a[236:239], v248 offset:6144\nM 56,176,204\n"
"R 174,184,36864\nM 56,180,220\nds_read_b128 a[240:243], v249 offset:0\nG 9\nM 60,160,204\nR 176,185,24576\n"
"ds_read_b128 a[244:247], v249 offset:2048\nM 60,164,220\nds_read_b128 a[248:251], v249 offset:4096\ns_add_u32 m0, s50, 73728\nM 44,160,200\nR 178,185,28672\n"
"s_add_u32 s63, s52, 0\nM 44,164,216\nds_read_b128 a[252:255], v249 offset:6144\nM 28,160,196\nR 180,185,32768\nM 28,164,212\n"
"buffer_load_dwordx4 v220, s[4:7], s63 offen lds\nM 12,160,192\nR 182,185,36864\nM 12,164,208\ns_waitcnt vmcnt(8)\ns_barrier\n"
"G 8\nM 0,168,224\nR 160,186,24576\ns_mov_b32 s85, 0x0c0c0400\nv_perm_b32 v144, v129, v128, s85\nG 8\n"
"M 0,172,240\nbuffer_load_dwordx4 v221, s[4:7], s63 offen offset:1024 lds\nv_perm_b32 v148, v131, v130, s85\nM 16,168,228\nR 162,186,28672\ns_add_u32 s63, s52, 196608\n"
"v_lshl_or_b32 v144, v148, 16, v144\nG 7\nM 16,172,244\ns_add_u32 m0, s50, 81920\nv_perm_b32 v145, v133, v132, s85\nv_perm_b32 v149, v135, v134, s85\n"
"v_lshl_or_b32 v145, v149, 16, v145\nM 32,168,232\nR 164,186,32768\ns_waitcnt vmcnt(8)\nv_perm_b32 v146, v137, v136, s85\nv_perm_b32 v150, v139, v138, s85\n"
"G 7\nM 32,172,248\nbuffer_load_dwordx4 v220, s[4:7], s63 offen lds\nv_lshl_or_b32 v146, v150, 16, v146\nM 48,168,236\nR 166,186,36864\n"
"s_lshl_b32 s60, s90, 14\ns_add_u32 s70, s49, s60\ns_xor_b32 s71, s70, 0x10000\ns_waitcnt vmcnt(8)\nv_perm_b32 v147, v141, v140, s85\nG 6\n"
"M 48,172,252\nG 4\nv_perm_b32 v151, v143, v142, s85\nv_lshl_or_b32 v147, v151, 16, v147\nv_min3_u32 v230, v144, v145, v146\nM 52,176,236\n"
"R 168,187,24576\nv_min3_u32 v230, v230, v147, v147\nv_max3_u32 v231, v144, v145, v146\nM 52,180,252\nbuffer_load_dwordx4 v221, s[4:7], s63 offen offset:1024 lds\nLpg3:\n"
"s_cbranch_execz LretQ0\nv_max3_u32 v231, v231, v147, v147\nM 36,176,232\nR 170,187,28672\ns_add_u32 s63, s52, 393216\nv_cmp_ne_u32 s[86:87], s46, v230\n"
"M 36,180,248\ns_add_u32 m0, s50, 90112\nv_cmp_ne_u32 vcc, s46, v231\nM 20,176,228\nR 172,187,32768\ns_or_b64 s[86:87], s[86:87], vcc\n"
"s_cbranch_scc1 Lq140\nLq141:\nM 20,180,244\nbuffer_load_dwordx4 v220, s[4:7], s63 offen lds\nv_and_b32 v128, 0xffffff00, v128\nM 4,176,224\n"
"R 174,187,36864\nv_and_b32 v129, 0xffffff00, v129\nv_and_b32 v130, 0xffffff00, v130\nM 4,180,240\nG 4\nv_and_b32 v131, 0xffffff00, v131\n"
"v_and_b32 v132, 0xffffff00, v132\nv_and_b32 v133, 0xffffff00, v133\nM 8,160,224\nR 176,184,40960\nv_and_b32 v134, 0xffffff00, v134\nv_and_b32 v135, 0xffffff00, v135\n"
"M 8,164,240\nbuffer_load_dwordx4 v221, s[4:7], s63 offen offset:1024 lds\nv_and_b32 v136, 0xffffff00, v136\nM 24,160,228\nR 178,184,45056\nv_and_b32 v137, 0xffffff00, v137\n"
"v_and_b32 v138, 0xffffff00, v138\nM 24,164,244\ns_add_u32 m0, s50, 98304\nv_and_b32 v139, 0xffffff00, v139\nv_and_b32 v140, 0xffffff00, v140\nv_and_b32 v141, 0xffffff00, v141\n"
"M 40,160,232\nR 180,184,49152\nv_and_b32 v142, 0xffffff00, v142\nv_and_b32 v143, 0xffffff00, v143\nM 40,164,248\ns_add_u32 s63, s52, 589824\n"
"v_add_f32 v128, v128, v136\nv_add_f32 v129, v129, v137\nv_add_f32 v130, v130, v138\nM 56,160,236\nR 182,184,53248\nv_add_f32 v131, v131, v139\n"
"v_add_f32 v132, v132, v140\nM 56,164,252\nG 4\nv_add_f32 v133, v133, v141\nv_add_f32 v134, v134, v142\nv_add_f32 v135, v135, v143\n"
"M 60,168,236\nR 160,185,40960\nv_add_f32 v128, v128, v132\nv_add_f32 v129, v129, v133\nM 60,172,252\nbuffer_load_dwordx4 v220, s[4:7], s63 offen lds\n"
"v_add_f32 v130, v130, v134\nM 44,168,232\nR 162,185,45056\nv_add_f32 v131, v131, v135\nv_permlane32_swap_b32 v128, v130\nM 44,172,248\n"
"v_permlane32_swap_b32 v129, v131\nv_add_f32 v128, v128, v130\nv_add_f32 v129, v129, v131\nM 28,168,228\nR 164,185,49152\nv_permlane16_swap_b32 v128, v129\n"
"v_add_f32 v230, v128, v129\nM 28,172,244\nbuffer_load_dwordx4 v221, s[4:7], s63 offen offset:1024 lds\nv_mov_b32 v232, v230\nM 12,168,224\nR 166,185,53248\n"
"v_permlane16_swap_b32 v230, v232\nv_mov_b32 v231, v230\nM 12,172,240\ns_waitcnt vmcnt(8)\ns_barrier\nG 4\n"
"v_mov_b32 v233, v232\nN 64,192,176\nR 168,186,40960\nv_permlane32_swap_b32 v230, v231\nv_permlane32_swap_b32 v232, v233\nN 64,208,180\n"
"s_mov_b32 m0, s51\nv_mul_f32 v230, 0xba800000, v230\nv_mul_f32 v214, v230, v230\nv_mul_f32 v231, 0x3a800000, v231\nN 68,196,176\nR 170,186,45056\n"
"s_lshl2_add_u32 s60, s45, s40\nv_lshlrev_b32 v224, 3, v252\ns_mul_i32 s60, s60, 7168\ns_bfm_b64 exec, 14, 0\nbuffer_load_dword v224, v224, s[12:15], s60 offen\ns_mov_b64 exec, -1\n"
"ds_write_addtid_b32 v32 offset:0\nv_sub_f32 v231, v231, v214\nN 68,212,180\nds_write_addtid_b32 v33 offset:256\nds_write_addtid_b32 v34 offset:512\nv_max_f32 v231, 0, v231\n"
"v_add_f32 v231, 0x3727c5ac, v231\nN 96,200,176\nR 172,186,49152\ns_mov_b32 s60, 156160\nv_bfe_u32 v246, v252, 4, 4\nv_lshl_add_u32 v246, v246, 3, s60\n"
"ds_write_addtid_b32 v35 offset:768\nv_rsq_f32 v234, v231\nN 96,216,180\nds_read_b64 v[238:239], v246 offset:0\nds_write_addtid_b32 v36 offset:1024\nv_and_b32 v208, s77, v230\n"
"v_mul_f32 v216, v231, v234\nN 100,204,176\nR 174,186,53248\nds_read_b64 v[240:241], v246 offset:128\nds_write_addtid_b32 v37 offset:1280\nv_sub_f32 v230, v230, v208\n"
"N 100,220,180\nds_read_b64 v[242:243], v246 offset:256\nds_write_addtid_b32 v38 offset:1536\nG 14\nv_cvt_pk_bf16_f32 v209, v230, v216\nN 108,204,160\n"
"R 176,187,40960\nds_read_b64 v[244:245], v246 offset:384\nds_write_addtid_b32 v39 offset:1792\nv_cvt_pk_bf16_f32 v208, v208, v208\nN 108,220,164\nds_read_b64 v[246:247], v246 offset:0\n"
"ds_write_addtid_b32 v40 offset:2048\nv_mov_b32 v214, v234\nv_mul_f32 v215, 0x3e8293ee, v234\nN 104,200,160\nR 178,187,45056\nds_write_addtid_b32 v41 offset:2304\n"
"v_mul_f32 v232, 0xba800000, v232\nv_mul_f32 v217, v232, v232\nN 104,216,164\nds_write_addtid_b32 v42 offset:2560\nds_write_addtid_b32 v43 offset:2816\nv_mul_f32 v233, 0x3a800000, v233\n"
"v_sub_f32 v233, v233, v217\nN 76,196,160\nR 180,187,49152\nds_write_addtid_b32 v44 offset:3072\nv_max_f32 v233, 0, v233\nv_add_f32 v233, 0x3727c5ac, v233\n"
"N 76,212,164\nds_write_addtid_b32 v45 offset:3328\nv_and_b32 v212, s77, v232\nv_rsq_f32 v235, v233\ns_nop 0\nv_mul_f32 v219, v233, v235\n"
"v_sub_f32 v232, v232, v212\nv_cvt_pk_bf16_f32 v213, v232, v219\nv_cvt_pk_bf16_f32 v212, v212, v212\nv_mov_b32 v217, v235\nv_mul_f32 v218, 0x3e8293ee, v235\ns_mov_b64 s[60:61], 0xffff\n"
"v_cndmask_b32 v208, 0, v208, s[60:61]\ns_mov_b64 s[60:61], 0xffff\nv_cndmask_b32 v209, 0, v209, s[60:61]\ns_mov_b64 s[60:61], 0xffff\nv_cndmask_b32 v212, 0, v212, s[60:61]\ns_mov_b64 s[60:61], 0xffff\n"
"v_cndmask_b32 v213, 0, v213, s[60:61]\nv_bfe_u32 v230, v252, 2, 6\ns_lshl_b32 s60, s40, 7\ns_add_u32 s60, s60, 161280\nv_add_u32 v230, s60, v230\nds_write_b32 v230, v214 offset:0\n"
"ds_write_b32 v230, v217 offset:64\nds_write_addtid_b32 v46 offset:3584\nN 72,192,160\nR 182,187,53248\nds_write_addtid_b32 v47 offset:3840\nN 72,208,164\n"
"ds_write_addtid_b32 v48 offset:4096\nds_write_addtid_b32 v49 offset:4352\nG 15\nN 80,192,168\nR 160,184,57344\nds_write_addtid_b32 v50 offset:4608\n"
"N 80,208,172\nds_write_addtid_b32 v51 offset:4864\nds_write_addtid_b32 v52 offset:5120\nN 84,196,168\nR 162,184,61440\nv_mfma_f32_16x16x32_bf16 v[0:3], v[238:241], v[208:211], v[0:3]\n"
"ds_write_addtid_b32 v53 offset:5376\nN 84,212,172\nv_mfma_f32_16x16x32_bf16 v[4:7], v[240:243], v[208:211], v[4:7]\nds_write_addtid_b32 v54 offset:5632\nds_write_addtid_b32 v55 offset:5888\nN 112,200,168\n"
"R 164,188,0\nv_mfma_f32_16x16x32_bf16 v[8:11], v[242:245], v[208:211], v[8:11]\nds_write_addtid_b32 v56 offset:6144\nN 112,216,172\nv_mfma_f32_16x16x32_bf16 v[12:15], v[244:247], v[208:211], v[12:15]\nds_write_addtid_b32 v57 offset:6400\n"
"ds_write_addtid_b32 v58 offset:6656\nN 116,204,168\nR 166,188,4096\nv_mfma_f32_16x16x32_bf16 v[16:19], v[244:247], v[210:213], v[16:19]\nds_write_addtid_b32 v59 offset:6912\nN 116,220,172\n"
"v_mfma_f32_16x16x32_bf16 v[20:23], v[238:241], v[210:213], v[20:23]\nds_write_addtid_b32 v60 offset:7168\nds_write_addtid_b32 v61 offset:7424\nds_write_addtid_b32 v62 offset:7680\nds_write_addtid_b32 v63 offset:7936\nG 15\n"
"N 124,204,176\nR 168,185,57344\nv_mfma_f32_16x16x32_bf16 v[24:27], v[240:243], v[210:213], v[24:27]\ns_add_u32 m0, s50, 106496\ns_add_u32 s63, s52, 786432\nN 124,220,180\n"
"v_mfma_f32_16x16x32_bf16 v[28:31], v[242:245], v[210:213], v[28:31]\nbuffer_load_dwordx4 v220, s[4:7], s63 offen lds\nN 120,200,176\nR 170,185,61440\ns_waitcnt vmcnt(6) lgkmcnt(2)\ns_barrier\n"
"N 120,216,180\nds_read2st64_b32 v[32:33], v225 offset0:0 offset1:1\nds_read2st64_b32 v[34:35], v225 offset0:2 offset1:3\nN 92,196,176\nR 172,189,0\nds_read2st64_b32 v[36:37], v225 offset0:4 offset1:5\n"
"N 92,212,180\nbuffer_load_dwordx4 v221, s[4:7], s63 offen offset:1024 lds\nds_read2st64_b32 v[38:39], v225 offset0:6 offset1:7\nN 88,192,176\nR 174,189,4096\nds_read2st64_b32 v[40:41], v225 offset0:8 offset1:9\n"
"N 88,208,180\nds_read2st64_b32 v[42:43], v225 offset0:10 offset1:11\nds_read2st64_b32 v[44:45], v225 offset0:12 offset1:13\nN 64,224,160\nR 176,186,57344\ns_mov_b32 s60, 156672\n"
"v_bfe_u32 v246, v252, 4, 4\nv_lshl_add_u32 v246, v246, 3, s60\nds_read2st64_b32 v[46:47], v225 offset0:14 offset1:15\nN 64,240,164\nds_read_b64 v[238:239], v246 offset:0\ns_add_u32 m0, s50, 114688\n"
"ds_read2st64_b32 v[48:49], v225 offset0:16 offset1:17\nN 68,228,160\nR 178,186,61440\nds_read_b64 v[240:241], v246 offset:128\nds_read2st64_b32 v[50:51], v225 offset0:18 offset1:19\nN 68,244,164\n"
"ds_read_b64 v[242:243], v246 offset:256\ns_add_u32 s63, s52, 983040\nds_read2st64_b32 v[52:53], v225 offset0:20 offset1:21\nN 96,232,160\nR 180,190,0\nds_read_b64 v[244:245], v246 offset:384\n"
"ds_read2st64_b32 v[54:55], v225 offset0:22 offset1:23\nN 96,248,164\nds_read_b64 v[246:247], v246 offset:0\nds_read2st64_b32 v[56:57], v225 offset0:24 offset1:25\nG 15\nv_add_f32 v0, v0, v32\n"
"N 100,236,160\nR 182,190,4096\nbuffer_load_dwordx4 v220, s[4:7], s63 offen lds\nds_read2st64_b32 v[58:59], v225 offset0:26 offset1:27\nN 100,252,164\nds_read2st64_b32 v[60:61], v225 offset0:28 offset1:29\n"
"ds_read2st64_b32 v[62:63], v225 offset0:30 offset1:31\nN 108,236,168\nR 160,187,57344\nv_add_f32 v1, v1, v33\nv_add_f32 v2, v2, v34\nN 108,252,172\n"
"buffer_load_dwordx4 v221, s[4:7], s63 offen offset:1024 lds\nv_add_f32 v3, v3, v35\nN 104,232,168\nR 162,187,61440\ns_add_u32 m0, s50, 0\ns_add_u32 s63, s52, 1179648\n"
"v_add_f32 v4, v4, v36\nN 104,248,172\nbuffer_load_dwordx4 v220, s[4:7], s63 offen lds\nv_add_f32 v5, v5, v37\nN 76,228,168\nR 164,191,0\n"
"v_add_f32 v6, v6, v38\nv_add_f32 v7, v7, v39\nN 76,244,172\nG 15\nv_mfma_f32_16x16x32_bf16 v[64:67], v[208:211], v[238:241], v[64:67]\nv_add_f32 v8, v8, v40\n"
"v_add_f32 v9, v9, v41\nv_add_f32 v10, v10, v42\nN 72,224,168\nR 166,191,4096\nv_add_f32 v11, v11, v43\nv_add_f32 v12, v12, v44\n"
"N 72,240,172\nG 14\nv_mfma_f32_16x16x32_bf16 v[72:75], v[208:211], v[240:243], v[72:75]\nG 7\nv_add_f32 v13, v13, v45\nv_add_f32 v14, v14, v46\n"
"v_add_f32 v15, v15, v47\nN 80,224,176\nR 168,188,8192\nbuffer_load_dwordx4 v221, s[4:7], s63 offen offset:1024 lds\nv_add_f32 v16, v16, v48\nN 80,240,180\n"
"v_mfma_f32_16x16x32_bf16 v[80:83], v[208:211], v[242:245], v[80:83]\ns_add_u32 m0, s50, 8192\ns_add_u32 s63, s52, 1376256\nbuffer_load_dwordx4 v220, s[4:7], s63 offen lds\nv_add_f32 v17, v17, v49\nN 84,228,176\n"
"R 170,188,12288\nv_add_f32 v18, v18, v50\nv_add_f32 v19, v19, v51\nN 84,244,180\nv_mfma_f32_16x16x32_bf16 v[88:91], v[208:211], v[244:247], v[88:91]\nv_add_f32 v20, v20, v52\n"
"v_add_f32 v21, v21, v53\nv_add_f32 v22, v22, v54\nN 112,232,176\nR 172,188,16384\nv_add_f32 v23, v23, v55\nv_add_f32 v24, v24, v56\n"
"N 112,248,180\nv_mfma_f32_16x16x32_bf16 v[68:71], v[210:213], v[244:247], v[68:71]\nbuffer_load_dwordx4 v221, s[4:7], s63 offen offset:1024 lds\nv_add_f32 v25, v25, v57\nN 116,236,176\nR 174,188,20480\n"
"s_add_u32 m0, s50, 32768\nG 10\nv_add_f32 v26, v26, v58\nN 116,252,180\nv_mfma_f32_16x16x32_bf16 v[76:79], v[210:213], v[238:241], v[76:79]\nG 4\n"
"v_add_f32 v27, v27, v59\nv_add_f32 v28, v28, v60\nv_add_f32 v29, v29, v61\nN 124,236,160\nR 176,189,8192\ns_add_u32 s63, s52, 1572864\n"
"v_add_f32 v30, v30, v62\nN 124,252,164\nv_mfma_f32_16x16x32_bf16 v[84:87], v[210:213], v[240:243], v[84:87]\nbuffer_load_dwordx4 v220, s[4:7], s63 offen lds\nv_add_f32 v31, v31, v63\nN 120,232,160\n"
"R 178,189,12288\ns_lshl4_add_u32 s64, s41, s42\nv_lshrrev_b32 v227, 1, v252\nN 120,248,164\nv_mfma_f32_16x16x32_bf16 v[92:95], v[210:213], v[242:245], v[92:95]\nbuffer_load_dwordx4 v221, s[4:7], s63 offen offset:1024 lds\n"
"v_mul_f32 v16, v16, v217\nN 92,228,160\nR 180,189,16384\ns_add_u32 m0, s50, 40960\ns_add_u32 s63, s52, 1769472\nv_mul_f32 v17, v17, v217\n"
"N 92,244,164\nbuffer_load_dwordx4 v220, s[4:7], s63 offen lds\nv_mul_f32 v18, v18, v217\nN 88,224,160\nR 182,189,20480\ns_lshl_b32 s64, s64, 12\n"
"v_mul_f32 v19, v19, v217\nN 88,240,164\ns_waitcnt vmcnt(12)\ns_barrier\nG 4\nv_mul_f32 v20, v20, v217\n"
"v_mfma_f32_16x16x32_bf16 v[128:131], v[168:171], a[0:3], 0\nR 160,190,8192\ns_add_u32 s64, s64, 77594624\nv_mul_f32 v21, v21, v217\nM 128,172,16\ns_mov_b32 m0, s51\n"
"s_cmp_eq_u32 s40, 3\ns_cselect_b64 s[66:67], -1, 0\nv_mul_f32 v22, v22, v217\nv_mfma_f32_16x16x32_bf16 v[144:147], v[168:171], a[4:7], 0\nR 162,190,12288\ns_lshl2_add_u32 s60, s45, s40\n"
"s_waitcnt vmcnt(11)\nv_lshlrev_b32 v224, 3, v252\ns_mul_i32 s60, s60, 7168\ns_bfm_b64 exec, 14, 14\nbuffer_load_dword v224, v224, s[12:15], s60 offen\ns_mov_b64 exec, -1\n"
"v_mul_f32 v23, v23, v217\nM 144,172,20\nv_mul_f32 v24, v24, v217\nv_mul_f32 v25, v25, v217\nv_mul_f32 v26, v26, v217\nv_mfma_f32_16x16x32_bf16 v[32:35], v[168:171], a[8:11], 0\n"
"R 164,190,16384\nv_mul_f32 v27, v27, v217\nv_mul_f32 v28, v28, v217\nM 32,172,24\nv_mul_f32 v29, v29, v217\nv_mul_f32 v30, v30, v217\n"
"v_mul_f32 v31, v31, v217\nv_mfma_f32_16x16x32_bf16 v[48:51], v[168:171], a[12:15], 0\nR 166,190,20480\nds_write_addtid_b32 v96 offset:0\nv_cvt_pk_bf16_f32 v16, v16, v17\nv_cvt_pk_bf16_f32 v17, v18, v19\n"
"M 48,172,28\nds_write_addtid_b32 v97 offset:256\nG 6\nv_cvt_pk_bf16_f32 v18, v20, v21\nv_cvt_pk_bf16_f32 v19, v22, v23\nv_cvt_pk_bf16_f32 v20, v24, v25\n"
"v_mfma_f32_16x16x32_bf16 v[52:55], v[176:179], a[12:15], 0\nR 168,191,8192\nds_write_addtid_b32 v98 offset:512\ns_lshr_b32 s100, s41, 1\ns_mul_i32 s100, s100, 458752\ns_add_u32 s100, s100, s55\n"
"v_cvt_pk_bf16_f32 v21, v26, v27\nv_cvt_pk_bf16_f32 v22, v28, v29\nM 52,180,28\nds_write_addtid_b32 v99 offset:768\ns_mul_i32 s101, s41, 229376\ns_add_u32 s101, s101, s54\n"
"v_cvt_pk_bf16_f32 v23, v30, v31\nv_mul_f32 v0, v0, v214\nv_mul_f32 v1, v1, v214\nv_mfma_f32_16x16x32_bf16 v[36:39], v[176:179], a[8:11], 0\nR 170,191,12288\nds_write_addtid_b32 v100 offset:1024\n"
"v_mul_f32 v2, v2, v214\nv_mul_f32 v3, v3, v214\nM 36,180,24\nds_write_addtid_b32 v101 offset:1280\nv_mul_f32 v4, v4, v214\nv_mul_f32 v5, v5, v214\n"
"v_mul_f32 v6, v6, v214\nv_mfma_f32_16x16x32_bf16 v[148:151], v[176:179], a[4:7], 0\nR 172,191,16384\nds_write_addtid_b32 v102 offset:1536\nv_mul_f32 v7, v7, v214\nv_mul_f32 v8, v8, v214\n"
"M 148,180,20\nds_write_addtid_b32 v103 offset:1792\nv_mul_f32 v9, v9, v214\nv_mul_f32 v10, v10, v214\nv_mul_f32 v11, v11, v214\nv_mfma_f32_16x16x32_bf16 v[132:135], v[176:179], a[0:3], 0\n"
"R 174,191,20480\nds_write_addtid_b32 v104 offset:2048\nv_mul_f32 v12, v12, v214\nv_mul_f32 v13, v13, v214\nM 132,180,16\nds_write_addtid_b32 v105 offset:2304\n"
"G 14\nv_mul_f32 v14, v14, v214\nv_mul_f32 v15, v15, v214\nv_cvt_pk_bf16_f32 v0, v0, v1\nv_mfma_f32_16x16x32_bf16 v[136:139], v[160:163], a[0:3], 0\nR 176,188,24576\n"
"ds_write_addtid_b32 v106 offset:2560\nv_cvt_pk_bf16_f32 v1, v2, v3\nv_cvt_pk_bf16_f32 v2, v4, v5\nM 136,164,16\nds_write_addtid_b32 v107 offset:2816\nv_cvt_pk_bf16_f32 v3, v6, v7\n"
"v_cvt_pk_bf16_f32 v4, v8, v9\nv_cvt_pk_bf16_f32 v5, v10, v11\nv_mfma_f32_16x16x32_bf16 v[152:155], v[160:163], a[4:7], 0\nR 178,188,28672\nds_write_addtid_b32 v108 offset:3072\nv_cvt_pk_bf16_f32 v6, v12, v13\n"
"v_cvt_pk_bf16_f32 v7, v14, v15\nM 152,164,20\nds_write_addtid_b32 v109 offset:3328\nds_write_addtid_b32 v110 offset:3584\ns_mov_b64 exec, s[66:67]\nbuffer_store_dwordx4 v[16:19], v252, s[20:23], s64 offen offset:0 sc1\n"
"s_mov_b64 exec, -1\nv_mfma_f32_16x16x32_bf16 v[40:43], v[160:163], a[8:11], 0\nR 180,188,32768\nds_write_addtid_b32 v111 offset:3840\nM 40,164,24\nds_write_addtid_b32 v112 offset:4096\n"
"ds_write_addtid_b32 v113 offset:4352\nv_mfma_f32_16x16x32_bf16 v[56:59], v[160:163], a[12:15], 0\nR 182,188,36864\nds_write_addtid_b32 v114 offset:4608\nM 56,164,28\nds_write_addtid_b32 v115 offset:4864\n"
"ds_write_addtid_b32 v116 offset:5120\ns_mov_b64 exec, s[66:67]\nbuffer_store_dwordx4 v[20:23], v252, s[20:23], s64 offen offset:1024 sc1\ns_mov_b64 exec, -1\nG 15\nv_mfma_f32_16x16x32_bf16 v[60:63], v[168:171], a[12:15], 0\n"
"R 160,189,24576\nds_write_addtid_b32 v117 offset:5376\nM 60,172,28\nds_write_addtid_b32 v118 offset:5632\nds_write_addtid_b32 v119 offset:5888\nv_mfma_f32_16x16x32_bf16 v[44:47], v[168:171], a[8:11], 0\n"
"R 162,189,28672\nds_write_addtid_b32 v120 offset:6144\nM 44,172,24\nds_write_addtid_b32 v121 offset:6400\nds_write_addtid_b32 v122 offset:6656\nds_write_addtid_b32 v123 offset:6912\n"
"ds_write_addtid_b32 v124 offset:7168\nds_write_addtid_b32 v125 offset:7424\nds_write_addtid_b32 v126 offset:7680\nds_write_addtid_b32 v127 offset:7936\nv_mfma_f32_16x16x32_bf16 v[156:159], v[168:171], a[4:7], 0\nR 164,189,32768\n"
"s_add_u32 m0, s50, 40960\nM 156,172,20\nbuffer_load_dwordx4 v221, s[4:7], s63 offen offset:1024 lds\nv_mfma_f32_16x16x32_bf16 v[140:143], v[168:171], a[0:3], 0\nR 166,189,36864\ns_waitcnt vmcnt(11) lgkmcnt(2)\n"
"s_barrier\nM 140,172,16\nds_read2st64_b32 v[96:97], v225 offset0:0 offset1:1\nds_read2st64_b32 v[98:99], v225 offset0:2 offset1:3\nv_lshrrev_b32 v226, 8, v252\nM 128,176,32\n"
"R 168,190,24576\ns_add_u32 s63, s52, 1966080\nds_read2st64_b32 v[100:101], v225 offset0:4 offset1:5\ns_lshl_b32 s60, s40, 7\ns_add_u32 s60, s60, 161280\nv_lshl_add_u32 v226, v226, 4, s60\n"
"M 128,180,48\ns_add_u32 m0, s50, 49152\nds_read2st64_b32 v[102:103], v225 offset0:6 offset1:7\nds_read2st64_b32 v[104:105], v225 offset0:8 offset1:9\nds_read_b128 v[230:233], v226 offset:0\nM 144,176,36\n"
"R 170,190,28672\nds_read2st64_b32 v[106:107], v225 offset0:10 offset1:11\nds_read_b128 v[234:237], v226 offset:64\nM 144,180,52\nbuffer_load_dwordx4 v220, s[4:7], s63 offen lds\nds_read2st64_b32 v[108:109], v225 offset0:12 offset1:13\n"
"M 32,176,40\nR 172,190,32768\nds_read2st64_b32 v[110:111], v225 offset0:14 offset1:15\nM 32,180,56\nds_read2st64_b32 v[112:113], v225 offset0:16 offset1:17\nds_read2st64_b32 v[114:115], v225 offset0:18 offset1:19\n"
"G 14\nv_add_f32 v64, v64, v96\nM 48,176,44\nR 174,190,36864\nds_read2st64_b32 v[116:117], v225 offset0:20 offset1:21\nv_add_f32 v65, v65, v97\n"
"M 48,180,60\nds_read2st64_b32 v[118:119], v225 offset0:22 offset1:23\nds_read2st64_b32 v[120:121], v225 offset0:24 offset1:25\nM 52,160,44\nR 176,191,24576\nds_read2st64_b32 v[122:123], v225 offset0:26 offset1:27\n"
"G 15\nv_add_f32 v66, v66, v98\nM 52,164,60\nbuffer_load_dwordx4 v221, s[4:7], s63 offen offset:1024 lds\nds_read2st64_b32 v[124:125], v225 offset0:28 offset1:29\nM 36,160,40\n"
"R 178,191,28672\nds_read2st64_b32 v[126:127], v225 offset0:30 offset1:31\nLpg4:\ns_cbranch_execz LretQ0\nv_add_f32 v67, v67, v99\nM 36,164,56\n"
"s_add_u32 m0, s50, 57344\nv_add_f32 v68, v68, v100\nv_add_f32 v69, v69, v101\nv_add_f32 v70, v70, v102\nM 148,160,36\nR 180,191,32768\n"
"v_add_f32 v71, v71, v103\nv_add_f32 v72, v72, v104\nM 148,164,52\ns_add_u32 s63, s52, 2162688\nv_add_f32 v73, v73, v105\nG 15\n"
"v_add_f32 v74, v74, v106\nv_add_f32 v75, v75, v107\nM 132,160,32\nR 182,191,36864\nG 15\nv_add_f32 v76, v76, v108\n"
"v_add_f32 v77, v77, v109\nM 132,164,48\nG 10\nv_add_f32 v78, v78, v110\nv_add_f32 v79, v79, v111\nv_add_f32 v80, v80, v112\n"
"M 136,168,32\nR 160,188,40960\nv_add_f32 v81, v81, v113\nv_add_f32 v82, v82, v114\nM 136,172,48\nbuffer_load_dwordx4 v220, s[4:7], s63 offen lds\n"
"v_add_f32 v83, v83, v115\nM 152,168,36\nR 162,188,45056\nG 11\nv_add_f32 v84, v84, v116\nM 152,172,52\n"
"v_add_f32 v85, v85, v117\nG 10\nv_add_f32 v86, v86, v118\nM 40,168,40\nR 164,188,49152\nv_add_f32 v87, v87, v119\n"
"M 40,172,56\nbuffer_load_dwordx4 v221, s[4:7], s63 offen offset:1024 lds\nM 56,168,44\nR 166,188,53248\ns_add_u32 m0, s50, 65536\nM 56,172,60\n"
"G 4\nv_add_f32 v88, v88, v120\nv_add_f32 v89, v89, v121\nM 60,176,44\nR 168,189,40960\ns_add_u32 s63, s52, 2359296\n"
"M 60,180,60\nbuffer_load_dwordx4 v220, s[4:7], s63 offen lds\nM 44,176,40\nR 170,189,45056\nv_add_f32 v90, v90, v122\nM 44,180,56\n"
"v_add_f32 v91, v91, v123\nv_add_f32 v92, v92, v124\nM 156,176,36\nR 172,189,49152\nv_add_f32 v93, v93, v125\nM 156,180,52\n"
"buffer_load_dwordx4 v221, s[4:7], s63 offen offset:1024 lds\nM 140,176,32\nR 174,189,53248\ns_add_u32 m0, s50, 73728\nM 140,180,48\nG 4\n"
"v_add_f32 v94, v94, v126\nv_add_f32 v95, v95, v127\nM 128,160,64\nR 176,190,40960\ns_add_u32 s63, s52, 2555904\nv_mul_f32 v64, v64, v230\n"
"M 128,164,80\nbuffer_load_dwordx4 v220, s[4:7], s63 offen lds\nv_mul_f32 v65, v65, v231\nM 144,160,68\nR 178,190,45056\nv_mul_f32 v66, v66, v232\n"
"v_mul_f32 v67, v67, v233\nM 144,164,84\nv_mul_f32 v68, v68, v234\nv_mul_f32 v69, v69, v235\nv_mul_f32 v70, v70, v236\nM 32,160,72\n"
"R 180,190,49152\nv_mul_f32 v71, v71, v237\nv_cvt_pk_bf16_f32 v67, v66, v67\nM 32,164,88\nbuffer_load_dwordx4 v221, s[4:7], s63 offen offset:1024 lds\nv_cvt_pk_bf16_f32 v66, v64, v65\n"
"M 48,160,76\nR 182,190,53248\ns_add_u32 s63, s52, 2752512\nv_cvt_pk_bf16_f32 v68, v68, v69\nM 48,164,92\nG 4\n"
"v_cvt_pk_bf16_f32 v69, v70, v71\nv_mul_f32 v72, v72, v230\nv_mul_f32 v73, v73, v231\nM 52,168,76\nR 160,191,40960\nv_mul_f32 v74, v74, v232\n"
"v_mul_f32 v75, v75, v233\nM 52,172,92\ns_add_u32 m0, s50, 81920\nv_mul_f32 v76, v76, v234\nv_mul_f32 v77, v77, v235\nv_mul_f32 v78, v78, v236\n"
"M 36,168,72\nR 162,191,45056\nv_mul_f32 v79, v79, v237\nv_cvt_pk_bf16_f32 v72, v72, v73\nM 36,172,88\nbuffer_load_dwordx4 v220, s[4:7], s63 offen lds\n"
"v_cvt_pk_bf16_f32 v73, v74, v75\nM 148,168,68\nR 164,191,49152\nv_cvt_pk_bf16_f32 v74, v76, v77\nv_cvt_pk_bf16_f32 v75, v78, v79\nM 148,172,84\n"
"s_mov_b64 exec, s[66:67]\nbuffer_store_dwordx2 v[68:69], v227, s[20:23], s64 offen offset:2048 sc1\ns_mov_b64 exec, -1\nv_mul_f32 v80, v80, v230\nv_mul_f32 v81, v81, v231\nM 132,168,64\n"
"R 166,191,53248\ns_waitcnt vmcnt(10)\ns_barrier\nv_mul_f32 v82, v82, v232\nM 132,172,80\nG 4\n"
"v_mul_f32 v83, v83, v233\nv_mul_f32 v84, v84, v234\nv_mul_f32 v85, v85, v235\nM 136,176,64\nR 168,184,0\ns_lshl2_add_u32 s60, s45, s40\n"
"v_lshlrev_b32 v224, 3, v252\ns_mul_i32 s60, s60, 7168\ns_bfm_b64 exec, 14, 28\nbuffer_load_dword v224, v224, s[12:15], s60 offen\ns_mov_b64 exec, -1\nv_mul_f32 v86, v86, v236\n"
"M 136,180,80\nv_mul_f32 v87, v87, v237\nv_cvt_pk_bf16_f32 v78, v80, v81\nv_cvt_pk_bf16_f32 v79, v82, v83\nM 152,176,68\nR 170,184,4096\n"
"v_cvt_pk_bf16_f32 v80, v84, v85\nv_cvt_pk_bf16_f32 v81, v86, v87\nM 152,180,84\nbuffer_load_dwordx4 v221, s[4:7], s63 offen offset:1024 lds\nv_mul_f32 v88, v88, v230\nM 40,176,72\n"
"R 172,184,8192\ns_mov_b32 s60, 155648\nv_bfe_u32 v246, v252, 4, 4\nv_lshl_add_u32 v246, v246, 3, s60\nv_mul_f32 v89, v89, v231\nM 40,180,88\n"
"s_add_u32 m0, s50, 90112\nv_mul_f32 v90, v90, v232\nv_mul_f32 v91, v91, v233\nv_mul_f32 v92, v92, v234\nM 56,176,76\nR 174,184,12288\n"
"ds_read_b64 v[238:239], v246 offset:0\nv_mul_f32 v93, v93, v235\nM 56,180,92\nG 5\nv_mul_f32 v94, v94, v236\nv_mul_f32 v95, v95, v237\n"
"v_cvt_pk_bf16_f32 v84, v88, v89\nM 60,160,76\nR 176,185,0\nds_read_b64 v[240:241], v246 offset:128\nv_cvt_pk_bf16_f32 v85, v90, v91\nM 60,164,92\n"
"s_add_u32 s63, s52, 2949120\ns_mov_b64 exec, s[66:67]\nbuffer_store_dwordx2 v[74:75], v227, s[20:23], s64 offen offset:2560 sc1\ns_mov_b64 exec, -1\nv_cvt_pk_bf16_f32 v86, v92, v93\nv_cvt_pk_bf16_f32 v87, v94, v95\n"
"M 44,160,72\nR 178,185,4096\nds_read_b64 v[242:243], v246 offset:256\ns_lshl_b32 s60, s40, 12\ns_add_u32 s60, s60, 53248\nv_add_u32 v230, s60, v252\n"
"v_add_u32 v231, s60, v227\nM 44,164,88\nbuffer_load_dwordx4 v220, s[4:7], s63 offen lds\nM 156,160,68\nR 180,185,8192\nds_read_b64 v[244:245], v246 offset:384\n"
"M 156,164,84\nv_add_u32 v108, 161792, v252\nds_read_b128 v[112:115], v108\nM 140,160,64\nR 182,185,12288\nds_read_b64 v[246:247], v246 offset:0\n"
"M 140,164,80\nds_read_b128 v[104:107], v108 offset:1024\ns_mov_b64 exec, s[66:67]\nbuffer_store_dwordx2 v[80:81], v227, s[20:23], s64 offen offset:3072 sc1\ns_mov_b64 exec, -1\nG 11\n"
"M 128,168,96\nR 160,186,0\nM 128,172,112\nM 144,168,100\nR 162,186,4096\nM 144,172,116\n"
"buffer_load_dwordx4 v221, s[4:7], s63 offen offset:1024 lds\nM 32,168,104\nR 164,186,8192\nG 11\nv_mfma_f32_16x16x32_bf16 v[128:131], v[238:241], v[208:211], v[128:131]\nM 32,172,120\n"
"s_mov_b64 exec, s[66:67]\nbuffer_store_dwordx2 v[86:87], v227, s[20:23], s64 offen offset:3584 sc1\ns_mov_b64 exec, -1\nG 3\nv_lshlrev_b32 v100, 16, v104\nv_and_b32 v101, s77, v104\n"
"M 48,168,108\nR 166,186,12288\nv_mfma_f32_16x16x32_bf16 v[132:135], v[240:243], v[208:211], v[132:135]\ns_not_b64 exec, s[66:67]\nds_write_b128 v230, v[16:19] offset:0\nds_write_b128 v230, v[20:23] offset:1024\n"
"s_mov_b64 exec, -1\nv_lshlrev_b32 v102, 16, v105\nM 48,172,124\nv_and_b32 v103, s77, v105\nv_lshlrev_b32 v96, 16, v106\nM 52,176,108\n"
"R 168,187,0\nv_mfma_f32_16x16x32_bf16 v[144:147], v[244:247], v[210:213], v[144:147]\nv_and_b32 v97, s77, v106\nM 52,180,124\ns_movk_i32 s63, 7168\nv_mad_u32_u24 v220, v228, s63, v229\n"
"v_add_u32 v221, 3668992, v220\ns_add_u32 m0, s50, 98304\ns_add_u32 s63, s101, 0\nbuffer_load_dwordx4 v220, s[12:15], s63 offen lds\nM 36,176,104\nR 170,187,4096\n"
"v_mfma_f32_16x16x32_bf16 v[148:151], v[238:241], v[210:213], v[148:151]\nv_lshlrev_b32 v98, 16, v107\nM 36,180,120\nv_and_b32 v99, s77, v107\nv_add_f32 v104, s78, v100\nM 148,176,100\n"
"R 172,187,8192\nv_add_f32 v105, s78, v101\nM 148,180,116\nv_add_f32 v106, s78, v102\nv_add_f32 v107, s78, v103\nM 132,176,96\n"
"R 174,187,12288\nM 132,180,112\nG 6\nM 136,160,96\nR 176,184,32768\nM 136,164,112\n"
"buffer_load_dwordx4 v221, s[12:15], s63 offen offset:1024 lds\nM 152,160,100\nR 178,184,36864\ns_not_b64 exec, s[66:67]\nds_write_b64 v231, v[68:69] offset:2048\nds_write_b64 v231, v[74:75] offset:2560\n"
"s_mov_b64 exec, -1\nM 152,164,116\nM 40,160,104\nR 180,184,40960\nM 40,164,120\nM 56,160,108\n"
"R 182,184,45056\nM 56,164,124\nG 6\nM 60,168,108\nR 160,185,32768\ns_not_b64 exec, s[66:67]\n"
"ds_write_b64 v231, v[80:81] offset:3072\nds_write_b64 v231, v[86:87] offset:3584\ns_mov_b64 exec, -1\nM 60,172,124\nM 44,168,104\nR 162,185,36864\n"
"M 44,172,120\ns_add_u32 m0, s50, 106496\nM 156,168,100\nR 164,185,40960\nM 156,172,116\ns_add_u32 s63, s101, 128\n"
"M 140,168,96\nR 166,185,45056\nM 140,172,112\nG 6\nM 128,176,128\nR 168,186,32768\n"
"s_waitcnt vmcnt(13) lgkmcnt(4)\ns_barrier\nM 128,180,144\nbuffer_load_dwordx4 v220, s[12:15], s63 offen lds\nM 144,176,132\nR 170,186,36864\n"
"M 144,180,148\ns_cmp_eq_u32 s78, 0\ns_cbranch_scc1 Lq151\nv_mov_b32 v230, 0\nv_mov_b32 v231, 0\nv_mov_b32 v232, 0\n"
"v_mov_b32 v233, 0\nv_mov_b32 v64, 0\nv_mov_b32 v65, 0\nv_mov_b32 v70, 0\nv_mov_b32 v71, 0\nv_mov_b32 v234, 0\n"
"v_mov_b32 v235, 0\nv_mov_b32 v236, 0\nv_mov_b32 v237, 0\nv_mov_b32 v76, 0\nv_mov_b32 v77, 0\nv_mov_b32 v82, 0\n"
"v_mov_b32 v83, 0\nLq151:\ns_lshl4_add_u32 s65, s41, s42\nM 32,176,136\nR 172,186,40960\ns_lshl_b32 s65, s65, 12\n"
"M 32,180,152\ns_add_u32 s65, s65, 77529088\nM 48,176,140\nR 174,186,45056\nM 48,180,156\nG 4\n"
"M 52,160,140\nR 176,187,32768\nM 52,164,156\nbuffer_load_dwordx4 v221, s[12:15], s63 offen offset:1024 lds\nM 36,160,136\nR 178,187,36864\n"
"M 36,164,152\ns_add_u32 m0, s50, 114688\nM 148,160,132\nR 180,187,40960\nM 148,164,148\ns_add_u32 s63, s101, 256\n"
"M 132,160,128\nR 182,187,45056\nM 132,164,144\nG 4\nM 136,168,128\nR 160,184,49152\n"
"M 136,172,144\nbuffer_load_dwordx4 v220, s[12:15], s63 offen lds\nM 152,168,132\nR 162,184,53248\nM 152,172,148\nM 40,168,136\n"
"R 164,184,57344\ns_lshl2_add_u32 s60, s45, s40\ns_waitcnt vmcnt(11)\nv_lshlrev_b32 v224, 3, v252\ns_mul_i32 s60, s60, 7168\ns_bfm_b64 exec, 14, 42\n"
"buffer_load_dword v224, v224, s[12:15], s60 offen\ns_mov_b64 exec, -1\nM 40,172,152\nM 56,168,140\nR 166,184,61440\nv_mfma_f32_16x16x32_bf16 v[136:139], v[242:245], v[208:211], v[136:139]\n"
"M 56,172,156\nG 4\nM 60,176,140\nR 168,185,49152\nv_mfma_f32_16x16x32_bf16 v[140:143], v[244:247], v[208:211], v[140:143]\nM 60,180,156\n"
"buffer_load_dwordx4 v221, s[12:15], s63 offen offset:1024 lds\nM 44,176,136\nR 170,185,53248\nv_mfma_f32_16x16x32_bf16 v[152:155], v[240:243], v[210:213], v[152:155]\ns_add_u32 s63, s100, 0\nM 44,180,152\n"
"s_add_u32 m0, s50, 0\nM 156,176,132\nR 172,185,57344\nv_mfma_f32_16x16x32_bf16 v[156:159], v[242:245], v[210:213], v[156:159]\nM 156,180,148\nbuffer_load_dwordx4 v222, s[12:15], s63 offen lds\n"
"M 140,176,128\nR 174,185,61440\nM 140,180,144\nG 4\nM 128,160,160\nR 176,186,49152\n"
"M 128,164,176\nM 144,160,164\nR 178,186,53248\nM 144,164,180\nbuffer_load_dwordx4 v223, s[12:15], s63 offen offset:1024 lds\nM 32,160,168\n"
"R 180,186,57344\nM 32,164,184\nM 48,160,172\nR 182,186,61440\nM 48,164,188\nG 4\n"
"M 52,168,172\nR 160,187,49152\nM 52,172,188\nM 36,168,168\nR 162,187,53248\nM 36,172,184\n"
"s_movk_i32 s63, 2048\nv_mad_u32_u24 v220, v228, s63, v229\nv_add_u32 v221, 1047552, v220\ns_add_u32 m0, s50, 8192\ns_add_u32 s63, s53, 0\nbuffer_load_dwordx4 v220, s[8:11], s63 offen lds\n"
"M 148,168,164\nR 164,187,57344\ns_lshl_b32 s60, s40, 12\ns_add_u32 s60, s60, 49152\nv_add_u32 v226, s60, v252\nv_add_u32 v111, s60, v227\n"
"M 148,172,180\nM 132,168,160\nR 166,187,61440\ns_cmp_eq_u32 s40, 0\ns_cselect_b64 s[86:87], 0, -1\ns_cselect_b32 s60, s41, 0\n"
"s_cmp_lg_u32 s60, 0\ns_cselect_b64 s[66:67], -1, 0\nM 132,172,176\nG 4\nM 136,176,160\nR 168,188,0\n"
"M 136,180,176\ns_mov_b64 exec, s[86:87]\nds_read_b128 v[230:233], v226 offset:0\nds_read_b64 v[64:65], v111 offset:2048\ns_mov_b64 exec, -1\nM 152,176,164\n"
"R 170,188,4096\ns_waitcnt vmcnt(11) lgkmcnt(1)\ns_barrier\nM 152,180,180\nbuffer_load_dwordx4 v221, s[8:11], s63 offen offset:1024 lds\nM 40,176,168\n"
"R 172,188,8192\nM 40,180,184\ns_mov_b64 exec, s[86:87]\nds_read_b64 v[70:71], v111 offset:2560\nds_read_b128 v[234:237], v226 offset:1024\ns_mov_b64 exec, -1\n"
"M 56,176,172\nR 174,188,12288\nM 56,180,188\nM 60,160,172\nR 176,189,0\nM 60,164,188\n"
"s_mov_b64 exec, s[86:87]\nds_read_b64 v[76:77], v111 offset:3072\nds_read_b64 v[82:83], v111 offset:3584\ns_mov_b64 exec, -1\nM 44,160,168\nR 178,189,4096\n"
"M 44,164,184\nM 156,160,164\nR 180,189,8192\nM 156,164,180\ns_mov_b64 exec, s[66:67]\nbuffer_load_dwordx4 v[230:233], v252, s[20:23], s65 offen offset:0 sc1\n"
"s_mov_b64 exec, -1\nM 140,160,160\nR 182,189,12288\nM 140,164,176\nG 6\nM 32,168,200\n"
"R 160,190,0\ns_mov_b64 exec, s[66:67]\nbuffer_load_dwordx2 v[64:65], v227, s[20:23], s65 offen offset:2048 sc1\ns_mov_b64 exec, -1\nM 32,172,216\nR 162,190,4096\n"
"M 48,168,204\nR 164,190,8192\nM 48,172,220\nR 166,190,12288\nG 4\nM 52,176,204\n"
"R 168,191,0\ns_mov_b64 exec, s[66:67]\nbuffer_load_dwordx2 v[70:71], v227, s[20:23], s65 offen offset:2560 sc1\ns_mov_b64 exec, -1\nM 52,180,220\nR 170,191,4096\n"
"M 36,176,200\nR 172,191,8192\nM 36,180,216\nR 174,191,12288\nG 4\nM 40,160,200\n"
"R 176,188,16384\ns_mov_b64 exec, s[66:67]\nbuffer_load_dwordx4 v[234:237], v252, s[20:23], s65 offen offset:1024 sc1\ns_mov_b64 exec, -1\nM 40,164,216\nR 178,188,20480\n"
"M 56,160,204\nR 180,188,24576\nM 56,164,220\nR 182,188,28672\nG 4\nM 60,168,204\n"
"R 160,189,16384\ns_mov_b64 exec, s[66:67]\nbuffer_load_dwordx2 v[76:77], v227, s[20:23], s65 offen offset:3072 sc1\ns_mov_b64 exec, -1\nM 60,172,220\nR 162,189,20480\n"
"M 44,168,200\nR 164,189,24576\nM 44,172,216\nR 166,189,28672\nG 4\nM 32,176,232\n"
"R 168,190,16384\ns_mov_b64 exec, s[66:67]\nbuffer_load_dwordx2 v[82:83], v227, s[20:23], s65 offen offset:3584 sc1\ns_mov_b64 exec, -1\nLq185:\ns_lshr_b32 s60, s45, 3\n"
"s_lshl2_add_u32 s60, s60, s40\ns_lshl_b32 s60, s60, 8\ns_waitcnt vmcnt(11)\nv_lshl_add_u32 v224, v252, 3, s60\nv_add_u32 v224, Lq185+4096-LqPC, v224\nv_min_u32 v224, LqE-LqPC-4, v224\n"
"s_mov_b64 exec, 3\nglobal_load_dword v224, v224, s[98:99]\ns_mov_b64 exec, -1\nM 32,180,248\nR 170,190,20480\nM 48,176,236\n"
"R 172,190,24576\nM 48,180,252\nR 174,190,28672\nG 4\nM 52,160,236\nR 176,191,16384\n"
"M 52,164,252\nR 178,191,20480\nM 36,160,232\nR 180,191,24576\nM 36,164,248\nR 182,191,28672\n"
"G 4\nM 40,168,232\nR 160,188,0\nM 40,172,248\nR 162,188,4096\nM 56,168,236\n"
"R 164,188,8192\nM 56,172,252\nR 166,188,12288\nG 4\nM 60,176,236\nR 168,189,0\n"
"M 60,180,252\nR 170,189,4096\nM 44,176,232\nR 172,189,8192\nM 44,180,248\nR 174,189,12288\n"
"G 4\nM 128,160,192\nR 176,190,0\nM 128,164,208\nR 178,190,4096\ns_mov_b32 m0, s51\n"
"M 144,160,196\nR 180,190,8192\nds_write_addtid_b32 v32 offset:0\nM 144,164,212\nR 182,190,12288\nds_write_addtid_b32 v33 offset:256\n"
"G 6\nM 148,168,196\nR 160,191,0\nds_write_addtid_b32 v34 offset:512\nM 148,172,212\nR 162,191,4096\n"
"ds_write_addtid_b32 v35 offset:768\nM 132,168,192\nR 164,191,8192\nds_write_addtid_b32 v36 offset:1024\nLpg5:\ns_cbranch_execz LretQ0\n"
"M 132,172,208\nR 166,191,12288\nds_write_addtid_b32 v37 offset:1280\nG 9\nM 136,176,192\nR 168,188,16384\n"
"ds_write_addtid_b32 v38 offset:1536\nM 136,180,208\nR 170,188,20480\nds_write_addtid_b32 v39 offset:1792\nM 152,176,196\nR 172,188,24576\n"
"ds_write_addtid_b32 v40 offset:2048\nM 152,180,212\nR 174,188,28672\nds_write_addtid_b32 v41 offset:2304\nG 9\nM 156,160,196\n"
"R 176,189,16384\nds_write_addtid_b32 v42 offset:2560\nM 156,164,212\nR 178,189,20480\nds_write_addtid_b32 v43 offset:2816\nM 140,160,192\n"
"R 180,189,24576\nds_write_addtid_b32 v44 offset:3072\nds_write_addtid_b32 v45 offset:3328\nds_write_addtid_b32 v46 offset:3584\nds_write_addtid_b32 v47 offset:3840\nds_write_addtid_b32 v48 offset:4096\n"
"ds_write_addtid_b32 v49 offset:4352\nds_write_addtid_b32 v50 offset:4608\nds_write_addtid_b32 v51 offset:4864\nds_write_addtid_b32 v52 offset:5120\nds_write_addtid_b32 v53 offset:5376\nds_write_addtid_b32 v54 offset:5632\n"
"ds_write_addtid_b32 v55 offset:5888\nds_write_addtid_b32 v56 offset:6144\nds_write_addtid_b32 v57 offset:6400\nds_write_addtid_b32 v58 offset:6656\nds_write_addtid_b32 v59 offset:6912\nds_write_addtid_b32 v60 offset:7168\n"
"ds_write_addtid_b32 v61 offset:7424\nds_write_addtid_b32 v62 offset:7680\nds_write_addtid_b32 v63 offset:7936\nM 140,164,208\nR 182,189,28672\nG 15\n"
"M 128,168,224\nR 160,190,16384\ns_add_u32 m0, s50, 16384\nM 128,172,240\nR 162,190,20480\nG 3\n"
"s_barrier\ns_add_u32 s63, s53, 65536\nM 144,168,228\nR 164,190,24576\nbuffer_load_dwordx4 v220, s[8:11], s63 offen lds\nds_read2st64_b32 v[32:33], v225 offset0:0 offset1:1\n"
"M 144,172,244\nR 166,190,28672\nbuffer_load_dwordx4 v221, s[8:11], s63 offen offset:1024 lds\nds_read2st64_b32 v[34:35], v225 offset0:2 offset1:3\nG 6\nM 148,176,228\n"
"R 168,191,16384\ns_add_u32 m0, s50, 24576\ns_add_u32 s63, s53, 131072\nbuffer_load_dwordx4 v220, s[8:11], s63 offen lds\nds_read2st64_b32 v[36:37], v225 offset0:4 offset1:5\nM 148,180,244\n"
"R 170,191,20480\nbuffer_load_dwordx4 v221, s[8:11], s63 offen offset:1024 lds\nds_read2st64_b32 v[38:39], v225 offset0:6 offset1:7\nM 132,176,224\nR 172,191,24576\ns_add_u32 m0, s50, 32768\n"
"s_add_u32 s63, s53, 196608\nbuffer_load_dwordx4 v220, s[8:11], s63 offen lds\nds_read2st64_b32 v[40:41], v225 offset0:8 offset1:9\nM 132,180,240\nR 174,191,28672\nbuffer_load_dwordx4 v221, s[8:11], s63 offen offset:1024 lds\n"
"ds_read2st64_b32 v[42:43], v225 offset0:10 offset1:11\nG 9\nM 136,160,224\ns_add_u32 m0, s50, 40960\ns_add_u32 s63, s53, 262144\nbuffer_load_dwordx4 v220, s[8:11], s63 offen lds\n"
"ds_read2st64_b32 v[44:45], v225 offset0:12 offset1:13\nM 136,164,240\nbuffer_load_dwordx4 v221, s[8:11], s63 offen offset:1024 lds\nds_read2st64_b32 v[46:47], v225 offset0:14 offset1:15\nM 152,160,228\ns_add_u32 m0, s50, 49152\n"
"s_add_u32 s63, s53, 327680\nbuffer_load_dwordx4 v220, s[8:11], s63 offen lds\nds_read2st64_b32 v[48:49], v225 offset0:16 offset1:17\nM 152,164,244\nds_read2st64_b32 v[50:51], v225 offset0:18 offset1:19\nds_read2st64_b32 v[52:53], v225 offset0:20 offset1:21\n"
"G 6\nM 156,168,228\nds_read2st64_b32 v[54:55], v225 offset0:22 offset1:23\nds_read2st64_b32 v[56:57], v225 offset0:24 offset1:25\nM 156,172,244\nds_read2st64_b32 v[58:59], v225 offset0:26 offset1:27\n"
"ds_read2st64_b32 v[60:61], v225 offset0:28 offset1:29\nM 140,168,224\nds_read2st64_b32 v[62:63], v225 offset0:30 offset1:31\nM 140,172,240\nG 7\nv_pk_add_f32 v[144:145], v[144:145], v[48:49]\n"
"G 6\nv_pk_add_f32 v[146:147], v[146:147], v[50:51]\nG 5\nv_pk_add_f32 v[148:149], v[148:149], v[52:53]\nG 4\nv_pk_add_f32 v[150:151], v[150:151], v[54:55]\n"
"G 3\nv_pk_add_f32 v[152:153], v[152:153], v[56:57]\nG 2\nv_pk_add_f32 v[154:155], v[154:155], v[58:59]\nG 1\nv_pk_add_f32 v[156:157], v[156:157], v[60:61]\n"
"G 0\nv_pk_add_f32 v[158:159], v[158:159], v[62:63]\nv_pk_add_f32 v[128:129], v[128:129], v[32:33]\nv_pk_add_f32 v[130:131], v[130:131], v[34:35]\nv_pk_add_f32 v[132:133], v[132:133], v[36:37]\nv_pk_add_f32 v[134:135], v[134:135], v[38:39]\n"
"v_pk_add_f32 v[136:137], v[136:137], v[40:41]\nv_pk_add_f32 v[138:139], v[138:139], v[42:43]\nv_pk_add_f32 v[140:141], v[140:141], v[44:45]\nv_pk_add_f32 v[142:143], v[142:143], v[46:47]\nv_cvt_pk_bf16_f32 v144, v144, v145\nv_cvt_pk_bf16_f32 v145, v146, v147\n"
"v_cvt_pk_bf16_f32 v146, v148, v149\nv_cvt_pk_bf16_f32 v147, v150, v151\nv_cvt_pk_bf16_f32 v152, v152, v153\nv_cvt_pk_bf16_f32 v153, v154, v155\nv_cvt_pk_bf16_f32 v154, v156, v157\nv_cvt_pk_bf16_f32 v155, v158, v159\n"
"v_cvt_pk_bf16_f32 v128, v128, v129\nv_cvt_pk_bf16_f32 v129, v130, v131\nv_cvt_pk_bf16_f32 v130, v132, v133\nv_cvt_pk_bf16_f32 v131, v134, v135\nv_cvt_pk_bf16_f32 v136, v136, v137\nv_cvt_pk_bf16_f32 v137, v138, v139\n"
"v_cvt_pk_bf16_f32 v138, v140, v141\nv_cvt_pk_bf16_f32 v139, v142, v143\nv_mfma_f32_16x16x32_bf16 v[52:55], v[16:19], v[144:147], v[96:99]\nv_mfma_f32_16x16x32_bf16 v[48:51], v[0:3], v[144:147], v[100:103]\nv_mfma_f32_16x16x32_bf16 v[60:63], v[20:23], v[152:155], v[96:99]\nv_mfma_f32_16x16x32_bf16 v[56:59], v[4:7], v[152:155], v[100:103]\n"
"s_waitcnt vmcnt(13)\nv_max3_u32 v248, v233, v65, v71\nv_cmp_eq_u32 vcc, -1, v248\ns_nop 0\ns_cbranch_vccnz Lq187\nLq188:\n"
"s_waitcnt vmcnt(10)\nv_max3_u32 v248, v237, v77, v83\nv_cmp_eq_u32 vcc, -1, v248\ns_nop 0\ns_cbranch_vccnz Lq189\nLq190:\n"
"v_mfma_f32_16x16x32_bf16 v[36:39], v[0:3], v[128:131], v[96:99]\nv_mfma_f32_16x16x32_bf16 v[32:35], v[230:233], v[128:131], v[104:107]\nv_mfma_f32_16x16x32_bf16 v[44:47], v[4:7], v[136:139], v[96:99]\nv_mfma_f32_16x16x32_bf16 v[40:43], v[234:237], v[136:139], v[104:107]\ns_lshl_b32 s84, s42, 11\ns_add_u32 s84, s84, s49\n"
"s_add_u32 s84, s84, 2097152\ns_add_u32 s85, s84, 32768\ns_lshl2_add_u32 s60, s45, s40\ns_waitcnt vmcnt(9)\nv_lshlrev_b32 v224, 3, v252\ns_mul_i32 s60, s60, 7168\n"
"s_bfm_b64 exec, 28, 28\nbuffer_load_dword v224, v224, s[16:19], s60 offen\ns_mov_b64 exec, -1\nv_mov_b32 v238, s46\nv_mov_b32 v239, s47\nv_max_f32 v52, v52, v53\n"
"v_max_f32 v54, v54, v55\nv_max_f32 v60, v60, v61\nv_max_f32 v62, v62, v63\nv_max3_f32 v52, v52, v48, v49\nv_max3_f32 v54, v54, v50, v51\nv_max3_f32 v60, v60, v56, v57\n"
"v_max3_f32 v62, v62, v58, v59\nv_max_f32 v36, v36, v37\nv_max_f32 v38, v38, v39\nv_max_f32 v44, v44, v45\nv_max_f32 v46, v46, v47\nv_max3_f32 v36, v36, v32, v33\n"
"v_max3_f32 v38, v38, v34, v35\nv_max3_f32 v44, v44, v40, v41\nv_max3_f32 v46, v46, v42, v43\nv_max_f32 v118, v52, v54\nv_max_f32 v119, v60, v62\nv_max_f32 v116, v36, v38\n"
"v_max_f32 v117, v44, v46\ns_nop 0\nv_permlane32_swap_b32 v116, v118\nv_permlane32_swap_b32 v117, v119\nv_max_f32 v116, v116, v118\nv_max_f32 v117, v117, v119\n"
"s_nop 1\nv_permlane16_swap_b32 v116, v117\nv_max_f32 v116, v116, v117\nv_mov_b32 v117, v116\ns_nop 1\nv_permlane16_swap_b32 v116, v117\n"
"v_mov_b32 v118, v116\nv_mov_b32 v119, v117\ns_nop 0\nv_permlane32_swap_b32 v116, v118\nv_permlane32_swap_b32 v117, v119\nv_mul_f32 v116, v116, v215\n"
"v_mul_f32 v118, v118, v218\nv_mul_f32 v117, v117, v215\nv_mul_f32 v119, v119, v218\nv_fma_f32 v52, v52, v218, -v118\nv_fma_f32 v54, v54, v218, -v118\nv_fma_f32 v60, v60, v218, -v119\n"
"v_fma_f32 v62, v62, v218, -v119\nv_fma_f32 v36, v36, v215, -v116\nv_fma_f32 v38, v38, v215, -v116\nv_fma_f32 v44, v44, v215, -v117\nv_fma_f32 v46, v46, v215, -v117\nv_exp_f32 v52, v52\n"
"v_exp_f32 v54, v54\nv_exp_f32 v60, v60\nv_exp_f32 v62, v62\nv_exp_f32 v36, v36\nv_exp_f32 v38, v38\nv_exp_f32 v44, v44\n"
"v_exp_f32 v46, v46\nv_add_f32 v118, v52, v54\nv_add_f32 v119, v60, v62\nv_add_f32 v116, v36, v38\nv_add_f32 v117, v44, v46\ns_nop 0\n"
"v_permlane32_swap_b32 v116, v118\nv_permlane32_swap_b32 v117, v119\nv_add_f32 v116, v116, v118\nv_add_f32 v117, v117, v119\ns_nop 1\nv_permlane16_swap_b32 v116, v117\n"
"v_add_f32 v116, v116, v117\ns_barrier\nv_rcp_f32 v116, v116\ns_nop 0\nv_mov_b32 v117, v116\ns_nop 1\n"
"v_permlane16_swap_b32 v116, v117\nv_mov_b32 v118, v116\nv_mov_b32 v119, v117\ns_nop 0\nv_permlane32_swap_b32 v116, v118\nv_permlane32_swap_b32 v117, v119\n"
"v_mul_f32 v52, v52, v118\nv_mul_f32 v54, v54, v118\nv_mul_f32 v60, v60, v119\nv_mul_f32 v62, v62, v119\nv_mul_f32 v36, v36, v116\nv_mul_f32 v38, v38, v116\n"
"v_mul_f32 v44, v44, v117\nv_mul_f32 v46, v46, v117\nv_cvt_pk_bf16_f32 v52, v52, v52\nv_cvt_pk_bf16_f32 v54, v54, v54\nv_cvt_pk_bf16_f32 v60, v60, v60\nv_cvt_pk_bf16_f32 v62, v62, v62\n"
"v_cvt_pk_bf16_f32 v36, v36, v36\nv_cvt_pk_bf16_f32 v38, v38, v38\nv_cvt_pk_bf16_f32 v44, v44, v44\nv_cvt_pk_bf16_f32 v46, v46, v46\nv_and_b32 v48, v52, v112\nv_and_b32 v49, v54, v113\n"
"v_and_b32 v50, v52, v114\nv_and_b32 v51, v54, v115\nv_and_b32 v56, v60, v112\nv_and_b32 v57, v62, v113\nv_and_b32 v58, v60, v114\nv_and_b32 v59, v62, v115\n"
"v_mfma_f32_16x16x32_bf16 v[144:147], v[66:69], v[48:51], 0\nv_mfma_f32_16x16x32_bf16 v[148:151], v[72:75], v[48:51], 0\nv_and_b32 v32, v36, v112\nv_and_b32 v33, v38, v113\nv_and_b32 v34, v36, v114\nv_and_b32 v35, v38, v115\n"
"v_mfma_f32_16x16x32_bf16 v[152:155], v[78:81], v[56:59], 0\nv_mfma_f32_16x16x32_bf16 v[156:159], v[84:87], v[56:59], 0\nv_and_b32 v40, v44, v112\nv_and_b32 v41, v46, v113\nv_and_b32 v42, v44, v114\nv_and_b32 v43, v46, v115\n"
"v_mfma_f32_16x16x32_bf16 v[128:131], v[64:67], v[32:35], 0\nv_mfma_f32_16x16x32_bf16 v[132:135], v[70:73], v[32:35], 0\nv_mfma_f32_16x16x32_bf16 v[136:139], v[76:79], v[40:43], 0\nv_mfma_f32_16x16x32_bf16 v[140:143], v[82:85], v[40:43], 0\nv_cvt_pk_bf16_f32 v144, v144, v145\nv_cvt_pk_bf16_f32 v145, v146, v147\n"
"v_cvt_pk_bf16_f32 v146, v148, v149\nv_cvt_pk_bf16_f32 v147, v150, v151\nbuffer_store_dwordx4 v[144:147], v252, s[20:23], s85 offen offset:0 sc1\nv_cvt_pk_bf16_f32 v152, v152, v153\nv_cvt_pk_bf16_f32 v153, v154, v155\nv_cvt_pk_bf16_f32 v154, v156, v157\n"
"v_cvt_pk_bf16_f32 v155, v158, v159\nbuffer_store_dwordx4 v[152:155], v252, s[20:23], s85 offen offset:1024 sc1\nv_cvt_pk_bf16_f32 v128, v128, v129\nv_cvt_pk_bf16_f32 v129, v130, v131\nv_cvt_pk_bf16_f32 v130, v132, v133\nv_cvt_pk_bf16_f32 v131, v134, v135\n"
"buffer_store_dwordx4 v[128:131], v252, s[20:23], s84 offen offset:0 sc1\nv_cvt_pk_bf16_f32 v136, v136, v137\nv_cvt_pk_bf16_f32 v137, v138, v139\nv_cvt_pk_bf16_f32 v138, v140, v141\nv_cvt_pk_bf16_f32 v139, v142, v143\nbuffer_store_dwordx4 v[136:139], v252, s[20:23], s84 offen offset:1024 sc1\n"
"s_mov_b64 exec, 0\ns_cmp_eq_u32 s40, 1\ns_cbranch_scc1 Lpg7\ns_cmp_eq_u32 s40, 2\ns_cbranch_scc1 LretO0\ns_cmp_eq_u32 s40, 3\n"
"s_cbranch_scc1 LretO0\ns_branch Lpg6\nLretO0:\ns_mov_b64 exec, -1\ns_add_u32 s61, s79, 88080384\ns_lshl_b32 s62, s42, 3\n"
"s_add_u32 s61, s61, s62\ns_waitcnt vmcnt(0)\ns_mov_b64 exec, 1\nbuffer_atomic_swap_x2 v[238:239], off, s[20:23], s61 sc1\ns_mov_b64 exec, -1\nv_bfe_u32 v250, v252, 7, 1\n"
"v_mul_u32_u24 v250, 2176, v250\nv_bfe_u32 v251, v252, 4, 3\nv_lshl_add_u32 v250, v251, 3, v250\ns_lshl_b32 s60, s90, 6\nv_add_u32 v250, s60, v250\ns_sleep 3\n"
"v_mov_b32 v248, 0\nv_mov_b32 v249, 0\ns_add_u32 s61, s80, 88080384\ns_mov_b64 exec, 0xffff\nbuffer_atomic_or_x2 v[248:249], v250, s[20:23], s61 offen sc0 sc1\ns_mov_b64 exec, -1\n"
"buffer_load_dwordx4 v221, s[8:11], s63 offen offset:1024 lds\ns_add_u32 m0, s50, 57344\ns_add_u32 s63, s53, 393216\nbuffer_load_dwordx4 v220, s[8:11], s63 offen lds\nbuffer_load_dwordx4 v221, s[8:11], s63 offen offset:1024 lds\ns_add_u32 m0, s50, 65536\n"
"s_add_u32 s63, s53, 458752\nbuffer_load_dwordx4 v220, s[8:11], s63 offen lds\nbuffer_load_dwordx4 v221, s[8:11], s63 offen offset:1024 lds\ns_add_u32 m0, s50, 73728\ns_add_u32 s63, s53, 524288\nbuffer_load_dwordx4 v220, s[8:11], s63 offen lds\n"
"buffer_load_dwordx4 v221, s[8:11], s63 offen offset:1024 lds\ns_add_u32 m0, s50, 81920\ns_add_u32 s63, s53, 589824\nbuffer_load_dwordx4 v220, s[8:11], s63 offen lds\nbuffer_load_dwordx4 v221, s[8:11], s63 offen offset:1024 lds\ns_add_u32 m0, s50, 90112\n"
"s_add_u32 s63, s53, 655360\nbuffer_load_dwordx4 v220, s[8:11], s63 offen lds\ns_waitcnt vmcnt(10)\nv_cmp_eq_u64 s[86:87], s[46:47], v[248:249]\ns_nop 1\ns_and_b32 s88, s86, 0xffff\n"
"s_cmp_eq_u32 s88, 0xffff\ns_cbranch_scc1 Lq201\nv_mov_b32 v246, 0\nv_mov_b32 v247, 0\ns_add_u32 s61, s80, 88080384\ns_mov_b64 exec, 0xffff\n"
"buffer_atomic_or_x2 v[246:247], v250, s[20:23], s61 offen sc0 sc1\ns_mov_b64 exec, -1\nLq201:\nbuffer_load_dwordx4 v221, s[8:11], s63 offen offset:1024 lds\nv_bfe_u32 v234, v252, 4, 4\nv_lshrrev_b32 v235, 8, v252\n"
"v_lshlrev_b32 v235, 2, v235\nv_sub_u32 v234, v234, v235\nv_lshlrev_b32 v235, 4, v234\ns_mov_b64 s[60:61], 0x3f80\nv_lshlrev_b64 v[208:209], v235, s[60:61]\nv_cmp_gt_u32 s[60:61], 4, v234\n"
"s_nop 1\nv_cndmask_b32 v212, 0, v208, s[60:61]\nv_cndmask_b32 v208, 0, v208, s[60:61]\nv_cndmask_b32 v213, 0, v209, s[60:61]\nv_cndmask_b32 v209, 0, v209, s[60:61]\ns_nop 1\n"
"v_mfma_f32_16x16x32_bf16 v[0:3], v[208:211], v[192:195], 0\nv_mfma_f32_16x16x32_bf16 v[4:7], v[210:213], v[192:195], 0\nv_mfma_f32_16x16x32_bf16 v[8:11], v[208:211], v[196:199], 0\nv_mfma_f32_16x16x32_bf16 v[12:15], v[210:213], v[196:199], 0\nv_mfma_f32_16x16x32_bf16 v[16:19], v[208:211], v[200:203], 0\nv_mfma_f32_16x16x32_bf16 v[20:23], v[210:213], v[200:203], 0\n"
"v_mfma_f32_16x16x32_bf16 v[24:27], v[208:211], v[204:207], 0\nv_mfma_f32_16x16x32_bf16 v[28:31], v[210:213], v[204:207], 0\ns_cmp_eq_u32 s88, 0xffff\ns_cbranch_scc1 Lq202\ns_waitcnt vmcnt(1)\nv_mov_b32 v248, v246\n"
"v_mov_b32 v249, v247\nv_cmp_eq_u64 s[86:87], s[46:47], v[248:249]\ns_nop 1\ns_and_b32 s62, s86, 0xffff\ns_cmp_eq_u32 s62, 0xffff\ns_cbranch_scc1 Lq193\n"
"Lq192:\nv_mov_b32 v248, 0\nv_mov_b32 v249, 0\ns_add_u32 s61, s80, 88080384\ns_mov_b64 exec, 0xffff\nbuffer_atomic_or_x2 v[248:249], v250, s[20:23], s61 offen sc0 sc1\n"
"s_mov_b64 exec, -1\ns_waitcnt vmcnt(0)\nv_cmp_eq_u64 s[86:87], s[46:47], v[248:249]\ns_nop 1\ns_and_b32 s62, s86, 0xffff\ns_cmp_eq_u32 s62, 0xffff\n"
"s_cbranch_scc1 Lq193\ns_sub_u32 s48, s48, 1\ns_cmp_gt_i32 s48, 0\ns_cbranch_scc1 Lq192\nLq193:\nLq202:\n"
"s_add_u32 s64, s70, 2097152\nbuffer_load_dwordx4 a[0:3], v252, s[20:23], s64 offen offset:0 sc1\ns_add_u32 s65, s70, 2129920\nbuffer_load_dwordx4 a[4:7], v252, s[20:23], s65 offen offset:0 sc1\ns_add_u32 s66, s71, 2097152\nbuffer_load_dwordx4 a[8:11], v252, s[20:23], s66 offen offset:0 sc1\n"
"s_add_u32 s67, s71, 2129920\nbuffer_load_dwordx4 a[12:15], v252, s[20:23], s67 offen offset:0 sc1\nbuffer_load_dwordx4 a[16:19], v252, s[20:23], s64 offen offset:1024 sc1\nbuffer_load_dwordx4 a[20:23], v252, s[20:23], s65 offen offset:1024 sc1\nbuffer_load_dwordx4 a[24:27], v252, s[20:23], s66 offen offset:1024 sc1\nbuffer_load_dwordx4 a[28:31], v252, s[20:23], s67 offen offset:1024 sc1\n"
"s_barrier\nR 160,184,8192\nR 162,184,12288\nR 164,184,16384\nR 166,184,20480\nR 168,185,8192\n"
"R 170,185,12288\nR 172,185,16384\nR 174,185,20480\nR 176,186,8192\nR 178,186,12288\nR 180,186,16384\n"
"R 182,186,20480\nbuffer_load_dwordx4 a[32:35], v252, s[20:23], s64 offen offset:2048 sc1\nbuffer_load_dwordx4 a[36:39], v252, s[20:23], s65 offen offset:2048 sc1\nbuffer_load_dwordx4 a[40:43], v252, s[20:23], s66 offen offset:2048 sc1\nbuffer_load_dwordx4 a[44:47], v252, s[20:23], s67 offen offset:2048 sc1\nbuffer_load_dwordx4 a[48:51], v252, s[20:23], s64 offen offset:3072 sc1\n"
"buffer_load_dwordx4 a[52:55], v252, s[20:23], s65 offen offset:3072 sc1\nbuffer_load_dwordx4 a[56:59], v252, s[20:23], s66 offen offset:3072 sc1\nbuffer_load_dwordx4 a[60:63], v252, s[20:23], s67 offen offset:3072 sc1\ns_add_u32 s64, s70, 2101248\nbuffer_load_dwordx4 a[64:67], v252, s[20:23], s64 offen offset:0 sc1\ns_add_u32 s65, s70, 2134016\n"
"buffer_load_dwordx4 a[68:71], v252, s[20:23], s65 offen offset:0 sc1\ns_add_u32 s66, s71, 2101248\nbuffer_load_dwordx4 a[72:75], v252, s[20:23], s66 offen offset:0 sc1\ns_add_u32 s67, s71, 2134016\nbuffer_load_dwordx4 a[76:79], v252, s[20:23], s67 offen offset:0 sc1\ns_waitcnt vmcnt(19) lgkmcnt(10)\n"
"M 0,160,0\nbuffer_load_dwordx4 a[80:83], v252, s[20:23], s64 offen offset:1024 sc1\ns_waitcnt vmcnt(16) lgkmcnt(8)\nM 0,164,16\nM 16,160,4\ns_lshl_b32 s60, s40, 5\n"
"v_xor_b32 v238, s60, v188\nv_and_b32 v238, 0xfffffbff, v238\ns_waitcnt vmcnt(15)\nM 16,164,20\nv_mov_b32 v242, s76\nv_mov_b32 v243, s76\n"
"v_mfma_f32_16x16x32_bf16 v[32:35], v[160:163], a[8:11], 0\nbuffer_load_dwordx4 a[84:87], v252, s[20:23], s65 offen offset:1024 sc1\ns_waitcnt vmcnt(15)\nM 32,164,24\nv_mov_b32 v244, s76\nv_mov_b32 v245, s76\n"
"v_mfma_f32_16x16x32_bf16 v[48:51], v[160:163], a[12:15], 0\ns_waitcnt vmcnt(14)\nM 48,164,28\nG 4\nv_mfma_f32_16x16x32_bf16 v[52:55], v[168:171], a[12:15], 0\nR 160,187,8192\n"
"M 52,172,28\nbuffer_load_dwordx4 a[88:91], v252, s[20:23], s66 offen offset:1024 sc1\nv_mfma_f32_16x16x32_bf16 v[36:39], v[168:171], a[8:11], 0\nR 162,187,12288\nM 36,172,24\nM 20,168,4\n"
"R 164,187,16384\nR 64,238,32768\nR 66,238,36864\nM 20,172,20\nbuffer_load_dwordx4 a[92:95], v252, s[20:23], s67 offen offset:1024 sc1\nM 4,168,0\n"
"R 166,187,20480\nR 68,238,33792\nR 70,238,37888\nM 4,172,16\nG 8\nM 8,176,0\n"
"R 168,184,24576\nM 8,180,16\nbuffer_load_dwordx4 a[96:99], v252, s[20:23], s64 offen offset:2048 sc1\nM 24,176,4\nR 170,184,28672\nM 24,180,20\n"
"v_mfma_f32_16x16x32_bf16 v[40:43], v[176:179], a[8:11], 0\nR 172,184,32768\nG 6\nv_mfma_f32_16x16x32_bf16 v[72:75], v[242:245], v[64:67], 0\nM 40,180,24\nG 3\n"
"v_mfma_f32_16x16x32_bf16 v[72:75], v[242:245], v[68:71], v[72:75]\nbuffer_load_dwordx4 a[100:103], v252, s[20:23], s65 offen offset:2048 sc1\nv_mfma_f32_16x16x32_bf16 v[56:59], v[176:179], a[12:15], 0\nR 174,184,36864\nM 56,180,28\nv_mfma_f32_16x16x32_bf16 v[60:63], v[160:163], a[12:15], 0\n"
"R 176,185,24576\nM 60,164,28\nbuffer_load_dwordx4 a[104:107], v252, s[20:23], s66 offen offset:2048 sc1\nv_lshrrev_b32 v64, 1, v252\nv_mfma_f32_16x16x32_bf16 v[44:47], v[160:163], a[8:11], 0\nR 178,185,28672\n"
"v_mov_b32 v73, s46\ns_and_b32 s61, s40, 3\ns_lshl_b32 s61, s61, 7\ns_add_u32 s61, s61, s91\ns_add_u32 s61, s61, 393216\ns_mov_b64 exec, 0xffff\n"
"buffer_store_dwordx2 v[72:73], v64, s[20:23], s61 offen sc1\ns_mov_b64 exec, -1\nM 44,164,24\nM 28,160,4\nR 180,185,32768\nM 28,164,20\n"
"buffer_load_dwordx4 a[108:111], v252, s[20:23], s67 offen offset:2048 sc1\nM 12,160,0\nR 182,185,36864\nM 12,164,16\ns_lshl_b32 s60, s40, 5\nv_xor_b32 v238, s60, v188\n"
"G 4\ns_waitcnt vmcnt(20)\nM 0,168,32\nR 160,186,24576\nv_and_b32 v238, 0xfffffbff, v238\ns_waitcnt vmcnt(16)\n"
"M 0,172,48\nbuffer_load_dwordx4 a[112:115], v252, s[20:23], s64 offen offset:3072 sc1\nM 16,168,36\nR 162,186,28672\ns_waitcnt vmcnt(16)\nM 16,172,52\n"
"M 32,168,40\nR 164,186,32768\ns_waitcnt vmcnt(15)\nM 32,172,56\nbuffer_load_dwordx4 a[116:119], v252, s[20:23], s65 offen offset:3072 sc1\ns_barrier\n"
"M 48,168,44\nR 166,186,36864\nR 64,238,40960\nR 66,238,45056\ns_waitcnt vmcnt(15)\nM 48,172,60\n"
"G 6\nM 52,176,44\nR 168,187,24576\nR 68,238,41984\nR 70,238,46080\nM 52,180,60\n"
"buffer_load_dwordx4 a[120:123], v252, s[20:23], s66 offen offset:3072 sc1\nM 36,176,40\nR 170,187,28672\nM 36,180,56\nM 20,176,36\nR 172,187,32768\n"
"M 20,180,52\nbuffer_load_dwordx4 a[124:127], v252, s[20:23], s67 offen offset:3072 sc1\nM 4,176,32\nR 174,187,36864\nG 6\nv_mfma_f32_16x16x32_bf16 v[72:75], v[242:245], v[64:67], 0\n"
"M 4,180,48\nG 3\nv_mfma_f32_16x16x32_bf16 v[72:75], v[242:245], v[68:71], v[72:75]\nM 8,160,32\nR 176,184,40960\nM 8,164,48\n"
"s_add_u32 s64, s70, 2105344\nbuffer_load_dwordx4 a[128:131], v252, s[20:23], s64 offen offset:0 sc1\nM 24,160,36\nR 178,184,45056\nM 24,164,52\nv_lshrrev_b32 v64, 1, v252\n"
"v_mov_b32 v73, s46\ns_and_b32 s61, s40, 3\nLpg6:\ns_cbranch_execz LretO0\ns_lshl_b32 s61, s61, 7\ns_add_u32 s61, s61, s91\n"
"s_add_u32 s61, s61, 524288\ns_mov_b64 exec, 0xffff\nbuffer_store_dwordx2 v[72:73], v64, s[20:23], s61 offen sc1\ns_mov_b64 exec, -1\nM 40,160,40\nR 180,184,49152\n"
"M 40,164,56\ns_add_u32 s65, s70, 2138112\nbuffer_load_dwordx4 a[132:135], v252, s[20:23], s65 offen offset:0 sc1\nM 56,160,44\nR 182,184,53248\nM 56,164,60\n"
"s_lshl_b32 s60, s40, 5\nv_xor_b32 v238, s60, v188\nG 4\nM 60,168,44\nR 160,185,40960\nv_and_b32 v238, 0xfffffbff, v238\n"
"M 60,172,60\ns_add_u32 s66, s71, 2105344\nbuffer_load_dwordx4 a[136:139], v252, s[20:23], s66 offen offset:0 sc1\nM 44,168,40\nR 162,185,45056\nM 44,172,56\n"
"M 28,168,36\nR 164,185,49152\nM 28,172,52\ns_add_u32 s67, s71, 2138112\nbuffer_load_dwordx4 a[140:143], v252, s[20:23], s67 offen offset:0 sc1\nM 12,168,32\n"
"R 166,185,53248\nM 12,172,48\ns_barrier\nG 4\ns_waitcnt vmcnt(21)\nM 0,176,64\n"
"R 168,186,40960\nR 64,238,49152\nR 66,238,53248\ns_waitcnt vmcnt(17)\nM 0,180,80\nbuffer_load_dwordx4 a[144:147], v252, s[20:23], s64 offen offset:1024 sc1\n"
"M 16,176,68\nR 170,186,45056\nR 68,238,50176\nR 70,238,54272\ns_waitcnt vmcnt(17)\nM 16,180,84\n"
"M 32,176,72\nR 172,186,49152\ns_waitcnt vmcnt(16)\nM 32,180,88\nbuffer_load_dwordx4 a[148:151], v252, s[20:23], s65 offen offset:1024 sc1\nM 48,176,76\n"
"R 174,186,53248\ns_add_u32 m0, s50, 98304\ns_waitcnt vmcnt(16)\nM 48,180,92\nG 8\nM 52,160,76\n"
"R 176,187,40960\nG 6\nv_mfma_f32_16x16x32_bf16 v[72:75], v[242:245], v[64:67], 0\ns_add_u32 s63, s53, 720896\nM 52,164,92\nG 3\n"
"v_mfma_f32_16x16x32_bf16 v[72:75], v[242:245], v[68:71], v[72:75]\nbuffer_load_dwordx4 a[152:155], v252, s[20:23], s66 offen offset:1024 sc1\nM 36,160,72\nR 178,187,45056\nM 36,164,88\nbuffer_load_dwordx4 v220, s[8:11], s63 offen lds\n"
"M 20,160,68\nR 180,187,49152\nM 20,164,84\nbuffer_load_dwordx4 a[156:159], v252, s[20:23], s67 offen offset:1024 sc1\nv_lshrrev_b32 v64, 1, v252\nM 4,160,64\n"
"R 182,187,53248\nv_mov_b32 v73, s46\ns_and_b32 s61, s40, 3\ns_lshl_b32 s61, s61, 7\ns_add_u32 s61, s61, s91\ns_add_u32 s61, s61, 655360\n"
"s_mov_b64 exec, 0xffff\nbuffer_store_dwordx2 v[72:73], v64, s[20:23], s61 offen sc1\ns_mov_b64 exec, -1\nM 4,164,80\nG 4\nM 8,168,64\n"
"R 160,184,57344\nM 8,172,80\nbuffer_load_dwordx4 a[160:163], v252, s[20:23], s64 offen offset:2048 sc1\nM 24,168,68\nR 162,184,61440\nbuffer_load_dwordx4 v221, s[8:11], s63 offen offset:1024 lds\n"
"M 24,172,84\nM 40,168,72\nR 164,188,0\nM 40,172,88\nbuffer_load_dwordx4 a[164:167], v252, s[20:23], s65 offen offset:2048 sc1\nM 56,168,76\n"
"R 166,188,4096\nM 56,172,92\ns_and_b32 s60, s41, 1\ns_lshl1_add_u32 s60, s60, s90\ns_lshl_b32 s60, s60, 5\nv_xor_b32 v238, s60, v184\n"
"G 4\nM 60,176,76\nR 168,185,57344\nv_and_b32 v238, 0xfffffbff, v238\nM 60,180,92\nbuffer_load_dwordx4 a[168:171], v252, s[20:23], s66 offen offset:2048 sc1\n"
"M 44,176,72\nR 170,185,61440\nR 64,238,0\nR 66,238,4096\nM 44,180,88\ns_add_u32 m0, s50, 106496\n"
"M 28,176,68\nR 172,189,0\nR 68,238,1024\nR 70,238,5120\nM 28,180,84\nbuffer_load_dwordx4 a[172:175], v252, s[20:23], s67 offen offset:2048 sc1\n"
"M 12,176,64\nR 174,189,4096\ns_add_u32 s63, s53, 786432\nM 12,180,80\ns_barrier\nG 8\n"
"s_waitcnt vmcnt(24)\nM 0,160,96\nR 176,186,57344\ns_waitcnt vmcnt(19)\nM 0,164,112\nbuffer_load_dwordx4 a[176:179], v252, s[20:23], s64 offen offset:3072 sc1\n"
"buffer_load_dwordx4 v220, s[8:11], s63 offen lds\nM 16,160,100\nR 178,186,61440\nG 6\nv_mfma_f32_16x16x32_bf16 v[72:75], v[242:245], v[64:67], 0\ns_waitcnt vmcnt(20)\n"
"M 16,164,116\nG 3\nv_mfma_f32_16x16x32_bf16 v[72:75], v[242:245], v[68:71], v[72:75]\nbuffer_load_dwordx4 v221, s[8:11], s63 offen offset:1024 lds\nM 32,160,104\nR 180,190,0\n"
"s_lshr_b32 s60, s45, 3\ns_lshl2_add_u32 s60, s60, s40\ns_lshl_b32 s60, s60, 8\nv_lshl_add_u32 v224, v252, 3, s60\nv_add_u32 v224, Lqw0+24576-LqPC, v224\nv_min_u32 v224, LqE-LqPC-4, v224\n"
"s_mov_b64 exec, 3\nglobal_load_dword v224, v224, s[98:99]\ns_mov_b64 exec, -1\ns_add_u32 s63, s53, 851968\ns_waitcnt vmcnt(21)\nM 32,164,120\n"
"buffer_load_dwordx4 a[180:183], v252, s[20:23], s65 offen offset:3072 sc1\nM 48,160,108\nR 182,190,4096\ns_add_u32 m0, s50, 114688\ns_waitcnt vmcnt(21)\nM 48,164,124\n"
"buffer_load_dwordx4 v220, s[8:11], s63 offen lds\nG 4\nv_lshrrev_b32 v64, 1, v252\nM 52,168,108\nR 160,187,57344\nv_mov_b32 v73, s46\n"
"s_and_b32 s61, s40, 1\ns_lshl_b32 s61, s61, 7\ns_add_u32 s61, s61, s91\ns_add_u32 s61, s61, 786432\ns_mov_b64 exec, 0xffff\nbuffer_store_dwordx2 v[72:73], v64, s[20:23], s61 offen sc1\n"
"s_mov_b64 exec, -1\nM 52,172,124\nbuffer_load_dwordx4 a[184:187], v252, s[20:23], s66 offen offset:3072 sc1\nM 36,168,104\nR 162,187,61440\nM 36,172,120\n"
"buffer_load_dwordx4 v221, s[8:11], s63 offen offset:1024 lds\nM 20,168,100\nR 164,191,0\nM 20,172,116\nbuffer_load_dwordx4 a[188:191], v252, s[20:23], s67 offen offset:3072 sc1\nM 4,168,96\n"
"R 166,191,4096\nM 4,172,112\nG 4\nM 8,176,96\nR 168,188,8192\nM 8,180,112\n"
"s_add_u32 s64, s70, 2109440\nbuffer_load_dwordx4 a[192:195], v252, s[20:23], s64 offen offset:0 sc1\nM 24,176,100\nR 170,188,12288\nM 24,180,116\nM 40,176,104\n"
"R 172,188,16384\nM 40,180,120\ns_add_u32 s65, s70, 2142208\nbuffer_load_dwordx4 a[196:199], v252, s[20:23], s65 offen offset:0 sc1\nM 56,176,108\nR 174,188,20480\n"
"M 56,180,124\nG 4\nM 60,160,108\nR 176,189,8192\nM 60,164,124\ns_add_u32 s66, s71, 2109440\n"
"buffer_load_dwordx4 a[200:203], v252, s[20:23], s66 offen offset:0 sc1\nM 44,160,104\nR 178,189,12288\ns_add_u32 s63, s53, 917504\nM 44,164,120\ns_add_u32 m0, s50, 0\n"
"M 28,160,100\nR 180,189,16384\nM 28,164,116\ns_add_u32 s67, s71, 2142208\nbuffer_load_dwordx4 a[204:207], v252, s[20:23], s67 offen offset:0 sc1\nM 12,160,96\n"
"R 182,189,20480\nM 12,164,112\ns_waitcnt vmcnt(17)\ns_barrier\nbuffer_load_dwordx4 v220, s[8:11], s63 offen lds\nG 4\n"
"M 0,168,128\nR 160,190,8192\nbuffer_load_dwordx4 v221, s[8:11], s63 offen offset:1024 lds\nM 0,172,144\nbuffer_load_dwordx4 a[208:211], v252, s[20:23], s64 offen offset:1024 sc1\ns_add_u32 m0, s50, 8192\n"
"s_add_u32 s63, s53, 983040\nbuffer_load_dwordx4 v220, s[8:11], s63 offen lds\nM 16,168,132\nR 162,190,12288\nbuffer_load_dwordx4 v221, s[8:11], s63 offen offset:1024 lds\nM 16,172,148\n"
"M 32,168,136\nR 164,190,16384\nM 32,172,152\nbuffer_load_dwordx4 a[212:215], v252, s[20:23], s65 offen offset:1024 sc1\nM 48,168,140\nR 166,190,20480\n"
"M 48,172,156\nG 4\nM 52,176,140\nR 168,191,8192\nM 52,180,156\nbuffer_load_dwordx4 a[216:219], v252, s[20:23], s66 offen offset:1024 sc1\n"
"M 36,176,136\nR 170,191,12288\nM 36,180,152\nM 20,176,132\nR 172,191,16384\nM 20,180,148\n"
"buffer_load_dwordx4 a[220:223], v252, s[20:23], s67 offen offset:1024 sc1\nM 4,176,128\nR 174,191,20480\nM 4,180,144\nG 4\nM 8,160,128\n"
"R 176,188,24576\nM 8,164,144\nbuffer_load_dwordx4 a[224:227], v252, s[20:23], s64 offen offset:2048 sc1\nM 24,160,132\nR 178,188,28672\nM 24,164,148\n"
"M 40,160,136\nR 180,188,32768\nM 40,164,152\nbuffer_load_dwordx4 a[228:231], v252, s[20:23], s65 offen offset:2048 sc1\nM 56,160,140\nR 182,188,36864\n"
"M 56,164,156\nG 4\nM 60,168,140\nR 160,189,24576\nM 60,172,156\nbuffer_load_dwordx4 a[232:235], v252, s[20:23], s66 offen offset:2048 sc1\n"
"M 44,168,136\nR 162,189,28672\nM 44,172,152\nM 28,168,132\nR 164,189,32768\nM 28,172,148\n"
"buffer_load_dwordx4 a[236:239], v252, s[20:23], s67 offen offset:2048 sc1\nM 12,168,128\nR 166,189,36864\nM 12,172,144\nG 4\nM 0,176,160\n"
"R 168,190,24576\ns_waitcnt vmcnt(25)\nM 0,180,176\nbuffer_load_dwordx4 a[240:243], v252, s[20:23], s64 offen offset:3072 sc1\nM 16,176,164\nR 170,190,28672\n"
"s_waitcnt vmcnt(22)\nM 16,180,180\nM 32,176,168\nR 172,190,32768\ns_waitcnt vmcnt(19)\nM 32,180,184\n"
"buffer_load_dwordx4 a[244:247], v252, s[20:23], s65 offen offset:3072 sc1\nM 48,176,172\nR 174,190,36864\ns_waitcnt vmcnt(18)\nM 48,180,188\nG 4\n"
"M 52,160,172\nR 176,191,24576\nM 52,164,188\nbuffer_load_dwordx4 a[248:251], v252, s[20:23], s66 offen offset:3072 sc1\nM 36,160,168\nR 178,191,28672\n"
"s_waitcnt vmcnt(10)\ns_barrier\nM 36,164,184\nM 20,160,164\nR 180,191,32768\nM 20,164,180\n"
"buffer_load_dwordx4 a[252:255], v252, s[20:23], s67 offen offset:3072 sc1\nM 4,160,160\nR 182,191,36864\nM 4,164,176\nG 4\nM 8,168,160\n"
"R 160,188,40960\nM 8,172,176\nM 24,168,164\nR 162,188,45056\nM 24,172,180\nM 40,168,168\n"
"R 164,188,49152\nM 40,172,184\nM 56,168,172\nR 166,188,53248\nM 56,172,188\nG 4\n"
"M 60,176,172\nR 168,189,40960\nM 60,180,188\ns_movk_i32 s63, 7168\nv_mad_u32_u24 v220, v228, s63, v229\nv_add_u32 v221, 3668992, v220\n"
"s_add_u32 m0, s50, 16384\ns_add_u32 s63, s54, 0\nbuffer_load_dwordx4 v220, s[12:15], s63 offen lds\nM 44,176,168\nR 170,189,45056\nM 44,180,184\n"
"M 28,176,164\nR 172,189,49152\nM 28,180,180\nbuffer_load_dwordx4 v221, s[12:15], s63 offen offset:1024 lds\nM 12,176,160\nR 174,189,53248\n"
"s_add_u32 m0, s50, 24576\nM 12,180,176\nG 4\nM 32,160,200\nR 176,190,40960\ns_add_u32 s63, s54, 229376\n"
"s_waitcnt vmcnt(11)\nM 32,164,216\nR 178,190,45056\nM 48,160,204\nR 180,190,49152\ns_waitcnt vmcnt(10)\n"
"M 48,164,220\nR 182,190,53248\nG 4\nM 52,168,204\nR 160,191,40960\nbuffer_load_dwordx4 v220, s[12:15], s63 offen lds\n"
"M 52,172,220\nR 162,191,45056\nM 36,168,200\nR 164,191,49152\nM 36,172,216\nR 166,191,53248\n"
"G 4\nM 40,176,200\nR 168,184,0\nbuffer_load_dwordx4 v221, s[12:15], s63 offen offset:1024 lds\nM 40,180,216\nR 170,184,4096\n"
"s_add_u32 m0, s50, 32768\nM 56,176,204\nR 172,184,8192\ns_add_u32 s63, s54, 128\nM 56,180,220\nR 174,184,12288\n"
"G 4\nM 60,160,204\nR 176,185,0\nbuffer_load_dwordx4 v220, s[12:15], s63 offen lds\nM 60,164,220\nR 178,185,4096\n"
"M 44,160,200\nR 180,185,8192\nM 44,164,216\nR 182,185,12288\nG 4\ns_waitcnt vmcnt(10)\n"
"M 32,168,232\nR 160,186,0\nbuffer_load_dwordx4 v221, s[12:15], s63 offen offset:1024 lds\ns_waitcnt vmcnt(7)\nM 32,172,248\nR 162,186,4096\n"
"s_add_u32 m0, s50, 40960\nM 48,168,236\nR 164,186,8192\ns_add_u32 s63, s54, 229504\ns_waitcnt vmcnt(6)\nM 48,172,252\n"
"R 166,186,12288\nG 4\nM 52,176,236\nR 168,187,0\nbuffer_load_dwordx4 v220, s[12:15], s63 offen lds\nM 52,180,252\n"
"R 170,187,4096\nM 36,176,232\nR 172,187,8192\nM 36,180,248\nR 174,187,12288\nG 4\n"
"M 40,160,232\nR 176,188,40960\nbuffer_load_dwordx4 v221, s[12:15], s63 offen offset:1024 lds\nM 40,164,248\nR 178,188,45056\nM 56,160,236\n"
"R 180,188,49152\nM 56,164,252\nR 182,188,53248\nG 4\nM 60,168,236\nR 160,189,40960\n"
"M 60,172,252\nR 162,189,45056\nM 44,168,232\nR 164,189,49152\nM 44,172,248\nR 166,189,53248\n"
"G 4\nM 0,176,192\nR 168,190,40960\nM 0,180,208\nR 170,190,45056\ns_mov_b32 m0, s51\n"
"M 16,176,196\nR 172,190,49152\nv_cvt_pk_bf16_f32 v32, v32, v33\nv_cvt_pk_bf16_f32 v33, v34, v35\nM 16,180,212\nR 174,190,53248\n"
"G 4\nv_cvt_pk_bf16_f32 v34, v36, v37\nM 20,160,196\nR 176,191,40960\nv_cvt_pk_bf16_f32 v35, v38, v39\nv_cvt_pk_bf16_f32 v36, v40, v41\n"
"M 20,164,212\nR 178,191,45056\nv_cvt_pk_bf16_f32 v37, v42, v43\nv_cvt_pk_bf16_f32 v38, v44, v45\nM 4,160,192\nR 180,191,49152\n"
"v_cvt_pk_bf16_f32 v39, v46, v47\nv_cvt_pk_bf16_f32 v40, v48, v49\nM 4,164,208\nR 182,191,53248\nG 4\nv_cvt_pk_bf16_f32 v41, v50, v51\n"
"M 8,168,192\nR 160,184,0\nv_cvt_pk_bf16_f32 v42, v52, v53\nv_cvt_pk_bf16_f32 v43, v54, v55\nM 8,172,208\nR 162,184,4096\n"
"v_cvt_pk_bf16_f32 v44, v56, v57\nv_cvt_pk_bf16_f32 v45, v58, v59\nM 24,168,196\nR 164,184,8192\nv_cvt_pk_bf16_f32 v46, v60, v61\nv_cvt_pk_bf16_f32 v47, v62, v63\n"
"M 24,172,212\nR 166,184,12288\nds_write_addtid_b32 v32 offset:0\nG 5\nM 28,176,196\nR 168,185,0\n"
"ds_write_addtid_b32 v33 offset:256\nM 28,180,212\nR 170,185,4096\nds_write_addtid_b32 v34 offset:512\nM 12,176,192\nR 172,185,8192\n"
"ds_write_addtid_b32 v35 offset:768\nds_write_addtid_b32 v36 offset:1024\nds_write_addtid_b32 v37 offset:1280\nds_write_addtid_b32 v38 offset:1536\nds_write_addtid_b32 v39 offset:1792\nds_write_addtid_b32 v40 offset:2048\n"
"ds_write_addtid_b32 v41 offset:2304\nds_write_addtid_b32 v42 offset:2560\nds_write_addtid_b32 v43 offset:2816\nds_write_addtid_b32 v44 offset:3072\nds_write_addtid_b32 v45 offset:3328\nds_write_addtid_b32 v46 offset:3584\n"
"ds_write_addtid_b32 v47 offset:3840\nM 12,180,208\nR 174,185,12288\nG 15\nM 0,160,224\nR 176,186,0\n"
"s_add_u32 m0, s50, 49152\nM 0,164,240\nR 178,186,4096\nG 3\ns_barrier\ns_add_u32 s63, s54, 458752\n"
"M 16,160,228\nR 180,186,8192\nbuffer_load_dwordx4 v220, s[12:15], s63 offen lds\nds_read2st64_b32 v[32:33], v225 offset0:0 offset1:1\nM 16,164,244\nR 182,186,12288\n"
"ds_read2st64_b32 v[34:35], v225 offset0:2 offset1:3\nG 6\nM 20,168,228\nR 160,187,0\nds_read2st64_b32 v[36:37], v225 offset0:4 offset1:5\nM 20,172,244\n"
"R 162,187,4096\nds_read2st64_b32 v[38:39], v225 offset0:6 offset1:7\nM 4,168,224\nR 164,187,8192\nbuffer_load_dwordx4 v221, s[12:15], s63 offen offset:1024 lds\nds_read2st64_b32 v[40:41], v225 offset0:8 offset1:9\n"
"M 4,172,240\nR 166,187,12288\ns_add_u32 m0, s50, 57344\ns_add_u32 s63, s54, 688128\nLpg7:\ns_cbranch_execz LretO0\n"
"ds_read2st64_b32 v[42:43], v225 offset0:10 offset1:11\nG 9\nM 8,176,224\nbuffer_load_dwordx4 v220, s[12:15], s63 offen lds\nds_read2st64_b32 v[44:45], v225 offset0:12 offset1:13\nM 8,180,240\n"
"ds_read2st64_b32 v[46:47], v225 offset0:14 offset1:15\nM 24,176,228\nM 24,180,244\nG 3\nM 28,160,228\nbuffer_load_dwordx4 v221, s[12:15], s63 offen offset:1024 lds\n"
"M 28,164,244\nM 12,160,224\nM 12,164,240\nG 0\ns_barrier\nv_mfma_f32_16x16x32_bf16 v[0:3], v[208:211], v[32:35], v[0:3]\n"
"v_mfma_f32_16x16x32_bf16 v[4:7], v[210:213], v[32:35], v[4:7]\nv_mfma_f32_16x16x32_bf16 v[8:11], v[208:211], v[36:39], v[8:11]\nv_mfma_f32_16x16x32_bf16 v[12:15], v[210:213], v[36:39], v[12:15]\nv_mfma_f32_16x16x32_bf16 v[16:19], v[208:211], v[40:43], v[16:19]\nv_mfma_f32_16x16x32_bf16 v[20:23], v[210:213], v[40:43], v[20:23]\nv_mfma_f32_16x16x32_bf16 v[24:27], v[208:211], v[44:47], v[24:27]\n"
"v_mfma_f32_16x16x32_bf16 v[28:31], v[210:213], v[44:47], v[28:31]\ns_lshl_b32 s84, s75, 11\ns_add_u32 s84, s84, s49\ns_add_u32 s84, s84, 10485760\ns_add_u32 s85, s84, 32768\nv_mov_b32 v238, s46\n"
"v_mov_b32 v239, s47\nv_cvt_pk_bf16_f32 v192, v0, v1\nv_cvt_pk_bf16_f32 v193, v2, v3\nv_cvt_pk_bf16_f32 v194, v4, v5\nv_cvt_pk_bf16_f32 v195, v6, v7\nbuffer_store_dwordx4 v[192:195], v252, s[20:23], s84 offen offset:0 sc1\n"
"v_cvt_pk_bf16_f32 v196, v8, v9\nv_cvt_pk_bf16_f32 v197, v10, v11\nv_cvt_pk_bf16_f32 v198, v12, v13\nv_cvt_pk_bf16_f32 v199, v14, v15\nbuffer_store_dwordx4 v[196:199], v252, s[20:23], s84 offen offset:1024 sc1\nv_cvt_pk_bf16_f32 v200, v16, v17\n"
"v_cvt_pk_bf16_f32 v201, v18, v19\nv_cvt_pk_bf16_f32 v202, v20, v21\nv_cvt_pk_bf16_f32 v203, v22, v23\nbuffer_store_dwordx4 v[200:203], v252, s[20:23], s85 offen offset:0 sc1\nv_cvt_pk_bf16_f32 v204, v24, v25\nv_cvt_pk_bf16_f32 v205, v26, v27\n"
"v_cvt_pk_bf16_f32 v206, v28, v29\nv_cvt_pk_bf16_f32 v207, v30, v31\nbuffer_store_dwordx4 v[204:207], v252, s[20:23], s85 offen offset:1024 sc1\ns_mov_b64 exec, 0\ns_cmp_eq_u32 s40, 1\ns_cbranch_scc1 Lpg9\n"
"s_cmp_eq_u32 s40, 2\ns_cbranch_scc1 Lpg10\ns_cmp_eq_u32 s40, 3\ns_cbranch_scc1 Lpg11\ns_branch Lpg8\nLretO0n:\n"
"s_mov_b64 exec, -1\ns_add_u32 s61, s79, 88219648\ns_lshl_b32 s62, s75, 3\ns_add_u32 s61, s61, s62\ns_waitcnt vmcnt(0)\ns_mov_b64 exec, 1\n"
"buffer_atomic_swap_x2 v[238:239], off, s[20:23], s61 sc1\ns_mov_b64 exec, -1\nLqw0:\nv_bfe_u32 v250, v252, 7, 1\nv_mul_u32_u24 v250, 2176, v250\nv_bfe_u32 v251, v252, 4, 3\n"
"v_lshl_add_u32 v250, v251, 3, v250\ns_lshl_b32 s60, s90, 6\nv_add_u32 v250, s60, v250\ns_sleep 3\nv_mov_b32 v248, 0\nv_mov_b32 v249, 0\n"
"s_add_u32 s61, s80, 88219648\ns_mov_b64 exec, 0xffff\nbuffer_atomic_or_x2 v[248:249], v250, s[20:23], s61 offen sc0 sc1\ns_mov_b64 exec, -1\ns_add_u32 m0, s50, 65536\ns_add_u32 s63, s54, 458880\n"
"buffer_load_dwordx4 v220, s[12:15], s63 offen lds\nbuffer_load_dwordx4 v221, s[12:15], s63 offen offset:1024 lds\ns_add_u32 m0, s50, 73728\ns_add_u32 s63, s54, 688256\nbuffer_load_dwordx4 v220, s[12:15], s63 offen lds\nbuffer_load_dwordx4 v221, s[12:15], s63 offen offset:1024 lds\n"
"s_add_u32 m0, s50, 81920\ns_add_u32 s63, s54, 917504\nbuffer_load_dwordx4 v220, s[12:15], s63 offen lds\nbuffer_load_dwordx4 v221, s[12:15], s63 offen offset:1024 lds\ns_add_u32 m0, s50, 90112\ns_add_u32 s63, s54, 1146880\n"
"buffer_load_dwordx4 v220, s[12:15], s63 offen lds\nbuffer_load_dwordx4 v221, s[12:15], s63 offen offset:1024 lds\ns_add_u32 m0, s50, 98304\ns_add_u32 s63, s54, 917632\nbuffer_load_dwordx4 v220, s[12:15], s63 offen lds\nbuffer_load_dwordx4 v221, s[12:15], s63 offen offset:1024 lds\n"
"s_waitcnt vmcnt(10)\nv_cmp_eq_u64 s[86:87], s[46:47], v[248:249]\ns_nop 1\ns_and_b32 s88, s86, 0xffff\ns_cmp_eq_u32 s88, 0xffff\ns_cbranch_scc1 Lq265\n"
"v_mov_b32 v246, 0\nv_mov_b32 v247, 0\ns_add_u32 s61, s80, 88219648\ns_mov_b64 exec, 0xffff\nbuffer_atomic_or_x2 v[246:247], v250, s[20:23], s61 offen sc0 sc1\ns_mov_b64 exec, -1\n"
"Lq265:\ns_add_u32 m0, s50, 106496\ns_add_u32 s63, s54, 1147008\nbuffer_load_dwordx4 v220, s[12:15], s63 offen lds\nbuffer_load_dwordx4 v221, s[12:15], s63 offen offset:1024 lds\ns_mov_b64 exec, 0\n"
"s_cmp_eq_u32 s40, 1\ns_cbranch_scc1 Lpg17\ns_cmp_eq_u32 s40, 2\ns_cbranch_scc1 LretW0z\ns_cmp_eq_u32 s40, 3\ns_cbranch_scc1 LretW0z\n"
"s_branch Lpg16\nLretW0z:\ns_mov_b64 exec, -1\nv_mov_b32 v230, 0\nv_mov_b32 v231, 0\nv_mov_b32 v232, 0\n"
"v_mov_b32 v233, 0\nv_dot2c_f32_bf16 v230, s76, v192\nv_dot2c_f32_bf16 v231, v192, v192\nv_dot2c_f32_bf16 v232, s76, v200\nv_dot2c_f32_bf16 v233, v200, v200\nv_dot2c_f32_bf16 v230, s76, v193\n"
"v_dot2c_f32_bf16 v231, v193, v193\nv_dot2c_f32_bf16 v232, s76, v201\nv_dot2c_f32_bf16 v233, v201, v201\nv_dot2c_f32_bf16 v230, s76, v194\nv_dot2c_f32_bf16 v231, v194, v194\nv_dot2c_f32_bf16 v232, s76, v202\n"
"v_dot2c_f32_bf16 v233, v202, v202\nv_dot2c_f32_bf16 v230, s76, v195\nv_dot2c_f32_bf16 v231, v195, v195\nv_dot2c_f32_bf16 v232, s76, v203\nv_dot2c_f32_bf16 v233, v203, v203\nv_dot2c_f32_bf16 v230, s76, v196\n"
"v_dot2c_f32_bf16 v231, v196, v196\nv_dot2c_f32_bf16 v232, s76, v204\nv_dot2c_f32_bf16 v233, v204, v204\nv_dot2c_f32_bf16 v230, s76, v197\nv_dot2c_f32_bf16 v231, v197, v197\nv_dot2c_f32_bf16 v232, s76, v205\n"
"v_dot2c_f32_bf16 v233, v205, v205\nv_dot2c_f32_bf16 v230, s76, v198\nv_dot2c_f32_bf16 v231, v198, v198\nv_dot2c_f32_bf16 v232, s76, v206\nv_dot2c_f32_bf16 v233, v206, v206\nv_dot2c_f32_bf16 v230, s76, v199\n"
"v_dot2c_f32_bf16 v231, v199, v199\nv_dot2c_f32_bf16 v232, s76, v207\nv_dot2c_f32_bf16 v233, v207, v207\nv_lshrrev_b32 v234, 5, v252\nv_and_b32 v234, 24, v234\nv_bfe_u32 v235, s46, v234, 8\n"
"v_lshrrev_b32 v234, 1, v234\nv_and_b32 v236, 0xf0, v252\nv_add_u32 v234, v234, v236\nv_permlane32_swap_b32 v230, v231\nv_permlane32_swap_b32 v232, v233\nv_add_f32 v230, v230, v231\n"
"v_add_f32 v232, v232, v233\ns_nop 1\nv_permlane16_swap_b32 v230, v232\nv_add_f32 v230, v230, v232\ns_movk_i32 s62, 0xff\nv_bfi_b32 v230, s62, v235, v230\n"
"s_lshr_b32 s61, s49, 4\ns_lshl1_add_u32 s61, s53, s61\ns_add_u32 s61, s61, 82313216\nbuffer_store_dword v230, v234, s[20:23], s61 offen sc1\ns_cmp_eq_u32 s88, 0xffff\ns_cbranch_scc1 Lq266\n"
"s_waitcnt vmcnt(3)\nv_mov_b32 v248, v246\nv_mov_b32 v249, v247\nv_cmp_eq_u64 s[86:87], s[46:47], v[248:249]\ns_nop 1\ns_and_b32 s62, s86, 0xffff\n"
"s_cmp_eq_u32 s62, 0xffff\ns_cbranch_scc1 Lq257\nLq256:\nv_mov_b32 v248, 0\nv_mov_b32 v249, 0\ns_add_u32 s61, s80, 88219648\n"
"s_mov_b64 exec, 0xffff\nbuffer_atomic_or_x2 v[248:249], v250, s[20:23], s61 offen sc0 sc1\ns_mov_b64 exec, -1\ns_waitcnt vmcnt(0)\nv_cmp_eq_u64 s[86:87], s[46:47], v[248:249]\ns_nop 1\n"
"s_and_b32 s62, s86, 0xffff\ns_cmp_eq_u32 s62, 0xffff\ns_cbranch_scc1 Lq257\ns_sub_u32 s48, s48, 1\ns_cmp_gt_i32 s48, 0\ns_cbranch_scc1 Lq256\n"
"Lq257:\nLq266:\ns_add_u32 s64, s70, 10485760\nbuffer_load_dwordx4 a[0:3], v252, s[20:23], s64 offen offset:0 sc1\ns_add_u32 s65, s70, 10518528\nbuffer_load_dwordx4 a[4:7], v252, s[20:23], s65 offen offset:0 sc1\n"
"s_add_u32 s66, s71, 10485760\nbuffer_load_dwordx4 a[8:11], v252, s[20:23], s66 offen offset:0 sc1\ns_add_u32 s67, s71, 10518528\nbuffer_load_dwordx4 a[12:15], v252, s[20:23], s67 offen offset:0 sc1\nbuffer_load_dwordx4 a[16:19], v252, s[20:23], s64 offen offset:1024 sc1\nbuffer_load_dwordx4 a[20:23], v252, s[20:23], s65 offen offset:1024 sc1\n"
"buffer_load_dwordx4 a[24:27], v252, s[20:23], s66 offen offset:1024 sc1\nbuffer_load_dwordx4 a[28:31], v252, s[20:23], s67 offen offset:1024 sc1\ns_barrier\nR 160,184,16384\nR 162,184,20480\nR 164,184,24576\n"
"R 166,184,28672\nR 168,185,16384\nR 170,185,20480\nR 172,185,24576\nR 174,185,28672\nR 176,186,16384\n"
"R 178,186,20480\nR 180,186,24576\nR 182,186,28672\nbuffer_load_dwordx4 a[32:35], v252, s[20:23], s64 offen offset:2048 sc1\nbuffer_load_dwordx4 a[36:39], v252, s[20:23], s65 offen offset:2048 sc1\nbuffer_load_dwordx4 a[40:43], v252, s[20:23], s66 offen offset:2048 sc1\n"
"buffer_load_dwordx4 a[44:47], v252, s[20:23], s67 offen offset:2048 sc1\nbuffer_load_dwordx4 a[48:51], v252, s[20:23], s64 offen offset:3072 sc1\nbuffer_load_dwordx4 a[52:55], v252, s[20:23], s65 offen offset:3072 sc1\nbuffer_load_dwordx4 a[56:59], v252, s[20:23], s66 offen offset:3072 sc1\nbuffer_load_dwordx4 a[60:63], v252, s[20:23], s67 offen offset:3072 sc1\ns_waitcnt vmcnt(15) lgkmcnt(10)\n"
"v_mfma_f32_16x16x32_bf16 v[32:35], v[160:163], a[0:3], 0\ns_add_u32 s64, s70, 10489856\nbuffer_load_dwordx4 a[64:67], v252, s[20:23], s64 offen offset:0 sc1\ns_waitcnt vmcnt(12) lgkmcnt(8)\nM 32,164,16\nv_mfma_f32_16x16x32_bf16 v[48:51], v[160:163], a[4:7], 0\n"
"s_waitcnt vmcnt(11)\nM 48,164,20\nv_mfma_f32_16x16x32_bf16 v[64:67], v[160:163], a[8:11], 0\nv_lshrrev_b32 v238, 7, v252\ns_waitcnt vmcnt(10)\nM 64,164,24\n"
"v_lshlrev_b32 v238, 9, v238\nv_mfma_f32_16x16x32_bf16 v[80:83], v[160:163], a[12:15], 0\ns_add_u32 s65, s70, 10522624\nbuffer_load_dwordx4 a[68:71], v252, s[20:23], s65 offen offset:0 sc1\ns_waitcnt vmcnt(10)\nM 80,164,28\n"
"G 4\nv_and_b32 v239, 0x70, v252\nv_mfma_f32_16x16x32_bf16 v[84:87], v[168:171], a[12:15], 0\nR 160,187,16384\nM 84,172,28\ns_and_b32 s60, s40, 3\n"
"s_lshl_b32 s60, s60, 7\nv_add3_u32 v238, v238, v239, s60\nv_mfma_f32_16x16x32_bf16 v[68:71], v[168:171], a[8:11], 0\nR 162,187,20480\ns_lshl_b32 s61, s42, 13\ns_add_u32 s61, s61, 84279296\n"
"buffer_load_dwordx4 v[0:3], v238, s[20:23], s61 offen sc1\nM 68,172,24\ns_lshl_b32 s61, s42, 13\ns_add_u32 s61, s61, 84283392\nbuffer_load_dwordx4 v[4:7], v238, s[20:23], s61 offen sc1\nv_mfma_f32_16x16x32_bf16 v[52:55], v[168:171], a[4:7], 0\n"
"R 164,187,24576\nM 52,172,20\ns_add_u32 s66, s71, 10489856\nbuffer_load_dwordx4 a[72:75], v252, s[20:23], s66 offen offset:0 sc1\nv_bfe_u32 v239, v238, 2, 7\nv_mfma_f32_16x16x32_bf16 v[36:39], v[168:171], a[0:3], 0\n"
"R 166,187,28672\nM 36,172,16\nbuffer_load_dword v239, v239, s[32:35], s54 offen offset:0\nG 4\nv_mfma_f32_16x16x32_bf16 v[40:43], v[176:179], a[0:3], 0\nR 168,184,32768\n"
"M 40,180,16\nv_mfma_f32_16x16x32_bf16 v[56:59], v[176:179], a[4:7], 0\nR 170,184,36864\nM 56,180,20\ns_add_u32 s67, s71, 10522624\nbuffer_load_dwordx4 a[76:79], v252, s[20:23], s67 offen offset:0 sc1\n"
"v_mfma_f32_16x16x32_bf16 v[72:75], v[176:179], a[8:11], 0\nR 172,184,40960\nM 72,180,24\nv_mfma_f32_16x16x32_bf16 v[88:91], v[176:179], a[12:15], 0\nR 174,184,45056\nM 88,180,28\n"
"G 4\nv_mfma_f32_16x16x32_bf16 v[92:95], v[160:163], a[12:15], 0\nR 176,185,32768\nM 92,164,28\nbuffer_load_dwordx4 a[80:83], v252, s[20:23], s64 offen offset:1024 sc1\nv_mfma_f32_16x16x32_bf16 v[76:79], v[160:163], a[8:11], 0\n"
"R 178,185,36864\nM 76,164,24\nv_mfma_f32_16x16x32_bf16 v[60:63], v[160:163], a[4:7], 0\nR 180,185,40960\nM 60,164,20\nv_mfma_f32_16x16x32_bf16 v[44:47], v[160:163], a[0:3], 0\n"
"R 182,185,45056\nM 44,164,16\nG 4\nv_mfma_f32_16x16x32_bf16 v[96:99], v[168:171], a[0:3], 0\nR 160,186,32768\nM 96,172,16\n"
"buffer_load_dwordx4 a[84:87], v252, s[20:23], s65 offen offset:1024 sc1\nv_mfma_f32_16x16x32_bf16 v[112:115], v[168:171], a[4:7], 0\nR 162,186,36864\nM 112,172,20\nv_mfma_f32_16x16x32_bf16 v[128:131], v[168:171], a[8:11], 0\nR 164,186,40960\n"
"M 128,172,24\nbuffer_load_dwordx4 a[88:91], v252, s[20:23], s66 offen offset:1024 sc1\nv_mfma_f32_16x16x32_bf16 v[144:147], v[168:171], a[12:15], 0\nR 166,186,45056\nM 144,172,28\ns_barrier\n"
"G 4\nv_mfma_f32_16x16x32_bf16 v[148:151], v[176:179], a[12:15], 0\nR 168,187,32768\nM 148,180,28\nv_mfma_f32_16x16x32_bf16 v[132:135], v[176:179], a[8:11], 0\nR 170,187,36864\n"
"M 132,180,24\nbuffer_load_dwordx4 a[92:95], v252, s[20:23], s67 offen offset:1024 sc1\nv_mfma_f32_16x16x32_bf16 v[116:119], v[176:179], a[4:7], 0\nR 172,187,40960\nM 116,180,20\nv_mfma_f32_16x16x32_bf16 v[100:103], v[176:179], a[0:3], 0\n"
"R 174,187,45056\nM 100,180,16\nG 4\nv_mfma_f32_16x16x32_bf16 v[104:107], v[160:163], a[0:3], 0\nR 176,184,49152\nv_lshrrev_b32 v242, 7, v252\n"
"M 104,164,16\nbuffer_load_dwordx4 a[96:99], v252, s[20:23], s64 offen offset:2048 sc1\nv_mfma_f32_16x16x32_bf16 v[120:123], v[160:163], a[4:7], 0\nR 178,184,53248\nv_lshlrev_b32 v242, 9, v242\ns_waitcnt vmcnt(8)\n"
"v_min3_u32 v240, v1, v3, v5\nM 120,164,20\nv_and_b32 v243, 0x70, v252\nv_min3_u32 v240, v240, v7, v7\nv_max3_u32 v241, v1, v3, v5\nv_mfma_f32_16x16x32_bf16 v[136:139], v[160:163], a[8:11], 0\n"
"R 180,184,57344\nv_max3_u32 v241, v241, v7, v7\nv_cmp_ne_u32 s[86:87], s46, v240\nM 136,164,24\ns_and_b32 s60, s40, 3\ns_lshl_b32 s60, s60, 7\n"
"v_add3_u32 v242, v242, v243, s60\nv_cmp_ne_u32 vcc, s46, v241\nv_mfma_f32_16x16x32_bf16 v[152:155], v[160:163], a[12:15], 0\nR 182,184,61440\ns_lshl_b32 s61, s42, 13\ns_add_u32 s61, s61, 84410368\n"
"buffer_load_dwordx4 v[16:19], v242, s[20:23], s61 offen sc1\ns_or_b64 s[86:87], s[86:87], vcc\ns_cbranch_scc1 Lq319\nLq320:\nM 152,164,28\ns_lshl_b32 s61, s42, 13\n"
"s_add_u32 s61, s61, 84414464\nbuffer_load_dwordx4 v[20:23], v242, s[20:23], s61 offen sc1\nG 4\nv_mfma_f32_16x16x32_bf16 v[156:159], v[168:171], a[12:15], 0\nR 160,185,49152\nv_add_f32 v0, v0, v4\n"
"M 156,172,28\nbuffer_load_dwordx4 a[100:103], v252, s[20:23], s65 offen offset:2048 sc1\nv_bfe_u32 v243, v242, 2, 7\nv_mfma_f32_16x16x32_bf16 v[140:143], v[168:171], a[8:11], 0\nR 162,185,53248\nv_add_f32 v2, v2, v6\n"
"M 140,172,24\nbuffer_load_dword v243, v243, s[32:35], s54 offen offset:128\nv_mfma_f32_16x16x32_bf16 v[124:127], v[168:171], a[4:7], 0\nR 164,185,57344\nv_add_f32_dpp v0, v0, v0 row_ror:8 row_mask:0xf bank_mask:0xf\nM 124,172,20\n"
"buffer_load_dwordx4 a[104:107], v252, s[20:23], s66 offen offset:2048 sc1\nv_mfma_f32_16x16x32_bf16 v[108:111], v[168:171], a[0:3], 0\nR 166,185,61440\nv_add_f32_dpp v2, v2, v2 row_ror:8 row_mask:0xf bank_mask:0xf\nM 108,172,16\nG 4\n"
"v_mov_b32 v4, v0\nv_mov_b32 v5, v2\nM 32,176,32\nR 168,186,49152\nv_permlane32_swap_b32 v4, v0\nM 32,180,48\n"
"v_permlane32_swap_b32 v5, v2\nv_add_f32 v0, v0, v4\nM 48,176,36\nR 170,186,53248\nv_add_f32 v2, v2, v5\nM 48,180,52\n"
"buffer_load_dwordx4 a[108:111], v252, s[20:23], s67 offen offset:2048 sc1\nM 64,176,40\nR 172,186,57344\nv_mov_b32 v4, v0\nM 64,180,56\nv_mov_b32 v5, v2\n"
"v_permlane16_swap_b32 v4, v0\nM 80,176,44\nR 174,186,61440\nv_permlane16_swap_b32 v5, v2\nM 80,180,60\ns_barrier\n"
"G 4\nM 84,160,44\nR 176,187,49152\nv_add_f32 v0, v0, v4\nM 84,164,60\nbuffer_load_dwordx4 a[112:115], v252, s[20:23], s64 offen offset:3072 sc1\n"
"M 68,160,40\nR 178,187,53248\nv_add_f32 v2, v2, v5\nM 68,164,56\nv_and_b32 v8, s77, v0\nv_sub_f32 v9, v0, v8\n"
"M 52,160,36\nR 180,187,57344\ns_waitcnt vmcnt(13)\nv_lshlrev_b32 v10, 16, v239\nM 52,164,52\nv_cvt_pk_bf16_f32 v4, v8, v9\n"
"v_cvt_pk_bf16_f32 v5, v8, v10\nM 36,160,32\nR 182,187,61440\nv_and_b32 v8, s77, v2\nM 36,164,48\nG 4\n"
"v_sub_f32 v9, v2, v8\nv_and_b32 v10, s77, v239\nM 40,168,32\nR 160,188,0\nv_cvt_pk_bf16_f32 v6, v8, v9\nM 40,172,48\n"
"buffer_load_dwordx4 a[116:119], v252, s[20:23], s65 offen offset:3072 sc1\nM 56,168,36\nR 162,188,4096\nv_cvt_pk_bf16_f32 v7, v8, v10\nM 56,172,52\nv_and_b32 v238, 0x1ff, v238\n"
"v_add_u32 v238, 157184, v238\nM 72,168,40\nR 164,188,8192\nds_write_b128 v238, v[4:7]\nM 72,172,56\nbuffer_load_dwordx4 a[120:123], v252, s[20:23], s66 offen offset:3072 sc1\n"
"M 88,168,44\nR 166,188,12288\nM 88,172,60\nG 5\nM 92,176,44\nR 168,189,0\n"
"M 92,180,60\nM 76,176,40\nR 170,189,4096\nM 76,180,56\nbuffer_load_dwordx4 a[124:127], v252, s[20:23], s67 offen offset:3072 sc1\nM 60,176,36\n"
"R 172,189,8192\nv_lshrrev_b32 v238, 7, v252\nM 60,180,52\nv_lshlrev_b32 v238, 9, v238\nM 44,176,32\nR 174,189,12288\n"
"s_waitcnt vmcnt(8)\nv_min3_u32 v244, v17, v19, v21\nv_min3_u32 v244, v244, v23, v23\nM 44,180,48\nG 4\nv_and_b32 v239, 0x70, v252\n"
"v_max3_u32 v245, v17, v19, v21\nv_max3_u32 v245, v245, v23, v23\nM 96,160,32\nR 176,190,0\ns_and_b32 s60, s40, 3\ns_lshl_b32 s60, s60, 7\n"
"v_add3_u32 v238, v238, v239, s60\nv_cmp_ne_u32 s[86:87], s46, v244\nM 96,164,48\ns_add_u32 s64, s70, 10493952\nbuffer_load_dwordx4 a[128:131], v252, s[20:23], s64 offen offset:0 sc1\nv_cmp_ne_u32 vcc, s46, v245\n"
"M 112,160,36\nR 178,190,4096\ns_lshl_b32 s61, s42, 13\ns_add_u32 s61, s61, 84541440\nbuffer_load_dwordx4 v[0:3], v238, s[20:23], s61 offen sc1\ns_or_b64 s[86:87], s[86:87], vcc\n"
"s_cbranch_scc1 Lq329\nLq330:\nM 112,164,52\ns_lshl_b32 s61, s42, 13\ns_add_u32 s61, s61, 84545536\nbuffer_load_dwordx4 v[4:7], v238, s[20:23], s61 offen sc1\n"
"v_add_f32 v16, v16, v20\nM 128,160,40\nR 180,190,8192\nv_add_f32 v18, v18, v22\nv_add_f32_dpp v16, v16, v16 row_ror:8 row_mask:0xf bank_mask:0xf\nM 128,164,56\n"
"s_add_u32 m0, s50, 114688\nv_bfe_u32 v239, v238, 2, 7\nv_add_f32_dpp v18, v18, v18 row_ror:8 row_mask:0xf bank_mask:0xf\nv_mov_b32 v20, v16\nM 144,160,44\nR 182,190,12288\n"
"s_add_u32 s63, s54, 1376256\nv_mov_b32 v21, v18\nM 144,164,60\nbuffer_load_dword v239, v239, s[32:35], s54 offen offset:256\ns_barrier\nG 4\n"
"v_permlane32_swap_b32 v20, v16\nM 148,168,44\nR 160,191,0\nv_permlane32_swap_b32 v21, v18\nv_add_f32 v16, v16, v20\nM 148,172,60\n"
"s_add_u32 s65, s70, 10526720\nbuffer_load_dwordx4 a[132:135], v252, s[20:23], s65 offen offset:0 sc1\nv_add_f32 v18, v18, v21\nM 132,168,40\nR 162,191,4096\nv_mov_b32 v20, v16\n"
"v_mov_b32 v21, v18\nM 132,172,56\nbuffer_load_dwordx4 v220, s[12:15], s63 offen lds\nM 116,168,36\nR 164,191,8192\nv_permlane16_swap_b32 v20, v16\n"
"M 116,172,52\ns_add_u32 s66, s71, 10493952\nbuffer_load_dwordx4 a[136:139], v252, s[20:23], s66 offen offset:0 sc1\nM 100,168,32\nR 166,191,12288\nv_permlane16_swap_b32 v21, v18\n"
"M 100,172,48\nG 4\nv_add_f32 v16, v16, v20\nv_add_f32 v18, v18, v21\nM 104,176,32\nR 168,188,16384\n"
"buffer_load_dwordx4 v221, s[12:15], s63 offen offset:1024 lds\nM 104,180,48\ns_add_u32 m0, s50, 0\ns_add_u32 s63, s54, 1605632\nbuffer_load_dwordx4 v220, s[12:15], s63 offen lds\nM 120,176,36\n"
"R 170,188,20480\nv_and_b32 v24, s77, v16\nM 120,180,52\ns_add_u32 s67, s71, 10526720\nbuffer_load_dwordx4 a[140:143], v252, s[20:23], s67 offen offset:0 sc1\nM 136,176,40\n"
"R 172,188,24576\nv_sub_f32 v25, v16, v24\nM 136,180,56\nbuffer_load_dwordx4 v221, s[12:15], s63 offen offset:1024 lds\nM 152,176,44\nR 174,188,28672\n"
"s_waitcnt vmcnt(17)\nv_lshlrev_b32 v26, 16, v243\nM 152,180,60\nG 4\nv_cvt_pk_bf16_f32 v20, v24, v25\nv_cvt_pk_bf16_f32 v21, v24, v26\n"
"M 156,160,44\nR 176,189,16384\nv_and_b32 v24, s77, v18\nM 156,164,60\nbuffer_load_dwordx4 a[144:147], v252, s[20:23], s64 offen offset:1024 sc1\nM 140,160,40\n"
"R 178,189,20480\nv_sub_f32 v25, v18, v24\nM 140,164,56\nv_and_b32 v26, s77, v243\nv_cvt_pk_bf16_f32 v22, v24, v25\nM 124,160,36\n"
"R 180,189,24576\nv_cvt_pk_bf16_f32 v23, v24, v26\nM 124,164,52\nv_and_b32 v242, 0x1ff, v242\nv_add_u32 v242, 157696, v242\nM 108,160,32\n"
"R 182,189,28672\nds_write_b128 v242, v[20:23]\nM 108,164,48\nG 5\nM 32,168,64\nR 160,190,16384\n"
"M 32,172,80\nbuffer_load_dwordx4 a[148:151], v252, s[20:23], s65 offen offset:1024 sc1\nM 48,168,68\nLpg8:\ns_cbranch_execz Lpg12\nR 162,190,20480\n"
"M 48,172,84\ns_add_u32 m0, s50, 8192\nM 64,168,72\nR 164,190,24576\ns_add_u32 s63, s54, 1376384\nM 64,172,88\n"
"buffer_load_dwordx4 a[152:155], v252, s[20:23], s66 offen offset:1024 sc1\nM 80,168,76\nR 166,190,28672\nM 80,172,92\nG 4\ns_barrier\n"
"M 84,176,76\nR 168,191,16384\nv_lshrrev_b32 v242, 7, v252\nM 84,180,92\nbuffer_load_dwordx4 v220, s[12:15], s63 offen lds\nv_lshlrev_b32 v242, 9, v242\n"
"M 68,176,72\nR 170,191,20480\ns_waitcnt vmcnt(12)\nv_min3_u32 v240, v1, v3, v5\nv_min3_u32 v240, v240, v7, v7\nM 68,180,88\n"
"buffer_load_dwordx4 a[156:159], v252, s[20:23], s67 offen offset:1024 sc1\nv_and_b32 v243, 0x70, v252\nv_max3_u32 v241, v1, v3, v5\nM 52,176,68\nR 172,191,24576\nv_max3_u32 v241, v241, v7, v7\n"
"v_cmp_ne_u32 s[86:87], s46, v240\nM 52,180,84\ns_and_b32 s60, s40, 1\ns_lshl_b32 s60, s60, 7\nv_add3_u32 v242, v242, v243, s60\nv_cmp_ne_u32 vcc, s46, v241\n"
"M 36,176,64\nR 174,191,28672\ns_lshl_b32 s61, s42, 13\ns_add_u32 s61, s61, 84672512\nbuffer_load_dwordx4 v[16:19], v242, s[20:23], s61 offen sc1\ns_or_b64 s[86:87], s[86:87], vcc\n"
"s_cbranch_scc1 Lq339\nLq340:\nM 36,180,80\ns_lshl_b32 s61, s42, 13\ns_add_u32 s61, s61, 84676608\nbuffer_load_dwordx4 v[20:23], v242, s[20:23], s61 offen sc1\n"
"G 4\nv_add_f32 v0, v0, v4\nM 40,160,64\nR 176,188,32768\nbuffer_load_dwordx4 v221, s[12:15], s63 offen offset:1024 lds\nv_add_f32 v2, v2, v6\n"
"M 40,164,80\nbuffer_load_dwordx4 a[160:163], v252, s[20:23], s64 offen offset:2048 sc1\ns_add_u32 s63, s54, 1605760\nv_bfe_u32 v243, v242, 2, 7\nv_add_f32_dpp v0, v0, v0 row_ror:8 row_mask:0xf bank_mask:0xf\nM 56,160,68\n"
"R 178,188,36864\ns_add_u32 m0, s50, 16384\nv_add_f32_dpp v2, v2, v2 row_ror:8 row_mask:0xf bank_mask:0xf\nM 56,164,84\nbuffer_load_dword v243, v243, s[32:35], s55 offen offset:0\nv_mov_b32 v4, v0\n"
"M 72,160,72\nR 180,188,40960\nbuffer_load_dwordx4 v220, s[12:15], s63 offen lds\nv_mov_b32 v5, v2\nM 72,164,88\nbuffer_load_dwordx4 v221, s[12:15], s63 offen offset:1024 lds\n"
"v_permlane32_swap_b32 v4, v0\nM 88,160,76\nR 182,188,45056\nv_permlane32_swap_b32 v5, v2\nv_add_f32 v0, v0, v4\nM 88,164,92\n"
"G 4\nv_add_f32 v2, v2, v5\nv_mov_b32 v4, v0\nv_mov_b32 v5, v2\nM 92,168,76\nR 160,189,32768\n"
"v_permlane16_swap_b32 v4, v0\nv_permlane16_swap_b32 v5, v2\nM 92,172,92\nbuffer_load_dwordx4 a[164:167], v252, s[20:23], s65 offen offset:2048 sc1\nv_add_f32 v0, v0, v4\nM 76,168,72\n"
"R 162,189,36864\nv_add_f32 v2, v2, v5\nv_and_b32 v8, s77, v0\nM 76,172,88\nv_sub_f32 v9, v0, v8\ns_waitcnt vmcnt(20)\n"
"v_lshlrev_b32 v10, 16, v239\nM 60,168,68\nR 164,189,40960\nv_cvt_pk_bf16_f32 v4, v8, v9\nM 60,172,84\nbuffer_load_dwordx4 a[168:171], v252, s[20:23], s66 offen offset:2048 sc1\n"
"M 44,168,64\nR 166,189,45056\nv_cvt_pk_bf16_f32 v5, v8, v10\nM 44,172,80\nG 4\nv_and_b32 v8, s77, v2\n"
"v_sub_f32 v9, v2, v8\nM 96,176,64\nR 168,190,32768\nv_and_b32 v10, s77, v239\nM 96,180,80\nv_cvt_pk_bf16_f32 v6, v8, v9\n"
"v_cvt_pk_bf16_f32 v7, v8, v10\nM 112,176,68\nR 170,190,36864\nv_and_b32 v238, 0x1ff, v238\nM 112,180,84\nbuffer_load_dwordx4 a[172:175], v252, s[20:23], s67 offen offset:2048 sc1\n"
"M 128,176,72\nR 172,190,40960\nv_add_u32 v238, 158208, v238\nM 128,180,88\ns_add_u32 m0, s50, 24576\nds_write_b128 v238, v[4:7]\n"
"M 144,176,76\nR 174,190,45056\ns_add_u32 s63, s54, 1835008\nM 144,180,92\ns_waitcnt vmcnt(15) lgkmcnt(1)\ns_barrier\n"
"M 148,160,76\nR 176,191,32768\nM 148,164,92\nbuffer_load_dwordx4 a[176:179], v252, s[20:23], s64 offen offset:3072 sc1\nM 132,160,72\nR 178,191,36864\n"
"M 132,164,88\nbuffer_load_dwordx4 v220, s[12:15], s63 offen lds\nM 116,160,68\nR 180,191,40960\nM 116,164,84\nbuffer_load_dwordx4 v221, s[12:15], s63 offen offset:1024 lds\n"
"M 100,160,64\nR 182,191,45056\ns_add_u32 m0, s50, 32768\nM 100,164,80\nG 4\nM 104,168,64\n"
"R 160,188,49152\ns_add_u32 s63, s54, 2064384\nM 104,172,80\nbuffer_load_dwordx4 a[180:183], v252, s[20:23], s65 offen offset:3072 sc1\nM 120,168,68\nR 162,188,53248\n"
"M 120,172,84\nbuffer_load_dwordx4 v220, s[12:15], s63 offen lds\nM 136,168,72\nR 164,184,0\nM 136,172,88\nbuffer_load_dwordx4 a[184:187], v252, s[20:23], s66 offen offset:3072 sc1\n"
"M 152,168,76\nR 166,184,4096\ns_waitcnt vmcnt(14)\nv_min3_u32 v244, v17, v19, v21\nv_min3_u32 v244, v244, v23, v23\nM 152,172,92\n"
"G 4\nv_max3_u32 v245, v17, v19, v21\nv_max3_u32 v245, v245, v23, v23\nv_cmp_ne_u32 s[86:87], s46, v244\nM 156,176,76\nR 168,189,49152\n"
"buffer_load_dwordx4 v221, s[12:15], s63 offen offset:1024 lds\nv_cmp_ne_u32 vcc, s46, v245\nM 156,180,92\ns_or_b64 s[86:87], s[86:87], vcc\ns_cbranch_scc1 Lq349\nLq350:\n"
"v_add_f32 v16, v16, v20\nv_add_f32 v18, v18, v22\nM 140,176,72\nR 170,189,53248\nv_add_f32_dpp v16, v16, v16 row_ror:8 row_mask:0xf bank_mask:0xf\nv_add_f32_dpp v18, v18, v18 row_ror:8 row_mask:0xf bank_mask:0xf\n"
"M 140,180,88\nbuffer_load_dwordx4 a[188:191], v252, s[20:23], s67 offen offset:3072 sc1\nv_mov_b32 v20, v16\nM 124,176,68\nR 172,185,0\nv_mov_b32 v21, v18\n"
"M 124,180,84\nv_permlane32_swap_b32 v20, v16\nv_permlane32_swap_b32 v21, v18\nM 108,176,64\nR 174,185,4096\nv_add_f32 v16, v16, v20\n"
"M 108,180,80\nG 4\nv_add_f32 v18, v18, v21\nv_mov_b32 v20, v16\nM 32,160,96\nR 176,190,49152\n"
"v_mov_b32 v21, v18\nM 32,164,112\ns_add_u32 s64, s70, 10498048\nbuffer_load_dwordx4 a[192:195], v252, s[20:23], s64 offen offset:0 sc1\nM 48,160,100\nR 178,190,53248\n"
"v_permlane16_swap_b32 v20, v16\nM 48,164,116\nv_permlane16_swap_b32 v21, v18\nv_add_f32 v16, v16, v20\nM 64,160,104\nR 180,186,0\n"
"v_add_f32 v18, v18, v21\nM 64,164,120\ns_add_u32 m0, s50, 40960\nv_and_b32 v24, s77, v16\nv_sub_f32 v25, v16, v24\nM 80,160,108\n"
"R 182,186,4096\ns_add_u32 s63, s54, 2293760\nM 80,164,124\ns_waitcnt vmcnt(12)\ns_barrier\nG 4\n"
"M 84,168,108\nR 160,191,49152\nv_lshlrev_b32 v26, 16, v243\nM 84,172,124\ns_add_u32 s65, s70, 10530816\nbuffer_load_dwordx4 a[196:199], v252, s[20:23], s65 offen offset:0 sc1\n"
"M 68,168,104\nR 162,191,53248\nv_cvt_pk_bf16_f32 v20, v24, v25\nM 68,172,120\nbuffer_load_dwordx4 v220, s[12:15], s63 offen lds\nM 52,168,100\n"
"R 164,187,0\nv_cvt_pk_bf16_f32 v21, v24, v26\nM 52,172,116\ns_add_u32 s66, s71, 10498048\nbuffer_load_dwordx4 a[200:203], v252, s[20:23], s66 offen offset:0 sc1\nM 36,168,96\n"
"R 166,187,4096\nv_and_b32 v24, s77, v18\nM 36,172,112\nG 4\nv_sub_f32 v25, v18, v24\nv_and_b32 v26, s77, v243\n"
"M 40,176,96\nR 168,184,8192\nbuffer_load_dwordx4 v221, s[12:15], s63 offen offset:1024 lds\nM 40,180,112\ns_add_u32 m0, s50, 49152\ns_add_u32 s63, s54, 2523136\n"
"buffer_load_dwordx4 v220, s[12:15], s63 offen lds\nM 56,176,100\nR 170,184,12288\nv_cvt_pk_bf16_f32 v22, v24, v25\nM 56,180,116\ns_add_u32 s67, s71, 10530816\n"
"buffer_load_dwordx4 a[204:207], v252, s[20:23], s67 offen offset:0 sc1\nM 72,176,104\nR 172,184,16384\nv_cvt_pk_bf16_f32 v23, v24, v26\nM 72,180,120\nbuffer_load_dwordx4 v221, s[12:15], s63 offen offset:1024 lds\n"
"M 88,176,108\nR 174,184,20480\nv_and_b32 v242, 0x1ff, v242\nM 88,180,124\nv_add_u32 v242, 158720, v242\nds_write_b128 v242, v[20:23]\n"
"G 5\nM 92,160,108\nR 176,185,8192\nM 92,164,124\nbuffer_load_dwordx4 a[208:211], v252, s[20:23], s64 offen offset:1024 sc1\nM 76,160,104\n"
"R 178,185,12288\nM 76,164,120\ns_lshr_b32 s84, s49, 4\nM 60,160,100\nR 180,185,16384\nLq573:\n"
"s_lshr_b32 s60, s45, 3\ns_lshl2_add_u32 s60, s60, s40\ns_lshl_b32 s60, s60, 8\nv_lshl_add_u32 v224, v252, 3, s60\nv_add_u32 v224, Lq573+4096-LqPC, v224\nv_min_u32 v224, LqE-LqPC-4, v224\n"
"s_mov_b64 exec, 3\nglobal_load_dword v224, v224, s[98:99]\ns_mov_b64 exec, -1\nM 60,164,116\ns_add_u32 s84, s84, 82313216\nM 44,160,96\n"
"R 182,185,20480\nM 44,164,112\nG 5\nM 96,168,96\nR 160,186,8192\nM 96,172,112\n"
"buffer_load_dwordx4 a[212:215], v252, s[20:23], s65 offen offset:1024 sc1\nM 112,168,100\nR 162,186,12288\nM 112,172,116\nbuffer_load_dwordx4 v[0:3], v252, s[20:23], s84 offen offset:0 sc1\nM 128,168,104\n"
"R 164,186,16384\nM 128,172,120\nbuffer_load_dwordx4 a[216:219], v252, s[20:23], s66 offen offset:1024 sc1\nM 144,168,108\nR 166,186,20480\ns_add_u32 m0, s50, 57344\n"
"M 144,172,124\nbuffer_load_dwordx4 v[4:7], v252, s[20:23], s84 offen offset:1024 sc1\ns_waitcnt vmcnt(15) lgkmcnt(8)\ns_barrier\nG 4\nM 148,176,108\n"
"R 168,187,8192\ns_add_u32 s63, s54, 2752512\nM 148,180,124\nbuffer_load_dwordx4 v[8:11], v252, s[20:23], s84 offen offset:2048 sc1\nM 132,176,104\nR 170,187,12288\n"
"M 132,180,120\nbuffer_load_dwordx4 a[220:223], v252, s[20:23], s67 offen offset:1024 sc1\nM 116,176,100\nR 172,187,16384\nbuffer_load_dwordx4 v220, s[12:15], s63 offen lds\nM 116,180,116\n"
"buffer_load_dwordx4 v[12:15], v252, s[20:23], s84 offen offset:3072 sc1\nM 100,176,96\nR 174,187,20480\nM 100,180,112\nG 4\nM 104,160,96\n"
"R 176,184,24576\nbuffer_load_dwordx4 v221, s[12:15], s63 offen offset:1024 lds\nM 104,164,112\nbuffer_load_dwordx4 a[224:227], v252, s[20:23], s64 offen offset:2048 sc1\ns_add_u32 s63, s54, 2981888\nM 120,160,100\n"
"R 178,184,28672\ns_add_u32 m0, s50, 65536\nM 120,164,116\nbuffer_load_dwordx4 v220, s[12:15], s63 offen lds\nM 136,160,104\nR 180,184,32768\n"
"M 136,164,120\nbuffer_load_dwordx4 v221, s[12:15], s63 offen offset:1024 lds\nM 152,160,108\nR 182,184,36864\nM 152,164,124\nG 4\n"
"M 156,168,108\nR 160,185,24576\nM 156,172,124\nbuffer_load_dwordx4 a[228:231], v252, s[20:23], s65 offen offset:2048 sc1\nM 140,168,104\nR 162,185,28672\n"
"M 140,172,120\nM 124,168,100\nR 164,185,32768\nM 124,172,116\nbuffer_load_dwordx4 a[232:235], v252, s[20:23], s66 offen offset:2048 sc1\nM 108,168,96\n"
"R 166,185,36864\nM 108,172,112\nG 4\nM 32,176,128\nR 168,186,24576\nM 32,180,144\n"
"M 48,176,132\nR 170,186,28672\nM 48,180,148\nbuffer_load_dwordx4 a[236:239], v252, s[20:23], s67 offen offset:2048 sc1\nM 64,176,136\nR 172,186,32768\n"
"M 64,180,152\ns_add_u32 m0, s50, 73728\nM 80,176,140\nR 174,186,36864\ns_add_u32 s63, s54, 3211264\nM 80,180,156\n"
"s_waitcnt vmcnt(17)\ns_barrier\nG 4\nM 84,160,140\nR 176,187,24576\nM 84,164,156\n"
"buffer_load_dwordx4 a[240:243], v252, s[20:23], s64 offen offset:3072 sc1\nM 68,160,136\nR 178,187,28672\nM 68,164,152\nbuffer_load_dwordx4 v220, s[12:15], s63 offen lds\nM 52,160,132\n"
"R 180,187,32768\nM 52,164,148\nbuffer_load_dwordx4 v221, s[12:15], s63 offen offset:1024 lds\nM 36,160,128\nR 182,187,36864\ns_add_u32 m0, s50, 81920\n"
"M 36,164,144\nG 4\nM 40,168,128\nR 160,184,40960\ns_add_u32 s63, s54, 3440640\nM 40,172,144\n"
"buffer_load_dwordx4 a[244:247], v252, s[20:23], s65 offen offset:3072 sc1\nM 56,168,132\nR 162,184,45056\nM 56,172,148\nbuffer_load_dwordx4 v220, s[12:15], s63 offen lds\nM 72,168,136\n"
"R 164,184,49152\nM 72,172,152\nbuffer_load_dwordx4 a[248:251], v252, s[20:23], s66 offen offset:3072 sc1\nM 88,168,140\nR 166,184,53248\ns_mov_b32 s85, 0x0c0c0400\n"
"s_waitcnt vmcnt(19)\nv_perm_b32 v16, v1, v0, s85\nM 88,172,156\nG 4\nv_perm_b32 v20, v3, v2, s85\nv_lshl_or_b32 v16, v20, 16, v16\n"
"s_waitcnt vmcnt(17)\nv_perm_b32 v17, v5, v4, s85\nM 92,176,140\nR 168,185,40960\nbuffer_load_dwordx4 v221, s[12:15], s63 offen offset:1024 lds\nv_perm_b32 v21, v7, v6, s85\n"
"M 92,180,156\nv_lshl_or_b32 v17, v21, 16, v17\ns_waitcnt vmcnt(17)\nv_perm_b32 v18, v9, v8, s85\nv_perm_b32 v22, v11, v10, s85\nM 76,176,136\n"
"R 170,185,45056\nv_lshl_or_b32 v18, v22, 16, v18\ns_waitcnt vmcnt(14)\nv_perm_b32 v19, v13, v12, s85\nM 76,180,152\nbuffer_load_dwordx4 a[252:255], v252, s[20:23], s67 offen offset:3072 sc1\n"
"v_perm_b32 v23, v15, v14, s85\nM 60,176,132\nR 172,185,49152\nv_lshl_or_b32 v19, v23, 16, v19\nv_min3_u32 v230, v16, v17, v18\nM 60,180,148\n"
"v_min3_u32 v230, v230, v19, v19\nv_max3_u32 v231, v16, v17, v18\nv_max3_u32 v231, v231, v19, v19\nM 44,176,128\nR 174,185,53248\nv_cmp_ne_u32 s[86:87], s46, v230\n"
"v_cmp_ne_u32 vcc, s46, v231\nM 44,180,144\ns_or_b64 s[86:87], s[86:87], vcc\ns_cbranch_scc1 Lq317\nLq318:\nG 4\n"
"v_and_b32 v0, 0xffffff00, v0\nM 32,160,160\nR 176,186,40960\nv_and_b32 v1, 0xffffff00, v1\nv_and_b32 v2, 0xffffff00, v2\nM 32,164,176\n"
"v_and_b32 v3, 0xffffff00, v3\nv_and_b32 v4, 0xffffff00, v4\nv_and_b32 v5, 0xffffff00, v5\nM 48,160,164\nR 178,186,45056\nv_and_b32 v6, 0xffffff00, v6\n"
"v_and_b32 v7, 0xffffff00, v7\nM 48,164,180\ns_add_u32 m0, s50, 90112\nv_and_b32 v8, 0xffffff00, v8\nv_and_b32 v9, 0xffffff00, v9\nv_and_b32 v10, 0xffffff00, v10\n"
"M 64,160,168\nR 180,186,49152\nv_and_b32 v11, 0xffffff00, v11\nv_and_b32 v12, 0xffffff00, v12\nM 64,164,184\ns_add_u32 s63, s54, 1835136\n"
"v_and_b32 v13, 0xffffff00, v13\nv_and_b32 v14, 0xffffff00, v14\nv_and_b32 v15, 0xffffff00, v15\nM 80,160,172\nR 182,186,53248\nv_add_f32 v0, v0, v8\n"
"v_add_f32 v1, v1, v9\nM 80,164,188\ns_waitcnt vmcnt(11)\ns_barrier\nG 4\nv_add_f32 v2, v2, v10\n"
"M 84,168,172\nR 160,187,40960\nv_add_f32 v3, v3, v11\nv_add_f32 v4, v4, v12\nM 84,172,188\nbuffer_load_dwordx4 v220, s[12:15], s63 offen lds\n"
"v_add_f32 v5, v5, v13\nM 68,168,168\nR 162,187,45056\nv_add_f32 v6, v6, v14\nv_add_f32 v7, v7, v15\nM 68,172,184\n"
"v_add_f32 v0, v0, v4\nv_add_f32 v1, v1, v5\nv_add_f32 v2, v2, v6\nM 52,168,164\nR 164,187,49152\nv_bfe_u32 v250, v252, 5, 1\n"
"v_add_f32 v3, v3, v7\nv_mul_u32_u24 v250, 2560, v250\nM 52,172,180\nbuffer_load_dwordx4 v221, s[12:15], s63 offen offset:1024 lds\nv_permlane32_swap_b32 v0, v2\nv_and_b32 v251, 16, v252\n"
"M 36,168,160\nR 166,187,53248\ns_add_u32 m0, s50, 98304\nv_permlane32_swap_b32 v1, v3\nv_lshl_add_u32 v250, v251, 1, v250\nM 36,172,176\n"
"s_lshl_b32 s60, s90, 6\nv_add_u32 v250, s60, v250\nG 4\nv_add_f32 v0, v0, v2\nM 40,176,160\nR 168,184,57344\n"
"s_add_u32 s63, s54, 2064512\nv_add_f32 v1, v1, v3\nM 40,180,176\nbuffer_load_dwordx4 v220, s[12:15], s63 offen lds\nv_permlane16_swap_b32 v0, v1\nM 56,176,164\n"
"R 170,184,61440\nv_add_f32 v230, v0, v1\nv_mov_b32 v232, v230\nM 56,180,180\ns_nop 0\nv_permlane16_swap_b32 v230, v232\n"
"v_mov_b32 v231, v230\nv_mov_b32 v233, v232\nM 72,176,168\nR 172,188,0\nv_permlane32_swap_b32 v230, v231\nv_permlane32_swap_b32 v232, v233\n"
"M 72,180,184\nbuffer_load_dwordx4 v221, s[12:15], s63 offen offset:1024 lds\nv_mul_f32 v230, 0xba800000, v230\nM 88,176,172\nR 174,188,4096\ns_add_u32 m0, s50, 106496\n"
"M 88,180,188\nG 4\nv_mul_f32 v214, v230, v230\nv_mul_f32 v231, 0x3a800000, v231\nM 92,160,172\nR 176,185,57344\n"
"s_add_u32 s63, s54, 2293888\nM 92,164,188\nbuffer_load_dwordx4 v220, s[12:15], s63 offen lds\nM 76,160,168\nR 178,185,61440\nv_sub_f32 v231, v231, v214\n"
"M 76,164,184\nv_max_f32 v231, 0, v231\nv_add_f32 v231, 0x3727c5ac, v231\nM 60,160,164\nR 180,189,0\nLpg9:\n"
"s_cbranch_execz Lpg13\nv_and_b32 v208, s77, v230\nM 60,164,180\nv_rsq_f32 v234, v231\nM 44,160,160\nR 182,189,4096\n"
"v_mul_f32 v216, v231, v234\nM 44,164,176\nG 4\nv_sub_f32 v230, v230, v208\nv_cvt_pk_bf16_f32 v209, v230, v216\nM 32,168,192\n"
"R 160,186,57344\nv_cvt_pk_bf16_f32 v208, v208, v208\nM 32,172,208\nbuffer_load_dwordx4 v221, s[12:15], s63 offen offset:1024 lds\nM 48,168,196\nR 162,186,61440\n"
"s_add_u32 s63, s54, 2523264\nM 48,172,212\ns_add_u32 m0, s50, 114688\ns_mov_b64 s[60:61], 0xffff\nv_cndmask_b32 v208, 0, v208, s[60:61]\nv_mul_f32 v214, 0xc0135761, v234\n"
"M 64,168,200\nR 164,190,0\nv_mul_f32 v215, v234, v234\nM 64,172,216\nbuffer_load_dwordx4 v220, s[12:15], s63 offen lds\nM 80,168,204\n"
"R 166,190,4096\nv_mul_f32 v215, v215, v214\nM 80,172,220\ns_waitcnt vmcnt(8)\ns_barrier\nG 4\n"
"M 84,176,204\nR 168,187,57344\nv_mul_f32 v215, 0x3d372713, v215\nM 84,180,220\nbuffer_load_dwordx4 v221, s[12:15], s63 offen offset:1024 lds\nM 68,176,200\n"
"R 170,187,61440\ns_add_u32 s63, s54, 2752640\nM 68,180,216\ns_add_u32 m0, s50, 0\ns_mov_b64 s[60:61], 0xffff\nv_cndmask_b32 v209, 0, v209, s[60:61]\n"
"v_mul_f32 v232, 0xba800000, v232\nM 52,176,196\nR 172,191,0\nv_mul_f32 v217, v232, v232\nM 52,180,212\nbuffer_load_dwordx4 v220, s[12:15], s63 offen lds\n"
"M 36,176,192\nR 174,191,4096\nv_mul_f32 v233, 0x3a800000, v233\nM 36,180,208\nG 4\nv_sub_f32 v233, v233, v217\n"
"v_max_f32 v233, 0, v233\nM 40,160,192\nR 176,188,8192\nv_add_f32 v233, 0x3727c5ac, v233\nM 40,164,208\nbuffer_load_dwordx4 v221, s[12:15], s63 offen offset:1024 lds\n"
"M 56,160,196\nR 178,188,12288\nv_and_b32 v212, s77, v232\nM 56,164,212\ns_add_u32 m0, s50, 8192\nv_rsq_f32 v235, v233\n"
"M 72,160,200\nR 180,188,16384\nv_mul_f32 v219, v233, v235\nM 72,164,216\ns_add_u32 s63, s54, 2982016\nv_sub_f32 v232, v232, v212\n"
"v_cvt_pk_bf16_f32 v213, v232, v219\nM 88,160,204\nR 182,188,20480\nv_cvt_pk_bf16_f32 v212, v212, v212\nM 88,164,220\ns_mov_b64 s[60:61], 0xffff\n"
"v_cndmask_b32 v212, 0, v212, s[60:61]\nG 4\nv_mul_f32 v217, 0xc0135761, v235\nM 92,168,204\nR 160,189,8192\nv_mul_f32 v218, v235, v235\n"
"M 92,172,220\nbuffer_load_dwordx4 v220, s[12:15], s63 offen lds\nM 76,168,200\nR 162,189,12288\nv_mul_f32 v218, v218, v217\nM 76,172,216\n"
"s_mov_b64 s[60:61], 0xffff\nv_cndmask_b32 v213, 0, v213, s[60:61]\nv_mul_f32 v218, 0x3d372713, v218\nM 60,168,196\nR 164,189,16384\nM 60,172,212\n"
"buffer_load_dwordx4 v221, s[12:15], s63 offen offset:1024 lds\nM 44,168,192\nR 166,189,20480\ns_mov_b32 s60, 157184\nv_bfe_u32 v246, v252, 4, 4\nv_lshl_add_u32 v246, v246, 3, s60\n"
"M 44,172,208\nds_read_b64 v[238:239], v246 offset:0\nG 5\nM 32,176,224\nR 168,190,8192\nds_read_b64 v[240:241], v246 offset:128\n"
"M 32,180,240\nds_read_b64 v[242:243], v246 offset:256\ns_add_u32 m0, s50, 16384\nM 48,176,228\nR 170,190,12288\nds_read_b64 v[244:245], v246 offset:384\n"
"M 48,180,244\nds_read_b64 v[246:247], v246 offset:0\ns_add_u32 s63, s54, 3211392\nM 64,176,232\nR 172,190,16384\nM 64,180,248\n"
"buffer_load_dwordx4 v220, s[12:15], s63 offen lds\nM 80,176,236\nR 174,190,20480\ns_waitcnt vmcnt(13)\nM 80,180,252\ns_waitcnt vmcnt(9) lgkmcnt(2)\n"
"s_barrier\nM 84,160,236\nR 176,191,8192\nM 84,164,252\nbuffer_load_dwordx4 v221, s[12:15], s63 offen offset:1024 lds\nM 68,160,232\n"
"R 178,191,12288\ns_add_u32 s63, s54, 3440768\nM 68,164,248\ns_add_u32 m0, s50, 24576\nM 52,160,228\nR 180,191,16384\n"
"v_mfma_f32_16x16x32_bf16 v[32:35], v[238:241], v[208:211], v[32:35]\nM 52,164,244\nbuffer_load_dwordx4 v220, s[12:15], s63 offen lds\nM 36,160,224\nR 182,191,20480\nv_mfma_f32_16x16x32_bf16 v[36:39], v[240:243], v[208:211], v[36:39]\n"
"M 36,164,240\nG 4\nM 40,168,224\nR 160,188,24576\nv_mfma_f32_16x16x32_bf16 v[40:43], v[242:245], v[208:211], v[40:43]\nM 40,172,240\n"
"buffer_load_dwordx4 v221, s[12:15], s63 offen offset:1024 lds\nM 56,168,228\nR 162,188,28672\nv_mfma_f32_16x16x32_bf16 v[44:47], v[244:247], v[208:211], v[44:47]\ns_add_u32 m0, s50, 32768\ns_add_u32 s63, s55, 0\n"
"M 56,172,244\nbuffer_load_dwordx4 v222, s[12:15], s63 offen lds\nM 72,168,232\nR 164,188,32768\nv_mfma_f32_16x16x32_bf16 v[48:51], v[244:247], v[210:213], v[48:51]\nM 72,172,248\n"
"M 88,168,236\nR 166,188,36864\nv_mfma_f32_16x16x32_bf16 v[52:55], v[238:241], v[210:213], v[52:55]\nM 88,172,252\nG 4\nM 92,176,236\n"
"R 168,189,24576\nv_mfma_f32_16x16x32_bf16 v[56:59], v[240:243], v[210:213], v[56:59]\nM 92,180,252\nbuffer_load_dwordx4 v223, s[12:15], s63 offen offset:1024 lds\nM 76,176,232\nR 170,189,28672\n"
"v_mfma_f32_16x16x32_bf16 v[60:63], v[242:245], v[210:213], v[60:63]\nM 76,180,248\nM 60,176,228\nR 172,189,32768\nM 60,180,244\nM 44,176,224\n"
"R 174,189,36864\nM 44,180,240\ns_waitcnt vmcnt(10)\ns_barrier\nG 4\nM 96,160,128\n"
"R 176,190,24576\nM 96,164,144\ns_mov_b32 m0, s51\nM 112,160,132\nR 178,190,28672\ns_lshl2_add_u32 s60, s45, s40\n"
"v_lshlrev_b32 v224, 3, v252\ns_mul_i32 s60, s60, 7168\ns_bfm_b64 exec, 14, 0\nbuffer_load_dword v224, v224, s[16:19], s60 offen\ns_mov_b64 exec, -1\nM 112,164,148\n"
"ds_write_addtid_b32 v64 offset:0\nM 128,160,136\nR 180,190,32768\nM 128,164,152\nds_write_addtid_b32 v65 offset:256\nM 144,160,140\n"
"R 182,190,36864\nds_write_addtid_b32 v66 offset:512\nM 144,164,156\nds_write_addtid_b32 v67 offset:768\nds_write_addtid_b32 v68 offset:1024\nG 9\n"
"M 148,168,140\nR 160,191,24576\nds_write_addtid_b32 v69 offset:1280\nM 148,172,156\nds_write_addtid_b32 v70 offset:1536\nds_write_addtid_b32 v71 offset:1792\n"
"M 132,168,136\nR 162,191,28672\nds_write_addtid_b32 v72 offset:2048\nM 132,172,152\nds_write_addtid_b32 v73 offset:2304\nds_write_addtid_b32 v74 offset:2560\n"
"M 116,168,132\nR 164,191,32768\nds_write_addtid_b32 v75 offset:2816\nM 116,172,148\nds_write_addtid_b32 v76 offset:3072\nds_write_addtid_b32 v77 offset:3328\n"
"M 100,168,128\nR 166,191,36864\nds_write_addtid_b32 v78 offset:3584\nM 100,172,144\nds_write_addtid_b32 v79 offset:3840\nds_write_addtid_b32 v80 offset:4096\n"
"G 15\nM 104,176,128\nR 168,188,40960\nds_write_addtid_b32 v81 offset:4352\nM 104,180,144\nds_write_addtid_b32 v82 offset:4608\n"
"ds_write_addtid_b32 v83 offset:4864\nM 120,176,132\nR 170,188,45056\nds_write_addtid_b32 v84 offset:5120\nM 120,180,148\nds_write_addtid_b32 v85 offset:5376\n"
"ds_write_addtid_b32 v86 offset:5632\nM 136,176,136\nR 172,188,49152\nds_write_addtid_b32 v87 offset:5888\nM 136,180,152\nds_write_addtid_b32 v88 offset:6144\n"
"ds_write_addtid_b32 v89 offset:6400\nM 152,176,140\nR 174,188,53248\nds_write_addtid_b32 v90 offset:6656\nM 152,180,156\nds_write_addtid_b32 v91 offset:6912\n"
"ds_write_addtid_b32 v92 offset:7168\nG 15\nM 156,160,140\nR 176,189,40960\nds_write_addtid_b32 v93 offset:7424\nM 156,164,156\n"
"ds_write_addtid_b32 v94 offset:7680\nds_write_addtid_b32 v95 offset:7936\nM 140,160,136\nR 178,189,45056\ns_add_u32 m0, s50, 40960\ns_add_u32 s63, s55, 458752\n"
"M 140,164,152\nbuffer_load_dwordx4 v222, s[12:15], s63 offen lds\nM 124,160,132\nR 180,189,49152\ns_waitcnt vmcnt(8) lgkmcnt(2)\ns_barrier\n"
"M 124,164,148\nds_read2st64_b32 v[64:65], v225 offset0:0 offset1:1\nds_read2st64_b32 v[66:67], v225 offset0:2 offset1:3\nM 108,160,128\nR 182,189,53248\nds_read2st64_b32 v[68:69], v225 offset0:4 offset1:5\n"
"M 108,164,144\nds_read2st64_b32 v[70:71], v225 offset0:6 offset1:7\nds_read2st64_b32 v[72:73], v225 offset0:8 offset1:9\nM 96,168,160\nR 160,190,40960\nds_read2st64_b32 v[74:75], v225 offset0:10 offset1:11\n"
"M 96,172,176\nbuffer_load_dwordx4 v223, s[12:15], s63 offen offset:1024 lds\nds_read2st64_b32 v[76:77], v225 offset0:12 offset1:13\nM 112,168,164\nR 162,190,45056\nds_read2st64_b32 v[78:79], v225 offset0:14 offset1:15\n"
"M 112,172,180\ns_add_u32 m0, s50, 49152\nds_read2st64_b32 v[80:81], v225 offset0:16 offset1:17\nds_read2st64_b32 v[82:83], v225 offset0:18 offset1:19\nM 128,168,168\nR 164,190,49152\n"
"ds_read2st64_b32 v[84:85], v225 offset0:20 offset1:21\nG 14\nv_add_f32 v32, v32, v64\nM 128,172,184\ns_add_u32 s63, s55, 917504\nds_read2st64_b32 v[86:87], v225 offset0:22 offset1:23\n"
"ds_read2st64_b32 v[88:89], v225 offset0:24 offset1:25\nM 144,168,172\nR 166,190,53248\nds_read2st64_b32 v[90:91], v225 offset0:26 offset1:27\nv_add_f32 v33, v33, v65\nM 144,172,188\n"
"ds_read2st64_b32 v[92:93], v225 offset0:28 offset1:29\nds_read2st64_b32 v[94:95], v225 offset0:30 offset1:31\nG 15\nM 148,176,172\nR 168,191,40960\nv_add_f32 v34, v34, v66\n"
"v_add_f32 v35, v35, v67\nM 148,180,188\nbuffer_load_dwordx4 v222, s[12:15], s63 offen lds\nv_add_f32 v36, v36, v68\nM 132,176,168\nR 170,191,45056\n"
"v_add_f32 v37, v37, v69\nv_add_f32 v38, v38, v70\nM 132,180,184\nv_add_f32 v39, v39, v71\nv_add_f32 v40, v40, v72\nM 116,176,164\n"
"R 172,191,49152\nv_add_f32 v41, v41, v73\nM 116,180,180\nG 15\nv_add_f32 v42, v42, v74\nv_add_f32 v43, v43, v75\n"
"M 100,176,160\nR 174,191,53248\nv_add_f32 v44, v44, v76\nM 100,180,176\nG 7\nv_add_f32 v45, v45, v77\n"
"v_add_f32 v46, v46, v78\nM 104,160,160\nR 176,184,0\nv_add_f32 v47, v47, v79\nM 104,164,176\nbuffer_load_dwordx4 v223, s[12:15], s63 offen offset:1024 lds\n"
"M 120,160,164\nR 178,184,4096\nv_add_f32 v48, v48, v80\nM 120,164,180\ns_add_u32 m0, s50, 57344\nv_add_f32 v49, v49, v81\n"
"v_add_f32 v50, v50, v82\nM 136,160,168\nR 180,184,8192\nv_add_f32 v51, v51, v83\nM 136,164,184\ns_add_u32 s63, s55, 1376256\n"
"v_add_f32 v52, v52, v84\nv_add_f32 v53, v53, v85\nM 152,160,172\nR 182,184,12288\nv_add_f32 v54, v54, v86\nM 152,164,188\n"
"G 4\nv_add_f32 v55, v55, v87\nv_add_f32 v56, v56, v88\nM 156,168,172\nR 160,185,0\nv_add_f32 v57, v57, v89\n"
"M 156,172,188\nbuffer_load_dwordx4 v222, s[12:15], s63 offen lds\nM 140,168,168\nR 162,185,4096\nv_add_f32 v58, v58, v90\nM 140,172,184\n"
"v_add_f32 v59, v59, v91\nv_add_f32 v60, v60, v92\nM 124,168,164\nR 164,185,8192\nv_add_f32 v61, v61, v93\nM 124,172,180\n"
"v_add_f32 v62, v62, v94\nv_add_f32 v63, v63, v95\nM 108,168,160\nR 166,185,12288\nv_mul_f32 v230, v32, v32\nv_mul_f32 v231, v33, v33\n"
"M 108,172,176\ns_add_u32 s84, s73, 23068672\ns_add_u32 s85, s73, 23183360\nG 4\nv_mul_f32 v232, v34, v34\nM 96,176,192\n"
"R 168,186,0\nv_mul_f32 v233, v35, v35\nv_fma_f32 v230, v230, v215, v214\nM 96,180,208\nv_fma_f32 v231, v231, v215, v214\nv_fma_f32 v232, v232, v215, v214\n"
"v_fma_f32 v233, v233, v215, v214\nM 112,176,196\nR 170,186,4096\ns_lshl2_add_u32 s60, s45, s40\ns_waitcnt vmcnt(5)\nv_lshlrev_b32 v224, 3, v252\n"
"s_mul_i32 s60, s60, 7168\ns_bfm_b64 exec, 14, 14\nbuffer_load_dword v224, v224, s[16:19], s60 offen\ns_mov_b64 exec, -1\ns_barrier\nv_mul_f32 v230, v32, v230\n"
"M 112,180,212\nv_exp_f32 v230, v230\nv_mul_f32 v231, v33, v231\nM 128,176,200\nR 172,186,8192\nv_mul_f32 v232, v34, v232\n"
"v_mul_f32 v233, v35, v233\nM 128,180,216\nv_exp_f32 v231, v231\nv_exp_f32 v232, v232\nM 144,176,204\nR 174,186,12288\n"
"v_fma_f32 v230, v230, v216, v216\nv_exp_f32 v233, v233\nM 144,180,220\nG 4\nv_rcp_f32 v230, v230\nv_fma_f32 v231, v231, v216, v216\n"
"M 148,160,204\nR 176,187,0\nv_fma_f32 v232, v232, v216, v216\nv_fma_f32 v233, v233, v216, v216\nM 148,164,220\nbuffer_load_dwordx4 v223, s[12:15], s63 offen offset:1024 lds\n"
"v_rcp_f32 v231, v231\nM 132,160,200\nR 178,187,4096\nv_mul_f32 v230, v32, v230\nv_rcp_f32 v232, v232\nM 132,164,216\n"
"s_add_u32 m0, s50, 65536\nv_rcp_f32 v233, v233\nv_mul_f32 v231, v33, v231\nM 116,160,196\nR 180,187,8192\nv_mul_f32 v232, v34, v232\n"
"v_mul_f32 v233, v35, v233\nM 116,164,212\ns_add_u32 s63, s55, 1835008\nv_cvt_pk_bf16_f32 v32, v230, v231\nv_cvt_pk_bf16_f32 v33, v232, v233\nv_mul_f32 v234, v36, v36\n"
"M 100,160,192\nR 182,187,12288\nv_mul_f32 v235, v37, v37\nv_mul_f32 v236, v38, v38\nM 100,164,208\nG 4\n"
"v_mul_f32 v237, v39, v39\nv_fma_f32 v234, v234, v215, v214\nv_fma_f32 v235, v235, v215, v214\nM 104,168,192\nR 160,184,16384\nv_fma_f32 v236, v236, v215, v214\n"
"v_fma_f32 v237, v237, v215, v214\nM 104,172,208\nbuffer_load_dwordx4 v222, s[12:15], s63 offen lds\nv_mul_f32 v234, v36, v234\nM 120,168,196\nR 162,184,20480\n"
"v_mul_f32 v235, v37, v235\nv_mul_f32 v236, v38, v236\nM 120,172,212\nv_exp_f32 v234, v234\nv_mul_f32 v237, v39, v237\nM 136,168,200\n"
"R 164,184,24576\nv_fma_f32 v234, v234, v216, v216\nv_exp_f32 v235, v235\nM 136,172,216\nv_exp_f32 v236, v236\nv_exp_f32 v237, v237\n"
"M 152,168,204\nR 166,184,28672\nv_fma_f32 v235, v235, v216, v216\nv_fma_f32 v236, v236, v216, v216\nM 152,172,220\nG 4\n"
"v_rcp_f32 v234, v234\nv_fma_f32 v237, v237, v216, v216\nM 156,176,204\nR 168,185,16384\nv_mul_f32 v234, v36, v234\nv_rcp_f32 v235, v235\n"
"M 156,180,220\nbuffer_load_dwordx4 v223, s[12:15], s63 offen offset:1024 lds\nv_rcp_f32 v236, v236\nM 140,176,200\nR 170,185,20480\nv_mul_f32 v235, v37, v235\n"
"v_rcp_f32 v237, v237\nM 140,180,216\nv_mul_f32 v236, v38, v236\nv_mul_f32 v237, v39, v237\nv_cvt_pk_bf16_f32 v34, v234, v235\nM 124,176,196\n"
"R 172,185,24576\nv_cvt_pk_bf16_f32 v35, v236, v237\nv_mul_f32 v230, v40, v40\nM 124,180,212\nbuffer_store_dwordx4 v[32:35], v252, s[20:23], s84 offen offset:0 sc1\nv_mul_f32 v231, v41, v41\n"
"M 108,176,192\nR 174,185,28672\ns_mov_b32 s60, 157696\nv_bfe_u32 v246, v252, 4, 4\nv_lshl_add_u32 v246, v246, 3, s60\nv_mul_f32 v232, v42, v42\n"
"M 108,180,208\nds_read_b64 v[238:239], v246 offset:0\nG 5\nv_mul_f32 v233, v43, v43\nM 96,160,224\nR 176,186,16384\n"
"ds_read_b64 v[240:241], v246 offset:128\nv_fma_f32 v230, v230, v215, v214\nM 96,164,240\nds_read_b64 v[242:243], v246 offset:256\ns_add_u32 m0, s50, 73728\nv_fma_f32 v231, v231, v215, v214\n"
"M 112,160,228\nR 178,186,20480\nds_read_b64 v[244:245], v246 offset:384\nv_fma_f32 v232, v232, v215, v214\nM 112,164,244\nds_read_b64 v[246:247], v246 offset:0\n"
"s_add_u32 s63, s55, 2293760\nv_fma_f32 v233, v233, v215, v214\nM 128,160,232\nR 180,186,24576\nv_mul_f32 v230, v40, v230\nv_mul_f32 v231, v41, v231\n"
"M 128,164,248\nbuffer_load_dwordx4 v222, s[12:15], s63 offen lds\nv_mul_f32 v232, v42, v232\nM 144,160,236\nR 182,186,28672\nv_mul_f32 v233, v43, v233\n"
"v_exp_f32 v230, v230\nM 144,164,252\nG 9\nv_exp_f32 v231, v231\nv_exp_f32 v232, v232\nM 148,168,236\n"
"R 160,187,16384\nv_fma_f32 v230, v230, v216, v216\nv_exp_f32 v233, v233\nM 148,172,252\nbuffer_load_dwordx4 v223, s[12:15], s63 offen offset:1024 lds\nv_fma_f32 v231, v231, v216, v216\n"
"M 132,168,232\nR 162,187,20480\ns_add_u32 s63, s55, 2752512\nv_fma_f32 v232, v232, v216, v216\nM 132,172,248\ns_add_u32 m0, s50, 81920\n"
"v_rcp_f32 v230, v230\nv_fma_f32 v233, v233, v216, v216\nM 116,168,228\nR 164,187,24576\nG 9\nv_mfma_f32_16x16x32_bf16 v[96:99], v[238:241], v[208:211], v[96:99]\n"
"v_mul_f32 v230, v40, v230\nv_rcp_f32 v231, v231\nM 116,172,244\nbuffer_load_dwordx4 v222, s[12:15], s63 offen lds\nLpg10:\ns_cbranch_execz Lpg14\n"
"v_rcp_f32 v232, v232\nM 100,168,224\nR 166,187,28672\nG 9\nv_mfma_f32_16x16x32_bf16 v[100:103], v[240:243], v[208:211], v[100:103]\nv_mul_f32 v231, v41, v231\n"
"v_rcp_f32 v233, v233\nM 100,172,240\nG 4\nv_mul_f32 v232, v42, v232\nv_mul_f32 v233, v43, v233\nv_cvt_pk_bf16_f32 v40, v230, v231\n"
"M 104,176,224\nR 168,184,32768\nv_mfma_f32_16x16x32_bf16 v[104:107], v[242:245], v[208:211], v[104:107]\nv_cvt_pk_bf16_f32 v41, v232, v233\nv_mul_f32 v234, v44, v44\nM 104,180,240\n"
"buffer_load_dwordx4 v223, s[12:15], s63 offen offset:1024 lds\nv_mul_f32 v235, v45, v45\nM 120,176,228\nR 170,184,36864\nv_mfma_f32_16x16x32_bf16 v[108:111], v[244:247], v[208:211], v[108:111]\nv_mul_f32 v236, v46, v46\n"
"v_mul_f32 v237, v47, v47\nM 120,180,244\ns_add_u32 m0, s50, 90112\nv_fma_f32 v234, v234, v215, v214\nv_fma_f32 v235, v235, v215, v214\nv_fma_f32 v236, v236, v215, v214\n"
"M 136,176,232\nR 172,186,32768\nv_mfma_f32_16x16x32_bf16 v[112:115], v[244:247], v[210:213], v[112:115]\nv_fma_f32 v237, v237, v215, v214\nv_mul_f32 v234, v44, v234\nM 136,180,248\n"
"s_add_u32 s63, s55, 3211264\nv_exp_f32 v234, v234\nv_mul_f32 v235, v45, v235\nM 152,176,236\nR 174,186,36864\nv_mfma_f32_16x16x32_bf16 v[116:119], v[238:241], v[210:213], v[116:119]\n"
"v_mul_f32 v236, v46, v236\nv_mul_f32 v237, v47, v237\nM 152,180,252\nG 4\nv_exp_f32 v235, v235\nv_exp_f32 v236, v236\n"
"M 156,160,236\nR 176,185,32768\nv_mfma_f32_16x16x32_bf16 v[120:123], v[240:243], v[210:213], v[120:123]\nv_fma_f32 v234, v234, v216, v216\nv_exp_f32 v237, v237\nM 156,164,252\n"
"buffer_load_dwordx4 v222, s[12:15], s63 offen lds\nv_fma_f32 v235, v235, v216, v216\nM 140,160,232\nR 178,185,36864\nv_mfma_f32_16x16x32_bf16 v[124:127], v[242:245], v[210:213], v[124:127]\nv_fma_f32 v236, v236, v216, v216\n"
"v_fma_f32 v237, v237, v216, v216\nM 140,164,248\nv_rcp_f32 v234, v234\nv_rcp_f32 v235, v235\nM 124,160,228\nR 180,187,32768\n"
"v_mul_f32 v234, v44, v234\nv_rcp_f32 v236, v236\nM 124,164,244\nbuffer_load_dwordx4 v223, s[12:15], s63 offen offset:1024 lds\nv_rcp_f32 v237, v237\nM 108,160,224\n"
"R 182,187,36864\nv_mul_f32 v235, v45, v235\nv_mul_f32 v236, v46, v236\nM 108,164,240\ns_waitcnt vmcnt(12)\ns_barrier\n"
"G 4\nv_mul_f32 v237, v47, v237\nv_mfma_f32_16x16x32_bf16 v[0:3], v[168:171], a[0:3], 0\nR 160,184,40960\nv_cvt_pk_bf16_f32 v42, v234, v235\nv_cvt_pk_bf16_f32 v43, v236, v237\n"
"M 0,172,16\ns_mov_b32 m0, s51\nv_mul_f32 v230, v48, v48\nv_mul_f32 v231, v49, v49\nv_mul_f32 v232, v50, v50\nv_mfma_f32_16x16x32_bf16 v[16:19], v[168:171], a[4:7], 0\n"
"R 162,184,45056\nv_mul_f32 v233, v51, v51\nv_fma_f32 v230, v230, v218, v217\nM 16,172,20\nbuffer_store_dwordx4 v[40:43], v252, s[20:23], s84 offen offset:1024 sc1\nv_fma_f32 v231, v231, v218, v217\n"
"v_mfma_f32_16x16x32_bf16 v[8:11], v[168:171], a[8:11], 0\nR 164,186,40960\nv_fma_f32 v232, v232, v218, v217\nv_fma_f32 v233, v233, v218, v217\nM 8,172,24\nds_write_addtid_b32 v128 offset:0\n"
"v_mul_f32 v230, v48, v230\nv_mul_f32 v231, v49, v231\nv_mul_f32 v232, v50, v232\nv_mfma_f32_16x16x32_bf16 v[24:27], v[168:171], a[12:15], 0\nR 166,186,45056\nds_write_addtid_b32 v129 offset:256\n"
"v_mul_f32 v233, v51, v233\nv_exp_f32 v230, v230\nM 24,172,28\nds_write_addtid_b32 v130 offset:512\nG 7\nv_exp_f32 v231, v231\n"
"v_exp_f32 v232, v232\nv_mfma_f32_16x16x32_bf16 v[28:31], v[176:179], a[12:15], 0\nR 168,185,40960\nds_write_addtid_b32 v131 offset:768\nv_fma_f32 v230, v230, v219, v219\nv_exp_f32 v233, v233\n"
"M 28,180,28\nds_write_addtid_b32 v132 offset:1024\nv_rcp_f32 v230, v230\nv_fma_f32 v231, v231, v219, v219\nv_mfma_f32_16x16x32_bf16 v[12:15], v[176:179], a[8:11], 0\nR 170,185,45056\n"
"ds_write_addtid_b32 v133 offset:1280\nv_fma_f32 v232, v232, v219, v219\nv_fma_f32 v233, v233, v219, v219\nM 12,180,24\nds_write_addtid_b32 v134 offset:1536\nv_rcp_f32 v231, v231\n"
"v_rcp_f32 v232, v232\nv_mfma_f32_16x16x32_bf16 v[20:23], v[176:179], a[4:7], 0\nR 172,187,40960\nds_write_addtid_b32 v135 offset:1792\nv_mul_f32 v230, v48, v230\nv_rcp_f32 v233, v233\n"
"M 20,180,20\nds_write_addtid_b32 v136 offset:2048\nv_mul_f32 v231, v49, v231\nv_mul_f32 v232, v50, v232\nv_mul_f32 v233, v51, v233\nv_mfma_f32_16x16x32_bf16 v[4:7], v[176:179], a[0:3], 0\n"
"R 174,187,45056\nds_write_addtid_b32 v137 offset:2304\nv_cvt_pk_bf16_f32 v48, v230, v231\nv_cvt_pk_bf16_f32 v49, v232, v233\nM 4,180,16\nds_write_addtid_b32 v138 offset:2560\n"
"G 14\nv_mul_f32 v234, v52, v52\nv_mul_f32 v235, v53, v53\nv_mul_f32 v236, v54, v54\nM 0,160,32\nR 176,184,49152\n"
"ds_write_addtid_b32 v139 offset:2816\nv_mul_f32 v237, v55, v55\nv_fma_f32 v234, v234, v218, v217\nM 0,164,48\nds_write_addtid_b32 v140 offset:3072\nv_fma_f32 v235, v235, v218, v217\n"
"v_fma_f32 v236, v236, v218, v217\nv_fma_f32 v237, v237, v218, v217\nM 16,160,36\nR 178,184,53248\nds_write_addtid_b32 v141 offset:3328\nv_mul_f32 v234, v52, v234\n"
"v_mul_f32 v235, v53, v235\nM 16,164,52\nds_write_addtid_b32 v142 offset:3584\nv_exp_f32 v234, v234\nv_mul_f32 v236, v54, v236\nM 8,160,40\n"
"R 180,186,49152\nds_write_addtid_b32 v143 offset:3840\nv_mul_f32 v237, v55, v237\nv_exp_f32 v235, v235\nM 8,164,56\nds_write_addtid_b32 v144 offset:4096\n"
"v_exp_f32 v236, v236\nv_exp_f32 v237, v237\nM 24,160,44\nR 182,186,53248\nds_write_addtid_b32 v145 offset:4352\nv_fma_f32 v234, v234, v219, v219\n"
"v_fma_f32 v235, v235, v219, v219\nM 24,164,60\nds_write_addtid_b32 v146 offset:4608\nG 14\nv_rcp_f32 v234, v234\nv_fma_f32 v236, v236, v219, v219\n"
"M 28,168,44\nR 160,185,49152\nds_write_addtid_b32 v147 offset:4864\nv_fma_f32 v237, v237, v219, v219\nv_rcp_f32 v235, v235\nM 28,172,60\n"
"ds_write_addtid_b32 v148 offset:5120\nds_write_addtid_b32 v149 offset:5376\nds_write_addtid_b32 v150 offset:5632\nds_write_addtid_b32 v151 offset:5888\nds_write_addtid_b32 v152 offset:6144\nds_write_addtid_b32 v153 offset:6400\n"
"ds_write_addtid_b32 v154 offset:6656\nds_write_addtid_b32 v155 offset:6912\nds_write_addtid_b32 v156 offset:7168\nds_write_addtid_b32 v157 offset:7424\nds_write_addtid_b32 v158 offset:7680\nds_write_addtid_b32 v159 offset:7936\n"
"v_rcp_f32 v236, v236\nv_rcp_f32 v237, v237\nM 12,168,40\nR 162,185,53248\ns_add_u32 m0, s50, 98304\ns_add_u32 s63, s54, 256\n"
"v_mul_f32 v234, v52, v234\nM 12,172,56\nbuffer_load_dwordx4 v220, s[12:15], s63 offen lds\nv_mul_f32 v235, v53, v235\nM 20,168,36\nR 164,187,49152\n"
"s_waitcnt vmcnt(4) lgkmcnt(2)\ns_barrier\nv_mul_f32 v236, v54, v236\nM 20,172,52\nds_read2st64_b32 v[128:129], v225 offset0:0 offset1:1\nv_mul_f32 v237, v55, v237\n"
"v_cvt_pk_bf16_f32 v50, v234, v235\nv_cvt_pk_bf16_f32 v51, v236, v237\nM 4,168,32\nR 166,187,53248\nds_read2st64_b32 v[130:131], v225 offset0:2 offset1:3\nv_mul_f32 v230, v56, v56\n"
"v_mul_f32 v231, v57, v57\nM 4,172,48\nds_read2st64_b32 v[132:133], v225 offset0:4 offset1:5\nv_mul_f32 v232, v58, v58\nv_mul_f32 v233, v59, v59\nv_fma_f32 v230, v230, v218, v217\n"
"M 0,176,64\nR 168,184,57344\nds_read2st64_b32 v[134:135], v225 offset0:6 offset1:7\nv_fma_f32 v231, v231, v218, v217\nv_fma_f32 v232, v232, v218, v217\nM 0,180,80\n"
"buffer_load_dwordx4 v221, s[12:15], s63 offen offset:1024 lds\nds_read2st64_b32 v[136:137], v225 offset0:8 offset1:9\nv_fma_f32 v233, v233, v218, v217\nM 16,176,68\nR 170,184,61440\ns_add_u32 m0, s50, 106496\n"
"s_add_u32 s63, s54, 229632\nds_read2st64_b32 v[138:139], v225 offset0:10 offset1:11\nv_mul_f32 v230, v56, v230\nM 16,180,84\nbuffer_load_dwordx4 v220, s[12:15], s63 offen lds\nds_read2st64_b32 v[140:141], v225 offset0:12 offset1:13\n"
"v_mul_f32 v231, v57, v231\nM 8,176,72\nR 172,186,57344\nds_read2st64_b32 v[142:143], v225 offset0:14 offset1:15\nv_mul_f32 v232, v58, v232\nv_mul_f32 v233, v59, v233\n"
"M 8,180,88\nbuffer_store_dwordx4 v[48:51], v252, s[20:23], s85 offen offset:0 sc1\nds_read2st64_b32 v[144:145], v225 offset0:16 offset1:17\nv_exp_f32 v230, v230\nM 24,176,76\nR 174,186,61440\n"
"ds_read2st64_b32 v[146:147], v225 offset0:18 offset1:19\nv_fma_f32 v230, v230, v219, v219\nv_exp_f32 v231, v231\nM 24,180,92\nds_read2st64_b32 v[148:149], v225 offset0:20 offset1:21\nG 14\n"
"v_exp_f32 v232, v232\nv_exp_f32 v233, v233\nM 28,160,76\nR 176,185,57344\nds_read2st64_b32 v[150:151], v225 offset0:22 offset1:23\nv_fma_f32 v231, v231, v219, v219\n"
"v_fma_f32 v232, v232, v219, v219\nM 28,164,92\nbuffer_load_dwordx4 v221, s[12:15], s63 offen offset:1024 lds\nds_read2st64_b32 v[152:153], v225 offset0:24 offset1:25\nv_fma_f32 v233, v233, v219, v219\nM 12,160,72\n"
"R 178,185,61440\ns_add_u32 s63, s54, 459008\nds_read2st64_b32 v[154:155], v225 offset0:26 offset1:27\nv_rcp_f32 v230, v230\nM 12,164,88\ns_add_u32 m0, s50, 114688\n"
"ds_read2st64_b32 v[156:157], v225 offset0:28 offset1:29\nv_rcp_f32 v231, v231\nv_rcp_f32 v232, v232\nM 20,160,68\nR 180,187,57344\nds_read2st64_b32 v[158:159], v225 offset0:30 offset1:31\n"
"v_mul_f32 v230, v56, v230\nv_rcp_f32 v233, v233\nM 20,164,84\nbuffer_load_dwordx4 v220, s[12:15], s63 offen lds\nv_add_f32 v96, v96, v128\nv_mul_f32 v231, v57, v231\n"
"M 4,160,64\nR 182,187,61440\nv_mul_f32 v232, v58, v232\nv_add_f32 v97, v97, v129\nv_mul_f32 v233, v59, v233\nM 4,164,80\n"
"G 11\nv_cvt_pk_bf16_f32 v56, v230, v231\nv_cvt_pk_bf16_f32 v57, v232, v233\nv_add_f32 v98, v98, v130\nv_mul_f32 v234, v60, v60\nM 0,168,96\n"
"R 160,188,0\nv_mul_f32 v235, v61, v61\nv_add_f32 v99, v99, v131\nv_mul_f32 v236, v62, v62\nM 0,172,112\nbuffer_load_dwordx4 v221, s[12:15], s63 offen offset:1024 lds\n"
"v_add_f32 v100, v100, v132\nv_mul_f32 v237, v63, v63\nM 16,168,100\nR 162,188,4096\nv_fma_f32 v234, v234, v218, v217\nv_add_f32 v101, v101, v133\n"
"v_fma_f32 v235, v235, v218, v217\nM 16,172,116\ns_add_u32 m0, s50, 0\nv_fma_f32 v236, v236, v218, v217\nv_fma_f32 v237, v237, v218, v217\nv_add_f32 v102, v102, v134\n"
"v_mul_f32 v234, v60, v234\nM 8,168,104\nR 164,190,0\nv_mul_f32 v235, v61, v235\nv_add_f32 v103, v103, v135\nv_mul_f32 v236, v62, v236\n"
"M 8,172,120\ns_add_u32 s63, s54, 688384\nv_exp_f32 v234, v234\nv_add_f32 v104, v104, v136\nv_mul_f32 v237, v63, v237\nM 24,168,108\n"
"R 166,190,4096\nv_add_f32 v105, v105, v137\nv_add_f32 v106, v106, v138\nv_exp_f32 v235, v235\nM 24,172,124\nG 4\n"
"v_add_f32 v107, v107, v139\nv_add_f32 v108, v108, v140\nv_add_f32 v109, v109, v141\nv_exp_f32 v236, v236\nM 28,176,108\nR 168,189,0\n"
"v_add_f32 v110, v110, v142\nv_add_f32 v111, v111, v143\nv_exp_f32 v237, v237\nM 28,180,124\nbuffer_load_dwordx4 v220, s[12:15], s63 offen lds\nv_add_f32 v112, v112, v144\n"
"v_fma_f32 v234, v234, v219, v219\nM 12,176,104\nR 170,189,4096\nv_add_f32 v113, v113, v145\nv_add_f32 v114, v114, v146\nv_fma_f32 v235, v235, v219, v219\n"
"M 12,180,120\nv_add_f32 v115, v115, v147\nv_add_f32 v116, v116, v148\nv_add_f32 v117, v117, v149\nv_fma_f32 v236, v236, v219, v219\nM 20,176,100\n"
"R 172,191,0\nv_add_f32 v118, v118, v150\nv_add_f32 v119, v119, v151\nM 20,180,116\nbuffer_load_dwordx4 v221, s[12:15], s63 offen offset:1024 lds\nv_add_f32 v120, v120, v152\n"
"v_add_f32 v121, v121, v153\nv_add_f32 v122, v122, v154\nv_add_f32 v123, v123, v155\nv_add_f32 v124, v124, v156\nv_add_f32 v125, v125, v157\nv_add_f32 v126, v126, v158\n"
"v_add_f32 v127, v127, v159\nM 4,176,96\nR 174,191,4096\ns_add_u32 m0, s50, 8192\nM 4,180,112\nG 4\n"
"v_rcp_f32 v234, v234\nM 0,160,128\nR 176,188,8192\ns_add_u32 s63, s54, 917760\nM 0,164,144\nbuffer_load_dwordx4 v220, s[12:15], s63 offen lds\n"
"M 16,160,132\nR 178,188,12288\nv_fma_f32 v237, v237, v219, v219\nM 16,164,148\nv_rcp_f32 v235, v235\nM 8,160,136\n"
"R 180,190,8192\nLq575:\ns_lshr_b32 s60, s45, 3\ns_lshl2_add_u32 s60, s60, s40\ns_lshl_b32 s60, s60, 8\nv_lshl_add_u32 v224, v252, 3, s60\n"
"v_add_u32 v224, Lq575+4096-LqPC, v224\nv_min_u32 v224, LqE-LqPC-4, v224\ns_mov_b64 exec, 3\nglobal_load_dword v224, v224, s[98:99]\ns_mov_b64 exec, -1\nM 8,164,152\n"
"v_rcp_f32 v236, v236\nM 24,160,140\nR 182,190,12288\nv_mul_f32 v234, v60, v234\nM 24,164,156\nG 4\n"
"v_rcp_f32 v237, v237\nM 28,168,140\nR 160,189,8192\nv_mul_f32 v235, v61, v235\nM 28,172,156\nbuffer_load_dwordx4 v221, s[12:15], s63 offen offset:1024 lds\n"
"M 12,168,136\nR 162,189,12288\nv_mul_f32 v236, v62, v236\nM 12,172,152\ns_add_u32 m0, s50, 16384\nv_mul_f32 v237, v63, v237\n"
"v_cvt_pk_bf16_f32 v58, v234, v235\nM 20,168,132\nR 164,191,8192\nv_cvt_pk_bf16_f32 v59, v236, v237\nM 20,172,148\ns_add_u32 s63, s54, 1147136\n"
"M 4,168,128\nR 166,191,12288\nM 4,172,144\nG 4\nM 0,176,160\nR 168,188,16384\n"
"M 0,180,176\nbuffer_load_dwordx4 v220, s[12:15], s63 offen lds\nM 16,176,164\nR 170,188,20480\nM 16,180,180\nbuffer_store_dwordx4 v[56:59], v252, s[20:23], s85 offen offset:1024 sc1\n"
"M 8,176,168\nR 172,190,16384\nM 8,180,184\nbuffer_load_dwordx4 v221, s[12:15], s63 offen offset:1024 lds\nM 24,176,172\nR 174,190,20480\n"
"s_add_u32 m0, s50, 24576\nM 24,180,188\ns_add_u32 s63, s54, 1376512\nG 4\nM 28,160,172\nR 176,189,16384\n"
"s_waitcnt vmcnt(10)\ns_barrier\nv_mul_f32 v230, v96, v96\nM 28,164,188\nbuffer_load_dwordx4 v220, s[12:15], s63 offen lds\nv_mul_f32 v231, v97, v97\n"
"M 12,160,168\nR 178,189,20480\nv_mul_f32 v232, v98, v98\nv_mul_f32 v233, v99, v99\nM 12,164,184\ns_add_u32 s84, s73, 23070720\n"
"s_add_u32 s85, s73, 23185408\nv_fma_f32 v230, v230, v215, v214\nv_fma_f32 v231, v231, v215, v214\nM 20,160,164\nR 180,191,16384\nv_fma_f32 v232, v232, v215, v214\n"
"v_fma_f32 v233, v233, v215, v214\nM 20,164,180\nbuffer_load_dwordx4 v221, s[12:15], s63 offen offset:1024 lds\nv_mul_f32 v230, v96, v230\nM 4,160,160\nR 182,191,20480\n"
"s_mov_b32 s60, 158720\nv_bfe_u32 v242, v252, 4, 4\nv_lshl_add_u32 v242, v242, 3, s60\nv_mul_f32 v231, v97, v231\nM 4,164,176\nds_read_b64 v[238:239], v242 offset:0\n"
"G 5\nv_mul_f32 v232, v98, v232\nM 0,168,192\nR 160,188,24576\nds_read_b64 v[240:241], v242 offset:128\nv_mul_f32 v233, v99, v233\n"
"M 0,172,208\nds_read_b64 v[242:243], v242 offset:0\ns_add_u32 m0, s50, 32768\nv_exp_f32 v230, v230\nM 16,168,196\nR 162,188,28672\n"
"s_add_u32 s63, s54, 1605888\nv_exp_f32 v231, v231\nM 16,172,212\nbuffer_load_dwordx4 v220, s[12:15], s63 offen lds\nv_exp_f32 v232, v232\nM 8,168,200\n"
"R 164,190,24576\nv_fma_f32 v230, v230, v216, v216\nv_exp_f32 v233, v233\nM 8,172,216\nv_rcp_f32 v230, v230\nv_fma_f32 v231, v231, v216, v216\n"
"M 24,168,204\nR 166,190,28672\nv_fma_f32 v232, v232, v216, v216\nv_fma_f32 v233, v233, v216, v216\nM 24,172,220\nG 7\n"
"v_rcp_f32 v231, v231\nv_rcp_f32 v232, v232\nM 28,176,204\nR 168,189,24576\nv_mul_f32 v230, v96, v230\nv_rcp_f32 v233, v233\n"
"M 28,180,220\nbuffer_load_dwordx4 v221, s[12:15], s63 offen offset:1024 lds\nv_mul_f32 v231, v97, v231\nM 12,176,200\nR 170,189,28672\ns_add_u32 s63, s54, 1835264\n"
"v_mul_f32 v232, v98, v232\nM 12,180,216\ns_add_u32 m0, s50, 40960\nv_mul_f32 v233, v99, v233\nv_cvt_pk_bf16_f32 v96, v230, v231\nv_cvt_pk_bf16_f32 v97, v232, v233\n"
"M 20,176,196\nR 172,191,24576\nG 7\nv_mfma_f32_16x16x32_bf16 v[0:3], v[238:241], v[208:211], v[0:3]\nv_mul_f32 v234, v100, v100\nv_mul_f32 v235, v101, v101\n"
"M 20,180,212\nbuffer_load_dwordx4 v220, s[12:15], s63 offen lds\nv_mul_f32 v236, v102, v102\nM 4,176,192\nR 174,191,28672\nG 7\n"
"v_mfma_f32_16x16x32_bf16 v[4:7], v[240:243], v[208:211], v[4:7]\nv_mul_f32 v237, v103, v103\nv_fma_f32 v234, v234, v215, v214\nM 4,180,208\nG 4\nv_fma_f32 v235, v235, v215, v214\n"
"v_fma_f32 v236, v236, v215, v214\nv_fma_f32 v237, v237, v215, v214\nM 0,160,224\nR 176,188,32768\nv_mfma_f32_16x16x32_bf16 v[16:19], v[240:243], v[210:213], v[16:19]\nv_mul_f32 v234, v100, v234\n"
"v_mul_f32 v235, v101, v235\nM 0,164,240\nbuffer_load_dwordx4 v221, s[12:15], s63 offen offset:1024 lds\nv_mul_f32 v236, v102, v236\nM 16,160,228\nR 178,188,36864\n"
"v_mfma_f32_16x16x32_bf16 v[20:23], v[238:241], v[210:213], v[20:23]\ns_add_u32 m0, s50, 49152\ns_add_u32 s63, s54, 2064640\nv_mul_f32 v237, v103, v237\nM 16,164,244\nbuffer_load_dwordx4 v220, s[12:15], s63 offen lds\n"
"v_exp_f32 v234, v234\nM 8,160,232\nR 180,188,40960\nv_fma_f32 v234, v234, v216, v216\nv_exp_f32 v235, v235\nM 8,164,248\n"
"v_exp_f32 v236, v236\nv_exp_f32 v237, v237\nM 24,160,236\nR 182,188,45056\nv_fma_f32 v235, v235, v216, v216\nv_fma_f32 v236, v236, v216, v216\n"
"M 24,164,252\nG 4\nv_rcp_f32 v234, v234\nv_fma_f32 v237, v237, v216, v216\nM 28,168,236\nR 160,189,32768\n"
"v_mul_f32 v234, v100, v234\nv_rcp_f32 v235, v235\nM 28,172,252\nbuffer_load_dwordx4 v221, s[12:15], s63 offen offset:1024 lds\nv_rcp_f32 v236, v236\nM 12,168,232\n"
"R 162,189,36864\ns_add_u32 s63, s54, 2294016\nv_rcp_f32 v237, v237\nM 12,172,248\ns_add_u32 m0, s50, 57344\nv_mul_f32 v235, v101, v235\n"
"v_mul_f32 v236, v102, v236\nv_mul_f32 v237, v103, v237\nM 20,168,228\nR 164,189,40960\nv_cvt_pk_bf16_f32 v98, v234, v235\nLpg11:\n"
"s_cbranch_execz Lpg15\nv_cvt_pk_bf16_f32 v99, v236, v237\nM 20,172,244\nbuffer_load_dwordx4 v220, s[12:15], s63 offen lds\nv_mul_f32 v230, v104, v104\nM 4,168,224\n"
"R 166,189,45056\nv_mul_f32 v231, v105, v105\nv_mul_f32 v232, v106, v106\nM 4,172,240\ns_waitcnt vmcnt(17)\ns_barrier\n"
"G 4\nv_mul_f32 v233, v107, v107\nv_mfma_f32_16x16x32_bf16 v[32:35], v[176:179], a[0:3], 0\nR 168,190,32768\nv_fma_f32 v230, v230, v215, v214\nv_fma_f32 v231, v231, v215, v214\n"
"M 32,180,16\ns_mov_b32 m0, s51\nv_fma_f32 v232, v232, v215, v214\nv_fma_f32 v233, v233, v215, v214\nv_mul_f32 v230, v104, v230\nv_mfma_f32_16x16x32_bf16 v[48:51], v[176:179], a[4:7], 0\n"
"R 170,190,36864\nds_write_addtid_b32 v8 offset:0\nv_mul_f32 v231, v105, v231\nv_mul_f32 v232, v106, v232\nM 48,180,20\nbuffer_store_dwordx4 v[96:99], v252, s[20:23], s84 offen offset:0 sc1\n"
"ds_write_addtid_b32 v9 offset:256\nv_mul_f32 v233, v107, v233\nv_mfma_f32_16x16x32_bf16 v[64:67], v[176:179], a[8:11], 0\nR 172,190,40960\nds_write_addtid_b32 v10 offset:512\nv_mov_b32 v226, s46\n"
"v_exp_f32 v230, v230\nM 64,180,24\nds_write_addtid_b32 v11 offset:768\nv_mov_b32 v227, s47\nv_fma_f32 v230, v230, v216, v216\nv_exp_f32 v231, v231\n"
"v_mfma_f32_16x16x32_bf16 v[80:83], v[176:179], a[12:15], 0\nR 174,190,45056\ns_add_u32 s61, s81, 385024\ns_lshl_b32 s62, s44, 5\ns_add_u32 s61, s61, s62\ns_waitcnt vmcnt(11)\n"
"s_mov_b64 exec, 1\nbuffer_atomic_swap_x2 v[226:227], off, s[20:23], s61 sc1\ns_mov_b64 exec, -1\nds_write_addtid_b32 v12 offset:1024\nv_exp_f32 v232, v232\nM 80,180,28\n"
"ds_write_addtid_b32 v13 offset:1280\nG 10\nv_exp_f32 v233, v233\nv_fma_f32 v231, v231, v216, v216\nv_mfma_f32_16x16x32_bf16 v[84:87], v[160:163], a[12:15], 0\nR 176,191,32768\n"
"ds_write_addtid_b32 v14 offset:1536\nv_fma_f32 v232, v232, v216, v216\nv_fma_f32 v233, v233, v216, v216\nM 84,164,28\nds_write_addtid_b32 v15 offset:1792\nv_rcp_f32 v230, v230\n"
"v_rcp_f32 v231, v231\nv_mfma_f32_16x16x32_bf16 v[68:71], v[160:163], a[8:11], 0\nR 178,191,36864\nds_write_addtid_b32 v24 offset:4096\nv_mul_f32 v230, v104, v230\nv_rcp_f32 v232, v232\n"
"M 68,164,24\nds_write_addtid_b32 v25 offset:4352\nv_rcp_f32 v233, v233\nv_mul_f32 v231, v105, v231\nv_mfma_f32_16x16x32_bf16 v[52:55], v[160:163], a[4:7], 0\nR 180,191,40960\n"
"ds_write_addtid_b32 v26 offset:4608\nv_mul_f32 v232, v106, v232\nv_mul_f32 v233, v107, v233\nM 52,164,20\nds_write_addtid_b32 v27 offset:4864\nds_write_addtid_b32 v28 offset:5120\n"
"ds_write_addtid_b32 v29 offset:5376\nds_write_addtid_b32 v30 offset:5632\nds_write_addtid_b32 v31 offset:5888\nv_cvt_pk_bf16_f32 v104, v230, v231\nv_cvt_pk_bf16_f32 v105, v232, v233\nv_mul_f32 v234, v108, v108\n"
"v_mfma_f32_16x16x32_bf16 v[36:39], v[160:163], a[0:3], 0\nR 182,191,45056\ns_add_u32 m0, s50, 57344\nv_mul_f32 v235, v109, v109\nM 36,164,16\nG 15\n"
"v_mul_f32 v236, v110, v110\nv_mul_f32 v237, v111, v111\nv_fma_f32 v234, v234, v215, v214\nv_mfma_f32_16x16x32_bf16 v[40:43], v[168:171], a[0:3], 0\nR 160,188,49152\ns_waitcnt vmcnt(9) lgkmcnt(2)\n"
"s_barrier\nv_fma_f32 v235, v235, v215, v214\nM 40,172,16\nbuffer_load_dwordx4 v221, s[12:15], s63 offen offset:1024 lds\nds_read2st64_b32 v[8:9], v225 offset0:0 offset1:1\nv_fma_f32 v236, v236, v215, v214\n"
"v_mfma_f32_16x16x32_bf16 v[56:59], v[168:171], a[4:7], 0\nR 162,188,53248\nds_read2st64_b32 v[10:11], v225 offset0:2 offset1:3\nv_fma_f32 v237, v237, v215, v214\nv_mul_f32 v234, v108, v234\nM 56,172,20\n"
"s_add_u32 m0, s50, 65536\nds_read2st64_b32 v[12:13], v225 offset0:4 offset1:5\nv_exp_f32 v234, v234\nv_mul_f32 v235, v109, v235\nv_mfma_f32_16x16x32_bf16 v[72:75], v[168:171], a[8:11], 0\nR 164,184,0\n"
"ds_read2st64_b32 v[14:15], v225 offset0:6 offset1:7\nv_mul_f32 v236, v110, v236\nv_mul_f32 v237, v111, v237\nM 72,172,24\ns_add_u32 s63, s54, 2523392\nds_read2st64_b32 v[24:25], v225 offset0:16 offset1:17\n"
"v_exp_f32 v235, v235\nv_exp_f32 v236, v236\nv_mfma_f32_16x16x32_bf16 v[88:91], v[168:171], a[12:15], 0\nR 166,184,4096\nds_read2st64_b32 v[26:27], v225 offset0:18 offset1:19\nv_fma_f32 v234, v234, v216, v216\n"
"v_exp_f32 v237, v237\nM 88,172,28\nds_read2st64_b32 v[28:29], v225 offset0:20 offset1:21\nG 11\nv_rcp_f32 v234, v234\nv_fma_f32 v235, v235, v216, v216\n"
"v_mfma_f32_16x16x32_bf16 v[92:95], v[176:179], a[12:15], 0\nR 168,189,49152\nds_read2st64_b32 v[30:31], v225 offset0:22 offset1:23\nv_fma_f32 v236, v236, v216, v216\nv_fma_f32 v237, v237, v216, v216\nM 92,180,28\n"
"buffer_load_dwordx4 v220, s[12:15], s63 offen lds\nG 11\nv_add_f32 v0, v0, v8\nv_rcp_f32 v235, v235\nv_mfma_f32_16x16x32_bf16 v[76:79], v[176:179], a[8:11], 0\nR 170,189,53248\n"
"v_mul_f32 v234, v108, v234\nv_add_f32 v1, v1, v9\nv_rcp_f32 v236, v236\nM 76,180,24\nv_rcp_f32 v237, v237\nG 10\n"
"v_add_f32 v2, v2, v10\nv_mul_f32 v235, v109, v235\nv_mfma_f32_16x16x32_bf16 v[60:63], v[176:179], a[4:7], 0\nR 172,185,0\nv_mul_f32 v236, v110, v236\nv_add_f32 v3, v3, v11\n"
"v_mul_f32 v237, v111, v237\nM 60,180,20\nv_cvt_pk_bf16_f32 v106, v234, v235\nv_cvt_pk_bf16_f32 v107, v236, v237\nbuffer_store_dwordx4 v[104:107], v252, s[20:23], s84 offen offset:1024 sc1\nG 10\n"
"v_add_f32 v4, v4, v12\nv_mfma_f32_16x16x32_bf16 v[44:47], v[176:179], a[0:3], 0\nR 174,185,4096\nv_mul_f32 v230, v112, v112\nv_add_f32 v5, v5, v13\nv_mul_f32 v231, v113, v113\n"
"M 44,180,16\nG 7\nv_mul_f32 v232, v114, v114\nv_mul_f32 v233, v115, v115\nv_add_f32 v6, v6, v14\nv_fma_f32 v230, v230, v218, v217\n"
"M 32,160,32\nR 176,190,49152\nv_mov_b32 v248, 0\nv_add_f32 v7, v7, v15\nv_fma_f32 v231, v231, v218, v217\nM 32,164,48\n"
"v_mov_b32 v249, 0\nv_fma_f32 v232, v232, v218, v217\nv_add_f32 v16, v16, v24\nv_fma_f32 v233, v233, v218, v217\nM 48,160,36\nR 178,190,53248\n"
"s_add_u32 s61, s82, 385024\ns_mov_b64 exec, 0xf\nbuffer_atomic_or_x2 v[248:249], v250, s[20:23], s61 offen sc0 sc1\ns_mov_b64 exec, -1\nv_add_f32 v17, v17, v25\nv_mul_f32 v230, v112, v230\n"
"M 48,164,52\nv_exp_f32 v230, v230\nG 8\nv_add_f32 v18, v18, v26\nv_mul_f32 v231, v113, v231\nM 64,160,40\n"
"R 180,186,0\nv_mul_f32 v232, v114, v232\nv_add_f32 v19, v19, v27\nG 8\nv_add_f32 v20, v20, v28\nv_add_f32 v21, v21, v29\n"
"G 6\nv_add_f32 v22, v22, v30\nv_add_f32 v23, v23, v31\nv_mul_f32 v233, v115, v233\nM 64,164,56\nbuffer_load_dwordx4 v221, s[12:15], s63 offen offset:1024 lds\n"
"v_exp_f32 v231, v231\nM 80,160,44\nR 182,186,4096\nv_fma_f32 v230, v230, v219, v219\nM 80,164,60\nG 4\n"
"v_exp_f32 v232, v232\nM 84,168,44\nR 160,191,49152\nv_fma_f32 v231, v231, v219, v219\nM 84,172,60\ns_add_u32 m0, s50, 73728\n"
"v_exp_f32 v233, v233\nM 68,168,40\nR 162,191,53248\nv_fma_f32 v232, v232, v219, v219\nM 68,172,56\ns_add_u32 s63, s54, 2752768\n"
"v_rcp_f32 v230, v230\nM 52,168,36\nR 164,187,0\nv_fma_f32 v233, v233, v219, v219\nM 52,172,52\nbuffer_load_dwordx4 v220, s[12:15], s63 offen lds\n"
"M 36,168,32\nR 166,187,4096\nv_mul_f32 v230, v112, v230\nM 36,172,48\nG 4\nv_rcp_f32 v231, v231\n"
"M 40,176,32\nR 168,184,8192\nv_mul_f32 v231, v113, v231\nM 40,180,48\nv_rcp_f32 v232, v232\nM 56,176,36\n"
"R 170,184,12288\nv_mul_f32 v232, v114, v232\nM 56,180,52\nbuffer_load_dwordx4 v221, s[12:15], s63 offen offset:1024 lds\nM 72,176,40\nR 172,184,16384\n"
"s_add_u32 s63, s54, 2982144\nM 72,180,56\ns_add_u32 m0, s50, 81920\nv_rcp_f32 v233, v233\nM 88,176,44\nR 174,184,20480\n"
"v_mul_f32 v233, v115, v233\nM 88,180,60\nG 4\nv_cvt_pk_bf16_f32 v112, v230, v231\nv_cvt_pk_bf16_f32 v113, v232, v233\nM 92,160,44\n"
"R 176,185,8192\nv_mul_f32 v234, v116, v116\nM 92,164,60\nbuffer_load_dwordx4 v220, s[12:15], s63 offen lds\nM 76,160,40\nR 178,185,12288\n"
"v_mul_f32 v235, v117, v117\nM 76,164,56\nv_mul_f32 v236, v118, v118\nv_mul_f32 v237, v119, v119\nM 60,160,36\nR 180,185,16384\n"
"v_fma_f32 v234, v234, v218, v217\nM 60,164,52\nv_fma_f32 v235, v235, v218, v217\nv_fma_f32 v236, v236, v218, v217\nM 44,160,32\nR 182,185,20480\n"
"v_fma_f32 v237, v237, v218, v217\nM 44,164,48\nG 4\nv_mul_f32 v234, v116, v234\nv_mul_f32 v235, v117, v235\nM 32,168,64\n"
"R 160,186,8192\nv_mul_f32 v236, v118, v236\nM 32,172,80\nbuffer_load_dwordx4 v221, s[12:15], s63 offen offset:1024 lds\nM 48,168,68\nR 162,186,12288\n"
"v_mul_f32 v237, v119, v237\nM 48,172,84\ns_add_u32 m0, s50, 90112\nv_exp_f32 v234, v234\nM 64,168,72\nR 164,186,16384\n"
"v_fma_f32 v234, v234, v219, v219\nM 64,172,88\ns_add_u32 s63, s54, 3211520\nv_exp_f32 v235, v235\nM 80,168,76\nR 166,186,20480\n"
"v_fma_f32 v235, v235, v219, v219\nM 80,172,92\nG 4\nv_exp_f32 v236, v236\nM 84,176,76\nR 168,187,8192\n"
"v_fma_f32 v236, v236, v219, v219\nM 84,180,92\nbuffer_load_dwordx4 v220, s[12:15], s63 offen lds\nM 68,176,72\nR 170,187,12288\nv_mul_f32 v230, v120, v120\n"
"M 68,180,88\nv_exp_f32 v237, v237\nM 52,176,68\nR 172,187,16384\nv_fma_f32 v237, v237, v219, v219\nM 52,180,84\n"
"s_add_u32 s64, s68, 23068672\nv_rcp_f32 v234, v234\nM 36,176,64\nR 174,187,20480\nv_mul_f32 v234, v116, v234\nM 36,180,80\n"
"G 4\nv_rcp_f32 v235, v235\nM 40,160,64\nR 176,184,24576\ns_waitcnt vmcnt(13)\ns_barrier\n"
"s_waitcnt vmcnt(6)\nv_cmp_eq_u64 s[86:87], s[46:47], v[248:249]\nM 40,164,80\ns_add_u32 s65, s68, 23183360\nv_rcp_f32 v236, v236\nM 56,160,68\n"
"R 178,184,28672\ns_and_b32 s62, s86, 0xf\ns_cmp_eq_u32 s62, 0xf\ns_cbranch_scc0 Lq377\nLq378:\nM 56,164,84\n"
"buffer_load_dwordx4 v221, s[12:15], s63 offen offset:1024 lds\nM 72,160,72\nR 180,184,32768\nv_mul_f32 v235, v117, v235\nM 72,164,88\nbuffer_load_dwordx4 a[0:3], v252, s[20:23], s64 offen sc1\n"
"M 88,160,76\nR 182,184,36864\nv_mul_f32 v236, v118, v236\nM 88,164,92\nG 4\nv_rcp_f32 v237, v237\n"
"M 92,168,76\nR 160,185,24576\nv_mul_f32 v237, v119, v237\nM 92,172,92\nbuffer_load_dwordx4 a[4:7], v252, s[20:23], s65 offen sc1\nM 76,168,72\n"
"R 162,185,28672\nv_cvt_pk_bf16_f32 v114, v234, v235\nM 76,172,88\ns_add_u32 s66, s69, 23068672\nv_cvt_pk_bf16_f32 v115, v236, v237\nv_mul_f32 v231, v121, v121\n"
"M 60,168,68\nR 164,185,32768\nv_mul_f32 v232, v122, v122\nM 60,172,84\ns_add_u32 s67, s69, 23183360\nv_mul_f32 v233, v123, v123\n"
"v_fma_f32 v230, v230, v218, v217\nM 44,168,64\nR 166,185,36864\nv_fma_f32 v231, v231, v218, v217\nM 44,172,80\nG 4\n"
"v_fma_f32 v232, v232, v218, v217\nv_fma_f32 v233, v233, v218, v217\nM 32,176,96\nR 168,186,24576\nv_mul_f32 v230, v120, v230\nM 32,180,112\n"
"buffer_load_dwordx4 a[8:11], v252, s[20:23], s66 offen sc1\nM 48,176,100\nR 170,186,28672\nv_mul_f32 v231, v121, v231\nM 48,180,116\nbuffer_load_dwordx4 a[12:15], v252, s[20:23], s67 offen sc1\n"
"M 64,176,104\nR 172,186,32768\nv_mul_f32 v232, v122, v232\nM 64,180,120\ns_add_u32 s64, s68, 23075840\nv_exp_f32 v230, v230\n"
"M 80,176,108\nR 174,186,36864\nv_mul_f32 v233, v123, v233\nM 80,180,124\nG 4\nv_exp_f32 v231, v231\n"
"M 84,160,108\nR 176,187,24576\nv_fma_f32 v230, v230, v219, v219\nM 84,164,124\nbuffer_load_dwordx4 a[16:19], v252, s[20:23], s64 offen sc1\nM 68,160,104\n"
"R 178,187,28672\nv_fma_f32 v231, v231, v219, v219\nM 68,164,120\ns_add_u32 s65, s68, 23190528\nv_exp_f32 v232, v232\nM 52,160,100\n"
"R 180,187,32768\nv_fma_f32 v232, v232, v219, v219\nM 52,164,116\ns_add_u32 s66, s69, 23075840\nv_exp_f32 v233, v233\nM 36,160,96\n"
"R 182,187,36864\nv_fma_f32 v233, v233, v219, v219\nM 36,164,112\nG 4\nv_rcp_f32 v230, v230\nM 40,168,96\n"
"R 160,184,40960\nv_mul_f32 v230, v120, v230\nM 40,172,112\nbuffer_load_dwordx4 a[20:23], v252, s[20:23], s65 offen sc1\nM 56,168,100\nR 162,184,45056\n"
"v_mul_f32 v234, v124, v124\nM 56,172,116\nbuffer_load_dwordx4 a[24:27], v252, s[20:23], s66 offen sc1\nM 72,168,104\nR 164,184,49152\nv_mul_f32 v235, v125, v125\n"
"M 72,172,120\ns_add_u32 s67, s69, 23190528\nv_rcp_f32 v231, v231\nM 88,168,108\nR 166,184,53248\nv_mul_f32 v231, v121, v231\n"
"M 88,172,124\nG 4\nv_rcp_f32 v232, v232\nM 92,176,108\nR 168,185,40960\nv_mul_f32 v232, v122, v232\n"
"M 92,180,124\nbuffer_load_dwordx4 a[28:31], v252, s[20:23], s67 offen sc1\nM 76,176,104\nR 170,185,45056\nv_cvt_pk_bf16_f32 v120, v230, v231\nM 76,180,120\n"
"s_add_u32 s64, s68, 23069696\nv_rcp_f32 v233, v233\nM 60,176,100\nR 172,185,49152\nv_mul_f32 v233, v123, v233\nM 60,180,116\n"
"s_add_u32 s65, s68, 23184384\nv_cvt_pk_bf16_f32 v121, v232, v233\nv_mul_f32 v236, v126, v126\nM 44,176,96\nR 174,185,53248\nv_mul_f32 v237, v127, v127\n"
"M 44,180,112\nG 4\nv_fma_f32 v234, v234, v218, v217\nv_fma_f32 v235, v235, v218, v217\nM 32,160,128\nR 176,186,40960\n"
"s_add_u32 m0, s50, 98304\nM 32,164,144\nbuffer_load_dwordx4 a[32:35], v252, s[20:23], s64 offen sc1\nM 48,160,132\nR 178,186,45056\ns_add_u32 s63, s54, 3440896\n"
"M 48,164,148\nbuffer_load_dwordx4 a[36:39], v252, s[20:23], s65 offen sc1\nM 64,160,136\nR 180,186,49152\nv_fma_f32 v236, v236, v218, v217\nM 64,164,152\n"
"s_add_u32 s66, s69, 23069696\nbuffer_load_dwordx4 v220, s[12:15], s63 offen lds\nM 80,160,140\nR 182,186,53248\nv_fma_f32 v237, v237, v218, v217\nM 80,164,156\n"
"G 4\nv_mul_f32 v234, v124, v234\nv_mul_f32 v235, v125, v235\nM 84,168,140\nR 160,187,40960\nv_mul_f32 v236, v126, v236\n"
"M 84,172,156\nbuffer_load_dwordx4 a[40:43], v252, s[20:23], s66 offen sc1\nbuffer_load_dwordx4 v221, s[12:15], s63 offen offset:1024 lds\nM 68,168,136\nR 162,187,45056\nv_mul_f32 v237, v127, v237\n"
"M 68,172,152\ns_add_u32 s67, s69, 23184384\nv_exp_f32 v234, v234\nM 52,168,132\nR 164,187,49152\ns_waitcnt vmcnt(15)\n"
"s_barrier\nM 52,172,148\nbuffer_load_dwordx4 a[44:47], v252, s[20:23], s67 offen sc1\ns_movk_i32 s63, 2048\nv_mad_u32_u24 v220, v228, s63, v229\nv_add_u32 v221, 916480, v220\n"
"s_add_u32 m0, s50, 106496\ns_add_u32 s63, s56, 0\nbuffer_load_dwordx4 v220, s[16:19], s63 offen lds\nM 36,168,128\nR 166,187,53248\nv_fma_f32 v234, v234, v219, v219\n"
"M 36,172,144\nG 4\nv_exp_f32 v235, v235\nM 40,176,128\nR 168,184,57344\nv_fma_f32 v235, v235, v219, v219\n"
"M 40,180,144\ns_add_u32 s64, s68, 23076864\nbuffer_load_dwordx4 v221, s[16:19], s63 offen offset:1024 lds\nM 56,176,132\nR 170,184,61440\ns_add_u32 m0, s50, 114688\n"
"M 56,180,148\nbuffer_load_dwordx4 a[48:51], v252, s[20:23], s64 offen sc1\nM 72,176,136\nR 172,188,0\ns_add_u32 s63, s56, 524288\nM 72,180,152\n"
"s_add_u32 s65, s68, 23191552\nbuffer_load_dwordx4 v220, s[16:19], s63 offen lds\nM 88,176,140\nR 174,188,4096\nv_exp_f32 v236, v236\nM 88,180,156\n"
"G 4\nv_exp_f32 v237, v237\nM 92,160,140\nR 176,185,57344\nv_fma_f32 v236, v236, v219, v219\nM 92,164,156\n"
"buffer_load_dwordx4 a[52:55], v252, s[20:23], s65 offen sc1\nbuffer_load_dwordx4 v221, s[16:19], s63 offen offset:1024 lds\nM 76,160,136\nR 178,185,61440\nv_fma_f32 v237, v237, v219, v219\nM 76,164,152\n"
"s_add_u32 s66, s69, 23076864\nv_rcp_f32 v234, v234\nM 60,160,132\nR 180,189,0\nv_mul_f32 v234, v124, v234\nM 60,164,148\n"
"buffer_load_dwordx4 a[56:59], v252, s[20:23], s66 offen sc1\nM 44,160,128\nR 182,189,4096\nM 44,164,144\nG 4\nv_rcp_f32 v235, v235\n"
"M 32,168,160\nR 160,186,57344\nv_mul_f32 v235, v125, v235\nM 32,172,176\ns_add_u32 s67, s69, 23191552\nv_rcp_f32 v236, v236\n"
"M 48,168,164\nR 162,186,61440\nv_mul_f32 v236, v126, v236\nM 48,172,180\nbuffer_load_dwordx4 a[60:63], v252, s[20:23], s67 offen sc1\nM 64,168,168\n"
"R 164,190,0\nv_cvt_pk_bf16_f32 v122, v234, v235\nM 64,172,184\nbuffer_store_dwordx4 v[112:115], v252, s[20:23], s85 offen offset:0 sc1\nM 80,168,172\nR 166,190,4096\n"
"v_rcp_f32 v237, v237\nM 80,172,188\nG 4\nv_mul_f32 v237, v127, v237\nv_cvt_pk_bf16_f32 v123, v236, v237\nM 84,176,172\n"
"R 168,187,57344\nM 84,180,188\ns_add_u32 m0, s50, 0\nLpg12:\ns_cbranch_execz LretO0n\nbuffer_store_dwordx4 v[120:123], v252, s[20:23], s85 offen offset:1024 sc1\n"
"M 68,176,168\nR 170,187,61440\nv_mul_f32 v230, v0, v0\nv_mul_f32 v231, v1, v1\nM 68,180,184\ns_add_u32 s63, s56, 65536\n"
"s_add_u32 s84, s73, 23074816\ns_add_u32 s85, s73, 23189504\nv_mul_f32 v232, v2, v2\nM 52,176,164\nR 172,191,0\nv_mul_f32 v233, v3, v3\n"
"v_fma_f32 v230, v230, v215, v214\nM 52,180,180\nbuffer_load_dwordx4 v220, s[16:19], s63 offen lds\nv_fma_f32 v231, v231, v215, v214\nM 36,176,160\nR 174,191,4096\n"
"v_fma_f32 v232, v232, v215, v214\nv_fma_f32 v233, v233, v215, v214\nM 36,180,176\nG 4\nv_mul_f32 v230, v0, v230\nv_mul_f32 v231, v1, v231\n"
"v_mul_f32 v232, v2, v232\nM 40,160,160\nR 176,188,8192\nv_mul_f32 v233, v3, v233\nv_exp_f32 v230, v230\nM 40,164,176\n"
"v_exp_f32 v231, v231\nv_exp_f32 v232, v232\nM 56,160,164\nR 178,188,12288\nv_fma_f32 v230, v230, v216, v216\nv_exp_f32 v233, v233\n"
"M 56,164,180\nbuffer_load_dwordx4 v221, s[16:19], s63 offen offset:1024 lds\nv_fma_f32 v231, v231, v216, v216\nM 72,160,168\nR 180,188,16384\ns_add_u32 s63, s56, 589824\n"
"v_fma_f32 v232, v232, v216, v216\nM 72,164,184\ns_add_u32 m0, s50, 8192\nv_rcp_f32 v230, v230\nv_fma_f32 v233, v233, v216, v216\nM 88,160,172\n"
"R 182,188,20480\nv_mul_f32 v230, v0, v230\nv_rcp_f32 v231, v231\nM 88,164,188\nG 4\nv_rcp_f32 v232, v232\n"
"v_rcp_f32 v233, v233\nM 92,168,172\nR 160,189,8192\nv_mul_f32 v231, v1, v231\nv_mul_f32 v232, v2, v232\nM 92,172,188\n"
"buffer_load_dwordx4 v220, s[16:19], s63 offen lds\nv_mul_f32 v233, v3, v233\nM 76,168,168\nR 162,189,12288\nv_cvt_pk_bf16_f32 v0, v230, v231\nv_cvt_pk_bf16_f32 v1, v232, v233\n"
"M 76,172,184\nv_mul_f32 v234, v4, v4\nv_mul_f32 v235, v5, v5\nv_mul_f32 v236, v6, v6\nM 60,168,164\nR 164,189,16384\n"
"v_mul_f32 v237, v7, v7\nv_fma_f32 v234, v234, v215, v214\nM 60,172,180\nbuffer_load_dwordx4 v221, s[16:19], s63 offen offset:1024 lds\nv_fma_f32 v235, v235, v215, v214\nM 44,168,160\n"
"R 166,189,20480\ns_add_u32 m0, s50, 16384\nv_fma_f32 v236, v236, v215, v214\nM 44,172,176\nG 4\nv_fma_f32 v237, v237, v215, v214\n"
"v_mul_f32 v234, v4, v234\nv_mul_f32 v235, v5, v235\nM 32,176,192\nR 168,190,8192\ns_add_u32 s63, s57, 0\nv_mul_f32 v236, v6, v236\n"
"M 32,180,208\nbuffer_load_dwordx4 v220, s[16:19], s63 offen lds\nv_mul_f32 v237, v7, v237\nM 48,176,196\nR 170,190,12288\nv_mul_f32 v230, v16, v16\n"
"v_exp_f32 v234, v234\nM 48,180,212\nv_exp_f32 v235, v235\nv_exp_f32 v236, v236\nM 64,176,200\nR 172,190,16384\n"
"v_fma_f32 v234, v234, v216, v216\nv_exp_f32 v237, v237\nM 64,180,216\nbuffer_load_dwordx4 v221, s[16:19], s63 offen offset:1024 lds\nv_fma_f32 v235, v235, v216, v216\nM 80,176,204\n"
"R 174,190,20480\ns_add_u32 s63, s57, 524288\nv_fma_f32 v236, v236, v216, v216\nM 80,180,220\nG 4\nv_rcp_f32 v234, v234\n"
"v_fma_f32 v237, v237, v216, v216\nM 84,160,204\nR 176,191,8192\ns_waitcnt vmcnt(10)\ns_barrier\nv_rcp_f32 v235, v235\n"
"M 84,164,220\ns_add_u32 m0, s50, 24576\nv_rcp_f32 v236, v236\nv_rcp_f32 v237, v237\nM 68,160,200\nR 178,191,12288\n"
"v_mul_f32 v234, v4, v234\nv_mul_f32 v235, v5, v235\nM 68,164,216\nbuffer_load_dwordx4 v220, s[16:19], s63 offen lds\nv_mul_f32 v236, v6, v236\nM 52,160,196\n"
"R 180,191,16384\nv_mul_f32 v237, v7, v237\nv_cvt_pk_bf16_f32 v2, v234, v235\nM 52,164,212\nv_cvt_pk_bf16_f32 v3, v236, v237\nv_mul_f32 v231, v17, v17\n"
"v_mul_f32 v232, v18, v18\nM 36,160,192\nR 182,191,20480\nv_mul_f32 v233, v19, v19\nv_fma_f32 v230, v230, v218, v217\nM 36,164,208\n"
"G 4\nv_fma_f32 v231, v231, v218, v217\nv_fma_f32 v232, v232, v218, v217\nv_fma_f32 v233, v233, v218, v217\nM 40,168,192\nR 160,188,24576\n"
"v_mul_f32 v230, v16, v230\nv_mul_f32 v231, v17, v231\nM 40,172,208\nbuffer_load_dwordx4 v221, s[16:19], s63 offen offset:1024 lds\nv_mul_f32 v232, v18, v232\nM 56,168,196\n"
"R 162,188,28672\nv_mul_f32 v233, v19, v233\nv_exp_f32 v230, v230\nM 56,172,212\nbuffer_store_dwordx4 v[0:3], v252, s[20:23], s84 offen offset:0 sc1\nv_exp_f32 v231, v231\n"
"M 72,168,200\nR 164,188,32768\nv_fma_f32 v230, v230, v219, v219\nv_exp_f32 v232, v232\nM 72,172,216\ns_add_u32 m0, s50, 32768\n"
"v_exp_f32 v233, v233\nv_fma_f32 v231, v231, v219, v219\nM 88,168,204\nR 166,188,36864\nv_mov_b32 v226, s46\nv_fma_f32 v232, v232, v219, v219\n"
"M 88,172,220\nG 4\nv_mov_b32 v227, s47\nv_fma_f32 v233, v233, v219, v219\nv_rcp_f32 v230, v230\nM 92,176,204\n"
"R 168,189,24576\ns_add_u32 s61, s81, 385032\ns_lshl_b32 s62, s44, 5\ns_add_u32 s61, s61, s62\ns_waitcnt vmcnt(9)\ns_mov_b64 exec, 1\n"
"buffer_atomic_swap_x2 v[226:227], off, s[20:23], s61 sc1\ns_mov_b64 exec, -1\nv_rcp_f32 v231, v231\nM 92,180,220\ns_add_u32 s63, s57, 65536\nv_rcp_f32 v232, v232\n"
"v_rcp_f32 v233, v233\nM 76,176,200\nR 170,189,28672\nv_mul_f32 v230, v16, v230\nv_mul_f32 v231, v17, v231\nM 76,180,216\n"
"buffer_load_dwordx4 v220, s[16:19], s63 offen lds\nv_mul_f32 v232, v18, v232\nM 60,176,196\nR 172,189,32768\nv_mul_f32 v233, v19, v233\nv_cvt_pk_bf16_f32 v16, v230, v231\n"
"M 60,180,212\nv_cvt_pk_bf16_f32 v17, v232, v233\nv_mul_f32 v234, v20, v20\nv_mul_f32 v235, v21, v21\nM 44,176,192\nR 174,189,36864\n"
"s_mov_b32 s60, 158208\nv_bfe_u32 v246, v252, 4, 4\nv_lshl_add_u32 v246, v246, 3, s60\nv_mul_f32 v236, v22, v22\nM 44,180,208\nds_read_b64 v[238:239], v246 offset:0\n"
"G 5\nv_mul_f32 v237, v23, v23\nM 32,160,224\nR 176,190,24576\nds_read_b64 v[240:241], v246 offset:128\nv_fma_f32 v234, v234, v218, v217\n"
"M 32,164,240\nds_read_b64 v[242:243], v246 offset:256\nv_fma_f32 v235, v235, v218, v217\nv_fma_f32 v236, v236, v218, v217\nM 48,160,228\nR 178,190,28672\n"
"ds_read_b64 v[244:245], v246 offset:384\nv_fma_f32 v237, v237, v218, v217\nM 48,164,244\nds_read_b64 v[246:247], v246 offset:0\nv_mul_f32 v234, v20, v234\nv_mul_f32 v235, v21, v235\n"
"M 64,160,232\nR 180,190,32768\nv_mul_f32 v236, v22, v236\nv_mul_f32 v237, v23, v237\nM 64,164,248\nbuffer_load_dwordx4 v221, s[16:19], s63 offen offset:1024 lds\n"
"v_exp_f32 v234, v234\nM 80,160,236\nR 182,190,36864\ns_add_u32 m0, s50, 40960\nv_exp_f32 v235, v235\nM 80,164,252\n"
"G 9\nv_exp_f32 v236, v236\nv_exp_f32 v237, v237\nM 84,168,236\nR 160,191,24576\ns_add_u32 s63, s57, 589824\n"
"v_fma_f32 v234, v234, v219, v219\nM 84,172,252\nbuffer_load_dwordx4 v220, s[16:19], s63 offen lds\nv_fma_f32 v235, v235, v219, v219\nM 68,168,232\nR 162,191,28672\n"
"v_fma_f32 v236, v236, v219, v219\nv_fma_f32 v237, v237, v219, v219\nM 68,172,248\nv_rcp_f32 v234, v234\nv_rcp_f32 v235, v235\nM 52,168,228\n"
"R 164,191,32768\nG 9\nv_mfma_f32_16x16x32_bf16 v[32:35], v[238:241], v[208:211], v[32:35]\nv_mul_f32 v234, v20, v234\nv_rcp_f32 v236, v236\nM 52,172,244\n"
"buffer_load_dwordx4 v221, s[16:19], s63 offen offset:1024 lds\nv_rcp_f32 v237, v237\nM 36,168,224\nR 166,191,36864\nG 9\nv_mfma_f32_16x16x32_bf16 v[36:39], v[240:243], v[208:211], v[36:39]\n"
"s_add_u32 m0, s50, 49152\nv_mul_f32 v235, v21, v235\nM 36,172,240\nG 4\nv_mul_f32 v236, v22, v236\nv_mul_f32 v237, v23, v237\n"
"v_cvt_pk_bf16_f32 v18, v234, v235\nM 40,176,224\nR 168,188,40960\nv_mfma_f32_16x16x32_bf16 v[40:43], v[242:245], v[208:211], v[40:43]\ns_add_u32 s63, s56, 131072\nv_cvt_pk_bf16_f32 v19, v236, v237\n"
"M 40,180,240\nbuffer_load_dwordx4 v220, s[16:19], s63 offen lds\nbuffer_store_dwordx4 v[16:19], v252, s[20:23], s85 offen offset:0 sc1\nM 56,176,228\nR 170,188,45056\nv_mfma_f32_16x16x32_bf16 v[44:47], v[244:247], v[208:211], v[44:47]\n"
"v_mov_b32 v248, 0\nM 56,180,244\nv_mov_b32 v249, 0\nM 72,176,232\nR 172,188,49152\nv_mfma_f32_16x16x32_bf16 v[48:51], v[244:247], v[210:213], v[48:51]\n"
"s_add_u32 s61, s82, 385032\ns_mov_b64 exec, 0xf\nbuffer_atomic_or_x2 v[248:249], v250, s[20:23], s61 offen sc0 sc1\ns_mov_b64 exec, -1\nM 72,180,248\nM 88,176,236\n"
"R 174,188,53248\nv_mfma_f32_16x16x32_bf16 v[52:55], v[238:241], v[210:213], v[52:55]\nM 88,180,252\nG 4\nM 92,160,236\nR 176,189,40960\n"
"v_mfma_f32_16x16x32_bf16 v[56:59], v[240:243], v[210:213], v[56:59]\nM 92,164,252\nbuffer_load_dwordx4 v221, s[16:19], s63 offen offset:1024 lds\nM 76,160,232\nR 178,189,45056\nv_mfma_f32_16x16x32_bf16 v[60:63], v[242:245], v[210:213], v[60:63]\n"
"s_add_u32 m0, s50, 57344\ns_add_u32 s63, s56, 655360\nM 76,164,248\nbuffer_load_dwordx4 v220, s[16:19], s63 offen lds\nM 60,160,228\nR 180,189,49152\n"
"M 60,164,244\nM 44,160,224\nR 182,189,53248\nM 44,164,240\ns_waitcnt vmcnt(15)\ns_barrier\n"
"G 4\nv_mfma_f32_16x16x32_bf16 v[96:99], v[168:171], a[0:3], 0\nR 160,190,40960\nM 96,172,16\ns_mov_b32 m0, s51\nv_mfma_f32_16x16x32_bf16 v[112:115], v[168:171], a[4:7], 0\n"
"R 162,190,45056\nM 112,172,20\nds_write_addtid_b32 v64 offset:0\nv_mfma_f32_16x16x32_bf16 v[128:131], v[168:171], a[8:11], 0\nR 164,190,49152\nM 128,172,24\n"
"ds_write_addtid_b32 v65 offset:256\nv_mfma_f32_16x16x32_bf16 v[144:147], v[168:171], a[12:15], 0\nR 166,190,53248\nds_write_addtid_b32 v66 offset:512\nM 144,172,28\nds_write_addtid_b32 v67 offset:768\n"
"ds_write_addtid_b32 v68 offset:1024\nG 9\nv_mfma_f32_16x16x32_bf16 v[148:151], v[176:179], a[12:15], 0\nR 168,191,40960\nds_write_addtid_b32 v69 offset:1280\nM 148,180,28\n"
"ds_write_addtid_b32 v70 offset:1536\nds_write_addtid_b32 v71 offset:1792\nv_mfma_f32_16x16x32_bf16 v[132:135], v[176:179], a[8:11], 0\nR 170,191,45056\nds_write_addtid_b32 v72 offset:2048\nM 132,180,24\n"
"ds_write_addtid_b32 v73 offset:2304\nds_write_addtid_b32 v74 offset:2560\nv_mfma_f32_16x16x32_bf16 v[116:119], v[176:179], a[4:7], 0\nR 172,191,49152\nds_write_addtid_b32 v75 offset:2816\nM 116,180,20\n"
"ds_write_addtid_b32 v76 offset:3072\nds_write_addtid_b32 v77 offset:3328\nv_mfma_f32_16x16x32_bf16 v[100:103], v[176:179], a[0:3], 0\nR 174,191,53248\nds_write_addtid_b32 v78 offset:3584\nM 100,180,16\n"
"ds_write_addtid_b32 v79 offset:3840\nds_write_addtid_b32 v80 offset:4096\nG 15\nv_mfma_f32_16x16x32_bf16 v[104:107], v[160:163], a[0:3], 0\nR 176,184,0\nds_write_addtid_b32 v81 offset:4352\n"
"M 104,164,16\nds_write_addtid_b32 v82 offset:4608\nds_write_addtid_b32 v83 offset:4864\nv_mfma_f32_16x16x32_bf16 v[120:123], v[160:163], a[4:7], 0\nR 178,184,4096\nds_write_addtid_b32 v84 offset:5120\n"
"M 120,164,20\nds_write_addtid_b32 v85 offset:5376\nds_write_addtid_b32 v86 offset:5632\nv_mfma_f32_16x16x32_bf16 v[136:139], v[160:163], a[8:11], 0\nR 180,184,8192\nds_write_addtid_b32 v87 offset:5888\n"
"M 136,164,24\nds_write_addtid_b32 v88 offset:6144\nds_write_addtid_b32 v89 offset:6400\nv_mfma_f32_16x16x32_bf16 v[152:155], v[160:163], a[12:15], 0\nR 182,184,12288\nds_write_addtid_b32 v90 offset:6656\n"
"M 152,164,28\nds_write_addtid_b32 v91 offset:6912\nds_write_addtid_b32 v92 offset:7168\nG 15\nv_mfma_f32_16x16x32_bf16 v[156:159], v[168:171], a[12:15], 0\nR 160,185,0\n"
"ds_write_addtid_b32 v93 offset:7424\nM 156,172,28\nds_write_addtid_b32 v94 offset:7680\nds_write_addtid_b32 v95 offset:7936\nv_mfma_f32_16x16x32_bf16 v[140:143], v[168:171], a[8:11], 0\nR 162,185,4096\n"
"s_add_u32 m0, s50, 57344\nM 140,172,24\nbuffer_load_dwordx4 v221, s[16:19], s63 offen offset:1024 lds\nv_mfma_f32_16x16x32_bf16 v[124:127], v[168:171], a[4:7], 0\nR 164,185,8192\ns_waitcnt vmcnt(6) lgkmcnt(2)\n"
"s_barrier\nM 124,172,20\nds_read2st64_b32 v[64:65], v225 offset0:0 offset1:1\nds_read2st64_b32 v[66:67], v225 offset0:2 offset1:3\nv_mfma_f32_16x16x32_bf16 v[108:111], v[168:171], a[0:3], 0\nR 166,185,12288\n"
"ds_read2st64_b32 v[68:69], v225 offset0:4 offset1:5\nM 108,172,16\nds_read2st64_b32 v[70:71], v225 offset0:6 offset1:7\nds_read2st64_b32 v[72:73], v225 offset0:8 offset1:9\nM 96,176,32\nR 168,186,0\n"
"ds_read2st64_b32 v[74:75], v225 offset0:10 offset1:11\nM 96,180,48\ns_add_u32 s64, s68, 23070720\nds_read2st64_b32 v[76:77], v225 offset0:12 offset1:13\nv_mov_b32 v226, s46\nM 112,176,36\n"
"R 170,186,4096\nds_read2st64_b32 v[78:79], v225 offset0:14 offset1:15\nv_mov_b32 v227, s47\ns_waitcnt vmcnt(3)\nv_cmp_eq_u64 s[86:87], s[46:47], v[248:249]\nM 112,180,52\n"
"s_add_u32 s61, s81, 385048\ns_lshl_b32 s62, s44, 5\ns_add_u32 s61, s61, s62\ns_mov_b64 exec, 1\nbuffer_atomic_swap_x2 v[226:227], off, s[20:23], s61 sc1\ns_mov_b64 exec, -1\n"
"ds_read2st64_b32 v[80:81], v225 offset0:16 offset1:17\nM 128,176,40\nR 172,186,8192\ns_and_b32 s62, s86, 0xf\ns_cmp_eq_u32 s62, 0xf\ns_cbranch_scc0 Lq412\n"
"Lq413:\nds_read2st64_b32 v[82:83], v225 offset0:18 offset1:19\nM 128,180,56\ns_add_u32 s65, s68, 23185408\nds_read2st64_b32 v[84:85], v225 offset0:20 offset1:21\nds_read2st64_b32 v[86:87], v225 offset0:22 offset1:23\n"
"M 144,176,44\nR 174,186,12288\ns_add_u32 m0, s50, 65536\nds_read2st64_b32 v[88:89], v225 offset0:24 offset1:25\nM 144,180,60\nds_read2st64_b32 v[90:91], v225 offset0:26 offset1:27\n"
"ds_read2st64_b32 v[92:93], v225 offset0:28 offset1:29\nG 15\nM 148,160,44\nR 176,187,0\ns_add_u32 s63, s56, 196608\nds_read2st64_b32 v[94:95], v225 offset0:30 offset1:31\n"
"M 148,164,60\nbuffer_load_dwordx4 a[64:67], v252, s[20:23], s64 offen sc1\nv_add_f32 v32, v32, v64\nM 132,160,40\nR 178,187,4096\nv_add_f32 v33, v33, v65\n"
"v_add_f32 v34, v34, v66\nM 132,164,56\ns_add_u32 s66, s69, 23070720\nv_add_f32 v35, v35, v67\nv_add_f32 v36, v36, v68\nv_add_f32 v37, v37, v69\n"
"M 116,160,36\nR 180,187,8192\nbuffer_load_dwordx4 v220, s[16:19], s63 offen lds\nv_add_f32 v38, v38, v70\nM 116,164,52\nbuffer_load_dwordx4 a[68:71], v252, s[20:23], s65 offen sc1\n"
"v_add_f32 v39, v39, v71\nM 100,160,32\nR 182,187,12288\nG 15\nv_add_f32 v40, v40, v72\nv_add_f32 v41, v41, v73\n"
"M 100,164,48\nG 8\nv_add_f32 v42, v42, v74\nv_add_f32 v43, v43, v75\nv_add_f32 v44, v44, v76\nM 104,168,32\n"
"R 160,184,16384\nbuffer_load_dwordx4 v221, s[16:19], s63 offen offset:1024 lds\nv_add_f32 v45, v45, v77\nM 104,172,48\ns_add_u32 s67, s69, 23185408\nv_add_f32 v46, v46, v78\n"
"v_add_f32 v47, v47, v79\nv_add_f32 v48, v48, v80\nM 120,168,36\nR 162,184,20480\nv_add_f32 v49, v49, v81\nv_add_f32 v50, v50, v82\n"
"M 120,172,52\nbuffer_load_dwordx4 a[72:75], v252, s[20:23], s66 offen sc1\nM 136,168,40\nR 164,184,24576\nv_add_f32 v51, v51, v83\nM 136,172,56\n"
"v_add_f32 v52, v52, v84\nv_add_f32 v53, v53, v85\nM 152,168,44\nR 166,184,28672\nv_add_f32 v54, v54, v86\nM 152,172,60\n"
"G 4\nv_add_f32 v55, v55, v87\nv_add_f32 v56, v56, v88\nM 156,176,44\nR 168,185,16384\nv_add_f32 v57, v57, v89\n"
"M 156,180,60\nbuffer_load_dwordx4 a[76:79], v252, s[20:23], s67 offen sc1\nM 140,176,40\nR 170,185,20480\nv_add_f32 v58, v58, v90\nM 140,180,56\n"
"s_add_u32 s64, s68, 23077888\nv_add_f32 v59, v59, v91\nv_add_f32 v60, v60, v92\nM 124,176,36\nR 172,185,24576\nv_add_f32 v61, v61, v93\n"
"M 124,180,52\ns_add_u32 s65, s68, 23192576\nv_add_f32 v62, v62, v94\nv_add_f32 v63, v63, v95\nM 108,176,32\nR 174,185,28672\n"
"v_mul_f32 v230, v32, v32\nv_mul_f32 v231, v33, v33\nM 108,180,48\ns_add_u32 s84, s73, 23072768\ns_add_u32 s85, s73, 23187456\nG 4\n"
"v_mul_f32 v232, v34, v34\nv_mfma_f32_16x16x32_bf16 v[0:3], v[160:163], a[0:3], 0\nR 176,186,16384\nLq577:\ns_lshr_b32 s60, s45, 3\ns_lshl2_add_u32 s60, s60, s40\n"
"s_lshl_b32 s60, s60, 8\nv_lshl_add_u32 v224, v252, 3, s60\nv_add_u32 v224, Lq577+4096-LqPC, v224\nv_min_u32 v224, LqE-LqPC-4, v224\ns_mov_b64 exec, 3\nglobal_load_dword v224, v224, s[98:99]\n"
"s_mov_b64 exec, -1\nv_mul_f32 v233, v35, v35\nM 0,164,16\nbuffer_load_dwordx4 a[80:83], v252, s[20:23], s64 offen sc1\nv_fma_f32 v230, v230, v215, v214\nv_mfma_f32_16x16x32_bf16 v[16:19], v[160:163], a[4:7], 0\n"
"R 178,186,20480\nv_fma_f32 v231, v231, v215, v214\nv_fma_f32 v232, v232, v215, v214\nM 16,164,20\ns_add_u32 s66, s69, 23077888\nv_fma_f32 v233, v233, v215, v214\n"
"v_mul_f32 v230, v32, v230\nv_mul_f32 v231, v33, v231\nv_mfma_f32_16x16x32_bf16 a[224:227], v[160:163], a[8:11], 0\nR 180,186,24576\nv_mul_f32 v232, v34, v232\nv_mul_f32 v233, v35, v233\n"
"v_mfma_f32_16x16x32_bf16 a[224:227], v[164:167], a[24:27], a[224:227]\nbuffer_load_dwordx4 a[84:87], v252, s[20:23], s65 offen sc1\nv_exp_f32 v230, v230\nv_mfma_f32_16x16x32_bf16 a[240:243], v[160:163], a[12:15], 0\nR 182,186,28672\nv_fma_f32 v230, v230, v216, v216\n"
"v_exp_f32 v231, v231\nv_mfma_f32_16x16x32_bf16 a[240:243], v[164:167], a[28:31], a[240:243]\nG 4\nv_exp_f32 v232, v232\nv_exp_f32 v233, v233\nv_mfma_f32_16x16x32_bf16 a[244:247], v[168:171], a[12:15], 0\n"
"R 160,187,16384\ns_add_u32 m0, s50, 73728\nv_fma_f32 v231, v231, v216, v216\nv_mfma_f32_16x16x32_bf16 a[244:247], v[172:175], a[28:31], a[244:247]\ns_add_u32 s67, s69, 23192576\nv_rcp_f32 v230, v230\n"
"v_fma_f32 v232, v232, v216, v216\nv_mfma_f32_16x16x32_bf16 a[228:231], v[168:171], a[8:11], 0\nR 162,187,20480\nLpg13:\ns_cbranch_execz LretO0n\ns_add_u32 s63, s56, 720896\n"
"v_fma_f32 v233, v233, v216, v216\nv_mfma_f32_16x16x32_bf16 a[228:231], v[172:175], a[24:27], a[228:231]\nbuffer_load_dwordx4 a[88:91], v252, s[20:23], s66 offen sc1\nv_rcp_f32 v231, v231\nv_mfma_f32_16x16x32_bf16 v[20:23], v[168:171], a[4:7], 0\nR 164,187,24576\n"
"v_mul_f32 v230, v32, v230\nv_rcp_f32 v232, v232\nM 20,172,20\nbuffer_load_dwordx4 v220, s[16:19], s63 offen lds\nv_rcp_f32 v233, v233\nv_mfma_f32_16x16x32_bf16 v[4:7], v[168:171], a[0:3], 0\n"
"R 166,187,28672\nv_mul_f32 v231, v33, v231\nv_mul_f32 v232, v34, v232\nM 4,172,16\nG 4\nv_mul_f32 v233, v35, v233\n"
"v_cvt_pk_bf16_f32 v32, v230, v231\nv_cvt_pk_bf16_f32 v33, v232, v233\nv_mfma_f32_16x16x32_bf16 v[8:11], v[176:179], a[0:3], 0\nR 168,184,32768\nv_mul_f32 v234, v36, v36\nv_mul_f32 v235, v37, v37\n"
"M 8,180,16\nbuffer_load_dwordx4 a[92:95], v252, s[20:23], s67 offen sc1\nbuffer_load_dwordx4 v221, s[16:19], s63 offen offset:1024 lds\nv_mul_f32 v236, v38, v38\nv_mfma_f32_16x16x32_bf16 v[24:27], v[176:179], a[4:7], 0\nR 170,184,36864\n"
"s_add_u32 m0, s50, 81920\nv_mul_f32 v237, v39, v39\nM 24,180,20\ns_add_u32 s64, s68, 23071744\nv_fma_f32 v234, v234, v215, v214\nv_fma_f32 v235, v235, v215, v214\n"
"v_fma_f32 v236, v236, v215, v214\nv_mfma_f32_16x16x32_bf16 a[232:235], v[176:179], a[8:11], 0\nR 172,184,40960\ns_add_u32 s63, s57, 131072\nv_fma_f32 v237, v237, v215, v214\nv_mfma_f32_16x16x32_bf16 a[232:235], v[180:183], a[24:27], a[232:235]\n"
"s_add_u32 s65, s68, 23186432\nbuffer_load_dwordx4 v220, s[16:19], s63 offen lds\nv_mul_f32 v234, v36, v234\nv_mfma_f32_16x16x32_bf16 a[248:251], v[176:179], a[12:15], 0\nR 174,184,45056\nv_mul_f32 v235, v37, v235\n"
"v_mul_f32 v236, v38, v236\nv_mfma_f32_16x16x32_bf16 a[248:251], v[180:183], a[28:31], a[248:251]\nG 4\nv_exp_f32 v234, v234\nv_mul_f32 v237, v39, v237\nv_mfma_f32_16x16x32_bf16 a[252:255], v[160:163], a[12:15], 0\n"
"R 176,185,32768\nv_fma_f32 v234, v234, v216, v216\nv_exp_f32 v235, v235\nv_mfma_f32_16x16x32_bf16 a[252:255], v[164:167], a[28:31], a[252:255]\nbuffer_load_dwordx4 a[96:99], v252, s[20:23], s64 offen sc1\nbuffer_load_dwordx4 v221, s[16:19], s63 offen offset:1024 lds\n"
"v_exp_f32 v236, v236\nv_mfma_f32_16x16x32_bf16 a[236:239], v[160:163], a[8:11], 0\nR 178,185,36864\ns_add_u32 m0, s50, 90112\nv_exp_f32 v237, v237\nv_mfma_f32_16x16x32_bf16 a[236:239], v[164:167], a[24:27], a[236:239]\n"
"s_add_u32 s66, s69, 23071744\nv_rcp_f32 v234, v234\nv_fma_f32 v235, v235, v216, v216\nv_mfma_f32_16x16x32_bf16 v[28:31], v[160:163], a[4:7], 0\nR 180,185,40960\ns_add_u32 s63, s57, 655360\n"
"v_fma_f32 v236, v236, v216, v216\nM 28,164,20\nbuffer_load_dwordx4 a[100:103], v252, s[20:23], s65 offen sc1\nbuffer_load_dwordx4 v220, s[16:19], s63 offen lds\nv_fma_f32 v237, v237, v216, v216\nv_mfma_f32_16x16x32_bf16 v[12:15], v[160:163], a[0:3], 0\n"
"R 182,185,45056\nv_mul_f32 v234, v36, v234\nv_rcp_f32 v235, v235\nM 12,164,16\ns_waitcnt vmcnt(14)\ns_barrier\n"
"G 4\nv_rcp_f32 v236, v236\nM 0,168,32\nR 160,186,32768\nv_mul_f32 v235, v37, v235\nv_rcp_f32 v237, v237\n"
"M 0,172,48\ns_add_u32 s67, s69, 23186432\nbuffer_load_dwordx4 v221, s[16:19], s63 offen offset:1024 lds\nv_mul_f32 v236, v38, v236\nM 16,168,36\nR 162,186,36864\n"
"v_mul_f32 v237, v39, v237\nv_cvt_pk_bf16_f32 v34, v234, v235\nM 16,172,52\nbuffer_load_dwordx4 a[104:107], v252, s[20:23], s66 offen sc1\nv_cvt_pk_bf16_f32 v35, v236, v237\nv_mfma_f32_16x16x32_bf16 a[224:227], v[168:171], a[40:43], a[224:227]\n"
"R 164,186,40960\nv_mul_f32 v230, v40, v40\nv_mul_f32 v231, v41, v41\nv_mfma_f32_16x16x32_bf16 a[224:227], v[172:175], a[56:59], a[224:227]\nv_mul_f32 v232, v42, v42\nv_mul_f32 v233, v43, v43\n"
"v_fma_f32 v230, v230, v215, v214\nv_mfma_f32_16x16x32_bf16 a[240:243], v[168:171], a[44:47], a[240:243]\nR 166,186,45056\nv_fma_f32 v231, v231, v215, v214\nv_fma_f32 v232, v232, v215, v214\nv_mfma_f32_16x16x32_bf16 a[240:243], v[172:175], a[60:63], a[240:243]\n"
"G 4\nv_fma_f32 v233, v233, v215, v214\nv_mul_f32 v230, v40, v230\nv_mul_f32 v231, v41, v231\nv_mfma_f32_16x16x32_bf16 a[244:247], v[176:179], a[44:47], a[244:247]\nR 168,187,32768\n"
"v_mul_f32 v232, v42, v232\nv_mul_f32 v233, v43, v233\nv_mfma_f32_16x16x32_bf16 a[244:247], v[180:183], a[60:63], a[244:247]\nbuffer_load_dwordx4 a[108:111], v252, s[20:23], s67 offen sc1\nv_exp_f32 v230, v230\nv_mfma_f32_16x16x32_bf16 a[228:231], v[176:179], a[40:43], a[228:231]\n"
"R 170,187,36864\nv_fma_f32 v230, v230, v216, v216\nv_exp_f32 v231, v231\nv_mfma_f32_16x16x32_bf16 a[228:231], v[180:183], a[56:59], a[228:231]\ns_add_u32 s64, s68, 23078912\nv_exp_f32 v232, v232\n"
"v_exp_f32 v233, v233\nM 20,176,36\nR 172,187,40960\nv_fma_f32 v231, v231, v216, v216\nv_fma_f32 v232, v232, v216, v216\nM 20,180,52\n"
"s_add_u32 s65, s68, 23193600\nv_rcp_f32 v230, v230\nv_fma_f32 v233, v233, v216, v216\nM 4,176,32\nR 174,187,45056\ns_add_u32 m0, s50, 98304\n"
"v_rcp_f32 v231, v231\nM 4,180,48\nG 4\nv_rcp_f32 v232, v232\nv_rcp_f32 v233, v233\nM 8,160,32\n"
"R 176,184,49152\ns_add_u32 s63, s57, 196608\nv_mul_f32 v230, v40, v230\nM 8,164,48\nbuffer_load_dwordx4 a[112:115], v252, s[20:23], s64 offen sc1\nv_mul_f32 v231, v41, v231\n"
"M 24,160,36\nR 178,184,53248\nv_mul_f32 v232, v42, v232\nv_mul_f32 v233, v43, v233\nM 24,164,52\ns_add_u32 s66, s69, 23078912\n"
"buffer_load_dwordx4 v220, s[16:19], s63 offen lds\nv_cvt_pk_bf16_f32 v40, v230, v231\nv_mfma_f32_16x16x32_bf16 a[232:235], v[160:163], a[40:43], a[232:235]\nR 180,184,57344\nv_cvt_pk_bf16_f32 v41, v232, v233\nv_mul_f32 v234, v44, v44\n"
"v_mfma_f32_16x16x32_bf16 a[232:235], v[164:167], a[56:59], a[232:235]\nbuffer_load_dwordx4 a[116:119], v252, s[20:23], s65 offen sc1\nv_mul_f32 v235, v45, v45\nv_mfma_f32_16x16x32_bf16 a[248:251], v[160:163], a[44:47], a[248:251]\nR 182,184,61440\nv_mul_f32 v236, v46, v46\n"
"v_mul_f32 v237, v47, v47\nv_mfma_f32_16x16x32_bf16 a[248:251], v[164:167], a[60:63], a[248:251]\nbuffer_load_dwordx4 v221, s[16:19], s63 offen offset:1024 lds\nG 4\nv_fma_f32 v234, v234, v215, v214\nv_mfma_f32_16x16x32_bf16 a[252:255], v[168:171], a[44:47], a[252:255]\n"
"R 160,185,49152\ns_add_u32 m0, s50, 106496\nv_fma_f32 v235, v235, v215, v214\nv_mfma_f32_16x16x32_bf16 a[252:255], v[172:175], a[60:63], a[252:255]\ns_add_u32 s67, s69, 23193600\ns_add_u32 s63, s57, 720896\n"
"v_fma_f32 v236, v236, v215, v214\nv_fma_f32 v237, v237, v215, v214\nv_mfma_f32_16x16x32_bf16 a[236:239], v[168:171], a[40:43], a[236:239]\nR 162,185,53248\nv_mov_b32 v248, 0\nv_mul_f32 v234, v44, v234\n"
"v_mfma_f32_16x16x32_bf16 a[236:239], v[172:175], a[56:59], a[236:239]\nbuffer_load_dwordx4 a[120:123], v252, s[20:23], s66 offen sc1\nbuffer_load_dwordx4 v220, s[16:19], s63 offen lds\nv_mov_b32 v249, 0\nv_mul_f32 v235, v45, v235\nM 28,168,36\n"
"R 164,185,57344\ns_add_u32 s61, s82, 385048\ns_mov_b64 exec, 0xf\nbuffer_atomic_or_x2 v[248:249], v250, s[20:23], s61 offen sc0 sc1\ns_mov_b64 exec, -1\nv_mul_f32 v236, v46, v236\n"
"M 28,172,52\nbuffer_load_dwordx4 v221, s[16:19], s63 offen offset:1024 lds\nv_mul_f32 v237, v47, v237\nM 12,168,32\nR 166,185,61440\ns_add_u32 m0, s50, 114688\n"
"v_exp_f32 v234, v234\nM 12,172,48\nG 4\nv_exp_f32 v235, v235\nv_exp_f32 v236, v236\nM 96,176,64\n"
"R 168,186,49152\ns_add_u32 s63, s58, 0\nv_exp_f32 v237, v237\ns_waitcnt vmcnt(21)\nM 96,180,80\nbuffer_load_dwordx4 a[124:127], v252, s[20:23], s67 offen sc1\n"
"v_fma_f32 v234, v234, v216, v216\nM 112,176,68\nR 170,186,53248\nv_fma_f32 v235, v235, v216, v216\nv_fma_f32 v236, v236, v216, v216\ns_waitcnt vmcnt(21)\n"
"M 112,180,84\nbuffer_load_dwordx4 v220, s[16:19], s63 offen lds\nv_fma_f32 v237, v237, v216, v216\nM 128,176,72\nR 172,186,57344\nv_mul_f32 v230, v48, v48\n"
"v_rcp_f32 v234, v234\ns_waitcnt vmcnt(21)\nM 128,180,88\nbuffer_load_dwordx4 v221, s[16:19], s63 offen offset:1024 lds\nv_rcp_f32 v235, v235\nM 144,176,76\n"
"R 174,186,61440\ns_add_u32 m0, s50, 0\nv_rcp_f32 v236, v236\ns_waitcnt vmcnt(20)\nM 144,180,92\nG 4\n"
"v_rcp_f32 v237, v237\nv_mul_f32 v234, v44, v234\nM 148,160,76\nR 176,187,49152\ns_add_u32 s63, s58, 524288\nv_mul_f32 v235, v45, v235\n"
"M 148,164,92\nbuffer_load_dwordx4 v220, s[16:19], s63 offen lds\nv_mul_f32 v236, v46, v236\nM 132,160,72\nR 178,187,53248\nv_mul_f32 v237, v47, v237\n"
"v_cvt_pk_bf16_f32 v42, v234, v235\nM 132,164,88\nbuffer_store_dwordx4 v[32:35], v252, s[20:23], s84 offen offset:0 sc1\nv_cvt_pk_bf16_f32 v43, v236, v237\nM 116,160,68\nR 180,187,57344\n"
"v_mul_f32 v231, v49, v49\nv_mul_f32 v232, v50, v50\nM 116,164,84\nbuffer_load_dwordx4 v221, s[16:19], s63 offen offset:1024 lds\nv_mul_f32 v233, v51, v51\nM 100,160,64\n"
"R 182,187,61440\nv_fma_f32 v230, v230, v218, v217\nv_fma_f32 v231, v231, v218, v217\nM 100,164,80\nG 4\nv_fma_f32 v232, v232, v218, v217\n"
"v_fma_f32 v233, v233, v218, v217\nv_mul_f32 v230, v48, v230\nM 104,168,64\nR 160,188,0\ns_waitcnt vmcnt(16)\ns_barrier\n"
"v_mul_f32 v231, v49, v231\nM 104,172,80\nbuffer_store_dwordx4 v[40:43], v252, s[20:23], s84 offen offset:1024 sc1\nv_mul_f32 v232, v50, v232\nM 120,168,68\nR 162,188,4096\n"
"v_mul_f32 v233, v51, v233\nv_exp_f32 v230, v230\nM 120,172,84\ns_add_u32 m0, s50, 8192\nv_exp_f32 v231, v231\nv_exp_f32 v232, v232\n"
"M 136,168,72\nR 164,188,8192\nv_fma_f32 v230, v230, v219, v219\nv_exp_f32 v233, v233\nM 136,172,88\ns_add_u32 s63, s58, 65536\n"
"v_rcp_f32 v230, v230\nv_fma_f32 v231, v231, v219, v219\nM 152,168,76\nR 166,188,12288\nv_fma_f32 v232, v232, v219, v219\nv_fma_f32 v233, v233, v219, v219\n"
"M 152,172,92\nG 4\nv_rcp_f32 v231, v231\nv_rcp_f32 v232, v232\nM 156,176,76\nR 168,189,0\n"
"v_mul_f32 v230, v48, v230\nv_rcp_f32 v233, v233\nM 156,180,92\nbuffer_load_dwordx4 v220, s[16:19], s63 offen lds\nv_mul_f32 v231, v49, v231\nM 140,176,72\n"
"R 170,189,4096\nv_mul_f32 v232, v50, v232\nv_mul_f32 v233, v51, v233\nM 140,180,88\nv_cvt_pk_bf16_f32 v48, v230, v231\nv_cvt_pk_bf16_f32 v49, v232, v233\n"
"v_mul_f32 v234, v52, v52\nM 124,176,68\nR 172,189,8192\nv_mul_f32 v235, v53, v53\nv_mul_f32 v236, v54, v54\nM 124,180,84\n"
"buffer_load_dwordx4 v221, s[16:19], s63 offen offset:1024 lds\nv_mul_f32 v237, v55, v55\nM 108,176,64\nR 174,189,12288\ns_add_u32 m0, s50, 16384\nv_fma_f32 v234, v234, v218, v217\n"
"M 108,180,80\nG 4\nv_fma_f32 v235, v235, v218, v217\nv_fma_f32 v236, v236, v218, v217\nv_fma_f32 v237, v237, v218, v217\nM 96,160,96\n"
"R 176,190,0\ns_add_u32 s63, s58, 589824\nv_mul_f32 v234, v52, v234\ns_waitcnt vmcnt(16)\nM 96,164,112\nbuffer_load_dwordx4 v220, s[16:19], s63 offen lds\n"
"v_mul_f32 v235, v53, v235\nM 112,160,100\nR 178,190,4096\nv_mul_f32 v236, v54, v236\nv_mul_f32 v237, v55, v237\ns_waitcnt vmcnt(15)\n"
"M 112,164,116\nv_exp_f32 v234, v234\nv_exp_f32 v235, v235\nM 128,160,104\nR 180,190,8192\nv_fma_f32 v234, v234, v219, v219\n"
"v_exp_f32 v236, v236\ns_waitcnt vmcnt(13)\nM 128,164,120\nv_exp_f32 v237, v237\nv_fma_f32 v235, v235, v219, v219\nM 144,160,108\n"
"R 182,190,12288\nv_fma_f32 v236, v236, v219, v219\nv_fma_f32 v237, v237, v219, v219\ns_waitcnt vmcnt(9)\nM 144,164,124\nG 4\n"
"v_rcp_f32 v234, v234\nv_rcp_f32 v235, v235\nM 148,168,108\nR 160,191,0\nv_mul_f32 v234, v52, v234\nv_rcp_f32 v236, v236\n"
"M 148,172,124\ns_add_u32 s64, s68, 23074816\nv_rcp_f32 v237, v237\nv_mul_f32 v235, v53, v235\nM 132,168,104\nR 162,191,4096\n"
"v_cmp_eq_u64 s[86:87], s[46:47], v[248:249]\nv_mul_f32 v236, v54, v236\nM 132,172,120\nbuffer_load_dwordx4 v221, s[16:19], s63 offen offset:1024 lds\nv_mul_f32 v237, v55, v237\nM 116,168,100\n"
"R 164,191,8192\ns_and_b32 s62, s86, 0xf\ns_cmp_eq_u32 s62, 0xf\ns_cbranch_scc0 Lq447\nLq448:\nM 116,172,116\n"
"s_add_u32 s65, s68, 23189504\nv_cvt_pk_bf16_f32 v50, v234, v235\nv_cvt_pk_bf16_f32 v51, v236, v237\nM 100,168,96\nR 166,191,12288\nv_mul_f32 v230, v56, v56\n"
"M 100,172,112\nG 4\nv_mul_f32 v231, v57, v57\nv_mul_f32 v232, v58, v58\nM 104,176,96\nR 168,188,16384\n"
"v_mul_f32 v233, v59, v59\nM 104,180,112\nbuffer_load_dwordx4 a[128:131], v252, s[20:23], s64 offen sc1\nM 120,176,100\nR 170,188,20480\nv_fma_f32 v230, v230, v218, v217\n"
"M 120,180,116\ns_add_u32 s66, s69, 23074816\nv_fma_f32 v231, v231, v218, v217\nv_fma_f32 v232, v232, v218, v217\nM 136,176,104\nR 172,188,24576\n"
"v_fma_f32 v233, v233, v218, v217\nM 136,180,120\nbuffer_load_dwordx4 a[132:135], v252, s[20:23], s65 offen sc1\nM 152,176,108\nR 174,188,28672\nv_mul_f32 v230, v56, v230\n"
"M 152,180,124\nG 4\nv_exp_f32 v230, v230\nM 156,160,108\nR 176,189,16384\nv_mul_f32 v231, v57, v231\n"
"M 156,164,124\ns_add_u32 s67, s69, 23189504\nv_exp_f32 v231, v231\nM 140,160,104\nR 178,189,20480\nv_mul_f32 v232, v58, v232\n"
"M 140,164,120\nbuffer_load_dwordx4 a[136:139], v252, s[20:23], s66 offen sc1\nM 124,160,100\nR 180,189,24576\nv_mul_f32 v233, v59, v233\nM 124,164,116\n"
"buffer_store_dwordx4 v[48:51], v252, s[20:23], s85 offen offset:0 sc1\nM 108,160,96\nR 182,189,28672\ns_add_u32 m0, s50, 24576\nM 108,164,112\ns_waitcnt vmcnt(9)\n"
"s_barrier\nG 4\nM 0,168,64\nR 160,190,16384\ns_add_u32 s63, s58, 131072\nM 0,172,80\n"
"buffer_load_dwordx4 a[140:143], v252, s[20:23], s67 offen sc1\nM 16,168,68\nR 162,190,20480\nv_fma_f32 v230, v230, v219, v219\nM 16,172,84\ns_add_u32 s64, s68, 23081984\n"
"v_exp_f32 v232, v232\nv_mfma_f32_16x16x32_bf16 a[224:227], v[168:171], a[72:75], a[224:227]\nR 164,190,24576\nbuffer_load_dwordx4 v220, s[16:19], s63 offen lds\nv_mfma_f32_16x16x32_bf16 a[224:227], v[172:175], a[88:91], a[224:227]\ns_add_u32 s65, s68, 23196672\n"
"v_exp_f32 v233, v233\nv_mfma_f32_16x16x32_bf16 a[240:243], v[168:171], a[76:79], a[240:243]\nR 166,190,28672\nv_fma_f32 v231, v231, v219, v219\nv_mfma_f32_16x16x32_bf16 a[240:243], v[172:175], a[92:95], a[240:243]\nG 4\n"
"v_rcp_f32 v230, v230\nv_mfma_f32_16x16x32_bf16 a[244:247], v[176:179], a[76:79], a[244:247]\nR 168,191,16384\nbuffer_load_dwordx4 v221, s[16:19], s63 offen offset:1024 lds\nv_mfma_f32_16x16x32_bf16 a[244:247], v[180:183], a[92:95], a[244:247]\nbuffer_load_dwordx4 a[144:147], v252, s[20:23], s64 offen sc1\n"
"v_mfma_f32_16x16x32_bf16 a[228:231], v[176:179], a[72:75], a[228:231]\nR 170,191,20480\ns_add_u32 m0, s50, 32768\nv_mfma_f32_16x16x32_bf16 a[228:231], v[180:183], a[88:91], a[228:231]\ns_add_u32 s66, s69, 23081984\ns_add_u32 s63, s58, 655360\n"
"v_fma_f32 v232, v232, v219, v219\nM 20,176,68\nR 172,191,24576\nbuffer_load_dwordx4 v220, s[16:19], s63 offen lds\nM 20,180,84\nbuffer_load_dwordx4 a[148:151], v252, s[20:23], s65 offen sc1\n"
"M 4,176,64\nR 174,191,28672\nv_fma_f32 v233, v233, v219, v219\nM 4,180,80\nG 4\nv_rcp_f32 v231, v231\n"
"M 8,160,64\nR 176,188,32768\nbuffer_load_dwordx4 v221, s[16:19], s63 offen offset:1024 lds\nM 8,164,80\ns_add_u32 s67, s69, 23196672\nv_rcp_f32 v232, v232\n"
"M 24,160,68\nR 178,188,36864\nv_mul_f32 v230, v56, v230\nM 24,164,84\nbuffer_load_dwordx4 a[152:155], v252, s[20:23], s66 offen sc1\nv_mfma_f32_16x16x32_bf16 a[232:235], v[160:163], a[72:75], a[232:235]\n"
"R 180,188,40960\nv_mul_f32 v231, v57, v231\nv_mfma_f32_16x16x32_bf16 a[232:235], v[164:167], a[88:91], a[232:235]\nv_rcp_f32 v233, v233\nv_mfma_f32_16x16x32_bf16 a[248:251], v[160:163], a[76:79], a[248:251]\nR 182,188,45056\n"
"v_mul_f32 v232, v58, v232\nv_mfma_f32_16x16x32_bf16 a[248:251], v[164:167], a[92:95], a[248:251]\nG 4\nv_mul_f32 v233, v59, v233\nv_cvt_pk_bf16_f32 v56, v230, v231\nv_mfma_f32_16x16x32_bf16 a[252:255], v[168:171], a[76:79], a[252:255]\n"
"R 160,189,32768\nv_cvt_pk_bf16_f32 v57, v232, v233\nv_mfma_f32_16x16x32_bf16 a[252:255], v[172:175], a[92:95], a[252:255]\nbuffer_load_dwordx4 a[156:159], v252, s[20:23], s67 offen sc1\nv_mfma_f32_16x16x32_bf16 a[236:239], v[168:171], a[72:75], a[236:239]\nR 162,189,36864\n"
"v_mul_f32 v234, v60, v60\nv_mfma_f32_16x16x32_bf16 a[236:239], v[172:175], a[88:91], a[236:239]\nv_mul_f32 v235, v61, v61\nv_mul_f32 v236, v62, v62\nM 28,168,68\nR 164,189,40960\n"
"v_mul_f32 v237, v63, v63\nM 28,172,84\ns_add_u32 m0, s50, 40960\nv_fma_f32 v234, v234, v218, v217\nv_fma_f32 v235, v235, v218, v217\nM 12,168,64\n"
"R 166,189,45056\nv_fma_f32 v236, v236, v218, v217\nM 12,172,80\nG 4\nv_fma_f32 v237, v237, v218, v217\nv_mul_f32 v234, v60, v234\n"
"M 0,176,96\nR 168,190,32768\nv_mul_f32 v235, v61, v235\nM 0,180,112\ns_add_u32 s63, s58, 196608\nv_exp_f32 v234, v234\n"
"M 16,176,100\nR 170,190,36864\nv_mul_f32 v236, v62, v236\nM 16,180,116\nbuffer_load_dwordx4 v220, s[16:19], s63 offen lds\nv_mfma_f32_16x16x32_bf16 a[224:227], v[176:179], a[104:107], a[224:227]\n"
"R 172,190,40960\nv_mul_f32 v237, v63, v237\nv_mfma_f32_16x16x32_bf16 a[224:227], v[180:183], a[120:123], a[224:227]\nv_exp_f32 v235, v235\nv_mfma_f32_16x16x32_bf16 a[240:243], v[176:179], a[108:111], a[240:243]\nR 174,190,45056\n"
"v_fma_f32 v234, v234, v219, v219\nv_mfma_f32_16x16x32_bf16 a[240:243], v[180:183], a[124:127], a[240:243]\nG 4\nv_exp_f32 v236, v236\nv_mfma_f32_16x16x32_bf16 a[244:247], v[160:163], a[108:111], a[244:247]\nR 176,191,32768\n"
"v_fma_f32 v235, v235, v219, v219\nv_mfma_f32_16x16x32_bf16 a[244:247], v[164:167], a[124:127], a[244:247]\nbuffer_load_dwordx4 v221, s[16:19], s63 offen offset:1024 lds\nv_mfma_f32_16x16x32_bf16 a[228:231], v[160:163], a[104:107], a[228:231]\nR 178,191,36864\ns_add_u32 s63, s58, 720896\n"
"v_mfma_f32_16x16x32_bf16 a[228:231], v[164:167], a[120:123], a[228:231]\ns_add_u32 m0, s50, 49152\nv_exp_f32 v237, v237\nM 20,160,100\nR 180,191,40960\nv_fma_f32 v236, v236, v219, v219\n"
"M 20,164,116\nbuffer_load_dwordx4 v220, s[16:19], s63 offen lds\nM 4,160,96\nR 182,191,45056\nv_fma_f32 v237, v237, v219, v219\nM 4,164,112\n"
"G 4\nv_rcp_f32 v234, v234\nM 8,168,96\nR 160,188,49152\nv_mul_f32 v234, v60, v234\nM 8,172,112\n"
"buffer_load_dwordx4 v221, s[16:19], s63 offen offset:1024 lds\nM 24,168,100\nR 162,188,53248\ns_add_u32 s63, s58, 393216\nM 24,172,116\ns_add_u32 m0, s50, 57344\n"
"v_rcp_f32 v235, v235\nv_mfma_f32_16x16x32_bf16 a[232:235], v[168:171], a[104:107], a[232:235]\nR 164,184,0\nv_mul_f32 v235, v61, v235\nv_mfma_f32_16x16x32_bf16 a[232:235], v[172:175], a[120:123], a[232:235]\nLpg14:\n"
"s_cbranch_execz LretO0n\nbuffer_load_dwordx4 v220, s[16:19], s63 offen lds\nv_mfma_f32_16x16x32_bf16 a[248:251], v[168:171], a[108:111], a[248:251]\nR 166,184,4096\nv_cvt_pk_bf16_f32 v58, v234, v235\nv_mfma_f32_16x16x32_bf16 a[248:251], v[172:175], a[124:127], a[248:251]\n"
"G 4\nv_rcp_f32 v236, v236\nv_mfma_f32_16x16x32_bf16 a[252:255], v[176:179], a[108:111], a[252:255]\nR 168,189,49152\nv_mul_f32 v236, v62, v236\nv_mfma_f32_16x16x32_bf16 a[252:255], v[180:183], a[124:127], a[252:255]\n"
"buffer_load_dwordx4 v221, s[16:19], s63 offen offset:1024 lds\nv_mfma_f32_16x16x32_bf16 a[236:239], v[176:179], a[104:107], a[236:239]\nR 170,189,53248\nv_rcp_f32 v237, v237\nv_mfma_f32_16x16x32_bf16 a[236:239], v[180:183], a[120:123], a[236:239]\ns_add_u32 m0, s50, 65536\n"
"v_mul_f32 v237, v63, v237\nv_cvt_pk_bf16_f32 v59, v236, v237\nM 28,176,100\nR 172,185,0\nM 28,180,116\ns_add_u32 s63, s58, 458752\n"
"buffer_store_dwordx4 v[56:59], v252, s[20:23], s85 offen offset:1024 sc1\nM 12,176,96\nR 174,185,4096\nM 12,180,112\ns_waitcnt vmcnt(9)\ns_barrier\n"
"G 4\nv_mfma_f32_16x16x32_bf16 v[32:35], v[160:163], a[0:3], 0\nR 176,190,49152\nM 32,164,16\nbuffer_load_dwordx4 v220, s[16:19], s63 offen lds\nv_mfma_f32_16x16x32_bf16 v[48:51], v[160:163], a[4:7], 0\n"
"R 178,190,53248\nM 48,164,20\nv_mfma_f32_16x16x32_bf16 v[64:67], v[160:163], a[8:11], 0\nR 180,186,0\nM 64,164,24\nv_mfma_f32_16x16x32_bf16 v[80:83], v[160:163], a[12:15], 0\n"
"R 182,186,4096\nM 80,164,28\nG 4\nv_mfma_f32_16x16x32_bf16 v[84:87], v[168:171], a[12:15], 0\nR 160,191,49152\nM 84,172,28\n"
"buffer_load_dwordx4 v221, s[16:19], s63 offen offset:1024 lds\nv_mfma_f32_16x16x32_bf16 v[68:71], v[168:171], a[8:11], 0\nR 162,191,53248\nM 68,172,24\ns_add_u32 m0, s50, 73728\nv_mfma_f32_16x16x32_bf16 v[52:55], v[168:171], a[4:7], 0\n"
"R 164,187,0\nM 52,172,20\ns_add_u32 s63, s56, 393216\nv_mfma_f32_16x16x32_bf16 v[36:39], v[168:171], a[0:3], 0\nR 166,187,4096\nM 36,172,16\n"
"G 4\nv_mfma_f32_16x16x32_bf16 v[40:43], v[176:179], a[0:3], 0\nR 168,184,8192\nM 40,180,16\nbuffer_load_dwordx4 v220, s[16:19], s63 offen lds\nv_mfma_f32_16x16x32_bf16 v[56:59], v[176:179], a[4:7], 0\n"
"R 170,184,12288\nM 56,180,20\nv_mfma_f32_16x16x32_bf16 v[72:75], v[176:179], a[8:11], 0\nR 172,184,16384\nM 72,180,24\nv_mfma_f32_16x16x32_bf16 v[88:91], v[176:179], a[12:15], 0\n"
"R 174,184,20480\nM 88,180,28\nG 4\nv_mfma_f32_16x16x32_bf16 v[92:95], v[160:163], a[12:15], 0\nR 176,185,8192\nM 92,164,28\n"
"buffer_load_dwordx4 v221, s[16:19], s63 offen offset:1024 lds\nv_mfma_f32_16x16x32_bf16 v[76:79], v[160:163], a[8:11], 0\nR 178,185,12288\nM 76,164,24\ns_add_u32 m0, s50, 81920\nv_mfma_f32_16x16x32_bf16 v[60:63], v[160:163], a[4:7], 0\n"
"R 180,185,16384\nM 60,164,20\ns_add_u32 s63, s56, 458752\nv_mfma_f32_16x16x32_bf16 v[44:47], v[160:163], a[0:3], 0\nR 182,185,20480\nM 44,164,16\n"
"G 4\nM 32,168,32\nR 160,186,8192\nM 32,172,48\nbuffer_load_dwordx4 v220, s[16:19], s63 offen lds\nM 48,168,36\n"
"R 162,186,12288\nM 48,172,52\nM 64,168,40\nR 164,186,16384\nM 64,172,56\nbuffer_load_dwordx4 v221, s[16:19], s63 offen offset:1024 lds\n"
"M 80,168,44\nR 166,186,20480\ns_add_u32 m0, s50, 90112\nM 80,172,60\nG 4\nM 84,176,44\n"
"R 168,187,8192\ns_add_u32 s63, s57, 393216\nM 84,180,60\nbuffer_load_dwordx4 v220, s[16:19], s63 offen lds\nM 68,176,40\nR 170,187,12288\n"
"v_mov_b32 v226, s46\nM 68,180,56\nv_mov_b32 v227, s47\nM 52,176,36\nR 172,187,16384\ns_add_u32 s61, s81, 385040\n"
"s_lshl_b32 s62, s44, 5\ns_add_u32 s61, s61, s62\ns_waitcnt vmcnt(7)\ns_mov_b64 exec, 1\nbuffer_atomic_swap_x2 v[226:227], off, s[20:23], s61 sc1\ns_mov_b64 exec, -1\n"
"M 52,180,52\nM 36,176,32\nR 174,187,20480\nM 36,180,48\nG 4\nM 40,160,32\n"
"R 176,184,24576\nM 40,164,48\nbuffer_load_dwordx4 v221, s[16:19], s63 offen offset:1024 lds\nM 56,160,36\nR 178,184,28672\nM 56,164,52\n"
"s_add_u32 m0, s50, 98304\nM 72,160,40\nR 180,184,32768\nM 72,164,56\ns_add_u32 s63, s57, 458752\nM 88,160,44\n"
"R 182,184,36864\nM 88,164,60\nG 4\nM 92,168,44\nR 160,185,24576\nM 92,172,60\n"
"buffer_load_dwordx4 v220, s[16:19], s63 offen lds\nM 76,168,40\nR 162,185,28672\nM 76,172,56\nM 60,168,36\nR 164,185,32768\n"
"M 60,172,52\nbuffer_load_dwordx4 v221, s[16:19], s63 offen offset:1024 lds\nM 44,168,32\nR 166,185,36864\ns_add_u32 s63, s56, 262144\nM 44,172,48\n"
"s_waitcnt vmcnt(9)\ns_barrier\nG 4\nM 32,176,64\nR 168,186,24576\nM 32,180,80\n"
"s_add_u32 m0, s50, 106496\nM 48,176,68\nR 170,186,28672\nM 48,180,84\nbuffer_load_dwordx4 v220, s[16:19], s63 offen lds\nM 64,176,72\n"
"R 172,186,32768\nM 64,180,88\nM 80,176,76\nR 174,186,36864\nM 80,180,92\nG 4\n"
"M 84,160,76\nR 176,187,24576\nM 84,164,92\nbuffer_load_dwordx4 v221, s[16:19], s63 offen offset:1024 lds\nM 68,160,72\nR 178,187,28672\n"
"M 68,164,88\ns_add_u32 m0, s50, 114688\nM 52,160,68\nR 180,187,32768\nv_mov_b32 v248, 0\nM 52,164,84\n"
"s_add_u32 s63, s56, 786432\nv_mov_b32 v249, 0\nM 36,160,64\nR 182,187,36864\ns_add_u32 s61, s82, 385040\ns_mov_b64 exec, 0xf\n"
"buffer_atomic_or_x2 v[248:249], v250, s[20:23], s61 offen sc0 sc1\ns_mov_b64 exec, -1\nM 36,164,80\nG 4\nM 40,168,64\nR 160,184,40960\n"
"M 40,172,80\nbuffer_load_dwordx4 v220, s[16:19], s63 offen lds\nM 56,168,68\nR 162,184,45056\nM 56,172,84\nM 72,168,72\n"
"R 164,184,49152\nM 72,172,88\nM 88,168,76\nR 166,184,53248\nM 88,172,92\nG 4\n"
"M 92,176,76\nR 168,185,40960\nM 92,180,92\nbuffer_load_dwordx4 v221, s[16:19], s63 offen offset:1024 lds\nM 76,176,72\nR 170,185,45056\n"
"M 76,180,88\ns_add_u32 m0, s50, 0\nM 60,176,68\nR 172,185,49152\nM 60,180,84\ns_add_u32 s63, s56, 327680\n"
"M 44,176,64\nR 174,185,53248\nM 44,180,80\nG 4\nM 32,160,96\nR 176,186,40960\n"
"M 32,164,112\nbuffer_load_dwordx4 v220, s[16:19], s63 offen lds\nM 48,160,100\nR 178,186,45056\nM 48,164,116\nM 64,160,104\n"
"R 180,186,49152\nM 64,164,120\nbuffer_load_dwordx4 v221, s[16:19], s63 offen offset:1024 lds\nM 80,160,108\nR 182,186,53248\ns_add_u32 m0, s50, 8192\n"
"M 80,164,124\nG 4\nM 84,168,108\nR 160,187,40960\ns_add_u32 s63, s56, 851968\nM 84,172,124\n"
"buffer_load_dwordx4 v220, s[16:19], s63 offen lds\nM 68,168,104\nR 162,187,45056\nM 68,172,120\nM 52,168,100\nR 164,187,49152\n"
"M 52,172,116\nbuffer_load_dwordx4 v221, s[16:19], s63 offen offset:1024 lds\nM 36,168,96\nR 166,187,53248\ns_add_u32 m0, s50, 16384\nM 36,172,112\n"
"G 4\nM 40,176,96\nR 168,184,57344\ns_add_u32 s63, s57, 262144\nM 40,180,112\nbuffer_load_dwordx4 v220, s[16:19], s63 offen lds\n"
"M 56,176,100\nR 170,184,61440\nM 56,180,116\nM 72,176,104\nR 172,188,0\nM 72,180,120\n"
"M 88,176,108\nR 174,188,4096\ns_waitcnt vmcnt(15)\ns_barrier\nM 88,180,124\nG 4\n"
"M 92,160,108\nR 176,185,57344\nM 92,164,124\ns_add_u32 s64, s68, 23072768\nM 76,160,104\nR 178,185,61440\n"
"M 76,164,120\nbuffer_load_dwordx4 v221, s[16:19], s63 offen offset:1024 lds\nM 60,160,100\nR 180,189,0\ns_waitcnt vmcnt(8)\nv_cmp_eq_u64 s[86:87], s[46:47], v[248:249]\n"
"M 60,164,116\ns_add_u32 s65, s68, 23187456\nM 44,160,96\nR 182,189,4096\ns_and_b32 s62, s86, 0xf\ns_cmp_eq_u32 s62, 0xf\n"
"s_cbranch_scc0 Lq466\nLq467:\nM 44,164,112\nG 4\nM 32,168,128\nR 160,186,57344\n"
"M 32,172,144\nbuffer_load_dwordx4 a[160:163], v252, s[20:23], s64 offen sc1\nM 48,168,132\nR 162,186,61440\nM 48,172,148\ns_add_u32 s66, s69, 23072768\n"
"M 64,168,136\nR 164,190,0\nM 64,172,152\nbuffer_load_dwordx4 a[164:167], v252, s[20:23], s65 offen sc1\nM 80,168,140\nR 166,190,4096\n"
"M 80,172,156\nG 4\nM 84,176,140\nR 168,187,57344\nM 84,180,156\ns_add_u32 s67, s69, 23187456\n"
"M 68,176,136\nR 170,187,61440\nM 68,180,152\nbuffer_load_dwordx4 a[168:171], v252, s[20:23], s66 offen sc1\nM 52,176,132\nR 172,191,0\n"
"M 52,180,148\ns_add_u32 s64, s68, 23079936\nM 36,176,128\nR 174,191,4096\nM 36,180,144\nG 4\n"
"M 40,160,128\nR 176,188,8192\nM 40,164,144\nbuffer_load_dwordx4 a[172:175], v252, s[20:23], s67 offen sc1\nM 56,160,132\nR 178,188,12288\n"
"M 56,164,148\ns_add_u32 s65, s68, 23194624\nM 72,160,136\nR 180,188,16384\nLq579:\ns_lshr_b32 s60, s45, 3\n"
"s_lshl2_add_u32 s60, s60, s40\ns_lshl_b32 s60, s60, 8\nv_lshl_add_u32 v224, v252, 3, s60\nv_add_u32 v224, Lq579+4096-LqPC, v224\nv_min_u32 v224, LqE-LqPC-4, v224\ns_mov_b64 exec, 3\n"
"global_load_dword v224, v224, s[98:99]\ns_mov_b64 exec, -1\nM 72,164,152\nbuffer_load_dwordx4 a[176:179], v252, s[20:23], s64 offen sc1\nM 88,160,140\nR 182,188,20480\n"
"s_add_u32 m0, s50, 24576\nM 88,164,156\nG 4\nM 92,168,140\nR 160,189,8192\ns_add_u32 s63, s57, 786432\n"
"M 92,172,156\ns_add_u32 s66, s69, 23079936\nM 76,168,136\nR 162,189,12288\nM 76,172,152\nbuffer_load_dwordx4 a[180:183], v252, s[20:23], s65 offen sc1\n"
"buffer_load_dwordx4 v220, s[16:19], s63 offen lds\nM 60,168,132\nR 164,189,16384\nM 60,172,148\ns_add_u32 s67, s69, 23194624\nM 44,168,128\n"
"R 166,189,20480\nM 44,172,144\nbuffer_load_dwordx4 v221, s[16:19], s63 offen offset:1024 lds\nG 4\nM 96,176,128\nR 168,190,8192\n"
"s_add_u32 m0, s50, 32768\nM 96,180,144\nbuffer_load_dwordx4 a[184:187], v252, s[20:23], s66 offen sc1\nM 112,176,132\nR 170,190,12288\ns_add_u32 s63, s57, 327680\n"
"M 112,180,148\ns_add_u32 s64, s68, 23073792\nbuffer_load_dwordx4 v220, s[16:19], s63 offen lds\nM 128,176,136\nR 172,190,16384\nM 128,180,152\n"
"buffer_load_dwordx4 a[188:191], v252, s[20:23], s67 offen sc1\nM 144,176,140\nR 174,190,20480\nM 144,180,156\nbuffer_load_dwordx4 v221, s[16:19], s63 offen offset:1024 lds\nG 4\n"
"M 148,160,140\nR 176,191,8192\ns_waitcnt vmcnt(19)\ns_barrier\nM 148,164,156\ns_add_u32 s65, s68, 23188480\n"
"s_add_u32 s63, s57, 851968\nM 132,160,136\nR 178,191,12288\ns_add_u32 m0, s50, 40960\nM 132,164,152\nbuffer_load_dwordx4 a[192:195], v252, s[20:23], s64 offen sc1\n"
"M 116,160,132\nR 180,191,16384\nbuffer_load_dwordx4 v220, s[16:19], s63 offen lds\nM 116,164,148\ns_add_u32 s66, s69, 23073792\nM 100,160,128\n"
"R 182,191,20480\nM 100,164,144\nG 4\nM 104,168,128\nR 160,188,24576\nbuffer_load_dwordx4 v221, s[16:19], s63 offen offset:1024 lds\n"
"M 104,172,144\nbuffer_load_dwordx4 a[196:199], v252, s[20:23], s65 offen sc1\nM 120,168,132\nR 162,188,28672\nM 120,172,148\ns_add_u32 s67, s69, 23188480\n"
"M 136,168,136\nR 164,188,32768\nM 136,172,152\nbuffer_load_dwordx4 a[200:203], v252, s[20:23], s66 offen sc1\nM 152,168,140\nR 166,188,36864\n"
"M 152,172,156\nG 4\nM 156,176,140\nR 168,189,24576\nM 156,180,156\nbuffer_load_dwordx4 a[204:207], v252, s[20:23], s67 offen sc1\n"
"M 140,176,136\nR 170,189,28672\nM 140,180,152\ns_add_u32 s64, s68, 23080960\nM 124,176,132\nR 172,189,32768\n"
"M 124,180,148\ns_add_u32 s65, s68, 23195648\nM 108,176,128\nR 174,189,36864\nM 108,180,144\nG 4\n"
"M 0,160,128\nR 176,190,24576\nM 0,164,144\nbuffer_load_dwordx4 a[208:211], v252, s[20:23], s64 offen sc1\nM 16,160,132\nR 178,190,28672\n"
"M 16,164,148\ns_add_u32 s66, s69, 23080960\nv_mfma_f32_16x16x32_bf16 a[224:227], v[160:163], a[136:139], a[224:227]\nR 180,190,32768\nv_mfma_f32_16x16x32_bf16 a[224:227], v[164:167], a[152:155], a[224:227]\nbuffer_load_dwordx4 a[212:215], v252, s[20:23], s65 offen sc1\n"
"v_mfma_f32_16x16x32_bf16 a[240:243], v[160:163], a[140:143], a[240:243]\nR 182,190,36864\nv_mfma_f32_16x16x32_bf16 a[240:243], v[164:167], a[156:159], a[240:243]\nG 4\nv_mfma_f32_16x16x32_bf16 a[244:247], v[168:171], a[140:143], a[244:247]\nR 160,191,24576\n"
"v_mfma_f32_16x16x32_bf16 a[244:247], v[172:175], a[156:159], a[244:247]\nbuffer_load_dwordx4 a[216:219], v252, s[20:23], s66 offen sc1\nv_mfma_f32_16x16x32_bf16 a[228:231], v[168:171], a[136:139], a[228:231]\nR 162,191,28672\nv_mfma_f32_16x16x32_bf16 a[228:231], v[172:175], a[152:155], a[228:231]\ns_add_u32 s67, s69, 23195648\n"
"M 20,168,132\nR 164,191,32768\nM 20,172,148\nM 4,168,128\nR 166,191,36864\nM 4,172,144\n"
"G 4\nM 8,176,128\nR 168,188,40960\nM 8,180,144\nbuffer_load_dwordx4 a[220:223], v252, s[20:23], s67 offen sc1\nM 24,176,132\n"
"R 170,188,45056\nM 24,180,148\ns_add_u32 m0, s50, 49152\nv_mfma_f32_16x16x32_bf16 a[232:235], v[176:179], a[136:139], a[232:235]\nR 172,188,49152\nv_mfma_f32_16x16x32_bf16 a[232:235], v[180:183], a[152:155], a[232:235]\n"
"s_add_u32 s63, s58, 262144\nv_mfma_f32_16x16x32_bf16 a[248:251], v[176:179], a[140:143], a[248:251]\nR 174,188,53248\nv_mfma_f32_16x16x32_bf16 a[248:251], v[180:183], a[156:159], a[248:251]\nG 4\nv_mfma_f32_16x16x32_bf16 a[252:255], v[160:163], a[140:143], a[252:255]\n"
"R 176,189,40960\nv_mfma_f32_16x16x32_bf16 a[252:255], v[164:167], a[156:159], a[252:255]\nbuffer_load_dwordx4 v220, s[16:19], s63 offen lds\nv_mfma_f32_16x16x32_bf16 a[236:239], v[160:163], a[136:139], a[236:239]\nR 178,189,45056\ns_add_u32 s84, s52, 2048\n"
"v_mfma_f32_16x16x32_bf16 a[236:239], v[164:167], a[152:155], a[236:239]\nv_lshrrev_b32 v244, 5, v252\nv_lshlrev_b32 v244, 5, v244\nM 28,160,132\nR 180,189,49152\nv_bfe_u32 v245, v252, 4, 1\n"
"M 28,164,148\nbuffer_load_dwordx4 v221, s[16:19], s63 offen offset:1024 lds\nM 12,160,128\nR 182,189,53248\nM 12,164,144\ns_waitcnt vmcnt(9)\n"
"s_barrier\nG 4\nM 96,168,160\nR 160,190,40960\nM 96,172,176\ns_add_u32 m0, s50, 57344\n"
"M 112,168,164\nR 162,190,45056\nM 112,172,180\ns_add_u32 s63, s58, 786432\nM 128,168,168\nR 164,190,49152\n"
"v_lshrrev_b32 v230, 8, v252\nM 128,172,184\nbuffer_load_dwordx4 v220, s[16:19], s63 offen lds\nM 144,168,172\nR 166,190,53248\nv_lshlrev_b32 v230, 3, v230\n"
"M 144,172,188\nG 4\nM 148,176,172\nR 168,191,40960\nM 148,180,188\ns_and_b32 s60, s41, 7\n"
"s_lshl2_add_u32 s60, s60, s40\nv_add_u32 v244, s60, v244\nM 132,176,168\nR 170,191,45056\nv_mul_u32_u24 v244, 6144, v244\nLpg15:\n"
"s_cbranch_execz LretO0n\nM 132,180,184\ns_sub_u32 s60, s84, s84\ns_add_u32 s60, s60, 2048\nv_mul_lo_u32 v245, v245, s60\nM 116,176,164\n"
"R 172,191,49152\nv_add_u32 v244, v244, v245\nM 116,180,180\nbuffer_load_dwordx2 v[232:233], v230, s[36:39], s53 offen offset:0\nM 100,176,160\nR 174,191,53248\n"
"s_bfm_b64 exec, 16, 0\nbuffer_load_dword v245, v244, s[4:7], s84 offen\ns_mov_b64 exec, -1\nM 100,180,176\nG 4\nM 104,160,160\n"
"R 176,184,0\nM 104,164,176\nbuffer_load_dwordx4 v221, s[16:19], s63 offen offset:1024 lds\nM 120,160,164\nR 178,184,4096\nM 120,164,180\n"
"s_add_u32 m0, s50, 65536\nM 136,160,168\nR 180,184,8192\nbuffer_load_dwordx2 v[234:235], v230, s[36:39], s53 offen offset:32\nM 136,164,184\ns_add_u32 s63, s58, 327680\n"
"buffer_load_dwordx2 v[236:237], v230, s[36:39], s53 offen offset:64\nM 152,160,172\nR 182,184,12288\nM 152,164,188\nbuffer_load_dwordx2 v[238:239], v230, s[36:39], s53 offen offset:96\nG 4\n"
"M 156,168,172\nR 160,185,0\nv_bfe_u32 v242, v252, 4, 4\nM 156,172,188\nbuffer_load_dwordx4 v220, s[16:19], s63 offen lds\nM 140,168,168\n"
"R 162,185,4096\nv_lshrrev_b32 v243, 8, v252\nM 140,172,184\nv_lshlrev_b32 v243, 2, v243\nv_sub_u32 v242, v242, v243\nM 124,168,164\n"
"R 164,185,8192\nv_lshlrev_b32 v243, 4, v242\nM 124,172,180\ns_mov_b64 s[60:61], 0x3f80\nv_lshlrev_b64 v[208:209], v243, s[60:61]\nM 108,168,160\n"
"R 166,185,12288\ns_bfm_b64 exec, 16, 16\nbuffer_load_dword v245, v244, s[4:7], s84 offen\ns_mov_b64 exec, -1\nM 108,172,176\nG 4\n"
"M 96,176,192\nR 168,186,0\ns_waitcnt vmcnt(14)\nM 96,180,208\nbuffer_load_dwordx4 v221, s[16:19], s63 offen offset:1024 lds\nM 112,176,196\n"
"R 170,186,4096\ns_waitcnt vmcnt(14)\nM 112,180,212\ns_add_u32 m0, s50, 73728\nM 128,176,200\nR 172,186,8192\n"
"s_waitcnt vmcnt(13)\nM 128,180,216\ns_add_u32 s63, s58, 851968\nM 144,176,204\nR 174,186,12288\ns_waitcnt vmcnt(12)\n"
"M 144,180,220\nG 4\nv_cmp_gt_u32 s[60:61], 4, v242\ns_nop 1\nv_cndmask_b32 v212, 0, v208, s[60:61]\nv_cndmask_b32 v208, 0, v208, s[60:61]\n"
"v_cndmask_b32 v213, 0, v209, s[60:61]\nv_cndmask_b32 v209, 0, v209, s[60:61]\nM 148,160,204\nR 176,187,0\nM 148,164,220\nbuffer_load_dwordx4 v220, s[16:19], s63 offen lds\n"
"M 132,160,200\nR 178,187,4096\nM 132,164,216\nM 116,160,196\nR 180,187,8192\nM 116,164,212\n"
"M 100,160,192\nR 182,187,12288\ns_bfm_b64 exec, 16, 32\nbuffer_load_dword v245, v244, s[4:7], s84 offen\ns_mov_b64 exec, -1\nM 100,164,208\n"
"G 4\nM 104,168,192\nR 160,184,16384\nM 104,172,208\nbuffer_load_dwordx4 v221, s[16:19], s63 offen offset:1024 lds\nM 120,168,196\n"
"R 162,184,20480\nM 120,172,212\ns_add_u32 m0, s50, 81920\nM 136,168,200\nR 164,184,24576\nM 136,172,216\n"
"s_add_u32 s63, s59, 0\nM 152,168,204\nR 166,184,28672\nM 152,172,220\nG 4\nM 156,176,204\n"
"R 168,185,16384\nM 156,180,220\nbuffer_load_dwordx4 v220, s[16:19], s63 offen lds\nM 140,176,200\nR 170,185,20480\nM 140,180,216\n"
"M 124,176,196\nR 172,185,24576\nM 124,180,212\nM 108,176,192\nR 174,185,28672\ns_bfm_b64 exec, 16, 48\n"
"buffer_load_dword v245, v244, s[4:7], s84 offen\ns_mov_b64 exec, -1\nM 108,180,208\nG 4\nM 0,160,160\nR 176,186,16384\n"
"M 0,164,176\ns_mov_b32 m0, s51\nM 16,160,164\nR 178,186,20480\nv_cvt_pk_bf16_f32 v128, v128, v129\nv_cvt_pk_bf16_f32 v129, v130, v131\n"
"M 16,164,180\nds_write_addtid_b32 v128 offset:0\nv_cvt_pk_bf16_f32 v130, v132, v133\nv_cvt_pk_bf16_f32 v131, v134, v135\nv_mfma_f32_16x16x32_bf16 a[224:227], v[160:163], a[168:171], a[224:227]\nR 180,186,24576\n"
"v_cvt_pk_bf16_f32 v132, v136, v137\nv_mfma_f32_16x16x32_bf16 a[224:227], v[164:167], a[184:187], a[224:227]\nds_write_addtid_b32 v129 offset:256\nv_cvt_pk_bf16_f32 v133, v138, v139\nv_mfma_f32_16x16x32_bf16 a[240:243], v[160:163], a[172:175], a[240:243]\nR 182,186,28672\n"
"v_cvt_pk_bf16_f32 v134, v140, v141\nv_mfma_f32_16x16x32_bf16 a[240:243], v[164:167], a[188:191], a[240:243]\nds_write_addtid_b32 v130 offset:512\nG 7\nv_cvt_pk_bf16_f32 v135, v142, v143\nv_mfma_f32_16x16x32_bf16 a[244:247], v[168:171], a[172:175], a[244:247]\n"
"R 160,187,16384\nv_cvt_pk_bf16_f32 v136, v144, v145\nv_mfma_f32_16x16x32_bf16 a[244:247], v[172:175], a[188:191], a[244:247]\nds_write_addtid_b32 v131 offset:768\nv_cvt_pk_bf16_f32 v137, v146, v147\nv_mfma_f32_16x16x32_bf16 a[228:231], v[168:171], a[168:171], a[228:231]\n"
"R 162,187,20480\nv_cvt_pk_bf16_f32 v138, v148, v149\nv_mfma_f32_16x16x32_bf16 a[228:231], v[172:175], a[184:187], a[228:231]\nds_write_addtid_b32 v132 offset:1024\nv_cvt_pk_bf16_f32 v139, v150, v151\nM 20,168,164\n"
"R 164,187,24576\nv_cvt_pk_bf16_f32 v140, v152, v153\nM 20,172,180\nds_write_addtid_b32 v133 offset:1280\nv_cvt_pk_bf16_f32 v141, v154, v155\nM 4,168,160\n"
"R 166,187,28672\nv_cvt_pk_bf16_f32 v142, v156, v157\nM 4,172,176\nds_write_addtid_b32 v134 offset:1536\nG 9\nv_cvt_pk_bf16_f32 v143, v158, v159\n"
"M 8,176,160\nR 168,184,32768\nds_write_addtid_b32 v135 offset:1792\nM 8,180,176\nds_write_addtid_b32 v136 offset:2048\nds_write_addtid_b32 v137 offset:2304\n"
"M 24,176,164\nR 170,184,36864\nds_write_addtid_b32 v138 offset:2560\nM 24,180,180\nds_write_addtid_b32 v139 offset:2816\nds_write_addtid_b32 v140 offset:3072\n"
"v_mfma_f32_16x16x32_bf16 a[232:235], v[176:179], a[168:171], a[232:235]\nR 172,184,40960\nds_write_addtid_b32 v141 offset:3328\nv_mfma_f32_16x16x32_bf16 a[232:235], v[180:183], a[184:187], a[232:235]\nds_write_addtid_b32 v142 offset:3584\nds_write_addtid_b32 v143 offset:3840\n"
"v_mfma_f32_16x16x32_bf16 a[248:251], v[176:179], a[172:175], a[248:251]\nR 174,184,45056\ns_add_u32 m0, s50, 81920\nv_mfma_f32_16x16x32_bf16 a[248:251], v[180:183], a[188:191], a[248:251]\nG 14\nv_mfma_f32_16x16x32_bf16 a[252:255], v[160:163], a[172:175], a[252:255]\n"
"R 176,185,32768\ns_waitcnt vmcnt(11) lgkmcnt(2)\ns_barrier\nv_mfma_f32_16x16x32_bf16 a[252:255], v[164:167], a[188:191], a[252:255]\nbuffer_load_dwordx4 v221, s[16:19], s63 offen offset:1024 lds\nv_mfma_f32_16x16x32_bf16 a[236:239], v[160:163], a[168:171], a[236:239]\n"
"R 178,185,36864\ns_add_u32 s63, s59, 524288\nv_mfma_f32_16x16x32_bf16 a[236:239], v[164:167], a[184:187], a[236:239]\ns_add_u32 m0, s50, 90112\nds_read2st64_b32 v[128:129], v225 offset0:0 offset1:1\nM 28,160,164\n"
"R 180,185,40960\nM 28,164,180\nbuffer_load_dwordx4 v220, s[16:19], s63 offen lds\nM 12,160,160\nR 182,185,45056\nM 12,164,176\n"
"ds_read2st64_b32 v[130:131], v225 offset0:2 offset1:3\nG 6\nM 0,168,192\nR 160,186,32768\nM 0,172,208\nbuffer_load_dwordx4 v221, s[16:19], s63 offen offset:1024 lds\n"
"ds_read2st64_b32 v[132:133], v225 offset0:4 offset1:5\nM 16,168,196\nR 162,186,36864\ns_add_u32 s63, s59, 65536\nds_read2st64_b32 v[134:135], v225 offset0:6 offset1:7\nM 16,172,212\n"
"s_add_u32 m0, s50, 98304\nds_read2st64_b32 v[136:137], v225 offset0:8 offset1:9\nv_mfma_f32_16x16x32_bf16 a[224:227], v[168:171], a[200:203], a[224:227]\nR 164,186,40960\nv_mfma_f32_16x16x32_bf16 a[224:227], v[172:175], a[216:219], a[224:227]\nbuffer_load_dwordx4 v220, s[16:19], s63 offen lds\n"
"G 6\nv_mfma_f32_16x16x32_bf16 v[96:99], v[208:211], v[128:131], v[96:99]\nv_mfma_f32_16x16x32_bf16 v[100:103], v[210:213], v[128:131], v[100:103]\nds_read2st64_b32 v[138:139], v225 offset0:10 offset1:11\nv_mfma_f32_16x16x32_bf16 a[240:243], v[168:171], a[204:207], a[240:243]\nR 166,186,45056\n"
"v_mfma_f32_16x16x32_bf16 a[240:243], v[172:175], a[220:223], a[240:243]\nds_read2st64_b32 v[140:141], v225 offset0:12 offset1:13\nv_mfma_f32_16x16x32_bf16 a[244:247], v[176:179], a[204:207], a[244:247]\nR 168,187,32768\nG 6\nv_mfma_f32_16x16x32_bf16 v[104:107], v[208:211], v[132:135], v[104:107]\n"
"v_mfma_f32_16x16x32_bf16 v[108:111], v[210:213], v[132:135], v[108:111]\nds_read2st64_b32 v[142:143], v225 offset0:14 offset1:15\nv_mfma_f32_16x16x32_bf16 a[244:247], v[180:183], a[220:223], a[244:247]\nbuffer_load_dwordx4 v221, s[16:19], s63 offen offset:1024 lds\nv_mfma_f32_16x16x32_bf16 a[228:231], v[176:179], a[200:203], a[228:231]\nR 170,187,36864\n"
"v_mfma_f32_16x16x32_bf16 a[228:231], v[180:183], a[216:219], a[228:231]\ns_add_u32 m0, s50, 106496\nG 5\nv_mfma_f32_16x16x32_bf16 v[112:115], v[208:211], v[136:139], v[112:115]\nv_mfma_f32_16x16x32_bf16 v[116:119], v[210:213], v[136:139], v[116:119]\nM 20,176,196\n"
"R 172,187,40960\nM 20,180,212\ns_add_u32 s63, s59, 589824\nM 4,176,192\nR 174,187,45056\nG 3\n"
"v_mfma_f32_16x16x32_bf16 v[120:123], v[208:211], v[140:143], v[120:123]\nv_mfma_f32_16x16x32_bf16 v[124:127], v[210:213], v[140:143], v[124:127]\nM 4,180,208\nM 8,160,192\nR 176,184,49152\nM 8,164,208\n"
"buffer_load_dwordx4 v220, s[16:19], s63 offen lds\nM 24,160,196\nR 178,184,53248\nM 24,164,212\nv_mfma_f32_16x16x32_bf16 a[232:235], v[160:163], a[200:203], a[232:235]\nR 180,184,57344\n"
"v_cvt_pk_bf16_f32 v120, v120, v121\nv_cvt_pk_bf16_f32 v96, v96, v97\nv_cvt_pk_bf16_f32 v112, v112, v113\nv_cvt_pk_bf16_f32 v121, v122, v123\nv_mfma_f32_16x16x32_bf16 a[232:235], v[164:167], a[216:219], a[232:235]\nbuffer_load_dwordx4 v221, s[16:19], s63 offen offset:1024 lds\n"
"v_cvt_pk_bf16_f32 v97, v98, v99\nv_cvt_pk_bf16_f32 v113, v114, v115\nv_cvt_pk_bf16_f32 v122, v124, v125\nv_mfma_f32_16x16x32_bf16 a[248:251], v[160:163], a[204:207], a[248:251]\nR 182,184,61440\nv_cvt_pk_bf16_f32 v123, v126, v127\n"
"v_cvt_pk_bf16_f32 v98, v100, v101\nv_cvt_pk_bf16_f32 v114, v116, v117\nv_mfma_f32_16x16x32_bf16 a[248:251], v[164:167], a[220:223], a[248:251]\ns_bfe_u32 s100, s56, 0x20007\ns_mul_i32 s100, s100, 49152\ns_add_u32 s100, s100, s72\n"
"s_add_u32 s100, s100, 52428800\nG 4\nv_cvt_pk_bf16_f32 v99, v102, v103\nv_cvt_pk_bf16_f32 v115, v118, v119\nv_mfma_f32_16x16x32_bf16 a[252:255], v[168:171], a[204:207], a[252:255]\nR 160,185,49152\n"
"s_add_u32 s63, s59, 131072\nv_mfma_f32_16x16x32_bf16 a[252:255], v[172:175], a[220:223], a[252:255]\nbuffer_store_dwordx4 v[96:99], v252, s[20:23], s100 offen offset:0 sc1\nv_mfma_f32_16x16x32_bf16 a[236:239], v[168:171], a[200:203], a[236:239]\nR 162,185,53248\nv_cvt_pk_bf16_f32 v104, v104, v105\n"
"v_mfma_f32_16x16x32_bf16 a[236:239], v[172:175], a[216:219], a[236:239]\ns_add_u32 m0, s50, 114688\nv_cvt_pk_bf16_f32 v105, v106, v107\nv_cvt_pk_bf16_f32 v106, v108, v109\nM 28,168,196\nR 164,185,57344\n"
"v_cvt_pk_bf16_f32 v107, v110, v111\nM 28,172,212\nbuffer_load_dwordx4 v220, s[16:19], s63 offen lds\nM 12,168,192\nR 166,185,61440\nM 12,172,208\n"
"buffer_store_dwordx4 v[104:107], v252, s[20:23], s100 offen offset:1024 sc1\ns_waitcnt vmcnt(12)\ns_barrier\nG 4\nM 32,176,160\nR 168,186,49152\n"
"M 32,180,176\nv_xor_b32 v240, 0x2000, v225\nM 48,176,164\nR 170,186,53248\nds_write2st64_b32 v240, a224, a225 offset0:0 offset1:1\nM 48,180,180\n"
"buffer_store_dwordx4 v[112:115], v252, s[20:23], s100 offen offset:2048 sc1\nds_write2st64_b32 v240, a226, a227 offset0:2 offset1:3\nM 64,176,168\nR 172,186,57344\nds_write2st64_b32 v240, a228, a229 offset0:4 offset1:5\nM 64,180,184\n"
"ds_write2st64_b32 v240, a230, a231 offset0:6 offset1:7\nM 80,176,172\nR 174,186,61440\nds_write2st64_b32 v240, a232, a233 offset0:8 offset1:9\nM 80,180,188\nds_write2st64_b32 v240, a234, a235 offset0:10 offset1:11\n"
"G 10\nM 84,160,172\nR 176,187,49152\nds_write2st64_b32 v240, a236, a237 offset0:12 offset1:13\nM 84,164,188\nbuffer_store_dwordx4 v[120:123], v252, s[20:23], s100 offen offset:3072 sc1\n"
"ds_write2st64_b32 v240, a238, a239 offset0:14 offset1:15\nM 68,160,168\nR 178,187,53248\nds_write2st64_b32 v240, a240, a241 offset0:16 offset1:17\nM 68,164,184\nds_write2st64_b32 v240, a242, a243 offset0:18 offset1:19\n"
"M 52,160,164\nR 180,187,57344\nds_write2st64_b32 v240, a244, a245 offset0:20 offset1:21\nM 52,164,180\nds_write2st64_b32 v240, a246, a247 offset0:22 offset1:23\nM 36,160,160\n"
"R 182,187,61440\nds_write2st64_b32 v240, a248, a249 offset0:24 offset1:25\nM 36,164,176\nds_write2st64_b32 v240, a250, a251 offset0:26 offset1:27\nG 14\nM 40,168,160\n"
"R 160,188,0\nds_write2st64_b32 v240, a252, a253 offset0:28 offset1:29\nM 40,172,176\nbuffer_load_dwordx4 v221, s[16:19], s63 offen offset:1024 lds\nM 56,168,164\nR 162,188,4096\n"
"s_add_u32 s63, s59, 655360\nM 56,172,180\ns_add_u32 m0, s50, 0\nds_write2st64_b32 v240, a254, a255 offset0:30 offset1:31\nM 72,168,168\nR 164,188,8192\n"
"M 72,172,184\nbuffer_load_dwordx4 v220, s[16:19], s63 offen lds\nM 88,168,172\nR 166,188,12288\nM 88,172,188\nG 8\n"
"M 92,176,172\nR 168,189,0\ns_waitcnt vmcnt(11) lgkmcnt(3)\ns_barrier\nM 92,180,188\nbuffer_load_dwordx4 v221, s[16:19], s63 offen offset:1024 lds\n"
"ds_read2st64_b32 v[128:129], v225 offset0:0 offset1:1\nM 76,176,168\nR 170,189,4096\ns_add_u32 s63, s59, 196608\nds_read2st64_b32 v[130:131], v225 offset0:2 offset1:3\nM 76,180,184\n"
"s_add_u32 m0, s50, 8192\nds_read2st64_b32 v[132:133], v225 offset0:4 offset1:5\nds_read2st64_b32 v[134:135], v225 offset0:6 offset1:7\nM 60,176,164\nR 172,189,8192\nds_read2st64_b32 v[136:137], v225 offset0:8 offset1:9\n"
"M 60,180,180\nbuffer_load_dwordx4 v220, s[16:19], s63 offen lds\nds_read2st64_b32 v[138:139], v225 offset0:10 offset1:11\nM 44,176,160\nR 174,189,12288\nds_read2st64_b32 v[140:141], v225 offset0:12 offset1:13\n"
"M 44,180,176\nds_read2st64_b32 v[142:143], v225 offset0:14 offset1:15\nds_read2st64_b32 v[144:145], v225 offset0:16 offset1:17\nG 13\nM 32,160,192\nR 176,190,0\n"
"ds_read2st64_b32 v[146:147], v225 offset0:18 offset1:19\nG 13\nv_add_f32 v0, v0, v128\nM 32,164,208\nbuffer_load_dwordx4 v221, s[16:19], s63 offen offset:1024 lds\nds_read2st64_b32 v[148:149], v225 offset0:20 offset1:21\n"
"M 48,160,196\nR 178,190,4096\nds_read2st64_b32 v[150:151], v225 offset0:22 offset1:23\nv_add_f32 v1, v1, v129\nM 48,164,212\ns_add_u32 m0, s50, 16384\n"
"ds_read2st64_b32 v[152:153], v225 offset0:24 offset1:25\nds_read2st64_b32 v[154:155], v225 offset0:26 offset1:27\nM 64,160,200\nR 180,190,8192\nds_read2st64_b32 v[156:157], v225 offset0:28 offset1:29\nG 15\n"
"v_add_f32 v2, v2, v130\nM 64,164,216\ns_add_u32 s63, s59, 720896\nds_read2st64_b32 v[158:159], v225 offset0:30 offset1:31\nv_add_f32 v3, v3, v131\nM 80,160,204\n"
"R 182,190,12288\nv_add_f32 v4, v4, v132\nv_add_f32 v5, v5, v133\nM 80,164,220\nG 14\nv_add_f32 v6, v6, v134\n"
"v_add_f32 v7, v7, v135\nv_add_f32 v8, v8, v136\nM 84,168,204\nR 160,191,0\nv_add_f32 v9, v9, v137\nv_add_f32 v10, v10, v138\n"
"M 84,172,220\nbuffer_load_dwordx4 v220, s[16:19], s63 offen lds\nv_add_f32 v11, v11, v139\nM 68,168,200\nR 162,191,4096\nG 15\n"
"v_add_f32 v12, v12, v140\nv_add_f32 v13, v13, v141\nM 68,172,216\nG 14\nv_add_f32 v14, v14, v142\nv_add_f32 v15, v15, v143\n"
"G 13\nv_add_f32 v16, v16, v144\nM 52,168,196\nR 164,191,8192\nv_add_f32 v17, v17, v145\nG 12\n"
"v_add_f32 v18, v18, v146\nM 52,172,212\nbuffer_load_dwordx4 v221, s[16:19], s63 offen offset:1024 lds\nv_add_f32 v19, v19, v147\nM 36,168,192\nR 166,191,12288\n"
"s_add_u32 m0, s50, 24576\nG 12\nv_add_f32 v20, v20, v148\nM 36,172,208\nG 4\nv_add_f32 v21, v21, v149\n"
"v_add_f32 v22, v22, v150\nv_add_f32 v23, v23, v151\nM 40,176,192\nR 168,188,16384\ns_add_u32 s63, s59, 393216\nv_add_f32 v24, v24, v152\n"
"M 40,180,208\nbuffer_load_dwordx4 v220, s[16:19], s63 offen lds\nv_add_f32 v25, v25, v153\nM 56,176,196\nR 170,188,20480\nv_add_f32 v26, v26, v154\n"
"v_add_f32 v27, v27, v155\nM 56,180,212\nv_add_f32 v28, v28, v156\nv_add_f32 v29, v29, v157\nv_add_f32 v30, v30, v158\nv_add_f32 v31, v31, v159\n"
"M 72,176,200\nR 172,188,24576\nv_cvt_pk_bf16_f32 v24, v24, v25\nv_cvt_pk_bf16_f32 v0, v0, v1\nv_cvt_pk_bf16_f32 v25, v26, v27\nM 72,180,216\n"
"buffer_load_dwordx4 v221, s[16:19], s63 offen offset:1024 lds\nv_cvt_pk_bf16_f32 v26, v28, v29\nM 88,176,204\nR 174,188,28672\nv_cvt_pk_bf16_f32 v27, v30, v31\nv_cvt_pk_bf16_f32 v1, v2, v3\n"
"M 88,180,220\nG 4\nv_cvt_pk_bf16_f32 v2, v4, v5\nv_cvt_pk_bf16_f32 v3, v6, v7\nM 92,160,204\nR 176,189,16384\n"
"s_bfe_u32 s100, s57, 0x20007\ns_mul_i32 s100, s100, 49152\ns_add_u32 s100, s100, s72\ns_add_u32 s100, s100, 52445184\nv_cvt_pk_bf16_f32 v16, v16, v17\nM 92,164,220\n"
"buffer_store_dwordx4 v[0:3], v252, s[20:23], s100 offen offset:0 sc1\nM 76,160,200\nR 178,189,20480\nv_cvt_pk_bf16_f32 v17, v18, v19\nM 76,164,216\nv_cvt_pk_bf16_f32 v8, v8, v9\n"
"v_cvt_pk_bf16_f32 v9, v10, v11\nM 60,160,196\nR 180,189,24576\nv_cvt_pk_bf16_f32 v10, v12, v13\nM 60,164,212\nv_cvt_pk_bf16_f32 v11, v14, v15\n"
"v_cvt_pk_bf16_f32 v18, v20, v21\nM 44,160,192\nR 182,189,28672\nv_cvt_pk_bf16_f32 v19, v22, v23\nM 44,164,208\nbuffer_store_dwordx4 v[8:11], v252, s[20:23], s100 offen offset:1024 sc1\n"
"s_waitcnt vmcnt(16)\ns_barrier\nG 4\nv_mfma_f32_16x16x32_bf16 v[96:99], v[168:171], a[0:3], 0\nR 160,190,16384\nM 96,172,16\n"
"s_mov_b32 m0, s51\nv_mfma_f32_16x16x32_bf16 v[112:115], v[168:171], a[4:7], 0\nR 162,190,20480\nLpg16:\ns_cbranch_execz LretW0z\nv_cvt_pk_bf16_f32 v64, v64, v65\n"
"v_cvt_pk_bf16_f32 v65, v66, v67\nM 112,172,20\nbuffer_store_dwordx4 v[16:19], v252, s[20:23], s100 offen offset:2048 sc1\nv_cvt_pk_bf16_f32 v66, v68, v69\nv_mfma_f32_16x16x32_bf16 v[128:131], v[168:171], a[8:11], 0\nR 164,190,24576\n"
"v_cvt_pk_bf16_f32 v67, v70, v71\nv_cvt_pk_bf16_f32 v68, v72, v73\nM 128,172,24\nds_write_addtid_b32 v64 offset:0\nv_cvt_pk_bf16_f32 v69, v74, v75\nv_cvt_pk_bf16_f32 v70, v76, v77\n"
"v_mfma_f32_16x16x32_bf16 v[144:147], v[168:171], a[12:15], 0\nR 166,190,28672\nv_cvt_pk_bf16_f32 v71, v78, v79\nv_cvt_pk_bf16_f32 v72, v80, v81\nM 144,172,28\nds_write_addtid_b32 v65 offset:256\n"
"G 6\nv_cvt_pk_bf16_f32 v73, v82, v83\nv_mfma_f32_16x16x32_bf16 v[148:151], v[176:179], a[12:15], 0\nR 168,191,16384\nv_cvt_pk_bf16_f32 v74, v84, v85\nv_cvt_pk_bf16_f32 v75, v86, v87\n"
"M 148,180,28\nbuffer_store_dwordx4 v[24:27], v252, s[20:23], s100 offen offset:3072 sc1\nv_mfma_f32_16x16x32_bf16 v[132:135], v[176:179], a[8:11], 0\nR 170,191,20480\nv_cvt_pk_bf16_f32 v76, v88, v89\nM 132,180,24\n"
"ds_write_addtid_b32 v66 offset:512\nv_cvt_pk_bf16_f32 v77, v90, v91\nv_mfma_f32_16x16x32_bf16 v[116:119], v[176:179], a[4:7], 0\nR 172,191,24576\nv_cvt_pk_bf16_f32 v78, v92, v93\nM 116,180,20\n"
"ds_write_addtid_b32 v67 offset:768\nv_cvt_pk_bf16_f32 v79, v94, v95\nv_mfma_f32_16x16x32_bf16 v[100:103], v[176:179], a[0:3], 0\nR 174,191,28672\nds_write_addtid_b32 v68 offset:1024\nM 100,180,16\n"
"ds_write_addtid_b32 v69 offset:1280\nds_write_addtid_b32 v70 offset:1536\nG 10\nv_mfma_f32_16x16x32_bf16 v[104:107], v[160:163], a[0:3], 0\nR 176,188,32768\nds_write_addtid_b32 v71 offset:1792\n"
"M 104,164,16\nds_write_addtid_b32 v72 offset:2048\nds_write_addtid_b32 v73 offset:2304\nv_mfma_f32_16x16x32_bf16 v[120:123], v[160:163], a[4:7], 0\nR 178,188,36864\nds_write_addtid_b32 v74 offset:2560\n"
"M 120,164,20\nds_write_addtid_b32 v75 offset:2816\nds_write_addtid_b32 v76 offset:3072\nv_mfma_f32_16x16x32_bf16 v[136:139], v[160:163], a[8:11], 0\nR 180,188,40960\nds_write_addtid_b32 v77 offset:3328\n"
"M 136,164,24\nds_write_addtid_b32 v78 offset:3584\nds_write_addtid_b32 v79 offset:3840\nv_mfma_f32_16x16x32_bf16 v[152:155], v[160:163], a[12:15], 0\nR 182,188,45056\ns_add_u32 m0, s50, 32768\n"
"M 152,164,28\ns_add_u32 s63, s59, 458752\nG 15\nv_mfma_f32_16x16x32_bf16 v[156:159], v[168:171], a[12:15], 0\nR 160,189,32768\ns_waitcnt vmcnt(10) lgkmcnt(2)\n"
"s_barrier\nM 156,172,28\nbuffer_load_dwordx4 v220, s[16:19], s63 offen lds\nv_mfma_f32_16x16x32_bf16 v[140:143], v[168:171], a[8:11], 0\nR 162,189,36864\nM 140,172,24\n"
"ds_read2st64_b32 v[64:65], v225 offset0:0 offset1:1\nv_mfma_f32_16x16x32_bf16 v[124:127], v[168:171], a[4:7], 0\nR 164,189,40960\nM 124,172,20\nbuffer_load_dwordx4 v221, s[16:19], s63 offen offset:1024 lds\nv_mfma_f32_16x16x32_bf16 v[108:111], v[168:171], a[0:3], 0\n"
"R 166,189,45056\ns_add_u32 m0, s50, 40960\nM 108,172,16\nds_read2st64_b32 v[66:67], v225 offset0:2 offset1:3\nG 6\nM 96,176,32\n"
"R 168,190,32768\ns_add_u32 s63, s59, 262144\nv_bfe_u32 v250, v252, 4, 2\nM 96,180,48\nbuffer_load_dwordx4 v220, s[16:19], s63 offen lds\nds_read2st64_b32 v[68:69], v225 offset0:4 offset1:5\n"
"v_lshlrev_b32 v250, 3, v250\nM 112,176,36\nR 170,190,36864\nds_read2st64_b32 v[70:71], v225 offset0:6 offset1:7\ns_lshl_b32 s60, s43, 5\nv_add_u32 v250, s60, v250\n"
"M 112,180,52\nds_read2st64_b32 v[72:73], v225 offset0:8 offset1:9\nM 128,176,40\nR 172,190,40960\nM 128,180,56\nbuffer_load_dwordx4 v221, s[16:19], s63 offen offset:1024 lds\n"
"G 6\nv_mfma_f32_16x16x32_bf16 v[32:35], v[208:211], v[64:67], v[32:35]\nv_mfma_f32_16x16x32_bf16 v[36:39], v[210:213], v[64:67], v[36:39]\nds_read2st64_b32 v[74:75], v225 offset0:10 offset1:11\nM 144,176,44\nR 174,190,45056\n"
"s_add_u32 m0, s50, 49152\nM 144,180,60\nds_read2st64_b32 v[76:77], v225 offset0:12 offset1:13\nM 148,160,44\nR 176,191,32768\ns_add_u32 s63, s59, 786432\n"
"G 6\nv_mfma_f32_16x16x32_bf16 v[40:43], v[208:211], v[68:71], v[40:43]\nv_mfma_f32_16x16x32_bf16 v[44:47], v[210:213], v[68:71], v[44:47]\nds_read2st64_b32 v[78:79], v225 offset0:14 offset1:15\nM 148,164,60\nbuffer_load_dwordx4 v220, s[16:19], s63 offen lds\n"
"M 132,160,40\nR 178,191,36864\nM 132,164,56\nG 5\nv_mfma_f32_16x16x32_bf16 v[48:51], v[208:211], v[72:75], v[48:51]\nv_mfma_f32_16x16x32_bf16 v[52:55], v[210:213], v[72:75], v[52:55]\n"
"M 116,160,36\nR 180,191,40960\nM 116,164,52\nM 100,160,32\nR 182,191,45056\nG 3\n"
"v_mfma_f32_16x16x32_bf16 v[56:59], v[208:211], v[76:79], v[56:59]\nv_mfma_f32_16x16x32_bf16 v[60:63], v[210:213], v[76:79], v[60:63]\nM 100,164,48\nM 104,168,32\nR 160,188,49152\nM 104,172,48\n"
"buffer_load_dwordx4 v221, s[16:19], s63 offen offset:1024 lds\nM 120,168,36\nR 162,188,53248\nv_mov_b32 v226, s46\nM 120,172,52\nv_mov_b32 v227, s47\n"
"M 136,168,40\nR 164,184,0\ns_bfe_u32 s62, s56, 0x20007\ns_lshl_b32 s62, s62, 5\ns_add_u32 s61, s74, 548864\ns_add_u32 s61, s61, s62\n"
"s_waitcnt vmcnt(6)\ns_mov_b64 exec, 1\nbuffer_atomic_swap_x2 v[226:227], off, s[20:23], s61 sc1\ns_mov_b64 exec, -1\nv_cvt_pk_bf16_f32 v32, v32, v33\nM 136,172,56\n"
"s_bfe_u32 s62, s57, 0x20007\ns_lshl_b32 s62, s62, 5\ns_add_u32 s61, s74, 548872\ns_add_u32 s61, s61, s62\ns_mov_b64 exec, 1\nbuffer_atomic_swap_x2 v[226:227], off, s[20:23], s61 sc1\n"
"s_mov_b64 exec, -1\nM 152,168,44\nR 166,184,4096\nv_cvt_pk_bf16_f32 v33, v34, v35\nM 152,172,60\nG 4\n"
"v_cvt_pk_bf16_f32 v34, v36, v37\nv_cvt_pk_bf16_f32 v35, v38, v39\nM 156,176,44\nR 168,189,49152\ns_bfe_u32 s100, s58, 0x20007\ns_mul_i32 s100, s100, 49152\n"
"s_add_u32 s100, s100, s72\ns_add_u32 s100, s100, 52461568\nv_cvt_pk_bf16_f32 v48, v48, v49\nM 156,180,60\nbuffer_store_dwordx4 v[32:35], v252, s[20:23], s100 offen offset:0 sc1\nM 140,176,40\n"
"R 170,189,53248\nv_cvt_pk_bf16_f32 v40, v40, v41\nM 140,180,56\nv_cvt_pk_bf16_f32 v41, v42, v43\nv_cvt_pk_bf16_f32 v42, v44, v45\nM 124,176,36\n"
"R 172,185,0\nv_cvt_pk_bf16_f32 v43, v46, v47\nM 124,180,52\nv_cvt_pk_bf16_f32 v49, v50, v51\nv_cvt_pk_bf16_f32 v50, v52, v53\nM 108,176,32\n"
"R 174,185,4096\nv_cvt_pk_bf16_f32 v51, v54, v55\nM 108,180,48\nbuffer_store_dwordx4 v[40:43], v252, s[20:23], s100 offen offset:1024 sc1\ns_barrier\nG 4\n"
"M 96,160,64\nR 176,190,49152\nv_cvt_pk_bf16_f32 v56, v56, v57\nM 96,164,80\nv_cvt_pk_bf16_f32 v57, v58, v59\nv_cvt_pk_bf16_f32 v58, v60, v61\n"
"M 112,160,68\nR 178,190,53248\nv_cvt_pk_bf16_f32 v59, v62, v63\nM 112,164,84\nbuffer_store_dwordx4 v[48:51], v252, s[20:23], s100 offen offset:2048 sc1\nM 128,160,72\n"
"R 180,186,0\nM 128,164,88\nM 144,160,76\nR 182,186,4096\nM 144,164,92\nG 4\n"
"M 148,168,76\nR 160,191,49152\nLq581:\ns_lshr_b32 s60, s45, 3\ns_lshl2_add_u32 s60, s60, s40\ns_lshl_b32 s60, s60, 8\n"
"v_lshl_add_u32 v224, v252, 3, s60\nv_add_u32 v224, Lq581+4096-LqPC, v224\nv_min_u32 v224, LqE-LqPC-4, v224\ns_mov_b64 exec, 3\nglobal_load_dword v224, v224, s[98:99]\ns_mov_b64 exec, -1\n"
"M 148,172,92\nbuffer_store_dwordx4 v[56:59], v252, s[20:23], s100 offen offset:3072 sc1\nM 132,168,72\nR 162,191,53248\nM 132,172,88\nM 116,168,68\n"
"R 164,187,0\nM 116,172,84\nM 100,168,64\nR 166,187,4096\nv_mov_b32 v248, 0\nM 100,172,80\n"
"G 4\nv_mov_b32 v249, 0\nM 104,176,64\nR 168,184,8192\ns_add_u32 s61, s74, 548864\ns_mov_b64 exec, 3\n"
"buffer_atomic_or_x2 v[248:249], v250, s[20:23], s61 offen sc0 sc1\ns_mov_b64 exec, -1\nM 104,180,80\nM 120,176,68\nR 170,184,12288\nv_mov_b32 v242, v208\n"
"M 120,180,84\ns_add_u32 m0, s50, 57344\nv_mov_b32 v243, v209\nv_mov_b32 v244, v208\nM 136,176,72\nR 172,184,16384\n"
"v_mov_b32 v245, v209\nM 136,180,88\ns_add_u32 s63, s59, 327680\nv_mov_b32 v64, v192\nv_mov_b32 v65, v193\nM 152,176,76\n"
"R 174,184,20480\nv_mov_b32 v70, v194\nM 152,180,92\nG 4\nv_mov_b32 v71, v195\nv_mov_b32 v72, v196\n"
"M 156,160,76\nR 176,185,8192\nv_mov_b32 v73, v197\nM 156,164,92\nbuffer_load_dwordx4 v220, s[16:19], s63 offen lds\nM 140,160,72\n"
"R 178,185,12288\nv_mov_b32 v78, v198\nM 140,164,88\nv_mov_b32 v79, v199\nv_mov_b32 v80, v200\nM 124,160,68\n"
"R 180,185,16384\nv_mov_b32 v81, v201\nM 124,164,84\nbuffer_load_dwordx4 v221, s[16:19], s63 offen offset:1024 lds\nM 108,160,64\nR 182,185,20480\n"
"s_add_u32 m0, s50, 65536\nM 108,164,80\nG 4\nv_mov_b32 v86, v202\nv_mov_b32 v87, v203\nM 96,168,96\n"
"R 160,186,8192\ns_add_u32 s63, s59, 851968\nM 96,172,112\nbuffer_load_dwordx4 v220, s[16:19], s63 offen lds\nM 112,168,100\nR 162,186,12288\n"
"v_mov_b32 v88, v204\nM 112,172,116\nv_mov_b32 v89, v205\nv_mov_b32 v94, v206\nM 128,168,104\nR 164,186,16384\n"
"v_mov_b32 v95, v207\nM 128,172,120\nbuffer_load_dwordx4 v221, s[16:19], s63 offen offset:1024 lds\nM 144,168,108\nR 166,186,20480\nv_mov_b32 v32, v232\n"
"M 144,172,124\nG 4\nv_mov_b32 v33, v233\nv_mov_b32 v38, v234\nM 148,176,108\nR 168,187,8192\n"
"v_mov_b32 v39, v235\nM 148,180,124\ns_movk_i32 s63, 6144\nv_mad_u32_u24 v220, v228, s63, v229\nv_add_u32 v221, 3144704, v220\ns_add_u32 m0, s50, 73728\n"
"s_add_u32 s63, s52, 2048\nbuffer_load_dwordx4 v220, s[4:7], s63 offen lds\nM 132,176,104\nR 170,187,12288\nv_mov_b32 v40, v236\nM 132,180,120\n"
"v_mov_b32 v41, v237\nv_mov_b32 v46, v238\nM 116,176,100\nR 172,187,16384\nv_mov_b32 v47, v239\nM 116,180,116\n"
"v_mov_b32 v48, v232\nv_mov_b32 v49, v233\nM 100,176,96\nR 174,187,20480\nv_mov_b32 v54, v234\nM 100,180,112\n"
"G 4\nv_mov_b32 v55, v235\nv_mov_b32 v56, v236\nM 104,160,96\nR 176,184,24576\ns_waitcnt vmcnt(13)\n"
"s_barrier\nM 104,164,112\nbuffer_load_dwordx4 v221, s[4:7], s63 offen offset:1024 lds\nM 120,160,100\nR 178,184,28672\ns_add_u32 s63, s52, 198656\n"
"M 120,164,116\ns_add_u32 m0, s50, 81920\nv_mov_b32 v57, v237\nv_mov_b32 v62, v238\nM 136,160,104\nR 180,184,32768\n"
"v_mov_b32 v63, v239\nM 136,164,120\nbuffer_load_dwordx4 v220, s[4:7], s63 offen lds\nM 152,160,108\nR 182,184,36864\nM 152,164,124\n"
"G 4\nM 156,168,108\nR 160,185,24576\nM 156,172,124\nbuffer_load_dwordx4 v221, s[4:7], s63 offen offset:1024 lds\nM 140,168,104\n"
"R 162,185,28672\ns_add_u32 s63, s52, 4096\nM 140,172,120\ns_add_u32 m0, s50, 90112\nM 124,168,100\nR 164,185,32768\n"
"M 124,172,116\nbuffer_load_dwordx4 v220, s[4:7], s63 offen lds\nM 108,168,96\nR 166,185,36864\nv_mov_b32 v226, s46\nM 108,172,112\n"
"G 4\nv_mov_b32 v227, s47\nM 96,176,128\nR 168,186,24576\ns_bfe_u32 s62, s58, 0x20007\ns_lshl_b32 s62, s62, 5\n"
"s_add_u32 s61, s74, 548880\ns_add_u32 s61, s61, s62\ns_waitcnt vmcnt(10)\ns_mov_b64 exec, 1\nbuffer_atomic_swap_x2 v[226:227], off, s[20:23], s61 sc1\ns_mov_b64 exec, -1\n"
"M 96,180,144\ns_mul_i32 s64, s43, 49152\nM 112,176,132\nR 170,186,28672\nM 112,180,148\ns_add_u32 s64, s64, s72\n"
"M 128,176,136\nR 172,186,32768\ns_waitcnt vmcnt(10)\nv_cmp_eq_u64 s[86:87], s[46:47], v[248:249]\ns_lshr_b32 s60, s45, 3\ns_lshl2_add_u32 s60, s60, s40\n"
"s_lshl_b32 s60, s60, 8\nv_lshl_add_u32 v224, v252, 3, s60\nv_add_u32 v224, LqM-LqPC, v224\nv_min_u32 v224, LqE-LqPC-4, v224\ns_mov_b64 exec, 3\nglobal_load_dword v224, v224, s[98:99]\n"
"s_mov_b64 exec, -1\nM 128,180,152\ns_add_u32 s64, s64, 52428800\nM 144,176,140\nR 174,186,36864\ns_and_b32 s62, s86, 3\n"
"s_cmp_eq_u32 s62, 3\ns_cbranch_scc0 Lq506\nLq507:\nM 144,180,156\nG 4\nM 148,160,140\n"
"R 176,187,24576\nM 148,164,156\nbuffer_load_dwordx4 v[2:5], v252, s[20:23], s64 offen offset:0 sc1\nM 132,160,136\nR 178,187,28672\nM 132,164,152\n"
"buffer_load_dwordx4 v[10:13], v252, s[20:23], s64 offen offset:1024 sc1\nM 116,160,132\nR 180,187,32768\nM 116,164,148\ns_mul_i32 s65, s43, 49152\nM 100,160,128\n"
"R 182,187,36864\nM 100,164,144\nbuffer_load_dwordx4 v[18:21], v252, s[20:23], s64 offen offset:2048 sc1\nG 4\nM 104,168,128\nR 160,184,40960\n"
"M 104,172,144\nbuffer_load_dwordx4 v[26:29], v252, s[20:23], s64 offen offset:3072 sc1\nM 120,168,132\nR 162,184,45056\nM 120,172,148\ns_add_u32 s65, s65, s72\n"
"M 136,168,136\nR 164,184,49152\ns_add_u32 s65, s65, 52445184\nM 136,172,152\nbuffer_load_dwordx4 v[66:69], v252, s[20:23], s65 offen offset:0 sc1\nM 152,168,140\n"
"R 166,184,53248\nM 152,172,156\nG 4\nM 156,176,140\nR 168,185,40960\ns_waitcnt vmcnt(12)\n"
"s_barrier\nM 156,180,156\nbuffer_load_dwordx4 v[74:77], v252, s[20:23], s65 offen offset:1024 sc1\nM 140,176,136\nR 170,185,45056\nv_mov_b32 v246, 0\n"
"M 140,180,152\nbuffer_load_dwordx4 v[82:85], v252, s[20:23], s65 offen offset:2048 sc1\nv_mov_b32 v247, 0\nM 124,176,132\nR 172,185,49152\ns_add_u32 s61, s74, 548864\n"
"s_mov_b64 exec, 4\nbuffer_atomic_or_x2 v[246:247], v250, s[20:23], s61 offen sc0 sc1\ns_mov_b64 exec, -1\nM 124,180,148\nbuffer_load_dwordx4 v[90:93], v252, s[20:23], s65 offen offset:3072 sc1\nM 108,176,128\n"
"R 174,185,53248\nM 108,180,144\nG 4\nM 128,160,168\nR 176,186,40960\nM 128,164,184\n"
"R 178,186,45056\nM 144,160,172\nR 180,186,49152\nM 144,164,188\nR 182,186,53248\nG 4\n"
"M 148,168,172\nR 160,187,40960\nM 148,172,188\nR 162,187,45056\nM 132,168,168\nR 164,187,49152\n"
"M 132,172,184\nR 166,187,53248\nG 4\nM 136,176,168\nR 168,184,57344\nM 136,180,184\n"
"R 170,184,61440\nM 152,176,172\nR 172,188,0\nM 152,180,188\nR 174,188,4096\nG 4\n"
"M 156,160,172\nR 176,185,57344\nM 156,164,188\nR 178,185,61440\nM 140,160,168\nR 180,189,0\n"
"M 140,164,184\nR 182,189,4096\nG 4\nM 128,168,200\nR 160,186,57344\nM 128,172,216\n"
"R 162,186,61440\nM 144,168,204\nR 164,190,0\nM 144,172,220\nR 166,190,4096\nG 4\n"
"M 148,176,204\nR 168,187,57344\nM 148,180,220\nR 170,187,61440\nM 132,176,200\nR 172,191,0\n"
"M 132,180,216\nR 174,191,4096\nG 4\nM 136,160,200\nR 176,184,40960\nbuffer_load_dwordx4 v221, s[4:7], s63 offen offset:1024 lds\n"
"M 136,164,216\nR 178,184,45056\ns_add_u32 m0, s50, 98304\nM 152,160,204\nR 180,184,49152\ns_add_u32 s63, s52, 200704\n"
"M 152,164,220\nR 182,184,53248\nG 4\nM 156,168,204\nR 160,185,40960\nbuffer_load_dwordx4 v220, s[4:7], s63 offen lds\n"
"M 156,172,220\nR 162,185,45056\nM 140,168,200\nR 164,185,49152\nM 140,172,216\nR 166,185,53248\n"
"G 4\nM 96,176,160\nR 168,186,40960\nbuffer_load_dwordx4 v221, s[4:7], s63 offen offset:1024 lds\nM 96,180,176\nR 170,186,45056\n"
"s_mov_b32 m0, s51\nM 112,176,164\nR 172,186,49152\nLpg17:\ns_cbranch_execz LretW0z\nv_cvt_pk_bf16_f32 v128, v128, v129\n"
"v_cvt_pk_bf16_f32 v129, v130, v131\nM 112,180,180\nR 174,186,53248\nG 4\nv_cvt_pk_bf16_f32 v130, v132, v133\nM 116,160,164\n"
"R 176,187,40960\ns_mul_i32 s66, s43, 49152\nv_cvt_pk_bf16_f32 v131, v134, v135\nM 116,164,180\nR 178,187,45056\ns_add_u32 s66, s66, s72\n"
"v_cvt_pk_bf16_f32 v132, v136, v137\nM 100,160,160\nR 180,187,49152\ns_add_u32 s66, s66, 52461568\nv_cvt_pk_bf16_f32 v133, v138, v139\nM 100,164,176\n"
"R 182,187,53248\nG 4\nv_cvt_pk_bf16_f32 v134, v140, v141\nM 104,168,160\nR 160,184,57344\ns_waitcnt vmcnt(4)\n"
"v_cmp_eq_u64 s[86:87], s[46:47], v[246:247]\nv_cvt_pk_bf16_f32 v135, v142, v143\nM 104,172,176\nR 162,184,61440\nv_cvt_pk_bf16_f32 v136, v144, v145\nv_cvt_pk_bf16_f32 v137, v146, v147\n"
"M 120,168,164\nR 164,188,0\ns_and_b32 s62, s86, 4\ns_cmp_eq_u32 s62, 4\ns_cbranch_scc0 Lq522\nLq523:\n"
"v_cvt_pk_bf16_f32 v138, v148, v149\nM 120,172,180\nR 166,188,4096\nG 4\nv_cvt_pk_bf16_f32 v139, v150, v151\nM 124,176,164\n"
"R 168,185,57344\nbuffer_load_dwordx4 v[34:37], v252, s[20:23], s66 offen offset:0 sc1\nv_cvt_pk_bf16_f32 v140, v152, v153\nM 124,180,180\nR 170,185,61440\nv_cvt_pk_bf16_f32 v141, v154, v155\n"
"v_cvt_pk_bf16_f32 v142, v156, v157\nM 108,176,160\nR 172,189,0\nbuffer_load_dwordx4 v[42:45], v252, s[20:23], s66 offen offset:1024 sc1\nds_write_addtid_b32 v128 offset:0\nds_write_addtid_b32 v129 offset:256\n"
"ds_write_addtid_b32 v130 offset:512\nds_write_addtid_b32 v131 offset:768\nds_write_addtid_b32 v132 offset:1024\nds_write_addtid_b32 v133 offset:1280\nds_write_addtid_b32 v134 offset:1536\nds_write_addtid_b32 v135 offset:1792\n"
"ds_write_addtid_b32 v136 offset:2048\nds_write_addtid_b32 v137 offset:2304\nds_write_addtid_b32 v138 offset:2560\nds_write_addtid_b32 v139 offset:2816\nds_write_addtid_b32 v140 offset:3072\nds_write_addtid_b32 v141 offset:3328\n"
"ds_write_addtid_b32 v142 offset:3584\nv_cvt_pk_bf16_f32 v143, v158, v159\nds_write_addtid_b32 v143 offset:3840\nM 108,180,176\nR 174,189,4096\ns_add_u32 m0, s50, 106496\n"
"s_add_u32 s63, s52, 395264\nG 15\nM 96,160,192\nR 176,186,57344\nbuffer_load_dwordx4 v220, s[4:7], s63 offen lds\nM 96,164,208\n"
"R 178,186,61440\nbuffer_load_dwordx4 v[50:53], v252, s[20:23], s66 offen offset:2048 sc1\nG 3\ns_barrier\nM 112,160,196\nR 180,190,0\n"
"ds_read2st64_b32 v[0:1], v225 offset0:0 offset1:1\nM 112,164,212\nR 182,190,4096\nds_read2st64_b32 v[6:7], v225 offset0:2 offset1:3\nG 6\nM 116,168,196\n"
"R 160,187,57344\nbuffer_load_dwordx4 v[58:61], v252, s[20:23], s66 offen offset:3072 sc1\nds_read2st64_b32 v[8:9], v225 offset0:4 offset1:5\nM 116,172,212\nR 162,187,61440\nbuffer_load_dwordx4 v221, s[4:7], s63 offen offset:1024 lds\n"
"ds_read2st64_b32 v[14:15], v225 offset0:6 offset1:7\nM 100,168,192\nR 164,191,0\ns_add_u32 m0, s50, 114688\ns_add_u32 s63, s52, 591872\nds_read2st64_b32 v[16:17], v225 offset0:8 offset1:9\n"
"M 100,172,208\nR 166,191,4096\nds_read2st64_b32 v[22:23], v225 offset0:10 offset1:11\nG 9\nM 104,176,192\nbuffer_load_dwordx4 v220, s[4:7], s63 offen lds\n"
"ds_read2st64_b32 v[24:25], v225 offset0:12 offset1:13\nM 104,180,208\nds_read2st64_b32 v[30:31], v225 offset0:14 offset1:15\nM 120,176,196\nbuffer_load_dwordx4 v221, s[4:7], s63 offen offset:1024 lds\nM 120,180,212\n"
"G 3\nM 124,160,196\nM 124,164,212\nM 108,160,192\nM 108,164,208\nG 0\n"
"s_barrier\ns_lshl_b32 s84, s75, 11\ns_add_u32 s84, s84, s49\ns_add_u32 s84, s84, 18874368\ns_add_u32 s85, s84, 32768\nv_mov_b32 v238, s46\n"
"v_mov_b32 v239, s47\nv_mfma_f32_16x16x32_bf16 v[96:99], v[242:245], v[64:67], v[96:99]\nv_mfma_f32_16x16x32_bf16 v[100:103], v[242:245], v[68:71], v[100:103]\nv_mfma_f32_16x16x32_bf16 v[104:107], v[242:245], v[72:75], v[104:107]\nv_mfma_f32_16x16x32_bf16 v[108:111], v[242:245], v[76:79], v[108:111]\nv_mfma_f32_16x16x32_bf16 v[112:115], v[242:245], v[80:83], v[112:115]\n"
"v_mfma_f32_16x16x32_bf16 v[116:119], v[242:245], v[84:87], v[116:119]\ns_waitcnt vmcnt(11)\nv_mfma_f32_16x16x32_bf16 v[120:123], v[242:245], v[88:91], v[120:123]\nv_mfma_f32_16x16x32_bf16 v[124:127], v[242:245], v[92:95], v[124:127]\nv_mfma_f32_16x16x32_bf16 v[96:99], v[242:245], v[0:3], v[96:99]\nv_mfma_f32_16x16x32_bf16 v[100:103], v[242:245], v[4:7], v[100:103]\n"
"v_mfma_f32_16x16x32_bf16 v[104:107], v[242:245], v[8:11], v[104:107]\nv_mfma_f32_16x16x32_bf16 v[108:111], v[242:245], v[12:15], v[108:111]\nv_mfma_f32_16x16x32_bf16 v[112:115], v[242:245], v[16:19], v[112:115]\nv_mfma_f32_16x16x32_bf16 v[116:119], v[242:245], v[20:23], v[116:119]\nv_mfma_f32_16x16x32_bf16 v[120:123], v[242:245], v[24:27], v[120:123]\nv_mfma_f32_16x16x32_bf16 v[124:127], v[242:245], v[28:31], v[124:127]\n"
"s_waitcnt vmcnt(7)\nv_mfma_f32_16x16x32_bf16 v[96:99], v[242:245], v[32:35], v[96:99]\nv_mfma_f32_16x16x32_bf16 v[100:103], v[242:245], v[36:39], v[100:103]\ns_waitcnt vmcnt(6)\nv_mfma_f32_16x16x32_bf16 v[104:107], v[242:245], v[40:43], v[104:107]\nv_mfma_f32_16x16x32_bf16 v[108:111], v[242:245], v[44:47], v[108:111]\n"
"s_waitcnt vmcnt(4)\nv_mfma_f32_16x16x32_bf16 v[112:115], v[242:245], v[48:51], v[112:115]\nv_mfma_f32_16x16x32_bf16 v[116:119], v[242:245], v[52:55], v[116:119]\ns_waitcnt vmcnt(3)\nv_mfma_f32_16x16x32_bf16 v[120:123], v[242:245], v[56:59], v[120:123]\nv_mfma_f32_16x16x32_bf16 v[124:127], v[242:245], v[60:63], v[124:127]\n"
"v_cvt_pk_bf16_f32 v192, v96, v97\nv_cvt_pk_bf16_f32 v193, v98, v99\nv_cvt_pk_bf16_f32 v194, v100, v101\nv_cvt_pk_bf16_f32 v195, v102, v103\nbuffer_store_dwordx4 v[192:195], v252, s[20:23], s84 offen offset:0 sc1\nv_cvt_pk_bf16_f32 v196, v104, v105\n"
"v_cvt_pk_bf16_f32 v197, v106, v107\nv_cvt_pk_bf16_f32 v198, v108, v109\nv_cvt_pk_bf16_f32 v199, v110, v111\nbuffer_store_dwordx4 v[196:199], v252, s[20:23], s84 offen offset:1024 sc1\nv_cvt_pk_bf16_f32 v200, v112, v113\nv_cvt_pk_bf16_f32 v201, v114, v115\n"
"v_cvt_pk_bf16_f32 v202, v116, v117\nv_cvt_pk_bf16_f32 v203, v118, v119\nbuffer_store_dwordx4 v[200:203], v252, s[20:23], s85 offen offset:0 sc1\nv_cvt_pk_bf16_f32 v204, v120, v121\nv_cvt_pk_bf16_f32 v205, v122, v123\nv_cvt_pk_bf16_f32 v206, v124, v125\n"
"v_cvt_pk_bf16_f32 v207, v126, v127\nbuffer_store_dwordx4 v[204:207], v252, s[20:23], s85 offen offset:1024 sc1\ns_mov_b64 exec, 0\ns_cmp_eq_u32 s40, 1\ns_cbranch_scc1 Lpg19\ns_cmp_eq_u32 s40, 2\n"
"s_cbranch_scc1 Lpg20\ns_cmp_eq_u32 s40, 3\ns_cbranch_scc1 Lpg21\ns_branch Lpg18\nLretW0n:\ns_mov_b64 exec, -1\n"
"s_branch LqM\nLq116:\ns_lshl_b32 s61, s42, 13\ns_add_u32 s61, s61, 84017152\nbuffer_load_dwordx4 v[128:131], v230, s[20:23], s61 offen sc1\ns_lshl_b32 s61, s42, 13\n"
"s_add_u32 s61, s61, 84021248\nbuffer_load_dwordx4 v[132:135], v230, s[20:23], s61 offen sc1\ns_waitcnt vmcnt(0)\nv_min3_u32 v232, v129, v131, v133\nv_min3_u32 v232, v232, v135, v135\nv_max3_u32 v233, v129, v131, v133\n"
"v_max3_u32 v233, v233, v135, v135\nv_cmp_ne_u32 s[86:87], s46, v232\nv_cmp_ne_u32 vcc, s46, v233\ns_nop 0\ns_or_b64 s[86:87], s[86:87], vcc\ns_cbranch_scc0 Lq117\n"
"s_sub_u32 s48, s48, 1\ns_cmp_gt_i32 s48, 0\ns_cbranch_scc1 Lq116\ns_branch Lq117\nLq124:\ns_lshl_b32 s61, s42, 13\n"
"s_add_u32 s61, s61, 84148224\nbuffer_load_dwordx4 v[144:147], v234, s[20:23], s61 offen sc1\ns_lshl_b32 s61, s42, 13\ns_add_u32 s61, s61, 84152320\nbuffer_load_dwordx4 v[148:151], v234, s[20:23], s61 offen sc1\ns_waitcnt vmcnt(0)\n"
"v_min3_u32 v236, v145, v147, v149\nv_min3_u32 v236, v236, v151, v151\nv_max3_u32 v237, v145, v147, v149\nv_max3_u32 v237, v237, v151, v151\nv_cmp_ne_u32 s[86:87], s46, v236\nv_cmp_ne_u32 vcc, s46, v237\n"
"s_nop 0\ns_or_b64 s[86:87], s[86:87], vcc\ns_cbranch_scc0 Lq125\ns_sub_u32 s48, s48, 1\ns_cmp_gt_i32 s48, 0\ns_cbranch_scc1 Lq124\n"
"s_branch Lq125\nLq132:\ns_lshl_b32 s61, s42, 13\ns_add_u32 s61, s61, 83886080\nbuffer_load_dwordx4 v[128:131], v230, s[20:23], s61 offen sc1\ns_lshl_b32 s61, s42, 13\n"
"s_add_u32 s61, s61, 83890176\nbuffer_load_dwordx4 v[132:135], v230, s[20:23], s61 offen sc1\ns_waitcnt vmcnt(0)\nv_min3_u32 v232, v129, v131, v133\nv_min3_u32 v232, v232, v135, v135\nv_max3_u32 v233, v129, v131, v133\n"
"v_max3_u32 v233, v233, v135, v135\nv_cmp_ne_u32 s[86:87], s46, v232\nv_cmp_ne_u32 vcc, s46, v233\ns_nop 0\ns_or_b64 s[86:87], s[86:87], vcc\ns_cbranch_scc0 Lq133\n"
"s_sub_u32 s48, s48, 1\ns_cmp_gt_i32 s48, 0\ns_cbranch_scc1 Lq132\ns_branch Lq133\nLq140:\ns_lshr_b32 s84, s49, 4\n"
"s_add_u32 s84, s84, 81788928\nbuffer_load_dwordx4 v[128:131], v252, s[20:23], s84 offen offset:0 sc1\nbuffer_load_dwordx4 v[132:135], v252, s[20:23], s84 offen offset:1024 sc1\nbuffer_load_dwordx4 v[136:139], v252, s[20:23], s84 offen offset:2048 sc1\nbuffer_load_dwordx4 v[140:143], v252, s[20:23], s84 offen offset:3072 sc1\ns_mov_b32 s85, 0x0c0c0400\n"
"s_waitcnt vmcnt(3)\nv_perm_b32 v144, v129, v128, s85\nv_perm_b32 v148, v131, v130, s85\nv_lshl_or_b32 v144, v148, 16, v144\ns_waitcnt vmcnt(2)\nv_perm_b32 v145, v133, v132, s85\n"
"v_perm_b32 v149, v135, v134, s85\nv_lshl_or_b32 v145, v149, 16, v145\ns_waitcnt vmcnt(1)\nv_perm_b32 v146, v137, v136, s85\nv_perm_b32 v150, v139, v138, s85\nv_lshl_or_b32 v146, v150, 16, v146\n"
"s_waitcnt vmcnt(0)\nv_perm_b32 v147, v141, v140, s85\nv_perm_b32 v151, v143, v142, s85\nv_lshl_or_b32 v147, v151, 16, v147\nv_min3_u32 v230, v144, v145, v146\nv_min3_u32 v230, v230, v147, v147\n"
"v_max3_u32 v231, v144, v145, v146\nv_max3_u32 v231, v231, v147, v147\nv_cmp_ne_u32 s[86:87], s46, v230\nv_cmp_ne_u32 vcc, s46, v231\ns_nop 0\ns_or_b64 s[86:87], s[86:87], vcc\n"
"s_cbranch_scc0 Lq141\ns_sub_u32 s48, s48, 1\ns_cmp_gt_i32 s48, 0\ns_cbranch_scc1 Lq140\ns_branch Lq141\nLq187:\n"
"s_mov_b64 exec, s[66:67]\nbuffer_load_dwordx4 v[230:233], v252, s[20:23], s65 offen offset:0 sc1\nbuffer_load_dwordx2 v[64:65], v227, s[20:23], s65 offen offset:2048 sc1\nbuffer_load_dwordx2 v[70:71], v227, s[20:23], s65 offen offset:2560 sc1\ns_mov_b64 exec, -1\ns_waitcnt vmcnt(0)\n"
"v_max3_u32 v248, v233, v65, v71\nv_cmp_eq_u32 vcc, -1, v248\ns_nop 1\ns_cbranch_vccz Lq188\ns_sub_u32 s48, s48, 1\ns_cmp_gt_i32 s48, 0\n"
"s_cbranch_scc1 Lq187\ns_branch Lq188\nLq189:\ns_mov_b64 exec, s[66:67]\nbuffer_load_dwordx4 v[234:237], v252, s[20:23], s65 offen offset:1024 sc1\nbuffer_load_dwordx2 v[76:77], v227, s[20:23], s65 offen offset:3072 sc1\n"
"buffer_load_dwordx2 v[82:83], v227, s[20:23], s65 offen offset:3584 sc1\ns_mov_b64 exec, -1\ns_waitcnt vmcnt(0)\nv_max3_u32 v248, v237, v77, v83\nv_cmp_eq_u32 vcc, -1, v248\ns_nop 1\n"
"s_cbranch_vccz Lq190\ns_sub_u32 s48, s48, 1\ns_cmp_gt_i32 s48, 0\ns_cbranch_scc1 Lq189\ns_branch Lq190\nLq317:\n"
"s_lshr_b32 s84, s49, 4\ns_add_u32 s84, s84, 82313216\nbuffer_load_dwordx4 v[0:3], v252, s[20:23], s84 offen offset:0 sc1\nbuffer_load_dwordx4 v[4:7], v252, s[20:23], s84 offen offset:1024 sc1\nbuffer_load_dwordx4 v[8:11], v252, s[20:23], s84 offen offset:2048 sc1\nbuffer_load_dwordx4 v[12:15], v252, s[20:23], s84 offen offset:3072 sc1\n"
"s_mov_b32 s85, 0x0c0c0400\ns_waitcnt vmcnt(3)\nv_perm_b32 v16, v1, v0, s85\nv_perm_b32 v20, v3, v2, s85\nv_lshl_or_b32 v16, v20, 16, v16\ns_waitcnt vmcnt(2)\n"
"v_perm_b32 v17, v5, v4, s85\nv_perm_b32 v21, v7, v6, s85\nv_lshl_or_b32 v17, v21, 16, v17\ns_waitcnt vmcnt(1)\nv_perm_b32 v18, v9, v8, s85\nv_perm_b32 v22, v11, v10, s85\n"
"v_lshl_or_b32 v18, v22, 16, v18\ns_waitcnt vmcnt(0)\nv_perm_b32 v19, v13, v12, s85\nv_perm_b32 v23, v15, v14, s85\nv_lshl_or_b32 v19, v23, 16, v19\nv_min3_u32 v230, v16, v17, v18\n"
"v_min3_u32 v230, v230, v19, v19\nv_max3_u32 v231, v16, v17, v18\nv_max3_u32 v231, v231, v19, v19\nv_cmp_ne_u32 s[86:87], s46, v230\nv_cmp_ne_u32 vcc, s46, v231\ns_nop 0\n"
"s_or_b64 s[86:87], s[86:87], vcc\ns_cbranch_scc0 Lq318\ns_sub_u32 s48, s48, 1\ns_cmp_gt_i32 s48, 0\ns_cbranch_scc1 Lq317\ns_branch Lq318\n"
"Lq319:\ns_lshl_b32 s61, s42, 13\ns_add_u32 s61, s61, 84279296\nbuffer_load_dwordx4 v[0:3], v238, s[20:23], s61 offen sc1\ns_lshl_b32 s61, s42, 13\ns_add_u32 s61, s61, 84283392\n"
"buffer_load_dwordx4 v[4:7], v238, s[20:23], s61 offen sc1\ns_waitcnt vmcnt(0)\nv_min3_u32 v240, v1, v3, v5\nv_min3_u32 v240, v240, v7, v7\nv_max3_u32 v241, v1, v3, v5\nv_max3_u32 v241, v241, v7, v7\n"
"v_cmp_ne_u32 s[86:87], s46, v240\nv_cmp_ne_u32 vcc, s46, v241\ns_nop 0\ns_or_b64 s[86:87], s[86:87], vcc\ns_cbranch_scc0 Lq320\ns_sub_u32 s48, s48, 1\n"
"s_cmp_gt_i32 s48, 0\ns_cbranch_scc1 Lq319\ns_branch Lq320\nLq329:\ns_lshl_b32 s61, s42, 13\ns_add_u32 s61, s61, 84410368\n"
"buffer_load_dwordx4 v[16:19], v242, s[20:23], s61 offen sc1\ns_lshl_b32 s61, s42, 13\ns_add_u32 s61, s61, 84414464\nbuffer_load_dwordx4 v[20:23], v242, s[20:23], s61 offen sc1\ns_waitcnt vmcnt(0)\nv_min3_u32 v244, v17, v19, v21\n"
"v_min3_u32 v244, v244, v23, v23\nv_max3_u32 v245, v17, v19, v21\nv_max3_u32 v245, v245, v23, v23\nv_cmp_ne_u32 s[86:87], s46, v244\nv_cmp_ne_u32 vcc, s46, v245\ns_nop 0\n"
"s_or_b64 s[86:87], s[86:87], vcc\ns_cbranch_scc0 Lq330\ns_sub_u32 s48, s48, 1\ns_cmp_gt_i32 s48, 0\ns_cbranch_scc1 Lq329\ns_branch Lq330\n"
"Lq339:\ns_lshl_b32 s61, s42, 13\ns_add_u32 s61, s61, 84541440\nbuffer_load_dwordx4 v[0:3], v238, s[20:23], s61 offen sc1\ns_lshl_b32 s61, s42, 13\ns_add_u32 s61, s61, 84545536\n"
"buffer_load_dwordx4 v[4:7], v238, s[20:23], s61 offen sc1\ns_waitcnt vmcnt(0)\nv_min3_u32 v240, v1, v3, v5\nv_min3_u32 v240, v240, v7, v7\nv_max3_u32 v241, v1, v3, v5\nv_max3_u32 v241, v241, v7, v7\n"
"v_cmp_ne_u32 s[86:87], s46, v240\nv_cmp_ne_u32 vcc, s46, v241\ns_nop 0\ns_or_b64 s[86:87], s[86:87], vcc\ns_cbranch_scc0 Lq340\ns_sub_u32 s48, s48, 1\n"
"s_cmp_gt_i32 s48, 0\ns_cbranch_scc1 Lq339\ns_branch Lq340\nLq349:\ns_lshl_b32 s61, s42, 13\ns_add_u32 s61, s61, 84672512\n"
"buffer_load_dwordx4 v[16:19], v242, s[20:23], s61 offen sc1\ns_lshl_b32 s61, s42, 13\ns_add_u32 s61, s61, 84676608\nbuffer_load_dwordx4 v[20:23], v242, s[20:23], s61 offen sc1\ns_waitcnt vmcnt(0)\nv_min3_u32 v244, v17, v19, v21\n"
"v_min3_u32 v244, v244, v23, v23\nv_max3_u32 v245, v17, v19, v21\nv_max3_u32 v245, v245, v23, v23\nv_cmp_ne_u32 s[86:87], s46, v244\nv_cmp_ne_u32 vcc, s46, v245\ns_nop 0\n"
"s_or_b64 s[86:87], s[86:87], vcc\ns_cbranch_scc0 Lq350\ns_sub_u32 s48, s48, 1\ns_cmp_gt_i32 s48, 0\ns_cbranch_scc1 Lq349\ns_branch Lq350\n"
"Lq377:\nv_mov_b32 v248, 0\nv_mov_b32 v249, 0\ns_add_u32 s61, s82, 385024\ns_mov_b64 exec, 0xf\nbuffer_atomic_or_x2 v[248:249], v250, s[20:23], s61 offen sc0 sc1\n"
"s_mov_b64 exec, -1\ns_waitcnt vmcnt(0)\nv_cmp_eq_u64 s[86:87], s[46:47], v[248:249]\ns_nop 1\ns_and_b32 s62, s86, 0xf\ns_cmp_eq_u32 s62, 0xf\n"
"s_cbranch_scc1 Lq378\ns_sleep 1\ns_sub_u32 s48, s48, 1\ns_cmp_gt_i32 s48, 0\ns_cbranch_scc1 Lq377\ns_branch Lq378\n"
"Lq412:\nv_mov_b32 v248, 0\nv_mov_b32 v249, 0\ns_add_u32 s61, s82, 385032\ns_mov_b64 exec, 0xf\nbuffer_atomic_or_x2 v[248:249], v250, s[20:23], s61 offen sc0 sc1\n"
"s_mov_b64 exec, -1\ns_waitcnt vmcnt(0)\nv_cmp_eq_u64 s[86:87], s[46:47], v[248:249]\ns_nop 1\ns_and_b32 s62, s86, 0xf\ns_cmp_eq_u32 s62, 0xf\n"
"s_cbranch_scc1 Lq413\ns_sleep 1\ns_sub_u32 s48, s48, 1\ns_cmp_gt_i32 s48, 0\ns_cbranch_scc1 Lq412\ns_branch Lq413\n"
"Lq447:\nv_mov_b32 v248, 0\nv_mov_b32 v249, 0\ns_add_u32 s61, s82, 385048\ns_mov_b64 exec, 0xf\nbuffer_atomic_or_x2 v[248:249], v250, s[20:23], s61 offen sc0 sc1\n"
"s_mov_b64 exec, -1\ns_waitcnt vmcnt(0)\nv_cmp_eq_u64 s[86:87], s[46:47], v[248:249]\ns_nop 1\ns_and_b32 s62, s86, 0xf\ns_cmp_eq_u32 s62, 0xf\n"
"s_cbranch_scc1 Lq448\ns_sleep 1\ns_sub_u32 s48, s48, 1\ns_cmp_gt_i32 s48, 0\ns_cbranch_scc1 Lq447\ns_branch Lq448\n"
"Lq466:\nv_mov_b32 v248, 0\nv_mov_b32 v249, 0\ns_add_u32 s61, s82, 385040\ns_mov_b64 exec, 0xf\nbuffer_atomic_or_x2 v[248:249], v250, s[20:23], s61 offen sc0 sc1\n"
"s_mov_b64 exec, -1\ns_waitcnt vmcnt(0)\nv_cmp_eq_u64 s[86:87], s[46:47], v[248:249]\ns_nop 1\ns_and_b32 s62, s86, 0xf\ns_cmp_eq_u32 s62, 0xf\n"
"s_cbranch_scc1 Lq467\ns_sleep 1\ns_sub_u32 s48, s48, 1\ns_cmp_gt_i32 s48, 0\ns_cbranch_scc1 Lq466\ns_branch Lq467\n"
"Lq506:\nv_mov_b32 v248, 0\nv_mov_b32 v249, 0\ns_add_u32 s61, s74, 548864\ns_mov_b64 exec, 3\nbuffer_atomic_or_x2 v[248:249], v250, s[20:23], s61 offen sc0 sc1\n"
"s_mov_b64 exec, -1\ns_waitcnt vmcnt(0)\nv_cmp_eq_u64 s[86:87], s[46:47], v[248:249]\ns_nop 1\ns_and_b32 s62, s86, 3\ns_cmp_eq_u32 s62, 3\n"
"s_cbranch_scc1 Lq507\ns_sleep 1\ns_sub_u32 s48, s48, 1\ns_cmp_gt_i32 s48, 0\ns_cbranch_scc1 Lq506\ns_branch Lq507\n"
"Lq522:\nv_mov_b32 v246, 0\nv_mov_b32 v247, 0\ns_add_u32 s61, s74, 548864\ns_mov_b64 exec, 4\nbuffer_atomic_or_x2 v[246:247], v250, s[20:23], s61 offen sc0 sc1\n"
"s_mov_b64 exec, -1\ns_waitcnt vmcnt(0)\nv_cmp_eq_u64 s[86:87], s[46:47], v[246:247]\ns_nop 1\ns_and_b32 s62, s86, 4\ns_cmp_eq_u32 s62, 4\n"
"s_cbranch_scc1 Lq523\ns_sleep 1\ns_sub_u32 s48, s48, 1\ns_cmp_gt_i32 s48, 0\ns_cbranch_scc1 Lq522\ns_branch Lq523\n"
"LqM:\ns_add_u32 s61, s79, 88358912\ns_lshl_b32 s62, s75, 3\ns_add_u32 s61, s61, s62\ns_waitcnt vmcnt(0)\ns_mov_b64 exec, 1\n"
"buffer_atomic_swap_x2 v[238:239], off, s[20:23], s61 sc1\ns_mov_b64 exec, -1\nLqq1:\nv_bfe_u32 v250, v252, 7, 1\nv_mul_u32_u24 v250, 2176, v250\nv_bfe_u32 v251, v252, 4, 3\n"
"v_lshl_add_u32 v250, v251, 3, v250\ns_lshl_b32 s60, s90, 6\nv_add_u32 v250, s60, v250\ns_sleep 3\nv_mov_b32 v248, 0\nv_mov_b32 v249, 0\n"
"s_add_u32 s61, s80, 88358912\ns_mov_b64 exec, 0xffff\nbuffer_atomic_or_x2 v[248:249], v250, s[20:23], s61 offen sc0 sc1\ns_mov_b64 exec, -1\ns_add_u32 m0, s50, 0\ns_add_u32 s63, s52, 397312\n"
"buffer_load_dwordx4 v220, s[4:7], s63 offen lds\nbuffer_load_dwordx4 v221, s[4:7], s63 offen offset:1024 lds\ns_add_u32 m0, s50, 8192\ns_add_u32 s63, s52, 593920\nbuffer_load_dwordx4 v220, s[4:7], s63 offen lds\nbuffer_load_dwordx4 v221, s[4:7], s63 offen offset:1024 lds\n"
"s_add_u32 m0, s50, 16384\ns_add_u32 s63, s52, 788480\nbuffer_load_dwordx4 v220, s[4:7], s63 offen lds\nbuffer_load_dwordx4 v221, s[4:7], s63 offen offset:1024 lds\ns_add_u32 m0, s50, 24576\ns_add_u32 s63, s52, 985088\n"
"buffer_load_dwordx4 v220, s[4:7], s63 offen lds\nbuffer_load_dwordx4 v221, s[4:7], s63 offen offset:1024 lds\ns_add_u32 m0, s50, 32768\ns_add_u32 s63, s52, 790528\nbuffer_load_dwordx4 v220, s[4:7], s63 offen lds\nbuffer_load_dwordx4 v221, s[4:7], s63 offen offset:1024 lds\n"
"s_waitcnt vmcnt(10)\nv_cmp_eq_u64 s[86:87], s[46:47], v[248:249]\ns_nop 1\ns_and_b32 s88, s86, 0xffff\ns_cmp_eq_u32 s88, 0xffff\ns_cbranch_scc1 Lq595\n"
"v_mov_b32 v246, 0\nv_mov_b32 v247, 0\ns_add_u32 s61, s80, 88358912\ns_mov_b64 exec, 0xffff\nbuffer_atomic_or_x2 v[246:247], v250, s[20:23], s61 offen sc0 sc1\ns_mov_b64 exec, -1\n"
"Lq595:\ns_add_u32 m0, s50, 40960\ns_add_u32 s63, s52, 987136\nbuffer_load_dwordx4 v220, s[4:7], s63 offen lds\nbuffer_load_dwordx4 v221, s[4:7], s63 offen offset:1024 lds\ns_mov_b64 exec, 0\n"
"s_cmp_eq_u32 s40, 1\ns_cbranch_scc1 LretQ1z\ns_cmp_eq_u32 s40, 2\ns_cbranch_scc1 LretQ1z\ns_cmp_eq_u32 s40, 3\ns_cbranch_scc1 LretQ1z\n"
"s_branch LretQ1z\nLretQ1z:\ns_mov_b64 exec, -1\nv_mov_b32 v230, 0\nv_mov_b32 v231, 0\nv_mov_b32 v232, 0\n"
"v_mov_b32 v233, 0\nv_dot2c_f32_bf16 v230, s76, v192\nv_dot2c_f32_bf16 v231, v192, v192\nv_dot2c_f32_bf16 v232, s76, v200\nv_dot2c_f32_bf16 v233, v200, v200\nv_dot2c_f32_bf16 v230, s76, v193\n"
"v_dot2c_f32_bf16 v231, v193, v193\nv_dot2c_f32_bf16 v232, s76, v201\nv_dot2c_f32_bf16 v233, v201, v201\nv_dot2c_f32_bf16 v230, s76, v194\nv_dot2c_f32_bf16 v231, v194, v194\nv_dot2c_f32_bf16 v232, s76, v202\n"
"v_dot2c_f32_bf16 v233, v202, v202\nv_dot2c_f32_bf16 v230, s76, v195\nv_dot2c_f32_bf16 v231, v195, v195\nv_dot2c_f32_bf16 v232, s76, v203\nv_dot2c_f32_bf16 v233, v203, v203\nv_dot2c_f32_bf16 v230, s76, v196\n"
"v_dot2c_f32_bf16 v231, v196, v196\nv_dot2c_f32_bf16 v232, s76, v204\nv_dot2c_f32_bf16 v233, v204, v204\nv_dot2c_f32_bf16 v230, s76, v197\nv_dot2c_f32_bf16 v231, v197, v197\nv_dot2c_f32_bf16 v232, s76, v205\n"
"v_dot2c_f32_bf16 v233, v205, v205\nv_dot2c_f32_bf16 v230, s76, v198\nv_dot2c_f32_bf16 v231, v198, v198\nv_dot2c_f32_bf16 v232, s76, v206\nv_dot2c_f32_bf16 v233, v206, v206\nv_dot2c_f32_bf16 v230, s76, v199\n"
"v_dot2c_f32_bf16 v231, v199, v199\nv_dot2c_f32_bf16 v232, s76, v207\nv_dot2c_f32_bf16 v233, v207, v207\nv_lshrrev_b32 v234, 5, v252\nv_and_b32 v234, 24, v234\nv_bfe_u32 v235, s46, v234, 8\n"
"v_lshrrev_b32 v234, 1, v234\nv_and_b32 v236, 0xf0, v252\nv_add_u32 v234, v234, v236\nv_permlane32_swap_b32 v230, v231\nv_permlane32_swap_b32 v232, v233\nv_add_f32 v230, v230, v231\n"
"v_add_f32 v232, v232, v233\ns_nop 1\nv_permlane16_swap_b32 v230, v232\nv_add_f32 v230, v230, v232\ns_movk_i32 s62, 0xff\nv_bfi_b32 v230, s62, v235, v230\n"
"s_lshr_b32 s61, s49, 4\ns_lshl1_add_u32 s61, s53, s61\ns_add_u32 s61, s61, 82837504\nbuffer_store_dword v230, v234, s[20:23], s61 offen sc1\ns_cmp_eq_u32 s88, 0xffff\ns_cbranch_scc1 Lq596\n"
"s_waitcnt vmcnt(3)\nv_mov_b32 v248, v246\nv_mov_b32 v249, v247\nv_cmp_eq_u64 s[86:87], s[46:47], v[248:249]\ns_nop 1\ns_and_b32 s62, s86, 0xffff\n"
"s_cmp_eq_u32 s62, 0xffff\ns_cbranch_scc1 Lq587\nLq586:\nv_mov_b32 v248, 0\nv_mov_b32 v249, 0\ns_add_u32 s61, s80, 88358912\n"
"s_mov_b64 exec, 0xffff\nbuffer_atomic_or_x2 v[248:249], v250, s[20:23], s61 offen sc0 sc1\ns_mov_b64 exec, -1\ns_waitcnt vmcnt(0)\nv_cmp_eq_u64 s[86:87], s[46:47], v[248:249]\ns_nop 1\n"
"s_and_b32 s62, s86, 0xffff\ns_cmp_eq_u32 s62, 0xffff\ns_cbranch_scc1 Lq587\ns_sub_u32 s48, s48, 1\ns_cmp_gt_i32 s48, 0\ns_cbranch_scc1 Lq586\n"
"Lq587:\nLq596:\ns_add_u32 s64, s70, 18874368\nbuffer_load_dwordx4 a[0:3], v252, s[20:23], s64 offen offset:0 sc1\ns_add_u32 s65, s70, 18907136\nbuffer_load_dwordx4 a[4:7], v252, s[20:23], s65 offen offset:0 sc1\n"
"s_add_u32 s66, s71, 18874368\nbuffer_load_dwordx4 a[8:11], v252, s[20:23], s66 offen offset:0 sc1\nLpg18:\ns_cbranch_execz Lpg22\ns_add_u32 s67, s71, 18907136\nbuffer_load_dwordx4 a[12:15], v252, s[20:23], s67 offen offset:0 sc1\n"
"buffer_load_dwordx4 a[16:19], v252, s[20:23], s64 offen offset:1024 sc1\nbuffer_load_dwordx4 a[20:23], v252, s[20:23], s65 offen offset:1024 sc1\nbuffer_load_dwordx4 a[24:27], v252, s[20:23], s66 offen offset:1024 sc1\nbuffer_load_dwordx4 a[28:31], v252, s[20:23], s67 offen offset:1024 sc1\ns_barrier\nR 160,188,8192\n"
"R 162,188,12288\nR 164,188,16384\nR 166,188,20480\nR 168,189,8192\nR 170,189,12288\nR 172,189,16384\n"
"R 174,189,20480\nR 176,190,8192\nR 178,190,12288\nR 180,190,16384\nR 182,190,20480\nbuffer_load_dwordx4 a[32:35], v252, s[20:23], s64 offen offset:2048 sc1\n"
"buffer_load_dwordx4 a[36:39], v252, s[20:23], s65 offen offset:2048 sc1\nbuffer_load_dwordx4 a[40:43], v252, s[20:23], s66 offen offset:2048 sc1\nbuffer_load_dwordx4 a[44:47], v252, s[20:23], s67 offen offset:2048 sc1\nbuffer_load_dwordx4 a[48:51], v252, s[20:23], s64 offen offset:3072 sc1\nbuffer_load_dwordx4 a[52:55], v252, s[20:23], s65 offen offset:3072 sc1\nbuffer_load_dwordx4 a[56:59], v252, s[20:23], s66 offen offset:3072 sc1\n"
"buffer_load_dwordx4 a[60:63], v252, s[20:23], s67 offen offset:3072 sc1\ns_waitcnt vmcnt(15) lgkmcnt(10)\nv_mfma_f32_16x16x32_bf16 v[0:3], v[160:163], a[0:3], 0\ns_add_u32 s64, s70, 18878464\nbuffer_load_dwordx4 a[64:67], v252, s[20:23], s64 offen offset:0 sc1\ns_waitcnt vmcnt(12) lgkmcnt(8)\n"
"M 0,164,16\nv_mfma_f32_16x16x32_bf16 v[16:19], v[160:163], a[4:7], 0\ns_waitcnt vmcnt(11)\nM 16,164,20\nv_mfma_f32_16x16x32_bf16 v[32:35], v[160:163], a[8:11], 0\ns_waitcnt vmcnt(10)\n"
"M 32,164,24\nv_mfma_f32_16x16x32_bf16 v[48:51], v[160:163], a[12:15], 0\ns_waitcnt vmcnt(9)\nM 48,164,28\nG 4\nv_mfma_f32_16x16x32_bf16 v[52:55], v[168:171], a[12:15], 0\n"
"R 160,191,8192\nM 52,172,28\ns_add_u32 s65, s70, 18911232\nbuffer_load_dwordx4 a[68:71], v252, s[20:23], s65 offen offset:0 sc1\nv_mfma_f32_16x16x32_bf16 v[36:39], v[168:171], a[8:11], 0\nR 162,191,12288\n"
"M 36,172,24\nv_mfma_f32_16x16x32_bf16 v[20:23], v[168:171], a[4:7], 0\nR 164,191,16384\nM 20,172,20\nv_mfma_f32_16x16x32_bf16 v[4:7], v[168:171], a[0:3], 0\nR 166,191,20480\n"
"M 4,172,16\nG 4\nv_mfma_f32_16x16x32_bf16 v[8:11], v[176:179], a[0:3], 0\nR 168,188,24576\nM 8,180,16\ns_add_u32 s66, s71, 18878464\n"
"buffer_load_dwordx4 a[72:75], v252, s[20:23], s66 offen offset:0 sc1\nv_mfma_f32_16x16x32_bf16 v[24:27], v[176:179], a[4:7], 0\nR 170,188,28672\nM 24,180,20\ns_add_u32 m0, s50, 49152\nv_mfma_f32_16x16x32_bf16 v[40:43], v[176:179], a[8:11], 0\n"
"R 172,188,32768\ns_add_u32 s63, s52, 1181696\nM 40,180,24\ns_add_u32 s67, s71, 18911232\nbuffer_load_dwordx4 a[76:79], v252, s[20:23], s67 offen offset:0 sc1\nv_mfma_f32_16x16x32_bf16 v[56:59], v[176:179], a[12:15], 0\n"
"R 174,188,36864\nM 56,180,28\nG 4\nv_mfma_f32_16x16x32_bf16 v[60:63], v[160:163], a[12:15], 0\nR 176,189,24576\nM 60,164,28\n"
"buffer_load_dwordx4 v220, s[4:7], s63 offen lds\nv_mfma_f32_16x16x32_bf16 v[44:47], v[160:163], a[8:11], 0\nR 178,189,28672\nM 44,164,24\nv_mfma_f32_16x16x32_bf16 v[28:31], v[160:163], a[4:7], 0\nR 180,189,32768\n"
"M 28,164,20\nbuffer_load_dwordx4 a[80:83], v252, s[20:23], s64 offen offset:1024 sc1\nv_mfma_f32_16x16x32_bf16 v[12:15], v[160:163], a[0:3], 0\nR 182,189,36864\nM 12,164,16\nG 4\n"
"v_mfma_f32_16x16x32_bf16 v[64:67], a[0:3], v[168:171], 0\nR 160,190,24576\nN 64,16,172\nv_mfma_f32_16x16x32_bf16 v[68:71], a[4:7], v[168:171], 0\nR 162,190,28672\nN 68,20,172\n"
"buffer_load_dwordx4 a[84:87], v252, s[20:23], s65 offen offset:1024 sc1\nv_mfma_f32_16x16x32_bf16 v[96:99], a[8:11], v[168:171], 0\nR 164,190,32768\nN 96,24,172\nv_mfma_f32_16x16x32_bf16 v[100:103], a[12:15], v[168:171], 0\nR 166,190,36864\n"
"N 100,28,172\ns_barrier\nG 4\nv_mfma_f32_16x16x32_bf16 v[108:111], a[12:15], v[176:179], 0\nR 168,191,24576\nN 108,28,180\n"
"buffer_load_dwordx4 v221, s[4:7], s63 offen offset:1024 lds\nv_mfma_f32_16x16x32_bf16 v[104:107], a[8:11], v[176:179], 0\nR 170,191,28672\nN 104,24,180\nbuffer_load_dwordx4 a[88:91], v252, s[20:23], s66 offen offset:1024 sc1\nv_mfma_f32_16x16x32_bf16 v[76:79], a[4:7], v[176:179], 0\n"
"R 172,191,32768\nN 76,20,180\nv_mfma_f32_16x16x32_bf16 v[72:75], a[0:3], v[176:179], 0\nR 174,191,36864\nN 72,16,180\nG 4\n"
"v_mfma_f32_16x16x32_bf16 v[80:83], a[0:3], v[160:163], 0\nR 176,188,40960\ns_add_u32 s63, s52, 1378304\nN 80,16,164\nbuffer_load_dwordx4 a[92:95], v252, s[20:23], s67 offen offset:1024 sc1\nv_mfma_f32_16x16x32_bf16 v[84:87], a[4:7], v[160:163], 0\n"
"R 178,188,45056\nN 84,20,164\ns_add_u32 m0, s50, 57344\nv_mfma_f32_16x16x32_bf16 v[112:115], a[8:11], v[160:163], 0\nR 180,188,49152\nN 112,24,164\n"
"buffer_load_dwordx4 v220, s[4:7], s63 offen lds\nv_mfma_f32_16x16x32_bf16 v[116:119], a[12:15], v[160:163], 0\nR 182,188,53248\nN 116,28,164\nG 4\nv_mfma_f32_16x16x32_bf16 v[124:127], a[12:15], v[168:171], 0\n"
"R 160,189,40960\nN 124,28,172\nbuffer_load_dwordx4 a[96:99], v252, s[20:23], s64 offen offset:2048 sc1\nv_mfma_f32_16x16x32_bf16 v[120:123], a[8:11], v[168:171], 0\nR 162,189,45056\nN 120,24,172\n"
"v_mfma_f32_16x16x32_bf16 v[92:95], a[4:7], v[168:171], 0\nR 164,189,49152\nN 92,20,172\nv_mfma_f32_16x16x32_bf16 v[88:91], a[0:3], v[168:171], 0\nR 166,189,53248\nN 88,16,172\n"
"G 4\ns_waitcnt vmcnt(19)\nM 0,176,32\nR 168,190,40960\ns_waitcnt vmcnt(15)\nM 0,180,48\n"
"buffer_load_dwordx4 a[100:103], v252, s[20:23], s65 offen offset:2048 sc1\nM 16,176,36\nR 170,190,45056\ns_waitcnt vmcnt(15)\nM 16,180,52\nM 32,176,40\n"
"R 172,190,49152\ns_waitcnt vmcnt(14)\nM 32,180,56\nbuffer_load_dwordx4 v221, s[4:7], s63 offen offset:1024 lds\nM 48,176,44\nR 174,190,53248\n"
"s_waitcnt vmcnt(14)\nM 48,180,60\ns_barrier\nG 4\nM 52,160,44\nR 176,191,40960\n"
"M 52,164,60\nbuffer_load_dwordx4 a[104:107], v252, s[20:23], s66 offen offset:2048 sc1\nM 36,160,40\nR 178,191,45056\nM 36,164,56\ns_add_u32 m0, s50, 65536\n"
"M 20,160,36\nR 180,191,49152\ns_add_u32 s63, s52, 1183744\nM 20,164,52\nbuffer_load_dwordx4 a[108:111], v252, s[20:23], s67 offen offset:2048 sc1\nM 4,160,32\n"
"R 182,191,53248\nM 4,164,48\nG 4\nM 8,168,32\nR 160,184,0\nM 8,172,48\n"
"buffer_load_dwordx4 v220, s[4:7], s63 offen lds\nM 24,168,36\nR 162,184,4096\nM 24,172,52\nM 40,168,40\nR 164,184,8192\n"
"M 40,172,56\nbuffer_load_dwordx4 a[112:115], v252, s[20:23], s64 offen offset:3072 sc1\nM 56,168,44\nR 166,184,12288\nM 56,172,60\nG 4\n"
"M 60,176,44\nR 168,185,0\nM 60,180,60\nM 44,176,40\nR 170,185,4096\nM 44,180,56\n"
"buffer_load_dwordx4 a[116:119], v252, s[20:23], s65 offen offset:3072 sc1\nM 28,176,36\nR 172,185,8192\nM 28,180,52\nM 12,176,32\nR 174,185,12288\n"
"M 12,180,48\nG 4\nN 64,32,160\nR 176,186,0\nN 64,48,164\nbuffer_load_dwordx4 v221, s[4:7], s63 offen offset:1024 lds\n"
"N 68,36,160\nR 178,186,4096\nN 68,52,164\nbuffer_load_dwordx4 a[120:123], v252, s[20:23], s66 offen offset:3072 sc1\nN 96,40,160\nR 180,186,8192\n"
"N 96,56,164\ns_lshr_b32 s84, s49, 4\nN 100,44,160\nR 182,186,12288\ns_add_u32 s84, s84, 82837504\nN 100,60,164\n"
"s_barrier\nG 4\nN 108,44,168\nR 160,187,0\nN 108,60,172\nbuffer_load_dwordx4 a[124:127], v252, s[20:23], s67 offen offset:3072 sc1\n"
"N 104,40,168\nR 162,187,4096\nN 104,56,172\nbuffer_load_dwordx4 v[128:131], v252, s[20:23], s84 offen offset:0 sc1\nN 76,36,168\nR 164,187,8192\n"
"N 76,52,172\nN 72,32,168\nR 166,187,12288\nN 72,48,172\nbuffer_load_dwordx4 v[132:135], v252, s[20:23], s84 offen offset:1024 sc1\nG 4\n"
"N 80,32,176\nR 168,184,16384\nN 80,48,180\ns_add_u32 s64, s70, 18882560\nbuffer_load_dwordx4 a[128:131], v252, s[20:23], s64 offen offset:0 sc1\nN 84,36,176\n"
"R 170,184,20480\nN 84,52,180\nbuffer_load_dwordx4 v[136:139], v252, s[20:23], s84 offen offset:2048 sc1\nN 112,40,176\nR 172,184,24576\nN 112,56,180\n"
"N 116,44,176\nR 174,184,28672\nN 116,60,180\nbuffer_load_dwordx4 v[140:143], v252, s[20:23], s84 offen offset:3072 sc1\nG 4\nN 124,44,160\n"
"R 176,185,16384\nN 124,60,164\ns_add_u32 s65, s70, 18915328\nbuffer_load_dwordx4 a[132:135], v252, s[20:23], s65 offen offset:0 sc1\nN 120,40,160\nR 178,185,20480\n"
"N 120,56,164\nN 92,36,160\nR 180,185,24576\nN 92,52,164\nN 88,32,160\nR 182,185,28672\n"
"N 88,48,164\nG 4\ns_waitcnt vmcnt(27)\nM 0,168,64\nR 160,186,16384\ns_waitcnt vmcnt(22)\n"
"M 0,172,80\ns_add_u32 s66, s71, 18882560\nbuffer_load_dwordx4 a[136:139], v252, s[20:23], s66 offen offset:0 sc1\nM 16,168,68\nR 162,186,20480\ns_waitcnt vmcnt(22)\n"
"M 16,172,84\nM 32,168,72\nR 164,186,24576\ns_waitcnt vmcnt(20)\nM 32,172,88\ns_add_u32 s67, s71, 18915328\n"
"buffer_load_dwordx4 a[140:143], v252, s[20:23], s67 offen offset:0 sc1\nM 48,168,76\nR 166,186,28672\ns_waitcnt vmcnt(20)\nM 48,172,92\ns_barrier\n"
"G 4\nM 52,176,76\nR 168,187,16384\nM 52,180,92\nM 36,176,72\nR 170,187,20480\n"
"M 36,180,88\ns_add_u32 m0, s50, 73728\nM 20,176,68\nR 172,187,24576\ns_add_u32 s63, s52, 1380352\nM 20,180,84\n"
"buffer_load_dwordx4 a[144:147], v252, s[20:23], s64 offen offset:1024 sc1\nM 4,176,64\nR 174,187,28672\nM 4,180,80\nG 4\nM 8,160,64\n"
"R 176,184,32768\nM 8,164,80\nbuffer_load_dwordx4 v220, s[4:7], s63 offen lds\nM 24,160,68\nR 178,184,36864\nM 24,164,84\n"
"buffer_load_dwordx4 a[148:151], v252, s[20:23], s65 offen offset:1024 sc1\nM 40,160,72\nR 180,184,40960\nM 40,164,88\nbuffer_load_dwordx4 v221, s[4:7], s63 offen offset:1024 lds\nM 56,160,76\n"
"R 182,184,45056\nM 56,164,92\nG 4\nM 60,168,76\nR 160,185,32768\nM 60,172,92\n"
"M 44,168,72\nR 162,185,36864\nM 44,172,88\nbuffer_load_dwordx4 a[152:155], v252, s[20:23], s66 offen offset:1024 sc1\nM 28,168,68\nR 164,185,40960\n"
"M 28,172,84\nM 12,168,64\nR 166,185,45056\nM 12,172,80\nG 4\nN 64,64,176\n"
"R 168,186,32768\nN 64,80,180\nbuffer_load_dwordx4 a[156:159], v252, s[20:23], s67 offen offset:1024 sc1\nN 68,68,176\nR 170,186,36864\nN 68,84,180\n"
"N 96,72,176\nR 172,186,40960\ns_mov_b32 s85, 0x0c0c0400\nN 96,88,180\ns_add_u32 m0, s50, 81920\ns_waitcnt vmcnt(13)\n"
"v_perm_b32 v144, v129, v128, s85\nv_perm_b32 v148, v131, v130, s85\nN 100,76,176\nR 174,186,45056\ns_add_u32 s63, s52, 1574912\nN 100,92,180\n"
"s_barrier\nG 4\nN 108,76,160\nR 176,187,32768\nv_lshl_or_b32 v144, v148, 16, v144\nN 108,92,164\n"
"buffer_load_dwordx4 a[160:163], v252, s[20:23], s64 offen offset:2048 sc1\nN 104,72,160\nR 178,187,36864\ns_waitcnt vmcnt(13)\nv_perm_b32 v145, v133, v132, s85\nN 104,88,164\n"
"buffer_load_dwordx4 v220, s[4:7], s63 offen lds\nN 76,68,160\nR 180,187,40960\nv_perm_b32 v149, v135, v134, s85\nN 76,84,164\nbuffer_load_dwordx4 v221, s[4:7], s63 offen offset:1024 lds\n"
"N 72,64,160\nR 182,187,45056\ns_add_u32 m0, s50, 90112\nN 72,80,164\nG 4\nv_lshl_or_b32 v145, v149, 16, v145\n"
"s_waitcnt vmcnt(13)\nv_perm_b32 v146, v137, v136, s85\nN 80,64,168\nR 160,184,49152\ns_add_u32 s63, s52, 1771520\nN 80,80,172\n"
"buffer_load_dwordx4 a[164:167], v252, s[20:23], s65 offen offset:2048 sc1\nN 84,68,168\nR 162,184,53248\nv_perm_b32 v150, v139, v138, s85\nN 84,84,172\nbuffer_load_dwordx4 v220, s[4:7], s63 offen lds\n"
"N 112,72,168\nR 164,184,57344\nv_lshl_or_b32 v146, v150, 16, v146\nN 112,88,172\nbuffer_load_dwordx4 v221, s[4:7], s63 offen offset:1024 lds\nN 116,76,168\n"
"R 166,184,61440\ns_waitcnt vmcnt(15)\nv_perm_b32 v147, v141, v140, s85\nN 116,92,172\nG 4\nv_perm_b32 v151, v143, v142, s85\n"
"v_lshl_or_b32 v147, v151, 16, v147\nN 124,76,176\nR 168,185,49152\nv_min3_u32 v230, v144, v145, v146\nN 124,92,180\nbuffer_load_dwordx4 a[168:171], v252, s[20:23], s66 offen offset:2048 sc1\n"
"N 120,72,176\nR 170,185,53248\nv_min3_u32 v230, v230, v147, v147\nN 120,88,180\nv_max3_u32 v231, v144, v145, v146\nv_max3_u32 v231, v231, v147, v147\n"
"N 92,68,176\nR 172,185,57344\nv_cmp_ne_u32 s[86:87], s46, v230\nN 92,84,180\nbuffer_load_dwordx4 a[172:175], v252, s[20:23], s67 offen offset:2048 sc1\nN 88,64,176\n"
"R 174,185,61440\nv_cmp_ne_u32 vcc, s46, v231\nN 88,80,180\ns_or_b64 s[86:87], s[86:87], vcc\ns_cbranch_scc1 Lq645\nLq646:\n"
"G 4\nM 0,160,96\nR 176,186,49152\nv_and_b32 v128, 0xffffff00, v128\nM 0,164,112\nv_and_b32 v129, 0xffffff00, v129\n"
"v_and_b32 v130, 0xffffff00, v130\nM 16,160,100\nR 178,186,53248\nv_and_b32 v131, 0xffffff00, v131\nM 16,164,116\ns_add_u32 m0, s50, 98304\n"
"v_and_b32 v132, 0xffffff00, v132\nv_and_b32 v133, 0xffffff00, v133\nM 32,160,104\nR 180,186,57344\ns_add_u32 s63, s52, 1576960\nM 32,164,120\n"
"buffer_load_dwordx4 a[176:179], v252, s[20:23], s64 offen offset:3072 sc1\nM 48,160,108\nR 182,186,61440\nv_and_b32 v134, 0xffffff00, v134\nM 48,164,124\ns_waitcnt vmcnt(11)\n"
"s_barrier\nG 4\nM 52,168,108\nR 160,187,49152\nv_and_b32 v135, 0xffffff00, v135\nM 52,172,124\n"
"buffer_load_dwordx4 v220, s[4:7], s63 offen lds\nM 36,168,104\nR 162,187,53248\nv_and_b32 v136, 0xffffff00, v136\nM 36,172,120\nbuffer_load_dwordx4 a[180:183], v252, s[20:23], s65 offen offset:3072 sc1\n"
"M 20,168,100\nR 164,187,57344\nv_and_b32 v137, 0xffffff00, v137\nM 20,172,116\nbuffer_load_dwordx4 v221, s[4:7], s63 offen offset:1024 lds\nM 4,168,96\n"
"R 166,187,61440\ns_add_u32 m0, s50, 106496\nM 4,172,112\nG 4\nv_and_b32 v138, 0xffffff00, v138\nv_and_b32 v139, 0xffffff00, v139\n"
"M 8,176,96\nR 168,188,0\ns_add_u32 s63, s52, 1773568\nM 8,180,112\nbuffer_load_dwordx4 v220, s[4:7], s63 offen lds\nM 24,176,100\n"
"R 170,188,4096\nv_and_b32 v140, 0xffffff00, v140\nM 24,180,116\nbuffer_load_dwordx4 a[184:187], v252, s[20:23], s66 offen offset:3072 sc1\nM 40,176,104\nR 172,188,8192\n"
"v_and_b32 v141, 0xffffff00, v141\nM 40,180,120\nbuffer_load_dwordx4 v221, s[4:7], s63 offen offset:1024 lds\nM 56,176,108\nR 174,188,12288\nv_and_b32 v142, 0xffffff00, v142\n"
"M 56,180,124\nG 4\nv_and_b32 v143, 0xffffff00, v143\nv_add_f32 v128, v128, v136\nM 60,160,108\nR 176,189,0\n"
"v_add_f32 v129, v129, v137\nM 60,164,124\nbuffer_load_dwordx4 a[188:191], v252, s[20:23], s67 offen offset:3072 sc1\nM 44,160,104\nR 178,189,4096\nv_add_f32 v130, v130, v138\n"
"M 44,164,120\nv_add_f32 v131, v131, v139\nv_add_f32 v132, v132, v140\nM 28,160,100\nR 180,189,8192\nv_add_f32 v133, v133, v141\n"
"M 28,164,116\nv_add_f32 v134, v134, v142\nv_add_f32 v135, v135, v143\nM 12,160,96\nR 182,189,12288\nv_add_f32 v128, v128, v132\n"
"M 12,164,112\nG 4\nv_add_f32 v129, v129, v133\nv_add_f32 v130, v130, v134\nN 64,96,168\nR 160,190,0\n"
"v_add_f32 v131, v131, v135\nN 64,112,172\ns_add_u32 s64, s70, 18886656\nbuffer_load_dwordx4 a[192:195], v252, s[20:23], s64 offen offset:0 sc1\nN 68,100,168\nR 162,190,4096\n"
"Lpg19:\ns_cbranch_execz LretW0n\nv_permlane32_swap_b32 v128, v130\nN 68,116,172\nv_permlane32_swap_b32 v129, v131\nv_add_f32 v128, v128, v130\n"
"N 96,104,168\nR 164,190,8192\nv_add_f32 v129, v129, v131\nN 96,120,172\ns_add_u32 m0, s50, 114688\nv_permlane16_swap_b32 v128, v129\n"
"v_add_f32 v230, v128, v129\nN 100,108,168\nR 166,190,12288\ns_add_u32 s63, s52, 1968128\nN 100,124,172\ns_waitcnt vmcnt(11)\n"
"s_barrier\nG 4\nN 108,108,176\nR 168,191,0\nv_mov_b32 v232, v230\nN 108,124,180\n"
"s_add_u32 s65, s70, 18919424\nbuffer_load_dwordx4 a[196:199], v252, s[20:23], s65 offen offset:0 sc1\nN 104,104,176\nR 170,191,4096\nv_permlane16_swap_b32 v230, v232\nN 104,120,180\n"
"buffer_load_dwordx4 v220, s[4:7], s63 offen lds\nN 76,100,176\nR 172,191,8192\nv_mov_b32 v231, v230\nN 76,116,180\nbuffer_load_dwordx4 v221, s[4:7], s63 offen offset:1024 lds\n"
"N 72,96,176\nR 174,191,12288\ns_add_u32 m0, s50, 0\nN 72,112,180\nG 4\nv_mov_b32 v233, v232\n"
"v_permlane32_swap_b32 v230, v231\nN 80,96,160\nR 176,188,16384\ns_add_u32 s63, s52, 2164736\nN 80,112,164\ns_add_u32 s66, s71, 18886656\n"
"buffer_load_dwordx4 a[200:203], v252, s[20:23], s66 offen offset:0 sc1\nN 84,100,160\nR 178,188,20480\nv_permlane32_swap_b32 v232, v233\nN 84,116,164\nbuffer_load_dwordx4 v220, s[4:7], s63 offen lds\n"
"N 112,104,160\nR 180,188,24576\nv_mul_f32 v230, 0xba800000, v230\nN 112,120,164\ns_add_u32 s67, s71, 18919424\nbuffer_load_dwordx4 a[204:207], v252, s[20:23], s67 offen offset:0 sc1\n"
"N 116,108,160\nR 182,188,28672\nv_mul_f32 v214, v230, v230\nN 116,124,164\nG 4\nv_mul_f32 v231, 0x3a800000, v231\n"
"v_sub_f32 v231, v231, v214\nN 124,108,168\nR 160,189,16384\nbuffer_load_dwordx4 v221, s[4:7], s63 offen offset:1024 lds\nN 124,124,172\nv_max_f32 v231, 0, v231\n"
"v_add_f32 v231, 0x3727c5ac, v231\nN 120,104,168\nR 162,189,20480\nv_and_b32 v208, s77, v230\nN 120,120,172\nv_rsq_f32 v234, v231\n"
"N 92,100,168\nR 164,189,24576\nv_mul_f32 v216, v231, v234\nN 92,116,172\nbuffer_load_dwordx4 a[208:211], v252, s[20:23], s64 offen offset:1024 sc1\nN 88,96,168\n"
"R 166,189,28672\nv_sub_f32 v230, v230, v208\nN 88,112,172\nG 4\nv_cvt_pk_bf16_f32 v209, v230, v216\nv_cvt_pk_bf16_f32 v208, v208, v208\n"
"M 0,176,128\nR 168,190,16384\nv_mov_b32 v214, v234\nM 0,180,144\ns_mov_b64 s[60:61], 0xffff\nv_cndmask_b32 v208, 0, v208, s[60:61]\n"
"v_mul_f32 v215, 0x3e8293ee, v234\nM 16,176,132\nR 170,190,20480\nv_mul_f32 v232, 0xba800000, v232\nM 16,180,148\nbuffer_load_dwordx4 a[212:215], v252, s[20:23], s65 offen offset:1024 sc1\n"
"M 32,176,136\nR 172,190,24576\ns_add_u32 s63, s52, 1970176\nM 32,180,152\ns_add_u32 m0, s50, 8192\ns_mov_b64 s[60:61], 0xffff\n"
"v_cndmask_b32 v209, 0, v209, s[60:61]\nM 48,176,140\nR 174,190,28672\nv_mul_f32 v217, v232, v232\nM 48,180,156\ns_waitcnt vmcnt(11)\n"
"s_barrier\nG 4\nM 52,160,140\nR 176,191,16384\nv_mul_f32 v233, 0x3a800000, v233\nM 52,164,156\n"
"buffer_load_dwordx4 v220, s[4:7], s63 offen lds\nM 36,160,136\nR 178,191,20480\nv_sub_f32 v233, v233, v217\nM 36,164,152\nbuffer_load_dwordx4 a[216:219], v252, s[20:23], s66 offen offset:1024 sc1\n"
"M 20,160,132\nR 180,191,24576\nv_max_f32 v233, 0, v233\nM 20,164,148\nbuffer_load_dwordx4 v221, s[4:7], s63 offen offset:1024 lds\nM 4,160,128\n"
"R 182,191,28672\ns_add_u32 m0, s50, 16384\nM 4,164,144\nG 4\nv_add_f32 v233, 0x3727c5ac, v233\nv_and_b32 v212, s77, v232\n"
"M 8,168,128\nR 160,188,32768\ns_add_u32 s63, s52, 2166784\nM 8,172,144\nbuffer_load_dwordx4 a[220:223], v252, s[20:23], s67 offen offset:1024 sc1\nM 24,168,132\n"
"R 162,188,36864\nv_sub_f32 v232, v232, v212\nM 24,172,148\nbuffer_load_dwordx4 v220, s[4:7], s63 offen lds\nM 40,168,136\nR 164,188,40960\n"
"v_cvt_pk_bf16_f32 v212, v212, v212\nM 40,172,152\nbuffer_load_dwordx4 v221, s[4:7], s63 offen offset:1024 lds\nM 56,168,140\nR 166,188,45056\nv_bfe_u32 v230, v252, 2, 6\n"
"M 56,172,156\nG 4\nv_rsq_f32 v235, v233\nM 60,176,140\nR 168,189,32768\nv_mul_f32 v219, v233, v235\n"
"M 60,180,156\nbuffer_load_dwordx4 a[224:227], v252, s[20:23], s64 offen offset:2048 sc1\nM 44,176,136\nR 170,189,36864\nv_cvt_pk_bf16_f32 v213, v232, v219\nM 44,180,152\n"
"s_mov_b64 s[60:61], 0xffff\nv_cndmask_b32 v212, 0, v212, s[60:61]\nv_mov_b32 v217, v235\nM 28,176,132\nR 172,189,40960\nv_mul_f32 v218, 0x3e8293ee, v235\n"
"M 28,180,148\ns_mov_b64 s[60:61], 0xffff\nv_cndmask_b32 v213, 0, v213, s[60:61]\nM 12,176,128\nR 174,189,45056\nM 12,180,144\n"
"G 4\nN 64,128,160\nR 176,190,32768\nN 64,144,164\nbuffer_load_dwordx4 a[228:231], v252, s[20:23], s65 offen offset:2048 sc1\nN 68,132,160\n"
"R 178,190,36864\nN 68,148,164\ns_lshl_b32 s60, s40, 7\ns_add_u32 s60, s60, 161280\nv_add_u32 v230, s60, v230\nN 96,136,160\n"
"R 180,190,40960\nN 96,152,164\ns_add_u32 m0, s50, 24576\nds_write_b32 v230, v214 offset:0\nN 100,140,160\nR 182,190,45056\n"
"s_add_u32 s63, s52, 2361344\nN 100,156,164\ns_waitcnt vmcnt(10)\ns_barrier\nG 5\nN 108,140,168\n"
"R 160,191,32768\nN 108,156,172\nbuffer_load_dwordx4 a[232:235], v252, s[20:23], s66 offen offset:2048 sc1\nN 104,136,168\nR 162,191,36864\nN 104,152,172\n"
"buffer_load_dwordx4 v220, s[4:7], s63 offen lds\nN 76,132,168\nR 164,191,40960\nLq688:\ns_lshr_b32 s60, s45, 3\ns_lshl2_add_u32 s60, s60, s40\n"
"s_lshl_b32 s60, s60, 8\nv_lshl_add_u32 v224, v252, 3, s60\nv_add_u32 v224, Lq688+4096-LqPC, v224\nv_min_u32 v224, LqE-LqPC-4, v224\ns_mov_b64 exec, 3\nglobal_load_dword v224, v224, s[98:99]\n"
"s_mov_b64 exec, -1\nN 76,148,172\nbuffer_load_dwordx4 a[236:239], v252, s[20:23], s67 offen offset:2048 sc1\nN 72,128,168\nR 166,191,45056\nN 72,144,172\n"
"ds_write_b32 v230, v217 offset:64\nG 5\nN 80,128,176\nR 168,188,49152\nbuffer_load_dwordx4 v221, s[4:7], s63 offen offset:1024 lds\nN 80,144,180\n"
"s_add_u32 m0, s50, 32768\ns_add_u32 s63, s52, 2557952\nbuffer_load_dwordx4 v220, s[4:7], s63 offen lds\nN 84,132,176\nR 170,188,53248\nN 84,148,180\n"
"N 112,136,176\nR 172,184,0\nN 112,152,180\nbuffer_load_dwordx4 a[240:243], v252, s[20:23], s64 offen offset:3072 sc1\nN 116,140,176\nR 174,184,4096\n"
"N 116,156,180\nG 5\nN 124,140,160\nR 176,189,49152\nbuffer_load_dwordx4 v221, s[4:7], s63 offen offset:1024 lds\nN 124,156,164\n"
"N 120,136,160\nR 178,189,53248\nN 120,152,164\nbuffer_load_dwordx4 a[244:247], v252, s[20:23], s65 offen offset:3072 sc1\nN 92,132,160\nR 180,185,0\n"
"N 92,148,164\nN 88,128,160\nR 182,185,4096\nN 88,144,164\nG 4\nM 0,168,160\n"
"R 160,190,49152\nM 0,172,176\nM 16,168,164\nR 162,190,53248\nM 16,172,180\nbuffer_load_dwordx4 a[248:251], v252, s[20:23], s66 offen offset:3072 sc1\n"
"M 32,168,168\nR 164,186,0\nM 32,172,184\ns_add_u32 m0, s50, 40960\nM 48,168,172\nR 166,186,4096\n"
"s_add_u32 s63, s52, 2754560\nM 48,172,188\ns_waitcnt vmcnt(12)\ns_barrier\nG 4\nM 52,176,172\n"
"R 168,191,49152\nM 52,180,188\nbuffer_load_dwordx4 a[252:255], v252, s[20:23], s67 offen offset:3072 sc1\nM 36,176,168\nR 170,191,53248\nM 36,180,184\n"
"buffer_load_dwordx4 v220, s[4:7], s63 offen lds\nM 20,176,164\nR 172,187,0\nM 20,180,180\nbuffer_load_dwordx4 v221, s[4:7], s63 offen offset:1024 lds\nM 4,176,160\n"
"R 174,187,4096\ns_add_u32 m0, s50, 49152\nM 4,180,176\nG 4\nM 8,160,160\nR 176,184,8192\n"
"s_add_u32 s63, s52, 2951168\nM 8,164,176\nbuffer_load_dwordx4 v220, s[4:7], s63 offen lds\nM 24,160,164\nR 178,184,12288\nM 24,164,180\n"
"M 40,160,168\nR 180,184,16384\nM 40,164,184\nbuffer_load_dwordx4 v221, s[4:7], s63 offen offset:1024 lds\nM 56,160,172\nR 182,184,20480\n"
"M 56,164,188\nG 4\nM 60,168,172\nR 160,185,8192\nM 60,172,188\nM 44,168,168\n"
"R 162,185,12288\nM 44,172,184\ns_add_u32 m0, s50, 57344\nM 28,168,164\nR 164,185,16384\nM 28,172,180\n"
"s_add_u32 s63, s52, 2363392\nM 12,168,160\nR 166,185,20480\nM 12,172,176\nG 4\nN 64,160,176\n"
"R 168,186,8192\nN 64,176,180\nbuffer_load_dwordx4 v220, s[4:7], s63 offen lds\nN 68,164,176\nR 170,186,12288\nN 68,180,180\n"
"N 96,168,176\nR 172,186,16384\nN 96,184,180\nbuffer_load_dwordx4 v221, s[4:7], s63 offen offset:1024 lds\nN 100,172,176\nR 174,186,20480\n"
"s_add_u32 m0, s50, 65536\nN 100,188,180\ns_waitcnt vmcnt(9)\ns_barrier\nG 4\nN 108,172,160\n"
"R 176,187,8192\ns_add_u32 s63, s52, 2560000\nN 108,188,164\nbuffer_load_dwordx4 v220, s[4:7], s63 offen lds\nN 104,168,160\nR 178,187,12288\n"
"N 104,184,164\nN 76,164,160\nR 180,187,16384\nN 76,180,164\nN 72,160,160\nR 182,187,20480\n"
"N 72,176,164\nG 4\nN 80,160,168\nR 160,184,24576\nN 80,176,172\nbuffer_load_dwordx4 v221, s[4:7], s63 offen offset:1024 lds\n"
"N 84,164,168\nR 162,184,28672\ns_add_u32 s63, s52, 2756608\nN 84,180,172\ns_add_u32 m0, s50, 73728\nN 112,168,168\n"
"R 164,184,32768\nN 112,184,172\nbuffer_load_dwordx4 v220, s[4:7], s63 offen lds\nN 116,172,168\nR 166,184,36864\nN 116,188,172\n"
"G 4\nN 124,172,176\nR 168,185,24576\nN 124,188,180\nbuffer_load_dwordx4 v221, s[4:7], s63 offen offset:1024 lds\nN 120,168,176\n"
"R 170,185,28672\ns_add_u32 s63, s52, 2953216\nN 120,184,180\ns_add_u32 m0, s50, 81920\nN 92,164,176\nR 172,185,32768\n"
"N 92,180,180\nbuffer_load_dwordx4 v220, s[4:7], s63 offen lds\nN 88,160,176\nR 174,185,36864\nN 88,176,180\nG 4\n"
"M 0,160,192\nR 176,186,24576\ns_mov_b32 s60, 156160\nv_bfe_u32 v246, v252, 4, 4\nv_lshl_add_u32 v246, v246, 3, s60\nM 0,164,208\n"
"ds_read_b64 v[238:239], v246 offset:0\nM 16,160,196\nR 178,186,28672\nds_read_b64 v[240:241], v246 offset:128\nM 16,164,212\nds_read_b64 v[242:243], v246 offset:256\n"
"M 32,160,200\nR 180,186,32768\nds_read_b64 v[244:245], v246 offset:384\nM 32,164,216\nds_read_b64 v[246:247], v246 offset:0\nM 48,160,204\n"
"R 182,186,36864\nM 48,164,220\ns_waitcnt vmcnt(7) lgkmcnt(1)\ns_barrier\nM 52,168,204\nR 160,187,24576\n"
"M 52,172,220\nbuffer_load_dwordx4 v221, s[4:7], s63 offen offset:1024 lds\nM 36,168,200\nR 162,187,28672\ns_add_u32 s63, s52, 0\nM 36,172,216\n"
"s_add_u32 m0, s50, 90112\nM 20,168,196\nR 164,187,32768\nM 20,172,212\nbuffer_load_dwordx4 v220, s[4:7], s63 offen lds\nM 4,168,192\n"
"R 166,187,36864\nv_mfma_f32_16x16x32_bf16 v[0:3], v[238:241], v[208:211], v[0:3]\nM 4,172,208\nG 4\nM 8,176,192\nR 168,184,40960\n"
"v_mfma_f32_16x16x32_bf16 v[4:7], v[240:243], v[208:211], v[4:7]\nM 8,180,208\nbuffer_load_dwordx4 v221, s[4:7], s63 offen offset:1024 lds\nM 24,176,196\nR 170,184,45056\nv_mfma_f32_16x16x32_bf16 v[8:11], v[242:245], v[208:211], v[8:11]\n"
"s_add_u32 m0, s50, 98304\ns_add_u32 s63, s52, 196608\nM 24,180,212\nbuffer_load_dwordx4 v220, s[4:7], s63 offen lds\nM 40,176,200\nR 172,184,49152\n"
"v_mfma_f32_16x16x32_bf16 v[12:15], v[244:247], v[208:211], v[12:15]\nM 40,180,216\nM 56,176,204\nR 174,184,53248\nv_mfma_f32_16x16x32_bf16 v[16:19], v[244:247], v[210:213], v[16:19]\nM 56,180,220\n"
"G 4\nM 60,160,204\nR 176,185,40960\nv_mfma_f32_16x16x32_bf16 v[20:23], v[238:241], v[210:213], v[20:23]\nM 60,164,220\nbuffer_load_dwordx4 v221, s[4:7], s63 offen offset:1024 lds\n"
"M 44,160,200\nR 178,185,45056\nv_mfma_f32_16x16x32_bf16 v[24:27], v[240:243], v[210:213], v[24:27]\ns_add_u32 m0, s50, 106496\ns_add_u32 s63, s52, 393216\nM 44,164,216\n"
"buffer_load_dwordx4 v220, s[4:7], s63 offen lds\nM 28,160,196\nR 180,185,49152\nv_mfma_f32_16x16x32_bf16 v[28:31], v[242:245], v[210:213], v[28:31]\nM 28,164,212\nM 12,160,192\n"
"R 182,185,53248\nM 12,164,208\nG 4\nM 0,168,224\nR 160,186,40960\nM 0,172,240\n"
"buffer_load_dwordx4 v221, s[4:7], s63 offen offset:1024 lds\nM 16,168,228\nR 162,186,45056\ns_add_u32 s63, s52, 589824\nM 16,172,244\ns_add_u32 m0, s50, 114688\n"
"M 32,168,232\nR 164,186,49152\nM 32,172,248\nbuffer_load_dwordx4 v220, s[4:7], s63 offen lds\nM 48,168,236\nR 166,186,53248\n"
"M 48,172,252\ns_waitcnt vmcnt(11)\ns_barrier\nG 4\nM 52,176,236\nR 168,187,40960\n"
"M 52,180,252\nbuffer_load_dwordx4 v221, s[4:7], s63 offen offset:1024 lds\nM 36,176,232\nR 170,187,45056\ns_add_u32 s63, s52, 786432\nM 36,180,248\n"
"s_add_u32 m0, s50, 0\nM 20,176,228\nR 172,187,49152\nM 20,180,244\nbuffer_load_dwordx4 v220, s[4:7], s63 offen lds\nM 4,176,224\n"
"R 174,187,53248\nM 4,180,240\nG 4\nM 8,160,224\nR 176,184,57344\nM 8,164,240\n"
"buffer_load_dwordx4 v221, s[4:7], s63 offen offset:1024 lds\nM 24,160,228\nR 178,184,61440\nM 24,164,244\ns_add_u32 m0, s50, 8192\nM 40,160,232\n"
"R 180,188,0\nM 40,164,248\ns_add_u32 s63, s52, 983040\nM 56,160,236\nR 182,188,4096\nM 56,164,252\n"
"G 4\nM 60,168,236\nR 160,185,57344\nM 60,172,252\nbuffer_load_dwordx4 v220, s[4:7], s63 offen lds\nM 44,168,232\n"
"R 162,185,61440\nM 44,172,248\nM 28,168,228\nR 164,189,0\nM 28,172,244\nbuffer_load_dwordx4 v221, s[4:7], s63 offen offset:1024 lds\n"
"M 12,168,224\nR 166,189,4096\nM 12,172,240\ns_waitcnt vmcnt(12)\ns_barrier\nG 4\n"
"N 64,192,176\nR 168,186,57344\nN 64,208,180\ns_mov_b32 m0, s51\nN 68,196,176\nR 170,186,61440\n"
"N 68,212,180\nds_write_addtid_b32 v32 offset:0\nds_write_addtid_b32 v33 offset:256\nLpg20:\ns_cbranch_execz LretW0n\nN 96,200,176\n"
"R 172,190,0\nds_write_addtid_b32 v34 offset:512\nN 96,216,180\nds_write_addtid_b32 v35 offset:768\nds_write_addtid_b32 v36 offset:1024\nN 100,204,176\n"
"R 174,190,4096\nds_write_addtid_b32 v37 offset:1280\nN 100,220,180\nds_write_addtid_b32 v38 offset:1536\nds_write_addtid_b32 v39 offset:1792\nG 12\n"
"N 108,204,160\nR 176,187,57344\nds_write_addtid_b32 v40 offset:2048\nN 108,220,164\nds_write_addtid_b32 v41 offset:2304\nds_write_addtid_b32 v42 offset:2560\n"
"N 104,200,160\nR 178,187,61440\nds_write_addtid_b32 v43 offset:2816\nN 104,216,164\nds_write_addtid_b32 v44 offset:3072\nds_write_addtid_b32 v45 offset:3328\n"
"N 76,196,160\nR 180,191,0\nds_write_addtid_b32 v46 offset:3584\nN 76,212,164\nds_write_addtid_b32 v47 offset:3840\nds_write_addtid_b32 v48 offset:4096\n"
"N 72,192,160\nR 182,191,4096\nds_write_addtid_b32 v49 offset:4352\nN 72,208,164\nds_write_addtid_b32 v50 offset:4608\nds_write_addtid_b32 v51 offset:4864\n"
"G 15\nN 80,192,168\nR 160,188,8192\nds_write_addtid_b32 v52 offset:5120\nN 80,208,172\nds_write_addtid_b32 v53 offset:5376\n"
"ds_write_addtid_b32 v54 offset:5632\nN 84,196,168\nR 162,188,12288\nds_write_addtid_b32 v55 offset:5888\nN 84,212,172\nds_write_addtid_b32 v56 offset:6144\n"
"ds_write_addtid_b32 v57 offset:6400\nN 112,200,168\nR 164,188,16384\nds_write_addtid_b32 v58 offset:6656\nN 112,216,172\nds_write_addtid_b32 v59 offset:6912\n"
"ds_write_addtid_b32 v60 offset:7168\nN 116,204,168\nR 166,188,20480\nds_write_addtid_b32 v61 offset:7424\nN 116,220,172\nds_write_addtid_b32 v62 offset:7680\n"
"ds_write_addtid_b32 v63 offset:7936\nG 15\nN 124,204,176\nR 168,189,8192\ns_add_u32 m0, s50, 16384\ns_add_u32 s63, s52, 1179648\n"
"N 124,220,180\nbuffer_load_dwordx4 v220, s[4:7], s63 offen lds\nN 120,200,176\nR 170,189,12288\ns_waitcnt vmcnt(9) lgkmcnt(2)\ns_barrier\n"
"N 120,216,180\nds_read2st64_b32 v[32:33], v225 offset0:0 offset1:1\nds_read2st64_b32 v[34:35], v225 offset0:2 offset1:3\nN 92,196,176\nR 172,189,16384\nds_read2st64_b32 v[36:37], v225 offset0:4 offset1:5\n"
"N 92,212,180\nds_read2st64_b32 v[38:39], v225 offset0:6 offset1:7\nds_read2st64_b32 v[40:41], v225 offset0:8 offset1:9\nN 88,192,176\nR 174,189,20480\nds_read2st64_b32 v[42:43], v225 offset0:10 offset1:11\n"
"N 88,208,180\nds_read2st64_b32 v[44:45], v225 offset0:12 offset1:13\nds_read2st64_b32 v[46:47], v225 offset0:14 offset1:15\nN 64,224,160\nR 176,190,8192\ns_mov_b32 s60, 156672\n"
"v_bfe_u32 v246, v252, 4, 4\nv_lshl_add_u32 v246, v246, 3, s60\nds_read2st64_b32 v[48:49], v225 offset0:16 offset1:17\nN 64,240,164\nds_read_b64 v[238:239], v246 offset:0\nds_read2st64_b32 v[50:51], v225 offset0:18 offset1:19\n"
"G 13\nv_add_f32 v0, v0, v32\nN 68,228,160\nR 178,190,12288\nds_read_b64 v[240:241], v246 offset:128\nds_read2st64_b32 v[52:53], v225 offset0:20 offset1:21\n"
"N 68,244,164\nds_read_b64 v[242:243], v246 offset:256\nds_read2st64_b32 v[54:55], v225 offset0:22 offset1:23\nv_add_f32 v1, v1, v33\nN 96,232,160\nR 180,190,16384\n"
"ds_read_b64 v[244:245], v246 offset:384\nds_read2st64_b32 v[56:57], v225 offset0:24 offset1:25\nN 96,248,164\nds_read_b64 v[246:247], v246 offset:0\nds_read2st64_b32 v[58:59], v225 offset0:26 offset1:27\nG 15\n"
"v_add_f32 v2, v2, v34\nN 100,236,160\nR 182,190,20480\nds_read2st64_b32 v[60:61], v225 offset0:28 offset1:29\nv_add_f32 v3, v3, v35\nN 100,252,164\n"
"ds_read2st64_b32 v[62:63], v225 offset0:30 offset1:31\nv_add_f32 v4, v4, v36\nN 108,236,168\nR 160,191,8192\nv_add_f32 v5, v5, v37\nv_add_f32 v6, v6, v38\n"
"N 108,252,172\nbuffer_load_dwordx4 v221, s[4:7], s63 offen offset:1024 lds\nv_add_f32 v7, v7, v39\nN 104,232,168\nR 162,191,12288\ns_add_u32 s63, s52, 1376256\n"
"v_add_f32 v8, v8, v40\nN 104,248,172\ns_add_u32 m0, s50, 24576\nv_add_f32 v9, v9, v41\nv_add_f32 v10, v10, v42\nv_add_f32 v11, v11, v43\n"
"N 76,228,168\nR 164,191,16384\nv_add_f32 v12, v12, v44\nv_add_f32 v13, v13, v45\nN 76,244,172\nG 14\n"
"v_mfma_f32_16x16x32_bf16 v[64:67], v[208:211], v[238:241], v[64:67]\nbuffer_load_dwordx4 v220, s[4:7], s63 offen lds\nv_add_f32 v14, v14, v46\nN 72,224,168\nR 166,191,20480\nv_add_f32 v15, v15, v47\n"
"v_add_f32 v16, v16, v48\nN 72,240,172\nG 13\nv_mfma_f32_16x16x32_bf16 v[72:75], v[208:211], v[240:243], v[72:75]\nG 6\nv_add_f32 v17, v17, v49\n"
"v_add_f32 v18, v18, v50\nv_add_f32 v19, v19, v51\nN 80,224,176\nR 168,188,24576\nv_add_f32 v20, v20, v52\nv_add_f32 v21, v21, v53\n"
"N 80,240,180\nv_mfma_f32_16x16x32_bf16 v[80:83], v[208:211], v[242:245], v[80:83]\nbuffer_load_dwordx4 v221, s[4:7], s63 offen offset:1024 lds\nv_add_f32 v22, v22, v54\nN 84,228,176\nR 170,188,28672\n"
"s_add_u32 m0, s50, 49152\ns_add_u32 s63, s52, 1572864\nv_add_f32 v23, v23, v55\nN 84,244,180\nv_mfma_f32_16x16x32_bf16 v[88:91], v[208:211], v[244:247], v[88:91]\nbuffer_load_dwordx4 v220, s[4:7], s63 offen lds\n"
"v_add_f32 v24, v24, v56\nN 112,232,176\nR 172,188,32768\nv_add_f32 v25, v25, v57\nN 112,248,180\nv_mfma_f32_16x16x32_bf16 v[68:71], v[210:213], v[244:247], v[68:71]\n"
"v_add_f32 v26, v26, v58\nv_add_f32 v27, v27, v59\nN 116,236,176\nR 174,188,36864\nG 9\nv_add_f32 v28, v28, v60\n"
"N 116,252,180\nv_mfma_f32_16x16x32_bf16 v[76:79], v[210:213], v[238:241], v[76:79]\nG 4\nv_add_f32 v29, v29, v61\nv_add_f32 v30, v30, v62\nN 124,236,160\n"
"R 176,189,24576\nv_add_f32 v31, v31, v63\nN 124,252,164\nv_mfma_f32_16x16x32_bf16 v[84:87], v[210:213], v[240:243], v[84:87]\nbuffer_load_dwordx4 v221, s[4:7], s63 offen offset:1024 lds\nN 120,232,160\n"
"R 178,189,28672\ns_add_u32 m0, s50, 57344\ns_add_u32 s63, s52, 1769472\nv_lshrrev_b32 v227, 1, v252\nN 120,248,164\nv_mfma_f32_16x16x32_bf16 v[92:95], v[210:213], v[242:245], v[92:95]\n"
"buffer_load_dwordx4 v220, s[4:7], s63 offen lds\nv_mul_f32 v16, v16, v217\nN 92,228,160\nR 180,189,32768\ns_lshl4_add_u32 s64, s41, s42\nv_mul_f32 v17, v17, v217\n"
"N 92,244,164\ns_cmp_eq_u32 s40, 3\ns_cselect_b64 s[66:67], -1, 0\ns_lshl_b32 s64, s64, 12\nv_mul_f32 v18, v18, v217\nN 88,224,160\n"
"R 182,189,36864\ns_add_u32 s64, s64, 78643200\nv_mul_f32 v19, v19, v217\nN 88,240,164\ns_waitcnt vmcnt(11)\ns_barrier\n"
"G 4\nv_mul_f32 v20, v20, v217\nv_mfma_f32_16x16x32_bf16 v[128:131], v[168:171], a[0:3], 0\nR 160,190,24576\nv_mul_f32 v21, v21, v217\nv_mul_f32 v22, v22, v217\n"
"M 128,172,16\ns_mov_b32 m0, s51\nv_mul_f32 v23, v23, v217\nv_mul_f32 v24, v24, v217\nv_mul_f32 v25, v25, v217\nv_mfma_f32_16x16x32_bf16 v[144:147], v[168:171], a[4:7], 0\n"
"R 162,190,28672\nv_mul_f32 v26, v26, v217\nv_mul_f32 v27, v27, v217\nM 144,172,20\nv_mul_f32 v28, v28, v217\nv_mul_f32 v29, v29, v217\n"
"v_mul_f32 v30, v30, v217\nv_mfma_f32_16x16x32_bf16 v[32:35], v[168:171], a[8:11], 0\nR 164,190,32768\nv_mul_f32 v31, v31, v217\nv_cvt_pk_bf16_f32 v16, v16, v17\nM 32,172,24\n"
"v_cvt_pk_bf16_f32 v17, v18, v19\nv_cvt_pk_bf16_f32 v18, v20, v21\nv_cvt_pk_bf16_f32 v19, v22, v23\nv_mfma_f32_16x16x32_bf16 v[48:51], v[168:171], a[12:15], 0\nR 166,190,36864\nv_cvt_pk_bf16_f32 v20, v24, v25\n"
"v_cvt_pk_bf16_f32 v21, v26, v27\nM 48,172,28\nds_write_addtid_b32 v96 offset:0\nG 5\nv_cvt_pk_bf16_f32 v22, v28, v29\nv_cvt_pk_bf16_f32 v23, v30, v31\n"
"v_mul_f32 v0, v0, v214\nv_mfma_f32_16x16x32_bf16 v[52:55], v[176:179], a[12:15], 0\nR 168,191,24576\nds_write_addtid_b32 v97 offset:256\nv_mul_f32 v1, v1, v214\nv_mul_f32 v2, v2, v214\n"
"M 52,180,28\nds_write_addtid_b32 v98 offset:512\nv_mul_f32 v3, v3, v214\nv_mul_f32 v4, v4, v214\nv_mul_f32 v5, v5, v214\nv_mfma_f32_16x16x32_bf16 v[36:39], v[176:179], a[8:11], 0\n"
"R 170,191,28672\nds_write_addtid_b32 v99 offset:768\nv_mul_f32 v6, v6, v214\nv_mul_f32 v7, v7, v214\nM 36,180,24\nds_write_addtid_b32 v100 offset:1024\n"
"v_mul_f32 v8, v8, v214\nv_mul_f32 v9, v9, v214\nv_mul_f32 v10, v10, v214\nv_mfma_f32_16x16x32_bf16 v[148:151], v[176:179], a[4:7], 0\nR 172,191,32768\nds_write_addtid_b32 v101 offset:1280\n"
"v_mul_f32 v11, v11, v214\nv_mul_f32 v12, v12, v214\nM 148,180,20\nds_write_addtid_b32 v102 offset:1536\nv_mul_f32 v13, v13, v214\nv_mul_f32 v14, v14, v214\n"
"v_mul_f32 v15, v15, v214\nv_mfma_f32_16x16x32_bf16 v[132:135], v[176:179], a[0:3], 0\nR 174,191,36864\nds_write_addtid_b32 v103 offset:1792\nv_cvt_pk_bf16_f32 v0, v0, v1\nv_cvt_pk_bf16_f32 v1, v2, v3\n"
"M 132,180,16\nds_write_addtid_b32 v104 offset:2048\nG 13\nv_cvt_pk_bf16_f32 v2, v4, v5\nv_cvt_pk_bf16_f32 v3, v6, v7\nv_cvt_pk_bf16_f32 v4, v8, v9\n"
"v_mfma_f32_16x16x32_bf16 v[136:139], v[160:163], a[0:3], 0\nR 176,188,40960\nds_write_addtid_b32 v105 offset:2304\nv_cvt_pk_bf16_f32 v5, v10, v11\nv_cvt_pk_bf16_f32 v6, v12, v13\nM 136,164,16\n"
"ds_write_addtid_b32 v106 offset:2560\nds_write_addtid_b32 v107 offset:2816\ns_mov_b64 exec, s[66:67]\nbuffer_store_dwordx4 v[16:19], v252, s[20:23], s64 offen offset:0 sc1\ns_mov_b64 exec, -1\nv_cvt_pk_bf16_f32 v7, v14, v15\n"
"v_mfma_f32_16x16x32_bf16 v[152:155], v[160:163], a[4:7], 0\nR 178,188,45056\nds_write_addtid_b32 v108 offset:3072\nM 152,164,20\nds_write_addtid_b32 v109 offset:3328\nds_write_addtid_b32 v110 offset:3584\n"
"v_mfma_f32_16x16x32_bf16 v[40:43], v[160:163], a[8:11], 0\nR 180,188,49152\nds_write_addtid_b32 v111 offset:3840\nM 40,164,24\nds_write_addtid_b32 v112 offset:4096\nds_write_addtid_b32 v113 offset:4352\n"
"s_mov_b64 exec, s[66:67]\nbuffer_store_dwordx4 v[20:23], v252, s[20:23], s64 offen offset:1024 sc1\ns_mov_b64 exec, -1\nv_mfma_f32_16x16x32_bf16 v[56:59], v[160:163], a[12:15], 0\nR 182,188,53248\nds_write_addtid_b32 v114 offset:4608\n"
"M 56,164,28\nds_write_addtid_b32 v115 offset:4864\nds_write_addtid_b32 v116 offset:5120\nG 15\nv_mfma_f32_16x16x32_bf16 v[60:63], v[168:171], a[12:15], 0\nR 160,189,40960\n"
"ds_write_addtid_b32 v117 offset:5376\nM 60,172,28\nds_write_addtid_b32 v118 offset:5632\nds_write_addtid_b32 v119 offset:5888\nv_mfma_f32_16x16x32_bf16 v[44:47], v[168:171], a[8:11], 0\nR 162,189,45056\n"
"ds_write_addtid_b32 v120 offset:6144\nM 44,172,24\nds_write_addtid_b32 v121 offset:6400\nds_write_addtid_b32 v122 offset:6656\nds_write_addtid_b32 v123 offset:6912\nds_write_addtid_b32 v124 offset:7168\n"
"ds_write_addtid_b32 v125 offset:7424\nds_write_addtid_b32 v126 offset:7680\nds_write_addtid_b32 v127 offset:7936\nv_mfma_f32_16x16x32_bf16 v[156:159], v[168:171], a[4:7], 0\nR 164,189,49152\ns_add_u32 m0, s50, 57344\n"
"M 156,172,20\nbuffer_load_dwordx4 v221, s[4:7], s63 offen offset:1024 lds\nv_mfma_f32_16x16x32_bf16 v[140:143], v[168:171], a[0:3], 0\nR 166,189,53248\ns_waitcnt vmcnt(10) lgkmcnt(2)\ns_barrier\n"
"M 140,172,16\nds_read2st64_b32 v[96:97], v225 offset0:0 offset1:1\nds_read2st64_b32 v[98:99], v225 offset0:2 offset1:3\nv_lshrrev_b32 v226, 8, v252\nM 128,176,32\nR 168,190,40960\n"
"s_add_u32 s63, s52, 1966080\nds_read2st64_b32 v[100:101], v225 offset0:4 offset1:5\ns_lshl_b32 s60, s40, 7\ns_add_u32 s60, s60, 161280\nv_lshl_add_u32 v226, v226, 4, s60\nM 128,180,48\n"
"s_add_u32 m0, s50, 65536\nds_read2st64_b32 v[102:103], v225 offset0:6 offset1:7\nds_read2st64_b32 v[104:105], v225 offset0:8 offset1:9\nds_read_b128 v[230:233], v226 offset:0\nM 144,176,36\nR 170,190,45056\n"
"ds_read2st64_b32 v[106:107], v225 offset0:10 offset1:11\nds_read_b128 v[234:237], v226 offset:64\nM 144,180,52\nbuffer_load_dwordx4 v220, s[4:7], s63 offen lds\nds_read2st64_b32 v[108:109], v225 offset0:12 offset1:13\nM 32,176,40\n"
"R 172,190,49152\nds_read2st64_b32 v[110:111], v225 offset0:14 offset1:15\nM 32,180,56\nds_read2st64_b32 v[112:113], v225 offset0:16 offset1:17\nds_read2st64_b32 v[114:115], v225 offset0:18 offset1:19\nG 14\n"
"v_add_f32 v64, v64, v96\nM 48,176,44\nR 174,190,53248\nds_read2st64_b32 v[116:117], v225 offset0:20 offset1:21\nv_add_f32 v65, v65, v97\nM 48,180,60\n"
"ds_read2st64_b32 v[118:119], v225 offset0:22 offset1:23\nds_read2st64_b32 v[120:121], v225 offset0:24 offset1:25\nM 52,160,44\nR 176,191,40960\nds_read2st64_b32 v[122:123], v225 offset0:26 offset1:27\nG 15\n"
"v_add_f32 v66, v66, v98\nM 52,164,60\nbuffer_load_dwordx4 v221, s[4:7], s63 offen offset:1024 lds\nds_read2st64_b32 v[124:125], v225 offset0:28 offset1:29\nM 36,160,40\nR 178,191,45056\n"
"ds_read2st64_b32 v[126:127], v225 offset0:30 offset1:31\nv_add_f32 v67, v67, v99\nM 36,164,56\ns_add_u32 m0, s50, 73728\nv_add_f32 v68, v68, v100\nv_add_f32 v69, v69, v101\n"
"v_add_f32 v70, v70, v102\nM 148,160,36\nR 180,191,49152\nv_add_f32 v71, v71, v103\nv_add_f32 v72, v72, v104\nM 148,164,52\n"
"s_add_u32 s63, s52, 2162688\nv_add_f32 v73, v73, v105\nG 15\nv_add_f32 v74, v74, v106\nv_add_f32 v75, v75, v107\nM 132,160,32\n"
"R 182,191,53248\nG 15\nv_add_f32 v76, v76, v108\nv_add_f32 v77, v77, v109\nM 132,164,48\nG 10\n"
"v_add_f32 v78, v78, v110\nv_add_f32 v79, v79, v111\nv_add_f32 v80, v80, v112\nM 136,168,32\nR 160,184,0\nv_add_f32 v81, v81, v113\n"
"v_add_f32 v82, v82, v114\nM 136,172,48\nbuffer_load_dwordx4 v220, s[4:7], s63 offen lds\nv_add_f32 v83, v83, v115\nM 152,168,36\nR 162,184,4096\n"
"G 11\nv_add_f32 v84, v84, v116\nM 152,172,52\nv_add_f32 v85, v85, v117\nG 10\nv_add_f32 v86, v86, v118\n"
"M 40,168,40\nR 164,184,8192\nv_add_f32 v87, v87, v119\nM 40,172,56\nbuffer_load_dwordx4 v221, s[4:7], s63 offen offset:1024 lds\nM 56,168,44\n"
"R 166,184,12288\ns_add_u32 m0, s50, 81920\nM 56,172,60\nG 4\nv_add_f32 v88, v88, v120\nv_add_f32 v89, v89, v121\n"
"M 60,176,44\nR 168,185,0\ns_add_u32 s63, s52, 2359296\nM 60,180,60\nbuffer_load_dwordx4 v220, s[4:7], s63 offen lds\nM 44,176,40\n"
"R 170,185,4096\nv_add_f32 v90, v90, v122\nM 44,180,56\nv_add_f32 v91, v91, v123\nv_add_f32 v92, v92, v124\nM 156,176,36\n"
"R 172,185,8192\nv_add_f32 v93, v93, v125\nM 156,180,52\nbuffer_load_dwordx4 v221, s[4:7], s63 offen offset:1024 lds\nM 140,176,32\nR 174,185,12288\n"
"s_add_u32 m0, s50, 90112\nM 140,180,48\nG 4\nv_add_f32 v94, v94, v126\nv_add_f32 v95, v95, v127\nM 128,160,64\n"
"R 176,186,0\ns_add_u32 s63, s52, 2555904\nv_mul_f32 v64, v64, v230\nM 128,164,80\nbuffer_load_dwordx4 v220, s[4:7], s63 offen lds\nv_mul_f32 v65, v65, v231\n"
"v_add_u32 v108, 161792, v252\nM 144,160,68\nR 178,186,4096\nds_read_b128 v[112:115], v108\nv_mul_f32 v66, v66, v232\nv_mul_f32 v67, v67, v233\n"
"M 144,164,84\nds_read_b128 v[104:107], v108 offset:1024\nv_mul_f32 v68, v68, v234\nv_mul_f32 v69, v69, v235\nv_mul_f32 v70, v70, v236\nM 32,160,72\n"
"R 180,186,8192\nv_mul_f32 v71, v71, v237\nv_cvt_pk_bf16_f32 v67, v66, v67\nM 32,164,88\nv_cvt_pk_bf16_f32 v66, v64, v65\nv_cvt_pk_bf16_f32 v68, v68, v69\n"
"v_cvt_pk_bf16_f32 v69, v70, v71\nM 48,160,76\nR 182,186,12288\nv_mul_f32 v72, v72, v230\nv_mul_f32 v73, v73, v231\nM 48,164,92\n"
"v_mul_f32 v74, v74, v232\nv_mul_f32 v75, v75, v233\nG 6\nv_mul_f32 v76, v76, v234\nM 52,168,76\nR 160,187,0\n"
"v_mul_f32 v77, v77, v235\nv_mul_f32 v78, v78, v236\nM 52,172,92\nbuffer_load_dwordx4 v221, s[4:7], s63 offen offset:1024 lds\nv_mul_f32 v79, v79, v237\nG 3\n"
"v_lshlrev_b32 v100, 16, v104\nM 36,168,72\nR 162,187,4096\ns_add_u32 s63, s52, 2752512\nv_cvt_pk_bf16_f32 v72, v72, v73\nv_and_b32 v101, s77, v104\n"
"M 36,172,88\ns_add_u32 m0, s50, 98304\nv_cvt_pk_bf16_f32 v73, v74, v75\nv_cvt_pk_bf16_f32 v74, v76, v77\nv_cvt_pk_bf16_f32 v75, v78, v79\nM 148,168,68\n"
"R 164,187,8192\nv_mul_f32 v80, v80, v230\nv_mul_f32 v81, v81, v231\nM 148,172,84\nbuffer_load_dwordx4 v220, s[4:7], s63 offen lds\nv_mul_f32 v82, v82, v232\n"
"M 132,168,64\nR 166,187,12288\ns_waitcnt vmcnt(9)\ns_barrier\nv_mul_f32 v83, v83, v233\nM 132,172,80\n"
"G 4\nv_mul_f32 v84, v84, v234\nv_mul_f32 v85, v85, v235\nv_mul_f32 v86, v86, v236\nM 136,176,64\nR 168,184,16384\n"
"v_mul_f32 v87, v87, v237\nv_cvt_pk_bf16_f32 v78, v80, v81\nM 136,180,80\ns_mov_b64 exec, s[66:67]\nbuffer_store_dwordx2 v[68:69], v227, s[20:23], s64 offen offset:2048 sc1\ns_mov_b64 exec, -1\n"
"v_cvt_pk_bf16_f32 v79, v82, v83\nv_cvt_pk_bf16_f32 v80, v84, v85\nM 152,176,68\nR 170,184,20480\nv_cvt_pk_bf16_f32 v81, v86, v87\nv_mul_f32 v88, v88, v230\n"
"M 152,180,84\nv_mul_f32 v89, v89, v231\nv_mul_f32 v90, v90, v232\nv_mul_f32 v91, v91, v233\nM 40,176,72\nR 172,184,24576\n"
"s_mov_b32 s60, 155648\nv_bfe_u32 v246, v252, 4, 4\nv_lshl_add_u32 v246, v246, 3, s60\nv_mul_f32 v92, v92, v234\nM 40,180,88\nbuffer_load_dwordx4 v221, s[4:7], s63 offen offset:1024 lds\n"
"v_mul_f32 v93, v93, v235\nM 56,176,76\nR 174,184,28672\nds_read_b64 v[238:239], v246 offset:0\nv_mul_f32 v94, v94, v236\nM 56,180,92\n"
"G 5\nv_mul_f32 v95, v95, v237\nv_cvt_pk_bf16_f32 v84, v88, v89\nv_cvt_pk_bf16_f32 v85, v90, v91\nM 60,160,76\nR 176,185,16384\n"
"ds_read_b64 v[240:241], v246 offset:128\nv_cvt_pk_bf16_f32 v86, v92, v93\nM 60,164,92\ns_add_u32 m0, s50, 106496\ns_mov_b64 exec, s[66:67]\nbuffer_store_dwordx2 v[74:75], v227, s[20:23], s64 offen offset:2560 sc1\n"
"s_mov_b64 exec, -1\nLpg21:\ns_cbranch_execz LretW0n\nv_cvt_pk_bf16_f32 v87, v94, v95\nv_lshlrev_b32 v102, 16, v105\nM 44,160,72\n"
"R 178,185,20480\nds_read_b64 v[242:243], v246 offset:256\ns_lshl_b32 s60, s40, 12\ns_add_u32 s60, s60, 69632\nv_add_u32 v230, s60, v252\nv_add_u32 v231, s60, v227\n"
"M 44,164,88\ns_add_u32 s63, s52, 2949120\nv_and_b32 v103, s77, v105\nv_lshlrev_b32 v96, 16, v106\nM 156,160,68\nR 180,185,24576\n"
"ds_read_b64 v[244:245], v246 offset:384\nM 156,164,84\nbuffer_load_dwordx4 v220, s[4:7], s63 offen lds\nM 140,160,64\nR 182,185,28672\nds_read_b64 v[246:247], v246 offset:0\n"
"M 140,164,80\ns_mov_b64 exec, s[66:67]\nbuffer_store_dwordx2 v[80:81], v227, s[20:23], s64 offen offset:3072 sc1\ns_mov_b64 exec, -1\nG 9\nv_and_b32 v97, s77, v106\n"
"v_lshlrev_b32 v98, 16, v107\nM 128,168,96\nR 160,186,16384\nv_and_b32 v99, s77, v107\nM 128,172,112\nv_add_f32 v104, s78, v100\n"
"v_add_f32 v105, s78, v101\nM 144,168,100\nR 162,186,20480\nv_add_f32 v106, s78, v102\nM 144,172,116\nv_add_f32 v107, s78, v103\n"
"M 32,168,104\nR 164,186,24576\nG 9\nv_mfma_f32_16x16x32_bf16 v[128:131], v[238:241], v[208:211], v[128:131]\nM 32,172,120\nbuffer_load_dwordx4 v221, s[4:7], s63 offen offset:1024 lds\n"
"M 48,168,108\nR 166,186,28672\nG 8\nv_mfma_f32_16x16x32_bf16 v[132:135], v[240:243], v[208:211], v[132:135]\nM 48,172,124\ns_mov_b64 exec, s[66:67]\n"
"buffer_store_dwordx2 v[86:87], v227, s[20:23], s64 offen offset:3584 sc1\ns_mov_b64 exec, -1\nG 5\nM 52,176,108\nR 168,187,16384\nG 5\n"
"v_mfma_f32_16x16x32_bf16 v[144:147], v[244:247], v[210:213], v[144:147]\ns_not_b64 exec, s[66:67]\nds_write_b128 v230, v[16:19] offset:0\nds_write_b128 v230, v[20:23] offset:1024\ns_mov_b64 exec, -1\nM 52,180,124\n"
"M 36,176,104\nR 170,187,20480\nv_mfma_f32_16x16x32_bf16 v[148:151], v[238:241], v[210:213], v[148:151]\nM 36,180,120\ns_movk_i32 s63, 2048\nv_mad_u32_u24 v220, v228, s63, v229\n"
"v_add_u32 v221, 1047552, v220\ns_add_u32 m0, s50, 114688\ns_add_u32 s63, s53, 0\nbuffer_load_dwordx4 v220, s[8:11], s63 offen lds\nM 148,176,100\nR 172,187,24576\n"
"M 148,180,116\ns_not_b64 exec, s[66:67]\nds_write_b64 v231, v[68:69] offset:2048\nds_write_b64 v231, v[74:75] offset:2560\ns_mov_b64 exec, -1\nM 132,176,96\n"
"R 174,187,28672\nM 132,180,112\nG 8\nM 136,160,96\nR 176,184,49152\nM 136,164,112\n"
"M 152,160,100\nR 178,184,53248\nv_lshrrev_b32 v248, 5, v252\nM 152,164,116\nbuffer_load_dwordx4 v221, s[8:11], s63 offen offset:1024 lds\nM 40,160,104\n"
"R 180,184,57344\nv_lshlrev_b32 v248, 5, v248\nM 40,164,120\ns_and_b32 s60, s41, 7\ns_lshl2_add_u32 s60, s60, s40\nv_add_u32 v248, s60, v248\n"
"M 56,160,108\nR 182,184,61440\nv_mul_u32_u24 v248, 7168, v248\nM 56,164,124\nG 4\nv_bfe_u32 v249, v252, 4, 1\n"
"M 60,168,108\nR 160,185,49152\ns_not_b64 exec, s[66:67]\nds_write_b64 v231, v[80:81] offset:3072\nds_write_b64 v231, v[86:87] offset:3584\ns_mov_b64 exec, -1\n"
"M 60,172,124\ns_sub_u32 s60, s54, s54\ns_add_u32 s60, s60, 128\nv_mul_lo_u32 v249, v249, s60\nM 44,168,104\nR 162,185,53248\n"
"v_add_u32 v248, v248, v249\nM 44,172,120\ns_add_u32 m0, s50, 0\nM 156,168,100\nR 164,185,57344\nM 156,172,116\n"
"s_add_u32 s63, s53, 65536\nM 140,168,96\nR 166,185,61440\nM 140,172,112\nG 6\nM 128,176,128\n"
"R 168,186,49152\ns_waitcnt vmcnt(12) lgkmcnt(4)\ns_barrier\nM 128,180,144\nbuffer_load_dwordx4 v220, s[8:11], s63 offen lds\nM 144,176,132\n"
"R 170,186,53248\nM 144,180,148\ns_cmp_eq_u32 s78, 0\ns_cbranch_scc1 Lq656\nv_mov_b32 v230, 0\nv_mov_b32 v231, 0\n"
"v_mov_b32 v232, 0\nv_mov_b32 v233, 0\nv_mov_b32 v64, 0\nv_mov_b32 v65, 0\nv_mov_b32 v70, 0\nv_mov_b32 v71, 0\n"
"v_mov_b32 v234, 0\nv_mov_b32 v235, 0\nv_mov_b32 v236, 0\nv_mov_b32 v237, 0\nv_mov_b32 v76, 0\nv_mov_b32 v77, 0\n"
"v_mov_b32 v82, 0\nv_mov_b32 v83, 0\nLq656:\ns_lshl4_add_u32 s65, s41, s42\nM 32,176,136\nR 172,186,57344\n"
"s_lshl_b32 s65, s65, 12\nM 32,180,152\ns_add_u32 s65, s65, 78577664\nM 48,176,140\nR 174,186,61440\ns_bfm_b64 exec, 16, 0\n"
"buffer_load_dword v249, v248, s[12:15], s54 offen\ns_mov_b64 exec, -1\nM 48,180,156\nG 4\nM 52,160,140\nR 176,187,49152\n"
"M 52,164,156\nbuffer_load_dwordx4 v221, s[8:11], s63 offen offset:1024 lds\nM 36,160,136\nR 178,187,53248\nM 36,164,152\ns_add_u32 m0, s50, 8192\n"
"M 148,160,132\nR 180,187,57344\nM 148,164,148\ns_add_u32 s63, s53, 131072\nM 132,160,128\nR 182,187,61440\n"
"M 132,164,144\nG 4\nM 136,168,128\nR 160,188,0\nM 136,172,144\nbuffer_load_dwordx4 v220, s[8:11], s63 offen lds\n"
"M 152,168,132\nR 162,188,4096\nM 152,172,148\nM 40,168,136\nR 164,188,8192\nM 40,172,152\n"
"M 56,168,140\nR 166,188,12288\nv_mfma_f32_16x16x32_bf16 v[136:139], v[242:245], v[208:211], v[136:139]\ns_bfm_b64 exec, 16, 16\nbuffer_load_dword v249, v248, s[12:15], s54 offen\ns_mov_b64 exec, -1\n"
"M 56,172,156\nG 4\nM 60,176,140\nR 168,189,0\nv_mfma_f32_16x16x32_bf16 v[140:143], v[244:247], v[208:211], v[140:143]\nM 60,180,156\n"
"buffer_load_dwordx4 v221, s[8:11], s63 offen offset:1024 lds\nM 44,176,136\nR 170,189,4096\nv_mfma_f32_16x16x32_bf16 v[152:155], v[240:243], v[210:213], v[152:155]\ns_add_u32 s63, s53, 196608\nM 44,180,152\n"
"s_add_u32 m0, s50, 16384\nM 156,176,132\nR 172,189,8192\nv_mfma_f32_16x16x32_bf16 v[156:159], v[242:245], v[210:213], v[156:159]\nM 156,180,148\nbuffer_load_dwordx4 v220, s[8:11], s63 offen lds\n"
"M 140,176,128\nR 174,189,12288\nM 140,180,144\nG 4\nM 128,160,160\nR 176,190,0\n"
"M 128,164,176\nM 144,160,164\nR 178,190,4096\nM 144,164,180\nbuffer_load_dwordx4 v221, s[8:11], s63 offen offset:1024 lds\nM 32,160,168\n"
"R 180,190,8192\nM 32,164,184\ns_add_u32 m0, s50, 24576\nM 48,160,172\nR 182,190,12288\ns_bfm_b64 exec, 16, 32\n"
"buffer_load_dword v249, v248, s[12:15], s54 offen\ns_mov_b64 exec, -1\nM 48,164,188\nG 4\nM 52,168,172\nR 160,191,0\n"
"M 52,172,188\ns_add_u32 s63, s53, 262144\nM 36,168,168\nR 162,191,4096\nM 36,172,184\nbuffer_load_dwordx4 v220, s[8:11], s63 offen lds\n"
"M 148,168,164\nR 164,191,8192\ns_lshl_b32 s60, s40, 12\ns_add_u32 s60, s60, 65536\nv_add_u32 v226, s60, v252\nv_add_u32 v111, s60, v227\n"
"M 148,172,180\nM 132,168,160\nR 166,191,12288\ns_cmp_eq_u32 s40, 0\ns_cselect_b64 s[86:87], 0, -1\ns_cselect_b32 s60, s41, 0\n"
"s_cmp_lg_u32 s60, 0\ns_cselect_b64 s[66:67], -1, 0\nM 132,172,176\nG 4\nM 136,176,160\nR 168,188,16384\n"
"M 136,180,176\ns_mov_b64 exec, s[86:87]\nds_read_b128 v[230:233], v226 offset:0\nds_read_b64 v[64:65], v111 offset:2048\ns_mov_b64 exec, -1\nM 152,176,164\n"
"R 170,188,20480\ns_waitcnt vmcnt(13) lgkmcnt(1)\ns_barrier\nM 152,180,180\nbuffer_load_dwordx4 v221, s[8:11], s63 offen offset:1024 lds\nM 40,176,168\n"
"R 172,188,24576\nM 40,180,184\ns_mov_b64 exec, s[86:87]\nds_read_b64 v[70:71], v111 offset:2560\nds_read_b128 v[234:237], v226 offset:1024\ns_mov_b64 exec, -1\n"
"M 56,176,172\nR 174,188,28672\ns_bfm_b64 exec, 16, 48\nbuffer_load_dword v249, v248, s[12:15], s54 offen\ns_mov_b64 exec, -1\nM 56,180,188\n"
"M 60,160,172\nR 176,189,16384\nM 60,164,188\ns_mov_b64 exec, s[86:87]\nds_read_b64 v[76:77], v111 offset:3072\nds_read_b64 v[82:83], v111 offset:3584\n"
"s_mov_b64 exec, -1\nM 44,160,168\nR 178,189,20480\nM 44,164,184\nM 156,160,164\nR 180,189,24576\n"
"M 156,164,180\ns_mov_b64 exec, s[66:67]\nbuffer_load_dwordx4 v[230:233], v252, s[20:23], s65 offen offset:0 sc1\ns_mov_b64 exec, -1\nM 140,160,160\nR 182,189,28672\n"
"M 140,164,176\nG 6\nM 32,168,200\nR 160,190,16384\ns_mov_b64 exec, s[66:67]\nbuffer_load_dwordx2 v[64:65], v227, s[20:23], s65 offen offset:2048 sc1\n"
"s_mov_b64 exec, -1\nM 32,172,216\nR 162,190,20480\nM 48,168,204\nR 164,190,24576\nM 48,172,220\n"
"R 166,190,28672\nG 4\nM 52,176,204\nR 168,191,16384\ns_mov_b64 exec, s[66:67]\nbuffer_load_dwordx2 v[70:71], v227, s[20:23], s65 offen offset:2560 sc1\n"
"s_mov_b64 exec, -1\nM 52,180,220\nR 170,191,20480\nM 36,176,200\nR 172,191,24576\nM 36,180,216\n"
"R 174,191,28672\nG 4\nM 40,160,200\nR 176,188,32768\ns_mov_b64 exec, s[66:67]\nbuffer_load_dwordx4 v[234:237], v252, s[20:23], s65 offen offset:1024 sc1\n"
"s_mov_b64 exec, -1\nM 40,164,216\nR 178,188,36864\nM 56,160,204\nR 180,188,40960\nM 56,164,220\n"
"R 182,188,45056\nG 4\nM 60,168,204\nR 160,189,32768\ns_mov_b64 exec, s[66:67]\nbuffer_load_dwordx2 v[76:77], v227, s[20:23], s65 offen offset:3072 sc1\n"
"s_mov_b64 exec, -1\nM 60,172,220\nR 162,189,36864\nM 44,168,200\nR 164,189,40960\nM 44,172,216\n"
"R 166,189,45056\nG 4\nM 32,176,232\nR 168,190,32768\ns_mov_b64 exec, s[66:67]\nbuffer_load_dwordx2 v[82:83], v227, s[20:23], s65 offen offset:3584 sc1\n"
"s_mov_b64 exec, -1\nLq690:\ns_lshr_b32 s60, s45, 3\ns_lshl2_add_u32 s60, s60, s40\ns_lshl_b32 s60, s60, 8\nv_lshl_add_u32 v224, v252, 3, s60\n"
"v_add_u32 v224, Lq690+4096-LqPC, v224\nv_min_u32 v224, LqE-LqPC-4, v224\ns_mov_b64 exec, 3\nglobal_load_dword v224, v224, s[98:99]\ns_mov_b64 exec, -1\nM 32,180,248\n"
"R 170,190,36864\nM 48,176,236\nR 172,190,40960\nM 48,180,252\nR 174,190,45056\nG 4\n"
"M 52,160,236\nR 176,191,32768\nM 52,164,252\nR 178,191,36864\nM 36,160,232\nR 180,191,40960\n"
"M 36,164,248\nR 182,191,45056\nG 4\nM 40,168,232\nR 160,188,16384\nM 40,172,248\n"
"R 162,188,20480\nM 56,168,236\nR 164,188,24576\nM 56,172,252\nR 166,188,28672\nG 4\n"
"M 60,176,236\nR 168,189,16384\nM 60,180,252\nR 170,189,20480\nM 44,176,232\nR 172,189,24576\n"
"M 44,180,248\nR 174,189,28672\nG 4\nM 128,160,192\nR 176,190,16384\nM 128,164,208\n"
"R 178,190,20480\ns_mov_b32 m0, s51\nM 144,160,196\nR 180,190,24576\nds_write_addtid_b32 v32 offset:0\nM 144,164,212\n"
"R 182,190,28672\nds_write_addtid_b32 v33 offset:256\nG 6\nM 148,168,196\nR 160,191,16384\nds_write_addtid_b32 v34 offset:512\n"
"M 148,172,212\nR 162,191,20480\nds_write_addtid_b32 v35 offset:768\nM 132,168,192\nR 164,191,24576\nds_write_addtid_b32 v36 offset:1024\n"
"M 132,172,208\nR 166,191,28672\nds_write_addtid_b32 v37 offset:1280\nG 9\nM 136,176,192\nR 168,188,32768\n"
"ds_write_addtid_b32 v38 offset:1536\nM 136,180,208\nR 170,188,36864\nds_write_addtid_b32 v39 offset:1792\nM 152,176,196\nR 172,188,40960\n"
"ds_write_addtid_b32 v40 offset:2048\nM 152,180,212\nR 174,188,45056\nds_write_addtid_b32 v41 offset:2304\nG 9\nM 156,160,196\n"
"R 176,189,32768\nds_write_addtid_b32 v42 offset:2560\nM 156,164,212\nR 178,189,36864\nds_write_addtid_b32 v43 offset:2816\nM 140,160,192\n"
"R 180,189,40960\nds_write_addtid_b32 v44 offset:3072\nds_write_addtid_b32 v45 offset:3328\nds_write_addtid_b32 v46 offset:3584\nds_write_addtid_b32 v47 offset:3840\nds_write_addtid_b32 v48 offset:4096\n"
"ds_write_addtid_b32 v49 offset:4352\nds_write_addtid_b32 v50 offset:4608\nds_write_addtid_b32 v51 offset:4864\nds_write_addtid_b32 v52 offset:5120\nds_write_addtid_b32 v53 offset:5376\nds_write_addtid_b32 v54 offset:5632\n"
"ds_write_addtid_b32 v55 offset:5888\nds_write_addtid_b32 v56 offset:6144\nds_write_addtid_b32 v57 offset:6400\nds_write_addtid_b32 v58 offset:6656\nds_write_addtid_b32 v59 offset:6912\nds_write_addtid_b32 v60 offset:7168\n"
"ds_write_addtid_b32 v61 offset:7424\nds_write_addtid_b32 v62 offset:7680\nds_write_addtid_b32 v63 offset:7936\nM 140,164,208\nR 182,189,45056\nG 15\n"
"M 128,168,224\nR 160,190,32768\ns_add_u32 m0, s50, 32768\nM 128,172,240\nR 162,190,36864\nG 3\n"
"s_barrier\ns_add_u32 s63, s53, 327680\nM 144,168,228\nR 164,190,40960\nbuffer_load_dwordx4 v220, s[8:11], s63 offen lds\nds_read2st64_b32 v[32:33], v225 offset0:0 offset1:1\n"
"M 144,172,244\nR 166,190,45056\nbuffer_load_dwordx4 v221, s[8:11], s63 offen offset:1024 lds\nds_read2st64_b32 v[34:35], v225 offset0:2 offset1:3\nG 6\nM 148,176,228\n"
"R 168,191,32768\ns_add_u32 m0, s50, 40960\nds_read2st64_b32 v[36:37], v225 offset0:4 offset1:5\nM 148,180,244\nR 170,191,36864\ns_add_u32 s63, s53, 393216\n"
"ds_read2st64_b32 v[38:39], v225 offset0:6 offset1:7\nM 132,176,224\nR 172,191,40960\nds_read2st64_b32 v[40:41], v225 offset0:8 offset1:9\nM 132,180,240\nR 174,191,45056\n"
"ds_read2st64_b32 v[42:43], v225 offset0:10 offset1:11\nG 9\nM 136,160,224\nbuffer_load_dwordx4 v220, s[8:11], s63 offen lds\nds_read2st64_b32 v[44:45], v225 offset0:12 offset1:13\nM 136,164,240\n"
"ds_read2st64_b32 v[46:47], v225 offset0:14 offset1:15\nds_read2st64_b32 v[48:49], v225 offset0:16 offset1:17\nM 152,160,228\nbuffer_load_dwordx4 v221, s[8:11], s63 offen offset:1024 lds\nds_read2st64_b32 v[50:51], v225 offset0:18 offset1:19\nM 152,164,244\n"
"ds_read2st64_b32 v[52:53], v225 offset0:20 offset1:21\nds_read2st64_b32 v[54:55], v225 offset0:22 offset1:23\nG 7\nM 156,168,228\nds_read2st64_b32 v[56:57], v225 offset0:24 offset1:25\nds_read2st64_b32 v[58:59], v225 offset0:26 offset1:27\n"
"M 156,172,244\nds_read2st64_b32 v[60:61], v225 offset0:28 offset1:29\nds_read2st64_b32 v[62:63], v225 offset0:30 offset1:31\nM 140,168,224\nM 140,172,240\nG 7\n"
"v_pk_add_f32 v[144:145], v[144:145], v[48:49]\nG 6\nv_pk_add_f32 v[146:147], v[146:147], v[50:51]\nG 5\nv_pk_add_f32 v[148:149], v[148:149], v[52:53]\nG 4\n"
"v_pk_add_f32 v[150:151], v[150:151], v[54:55]\nG 3\nv_pk_add_f32 v[152:153], v[152:153], v[56:57]\nG 2\nv_pk_add_f32 v[154:155], v[154:155], v[58:59]\nG 1\n"
"v_pk_add_f32 v[156:157], v[156:157], v[60:61]\nG 0\nv_pk_add_f32 v[158:159], v[158:159], v[62:63]\nv_pk_add_f32 v[128:129], v[128:129], v[32:33]\nv_pk_add_f32 v[130:131], v[130:131], v[34:35]\nv_pk_add_f32 v[132:133], v[132:133], v[36:37]\n"
"v_pk_add_f32 v[134:135], v[134:135], v[38:39]\nv_pk_add_f32 v[136:137], v[136:137], v[40:41]\nv_pk_add_f32 v[138:139], v[138:139], v[42:43]\nv_pk_add_f32 v[140:141], v[140:141], v[44:45]\nv_pk_add_f32 v[142:143], v[142:143], v[46:47]\nv_cvt_pk_bf16_f32 v144, v144, v145\n"
"v_cvt_pk_bf16_f32 v145, v146, v147\nv_cvt_pk_bf16_f32 v146, v148, v149\nv_cvt_pk_bf16_f32 v147, v150, v151\nv_cvt_pk_bf16_f32 v152, v152, v153\nv_cvt_pk_bf16_f32 v153, v154, v155\nv_cvt_pk_bf16_f32 v154, v156, v157\n"
"v_cvt_pk_bf16_f32 v155, v158, v159\nv_cvt_pk_bf16_f32 v128, v128, v129\nv_cvt_pk_bf16_f32 v129, v130, v131\nv_cvt_pk_bf16_f32 v130, v132, v133\nv_cvt_pk_bf16_f32 v131, v134, v135\nv_cvt_pk_bf16_f32 v136, v136, v137\n"
"v_cvt_pk_bf16_f32 v137, v138, v139\nv_cvt_pk_bf16_f32 v138, v140, v141\nv_cvt_pk_bf16_f32 v139, v142, v143\nv_mfma_f32_16x16x32_bf16 v[52:55], v[16:19], v[144:147], v[96:99]\nv_mfma_f32_16x16x32_bf16 v[48:51], v[0:3], v[144:147], v[100:103]\nv_mfma_f32_16x16x32_bf16 v[60:63], v[20:23], v[152:155], v[96:99]\n"
"v_mfma_f32_16x16x32_bf16 v[56:59], v[4:7], v[152:155], v[100:103]\ns_waitcnt vmcnt(8)\nv_max3_u32 v248, v233, v65, v71\nv_cmp_eq_u32 vcc, -1, v248\ns_nop 0\ns_cbranch_vccnz Lq692\n"
"Lq693:\ns_waitcnt vmcnt(5)\nv_max3_u32 v248, v237, v77, v83\nv_cmp_eq_u32 vcc, -1, v248\ns_nop 0\ns_cbranch_vccnz Lq694\n"
"Lq695:\nv_mfma_f32_16x16x32_bf16 v[36:39], v[0:3], v[128:131], v[96:99]\nv_mfma_f32_16x16x32_bf16 v[32:35], v[230:233], v[128:131], v[104:107]\nLpg22:\ns_cbranch_execz LretW0n\nv_mfma_f32_16x16x32_bf16 v[44:47], v[4:7], v[136:139], v[96:99]\n"
"v_mfma_f32_16x16x32_bf16 v[40:43], v[234:237], v[136:139], v[104:107]\ns_lshl_b32 s84, s42, 11\ns_add_u32 s84, s84, s49\ns_add_u32 s84, s84, 6291456\ns_add_u32 s85, s84, 32768\nv_mov_b32 v238, s46\n"
"v_mov_b32 v239, s47\nv_max_f32 v52, v52, v53\nv_max_f32 v54, v54, v55\nv_max_f32 v60, v60, v61\nv_max_f32 v62, v62, v63\nv_max3_f32 v52, v52, v48, v49\n"
"v_max3_f32 v54, v54, v50, v51\nv_max3_f32 v60, v60, v56, v57\nv_max3_f32 v62, v62, v58, v59\nv_max_f32 v36, v36, v37\nv_max_f32 v38, v38, v39\nv_max_f32 v44, v44, v45\n"
"v_max_f32 v46, v46, v47\nv_max3_f32 v36, v36, v32, v33\nv_max3_f32 v38, v38, v34, v35\nv_max3_f32 v44, v44, v40, v41\nv_max3_f32 v46, v46, v42, v43\nv_max_f32 v118, v52, v54\n"
"v_max_f32 v119, v60, v62\nv_max_f32 v116, v36, v38\nv_max_f32 v117, v44, v46\ns_nop 0\nv_permlane32_swap_b32 v116, v118\nv_permlane32_swap_b32 v117, v119\n"
"v_max_f32 v116, v116, v118\nv_max_f32 v117, v117, v119\ns_nop 1\nv_permlane16_swap_b32 v116, v117\nv_max_f32 v116, v116, v117\nv_mov_b32 v117, v116\n"
"s_nop 1\nv_permlane16_swap_b32 v116, v117\nv_mov_b32 v118, v116\nv_mov_b32 v119, v117\ns_nop 0\nv_permlane32_swap_b32 v116, v118\n"
"v_permlane32_swap_b32 v117, v119\nv_mul_f32 v116, v116, v215\nv_mul_f32 v118, v118, v218\nv_mul_f32 v117, v117, v215\nv_mul_f32 v119, v119, v218\nv_fma_f32 v52, v52, v218, -v118\n"
"v_fma_f32 v54, v54, v218, -v118\nv_fma_f32 v60, v60, v218, -v119\nv_fma_f32 v62, v62, v218, -v119\nv_fma_f32 v36, v36, v215, -v116\nv_fma_f32 v38, v38, v215, -v116\nv_fma_f32 v44, v44, v215, -v117\n"
"v_fma_f32 v46, v46, v215, -v117\nv_exp_f32 v52, v52\nv_exp_f32 v54, v54\nv_exp_f32 v60, v60\nv_exp_f32 v62, v62\nv_exp_f32 v36, v36\n"
"v_exp_f32 v38, v38\nv_exp_f32 v44, v44\nv_exp_f32 v46, v46\nv_add_f32 v118, v52, v54\nv_add_f32 v119, v60, v62\nv_add_f32 v116, v36, v38\n"
"v_add_f32 v117, v44, v46\ns_nop 0\nv_permlane32_swap_b32 v116, v118\nv_permlane32_swap_b32 v117, v119\nv_add_f32 v116, v116, v118\nv_add_f32 v117, v117, v119\n"
"s_nop 1\nv_permlane16_swap_b32 v116, v117\nv_add_f32 v116, v116, v117\ns_barrier\nv_rcp_f32 v116, v116\ns_nop 0\n"
"v_mov_b32 v117, v116\ns_nop 1\nv_permlane16_swap_b32 v116, v117\nv_mov_b32 v118, v116\nv_mov_b32 v119, v117\ns_nop 0\n"
"v_permlane32_swap_b32 v116, v118\nv_permlane32_swap_b32 v117, v119\nv_mul_f32 v52, v52, v118\nv_mul_f32 v54, v54, v118\nv_mul_f32 v60, v60, v119\nv_mul_f32 v62, v62, v119\n"
"v_mul_f32 v36, v36, v116\nv_mul_f32 v38, v38, v116\nv_mul_f32 v44, v44, v117\nv_mul_f32 v46, v46, v117\nv_cvt_pk_bf16_f32 v52, v52, v52\nv_cvt_pk_bf16_f32 v54, v54, v54\n"
"v_cvt_pk_bf16_f32 v60, v60, v60\nv_cvt_pk_bf16_f32 v62, v62, v62\nv_cvt_pk_bf16_f32 v36, v36, v36\nv_cvt_pk_bf16_f32 v38, v38, v38\nv_cvt_pk_bf16_f32 v44, v44, v44\nv_cvt_pk_bf16_f32 v46, v46, v46\n"
"v_and_b32 v48, v52, v112\nv_and_b32 v49, v54, v113\nv_and_b32 v50, v52, v114\nv_and_b32 v51, v54, v115\nv_and_b32 v56, v60, v112\nv_and_b32 v57, v62, v113\n"
"v_and_b32 v58, v60, v114\nv_and_b32 v59, v62, v115\nv_mfma_f32_16x16x32_bf16 v[144:147], v[66:69], v[48:51], 0\nv_mfma_f32_16x16x32_bf16 v[148:151], v[72:75], v[48:51], 0\nv_and_b32 v32, v36, v112\nv_and_b32 v33, v38, v113\n"
"v_and_b32 v34, v36, v114\nv_and_b32 v35, v38, v115\nv_mfma_f32_16x16x32_bf16 v[152:155], v[78:81], v[56:59], 0\nv_mfma_f32_16x16x32_bf16 v[156:159], v[84:87], v[56:59], 0\nv_and_b32 v40, v44, v112\nv_and_b32 v41, v46, v113\n"
"v_and_b32 v42, v44, v114\nv_and_b32 v43, v46, v115\nv_mfma_f32_16x16x32_bf16 v[128:131], v[64:67], v[32:35], 0\nv_mfma_f32_16x16x32_bf16 v[132:135], v[70:73], v[32:35], 0\nv_mfma_f32_16x16x32_bf16 v[136:139], v[76:79], v[40:43], 0\nv_mfma_f32_16x16x32_bf16 v[140:143], v[82:85], v[40:43], 0\n"
"v_cvt_pk_bf16_f32 v144, v144, v145\nv_cvt_pk_bf16_f32 v145, v146, v147\nv_cvt_pk_bf16_f32 v146, v148, v149\nv_cvt_pk_bf16_f32 v147, v150, v151\nbuffer_store_dwordx4 v[144:147], v252, s[20:23], s85 offen offset:0 sc1\nv_cvt_pk_bf16_f32 v152, v152, v153\n"
"v_cvt_pk_bf16_f32 v153, v154, v155\nv_cvt_pk_bf16_f32 v154, v156, v157\nv_cvt_pk_bf16_f32 v155, v158, v159\nbuffer_store_dwordx4 v[152:155], v252, s[20:23], s85 offen offset:1024 sc1\nv_cvt_pk_bf16_f32 v128, v128, v129\nv_cvt_pk_bf16_f32 v129, v130, v131\n"
"v_cvt_pk_bf16_f32 v130, v132, v133\nv_cvt_pk_bf16_f32 v131, v134, v135\nbuffer_store_dwordx4 v[128:131], v252, s[20:23], s84 offen offset:0 sc1\nv_cvt_pk_bf16_f32 v136, v136, v137\nv_cvt_pk_bf16_f32 v137, v138, v139\nv_cvt_pk_bf16_f32 v138, v140, v141\n"
"v_cvt_pk_bf16_f32 v139, v142, v143\nbuffer_store_dwordx4 v[136:139], v252, s[20:23], s84 offen offset:1024 sc1\ns_mov_b64 exec, 0\ns_cmp_eq_u32 s40, 1\ns_cbranch_scc1 LretO1\ns_cmp_eq_u32 s40, 2\n"
"s_cbranch_scc1 LretO1\ns_cmp_eq_u32 s40, 3\ns_cbranch_scc1 LretO1\ns_branch Lpg23\nLretO1:\ns_mov_b64 exec, -1\n"
"s_add_u32 s61, s79, 88498176\ns_lshl_b32 s62, s42, 3\ns_add_u32 s61, s61, s62\ns_waitcnt vmcnt(0)\ns_mov_b64 exec, 1\nbuffer_atomic_swap_x2 v[238:239], off, s[20:23], s61 sc1\n"
"s_mov_b64 exec, -1\nv_bfe_u32 v250, v252, 7, 1\nv_mul_u32_u24 v250, 2176, v250\nv_bfe_u32 v251, v252, 4, 3\nv_lshl_add_u32 v250, v251, 3, v250\ns_lshl_b32 s60, s90, 6\n"
"v_add_u32 v250, s60, v250\ns_sleep 3\nv_mov_b32 v248, 0\nv_mov_b32 v249, 0\ns_add_u32 s61, s80, 88498176\ns_mov_b64 exec, 0xffff\n"
"buffer_atomic_or_x2 v[248:249], v250, s[20:23], s61 offen sc0 sc1\ns_mov_b64 exec, -1\ns_add_u32 m0, s50, 49152\ns_add_u32 s63, s53, 458752\nbuffer_load_dwordx4 v220, s[8:11], s63 offen lds\nbuffer_load_dwordx4 v221, s[8:11], s63 offen offset:1024 lds\n"
"s_add_u32 m0, s50, 57344\ns_add_u32 s63, s53, 524288\nbuffer_load_dwordx4 v220, s[8:11], s63 offen lds\nbuffer_load_dwordx4 v221, s[8:11], s63 offen offset:1024 lds\ns_add_u32 m0, s50, 65536\ns_add_u32 s63, s53, 589824\n"
"buffer_load_dwordx4 v220, s[8:11], s63 offen lds\nbuffer_load_dwordx4 v221, s[8:11], s63 offen offset:1024 lds\ns_add_u32 m0, s50, 73728\ns_add_u32 s63, s53, 655360\nbuffer_load_dwordx4 v220, s[8:11], s63 offen lds\nbuffer_load_dwordx4 v221, s[8:11], s63 offen offset:1024 lds\n"
"s_add_u32 m0, s50, 81920\ns_add_u32 s63, s53, 720896\nbuffer_load_dwordx4 v220, s[8:11], s63 offen lds\nbuffer_load_dwordx4 v221, s[8:11], s63 offen offset:1024 lds\ns_waitcnt vmcnt(10)\nv_cmp_eq_u64 s[86:87], s[46:47], v[248:249]\n"
"s_nop 1\ns_and_b32 s88, s86, 0xffff\ns_cmp_eq_u32 s88, 0xffff\ns_cbranch_scc1 Lq706\nv_mov_b32 v246, 0\nv_mov_b32 v247, 0\n"
"s_add_u32 s61, s80, 88498176\ns_mov_b64 exec, 0xffff\nbuffer_atomic_or_x2 v[246:247], v250, s[20:23], s61 offen sc0 sc1\ns_mov_b64 exec, -1\nLq706:\ns_add_u32 m0, s50, 90112\n"
"s_add_u32 s63, s53, 786432\nbuffer_load_dwordx4 v220, s[8:11], s63 offen lds\nbuffer_load_dwordx4 v221, s[8:11], s63 offen offset:1024 lds\ns_add_u32 m0, s50, 98304\ns_add_u32 s63, s53, 851968\nbuffer_load_dwordx4 v220, s[8:11], s63 offen lds\n"
"buffer_load_dwordx4 v221, s[8:11], s63 offen offset:1024 lds\ns_add_u32 m0, s50, 106496\ns_add_u32 s63, s53, 917504\nbuffer_load_dwordx4 v220, s[8:11], s63 offen lds\nbuffer_load_dwordx4 v221, s[8:11], s63 offen offset:1024 lds\nv_bfe_u32 v234, v252, 4, 4\n"
"v_lshrrev_b32 v235, 8, v252\nv_lshlrev_b32 v235, 2, v235\nv_sub_u32 v234, v234, v235\nv_lshlrev_b32 v235, 4, v234\ns_mov_b64 s[60:61], 0x3f80\nv_lshlrev_b64 v[208:209], v235, s[60:61]\n"
"v_cmp_gt_u32 s[60:61], 4, v234\ns_nop 1\nv_cndmask_b32 v212, 0, v208, s[60:61]\nv_cndmask_b32 v208, 0, v208, s[60:61]\nv_cndmask_b32 v213, 0, v209, s[60:61]\nv_cndmask_b32 v209, 0, v209, s[60:61]\n"
"s_nop 1\nv_mfma_f32_16x16x32_bf16 v[0:3], v[208:211], v[192:195], 0\nv_mfma_f32_16x16x32_bf16 v[4:7], v[210:213], v[192:195], 0\nv_mfma_f32_16x16x32_bf16 v[8:11], v[208:211], v[196:199], 0\nv_mfma_f32_16x16x32_bf16 v[12:15], v[210:213], v[196:199], 0\nv_mfma_f32_16x16x32_bf16 v[16:19], v[208:211], v[200:203], 0\n"
"v_mfma_f32_16x16x32_bf16 v[20:23], v[210:213], v[200:203], 0\nv_mfma_f32_16x16x32_bf16 v[24:27], v[208:211], v[204:207], 0\nv_mfma_f32_16x16x32_bf16 v[28:31], v[210:213], v[204:207], 0\ns_cmp_eq_u32 s88, 0xffff\ns_cbranch_scc1 Lq707\ns_waitcnt vmcnt(6)\n"
"v_mov_b32 v248, v246\nv_mov_b32 v249, v247\nv_cmp_eq_u64 s[86:87], s[46:47], v[248:249]\ns_nop 1\ns_and_b32 s62, s86, 0xffff\ns_cmp_eq_u32 s62, 0xffff\n"
"s_cbranch_scc1 Lq698\nLq697:\nv_mov_b32 v248, 0\nv_mov_b32 v249, 0\ns_add_u32 s61, s80, 88498176\ns_mov_b64 exec, 0xffff\n"
"buffer_atomic_or_x2 v[248:249], v250, s[20:23], s61 offen sc0 sc1\ns_mov_b64 exec, -1\ns_waitcnt vmcnt(0)\nv_cmp_eq_u64 s[86:87], s[46:47], v[248:249]\ns_nop 1\ns_and_b32 s62, s86, 0xffff\n"
"s_cmp_eq_u32 s62, 0xffff\ns_cbranch_scc1 Lq698\ns_sub_u32 s48, s48, 1\ns_cmp_gt_i32 s48, 0\ns_cbranch_scc1 Lq697\nLq698:\n"
"Lq707:\ns_add_u32 s64, s70, 6291456\nbuffer_load_dwordx4 a[0:3], v252, s[20:23], s64 offen offset:0 sc1\ns_add_u32 s65, s70, 6324224\nbuffer_load_dwordx4 a[4:7], v252, s[20:23], s65 offen offset:0 sc1\ns_add_u32 s66, s71, 6291456\n"
"buffer_load_dwordx4 a[8:11], v252, s[20:23], s66 offen offset:0 sc1\ns_add_u32 s67, s71, 6324224\nbuffer_load_dwordx4 a[12:15], v252, s[20:23], s67 offen offset:0 sc1\nbuffer_load_dwordx4 a[16:19], v252, s[20:23], s64 offen offset:1024 sc1\nbuffer_load_dwordx4 a[20:23], v252, s[20:23], s65 offen offset:1024 sc1\nbuffer_load_dwordx4 a[24:27], v252, s[20:23], s66 offen offset:1024 sc1\n"
"buffer_load_dwordx4 a[28:31], v252, s[20:23], s67 offen offset:1024 sc1\ns_barrier\nR 160,188,49152\nR 162,188,53248\nR 164,184,0\nR 166,184,4096\n"
"R 168,189,49152\nR 170,189,53248\nR 172,185,0\nR 174,185,4096\nR 176,190,49152\nR 178,190,53248\n"
"R 180,186,0\nR 182,186,4096\nbuffer_load_dwordx4 a[32:35], v252, s[20:23], s64 offen offset:2048 sc1\nbuffer_load_dwordx4 a[36:39], v252, s[20:23], s65 offen offset:2048 sc1\nbuffer_load_dwordx4 a[40:43], v252, s[20:23], s66 offen offset:2048 sc1\nbuffer_load_dwordx4 a[44:47], v252, s[20:23], s67 offen offset:2048 sc1\n"
"buffer_load_dwordx4 a[48:51], v252, s[20:23], s64 offen offset:3072 sc1\nbuffer_load_dwordx4 a[52:55], v252, s[20:23], s65 offen offset:3072 sc1\nbuffer_load_dwordx4 a[56:59], v252, s[20:23], s66 offen offset:3072 sc1\nbuffer_load_dwordx4 a[60:63], v252, s[20:23], s67 offen offset:3072 sc1\ns_add_u32 s64, s70, 6295552\nbuffer_load_dwordx4 a[64:67], v252, s[20:23], s64 offen offset:0 sc1\n"
"s_add_u32 s65, s70, 6328320\nbuffer_load_dwordx4 a[68:71], v252, s[20:23], s65 offen offset:0 sc1\ns_add_u32 s66, s71, 6295552\nbuffer_load_dwordx4 a[72:75], v252, s[20:23], s66 offen offset:0 sc1\ns_add_u32 s67, s71, 6328320\nbuffer_load_dwordx4 a[76:79], v252, s[20:23], s67 offen offset:0 sc1\n"
"s_waitcnt vmcnt(19) lgkmcnt(10)\nM 0,160,0\nbuffer_load_dwordx4 a[80:83], v252, s[20:23], s64 offen offset:1024 sc1\ns_waitcnt vmcnt(16) lgkmcnt(8)\nM 0,164,16\nM 16,160,4\n"
"s_waitcnt vmcnt(15)\nM 16,164,20\nv_mfma_f32_16x16x32_bf16 v[32:35], v[160:163], a[8:11], 0\nbuffer_load_dwordx4 a[84:87], v252, s[20:23], s65 offen offset:1024 sc1\ns_waitcnt vmcnt(15)\nM 32,164,24\n"
"v_mfma_f32_16x16x32_bf16 v[48:51], v[160:163], a[12:15], 0\ns_waitcnt vmcnt(14)\nM 48,164,28\nG 4\nv_mfma_f32_16x16x32_bf16 v[52:55], v[168:171], a[12:15], 0\nR 160,191,49152\n"
"M 52,172,28\nbuffer_load_dwordx4 a[88:91], v252, s[20:23], s66 offen offset:1024 sc1\nv_mfma_f32_16x16x32_bf16 v[36:39], v[168:171], a[8:11], 0\nR 162,191,53248\nM 36,172,24\nM 20,168,4\n"
"R 164,187,0\nM 20,172,20\nbuffer_load_dwordx4 a[92:95], v252, s[20:23], s67 offen offset:1024 sc1\nM 4,168,0\nR 166,187,4096\nM 4,172,16\n"
"G 4\nM 8,176,0\nR 168,184,8192\nM 8,180,16\nbuffer_load_dwordx4 a[96:99], v252, s[20:23], s64 offen offset:2048 sc1\nM 24,176,4\n"
"R 170,184,12288\nM 24,180,20\nv_mfma_f32_16x16x32_bf16 v[40:43], v[176:179], a[8:11], 0\nR 172,184,16384\nM 40,180,24\nbuffer_load_dwordx4 a[100:103], v252, s[20:23], s65 offen offset:2048 sc1\n"
"v_mfma_f32_16x16x32_bf16 v[56:59], v[176:179], a[12:15], 0\nR 174,184,20480\nM 56,180,28\nG 4\nv_mfma_f32_16x16x32_bf16 v[60:63], v[160:163], a[12:15], 0\nR 176,185,8192\n"
"M 60,164,28\nbuffer_load_dwordx4 a[104:107], v252, s[20:23], s66 offen offset:2048 sc1\nv_mfma_f32_16x16x32_bf16 v[44:47], v[160:163], a[8:11], 0\nR 178,185,12288\nM 44,164,24\nM 28,160,4\n"
"R 180,185,16384\nM 28,164,20\nbuffer_load_dwordx4 a[108:111], v252, s[20:23], s67 offen offset:2048 sc1\nM 12,160,0\nR 182,185,20480\nM 12,164,16\n"
"G 4\ns_waitcnt vmcnt(19)\nM 0,168,32\nR 160,186,8192\ns_waitcnt vmcnt(15)\nM 0,172,48\n"
"buffer_load_dwordx4 a[112:115], v252, s[20:23], s64 offen offset:3072 sc1\nM 16,168,36\nR 162,186,12288\ns_waitcnt vmcnt(15)\nM 16,172,52\nM 32,168,40\n"
"R 164,186,16384\ns_waitcnt vmcnt(14)\nM 32,172,56\nbuffer_load_dwordx4 a[116:119], v252, s[20:23], s65 offen offset:3072 sc1\nM 48,168,44\nR 166,186,20480\n"
"s_waitcnt vmcnt(14)\nM 48,172,60\nG 4\nM 52,176,44\nR 168,187,8192\nM 52,180,60\n"
"buffer_load_dwordx4 a[120:123], v252, s[20:23], s66 offen offset:3072 sc1\nM 36,176,40\nR 170,187,12288\nM 36,180,56\nM 20,176,36\nR 172,187,16384\n"
"M 20,180,52\nbuffer_load_dwordx4 a[124:127], v252, s[20:23], s67 offen offset:3072 sc1\nM 4,176,32\nR 174,187,20480\nM 4,180,48\nG 4\n"
"M 8,160,32\nR 176,184,24576\nM 8,164,48\ns_add_u32 s64, s70, 6299648\nbuffer_load_dwordx4 a[128:131], v252, s[20:23], s64 offen offset:0 sc1\nM 24,160,36\n"
"R 178,184,28672\nM 24,164,52\nM 40,160,40\nR 180,184,32768\nM 40,164,56\ns_add_u32 s65, s70, 6332416\n"
"buffer_load_dwordx4 a[132:135], v252, s[20:23], s65 offen offset:0 sc1\nM 56,160,44\nR 182,184,36864\nM 56,164,60\nG 4\nM 60,168,44\n"
"R 160,185,24576\nM 60,172,60\ns_add_u32 s66, s71, 6299648\nbuffer_load_dwordx4 a[136:139], v252, s[20:23], s66 offen offset:0 sc1\nM 44,168,40\nR 162,185,28672\n"
"M 44,172,56\nM 28,168,36\nR 164,185,32768\nM 28,172,52\ns_add_u32 s67, s71, 6332416\nbuffer_load_dwordx4 a[140:143], v252, s[20:23], s67 offen offset:0 sc1\n"
"M 12,168,32\nR 166,185,36864\nM 12,172,48\ns_barrier\nG 4\ns_waitcnt vmcnt(19)\n"
"M 0,176,64\nR 168,186,24576\ns_waitcnt vmcnt(15)\nM 0,180,80\nbuffer_load_dwordx4 a[144:147], v252, s[20:23], s64 offen offset:1024 sc1\nM 16,176,68\n"
"R 170,186,28672\ns_waitcnt vmcnt(15)\nM 16,180,84\nM 32,176,72\nR 172,186,32768\ns_waitcnt vmcnt(14)\n"
"M 32,180,88\nbuffer_load_dwordx4 a[148:151], v252, s[20:23], s65 offen offset:1024 sc1\nM 48,176,76\nR 174,186,36864\ns_waitcnt vmcnt(14)\nM 48,180,92\n"
"G 4\nM 52,160,76\nR 176,187,24576\nM 52,164,92\nbuffer_load_dwordx4 a[152:155], v252, s[20:23], s66 offen offset:1024 sc1\nM 36,160,72\n"
"R 178,187,28672\nM 36,164,88\nM 20,160,68\nR 180,187,32768\nM 20,164,84\nbuffer_load_dwordx4 a[156:159], v252, s[20:23], s67 offen offset:1024 sc1\n"
"M 4,160,64\nR 182,187,36864\nM 4,164,80\nG 4\nM 8,168,64\nR 160,184,40960\n"
"M 8,172,80\nbuffer_load_dwordx4 a[160:163], v252, s[20:23], s64 offen offset:2048 sc1\nM 24,168,68\nR 162,184,45056\nM 24,172,84\nM 40,168,72\n"
"R 164,184,49152\nM 40,172,88\nbuffer_load_dwordx4 a[164:167], v252, s[20:23], s65 offen offset:2048 sc1\nM 56,168,76\nR 166,184,53248\nM 56,172,92\n"
"G 4\nM 60,176,76\nR 168,185,40960\nM 60,180,92\nbuffer_load_dwordx4 a[168:171], v252, s[20:23], s66 offen offset:2048 sc1\nM 44,176,72\n"
"R 170,185,45056\nM 44,180,88\nM 28,176,68\nR 172,185,49152\nM 28,180,84\nbuffer_load_dwordx4 a[172:175], v252, s[20:23], s67 offen offset:2048 sc1\n"
"M 12,176,64\nR 174,185,53248\nM 12,180,80\nG 4\ns_waitcnt vmcnt(19)\nM 0,160,96\n"
"R 176,186,40960\ns_waitcnt vmcnt(15)\nM 0,164,112\nbuffer_load_dwordx4 a[176:179], v252, s[20:23], s64 offen offset:3072 sc1\nM 16,160,100\nR 178,186,45056\n"
"s_waitcnt vmcnt(15)\nM 16,164,116\nM 32,160,104\nR 180,186,49152\ns_lshr_b32 s60, s45, 3\ns_lshl2_add_u32 s60, s60, s40\n"
"s_lshl_b32 s60, s60, 8\nv_lshl_add_u32 v224, v252, 3, s60\nv_add_u32 v224, Lqw1+24576-LqPC, v224\nv_min_u32 v224, LqE-LqPC-4, v224\ns_mov_b64 exec, 3\nglobal_load_dword v224, v224, s[98:99]\n"
"s_mov_b64 exec, -1\ns_waitcnt vmcnt(15)\nM 32,164,120\nbuffer_load_dwordx4 a[180:183], v252, s[20:23], s65 offen offset:3072 sc1\nM 48,160,108\nR 182,186,53248\n"
"s_waitcnt vmcnt(15)\nM 48,164,124\nG 4\nM 52,168,108\nR 160,187,40960\nM 52,172,124\n"
"buffer_load_dwordx4 a[184:187], v252, s[20:23], s66 offen offset:3072 sc1\nM 36,168,104\nR 162,187,45056\nM 36,172,120\nM 20,168,100\nR 164,187,49152\n"
"M 20,172,116\nbuffer_load_dwordx4 a[188:191], v252, s[20:23], s67 offen offset:3072 sc1\nM 4,168,96\nR 166,187,53248\nM 4,172,112\nG 4\n"
"M 8,176,96\nR 168,184,57344\nM 8,180,112\ns_add_u32 s64, s70, 6303744\nbuffer_load_dwordx4 a[192:195], v252, s[20:23], s64 offen offset:0 sc1\nM 24,176,100\n"
"R 170,184,61440\ns_add_u32 s63, s53, 983040\nM 24,180,116\ns_add_u32 m0, s50, 114688\nM 40,176,104\nR 172,188,0\n"
"M 40,180,120\ns_add_u32 s65, s70, 6336512\nbuffer_load_dwordx4 a[196:199], v252, s[20:23], s65 offen offset:0 sc1\nM 56,176,108\nR 174,188,4096\nM 56,180,124\n"
"buffer_load_dwordx4 v220, s[8:11], s63 offen lds\nG 4\nM 60,160,108\nR 176,185,57344\nM 60,164,124\ns_add_u32 s66, s71, 6303744\n"
"buffer_load_dwordx4 a[200:203], v252, s[20:23], s66 offen offset:0 sc1\nM 44,160,104\nR 178,185,61440\nM 44,164,120\nbuffer_load_dwordx4 v221, s[8:11], s63 offen offset:1024 lds\nM 28,160,100\n"
"R 180,189,0\nM 28,164,116\ns_add_u32 s67, s71, 6336512\nbuffer_load_dwordx4 a[204:207], v252, s[20:23], s67 offen offset:0 sc1\nM 12,160,96\nR 182,189,4096\n"
"M 12,164,112\ns_barrier\nG 4\ns_waitcnt vmcnt(22)\nM 0,168,128\nR 160,186,57344\n"
"s_waitcnt vmcnt(18)\nM 0,172,144\nbuffer_load_dwordx4 a[208:211], v252, s[20:23], s64 offen offset:1024 sc1\nM 16,168,132\nR 162,186,61440\ns_waitcnt vmcnt(18)\n"
"M 16,172,148\nM 32,168,136\nR 164,190,0\ns_waitcnt vmcnt(17)\nM 32,172,152\nbuffer_load_dwordx4 a[212:215], v252, s[20:23], s65 offen offset:1024 sc1\n"
"M 48,168,140\nR 166,190,4096\ns_waitcnt vmcnt(17)\nLpg23:\ns_cbranch_execz LretO1\nM 48,172,156\n"
"G 4\nM 52,176,140\nR 168,187,57344\nM 52,180,156\nbuffer_load_dwordx4 a[216:219], v252, s[20:23], s66 offen offset:1024 sc1\nM 36,176,136\n"
"R 170,187,61440\nM 36,180,152\nM 20,176,132\nR 172,191,0\nM 20,180,148\nbuffer_load_dwordx4 a[220:223], v252, s[20:23], s67 offen offset:1024 sc1\n"
"M 4,176,128\nR 174,191,4096\nM 4,180,144\nG 4\nM 8,160,128\nR 176,188,8192\n"
"M 8,164,144\nbuffer_load_dwordx4 a[224:227], v252, s[20:23], s64 offen offset:2048 sc1\nM 24,160,132\nR 178,188,12288\nM 24,164,148\nM 40,160,136\n"
"R 180,188,16384\nM 40,164,152\nbuffer_load_dwordx4 a[228:231], v252, s[20:23], s65 offen offset:2048 sc1\nM 56,160,140\nR 182,188,20480\nM 56,164,156\n"
"G 4\nM 60,168,140\nR 160,189,8192\nM 60,172,156\nbuffer_load_dwordx4 a[232:235], v252, s[20:23], s66 offen offset:2048 sc1\nM 44,168,136\n"
"R 162,189,12288\nM 44,172,152\nM 28,168,132\nR 164,189,16384\nM 28,172,148\nbuffer_load_dwordx4 a[236:239], v252, s[20:23], s67 offen offset:2048 sc1\n"
"M 12,168,128\nR 166,189,20480\nM 12,172,144\nG 4\ns_waitcnt vmcnt(22)\nM 0,176,160\n"
"R 168,190,8192\ns_waitcnt vmcnt(18)\nM 0,180,176\nbuffer_load_dwordx4 a[240:243], v252, s[20:23], s64 offen offset:3072 sc1\nM 16,176,164\nR 170,190,12288\n"
"s_waitcnt vmcnt(17)\nM 16,180,180\nM 32,176,168\nR 172,190,16384\ns_waitcnt vmcnt(16)\nM 32,180,184\n"
"buffer_load_dwordx4 a[244:247], v252, s[20:23], s65 offen offset:3072 sc1\nM 48,176,172\nR 174,190,20480\ns_waitcnt vmcnt(16)\nM 48,180,188\nG 4\n"
"M 52,160,172\nR 176,191,8192\nM 52,164,188\nbuffer_load_dwordx4 a[248:251], v252, s[20:23], s66 offen offset:3072 sc1\nM 36,160,168\nR 178,191,12288\n"
"s_waitcnt vmcnt(12)\ns_barrier\nM 36,164,184\nM 20,160,164\nR 180,191,16384\nM 20,164,180\n"
"buffer_load_dwordx4 a[252:255], v252, s[20:23], s67 offen offset:3072 sc1\nM 4,160,160\nR 182,191,20480\nM 4,164,176\nG 4\nM 8,168,160\n"
"R 160,188,24576\nM 8,172,176\nM 24,168,164\nR 162,188,28672\nM 24,172,180\nM 40,168,168\n"
"R 164,188,32768\nM 40,172,184\nM 56,168,172\nR 166,188,36864\nM 56,172,188\nG 4\n"
"M 60,176,172\nR 168,189,24576\nM 60,180,188\ns_movk_i32 s63, 7168\nv_mad_u32_u24 v220, v228, s63, v229\nv_add_u32 v221, 3668992, v220\n"
"s_add_u32 m0, s50, 0\ns_add_u32 s63, s54, 0\nbuffer_load_dwordx4 v220, s[12:15], s63 offen lds\nM 44,176,168\nR 170,189,28672\nM 44,180,184\n"
"M 28,176,164\nR 172,189,32768\nM 28,180,180\nbuffer_load_dwordx4 v221, s[12:15], s63 offen offset:1024 lds\nM 12,176,160\nR 174,189,36864\n"
"s_add_u32 m0, s50, 8192\nM 12,180,176\nG 4\nM 32,160,200\nR 176,190,24576\ns_add_u32 s63, s54, 229376\n"
"s_waitcnt vmcnt(11)\nM 32,164,216\nR 178,190,28672\nM 48,160,204\nR 180,190,32768\ns_waitcnt vmcnt(10)\n"
"M 48,164,220\nR 182,190,36864\nG 4\nM 52,168,204\nR 160,191,24576\nbuffer_load_dwordx4 v220, s[12:15], s63 offen lds\n"
"M 52,172,220\nR 162,191,28672\nM 36,168,200\nR 164,191,32768\nM 36,172,216\nR 166,191,36864\n"
"G 4\nM 40,176,200\nR 168,188,40960\nbuffer_load_dwordx4 v221, s[12:15], s63 offen offset:1024 lds\nM 40,180,216\nR 170,188,45056\n"
"s_add_u32 m0, s50, 16384\nM 56,176,204\nR 172,188,49152\ns_add_u32 s63, s54, 128\nM 56,180,220\nR 174,188,53248\n"
"G 4\nM 60,160,204\nR 176,189,40960\nbuffer_load_dwordx4 v220, s[12:15], s63 offen lds\nM 60,164,220\nR 178,189,45056\n"
"M 44,160,200\nR 180,189,49152\nM 44,164,216\nR 182,189,53248\nG 4\ns_waitcnt vmcnt(10)\n"
"M 32,168,232\nR 160,190,40960\nbuffer_load_dwordx4 v221, s[12:15], s63 offen offset:1024 lds\ns_waitcnt vmcnt(7)\nM 32,172,248\nR 162,190,45056\n"
"s_add_u32 m0, s50, 24576\nM 48,168,236\nR 164,190,49152\ns_add_u32 s63, s54, 229504\ns_waitcnt vmcnt(6)\nM 48,172,252\n"
"R 166,190,53248\nG 4\nM 52,176,236\nR 168,191,40960\nbuffer_load_dwordx4 v220, s[12:15], s63 offen lds\nM 52,180,252\n"
"R 170,191,45056\nM 36,176,232\nR 172,191,49152\nM 36,180,248\nR 174,191,53248\nG 4\n"
"M 40,160,232\nR 176,188,24576\nbuffer_load_dwordx4 v221, s[12:15], s63 offen offset:1024 lds\nM 40,164,248\nR 178,188,28672\nM 56,160,236\n"
"R 180,188,32768\nM 56,164,252\nR 182,188,36864\nG 4\nM 60,168,236\nR 160,189,24576\n"
"M 60,172,252\nR 162,189,28672\nM 44,168,232\nR 164,189,32768\nM 44,172,248\nR 166,189,36864\n"
"G 4\nM 0,176,192\nR 168,190,24576\nM 0,180,208\nR 170,190,28672\ns_mov_b32 m0, s51\n"
"M 16,176,196\nR 172,190,32768\nv_cvt_pk_bf16_f32 v32, v32, v33\nv_cvt_pk_bf16_f32 v33, v34, v35\nM 16,180,212\nR 174,190,36864\n"
"G 4\nv_cvt_pk_bf16_f32 v34, v36, v37\nM 20,160,196\nR 176,191,24576\nv_cvt_pk_bf16_f32 v35, v38, v39\nv_cvt_pk_bf16_f32 v36, v40, v41\n"
"M 20,164,212\nR 178,191,28672\nv_cvt_pk_bf16_f32 v37, v42, v43\nv_cvt_pk_bf16_f32 v38, v44, v45\nM 4,160,192\nR 180,191,32768\n"
"v_cvt_pk_bf16_f32 v39, v46, v47\nv_cvt_pk_bf16_f32 v40, v48, v49\nM 4,164,208\nR 182,191,36864\nG 4\nv_cvt_pk_bf16_f32 v41, v50, v51\n"
"M 8,168,192\nR 160,188,40960\nv_cvt_pk_bf16_f32 v42, v52, v53\nv_cvt_pk_bf16_f32 v43, v54, v55\nM 8,172,208\nR 162,188,45056\n"
"v_cvt_pk_bf16_f32 v44, v56, v57\nv_cvt_pk_bf16_f32 v45, v58, v59\nM 24,168,196\nR 164,188,49152\nv_cvt_pk_bf16_f32 v46, v60, v61\nv_cvt_pk_bf16_f32 v47, v62, v63\n"
"M 24,172,212\nR 166,188,53248\nds_write_addtid_b32 v32 offset:0\nG 5\nM 28,176,196\nR 168,189,40960\n"
"ds_write_addtid_b32 v33 offset:256\nM 28,180,212\nR 170,189,45056\nds_write_addtid_b32 v34 offset:512\nM 12,176,192\nR 172,189,49152\n"
"ds_write_addtid_b32 v35 offset:768\nds_write_addtid_b32 v36 offset:1024\nds_write_addtid_b32 v37 offset:1280\nds_write_addtid_b32 v38 offset:1536\nds_write_addtid_b32 v39 offset:1792\nds_write_addtid_b32 v40 offset:2048\n"
"ds_write_addtid_b32 v41 offset:2304\nds_write_addtid_b32 v42 offset:2560\nds_write_addtid_b32 v43 offset:2816\nds_write_addtid_b32 v44 offset:3072\nds_write_addtid_b32 v45 offset:3328\nds_write_addtid_b32 v46 offset:3584\n"
"ds_write_addtid_b32 v47 offset:3840\nM 12,180,208\nR 174,189,53248\nG 15\nM 0,160,224\nR 176,190,40960\n"
"s_add_u32 m0, s50, 32768\nM 0,164,240\nR 178,190,45056\nG 3\ns_barrier\ns_add_u32 s63, s54, 458752\n"
"M 16,160,228\nR 180,190,49152\nbuffer_load_dwordx4 v220, s[12:15], s63 offen lds\nds_read2st64_b32 v[32:33], v225 offset0:0 offset1:1\nM 16,164,244\nR 182,190,53248\n"
"ds_read2st64_b32 v[34:35], v225 offset0:2 offset1:3\nG 6\nM 20,168,228\nR 160,191,40960\nds_read2st64_b32 v[36:37], v225 offset0:4 offset1:5\nM 20,172,244\n"
"R 162,191,45056\nds_read2st64_b32 v[38:39], v225 offset0:6 offset1:7\nM 4,168,224\nR 164,191,49152\nbuffer_load_dwordx4 v221, s[12:15], s63 offen offset:1024 lds\nds_read2st64_b32 v[40:41], v225 offset0:8 offset1:9\n"
"M 4,172,240\nR 166,191,53248\ns_add_u32 m0, s50, 40960\ns_add_u32 s63, s54, 688128\nds_read2st64_b32 v[42:43], v225 offset0:10 offset1:11\nG 9\n"
"M 8,176,224\nbuffer_load_dwordx4 v220, s[12:15], s63 offen lds\nds_read2st64_b32 v[44:45], v225 offset0:12 offset1:13\nM 8,180,240\nds_read2st64_b32 v[46:47], v225 offset0:14 offset1:15\nM 24,176,228\n"
"M 24,180,244\nG 3\nM 28,160,228\nbuffer_load_dwordx4 v221, s[12:15], s63 offen offset:1024 lds\nM 28,164,244\nM 12,160,224\n"
"M 12,164,240\nG 0\ns_barrier\nv_mfma_f32_16x16x32_bf16 v[0:3], v[208:211], v[32:35], v[0:3]\nv_mfma_f32_16x16x32_bf16 v[4:7], v[210:213], v[32:35], v[4:7]\nv_mfma_f32_16x16x32_bf16 v[8:11], v[208:211], v[36:39], v[8:11]\n"
"v_mfma_f32_16x16x32_bf16 v[12:15], v[210:213], v[36:39], v[12:15]\nv_mfma_f32_16x16x32_bf16 v[16:19], v[208:211], v[40:43], v[16:19]\nv_mfma_f32_16x16x32_bf16 v[20:23], v[210:213], v[40:43], v[20:23]\nv_mfma_f32_16x16x32_bf16 v[24:27], v[208:211], v[44:47], v[24:27]\nv_mfma_f32_16x16x32_bf16 v[28:31], v[210:213], v[44:47], v[28:31]\ns_lshl_b32 s84, s75, 11\n"
"s_add_u32 s84, s84, s49\ns_add_u32 s84, s84, 14680064\ns_add_u32 s85, s84, 32768\nv_mov_b32 v238, s46\nv_mov_b32 v239, s47\nv_cvt_pk_bf16_f32 v192, v0, v1\n"
"v_cvt_pk_bf16_f32 v193, v2, v3\nv_cvt_pk_bf16_f32 v194, v4, v5\nv_cvt_pk_bf16_f32 v195, v6, v7\nbuffer_store_dwordx4 v[192:195], v252, s[20:23], s84 offen offset:0 sc1\nv_cvt_pk_bf16_f32 v196, v8, v9\nv_cvt_pk_bf16_f32 v197, v10, v11\n"
"v_cvt_pk_bf16_f32 v198, v12, v13\nv_cvt_pk_bf16_f32 v199, v14, v15\nbuffer_store_dwordx4 v[196:199], v252, s[20:23], s84 offen offset:1024 sc1\nv_cvt_pk_bf16_f32 v200, v16, v17\nv_cvt_pk_bf16_f32 v201, v18, v19\nv_cvt_pk_bf16_f32 v202, v20, v21\n"
"v_cvt_pk_bf16_f32 v203, v22, v23\nbuffer_store_dwordx4 v[200:203], v252, s[20:23], s85 offen offset:0 sc1\nv_cvt_pk_bf16_f32 v204, v24, v25\nv_cvt_pk_bf16_f32 v205, v26, v27\nv_cvt_pk_bf16_f32 v206, v28, v29\nv_cvt_pk_bf16_f32 v207, v30, v31\n"
"buffer_store_dwordx4 v[204:207], v252, s[20:23], s85 offen offset:1024 sc1\ns_mov_b64 exec, 0\ns_cmp_eq_u32 s40, 1\ns_cbranch_scc1 Lpg25\ns_cmp_eq_u32 s40, 2\ns_cbranch_scc1 Lpg26\n"
"s_cmp_eq_u32 s40, 3\ns_cbranch_scc1 Lpg27\ns_branch Lpg24\nLretO1n:\ns_mov_b64 exec, -1\ns_add_u32 s61, s79, 88637440\n"
"s_lshl_b32 s62, s75, 3\ns_add_u32 s61, s61, s62\ns_waitcnt vmcnt(0)\ns_mov_b64 exec, 1\nbuffer_atomic_swap_x2 v[238:239], off, s[20:23], s61 sc1\ns_mov_b64 exec, -1\n"
"Lqw1:\nv_bfe_u32 v250, v252, 7, 1\nv_mul_u32_u24 v250, 2176, v250\nv_bfe_u32 v251, v252, 4, 3\nv_lshl_add_u32 v250, v251, 3, v250\ns_lshl_b32 s60, s90, 6\n"
"v_add_u32 v250, s60, v250\ns_sleep 3\nv_mov_b32 v248, 0\nv_mov_b32 v249, 0\ns_add_u32 s61, s80, 88637440\ns_mov_b64 exec, 0xffff\n"
"buffer_atomic_or_x2 v[248:249], v250, s[20:23], s61 offen sc0 sc1\ns_mov_b64 exec, -1\ns_add_u32 m0, s50, 49152\ns_add_u32 s63, s54, 458880\nbuffer_load_dwordx4 v220, s[12:15], s63 offen lds\nbuffer_load_dwordx4 v221, s[12:15], s63 offen offset:1024 lds\n"
"s_add_u32 m0, s50, 57344\ns_add_u32 s63, s54, 688256\nbuffer_load_dwordx4 v220, s[12:15], s63 offen lds\nbuffer_load_dwordx4 v221, s[12:15], s63 offen offset:1024 lds\ns_add_u32 m0, s50, 65536\ns_add_u32 s63, s54, 917504\n"
"buffer_load_dwordx4 v220, s[12:15], s63 offen lds\nbuffer_load_dwordx4 v221, s[12:15], s63 offen offset:1024 lds\ns_add_u32 m0, s50, 73728\ns_add_u32 s63, s54, 1146880\nbuffer_load_dwordx4 v220, s[12:15], s63 offen lds\nbuffer_load_dwordx4 v221, s[12:15], s63 offen offset:1024 lds\n"
"s_add_u32 m0, s50, 81920\ns_add_u32 s63, s54, 917632\nbuffer_load_dwordx4 v220, s[12:15], s63 offen lds\nbuffer_load_dwordx4 v221, s[12:15], s63 offen offset:1024 lds\ns_waitcnt vmcnt(10)\nv_cmp_eq_u64 s[86:87], s[46:47], v[248:249]\n"
"s_nop 1\ns_and_b32 s88, s86, 0xffff\ns_cmp_eq_u32 s88, 0xffff\ns_cbranch_scc1 Lq768\nv_mov_b32 v246, 0\nv_mov_b32 v247, 0\n"
"s_add_u32 s61, s80, 88637440\ns_mov_b64 exec, 0xffff\nbuffer_atomic_or_x2 v[246:247], v250, s[20:23], s61 offen sc0 sc1\ns_mov_b64 exec, -1\nLq768:\ns_add_u32 m0, s50, 90112\n"
"s_add_u32 s63, s54, 1147008\nbuffer_load_dwordx4 v220, s[12:15], s63 offen lds\nbuffer_load_dwordx4 v221, s[12:15], s63 offen offset:1024 lds\ns_mov_b64 exec, 0\ns_cmp_eq_u32 s40, 1\ns_cbranch_scc1 Lpg33\n"
"s_cmp_eq_u32 s40, 2\ns_cbranch_scc1 LretW1z\ns_cmp_eq_u32 s40, 3\ns_cbranch_scc1 LretW1z\ns_branch Lpg32\nLretW1z:\n"
"s_mov_b64 exec, -1\nv_mov_b32 v230, 0\nv_mov_b32 v231, 0\nv_mov_b32 v232, 0\nv_mov_b32 v233, 0\nv_dot2c_f32_bf16 v230, s76, v192\n"
"v_dot2c_f32_bf16 v231, v192, v192\nv_dot2c_f32_bf16 v232, s76, v200\nv_dot2c_f32_bf16 v233, v200, v200\nv_dot2c_f32_bf16 v230, s76, v193\nv_dot2c_f32_bf16 v231, v193, v193\nv_dot2c_f32_bf16 v232, s76, v201\n"
"v_dot2c_f32_bf16 v233, v201, v201\nv_dot2c_f32_bf16 v230, s76, v194\nv_dot2c_f32_bf16 v231, v194, v194\nv_dot2c_f32_bf16 v232, s76, v202\nv_dot2c_f32_bf16 v233, v202, v202\nv_dot2c_f32_bf16 v230, s76, v195\n"
"v_dot2c_f32_bf16 v231, v195, v195\nv_dot2c_f32_bf16 v232, s76, v203\nv_dot2c_f32_bf16 v233, v203, v203\nv_dot2c_f32_bf16 v230, s76, v196\nv_dot2c_f32_bf16 v231, v196, v196\nv_dot2c_f32_bf16 v232, s76, v204\n"
"v_dot2c_f32_bf16 v233, v204, v204\nv_dot2c_f32_bf16 v230, s76, v197\nv_dot2c_f32_bf16 v231, v197, v197\nv_dot2c_f32_bf16 v232, s76, v205\nv_dot2c_f32_bf16 v233, v205, v205\nv_dot2c_f32_bf16 v230, s76, v198\n"
"v_dot2c_f32_bf16 v231, v198, v198\nv_dot2c_f32_bf16 v232, s76, v206\nv_dot2c_f32_bf16 v233, v206, v206\nv_dot2c_f32_bf16 v230, s76, v199\nv_dot2c_f32_bf16 v231, v199, v199\nv_dot2c_f32_bf16 v232, s76, v207\n"
"v_dot2c_f32_bf16 v233, v207, v207\nv_lshrrev_b32 v234, 5, v252\nv_and_b32 v234, 24, v234\nv_bfe_u32 v235, s46, v234, 8\nv_lshrrev_b32 v234, 1, v234\nv_and_b32 v236, 0xf0, v252\n"
"v_add_u32 v234, v234, v236\nv_permlane32_swap_b32 v230, v231\nv_permlane32_swap_b32 v232, v233\nv_add_f32 v230, v230, v231\nv_add_f32 v232, v232, v233\ns_nop 1\n"
"v_permlane16_swap_b32 v230, v232\nv_add_f32 v230, v230, v232\ns_movk_i32 s62, 0xff\nv_bfi_b32 v230, s62, v235, v230\ns_lshr_b32 s61, s49, 4\ns_lshl1_add_u32 s61, s53, s61\n"
"s_add_u32 s61, s61, 83361792\nbuffer_store_dword v230, v234, s[20:23], s61 offen sc1\ns_cmp_eq_u32 s88, 0xffff\ns_cbranch_scc1 Lq769\ns_waitcnt vmcnt(3)\nv_mov_b32 v248, v246\n"
"v_mov_b32 v249, v247\nv_cmp_eq_u64 s[86:87], s[46:47], v[248:249]\ns_nop 1\ns_and_b32 s62, s86, 0xffff\ns_cmp_eq_u32 s62, 0xffff\ns_cbranch_scc1 Lq760\n"
"Lq759:\nv_mov_b32 v248, 0\nv_mov_b32 v249, 0\ns_add_u32 s61, s80, 88637440\ns_mov_b64 exec, 0xffff\nbuffer_atomic_or_x2 v[248:249], v250, s[20:23], s61 offen sc0 sc1\n"
"s_mov_b64 exec, -1\ns_waitcnt vmcnt(0)\nv_cmp_eq_u64 s[86:87], s[46:47], v[248:249]\ns_nop 1\ns_and_b32 s62, s86, 0xffff\ns_cmp_eq_u32 s62, 0xffff\n"
"s_cbranch_scc1 Lq760\ns_sub_u32 s48, s48, 1\ns_cmp_gt_i32 s48, 0\ns_cbranch_scc1 Lq759\nLq760:\nLq769:\n"
"s_add_u32 s64, s70, 14680064\nbuffer_load_dwordx4 a[0:3], v252, s[20:23], s64 offen offset:0 sc1\ns_add_u32 s65, s70, 14712832\nbuffer_load_dwordx4 a[4:7], v252, s[20:23], s65 offen offset:0 sc1\ns_add_u32 s66, s71, 14680064\nbuffer_load_dwordx4 a[8:11], v252, s[20:23], s66 offen offset:0 sc1\n"
"s_add_u32 s67, s71, 14712832\nbuffer_load_dwordx4 a[12:15], v252, s[20:23], s67 offen offset:0 sc1\nbuffer_load_dwordx4 a[16:19], v252, s[20:23], s64 offen offset:1024 sc1\nbuffer_load_dwordx4 a[20:23], v252, s[20:23], s65 offen offset:1024 sc1\nbuffer_load_dwordx4 a[24:27], v252, s[20:23], s66 offen offset:1024 sc1\nbuffer_load_dwordx4 a[28:31], v252, s[20:23], s67 offen offset:1024 sc1\n"
"s_barrier\nR 160,184,0\nR 162,184,4096\nR 164,184,8192\nR 166,184,12288\nR 168,185,0\n"
"R 170,185,4096\nR 172,185,8192\nR 174,185,12288\nR 176,186,0\nR 178,186,4096\nR 180,186,8192\n"
"R 182,186,12288\nbuffer_load_dwordx4 a[32:35], v252, s[20:23], s64 offen offset:2048 sc1\nbuffer_load_dwordx4 a[36:39], v252, s[20:23], s65 offen offset:2048 sc1\nbuffer_load_dwordx4 a[40:43], v252, s[20:23], s66 offen offset:2048 sc1\nbuffer_load_dwordx4 a[44:47], v252, s[20:23], s67 offen offset:2048 sc1\nbuffer_load_dwordx4 a[48:51], v252, s[20:23], s64 offen offset:3072 sc1\n"
"buffer_load_dwordx4 a[52:55], v252, s[20:23], s65 offen offset:3072 sc1\nbuffer_load_dwordx4 a[56:59], v252, s[20:23], s66 offen offset:3072 sc1\nbuffer_load_dwordx4 a[60:63], v252, s[20:23], s67 offen offset:3072 sc1\ns_waitcnt vmcnt(15) lgkmcnt(10)\nv_mfma_f32_16x16x32_bf16 v[32:35], v[160:163], a[0:3], 0\ns_add_u32 s64, s70, 14684160\n"
"buffer_load_dwordx4 a[64:67], v252, s[20:23], s64 offen offset:0 sc1\ns_waitcnt vmcnt(12) lgkmcnt(8)\nM 32,164,16\nv_mfma_f32_16x16x32_bf16 v[48:51], v[160:163], a[4:7], 0\ns_waitcnt vmcnt(11)\nM 48,164,20\n"
"v_mfma_f32_16x16x32_bf16 v[64:67], v[160:163], a[8:11], 0\ns_waitcnt vmcnt(10)\nM 64,164,24\nv_mfma_f32_16x16x32_bf16 v[80:83], v[160:163], a[12:15], 0\ns_add_u32 s65, s70, 14716928\nbuffer_load_dwordx4 a[68:71], v252, s[20:23], s65 offen offset:0 sc1\n"
"s_waitcnt vmcnt(10)\nM 80,164,28\nG 4\nv_mfma_f32_16x16x32_bf16 v[84:87], v[168:171], a[12:15], 0\nR 160,187,0\nM 84,172,28\n"
"v_mfma_f32_16x16x32_bf16 v[68:71], v[168:171], a[8:11], 0\nR 162,187,4096\nM 68,172,24\nv_mfma_f32_16x16x32_bf16 v[52:55], v[168:171], a[4:7], 0\nR 164,187,8192\nM 52,172,20\n"
"s_add_u32 s66, s71, 14684160\nbuffer_load_dwordx4 a[72:75], v252, s[20:23], s66 offen offset:0 sc1\nv_mfma_f32_16x16x32_bf16 v[36:39], v[168:171], a[0:3], 0\nR 166,187,12288\nM 36,172,16\nG 4\n"
"v_mfma_f32_16x16x32_bf16 v[40:43], v[176:179], a[0:3], 0\nR 168,184,16384\nM 40,180,16\nv_mfma_f32_16x16x32_bf16 v[56:59], v[176:179], a[4:7], 0\nR 170,184,20480\nM 56,180,20\n"
"s_add_u32 s67, s71, 14716928\nbuffer_load_dwordx4 a[76:79], v252, s[20:23], s67 offen offset:0 sc1\nv_mfma_f32_16x16x32_bf16 v[72:75], v[176:179], a[8:11], 0\nR 172,184,24576\nM 72,180,24\nv_mfma_f32_16x16x32_bf16 v[88:91], v[176:179], a[12:15], 0\n"
"R 174,184,28672\nLpg24:\ns_cbranch_execz Lpg28\nM 88,180,28\nG 4\nv_mfma_f32_16x16x32_bf16 v[92:95], v[160:163], a[12:15], 0\n"
"R 176,185,16384\ns_add_u32 s63, s54, 1376256\nM 92,164,28\nbuffer_load_dwordx4 a[80:83], v252, s[20:23], s64 offen offset:1024 sc1\nv_mfma_f32_16x16x32_bf16 v[76:79], v[160:163], a[8:11], 0\nR 178,185,20480\n"
"M 76,164,24\ns_add_u32 m0, s50, 98304\nv_mfma_f32_16x16x32_bf16 v[60:63], v[160:163], a[4:7], 0\nR 180,185,24576\nM 60,164,20\nbuffer_load_dwordx4 v220, s[12:15], s63 offen lds\n"
"v_mfma_f32_16x16x32_bf16 v[44:47], v[160:163], a[0:3], 0\nR 182,185,28672\nM 44,164,16\nG 4\nv_mfma_f32_16x16x32_bf16 v[96:99], v[168:171], a[0:3], 0\nR 160,186,16384\n"
"M 96,172,16\nbuffer_load_dwordx4 a[84:87], v252, s[20:23], s65 offen offset:1024 sc1\nv_mfma_f32_16x16x32_bf16 v[112:115], v[168:171], a[4:7], 0\nR 162,186,20480\nM 112,172,20\nv_mfma_f32_16x16x32_bf16 v[128:131], v[168:171], a[8:11], 0\n"
"R 164,186,24576\nM 128,172,24\nbuffer_load_dwordx4 a[88:91], v252, s[20:23], s66 offen offset:1024 sc1\nv_mfma_f32_16x16x32_bf16 v[144:147], v[168:171], a[12:15], 0\nR 166,186,28672\nM 144,172,28\n"
"s_barrier\nG 4\nv_mfma_f32_16x16x32_bf16 v[148:151], v[176:179], a[12:15], 0\nR 168,187,16384\nM 148,180,28\nv_mfma_f32_16x16x32_bf16 v[132:135], v[176:179], a[8:11], 0\n"
"R 170,187,20480\nM 132,180,24\nbuffer_load_dwordx4 a[92:95], v252, s[20:23], s67 offen offset:1024 sc1\nv_mfma_f32_16x16x32_bf16 v[116:119], v[176:179], a[4:7], 0\nR 172,187,24576\nM 116,180,20\n"
"v_mfma_f32_16x16x32_bf16 v[100:103], v[176:179], a[0:3], 0\nR 174,187,28672\nM 100,180,16\nG 4\nv_mfma_f32_16x16x32_bf16 v[104:107], v[160:163], a[0:3], 0\nR 176,184,32768\n"
"M 104,164,16\nbuffer_load_dwordx4 a[96:99], v252, s[20:23], s64 offen offset:2048 sc1\nv_mfma_f32_16x16x32_bf16 v[120:123], v[160:163], a[4:7], 0\nR 178,184,36864\nM 120,164,20\nv_mfma_f32_16x16x32_bf16 v[136:139], v[160:163], a[8:11], 0\n"
"R 180,184,40960\nM 136,164,24\nbuffer_load_dwordx4 v221, s[12:15], s63 offen offset:1024 lds\nv_mfma_f32_16x16x32_bf16 v[152:155], v[160:163], a[12:15], 0\nR 182,184,45056\nM 152,164,28\n"
"G 4\nv_mfma_f32_16x16x32_bf16 v[156:159], v[168:171], a[12:15], 0\nR 160,185,32768\nM 156,172,28\nbuffer_load_dwordx4 a[100:103], v252, s[20:23], s65 offen offset:2048 sc1\nv_mfma_f32_16x16x32_bf16 v[140:143], v[168:171], a[8:11], 0\n"
"R 162,185,36864\nM 140,172,24\ns_add_u32 m0, s50, 106496\nv_mfma_f32_16x16x32_bf16 v[124:127], v[168:171], a[4:7], 0\nR 164,185,40960\ns_add_u32 s63, s54, 1605632\n"
"M 124,172,20\nbuffer_load_dwordx4 a[104:107], v252, s[20:23], s66 offen offset:2048 sc1\nv_mfma_f32_16x16x32_bf16 v[108:111], v[168:171], a[0:3], 0\nR 166,185,45056\nM 108,172,16\nG 4\n"
"s_waitcnt vmcnt(20)\nM 32,176,32\nR 168,186,32768\ns_waitcnt vmcnt(16)\nM 32,180,48\nbuffer_load_dwordx4 v220, s[12:15], s63 offen lds\n"
"M 48,176,36\nR 170,186,36864\ns_waitcnt vmcnt(16)\nM 48,180,52\nbuffer_load_dwordx4 a[108:111], v252, s[20:23], s67 offen offset:2048 sc1\nM 64,176,40\n"
"R 172,186,40960\ns_waitcnt vmcnt(16)\nM 64,180,56\nM 80,176,44\nR 174,186,45056\ns_waitcnt vmcnt(15)\n"
"M 80,180,60\ns_barrier\nG 4\nM 84,160,44\nR 176,187,32768\nM 84,164,60\n"
"buffer_load_dwordx4 a[112:115], v252, s[20:23], s64 offen offset:3072 sc1\nM 68,160,40\nR 178,187,36864\nM 68,164,56\nM 52,160,36\nR 180,187,40960\n"
"M 52,164,52\nM 36,160,32\nR 182,187,45056\nM 36,164,48\nG 4\nM 40,168,32\n"
"R 160,184,49152\nM 40,172,48\nbuffer_load_dwordx4 a[116:119], v252, s[20:23], s65 offen offset:3072 sc1\nM 56,168,36\nR 162,184,53248\nM 56,172,52\n"
"M 72,168,40\nR 164,184,57344\nM 72,172,56\nbuffer_load_dwordx4 a[120:123], v252, s[20:23], s66 offen offset:3072 sc1\nM 88,168,44\nR 166,184,61440\n"
"M 88,172,60\nG 4\nM 92,176,44\nR 168,185,49152\nM 92,180,60\nbuffer_load_dwordx4 v221, s[12:15], s63 offen offset:1024 lds\n"
"M 76,176,40\nR 170,185,53248\nM 76,180,56\nbuffer_load_dwordx4 a[124:127], v252, s[20:23], s67 offen offset:3072 sc1\nM 60,176,36\nR 172,185,57344\n"
"M 60,180,52\nM 44,176,32\nR 174,185,61440\nM 44,180,48\nG 4\nM 96,160,32\n"
"R 176,186,49152\nM 96,164,48\ns_add_u32 s64, s70, 14688256\nbuffer_load_dwordx4 a[128:131], v252, s[20:23], s64 offen offset:0 sc1\nM 112,160,36\nR 178,186,53248\n"
"M 112,164,52\ns_lshr_b32 s84, s49, 4\nM 128,160,40\nR 180,186,57344\nM 128,164,56\ns_add_u32 s84, s84, 83361792\n"
"M 144,160,44\nR 182,186,61440\nM 144,164,60\ns_barrier\nG 4\nM 148,168,44\n"
"R 160,187,49152\nM 148,172,60\ns_add_u32 s65, s70, 14721024\nbuffer_load_dwordx4 a[132:135], v252, s[20:23], s65 offen offset:0 sc1\nM 132,168,40\nR 162,187,53248\n"
"M 132,172,56\nbuffer_load_dwordx4 v[0:3], v252, s[20:23], s84 offen offset:0 sc1\nM 116,168,36\nR 164,187,57344\nM 116,172,52\ns_add_u32 s66, s71, 14688256\n"
"buffer_load_dwordx4 a[136:139], v252, s[20:23], s66 offen offset:0 sc1\nM 100,168,32\nR 166,187,61440\nM 100,172,48\nG 4\nM 104,176,32\n"
"R 168,188,0\nM 104,180,48\nbuffer_load_dwordx4 v[4:7], v252, s[20:23], s84 offen offset:1024 sc1\nM 120,176,36\nR 170,188,4096\nM 120,180,52\n"
"s_add_u32 s67, s71, 14721024\nbuffer_load_dwordx4 a[140:143], v252, s[20:23], s67 offen offset:0 sc1\nM 136,176,40\nR 172,188,8192\nM 136,180,56\nbuffer_load_dwordx4 v[8:11], v252, s[20:23], s84 offen offset:2048 sc1\n"
"M 152,176,44\nR 174,188,12288\nM 152,180,60\nbuffer_load_dwordx4 v[12:15], v252, s[20:23], s84 offen offset:3072 sc1\nG 4\nM 156,160,44\n"
"R 176,189,0\nM 156,164,60\nbuffer_load_dwordx4 a[144:147], v252, s[20:23], s64 offen offset:1024 sc1\nM 140,160,40\nR 178,189,4096\nM 140,164,56\n"
"M 124,160,36\nR 180,189,8192\nM 124,164,52\nM 108,160,32\nR 182,189,12288\nM 108,164,48\n"
"G 4\ns_waitcnt vmcnt(28)\nM 32,168,64\nR 160,190,0\ns_waitcnt vmcnt(24)\nM 32,172,80\n"
"buffer_load_dwordx4 a[148:151], v252, s[20:23], s65 offen offset:1024 sc1\nM 48,168,68\nR 162,190,4096\ns_waitcnt vmcnt(23)\nM 48,172,84\ns_add_u32 m0, s50, 114688\n"
"M 64,168,72\nR 164,190,8192\ns_add_u32 s63, s54, 1376384\ns_waitcnt vmcnt(22)\nM 64,172,88\nbuffer_load_dwordx4 a[152:155], v252, s[20:23], s66 offen offset:1024 sc1\n"
"M 80,168,76\nR 166,190,12288\ns_waitcnt vmcnt(22)\nM 80,172,92\ns_barrier\nG 4\n"
"M 84,176,76\nR 168,191,0\nM 84,180,92\nbuffer_load_dwordx4 v220, s[12:15], s63 offen lds\nM 68,176,72\nR 170,191,4096\n"
"M 68,180,88\nbuffer_load_dwordx4 a[156:159], v252, s[20:23], s67 offen offset:1024 sc1\nM 52,176,68\nR 172,191,8192\nM 52,180,84\nbuffer_load_dwordx4 v221, s[12:15], s63 offen offset:1024 lds\n"
"M 36,176,64\nR 174,191,12288\ns_add_u32 m0, s50, 0\nM 36,180,80\nG 4\nM 40,160,64\n"
"R 176,188,16384\ns_add_u32 s63, s54, 1605760\nM 40,164,80\nbuffer_load_dwordx4 a[160:163], v252, s[20:23], s64 offen offset:2048 sc1\nM 56,160,68\nR 178,188,20480\n"
"M 56,164,84\nbuffer_load_dwordx4 v220, s[12:15], s63 offen lds\nM 72,160,72\nR 180,188,24576\nM 72,164,88\nbuffer_load_dwordx4 v221, s[12:15], s63 offen offset:1024 lds\n"
"M 88,160,76\nR 182,188,28672\nM 88,164,92\nG 4\nM 92,168,76\nR 160,189,16384\n"
"M 92,172,92\nbuffer_load_dwordx4 a[164:167], v252, s[20:23], s65 offen offset:2048 sc1\nM 76,168,72\nR 162,189,20480\nM 76,172,88\nM 60,168,68\n"
"R 164,189,24576\nM 60,172,84\nbuffer_load_dwordx4 a[168:171], v252, s[20:23], s66 offen offset:2048 sc1\nM 44,168,64\nR 166,189,28672\nM 44,172,80\n"
"G 4\nM 96,176,64\nR 168,190,16384\ns_mov_b32 s85, 0x0c0c0400\nM 96,180,80\ns_waitcnt vmcnt(16)\n"
"v_perm_b32 v16, v1, v0, s85\nv_perm_b32 v20, v3, v2, s85\nM 112,176,68\nR 170,190,20480\nv_lshl_or_b32 v16, v20, 16, v16\nM 112,180,84\n"
"buffer_load_dwordx4 a[172:175], v252, s[20:23], s67 offen offset:2048 sc1\nM 128,176,72\nR 172,190,24576\ns_waitcnt vmcnt(15)\nv_perm_b32 v17, v5, v4, s85\nM 128,180,88\n"
"s_add_u32 m0, s50, 8192\nv_perm_b32 v21, v7, v6, s85\nv_lshl_or_b32 v17, v21, 16, v17\nM 144,176,76\nR 174,190,28672\ns_add_u32 s63, s54, 1835008\n"
"M 144,180,92\ns_barrier\nG 4\nM 148,160,76\nR 176,191,16384\ns_waitcnt vmcnt(13)\n"
"v_perm_b32 v18, v9, v8, s85\nM 148,164,92\nbuffer_load_dwordx4 a[176:179], v252, s[20:23], s64 offen offset:3072 sc1\nM 132,160,72\nR 178,191,20480\nv_perm_b32 v22, v11, v10, s85\n"
"M 132,164,88\nbuffer_load_dwordx4 v220, s[12:15], s63 offen lds\nM 116,160,68\nR 180,191,24576\nv_lshl_or_b32 v18, v22, 16, v18\nM 116,164,84\n"
"buffer_load_dwordx4 v221, s[12:15], s63 offen offset:1024 lds\nM 100,160,64\nR 182,191,28672\ns_add_u32 m0, s50, 16384\nM 100,164,80\nG 4\n"
"s_waitcnt vmcnt(15)\nv_perm_b32 v19, v13, v12, s85\nv_perm_b32 v23, v15, v14, s85\nM 104,168,64\nR 160,188,32768\ns_add_u32 s63, s54, 2064384\n"
"M 104,172,80\nbuffer_load_dwordx4 a[180:183], v252, s[20:23], s65 offen offset:3072 sc1\nM 120,168,68\nR 162,188,36864\nv_lshl_or_b32 v19, v23, 16, v19\nM 120,172,84\n"
"buffer_load_dwordx4 v220, s[12:15], s63 offen lds\nM 136,168,72\nR 164,188,40960\nv_min3_u32 v230, v16, v17, v18\nM 136,172,88\nbuffer_load_dwordx4 a[184:187], v252, s[20:23], s66 offen offset:3072 sc1\n"
"M 152,168,76\nR 166,188,45056\nv_min3_u32 v230, v230, v19, v19\nM 152,172,92\nG 4\nv_max3_u32 v231, v16, v17, v18\n"
"v_max3_u32 v231, v231, v19, v19\nM 156,176,76\nR 168,189,32768\nbuffer_load_dwordx4 v221, s[12:15], s63 offen offset:1024 lds\nM 156,180,92\nv_cmp_ne_u32 s[86:87], s46, v230\n"
"v_cmp_ne_u32 vcc, s46, v231\nM 140,176,72\nR 170,189,36864\nM 140,180,88\nbuffer_load_dwordx4 a[188:191], v252, s[20:23], s67 offen offset:3072 sc1\nM 124,176,68\n"
"R 172,189,40960\nM 124,180,84\ns_or_b64 s[86:87], s[86:87], vcc\ns_cbranch_scc1 Lq818\nLq819:\nv_and_b32 v0, 0xffffff00, v0\n"
"M 108,176,64\nR 174,189,45056\nv_and_b32 v1, 0xffffff00, v1\nM 108,180,80\nG 4\nv_and_b32 v2, 0xffffff00, v2\n"
"v_and_b32 v3, 0xffffff00, v3\nM 32,160,96\nR 176,190,32768\nv_and_b32 v4, 0xffffff00, v4\nM 32,164,112\ns_add_u32 s64, s70, 14692352\n"
"buffer_load_dwordx4 a[192:195], v252, s[20:23], s64 offen offset:0 sc1\nM 48,160,100\nR 178,190,36864\nv_and_b32 v5, 0xffffff00, v5\nM 48,164,116\nv_and_b32 v6, 0xffffff00, v6\n"
"v_and_b32 v7, 0xffffff00, v7\nM 64,160,104\nR 180,190,40960\nv_and_b32 v8, 0xffffff00, v8\nM 64,164,120\ns_add_u32 m0, s50, 24576\n"
"v_and_b32 v9, 0xffffff00, v9\nv_and_b32 v10, 0xffffff00, v10\nM 80,160,108\nR 182,190,45056\ns_add_u32 s63, s54, 2293760\nM 80,164,124\n"
"s_waitcnt vmcnt(12)\ns_barrier\nG 4\nM 84,168,108\nR 160,191,32768\nv_and_b32 v11, 0xffffff00, v11\n"
"M 84,172,124\ns_add_u32 s65, s70, 14725120\nbuffer_load_dwordx4 a[196:199], v252, s[20:23], s65 offen offset:0 sc1\nM 68,168,104\nR 162,191,36864\nv_and_b32 v12, 0xffffff00, v12\n"
"M 68,172,120\nbuffer_load_dwordx4 v220, s[12:15], s63 offen lds\nM 52,168,100\nR 164,191,40960\nv_and_b32 v13, 0xffffff00, v13\nM 52,172,116\n"
"s_add_u32 s66, s71, 14692352\nbuffer_load_dwordx4 a[200:203], v252, s[20:23], s66 offen offset:0 sc1\nM 36,168,96\nR 166,191,45056\nv_and_b32 v14, 0xffffff00, v14\nM 36,172,112\n"
"G 4\nv_and_b32 v15, 0xffffff00, v15\nv_add_f32 v0, v0, v8\nM 40,176,96\nR 168,188,49152\nbuffer_load_dwordx4 v221, s[12:15], s63 offen offset:1024 lds\n"
"M 40,180,112\ns_add_u32 m0, s50, 32768\ns_add_u32 s63, s54, 2523136\nbuffer_load_dwordx4 v220, s[12:15], s63 offen lds\nM 56,176,100\nR 170,188,53248\n"
"v_add_f32 v1, v1, v9\nM 56,180,116\ns_add_u32 s67, s71, 14725120\nbuffer_load_dwordx4 a[204:207], v252, s[20:23], s67 offen offset:0 sc1\nM 72,176,104\nR 172,184,0\n"
"v_add_f32 v2, v2, v10\nM 72,180,120\nbuffer_load_dwordx4 v221, s[12:15], s63 offen offset:1024 lds\nM 88,176,108\nR 174,184,4096\nv_add_f32 v3, v3, v11\n"
"M 88,180,124\nG 4\nv_add_f32 v4, v4, v12\nv_add_f32 v5, v5, v13\nM 92,160,108\nR 176,189,49152\n"
"v_add_f32 v6, v6, v14\nM 92,164,124\nbuffer_load_dwordx4 a[208:211], v252, s[20:23], s64 offen offset:1024 sc1\nM 76,160,104\nR 178,189,53248\nv_add_f32 v7, v7, v15\n"
"M 76,164,120\nv_add_f32 v0, v0, v4\nv_add_f32 v1, v1, v5\nM 60,160,100\nR 180,185,0\nLq1034:\n"
"s_lshr_b32 s60, s45, 3\ns_lshl2_add_u32 s60, s60, s40\ns_lshl_b32 s60, s60, 8\nv_lshl_add_u32 v224, v252, 3, s60\nv_add_u32 v224, Lq1034+4096-LqPC, v224\nv_min_u32 v224, LqE-LqPC-4, v224\n"
"s_mov_b64 exec, 3\nglobal_load_dword v224, v224, s[98:99]\ns_mov_b64 exec, -1\nM 60,164,116\nv_add_f32 v2, v2, v6\nv_add_f32 v3, v3, v7\n"
"M 44,160,96\nR 182,185,4096\nv_permlane32_swap_b32 v0, v2\nM 44,164,112\nG 4\nv_permlane32_swap_b32 v1, v3\n"
"v_add_f32 v0, v0, v2\nM 96,168,96\nR 160,190,49152\nv_add_f32 v1, v1, v3\nM 96,172,112\nbuffer_load_dwordx4 a[212:215], v252, s[20:23], s65 offen offset:1024 sc1\n"
"M 112,168,100\nR 162,190,53248\nv_permlane16_swap_b32 v0, v1\nM 112,172,116\ns_add_u32 m0, s50, 40960\nv_add_f32 v230, v0, v1\n"
"v_mov_b32 v232, v230\nM 128,168,104\nR 164,186,0\ns_add_u32 s63, s54, 2752512\nM 128,172,120\nbuffer_load_dwordx4 a[216:219], v252, s[20:23], s66 offen offset:1024 sc1\n"
"M 144,168,108\nR 166,186,4096\nv_permlane16_swap_b32 v230, v232\nM 144,172,124\ns_waitcnt vmcnt(13)\ns_barrier\n"
"G 4\nM 148,176,108\nR 168,191,49152\nv_mov_b32 v231, v230\nM 148,180,124\nbuffer_load_dwordx4 v220, s[12:15], s63 offen lds\n"
"M 132,176,104\nR 170,191,53248\nv_mov_b32 v233, v232\nM 132,180,120\nbuffer_load_dwordx4 a[220:223], v252, s[20:23], s67 offen offset:1024 sc1\nM 116,176,100\n"
"R 172,187,0\nv_permlane32_swap_b32 v230, v231\nM 116,180,116\nbuffer_load_dwordx4 v221, s[12:15], s63 offen offset:1024 lds\nM 100,176,96\nR 174,187,4096\n"
"s_add_u32 m0, s50, 49152\nM 100,180,112\nG 4\nv_permlane32_swap_b32 v232, v233\nv_mul_f32 v230, 0xba800000, v230\nM 104,160,96\n"
"R 176,184,8192\ns_add_u32 s63, s54, 2981888\nM 104,164,112\nbuffer_load_dwordx4 a[224:227], v252, s[20:23], s64 offen offset:2048 sc1\nM 120,160,100\nR 178,184,12288\n"
"v_mul_f32 v214, v230, v230\nM 120,164,116\nbuffer_load_dwordx4 v220, s[12:15], s63 offen lds\nM 136,160,104\nR 180,184,16384\nv_mul_f32 v231, 0x3a800000, v231\n"
"M 136,164,120\nbuffer_load_dwordx4 v221, s[12:15], s63 offen offset:1024 lds\nM 152,160,108\nR 182,184,20480\nv_sub_f32 v231, v231, v214\nM 152,164,124\n"
"G 4\nv_max_f32 v231, 0, v231\nv_add_f32 v231, 0x3727c5ac, v231\nLpg25:\ns_cbranch_execz Lpg29\nM 156,168,108\n"
"R 160,185,8192\nv_and_b32 v208, s77, v230\nM 156,172,124\nbuffer_load_dwordx4 a[228:231], v252, s[20:23], s65 offen offset:2048 sc1\nM 140,168,104\nR 162,185,12288\n"
"v_sub_f32 v230, v230, v208\nM 140,172,120\nv_rsq_f32 v234, v231\nM 124,168,100\nR 164,185,16384\nv_mul_f32 v216, v231, v234\n"
"M 124,172,116\nbuffer_load_dwordx4 a[232:235], v252, s[20:23], s66 offen offset:2048 sc1\nM 108,168,96\nR 166,185,20480\nv_cvt_pk_bf16_f32 v209, v230, v216\nM 108,172,112\n"
"G 4\nv_cvt_pk_bf16_f32 v208, v208, v208\nv_mul_f32 v214, 0xc0135761, v234\nM 32,176,128\nR 168,186,8192\nv_mul_f32 v215, v234, v234\n"
"M 32,180,144\ns_mov_b64 s[60:61], 0xffff\nv_cndmask_b32 v208, 0, v208, s[60:61]\nv_mul_f32 v215, v215, v214\nM 48,176,132\nR 170,186,12288\n"
"v_mul_f32 v215, 0x3d372713, v215\nM 48,180,148\nbuffer_load_dwordx4 a[236:239], v252, s[20:23], s67 offen offset:2048 sc1\nM 64,176,136\nR 172,186,16384\nv_mul_f32 v232, 0xba800000, v232\n"
"M 64,180,152\ns_add_u32 m0, s50, 57344\ns_mov_b64 s[60:61], 0xffff\nv_cndmask_b32 v209, 0, v209, s[60:61]\nM 80,176,140\nR 174,186,20480\n"
"s_add_u32 s63, s54, 3211264\nM 80,180,156\ns_waitcnt vmcnt(13)\ns_barrier\nG 4\nM 84,160,140\n"
"R 176,187,8192\nv_mul_f32 v217, v232, v232\nM 84,164,156\nbuffer_load_dwordx4 a[240:243], v252, s[20:23], s64 offen offset:3072 sc1\nM 68,160,136\nR 178,187,12288\n"
"v_mul_f32 v233, 0x3a800000, v233\nM 68,164,152\nbuffer_load_dwordx4 v220, s[12:15], s63 offen lds\nM 52,160,132\nR 180,187,16384\nv_sub_f32 v233, v233, v217\n"
"M 52,164,148\nbuffer_load_dwordx4 v221, s[12:15], s63 offen offset:1024 lds\nM 36,160,128\nR 182,187,20480\ns_add_u32 m0, s50, 65536\nM 36,164,144\n"
"G 4\nv_max_f32 v233, 0, v233\nv_add_f32 v233, 0x3727c5ac, v233\nM 40,168,128\nR 160,184,24576\ns_add_u32 s63, s54, 3440640\n"
"M 40,172,144\nbuffer_load_dwordx4 a[244:247], v252, s[20:23], s65 offen offset:3072 sc1\nM 56,168,132\nR 162,184,28672\nv_and_b32 v212, s77, v232\nM 56,172,148\n"
"buffer_load_dwordx4 v220, s[12:15], s63 offen lds\nM 72,168,136\nR 164,184,32768\nv_sub_f32 v232, v232, v212\nM 72,172,152\nbuffer_load_dwordx4 a[248:251], v252, s[20:23], s66 offen offset:3072 sc1\n"
"M 88,168,140\nR 166,184,36864\nv_cvt_pk_bf16_f32 v212, v212, v212\nM 88,172,156\nG 4\nv_rsq_f32 v235, v233\n"
"M 92,176,140\nR 168,185,24576\nbuffer_load_dwordx4 v221, s[12:15], s63 offen offset:1024 lds\nM 92,180,156\ns_mov_b64 s[60:61], 0xffff\nv_cndmask_b32 v212, 0, v212, s[60:61]\n"
"v_mul_f32 v219, v233, v235\nM 76,176,136\nR 170,185,28672\nv_cvt_pk_bf16_f32 v213, v232, v219\nM 76,180,152\nbuffer_load_dwordx4 a[252:255], v252, s[20:23], s67 offen offset:3072 sc1\n"
"M 60,176,132\nR 172,185,32768\nv_mul_f32 v217, 0xc0135761, v235\nM 60,180,148\ns_mov_b64 s[60:61], 0xffff\nv_cndmask_b32 v213, 0, v213, s[60:61]\n"
"v_mul_f32 v218, v235, v235\nM 44,176,128\nR 174,185,36864\nv_mul_f32 v218, v218, v217\nM 44,180,144\nG 4\n"
"v_mul_f32 v218, 0x3d372713, v218\nM 32,160,160\nR 176,186,24576\nM 32,164,176\nM 48,160,164\nR 178,186,28672\n"
"M 48,164,180\ns_add_u32 m0, s50, 73728\nM 64,160,168\nR 180,186,32768\nM 64,164,184\ns_add_u32 s63, s54, 1835136\n"
"M 80,160,172\nR 182,186,36864\nM 80,164,188\ns_waitcnt vmcnt(11)\ns_barrier\nG 4\n"
"M 84,168,172\nR 160,187,24576\nM 84,172,188\nbuffer_load_dwordx4 v220, s[12:15], s63 offen lds\nM 68,168,168\nR 162,187,28672\n"
"M 68,172,184\nM 52,168,164\nR 164,187,32768\nv_bfe_u32 v250, v252, 5, 1\nv_mul_u32_u24 v250, 2560, v250\nM 52,172,180\n"
"buffer_load_dwordx4 v221, s[12:15], s63 offen offset:1024 lds\nv_and_b32 v251, 16, v252\nM 36,168,160\nR 166,187,36864\ns_add_u32 m0, s50, 81920\nv_lshl_add_u32 v250, v251, 1, v250\n"
"M 36,172,176\ns_lshl_b32 s60, s90, 6\nv_add_u32 v250, s60, v250\nG 4\nM 40,176,160\nR 168,184,40960\n"
"s_add_u32 s63, s54, 2064512\nM 40,180,176\nbuffer_load_dwordx4 v220, s[12:15], s63 offen lds\nM 56,176,164\nR 170,184,45056\nM 56,180,180\n"
"M 72,176,168\nR 172,184,49152\nM 72,180,184\nbuffer_load_dwordx4 v221, s[12:15], s63 offen offset:1024 lds\nM 88,176,172\nR 174,184,53248\n"
"s_add_u32 m0, s50, 90112\nM 88,180,188\nG 4\nM 92,160,172\nR 176,185,40960\ns_add_u32 s63, s54, 2293888\n"
"M 92,164,188\nbuffer_load_dwordx4 v220, s[12:15], s63 offen lds\nM 76,160,168\nR 178,185,45056\nM 76,164,184\nM 60,160,164\n"
"R 180,185,49152\nM 60,164,180\nbuffer_load_dwordx4 v221, s[12:15], s63 offen offset:1024 lds\nM 44,160,160\nR 182,185,53248\ns_add_u32 s63, s54, 2523264\n"
"M 44,164,176\nG 4\nM 32,168,192\nR 160,186,40960\nM 32,172,208\ns_add_u32 m0, s50, 98304\n"
"M 48,168,196\nR 162,186,45056\nM 48,172,212\nbuffer_load_dwordx4 v220, s[12:15], s63 offen lds\nM 64,168,200\nR 164,186,49152\n"
"M 64,172,216\nM 80,168,204\nR 166,186,53248\nM 80,172,220\ns_waitcnt vmcnt(8)\ns_barrier\n"
"G 4\nM 84,176,204\nR 168,187,40960\nM 84,180,220\nbuffer_load_dwordx4 v221, s[12:15], s63 offen offset:1024 lds\nM 68,176,200\n"
"R 170,187,45056\ns_add_u32 s63, s54, 2752640\nM 68,180,216\ns_add_u32 m0, s50, 106496\nM 52,176,196\nR 172,187,49152\n"
"M 52,180,212\nbuffer_load_dwordx4 v220, s[12:15], s63 offen lds\nM 36,176,192\nR 174,187,53248\nM 36,180,208\nG 4\n"
"M 40,160,192\nR 176,184,57344\nM 40,164,208\nbuffer_load_dwordx4 v221, s[12:15], s63 offen offset:1024 lds\nM 56,160,196\nR 178,184,61440\n"
"s_add_u32 s63, s54, 2982016\nM 56,164,212\ns_add_u32 m0, s50, 114688\nM 72,160,200\nR 180,188,0\nM 72,164,216\n"
"buffer_load_dwordx4 v220, s[12:15], s63 offen lds\nM 88,160,204\nR 182,188,4096\nM 88,164,220\nG 4\nM 92,168,204\n"
"R 160,185,57344\nM 92,172,220\nbuffer_load_dwordx4 v221, s[12:15], s63 offen offset:1024 lds\nM 76,168,200\nR 162,185,61440\ns_add_u32 s63, s54, 3211392\n"
"M 76,172,216\ns_add_u32 m0, s50, 0\nM 60,168,196\nR 164,189,0\nM 60,172,212\nbuffer_load_dwordx4 v220, s[12:15], s63 offen lds\n"
"M 44,168,192\nR 166,189,4096\ns_mov_b32 s60, 157184\nv_bfe_u32 v246, v252, 4, 4\nv_lshl_add_u32 v246, v246, 3, s60\nM 44,172,208\n"
"ds_read_b64 v[238:239], v246 offset:0\nG 5\nM 32,176,224\nR 168,186,57344\nds_read_b64 v[240:241], v246 offset:128\nM 32,180,240\n"
"ds_read_b64 v[242:243], v246 offset:256\nM 48,176,228\nR 170,186,61440\nds_read_b64 v[244:245], v246 offset:384\nM 48,180,244\nds_read_b64 v[246:247], v246 offset:0\n"
"M 64,176,232\nR 172,190,0\nM 64,180,248\nbuffer_load_dwordx4 v221, s[12:15], s63 offen offset:1024 lds\nM 80,176,236\nR 174,190,4096\n"
"s_add_u32 m0, s50, 8192\ns_waitcnt vmcnt(14)\nM 80,180,252\ns_waitcnt vmcnt(10) lgkmcnt(2)\ns_barrier\nM 84,160,236\n"
"R 176,187,57344\ns_add_u32 s63, s54, 3440768\nM 84,164,252\nbuffer_load_dwordx4 v220, s[12:15], s63 offen lds\nM 68,160,232\nR 178,187,61440\n"
"M 68,164,248\nM 52,160,228\nR 180,191,0\nv_mfma_f32_16x16x32_bf16 v[32:35], v[238:241], v[208:211], v[32:35]\nM 52,164,244\nbuffer_load_dwordx4 v221, s[12:15], s63 offen offset:1024 lds\n"
"M 36,160,224\nR 182,191,4096\nv_mfma_f32_16x16x32_bf16 v[36:39], v[240:243], v[208:211], v[36:39]\ns_add_u32 m0, s50, 16384\nM 36,164,240\nG 4\n"
"M 40,168,224\nR 160,188,8192\nv_mfma_f32_16x16x32_bf16 v[40:43], v[242:245], v[208:211], v[40:43]\ns_add_u32 s63, s55, 0\nM 40,172,240\nbuffer_load_dwordx4 v222, s[12:15], s63 offen lds\n"
"M 56,168,228\nR 162,188,12288\nv_mfma_f32_16x16x32_bf16 v[44:47], v[244:247], v[208:211], v[44:47]\nM 56,172,244\nM 72,168,232\nR 164,188,16384\n"
"v_mfma_f32_16x16x32_bf16 v[48:51], v[244:247], v[210:213], v[48:51]\nM 72,172,248\nbuffer_load_dwordx4 v223, s[12:15], s63 offen offset:1024 lds\nM 88,168,236\nR 166,188,20480\nv_mfma_f32_16x16x32_bf16 v[52:55], v[238:241], v[210:213], v[52:55]\n"
"s_add_u32 s63, s55, 458752\nM 88,172,252\nG 4\nM 92,176,236\nR 168,189,8192\nv_mfma_f32_16x16x32_bf16 v[56:59], v[240:243], v[210:213], v[56:59]\n"
"M 92,180,252\ns_add_u32 m0, s50, 24576\nM 76,176,232\nR 170,189,12288\nv_mfma_f32_16x16x32_bf16 v[60:63], v[242:245], v[210:213], v[60:63]\nM 76,180,248\n"
"buffer_load_dwordx4 v222, s[12:15], s63 offen lds\nM 60,176,228\nR 172,189,16384\nM 60,180,244\nM 44,176,224\nR 174,189,20480\n"
"M 44,180,240\ns_waitcnt vmcnt(11)\ns_barrier\nG 4\nM 96,160,128\nR 176,190,8192\n"
"M 96,164,144\ns_mov_b32 m0, s51\nM 112,160,132\nR 178,190,12288\nM 112,164,148\nds_write_addtid_b32 v64 offset:0\n"
"M 128,160,136\nR 180,190,16384\nM 128,164,152\nds_write_addtid_b32 v65 offset:256\nM 144,160,140\nR 182,190,20480\n"
"ds_write_addtid_b32 v66 offset:512\nM 144,164,156\nds_write_addtid_b32 v67 offset:768\nds_write_addtid_b32 v68 offset:1024\nG 9\nM 148,168,140\n"
"R 160,191,8192\nds_write_addtid_b32 v69 offset:1280\nM 148,172,156\nds_write_addtid_b32 v70 offset:1536\nds_write_addtid_b32 v71 offset:1792\nM 132,168,136\n"
"R 162,191,12288\nds_write_addtid_b32 v72 offset:2048\nM 132,172,152\nds_write_addtid_b32 v73 offset:2304\nds_write_addtid_b32 v74 offset:2560\nM 116,168,132\n"
"R 164,191,16384\nds_write_addtid_b32 v75 offset:2816\nM 116,172,148\nds_write_addtid_b32 v76 offset:3072\nds_write_addtid_b32 v77 offset:3328\nM 100,168,128\n"
"R 166,191,20480\nds_write_addtid_b32 v78 offset:3584\nM 100,172,144\nds_write_addtid_b32 v79 offset:3840\nds_write_addtid_b32 v80 offset:4096\nG 15\n"
"M 104,176,128\nR 168,188,24576\nds_write_addtid_b32 v81 offset:4352\nM 104,180,144\nds_write_addtid_b32 v82 offset:4608\nds_write_addtid_b32 v83 offset:4864\n"
"M 120,176,132\nR 170,188,28672\nds_write_addtid_b32 v84 offset:5120\nM 120,180,148\nds_write_addtid_b32 v85 offset:5376\nds_write_addtid_b32 v86 offset:5632\n"
"M 136,176,136\nR 172,188,32768\nds_write_addtid_b32 v87 offset:5888\nM 136,180,152\nds_write_addtid_b32 v88 offset:6144\nds_write_addtid_b32 v89 offset:6400\n"
"M 152,176,140\nR 174,188,36864\nds_write_addtid_b32 v90 offset:6656\nM 152,180,156\nds_write_addtid_b32 v91 offset:6912\nds_write_addtid_b32 v92 offset:7168\n"
"G 15\nM 156,160,140\nR 176,189,24576\nds_write_addtid_b32 v93 offset:7424\nM 156,164,156\nds_write_addtid_b32 v94 offset:7680\n"
"ds_write_addtid_b32 v95 offset:7936\nM 140,160,136\nR 178,189,28672\ns_add_u32 m0, s50, 24576\nM 140,164,152\nbuffer_load_dwordx4 v223, s[12:15], s63 offen offset:1024 lds\n"
"M 124,160,132\nR 180,189,32768\ns_waitcnt vmcnt(8) lgkmcnt(2)\ns_barrier\nM 124,164,148\ns_add_u32 m0, s50, 32768\n"
"ds_read2st64_b32 v[64:65], v225 offset0:0 offset1:1\nds_read2st64_b32 v[66:67], v225 offset0:2 offset1:3\nM 108,160,128\nR 182,189,36864\ns_add_u32 s63, s55, 917504\nds_read2st64_b32 v[68:69], v225 offset0:4 offset1:5\n"
"M 108,164,144\nds_read2st64_b32 v[70:71], v225 offset0:6 offset1:7\nds_read2st64_b32 v[72:73], v225 offset0:8 offset1:9\nM 96,168,160\nR 160,190,24576\nds_read2st64_b32 v[74:75], v225 offset0:10 offset1:11\n"
"M 96,172,176\nbuffer_load_dwordx4 v222, s[12:15], s63 offen lds\nds_read2st64_b32 v[76:77], v225 offset0:12 offset1:13\nM 112,168,164\nR 162,190,28672\nds_read2st64_b32 v[78:79], v225 offset0:14 offset1:15\n"
"M 112,172,180\nds_read2st64_b32 v[80:81], v225 offset0:16 offset1:17\nds_read2st64_b32 v[82:83], v225 offset0:18 offset1:19\nG 12\nv_add_f32 v32, v32, v64\nM 128,168,168\n"
"R 164,190,32768\nds_read2st64_b32 v[84:85], v225 offset0:20 offset1:21\nv_add_f32 v33, v33, v65\nM 128,172,184\nds_read2st64_b32 v[86:87], v225 offset0:22 offset1:23\nds_read2st64_b32 v[88:89], v225 offset0:24 offset1:25\n"
"G 15\nv_add_f32 v34, v34, v66\nM 144,168,172\nR 166,190,36864\nds_read2st64_b32 v[90:91], v225 offset0:26 offset1:27\nv_add_f32 v35, v35, v67\n"
"M 144,172,188\nds_read2st64_b32 v[92:93], v225 offset0:28 offset1:29\nds_read2st64_b32 v[94:95], v225 offset0:30 offset1:31\nG 15\nM 148,176,172\nR 168,191,24576\n"
"v_add_f32 v36, v36, v68\nv_add_f32 v37, v37, v69\nM 148,180,188\nbuffer_load_dwordx4 v223, s[12:15], s63 offen offset:1024 lds\nM 132,176,168\nR 170,191,28672\n"
"v_add_f32 v38, v38, v70\nM 132,180,184\ns_add_u32 m0, s50, 40960\nv_add_f32 v39, v39, v71\nv_add_f32 v40, v40, v72\nM 116,176,164\n"
"R 172,191,32768\nv_add_f32 v41, v41, v73\nM 116,180,180\ns_add_u32 s63, s55, 1376256\nG 15\nv_add_f32 v42, v42, v74\n"
"v_add_f32 v43, v43, v75\nM 100,176,160\nR 174,191,36864\nv_add_f32 v44, v44, v76\nM 100,180,176\nG 7\n"
"v_add_f32 v45, v45, v77\nv_add_f32 v46, v46, v78\nM 104,160,160\nR 176,188,40960\nv_add_f32 v47, v47, v79\nM 104,164,176\n"
"buffer_load_dwordx4 v222, s[12:15], s63 offen lds\nM 120,160,164\nR 178,188,45056\nv_add_f32 v48, v48, v80\nM 120,164,180\nv_add_f32 v49, v49, v81\n"
"v_add_f32 v50, v50, v82\nM 136,160,168\nR 180,188,49152\nv_add_f32 v51, v51, v83\nM 136,164,184\nv_add_f32 v52, v52, v84\n"
"v_add_f32 v53, v53, v85\nM 152,160,172\nR 182,188,53248\nv_add_f32 v54, v54, v86\nM 152,164,188\nG 4\n"
"v_add_f32 v55, v55, v87\nv_add_f32 v56, v56, v88\nM 156,168,172\nR 160,189,40960\nv_add_f32 v57, v57, v89\nM 156,172,188\n"
"buffer_load_dwordx4 v223, s[12:15], s63 offen offset:1024 lds\nM 140,168,168\nR 162,189,45056\nv_add_f32 v58, v58, v90\nM 140,172,184\ns_add_u32 m0, s50, 49152\n"
"v_add_f32 v59, v59, v91\nv_add_f32 v60, v60, v92\nM 124,168,164\nR 164,189,49152\nv_add_f32 v61, v61, v93\nM 124,172,180\n"
"s_add_u32 s63, s55, 1835008\nv_add_f32 v62, v62, v94\nv_add_f32 v63, v63, v95\nM 108,168,160\nR 166,189,53248\nv_mul_f32 v230, v32, v32\n"
"v_mul_f32 v231, v33, v33\nM 108,172,176\ns_add_u32 s84, s73, 37748736\ns_add_u32 s85, s73, 37863424\nG 4\nv_mul_f32 v232, v34, v34\n"
"M 96,176,192\nR 168,190,40960\nv_mul_f32 v233, v35, v35\nv_fma_f32 v230, v230, v215, v214\nM 96,180,208\nbuffer_load_dwordx4 v222, s[12:15], s63 offen lds\n"
"v_fma_f32 v231, v231, v215, v214\nM 112,176,196\nR 170,190,45056\ns_waitcnt vmcnt(7)\ns_barrier\nv_fma_f32 v232, v232, v215, v214\n"
"M 112,180,212\nv_fma_f32 v233, v233, v215, v214\nLpg26:\ns_cbranch_execz Lpg30\nv_mul_f32 v230, v32, v230\nv_mul_f32 v231, v33, v231\n"
"M 128,176,200\nR 172,190,49152\nv_mul_f32 v232, v34, v232\nv_mul_f32 v233, v35, v233\nM 128,180,216\nv_exp_f32 v230, v230\n"
"v_exp_f32 v231, v231\nM 144,176,204\nR 174,190,53248\nv_fma_f32 v230, v230, v216, v216\nv_exp_f32 v232, v232\nM 144,180,220\n"
"G 4\nv_exp_f32 v233, v233\nv_fma_f32 v231, v231, v216, v216\nM 148,160,204\nR 176,191,40960\nv_fma_f32 v232, v232, v216, v216\n"
"v_fma_f32 v233, v233, v216, v216\nM 148,164,220\nbuffer_load_dwordx4 v223, s[12:15], s63 offen offset:1024 lds\nv_rcp_f32 v230, v230\nM 132,160,200\nR 178,191,45056\n"
"v_mul_f32 v230, v32, v230\nv_rcp_f32 v231, v231\nM 132,164,216\ns_add_u32 m0, s50, 57344\nv_rcp_f32 v232, v232\nv_rcp_f32 v233, v233\n"
"M 116,160,196\nR 180,191,49152\nv_mul_f32 v231, v33, v231\nv_mul_f32 v232, v34, v232\nM 116,164,212\ns_add_u32 s63, s55, 2293760\n"
"v_mul_f32 v233, v35, v233\nv_cvt_pk_bf16_f32 v32, v230, v231\nv_cvt_pk_bf16_f32 v33, v232, v233\nM 100,160,192\nR 182,191,53248\nv_mul_f32 v234, v36, v36\n"
"v_mul_f32 v235, v37, v37\nM 100,164,208\nG 4\nv_mul_f32 v236, v38, v38\nv_mul_f32 v237, v39, v39\nv_fma_f32 v234, v234, v215, v214\n"
"M 104,168,192\nR 160,184,0\nv_fma_f32 v235, v235, v215, v214\nv_fma_f32 v236, v236, v215, v214\nM 104,172,208\nbuffer_load_dwordx4 v222, s[12:15], s63 offen lds\n"
"v_fma_f32 v237, v237, v215, v214\nM 120,168,196\nR 162,184,4096\nv_mul_f32 v234, v36, v234\nv_mul_f32 v235, v37, v235\nM 120,172,212\n"
"v_exp_f32 v234, v234\nv_mul_f32 v236, v38, v236\nM 136,168,200\nR 164,184,8192\nv_mul_f32 v237, v39, v237\nv_exp_f32 v235, v235\n"
"M 136,172,216\nv_exp_f32 v236, v236\nv_exp_f32 v237, v237\nM 152,168,204\nR 166,184,12288\nv_fma_f32 v234, v234, v216, v216\n"
"v_fma_f32 v235, v235, v216, v216\nM 152,172,220\nG 4\nv_rcp_f32 v234, v234\nv_fma_f32 v236, v236, v216, v216\nM 156,176,204\n"
"R 168,185,0\nv_fma_f32 v237, v237, v216, v216\nv_rcp_f32 v235, v235\nM 156,180,220\nbuffer_load_dwordx4 v223, s[12:15], s63 offen offset:1024 lds\nv_rcp_f32 v236, v236\n"
"M 140,176,200\nR 170,185,4096\nv_mul_f32 v234, v36, v234\nv_rcp_f32 v237, v237\nM 140,180,216\nv_mul_f32 v235, v37, v235\n"
"v_mul_f32 v236, v38, v236\nv_mul_f32 v237, v39, v237\nM 124,176,196\nR 172,185,8192\nv_cvt_pk_bf16_f32 v34, v234, v235\nv_cvt_pk_bf16_f32 v35, v236, v237\n"
"M 124,180,212\nbuffer_store_dwordx4 v[32:35], v252, s[20:23], s84 offen offset:0 sc1\nv_mul_f32 v230, v40, v40\nM 108,176,192\nR 174,185,12288\ns_mov_b32 s60, 157696\n"
"v_bfe_u32 v246, v252, 4, 4\nv_lshl_add_u32 v246, v246, 3, s60\nv_mul_f32 v231, v41, v41\nM 108,180,208\nds_read_b64 v[238:239], v246 offset:0\nG 5\n"
"v_mul_f32 v232, v42, v42\nM 96,160,224\nR 176,186,0\nds_read_b64 v[240:241], v246 offset:128\nv_mul_f32 v233, v43, v43\nM 96,164,240\n"
"ds_read_b64 v[242:243], v246 offset:256\ns_add_u32 m0, s50, 65536\nv_fma_f32 v230, v230, v215, v214\nM 112,160,228\nR 178,186,4096\nds_read_b64 v[244:245], v246 offset:384\n"
"v_fma_f32 v231, v231, v215, v214\nM 112,164,244\nds_read_b64 v[246:247], v246 offset:0\ns_add_u32 s63, s55, 2752512\nv_fma_f32 v232, v232, v215, v214\nM 128,160,232\n"
"R 180,186,8192\nv_fma_f32 v233, v233, v215, v214\nv_mul_f32 v230, v40, v230\nM 128,164,248\nbuffer_load_dwordx4 v222, s[12:15], s63 offen lds\nv_mul_f32 v231, v41, v231\n"
"M 144,160,236\nR 182,186,12288\nv_mul_f32 v232, v42, v232\nv_mul_f32 v233, v43, v233\nM 144,164,252\nG 9\n"
"v_exp_f32 v230, v230\nv_exp_f32 v231, v231\nM 148,168,236\nR 160,187,0\nv_fma_f32 v230, v230, v216, v216\nv_exp_f32 v232, v232\n"
"M 148,172,252\nv_exp_f32 v233, v233\nv_fma_f32 v231, v231, v216, v216\nM 132,168,232\nR 162,187,4096\nv_fma_f32 v232, v232, v216, v216\n"
"v_fma_f32 v233, v233, v216, v216\nM 132,172,248\nbuffer_load_dwordx4 v223, s[12:15], s63 offen offset:1024 lds\nv_rcp_f32 v230, v230\nM 116,168,228\nR 164,187,8192\n"
"G 9\nv_mfma_f32_16x16x32_bf16 v[96:99], v[238:241], v[208:211], v[96:99]\ns_add_u32 s63, s55, 3211264\nv_rcp_f32 v231, v231\nM 116,172,244\ns_add_u32 m0, s50, 73728\n"
"v_rcp_f32 v232, v232\nv_rcp_f32 v233, v233\nM 100,168,224\nR 166,187,12288\nG 9\nv_mfma_f32_16x16x32_bf16 v[100:103], v[240:243], v[208:211], v[100:103]\n"
"v_mul_f32 v230, v40, v230\nv_mul_f32 v231, v41, v231\nM 100,172,240\nG 4\nv_mul_f32 v232, v42, v232\nv_mul_f32 v233, v43, v233\n"
"v_cvt_pk_bf16_f32 v40, v230, v231\nM 104,176,224\nR 168,184,16384\nv_mfma_f32_16x16x32_bf16 v[104:107], v[242:245], v[208:211], v[104:107]\nv_cvt_pk_bf16_f32 v41, v232, v233\nv_mul_f32 v234, v44, v44\n"
"M 104,180,240\nbuffer_load_dwordx4 v222, s[12:15], s63 offen lds\nv_mul_f32 v235, v45, v45\nM 120,176,228\nR 170,184,20480\nv_mfma_f32_16x16x32_bf16 v[108:111], v[244:247], v[208:211], v[108:111]\n"
"v_mul_f32 v236, v46, v46\nv_mul_f32 v237, v47, v47\nM 120,180,244\nv_fma_f32 v234, v234, v215, v214\nv_fma_f32 v235, v235, v215, v214\nv_fma_f32 v236, v236, v215, v214\n"
"M 136,176,232\nR 172,186,16384\nv_mfma_f32_16x16x32_bf16 v[112:115], v[244:247], v[210:213], v[112:115]\nv_fma_f32 v237, v237, v215, v214\nv_mul_f32 v234, v44, v234\nM 136,180,248\n"
"v_exp_f32 v234, v234\nv_mul_f32 v235, v45, v235\nM 152,176,236\nR 174,186,20480\nv_mfma_f32_16x16x32_bf16 v[116:119], v[238:241], v[210:213], v[116:119]\nv_mul_f32 v236, v46, v236\n"
"v_mul_f32 v237, v47, v237\nM 152,180,252\nG 4\nv_exp_f32 v235, v235\nv_exp_f32 v236, v236\nM 156,160,236\n"
"R 176,185,16384\nv_mfma_f32_16x16x32_bf16 v[120:123], v[240:243], v[210:213], v[120:123]\nv_fma_f32 v234, v234, v216, v216\nv_exp_f32 v237, v237\nM 156,164,252\nbuffer_load_dwordx4 v223, s[12:15], s63 offen offset:1024 lds\n"
"v_fma_f32 v235, v235, v216, v216\nM 140,160,232\nR 178,185,20480\nv_mfma_f32_16x16x32_bf16 v[124:127], v[242:245], v[210:213], v[124:127]\nv_fma_f32 v236, v236, v216, v216\nv_fma_f32 v237, v237, v216, v216\n"
"M 140,164,248\nv_rcp_f32 v234, v234\nv_rcp_f32 v235, v235\nM 124,160,228\nR 180,187,16384\nv_mul_f32 v234, v44, v234\n"
"v_rcp_f32 v236, v236\nM 124,164,244\nv_rcp_f32 v237, v237\nv_mul_f32 v235, v45, v235\nM 108,160,224\nR 182,187,20480\n"
"v_mul_f32 v236, v46, v236\nv_mul_f32 v237, v47, v237\nM 108,164,240\ns_waitcnt vmcnt(11)\ns_barrier\nG 4\n"
"v_cvt_pk_bf16_f32 v42, v234, v235\nv_mfma_f32_16x16x32_bf16 v[0:3], v[168:171], a[0:3], 0\nR 160,184,24576\nv_cvt_pk_bf16_f32 v43, v236, v237\nv_mul_f32 v230, v48, v48\nM 0,172,16\n"
"s_mov_b32 m0, s51\nv_mul_f32 v231, v49, v49\nv_mul_f32 v232, v50, v50\nv_mul_f32 v233, v51, v51\nv_mfma_f32_16x16x32_bf16 v[16:19], v[168:171], a[4:7], 0\nR 162,184,28672\n"
"v_fma_f32 v230, v230, v218, v217\nv_fma_f32 v231, v231, v218, v217\nM 16,172,20\nbuffer_store_dwordx4 v[40:43], v252, s[20:23], s84 offen offset:1024 sc1\nv_fma_f32 v232, v232, v218, v217\nv_mfma_f32_16x16x32_bf16 v[8:11], v[168:171], a[8:11], 0\n"
"R 164,186,24576\nv_fma_f32 v233, v233, v218, v217\nv_mul_f32 v230, v48, v230\nM 8,172,24\nds_write_addtid_b32 v128 offset:0\nv_exp_f32 v230, v230\n"
"v_mul_f32 v231, v49, v231\nv_mfma_f32_16x16x32_bf16 v[24:27], v[168:171], a[12:15], 0\nR 166,186,28672\nds_write_addtid_b32 v129 offset:256\nv_mul_f32 v232, v50, v232\nv_mul_f32 v233, v51, v233\n"
"M 24,172,28\nds_write_addtid_b32 v130 offset:512\nG 7\nv_exp_f32 v231, v231\nv_exp_f32 v232, v232\nv_mfma_f32_16x16x32_bf16 v[28:31], v[176:179], a[12:15], 0\n"
"R 168,185,24576\nds_write_addtid_b32 v131 offset:768\nv_fma_f32 v230, v230, v219, v219\nv_exp_f32 v233, v233\nM 28,180,28\nds_write_addtid_b32 v132 offset:1024\n"
"v_rcp_f32 v230, v230\nv_fma_f32 v231, v231, v219, v219\nv_mfma_f32_16x16x32_bf16 v[12:15], v[176:179], a[8:11], 0\nR 170,185,28672\nds_write_addtid_b32 v133 offset:1280\nv_fma_f32 v232, v232, v219, v219\n"
"v_fma_f32 v233, v233, v219, v219\nM 12,180,24\nds_write_addtid_b32 v134 offset:1536\nv_rcp_f32 v231, v231\nv_rcp_f32 v232, v232\nv_mfma_f32_16x16x32_bf16 v[20:23], v[176:179], a[4:7], 0\n"
"R 172,187,24576\nds_write_addtid_b32 v135 offset:1792\nv_mul_f32 v230, v48, v230\nv_rcp_f32 v233, v233\nM 20,180,20\nds_write_addtid_b32 v136 offset:2048\n"
"v_mul_f32 v231, v49, v231\nv_mul_f32 v232, v50, v232\nv_mul_f32 v233, v51, v233\nv_mfma_f32_16x16x32_bf16 v[4:7], v[176:179], a[0:3], 0\nR 174,187,28672\nds_write_addtid_b32 v137 offset:2304\n"
"v_cvt_pk_bf16_f32 v48, v230, v231\nv_cvt_pk_bf16_f32 v49, v232, v233\nM 4,180,16\nds_write_addtid_b32 v138 offset:2560\nG 14\nv_mul_f32 v234, v52, v52\n"
"v_mul_f32 v235, v53, v53\nv_mul_f32 v236, v54, v54\nM 0,160,32\nR 176,184,32768\nds_write_addtid_b32 v139 offset:2816\nv_mul_f32 v237, v55, v55\n"
"v_fma_f32 v234, v234, v218, v217\nM 0,164,48\nds_write_addtid_b32 v140 offset:3072\nv_fma_f32 v235, v235, v218, v217\nv_fma_f32 v236, v236, v218, v217\nv_fma_f32 v237, v237, v218, v217\n"
"M 16,160,36\nR 178,184,36864\nds_write_addtid_b32 v141 offset:3328\nv_mul_f32 v234, v52, v234\nv_mul_f32 v235, v53, v235\nM 16,164,52\n"
"ds_write_addtid_b32 v142 offset:3584\nv_exp_f32 v234, v234\nv_mul_f32 v236, v54, v236\nM 8,160,40\nR 180,186,32768\nds_write_addtid_b32 v143 offset:3840\n"
"v_mul_f32 v237, v55, v237\nv_exp_f32 v235, v235\nM 8,164,56\nds_write_addtid_b32 v144 offset:4096\nv_exp_f32 v236, v236\nv_exp_f32 v237, v237\n"
"M 24,160,44\nR 182,186,36864\nds_write_addtid_b32 v145 offset:4352\nv_fma_f32 v234, v234, v219, v219\nv_fma_f32 v235, v235, v219, v219\nM 24,164,60\n"
"ds_write_addtid_b32 v146 offset:4608\nG 14\nv_rcp_f32 v234, v234\nv_fma_f32 v236, v236, v219, v219\nM 28,168,44\nR 160,185,32768\n"
"ds_write_addtid_b32 v147 offset:4864\nv_fma_f32 v237, v237, v219, v219\nv_rcp_f32 v235, v235\nM 28,172,60\nds_write_addtid_b32 v148 offset:5120\nds_write_addtid_b32 v149 offset:5376\n"
"ds_write_addtid_b32 v150 offset:5632\nds_write_addtid_b32 v151 offset:5888\nds_write_addtid_b32 v152 offset:6144\nds_write_addtid_b32 v153 offset:6400\nds_write_addtid_b32 v154 offset:6656\nds_write_addtid_b32 v155 offset:6912\n"
"ds_write_addtid_b32 v156 offset:7168\nds_write_addtid_b32 v157 offset:7424\nds_write_addtid_b32 v158 offset:7680\nds_write_addtid_b32 v159 offset:7936\nv_rcp_f32 v236, v236\nv_rcp_f32 v237, v237\n"
"M 12,168,40\nR 162,185,36864\ns_add_u32 m0, s50, 81920\ns_add_u32 s63, s54, 256\nv_mul_f32 v234, v52, v234\nM 12,172,56\n"
"buffer_load_dwordx4 v220, s[12:15], s63 offen lds\nv_mul_f32 v235, v53, v235\nM 20,168,36\nR 164,187,32768\ns_waitcnt vmcnt(4) lgkmcnt(2)\ns_barrier\n"
"v_mul_f32 v236, v54, v236\nM 20,172,52\nds_read2st64_b32 v[128:129], v225 offset0:0 offset1:1\nv_mul_f32 v237, v55, v237\nv_cvt_pk_bf16_f32 v50, v234, v235\nv_cvt_pk_bf16_f32 v51, v236, v237\n"
"M 4,168,32\nR 166,187,36864\nds_read2st64_b32 v[130:131], v225 offset0:2 offset1:3\nv_mul_f32 v230, v56, v56\nv_mul_f32 v231, v57, v57\nM 4,172,48\n"
"ds_read2st64_b32 v[132:133], v225 offset0:4 offset1:5\nv_mul_f32 v232, v58, v58\nv_mul_f32 v233, v59, v59\nv_fma_f32 v230, v230, v218, v217\nM 0,176,64\nR 168,184,40960\n"
"ds_read2st64_b32 v[134:135], v225 offset0:6 offset1:7\nv_fma_f32 v231, v231, v218, v217\nv_fma_f32 v232, v232, v218, v217\nM 0,180,80\nbuffer_load_dwordx4 v221, s[12:15], s63 offen offset:1024 lds\nds_read2st64_b32 v[136:137], v225 offset0:8 offset1:9\n"
"v_fma_f32 v233, v233, v218, v217\nM 16,176,68\nR 170,184,45056\ns_add_u32 m0, s50, 90112\ns_add_u32 s63, s54, 229632\nds_read2st64_b32 v[138:139], v225 offset0:10 offset1:11\n"
"v_mul_f32 v230, v56, v230\nM 16,180,84\nbuffer_load_dwordx4 v220, s[12:15], s63 offen lds\nds_read2st64_b32 v[140:141], v225 offset0:12 offset1:13\nv_mul_f32 v231, v57, v231\nM 8,176,72\n"
"R 172,186,40960\nds_read2st64_b32 v[142:143], v225 offset0:14 offset1:15\nv_mul_f32 v232, v58, v232\nv_mul_f32 v233, v59, v233\nM 8,180,88\nbuffer_store_dwordx4 v[48:51], v252, s[20:23], s85 offen offset:0 sc1\n"
"ds_read2st64_b32 v[144:145], v225 offset0:16 offset1:17\nv_exp_f32 v230, v230\nM 24,176,76\nR 174,186,45056\nds_read2st64_b32 v[146:147], v225 offset0:18 offset1:19\nv_fma_f32 v230, v230, v219, v219\n"
"v_exp_f32 v231, v231\nM 24,180,92\nds_read2st64_b32 v[148:149], v225 offset0:20 offset1:21\nG 14\nv_exp_f32 v232, v232\nv_exp_f32 v233, v233\n"
"M 28,160,76\nR 176,185,40960\nds_read2st64_b32 v[150:151], v225 offset0:22 offset1:23\nv_fma_f32 v231, v231, v219, v219\nv_fma_f32 v232, v232, v219, v219\nM 28,164,92\n"
"buffer_load_dwordx4 v221, s[12:15], s63 offen offset:1024 lds\nds_read2st64_b32 v[152:153], v225 offset0:24 offset1:25\nv_fma_f32 v233, v233, v219, v219\nM 12,160,72\nR 178,185,45056\ns_add_u32 s63, s54, 459008\n"
"ds_read2st64_b32 v[154:155], v225 offset0:26 offset1:27\nv_rcp_f32 v230, v230\nM 12,164,88\ns_add_u32 m0, s50, 98304\nds_read2st64_b32 v[156:157], v225 offset0:28 offset1:29\nv_rcp_f32 v231, v231\n"
"v_rcp_f32 v232, v232\nM 20,160,68\nR 180,187,40960\nds_read2st64_b32 v[158:159], v225 offset0:30 offset1:31\nv_mul_f32 v230, v56, v230\nv_rcp_f32 v233, v233\n"
"M 20,164,84\nbuffer_load_dwordx4 v220, s[12:15], s63 offen lds\nv_add_f32 v96, v96, v128\nv_mul_f32 v231, v57, v231\nM 4,160,64\nR 182,187,45056\n"
"v_mul_f32 v232, v58, v232\nv_add_f32 v97, v97, v129\nv_mul_f32 v233, v59, v233\nM 4,164,80\nG 11\nv_cvt_pk_bf16_f32 v56, v230, v231\n"
"v_cvt_pk_bf16_f32 v57, v232, v233\nv_add_f32 v98, v98, v130\nv_mul_f32 v234, v60, v60\nM 0,168,96\nR 160,184,49152\nv_mul_f32 v235, v61, v61\n"
"v_add_f32 v99, v99, v131\nv_mul_f32 v236, v62, v62\nM 0,172,112\nbuffer_load_dwordx4 v221, s[12:15], s63 offen offset:1024 lds\nv_add_f32 v100, v100, v132\nv_mul_f32 v237, v63, v63\n"
"M 16,168,100\nR 162,184,53248\ns_add_u32 s63, s54, 688384\nv_add_f32 v101, v101, v133\nv_fma_f32 v234, v234, v218, v217\nM 16,172,116\n"
"s_add_u32 m0, s50, 106496\nv_fma_f32 v235, v235, v218, v217\nv_fma_f32 v236, v236, v218, v217\nv_add_f32 v102, v102, v134\nv_fma_f32 v237, v237, v218, v217\nM 8,168,104\n"
"R 164,186,49152\nv_mul_f32 v234, v60, v234\nv_add_f32 v103, v103, v135\nv_mul_f32 v235, v61, v235\nM 8,172,120\nbuffer_load_dwordx4 v220, s[12:15], s63 offen lds\n"
"v_add_f32 v104, v104, v136\nv_mul_f32 v236, v62, v236\nM 24,168,108\nR 166,186,53248\nv_mul_f32 v237, v63, v237\nv_add_f32 v105, v105, v137\n"
"v_exp_f32 v234, v234\nM 24,172,124\nG 4\nv_add_f32 v106, v106, v138\nv_add_f32 v107, v107, v139\nv_add_f32 v108, v108, v140\n"
"v_exp_f32 v235, v235\nM 28,176,108\nR 168,185,49152\nv_add_f32 v109, v109, v141\nv_add_f32 v110, v110, v142\nv_exp_f32 v236, v236\n"
"M 28,180,124\nv_add_f32 v111, v111, v143\nv_add_f32 v112, v112, v144\nv_add_f32 v113, v113, v145\nv_exp_f32 v237, v237\nM 12,176,104\n"
"R 170,185,53248\nv_add_f32 v114, v114, v146\nv_add_f32 v115, v115, v147\nv_fma_f32 v234, v234, v219, v219\nM 12,180,120\nbuffer_load_dwordx4 v221, s[12:15], s63 offen offset:1024 lds\n"
"v_add_f32 v116, v116, v148\nv_fma_f32 v235, v235, v219, v219\nM 20,176,100\nR 172,187,49152\ns_add_u32 s63, s54, 917760\nv_add_f32 v117, v117, v149\n"
"M 20,180,116\ns_add_u32 m0, s50, 114688\nv_add_f32 v118, v118, v150\nv_add_f32 v119, v119, v151\nv_add_f32 v120, v120, v152\nv_add_f32 v121, v121, v153\n"
"v_add_f32 v122, v122, v154\nv_add_f32 v123, v123, v155\nv_add_f32 v124, v124, v156\nv_add_f32 v125, v125, v157\nv_add_f32 v126, v126, v158\nv_add_f32 v127, v127, v159\n"
"M 4,176,96\nR 174,187,53248\nv_fma_f32 v236, v236, v219, v219\nM 4,180,112\nG 4\nv_rcp_f32 v234, v234\n"
"M 0,160,128\nR 176,184,57344\nv_fma_f32 v237, v237, v219, v219\nM 0,164,144\nbuffer_load_dwordx4 v220, s[12:15], s63 offen lds\nM 16,160,132\n"
"R 178,184,61440\nv_mul_f32 v234, v60, v234\nM 16,164,148\nv_rcp_f32 v235, v235\nM 8,160,136\nR 180,186,57344\n"
"Lq1036:\ns_lshr_b32 s60, s45, 3\ns_lshl2_add_u32 s60, s60, s40\ns_lshl_b32 s60, s60, 8\nv_lshl_add_u32 v224, v252, 3, s60\nv_add_u32 v224, Lq1036+4096-LqPC, v224\n"
"v_min_u32 v224, LqE-LqPC-4, v224\ns_mov_b64 exec, 3\nglobal_load_dword v224, v224, s[98:99]\ns_mov_b64 exec, -1\nM 8,164,152\nv_rcp_f32 v236, v236\n"
"M 24,160,140\nR 182,186,61440\nv_mul_f32 v235, v61, v235\nM 24,164,156\nG 4\nv_rcp_f32 v237, v237\n"
"M 28,168,140\nR 160,185,57344\nv_mul_f32 v236, v62, v236\nM 28,172,156\nbuffer_load_dwordx4 v221, s[12:15], s63 offen offset:1024 lds\nM 12,168,136\n"
"R 162,185,61440\ns_add_u32 s63, s54, 1147136\nM 12,172,152\ns_add_u32 m0, s50, 0\nv_mul_f32 v237, v63, v237\nv_cvt_pk_bf16_f32 v58, v234, v235\n"
"M 20,168,132\nR 164,187,57344\nv_cvt_pk_bf16_f32 v59, v236, v237\nM 20,172,148\nbuffer_load_dwordx4 v220, s[12:15], s63 offen lds\nM 4,168,128\n"
"R 166,187,61440\nM 4,172,144\nG 4\nM 0,176,160\nR 168,188,0\nM 0,180,176\n"
"buffer_load_dwordx4 v221, s[12:15], s63 offen offset:1024 lds\nM 16,176,164\nR 170,188,4096\ns_add_u32 s63, s54, 1376512\nM 16,180,180\ns_add_u32 m0, s50, 8192\n"
"M 8,176,168\nLpg27:\ns_cbranch_execz Lpg31\nR 172,190,0\nM 8,180,184\nbuffer_load_dwordx4 v220, s[12:15], s63 offen lds\n"
"M 24,176,172\nR 174,190,4096\nM 24,180,188\nbuffer_store_dwordx4 v[56:59], v252, s[20:23], s85 offen offset:1024 sc1\nG 4\nM 28,160,172\n"
"R 176,189,0\ns_waitcnt vmcnt(11)\ns_barrier\nv_mul_f32 v230, v96, v96\nM 28,164,188\ns_add_u32 s84, s73, 37750784\n"
"s_add_u32 s85, s73, 37865472\nv_mul_f32 v231, v97, v97\nv_mul_f32 v232, v98, v98\nM 12,160,168\nR 178,189,4096\nv_mul_f32 v233, v99, v99\n"
"v_fma_f32 v230, v230, v215, v214\nM 12,164,184\nbuffer_load_dwordx4 v221, s[12:15], s63 offen offset:1024 lds\nv_fma_f32 v231, v231, v215, v214\nM 20,160,164\nR 180,191,0\n"
"v_fma_f32 v232, v232, v215, v214\nv_fma_f32 v233, v233, v215, v214\nM 20,164,180\ns_add_u32 m0, s50, 16384\nv_mul_f32 v230, v96, v230\nv_mul_f32 v231, v97, v231\n"
"v_mul_f32 v232, v98, v232\nM 4,160,160\nR 182,191,4096\ns_mov_b32 s60, 158720\nv_bfe_u32 v242, v252, 4, 4\nv_lshl_add_u32 v242, v242, 3, s60\n"
"v_mul_f32 v233, v99, v233\nM 4,164,176\nds_read_b64 v[238:239], v242 offset:0\nG 5\nv_exp_f32 v230, v230\nM 0,168,192\n"
"R 160,188,8192\nds_read_b64 v[240:241], v242 offset:128\nv_exp_f32 v231, v231\nM 0,172,208\nds_read_b64 v[242:243], v242 offset:0\ns_add_u32 s63, s54, 1605888\n"
"v_exp_f32 v232, v232\nM 16,168,196\nR 162,188,12288\nv_fma_f32 v230, v230, v216, v216\nv_exp_f32 v233, v233\nM 16,172,212\n"
"buffer_load_dwordx4 v220, s[12:15], s63 offen lds\nv_fma_f32 v231, v231, v216, v216\nM 8,168,200\nR 164,190,8192\nv_fma_f32 v232, v232, v216, v216\nv_fma_f32 v233, v233, v216, v216\n"
"M 8,172,216\nv_rcp_f32 v230, v230\nv_rcp_f32 v231, v231\nM 24,168,204\nR 166,190,12288\nv_mul_f32 v230, v96, v230\n"
"v_rcp_f32 v232, v232\nM 24,172,220\nG 7\nv_rcp_f32 v233, v233\nv_mul_f32 v231, v97, v231\nM 28,176,204\n"
"R 168,189,8192\nv_mul_f32 v232, v98, v232\nv_mul_f32 v233, v99, v233\nM 28,180,220\nbuffer_load_dwordx4 v221, s[12:15], s63 offen offset:1024 lds\nv_cvt_pk_bf16_f32 v96, v230, v231\n"
"M 12,176,200\nR 170,189,12288\ns_add_u32 s63, s54, 1835264\nv_cvt_pk_bf16_f32 v97, v232, v233\nM 12,180,216\ns_add_u32 m0, s50, 24576\n"
"v_mul_f32 v234, v100, v100\nv_mul_f32 v235, v101, v101\nv_mul_f32 v236, v102, v102\nM 20,176,196\nR 172,191,8192\nG 7\n"
"v_mfma_f32_16x16x32_bf16 v[0:3], v[238:241], v[208:211], v[0:3]\nv_mul_f32 v237, v103, v103\nv_fma_f32 v234, v234, v215, v214\nM 20,180,212\nbuffer_load_dwordx4 v220, s[12:15], s63 offen lds\nv_fma_f32 v235, v235, v215, v214\n"
"M 4,176,192\nR 174,191,12288\nG 7\nv_mfma_f32_16x16x32_bf16 v[4:7], v[240:243], v[208:211], v[4:7]\nv_fma_f32 v236, v236, v215, v214\nv_fma_f32 v237, v237, v215, v214\n"
"M 4,180,208\nG 4\nv_mul_f32 v234, v100, v234\nv_mul_f32 v235, v101, v235\nv_mul_f32 v236, v102, v236\nM 0,160,224\n"
"R 176,188,16384\nv_mfma_f32_16x16x32_bf16 v[16:19], v[240:243], v[210:213], v[16:19]\nv_mul_f32 v237, v103, v237\nv_exp_f32 v234, v234\nM 0,164,240\nbuffer_load_dwordx4 v221, s[12:15], s63 offen offset:1024 lds\n"
"v_exp_f32 v235, v235\nM 16,160,228\nR 178,188,20480\nv_mfma_f32_16x16x32_bf16 v[20:23], v[238:241], v[210:213], v[20:23]\ns_add_u32 m0, s50, 32768\ns_add_u32 s63, s54, 2064640\n"
"v_exp_f32 v236, v236\nM 16,164,244\nbuffer_load_dwordx4 v220, s[12:15], s63 offen lds\nv_exp_f32 v237, v237\nM 8,160,232\nR 180,188,24576\n"
"v_fma_f32 v234, v234, v216, v216\nv_fma_f32 v235, v235, v216, v216\nM 8,164,248\nv_rcp_f32 v234, v234\nv_fma_f32 v236, v236, v216, v216\nM 24,160,236\n"
"R 182,188,28672\nv_fma_f32 v237, v237, v216, v216\nv_rcp_f32 v235, v235\nM 24,164,252\nG 4\nv_rcp_f32 v236, v236\n"
"v_rcp_f32 v237, v237\nM 28,168,236\nR 160,189,16384\nv_mul_f32 v234, v100, v234\nv_mul_f32 v235, v101, v235\nM 28,172,252\n"
"buffer_load_dwordx4 v221, s[12:15], s63 offen offset:1024 lds\nv_mul_f32 v236, v102, v236\nM 12,168,232\nR 162,189,20480\ns_add_u32 s63, s54, 2294016\nv_mul_f32 v237, v103, v237\n"
"M 12,172,248\ns_add_u32 m0, s50, 40960\nv_cvt_pk_bf16_f32 v98, v234, v235\nv_cvt_pk_bf16_f32 v99, v236, v237\nv_mul_f32 v230, v104, v104\nM 20,168,228\n"
"R 164,189,24576\nv_mul_f32 v231, v105, v105\nv_mul_f32 v232, v106, v106\nM 20,172,244\nbuffer_load_dwordx4 v220, s[12:15], s63 offen lds\nv_mul_f32 v233, v107, v107\n"
"M 4,168,224\nR 166,189,28672\nv_fma_f32 v230, v230, v215, v214\nv_fma_f32 v231, v231, v215, v214\nM 4,172,240\ns_waitcnt vmcnt(17)\n"
"s_barrier\nG 4\nv_fma_f32 v232, v232, v215, v214\nv_mfma_f32_16x16x32_bf16 v[32:35], v[176:179], a[0:3], 0\nR 168,190,16384\nv_fma_f32 v233, v233, v215, v214\n"
"v_mul_f32 v230, v104, v230\nM 32,180,16\ns_mov_b32 m0, s51\nv_exp_f32 v230, v230\nv_mul_f32 v231, v105, v231\nv_mfma_f32_16x16x32_bf16 v[48:51], v[176:179], a[4:7], 0\n"
"R 170,190,20480\nds_write_addtid_b32 v8 offset:0\nv_mul_f32 v232, v106, v232\nv_mul_f32 v233, v107, v233\nM 48,180,20\nbuffer_store_dwordx4 v[96:99], v252, s[20:23], s84 offen offset:0 sc1\n"
"ds_write_addtid_b32 v9 offset:256\nv_exp_f32 v231, v231\nv_mfma_f32_16x16x32_bf16 v[64:67], v[176:179], a[8:11], 0\nR 172,190,24576\nds_write_addtid_b32 v10 offset:512\nv_mov_b32 v226, s46\n"
"v_exp_f32 v232, v232\nM 64,180,24\nds_write_addtid_b32 v11 offset:768\nv_mov_b32 v227, s47\nv_fma_f32 v230, v230, v216, v216\nv_exp_f32 v233, v233\n"
"v_mfma_f32_16x16x32_bf16 v[80:83], v[176:179], a[12:15], 0\nR 174,190,28672\ns_add_u32 s61, s81, 845824\ns_lshl_b32 s62, s44, 5\ns_add_u32 s61, s61, s62\ns_waitcnt vmcnt(9)\n"
"s_mov_b64 exec, 1\nbuffer_atomic_swap_x2 v[226:227], off, s[20:23], s61 sc1\ns_mov_b64 exec, -1\nds_write_addtid_b32 v12 offset:1024\nv_fma_f32 v231, v231, v216, v216\nM 80,180,28\n"
"ds_write_addtid_b32 v13 offset:1280\nG 10\nv_rcp_f32 v230, v230\nv_fma_f32 v232, v232, v216, v216\nv_mfma_f32_16x16x32_bf16 v[84:87], v[160:163], a[12:15], 0\nR 176,191,16384\n"
"ds_write_addtid_b32 v14 offset:1536\nv_fma_f32 v233, v233, v216, v216\nv_rcp_f32 v231, v231\nM 84,164,28\nds_write_addtid_b32 v15 offset:1792\nv_rcp_f32 v232, v232\n"
"v_rcp_f32 v233, v233\nv_mfma_f32_16x16x32_bf16 v[68:71], v[160:163], a[8:11], 0\nR 178,191,20480\nds_write_addtid_b32 v24 offset:4096\nv_mul_f32 v230, v104, v230\nv_mul_f32 v231, v105, v231\n"
"M 68,164,24\nds_write_addtid_b32 v25 offset:4352\nv_mul_f32 v232, v106, v232\nv_mul_f32 v233, v107, v233\nv_cvt_pk_bf16_f32 v104, v230, v231\nv_mfma_f32_16x16x32_bf16 v[52:55], v[160:163], a[4:7], 0\n"
"R 180,191,24576\nds_write_addtid_b32 v26 offset:4608\nv_cvt_pk_bf16_f32 v105, v232, v233\nv_mul_f32 v234, v108, v108\nM 52,164,20\nds_write_addtid_b32 v27 offset:4864\n"
"ds_write_addtid_b32 v28 offset:5120\nds_write_addtid_b32 v29 offset:5376\nds_write_addtid_b32 v30 offset:5632\nds_write_addtid_b32 v31 offset:5888\nv_mul_f32 v235, v109, v109\nv_mul_f32 v236, v110, v110\n"
"v_mul_f32 v237, v111, v111\nv_mfma_f32_16x16x32_bf16 v[36:39], v[160:163], a[0:3], 0\nR 182,191,28672\ns_add_u32 m0, s50, 40960\nv_fma_f32 v234, v234, v215, v214\nM 36,164,16\n"
"G 15\nv_fma_f32 v235, v235, v215, v214\nv_fma_f32 v236, v236, v215, v214\nv_fma_f32 v237, v237, v215, v214\nv_mfma_f32_16x16x32_bf16 v[40:43], v[168:171], a[0:3], 0\nR 160,188,32768\n"
"s_waitcnt vmcnt(9) lgkmcnt(2)\ns_barrier\nv_mul_f32 v234, v108, v234\nM 40,172,16\nbuffer_load_dwordx4 v221, s[12:15], s63 offen offset:1024 lds\nds_read2st64_b32 v[8:9], v225 offset0:0 offset1:1\n"
"v_mul_f32 v235, v109, v235\nv_mfma_f32_16x16x32_bf16 v[56:59], v[168:171], a[4:7], 0\nR 162,188,36864\nds_read2st64_b32 v[10:11], v225 offset0:2 offset1:3\nv_mul_f32 v236, v110, v236\nv_mul_f32 v237, v111, v237\n"
"M 56,172,20\ns_add_u32 m0, s50, 49152\nds_read2st64_b32 v[12:13], v225 offset0:4 offset1:5\nv_exp_f32 v234, v234\nv_exp_f32 v235, v235\nv_mfma_f32_16x16x32_bf16 v[72:75], v[168:171], a[8:11], 0\n"
"R 164,188,40960\nds_read2st64_b32 v[14:15], v225 offset0:6 offset1:7\nv_fma_f32 v234, v234, v216, v216\nv_exp_f32 v236, v236\nM 72,172,24\ns_add_u32 s63, s54, 2523392\n"
"ds_read2st64_b32 v[24:25], v225 offset0:16 offset1:17\nv_exp_f32 v237, v237\nv_fma_f32 v235, v235, v216, v216\nv_mfma_f32_16x16x32_bf16 v[88:91], v[168:171], a[12:15], 0\nR 166,188,45056\nds_read2st64_b32 v[26:27], v225 offset0:18 offset1:19\n"
"v_fma_f32 v236, v236, v216, v216\nv_fma_f32 v237, v237, v216, v216\nM 88,172,28\nds_read2st64_b32 v[28:29], v225 offset0:20 offset1:21\nG 11\nv_rcp_f32 v234, v234\n"
"v_rcp_f32 v235, v235\nv_mfma_f32_16x16x32_bf16 v[92:95], v[176:179], a[12:15], 0\nR 168,189,32768\nds_read2st64_b32 v[30:31], v225 offset0:22 offset1:23\nv_mul_f32 v234, v108, v234\nv_rcp_f32 v236, v236\n"
"M 92,180,28\nbuffer_load_dwordx4 v220, s[12:15], s63 offen lds\nG 11\nv_add_f32 v0, v0, v8\nv_rcp_f32 v237, v237\nv_mfma_f32_16x16x32_bf16 v[76:79], v[176:179], a[8:11], 0\n"
"R 170,189,36864\nv_mul_f32 v235, v109, v235\nv_add_f32 v1, v1, v9\nv_mul_f32 v236, v110, v236\nM 76,180,24\nv_mul_f32 v237, v111, v237\n"
"v_cvt_pk_bf16_f32 v106, v234, v235\nG 10\nv_add_f32 v2, v2, v10\nv_cvt_pk_bf16_f32 v107, v236, v237\nv_mfma_f32_16x16x32_bf16 v[60:63], v[176:179], a[4:7], 0\nR 172,189,40960\n"
"v_mul_f32 v230, v112, v112\nv_add_f32 v3, v3, v11\nv_mul_f32 v231, v113, v113\nM 60,180,20\nbuffer_store_dwordx4 v[104:107], v252, s[20:23], s84 offen offset:1024 sc1\nG 10\n"
"v_add_f32 v4, v4, v12\nv_mul_f32 v232, v114, v114\nv_mfma_f32_16x16x32_bf16 v[44:47], v[176:179], a[0:3], 0\nR 174,189,45056\nv_mul_f32 v233, v115, v115\nv_add_f32 v5, v5, v13\n"
"v_fma_f32 v230, v230, v218, v217\nM 44,180,16\nG 7\nv_fma_f32 v231, v231, v218, v217\nv_fma_f32 v232, v232, v218, v217\nv_add_f32 v6, v6, v14\n"
"v_fma_f32 v233, v233, v218, v217\nM 32,160,32\nR 176,190,32768\nv_mov_b32 v248, 0\nv_add_f32 v7, v7, v15\nv_mul_f32 v230, v112, v230\n"
"M 32,164,48\nv_mov_b32 v249, 0\nv_mul_f32 v231, v113, v231\nv_add_f32 v16, v16, v24\nv_mul_f32 v232, v114, v232\nM 48,160,36\n"
"R 178,190,36864\ns_add_u32 s61, s82, 845824\ns_mov_b64 exec, 0xf\nbuffer_atomic_or_x2 v[248:249], v250, s[20:23], s61 offen sc0 sc1\ns_mov_b64 exec, -1\nv_add_f32 v17, v17, v25\n"
"v_mul_f32 v233, v115, v233\nM 48,164,52\nv_exp_f32 v230, v230\nG 8\nv_add_f32 v18, v18, v26\nv_exp_f32 v231, v231\n"
"M 64,160,40\nR 180,190,40960\nv_add_f32 v19, v19, v27\nG 8\nv_add_f32 v20, v20, v28\nv_add_f32 v21, v21, v29\n"
"G 6\nv_add_f32 v22, v22, v30\nv_add_f32 v23, v23, v31\nM 64,164,56\nbuffer_load_dwordx4 v221, s[12:15], s63 offen offset:1024 lds\nM 80,160,44\n"
"R 182,190,45056\nv_fma_f32 v230, v230, v219, v219\nM 80,164,60\nG 4\nv_exp_f32 v232, v232\nM 84,168,44\n"
"R 160,191,32768\nv_fma_f32 v231, v231, v219, v219\nM 84,172,60\ns_add_u32 m0, s50, 57344\nv_exp_f32 v233, v233\nM 68,168,40\n"
"R 162,191,36864\nv_fma_f32 v232, v232, v219, v219\nM 68,172,56\ns_add_u32 s63, s54, 2752768\nv_rcp_f32 v230, v230\nM 52,168,36\n"
"R 164,191,40960\nv_fma_f32 v233, v233, v219, v219\nM 52,172,52\nbuffer_load_dwordx4 v220, s[12:15], s63 offen lds\nM 36,168,32\nR 166,191,45056\n"
"v_mul_f32 v230, v112, v230\nM 36,172,48\nG 4\nv_rcp_f32 v231, v231\nM 40,176,32\nR 168,188,49152\n"
"v_mul_f32 v231, v113, v231\nM 40,180,48\nv_rcp_f32 v232, v232\nM 56,176,36\nR 170,188,53248\nv_mul_f32 v232, v114, v232\n"
"M 56,180,52\nbuffer_load_dwordx4 v221, s[12:15], s63 offen offset:1024 lds\nM 72,176,40\nR 172,184,0\ns_add_u32 s63, s54, 2982144\nM 72,180,56\n"
"s_add_u32 m0, s50, 65536\nv_rcp_f32 v233, v233\nM 88,176,44\nR 174,184,4096\nv_mul_f32 v233, v115, v233\nM 88,180,60\n"
"G 4\nv_cvt_pk_bf16_f32 v112, v230, v231\nv_cvt_pk_bf16_f32 v113, v232, v233\nM 92,160,44\nR 176,189,49152\nv_mul_f32 v234, v116, v116\n"
"M 92,164,60\nbuffer_load_dwordx4 v220, s[12:15], s63 offen lds\nM 76,160,40\nR 178,189,53248\nv_mul_f32 v235, v117, v117\nM 76,164,56\n"
"v_mul_f32 v236, v118, v118\nv_mul_f32 v237, v119, v119\nM 60,160,36\nR 180,185,0\nv_fma_f32 v234, v234, v218, v217\nM 60,164,52\n"
"v_fma_f32 v235, v235, v218, v217\nv_fma_f32 v236, v236, v218, v217\nM 44,160,32\nR 182,185,4096\nv_fma_f32 v237, v237, v218, v217\nM 44,164,48\n"
"G 4\nv_mul_f32 v234, v116, v234\nv_mul_f32 v235, v117, v235\nM 32,168,64\nR 160,190,49152\nv_mul_f32 v236, v118, v236\n"
"M 32,172,80\nbuffer_load_dwordx4 v221, s[12:15], s63 offen offset:1024 lds\nM 48,168,68\nR 162,190,53248\nv_mul_f32 v237, v119, v237\nM 48,172,84\n"
"s_add_u32 m0, s50, 73728\nv_exp_f32 v234, v234\nM 64,168,72\nR 164,186,0\nv_fma_f32 v234, v234, v219, v219\nM 64,172,88\n"
"s_add_u32 s63, s54, 3211520\nv_exp_f32 v235, v235\nM 80,168,76\nR 166,186,4096\nv_fma_f32 v235, v235, v219, v219\nM 80,172,92\n"
"G 4\nv_exp_f32 v236, v236\nM 84,176,76\nR 168,191,49152\nv_fma_f32 v236, v236, v219, v219\nM 84,180,92\n"
"buffer_load_dwordx4 v220, s[12:15], s63 offen lds\nM 68,176,72\nR 170,191,53248\nv_mul_f32 v230, v120, v120\nM 68,180,88\nv_exp_f32 v237, v237\n"
"M 52,176,68\nR 172,187,0\nv_fma_f32 v237, v237, v219, v219\nM 52,180,84\ns_add_u32 s64, s68, 37748736\nv_rcp_f32 v234, v234\n"
"M 36,176,64\nR 174,187,4096\nv_mul_f32 v234, v116, v234\nM 36,180,80\nG 4\nv_rcp_f32 v235, v235\n"
"M 40,160,64\nR 176,184,8192\ns_waitcnt vmcnt(13)\ns_barrier\ns_waitcnt vmcnt(6)\nv_cmp_eq_u64 s[86:87], s[46:47], v[248:249]\n"
"M 40,164,80\ns_add_u32 s65, s68, 37863424\nv_rcp_f32 v236, v236\nM 56,160,68\nR 178,184,12288\ns_and_b32 s62, s86, 0xf\n"
"s_cmp_eq_u32 s62, 0xf\ns_cbranch_scc0 Lq838\nLq839:\nM 56,164,84\nbuffer_load_dwordx4 v221, s[12:15], s63 offen offset:1024 lds\nM 72,160,72\n"
"R 180,184,16384\nv_mul_f32 v235, v117, v235\nM 72,164,88\nbuffer_load_dwordx4 a[0:3], v252, s[20:23], s64 offen sc1\nM 88,160,76\nR 182,184,20480\n"
"v_mul_f32 v236, v118, v236\nM 88,164,92\nG 4\nv_rcp_f32 v237, v237\nM 92,168,76\nR 160,185,8192\n"
"v_mul_f32 v237, v119, v237\nM 92,172,92\nbuffer_load_dwordx4 a[4:7], v252, s[20:23], s65 offen sc1\nM 76,168,72\nR 162,185,12288\nv_cvt_pk_bf16_f32 v114, v234, v235\n"
"M 76,172,88\ns_add_u32 s66, s69, 37748736\nv_cvt_pk_bf16_f32 v115, v236, v237\nv_mul_f32 v231, v121, v121\nM 60,168,68\nR 164,185,16384\n"
"v_mul_f32 v232, v122, v122\nM 60,172,84\ns_add_u32 s67, s69, 37863424\nv_mul_f32 v233, v123, v123\nv_fma_f32 v230, v230, v218, v217\nM 44,168,64\n"
"R 166,185,20480\nv_fma_f32 v231, v231, v218, v217\nM 44,172,80\nG 4\nv_fma_f32 v232, v232, v218, v217\nv_fma_f32 v233, v233, v218, v217\n"
"M 32,176,96\nR 168,186,8192\nv_mul_f32 v230, v120, v230\nM 32,180,112\nbuffer_load_dwordx4 a[8:11], v252, s[20:23], s66 offen sc1\nM 48,176,100\n"
"R 170,186,12288\nv_mul_f32 v231, v121, v231\nM 48,180,116\nbuffer_load_dwordx4 a[12:15], v252, s[20:23], s67 offen sc1\nM 64,176,104\nR 172,186,16384\n"
"v_mul_f32 v232, v122, v232\nM 64,180,120\ns_add_u32 s64, s68, 37755904\nv_exp_f32 v230, v230\nM 80,176,108\nR 174,186,20480\n"
"v_mul_f32 v233, v123, v233\nM 80,180,124\nG 4\nv_exp_f32 v231, v231\nM 84,160,108\nR 176,187,8192\n"
"v_fma_f32 v230, v230, v219, v219\nM 84,164,124\nbuffer_load_dwordx4 a[16:19], v252, s[20:23], s64 offen sc1\nM 68,160,104\nR 178,187,12288\nv_fma_f32 v231, v231, v219, v219\n"
"M 68,164,120\ns_add_u32 s65, s68, 37870592\nv_exp_f32 v232, v232\nM 52,160,100\nR 180,187,16384\nv_fma_f32 v232, v232, v219, v219\n"
"M 52,164,116\ns_add_u32 s66, s69, 37755904\nv_exp_f32 v233, v233\nM 36,160,96\nR 182,187,20480\nv_fma_f32 v233, v233, v219, v219\n"
"M 36,164,112\nG 4\nv_rcp_f32 v230, v230\nM 40,168,96\nR 160,184,24576\nv_mul_f32 v230, v120, v230\n"
"M 40,172,112\nbuffer_load_dwordx4 a[20:23], v252, s[20:23], s65 offen sc1\nM 56,168,100\nR 162,184,28672\nv_mul_f32 v234, v124, v124\nM 56,172,116\n"
"buffer_load_dwordx4 a[24:27], v252, s[20:23], s66 offen sc1\nM 72,168,104\nR 164,184,32768\nv_mul_f32 v235, v125, v125\nM 72,172,120\ns_add_u32 s67, s69, 37870592\n"
"v_rcp_f32 v231, v231\nM 88,168,108\nR 166,184,36864\nv_mul_f32 v231, v121, v231\nM 88,172,124\nG 4\n"
"v_rcp_f32 v232, v232\nM 92,176,108\nLpg28:\ns_cbranch_execz LretO1n\nR 168,185,24576\nv_mul_f32 v232, v122, v232\n"
"M 92,180,124\nbuffer_load_dwordx4 a[28:31], v252, s[20:23], s67 offen sc1\nM 76,176,104\nR 170,185,28672\nv_cvt_pk_bf16_f32 v120, v230, v231\nM 76,180,120\n"
"s_add_u32 s64, s68, 37749760\nv_rcp_f32 v233, v233\nM 60,176,100\nR 172,185,32768\nv_mul_f32 v233, v123, v233\nM 60,180,116\n"
"s_add_u32 s65, s68, 37864448\nv_cvt_pk_bf16_f32 v121, v232, v233\nv_mul_f32 v236, v126, v126\nM 44,176,96\nR 174,185,36864\nv_mul_f32 v237, v127, v127\n"
"M 44,180,112\nG 4\nv_fma_f32 v234, v234, v218, v217\nv_fma_f32 v235, v235, v218, v217\nM 32,160,128\nR 176,186,24576\n"
"s_add_u32 m0, s50, 81920\nM 32,164,144\nbuffer_load_dwordx4 a[32:35], v252, s[20:23], s64 offen sc1\nM 48,160,132\nR 178,186,28672\ns_add_u32 s63, s54, 3440896\n"
"M 48,164,148\nbuffer_load_dwordx4 a[36:39], v252, s[20:23], s65 offen sc1\nM 64,160,136\nR 180,186,32768\nv_fma_f32 v236, v236, v218, v217\nM 64,164,152\n"
"s_add_u32 s66, s69, 37749760\nbuffer_load_dwordx4 v220, s[12:15], s63 offen lds\nM 80,160,140\nR 182,186,36864\nv_fma_f32 v237, v237, v218, v217\nM 80,164,156\n"
"G 4\nv_mul_f32 v234, v124, v234\nv_mul_f32 v235, v125, v235\nM 84,168,140\nR 160,187,24576\nv_mul_f32 v236, v126, v236\n"
"M 84,172,156\nbuffer_load_dwordx4 a[40:43], v252, s[20:23], s66 offen sc1\nbuffer_load_dwordx4 v221, s[12:15], s63 offen offset:1024 lds\nM 68,168,136\nR 162,187,28672\nv_mul_f32 v237, v127, v237\n"
"M 68,172,152\ns_add_u32 s67, s69, 37864448\nv_exp_f32 v234, v234\nM 52,168,132\nR 164,187,32768\ns_waitcnt vmcnt(15)\n"
"s_barrier\nM 52,172,148\nbuffer_load_dwordx4 a[44:47], v252, s[20:23], s67 offen sc1\ns_movk_i32 s63, 2048\nv_mad_u32_u24 v220, v228, s63, v229\nv_add_u32 v221, 916480, v220\n"
"s_add_u32 m0, s50, 90112\ns_add_u32 s63, s56, 0\nbuffer_load_dwordx4 v220, s[16:19], s63 offen lds\nM 36,168,128\nR 166,187,36864\nv_fma_f32 v234, v234, v219, v219\n"
"M 36,172,144\nG 4\nv_exp_f32 v235, v235\nM 40,176,128\nR 168,184,40960\nv_fma_f32 v235, v235, v219, v219\n"
"M 40,180,144\ns_add_u32 s64, s68, 37756928\nbuffer_load_dwordx4 v221, s[16:19], s63 offen offset:1024 lds\nM 56,176,132\nR 170,184,45056\ns_add_u32 m0, s50, 98304\n"
"M 56,180,148\nbuffer_load_dwordx4 a[48:51], v252, s[20:23], s64 offen sc1\nM 72,176,136\nR 172,184,49152\ns_add_u32 s63, s56, 524288\nM 72,180,152\n"
"s_add_u32 s65, s68, 37871616\nbuffer_load_dwordx4 v220, s[16:19], s63 offen lds\nM 88,176,140\nR 174,184,53248\nv_exp_f32 v236, v236\nM 88,180,156\n"
"G 4\nv_exp_f32 v237, v237\nM 92,160,140\nR 176,185,40960\nv_fma_f32 v236, v236, v219, v219\nM 92,164,156\n"
"buffer_load_dwordx4 a[52:55], v252, s[20:23], s65 offen sc1\nbuffer_load_dwordx4 v221, s[16:19], s63 offen offset:1024 lds\nM 76,160,136\nR 178,185,45056\nv_fma_f32 v237, v237, v219, v219\nM 76,164,152\n"
"s_add_u32 s66, s69, 37756928\nv_rcp_f32 v234, v234\nM 60,160,132\nR 180,185,49152\nv_mul_f32 v234, v124, v234\nM 60,164,148\n"
"buffer_load_dwordx4 a[56:59], v252, s[20:23], s66 offen sc1\nM 44,160,128\nR 182,185,53248\nM 44,164,144\nG 4\nv_rcp_f32 v235, v235\n"
"M 32,168,160\nR 160,186,40960\nv_mul_f32 v235, v125, v235\nM 32,172,176\ns_add_u32 s67, s69, 37871616\nv_rcp_f32 v236, v236\n"
"M 48,168,164\nR 162,186,45056\nv_mul_f32 v236, v126, v236\nM 48,172,180\nbuffer_load_dwordx4 a[60:63], v252, s[20:23], s67 offen sc1\nM 64,168,168\n"
"R 164,186,49152\nv_cvt_pk_bf16_f32 v122, v234, v235\nM 64,172,184\nbuffer_store_dwordx4 v[112:115], v252, s[20:23], s85 offen offset:0 sc1\nM 80,168,172\nR 166,186,53248\n"
"v_rcp_f32 v237, v237\nM 80,172,188\nG 4\nv_mul_f32 v237, v127, v237\nv_cvt_pk_bf16_f32 v123, v236, v237\nM 84,176,172\n"
"R 168,187,40960\nM 84,180,188\ns_add_u32 m0, s50, 106496\nbuffer_store_dwordx4 v[120:123], v252, s[20:23], s85 offen offset:1024 sc1\nM 68,176,168\nR 170,187,45056\n"
"v_mul_f32 v230, v0, v0\nv_mul_f32 v231, v1, v1\nM 68,180,184\ns_add_u32 s63, s56, 65536\ns_add_u32 s84, s73, 37754880\ns_add_u32 s85, s73, 37869568\n"
"v_mul_f32 v232, v2, v2\nM 52,176,164\nR 172,187,49152\nv_mul_f32 v233, v3, v3\nv_fma_f32 v230, v230, v215, v214\nM 52,180,180\n"
"buffer_load_dwordx4 v220, s[16:19], s63 offen lds\nv_fma_f32 v231, v231, v215, v214\nM 36,176,160\nR 174,187,53248\nv_fma_f32 v232, v232, v215, v214\nv_fma_f32 v233, v233, v215, v214\n"
"M 36,180,176\nG 4\nv_mul_f32 v230, v0, v230\nv_mul_f32 v231, v1, v231\nv_mul_f32 v232, v2, v232\nM 40,160,160\n"
"R 176,184,57344\nv_mul_f32 v233, v3, v233\nv_exp_f32 v230, v230\nM 40,164,176\nbuffer_load_dwordx4 v221, s[16:19], s63 offen offset:1024 lds\nv_exp_f32 v231, v231\n"
"M 56,160,164\nR 178,184,61440\nv_fma_f32 v230, v230, v216, v216\nv_exp_f32 v232, v232\nM 56,164,180\ns_add_u32 m0, s50, 114688\n"
"v_exp_f32 v233, v233\nv_fma_f32 v231, v231, v216, v216\nM 72,160,168\nR 180,188,0\nv_fma_f32 v232, v232, v216, v216\nv_fma_f32 v233, v233, v216, v216\n"
"M 72,164,184\ns_add_u32 s63, s56, 589824\nv_rcp_f32 v230, v230\nv_rcp_f32 v231, v231\nM 88,160,172\nR 182,188,4096\n"
"v_mul_f32 v230, v0, v230\nv_rcp_f32 v232, v232\nM 88,164,188\nG 4\nv_rcp_f32 v233, v233\nv_mul_f32 v231, v1, v231\n"
"M 92,168,172\nR 160,185,57344\nv_mul_f32 v232, v2, v232\nv_mul_f32 v233, v3, v233\nM 92,172,188\nbuffer_load_dwordx4 v220, s[16:19], s63 offen lds\n"
"v_cvt_pk_bf16_f32 v0, v230, v231\nM 76,168,168\nR 162,185,61440\nv_cvt_pk_bf16_f32 v1, v232, v233\nv_mul_f32 v234, v4, v4\nM 76,172,184\n"
"v_mul_f32 v235, v5, v5\nv_mul_f32 v236, v6, v6\nv_mul_f32 v237, v7, v7\nM 60,168,164\nR 164,189,0\nv_fma_f32 v234, v234, v215, v214\n"
"v_fma_f32 v235, v235, v215, v214\nM 60,172,180\nbuffer_load_dwordx4 v221, s[16:19], s63 offen offset:1024 lds\nv_fma_f32 v236, v236, v215, v214\nM 44,168,160\nR 166,189,4096\n"
"s_add_u32 m0, s50, 0\nv_fma_f32 v237, v237, v215, v214\nM 44,172,176\nG 4\nv_mul_f32 v234, v4, v234\nv_mul_f32 v235, v5, v235\n"
"v_mul_f32 v236, v6, v236\nM 32,176,192\nR 168,186,57344\ns_add_u32 s63, s57, 0\nv_mul_f32 v237, v7, v237\nM 32,180,208\n"
"buffer_load_dwordx4 v220, s[16:19], s63 offen lds\nv_exp_f32 v234, v234\nM 48,176,196\nR 170,186,61440\nv_fma_f32 v234, v234, v216, v216\nv_exp_f32 v235, v235\n"
"M 48,180,212\nv_exp_f32 v236, v236\nv_exp_f32 v237, v237\nM 64,176,200\nR 172,190,0\nv_fma_f32 v235, v235, v216, v216\n"
"v_fma_f32 v236, v236, v216, v216\nM 64,180,216\nbuffer_load_dwordx4 v221, s[16:19], s63 offen offset:1024 lds\nv_fma_f32 v237, v237, v216, v216\nM 80,176,204\nR 174,190,4096\n"
"s_add_u32 s63, s57, 524288\nv_rcp_f32 v234, v234\nM 80,180,220\nG 4\nv_rcp_f32 v235, v235\nv_rcp_f32 v236, v236\n"
"M 84,160,204\nR 176,187,57344\ns_waitcnt vmcnt(10)\ns_barrier\nv_rcp_f32 v237, v237\nM 84,164,220\n"
"s_add_u32 m0, s50, 8192\nv_mul_f32 v234, v4, v234\nv_mul_f32 v235, v5, v235\nv_mul_f32 v236, v6, v236\nM 68,160,200\nR 178,187,61440\n"
"v_mul_f32 v237, v7, v237\nv_cvt_pk_bf16_f32 v2, v234, v235\nM 68,164,216\nbuffer_load_dwordx4 v220, s[16:19], s63 offen lds\nv_cvt_pk_bf16_f32 v3, v236, v237\nM 52,160,196\n"
"R 180,191,0\nv_mul_f32 v230, v16, v16\nv_mul_f32 v231, v17, v17\nM 52,164,212\nv_mul_f32 v232, v18, v18\nv_mul_f32 v233, v19, v19\n"
"v_fma_f32 v230, v230, v218, v217\nM 36,160,192\nR 182,191,4096\nv_fma_f32 v231, v231, v218, v217\nv_fma_f32 v232, v232, v218, v217\nM 36,164,208\n"
"G 4\nv_fma_f32 v233, v233, v218, v217\nv_mul_f32 v230, v16, v230\nv_mul_f32 v231, v17, v231\nM 40,168,192\nR 160,188,8192\n"
"v_mul_f32 v232, v18, v232\nv_mul_f32 v233, v19, v233\nM 40,172,208\nbuffer_load_dwordx4 v221, s[16:19], s63 offen offset:1024 lds\nv_exp_f32 v230, v230\nM 56,168,196\n"
"R 162,188,12288\nv_fma_f32 v230, v230, v219, v219\nv_exp_f32 v231, v231\nM 56,172,212\nbuffer_store_dwordx4 v[0:3], v252, s[20:23], s84 offen offset:0 sc1\nv_exp_f32 v232, v232\n"
"M 72,168,200\nR 164,188,16384\nv_fma_f32 v231, v231, v219, v219\nv_exp_f32 v233, v233\nM 72,172,216\ns_add_u32 m0, s50, 16384\n"
"v_rcp_f32 v230, v230\nv_fma_f32 v232, v232, v219, v219\nM 88,168,204\nR 166,188,20480\nv_mov_b32 v226, s46\nv_fma_f32 v233, v233, v219, v219\n"
"M 88,172,220\nG 4\nv_mov_b32 v227, s47\nv_mul_f32 v230, v16, v230\nv_rcp_f32 v231, v231\nM 92,176,204\n"
"R 168,189,8192\ns_add_u32 s61, s81, 845832\ns_lshl_b32 s62, s44, 5\ns_add_u32 s61, s61, s62\ns_waitcnt vmcnt(9)\ns_mov_b64 exec, 1\n"
"buffer_atomic_swap_x2 v[226:227], off, s[20:23], s61 sc1\ns_mov_b64 exec, -1\nv_rcp_f32 v232, v232\nM 92,180,220\ns_add_u32 s63, s57, 65536\nv_rcp_f32 v233, v233\n"
"v_mul_f32 v231, v17, v231\nM 76,176,200\nR 170,189,12288\nv_mul_f32 v232, v18, v232\nv_mul_f32 v233, v19, v233\nM 76,180,216\n"
"buffer_load_dwordx4 v220, s[16:19], s63 offen lds\nv_cvt_pk_bf16_f32 v16, v230, v231\nM 60,176,196\nR 172,189,16384\nv_cvt_pk_bf16_f32 v17, v232, v233\nv_mul_f32 v234, v20, v20\n"
"M 60,180,212\nv_mul_f32 v235, v21, v21\nv_mul_f32 v236, v22, v22\nv_mul_f32 v237, v23, v23\nM 44,176,192\nR 174,189,20480\n"
"s_mov_b32 s60, 158208\nv_bfe_u32 v246, v252, 4, 4\nv_lshl_add_u32 v246, v246, 3, s60\nv_fma_f32 v234, v234, v218, v217\nM 44,180,208\nds_read_b64 v[238:239], v246 offset:0\n"
"G 5\nv_fma_f32 v235, v235, v218, v217\nM 32,160,224\nR 176,190,8192\nds_read_b64 v[240:241], v246 offset:128\nv_fma_f32 v236, v236, v218, v217\n"
"M 32,164,240\nds_read_b64 v[242:243], v246 offset:256\nv_fma_f32 v237, v237, v218, v217\nv_mul_f32 v234, v20, v234\nM 48,160,228\nR 178,190,12288\n"
"ds_read_b64 v[244:245], v246 offset:384\nv_mul_f32 v235, v21, v235\nM 48,164,244\nds_read_b64 v[246:247], v246 offset:0\nv_mul_f32 v236, v22, v236\nv_mul_f32 v237, v23, v237\n"
"M 64,160,232\nR 180,190,16384\nv_exp_f32 v234, v234\nM 64,164,248\nbuffer_load_dwordx4 v221, s[16:19], s63 offen offset:1024 lds\nv_exp_f32 v235, v235\n"
"M 80,160,236\nR 182,190,20480\ns_add_u32 m0, s50, 24576\nv_exp_f32 v236, v236\nM 80,164,252\nG 9\n"
"v_exp_f32 v237, v237\nv_fma_f32 v234, v234, v219, v219\nM 84,168,236\nR 160,191,8192\ns_add_u32 s63, s57, 589824\nv_fma_f32 v235, v235, v219, v219\n"
"M 84,172,252\nbuffer_load_dwordx4 v220, s[16:19], s63 offen lds\nv_fma_f32 v236, v236, v219, v219\nM 68,168,232\nR 162,191,12288\nv_fma_f32 v237, v237, v219, v219\n"
"v_rcp_f32 v234, v234\nM 68,172,248\nv_rcp_f32 v235, v235\nv_rcp_f32 v236, v236\nM 52,168,228\nR 164,191,16384\n"
"G 9\nv_mfma_f32_16x16x32_bf16 v[32:35], v[238:241], v[208:211], v[32:35]\nv_mul_f32 v234, v20, v234\nv_rcp_f32 v237, v237\nM 52,172,244\nbuffer_load_dwordx4 v221, s[16:19], s63 offen offset:1024 lds\n"
"v_mul_f32 v235, v21, v235\nM 36,168,224\nR 166,191,20480\nG 9\nv_mfma_f32_16x16x32_bf16 v[36:39], v[240:243], v[208:211], v[36:39]\ns_add_u32 m0, s50, 32768\n"
"v_mul_f32 v236, v22, v236\nM 36,172,240\nG 4\nv_mul_f32 v237, v23, v237\nv_cvt_pk_bf16_f32 v18, v234, v235\nv_cvt_pk_bf16_f32 v19, v236, v237\n"
"M 40,176,224\nR 168,188,24576\nv_mfma_f32_16x16x32_bf16 v[40:43], v[242:245], v[208:211], v[40:43]\ns_add_u32 s63, s56, 131072\nbuffer_store_dwordx4 v[16:19], v252, s[20:23], s85 offen offset:0 sc1\nM 40,180,240\n"
"buffer_load_dwordx4 v220, s[16:19], s63 offen lds\nM 56,176,228\nR 170,188,28672\nv_mfma_f32_16x16x32_bf16 v[44:47], v[244:247], v[208:211], v[44:47]\nv_mov_b32 v248, 0\nM 56,180,244\n"
"v_mov_b32 v249, 0\nM 72,176,232\nR 172,188,32768\nv_mfma_f32_16x16x32_bf16 v[48:51], v[244:247], v[210:213], v[48:51]\ns_add_u32 s61, s82, 845832\ns_mov_b64 exec, 0xf\n"
"buffer_atomic_or_x2 v[248:249], v250, s[20:23], s61 offen sc0 sc1\ns_mov_b64 exec, -1\nM 72,180,248\nM 88,176,236\nR 174,188,36864\nv_mfma_f32_16x16x32_bf16 v[52:55], v[238:241], v[210:213], v[52:55]\n"
"M 88,180,252\nG 4\nM 92,160,236\nR 176,189,24576\nv_mfma_f32_16x16x32_bf16 v[56:59], v[240:243], v[210:213], v[56:59]\nM 92,164,252\n"
"buffer_load_dwordx4 v221, s[16:19], s63 offen offset:1024 lds\nM 76,160,232\nR 178,189,28672\nv_mfma_f32_16x16x32_bf16 v[60:63], v[242:245], v[210:213], v[60:63]\ns_add_u32 m0, s50, 40960\ns_add_u32 s63, s56, 655360\n"
"M 76,164,248\nbuffer_load_dwordx4 v220, s[16:19], s63 offen lds\nM 60,160,228\nR 180,189,32768\nM 60,164,244\nM 44,160,224\n"
"R 182,189,36864\nM 44,164,240\ns_waitcnt vmcnt(15)\ns_barrier\nG 4\nv_mfma_f32_16x16x32_bf16 v[96:99], v[168:171], a[0:3], 0\n"
"R 160,190,24576\nM 96,172,16\ns_mov_b32 m0, s51\nv_mfma_f32_16x16x32_bf16 v[112:115], v[168:171], a[4:7], 0\nR 162,190,28672\nM 112,172,20\n"
"ds_write_addtid_b32 v64 offset:0\nv_mfma_f32_16x16x32_bf16 v[128:131], v[168:171], a[8:11], 0\nR 164,190,32768\nM 128,172,24\nds_write_addtid_b32 v65 offset:256\nv_mfma_f32_16x16x32_bf16 v[144:147], v[168:171], a[12:15], 0\n"
"R 166,190,36864\nds_write_addtid_b32 v66 offset:512\nM 144,172,28\nds_write_addtid_b32 v67 offset:768\nds_write_addtid_b32 v68 offset:1024\nG 9\n"
"v_mfma_f32_16x16x32_bf16 v[148:151], v[176:179], a[12:15], 0\nR 168,191,24576\nds_write_addtid_b32 v69 offset:1280\nM 148,180,28\nds_write_addtid_b32 v70 offset:1536\nds_write_addtid_b32 v71 offset:1792\n"
"v_mfma_f32_16x16x32_bf16 v[132:135], v[176:179], a[8:11], 0\nR 170,191,28672\nds_write_addtid_b32 v72 offset:2048\nM 132,180,24\nds_write_addtid_b32 v73 offset:2304\nds_write_addtid_b32 v74 offset:2560\n"
"v_mfma_f32_16x16x32_bf16 v[116:119], v[176:179], a[4:7], 0\nR 172,191,32768\nds_write_addtid_b32 v75 offset:2816\nM 116,180,20\nds_write_addtid_b32 v76 offset:3072\nds_write_addtid_b32 v77 offset:3328\n"
"v_mfma_f32_16x16x32_bf16 v[100:103], v[176:179], a[0:3], 0\nR 174,191,36864\nds_write_addtid_b32 v78 offset:3584\nM 100,180,16\nds_write_addtid_b32 v79 offset:3840\nds_write_addtid_b32 v80 offset:4096\n"
"G 15\nv_mfma_f32_16x16x32_bf16 v[104:107], v[160:163], a[0:3], 0\nR 176,188,40960\nds_write_addtid_b32 v81 offset:4352\nM 104,164,16\nds_write_addtid_b32 v82 offset:4608\n"
"ds_write_addtid_b32 v83 offset:4864\nv_mfma_f32_16x16x32_bf16 v[120:123], v[160:163], a[4:7], 0\nR 178,188,45056\nds_write_addtid_b32 v84 offset:5120\nM 120,164,20\nds_write_addtid_b32 v85 offset:5376\n"
"ds_write_addtid_b32 v86 offset:5632\nv_mfma_f32_16x16x32_bf16 v[136:139], v[160:163], a[8:11], 0\nR 180,188,49152\nds_write_addtid_b32 v87 offset:5888\nM 136,164,24\nds_write_addtid_b32 v88 offset:6144\n"
"ds_write_addtid_b32 v89 offset:6400\nv_mfma_f32_16x16x32_bf16 v[152:155], v[160:163], a[12:15], 0\nR 182,188,53248\nds_write_addtid_b32 v90 offset:6656\nM 152,164,28\nds_write_addtid_b32 v91 offset:6912\n"
"ds_write_addtid_b32 v92 offset:7168\nG 15\nv_mfma_f32_16x16x32_bf16 v[156:159], v[168:171], a[12:15], 0\nR 160,189,40960\nds_write_addtid_b32 v93 offset:7424\nM 156,172,28\n"
"ds_write_addtid_b32 v94 offset:7680\nds_write_addtid_b32 v95 offset:7936\nv_mfma_f32_16x16x32_bf16 v[140:143], v[168:171], a[8:11], 0\nR 162,189,45056\ns_add_u32 m0, s50, 40960\nM 140,172,24\n"
"buffer_load_dwordx4 v221, s[16:19], s63 offen offset:1024 lds\nv_mfma_f32_16x16x32_bf16 v[124:127], v[168:171], a[4:7], 0\nR 164,189,49152\ns_waitcnt vmcnt(6) lgkmcnt(2)\ns_barrier\nM 124,172,20\n"
"ds_read2st64_b32 v[64:65], v225 offset0:0 offset1:1\nds_read2st64_b32 v[66:67], v225 offset0:2 offset1:3\nv_mfma_f32_16x16x32_bf16 v[108:111], v[168:171], a[0:3], 0\nR 166,189,53248\nds_read2st64_b32 v[68:69], v225 offset0:4 offset1:5\nM 108,172,16\n"
"ds_read2st64_b32 v[70:71], v225 offset0:6 offset1:7\nds_read2st64_b32 v[72:73], v225 offset0:8 offset1:9\nM 96,176,32\nR 168,190,40960\nds_read2st64_b32 v[74:75], v225 offset0:10 offset1:11\nM 96,180,48\n"
"s_add_u32 s64, s68, 37750784\nds_read2st64_b32 v[76:77], v225 offset0:12 offset1:13\nv_mov_b32 v226, s46\nM 112,176,36\nR 170,190,45056\nds_read2st64_b32 v[78:79], v225 offset0:14 offset1:15\n"
"v_mov_b32 v227, s47\ns_waitcnt vmcnt(3)\nLpg29:\ns_cbranch_execz LretO1n\nv_cmp_eq_u64 s[86:87], s[46:47], v[248:249]\nM 112,180,52\n"
"s_add_u32 s61, s81, 845848\ns_lshl_b32 s62, s44, 5\ns_add_u32 s61, s61, s62\ns_mov_b64 exec, 1\nbuffer_atomic_swap_x2 v[226:227], off, s[20:23], s61 sc1\ns_mov_b64 exec, -1\n"
"ds_read2st64_b32 v[80:81], v225 offset0:16 offset1:17\nM 128,176,40\nR 172,190,49152\ns_and_b32 s62, s86, 0xf\ns_cmp_eq_u32 s62, 0xf\ns_cbranch_scc0 Lq873\n"
"Lq874:\nds_read2st64_b32 v[82:83], v225 offset0:18 offset1:19\nM 128,180,56\ns_add_u32 s65, s68, 37865472\nds_read2st64_b32 v[84:85], v225 offset0:20 offset1:21\nds_read2st64_b32 v[86:87], v225 offset0:22 offset1:23\n"
"M 144,176,44\nR 174,190,53248\ns_add_u32 m0, s50, 49152\nds_read2st64_b32 v[88:89], v225 offset0:24 offset1:25\nM 144,180,60\nds_read2st64_b32 v[90:91], v225 offset0:26 offset1:27\n"
"ds_read2st64_b32 v[92:93], v225 offset0:28 offset1:29\nG 15\nM 148,160,44\nR 176,191,40960\ns_add_u32 s63, s56, 196608\nds_read2st64_b32 v[94:95], v225 offset0:30 offset1:31\n"
"M 148,164,60\nbuffer_load_dwordx4 a[64:67], v252, s[20:23], s64 offen sc1\nv_add_f32 v32, v32, v64\nM 132,160,40\nR 178,191,45056\nv_add_f32 v33, v33, v65\n"
"v_add_f32 v34, v34, v66\nM 132,164,56\ns_add_u32 s66, s69, 37750784\nv_add_f32 v35, v35, v67\nv_add_f32 v36, v36, v68\nv_add_f32 v37, v37, v69\n"
"M 116,160,36\nR 180,191,49152\nbuffer_load_dwordx4 v220, s[16:19], s63 offen lds\nv_add_f32 v38, v38, v70\nM 116,164,52\nbuffer_load_dwordx4 a[68:71], v252, s[20:23], s65 offen sc1\n"
"v_add_f32 v39, v39, v71\nM 100,160,32\nR 182,191,53248\nG 15\nv_add_f32 v40, v40, v72\nv_add_f32 v41, v41, v73\n"
"M 100,164,48\nG 8\nv_add_f32 v42, v42, v74\nv_add_f32 v43, v43, v75\nv_add_f32 v44, v44, v76\nM 104,168,32\n"
"R 160,184,0\nbuffer_load_dwordx4 v221, s[16:19], s63 offen offset:1024 lds\nv_add_f32 v45, v45, v77\nM 104,172,48\ns_add_u32 s67, s69, 37865472\nv_add_f32 v46, v46, v78\n"
"v_add_f32 v47, v47, v79\nv_add_f32 v48, v48, v80\nM 120,168,36\nR 162,184,4096\nv_add_f32 v49, v49, v81\nv_add_f32 v50, v50, v82\n"
"M 120,172,52\nbuffer_load_dwordx4 a[72:75], v252, s[20:23], s66 offen sc1\nM 136,168,40\nR 164,184,8192\nv_add_f32 v51, v51, v83\nM 136,172,56\n"
"v_add_f32 v52, v52, v84\nv_add_f32 v53, v53, v85\nM 152,168,44\nR 166,184,12288\nv_add_f32 v54, v54, v86\nM 152,172,60\n"
"G 4\nv_add_f32 v55, v55, v87\nv_add_f32 v56, v56, v88\nM 156,176,44\nR 168,185,0\nv_add_f32 v57, v57, v89\n"
"M 156,180,60\nbuffer_load_dwordx4 a[76:79], v252, s[20:23], s67 offen sc1\nM 140,176,40\nR 170,185,4096\nv_add_f32 v58, v58, v90\nM 140,180,56\n"
"s_add_u32 s64, s68, 37757952\nv_add_f32 v59, v59, v91\nv_add_f32 v60, v60, v92\nM 124,176,36\nR 172,185,8192\nv_add_f32 v61, v61, v93\n"
"M 124,180,52\ns_add_u32 s65, s68, 37872640\nv_add_f32 v62, v62, v94\nv_add_f32 v63, v63, v95\nM 108,176,32\nR 174,185,12288\n"
"v_mul_f32 v230, v32, v32\nv_mul_f32 v231, v33, v33\nM 108,180,48\ns_add_u32 s84, s73, 37752832\ns_add_u32 s85, s73, 37867520\nG 4\n"
"v_mul_f32 v232, v34, v34\nv_mfma_f32_16x16x32_bf16 v[0:3], v[160:163], a[0:3], 0\nR 176,186,0\ns_lshr_b32 s60, s45, 3\ns_lshl2_add_u32 s60, s60, s40\ns_lshl_b32 s60, s60, 8\n"
"v_lshl_add_u32 v224, v252, 3, s60\nv_add_u32 v224, LqF-32768-LqPC, v224\nv_min_u32 v224, LqE-LqPC-4, v224\ns_mov_b64 exec, 3\nglobal_load_dword v224, v224, s[98:99]\ns_mov_b64 exec, -1\n"
"v_mul_f32 v233, v35, v35\nM 0,164,16\nbuffer_load_dwordx4 a[80:83], v252, s[20:23], s64 offen sc1\nv_fma_f32 v230, v230, v215, v214\nv_mfma_f32_16x16x32_bf16 v[16:19], v[160:163], a[4:7], 0\nR 178,186,4096\n"
"v_fma_f32 v231, v231, v215, v214\nv_fma_f32 v232, v232, v215, v214\nM 16,164,20\ns_add_u32 s66, s69, 37757952\nv_fma_f32 v233, v233, v215, v214\nv_mul_f32 v230, v32, v230\n"
"v_mul_f32 v231, v33, v231\nv_mfma_f32_16x16x32_bf16 a[224:227], v[160:163], a[8:11], 0\nR 180,186,8192\nv_mul_f32 v232, v34, v232\nv_mul_f32 v233, v35, v233\nv_mfma_f32_16x16x32_bf16 a[224:227], v[164:167], a[24:27], a[224:227]\n"
"buffer_load_dwordx4 a[84:87], v252, s[20:23], s65 offen sc1\nv_exp_f32 v230, v230\nv_mfma_f32_16x16x32_bf16 a[240:243], v[160:163], a[12:15], 0\nR 182,186,12288\nv_fma_f32 v230, v230, v216, v216\nv_exp_f32 v231, v231\n"
"v_mfma_f32_16x16x32_bf16 a[240:243], v[164:167], a[28:31], a[240:243]\nG 4\nv_exp_f32 v232, v232\nv_exp_f32 v233, v233\nv_mfma_f32_16x16x32_bf16 a[244:247], v[168:171], a[12:15], 0\nR 160,187,0\n"
"s_add_u32 m0, s50, 57344\nv_fma_f32 v231, v231, v216, v216\nv_mfma_f32_16x16x32_bf16 a[244:247], v[172:175], a[28:31], a[244:247]\ns_add_u32 s67, s69, 37872640\nv_rcp_f32 v230, v230\nv_fma_f32 v232, v232, v216, v216\n"
"v_mfma_f32_16x16x32_bf16 a[228:231], v[168:171], a[8:11], 0\nR 162,187,4096\ns_add_u32 s63, s56, 720896\nv_fma_f32 v233, v233, v216, v216\nv_mfma_f32_16x16x32_bf16 a[228:231], v[172:175], a[24:27], a[228:231]\nbuffer_load_dwordx4 a[88:91], v252, s[20:23], s66 offen sc1\n"
"v_rcp_f32 v231, v231\nv_mfma_f32_16x16x32_bf16 v[20:23], v[168:171], a[4:7], 0\nR 164,187,8192\nv_mul_f32 v230, v32, v230\nv_rcp_f32 v232, v232\nM 20,172,20\n"
"buffer_load_dwordx4 v220, s[16:19], s63 offen lds\nv_rcp_f32 v233, v233\nv_mfma_f32_16x16x32_bf16 v[4:7], v[168:171], a[0:3], 0\nR 166,187,12288\nv_mul_f32 v231, v33, v231\nv_mul_f32 v232, v34, v232\n"
"M 4,172,16\nG 4\nv_mul_f32 v233, v35, v233\nv_cvt_pk_bf16_f32 v32, v230, v231\nv_cvt_pk_bf16_f32 v33, v232, v233\nv_mfma_f32_16x16x32_bf16 v[8:11], v[176:179], a[0:3], 0\n"
"R 168,184,16384\nv_mul_f32 v234, v36, v36\nv_mul_f32 v235, v37, v37\nM 8,180,16\nbuffer_load_dwordx4 a[92:95], v252, s[20:23], s67 offen sc1\nbuffer_load_dwordx4 v221, s[16:19], s63 offen offset:1024 lds\n"
"v_mul_f32 v236, v38, v38\nv_mfma_f32_16x16x32_bf16 v[24:27], v[176:179], a[4:7], 0\nR 170,184,20480\ns_add_u32 m0, s50, 65536\nv_mul_f32 v237, v39, v39\nM 24,180,20\n"
"s_add_u32 s64, s68, 37751808\nv_fma_f32 v234, v234, v215, v214\nv_fma_f32 v235, v235, v215, v214\nv_fma_f32 v236, v236, v215, v214\nv_mfma_f32_16x16x32_bf16 a[232:235], v[176:179], a[8:11], 0\nR 172,184,24576\n"
"s_add_u32 s63, s57, 131072\nv_fma_f32 v237, v237, v215, v214\nv_mfma_f32_16x16x32_bf16 a[232:235], v[180:183], a[24:27], a[232:235]\ns_add_u32 s65, s68, 37866496\nbuffer_load_dwordx4 v220, s[16:19], s63 offen lds\nv_mul_f32 v234, v36, v234\n"
"v_mfma_f32_16x16x32_bf16 a[248:251], v[176:179], a[12:15], 0\nR 174,184,28672\nv_mul_f32 v235, v37, v235\nv_mul_f32 v236, v38, v236\nv_mfma_f32_16x16x32_bf16 a[248:251], v[180:183], a[28:31], a[248:251]\nG 4\n"
"v_exp_f32 v234, v234\nv_mul_f32 v237, v39, v237\nv_mfma_f32_16x16x32_bf16 a[252:255], v[160:163], a[12:15], 0\nR 176,185,16384\nv_fma_f32 v234, v234, v216, v216\nv_exp_f32 v235, v235\n"
"v_mfma_f32_16x16x32_bf16 a[252:255], v[164:167], a[28:31], a[252:255]\nbuffer_load_dwordx4 a[96:99], v252, s[20:23], s64 offen sc1\nbuffer_load_dwordx4 v221, s[16:19], s63 offen offset:1024 lds\nv_exp_f32 v236, v236\nv_mfma_f32_16x16x32_bf16 a[236:239], v[160:163], a[8:11], 0\nR 178,185,20480\n"
"s_add_u32 m0, s50, 73728\nv_exp_f32 v237, v237\nv_mfma_f32_16x16x32_bf16 a[236:239], v[164:167], a[24:27], a[236:239]\ns_add_u32 s66, s69, 37751808\nv_rcp_f32 v234, v234\nv_fma_f32 v235, v235, v216, v216\n"
"v_mfma_f32_16x16x32_bf16 v[28:31], v[160:163], a[4:7], 0\nR 180,185,24576\ns_add_u32 s63, s57, 655360\nv_fma_f32 v236, v236, v216, v216\nM 28,164,20\nbuffer_load_dwordx4 a[100:103], v252, s[20:23], s65 offen sc1\n"
"buffer_load_dwordx4 v220, s[16:19], s63 offen lds\nv_fma_f32 v237, v237, v216, v216\nv_mfma_f32_16x16x32_bf16 v[12:15], v[160:163], a[0:3], 0\nR 182,185,28672\nv_mul_f32 v234, v36, v234\nv_rcp_f32 v235, v235\n"
"M 12,164,16\ns_waitcnt vmcnt(14)\ns_barrier\nG 4\nv_rcp_f32 v236, v236\nM 0,168,32\n"
"R 160,186,16384\nv_mul_f32 v235, v37, v235\nv_rcp_f32 v237, v237\nM 0,172,48\ns_add_u32 s67, s69, 37866496\nbuffer_load_dwordx4 v221, s[16:19], s63 offen offset:1024 lds\n"
"v_mul_f32 v236, v38, v236\nM 16,168,36\nR 162,186,20480\nv_mul_f32 v237, v39, v237\nv_cvt_pk_bf16_f32 v34, v234, v235\nM 16,172,52\n"
"buffer_load_dwordx4 a[104:107], v252, s[20:23], s66 offen sc1\nv_cvt_pk_bf16_f32 v35, v236, v237\nv_mfma_f32_16x16x32_bf16 a[224:227], v[168:171], a[40:43], a[224:227]\nR 164,186,24576\nv_mul_f32 v230, v40, v40\nv_mul_f32 v231, v41, v41\n"
"v_mfma_f32_16x16x32_bf16 a[224:227], v[172:175], a[56:59], a[224:227]\nv_mul_f32 v232, v42, v42\nv_mul_f32 v233, v43, v43\nv_fma_f32 v230, v230, v215, v214\nv_mfma_f32_16x16x32_bf16 a[240:243], v[168:171], a[44:47], a[240:243]\nR 166,186,28672\n"
"v_fma_f32 v231, v231, v215, v214\nv_fma_f32 v232, v232, v215, v214\nv_mfma_f32_16x16x32_bf16 a[240:243], v[172:175], a[60:63], a[240:243]\nG 4\nv_fma_f32 v233, v233, v215, v214\nv_mul_f32 v230, v40, v230\n"
"v_mul_f32 v231, v41, v231\nv_mfma_f32_16x16x32_bf16 a[244:247], v[176:179], a[44:47], a[244:247]\nR 168,187,16384\nv_mul_f32 v232, v42, v232\nv_mul_f32 v233, v43, v233\nv_mfma_f32_16x16x32_bf16 a[244:247], v[180:183], a[60:63], a[244:247]\n"
"buffer_load_dwordx4 a[108:111], v252, s[20:23], s67 offen sc1\nv_exp_f32 v230, v230\nv_mfma_f32_16x16x32_bf16 a[228:231], v[176:179], a[40:43], a[228:231]\nR 170,187,20480\nv_fma_f32 v230, v230, v216, v216\nv_exp_f32 v231, v231\n"
"v_mfma_f32_16x16x32_bf16 a[228:231], v[180:183], a[56:59], a[228:231]\ns_add_u32 s64, s68, 37758976\nv_exp_f32 v232, v232\nv_exp_f32 v233, v233\nM 20,176,36\nR 172,187,24576\n"
"v_fma_f32 v231, v231, v216, v216\nv_fma_f32 v232, v232, v216, v216\nM 20,180,52\ns_add_u32 s65, s68, 37873664\nv_rcp_f32 v230, v230\nv_fma_f32 v233, v233, v216, v216\n"
"M 4,176,32\nR 174,187,28672\ns_add_u32 m0, s50, 81920\nv_rcp_f32 v231, v231\nM 4,180,48\nG 4\n"
"v_rcp_f32 v232, v232\nv_rcp_f32 v233, v233\nM 8,160,32\nR 176,184,32768\ns_add_u32 s63, s57, 196608\nv_mul_f32 v230, v40, v230\n"
"M 8,164,48\nbuffer_load_dwordx4 a[112:115], v252, s[20:23], s64 offen sc1\nv_mul_f32 v231, v41, v231\nM 24,160,36\nR 178,184,36864\nv_mul_f32 v232, v42, v232\n"
"v_mul_f32 v233, v43, v233\nM 24,164,52\ns_add_u32 s66, s69, 37758976\nbuffer_load_dwordx4 v220, s[16:19], s63 offen lds\nv_cvt_pk_bf16_f32 v40, v230, v231\nv_mfma_f32_16x16x32_bf16 a[232:235], v[160:163], a[40:43], a[232:235]\n"
"R 180,184,40960\nv_cvt_pk_bf16_f32 v41, v232, v233\nv_mul_f32 v234, v44, v44\nv_mfma_f32_16x16x32_bf16 a[232:235], v[164:167], a[56:59], a[232:235]\nbuffer_load_dwordx4 a[116:119], v252, s[20:23], s65 offen sc1\nv_mul_f32 v235, v45, v45\n"
"v_mfma_f32_16x16x32_bf16 a[248:251], v[160:163], a[44:47], a[248:251]\nR 182,184,45056\nv_mul_f32 v236, v46, v46\nv_mul_f32 v237, v47, v47\nv_mfma_f32_16x16x32_bf16 a[248:251], v[164:167], a[60:63], a[248:251]\nbuffer_load_dwordx4 v221, s[16:19], s63 offen offset:1024 lds\n"
"G 4\nv_fma_f32 v234, v234, v215, v214\nv_mfma_f32_16x16x32_bf16 a[252:255], v[168:171], a[44:47], a[252:255]\nR 160,185,32768\ns_add_u32 m0, s50, 90112\nv_fma_f32 v235, v235, v215, v214\n"
"v_mfma_f32_16x16x32_bf16 a[252:255], v[172:175], a[60:63], a[252:255]\ns_add_u32 s67, s69, 37873664\ns_add_u32 s63, s57, 720896\nv_fma_f32 v236, v236, v215, v214\nv_fma_f32 v237, v237, v215, v214\nv_mfma_f32_16x16x32_bf16 a[236:239], v[168:171], a[40:43], a[236:239]\n"
"R 162,185,36864\nv_mov_b32 v248, 0\nv_mul_f32 v234, v44, v234\nv_mfma_f32_16x16x32_bf16 a[236:239], v[172:175], a[56:59], a[236:239]\nbuffer_load_dwordx4 a[120:123], v252, s[20:23], s66 offen sc1\nbuffer_load_dwordx4 v220, s[16:19], s63 offen lds\n"
"v_mov_b32 v249, 0\nv_mul_f32 v235, v45, v235\nM 28,168,36\nR 164,185,40960\ns_add_u32 s61, s82, 845848\ns_mov_b64 exec, 0xf\n"
"buffer_atomic_or_x2 v[248:249], v250, s[20:23], s61 offen sc0 sc1\ns_mov_b64 exec, -1\nv_mul_f32 v236, v46, v236\nM 28,172,52\nbuffer_load_dwordx4 v221, s[16:19], s63 offen offset:1024 lds\nv_mul_f32 v237, v47, v237\n"
"M 12,168,32\nR 166,185,45056\ns_add_u32 m0, s50, 98304\nv_exp_f32 v234, v234\nM 12,172,48\nG 4\n"
"v_exp_f32 v235, v235\nv_exp_f32 v236, v236\nM 96,176,64\nR 168,186,32768\ns_add_u32 s63, s58, 0\nv_exp_f32 v237, v237\n"
"s_waitcnt vmcnt(21)\nM 96,180,80\nbuffer_load_dwordx4 a[124:127], v252, s[20:23], s67 offen sc1\nv_fma_f32 v234, v234, v216, v216\nM 112,176,68\nR 170,186,36864\n"
"v_fma_f32 v235, v235, v216, v216\nv_fma_f32 v236, v236, v216, v216\ns_waitcnt vmcnt(21)\nM 112,180,84\nbuffer_load_dwordx4 v220, s[16:19], s63 offen lds\nv_fma_f32 v237, v237, v216, v216\n"
"M 128,176,72\nR 172,186,40960\nv_mul_f32 v230, v48, v48\nv_rcp_f32 v234, v234\ns_waitcnt vmcnt(21)\nM 128,180,88\n"
"buffer_load_dwordx4 v221, s[16:19], s63 offen offset:1024 lds\nv_rcp_f32 v235, v235\nM 144,176,76\nR 174,186,45056\ns_add_u32 m0, s50, 106496\nv_rcp_f32 v236, v236\n"
"s_waitcnt vmcnt(20)\nM 144,180,92\nG 4\nv_rcp_f32 v237, v237\nv_mul_f32 v234, v44, v234\nM 148,160,76\n"
"R 176,187,32768\ns_add_u32 s63, s58, 524288\nv_mul_f32 v235, v45, v235\nM 148,164,92\nbuffer_load_dwordx4 v220, s[16:19], s63 offen lds\nv_mul_f32 v236, v46, v236\n"
"M 132,160,72\nR 178,187,36864\nv_mul_f32 v237, v47, v237\nv_cvt_pk_bf16_f32 v42, v234, v235\nM 132,164,88\nbuffer_store_dwordx4 v[32:35], v252, s[20:23], s84 offen offset:0 sc1\n"
"v_cvt_pk_bf16_f32 v43, v236, v237\nM 116,160,68\nR 180,187,40960\nv_mul_f32 v231, v49, v49\nv_mul_f32 v232, v50, v50\nM 116,164,84\n"
"buffer_load_dwordx4 v221, s[16:19], s63 offen offset:1024 lds\nv_mul_f32 v233, v51, v51\nM 100,160,64\nR 182,187,45056\nv_fma_f32 v230, v230, v218, v217\nv_fma_f32 v231, v231, v218, v217\n"
"M 100,164,80\nG 4\nv_fma_f32 v232, v232, v218, v217\nv_fma_f32 v233, v233, v218, v217\nv_mul_f32 v230, v48, v230\nM 104,168,64\n"
"R 160,184,49152\ns_waitcnt vmcnt(16)\ns_barrier\nv_mul_f32 v231, v49, v231\nM 104,172,80\nbuffer_store_dwordx4 v[40:43], v252, s[20:23], s84 offen offset:1024 sc1\n"
"v_mul_f32 v232, v50, v232\nM 120,168,68\nR 162,184,53248\nv_mul_f32 v233, v51, v233\nv_exp_f32 v230, v230\nM 120,172,84\n"
"s_add_u32 m0, s50, 114688\nv_exp_f32 v231, v231\nv_exp_f32 v232, v232\nM 136,168,72\nR 164,184,57344\nv_fma_f32 v230, v230, v219, v219\n"
"v_exp_f32 v233, v233\nM 136,172,88\ns_add_u32 s63, s58, 65536\nv_rcp_f32 v230, v230\nv_fma_f32 v231, v231, v219, v219\nM 152,168,76\n"
"R 166,184,61440\nv_fma_f32 v232, v232, v219, v219\nv_fma_f32 v233, v233, v219, v219\nM 152,172,92\nG 4\nv_rcp_f32 v231, v231\n"
"v_rcp_f32 v232, v232\nM 156,176,76\nR 168,185,49152\nv_mul_f32 v230, v48, v230\nv_rcp_f32 v233, v233\nM 156,180,92\n"
"buffer_load_dwordx4 v220, s[16:19], s63 offen lds\nv_mul_f32 v231, v49, v231\nM 140,176,72\nR 170,185,53248\nv_mul_f32 v232, v50, v232\nv_mul_f32 v233, v51, v233\n"
"M 140,180,88\nv_cvt_pk_bf16_f32 v48, v230, v231\nv_cvt_pk_bf16_f32 v49, v232, v233\nv_mul_f32 v234, v52, v52\nM 124,176,68\nR 172,185,57344\n"
"v_mul_f32 v235, v53, v53\nv_mul_f32 v236, v54, v54\nM 124,180,84\nbuffer_load_dwordx4 v221, s[16:19], s63 offen offset:1024 lds\nv_mul_f32 v237, v55, v55\nM 108,176,64\n"
"R 174,185,61440\ns_add_u32 m0, s50, 0\nv_fma_f32 v234, v234, v218, v217\nM 108,180,80\nG 4\nv_fma_f32 v235, v235, v218, v217\n"
"v_fma_f32 v236, v236, v218, v217\nv_fma_f32 v237, v237, v218, v217\nM 96,160,96\nR 176,186,49152\ns_add_u32 s63, s58, 589824\nv_mul_f32 v234, v52, v234\n"
"s_waitcnt vmcnt(16)\nM 96,164,112\nbuffer_load_dwordx4 v220, s[16:19], s63 offen lds\nv_mul_f32 v235, v53, v235\nM 112,160,100\nR 178,186,53248\n"
"v_mul_f32 v236, v54, v236\nv_mul_f32 v237, v55, v237\ns_waitcnt vmcnt(15)\nM 112,164,116\nv_exp_f32 v234, v234\nv_exp_f32 v235, v235\n"
"M 128,160,104\nR 180,186,57344\nv_fma_f32 v234, v234, v219, v219\nv_exp_f32 v236, v236\ns_waitcnt vmcnt(13)\nM 128,164,120\n"
"v_exp_f32 v237, v237\nv_fma_f32 v235, v235, v219, v219\nM 144,160,108\nR 182,186,61440\nv_fma_f32 v236, v236, v219, v219\nv_fma_f32 v237, v237, v219, v219\n"
"s_waitcnt vmcnt(9)\nM 144,164,124\nG 4\nv_rcp_f32 v234, v234\nv_rcp_f32 v235, v235\nM 148,168,108\n"
"R 160,187,49152\nv_mul_f32 v234, v52, v234\nv_rcp_f32 v236, v236\nM 148,172,124\ns_add_u32 s64, s68, 37754880\nv_rcp_f32 v237, v237\n"
"v_mul_f32 v235, v53, v235\nM 132,168,104\nR 162,187,53248\nv_cmp_eq_u64 s[86:87], s[46:47], v[248:249]\nv_mul_f32 v236, v54, v236\nM 132,172,120\n"
"buffer_load_dwordx4 v221, s[16:19], s63 offen offset:1024 lds\nv_mul_f32 v237, v55, v237\nM 116,168,100\nR 164,187,57344\ns_and_b32 s62, s86, 0xf\ns_cmp_eq_u32 s62, 0xf\n"
"s_cbranch_scc0 Lq908\nLq909:\nv_cvt_pk_bf16_f32 v50, v234, v235\nM 116,172,116\ns_add_u32 s65, s68, 37869568\nv_cvt_pk_bf16_f32 v51, v236, v237\n"
"v_mul_f32 v230, v56, v56\nv_mul_f32 v231, v57, v57\nM 100,168,96\nR 166,187,61440\nv_mul_f32 v232, v58, v58\nv_mul_f32 v233, v59, v59\n"
"M 100,172,112\nG 4\nv_fma_f32 v230, v230, v218, v217\nv_fma_f32 v231, v231, v218, v217\nM 104,176,96\nR 168,188,0\n"
"v_fma_f32 v232, v232, v218, v217\nM 104,180,112\nbuffer_load_dwordx4 a[128:131], v252, s[20:23], s64 offen sc1\nM 120,176,100\nR 170,188,4096\nv_fma_f32 v233, v233, v218, v217\n"
"M 120,180,116\ns_add_u32 s66, s69, 37754880\nv_mul_f32 v230, v56, v230\nv_mul_f32 v231, v57, v231\nM 136,176,104\nR 172,188,8192\n"
"v_mul_f32 v232, v58, v232\nM 136,180,120\nbuffer_load_dwordx4 a[132:135], v252, s[20:23], s65 offen sc1\nM 152,176,108\nR 174,188,12288\nv_mul_f32 v233, v59, v233\n"
"M 152,180,124\nG 4\nv_exp_f32 v230, v230\nM 156,160,108\nR 176,189,0\nv_fma_f32 v230, v230, v219, v219\n"
"M 156,164,124\ns_add_u32 s67, s69, 37869568\nv_exp_f32 v231, v231\nM 140,160,104\nR 178,189,4096\nv_fma_f32 v231, v231, v219, v219\n"
"M 140,164,120\nbuffer_load_dwordx4 a[136:139], v252, s[20:23], s66 offen sc1\nM 124,160,100\nR 180,189,8192\nv_mul_f32 v234, v60, v60\nM 124,164,116\n"
"buffer_store_dwordx4 v[48:51], v252, s[20:23], s85 offen offset:0 sc1\nM 108,160,96\nR 182,189,12288\ns_add_u32 m0, s50, 8192\nM 108,164,112\ns_waitcnt vmcnt(9)\n"
"s_barrier\nG 4\nM 0,168,64\nLpg30:\ns_cbranch_execz LretO1n\nR 160,190,0\n"
"s_add_u32 s63, s58, 131072\nM 0,172,80\nbuffer_load_dwordx4 a[140:143], v252, s[20:23], s67 offen sc1\nM 16,168,68\nR 162,190,4096\nv_mul_f32 v235, v61, v61\n"
"M 16,172,84\ns_add_u32 s64, s68, 37762048\nv_exp_f32 v232, v232\nv_mfma_f32_16x16x32_bf16 a[224:227], v[168:171], a[72:75], a[224:227]\nR 164,190,8192\nbuffer_load_dwordx4 v220, s[16:19], s63 offen lds\n"
"v_mfma_f32_16x16x32_bf16 a[224:227], v[172:175], a[88:91], a[224:227]\ns_add_u32 s65, s68, 37876736\nv_exp_f32 v233, v233\nv_mfma_f32_16x16x32_bf16 a[240:243], v[168:171], a[76:79], a[240:243]\nR 166,190,12288\nv_fma_f32 v232, v232, v219, v219\n"
"v_mfma_f32_16x16x32_bf16 a[240:243], v[172:175], a[92:95], a[240:243]\nG 4\nv_rcp_f32 v230, v230\nv_mfma_f32_16x16x32_bf16 a[244:247], v[176:179], a[76:79], a[244:247]\nR 168,191,0\nbuffer_load_dwordx4 v221, s[16:19], s63 offen offset:1024 lds\n"
"v_mfma_f32_16x16x32_bf16 a[244:247], v[180:183], a[92:95], a[244:247]\nbuffer_load_dwordx4 a[144:147], v252, s[20:23], s64 offen sc1\nv_mfma_f32_16x16x32_bf16 a[228:231], v[176:179], a[72:75], a[228:231]\nR 170,191,4096\ns_add_u32 m0, s50, 16384\nv_mfma_f32_16x16x32_bf16 a[228:231], v[180:183], a[88:91], a[228:231]\n"
"s_add_u32 s66, s69, 37762048\ns_add_u32 s63, s58, 655360\nv_fma_f32 v233, v233, v219, v219\nM 20,176,68\nR 172,191,8192\nbuffer_load_dwordx4 v220, s[16:19], s63 offen lds\n"
"M 20,180,84\nbuffer_load_dwordx4 a[148:151], v252, s[20:23], s65 offen sc1\nM 4,176,64\nR 174,191,12288\nv_mul_f32 v230, v56, v230\nM 4,180,80\n"
"G 4\nv_rcp_f32 v231, v231\nM 8,160,64\nR 176,188,16384\nbuffer_load_dwordx4 v221, s[16:19], s63 offen offset:1024 lds\nM 8,164,80\n"
"s_add_u32 s67, s69, 37876736\nv_rcp_f32 v232, v232\nM 24,160,68\nR 178,188,20480\nv_mul_f32 v231, v57, v231\nM 24,164,84\n"
"buffer_load_dwordx4 a[152:155], v252, s[20:23], s66 offen sc1\nv_mfma_f32_16x16x32_bf16 a[232:235], v[160:163], a[72:75], a[232:235]\nR 180,188,24576\nv_mul_f32 v232, v58, v232\nv_mfma_f32_16x16x32_bf16 a[232:235], v[164:167], a[88:91], a[232:235]\nv_rcp_f32 v233, v233\n"
"v_mfma_f32_16x16x32_bf16 a[248:251], v[160:163], a[76:79], a[248:251]\nR 182,188,28672\nv_mul_f32 v233, v59, v233\nv_mfma_f32_16x16x32_bf16 a[248:251], v[164:167], a[92:95], a[248:251]\nG 4\nv_cvt_pk_bf16_f32 v56, v230, v231\n"
"v_cvt_pk_bf16_f32 v57, v232, v233\nv_mfma_f32_16x16x32_bf16 a[252:255], v[168:171], a[76:79], a[252:255]\nR 160,189,16384\nv_mul_f32 v236, v62, v62\nv_mfma_f32_16x16x32_bf16 a[252:255], v[172:175], a[92:95], a[252:255]\nbuffer_load_dwordx4 a[156:159], v252, s[20:23], s67 offen sc1\n"
"v_mfma_f32_16x16x32_bf16 a[236:239], v[168:171], a[72:75], a[236:239]\nR 162,189,20480\nv_mul_f32 v237, v63, v63\nv_mfma_f32_16x16x32_bf16 a[236:239], v[172:175], a[88:91], a[236:239]\nv_fma_f32 v234, v234, v218, v217\nv_fma_f32 v235, v235, v218, v217\n"
"M 28,168,68\nR 164,189,24576\nv_fma_f32 v236, v236, v218, v217\nM 28,172,84\ns_add_u32 m0, s50, 24576\nv_fma_f32 v237, v237, v218, v217\n"
"v_mul_f32 v234, v60, v234\nM 12,168,64\nR 166,189,28672\nv_mul_f32 v235, v61, v235\nM 12,172,80\nG 4\n"
"v_exp_f32 v234, v234\nM 0,176,96\nR 168,190,16384\nv_mul_f32 v236, v62, v236\nM 0,180,112\ns_add_u32 s63, s58, 196608\n"
"v_exp_f32 v235, v235\nM 16,176,100\nR 170,190,20480\nv_mul_f32 v237, v63, v237\nM 16,180,116\nbuffer_load_dwordx4 v220, s[16:19], s63 offen lds\n"
"v_mfma_f32_16x16x32_bf16 a[224:227], v[176:179], a[104:107], a[224:227]\nR 172,190,24576\nv_fma_f32 v234, v234, v219, v219\nv_mfma_f32_16x16x32_bf16 a[224:227], v[180:183], a[120:123], a[224:227]\nv_exp_f32 v236, v236\nv_mfma_f32_16x16x32_bf16 a[240:243], v[176:179], a[108:111], a[240:243]\n"
"R 174,190,28672\nv_fma_f32 v235, v235, v219, v219\nv_mfma_f32_16x16x32_bf16 a[240:243], v[180:183], a[124:127], a[240:243]\nG 4\nv_exp_f32 v237, v237\nv_mfma_f32_16x16x32_bf16 a[244:247], v[160:163], a[108:111], a[244:247]\n"
"R 176,191,16384\nv_fma_f32 v236, v236, v219, v219\nv_mfma_f32_16x16x32_bf16 a[244:247], v[164:167], a[124:127], a[244:247]\nbuffer_load_dwordx4 v221, s[16:19], s63 offen offset:1024 lds\nv_mfma_f32_16x16x32_bf16 a[228:231], v[160:163], a[104:107], a[228:231]\nR 178,191,20480\n"
"s_add_u32 s63, s58, 720896\nv_mfma_f32_16x16x32_bf16 a[228:231], v[164:167], a[120:123], a[228:231]\ns_add_u32 m0, s50, 32768\nv_rcp_f32 v234, v234\nM 20,160,100\nR 180,191,24576\n"
"v_fma_f32 v237, v237, v219, v219\nM 20,164,116\nbuffer_load_dwordx4 v220, s[16:19], s63 offen lds\nM 4,160,96\nR 182,191,28672\nv_mul_f32 v234, v60, v234\n"
"M 4,164,112\nG 4\nv_rcp_f32 v235, v235\nM 8,168,96\nR 160,188,32768\nv_mul_f32 v235, v61, v235\n"
"M 8,172,112\nbuffer_load_dwordx4 v221, s[16:19], s63 offen offset:1024 lds\nM 24,168,100\nR 162,188,36864\ns_add_u32 s63, s58, 393216\nM 24,172,116\n"
"s_add_u32 m0, s50, 40960\nv_rcp_f32 v236, v236\nv_mfma_f32_16x16x32_bf16 a[232:235], v[168:171], a[104:107], a[232:235]\nR 164,188,40960\nv_mul_f32 v236, v62, v236\nv_mfma_f32_16x16x32_bf16 a[232:235], v[172:175], a[120:123], a[232:235]\n"
"buffer_load_dwordx4 v220, s[16:19], s63 offen lds\nv_mfma_f32_16x16x32_bf16 a[248:251], v[168:171], a[108:111], a[248:251]\nR 166,188,45056\nv_cvt_pk_bf16_f32 v58, v234, v235\nv_mfma_f32_16x16x32_bf16 a[248:251], v[172:175], a[124:127], a[248:251]\nG 4\n"
"v_rcp_f32 v237, v237\nv_mfma_f32_16x16x32_bf16 a[252:255], v[176:179], a[108:111], a[252:255]\nR 168,189,32768\nv_mul_f32 v237, v63, v237\nv_mfma_f32_16x16x32_bf16 a[252:255], v[180:183], a[124:127], a[252:255]\nbuffer_load_dwordx4 v221, s[16:19], s63 offen offset:1024 lds\n"
"v_mfma_f32_16x16x32_bf16 a[236:239], v[176:179], a[104:107], a[236:239]\nR 170,189,36864\ns_add_u32 s63, s58, 458752\nv_mfma_f32_16x16x32_bf16 a[236:239], v[180:183], a[120:123], a[236:239]\ns_add_u32 m0, s50, 49152\nv_cvt_pk_bf16_f32 v59, v236, v237\n"
"buffer_store_dwordx4 v[56:59], v252, s[20:23], s85 offen offset:1024 sc1\nM 28,176,100\nR 172,189,40960\nM 28,180,116\nbuffer_load_dwordx4 v220, s[16:19], s63 offen lds\nM 12,176,96\n"
"R 174,189,45056\nM 12,180,112\ns_waitcnt vmcnt(10)\ns_barrier\nG 4\nv_mfma_f32_16x16x32_bf16 v[32:35], v[160:163], a[0:3], 0\n"
"R 176,190,32768\nM 32,164,16\nv_mfma_f32_16x16x32_bf16 v[48:51], v[160:163], a[4:7], 0\nR 178,190,36864\nM 48,164,20\nbuffer_load_dwordx4 v221, s[16:19], s63 offen offset:1024 lds\n"
"v_mfma_f32_16x16x32_bf16 v[64:67], v[160:163], a[8:11], 0\nR 180,190,40960\ns_add_u32 s63, s56, 393216\nM 64,164,24\ns_add_u32 m0, s50, 57344\nv_mfma_f32_16x16x32_bf16 v[80:83], v[160:163], a[12:15], 0\n"
"R 182,190,45056\nM 80,164,28\nG 4\nv_mfma_f32_16x16x32_bf16 v[84:87], v[168:171], a[12:15], 0\nR 160,191,32768\nM 84,172,28\n"
"buffer_load_dwordx4 v220, s[16:19], s63 offen lds\nv_mfma_f32_16x16x32_bf16 v[68:71], v[168:171], a[8:11], 0\nR 162,191,36864\nM 68,172,24\nv_mfma_f32_16x16x32_bf16 v[52:55], v[168:171], a[4:7], 0\nR 164,191,40960\n"
"M 52,172,20\nv_mfma_f32_16x16x32_bf16 v[36:39], v[168:171], a[0:3], 0\nR 166,191,45056\nM 36,172,16\nG 4\nv_mfma_f32_16x16x32_bf16 v[40:43], v[176:179], a[0:3], 0\n"
"R 168,188,49152\nM 40,180,16\nbuffer_load_dwordx4 v221, s[16:19], s63 offen offset:1024 lds\nv_mfma_f32_16x16x32_bf16 v[56:59], v[176:179], a[4:7], 0\nR 170,188,53248\nM 56,180,20\n"
"s_add_u32 m0, s50, 65536\nv_mfma_f32_16x16x32_bf16 v[72:75], v[176:179], a[8:11], 0\nR 172,184,0\nM 72,180,24\ns_add_u32 s63, s56, 458752\nv_mfma_f32_16x16x32_bf16 v[88:91], v[176:179], a[12:15], 0\n"
"R 174,184,4096\nM 88,180,28\nG 4\nv_mfma_f32_16x16x32_bf16 v[92:95], v[160:163], a[12:15], 0\nR 176,189,49152\nM 92,164,28\n"
"buffer_load_dwordx4 v220, s[16:19], s63 offen lds\nv_mfma_f32_16x16x32_bf16 v[76:79], v[160:163], a[8:11], 0\nR 178,189,53248\nM 76,164,24\nv_mfma_f32_16x16x32_bf16 v[60:63], v[160:163], a[4:7], 0\nR 180,185,0\n"
"M 60,164,20\nv_mfma_f32_16x16x32_bf16 v[44:47], v[160:163], a[0:3], 0\nR 182,185,4096\nM 44,164,16\nG 4\nM 32,168,32\n"
"R 160,190,49152\nM 32,172,48\nbuffer_load_dwordx4 v221, s[16:19], s63 offen offset:1024 lds\nM 48,168,36\nR 162,190,53248\nM 48,172,52\n"
"s_add_u32 m0, s50, 73728\nM 64,168,40\nR 164,186,0\nM 64,172,56\ns_add_u32 s63, s57, 393216\nM 80,168,44\n"
"R 166,186,4096\nM 80,172,60\nG 4\nM 84,176,44\nR 168,191,49152\nM 84,180,60\n"
"buffer_load_dwordx4 v220, s[16:19], s63 offen lds\nM 68,176,40\nR 170,191,53248\nv_mov_b32 v226, s46\nM 68,180,56\nv_mov_b32 v227, s47\n"
"M 52,176,36\nR 172,187,0\ns_add_u32 s61, s81, 845840\ns_lshl_b32 s62, s44, 5\ns_add_u32 s61, s61, s62\ns_waitcnt vmcnt(7)\n"
"s_mov_b64 exec, 1\nbuffer_atomic_swap_x2 v[226:227], off, s[20:23], s61 sc1\ns_mov_b64 exec, -1\nM 52,180,52\nM 36,176,32\nR 174,187,4096\n"
"M 36,180,48\nG 4\nM 40,160,32\nR 176,184,8192\nM 40,164,48\nbuffer_load_dwordx4 v221, s[16:19], s63 offen offset:1024 lds\n"
"M 56,160,36\nR 178,184,12288\ns_add_u32 s63, s57, 458752\nM 56,164,52\ns_add_u32 m0, s50, 81920\nM 72,160,40\n"
"R 180,184,16384\nM 72,164,56\nbuffer_load_dwordx4 v220, s[16:19], s63 offen lds\nM 88,160,44\nR 182,184,20480\nM 88,164,60\n"
"G 4\nM 92,168,44\nR 160,185,8192\nM 92,172,60\nbuffer_load_dwordx4 v221, s[16:19], s63 offen offset:1024 lds\nM 76,168,40\n"
"R 162,185,12288\ns_add_u32 s63, s56, 262144\nM 76,172,56\ns_add_u32 m0, s50, 90112\nM 60,168,36\nR 164,185,16384\n"
"M 60,172,52\nbuffer_load_dwordx4 v220, s[16:19], s63 offen lds\nM 44,168,32\nR 166,185,20480\nM 44,172,48\ns_waitcnt vmcnt(10)\n"
"s_barrier\nG 4\nM 32,176,64\nR 168,186,8192\nM 32,180,80\nM 48,176,68\n"
"R 170,186,12288\nM 48,180,84\nbuffer_load_dwordx4 v221, s[16:19], s63 offen offset:1024 lds\nM 64,176,72\nR 172,186,16384\ns_add_u32 s63, s56, 786432\n"
"M 64,180,88\ns_add_u32 m0, s50, 98304\nM 80,176,76\nR 174,186,20480\nM 80,180,92\nG 4\n"
"M 84,160,76\nR 176,187,8192\nM 84,164,92\nbuffer_load_dwordx4 v220, s[16:19], s63 offen lds\nM 68,160,72\nR 178,187,12288\n"
"M 68,164,88\nM 52,160,68\nR 180,187,16384\nv_mov_b32 v248, 0\nM 52,164,84\nv_mov_b32 v249, 0\n"
"M 36,160,64\nR 182,187,20480\ns_add_u32 s61, s82, 845840\ns_mov_b64 exec, 0xf\nbuffer_atomic_or_x2 v[248:249], v250, s[20:23], s61 offen sc0 sc1\ns_mov_b64 exec, -1\n"
"M 36,164,80\nG 4\nM 40,168,64\nR 160,184,24576\nM 40,172,80\nbuffer_load_dwordx4 v221, s[16:19], s63 offen offset:1024 lds\n"
"M 56,168,68\nR 162,184,28672\nM 56,172,84\ns_add_u32 m0, s50, 106496\nM 72,168,72\nR 164,184,32768\n"
"M 72,172,88\ns_add_u32 s63, s56, 327680\nM 88,168,76\nR 166,184,36864\nM 88,172,92\nG 4\n"
"M 92,176,76\nR 168,185,24576\nM 92,180,92\nbuffer_load_dwordx4 v220, s[16:19], s63 offen lds\nM 76,176,72\nR 170,185,28672\n"
"M 76,180,88\nM 60,176,68\nR 172,185,32768\nM 60,180,84\nM 44,176,64\nR 174,185,36864\n"
"M 44,180,80\nG 4\nM 32,160,96\nR 176,186,24576\nM 32,164,112\nbuffer_load_dwordx4 v221, s[16:19], s63 offen offset:1024 lds\n"
"M 48,160,100\nR 178,186,28672\nM 48,164,116\ns_add_u32 m0, s50, 114688\nM 64,160,104\nR 180,186,32768\n"
"M 64,164,120\ns_add_u32 s63, s56, 851968\nM 80,160,108\nR 182,186,36864\nM 80,164,124\nG 4\n"
"M 84,168,108\nR 160,187,24576\nM 84,172,124\nbuffer_load_dwordx4 v220, s[16:19], s63 offen lds\nM 68,168,104\nR 162,187,28672\n"
"M 68,172,120\nM 52,168,100\nR 164,187,32768\nM 52,172,116\nbuffer_load_dwordx4 v221, s[16:19], s63 offen offset:1024 lds\nM 36,168,96\n"
"R 166,187,36864\ns_add_u32 m0, s50, 0\nM 36,172,112\nG 4\nM 40,176,96\nR 168,184,40960\n"
"s_add_u32 s63, s57, 262144\nM 40,180,112\nbuffer_load_dwordx4 v220, s[16:19], s63 offen lds\nM 56,176,100\nR 170,184,45056\nM 56,180,116\n"
"M 72,176,104\nR 172,184,49152\nM 72,180,120\nbuffer_load_dwordx4 v221, s[16:19], s63 offen offset:1024 lds\nM 88,176,108\nR 174,184,53248\n"
"s_waitcnt vmcnt(16)\ns_barrier\nM 88,180,124\nG 4\nM 92,160,108\nR 176,185,40960\n"
"M 92,164,124\ns_add_u32 s64, s68, 37752832\nM 76,160,104\nR 178,185,45056\nM 76,164,120\nM 60,160,100\n"
"R 180,185,49152\ns_waitcnt vmcnt(7)\nv_cmp_eq_u64 s[86:87], s[46:47], v[248:249]\nM 60,164,116\ns_add_u32 s65, s68, 37867520\nM 44,160,96\n"
"R 182,185,53248\ns_and_b32 s62, s86, 0xf\ns_cmp_eq_u32 s62, 0xf\ns_cbranch_scc0 Lq927\nLq928:\nM 44,164,112\n"
"G 4\nM 32,168,128\nR 160,186,40960\nM 32,172,144\nbuffer_load_dwordx4 a[160:163], v252, s[20:23], s64 offen sc1\nM 48,168,132\n"
"R 162,186,45056\nM 48,172,148\ns_add_u32 s66, s69, 37752832\nM 64,168,136\nR 164,186,49152\nM 64,172,152\n"
"buffer_load_dwordx4 a[164:167], v252, s[20:23], s65 offen sc1\nM 80,168,140\nR 166,186,53248\nM 80,172,156\nG 4\nM 84,176,140\n"
"R 168,187,40960\nM 84,180,156\ns_add_u32 s67, s69, 37867520\nM 68,176,136\nR 170,187,45056\nM 68,180,152\n"
"buffer_load_dwordx4 a[168:171], v252, s[20:23], s66 offen sc1\nM 52,176,132\nR 172,187,49152\nM 52,180,148\ns_add_u32 s64, s68, 37760000\nM 36,176,128\n"
"R 174,187,53248\nM 36,180,144\nG 4\nM 40,160,128\nR 176,184,57344\nM 40,164,144\n"
"buffer_load_dwordx4 a[172:175], v252, s[20:23], s67 offen sc1\nM 56,160,132\nR 178,184,61440\nM 56,164,148\ns_add_u32 s65, s68, 37874688\nM 72,160,136\n"
"R 180,188,0\nM 72,164,152\nbuffer_load_dwordx4 a[176:179], v252, s[20:23], s64 offen sc1\nM 88,160,140\nR 182,188,4096\ns_add_u32 m0, s50, 8192\n"
"M 88,164,156\nG 4\nM 92,168,140\nR 160,185,57344\ns_add_u32 s63, s57, 786432\nM 92,172,156\n"
"s_add_u32 s66, s69, 37760000\nM 76,168,136\nR 162,185,61440\nM 76,172,152\nbuffer_load_dwordx4 a[180:183], v252, s[20:23], s65 offen sc1\nbuffer_load_dwordx4 v220, s[16:19], s63 offen lds\n"
"M 60,168,132\nR 164,189,0\nM 60,172,148\ns_add_u32 s67, s69, 37874688\nM 44,168,128\nR 166,189,4096\n"
"M 44,172,144\nbuffer_load_dwordx4 v221, s[16:19], s63 offen offset:1024 lds\nG 4\nM 96,176,128\nR 168,186,57344\ns_add_u32 m0, s50, 16384\n"
"M 96,180,144\nbuffer_load_dwordx4 a[184:187], v252, s[20:23], s66 offen sc1\nM 112,176,132\nR 170,186,61440\ns_add_u32 s63, s57, 327680\nM 112,180,148\n"
"s_add_u32 s64, s68, 37753856\nbuffer_load_dwordx4 v220, s[16:19], s63 offen lds\nM 128,176,136\nR 172,190,0\nM 128,180,152\nbuffer_load_dwordx4 a[188:191], v252, s[20:23], s67 offen sc1\n"
"M 144,176,140\nR 174,190,4096\nM 144,180,156\nbuffer_load_dwordx4 v221, s[16:19], s63 offen offset:1024 lds\nG 4\nM 148,160,140\n"
"R 176,187,57344\ns_waitcnt vmcnt(18)\ns_barrier\nM 148,164,156\ns_add_u32 s65, s68, 37868544\ns_add_u32 s63, s57, 851968\n"
"M 132,160,136\nR 178,187,61440\ns_add_u32 m0, s50, 24576\nM 132,164,152\nbuffer_load_dwordx4 a[192:195], v252, s[20:23], s64 offen sc1\nM 116,160,132\n"
"R 180,191,0\nLpg31:\ns_cbranch_execz LretO1n\nbuffer_load_dwordx4 v220, s[16:19], s63 offen lds\nM 116,164,148\ns_add_u32 s66, s69, 37753856\n"
"M 100,160,128\nR 182,191,4096\nM 100,164,144\nG 4\nM 104,168,128\nR 160,188,8192\n"
"buffer_load_dwordx4 v221, s[16:19], s63 offen offset:1024 lds\nM 104,172,144\nbuffer_load_dwordx4 a[196:199], v252, s[20:23], s65 offen sc1\nM 120,168,132\nR 162,188,12288\nM 120,172,148\n"
"s_add_u32 s67, s69, 37868544\nM 136,168,136\nR 164,188,16384\nM 136,172,152\nbuffer_load_dwordx4 a[200:203], v252, s[20:23], s66 offen sc1\nM 152,168,140\n"
"R 166,188,20480\nM 152,172,156\nG 4\nM 156,176,140\nR 168,189,8192\nM 156,180,156\n"
"buffer_load_dwordx4 a[204:207], v252, s[20:23], s67 offen sc1\nM 140,176,136\nR 170,189,12288\nM 140,180,152\ns_add_u32 s64, s68, 37761024\nM 124,176,132\n"
"R 172,189,16384\nM 124,180,148\ns_add_u32 s65, s68, 37875712\nM 108,176,128\nR 174,189,20480\nM 108,180,144\n"
"G 4\nM 0,160,128\nR 176,190,8192\nM 0,164,144\nbuffer_load_dwordx4 a[208:211], v252, s[20:23], s64 offen sc1\nM 16,160,132\n"
"R 178,190,12288\nM 16,164,148\ns_add_u32 s66, s69, 37761024\nv_mfma_f32_16x16x32_bf16 a[224:227], v[160:163], a[136:139], a[224:227]\nR 180,190,16384\nv_mfma_f32_16x16x32_bf16 a[224:227], v[164:167], a[152:155], a[224:227]\n"
"buffer_load_dwordx4 a[212:215], v252, s[20:23], s65 offen sc1\nv_mfma_f32_16x16x32_bf16 a[240:243], v[160:163], a[140:143], a[240:243]\nR 182,190,20480\nv_mfma_f32_16x16x32_bf16 a[240:243], v[164:167], a[156:159], a[240:243]\nG 4\nv_mfma_f32_16x16x32_bf16 a[244:247], v[168:171], a[140:143], a[244:247]\n"
"R 160,191,8192\nv_mfma_f32_16x16x32_bf16 a[244:247], v[172:175], a[156:159], a[244:247]\nbuffer_load_dwordx4 a[216:219], v252, s[20:23], s66 offen sc1\nv_mfma_f32_16x16x32_bf16 a[228:231], v[168:171], a[136:139], a[228:231]\nR 162,191,12288\nv_mfma_f32_16x16x32_bf16 a[228:231], v[172:175], a[152:155], a[228:231]\n"
"s_add_u32 s67, s69, 37875712\nM 20,168,132\nR 164,191,16384\nM 20,172,148\nM 4,168,128\nR 166,191,20480\n"
"M 4,172,144\nG 4\nM 8,176,128\nR 168,188,24576\nM 8,180,144\nbuffer_load_dwordx4 a[220:223], v252, s[20:23], s67 offen sc1\n"
"M 24,176,132\nR 170,188,28672\nM 24,180,148\ns_add_u32 m0, s50, 32768\nv_mfma_f32_16x16x32_bf16 a[232:235], v[176:179], a[136:139], a[232:235]\nR 172,188,32768\n"
"v_mfma_f32_16x16x32_bf16 a[232:235], v[180:183], a[152:155], a[232:235]\ns_add_u32 s63, s58, 262144\nv_mfma_f32_16x16x32_bf16 a[248:251], v[176:179], a[140:143], a[248:251]\nR 174,188,36864\nv_mfma_f32_16x16x32_bf16 a[248:251], v[180:183], a[156:159], a[248:251]\nG 4\n"
"v_mfma_f32_16x16x32_bf16 a[252:255], v[160:163], a[140:143], a[252:255]\nR 176,189,24576\nv_mfma_f32_16x16x32_bf16 a[252:255], v[164:167], a[156:159], a[252:255]\nbuffer_load_dwordx4 v220, s[16:19], s63 offen lds\nv_mfma_f32_16x16x32_bf16 a[236:239], v[160:163], a[136:139], a[236:239]\nR 178,189,28672\n"
"v_mfma_f32_16x16x32_bf16 a[236:239], v[164:167], a[152:155], a[236:239]\nM 28,160,132\nR 180,189,32768\nM 28,164,148\nbuffer_load_dwordx4 v221, s[16:19], s63 offen offset:1024 lds\nM 12,160,128\n"
"R 182,189,36864\nM 12,164,144\ns_waitcnt vmcnt(9)\ns_barrier\nG 4\nM 96,168,160\n"
"R 160,190,24576\nM 96,172,176\ns_add_u32 m0, s50, 40960\nM 112,168,164\nR 162,190,28672\nM 112,172,180\n"
"s_add_u32 s63, s58, 786432\nM 128,168,168\nR 164,190,32768\nv_lshrrev_b32 v230, 8, v252\nM 128,172,184\nbuffer_load_dwordx4 v220, s[16:19], s63 offen lds\n"
"M 144,168,172\nR 166,190,36864\nv_lshlrev_b32 v230, 3, v230\nM 144,172,188\nG 4\nM 148,176,172\n"
"R 168,191,24576\nM 148,180,188\nbuffer_load_dwordx2 v[232:233], v230, s[36:39], s53 offen offset:0\nM 132,176,168\nR 170,191,28672\nM 132,180,184\n"
"buffer_load_dwordx2 v[234:235], v230, s[36:39], s53 offen offset:32\nM 116,176,164\nR 172,191,32768\nM 116,180,180\nbuffer_load_dwordx4 v221, s[16:19], s63 offen offset:1024 lds\nM 100,176,160\n"
"R 174,191,36864\nM 100,180,176\nG 4\nM 104,160,160\nR 176,188,40960\nM 104,164,176\n"
"s_add_u32 m0, s50, 49152\nM 120,160,164\nR 178,188,45056\nM 120,164,180\ns_add_u32 s63, s58, 327680\nM 136,160,168\n"
"R 180,188,49152\nbuffer_load_dwordx2 v[236:237], v230, s[36:39], s53 offen offset:64\nM 136,164,184\nbuffer_load_dwordx4 v220, s[16:19], s63 offen lds\nM 152,160,172\nR 182,188,53248\n"
"M 152,164,188\nbuffer_load_dwordx2 v[238:239], v230, s[36:39], s53 offen offset:96\nG 4\nM 156,168,172\nR 160,189,40960\nv_bfe_u32 v242, v252, 4, 4\n"
"M 156,172,188\nv_lshrrev_b32 v243, 8, v252\nv_lshlrev_b32 v243, 2, v243\nM 140,168,168\nR 162,189,45056\nv_sub_u32 v242, v242, v243\n"
"M 140,172,184\nv_lshlrev_b32 v243, 4, v242\ns_mov_b64 s[60:61], 0x3f80\nv_lshlrev_b64 v[208:209], v243, s[60:61]\nM 124,168,164\nR 164,189,49152\n"
"M 124,172,180\nbuffer_load_dwordx4 v221, s[16:19], s63 offen offset:1024 lds\nM 108,168,160\nR 166,189,53248\nM 108,172,176\nG 4\n"
"M 96,176,192\nR 168,190,40960\ns_waitcnt vmcnt(13)\nM 96,180,208\ns_add_u32 m0, s50, 57344\nM 112,176,196\n"
"R 170,190,45056\ns_waitcnt vmcnt(12)\nM 112,180,212\ns_add_u32 s63, s58, 851968\nM 128,176,200\nR 172,190,49152\n"
"s_waitcnt vmcnt(11)\nM 128,180,216\nbuffer_load_dwordx4 v220, s[16:19], s63 offen lds\nM 144,176,204\nR 174,190,53248\ns_waitcnt vmcnt(11)\n"
"M 144,180,220\nG 4\nM 148,160,204\nR 176,191,40960\nM 148,164,220\nv_cmp_gt_u32 s[60:61], 4, v242\n"
"s_nop 1\nv_cndmask_b32 v212, 0, v208, s[60:61]\nv_cndmask_b32 v208, 0, v208, s[60:61]\nv_cndmask_b32 v213, 0, v209, s[60:61]\nv_cndmask_b32 v209, 0, v209, s[60:61]\nM 132,160,200\n"
"R 178,191,45056\nM 132,164,216\nM 116,160,196\nR 180,191,49152\nM 116,164,212\nbuffer_load_dwordx4 v221, s[16:19], s63 offen offset:1024 lds\n"
"M 100,160,192\nR 182,191,53248\nM 100,164,208\nG 4\nM 104,168,192\nR 160,184,0\n"
"M 104,172,208\ns_add_u32 m0, s50, 65536\nM 120,168,196\nR 162,184,4096\nM 120,172,212\ns_add_u32 s63, s59, 0\n"
"M 136,168,200\nR 164,184,8192\nM 136,172,216\nbuffer_load_dwordx4 v220, s[16:19], s63 offen lds\nM 152,168,204\nR 166,184,12288\n"
"M 152,172,220\nG 4\nM 156,176,204\nR 168,185,0\nM 156,180,220\nM 140,176,200\n"
"R 170,185,4096\nM 140,180,216\nbuffer_load_dwordx4 v221, s[16:19], s63 offen offset:1024 lds\nM 124,176,196\nR 172,185,8192\nM 124,180,212\n"
"M 108,176,192\nR 174,185,12288\nM 108,180,208\nG 4\nM 0,160,160\nR 176,186,0\n"
"M 0,164,176\ns_mov_b32 m0, s51\nM 16,160,164\nR 178,186,4096\nv_cvt_pk_bf16_f32 v128, v128, v129\nv_cvt_pk_bf16_f32 v129, v130, v131\n"
"M 16,164,180\nds_write_addtid_b32 v128 offset:0\nv_cvt_pk_bf16_f32 v130, v132, v133\nv_cvt_pk_bf16_f32 v131, v134, v135\nv_mfma_f32_16x16x32_bf16 a[224:227], v[160:163], a[168:171], a[224:227]\nR 180,186,8192\n"
"v_cvt_pk_bf16_f32 v132, v136, v137\nv_mfma_f32_16x16x32_bf16 a[224:227], v[164:167], a[184:187], a[224:227]\nds_write_addtid_b32 v129 offset:256\nv_cvt_pk_bf16_f32 v133, v138, v139\nv_mfma_f32_16x16x32_bf16 a[240:243], v[160:163], a[172:175], a[240:243]\nR 182,186,12288\n"
"v_cvt_pk_bf16_f32 v134, v140, v141\nv_mfma_f32_16x16x32_bf16 a[240:243], v[164:167], a[188:191], a[240:243]\nds_write_addtid_b32 v130 offset:512\nG 7\nv_cvt_pk_bf16_f32 v135, v142, v143\nv_mfma_f32_16x16x32_bf16 a[244:247], v[168:171], a[172:175], a[244:247]\n"
"R 160,187,0\nv_cvt_pk_bf16_f32 v136, v144, v145\nv_mfma_f32_16x16x32_bf16 a[244:247], v[172:175], a[188:191], a[244:247]\nds_write_addtid_b32 v131 offset:768\nv_cvt_pk_bf16_f32 v137, v146, v147\nv_mfma_f32_16x16x32_bf16 a[228:231], v[168:171], a[168:171], a[228:231]\n"
"R 162,187,4096\nv_cvt_pk_bf16_f32 v138, v148, v149\nv_mfma_f32_16x16x32_bf16 a[228:231], v[172:175], a[184:187], a[228:231]\nds_write_addtid_b32 v132 offset:1024\nv_cvt_pk_bf16_f32 v139, v150, v151\nM 20,168,164\n"
"R 164,187,8192\nv_cvt_pk_bf16_f32 v140, v152, v153\nM 20,172,180\nds_write_addtid_b32 v133 offset:1280\nv_cvt_pk_bf16_f32 v141, v154, v155\nM 4,168,160\n"
"R 166,187,12288\nv_cvt_pk_bf16_f32 v142, v156, v157\nM 4,172,176\nds_write_addtid_b32 v134 offset:1536\nG 9\nv_cvt_pk_bf16_f32 v143, v158, v159\n"
"M 8,176,160\nR 168,184,16384\nds_write_addtid_b32 v135 offset:1792\nM 8,180,176\nds_write_addtid_b32 v136 offset:2048\nds_write_addtid_b32 v137 offset:2304\n"
"M 24,176,164\nR 170,184,20480\nds_write_addtid_b32 v138 offset:2560\nM 24,180,180\nds_write_addtid_b32 v139 offset:2816\nds_write_addtid_b32 v140 offset:3072\n"
"v_mfma_f32_16x16x32_bf16 a[232:235], v[176:179], a[168:171], a[232:235]\nR 172,184,24576\nds_write_addtid_b32 v141 offset:3328\nv_mfma_f32_16x16x32_bf16 a[232:235], v[180:183], a[184:187], a[232:235]\nds_write_addtid_b32 v142 offset:3584\nds_write_addtid_b32 v143 offset:3840\n"
"v_mfma_f32_16x16x32_bf16 a[248:251], v[176:179], a[172:175], a[248:251]\nR 174,184,28672\ns_add_u32 m0, s50, 73728\nv_mfma_f32_16x16x32_bf16 a[248:251], v[180:183], a[188:191], a[248:251]\ns_add_u32 s63, s59, 524288\nG 14\n"
"v_mfma_f32_16x16x32_bf16 a[252:255], v[160:163], a[172:175], a[252:255]\nR 176,185,16384\ns_waitcnt vmcnt(8) lgkmcnt(2)\ns_barrier\nv_mfma_f32_16x16x32_bf16 a[252:255], v[164:167], a[188:191], a[252:255]\nbuffer_load_dwordx4 v220, s[16:19], s63 offen lds\n"
"v_mfma_f32_16x16x32_bf16 a[236:239], v[160:163], a[168:171], a[236:239]\nR 178,185,20480\nv_mfma_f32_16x16x32_bf16 a[236:239], v[164:167], a[184:187], a[236:239]\nds_read2st64_b32 v[128:129], v225 offset0:0 offset1:1\nM 28,160,164\nR 180,185,24576\n"
"M 28,164,180\nbuffer_load_dwordx4 v221, s[16:19], s63 offen offset:1024 lds\nM 12,160,160\nR 182,185,28672\ns_add_u32 m0, s50, 81920\nM 12,164,176\n"
"ds_read2st64_b32 v[130:131], v225 offset0:2 offset1:3\nG 6\nM 0,168,192\nR 160,186,16384\ns_add_u32 s63, s59, 65536\nM 0,172,208\n"
"buffer_load_dwordx4 v220, s[16:19], s63 offen lds\nds_read2st64_b32 v[132:133], v225 offset0:4 offset1:5\nM 16,168,196\nR 162,186,20480\nds_read2st64_b32 v[134:135], v225 offset0:6 offset1:7\nM 16,172,212\n"
"ds_read2st64_b32 v[136:137], v225 offset0:8 offset1:9\nv_mfma_f32_16x16x32_bf16 a[224:227], v[168:171], a[200:203], a[224:227]\nR 164,186,24576\nv_mfma_f32_16x16x32_bf16 a[224:227], v[172:175], a[216:219], a[224:227]\nbuffer_load_dwordx4 v221, s[16:19], s63 offen offset:1024 lds\nG 6\n"
"v_mfma_f32_16x16x32_bf16 v[96:99], v[208:211], v[128:131], v[96:99]\nv_mfma_f32_16x16x32_bf16 v[100:103], v[210:213], v[128:131], v[100:103]\nds_read2st64_b32 v[138:139], v225 offset0:10 offset1:11\nv_mfma_f32_16x16x32_bf16 a[240:243], v[168:171], a[204:207], a[240:243]\nR 166,186,28672\ns_add_u32 m0, s50, 90112\n"
"v_mfma_f32_16x16x32_bf16 a[240:243], v[172:175], a[220:223], a[240:243]\nds_read2st64_b32 v[140:141], v225 offset0:12 offset1:13\nv_mfma_f32_16x16x32_bf16 a[244:247], v[176:179], a[204:207], a[244:247]\nR 168,187,16384\ns_add_u32 s63, s59, 589824\nG 6\n"
"v_mfma_f32_16x16x32_bf16 v[104:107], v[208:211], v[132:135], v[104:107]\nv_mfma_f32_16x16x32_bf16 v[108:111], v[210:213], v[132:135], v[108:111]\nds_read2st64_b32 v[142:143], v225 offset0:14 offset1:15\nv_mfma_f32_16x16x32_bf16 a[244:247], v[180:183], a[220:223], a[244:247]\nbuffer_load_dwordx4 v220, s[16:19], s63 offen lds\nv_mfma_f32_16x16x32_bf16 a[228:231], v[176:179], a[200:203], a[228:231]\n"
"R 170,187,20480\nv_mfma_f32_16x16x32_bf16 a[228:231], v[180:183], a[216:219], a[228:231]\nG 5\nv_mfma_f32_16x16x32_bf16 v[112:115], v[208:211], v[136:139], v[112:115]\nv_mfma_f32_16x16x32_bf16 v[116:119], v[210:213], v[136:139], v[116:119]\nM 20,176,196\n"
"R 172,187,24576\nM 20,180,212\nbuffer_load_dwordx4 v221, s[16:19], s63 offen offset:1024 lds\nM 4,176,192\nR 174,187,28672\ns_add_u32 s63, s59, 131072\n"
"G 3\nv_mfma_f32_16x16x32_bf16 v[120:123], v[208:211], v[140:143], v[120:123]\nv_mfma_f32_16x16x32_bf16 v[124:127], v[210:213], v[140:143], v[124:127]\nM 4,180,208\nM 8,160,192\nR 176,184,32768\n"
"M 8,164,208\ns_add_u32 m0, s50, 98304\nM 24,160,196\nR 178,184,36864\nM 24,164,212\nbuffer_load_dwordx4 v220, s[16:19], s63 offen lds\n"
"v_mfma_f32_16x16x32_bf16 a[232:235], v[160:163], a[200:203], a[232:235]\nR 180,184,40960\nv_cvt_pk_bf16_f32 v120, v120, v121\nv_cvt_pk_bf16_f32 v112, v112, v113\nv_cvt_pk_bf16_f32 v121, v122, v123\nv_mfma_f32_16x16x32_bf16 a[232:235], v[164:167], a[216:219], a[232:235]\n"
"v_cvt_pk_bf16_f32 v122, v124, v125\nv_cvt_pk_bf16_f32 v123, v126, v127\nv_cvt_pk_bf16_f32 v96, v96, v97\nv_cvt_pk_bf16_f32 v113, v114, v115\nv_mfma_f32_16x16x32_bf16 a[248:251], v[160:163], a[204:207], a[248:251]\nR 182,184,45056\n"
"v_cvt_pk_bf16_f32 v114, v116, v117\nv_cvt_pk_bf16_f32 v97, v98, v99\nv_mfma_f32_16x16x32_bf16 a[248:251], v[164:167], a[220:223], a[248:251]\nG 4\nv_cvt_pk_bf16_f32 v98, v100, v101\nv_cvt_pk_bf16_f32 v99, v102, v103\n"
"v_mfma_f32_16x16x32_bf16 a[252:255], v[168:171], a[204:207], a[252:255]\nR 160,185,32768\ns_bfe_u32 s100, s56, 0x20007\ns_mul_i32 s100, s100, 49152\ns_add_u32 s100, s100, s72\ns_add_u32 s100, s100, 65011712\n"
"v_cvt_pk_bf16_f32 v115, v118, v119\nv_mfma_f32_16x16x32_bf16 a[252:255], v[172:175], a[220:223], a[252:255]\nbuffer_store_dwordx4 v[96:99], v252, s[20:23], s100 offen offset:0 sc1\nv_mfma_f32_16x16x32_bf16 a[236:239], v[168:171], a[200:203], a[236:239]\nR 162,185,36864\nv_cvt_pk_bf16_f32 v104, v104, v105\n"
"v_mfma_f32_16x16x32_bf16 a[236:239], v[172:175], a[216:219], a[236:239]\nv_cvt_pk_bf16_f32 v105, v106, v107\nv_cvt_pk_bf16_f32 v106, v108, v109\nM 28,168,196\nR 164,185,40960\nv_cvt_pk_bf16_f32 v107, v110, v111\n"
"M 28,172,212\nbuffer_load_dwordx4 v221, s[16:19], s63 offen offset:1024 lds\nM 12,168,192\nR 166,185,45056\nM 12,172,208\nbuffer_store_dwordx4 v[104:107], v252, s[20:23], s100 offen offset:1024 sc1\n"
"s_waitcnt vmcnt(12)\ns_barrier\nG 4\nM 32,176,160\nR 168,186,32768\nM 32,180,176\n"
"v_xor_b32 v240, 0x2000, v225\nM 48,176,164\nR 170,186,36864\nds_write2st64_b32 v240, a224, a225 offset0:0 offset1:1\nM 48,180,180\nbuffer_store_dwordx4 v[112:115], v252, s[20:23], s100 offen offset:2048 sc1\n"
"ds_write2st64_b32 v240, a226, a227 offset0:2 offset1:3\nM 64,176,168\nR 172,186,40960\nds_write2st64_b32 v240, a228, a229 offset0:4 offset1:5\nM 64,180,184\nds_write2st64_b32 v240, a230, a231 offset0:6 offset1:7\n"
"M 80,176,172\nR 174,186,45056\nds_write2st64_b32 v240, a232, a233 offset0:8 offset1:9\nM 80,180,188\nds_write2st64_b32 v240, a234, a235 offset0:10 offset1:11\nG 10\n"
"M 84,160,172\nR 176,187,32768\ns_add_u32 s63, s59, 655360\nds_write2st64_b32 v240, a236, a237 offset0:12 offset1:13\nM 84,164,188\nbuffer_store_dwordx4 v[120:123], v252, s[20:23], s100 offen offset:3072 sc1\n"
"ds_write2st64_b32 v240, a238, a239 offset0:14 offset1:15\nM 68,160,168\nR 178,187,36864\nds_write2st64_b32 v240, a240, a241 offset0:16 offset1:17\nM 68,164,184\ns_add_u32 m0, s50, 106496\n"
"ds_write2st64_b32 v240, a242, a243 offset0:18 offset1:19\nM 52,160,164\nR 180,187,40960\nds_write2st64_b32 v240, a244, a245 offset0:20 offset1:21\nM 52,164,180\nbuffer_load_dwordx4 v220, s[16:19], s63 offen lds\n"
"ds_write2st64_b32 v240, a246, a247 offset0:22 offset1:23\nM 36,160,160\nR 182,187,45056\nds_write2st64_b32 v240, a248, a249 offset0:24 offset1:25\nM 36,164,176\nds_write2st64_b32 v240, a250, a251 offset0:26 offset1:27\n"
"G 14\nM 40,168,160\nR 160,184,49152\nds_write2st64_b32 v240, a252, a253 offset0:28 offset1:29\nM 40,172,176\nM 56,168,164\n"
"R 162,184,53248\nds_write2st64_b32 v240, a254, a255 offset0:30 offset1:31\nM 56,172,180\nbuffer_load_dwordx4 v221, s[16:19], s63 offen offset:1024 lds\nM 72,168,168\nR 164,184,57344\n"
"s_add_u32 s63, s59, 196608\nM 72,172,184\ns_add_u32 m0, s50, 114688\nM 88,168,172\nR 166,184,61440\nM 88,172,188\n"
"G 8\nM 92,176,172\nR 168,185,49152\ns_waitcnt vmcnt(12) lgkmcnt(3)\ns_barrier\nM 92,180,188\n"
"buffer_load_dwordx4 v220, s[16:19], s63 offen lds\nds_read2st64_b32 v[128:129], v225 offset0:0 offset1:1\nM 76,176,168\nR 170,185,53248\nds_read2st64_b32 v[130:131], v225 offset0:2 offset1:3\nM 76,180,184\n"
"ds_read2st64_b32 v[132:133], v225 offset0:4 offset1:5\nds_read2st64_b32 v[134:135], v225 offset0:6 offset1:7\nM 60,176,164\nR 172,185,57344\nds_read2st64_b32 v[136:137], v225 offset0:8 offset1:9\nM 60,180,180\n"
"buffer_load_dwordx4 v221, s[16:19], s63 offen offset:1024 lds\nds_read2st64_b32 v[138:139], v225 offset0:10 offset1:11\nM 44,176,160\nR 174,185,61440\ns_add_u32 m0, s50, 0\nds_read2st64_b32 v[140:141], v225 offset0:12 offset1:13\n"
"M 44,180,176\nds_read2st64_b32 v[142:143], v225 offset0:14 offset1:15\nds_read2st64_b32 v[144:145], v225 offset0:16 offset1:17\nG 13\nM 32,160,192\nR 176,186,49152\n"
"Lpg32:\ns_cbranch_execz LretW1z\ns_add_u32 s63, s59, 720896\nds_read2st64_b32 v[146:147], v225 offset0:18 offset1:19\nM 32,164,208\nbuffer_load_dwordx4 v220, s[16:19], s63 offen lds\n"
"ds_read2st64_b32 v[148:149], v225 offset0:20 offset1:21\nM 48,160,196\nR 178,186,53248\nds_read2st64_b32 v[150:151], v225 offset0:22 offset1:23\nG 15\nv_add_f32 v0, v0, v128\n"
"M 48,164,212\nds_read2st64_b32 v[152:153], v225 offset0:24 offset1:25\nds_read2st64_b32 v[154:155], v225 offset0:26 offset1:27\nv_add_f32 v1, v1, v129\nM 64,160,200\nR 180,186,57344\n"
"ds_read2st64_b32 v[156:157], v225 offset0:28 offset1:29\nG 15\nv_add_f32 v2, v2, v130\nM 64,164,216\nbuffer_load_dwordx4 v221, s[16:19], s63 offen offset:1024 lds\nds_read2st64_b32 v[158:159], v225 offset0:30 offset1:31\n"
"M 80,160,204\nR 182,186,61440\ns_add_u32 m0, s50, 8192\nv_add_f32 v3, v3, v131\nM 80,164,220\nG 14\n"
"v_add_f32 v4, v4, v132\nv_add_f32 v5, v5, v133\nv_add_f32 v6, v6, v134\nM 84,168,204\nR 160,187,49152\ns_add_u32 s63, s59, 393216\n"
"v_add_f32 v7, v7, v135\nM 84,172,220\nbuffer_load_dwordx4 v220, s[16:19], s63 offen lds\nv_add_f32 v8, v8, v136\nM 68,168,200\nR 162,187,53248\n"
"v_add_f32 v9, v9, v137\nv_add_f32 v10, v10, v138\nM 68,172,216\nv_add_f32 v11, v11, v139\nG 15\nv_add_f32 v12, v12, v140\n"
"v_add_f32 v13, v13, v141\nM 52,168,196\nR 164,187,57344\nG 15\nv_add_f32 v14, v14, v142\nv_add_f32 v15, v15, v143\n"
"M 52,172,212\nbuffer_load_dwordx4 v221, s[16:19], s63 offen offset:1024 lds\nG 14\nv_add_f32 v16, v16, v144\nM 36,168,192\nR 166,187,61440\n"
"s_add_u32 s63, s59, 458752\nv_add_f32 v17, v17, v145\nM 36,172,208\nG 4\nv_add_f32 v18, v18, v146\nv_add_f32 v19, v19, v147\n"
"v_add_f32 v20, v20, v148\nM 40,176,192\nR 168,188,0\nv_add_f32 v21, v21, v149\nv_add_f32 v22, v22, v150\nM 40,180,208\n"
"s_add_u32 m0, s50, 16384\nv_add_f32 v23, v23, v151\nv_add_f32 v24, v24, v152\nv_add_f32 v25, v25, v153\nM 56,176,196\nR 170,188,4096\n"
"v_add_f32 v26, v26, v154\nv_add_f32 v27, v27, v155\nM 56,180,212\nbuffer_load_dwordx4 v220, s[16:19], s63 offen lds\nv_add_f32 v28, v28, v156\nv_add_f32 v29, v29, v157\n"
"v_add_f32 v30, v30, v158\nv_add_f32 v31, v31, v159\nM 72,176,200\nR 172,188,8192\nv_cvt_pk_bf16_f32 v24, v24, v25\nv_cvt_pk_bf16_f32 v25, v26, v27\n"
"M 72,180,216\nv_cvt_pk_bf16_f32 v26, v28, v29\nv_cvt_pk_bf16_f32 v27, v30, v31\nv_cvt_pk_bf16_f32 v0, v0, v1\nv_cvt_pk_bf16_f32 v16, v16, v17\nM 88,176,204\n"
"R 174,188,12288\nv_cvt_pk_bf16_f32 v1, v2, v3\nM 88,180,220\nG 4\nv_cvt_pk_bf16_f32 v2, v4, v5\nv_cvt_pk_bf16_f32 v3, v6, v7\n"
"M 92,160,204\nR 176,189,0\ns_bfe_u32 s100, s57, 0x20007\ns_mul_i32 s100, s100, 49152\ns_add_u32 s100, s100, s72\ns_add_u32 s100, s100, 65028096\n"
"v_cvt_pk_bf16_f32 v17, v18, v19\nM 92,164,220\nbuffer_store_dwordx4 v[0:3], v252, s[20:23], s100 offen offset:0 sc1\nM 76,160,200\nR 178,189,4096\nv_cvt_pk_bf16_f32 v8, v8, v9\n"
"M 76,164,216\nv_cvt_pk_bf16_f32 v9, v10, v11\nv_cvt_pk_bf16_f32 v10, v12, v13\nM 60,160,196\nR 180,189,8192\nv_cvt_pk_bf16_f32 v11, v14, v15\n"
"M 60,164,212\nbuffer_load_dwordx4 v221, s[16:19], s63 offen offset:1024 lds\nM 44,160,192\nR 182,189,12288\nv_cvt_pk_bf16_f32 v18, v20, v21\nM 44,164,208\n"
"buffer_store_dwordx4 v[8:11], v252, s[20:23], s100 offen offset:1024 sc1\ns_waitcnt vmcnt(18)\ns_barrier\nG 4\nv_mfma_f32_16x16x32_bf16 v[96:99], v[168:171], a[0:3], 0\nR 160,190,0\n"
"v_cvt_pk_bf16_f32 v19, v22, v23\nM 96,172,16\ns_mov_b32 m0, s51\nv_mfma_f32_16x16x32_bf16 v[112:115], v[168:171], a[4:7], 0\nR 162,190,4096\nv_cvt_pk_bf16_f32 v64, v64, v65\n"
"v_cvt_pk_bf16_f32 v65, v66, v67\nM 112,172,20\nbuffer_store_dwordx4 v[16:19], v252, s[20:23], s100 offen offset:2048 sc1\nv_cvt_pk_bf16_f32 v66, v68, v69\nv_mfma_f32_16x16x32_bf16 v[128:131], v[168:171], a[8:11], 0\nR 164,190,8192\n"
"v_cvt_pk_bf16_f32 v67, v70, v71\nv_cvt_pk_bf16_f32 v68, v72, v73\nM 128,172,24\nds_write_addtid_b32 v64 offset:0\nv_cvt_pk_bf16_f32 v69, v74, v75\nv_cvt_pk_bf16_f32 v70, v76, v77\n"
"v_mfma_f32_16x16x32_bf16 v[144:147], v[168:171], a[12:15], 0\nR 166,190,12288\nv_cvt_pk_bf16_f32 v71, v78, v79\nv_cvt_pk_bf16_f32 v72, v80, v81\nM 144,172,28\nds_write_addtid_b32 v65 offset:256\n"
"G 6\nv_cvt_pk_bf16_f32 v73, v82, v83\nv_mfma_f32_16x16x32_bf16 v[148:151], v[176:179], a[12:15], 0\nR 168,191,0\nv_cvt_pk_bf16_f32 v74, v84, v85\nv_cvt_pk_bf16_f32 v75, v86, v87\n"
"M 148,180,28\nbuffer_store_dwordx4 v[24:27], v252, s[20:23], s100 offen offset:3072 sc1\nv_mfma_f32_16x16x32_bf16 v[132:135], v[176:179], a[8:11], 0\nR 170,191,4096\nv_cvt_pk_bf16_f32 v76, v88, v89\nM 132,180,24\n"
"ds_write_addtid_b32 v66 offset:512\nv_cvt_pk_bf16_f32 v77, v90, v91\nv_mfma_f32_16x16x32_bf16 v[116:119], v[176:179], a[4:7], 0\nR 172,191,8192\nv_cvt_pk_bf16_f32 v78, v92, v93\nM 116,180,20\n"
"ds_write_addtid_b32 v67 offset:768\nv_cvt_pk_bf16_f32 v79, v94, v95\nv_mfma_f32_16x16x32_bf16 v[100:103], v[176:179], a[0:3], 0\nR 174,191,12288\nds_write_addtid_b32 v68 offset:1024\nM 100,180,16\n"
"ds_write_addtid_b32 v69 offset:1280\nds_write_addtid_b32 v70 offset:1536\nG 10\nv_mfma_f32_16x16x32_bf16 v[104:107], v[160:163], a[0:3], 0\nR 176,188,16384\nds_write_addtid_b32 v71 offset:1792\n"
"M 104,164,16\nds_write_addtid_b32 v72 offset:2048\nds_write_addtid_b32 v73 offset:2304\nv_mfma_f32_16x16x32_bf16 v[120:123], v[160:163], a[4:7], 0\nR 178,188,20480\nds_write_addtid_b32 v74 offset:2560\n"
"M 120,164,20\nds_write_addtid_b32 v75 offset:2816\nds_write_addtid_b32 v76 offset:3072\nv_mfma_f32_16x16x32_bf16 v[136:139], v[160:163], a[8:11], 0\nR 180,188,24576\nds_write_addtid_b32 v77 offset:3328\n"
"M 136,164,24\nds_write_addtid_b32 v78 offset:3584\nds_write_addtid_b32 v79 offset:3840\nv_mfma_f32_16x16x32_bf16 v[152:155], v[160:163], a[12:15], 0\nR 182,188,28672\ns_add_u32 m0, s50, 24576\n"
"M 152,164,28\ns_add_u32 s63, s59, 262144\nG 15\nv_mfma_f32_16x16x32_bf16 v[156:159], v[168:171], a[12:15], 0\nR 160,189,16384\ns_waitcnt vmcnt(12) lgkmcnt(2)\n"
"s_barrier\nM 156,172,28\nbuffer_load_dwordx4 v220, s[16:19], s63 offen lds\nv_mfma_f32_16x16x32_bf16 v[140:143], v[168:171], a[8:11], 0\nR 162,189,20480\nM 140,172,24\n"
"ds_read2st64_b32 v[64:65], v225 offset0:0 offset1:1\nv_mfma_f32_16x16x32_bf16 v[124:127], v[168:171], a[4:7], 0\nR 164,189,24576\nM 124,172,20\nds_read2st64_b32 v[66:67], v225 offset0:2 offset1:3\nv_mfma_f32_16x16x32_bf16 v[108:111], v[168:171], a[0:3], 0\n"
"R 166,189,28672\nM 108,172,16\nds_read2st64_b32 v[68:69], v225 offset0:4 offset1:5\nG 7\nM 96,176,32\nR 168,190,16384\n"
"v_bfe_u32 v250, v252, 4, 2\nv_lshlrev_b32 v250, 3, v250\nM 96,180,48\nbuffer_load_dwordx4 v221, s[16:19], s63 offen offset:1024 lds\ns_lshl_b32 s60, s43, 5\nv_add_u32 v250, s60, v250\n"
"M 112,176,36\nR 170,190,20480\nds_read2st64_b32 v[70:71], v225 offset0:6 offset1:7\nM 112,180,52\ns_add_u32 m0, s50, 32768\nds_read2st64_b32 v[72:73], v225 offset0:8 offset1:9\n"
"G 6\nv_mfma_f32_16x16x32_bf16 v[32:35], v[208:211], v[64:67], v[32:35]\nv_mfma_f32_16x16x32_bf16 v[36:39], v[210:213], v[64:67], v[36:39]\nM 128,176,40\nR 172,190,24576\nM 128,180,56\n"
"s_add_u32 s63, s59, 786432\nds_read2st64_b32 v[74:75], v225 offset0:10 offset1:11\nM 144,176,44\nR 174,190,28672\nM 144,180,60\nds_read2st64_b32 v[76:77], v225 offset0:12 offset1:13\n"
"G 9\nM 148,160,44\nR 176,191,16384\nG 6\nv_mfma_f32_16x16x32_bf16 v[40:43], v[208:211], v[68:71], v[40:43]\nv_mfma_f32_16x16x32_bf16 v[44:47], v[210:213], v[68:71], v[44:47]\n"
"ds_read2st64_b32 v[78:79], v225 offset0:14 offset1:15\nM 148,164,60\nbuffer_load_dwordx4 v220, s[16:19], s63 offen lds\nM 132,160,40\nR 178,191,20480\nM 132,164,56\n"
"G 5\nv_mfma_f32_16x16x32_bf16 v[48:51], v[208:211], v[72:75], v[48:51]\nv_mfma_f32_16x16x32_bf16 v[52:55], v[210:213], v[72:75], v[52:55]\nM 116,160,36\nR 180,191,24576\nM 116,164,52\n"
"buffer_load_dwordx4 v221, s[16:19], s63 offen offset:1024 lds\nM 100,160,32\nR 182,191,28672\ns_add_u32 m0, s50, 40960\nG 3\nv_mfma_f32_16x16x32_bf16 v[56:59], v[208:211], v[76:79], v[56:59]\n"
"v_mfma_f32_16x16x32_bf16 v[60:63], v[210:213], v[76:79], v[60:63]\nM 100,164,48\nM 104,168,32\nR 160,188,32768\ns_add_u32 s63, s59, 327680\nM 104,172,48\n"
"buffer_load_dwordx4 v220, s[16:19], s63 offen lds\nM 120,168,36\nR 162,188,36864\nv_mov_b32 v226, s46\nM 120,172,52\nv_mov_b32 v227, s47\n"
"M 136,168,40\nR 164,188,40960\ns_bfe_u32 s62, s56, 0x20007\ns_lshl_b32 s62, s62, 5\ns_add_u32 s61, s74, 1009664\ns_add_u32 s61, s61, s62\n"
"s_waitcnt vmcnt(5)\ns_mov_b64 exec, 1\nbuffer_atomic_swap_x2 v[226:227], off, s[20:23], s61 sc1\ns_mov_b64 exec, -1\nv_cvt_pk_bf16_f32 v32, v32, v33\nM 136,172,56\n"
"s_bfe_u32 s62, s57, 0x20007\ns_lshl_b32 s62, s62, 5\ns_add_u32 s61, s74, 1009672\ns_add_u32 s61, s61, s62\ns_mov_b64 exec, 1\nbuffer_atomic_swap_x2 v[226:227], off, s[20:23], s61 sc1\n"
"s_mov_b64 exec, -1\nM 152,168,44\nR 166,188,45056\nv_cvt_pk_bf16_f32 v33, v34, v35\nM 152,172,60\nG 4\n"
"v_cvt_pk_bf16_f32 v34, v36, v37\nv_cvt_pk_bf16_f32 v35, v38, v39\nM 156,176,44\nR 168,189,32768\ns_bfe_u32 s100, s58, 0x20007\ns_mul_i32 s100, s100, 49152\n"
"s_add_u32 s100, s100, s72\ns_add_u32 s100, s100, 65044480\nv_cvt_pk_bf16_f32 v48, v48, v49\nM 156,180,60\nbuffer_store_dwordx4 v[32:35], v252, s[20:23], s100 offen offset:0 sc1\nM 140,176,40\n"
"R 170,189,36864\nv_cvt_pk_bf16_f32 v40, v40, v41\nM 140,180,56\nv_cvt_pk_bf16_f32 v41, v42, v43\nv_cvt_pk_bf16_f32 v42, v44, v45\nM 124,176,36\n"
"R 172,189,40960\nv_cvt_pk_bf16_f32 v43, v46, v47\nM 124,180,52\nv_cvt_pk_bf16_f32 v49, v50, v51\nv_cvt_pk_bf16_f32 v50, v52, v53\nM 108,176,32\n"
"R 174,189,45056\nv_cvt_pk_bf16_f32 v51, v54, v55\nM 108,180,48\nbuffer_store_dwordx4 v[40:43], v252, s[20:23], s100 offen offset:1024 sc1\ns_barrier\nG 4\n"
"M 96,160,64\nR 176,190,32768\nv_cvt_pk_bf16_f32 v56, v56, v57\nM 96,164,80\nv_cvt_pk_bf16_f32 v57, v58, v59\nv_cvt_pk_bf16_f32 v58, v60, v61\n"
"M 112,160,68\nR 178,190,36864\nv_cvt_pk_bf16_f32 v59, v62, v63\nM 112,164,84\nbuffer_store_dwordx4 v[48:51], v252, s[20:23], s100 offen offset:2048 sc1\nM 128,160,72\n"
"R 180,190,40960\nM 128,164,88\nM 144,160,76\nR 182,190,45056\nM 144,164,92\nG 4\n"
"M 148,168,76\nR 160,191,32768\nM 148,172,92\nbuffer_store_dwordx4 v[56:59], v252, s[20:23], s100 offen offset:3072 sc1\nM 132,168,72\nR 162,191,36864\n"
"M 132,172,88\nM 116,168,68\nR 164,191,40960\nM 116,172,84\nM 100,168,64\nR 166,191,45056\n"
"v_mov_b32 v248, 0\nM 100,172,80\nG 4\nv_mov_b32 v249, 0\nM 104,176,64\nR 168,188,49152\n"
"s_add_u32 s61, s74, 1009664\ns_mov_b64 exec, 3\nbuffer_atomic_or_x2 v[248:249], v250, s[20:23], s61 offen sc0 sc1\ns_mov_b64 exec, -1\nM 104,180,80\nM 120,176,68\n"
"R 170,188,53248\nv_mov_b32 v242, v208\nM 120,180,84\nbuffer_load_dwordx4 v221, s[16:19], s63 offen offset:1024 lds\nM 136,176,72\nR 172,184,0\n"
"v_mov_b32 v243, v209\nM 136,180,88\ns_add_u32 m0, s50, 49152\nv_mov_b32 v244, v208\nv_mov_b32 v245, v209\nM 152,176,76\n"
"R 174,184,4096\nv_mov_b32 v64, v192\nM 152,180,92\nG 4\nv_mov_b32 v65, v193\nv_mov_b32 v70, v194\n"
"M 156,160,76\nR 176,189,49152\nv_mov_b32 v71, v195\nM 156,164,92\ns_add_u32 s63, s59, 851968\nv_mov_b32 v72, v196\n"
"v_mov_b32 v73, v197\nM 140,160,72\nR 178,189,53248\nv_mov_b32 v78, v198\nM 140,164,88\nbuffer_load_dwordx4 v220, s[16:19], s63 offen lds\n"
"M 124,160,68\nR 180,185,0\nv_mov_b32 v79, v199\nM 124,164,84\nv_mov_b32 v80, v200\nv_mov_b32 v81, v201\n"
"M 108,160,64\nR 182,185,4096\nv_mov_b32 v86, v202\nM 108,164,80\nG 4\nv_mov_b32 v87, v203\n"
"v_mov_b32 v88, v204\nM 96,168,96\nR 160,190,49152\nv_mov_b32 v89, v205\nM 96,172,112\nv_mov_b32 v94, v206\n"
"v_mov_b32 v95, v207\nM 112,168,100\nR 162,190,53248\nv_mov_b32 v32, v232\nM 112,172,116\nbuffer_load_dwordx4 v221, s[16:19], s63 offen offset:1024 lds\n"
"M 128,168,104\nR 164,186,0\nv_mov_b32 v33, v233\nM 128,172,120\nv_mov_b32 v38, v234\nv_mov_b32 v39, v235\n"
"M 144,168,108\nR 166,186,4096\nv_mov_b32 v40, v236\nM 144,172,124\nG 4\nv_mov_b32 v41, v237\n"
"v_mov_b32 v46, v238\nM 148,176,108\nR 168,191,49152\nv_mov_b32 v47, v239\nM 148,180,124\nv_mov_b32 v48, v232\n"
"v_mov_b32 v49, v233\nM 132,176,104\nR 170,191,53248\nv_mov_b32 v54, v234\nM 132,180,120\nv_mov_b32 v55, v235\n"
"v_mov_b32 v56, v236\nM 116,176,100\nR 172,187,0\nv_mov_b32 v57, v237\nM 116,180,116\nv_mov_b32 v62, v238\n"
"v_mov_b32 v63, v239\nM 100,176,96\nR 174,187,4096\nM 100,180,112\nG 4\nM 104,160,96\n"
"R 176,184,8192\ns_waitcnt vmcnt(11)\ns_barrier\nM 104,164,112\nM 120,160,100\nR 178,184,12288\n"
"M 120,164,116\nM 136,160,104\nR 180,184,16384\nM 136,164,120\nM 152,160,108\nR 182,184,20480\n"
"M 152,164,124\nG 4\nM 156,168,108\nR 160,185,8192\nM 156,172,124\nM 140,168,104\n"
"R 162,185,12288\nM 140,172,120\nM 124,168,100\nR 164,185,16384\nM 124,172,116\nM 108,168,96\n"
"R 166,185,20480\nv_mov_b32 v226, s46\nM 108,172,112\nG 4\nv_mov_b32 v227, s47\nM 96,176,128\n"
"R 168,186,8192\ns_bfe_u32 s62, s58, 0x20007\ns_lshl_b32 s62, s62, 5\ns_add_u32 s61, s74, 1009680\ns_add_u32 s61, s61, s62\ns_waitcnt vmcnt(4)\n"
"s_mov_b64 exec, 1\nbuffer_atomic_swap_x2 v[226:227], off, s[20:23], s61 sc1\ns_mov_b64 exec, -1\nM 96,180,144\ns_mul_i32 s64, s43, 49152\nM 112,176,132\n"
"R 170,186,12288\nM 112,180,148\ns_add_u32 s64, s64, s72\nM 128,176,136\nR 172,186,16384\ns_waitcnt vmcnt(4)\n"
"v_cmp_eq_u64 s[86:87], s[46:47], v[248:249]\nM 128,180,152\ns_add_u32 s64, s64, 65011712\nM 144,176,140\nR 174,186,20480\ns_and_b32 s62, s86, 3\n"
"s_cmp_eq_u32 s62, 3\ns_cbranch_scc0 Lq967\nLq968:\nM 144,180,156\nG 4\nM 148,160,140\n"
"R 176,187,8192\nM 148,164,156\nbuffer_load_dwordx4 v[2:5], v252, s[20:23], s64 offen offset:0 sc1\nM 132,160,136\nR 178,187,12288\nM 132,164,152\n"
"buffer_load_dwordx4 v[10:13], v252, s[20:23], s64 offen offset:1024 sc1\nM 116,160,132\nR 180,187,16384\nM 116,164,148\ns_mul_i32 s65, s43, 49152\nM 100,160,128\n"
"R 182,187,20480\nM 100,164,144\nbuffer_load_dwordx4 v[18:21], v252, s[20:23], s64 offen offset:2048 sc1\nG 4\nM 104,168,128\nR 160,184,24576\n"
"M 104,172,144\nbuffer_load_dwordx4 v[26:29], v252, s[20:23], s64 offen offset:3072 sc1\nM 120,168,132\nR 162,184,28672\nM 120,172,148\ns_add_u32 s65, s65, s72\n"
"M 136,168,136\nR 164,184,32768\ns_add_u32 s65, s65, 65028096\nM 136,172,152\nbuffer_load_dwordx4 v[66:69], v252, s[20:23], s65 offen offset:0 sc1\nM 152,168,140\n"
"R 166,184,36864\nM 152,172,156\nG 4\nM 156,176,140\nR 168,185,24576\ns_waitcnt vmcnt(6)\n"
"s_barrier\nM 156,180,156\nbuffer_load_dwordx4 v[74:77], v252, s[20:23], s65 offen offset:1024 sc1\nM 140,176,136\nR 170,185,28672\nv_mov_b32 v246, 0\n"
"M 140,180,152\nbuffer_load_dwordx4 v[82:85], v252, s[20:23], s65 offen offset:2048 sc1\nv_mov_b32 v247, 0\nM 124,176,132\nR 172,185,32768\ns_add_u32 s61, s74, 1009664\n"
"s_mov_b64 exec, 4\nbuffer_atomic_or_x2 v[246:247], v250, s[20:23], s61 offen sc0 sc1\ns_mov_b64 exec, -1\nM 124,180,148\nbuffer_load_dwordx4 v[90:93], v252, s[20:23], s65 offen offset:3072 sc1\nM 108,176,128\n"
"Lpg33:\ns_cbranch_execz LretW1z\nR 174,185,36864\nM 108,180,144\nG 4\nM 128,160,168\n"
"R 176,186,24576\nM 128,164,184\nR 178,186,28672\nM 144,160,172\nR 180,186,32768\nM 144,164,188\n"
"R 182,186,36864\nG 4\nM 148,168,172\nR 160,187,24576\nM 148,172,188\nR 162,187,28672\n"
"M 132,168,168\nR 164,187,32768\nM 132,172,184\nR 166,187,36864\nG 4\nM 136,176,168\n"
"R 168,184,40960\nM 136,180,184\nR 170,184,45056\nM 152,176,172\nR 172,184,49152\nM 152,180,188\n"
"R 174,184,53248\nG 4\nM 156,160,172\nR 176,185,40960\nM 156,164,188\nR 178,185,45056\n"
"M 140,160,168\nR 180,185,49152\nM 140,164,184\nR 182,185,53248\nG 4\nM 128,168,200\n"
"R 160,186,40960\nM 128,172,216\nR 162,186,45056\nM 144,168,204\nR 164,186,49152\nM 144,172,220\n"
"R 166,186,53248\nG 4\nM 148,176,204\nR 168,187,40960\nM 148,180,220\nR 170,187,45056\n"
"M 132,176,200\nR 172,187,49152\nM 132,180,216\nR 174,187,53248\nG 4\nM 136,160,200\n"
"R 176,184,24576\nM 136,164,216\nR 178,184,28672\nM 152,160,204\nR 180,184,32768\nM 152,164,220\n"
"R 182,184,36864\nG 4\nM 156,168,204\nR 160,185,24576\nM 156,172,220\nR 162,185,28672\n"
"M 140,168,200\nR 164,185,32768\nM 140,172,216\nR 166,185,36864\nG 4\nM 96,176,160\n"
"R 168,186,24576\nM 96,180,176\nR 170,186,28672\ns_mov_b32 m0, s51\nM 112,176,164\nR 172,186,32768\n"
"v_cvt_pk_bf16_f32 v128, v128, v129\nv_cvt_pk_bf16_f32 v129, v130, v131\nM 112,180,180\nR 174,186,36864\nG 4\nv_cvt_pk_bf16_f32 v130, v132, v133\n"
"M 116,160,164\nR 176,187,24576\ns_mul_i32 s66, s43, 49152\nv_cvt_pk_bf16_f32 v131, v134, v135\nM 116,164,180\nR 178,187,28672\n"
"s_add_u32 s66, s66, s72\nv_cvt_pk_bf16_f32 v132, v136, v137\nM 100,160,160\nR 180,187,32768\ns_add_u32 s66, s66, 65044480\nv_cvt_pk_bf16_f32 v133, v138, v139\n"
"M 100,164,176\nR 182,187,36864\nG 4\nv_cvt_pk_bf16_f32 v134, v140, v141\nM 104,168,160\nR 160,184,40960\n"
"s_waitcnt vmcnt(1)\nv_cmp_eq_u64 s[86:87], s[46:47], v[246:247]\nv_cvt_pk_bf16_f32 v135, v142, v143\nM 104,172,176\nR 162,184,45056\nv_cvt_pk_bf16_f32 v136, v144, v145\n"
"v_cvt_pk_bf16_f32 v137, v146, v147\nM 120,168,164\nR 164,184,49152\ns_and_b32 s62, s86, 4\ns_cmp_eq_u32 s62, 4\ns_cbranch_scc0 Lq983\n"
"Lq984:\nv_cvt_pk_bf16_f32 v138, v148, v149\nM 120,172,180\nR 166,184,53248\nG 4\nv_cvt_pk_bf16_f32 v139, v150, v151\n"
"M 124,176,164\nR 168,185,40960\nbuffer_load_dwordx4 v[34:37], v252, s[20:23], s66 offen offset:0 sc1\nv_cvt_pk_bf16_f32 v140, v152, v153\nM 124,180,180\nR 170,185,45056\n"
"v_cvt_pk_bf16_f32 v141, v154, v155\nv_cvt_pk_bf16_f32 v142, v156, v157\nM 108,176,160\nR 172,185,49152\nbuffer_load_dwordx4 v[42:45], v252, s[20:23], s66 offen offset:1024 sc1\nds_write_addtid_b32 v128 offset:0\n"
"ds_write_addtid_b32 v129 offset:256\nds_write_addtid_b32 v130 offset:512\nds_write_addtid_b32 v131 offset:768\nds_write_addtid_b32 v132 offset:1024\nds_write_addtid_b32 v133 offset:1280\nds_write_addtid_b32 v134 offset:1536\n"
"ds_write_addtid_b32 v135 offset:1792\nds_write_addtid_b32 v136 offset:2048\nds_write_addtid_b32 v137 offset:2304\nds_write_addtid_b32 v138 offset:2560\nds_write_addtid_b32 v139 offset:2816\nds_write_addtid_b32 v140 offset:3072\n"
"ds_write_addtid_b32 v141 offset:3328\nds_write_addtid_b32 v142 offset:3584\nv_cvt_pk_bf16_f32 v143, v158, v159\nds_write_addtid_b32 v143 offset:3840\nM 108,180,176\nR 174,185,53248\n"
"G 15\nM 96,160,192\nR 176,186,40960\nM 96,164,208\nR 178,186,45056\nbuffer_load_dwordx4 v[50:53], v252, s[20:23], s66 offen offset:2048 sc1\n"
"G 3\ns_barrier\nM 112,160,196\nR 180,186,49152\nds_read2st64_b32 v[0:1], v225 offset0:0 offset1:1\nM 112,164,212\n"
"R 182,186,53248\nds_read2st64_b32 v[6:7], v225 offset0:2 offset1:3\nG 6\nM 116,168,196\nR 160,187,40960\nbuffer_load_dwordx4 v[58:61], v252, s[20:23], s66 offen offset:3072 sc1\n"
"ds_read2st64_b32 v[8:9], v225 offset0:4 offset1:5\nM 116,172,212\nR 162,187,45056\nds_read2st64_b32 v[14:15], v225 offset0:6 offset1:7\nM 100,168,192\nR 164,187,49152\n"
"ds_read2st64_b32 v[16:17], v225 offset0:8 offset1:9\nM 100,172,208\nR 166,187,53248\nds_read2st64_b32 v[22:23], v225 offset0:10 offset1:11\nG 9\nM 104,176,192\n"
"ds_read2st64_b32 v[24:25], v225 offset0:12 offset1:13\nds_read2st64_b32 v[30:31], v225 offset0:14 offset1:15\nM 104,180,208\nM 120,176,196\nM 120,180,212\nG 3\n"
"M 124,160,196\nM 124,164,212\nM 108,160,192\nM 108,164,208\nv_mfma_f32_16x16x32_bf16 v[96:99], v[242:245], v[64:67], v[96:99]\nv_mfma_f32_16x16x32_bf16 v[100:103], v[242:245], v[68:71], v[100:103]\n"
"v_mfma_f32_16x16x32_bf16 v[104:107], v[242:245], v[72:75], v[104:107]\nv_mfma_f32_16x16x32_bf16 v[108:111], v[242:245], v[76:79], v[108:111]\nv_mfma_f32_16x16x32_bf16 v[112:115], v[242:245], v[80:83], v[112:115]\nv_mfma_f32_16x16x32_bf16 v[116:119], v[242:245], v[84:87], v[116:119]\ns_waitcnt vmcnt(4)\nv_mfma_f32_16x16x32_bf16 v[120:123], v[242:245], v[88:91], v[120:123]\n"
"v_mfma_f32_16x16x32_bf16 v[124:127], v[242:245], v[92:95], v[124:127]\nv_mfma_f32_16x16x32_bf16 v[96:99], v[242:245], v[0:3], v[96:99]\nv_mfma_f32_16x16x32_bf16 v[100:103], v[242:245], v[4:7], v[100:103]\nv_mfma_f32_16x16x32_bf16 v[104:107], v[242:245], v[8:11], v[104:107]\nv_mfma_f32_16x16x32_bf16 v[108:111], v[242:245], v[12:15], v[108:111]\nv_mfma_f32_16x16x32_bf16 v[112:115], v[242:245], v[16:19], v[112:115]\n"
"G 2\nv_mfma_f32_16x16x32_bf16 v[116:119], v[242:245], v[20:23], v[116:119]\nG 1\nv_mfma_f32_16x16x32_bf16 v[120:123], v[242:245], v[24:27], v[120:123]\nG 0\nv_mfma_f32_16x16x32_bf16 v[124:127], v[242:245], v[28:31], v[124:127]\n"
"s_waitcnt vmcnt(3)\nv_mfma_f32_16x16x32_bf16 v[96:99], v[242:245], v[32:35], v[96:99]\nv_mfma_f32_16x16x32_bf16 v[100:103], v[242:245], v[36:39], v[100:103]\ns_waitcnt vmcnt(2)\nv_mfma_f32_16x16x32_bf16 v[104:107], v[242:245], v[40:43], v[104:107]\nv_mfma_f32_16x16x32_bf16 v[108:111], v[242:245], v[44:47], v[108:111]\n"
"s_waitcnt vmcnt(1)\nv_mfma_f32_16x16x32_bf16 v[112:115], v[242:245], v[48:51], v[112:115]\nv_mfma_f32_16x16x32_bf16 v[116:119], v[242:245], v[52:55], v[116:119]\ns_waitcnt vmcnt(0)\nv_mfma_f32_16x16x32_bf16 v[120:123], v[242:245], v[56:59], v[120:123]\nv_mfma_f32_16x16x32_bf16 v[124:127], v[242:245], v[60:63], v[124:127]\n"
"v_cvt_pk_bf16_f32 v192, v96, v97\nv_cvt_pk_bf16_f32 v193, v98, v99\nv_cvt_pk_bf16_f32 v194, v100, v101\nv_cvt_pk_bf16_f32 v195, v102, v103\nv_cvt_pk_bf16_f32 v196, v104, v105\nv_cvt_pk_bf16_f32 v197, v106, v107\n"
"v_cvt_pk_bf16_f32 v198, v108, v109\nv_cvt_pk_bf16_f32 v199, v110, v111\nv_cvt_pk_bf16_f32 v200, v112, v113\nv_cvt_pk_bf16_f32 v201, v114, v115\nv_cvt_pk_bf16_f32 v202, v116, v117\nv_cvt_pk_bf16_f32 v203, v118, v119\n"
"v_cvt_pk_bf16_f32 v204, v120, v121\nv_cvt_pk_bf16_f32 v205, v122, v123\nv_cvt_pk_bf16_f32 v206, v124, v125\nv_cvt_pk_bf16_f32 v207, v126, v127\nv_and_b32 v230, 0xf0, v252\nv_lshlrev_b32 v230, 7, v230\n"
"v_bfe_u32 v231, v252, 8, 1\nv_lshl_add_u32 v230, v231, 5, v230\nv_lshrrev_b32 v231, 9, v252\nv_lshl_add_u32 v230, v231, 4, v230\ns_add_u32 s61, s49, s53\ns_add_u32 s62, s61, 32768\n"
"v_permlane16_swap_b32 v192, v194\nv_permlane16_swap_b32 v193, v195\nbuffer_store_dwordx4 v[192:195], v230, s[28:31], s61 offen offset:0\nv_permlane16_swap_b32 v196, v198\nv_permlane16_swap_b32 v197, v199\nbuffer_store_dwordx4 v[196:199], v230, s[28:31], s61 offen offset:64\n"
"v_permlane16_swap_b32 v200, v202\nv_permlane16_swap_b32 v201, v203\nbuffer_store_dwordx4 v[200:203], v230, s[28:31], s62 offen offset:0\nv_permlane16_swap_b32 v204, v206\nv_permlane16_swap_b32 v205, v207\nbuffer_store_dwordx4 v[204:207], v230, s[28:31], s62 offen offset:64\n"
"s_add_u32 s60, s83, 1\nv_mov_b32 v234, s60\ns_or_b32 s60, s45, s40\ns_cmp_eq_u32 s60, 0\ns_cselect_b64 exec, 1, 0\nbuffer_store_dword v234, off, s[20:23], 0 sc1\n"
"s_mov_b64 exec, -1\ns_endpgm\nLqF:\nLq645:\ns_lshr_b32 s84, s49, 4\ns_add_u32 s84, s84, 82837504\n"
"buffer_load_dwordx4 v[128:131], v252, s[20:23], s84 offen offset:0 sc1\nbuffer_load_dwordx4 v[132:135], v252, s[20:23], s84 offen offset:1024 sc1\nbuffer_load_dwordx4 v[136:139], v252, s[20:23], s84 offen offset:2048 sc1\nbuffer_load_dwordx4 v[140:143], v252, s[20:23], s84 offen offset:3072 sc1\ns_mov_b32 s85, 0x0c0c0400\ns_waitcnt vmcnt(3)\n"
"v_perm_b32 v144, v129, v128, s85\nv_perm_b32 v148, v131, v130, s85\nv_lshl_or_b32 v144, v148, 16, v144\ns_waitcnt vmcnt(2)\nv_perm_b32 v145, v133, v132, s85\nv_perm_b32 v149, v135, v134, s85\n"
"v_lshl_or_b32 v145, v149, 16, v145\ns_waitcnt vmcnt(1)\nv_perm_b32 v146, v137, v136, s85\nv_perm_b32 v150, v139, v138, s85\nv_lshl_or_b32 v146, v150, 16, v146\ns_waitcnt vmcnt(0)\n"
"v_perm_b32 v147, v141, v140, s85\nv_perm_b32 v151, v143, v142, s85\nv_lshl_or_b32 v147, v151, 16, v147\nv_min3_u32 v230, v144, v145, v146\nv_min3_u32 v230, v230, v147, v147\nv_max3_u32 v231, v144, v145, v146\n"
"v_max3_u32 v231, v231, v147, v147\nv_cmp_ne_u32 s[86:87], s46, v230\nv_cmp_ne_u32 vcc, s46, v231\ns_nop 0\ns_or_b64 s[86:87], s[86:87], vcc\ns_cbranch_scc0 Lq646\n"
"s_sub_u32 s48, s48, 1\ns_cmp_gt_i32 s48, 0\ns_cbranch_scc1 Lq645\ns_branch Lq646\nLq692:\ns_mov_b64 exec, s[66:67]\n"
"buffer_load_dwordx4 v[230:233], v252, s[20:23], s65 offen offset:0 sc1\nbuffer_load_dwordx2 v[64:65], v227, s[20:23], s65 offen offset:2048 sc1\nbuffer_load_dwordx2 v[70:71], v227, s[20:23], s65 offen offset:2560 sc1\ns_mov_b64 exec, -1\ns_waitcnt vmcnt(0)\nv_max3_u32 v248, v233, v65, v71\n"
"v_cmp_eq_u32 vcc, -1, v248\ns_nop 1\ns_cbranch_vccz Lq693\ns_sub_u32 s48, s48, 1\ns_cmp_gt_i32 s48, 0\ns_cbranch_scc1 Lq692\n"
"s_branch Lq693\nLq694:\ns_mov_b64 exec, s[66:67]\nbuffer_load_dwordx4 v[234:237], v252, s[20:23], s65 offen offset:1024 sc1\nbuffer_load_dwordx2 v[76:77], v227, s[20:23], s65 offen offset:3072 sc1\nbuffer_load_dwordx2 v[82:83], v227, s[20:23], s65 offen offset:3584 sc1\n"
"s_mov_b64 exec, -1\ns_waitcnt vmcnt(0)\nv_max3_u32 v248, v237, v77, v83\nv_cmp_eq_u32 vcc, -1, v248\ns_nop 1\ns_cbranch_vccz Lq695\n"
"s_sub_u32 s48, s48, 1\ns_cmp_gt_i32 s48, 0\ns_cbranch_scc1 Lq694\ns_branch Lq695\nLq818:\ns_lshr_b32 s84, s49, 4\n"
"s_add_u32 s84, s84, 83361792\nbuffer_load_dwordx4 v[0:3], v252, s[20:23], s84 offen offset:0 sc1\nbuffer_load_dwordx4 v[4:7], v252, s[20:23], s84 offen offset:1024 sc1\nbuffer_load_dwordx4 v[8:11], v252, s[20:23], s84 offen offset:2048 sc1\nbuffer_load_dwordx4 v[12:15], v252, s[20:23], s84 offen offset:3072 sc1\ns_mov_b32 s85, 0x0c0c0400\n"
"s_waitcnt vmcnt(3)\nv_perm_b32 v16, v1, v0, s85\nv_perm_b32 v20, v3, v2, s85\nv_lshl_or_b32 v16, v20, 16, v16\ns_waitcnt vmcnt(2)\nv_perm_b32 v17, v5, v4, s85\n"
"v_perm_b32 v21, v7, v6, s85\nv_lshl_or_b32 v17, v21, 16, v17\ns_waitcnt vmcnt(1)\nv_perm_b32 v18, v9, v8, s85\nv_perm_b32 v22, v11, v10, s85\nv_lshl_or_b32 v18, v22, 16, v18\n"
"s_waitcnt vmcnt(0)\nv_perm_b32 v19, v13, v12, s85\nv_perm_b32 v23, v15, v14, s85\nv_lshl_or_b32 v19, v23, 16, v19\nv_min3_u32 v230, v16, v17, v18\nv_min3_u32 v230, v230, v19, v19\n"
"v_max3_u32 v231, v16, v17, v18\nv_max3_u32 v231, v231, v19, v19\nv_cmp_ne_u32 s[86:87], s46, v230\nv_cmp_ne_u32 vcc, s46, v231\ns_nop 0\ns_or_b64 s[86:87], s[86:87], vcc\n"
"s_cbranch_scc0 Lq819\ns_sub_u32 s48, s48, 1\ns_cmp_gt_i32 s48, 0\ns_cbranch_scc1 Lq818\ns_branch Lq819\nLq838:\n"
"v_mov_b32 v248, 0\nv_mov_b32 v249, 0\ns_add_u32 s61, s82, 845824\ns_mov_b64 exec, 0xf\nbuffer_atomic_or_x2 v[248:249], v250, s[20:23], s61 offen sc0 sc1\ns_mov_b64 exec, -1\n"
"s_waitcnt vmcnt(0)\nv_cmp_eq_u64 s[86:87], s[46:47], v[248:249]\ns_nop 1\ns_and_b32 s62, s86, 0xf\ns_cmp_eq_u32 s62, 0xf\ns_cbranch_scc1 Lq839\n"
"s_sleep 1\ns_sub_u32 s48, s48, 1\ns_cmp_gt_i32 s48, 0\ns_cbranch_scc1 Lq838\ns_branch Lq839\nLq873:\n"
"v_mov_b32 v248, 0\nv_mov_b32 v249, 0\ns_add_u32 s61, s82, 845832\ns_mov_b64 exec, 0xf\nbuffer_atomic_or_x2 v[248:249], v250, s[20:23], s61 offen sc0 sc1\ns_mov_b64 exec, -1\n"
"s_waitcnt vmcnt(0)\nv_cmp_eq_u64 s[86:87], s[46:47], v[248:249]\ns_nop 1\ns_and_b32 s62, s86, 0xf\ns_cmp_eq_u32 s62, 0xf\ns_cbranch_scc1 Lq874\n"
"s_sleep 1\ns_sub_u32 s48, s48, 1\ns_cmp_gt_i32 s48, 0\ns_cbranch_scc1 Lq873\ns_branch Lq874\nLq908:\n"
"v_mov_b32 v248, 0\nv_mov_b32 v249, 0\ns_add_u32 s61, s82, 845848\ns_mov_b64 exec, 0xf\nbuffer_atomic_or_x2 v[248:249], v250, s[20:23], s61 offen sc0 sc1\ns_mov_b64 exec, -1\n"
"s_waitcnt vmcnt(0)\nv_cmp_eq_u64 s[86:87], s[46:47], v[248:249]\ns_nop 1\ns_and_b32 s62, s86, 0xf\ns_cmp_eq_u32 s62, 0xf\ns_cbranch_scc1 Lq909\n"
"s_sleep 1\ns_sub_u32 s48, s48, 1\ns_cmp_gt_i32 s48, 0\ns_cbranch_scc1 Lq908\ns_branch Lq909\nLq927:\n"
"v_mov_b32 v248, 0\nv_mov_b32 v249, 0\ns_add_u32 s61, s82, 845840\ns_mov_b64 exec, 0xf\nbuffer_atomic_or_x2 v[248:249], v250, s[20:23], s61 offen sc0 sc1\ns_mov_b64 exec, -1\n"
"s_waitcnt vmcnt(0)\nv_cmp_eq_u64 s[86:87], s[46:47], v[248:249]\ns_nop 1\ns_and_b32 s62, s86, 0xf\ns_cmp_eq_u32 s62, 0xf\ns_cbranch_scc1 Lq928\n"
"s_sleep 1\ns_sub_u32 s48, s48, 1\ns_cmp_gt_i32 s48, 0\ns_cbranch_scc1 Lq927\ns_branch Lq928\nLq967:\n"
"v_mov_b32 v248, 0\nv_mov_b32 v249, 0\ns_add_u32 s61, s74, 1009664\ns_mov_b64 exec, 3\nbuffer_atomic_or_x2 v[248:249], v250, s[20:23], s61 offen sc0 sc1\ns_mov_b64 exec, -1\n"
"s_waitcnt vmcnt(0)\nv_cmp_eq_u64 s[86:87], s[46:47], v[248:249]\ns_nop 1\ns_and_b32 s62, s86, 3\ns_cmp_eq_u32 s62, 3\ns_cbranch_scc1 Lq968\n"
"s_sleep 1\ns_sub_u32 s48, s48, 1\ns_cmp_gt_i32 s48, 0\ns_cbranch_scc1 Lq967\ns_branch Lq968\nLq983:\n"
"v_mov_b32 v246, 0\nv_mov_b32 v247, 0\ns_add_u32 s61, s74, 1009664\ns_mov_b64 exec, 4\nbuffer_atomic_or_x2 v[246:247], v250, s[20:23], s61 offen sc0 sc1\ns_mov_b64 exec, -1\n"
"s_waitcnt vmcnt(0)\nv_cmp_eq_u64 s[86:87], s[46:47], v[246:247]\ns_nop 1\ns_and_b32 s62, s86, 4\ns_cmp_eq_u32 s62, 4\ns_cbranch_scc1 Lq984\n"
"s_sleep 1\ns_sub_u32 s48, s48, 1\ns_cmp_gt_i32 s48, 0\ns_cbranch_scc1 Lq983\ns_branch Lq984\nLqE:\n"
  : "+{v0}"(tid), "+{s[92:93]}"(ka), "+{s45}"(bid) :
  : "a0", "a1", "a2", "a3", "a4", "a5", "a6", "a7", "a8", "a9", "a10", "a11", "a12", "a13", "a14", "a15", "a16", "a17", "a18", "a19", "a20", "a21", "a22", "a23", "a24", "a25", "a26", "a27", "a28", "a29", "a30", "a31", "a32", "a33", "a34", "a35", "a36", "a37", "a38", "a39", "a40", "a41", "a42", "a43", "a44", "a45", "a46", "a47", "a48", "a49", "a50", "a51", "a52", "a53", "a54", "a55", "a56", "a57", "a58", "a59", "a60", "a61", "a62", "a63", "a64", "a65", "a66", "a67", "a68", "a69", "a70", "a71", "a72", "a73", "a74", "a75", "a76", "a77", "a78", "a79", "a80", "a81", "a82", "a83", "a84", "a85", "a86", "a87", "a88", "a89", "a90", "a91", "a92", "a93", "a94", "a95", "a96", "a97", "a98", "a99", "a100", "a101", "a102", "a103", "a104", "a105", "a106", "a107", "a108", "a109", "a110", "a111", "a112", "a113", "a114", "a115", "a116", "a117", "a118", "a119", "a120", "a121", "a122", "a123", "a124", "a125", "a126", "a127", "a128", "a129", "a130", "a131", "a132", "a133", "a134", "a135", "a136", "a137", "a138", "a139", "a140", "a141", "a142", "a143", "a144", "a145", "a146", "a147", "a148", "a149", "a150", "a151", "a152", "a153", "a154", "a155", "a156", "a157", "a158", "a159", "a160", "a161", "a162", "a163", "a164", "a165", "a166", "a167", "a168", "a169", "a170", "a171", "a172", "a173", "a174", "a175", "a176", "a177", "a178", "a179", "a180", "a181", "a182", "a183", "a184", "a185", "a186", "a187", "a188", "a189", "a190", "a191", "a192", "a193", "a194", "a195", "a196", "a197", "a198", "a199", "a200", "a201", "a202", "a203", "a204", "a205", "a206", "a207", "a208", "a209", "a210", "a211", "a212", "a213", "a214", "a215", "a216", "a217", "a218", "a219", "a220", "a221", "a222", "a223", "a224", "a225", "a226", "a227", "a228", "a229", "a230", "a231", "a232", "a233", "a234", "a235", "a236", "a237", "a238", "a239", "a240", "a241", "a242", "a243", "a244", "a245", "a246", "a247", "a248", "a249", "a250", "a251", "a252", "a253", "a254", "a255", "v1", "v2", "v3", "v4", "v5", "v6", "v7", "v8", "v9", "v10", "v11", "v12", "v13", "v14", "v15", "v16", "v17", "v18", "v19", "v20", "v21", "v22", "v23", "v24", "v25", "v26", "v27", "v28", "v29", "v30", "v31", "v32", "v33", "v34", "v35", "v36", "v37", "v38", "v39", "v40", "v41", "v42", "v43", "v44", "v45", "v46", "v47", "v48", "v49", "v50", "v51", "v52", "v53", "v54", "v55", "v56", "v57", "v58", "v59", "v60", "v61", "v62", "v63", "v64", "v65", "v66", "v67", "v68", "v69", "v70", "v71", "v72", "v73", "v74", "v75", "v76", "v77", "v78", "v79", "v80", "v81", "v82", "v83", "v84", "v85", "v86", "v87", "v88", "v89", "v90", "v91", "v92", "v93", "v94", "v95", "v96", "v97", "v98", "v99", "v100", "v101", "v102", "v103", "v104", "v105", "v106", "v107", "v108", "v109", "v110", "v111", "v112", "v113", "v114", "v115", "v116", "v117", "v118", "v119", "v120", "v121", "v122", "v123", "v124", "v125", "v126", "v127", "v128", "v129", "v130", "v131", "v132", "v133", "v134", "v135", "v136", "v137", "v138", "v139", "v140", "v141", "v142", "v143", "v144", "v145", "v146", "v147", "v148", "v149", "v150", "v151", "v152", "v153", "v154", "v155", "v156", "v157", "v158", "v159", "v160", "v161", "v162", "v163", "v164", "v165", "v166", "v167", "v168", "v169", "v170", "v171", "v172", "v173", "v174", "v175", "v176", "v177", "v178", "v179", "v180", "v181", "v182", "v183", "v184", "v185", "v186", "v187", "v188", "v189", "v190", "v191", "v192", "v193", "v194", "v195", "v196", "v197", "v198", "v199", "v200", "v201", "v202", "v203", "v204", "v205", "v206", "v207", "v208", "v209", "v210", "v211", "v212", "v213", "v214", "v215", "v216", "v217", "v218", "v219", "v220", "v221", "v222", "v223", "v224", "v225", "v226", "v227", "v228", "v229", "v230", "v231", "v232", "v233", "v234", "v235", "v236", "v237", "v238", "v239", "v240", "v241", "v242", "v243", "v244", "v245", "v246", "v247", "v248", "v249", "v250", "v251", "v252", "v253", "v254", "v255", "s4", "s5", "s6", "s7", "s8", "s9", "s10", "s11", "s12", "s13", "s14", "s15", "s16", "s17", "s18", "s19", "s20", "s21", "s22", "s23", "s24", "s25", "s26", "s27", "s28", "s29", "s30", "s31", "s32", "s33", "s34", "s35", "s36", "s37", "s38", "s39", "s40", "s41", "s42", "s43", "s44", "s46", "s47", "s48", "s49", "s50", "s51", "s52", "s53", "s54", "s55", "s56", "s57", "s58", "s59", "s60", "s61", "s62", "s63", "s64", "s65", "s66", "s67", "s68", "s69", "s70", "s71", "s72", "s73", "s74", "s75", "s76", "s77", "s78", "s79", "s80", "s81", "s82", "s83", "s84", "s85", "s86", "s87", "s88", "s89", "s90", "s91", "s94", "s95", "s96", "s97", "s98", "s99", "s100", "s101", "vcc", "m0", "scc", "memory");
#endif
}
extern "C" void kernel_launch(void* const* d_in, const int* in_sizes, int n_in, void* d_out, int out_size, void* d_ws, size_t ws_size, hipStream_t stream) {
  (void)hipFuncSetAttribute((const void*)layers, hipFuncAttributeMaxDynamicSharedMemorySize, 160 * 1024);
  hipLaunchKernelGGL(layers, dim3(256), dim3(256), 160 * 1024, stream, d_in[0], d_in[1], d_in[2], d_in[4], d_in[5], d_in[6], d_in[7], d_out, d_ws);
}
```

```cpp
#include <hip/hip_runtime.h>
__global__ __launch_bounds__(256, 1) __attribute__((aligned(4096))) void layers(const void* x, const void* wqkv, const void* wo, const void* w1, const void* b1, const void* w2, const void* b2, void* out, void* ws) {
#if defined(__HIP_DEVICE_COMPILE__)
  int tid = threadIdx.x, bid = blockIdx.x;
  unsigned long long ka = (unsigned long long)__builtin_amdgcn_kernarg_segment_ptr();
  asm volatile(
".macro M d,w,a\nv_mfma_f32_16x16x32_bf16 v[\\d:\\d+3], v[\\w:\\w+3], a[\\a:\\a+3], v[\\d:\\d+3]\n.endm\n.macro N d,a,w\nv_mfma_f32_16x16x32_bf16 v[\\d:\\d+3], a[\\a:\\a+3], v[\\w:\\w+3], v[\\d:\\d+3]\n.endm\n"
".macro R d,b,o\nds_read_b64_tr_b16 v[\\d:\\d+1], v\\b offset:\\o\n.endm\n.macro G n\ns_waitcnt lgkmcnt(\\n)\n.endm\n"
"s_load_dwordx2 s[24:25], s[92:93], 0\ns_load_dwordx2 s[4:5], s[92:93], 8\ns_load_dwordx2 s[8:9], s[92:93], 16\ns_load_dwordx2 s[12:13], s[92:93], 24\ns_load_dwordx2 s[32:33], s[92:93], 32\ns_load_dwordx2 s[16:17], s[92:93], 40\n"
"s_load_dwordx2 s[36:37], s[92:93], 48\ns_load_dwordx2 s[28:29], s[92:93], 56\ns_load_dwordx2 s[20:21], s[92:93], 64\nv_lshrrev_b32 v1, 6, v0\nv_and_b32 v0, 63, v0\nv_readfirstlane_b32 s40, v1\n"
"v_lshlrev_b32 v252, 4, v0\ns_getpc_b64 s[98:99]\nLqPC:\ns_lshr_b32 s60, s45, 3\ns_lshl2_add_u32 s60, s60, s40\ns_lshl_b32 s60, s60, 8\n"
"v_lshl_add_u32 v224, v252, 3, s60\nv_add_u32 v224, LqPC-LqPC, v224\nv_min_u32 v224, LqE-LqPC-4, v224\ns_mov_b64 exec, 3\nglobal_load_dword v224, v224, s[98:99]\ns_mov_b64 exec, -1\n"
"s_mov_b32 s6, 6291456\ns_mov_b32 s7, 0x20000\ns_mov_b32 s10, 2097152\ns_mov_b32 s11, 0x20000\ns_mov_b32 s14, 7340032\ns_mov_b32 s15, 0x20000\n"
"s_mov_b32 s18, 7340032\ns_mov_b32 s19, 0x20000\ns_mov_b32 s22, 89128960\ns_mov_b32 s23, 0x20000\ns_mov_b32 s26, 4194304\ns_mov_b32 s27, 0x20000\n"
"s_mov_b32 s30, 4194304\ns_mov_b32 s31, 0x20000\ns_mov_b32 s34, 7168\ns_mov_b32 s35, 0x20000\ns_mov_b32 s38, 2048\ns_mov_b32 s39, 0x20000\n"
"s_bfe_u32 s41, s45, 0x30003\ns_bfe_u32 s60, s45, 65536\ns_lshl3_add_u32 s41, s60, s41\ns_bfe_u32 s43, s45, 131073\ns_lshr_b32 s44, s45, 6\ns_lshl2_add_u32 s42, s43, s44\n"
"s_lshl2_add_u32 s75, s44, s43\ns_and_b32 s90, s40, 1\ns_lshl2_add_u32 s60, s41, s40\ns_lshl_b32 s49, s60, 16\ns_lshl_b32 s50, s40, 11\ns_add_u32 s50, s50, 32768\n"
"s_lshl_b32 s52, s42, 7\ns_lshl_b32 s51, s40, 13\ns_lshl_b32 s60, s90, 10\ns_add_u32 s70, s49, s60\ns_xor_b32 s71, s70, 0x10000\ns_mul_i32 s101, s41, 196608\n"
"s_add_u32 s101, s101, s52\ns_lshl_b32 s53, s75, 7\ns_and_b32 s60, s44, 2\ns_lshl2_add_u32 s89, s43, s60\ns_lshr_b32 s60, s40, 1\ns_add_u32 s89, s89, s60\n"
"Lqq0:\nv_lshrrev_b32 v96, 7, v252\nv_lshlrev_b32 v96, 11, v96\nv_lshrrev_b32 v97, 8, v252\nv_lshlrev_b32 v97, 4, v97\nv_and_b32 v98, 0x70, v252\n"
"v_xor_b32 v98, v98, v97\nv_add_u32 v226, v96, v98\nv_xor_b32 v227, 64, v226\nG 0\ns_add_u32 m0, s51, 0\ns_add_u32 s60, s70, 0\n"
"buffer_load_dwordx4 v226, s[24:27], s60 offen lds\ns_add_u32 m0, s51, 1024\ns_add_u32 s60, s70, 16384\nbuffer_load_dwordx4 v227, s[24:27], s60 offen lds\ns_add_u32 m0, s51, 2048\ns_add_u32 s60, s70, 32768\n"
"buffer_load_dwordx4 v226, s[24:27], s60 offen lds\ns_add_u32 m0, s51, 3072\ns_add_u32 s60, s70, 49152\nbuffer_load_dwordx4 v227, s[24:27], s60 offen lds\ns_add_u32 m0, s51, 4096\ns_add_u32 s60, s71, 0\n"
"buffer_load_dwordx4 v226, s[24:27], s60 offen lds\ns_add_u32 m0, s51, 5120\ns_add_u32 s60, s71, 16384\nbuffer_load_dwordx4 v227, s[24:27], s60 offen lds\ns_add_u32 m0, s51, 6144\ns_add_u32 s60, s71, 32768\n"
"buffer_load_dwordx4 v226, s[24:27], s60 offen lds\ns_add_u32 m0, s51, 7168\ns_add_u32 s60, s71, 49152\nbuffer_load_dwordx4 v227, s[24:27], s60 offen lds\nbuffer_load_dwordx2 v[230:231], off, s[20:23], 0 sc1\nv_bfe_u32 v96, v252, 5, 2\n"
"v_bfe_u32 v97, v252, 8, 2\nv_xor_b32 v96, v96, v97\ns_and_b32 s60, s40, 1\ns_lshl_b32 s60, s60, 4\ns_and_b32 s61, s40, 2\ns_lshl1_add_u32 s60, s61, s60\n"
"v_bfe_u32 v97, v252, 7, 2\nv_lshrrev_b32 v98, 9, v252\nv_lshl_add_u32 v97, v98, 3, v97\nv_add_u32 v97, s60, v97\nv_mul_u32_u24 v97, 6144, v97\nv_lshl_add_u32 v97, v96, 5, v97\n"
"v_and_b32 v96, 16, v252\nv_add_u32 v220, v96, v97\nv_add_u32 v221, 3144704, v220\ns_add_u32 m0, s50, 0\ns_add_u32 s63, s52, 2048\nbuffer_load_dwordx4 v220, s[4:7], s63 offen lds\n"
"buffer_load_dwordx4 v221, s[4:7], s63 offen offset:1024 lds\ns_add_u32 m0, s50, 8192\ns_add_u32 s63, s52, 198656\nbuffer_load_dwordx4 v220, s[4:7], s63 offen lds\nbuffer_load_dwordx4 v221, s[4:7], s63 offen offset:1024 lds\ns_lshl2_add_u32 s60, s41, s40\n"
"s_mul_i32 s79, s60, 2176\ns_and_b32 s61, s60, -2\ns_mul_i32 s80, s61, 2176\ns_lshl2_add_u32 s61, s61, s43\ns_mul_i32 s82, s61, 640\ns_mul_i32 s61, s43, 28672\n"
"s_mul_i32 s73, s60, 229376\ns_add_u32 s73, s73, s61\ns_mul_i32 s62, s90, 14336\ns_add_u32 s68, s73, s62\ns_add_u32 s61, s61, s62\ns_xor_b32 s62, s60, 1\n"
"s_mul_i32 s69, s62, 229376\ns_add_u32 s69, s69, s61\ns_mul_i32 s61, s44, 7168\ns_add_u32 s73, s73, s61\ns_lshl2_add_u32 s61, s60, s43\ns_mul_i32 s81, s61, 640\n"
"s_lshl2_add_u32 s61, s60, s44\ns_mul_i32 s74, s61, 640\ns_lshl2_add_u32 s61, s41, s44\ns_mul_i32 s61, s61, 48\ns_add_u32 s61, s61, s40\ns_lshl_b32 s72, s61, 12\n"
"s_mul_i32 s55, s42, 448\ns_add_u32 s54, s55, 64\ns_and_b32 s54, s54, 0xffffff80\ns_bitcmp0_b32 s42, 0\ns_cselect_b32 s61, 384, 0\ns_add_u32 s55, s55, s61\n"
"s_lshl_b32 s61, s41, 9\ns_lshl_b32 s91, s42, 13\ns_add_u32 s91, s91, s61\ns_add_u32 s91, s91, 83886080\ns_mul_i32 s61, s43, 1835008\ns_lshl_b32 s62, s44, 9\n"
"s_add_u32 s61, s61, s62\ns_add_u32 s62, s43, 1\ns_and_b32 s62, s62, 3\ns_lshl_b32 s62, s62, 7\ns_add_u32 s56, s61, s62\ns_add_u32 s62, s43, 2\n"
"s_and_b32 s62, s62, 3\ns_lshl_b32 s62, s62, 7\ns_add_u32 s57, s61, s62\ns_add_u32 s62, s43, 3\ns_and_b32 s62, s62, 3\ns_lshl_b32 s62, s62, 7\n"
"s_add_u32 s58, s61, s62\ns_add_u32 s62, s43, 4\ns_and_b32 s62, s62, 3\ns_lshl_b32 s62, s62, 7\ns_add_u32 s59, s61, s62\ns_mov_b32 s76, 0x3f803f80\n"
"s_mov_b32 s77, 0xffff0000\ns_mov_b32 s48, 131072\ns_cmp_eq_u32 s60, 0\ns_cselect_b32 s78, 0xf149f2ca, 0\nv_bfe_u32 v96, v252, 6, 3\nv_lshlrev_b32 v96, 7, v96\n"
"v_lshrrev_b32 v97, 9, v252\nv_lshl_add_u32 v96, v97, 11, v96\nv_bfe_u32 v97, v252, 4, 2\nv_lshl_add_u32 v96, v97, 3, v96\ns_lshl_b32 s60, s90, 10\ns_add_u32 s60, s60, 32768\n"
"v_add_u32 v96, s60, v96\nv_bfe_u32 v97, v252, 7, 2\nv_xor_b32 v98, 0, v97\nv_lshl_add_u32 v184, v98, 5, v96\nv_add_u32 v188, 0x10000, v184\nv_xor_b32 v98, 1, v97\n"
"v_lshl_add_u32 v185, v98, 5, v96\nv_add_u32 v189, 0x10000, v185\nv_xor_b32 v98, 2, v97\nv_lshl_add_u32 v186, v98, 5, v96\nv_add_u32 v190, 0x10000, v186\nv_xor_b32 v98, 3, v97\n"
"v_lshl_add_u32 v187, v98, 5, v96\nv_add_u32 v191, 0x10000, v187\nv_lshrrev_b32 v96, 2, v252\ns_xor_b32 s60, s51, 0x2000\nv_add_u32 v225, s60, v96\nv_mov_b32 v210, 0\n"
"v_mov_b32 v211, 0\nv_bfe_u32 v96, v252, 5, 2\nv_bfe_u32 v97, v252, 8, 2\nv_xor_b32 v96, v96, v97\nv_lshrrev_b32 v97, 7, v252\ns_lshl_b32 s60, s40, 3\n"
"v_add_u32 v97, s60, v97\nv_mov_b32 v228, v97\nv_and_b32 v97, 16, v252\nv_lshl_add_u32 v229, v96, 5, v97\nv_bfe_u32 v96, v252, 5, 2\nv_bfe_u32 v97, v252, 8, 2\n"
"v_xor_b32 v96, v96, v97\nv_lshrrev_b32 v97, 7, v252\ns_lshl_b32 s60, s40, 3\nv_add_u32 v97, s60, v97\nv_lshrrev_b32 v98, 1, v96\nv_lshl_add_u32 v97, v98, 5, v97\n"
"v_and_b32 v96, 1, v96\nv_mul_u32_u24 v97, 7168, v97\nv_lshl_add_u32 v97, v96, 5, v97\nv_and_b32 v96, 16, v252\nv_add_u32 v222, v96, v97\nv_add_u32 v223, 3668992, v222\n"
"v_bfe_u32 v96, v252, 4, 4\nv_lshrrev_b32 v97, 8, v252\nv_lshrrev_b32 v98, 1, v96\nv_xor_b32 v97, v97, v98\nv_lshlrev_b32 v96, 7, v96\nv_lshl_add_u32 v96, v97, 4, v96\n"
"v_add_u32 v248, s51, v96\nv_xor_b32 v249, 64, v248\nv_add_u32 v250, 122880, v248\nv_add_u32 v251, 122880, v249\ns_add_u32 m0, s51, 122880\ns_add_u32 s60, s70, 128\n"
"buffer_load_dwordx4 v226, s[24:27], s60 offen lds\ns_add_u32 m0, s51, 123904\ns_add_u32 s60, s70, 16512\nbuffer_load_dwordx4 v227, s[24:27], s60 offen lds\ns_add_u32 m0, s51, 124928\ns_add_u32 s60, s70, 32896\n"
"buffer_load_dwordx4 v226, s[24:27], s60 offen lds\ns_add_u32 m0, s51, 125952\ns_add_u32 s60, s70, 49280\nbuffer_load_dwordx4 v227, s[24:27], s60 offen lds\ns_add_u32 m0, s51, 126976\ns_add_u32 s60, s71, 128\n"
"buffer_load_dwordx4 v226, s[24:27], s60 offen lds\ns_add_u32 m0, s51, 128000\ns_add_u32 s60, s71, 16512\nbuffer_load_dwordx4 v227, s[24:27], s60 offen lds\ns_add_u32 m0, s51, 129024\ns_add_u32 s60, s71, 32896\n"
"buffer_load_dwordx4 v226, s[24:27], s60 offen lds\ns_add_u32 m0, s51, 130048\ns_add_u32 s60, s71, 49280\nbuffer_load_dwordx4 v227, s[24:27], s60 offen lds\ns_add_u32 m0, s50, 16384\ns_add_u32 s63, s52, 4096\n"
"buffer_load_dwordx4 v220, s[4:7], s63 offen lds\nbuffer_load_dwordx4 v221, s[4:7], s63 offen offset:1024 lds\ns_add_u32 m0, s50, 24576\ns_add_u32 s63, s52, 200704\nbuffer_load_dwordx4 v220, s[4:7], s63 offen lds\nbuffer_load_dwordx4 v221, s[4:7], s63 offen offset:1024 lds\n"
"s_add_u32 m0, s50, 32768\ns_add_u32 s63, s101, 2048\nbuffer_load_dwordx4 v220, s[4:7], s63 offen lds\nbuffer_load_dwordx4 v221, s[4:7], s63 offen offset:1024 lds\ns_add_u32 m0, s50, 40960\ns_add_u32 s63, s101, 4096\n"
"buffer_load_dwordx4 v220, s[4:7], s63 offen lds\nbuffer_load_dwordx4 v221, s[4:7], s63 offen offset:1024 lds\nv_bfe_u32 v246, v252, 4, 4\nv_lshlrev_b32 v246, 11, v246\nv_lshrrev_b32 v247, 8, v252\nv_lshl_add_u32 v246, v247, 3, v246\n"
"s_add_u32 s84, s49, s53\ns_add_u32 s85, s84, 32768\nbuffer_load_dwordx2 v[192:193], v246, s[24:27], s84 offen offset:0\nbuffer_load_dwordx2 v[194:195], v246, s[24:27], s84 offen offset:32\nbuffer_load_dwordx2 v[196:197], v246, s[24:27], s84 offen offset:64\nbuffer_load_dwordx2 v[198:199], v246, s[24:27], s84 offen offset:96\n"
"buffer_load_dwordx2 v[200:201], v246, s[24:27], s85 offen offset:0\nbuffer_load_dwordx2 v[202:203], v246, s[24:27], s85 offen offset:32\nbuffer_load_dwordx2 v[204:205], v246, s[24:27], s85 offen offset:64\nbuffer_load_dwordx2 v[206:207], v246, s[24:27], s85 offen offset:96\nv_mov_b32 v106, 0xf149f149\nv_mov_b32 v104, 0xffff\n"
"v_mov_b32 v105, 0xffff0000\ns_mov_b32 s60, 0x50000\ns_mov_b32 s61, 0x5550055\nv_cndmask_b32 v96, 0, v104, s[60:61]\ns_mov_b32 s60, 0xa0000\ns_mov_b32 s61, 0xaaa00aa\n"
"v_cndmask_b32 v96, v96, v105, s[60:61]\nv_bfi_b32 v100, v96, 0, v106\ns_mov_b32 s60, 0x150001\ns_mov_b32 s61, 0x15550155\nv_cndmask_b32 v97, 0, v104, s[60:61]\ns_mov_b32 s60, 0x2a0002\n"
"s_mov_b32 s61, 0x2aaa02aa\nv_cndmask_b32 v97, v97, v105, s[60:61]\nv_bfi_b32 v101, v97, 0, v106\ns_mov_b32 s60, 0x55505555\ns_mov_b32 s61, 0x50005500\nv_cndmask_b32 v98, 0, v104, s[60:61]\n"
"s_mov_b32 s60, 0xaaa0aaaa\ns_mov_b32 s61, 0xa000aa00\nv_cndmask_b32 v98, v98, v105, s[60:61]\nv_bfi_b32 v102, v98, 0, v106\ns_mov_b32 s60, 0x55405554\ns_mov_b32 s61, 0x40005400\n"
"v_cndmask_b32 v99, 0, v104, s[60:61]\ns_mov_b32 s60, 0xaa80aaa8\ns_mov_b32 s61, 0x8000a800\nv_cndmask_b32 v99, v99, v105, s[60:61]\nv_bfi_b32 v103, v99, 0, v106\nv_add_u32 v104, 161792, v252\n"
"ds_write_b128 v104, v[96:99] offset:0\nds_write_b128 v104, v[100:103] offset:1024\ns_waitcnt vmcnt(28)\nv_readfirstlane_b32 s83, v230\nv_readfirstlane_b32 s60, v231\ns_mul_i32 s46, s83, 0x9e3779b1\n"
"s_add_u32 s46, s46, 0x7f4a7c15\ns_xor_b32 s47, s83, s60\ns_mul_i32 s47, s47, 0x85ebca6b\ns_add_u32 s47, s47, 0xc2b2ae35\nv_mov_b32 v144, -1\nv_mov_b32 v145, -1\n"
"v_mov_b32 v146, -1\nv_mov_b32 v147, -1\ns_mov_b64 exec, 0\ns_cmp_eq_u32 s40, 1\ns_cbranch_scc1 Lpg2\ns_cmp_eq_u32 s40, 2\n"
"s_cbranch_scc1 Lpg3\ns_cmp_eq_u32 s40, 3\ns_cbranch_scc1 Lpg4\ns_branch Lpg1\nLretQ0:\ns_mov_b64 exec, -1\n"
"ds_read_b128 a[0:3], v248 offset:0\nds_read_b128 a[4:7], v248 offset:2048\nds_read_b128 a[8:11], v248 offset:4096\nds_read_b128 a[12:15], v248 offset:6144\nds_read_b128 a[16:19], v249 offset:0\nds_read_b128 a[20:23], v249 offset:2048\n"
"ds_read_b128 a[24:27], v249 offset:4096\nds_read_b128 a[28:31], v249 offset:6144\ns_waitcnt vmcnt(12) lgkmcnt(8)\ns_barrier\nR 160,184,0\nR 162,184,4096\n"
"R 164,184,8192\nR 166,184,12288\nR 168,185,0\nR 170,185,4096\nR 172,185,8192\nR 174,185,12288\n"
"R 176,186,0\nR 178,186,4096\nR 180,186,8192\nR 182,186,12288\nG 10\nv_mfma_f32_16x16x32_bf16 v[0:3], v[160:163], a[0:3], 0\n"
"s_add_u32 m0, s50, 49152\ns_add_u32 s63, s52, 395264\nbuffer_load_dwordx4 v220, s[4:7], s63 offen lds\nG 8\nM 0,164,16\nbuffer_load_dwordx4 v221, s[4:7], s63 offen offset:1024 lds\n"
"v_mfma_f32_16x16x32_bf16 v[16:19], v[160:163], a[4:7], 0\ns_add_u32 m0, s51, 0\ns_add_u32 s60, s70, 256\nbuffer_load_dwordx4 v226, s[24:27], s60 offen lds\nM 16,164,20\ns_lshl4_add_u32 s62, s41, s42\n"
"s_lshl_b32 s62, s62, 12\ns_add_u32 s62, s62, 77594624\ns_lshl_b32 s60, s40, 10\ns_add_u32 s62, s62, s60\nbuffer_store_dwordx4 v[144:147], v252, s[20:23], s62 offen sc1\ns_add_u32 m0, s50, 57344\n"
"s_add_u32 s63, s52, 591872\nv_mfma_f32_16x16x32_bf16 v[32:35], v[160:163], a[8:11], 0\nbuffer_load_dwordx4 v220, s[4:7], s63 offen lds\nM 32,164,24\nbuffer_load_dwordx4 v221, s[4:7], s63 offen offset:1024 lds\nv_mfma_f32_16x16x32_bf16 v[48:51], v[160:163], a[12:15], 0\n"
"s_add_u32 m0, s51, 1024\ns_add_u32 s60, s70, 16640\nbuffer_load_dwordx4 v227, s[24:27], s60 offen lds\nM 48,164,28\ns_add_u32 m0, s50, 65536\ns_add_u32 s63, s52, 397312\n"
"buffer_load_dwordx4 v220, s[4:7], s63 offen lds\nG 4\nv_mfma_f32_16x16x32_bf16 v[52:55], v[168:171], a[12:15], 0\nR 160,187,0\nM 52,172,28\nbuffer_load_dwordx4 v221, s[4:7], s63 offen offset:1024 lds\n"
"v_mfma_f32_16x16x32_bf16 v[36:39], v[168:171], a[8:11], 0\nR 162,187,4096\nM 36,172,24\ns_add_u32 m0, s51, 2048\ns_add_u32 s60, s70, 33024\nbuffer_load_dwordx4 v226, s[24:27], s60 offen lds\n"
"v_mfma_f32_16x16x32_bf16 v[20:23], v[168:171], a[4:7], 0\nR 164,187,8192\ns_add_u32 m0, s50, 73728\nM 20,172,20\ns_lshl4_add_u32 s62, s41, s42\ns_lshl_b32 s62, s62, 12\n"
"s_add_u32 s62, s62, 78643200\ns_lshl_b32 s60, s40, 10\ns_add_u32 s62, s62, s60\nbuffer_store_dwordx4 v[144:147], v252, s[20:23], s62 offen sc1\ns_add_u32 s63, s52, 593920\nv_mfma_f32_16x16x32_bf16 v[4:7], v[168:171], a[0:3], 0\n"
"R 166,187,12288\nbuffer_load_dwordx4 v220, s[4:7], s63 offen lds\nM 4,172,16\nG 4\nv_mfma_f32_16x16x32_bf16 v[8:11], v[176:179], a[0:3], 0\nR 168,184,16384\n"
"M 8,180,16\ns_add_u32 m0, s51, 3072\ns_add_u32 s60, s70, 49408\nbuffer_load_dwordx4 v227, s[24:27], s60 offen lds\nv_mfma_f32_16x16x32_bf16 v[24:27], v[176:179], a[4:7], 0\nR 170,184,20480\n"
"s_add_u32 m0, s50, 73728\ns_nop 0\nbuffer_load_dwordx4 v221, s[4:7], s63 offen offset:1024 lds\nM 24,180,20\ns_add_u32 m0, s51, 4096\ns_add_u32 s60, s71, 256\n"
"buffer_load_dwordx4 v226, s[24:27], s60 offen lds\nv_mfma_f32_16x16x32_bf16 v[40:43], v[176:179], a[8:11], 0\nR 172,184,24576\nM 40,180,24\nv_mfma_f32_16x16x32_bf16 v[56:59], v[176:179], a[12:15], 0\nR 174,184,28672\n"
"M 56,180,28\nG 4\nv_mfma_f32_16x16x32_bf16 v[60:63], v[160:163], a[12:15], 0\nR 176,185,16384\nM 60,164,28\ns_add_u32 m0, s51, 5120\n"
"s_add_u32 s60, s71, 16640\nbuffer_load_dwordx4 v227, s[24:27], s60 offen lds\nv_mfma_f32_16x16x32_bf16 v[44:47], v[160:163], a[8:11], 0\nR 178,185,20480\nM 44,164,24\ns_add_u32 m0, s51, 6144\n"
"s_add_u32 s60, s71, 33024\nbuffer_load_dwordx4 v226, s[24:27], s60 offen lds\nv_mfma_f32_16x16x32_bf16 v[28:31], v[160:163], a[4:7], 0\nR 180,185,24576\ns_add_u32 m0, s50, 81920\ns_add_u32 s63, s101, 0\n"
"M 28,164,20\nbuffer_load_dwordx4 v220, s[4:7], s63 offen lds\nv_mfma_f32_16x16x32_bf16 v[12:15], v[160:163], a[0:3], 0\nR 182,185,28672\nM 12,164,16\ns_waitcnt vmcnt(12)\n"
"s_barrier\nG 4\nv_mfma_f32_16x16x32_bf16 v[64:67], a[0:3], v[168:171], 0\nR 160,186,16384\nN 64,16,172\ns_add_u32 m0, s51, 7168\n"
"s_add_u32 s60, s71, 49408\nbuffer_load_dwordx4 v227, s[24:27], s60 offen lds\nv_mfma_f32_16x16x32_bf16 v[68:71], a[4:7], v[168:171], 0\nR 162,186,20480\ns_add_u32 m0, s50, 81920\nN 68,20,172\n"
"buffer_load_dwordx4 v221, s[4:7], s63 offen offset:1024 lds\nv_mfma_f32_16x16x32_bf16 v[96:99], a[8:11], v[168:171], 0\nR 164,186,24576\ns_add_u32 m0, s50, 0\ns_add_u32 s63, s52, 788480\nN 96,24,172\n"
"buffer_load_dwordx4 v220, s[4:7], s63 offen lds\nv_mfma_f32_16x16x32_bf16 v[100:103], a[12:15], v[168:171], 0\nR 166,186,28672\nN 100,28,172\nG 4\nv_mfma_f32_16x16x32_bf16 v[108:111], a[12:15], v[176:179], 0\n"
"R 168,187,16384\nv_mov_b32 v242, s76\nv_mov_b32 v243, s76\nN 108,28,180\nbuffer_load_dwordx4 v221, s[4:7], s63 offen offset:1024 lds\nv_mov_b32 v244, s76\n"
"v_mfma_f32_16x16x32_bf16 v[104:107], a[8:11], v[176:179], 0\nR 170,187,20480\ns_add_u32 s63, s52, 985088\nN 104,24,180\ns_add_u32 m0, s50, 8192\ns_lshl_b32 s60, s40, 5\n"
"v_xor_b32 v238, s60, v184\nv_mfma_f32_16x16x32_bf16 v[76:79], a[4:7], v[176:179], 0\nR 172,187,24576\nv_and_b32 v238, 0xfffffbff, v238\nN 76,20,180\nbuffer_load_dwordx4 v220, s[4:7], s63 offen lds\n"
"v_mfma_f32_16x16x32_bf16 v[72:75], a[0:3], v[176:179], 0\nR 174,187,28672\nv_mov_b32 v245, s76\nN 72,16,180\nG 4\nv_mfma_f32_16x16x32_bf16 v[80:83], a[0:3], v[160:163], 0\n"
"R 176,184,49152\nds_read_b128 a[32:35], v250 offset:0\nR 128,238,32768\nR 130,238,36864\nN 80,16,164\nds_read_b128 a[36:39], v250 offset:2048\n"
"v_mfma_f32_16x16x32_bf16 v[84:87], a[4:7], v[160:163], 0\nR 178,184,53248\nds_read_b128 a[40:43], v250 offset:4096\nR 132,238,33792\nR 134,238,37888\nN 84,20,164\n"
"ds_read_b128 a[44:47], v250 offset:6144\nv_mfma_f32_16x16x32_bf16 v[112:115], a[8:11], v[160:163], 0\nR 180,184,57344\nbuffer_load_dwordx4 v221, s[4:7], s63 offen offset:1024 lds\nN 112,24,164\nds_read_b128 a[48:51], v251 offset:0\n"
"v_mfma_f32_16x16x32_bf16 v[116:119], a[12:15], v[160:163], 0\nR 182,184,61440\nds_read_b128 a[52:55], v251 offset:2048\nN 116,28,164\nds_read_b128 a[56:59], v251 offset:4096\nG 15\n"
"v_mfma_f32_16x16x32_bf16 v[124:127], a[12:15], v[168:171], 0\nR 160,185,49152\nG 12\nv_mfma_f32_16x16x32_bf16 v[136:139], v[242:245], v[128:131], 0\nN 124,28,172\nds_read_b128 a[60:63], v251 offset:6144\n"
"G 8\nv_mfma_f32_16x16x32_bf16 v[136:139], v[242:245], v[132:135], v[136:139]\nv_mfma_f32_16x16x32_bf16 v[120:123], a[8:11], v[168:171], 0\nR 162,185,53248\nN 120,24,172\nv_mfma_f32_16x16x32_bf16 v[92:95], a[4:7], v[168:171], 0\n"
"R 164,185,57344\nN 92,20,172\nG 2\ns_add_u32 m0, s51, 122880\ns_add_u32 s60, s70, 384\nbuffer_load_dwordx4 v226, s[24:27], s60 offen lds\n"
"v_mfma_f32_16x16x32_bf16 v[88:91], a[0:3], v[168:171], 0\nR 166,185,61440\nv_lshrrev_b32 v128, 1, v252\nv_mov_b32 v137, s46\nN 88,16,172\ns_waitcnt vmcnt(11)\n"
"s_barrier\nM 0,176,32\nR 168,186,49152\ns_lshl_b32 s60, s40, 5\nv_xor_b32 v238, s60, v184\nM 0,180,48\n"
"s_add_u32 m0, s51, 123904\ns_add_u32 s60, s70, 16768\nbuffer_load_dwordx4 v227, s[24:27], s60 offen lds\ns_and_b32 s61, s40, 3\ns_lshl_b32 s61, s61, 7\ns_add_u32 s61, s61, s91\n"
"s_add_u32 s61, s61, 131072\ns_mov_b64 exec, 0xffff\nbuffer_store_dwordx2 v[136:137], v128, s[20:23], s61 offen sc1\ns_mov_b64 exec, -1\nM 16,176,36\nR 170,186,53248\n"
"s_add_u32 m0, s50, 16384\ns_add_u32 s63, s52, 790528\nM 16,180,52\nbuffer_load_dwordx4 v220, s[4:7], s63 offen lds\nM 32,176,40\nR 172,186,57344\n"
"v_and_b32 v238, 0xfffffbff, v238\nM 32,180,56\ns_add_u32 m0, s51, 124928\ns_add_u32 s60, s70, 33152\nbuffer_load_dwordx4 v226, s[24:27], s60 offen lds\nM 48,176,44\n"
"R 174,186,61440\nLpg1:\ns_cbranch_execz Lpg5\ns_add_u32 m0, s50, 16384\nM 48,180,60\nbuffer_load_dwordx4 v221, s[4:7], s63 offen offset:1024 lds\n"
"G 4\nM 52,160,44\nR 176,187,49152\nR 128,238,40960\nR 130,238,45056\nM 52,164,60\n"
"s_add_u32 m0, s51, 125952\ns_add_u32 s60, s70, 49536\nbuffer_load_dwordx4 v227, s[24:27], s60 offen lds\nM 36,160,40\nR 178,187,53248\nR 132,238,41984\n"
"R 134,238,46080\ns_add_u32 m0, s50, 24576\ns_add_u32 s63, s52, 987136\nM 36,164,56\nbuffer_load_dwordx4 v220, s[4:7], s63 offen lds\nM 20,160,36\n"
"R 180,187,57344\nM 20,164,52\ns_add_u32 m0, s51, 126976\ns_add_u32 s60, s71, 384\nbuffer_load_dwordx4 v226, s[24:27], s60 offen lds\nM 4,160,32\n"
"R 182,187,61440\ns_add_u32 m0, s50, 24576\nM 4,164,48\nG 8\nM 8,168,32\nR 160,188,0\n"
"G 6\nv_mfma_f32_16x16x32_bf16 v[136:139], v[242:245], v[128:131], 0\nbuffer_load_dwordx4 v221, s[4:7], s63 offen offset:1024 lds\nM 8,172,48\nG 3\nv_mfma_f32_16x16x32_bf16 v[136:139], v[242:245], v[132:135], v[136:139]\n"
"s_add_u32 m0, s51, 128000\ns_add_u32 s60, s71, 16768\nbuffer_load_dwordx4 v227, s[24:27], s60 offen lds\nM 24,168,36\nR 162,188,4096\nM 24,172,52\n"
"M 40,168,40\nR 164,188,8192\nM 40,172,56\ns_add_u32 m0, s51, 129024\ns_add_u32 s60, s71, 33152\nbuffer_load_dwordx4 v226, s[24:27], s60 offen lds\n"
"M 56,168,44\nR 166,188,12288\nv_lshrrev_b32 v128, 1, v252\nM 56,172,60\nG 4\nv_mov_b32 v137, s46\n"
"M 60,176,44\nR 168,189,0\nM 60,180,60\ns_add_u32 m0, s51, 130048\ns_add_u32 s60, s71, 49536\nbuffer_load_dwordx4 v227, s[24:27], s60 offen lds\n"
"s_and_b32 s61, s40, 3\ns_lshl_b32 s61, s61, 7\ns_add_u32 s61, s61, s91\ns_add_u32 s61, s61, 262144\ns_mov_b64 exec, 0xffff\nbuffer_store_dwordx2 v[136:137], v128, s[20:23], s61 offen sc1\n"
"s_mov_b64 exec, -1\nM 44,176,40\nR 170,189,4096\nM 44,180,56\nM 28,176,36\nR 172,189,8192\n"
"M 28,180,52\ns_add_u32 m0, s50, 32768\nM 12,176,32\nR 174,189,12288\nM 12,180,48\ns_waitcnt vmcnt(14)\n"
"s_barrier\nG 4\nN 64,32,160\nR 176,190,0\nv_mov_b32 v144, s76\nv_mfma_f32_16x16x32_bf16 v[132:135], v[192:195], v[192:195], 0\n"
"v_mov_b32 v145, s76\nv_mov_b32 v146, s76\nv_mov_b32 v147, s76\ns_nop 1\nv_mfma_f32_16x16x32_bf16 v[128:131], v[144:147], v[192:195], 0\nv_lshrrev_b32 v234, 5, v252\n"
"v_and_b32 v234, 24, v234\nv_mfma_f32_16x16x32_bf16 v[136:139], v[144:147], v[200:203], 0\nv_bfe_u32 v235, s46, v234, 8\nv_lshrrev_b32 v234, 1, v234\nv_mfma_f32_16x16x32_bf16 v[140:143], v[200:203], v[200:203], 0\nv_and_b32 v236, 0xf0, v252\n"
"v_add_u32 v234, v234, v236\nN 64,48,164\ns_add_u32 s63, s52, 1181696\nN 68,36,160\nR 178,190,4096\nN 68,52,164\n"
"buffer_load_dwordx4 v220, s[4:7], s63 offen lds\nv_mfma_f32_16x16x32_bf16 v[128:131], v[144:147], v[196:199], v[128:131]\nv_mfma_f32_16x16x32_bf16 v[132:135], v[196:199], v[196:199], v[132:135]\nv_mfma_f32_16x16x32_bf16 v[136:139], v[144:147], v[204:207], v[136:139]\nv_mfma_f32_16x16x32_bf16 v[140:143], v[204:207], v[204:207], v[140:143]\nN 96,40,160\n"
"R 180,190,8192\nds_read_b128 a[64:67], v248 offset:0\nN 96,56,164\nds_read_b128 a[68:71], v248 offset:2048\nN 100,44,160\nR 182,190,12288\n"
"ds_read_b128 a[72:75], v248 offset:4096\nv_mul_f32 v230, 0x3e800000, v128\nN 100,60,164\nds_read_b128 a[76:79], v248 offset:6144\nG 8\nv_mul_f32 v232, 0x3e800000, v136\n"
"N 108,44,168\nR 160,191,0\ns_mov_b32 s60, 0x100001\ns_mov_b32 s61, 0x10000100\nv_cndmask_b32 v231, 0, v132, s[60:61]\nv_cndmask_b32 v233, 0, v140, s[60:61]\n"
"N 108,60,172\nds_read_b128 a[80:83], v249 offset:0\nbuffer_load_dwordx4 v221, s[4:7], s63 offen offset:1024 lds\ns_mov_b32 s60, 0x200002\ns_mov_b32 s61, 0x20000200\nv_cndmask_b32 v231, v231, v133, s[60:61]\n"
"v_cndmask_b32 v233, v233, v141, s[60:61]\nN 104,40,168\nR 162,191,4096\nds_read_b128 a[84:87], v249 offset:2048\ns_mov_b32 s60, 0x400004\ns_mov_b32 s61, 0x40000400\n"
"v_cndmask_b32 v231, v231, v134, s[60:61]\nv_cndmask_b32 v233, v233, v142, s[60:61]\nN 104,56,172\nds_read_b128 a[88:91], v249 offset:4096\ns_add_u32 m0, s50, 40960\ns_mov_b32 s60, 0x800008\n"
"s_mov_b32 s61, 0x80000800\nv_cndmask_b32 v231, v231, v135, s[60:61]\nv_cndmask_b32 v233, v233, v143, s[60:61]\nN 76,36,168\nR 164,191,8192\ns_add_u32 s63, s52, 1378304\n"
"v_permlane32_swap_b32 v230, v231\nN 76,52,172\nds_read_b128 a[92:95], v249 offset:6144\nv_permlane32_swap_b32 v232, v233\nv_add_f32 v230, v230, v231\nN 72,32,168\n"
"R 166,191,12288\nbuffer_load_dwordx4 v220, s[4:7], s63 offen lds\nv_add_f32 v232, v232, v233\nN 72,48,172\nG 10\nv_permlane16_swap_b32 v230, v232\n"
"v_add_f32 v230, v230, v232\nN 80,32,176\nR 168,184,0\ns_movk_i32 s62, 0xff\nv_bfi_b32 v230, s62, v235, v230\nN 80,48,180\n"
"G 2\ns_add_u32 m0, s51, 0\ns_add_u32 s60, s70, 512\nbuffer_load_dwordx4 v226, s[24:27], s60 offen lds\nN 84,36,176\nR 170,184,4096\n"
"s_add_u32 m0, s50, 40960\ns_nop 0\nbuffer_load_dwordx4 v221, s[4:7], s63 offen offset:1024 lds\nN 84,52,180\ns_lshr_b32 s61, s49, 4\ns_lshl1_add_u32 s61, s53, s61\n"
"s_add_u32 s61, s61, 81788928\nbuffer_store_dword v230, v234, s[20:23], s61 offen sc1\nN 112,40,176\nR 172,184,8192\nN 112,56,180\ns_add_u32 m0, s51, 1024\n"
"s_add_u32 s60, s70, 16896\nbuffer_load_dwordx4 v227, s[24:27], s60 offen lds\nN 116,44,176\nR 174,184,12288\nN 116,60,180\nG 4\n"
"N 124,44,160\nR 176,185,0\nN 124,60,164\ns_add_u32 m0, s51, 2048\ns_add_u32 s60, s70, 33280\nbuffer_load_dwordx4 v226, s[24:27], s60 offen lds\n"
"N 120,40,160\nR 178,185,4096\nN 120,56,164\ns_lshl_b32 s60, s40, 5\nv_xor_b32 v238, s60, v188\nv_and_b32 v238, 0xfffffbff, v238\n"
"N 92,36,160\nR 180,185,8192\nN 92,52,164\ns_add_u32 m0, s51, 3072\ns_add_u32 s60, s70, 49664\nbuffer_load_dwordx4 v227, s[24:27], s60 offen lds\n"
"N 88,32,160\nR 182,185,12288\nN 88,48,164\ns_waitcnt vmcnt(13)\ns_barrier\nG 4\n"
"M 0,168,64\nR 160,186,0\nM 0,172,80\ns_add_u32 m0, s51, 4096\ns_add_u32 s60, s71, 512\nbuffer_load_dwordx4 v226, s[24:27], s60 offen lds\n"
"M 16,168,68\nR 162,186,4096\nR 128,238,16384\nR 130,238,20480\ns_add_u32 m0, s50, 49152\ns_add_u32 s63, s52, 1183744\n"
"M 16,172,84\nbuffer_load_dwordx4 v220, s[4:7], s63 offen lds\nM 32,168,72\nR 164,186,8192\nR 132,238,17408\nR 134,238,21504\n"
"M 32,172,88\ns_add_u32 m0, s51, 5120\ns_add_u32 s60, s71, 16896\nbuffer_load_dwordx4 v227, s[24:27], s60 offen lds\nM 48,168,76\nR 166,186,12288\n"
"s_add_u32 m0, s50, 49152\nM 48,172,92\nG 8\nM 52,176,76\nR 168,187,0\nbuffer_load_dwordx4 v221, s[4:7], s63 offen offset:1024 lds\n"
"M 52,180,92\ns_add_u32 m0, s51, 6144\ns_add_u32 s60, s71, 33280\nbuffer_load_dwordx4 v226, s[24:27], s60 offen lds\nM 36,176,72\nR 170,187,4096\n"
"s_add_u32 m0, s50, 57344\nG 6\nv_mfma_f32_16x16x32_bf16 v[136:139], v[242:245], v[128:131], 0\ns_add_u32 s63, s52, 1380352\nM 36,180,88\nbuffer_load_dwordx4 v220, s[4:7], s63 offen lds\n"
"G 3\nv_mfma_f32_16x16x32_bf16 v[136:139], v[242:245], v[132:135], v[136:139]\nM 20,176,68\nR 172,187,8192\nM 20,180,84\ns_add_u32 m0, s51, 7168\n"
"s_add_u32 s60, s71, 49664\nbuffer_load_dwordx4 v227, s[24:27], s60 offen lds\nM 4,176,64\nR 174,187,12288\ns_add_u32 m0, s50, 57344\nM 4,180,80\n"
"G 4\nM 8,160,64\nR 176,184,16384\nbuffer_load_dwordx4 v221, s[4:7], s63 offen offset:1024 lds\nM 8,164,80\nv_lshrrev_b32 v128, 1, v252\n"
"v_mov_b32 v137, s46\nM 24,160,68\nR 178,184,20480\nM 24,164,84\ns_and_b32 s61, s40, 3\ns_lshl_b32 s61, s61, 7\n"
"s_add_u32 s61, s61, s91\ns_add_u32 s61, s61, 0\ns_mov_b64 exec, 0xffff\nbuffer_store_dwordx2 v[136:137], v128, s[20:23], s61 offen sc1\ns_mov_b64 exec, -1\nM 40,160,72\n"
"R 180,184,24576\nM 40,164,88\nM 56,160,76\nR 182,184,28672\nM 56,164,92\nG 4\n"
"M 60,168,76\nR 160,185,16384\nM 60,172,92\ns_add_u32 m0, s50, 65536\nM 44,168,72\nR 162,185,20480\n"
"M 44,172,88\ns_add_u32 s63, s52, 1574912\nM 28,168,68\nR 164,185,24576\nM 28,172,84\nbuffer_load_dwordx4 v220, s[4:7], s63 offen lds\n"
"M 12,168,64\nR 166,185,28672\nM 12,172,80\ns_waitcnt vmcnt(14)\ns_barrier\nG 4\n"
"N 64,64,176\nR 168,186,16384\nN 64,80,180\nv_lshrrev_b32 v230, 7, v252\nN 68,68,176\nR 170,186,20480\n"
"N 68,84,180\nbuffer_load_dwordx4 v221, s[4:7], s63 offen offset:1024 lds\nv_lshlrev_b32 v230, 9, v230\nN 96,72,176\nR 172,186,24576\ns_add_u32 m0, s50, 73728\n"
"N 96,88,180\nds_read_b128 a[96:99], v250 offset:0\nN 100,76,176\nR 174,186,28672\nds_read_b128 a[100:103], v250 offset:2048\ns_add_u32 s63, s52, 1771520\n"
"N 100,92,180\nds_read_b128 a[104:107], v250 offset:4096\nG 7\nN 108,76,160\nR 176,187,16384\nv_and_b32 v231, 0x70, v252\n"
"N 108,92,164\nds_read_b128 a[108:111], v250 offset:6144\nbuffer_load_dwordx4 v220, s[4:7], s63 offen lds\nN 104,72,160\nR 178,187,20480\nds_read_b128 a[112:115], v251 offset:0\n"
"N 104,88,164\nds_read_b128 a[116:119], v251 offset:2048\ns_and_b32 s60, s40, 3\ns_lshl_b32 s60, s60, 7\nv_add3_u32 v230, v230, v231, s60\nN 76,68,160\n"
"R 180,187,24576\nds_read_b128 a[120:123], v251 offset:4096\nN 76,84,164\nds_read_b128 a[124:127], v251 offset:6144\nN 72,64,160\nR 182,187,28672\n"
"N 72,80,164\ns_lshl_b32 s61, s42, 13\ns_add_u32 s61, s61, 84017152\nbuffer_load_dwordx4 v[128:131], v230, s[20:23], s61 offen sc1\nG 11\nN 80,64,168\n"
"R 160,184,32768\nbuffer_load_dwordx4 v221, s[4:7], s63 offen offset:1024 lds\nN 80,80,172\ns_lshl_b32 s61, s42, 13\ns_add_u32 s61, s61, 84021248\nbuffer_load_dwordx4 v[132:135], v230, s[20:23], s61 offen sc1\n"
"N 84,68,168\nR 162,184,36864\nN 84,84,172\nN 112,72,168\nR 164,184,40960\ns_waitcnt vmcnt(8)\n"
"ds_read_b128 a[128:131], v248 offset:0\nN 112,88,172\nds_read_b128 a[132:135], v248 offset:2048\nN 116,76,168\nR 166,184,45056\nds_read_b128 a[136:139], v248 offset:4096\n"
"N 116,92,172\nds_read_b128 a[140:143], v248 offset:6144\nG 8\nN 124,76,176\nR 168,185,32768\nN 124,92,180\n"
"ds_read_b128 a[144:147], v249 offset:0\nN 120,72,176\nR 170,185,36864\nds_read_b128 a[148:151], v249 offset:2048\nN 120,88,180\nds_read_b128 a[152:155], v249 offset:4096\n"
"N 92,68,176\nR 172,185,40960\nN 92,84,180\nds_read_b128 a[156:159], v249 offset:6144\nN 88,64,176\nR 174,185,45056\n"
"N 88,80,180\ns_waitcnt vmcnt(7)\ns_barrier\nG 10\nM 0,160,96\nR 176,186,32768\n"
"M 0,164,112\nG 2\ns_add_u32 m0, s51, 0\ns_add_u32 s60, s70, 640\nbuffer_load_dwordx4 v226, s[24:27], s60 offen lds\nM 16,160,100\n"
"R 178,186,36864\nM 16,164,116\ns_add_u32 m0, s51, 1024\ns_add_u32 s60, s70, 17024\nbuffer_load_dwordx4 v227, s[24:27], s60 offen lds\nM 32,160,104\n"
"R 180,186,40960\ns_add_u32 m0, s50, 81920\ns_add_u32 s63, s52, 1576960\nbuffer_load_dwordx4 v220, s[4:7], s63 offen lds\nv_lshrrev_b32 v234, 7, v252\nM 32,164,120\n"
"s_add_u32 m0, s51, 2048\ns_add_u32 s60, s70, 33408\nbuffer_load_dwordx4 v226, s[24:27], s60 offen lds\nM 48,160,108\nR 182,186,45056\nM 48,164,124\n"
"s_add_u32 m0, s51, 3072\ns_add_u32 s60, s70, 49792\nbuffer_load_dwordx4 v227, s[24:27], s60 offen lds\nG 4\nM 52,168,108\nR 160,187,32768\n"
"s_add_u32 m0, s50, 81920\ns_nop 0\nbuffer_load_dwordx4 v221, s[4:7], s63 offen offset:1024 lds\nv_lshlrev_b32 v234, 9, v234\nM 52,172,124\ns_add_u32 m0, s51, 4096\n"
"s_add_u32 s60, s71, 640\nbuffer_load_dwordx4 v226, s[24:27], s60 offen lds\nM 36,168,104\nR 162,187,36864\nv_and_b32 v235, 0x70, v252\nM 36,172,120\n"
"s_add_u32 m0, s51, 5120\ns_add_u32 s60, s71, 17024\nbuffer_load_dwordx4 v227, s[24:27], s60 offen lds\nM 20,168,100\nR 164,187,40960\ns_add_u32 m0, s50, 90112\n"
"s_add_u32 s63, s52, 1773568\nbuffer_load_dwordx4 v220, s[4:7], s63 offen lds\nM 20,172,116\ns_add_u32 m0, s51, 6144\ns_add_u32 s60, s71, 33408\nbuffer_load_dwordx4 v226, s[24:27], s60 offen lds\n"
"M 4,168,96\nR 166,187,45056\ns_and_b32 s60, s40, 3\ns_lshl_b32 s60, s60, 7\nv_add3_u32 v234, v234, v235, s60\nM 4,172,112\n"
"s_add_u32 m0, s51, 7168\ns_add_u32 s60, s71, 49792\nbuffer_load_dwordx4 v227, s[24:27], s60 offen lds\nG 4\nM 8,176,96\nR 168,184,49152\n"
"s_add_u32 m0, s50, 90112\ns_nop 0\nbuffer_load_dwordx4 v221, s[4:7], s63 offen offset:1024 lds\nM 8,180,112\ns_lshl_b32 s61, s42, 13\ns_add_u32 s61, s61, 84148224\n"
"buffer_load_dwordx4 v[144:147], v234, s[20:23], s61 offen sc1\nM 24,176,100\nR 170,184,53248\nM 24,180,116\nM 40,176,104\nR 172,184,57344\n"
"M 40,180,120\ns_lshl_b32 s61, s42, 13\ns_add_u32 s61, s61, 84152320\nbuffer_load_dwordx4 v[148:151], v234, s[20:23], s61 offen sc1\nM 56,176,108\nR 174,184,61440\n"
"M 56,180,124\nG 4\nM 60,160,108\nR 176,185,49152\nM 60,164,124\nM 44,160,104\n"
"R 178,185,53248\nM 44,164,120\ns_add_u32 m0, s50, 98304\nM 28,160,100\nR 180,185,57344\nM 28,164,116\n"
"s_add_u32 s63, s52, 1968128\nM 12,160,96\nR 182,185,61440\nM 12,164,112\ns_waitcnt vmcnt(15)\ns_barrier\n"
"G 4\nN 64,96,168\nR 160,186,49152\ns_waitcnt vmcnt(14)\nv_min3_u32 v232, v129, v131, v133\nv_min3_u32 v232, v232, v135, v135\n"
"N 64,112,172\nbuffer_load_dwordx4 v220, s[4:7], s63 offen lds\nv_max3_u32 v233, v129, v131, v133\nN 68,100,168\nR 162,186,53248\nv_max3_u32 v233, v233, v135, v135\n"
"v_cmp_ne_u32 s[86:87], s46, v232\nN 68,116,172\nv_cmp_ne_u32 vcc, s46, v233\nN 96,104,168\nR 164,186,57344\ns_or_b64 s[86:87], s[86:87], vcc\n"
"s_cbranch_scc1 Lq116\nLq117:\nN 96,120,172\nbuffer_load_dwordx4 v221, s[4:7], s63 offen offset:1024 lds\nv_add_f32 v128, v128, v132\nN 100,108,168\n"
"R 166,186,61440\ns_add_u32 m0, s50, 106496\nv_add_f32 v130, v130, v134\nN 100,124,172\nG 4\nv_add_f32_dpp v128, v128, v128 row_ror:8 row_mask:0xf bank_mask:0xf\n"
"v_add_f32_dpp v130, v130, v130 row_ror:8 row_mask:0xf bank_mask:0xf\nv_mov_b32 v132, v128\nN 108,108,176\nR 168,187,49152\ns_add_u32 s63, s52, 2164736\nv_mov_b32 v133, v130\n"
"N 108,124,180\nbuffer_load_dwordx4 v220, s[4:7], s63 offen lds\nv_permlane32_swap_b32 v132, v128\nN 104,104,176\nR 170,187,53248\nv_permlane32_swap_b32 v133, v130\n"
"v_add_f32 v128, v128, v132\nN 104,120,180\nv_add_f32 v130, v130, v133\nv_mov_b32 v132, v128\nv_mov_b32 v133, v130\nN 76,100,176\n"
"R 172,187,57344\nv_permlane16_swap_b32 v132, v128\nv_permlane16_swap_b32 v133, v130\nN 76,116,180\nbuffer_load_dwordx4 v221, s[4:7], s63 offen offset:1024 lds\nv_add_f32 v128, v128, v132\n"
"N 72,96,176\nR 174,187,61440\ns_add_u32 s63, s52, 1970176\nN 72,112,180\nLpg2:\ns_cbranch_execz LretQ0\n"
"G 4\nv_add_f32 v130, v130, v133\nv_and_b32 v136, s77, v128\nN 80,96,160\nR 176,188,0\nv_sub_f32 v137, v128, v136\n"
"N 80,112,164\ns_add_u32 m0, s50, 114688\nv_cvt_pk_bf16_f32 v132, v136, v137\nv_cvt_pk_bf16_f32 v133, v136, 0\nN 84,100,160\nR 178,188,4096\n"
"v_and_b32 v136, s77, v130\nN 84,116,164\nbuffer_load_dwordx4 v220, s[4:7], s63 offen lds\nN 112,104,160\nR 180,188,8192\nv_sub_f32 v137, v130, v136\n"
"N 112,120,164\nv_cvt_pk_bf16_f32 v134, v136, v137\nv_cvt_pk_bf16_f32 v135, v136, 0\nN 116,108,160\nR 182,188,12288\nv_and_b32 v230, 0x1ff, v230\n"
"N 116,124,164\nv_add_u32 v230, 156160, v230\nds_write_b128 v230, v[132:135]\nG 5\nN 124,108,168\nR 160,189,0\n"
"N 124,124,172\nbuffer_load_dwordx4 v221, s[4:7], s63 offen offset:1024 lds\nN 120,104,168\nR 162,189,4096\ns_add_u32 s63, s52, 2166784\nN 120,120,172\n"
"s_add_u32 m0, s50, 0\nN 92,100,168\nR 164,189,8192\nN 92,116,172\nbuffer_load_dwordx4 v220, s[4:7], s63 offen lds\nN 88,96,168\n"
"R 166,189,12288\nN 88,112,172\ns_waitcnt vmcnt(10)\nds_read_b128 a[160:163], v248 offset:0\ns_waitcnt vmcnt(9) lgkmcnt(5)\ns_barrier\n"
"M 0,176,128\nR 168,190,0\nds_read_b128 a[164:167], v248 offset:2048\nM 0,180,144\nds_read_b128 a[168:171], v248 offset:4096\nM 16,176,132\n"
"R 170,190,4096\ns_waitcnt vmcnt(7)\nv_min3_u32 v236, v145, v147, v149\nv_min3_u32 v236, v236, v151, v151\nM 16,180,148\nds_read_b128 a[172:175], v248 offset:6144\n"
"v_max3_u32 v237, v145, v147, v149\nv_max3_u32 v237, v237, v151, v151\nM 32,176,136\nR 172,190,8192\nds_read_b128 a[176:179], v249 offset:0\nv_cmp_ne_u32 s[86:87], s46, v236\n"
"M 32,180,152\nds_read_b128 a[180:183], v249 offset:2048\nv_lshrrev_b32 v230, 7, v252\nv_cmp_ne_u32 vcc, s46, v237\nM 48,176,140\nR 174,190,12288\n"
"ds_read_b128 a[184:187], v249 offset:4096\ns_or_b64 s[86:87], s[86:87], vcc\ns_cbranch_scc1 Lq124\nLq125:\nM 48,180,156\nds_read_b128 a[188:191], v249 offset:6144\n"
"G 12\nv_add_f32 v144, v144, v148\nM 52,160,140\nR 176,191,0\nv_add_f32 v146, v146, v150\nv_add_f32_dpp v144, v144, v144 row_ror:8 row_mask:0xf bank_mask:0xf\n"
"M 52,164,156\nbuffer_load_dwordx4 v221, s[4:7], s63 offen offset:1024 lds\nv_lshlrev_b32 v230, 9, v230\nv_add_f32_dpp v146, v146, v146 row_ror:8 row_mask:0xf bank_mask:0xf\nM 36,160,136\nR 178,191,4096\n"
"v_and_b32 v231, 0x70, v252\nv_mov_b32 v148, v144\nM 36,164,152\nG 2\ns_add_u32 m0, s51, 0\ns_add_u32 s60, s70, 768\n"
"buffer_load_dwordx4 v226, s[24:27], s60 offen lds\nv_mov_b32 v149, v146\nM 20,160,132\nR 180,191,8192\nv_permlane32_swap_b32 v148, v144\nv_permlane32_swap_b32 v149, v146\n"
"M 20,164,148\ns_add_u32 m0, s51, 1024\ns_add_u32 s60, s70, 17152\nbuffer_load_dwordx4 v227, s[24:27], s60 offen lds\nv_add_f32 v144, v144, v148\nM 4,160,128\n"
"R 182,191,12288\ns_and_b32 s60, s40, 3\ns_lshl_b32 s60, s60, 7\nv_add3_u32 v230, v230, v231, s60\nv_add_f32 v146, v146, v149\nM 4,164,144\n"
"s_add_u32 m0, s51, 2048\ns_add_u32 s60, s70, 33536\nbuffer_load_dwordx4 v226, s[24:27], s60 offen lds\nv_mov_b32 v148, v144\nM 8,168,128\nR 160,188,16384\n"
"v_mov_b32 v149, v146\nv_permlane16_swap_b32 v148, v144\nM 8,172,144\ns_add_u32 m0, s51, 3072\ns_add_u32 s60, s70, 49920\nbuffer_load_dwordx4 v227, s[24:27], s60 offen lds\n"
"v_permlane16_swap_b32 v149, v146\nM 24,168,132\nR 162,188,20480\ns_lshl_b32 s61, s42, 13\ns_add_u32 s61, s61, 83886080\nbuffer_load_dwordx4 v[128:131], v230, s[20:23], s61 offen sc1\n"
"v_add_f32 v144, v144, v148\nM 24,172,148\ns_add_u32 m0, s51, 4096\ns_add_u32 s60, s71, 768\nbuffer_load_dwordx4 v226, s[24:27], s60 offen lds\nv_add_f32 v146, v146, v149\n"
"M 40,168,136\nR 164,188,24576\ns_lshl_b32 s61, s42, 13\ns_add_u32 s61, s61, 83890176\nbuffer_load_dwordx4 v[132:135], v230, s[20:23], s61 offen sc1\nv_and_b32 v152, s77, v144\n"
"M 40,172,152\ns_add_u32 m0, s51, 5120\ns_add_u32 s60, s71, 17152\nbuffer_load_dwordx4 v227, s[24:27], s60 offen lds\nM 56,168,140\nR 166,188,28672\n"
"v_sub_f32 v153, v144, v152\nM 56,172,156\ns_add_u32 m0, s51, 6144\ns_add_u32 s60, s71, 33536\nbuffer_load_dwordx4 v226, s[24:27], s60 offen lds\nG 4\n"
"M 60,176,140\nR 168,189,16384\nv_cvt_pk_bf16_f32 v148, v152, v153\nM 60,180,156\ns_add_u32 m0, s51, 7168\ns_add_u32 s60, s71, 49920\n"
"buffer_load_dwordx4 v227, s[24:27], s60 offen lds\nM 44,176,136\nR 170,189,20480\nv_cvt_pk_bf16_f32 v149, v152, 0\nM 44,180,152\ns_add_u32 m0, s50, 8192\n"
"v_and_b32 v152, s77, v146\nv_sub_f32 v153, v146, v152\nM 28,176,132\nR 172,189,24576\nv_cvt_pk_bf16_f32 v150, v152, v153\nM 28,180,148\n"
"s_add_u32 s63, s52, 2361344\nv_cvt_pk_bf16_f32 v151, v152, 0\nv_and_b32 v234, 0x1ff, v234\nM 12,176,128\nR 174,189,28672\nv_add_u32 v234, 156672, v234\n"
"M 12,180,144\ns_waitcnt vmcnt(14)\ns_barrier\nG 4\nN 64,128,160\nR 176,190,16384\n"
"ds_write_b128 v234, v[148:151]\nN 64,144,164\nbuffer_load_dwordx4 v220, s[4:7], s63 offen lds\nN 68,132,160\nR 178,190,20480\nN 68,148,164\n"
"N 96,136,160\nR 180,190,24576\nN 96,152,164\nbuffer_load_dwordx4 v221, s[4:7], s63 offen offset:1024 lds\nN 100,140,160\nR 182,190,28672\n"
"s_add_u32 m0, s50, 16384\nN 100,156,164\nG 5\nN 108,140,168\nR 160,191,16384\ns_add_u32 s63, s52, 2557952\n"
"N 108,156,172\nbuffer_load_dwordx4 v220, s[4:7], s63 offen lds\nN 104,136,168\nR 162,191,20480\nN 104,152,172\nN 76,132,168\n"
"R 164,191,24576\nLq183:\ns_lshr_b32 s60, s45, 3\ns_lshl2_add_u32 s60, s60, s40\ns_lshl_b32 s60, s60, 8\nv_lshl_add_u32 v224, v252, 3, s60\n"
"v_add_u32 v224, Lq183+4096-LqPC, v224\nv_min_u32 v224, LqE-LqPC-4, v224\ns_mov_b64 exec, 3\nglobal_load_dword v224, v224, s[98:99]\ns_mov_b64 exec, -1\nN 76,148,172\n"
"buffer_load_dwordx4 v221, s[4:7], s63 offen offset:1024 lds\nN 72,128,168\nR 166,191,28672\nN 72,144,172\nG 4\nN 80,128,176\n"
"R 168,188,32768\nN 80,144,180\nN 84,132,176\nR 170,188,36864\nN 84,148,180\ns_add_u32 m0, s50, 24576\n"
"N 112,136,176\nR 172,188,40960\nN 112,152,180\ns_add_u32 s63, s52, 2754560\nN 116,140,176\nR 174,188,45056\n"
"N 116,156,180\nG 4\nN 124,140,160\nR 176,189,32768\nN 124,156,164\nbuffer_load_dwordx4 v220, s[4:7], s63 offen lds\n"
"N 120,136,160\nR 178,189,36864\nN 120,152,164\nN 92,132,160\nR 180,189,40960\ns_waitcnt vmcnt(9)\n"
"v_min3_u32 v232, v129, v131, v133\nv_min3_u32 v232, v232, v135, v135\nN 92,148,164\nbuffer_load_dwordx4 v221, s[4:7], s63 offen offset:1024 lds\nv_max3_u32 v233, v129, v131, v133\nN 88,128,160\n"
"R 182,189,45056\ns_add_u32 m0, s50, 32768\nv_max3_u32 v233, v233, v135, v135\nN 88,144,164\ns_barrier\nG 4\n"
"v_cmp_ne_u32 s[86:87], s46, v232\nM 0,168,160\nR 160,190,32768\ns_add_u32 s63, s52, 2951168\nv_cmp_ne_u32 vcc, s46, v233\nM 0,172,176\n"
"buffer_load_dwordx4 v220, s[4:7], s63 offen lds\ns_or_b64 s[86:87], s[86:87], vcc\ns_cbranch_scc1 Lq132\nLq133:\nM 16,168,164\nR 162,190,36864\n"
"v_add_f32 v128, v128, v132\nv_add_f32 v130, v130, v134\nM 16,172,180\nv_add_f32_dpp v128, v128, v128 row_ror:8 row_mask:0xf bank_mask:0xf\nv_add_f32_dpp v130, v130, v130 row_ror:8 row_mask:0xf bank_mask:0xf\nv_mov_b32 v132, v128\n"
"M 32,168,168\nR 164,190,40960\nv_mov_b32 v133, v130\nv_permlane32_swap_b32 v132, v128\nM 32,172,184\nv_permlane32_swap_b32 v133, v130\n"
"v_add_f32 v128, v128, v132\nv_add_f32 v130, v130, v133\nM 48,168,172\nR 166,190,45056\nv_mov_b32 v132, v128\nv_mov_b32 v133, v130\n"
"M 48,172,188\nG 4\nv_permlane16_swap_b32 v132, v128\nv_permlane16_swap_b32 v133, v130\nv_add_f32 v128, v128, v132\nM 52,176,172\n"
"R 168,191,32768\nv_add_f32 v130, v130, v133\nv_and_b32 v136, s77, v128\nM 52,180,188\nbuffer_load_dwordx4 v221, s[4:7], s63 offen offset:1024 lds\nv_sub_f32 v137, v128, v136\n"
"M 36,176,168\nR 170,191,36864\ns_waitcnt vmcnt(9)\nds_read_b128 a[192:195], v248 offset:0\nv_cvt_pk_bf16_f32 v132, v136, v137\nM 36,180,184\n"
"ds_read_b128 a[196:199], v248 offset:2048\nv_cvt_pk_bf16_f32 v133, v136, 0\nv_and_b32 v136, s77, v130\nM 20,176,164\nR 172,191,40960\nds_read_b128 a[200:203], v248 offset:4096\n"
"v_sub_f32 v137, v130, v136\nM 20,180,180\nds_read_b128 a[204:207], v248 offset:6144\nv_cvt_pk_bf16_f32 v134, v136, v137\nv_cvt_pk_bf16_f32 v135, v136, 0\nM 4,176,160\n"
"R 174,191,45056\nv_and_b32 v230, 0x1ff, v230\nv_add_u32 v230, 155648, v230\nM 4,180,176\nds_read_b128 a[208:211], v249 offset:0\nds_write_b128 v230, v[132:135]\n"
"G 10\nM 8,160,160\nR 176,188,49152\nds_read_b128 a[212:215], v249 offset:2048\nM 8,164,176\nds_read_b128 a[216:219], v249 offset:4096\n"
"s_add_u32 m0, s50, 40960\nM 24,160,164\nR 178,188,53248\ns_add_u32 s63, s52, 2363392\nM 24,164,180\nds_read_b128 a[220:223], v249 offset:6144\n"
"M 40,160,168\nR 180,184,0\nM 40,164,184\nbuffer_load_dwordx4 v220, s[4:7], s63 offen lds\nM 56,160,172\nR 182,184,4096\n"
"M 56,164,188\nG 2\ns_add_u32 m0, s51, 0\ns_add_u32 s60, s70, 896\nbuffer_load_dwordx4 v226, s[24:27], s60 offen lds\nM 60,168,172\n"
"R 160,189,49152\nM 60,172,188\ns_add_u32 m0, s51, 1024\ns_add_u32 s60, s70, 17280\nbuffer_load_dwordx4 v227, s[24:27], s60 offen lds\nM 44,168,168\n"
"R 162,189,53248\nM 44,172,184\ns_add_u32 m0, s51, 2048\ns_add_u32 s60, s70, 33664\nbuffer_load_dwordx4 v226, s[24:27], s60 offen lds\nM 28,168,164\n"
"R 164,185,0\nM 28,172,180\ns_add_u32 m0, s51, 3072\ns_add_u32 s60, s70, 50048\nbuffer_load_dwordx4 v227, s[24:27], s60 offen lds\nM 12,168,160\n"
"R 166,185,4096\nM 12,172,176\ns_add_u32 m0, s51, 4096\ns_add_u32 s60, s71, 896\nbuffer_load_dwordx4 v226, s[24:27], s60 offen lds\ns_waitcnt vmcnt(10)\n"
"s_barrier\nG 4\nN 64,160,176\nR 168,190,49152\nN 64,176,180\ns_add_u32 m0, s51, 5120\n"
"s_add_u32 s60, s71, 17280\nbuffer_load_dwordx4 v227, s[24:27], s60 offen lds\nN 68,164,176\nR 170,190,53248\ns_lshr_b32 s84, s49, 4\nN 68,180,180\n"
"s_add_u32 m0, s51, 6144\ns_add_u32 s60, s71, 33664\nbuffer_load_dwordx4 v226, s[24:27], s60 offen lds\nN 96,168,176\nR 172,186,0\ns_add_u32 s84, s84, 81788928\n"
"N 96,184,180\ns_add_u32 m0, s51, 7168\ns_add_u32 s60, s71, 50048\nbuffer_load_dwordx4 v227, s[24:27], s60 offen lds\nN 100,172,176\nR 174,186,4096\n"
"s_add_u32 m0, s50, 40960\nN 100,188,180\nG 4\nN 108,172,160\nR 176,191,49152\nbuffer_load_dwordx4 v221, s[4:7], s63 offen offset:1024 lds\n"
"N 108,188,164\nbuffer_load_dwordx4 v[128:131], v252, s[20:23], s84 offen offset:0 sc1\ns_add_u32 s63, s52, 2560000\nN 104,168,160\nR 178,191,53248\ns_add_u32 m0, s50, 49152\n"
"N 104,184,164\nbuffer_load_dwordx4 v[132:135], v252, s[20:23], s84 offen offset:1024 sc1\nN 76,164,160\nR 180,187,0\nbuffer_load_dwordx4 v220, s[4:7], s63 offen lds\nN 76,180,164\n"
"buffer_load_dwordx4 v221, s[4:7], s63 offen offset:1024 lds\nN 72,160,160\nR 182,187,4096\nN 72,176,164\nG 4\nN 80,160,168\n"
"R 160,184,8192\nN 80,176,172\nbuffer_load_dwordx4 v[136:139], v252, s[20:23], s84 offen offset:2048 sc1\nN 84,164,168\nR 162,184,12288\nN 84,180,172\n"
"buffer_load_dwordx4 v[140:143], v252, s[20:23], s84 offen offset:3072 sc1\nN 112,168,168\nR 164,184,16384\nN 112,184,172\nN 116,172,168\nR 166,184,20480\n"
"N 116,188,172\nG 4\nN 124,172,176\nR 168,185,8192\nN 124,188,180\nN 120,168,176\n"
"R 170,185,12288\nN 120,184,180\ns_add_u32 m0, s50, 57344\nN 92,164,176\nR 172,185,16384\nN 92,180,180\n"
"s_add_u32 s63, s52, 2756608\nN 88,160,176\nR 174,185,20480\nN 88,176,180\ns_waitcnt vmcnt(16)\ns_barrier\n"
"G 4\nM 0,160,192\nR 176,186,8192\nM 0,164,208\nbuffer_load_dwordx4 v220, s[4:7], s63 offen lds\nM 16,160,196\n"
"R 178,186,12288\nM 16,164,212\nM 32,160,200\nR 180,186,16384\nM 32,164,216\nbuffer_load_dwordx4 v221, s[4:7], s63 offen offset:1024 lds\n"
"M 48,160,204\nR 182,186,20480\ns_lshl2_add_u32 s60, s45, s40\nv_lshlrev_b32 v224, 3, v252\ns_mul_i32 s60, s60, 2048\ns_bfm_b64 exec, 16, 0\n"
"buffer_load_dword v224, v224, s[8:11], s60 offen\ns_mov_b64 exec, -1\nM 48,164,220\ns_add_u32 s63, s52, 2953216\nG 4\nM 52,168,204\n"
"R 160,187,8192\ns_add_u32 m0, s50, 65536\nM 52,172,220\nbuffer_load_dwordx4 v220, s[4:7], s63 offen lds\nM 36,168,200\nR 162,187,12288\n"
"M 36,172,216\nM 20,168,196\nR 164,187,16384\nM 20,172,212\nbuffer_load_dwordx4 v221, s[4:7], s63 offen offset:1024 lds\nM 4,168,192\n"
"R 166,187,20480\nM 4,172,208\nG 4\nM 8,176,192\nR 168,184,24576\nM 8,180,208\n"
"M 24,176,196\nR 170,184,28672\ns_waitcnt vmcnt(12)\nds_read_b128 a[224:227], v248 offset:0\nM 24,180,212\nds_read_b128 a[228:231], v248 offset:2048\n"
"M 40,176,200\nR 172,184,32768\nds_read_b128 a[232:235], v248 offset:4096\nM 40,180,216\nds_read_b128 a[236:239], v248 offset:6144\nM 56,176,204\n"
"R 174,184,36864\nM 56,180,220\nds_read_b128 a[240:243], v249 offset:0\nG 9\nM 60,160,204\nR 176,185,24576\n"
"ds_read_b128 a[244:247], v249 offset:2048\nM 60,164,220\nds_read_b128 a[248:251], v249 offset:4096\ns_add_u32 m0, s50, 73728\nM 44,160,200\nR 178,185,28672\n"
"s_add_u32 s63, s52, 0\nM 44,164,216\nds_read_b128 a[252:255], v249 offset:6144\nM 28,160,196\nR 180,185,32768\nM 28,164,212\n"
"buffer_load_dwordx4 v220, s[4:7], s63 offen lds\nM 12,160,192\nR 182,185,36864\nM 12,164,208\ns_waitcnt vmcnt(8)\ns_barrier\n"
"G 8\nM 0,168,224\nR 160,186,24576\ns_mov_b32 s85, 0x0c0c0400\nv_perm_b32 v144, v129, v128, s85\nG 8\n"
"M 0,172,240\nbuffer_load_dwordx4 v221, s[4:7], s63 offen offset:1024 lds\nv_perm_b32 v148, v131, v130, s85\nM 16,168,228\nR 162,186,28672\ns_add_u32 s63, s52, 196608\n"
"v_lshl_or_b32 v144, v148, 16, v144\nG 7\nM 16,172,244\ns_add_u32 m0, s50, 81920\nv_perm_b32 v145, v133, v132, s85\nv_perm_b32 v149, v135, v134, s85\n"
"v_lshl_or_b32 v145, v149, 16, v145\nM 32,168,232\nR 164,186,32768\ns_waitcnt vmcnt(8)\nv_perm_b32 v146, v137, v136, s85\nv_perm_b32 v150, v139, v138, s85\n"
"G 7\nM 32,172,248\nbuffer_load_dwordx4 v220, s[4:7], s63 offen lds\nv_lshl_or_b32 v146, v150, 16, v146\nM 48,168,236\nR 166,186,36864\n"
"s_lshl_b32 s60, s90, 14\ns_add_u32 s70, s49, s60\ns_xor_b32 s71, s70, 0x10000\ns_waitcnt vmcnt(8)\nv_perm_b32 v147, v141, v140, s85\nG 6\n"
"M 48,172,252\nLpg3:\ns_cbranch_execz LretQ0\nG 4\nv_perm_b32 v151, v143, v142, s85\nv_lshl_or_b32 v147, v151, 16, v147\n"
"v_min3_u32 v230, v144, v145, v146\nM 52,176,236\nR 168,187,24576\nv_min3_u32 v230, v230, v147, v147\nv_max3_u32 v231, v144, v145, v146\nM 52,180,252\n"
"buffer_load_dwordx4 v221, s[4:7], s63 offen offset:1024 lds\nv_max3_u32 v231, v231, v147, v147\nM 36,176,232\nR 170,187,28672\ns_add_u32 s63, s52, 393216\nv_cmp_ne_u32 s[86:87], s46, v230\n"
"M 36,180,248\ns_add_u32 m0, s50, 90112\nv_cmp_ne_u32 vcc, s46, v231\nM 20,176,228\nR 172,187,32768\ns_or_b64 s[86:87], s[86:87], vcc\n"
"s_cbranch_scc1 Lq140\nLq141:\nM 20,180,244\nbuffer_load_dwordx4 v220, s[4:7], s63 offen lds\nv_and_b32 v128, 0xffffff00, v128\nM 4,176,224\n"
"R 174,187,36864\nv_and_b32 v129, 0xffffff00, v129\nv_and_b32 v130, 0xffffff00, v130\nM 4,180,240\nG 4\nv_and_b32 v131, 0xffffff00, v131\n"
"v_and_b32 v132, 0xffffff00, v132\nv_and_b32 v133, 0xffffff00, v133\nM 8,160,224\nR 176,184,40960\nv_and_b32 v134, 0xffffff00, v134\nv_and_b32 v135, 0xffffff00, v135\n"
"M 8,164,240\nbuffer_load_dwordx4 v221, s[4:7], s63 offen offset:1024 lds\nv_and_b32 v136, 0xffffff00, v136\nM 24,160,228\nR 178,184,45056\nv_and_b32 v137, 0xffffff00, v137\n"
"v_and_b32 v138, 0xffffff00, v138\nM 24,164,244\ns_add_u32 m0, s50, 98304\nv_and_b32 v139, 0xffffff00, v139\nv_and_b32 v140, 0xffffff00, v140\nv_and_b32 v141, 0xffffff00, v141\n"
"M 40,160,232\nR 180,184,49152\nv_and_b32 v142, 0xffffff00, v142\nv_and_b32 v143, 0xffffff00, v143\nM 40,164,248\ns_add_u32 s63, s52, 589824\n"
"v_add_f32 v128, v128, v136\nv_add_f32 v129, v129, v137\nv_add_f32 v130, v130, v138\nM 56,160,236\nR 182,184,53248\nv_add_f32 v131, v131, v139\n"
"v_add_f32 v132, v132, v140\nM 56,164,252\nG 4\nv_add_f32 v133, v133, v141\nv_add_f32 v134, v134, v142\nv_add_f32 v135, v135, v143\n"
"M 60,168,236\nR 160,185,40960\nv_add_f32 v128, v128, v132\nv_add_f32 v129, v129, v133\nM 60,172,252\nbuffer_load_dwordx4 v220, s[4:7], s63 offen lds\n"
"v_add_f32 v130, v130, v134\nM 44,168,232\nR 162,185,45056\nv_add_f32 v131, v131, v135\nv_permlane32_swap_b32 v128, v130\nM 44,172,248\n"
"v_permlane32_swap_b32 v129, v131\nv_add_f32 v128, v128, v130\nv_add_f32 v129, v129, v131\nM 28,168,228\nR 164,185,49152\nv_permlane16_swap_b32 v128, v129\n"
"v_add_f32 v230, v128, v129\nM 28,172,244\nbuffer_load_dwordx4 v221, s[4:7], s63 offen offset:1024 lds\nv_mov_b32 v232, v230\nM 12,168,224\nR 166,185,53248\n"
"v_permlane16_swap_b32 v230, v232\nv_mov_b32 v231, v230\nM 12,172,240\ns_waitcnt vmcnt(8)\ns_barrier\nG 4\n"
"v_mov_b32 v233, v232\nN 64,192,176\nR 168,186,40960\nv_permlane32_swap_b32 v230, v231\nv_permlane32_swap_b32 v232, v233\nN 64,208,180\n"
"s_mov_b32 m0, s51\nv_mul_f32 v230, 0xba800000, v230\nv_mul_f32 v214, v230, v230\nv_mul_f32 v231, 0x3a800000, v231\nN 68,196,176\nR 170,186,45056\n"
"s_lshl2_add_u32 s60, s45, s40\nv_lshlrev_b32 v224, 3, v252\ns_mul_i32 s60, s60, 7168\ns_bfm_b64 exec, 14, 0\nbuffer_load_dword v224, v224, s[12:15], s60 offen\ns_mov_b64 exec, -1\n"
"ds_write_addtid_b32 v32 offset:0\nv_sub_f32 v231, v231, v214\nN 68,212,180\nds_write_addtid_b32 v33 offset:256\nds_write_addtid_b32 v34 offset:512\nv_max_f32 v231, 0, v231\n"
"v_add_f32 v231, 0x3727c5ac, v231\nN 96,200,176\nR 172,186,49152\ns_mov_b32 s60, 156160\nv_bfe_u32 v246, v252, 4, 4\nv_lshl_add_u32 v246, v246, 3, s60\n"
"ds_write_addtid_b32 v35 offset:768\nv_rsq_f32 v234, v231\nN 96,216,180\nds_read_b64 v[238:239], v246 offset:0\nds_write_addtid_b32 v36 offset:1024\nv_and_b32 v208, s77, v230\n"
"v_mul_f32 v216, v231, v234\nN 100,204,176\nR 174,186,53248\nds_read_b64 v[240:241], v246 offset:128\nds_write_addtid_b32 v37 offset:1280\nv_sub_f32 v230, v230, v208\n"
"N 100,220,180\nds_read_b64 v[242:243], v246 offset:256\nds_write_addtid_b32 v38 offset:1536\nG 14\nv_cvt_pk_bf16_f32 v209, v230, v216\nN 108,204,160\n"
"R 176,187,40960\nds_read_b64 v[244:245], v246 offset:384\nds_write_addtid_b32 v39 offset:1792\nv_cvt_pk_bf16_f32 v208, v208, v208\nN 108,220,164\nds_read_b64 v[246:247], v246 offset:0\n"
"ds_write_addtid_b32 v40 offset:2048\nv_mov_b32 v214, v234\nv_mul_f32 v215, 0x3e8293ee, v234\nN 104,200,160\nR 178,187,45056\nds_write_addtid_b32 v41 offset:2304\n"
"v_mul_f32 v232, 0xba800000, v232\nv_mul_f32 v217, v232, v232\nN 104,216,164\ns_mov_b64 s[60:61], 0xffff\nv_cndmask_b32 v208, 0, v208, s[60:61]\nds_write_addtid_b32 v42 offset:2560\n"
"v_mul_f32 v233, 0x3a800000, v233\nv_sub_f32 v233, v233, v217\nN 76,196,160\nR 180,187,49152\nds_write_addtid_b32 v43 offset:2816\nv_max_f32 v233, 0, v233\n"
"v_add_f32 v233, 0x3727c5ac, v233\nN 76,212,164\nv_rsq_f32 v235, v233\nv_and_b32 v212, s77, v232\nv_mul_f32 v219, v233, v235\nv_sub_f32 v232, v232, v212\n"
"v_cvt_pk_bf16_f32 v213, v232, v219\nv_cvt_pk_bf16_f32 v212, v212, v212\nv_mov_b32 v217, v235\nv_mul_f32 v218, 0x3e8293ee, v235\ns_mov_b64 s[60:61], 0xffff\nv_cndmask_b32 v209, 0, v209, s[60:61]\n"
"s_mov_b64 s[60:61], 0xffff\nv_cndmask_b32 v212, 0, v212, s[60:61]\ns_mov_b64 s[60:61], 0xffff\nv_cndmask_b32 v213, 0, v213, s[60:61]\nv_bfe_u32 v230, v252, 2, 6\ns_lshl_b32 s60, s40, 7\n"
"s_add_u32 s60, s60, 161280\nv_add_u32 v230, s60, v230\nds_write_b32 v230, v214 offset:0\nds_write_b32 v230, v217 offset:64\nN 72,192,160\nR 182,187,53248\n"
"N 72,208,164\nds_write_addtid_b32 v44 offset:3072\nG 15\nN 80,192,168\nR 160,184,57344\nN 80,208,172\n"
"ds_write_addtid_b32 v45 offset:3328\nN 84,196,168\nR 162,184,61440\nv_mfma_f32_16x16x32_bf16 v[0:3], v[238:241], v[208:211], v[0:3]\nds_write_addtid_b32 v46 offset:3584\nds_write_addtid_b32 v47 offset:3840\n"
"ds_write_addtid_b32 v48 offset:4096\nN 84,212,172\nds_write_addtid_b32 v49 offset:4352\nds_write_addtid_b32 v50 offset:4608\nN 112,200,168\nR 164,188,0\n"
"v_mfma_f32_16x16x32_bf16 v[4:7], v[240:243], v[208:211], v[4:7]\nds_write_addtid_b32 v51 offset:4864\nds_write_addtid_b32 v52 offset:5120\nds_write_addtid_b32 v53 offset:5376\nN 112,216,172\nds_write_addtid_b32 v54 offset:5632\n"
"ds_write_addtid_b32 v55 offset:5888\nN 116,204,168\nR 166,188,4096\nG 15\nv_mfma_f32_16x16x32_bf16 v[8:11], v[242:245], v[208:211], v[8:11]\nds_write_addtid_b32 v56 offset:6144\n"
"ds_write_addtid_b32 v57 offset:6400\nds_write_addtid_b32 v58 offset:6656\nN 116,220,172\nds_write_addtid_b32 v59 offset:6912\nds_write_addtid_b32 v60 offset:7168\nds_write_addtid_b32 v61 offset:7424\n"
"ds_write_addtid_b32 v62 offset:7680\nds_write_addtid_b32 v63 offset:7936\nN 124,204,176\nR 168,185,57344\ns_add_u32 m0, s50, 106496\nv_mfma_f32_16x16x32_bf16 v[12:15], v[244:247], v[208:211], v[12:15]\n"
"s_add_u32 s63, s52, 786432\nN 124,220,180\nbuffer_load_dwordx4 v220, s[4:7], s63 offen lds\nN 120,200,176\nR 170,185,61440\nv_mfma_f32_16x16x32_bf16 v[16:19], v[244:247], v[210:213], v[16:19]\n"
"s_waitcnt vmcnt(6) lgkmcnt(2)\ns_barrier\nN 120,216,180\nds_read2st64_b32 v[32:33], v225 offset0:0 offset1:1\nds_read2st64_b32 v[34:35], v225 offset0:2 offset1:3\nN 92,196,176\n"
"R 172,189,0\nv_mfma_f32_16x16x32_bf16 v[20:23], v[238:241], v[210:213], v[20:23]\nds_read2st64_b32 v[36:37], v225 offset0:4 offset1:5\nds_read2st64_b32 v[38:39], v225 offset0:6 offset1:7\nds_read2st64_b32 v[40:41], v225 offset0:8 offset1:9\nN 92,212,180\n"
"buffer_load_dwordx4 v221, s[4:7], s63 offen offset:1024 lds\nds_read2st64_b32 v[42:43], v225 offset0:10 offset1:11\nN 88,192,176\nR 174,189,4096\nv_mfma_f32_16x16x32_bf16 v[24:27], v[240:243], v[210:213], v[24:27]\nds_read2st64_b32 v[44:45], v225 offset0:12 offset1:13\n"
"ds_read2st64_b32 v[46:47], v225 offset0:14 offset1:15\nds_read2st64_b32 v[48:49], v225 offset0:16 offset1:17\nN 88,208,180\nds_read2st64_b32 v[50:51], v225 offset0:18 offset1:19\nds_read2st64_b32 v[52:53], v225 offset0:20 offset1:21\nN 64,224,160\n"
"R 176,186,57344\nv_mfma_f32_16x16x32_bf16 v[28:31], v[242:245], v[210:213], v[28:31]\ns_mov_b32 s60, 156672\nv_bfe_u32 v246, v252, 4, 4\nv_lshl_add_u32 v246, v246, 3, s60\ns_add_u32 m0, s50, 114688\n"
"ds_read2st64_b32 v[54:55], v225 offset0:22 offset1:23\nN 64,240,164\nds_read_b64 v[238:239], v246 offset:0\ns_add_u32 s63, s52, 983040\nds_read2st64_b32 v[56:57], v225 offset0:24 offset1:25\nN 68,228,160\n"
"R 178,186,61440\nds_read_b64 v[240:241], v246 offset:128\nds_read2st64_b32 v[58:59], v225 offset0:26 offset1:27\nN 68,244,164\nds_read_b64 v[242:243], v246 offset:256\nds_read2st64_b32 v[60:61], v225 offset0:28 offset1:29\n"
"G 15\nv_add_f32 v0, v0, v32\nN 96,232,160\nR 180,190,0\nds_read_b64 v[244:245], v246 offset:384\nds_read2st64_b32 v[62:63], v225 offset0:30 offset1:31\n"
"N 96,248,164\nds_read_b64 v[246:247], v246 offset:0\nv_add_f32 v1, v1, v33\nN 100,236,160\nR 182,190,4096\nbuffer_load_dwordx4 v220, s[4:7], s63 offen lds\n"
"N 100,252,164\nG 15\nv_add_f32 v2, v2, v34\nv_add_f32 v3, v3, v35\nN 108,236,168\nR 160,187,57344\n"
"v_add_f32 v4, v4, v36\nN 108,252,172\nbuffer_load_dwordx4 v221, s[4:7], s63 offen offset:1024 lds\nN 104,232,168\nR 162,187,61440\ns_add_u32 m0, s50, 0\n"
"s_add_u32 s63, s52, 1179648\nN 104,248,172\nbuffer_load_dwordx4 v220, s[4:7], s63 offen lds\nN 76,228,168\nR 164,191,0\nv_add_f32 v5, v5, v37\n"
"N 76,244,172\nv_add_f32 v6, v6, v38\nv_add_f32 v7, v7, v39\nG 11\nv_mfma_f32_16x16x32_bf16 v[64:67], v[208:211], v[238:241], v[64:67]\nv_add_f32 v8, v8, v40\n"
"v_add_f32 v9, v9, v41\nN 72,224,168\nR 166,191,4096\nv_add_f32 v10, v10, v42\nN 72,240,172\nG 4\n"
"v_add_f32 v11, v11, v43\nv_add_f32 v12, v12, v44\nv_mfma_f32_16x16x32_bf16 v[72:75], v[208:211], v[240:243], v[72:75]\nv_add_f32 v13, v13, v45\nv_add_f32 v14, v14, v46\nN 80,224,176\n"
"R 168,188,8192\nbuffer_load_dwordx4 v221, s[4:7], s63 offen offset:1024 lds\nN 80,240,180\nv_add_f32 v15, v15, v47\nv_mfma_f32_16x16x32_bf16 v[80:83], v[208:211], v[242:245], v[80:83]\ns_add_u32 m0, s50, 8192\n"
"s_add_u32 s63, s52, 1376256\nbuffer_load_dwordx4 v220, s[4:7], s63 offen lds\nN 84,228,176\nR 170,188,12288\nv_add_f32 v16, v16, v48\nN 84,244,180\n"
"v_add_f32 v17, v17, v49\nv_add_f32 v18, v18, v50\nv_mfma_f32_16x16x32_bf16 v[88:91], v[208:211], v[244:247], v[88:91]\nv_add_f32 v19, v19, v51\nv_add_f32 v20, v20, v52\nN 112,232,176\n"
"R 172,188,16384\nv_add_f32 v21, v21, v53\nN 112,248,180\nbuffer_load_dwordx4 v221, s[4:7], s63 offen offset:1024 lds\nv_mfma_f32_16x16x32_bf16 v[68:71], v[210:213], v[244:247], v[68:71]\nv_add_f32 v22, v22, v54\n"
"v_add_f32 v23, v23, v55\nv_add_f32 v24, v24, v56\nN 116,236,176\nR 174,188,20480\ns_add_u32 m0, s50, 32768\nN 116,252,180\n"
"G 4\nv_add_f32 v25, v25, v57\nv_add_f32 v26, v26, v58\nv_mfma_f32_16x16x32_bf16 v[76:79], v[210:213], v[238:241], v[76:79]\nv_add_f32 v27, v27, v59\nv_add_f32 v28, v28, v60\n"
"N 124,236,160\nR 176,189,8192\ns_add_u32 s63, s52, 1572864\nN 124,252,164\nbuffer_load_dwordx4 v220, s[4:7], s63 offen lds\nv_mfma_f32_16x16x32_bf16 v[84:87], v[210:213], v[240:243], v[84:87]\n"
"v_add_f32 v29, v29, v61\nv_add_f32 v30, v30, v62\nv_add_f32 v31, v31, v63\nN 120,232,160\nR 178,189,12288\ns_lshl4_add_u32 s64, s41, s42\n"
"v_lshrrev_b32 v227, 1, v252\nN 120,248,164\nbuffer_load_dwordx4 v221, s[4:7], s63 offen offset:1024 lds\nv_mfma_f32_16x16x32_bf16 v[92:95], v[210:213], v[242:245], v[92:95]\ns_cmp_eq_u32 s40, 3\ns_cselect_b64 s[66:67], -1, 0\n"
"s_lshl_b32 s64, s64, 12\nv_mul_f32 v16, v16, v217\nN 92,228,160\nR 180,189,16384\ns_add_u32 m0, s50, 40960\ns_add_u32 s63, s52, 1769472\n"
"v_mul_f32 v17, v17, v217\nN 92,244,164\nbuffer_load_dwordx4 v220, s[4:7], s63 offen lds\nv_mul_f32 v18, v18, v217\nN 88,224,160\nR 182,189,20480\n"
"s_add_u32 s64, s64, 77594624\nv_mul_f32 v19, v19, v217\nN 88,240,164\ns_waitcnt vmcnt(12)\ns_barrier\nG 4\n"
"v_mul_f32 v20, v20, v217\nv_mfma_f32_16x16x32_bf16 v[128:131], v[168:171], a[0:3], 0\nR 160,190,8192\nv_mul_f32 v21, v21, v217\nv_mul_f32 v22, v22, v217\nM 128,172,16\n"
"s_mov_b32 m0, s51\nv_mul_f32 v23, v23, v217\nv_mul_f32 v24, v24, v217\nv_mul_f32 v25, v25, v217\nv_mfma_f32_16x16x32_bf16 v[144:147], v[168:171], a[4:7], 0\nR 162,190,12288\n"
"s_lshl2_add_u32 s60, s45, s40\ns_waitcnt vmcnt(11)\nv_lshlrev_b32 v224, 3, v252\ns_mul_i32 s60, s60, 7168\ns_bfm_b64 exec, 14, 14\nbuffer_load_dword v224, v224, s[12:15], s60 offen\n"
"s_mov_b64 exec, -1\nv_mul_f32 v26, v26, v217\nM 144,172,20\nv_mul_f32 v27, v27, v217\nv_mul_f32 v28, v28, v217\nv_mul_f32 v29, v29, v217\n"
"v_mfma_f32_16x16x32_bf16 v[32:35], v[168:171], a[8:11], 0\nR 164,190,16384\nv_mul_f32 v30, v30, v217\nv_mul_f32 v31, v31, v217\nM 32,172,24\nv_cvt_pk_bf16_f32 v16, v16, v17\n"
"v_cvt_pk_bf16_f32 v17, v18, v19\nv_cvt_pk_bf16_f32 v18, v20, v21\nv_mfma_f32_16x16x32_bf16 v[48:51], v[168:171], a[12:15], 0\nR 166,190,20480\nds_write_addtid_b32 v96 offset:0\nv_cvt_pk_bf16_f32 v19, v22, v23\n"
"v_cvt_pk_bf16_f32 v20, v24, v25\nM 48,172,28\nds_write_addtid_b32 v97 offset:256\nG 6\nv_cvt_pk_bf16_f32 v21, v26, v27\nv_cvt_pk_bf16_f32 v22, v28, v29\n"
"v_cvt_pk_bf16_f32 v23, v30, v31\nv_mfma_f32_16x16x32_bf16 v[52:55], v[176:179], a[12:15], 0\nR 168,191,8192\nds_write_addtid_b32 v98 offset:512\ns_lshr_b32 s100, s41, 1\ns_mul_i32 s100, s100, 458752\n"
"s_add_u32 s100, s100, s55\nv_mul_f32 v0, v0, v214\nv_mul_f32 v1, v1, v214\nM 52,180,28\nds_write_addtid_b32 v99 offset:768\ns_mul_i32 s101, s41, 229376\n"
"s_add_u32 s101, s101, s54\nv_mul_f32 v2, v2, v214\nv_mul_f32 v3, v3, v214\nv_mul_f32 v4, v4, v214\nv_mfma_f32_16x16x32_bf16 v[36:39], v[176:179], a[8:11], 0\nR 170,191,12288\n"
"ds_write_addtid_b32 v100 offset:1024\nv_mul_f32 v5, v5, v214\nv_mul_f32 v6, v6, v214\nM 36,180,24\nds_write_addtid_b32 v101 offset:1280\nv_mul_f32 v7, v7, v214\n"
"v_mul_f32 v8, v8, v214\nv_mul_f32 v9, v9, v214\nv_mfma_f32_16x16x32_bf16 v[148:151], v[176:179], a[4:7], 0\nR 172,191,16384\nds_write_addtid_b32 v102 offset:1536\nv_mul_f32 v10, v10, v214\n"
"v_mul_f32 v11, v11, v214\nM 148,180,20\nds_write_addtid_b32 v103 offset:1792\nv_mul_f32 v12, v12, v214\nv_mul_f32 v13, v13, v214\nv_mul_f32 v14, v14, v214\n"
"v_mfma_f32_16x16x32_bf16 v[132:135], v[176:179], a[0:3], 0\nR 174,191,20480\nds_write_addtid_b32 v104 offset:2048\nv_mul_f32 v15, v15, v214\nv_cvt_pk_bf16_f32 v0, v0, v1\nM 132,180,16\n"
"ds_write_addtid_b32 v105 offset:2304\nG 14\nv_cvt_pk_bf16_f32 v1, v2, v3\nv_cvt_pk_bf16_f32 v2, v4, v5\nv_cvt_pk_bf16_f32 v3, v6, v7\nv_mfma_f32_16x16x32_bf16 v[136:139], v[160:163], a[0:3], 0\n"
"R 176,188,24576\nds_write_addtid_b32 v106 offset:2560\nv_cvt_pk_bf16_f32 v4, v8, v9\nv_cvt_pk_bf16_f32 v5, v10, v11\nM 136,164,16\nds_write_addtid_b32 v107 offset:2816\n"
"s_mov_b64 exec, s[66:67]\nbuffer_store_dwordx4 v[16:19], v252, s[20:23], s64 offen offset:0 sc1\ns_mov_b64 exec, -1\nv_cvt_pk_bf16_f32 v6, v12, v13\nv_cvt_pk_bf16_f32 v7, v14, v15\nv_mfma_f32_16x16x32_bf16 v[152:155], v[160:163], a[4:7], 0\n"
"R 178,188,28672\nds_write_addtid_b32 v108 offset:3072\nM 152,164,20\nds_write_addtid_b32 v109 offset:3328\nds_write_addtid_b32 v110 offset:3584\nv_mfma_f32_16x16x32_bf16 v[40:43], v[160:163], a[8:11], 0\n"
"R 180,188,32768\nds_write_addtid_b32 v111 offset:3840\nM 40,164,24\nds_write_addtid_b32 v112 offset:4096\nds_write_addtid_b32 v113 offset:4352\ns_mov_b64 exec, s[66:67]\n"
"buffer_store_dwordx4 v[20:23], v252, s[20:23], s64 offen offset:1024 sc1\ns_mov_b64 exec, -1\nv_mfma_f32_16x16x32_bf16 v[56:59], v[160:163], a[12:15], 0\nR 182,188,36864\nds_write_addtid_b32 v114 offset:4608\nM 56,164,28\n"
"ds_write_addtid_b32 v115 offset:4864\nds_write_addtid_b32 v116 offset:5120\nG 15\nv_mfma_f32_16x16x32_bf16 v[60:63], v[168:171], a[12:15], 0\nR 160,189,24576\nds_write_addtid_b32 v117 offset:5376\n"
"M 60,172,28\nds_write_addtid_b32 v118 offset:5632\nds_write_addtid_b32 v119 offset:5888\nv_mfma_f32_16x16x32_bf16 v[44:47], v[168:171], a[8:11], 0\nR 162,189,28672\nds_write_addtid_b32 v120 offset:6144\n"
"M 44,172,24\nds_write_addtid_b32 v121 offset:6400\nds_write_addtid_b32 v122 offset:6656\nds_write_addtid_b32 v123 offset:6912\nds_write_addtid_b32 v124 offset:7168\nds_write_addtid_b32 v125 offset:7424\n"
"ds_write_addtid_b32 v126 offset:7680\nds_write_addtid_b32 v127 offset:7936\nv_mfma_f32_16x16x32_bf16 v[156:159], v[168:171], a[4:7], 0\nR 164,189,32768\ns_add_u32 m0, s50, 40960\nM 156,172,20\n"
"buffer_load_dwordx4 v221, s[4:7], s63 offen offset:1024 lds\nv_mfma_f32_16x16x32_bf16 v[140:143], v[168:171], a[0:3], 0\nR 166,189,36864\ns_waitcnt vmcnt(11) lgkmcnt(2)\ns_barrier\nM 140,172,16\n"
"ds_read2st64_b32 v[96:97], v225 offset0:0 offset1:1\nds_read2st64_b32 v[98:99], v225 offset0:2 offset1:3\nv_lshrrev_b32 v226, 8, v252\nM 128,176,32\nR 168,190,24576\ns_add_u32 s63, s52, 1966080\n"
"ds_read2st64_b32 v[100:101], v225 offset0:4 offset1:5\ns_lshl_b32 s60, s40, 7\ns_add_u32 s60, s60, 161280\nv_lshl_add_u32 v226, v226, 4, s60\nM 128,180,48\ns_add_u32 m0, s50, 49152\n"
"ds_read2st64_b32 v[102:103], v225 offset0:6 offset1:7\nds_read2st64_b32 v[104:105], v225 offset0:8 offset1:9\nds_read_b128 v[230:233], v226 offset:0\nM 144,176,36\nR 170,190,28672\nds_read2st64_b32 v[106:107], v225 offset0:10 offset1:11\n"
"ds_read_b128 v[234:237], v226 offset:64\nM 144,180,52\nbuffer_load_dwordx4 v220, s[4:7], s63 offen lds\nds_read2st64_b32 v[108:109], v225 offset0:12 offset1:13\nM 32,176,40\nR 172,190,32768\n"
"ds_read2st64_b32 v[110:111], v225 offset0:14 offset1:15\nM 32,180,56\nds_read2st64_b32 v[112:113], v225 offset0:16 offset1:17\nds_read2st64_b32 v[114:115], v225 offset0:18 offset1:19\nG 14\nv_add_f32 v64, v64, v96\n"
"M 48,176,44\nR 174,190,36864\nds_read2st64_b32 v[116:117], v225 offset0:20 offset1:21\nv_add_f32 v65, v65, v97\nM 48,180,60\nds_read2st64_b32 v[118:119], v225 offset0:22 offset1:23\n"
"ds_read2st64_b32 v[120:121], v225 offset0:24 offset1:25\nM 52,160,44\nLpg4:\ns_cbranch_execz LretQ0\nR 176,191,24576\nds_read2st64_b32 v[122:123], v225 offset0:26 offset1:27\n"
"G 15\nv_add_f32 v66, v66, v98\nM 52,164,60\nbuffer_load_dwordx4 v221, s[4:7], s63 offen offset:1024 lds\nds_read2st64_b32 v[124:125], v225 offset0:28 offset1:29\nM 36,160,40\n"
"R 178,191,28672\nds_read2st64_b32 v[126:127], v225 offset0:30 offset1:31\nv_add_f32 v67, v67, v99\nM 36,164,56\ns_add_u32 m0, s50, 57344\nv_add_f32 v68, v68, v100\n"
"v_add_f32 v69, v69, v101\nv_add_f32 v70, v70, v102\nM 148,160,36\nR 180,191,32768\nv_add_f32 v71, v71, v103\nv_add_f32 v72, v72, v104\n"
"M 148,164,52\ns_add_u32 s63, s52, 2162688\nv_add_f32 v73, v73, v105\nG 15\nv_add_f32 v74, v74, v106\nv_add_f32 v75, v75, v107\n"
"M 132,160,32\nR 182,191,36864\nG 15\nv_add_f32 v76, v76, v108\nv_add_f32 v77, v77, v109\nM 132,164,48\n"
"G 10\nv_add_f32 v78, v78, v110\nv_add_f32 v79, v79, v111\nv_add_f32 v80, v80, v112\nM 136,168,32\nR 160,188,40960\n"
"v_add_f32 v81, v81, v113\nv_add_f32 v82, v82, v114\nM 136,172,48\nbuffer_load_dwordx4 v220, s[4:7], s63 offen lds\nv_add_f32 v83, v83, v115\nM 152,168,36\n"
"R 162,188,45056\nG 11\nv_add_f32 v84, v84, v116\nM 152,172,52\nv_add_f32 v85, v85, v117\nG 10\n"
"v_add_f32 v86, v86, v118\nM 40,168,40\nR 164,188,49152\nv_add_f32 v87, v87, v119\nM 40,172,56\nbuffer_load_dwordx4 v221, s[4:7], s63 offen offset:1024 lds\n"
"M 56,168,44\nR 166,188,53248\ns_add_u32 m0, s50, 65536\nM 56,172,60\nG 4\nv_add_f32 v88, v88, v120\n"
"v_add_f32 v89, v89, v121\nM 60,176,44\nR 168,189,40960\ns_add_u32 s63, s52, 2359296\nM 60,180,60\nbuffer_load_dwordx4 v220, s[4:7], s63 offen lds\n"
"M 44,176,40\nR 170,189,45056\nv_add_f32 v90, v90, v122\nM 44,180,56\nv_add_f32 v91, v91, v123\nv_add_f32 v92, v92, v124\n"
"M 156,176,36\nR 172,189,49152\nv_add_f32 v93, v93, v125\nM 156,180,52\nbuffer_load_dwordx4 v221, s[4:7], s63 offen offset:1024 lds\nM 140,176,32\n"
"R 174,189,53248\ns_add_u32 m0, s50, 73728\nM 140,180,48\nG 4\nv_add_f32 v94, v94, v126\nv_add_f32 v95, v95, v127\n"
"M 128,160,64\nR 176,190,40960\ns_add_u32 s63, s52, 2555904\nv_mul_f32 v64, v64, v230\nM 128,164,80\nbuffer_load_dwordx4 v220, s[4:7], s63 offen lds\n"
"v_mul_f32 v65, v65, v231\nM 144,160,68\nR 178,190,45056\nv_mul_f32 v66, v66, v232\nv_mul_f32 v67, v67, v233\nM 144,164,84\n"
"v_mul_f32 v68, v68, v234\nv_mul_f32 v69, v69, v235\nv_mul_f32 v70, v70, v236\nM 32,160,72\nR 180,190,49152\nv_mul_f32 v71, v71, v237\n"
"v_cvt_pk_bf16_f32 v67, v66, v67\nM 32,164,88\nbuffer_load_dwordx4 v221, s[4:7], s63 offen offset:1024 lds\nv_cvt_pk_bf16_f32 v66, v64, v65\nM 48,160,76\nR 182,190,53248\n"
"s_add_u32 s63, s52, 2752512\nv_cvt_pk_bf16_f32 v68, v68, v69\nM 48,164,92\nG 4\nv_cvt_pk_bf16_f32 v69, v70, v71\nv_mul_f32 v72, v72, v230\n"
"v_mul_f32 v73, v73, v231\nM 52,168,76\nR 160,191,40960\nv_mul_f32 v74, v74, v232\nv_mul_f32 v75, v75, v233\nM 52,172,92\n"
"s_add_u32 m0, s50, 81920\nv_mul_f32 v76, v76, v234\nv_mul_f32 v77, v77, v235\nv_mul_f32 v78, v78, v236\nM 36,168,72\nR 162,191,45056\n"
"v_mul_f32 v79, v79, v237\nv_cvt_pk_bf16_f32 v72, v72, v73\nM 36,172,88\nbuffer_load_dwordx4 v220, s[4:7], s63 offen lds\nM 148,168,68\nR 164,191,49152\n"
"v_cvt_pk_bf16_f32 v73, v74, v75\nM 148,172,84\nv_cvt_pk_bf16_f32 v74, v76, v77\nv_cvt_pk_bf16_f32 v75, v78, v79\nM 132,168,64\nR 166,191,53248\n"
"s_waitcnt vmcnt(9)\ns_barrier\nM 132,172,80\nG 4\nv_mul_f32 v80, v80, v230\nv_mul_f32 v81, v81, v231\n"
"M 136,176,64\nR 168,184,0\ns_lshl2_add_u32 s60, s45, s40\nv_lshlrev_b32 v224, 3, v252\ns_mul_i32 s60, s60, 7168\ns_bfm_b64 exec, 14, 28\n"
"buffer_load_dword v224, v224, s[12:15], s60 offen\ns_mov_b64 exec, -1\nM 136,180,80\nv_mul_f32 v82, v82, v232\nv_mul_f32 v83, v83, v233\nM 152,176,68\n"
"R 170,184,4096\nv_mul_f32 v84, v84, v234\nM 152,180,84\nbuffer_load_dwordx4 v221, s[4:7], s63 offen offset:1024 lds\nM 40,176,72\nR 172,184,8192\n"
"s_mov_b32 s60, 155648\nv_bfe_u32 v246, v252, 4, 4\nv_lshl_add_u32 v246, v246, 3, s60\nM 40,180,88\ns_add_u32 m0, s50, 90112\nv_mul_f32 v85, v85, v235\n"
"v_mul_f32 v86, v86, v236\nM 56,176,76\nR 174,184,12288\nds_read_b64 v[238:239], v246 offset:0\nM 56,180,92\nG 5\n"
"v_mul_f32 v87, v87, v237\nv_cvt_pk_bf16_f32 v78, v80, v81\nM 60,160,76\nR 176,185,0\nds_read_b64 v[240:241], v246 offset:128\nM 60,164,92\n"
"s_add_u32 s63, s52, 2949120\nv_cvt_pk_bf16_f32 v79, v82, v83\nv_cvt_pk_bf16_f32 v80, v84, v85\nM 44,160,72\nR 178,185,4096\nds_read_b64 v[242:243], v246 offset:256\n"
"M 44,164,88\nbuffer_load_dwordx4 v220, s[4:7], s63 offen lds\nM 156,160,68\nR 180,185,8192\nds_read_b64 v[244:245], v246 offset:384\nM 156,164,84\n"
"v_cvt_pk_bf16_f32 v81, v86, v87\nv_mul_f32 v88, v88, v230\nM 140,160,64\nR 182,185,12288\nds_read_b64 v[246:247], v246 offset:0\nM 140,164,80\n"
"G 9\nv_mul_f32 v89, v89, v231\nv_mul_f32 v90, v90, v232\nM 128,168,96\nR 160,186,0\nv_mul_f32 v91, v91, v233\n"
"M 128,172,112\nv_mul_f32 v92, v92, v234\nv_mul_f32 v93, v93, v235\nM 144,168,100\nR 162,186,4096\nv_mul_f32 v94, v94, v236\n"
"M 144,172,116\nbuffer_load_dwordx4 v221, s[4:7], s63 offen offset:1024 lds\nM 32,168,104\nR 164,186,8192\nv_mul_f32 v95, v95, v237\nG 9\n"
"v_mfma_f32_16x16x32_bf16 v[128:131], v[238:241], v[208:211], v[128:131]\ns_lshl_b32 s60, s40, 12\ns_add_u32 s60, s60, 53248\nv_add_u32 v230, s60, v252\nv_add_u32 v231, s60, v227\nM 32,172,120\n"
"v_cvt_pk_bf16_f32 v84, v88, v89\nv_cvt_pk_bf16_f32 v85, v90, v91\nM 48,168,108\nR 166,186,12288\nv_cvt_pk_bf16_f32 v86, v92, v93\nG 8\n"
"v_mfma_f32_16x16x32_bf16 v[132:135], v[240:243], v[208:211], v[132:135]\ns_mov_b64 exec, s[66:67]\nbuffer_store_dwordx2 v[68:69], v227, s[20:23], s64 offen offset:2048 sc1\ns_mov_b64 exec, -1\nv_cvt_pk_bf16_f32 v87, v94, v95\nv_add_u32 v108, 161792, v252\n"
"M 48,172,124\nds_read_b128 v[112:115], v108\nG 6\nM 52,176,108\nR 168,187,0\nG 6\n"
"v_mfma_f32_16x16x32_bf16 v[144:147], v[244:247], v[210:213], v[144:147]\nds_read_b128 v[104:107], v108 offset:1024\nM 52,180,124\ns_movk_i32 s63, 7168\nv_mad_u32_u24 v220, v228, s63, v229\nv_add_u32 v221, 3668992, v220\n"
"s_add_u32 m0, s50, 98304\ns_add_u32 s63, s101, 0\nbuffer_load_dwordx4 v220, s[12:15], s63 offen lds\nM 36,176,104\nR 170,187,4096\nv_mfma_f32_16x16x32_bf16 v[148:151], v[238:241], v[210:213], v[148:151]\n"
"M 36,180,120\nM 148,176,100\nR 172,187,8192\nv_mfma_f32_16x16x32_bf16 v[136:139], v[242:245], v[208:211], v[136:139]\ns_mov_b64 exec, s[66:67]\nbuffer_store_dwordx2 v[74:75], v227, s[20:23], s64 offen offset:2560 sc1\n"
"s_mov_b64 exec, -1\nM 148,180,116\nM 132,176,96\nR 174,187,12288\nG 3\nv_lshlrev_b32 v100, 16, v104\n"
"v_mfma_f32_16x16x32_bf16 v[140:143], v[244:247], v[208:211], v[140:143]\nv_and_b32 v101, s77, v104\nv_lshlrev_b32 v102, 16, v105\nv_and_b32 v103, s77, v105\nM 132,180,112\nv_lshlrev_b32 v96, 16, v106\n"
"v_and_b32 v97, s77, v106\nM 136,160,96\nR 176,184,32768\nv_lshlrev_b32 v98, 16, v107\nv_mfma_f32_16x16x32_bf16 v[152:155], v[240:243], v[210:213], v[152:155]\nv_and_b32 v99, s77, v107\n"
"v_add_f32 v104, s78, v100\nv_add_f32 v105, s78, v101\nM 136,164,112\nbuffer_load_dwordx4 v221, s[12:15], s63 offen offset:1024 lds\nM 152,160,100\nR 178,184,36864\n"
"v_add_f32 v106, s78, v102\nv_mfma_f32_16x16x32_bf16 v[156:159], v[242:245], v[210:213], v[156:159]\nv_add_f32 v107, s78, v103\nM 152,164,116\ns_mov_b64 exec, s[66:67]\nbuffer_store_dwordx2 v[80:81], v227, s[20:23], s64 offen offset:3072 sc1\n"
"s_mov_b64 exec, -1\nM 40,160,104\nR 180,184,40960\nM 40,164,120\nM 56,160,108\nR 182,184,45056\n"
"M 56,164,124\ns_mov_b64 exec, s[66:67]\nbuffer_store_dwordx2 v[86:87], v227, s[20:23], s64 offen offset:3584 sc1\ns_mov_b64 exec, -1\nG 4\nM 60,168,108\n"
"R 160,185,32768\ns_not_b64 exec, s[66:67]\nds_write_b128 v230, v[16:19] offset:0\nds_write_b128 v230, v[20:23] offset:1024\ns_mov_b64 exec, -1\nM 60,172,124\n"
"s_not_b64 exec, s[66:67]\nds_write_b64 v231, v[68:69] offset:2048\nds_write_b64 v231, v[74:75] offset:2560\ns_mov_b64 exec, -1\nM 44,168,104\nR 162,185,36864\n"
"s_not_b64 exec, s[66:67]\nds_write_b64 v231, v[80:81] offset:3072\nds_write_b64 v231, v[86:87] offset:3584\ns_mov_b64 exec, -1\nM 44,172,120\ns_add_u32 m0, s50, 106496\n"
"M 156,168,100\nR 164,185,40960\nM 156,172,116\ns_add_u32 s63, s101, 128\nM 140,168,96\nR 166,185,45056\n"
"M 140,172,112\nG 10\nM 128,176,128\nR 168,186,32768\ns_waitcnt vmcnt(13) lgkmcnt(3)\ns_barrier\n"
"M 128,180,144\nbuffer_load_dwordx4 v220, s[12:15], s63 offen lds\nM 144,176,132\nR 170,186,36864\nM 144,180,148\ns_cmp_eq_u32 s78, 0\n"
"s_cbranch_scc1 Lq151\nv_mov_b32 v230, 0\nv_mov_b32 v231, 0\nv_mov_b32 v232, 0\nv_mov_b32 v233, 0\nv_mov_b32 v64, 0\n"
"v_mov_b32 v65, 0\nv_mov_b32 v70, 0\nv_mov_b32 v71, 0\nv_mov_b32 v234, 0\nv_mov_b32 v235, 0\nv_mov_b32 v236, 0\n"
"v_mov_b32 v237, 0\nv_mov_b32 v76, 0\nv_mov_b32 v77, 0\nv_mov_b32 v82, 0\nv_mov_b32 v83, 0\nLq151:\n"
"s_lshl4_add_u32 s65, s41, s42\nM 32,176,136\nR 172,186,40960\ns_lshl_b32 s65, s65, 12\nM 32,180,152\ns_add_u32 s65, s65, 77529088\n"
"M 48,176,140\nR 174,186,45056\nM 48,180,156\nG 4\nM 52,160,140\nR 176,187,32768\n"
"M 52,164,156\nbuffer_load_dwordx4 v221, s[12:15], s63 offen offset:1024 lds\nM 36,160,136\nR 178,187,36864\nM 36,164,152\ns_add_u32 m0, s50, 114688\n"
"M 148,160,132\nR 180,187,40960\nM 148,164,148\ns_add_u32 s63, s101, 256\nM 132,160,128\nR 182,187,45056\n"
"M 132,164,144\nG 4\nM 136,168,128\nR 160,184,49152\nM 136,172,144\nbuffer_load_dwordx4 v220, s[12:15], s63 offen lds\n"
"M 152,168,132\nR 162,184,53248\nM 152,172,148\nM 40,168,136\nR 164,184,57344\ns_lshl2_add_u32 s60, s45, s40\n"
"s_waitcnt vmcnt(12)\nv_lshlrev_b32 v224, 3, v252\ns_mul_i32 s60, s60, 7168\ns_bfm_b64 exec, 14, 42\nbuffer_load_dword v224, v224, s[12:15], s60 offen\ns_mov_b64 exec, -1\n"
"M 40,172,152\nM 56,168,140\nR 166,184,61440\nM 56,172,156\nG 4\nM 60,176,140\n"
"R 168,185,49152\nM 60,180,156\nbuffer_load_dwordx4 v221, s[12:15], s63 offen offset:1024 lds\nM 44,176,136\nR 170,185,53248\nM 44,180,152\n"
"s_add_u32 m0, s50, 0\nM 156,176,132\nR 172,185,57344\nM 156,180,148\ns_add_u32 s63, s100, 0\nM 140,176,128\n"
"R 174,185,61440\nM 140,180,144\nG 4\nM 128,160,160\nR 176,186,49152\nM 128,164,176\n"
"buffer_load_dwordx4 v222, s[12:15], s63 offen lds\nM 144,160,164\nR 178,186,53248\nM 144,164,180\nM 32,160,168\nR 180,186,57344\n"
"M 32,164,184\nM 48,160,172\nR 182,186,61440\nM 48,164,188\nG 4\nM 52,168,172\n"
"R 160,187,49152\nM 52,172,188\nbuffer_load_dwordx4 v223, s[12:15], s63 offen offset:1024 lds\nM 36,168,168\nR 162,187,53248\nM 36,172,184\n"
"M 148,168,164\nR 164,187,57344\ns_lshl_b32 s60, s40, 12\ns_add_u32 s60, s60, 49152\nv_add_u32 v226, s60, v252\nv_add_u32 v111, s60, v227\n"
"M 148,172,180\nM 132,168,160\nR 166,187,61440\ns_cmp_eq_u32 s40, 0\ns_cselect_b64 s[86:87], 0, -1\ns_cselect_b32 s60, s41, 0\n"
"s_cmp_lg_u32 s60, 0\ns_cselect_b64 s[66:67], -1, 0\nM 132,172,176\nG 4\nM 136,176,160\nR 168,188,0\n"
"M 136,180,176\ns_mov_b64 exec, s[86:87]\nds_read_b128 v[230:233], v226 offset:0\nds_read_b64 v[64:65], v111 offset:2048\ns_mov_b64 exec, -1\nM 152,176,164\n"
"R 170,188,4096\ns_waitcnt vmcnt(13) lgkmcnt(1)\ns_barrier\nM 152,180,180\ns_movk_i32 s63, 2048\nv_mad_u32_u24 v220, v228, s63, v229\n"
"v_add_u32 v221, 1047552, v220\ns_add_u32 m0, s50, 8192\ns_add_u32 s63, s53, 0\nbuffer_load_dwordx4 v220, s[8:11], s63 offen lds\nM 40,176,168\nR 172,188,8192\n"
"M 40,180,184\ns_mov_b64 exec, s[86:87]\nds_read_b64 v[70:71], v111 offset:2560\nds_read_b128 v[234:237], v226 offset:1024\ns_mov_b64 exec, -1\nM 56,176,172\n"
"R 174,188,12288\nM 56,180,188\nM 60,160,172\nR 176,189,0\nM 60,164,188\ns_mov_b64 exec, s[86:87]\n"
"ds_read_b64 v[76:77], v111 offset:3072\nds_read_b64 v[82:83], v111 offset:3584\ns_mov_b64 exec, -1\nM 44,160,168\nR 178,189,4096\nM 44,164,184\n"
"buffer_load_dwordx4 v221, s[8:11], s63 offen offset:1024 lds\nM 156,160,164\nR 180,189,8192\nM 156,164,180\ns_mov_b64 exec, s[66:67]\nbuffer_load_dwordx4 v[230:233], v252, s[20:23], s65 offen offset:0 sc1\n"
"s_mov_b64 exec, -1\nM 140,160,160\nR 182,189,12288\nM 140,164,176\nG 6\nM 32,168,200\n"
"R 160,190,0\ns_mov_b64 exec, s[66:67]\nbuffer_load_dwordx2 v[64:65], v227, s[20:23], s65 offen offset:2048 sc1\ns_mov_b64 exec, -1\nM 32,172,216\nR 162,190,4096\n"
"M 48,168,204\nR 164,190,8192\nM 48,172,220\nR 166,190,12288\nG 4\nM 52,176,204\n"
"R 168,191,0\ns_mov_b64 exec, s[66:67]\nbuffer_load_dwordx2 v[70:71], v227, s[20:23], s65 offen offset:2560 sc1\ns_mov_b64 exec, -1\nM 52,180,220\nR 170,191,4096\n"
"M 36,176,200\nR 172,191,8192\nM 36,180,216\nR 174,191,12288\nG 4\nM 40,160,200\n"
"R 176,188,16384\ns_mov_b64 exec, s[66:67]\nbuffer_load_dwordx4 v[234:237], v252, s[20:23], s65 offen offset:1024 sc1\ns_mov_b64 exec, -1\nM 40,164,216\nR 178,188,20480\n"
"M 56,160,204\nR 180,188,24576\nM 56,164,220\nR 182,188,28672\nG 4\nM 60,168,204\n"
"R 160,189,16384\ns_mov_b64 exec, s[66:67]\nbuffer_load_dwordx2 v[76:77], v227, s[20:23], s65 offen offset:3072 sc1\ns_mov_b64 exec, -1\nM 60,172,220\nR 162,189,20480\n"
"M 44,168,200\nR 164,189,24576\nM 44,172,216\nR 166,189,28672\nG 4\nM 32,176,232\n"
"R 168,190,16384\ns_mov_b64 exec, s[66:67]\nbuffer_load_dwordx2 v[82:83], v227, s[20:23], s65 offen offset:3584 sc1\ns_mov_b64 exec, -1\nLq185:\ns_lshr_b32 s60, s45, 3\n"
"s_lshl2_add_u32 s60, s60, s40\ns_lshl_b32 s60, s60, 8\ns_waitcnt vmcnt(11)\nv_lshl_add_u32 v224, v252, 3, s60\nv_add_u32 v224, Lq185+4096-LqPC, v224\nv_min_u32 v224, LqE-LqPC-4, v224\n"
"s_mov_b64 exec, 3\nglobal_load_dword v224, v224, s[98:99]\ns_mov_b64 exec, -1\nM 32,180,248\nR 170,190,20480\nM 48,176,236\n"
"R 172,190,24576\nM 48,180,252\nR 174,190,28672\nG 4\nM 52,160,236\nR 176,191,16384\n"
"M 52,164,252\nR 178,191,20480\nM 36,160,232\nR 180,191,24576\nM 36,164,248\nR 182,191,28672\n"
"G 4\nM 40,168,232\nR 160,188,0\nM 40,172,248\nR 162,188,4096\nM 56,168,236\n"
"R 164,188,8192\nM 56,172,252\nR 166,188,12288\nG 4\nM 60,176,236\nR 168,189,0\n"
"M 60,180,252\nR 170,189,4096\nM 44,176,232\nR 172,189,8192\nM 44,180,248\nR 174,189,12288\n"
"G 4\nM 128,160,192\nR 176,190,0\nM 128,164,208\nR 178,190,4096\ns_mov_b32 m0, s51\n"
"M 144,160,196\nR 180,190,8192\nds_write_addtid_b32 v32 offset:0\nM 144,164,212\nR 182,190,12288\nLpg5:\n"
"s_cbranch_execz LretQ0\nds_write_addtid_b32 v33 offset:256\nG 6\nM 148,168,196\nR 160,191,0\nds_write_addtid_b32 v34 offset:512\n"
"M 148,172,212\nR 162,191,4096\nds_write_addtid_b32 v35 offset:768\nM 132,168,192\nR 164,191,8192\nds_write_addtid_b32 v36 offset:1024\n"
"M 132,172,208\nR 166,191,12288\nds_write_addtid_b32 v37 offset:1280\nG 9\nM 136,176,192\nR 168,188,16384\n"
"ds_write_addtid_b32 v38 offset:1536\nM 136,180,208\nR 170,188,20480\nds_write_addtid_b32 v39 offset:1792\nM 152,176,196\nR 172,188,24576\n"
"ds_write_addtid_b32 v40 offset:2048\nM 152,180,212\nR 174,188,28672\nds_write_addtid_b32 v41 offset:2304\nG 9\nM 156,160,196\n"
"R 176,189,16384\nds_write_addtid_b32 v42 offset:2560\nM 156,164,212\nR 178,189,20480\nds_write_addtid_b32 v43 offset:2816\nM 140,160,192\n"
"R 180,189,24576\nds_write_addtid_b32 v44 offset:3072\nds_write_addtid_b32 v45 offset:3328\nds_write_addtid_b32 v46 offset:3584\nds_write_addtid_b32 v47 offset:3840\nds_write_addtid_b32 v48 offset:4096\n"
"ds_write_addtid_b32 v49 offset:4352\nds_write_addtid_b32 v50 offset:4608\nds_write_addtid_b32 v51 offset:4864\nds_write_addtid_b32 v52 offset:5120\nds_write_addtid_b32 v53 offset:5376\nds_write_addtid_b32 v54 offset:5632\n"
"ds_write_addtid_b32 v55 offset:5888\nds_write_addtid_b32 v56 offset:6144\nds_write_addtid_b32 v57 offset:6400\nds_write_addtid_b32 v58 offset:6656\nds_write_addtid_b32 v59 offset:6912\nds_write_addtid_b32 v60 offset:7168\n"
"ds_write_addtid_b32 v61 offset:7424\nds_write_addtid_b32 v62 offset:7680\nds_write_addtid_b32 v63 offset:7936\nM 140,164,208\nR 182,189,28672\nG 15\n"
"M 128,168,224\nR 160,190,16384\ns_add_u32 m0, s50, 16384\nM 128,172,240\nR 162,190,20480\nG 3\n"
"s_barrier\ns_add_u32 s63, s53, 65536\nM 144,168,228\nR 164,190,24576\nbuffer_load_dwordx4 v220, s[8:11], s63 offen lds\nds_read2st64_b32 v[32:33], v225 offset0:0 offset1:1\n"
"M 144,172,244\nR 166,190,28672\nbuffer_load_dwordx4 v221, s[8:11], s63 offen offset:1024 lds\nds_read2st64_b32 v[34:35], v225 offset0:2 offset1:3\nG 6\nM 148,176,228\n"
"R 168,191,16384\ns_add_u32 m0, s50, 24576\ns_add_u32 s63, s53, 131072\nbuffer_load_dwordx4 v220, s[8:11], s63 offen lds\nds_read2st64_b32 v[36:37], v225 offset0:4 offset1:5\nM 148,180,244\n"
"R 170,191,20480\nbuffer_load_dwordx4 v221, s[8:11], s63 offen offset:1024 lds\nds_read2st64_b32 v[38:39], v225 offset0:6 offset1:7\nM 132,176,224\nR 172,191,24576\ns_add_u32 m0, s50, 32768\n"
"s_add_u32 s63, s53, 196608\nbuffer_load_dwordx4 v220, s[8:11], s63 offen lds\nds_read2st64_b32 v[40:41], v225 offset0:8 offset1:9\nM 132,180,240\nR 174,191,28672\nbuffer_load_dwordx4 v221, s[8:11], s63 offen offset:1024 lds\n"
"ds_read2st64_b32 v[42:43], v225 offset0:10 offset1:11\nG 9\nM 136,160,224\ns_add_u32 m0, s50, 40960\ns_add_u32 s63, s53, 262144\nbuffer_load_dwordx4 v220, s[8:11], s63 offen lds\n"
"ds_read2st64_b32 v[44:45], v225 offset0:12 offset1:13\nM 136,164,240\nbuffer_load_dwordx4 v221, s[8:11], s63 offen offset:1024 lds\nds_read2st64_b32 v[46:47], v225 offset0:14 offset1:15\nM 152,160,228\ns_add_u32 m0, s50, 49152\n"
"s_add_u32 s63, s53, 327680\nbuffer_load_dwordx4 v220, s[8:11], s63 offen lds\nds_read2st64_b32 v[48:49], v225 offset0:16 offset1:17\nM 152,164,244\nds_read2st64_b32 v[50:51], v225 offset0:18 offset1:19\nds_read2st64_b32 v[52:53], v225 offset0:20 offset1:21\n"
"G 6\nM 156,168,228\nds_read2st64_b32 v[54:55], v225 offset0:22 offset1:23\nds_read2st64_b32 v[56:57], v225 offset0:24 offset1:25\nM 156,172,244\nds_read2st64_b32 v[58:59], v225 offset0:26 offset1:27\n"
"ds_read2st64_b32 v[60:61], v225 offset0:28 offset1:29\nM 140,168,224\nds_read2st64_b32 v[62:63], v225 offset0:30 offset1:31\nM 140,172,240\nG 7\nv_pk_add_f32 v[144:145], v[144:145], v[48:49]\n"
"G 6\nv_pk_add_f32 v[146:147], v[146:147], v[50:51]\nG 5\nv_pk_add_f32 v[148:149], v[148:149], v[52:53]\nG 4\nv_pk_add_f32 v[150:151], v[150:151], v[54:55]\n"
"G 3\nv_pk_add_f32 v[152:153], v[152:153], v[56:57]\nG 2\nv_pk_add_f32 v[154:155], v[154:155], v[58:59]\nG 1\nv_pk_add_f32 v[156:157], v[156:157], v[60:61]\n"
"G 0\nv_pk_add_f32 v[158:159], v[158:159], v[62:63]\nv_pk_add_f32 v[128:129], v[128:129], v[32:33]\nv_pk_add_f32 v[130:131], v[130:131], v[34:35]\nv_pk_add_f32 v[132:133], v[132:133], v[36:37]\nv_pk_add_f32 v[134:135], v[134:135], v[38:39]\n"
"v_pk_add_f32 v[136:137], v[136:137], v[40:41]\nv_pk_add_f32 v[138:139], v[138:139], v[42:43]\nv_pk_add_f32 v[140:141], v[140:141], v[44:45]\nv_pk_add_f32 v[142:143], v[142:143], v[46:47]\nv_cvt_pk_bf16_f32 v144, v144, v145\nv_cvt_pk_bf16_f32 v145, v146, v147\n"
"v_cvt_pk_bf16_f32 v146, v148, v149\nv_cvt_pk_bf16_f32 v147, v150, v151\nv_cvt_pk_bf16_f32 v152, v152, v153\nv_cvt_pk_bf16_f32 v153, v154, v155\nv_cvt_pk_bf16_f32 v154, v156, v157\nv_cvt_pk_bf16_f32 v155, v158, v159\n"
"v_cvt_pk_bf16_f32 v128, v128, v129\nv_cvt_pk_bf16_f32 v129, v130, v131\nv_cvt_pk_bf16_f32 v130, v132, v133\nv_cvt_pk_bf16_f32 v131, v134, v135\nv_cvt_pk_bf16_f32 v136, v136, v137\nv_cvt_pk_bf16_f32 v137, v138, v139\n"
"v_cvt_pk_bf16_f32 v138, v140, v141\nv_cvt_pk_bf16_f32 v139, v142, v143\nv_mfma_f32_16x16x32_bf16 v[52:55], v[16:19], v[144:147], v[96:99]\nv_mfma_f32_16x16x32_bf16 v[48:51], v[0:3], v[144:147], v[100:103]\nv_mfma_f32_16x16x32_bf16 v[60:63], v[20:23], v[152:155], v[96:99]\nv_mfma_f32_16x16x32_bf16 v[56:59], v[4:7], v[152:155], v[100:103]\n"
"s_waitcnt vmcnt(13)\nv_max3_u32 v248, v233, v65, v71\nv_cmp_eq_u32 vcc, -1, v248\ns_nop 0\ns_cbranch_vccnz Lq187\nLq188:\n"
"s_waitcnt vmcnt(10)\nv_max3_u32 v248, v237, v77, v83\nv_cmp_eq_u32 vcc, -1, v248\ns_nop 0\ns_cbranch_vccnz Lq189\nLq190:\n"
"v_mfma_f32_16x16x32_bf16 v[36:39], v[0:3], v[128:131], v[96:99]\nv_mfma_f32_16x16x32_bf16 v[32:35], v[230:233], v[128:131], v[104:107]\nv_mfma_f32_16x16x32_bf16 v[44:47], v[4:7], v[136:139], v[96:99]\nv_mfma_f32_16x16x32_bf16 v[40:43], v[234:237], v[136:139], v[104:107]\ns_lshl_b32 s84, s42, 11\ns_add_u32 s84, s84, s49\n"
"s_add_u32 s84, s84, 2097152\ns_add_u32 s85, s84, 32768\ns_lshl2_add_u32 s60, s45, s40\ns_waitcnt vmcnt(9)\nv_lshlrev_b32 v224, 3, v252\ns_mul_i32 s60, s60, 7168\n"
"s_bfm_b64 exec, 28, 28\nbuffer_load_dword v224, v224, s[16:19], s60 offen\ns_mov_b64 exec, -1\nv_mov_b32 v238, s46\nv_mov_b32 v239, s47\nv_max_f32 v52, v52, v53\n"
"v_max_f32 v54, v54, v55\nv_max_f32 v60, v60, v61\nv_max_f32 v62, v62, v63\nv_max3_f32 v52, v52, v48, v49\nv_max3_f32 v54, v54, v50, v51\nv_max3_f32 v60, v60, v56, v57\n"
"v_max3_f32 v62, v62, v58, v59\nv_max_f32 v36, v36, v37\nv_max_f32 v38, v38, v39\nv_max_f32 v44, v44, v45\nv_max_f32 v46, v46, v47\nv_max3_f32 v36, v36, v32, v33\n"
"v_max3_f32 v38, v38, v34, v35\nv_max3_f32 v44, v44, v40, v41\nv_max3_f32 v46, v46, v42, v43\nv_max_f32 v118, v52, v54\nv_max_f32 v119, v60, v62\nv_max_f32 v116, v36, v38\n"
"v_max_f32 v117, v44, v46\ns_nop 0\nv_permlane32_swap_b32 v116, v118\nv_permlane32_swap_b32 v117, v119\nv_max_f32 v116, v116, v118\nv_max_f32 v117, v117, v119\n"
"s_nop 1\nv_permlane16_swap_b32 v116, v117\nv_max_f32 v116, v116, v117\nv_mov_b32 v117, v116\ns_nop 1\nv_permlane16_swap_b32 v116, v117\n"
"v_mov_b32 v118, v116\nv_mov_b32 v119, v117\ns_nop 0\nv_permlane32_swap_b32 v116, v118\nv_permlane32_swap_b32 v117, v119\nv_mul_f32 v116, v116, v215\n"
"v_mul_f32 v118, v118, v218\nv_mul_f32 v117, v117, v215\nv_mul_f32 v119, v119, v218\nv_fma_f32 v52, v52, v218, -v118\nv_fma_f32 v54, v54, v218, -v118\nv_fma_f32 v60, v60, v218, -v119\n"
"v_fma_f32 v62, v62, v218, -v119\nv_fma_f32 v36, v36, v215, -v116\nv_fma_f32 v38, v38, v215, -v116\nv_fma_f32 v44, v44, v215, -v117\nv_fma_f32 v46, v46, v215, -v117\nv_exp_f32 v52, v52\n"
"v_exp_f32 v54, v54\nv_exp_f32 v60, v60\nv_exp_f32 v62, v62\nv_exp_f32 v36, v36\nv_exp_f32 v38, v38\nv_exp_f32 v44, v44\n"
"v_exp_f32 v46, v46\nv_add_f32 v118, v52, v54\nv_add_f32 v119, v60, v62\nv_add_f32 v116, v36, v38\nv_add_f32 v117, v44, v46\ns_nop 0\n"
"v_permlane32_swap_b32 v116, v118\nv_permlane32_swap_b32 v117, v119\nv_add_f32 v116, v116, v118\nv_add_f32 v117, v117, v119\ns_nop 1\nv_permlane16_swap_b32 v116, v117\n"
"v_add_f32 v116, v116, v117\ns_barrier\nv_rcp_f32 v116, v116\ns_nop 0\nv_mov_b32 v117, v116\ns_nop 1\n"
"v_permlane16_swap_b32 v116, v117\nv_mov_b32 v118, v116\nv_mov_b32 v119, v117\ns_nop 0\nv_permlane32_swap_b32 v116, v118\nv_permlane32_swap_b32 v117, v119\n"
"v_mul_f32 v52, v52, v118\nv_mul_f32 v54, v54, v118\nv_mul_f32 v60, v60, v119\nv_mul_f32 v62, v62, v119\nv_mul_f32 v36, v36, v116\nv_mul_f32 v38, v38, v116\n"
"v_mul_f32 v44, v44, v117\nv_mul_f32 v46, v46, v117\nv_cvt_pk_bf16_f32 v52, v52, v52\nv_cvt_pk_bf16_f32 v54, v54, v54\nv_cvt_pk_bf16_f32 v60, v60, v60\nv_cvt_pk_bf16_f32 v62, v62, v62\n"
"v_cvt_pk_bf16_f32 v36, v36, v36\nv_cvt_pk_bf16_f32 v38, v38, v38\nv_cvt_pk_bf16_f32 v44, v44, v44\nv_cvt_pk_bf16_f32 v46, v46, v46\nv_and_b32 v48, v52, v112\nv_and_b32 v49, v54, v113\n"
"v_and_b32 v50, v52, v114\nv_and_b32 v51, v54, v115\nv_and_b32 v56, v60, v112\nv_and_b32 v57, v62, v113\nv_and_b32 v58, v60, v114\nv_and_b32 v59, v62, v115\n"
"v_mfma_f32_16x16x32_bf16 v[144:147], v[66:69], v[48:51], 0\nv_mfma_f32_16x16x32_bf16 v[148:151], v[72:75], v[48:51], 0\nv_and_b32 v32, v36, v112\nv_and_b32 v33, v38, v113\nv_and_b32 v34, v36, v114\nv_and_b32 v35, v38, v115\n"
"v_mfma_f32_16x16x32_bf16 v[152:155], v[78:81], v[56:59], 0\nv_mfma_f32_16x16x32_bf16 v[156:159], v[84:87], v[56:59], 0\nv_and_b32 v40, v44, v112\nv_and_b32 v41, v46, v113\nv_and_b32 v42, v44, v114\nv_and_b32 v43, v46, v115\n"
"v_mfma_f32_16x16x32_bf16 v[128:131], v[64:67], v[32:35], 0\nv_mfma_f32_16x16x32_bf16 v[132:135], v[70:73], v[32:35], 0\nv_mfma_f32_16x16x32_bf16 v[136:139], v[76:79], v[40:43], 0\nv_mfma_f32_16x16x32_bf16 v[140:143], v[82:85], v[40:43], 0\nv_cvt_pk_bf16_f32 v144, v144, v145\nv_cvt_pk_bf16_f32 v145, v146, v147\n"
"v_cvt_pk_bf16_f32 v146, v148, v149\nv_cvt_pk_bf16_f32 v147, v150, v151\nbuffer_store_dwordx4 v[144:147], v252, s[20:23], s85 offen offset:0 sc1\nv_cvt_pk_bf16_f32 v152, v152, v153\nv_cvt_pk_bf16_f32 v153, v154, v155\nv_cvt_pk_bf16_f32 v154, v156, v157\n"
"v_cvt_pk_bf16_f32 v155, v158, v159\nbuffer_store_dwordx4 v[152:155], v252, s[20:23], s85 offen offset:1024 sc1\nv_cvt_pk_bf16_f32 v128, v128, v129\nv_cvt_pk_bf16_f32 v129, v130, v131\nv_cvt_pk_bf16_f32 v130, v132, v133\nv_cvt_pk_bf16_f32 v131, v134, v135\n"
"buffer_store_dwordx4 v[128:131], v252, s[20:23], s84 offen offset:0 sc1\nv_cvt_pk_bf16_f32 v136, v136, v137\nv_cvt_pk_bf16_f32 v137, v138, v139\nv_cvt_pk_bf16_f32 v138, v140, v141\nv_cvt_pk_bf16_f32 v139, v142, v143\nbuffer_store_dwordx4 v[136:139], v252, s[20:23], s84 offen offset:1024 sc1\n"
"s_mov_b64 exec, 0\ns_cmp_eq_u32 s40, 1\ns_cbranch_scc1 Lpg7\ns_cmp_eq_u32 s40, 2\ns_cbranch_scc1 LretO0\ns_cmp_eq_u32 s40, 3\n"
"s_cbranch_scc1 LretO0\ns_branch Lpg6\nLretO0:\ns_mov_b64 exec, -1\ns_add_u32 s61, s79, 88080384\ns_lshl_b32 s62, s42, 3\n"
"s_add_u32 s61, s61, s62\ns_waitcnt vmcnt(0)\ns_mov_b64 exec, 1\nbuffer_atomic_swap_x2 v[238:239], off, s[20:23], s61 sc1\ns_mov_b64 exec, -1\nv_bfe_u32 v250, v252, 7, 1\n"
"v_mul_u32_u24 v250, 2176, v250\nv_bfe_u32 v251, v252, 4, 3\nv_lshl_add_u32 v250, v251, 3, v250\ns_lshl_b32 s60, s90, 6\nv_add_u32 v250, s60, v250\ns_sleep 3\n"
"v_mov_b32 v248, 0\nv_mov_b32 v249, 0\ns_add_u32 s61, s80, 88080384\ns_mov_b64 exec, 0xffff\nbuffer_atomic_or_x2 v[248:249], v250, s[20:23], s61 offen sc0 sc1\ns_mov_b64 exec, -1\n"
"buffer_load_dwordx4 v221, s[8:11], s63 offen offset:1024 lds\ns_add_u32 m0, s50, 57344\ns_add_u32 s63, s53, 393216\nbuffer_load_dwordx4 v220, s[8:11], s63 offen lds\nbuffer_load_dwordx4 v221, s[8:11], s63 offen offset:1024 lds\ns_add_u32 m0, s50, 65536\n"
"s_add_u32 s63, s53, 458752\nbuffer_load_dwordx4 v220, s[8:11], s63 offen lds\nbuffer_load_dwordx4 v221, s[8:11], s63 offen offset:1024 lds\ns_add_u32 m0, s50, 73728\ns_add_u32 s63, s53, 524288\nbuffer_load_dwordx4 v220, s[8:11], s63 offen lds\n"
"buffer_load_dwordx4 v221, s[8:11], s63 offen offset:1024 lds\ns_add_u32 m0, s50, 81920\ns_add_u32 s63, s53, 589824\nbuffer_load_dwordx4 v220, s[8:11], s63 offen lds\nbuffer_load_dwordx4 v221, s[8:11], s63 offen offset:1024 lds\ns_add_u32 m0, s50, 90112\n"
"s_add_u32 s63, s53, 655360\nbuffer_load_dwordx4 v220, s[8:11], s63 offen lds\ns_waitcnt vmcnt(10)\nv_cmp_eq_u64 s[86:87], s[46:47], v[248:249]\ns_nop 1\ns_and_b32 s88, s86, 0xffff\n"
"s_cmp_eq_u32 s88, 0xffff\ns_cbranch_scc1 Lq201\nv_mov_b32 v246, 0\nv_mov_b32 v247, 0\ns_add_u32 s61, s80, 88080384\ns_mov_b64 exec, 0xffff\n"
"buffer_atomic_or_x2 v[246:247], v250, s[20:23], s61 offen sc0 sc1\ns_mov_b64 exec, -1\nLq201:\nbuffer_load_dwordx4 v221, s[8:11], s63 offen offset:1024 lds\nv_bfe_u32 v234, v252, 4, 4\nv_lshrrev_b32 v235, 8, v252\n"
"v_lshlrev_b32 v235, 2, v235\nv_sub_u32 v234, v234, v235\nv_lshlrev_b32 v235, 4, v234\ns_mov_b64 s[60:61], 0x3f80\nv_lshlrev_b64 v[208:209], v235, s[60:61]\nv_cmp_gt_u32 s[60:61], 4, v234\n"
"s_nop 1\nv_cndmask_b32 v212, 0, v208, s[60:61]\nv_cndmask_b32 v208, 0, v208, s[60:61]\nv_cndmask_b32 v213, 0, v209, s[60:61]\nv_cndmask_b32 v209, 0, v209, s[60:61]\ns_nop 1\n"
"v_mfma_f32_16x16x32_bf16 v[0:3], v[208:211], v[192:195], 0\nv_mfma_f32_16x16x32_bf16 v[4:7], v[210:213], v[192:195], 0\nv_mfma_f32_16x16x32_bf16 v[8:11], v[208:211], v[196:199], 0\nv_mfma_f32_16x16x32_bf16 v[12:15], v[210:213], v[196:199], 0\nv_mfma_f32_16x16x32_bf16 v[16:19], v[208:211], v[200:203], 0\nv_mfma_f32_16x16x32_bf16 v[20:23], v[210:213], v[200:203], 0\n"
"v_mfma_f32_16x16x32_bf16 v[24:27], v[208:211], v[204:207], 0\nv_mfma_f32_16x16x32_bf16 v[28:31], v[210:213], v[204:207], 0\ns_cmp_eq_u32 s88, 0xffff\ns_cbranch_scc1 Lq202\ns_waitcnt vmcnt(1)\nv_mov_b32 v248, v246\n"
"v_mov_b32 v249, v247\nv_cmp_eq_u64 s[86:87], s[46:47], v[248:249]\ns_nop 1\ns_and_b32 s62, s86, 0xffff\ns_cmp_eq_u32 s62, 0xffff\ns_cbranch_scc1 Lq193\n"
"Lq192:\nv_mov_b32 v248, 0\nv_mov_b32 v249, 0\ns_add_u32 s61, s80, 88080384\ns_mov_b64 exec, 0xffff\nbuffer_atomic_or_x2 v[248:249], v250, s[20:23], s61 offen sc0 sc1\n"
"s_mov_b64 exec, -1\ns_waitcnt vmcnt(0)\nv_cmp_eq_u64 s[86:87], s[46:47], v[248:249]\ns_nop 1\ns_and_b32 s62, s86, 0xffff\ns_cmp_eq_u32 s62, 0xffff\n"
"s_cbranch_scc1 Lq193\ns_sub_u32 s48, s48, 1\ns_cmp_gt_i32 s48, 0\ns_cbranch_scc1 Lq192\nLq193:\nLq202:\n"
"s_add_u32 s64, s70, 2097152\nbuffer_load_dwordx4 a[0:3], v252, s[20:23], s64 offen offset:0 sc1\ns_add_u32 s65, s70, 2129920\nbuffer_load_dwordx4 a[4:7], v252, s[20:23], s65 offen offset:0 sc1\ns_add_u32 s66, s71, 2097152\nbuffer_load_dwordx4 a[8:11], v252, s[20:23], s66 offen offset:0 sc1\n"
"s_add_u32 s67, s71, 2129920\nbuffer_load_dwordx4 a[12:15], v252, s[20:23], s67 offen offset:0 sc1\nbuffer_load_dwordx4 a[16:19], v252, s[20:23], s64 offen offset:1024 sc1\nbuffer_load_dwordx4 a[20:23], v252, s[20:23], s65 offen offset:1024 sc1\nbuffer_load_dwordx4 a[24:27], v252, s[20:23], s66 offen offset:1024 sc1\nbuffer_load_dwordx4 a[28:31], v252, s[20:23], s67 offen offset:1024 sc1\n"
"s_barrier\nR 160,184,8192\nR 162,184,12288\nR 164,184,16384\nR 166,184,20480\nR 168,185,8192\n"
"R 170,185,12288\nR 172,185,16384\nR 174,185,20480\nR 176,186,8192\nR 178,186,12288\nR 180,186,16384\n"
"R 182,186,20480\nbuffer_load_dwordx4 a[32:35], v252, s[20:23], s64 offen offset:2048 sc1\nbuffer_load_dwordx4 a[36:39], v252, s[20:23], s65 offen offset:2048 sc1\nbuffer_load_dwordx4 a[40:43], v252, s[20:23], s66 offen offset:2048 sc1\nbuffer_load_dwordx4 a[44:47], v252, s[20:23], s67 offen offset:2048 sc1\nbuffer_load_dwordx4 a[48:51], v252, s[20:23], s64 offen offset:3072 sc1\n"
"buffer_load_dwordx4 a[52:55], v252, s[20:23], s65 offen offset:3072 sc1\nbuffer_load_dwordx4 a[56:59], v252, s[20:23], s66 offen offset:3072 sc1\nbuffer_load_dwordx4 a[60:63], v252, s[20:23], s67 offen offset:3072 sc1\ns_add_u32 s64, s70, 2101248\nbuffer_load_dwordx4 a[64:67], v252, s[20:23], s64 offen offset:0 sc1\ns_add_u32 s65, s70, 2134016\n"
"buffer_load_dwordx4 a[68:71], v252, s[20:23], s65 offen offset:0 sc1\ns_add_u32 s66, s71, 2101248\nbuffer_load_dwordx4 a[72:75], v252, s[20:23], s66 offen offset:0 sc1\ns_add_u32 s67, s71, 2134016\nbuffer_load_dwordx4 a[76:79], v252, s[20:23], s67 offen offset:0 sc1\ns_waitcnt vmcnt(19) lgkmcnt(10)\n"
"M 0,160,0\nbuffer_load_dwordx4 a[80:83], v252, s[20:23], s64 offen offset:1024 sc1\ns_waitcnt vmcnt(16) lgkmcnt(8)\nM 0,164,16\nM 16,160,4\ns_lshl_b32 s60, s40, 5\n"
"v_xor_b32 v238, s60, v188\nv_and_b32 v238, 0xfffffbff, v238\ns_waitcnt vmcnt(15)\nM 16,164,20\nv_mov_b32 v242, s76\nv_mov_b32 v243, s76\n"
"v_mfma_f32_16x16x32_bf16 v[32:35], v[160:163], a[8:11], 0\nbuffer_load_dwordx4 a[84:87], v252, s[20:23], s65 offen offset:1024 sc1\ns_waitcnt vmcnt(15)\nM 32,164,24\nv_mov_b32 v244, s76\nv_mov_b32 v245, s76\n"
"v_mfma_f32_16x16x32_bf16 v[48:51], v[160:163], a[12:15], 0\ns_waitcnt vmcnt(14)\nM 48,164,28\nG 4\nv_mfma_f32_16x16x32_bf16 v[52:55], v[168:171], a[12:15], 0\nR 160,187,8192\n"
"M 52,172,28\nbuffer_load_dwordx4 a[88:91], v252, s[20:23], s66 offen offset:1024 sc1\nv_mfma_f32_16x16x32_bf16 v[36:39], v[168:171], a[8:11], 0\nR 162,187,12288\nM 36,172,24\nM 20,168,4\n"
"R 164,187,16384\nR 64,238,32768\nR 66,238,36864\nM 20,172,20\nbuffer_load_dwordx4 a[92:95], v252, s[20:23], s67 offen offset:1024 sc1\nM 4,168,0\n"
"R 166,187,20480\nR 68,238,33792\nR 70,238,37888\nM 4,172,16\nG 8\nM 8,176,0\n"
"R 168,184,24576\nM 8,180,16\nbuffer_load_dwordx4 a[96:99], v252, s[20:23], s64 offen offset:2048 sc1\nM 24,176,4\nR 170,184,28672\nM 24,180,20\n"
"v_mfma_f32_16x16x32_bf16 v[40:43], v[176:179], a[8:11], 0\nR 172,184,32768\nG 6\nv_mfma_f32_16x16x32_bf16 v[72:75], v[242:245], v[64:67], 0\nM 40,180,24\nbuffer_load_dwordx4 a[100:103], v252, s[20:23], s65 offen offset:2048 sc1\n"
"G 3\nv_mfma_f32_16x16x32_bf16 v[72:75], v[242:245], v[68:71], v[72:75]\nv_mfma_f32_16x16x32_bf16 v[56:59], v[176:179], a[12:15], 0\nR 174,184,36864\nM 56,180,28\nv_mfma_f32_16x16x32_bf16 v[60:63], v[160:163], a[12:15], 0\n"
"R 176,185,24576\nM 60,164,28\nbuffer_load_dwordx4 a[104:107], v252, s[20:23], s66 offen offset:2048 sc1\nv_lshrrev_b32 v64, 1, v252\nv_mfma_f32_16x16x32_bf16 v[44:47], v[160:163], a[8:11], 0\nR 178,185,28672\n"
"v_mov_b32 v73, s46\ns_and_b32 s61, s40, 3\ns_lshl_b32 s61, s61, 7\ns_add_u32 s61, s61, s91\ns_add_u32 s61, s61, 393216\ns_mov_b64 exec, 0xffff\n"
"buffer_store_dwordx2 v[72:73], v64, s[20:23], s61 offen sc1\ns_mov_b64 exec, -1\nM 44,164,24\nM 28,160,4\nR 180,185,32768\nM 28,164,20\n"
"buffer_load_dwordx4 a[108:111], v252, s[20:23], s67 offen offset:2048 sc1\nM 12,160,0\nR 182,185,36864\nM 12,164,16\ns_lshl_b32 s60, s40, 5\nv_xor_b32 v238, s60, v188\n"
"G 4\ns_waitcnt vmcnt(20)\nM 0,168,32\nR 160,186,24576\nv_and_b32 v238, 0xfffffbff, v238\ns_waitcnt vmcnt(16)\n"
"M 0,172,48\nbuffer_load_dwordx4 a[112:115], v252, s[20:23], s64 offen offset:3072 sc1\nM 16,168,36\nR 162,186,28672\ns_waitcnt vmcnt(16)\nM 16,172,52\n"
"M 32,168,40\nR 164,186,32768\ns_waitcnt vmcnt(15)\nM 32,172,56\nbuffer_load_dwordx4 a[116:119], v252, s[20:23], s65 offen offset:3072 sc1\ns_barrier\n"
"M 48,168,44\nR 166,186,36864\nR 64,238,40960\nR 66,238,45056\ns_waitcnt vmcnt(15)\nM 48,172,60\n"
"G 6\nM 52,176,44\nR 168,187,24576\nR 68,238,41984\nR 70,238,46080\nM 52,180,60\n"
"buffer_load_dwordx4 a[120:123], v252, s[20:23], s66 offen offset:3072 sc1\nM 36,176,40\nR 170,187,28672\nM 36,180,56\nM 20,176,36\nR 172,187,32768\n"
"M 20,180,52\nbuffer_load_dwordx4 a[124:127], v252, s[20:23], s67 offen offset:3072 sc1\nM 4,176,32\nR 174,187,36864\nG 6\nv_mfma_f32_16x16x32_bf16 v[72:75], v[242:245], v[64:67], 0\n"
"M 4,180,48\nG 3\nv_mfma_f32_16x16x32_bf16 v[72:75], v[242:245], v[68:71], v[72:75]\nLpg6:\ns_cbranch_execz LretO0\nM 8,160,32\n"
"R 176,184,40960\nM 8,164,48\ns_add_u32 s64, s70, 2105344\nbuffer_load_dwordx4 a[128:131], v252, s[20:23], s64 offen offset:0 sc1\nM 24,160,36\nR 178,184,45056\n"
"M 24,164,52\nv_lshrrev_b32 v64, 1, v252\nv_mov_b32 v73, s46\ns_and_b32 s61, s40, 3\ns_lshl_b32 s61, s61, 7\ns_add_u32 s61, s61, s91\n"
"s_add_u32 s61, s61, 524288\ns_mov_b64 exec, 0xffff\nbuffer_store_dwordx2 v[72:73], v64, s[20:23], s61 offen sc1\ns_mov_b64 exec, -1\nM 40,160,40\nR 180,184,49152\n"
"M 40,164,56\ns_add_u32 s65, s70, 2138112\nbuffer_load_dwordx4 a[132:135], v252, s[20:23], s65 offen offset:0 sc1\nM 56,160,44\nR 182,184,53248\nM 56,164,60\n"
"s_lshl_b32 s60, s40, 5\nv_xor_b32 v238, s60, v188\nG 4\nM 60,168,44\nR 160,185,40960\nv_and_b32 v238, 0xfffffbff, v238\n"
"M 60,172,60\ns_add_u32 s66, s71, 2105344\nbuffer_load_dwordx4 a[136:139], v252, s[20:23], s66 offen offset:0 sc1\nM 44,168,40\nR 162,185,45056\nM 44,172,56\n"
"M 28,168,36\nR 164,185,49152\nM 28,172,52\ns_add_u32 s67, s71, 2138112\nbuffer_load_dwordx4 a[140:143], v252, s[20:23], s67 offen offset:0 sc1\nM 12,168,32\n"
"R 166,185,53248\nM 12,172,48\ns_barrier\nG 4\ns_waitcnt vmcnt(21)\nM 0,176,64\n"
"R 168,186,40960\nR 64,238,49152\nR 66,238,53248\ns_waitcnt vmcnt(17)\nM 0,180,80\nbuffer_load_dwordx4 a[144:147], v252, s[20:23], s64 offen offset:1024 sc1\n"
"M 16,176,68\nR 170,186,45056\nR 68,238,50176\nR 70,238,54272\ns_waitcnt vmcnt(17)\nM 16,180,84\n"
"M 32,176,72\nR 172,186,49152\ns_waitcnt vmcnt(16)\nM 32,180,88\nbuffer_load_dwordx4 a[148:151], v252, s[20:23], s65 offen offset:1024 sc1\nM 48,176,76\n"
"R 174,186,53248\ns_waitcnt vmcnt(16)\nM 48,180,92\nG 8\nM 52,160,76\nR 176,187,40960\n"
"G 6\nv_mfma_f32_16x16x32_bf16 v[72:75], v[242:245], v[64:67], 0\nM 52,164,92\nbuffer_load_dwordx4 a[152:155], v252, s[20:23], s66 offen offset:1024 sc1\nG 3\nv_mfma_f32_16x16x32_bf16 v[72:75], v[242:245], v[68:71], v[72:75]\n"
"s_add_u32 m0, s50, 98304\ns_add_u32 s63, s53, 720896\nM 36,160,72\nR 178,187,45056\nM 36,164,88\nbuffer_load_dwordx4 v220, s[8:11], s63 offen lds\n"
"M 20,160,68\nR 180,187,49152\nM 20,164,84\nbuffer_load_dwordx4 a[156:159], v252, s[20:23], s67 offen offset:1024 sc1\nv_lshrrev_b32 v64, 1, v252\nM 4,160,64\n"
"R 182,187,53248\nv_mov_b32 v73, s46\ns_and_b32 s61, s40, 3\ns_lshl_b32 s61, s61, 7\ns_add_u32 s61, s61, s91\ns_add_u32 s61, s61, 655360\n"
"s_mov_b64 exec, 0xffff\nbuffer_store_dwordx2 v[72:73], v64, s[20:23], s61 offen sc1\ns_mov_b64 exec, -1\nM 4,164,80\nG 4\nM 8,168,64\n"
"R 160,184,57344\nM 8,172,80\nbuffer_load_dwordx4 a[160:163], v252, s[20:23], s64 offen offset:2048 sc1\nM 24,168,68\nR 162,184,61440\nbuffer_load_dwordx4 v221, s[8:11], s63 offen offset:1024 lds\n"
"M 24,172,84\nM 40,168,72\nR 164,188,0\nM 40,172,88\nbuffer_load_dwordx4 a[164:167], v252, s[20:23], s65 offen offset:2048 sc1\nM 56,168,76\n"
"R 166,188,4096\nM 56,172,92\ns_and_b32 s60, s41, 1\ns_lshl1_add_u32 s60, s60, s90\ns_lshl_b32 s60, s60, 5\nv_xor_b32 v238, s60, v184\n"
"G 4\nM 60,176,76\nR 168,185,57344\nv_and_b32 v238, 0xfffffbff, v238\nM 60,180,92\nbuffer_load_dwordx4 a[168:171], v252, s[20:23], s66 offen offset:2048 sc1\n"
"M 44,176,72\nR 170,185,61440\nR 64,238,0\nR 66,238,4096\nM 44,180,88\ns_add_u32 m0, s50, 106496\n"
"M 28,176,68\nR 172,189,0\nR 68,238,1024\nR 70,238,5120\nM 28,180,84\nbuffer_load_dwordx4 a[172:175], v252, s[20:23], s67 offen offset:2048 sc1\n"
"M 12,176,64\nR 174,189,4096\ns_add_u32 s63, s53, 786432\nM 12,180,80\ns_barrier\nG 8\n"
"s_waitcnt vmcnt(24)\nM 0,160,96\nR 176,186,57344\ns_waitcnt vmcnt(19)\nM 0,164,112\nbuffer_load_dwordx4 a[176:179], v252, s[20:23], s64 offen offset:3072 sc1\n"
"buffer_load_dwordx4 v220, s[8:11], s63 offen lds\nM 16,160,100\nR 178,186,61440\nG 6\nv_mfma_f32_16x16x32_bf16 v[72:75], v[242:245], v[64:67], 0\ns_waitcnt vmcnt(20)\n"
"M 16,164,116\nbuffer_load_dwordx4 v221, s[8:11], s63 offen offset:1024 lds\nG 3\nv_mfma_f32_16x16x32_bf16 v[72:75], v[242:245], v[68:71], v[72:75]\nM 32,160,104\nR 180,190,0\n"
"s_lshr_b32 s60, s45, 3\ns_lshl2_add_u32 s60, s60, s40\ns_lshl_b32 s60, s60, 8\nv_lshl_add_u32 v224, v252, 3, s60\nv_add_u32 v224, Lqw0+24576-LqPC, v224\nv_min_u32 v224, LqE-LqPC-4, v224\n"
"s_mov_b64 exec, 3\nglobal_load_dword v224, v224, s[98:99]\ns_mov_b64 exec, -1\ns_add_u32 s63, s53, 851968\ns_waitcnt vmcnt(21)\nM 32,164,120\n"
"buffer_load_dwordx4 a[180:183], v252, s[20:23], s65 offen offset:3072 sc1\nM 48,160,108\nR 182,190,4096\ns_add_u32 m0, s50, 114688\ns_waitcnt vmcnt(21)\nM 48,164,124\n"
"buffer_load_dwordx4 v220, s[8:11], s63 offen lds\nG 4\nv_lshrrev_b32 v64, 1, v252\nM 52,168,108\nR 160,187,57344\nv_mov_b32 v73, s46\n"
"s_and_b32 s61, s40, 1\ns_lshl_b32 s61, s61, 7\ns_add_u32 s61, s61, s91\ns_add_u32 s61, s61, 786432\ns_mov_b64 exec, 0xffff\nbuffer_store_dwordx2 v[72:73], v64, s[20:23], s61 offen sc1\n"
"s_mov_b64 exec, -1\nM 52,172,124\nbuffer_load_dwordx4 a[184:187], v252, s[20:23], s66 offen offset:3072 sc1\nM 36,168,104\nR 162,187,61440\nM 36,172,120\n"
"buffer_load_dwordx4 v221, s[8:11], s63 offen offset:1024 lds\nM 20,168,100\nR 164,191,0\nM 20,172,116\nbuffer_load_dwordx4 a[188:191], v252, s[20:23], s67 offen offset:3072 sc1\nM 4,168,96\n"
"R 166,191,4096\nM 4,172,112\nG 4\nM 8,176,96\nR 168,188,8192\nM 8,180,112\n"
"s_add_u32 s64, s70, 2109440\nbuffer_load_dwordx4 a[192:195], v252, s[20:23], s64 offen offset:0 sc1\nM 24,176,100\nR 170,188,12288\nM 24,180,116\nM 40,176,104\n"
"R 172,188,16384\nM 40,180,120\ns_add_u32 s65, s70, 2142208\nbuffer_load_dwordx4 a[196:199], v252, s[20:23], s65 offen offset:0 sc1\nM 56,176,108\nR 174,188,20480\n"
"M 56,180,124\nG 4\nM 60,160,108\nR 176,189,8192\nM 60,164,124\ns_add_u32 s66, s71, 2109440\n"
"buffer_load_dwordx4 a[200:203], v252, s[20:23], s66 offen offset:0 sc1\nM 44,160,104\nR 178,189,12288\ns_add_u32 s63, s53, 917504\nM 44,164,120\ns_add_u32 m0, s50, 0\n"
"M 28,160,100\nR 180,189,16384\nM 28,164,116\ns_add_u32 s67, s71, 2142208\nbuffer_load_dwordx4 a[204:207], v252, s[20:23], s67 offen offset:0 sc1\nM 12,160,96\n"
"R 182,189,20480\nM 12,164,112\ns_waitcnt vmcnt(17)\ns_barrier\nbuffer_load_dwordx4 v220, s[8:11], s63 offen lds\nG 4\n"
"M 0,168,128\nR 160,190,8192\nbuffer_load_dwordx4 v221, s[8:11], s63 offen offset:1024 lds\nM 0,172,144\nbuffer_load_dwordx4 a[208:211], v252, s[20:23], s64 offen offset:1024 sc1\ns_add_u32 m0, s50, 8192\n"
"s_add_u32 s63, s53, 983040\nbuffer_load_dwordx4 v220, s[8:11], s63 offen lds\nM 16,168,132\nR 162,190,12288\nbuffer_load_dwordx4 v221, s[8:11], s63 offen offset:1024 lds\nM 16,172,148\n"
"M 32,168,136\nR 164,190,16384\nM 32,172,152\nbuffer_load_dwordx4 a[212:215], v252, s[20:23], s65 offen offset:1024 sc1\nM 48,168,140\nR 166,190,20480\n"
"M 48,172,156\nG 4\nM 52,176,140\nR 168,191,8192\nM 52,180,156\nbuffer_load_dwordx4 a[216:219], v252, s[20:23], s66 offen offset:1024 sc1\n"
"M 36,176,136\nR 170,191,12288\nM 36,180,152\nM 20,176,132\nR 172,191,16384\nM 20,180,148\n"
"buffer_load_dwordx4 a[220:223], v252, s[20:23], s67 offen offset:1024 sc1\nM 4,176,128\nR 174,191,20480\nM 4,180,144\nG 4\nM 8,160,128\n"
"R 176,188,24576\nM 8,164,144\nbuffer_load_dwordx4 a[224:227], v252, s[20:23], s64 offen offset:2048 sc1\nM 24,160,132\nR 178,188,28672\nM 24,164,148\n"
"M 40,160,136\nR 180,188,32768\nM 40,164,152\nbuffer_load_dwordx4 a[228:231], v252, s[20:23], s65 offen offset:2048 sc1\nM 56,160,140\nR 182,188,36864\n"
"M 56,164,156\nG 4\nM 60,168,140\nR 160,189,24576\nM 60,172,156\nbuffer_load_dwordx4 a[232:235], v252, s[20:23], s66 offen offset:2048 sc1\n"
"M 44,168,136\nR 162,189,28672\nM 44,172,152\nM 28,168,132\nR 164,189,32768\nM 28,172,148\n"
"buffer_load_dwordx4 a[236:239], v252, s[20:23], s67 offen offset:2048 sc1\nM 12,168,128\nR 166,189,36864\nM 12,172,144\nG 4\nM 0,176,160\n"
"R 168,190,24576\ns_waitcnt vmcnt(25)\nM 0,180,176\nbuffer_load_dwordx4 a[240:243], v252, s[20:23], s64 offen offset:3072 sc1\nM 16,176,164\nR 170,190,28672\n"
"s_waitcnt vmcnt(22)\nM 16,180,180\nM 32,176,168\nR 172,190,32768\ns_waitcnt vmcnt(19)\nM 32,180,184\n"
"buffer_load_dwordx4 a[244:247], v252, s[20:23], s65 offen offset:3072 sc1\nM 48,176,172\nR 174,190,36864\ns_waitcnt vmcnt(18)\nM 48,180,188\nG 4\n"
"M 52,160,172\nR 176,191,24576\nM 52,164,188\nbuffer_load_dwordx4 a[248:251], v252, s[20:23], s66 offen offset:3072 sc1\nM 36,160,168\nR 178,191,28672\n"
"s_waitcnt vmcnt(10)\ns_barrier\nM 36,164,184\nM 20,160,164\nR 180,191,32768\nM 20,164,180\n"
"buffer_load_dwordx4 a[252:255], v252, s[20:23], s67 offen offset:3072 sc1\nM 4,160,160\nR 182,191,36864\nM 4,164,176\nG 4\nM 8,168,160\n"
"R 160,188,40960\nM 8,172,176\nM 24,168,164\nR 162,188,45056\nM 24,172,180\nM 40,168,168\n"
"R 164,188,49152\nM 40,172,184\nM 56,168,172\nR 166,188,53248\nM 56,172,188\nG 4\n"
"M 60,176,172\nR 168,189,40960\nM 60,180,188\ns_movk_i32 s63, 7168\nv_mad_u32_u24 v220, v228, s63, v229\nv_add_u32 v221, 3668992, v220\n"
"s_add_u32 m0, s50, 16384\ns_add_u32 s63, s54, 0\nbuffer_load_dwordx4 v220, s[12:15], s63 offen lds\nM 44,176,168\nR 170,189,45056\nM 44,180,184\n"
"M 28,176,164\nR 172,189,49152\nM 28,180,180\nbuffer_load_dwordx4 v221, s[12:15], s63 offen offset:1024 lds\nM 12,176,160\nR 174,189,53248\n"
"s_add_u32 m0, s50, 24576\nM 12,180,176\nG 4\nM 32,160,200\nR 176,190,40960\ns_add_u32 s63, s54, 229376\n"
"s_waitcnt vmcnt(11)\nM 32,164,216\nR 178,190,45056\nM 48,160,204\nR 180,190,49152\ns_waitcnt vmcnt(10)\n"
"M 48,164,220\nR 182,190,53248\nG 4\nM 52,168,204\nR 160,191,40960\nbuffer_load_dwordx4 v220, s[12:15], s63 offen lds\n"
"M 52,172,220\nR 162,191,45056\nM 36,168,200\nR 164,191,49152\nM 36,172,216\nR 166,191,53248\n"
"G 4\nM 40,176,200\nR 168,184,0\nbuffer_load_dwordx4 v221, s[12:15], s63 offen offset:1024 lds\nM 40,180,216\nR 170,184,4096\n"
"s_add_u32 m0, s50, 32768\nM 56,176,204\nR 172,184,8192\ns_add_u32 s63, s54, 128\nM 56,180,220\nR 174,184,12288\n"
"G 4\nM 60,160,204\nR 176,185,0\nbuffer_load_dwordx4 v220, s[12:15], s63 offen lds\nM 60,164,220\nR 178,185,4096\n"
"M 44,160,200\nR 180,185,8192\nM 44,164,216\nR 182,185,12288\nG 4\ns_waitcnt vmcnt(10)\n"
"M 32,168,232\nR 160,186,0\nbuffer_load_dwordx4 v221, s[12:15], s63 offen offset:1024 lds\ns_waitcnt vmcnt(7)\nM 32,172,248\nR 162,186,4096\n"
"s_add_u32 m0, s50, 40960\nM 48,168,236\nR 164,186,8192\ns_add_u32 s63, s54, 229504\ns_waitcnt vmcnt(6)\nM 48,172,252\n"
"R 166,186,12288\nG 4\nM 52,176,236\nR 168,187,0\nbuffer_load_dwordx4 v220, s[12:15], s63 offen lds\nM 52,180,252\n"
"R 170,187,4096\nM 36,176,232\nR 172,187,8192\nM 36,180,248\nR 174,187,12288\nG 4\n"
"M 40,160,232\nR 176,188,40960\nbuffer_load_dwordx4 v221, s[12:15], s63 offen offset:1024 lds\nM 40,164,248\nR 178,188,45056\nM 56,160,236\n"
"R 180,188,49152\nM 56,164,252\nR 182,188,53248\nG 4\nM 60,168,236\nR 160,189,40960\n"
"M 60,172,252\nR 162,189,45056\nM 44,168,232\nR 164,189,49152\nM 44,172,248\nR 166,189,53248\n"
"G 4\nM 0,176,192\nR 168,190,40960\nM 0,180,208\nR 170,190,45056\ns_mov_b32 m0, s51\n"
"M 16,176,196\nR 172,190,49152\nv_cvt_pk_bf16_f32 v32, v32, v33\nv_cvt_pk_bf16_f32 v33, v34, v35\nM 16,180,212\nR 174,190,53248\n"
"G 4\nv_cvt_pk_bf16_f32 v34, v36, v37\nM 20,160,196\nR 176,191,40960\nv_cvt_pk_bf16_f32 v35, v38, v39\nv_cvt_pk_bf16_f32 v36, v40, v41\n"
"M 20,164,212\nR 178,191,45056\nv_cvt_pk_bf16_f32 v37, v42, v43\nv_cvt_pk_bf16_f32 v38, v44, v45\nM 4,160,192\nR 180,191,49152\n"
"v_cvt_pk_bf16_f32 v39, v46, v47\nv_cvt_pk_bf16_f32 v40, v48, v49\nM 4,164,208\nR 182,191,53248\nG 4\nv_cvt_pk_bf16_f32 v41, v50, v51\n"
"M 8,168,192\nR 160,184,0\nv_cvt_pk_bf16_f32 v42, v52, v53\nv_cvt_pk_bf16_f32 v43, v54, v55\nM 8,172,208\nR 162,184,4096\n"
"v_cvt_pk_bf16_f32 v44, v56, v57\nv_cvt_pk_bf16_f32 v45, v58, v59\nM 24,168,196\nR 164,184,8192\nv_cvt_pk_bf16_f32 v46, v60, v61\nv_cvt_pk_bf16_f32 v47, v62, v63\n"
"M 24,172,212\nR 166,184,12288\nds_write_addtid_b32 v32 offset:0\nG 5\nM 28,176,196\nR 168,185,0\n"
"ds_write_addtid_b32 v33 offset:256\nM 28,180,212\nR 170,185,4096\nds_write_addtid_b32 v34 offset:512\nM 12,176,192\nR 172,185,8192\n"
"ds_write_addtid_b32 v35 offset:768\nds_write_addtid_b32 v36 offset:1024\nds_write_addtid_b32 v37 offset:1280\nds_write_addtid_b32 v38 offset:1536\nds_write_addtid_b32 v39 offset:1792\nds_write_addtid_b32 v40 offset:2048\n"
"ds_write_addtid_b32 v41 offset:2304\nds_write_addtid_b32 v42 offset:2560\nds_write_addtid_b32 v43 offset:2816\nds_write_addtid_b32 v44 offset:3072\nds_write_addtid_b32 v45 offset:3328\nds_write_addtid_b32 v46 offset:3584\n"
"ds_write_addtid_b32 v47 offset:3840\nM 12,180,208\nR 174,185,12288\nG 15\nM 0,160,224\nR 176,186,0\n"
"s_add_u32 m0, s50, 49152\nM 0,164,240\nR 178,186,4096\nG 3\ns_barrier\ns_add_u32 s63, s54, 458752\n"
"M 16,160,228\nR 180,186,8192\nbuffer_load_dwordx4 v220, s[12:15], s63 offen lds\nds_read2st64_b32 v[32:33], v225 offset0:0 offset1:1\nM 16,164,244\nR 182,186,12288\n"
"ds_read2st64_b32 v[34:35], v225 offset0:2 offset1:3\nG 6\nM 20,168,228\nR 160,187,0\nds_read2st64_b32 v[36:37], v225 offset0:4 offset1:5\nM 20,172,244\n"
"Lpg7:\ns_cbranch_execz LretO0\nR 162,187,4096\nds_read2st64_b32 v[38:39], v225 offset0:6 offset1:7\nM 4,168,224\nR 164,187,8192\n"
"buffer_load_dwordx4 v221, s[12:15], s63 offen offset:1024 lds\nds_read2st64_b32 v[40:41], v225 offset0:8 offset1:9\nM 4,172,240\nR 166,187,12288\ns_add_u32 m0, s50, 57344\ns_add_u32 s63, s54, 688128\n"
"ds_read2st64_b32 v[42:43], v225 offset0:10 offset1:11\nG 9\nM 8,176,224\nbuffer_load_dwordx4 v220, s[12:15], s63 offen lds\nds_read2st64_b32 v[44:45], v225 offset0:12 offset1:13\nM 8,180,240\n"
"ds_read2st64_b32 v[46:47], v225 offset0:14 offset1:15\nM 24,176,228\nM 24,180,244\nG 3\nM 28,160,228\nbuffer_load_dwordx4 v221, s[12:15], s63 offen offset:1024 lds\n"
"M 28,164,244\nM 12,160,224\nM 12,164,240\nG 0\ns_barrier\nv_mfma_f32_16x16x32_bf16 v[0:3], v[208:211], v[32:35], v[0:3]\n"
"v_mfma_f32_16x16x32_bf16 v[4:7], v[210:213], v[32:35], v[4:7]\nv_mfma_f32_16x16x32_bf16 v[8:11], v[208:211], v[36:39], v[8:11]\nv_mfma_f32_16x16x32_bf16 v[12:15], v[210:213], v[36:39], v[12:15]\nv_mfma_f32_16x16x32_bf16 v[16:19], v[208:211], v[40:43], v[16:19]\nv_mfma_f32_16x16x32_bf16 v[20:23], v[210:213], v[40:43], v[20:23]\nv_mfma_f32_16x16x32_bf16 v[24:27], v[208:211], v[44:47], v[24:27]\n"
"v_mfma_f32_16x16x32_bf16 v[28:31], v[210:213], v[44:47], v[28:31]\ns_lshl_b32 s84, s75, 11\ns_add_u32 s84, s84, s49\ns_add_u32 s84, s84, 10485760\ns_add_u32 s85, s84, 32768\nv_mov_b32 v238, s46\n"
"v_mov_b32 v239, s47\nv_cvt_pk_bf16_f32 v192, v0, v1\nv_cvt_pk_bf16_f32 v193, v2, v3\nv_cvt_pk_bf16_f32 v194, v4, v5\nv_cvt_pk_bf16_f32 v195, v6, v7\nbuffer_store_dwordx4 v[192:195], v252, s[20:23], s84 offen offset:0 sc1\n"
"v_cvt_pk_bf16_f32 v196, v8, v9\nv_cvt_pk_bf16_f32 v197, v10, v11\nv_cvt_pk_bf16_f32 v198, v12, v13\nv_cvt_pk_bf16_f32 v199, v14, v15\nbuffer_store_dwordx4 v[196:199], v252, s[20:23], s84 offen offset:1024 sc1\nv_cvt_pk_bf16_f32 v200, v16, v17\n"
"v_cvt_pk_bf16_f32 v201, v18, v19\nv_cvt_pk_bf16_f32 v202, v20, v21\nv_cvt_pk_bf16_f32 v203, v22, v23\nbuffer_store_dwordx4 v[200:203], v252, s[20:23], s85 offen offset:0 sc1\nv_cvt_pk_bf16_f32 v204, v24, v25\nv_cvt_pk_bf16_f32 v205, v26, v27\n"
"v_cvt_pk_bf16_f32 v206, v28, v29\nv_cvt_pk_bf16_f32 v207, v30, v31\nbuffer_store_dwordx4 v[204:207], v252, s[20:23], s85 offen offset:1024 sc1\ns_mov_b64 exec, 0\ns_cmp_eq_u32 s40, 1\ns_cbranch_scc1 Lpg9\n"
"s_cmp_eq_u32 s40, 2\ns_cbranch_scc1 Lpg10\ns_cmp_eq_u32 s40, 3\ns_cbranch_scc1 Lpg11\ns_branch Lpg8\nLretO0n:\n"
"s_mov_b64 exec, -1\ns_add_u32 s61, s79, 88219648\ns_lshl_b32 s62, s75, 3\ns_add_u32 s61, s61, s62\ns_waitcnt vmcnt(0)\ns_mov_b64 exec, 1\n"
"buffer_atomic_swap_x2 v[238:239], off, s[20:23], s61 sc1\ns_mov_b64 exec, -1\nLqw0:\nv_bfe_u32 v250, v252, 7, 1\nv_mul_u32_u24 v250, 2176, v250\nv_bfe_u32 v251, v252, 4, 3\n"
"v_lshl_add_u32 v250, v251, 3, v250\ns_lshl_b32 s60, s90, 6\nv_add_u32 v250, s60, v250\ns_sleep 3\nv_mov_b32 v248, 0\nv_mov_b32 v249, 0\n"
"s_add_u32 s61, s80, 88219648\ns_mov_b64 exec, 0xffff\nbuffer_atomic_or_x2 v[248:249], v250, s[20:23], s61 offen sc0 sc1\ns_mov_b64 exec, -1\ns_add_u32 m0, s50, 65536\ns_add_u32 s63, s54, 458880\n"
"buffer_load_dwordx4 v220, s[12:15], s63 offen lds\nbuffer_load_dwordx4 v221, s[12:15], s63 offen offset:1024 lds\ns_add_u32 m0, s50, 73728\ns_add_u32 s63, s54, 688256\nbuffer_load_dwordx4 v220, s[12:15], s63 offen lds\nbuffer_load_dwordx4 v221, s[12:15], s63 offen offset:1024 lds\n"
"s_add_u32 m0, s50, 81920\ns_add_u32 s63, s54, 917504\nbuffer_load_dwordx4 v220, s[12:15], s63 offen lds\nbuffer_load_dwordx4 v221, s[12:15], s63 offen offset:1024 lds\ns_add_u32 m0, s50, 90112\ns_add_u32 s63, s54, 1146880\n"
"buffer_load_dwordx4 v220, s[12:15], s63 offen lds\nbuffer_load_dwordx4 v221, s[12:15], s63 offen offset:1024 lds\ns_add_u32 m0, s50, 98304\ns_add_u32 s63, s54, 917632\nbuffer_load_dwordx4 v220, s[12:15], s63 offen lds\nbuffer_load_dwordx4 v221, s[12:15], s63 offen offset:1024 lds\n"
"s_waitcnt vmcnt(10)\nv_cmp_eq_u64 s[86:87], s[46:47], v[248:249]\ns_nop 1\ns_and_b32 s88, s86, 0xffff\ns_cmp_eq_u32 s88, 0xffff\ns_cbranch_scc1 Lq265\n"
"v_mov_b32 v246, 0\nv_mov_b32 v247, 0\ns_add_u32 s61, s80, 88219648\ns_mov_b64 exec, 0xffff\nbuffer_atomic_or_x2 v[246:247], v250, s[20:23], s61 offen sc0 sc1\ns_mov_b64 exec, -1\n"
"Lq265:\ns_add_u32 m0, s50, 106496\ns_add_u32 s63, s54, 1147008\nbuffer_load_dwordx4 v220, s[12:15], s63 offen lds\nbuffer_load_dwordx4 v221, s[12:15], s63 offen offset:1024 lds\ns_mov_b64 exec, 0\n"
"s_cmp_eq_u32 s40, 1\ns_cbranch_scc1 Lpg17\ns_cmp_eq_u32 s40, 2\ns_cbranch_scc1 LretW0z\ns_cmp_eq_u32 s40, 3\ns_cbranch_scc1 LretW0z\n"
"s_branch Lpg16\nLretW0z:\ns_mov_b64 exec, -1\nv_mov_b32 v230, 0\nv_mov_b32 v231, 0\nv_mov_b32 v232, 0\n"
"v_mov_b32 v233, 0\nv_dot2c_f32_bf16 v230, s76, v192\nv_dot2c_f32_bf16 v231, v192, v192\nv_dot2c_f32_bf16 v232, s76, v200\nv_dot2c_f32_bf16 v233, v200, v200\nv_dot2c_f32_bf16 v230, s76, v193\n"
"v_dot2c_f32_bf16 v231, v193, v193\nv_dot2c_f32_bf16 v232, s76, v201\nv_dot2c_f32_bf16 v233, v201, v201\nv_dot2c_f32_bf16 v230, s76, v194\nv_dot2c_f32_bf16 v231, v194, v194\nv_dot2c_f32_bf16 v232, s76, v202\n"
"v_dot2c_f32_bf16 v233, v202, v202\nv_dot2c_f32_bf16 v230, s76, v195\nv_dot2c_f32_bf16 v231, v195, v195\nv_dot2c_f32_bf16 v232, s76, v203\nv_dot2c_f32_bf16 v233, v203, v203\nv_dot2c_f32_bf16 v230, s76, v196\n"
"v_dot2c_f32_bf16 v231, v196, v196\nv_dot2c_f32_bf16 v232, s76, v204\nv_dot2c_f32_bf16 v233, v204, v204\nv_dot2c_f32_bf16 v230, s76, v197\nv_dot2c_f32_bf16 v231, v197, v197\nv_dot2c_f32_bf16 v232, s76, v205\n"
"v_dot2c_f32_bf16 v233, v205, v205\nv_dot2c_f32_bf16 v230, s76, v198\nv_dot2c_f32_bf16 v231, v198, v198\nv_dot2c_f32_bf16 v232, s76, v206\nv_dot2c_f32_bf16 v233, v206, v206\nv_dot2c_f32_bf16 v230, s76, v199\n"
"v_dot2c_f32_bf16 v231, v199, v199\nv_dot2c_f32_bf16 v232, s76, v207\nv_dot2c_f32_bf16 v233, v207, v207\nv_lshrrev_b32 v234, 5, v252\nv_and_b32 v234, 24, v234\nv_bfe_u32 v235, s46, v234, 8\n"
"v_lshrrev_b32 v234, 1, v234\nv_and_b32 v236, 0xf0, v252\nv_add_u32 v234, v234, v236\nv_permlane32_swap_b32 v230, v231\nv_permlane32_swap_b32 v232, v233\nv_add_f32 v230, v230, v231\n"
"v_add_f32 v232, v232, v233\ns_nop 1\nv_permlane16_swap_b32 v230, v232\nv_add_f32 v230, v230, v232\ns_movk_i32 s62, 0xff\nv_bfi_b32 v230, s62, v235, v230\n"
"s_lshr_b32 s61, s49, 4\ns_lshl1_add_u32 s61, s53, s61\ns_add_u32 s61, s61, 82313216\nbuffer_store_dword v230, v234, s[20:23], s61 offen sc1\ns_cmp_eq_u32 s88, 0xffff\ns_cbranch_scc1 Lq266\n"
"s_waitcnt vmcnt(3)\nv_mov_b32 v248, v246\nv_mov_b32 v249, v247\nv_cmp_eq_u64 s[86:87], s[46:47], v[248:249]\ns_nop 1\ns_and_b32 s62, s86, 0xffff\n"
"s_cmp_eq_u32 s62, 0xffff\ns_cbranch_scc1 Lq257\nLq256:\nv_mov_b32 v248, 0\nv_mov_b32 v249, 0\ns_add_u32 s61, s80, 88219648\n"
"s_mov_b64 exec, 0xffff\nbuffer_atomic_or_x2 v[248:249], v250, s[20:23], s61 offen sc0 sc1\ns_mov_b64 exec, -1\ns_waitcnt vmcnt(0)\nv_cmp_eq_u64 s[86:87], s[46:47], v[248:249]\ns_nop 1\n"
"s_and_b32 s62, s86, 0xffff\ns_cmp_eq_u32 s62, 0xffff\ns_cbranch_scc1 Lq257\ns_sub_u32 s48, s48, 1\ns_cmp_gt_i32 s48, 0\ns_cbranch_scc1 Lq256\n"
"Lq257:\nLq266:\ns_add_u32 s64, s70, 10485760\nbuffer_load_dwordx4 a[0:3], v252, s[20:23], s64 offen offset:0 sc1\ns_add_u32 s65, s70, 10518528\nbuffer_load_dwordx4 a[4:7], v252, s[20:23], s65 offen offset:0 sc1\n"
"s_add_u32 s66, s71, 10485760\nbuffer_load_dwordx4 a[8:11], v252, s[20:23], s66 offen offset:0 sc1\ns_add_u32 s67, s71, 10518528\nbuffer_load_dwordx4 a[12:15], v252, s[20:23], s67 offen offset:0 sc1\nbuffer_load_dwordx4 a[16:19], v252, s[20:23], s64 offen offset:1024 sc1\nbuffer_load_dwordx4 a[20:23], v252, s[20:23], s65 offen offset:1024 sc1\n"
"buffer_load_dwordx4 a[24:27], v252, s[20:23], s66 offen offset:1024 sc1\nbuffer_load_dwordx4 a[28:31], v252, s[20:23], s67 offen offset:1024 sc1\ns_barrier\nR 160,184,16384\nR 162,184,20480\nR 164,184,24576\n"
"R 166,184,28672\nR 168,185,16384\nR 170,185,20480\nR 172,185,24576\nR 174,185,28672\nR 176,186,16384\n"
"R 178,186,20480\nR 180,186,24576\nR 182,186,28672\nbuffer_load_dwordx4 a[32:35], v252, s[20:23], s64 offen offset:2048 sc1\nbuffer_load_dwordx4 a[36:39], v252, s[20:23], s65 offen offset:2048 sc1\nbuffer_load_dwordx4 a[40:43], v252, s[20:23], s66 offen offset:2048 sc1\n"
"buffer_load_dwordx4 a[44:47], v252, s[20:23], s67 offen offset:2048 sc1\nbuffer_load_dwordx4 a[48:51], v252, s[20:23], s64 offen offset:3072 sc1\nbuffer_load_dwordx4 a[52:55], v252, s[20:23], s65 offen offset:3072 sc1\nbuffer_load_dwordx4 a[56:59], v252, s[20:23], s66 offen offset:3072 sc1\nbuffer_load_dwordx4 a[60:63], v252, s[20:23], s67 offen offset:3072 sc1\ns_waitcnt vmcnt(15) lgkmcnt(10)\n"
"v_mfma_f32_16x16x32_bf16 v[32:35], v[160:163], a[0:3], 0\ns_add_u32 s64, s70, 10489856\nbuffer_load_dwordx4 a[64:67], v252, s[20:23], s64 offen offset:0 sc1\ns_waitcnt vmcnt(12) lgkmcnt(8)\nM 32,164,16\nv_mfma_f32_16x16x32_bf16 v[48:51], v[160:163], a[4:7], 0\n"
"s_waitcnt vmcnt(11)\nM 48,164,20\nv_mfma_f32_16x16x32_bf16 v[64:67], v[160:163], a[8:11], 0\nv_lshrrev_b32 v238, 7, v252\ns_waitcnt vmcnt(10)\nM 64,164,24\n"
"v_lshlrev_b32 v238, 9, v238\nv_mfma_f32_16x16x32_bf16 v[80:83], v[160:163], a[12:15], 0\ns_add_u32 s65, s70, 10522624\nbuffer_load_dwordx4 a[68:71], v252, s[20:23], s65 offen offset:0 sc1\ns_waitcnt vmcnt(10)\nM 80,164,28\n"
"G 4\nv_and_b32 v239, 0x70, v252\nv_mfma_f32_16x16x32_bf16 v[84:87], v[168:171], a[12:15], 0\nR 160,187,16384\nM 84,172,28\ns_and_b32 s60, s40, 3\n"
"s_lshl_b32 s60, s60, 7\nv_add3_u32 v238, v238, v239, s60\nv_mfma_f32_16x16x32_bf16 v[68:71], v[168:171], a[8:11], 0\nR 162,187,20480\ns_lshl_b32 s61, s42, 13\ns_add_u32 s61, s61, 84279296\n"
"buffer_load_dwordx4 v[0:3], v238, s[20:23], s61 offen sc1\nM 68,172,24\ns_lshl_b32 s61, s42, 13\ns_add_u32 s61, s61, 84283392\nbuffer_load_dwordx4 v[4:7], v238, s[20:23], s61 offen sc1\nv_mfma_f32_16x16x32_bf16 v[52:55], v[168:171], a[4:7], 0\n"
"R 164,187,24576\nM 52,172,20\ns_add_u32 s66, s71, 10489856\nbuffer_load_dwordx4 a[72:75], v252, s[20:23], s66 offen offset:0 sc1\nv_bfe_u32 v239, v238, 2, 7\nv_mfma_f32_16x16x32_bf16 v[36:39], v[168:171], a[0:3], 0\n"
"R 166,187,28672\nM 36,172,16\nbuffer_load_dword v239, v239, s[32:35], s54 offen offset:0\nG 4\nv_mfma_f32_16x16x32_bf16 v[40:43], v[176:179], a[0:3], 0\nR 168,184,32768\n"
"M 40,180,16\nv_mfma_f32_16x16x32_bf16 v[56:59], v[176:179], a[4:7], 0\nR 170,184,36864\nM 56,180,20\ns_add_u32 s67, s71, 10522624\nbuffer_load_dwordx4 a[76:79], v252, s[20:23], s67 offen offset:0 sc1\n"
"v_mfma_f32_16x16x32_bf16 v[72:75], v[176:179], a[8:11], 0\nR 172,184,40960\nM 72,180,24\nv_mfma_f32_16x16x32_bf16 v[88:91], v[176:179], a[12:15], 0\nR 174,184,45056\nM 88,180,28\n"
"G 4\nv_mfma_f32_16x16x32_bf16 v[92:95], v[160:163], a[12:15], 0\nR 176,185,32768\nM 92,164,28\nbuffer_load_dwordx4 a[80:83], v252, s[20:23], s64 offen offset:1024 sc1\nv_mfma_f32_16x16x32_bf16 v[76:79], v[160:163], a[8:11], 0\n"
"R 178,185,36864\nM 76,164,24\nv_mfma_f32_16x16x32_bf16 v[60:63], v[160:163], a[4:7], 0\nR 180,185,40960\nM 60,164,20\nv_mfma_f32_16x16x32_bf16 v[44:47], v[160:163], a[0:3], 0\n"
"R 182,185,45056\nM 44,164,16\nG 4\nv_mfma_f32_16x16x32_bf16 v[96:99], v[168:171], a[0:3], 0\nR 160,186,32768\nM 96,172,16\n"
"buffer_load_dwordx4 a[84:87], v252, s[20:23], s65 offen offset:1024 sc1\nv_mfma_f32_16x16x32_bf16 v[112:115], v[168:171], a[4:7], 0\nR 162,186,36864\nM 112,172,20\nv_mfma_f32_16x16x32_bf16 v[128:131], v[168:171], a[8:11], 0\nR 164,186,40960\n"
"M 128,172,24\nbuffer_load_dwordx4 a[88:91], v252, s[20:23], s66 offen offset:1024 sc1\nv_mfma_f32_16x16x32_bf16 v[144:147], v[168:171], a[12:15], 0\nR 166,186,45056\nM 144,172,28\ns_barrier\n"
"G 4\nv_mfma_f32_16x16x32_bf16 v[148:151], v[176:179], a[12:15], 0\nR 168,187,32768\nM 148,180,28\nv_mfma_f32_16x16x32_bf16 v[132:135], v[176:179], a[8:11], 0\nR 170,187,36864\n"
"M 132,180,24\nbuffer_load_dwordx4 a[92:95], v252, s[20:23], s67 offen offset:1024 sc1\nv_mfma_f32_16x16x32_bf16 v[116:119], v[176:179], a[4:7], 0\nR 172,187,40960\nM 116,180,20\nv_mfma_f32_16x16x32_bf16 v[100:103], v[176:179], a[0:3], 0\n"
"R 174,187,45056\nM 100,180,16\nG 4\nv_mfma_f32_16x16x32_bf16 v[104:107], v[160:163], a[0:3], 0\nR 176,184,49152\nv_lshrrev_b32 v242, 7, v252\n"
"M 104,164,16\nbuffer_load_dwordx4 a[96:99], v252, s[20:23], s64 offen offset:2048 sc1\nv_mfma_f32_16x16x32_bf16 v[120:123], v[160:163], a[4:7], 0\nR 178,184,53248\nv_lshlrev_b32 v242, 9, v242\ns_waitcnt vmcnt(8)\n"
"v_min3_u32 v240, v1, v3, v5\nM 120,164,20\nv_and_b32 v243, 0x70, v252\nv_min3_u32 v240, v240, v7, v7\nv_max3_u32 v241, v1, v3, v5\nv_mfma_f32_16x16x32_bf16 v[136:139], v[160:163], a[8:11], 0\n"
"R 180,184,57344\nv_max3_u32 v241, v241, v7, v7\nv_cmp_ne_u32 s[86:87], s46, v240\nM 136,164,24\ns_and_b32 s60, s40, 3\ns_lshl_b32 s60, s60, 7\n"
"v_add3_u32 v242, v242, v243, s60\nv_cmp_ne_u32 vcc, s46, v241\nv_mfma_f32_16x16x32_bf16 v[152:155], v[160:163], a[12:15], 0\nR 182,184,61440\ns_lshl_b32 s61, s42, 13\ns_add_u32 s61, s61, 84410368\n"
"buffer_load_dwordx4 v[16:19], v242, s[20:23], s61 offen sc1\ns_or_b64 s[86:87], s[86:87], vcc\ns_cbranch_scc1 Lq319\nLq320:\nM 152,164,28\ns_lshl_b32 s61, s42, 13\n"
"s_add_u32 s61, s61, 84414464\nbuffer_load_dwordx4 v[20:23], v242, s[20:23], s61 offen sc1\nG 4\nv_mfma_f32_16x16x32_bf16 v[156:159], v[168:171], a[12:15], 0\nR 160,185,49152\nv_add_f32 v0, v0, v4\n"
"M 156,172,28\nbuffer_load_dwordx4 a[100:103], v252, s[20:23], s65 offen offset:2048 sc1\nv_bfe_u32 v243, v242, 2, 7\nv_mfma_f32_16x16x32_bf16 v[140:143], v[168:171], a[8:11], 0\nR 162,185,53248\nv_add_f32 v2, v2, v6\n"
"M 140,172,24\nbuffer_load_dword v243, v243, s[32:35], s54 offen offset:128\nv_mfma_f32_16x16x32_bf16 v[124:127], v[168:171], a[4:7], 0\nR 164,185,57344\nv_add_f32_dpp v0, v0, v0 row_ror:8 row_mask:0xf bank_mask:0xf\nM 124,172,20\n"
"buffer_load_dwordx4 a[104:107], v252, s[20:23], s66 offen offset:2048 sc1\nv_mfma_f32_16x16x32_bf16 v[108:111], v[168:171], a[0:3], 0\nR 166,185,61440\nv_add_f32_dpp v2, v2, v2 row_ror:8 row_mask:0xf bank_mask:0xf\nM 108,172,16\nG 4\n"
"v_mov_b32 v4, v0\nv_mov_b32 v5, v2\nM 32,176,32\nR 168,186,49152\nv_permlane32_swap_b32 v4, v0\nM 32,180,48\n"
"v_permlane32_swap_b32 v5, v2\nv_add_f32 v0, v0, v4\nM 48,176,36\nR 170,186,53248\nv_add_f32 v2, v2, v5\nM 48,180,52\n"
"buffer_load_dwordx4 a[108:111], v252, s[20:23], s67 offen offset:2048 sc1\nM 64,176,40\nR 172,186,57344\nv_mov_b32 v4, v0\nM 64,180,56\nv_mov_b32 v5, v2\n"
"v_permlane16_swap_b32 v4, v0\nM 80,176,44\nR 174,186,61440\nv_permlane16_swap_b32 v5, v2\nM 80,180,60\ns_barrier\n"
"G 4\nM 84,160,44\nR 176,187,49152\nv_add_f32 v0, v0, v4\nM 84,164,60\nbuffer_load_dwordx4 a[112:115], v252, s[20:23], s64 offen offset:3072 sc1\n"
"M 68,160,40\nR 178,187,53248\nv_add_f32 v2, v2, v5\nM 68,164,56\nv_and_b32 v8, s77, v0\nv_sub_f32 v9, v0, v8\n"
"M 52,160,36\nR 180,187,57344\ns_waitcnt vmcnt(13)\nv_lshlrev_b32 v10, 16, v239\nM 52,164,52\nv_cvt_pk_bf16_f32 v4, v8, v9\n"
"v_cvt_pk_bf16_f32 v5, v8, v10\nM 36,160,32\nR 182,187,61440\nv_and_b32 v8, s77, v2\nM 36,164,48\nG 4\n"
"v_sub_f32 v9, v2, v8\nv_and_b32 v10, s77, v239\nM 40,168,32\nR 160,188,0\nv_cvt_pk_bf16_f32 v6, v8, v9\nM 40,172,48\n"
"buffer_load_dwordx4 a[116:119], v252, s[20:23], s65 offen offset:3072 sc1\nM 56,168,36\nR 162,188,4096\nv_cvt_pk_bf16_f32 v7, v8, v10\nM 56,172,52\nv_and_b32 v238, 0x1ff, v238\n"
"v_add_u32 v238, 157184, v238\nM 72,168,40\nR 164,188,8192\nds_write_b128 v238, v[4:7]\nM 72,172,56\nbuffer_load_dwordx4 a[120:123], v252, s[20:23], s66 offen offset:3072 sc1\n"
"M 88,168,44\nR 166,188,12288\nM 88,172,60\nG 5\nM 92,176,44\nR 168,189,0\n"
"M 92,180,60\nM 76,176,40\nR 170,189,4096\nM 76,180,56\nbuffer_load_dwordx4 a[124:127], v252, s[20:23], s67 offen offset:3072 sc1\nM 60,176,36\n"
"R 172,189,8192\nv_lshrrev_b32 v238, 7, v252\nM 60,180,52\nv_lshlrev_b32 v238, 9, v238\nM 44,176,32\nR 174,189,12288\n"
"s_waitcnt vmcnt(8)\nv_min3_u32 v244, v17, v19, v21\nv_min3_u32 v244, v244, v23, v23\nM 44,180,48\nG 4\nv_and_b32 v239, 0x70, v252\n"
"v_max3_u32 v245, v17, v19, v21\nv_max3_u32 v245, v245, v23, v23\nM 96,160,32\nR 176,190,0\ns_and_b32 s60, s40, 3\ns_lshl_b32 s60, s60, 7\n"
"v_add3_u32 v238, v238, v239, s60\nv_cmp_ne_u32 s[86:87], s46, v244\nM 96,164,48\ns_add_u32 s64, s70, 10493952\nbuffer_load_dwordx4 a[128:131], v252, s[20:23], s64 offen offset:0 sc1\nv_cmp_ne_u32 vcc, s46, v245\n"
"M 112,160,36\nR 178,190,4096\ns_lshl_b32 s61, s42, 13\ns_add_u32 s61, s61, 84541440\nbuffer_load_dwordx4 v[0:3], v238, s[20:23], s61 offen sc1\ns_or_b64 s[86:87], s[86:87], vcc\n"
"s_cbranch_scc1 Lq329\nLq330:\nM 112,164,52\ns_lshl_b32 s61, s42, 13\ns_add_u32 s61, s61, 84545536\nbuffer_load_dwordx4 v[4:7], v238, s[20:23], s61 offen sc1\n"
"v_add_f32 v16, v16, v20\nM 128,160,40\nR 180,190,8192\nv_add_f32 v18, v18, v22\nv_add_f32_dpp v16, v16, v16 row_ror:8 row_mask:0xf bank_mask:0xf\nM 128,164,56\n"
"s_add_u32 m0, s50, 114688\nv_bfe_u32 v239, v238, 2, 7\nv_add_f32_dpp v18, v18, v18 row_ror:8 row_mask:0xf bank_mask:0xf\nv_mov_b32 v20, v16\nM 144,160,44\nR 182,190,12288\n"
"s_add_u32 s63, s54, 1376256\nv_mov_b32 v21, v18\nM 144,164,60\nbuffer_load_dword v239, v239, s[32:35], s54 offen offset:256\ns_barrier\nG 4\n"
"v_permlane32_swap_b32 v20, v16\nM 148,168,44\nR 160,191,0\nv_permlane32_swap_b32 v21, v18\nv_add_f32 v16, v16, v20\nM 148,172,60\n"
"s_add_u32 s65, s70, 10526720\nbuffer_load_dwordx4 a[132:135], v252, s[20:23], s65 offen offset:0 sc1\nv_add_f32 v18, v18, v21\nM 132,168,40\nR 162,191,4096\nv_mov_b32 v20, v16\n"
"v_mov_b32 v21, v18\nM 132,172,56\nbuffer_load_dwordx4 v220, s[12:15], s63 offen lds\nM 116,168,36\nR 164,191,8192\nv_permlane16_swap_b32 v20, v16\n"
"M 116,172,52\ns_add_u32 s66, s71, 10493952\nbuffer_load_dwordx4 a[136:139], v252, s[20:23], s66 offen offset:0 sc1\nM 100,168,32\nR 166,191,12288\nv_permlane16_swap_b32 v21, v18\n"
"M 100,172,48\nG 4\nv_add_f32 v16, v16, v20\nv_add_f32 v18, v18, v21\nM 104,176,32\nR 168,188,16384\n"
"buffer_load_dwordx4 v221, s[12:15], s63 offen offset:1024 lds\nM 104,180,48\ns_add_u32 m0, s50, 0\ns_add_u32 s63, s54, 1605632\nbuffer_load_dwordx4 v220, s[12:15], s63 offen lds\nM 120,176,36\n"
"R 170,188,20480\nv_and_b32 v24, s77, v16\nM 120,180,52\ns_add_u32 s67, s71, 10526720\nbuffer_load_dwordx4 a[140:143], v252, s[20:23], s67 offen offset:0 sc1\nM 136,176,40\n"
"R 172,188,24576\nv_sub_f32 v25, v16, v24\nM 136,180,56\nbuffer_load_dwordx4 v221, s[12:15], s63 offen offset:1024 lds\nM 152,176,44\nR 174,188,28672\n"
"s_waitcnt vmcnt(17)\nv_lshlrev_b32 v26, 16, v243\nM 152,180,60\nG 4\nv_cvt_pk_bf16_f32 v20, v24, v25\nv_cvt_pk_bf16_f32 v21, v24, v26\n"
"M 156,160,44\nR 176,189,16384\nv_and_b32 v24, s77, v18\nM 156,164,60\nbuffer_load_dwordx4 a[144:147], v252, s[20:23], s64 offen offset:1024 sc1\nM 140,160,40\n"
"R 178,189,20480\nv_sub_f32 v25, v18, v24\nM 140,164,56\nv_and_b32 v26, s77, v243\nv_cvt_pk_bf16_f32 v22, v24, v25\nM 124,160,36\n"
"R 180,189,24576\nv_cvt_pk_bf16_f32 v23, v24, v26\nM 124,164,52\nv_and_b32 v242, 0x1ff, v242\nv_add_u32 v242, 157696, v242\nLpg8:\n"
"s_cbranch_execz Lpg12\nM 108,160,32\nR 182,189,28672\nds_write_b128 v242, v[20:23]\nM 108,164,48\nG 5\n"
"M 32,168,64\nR 160,190,16384\nM 32,172,80\nbuffer_load_dwordx4 a[148:151], v252, s[20:23], s65 offen offset:1024 sc1\nM 48,168,68\nR 162,190,20480\n"
"M 48,172,84\ns_add_u32 m0, s50, 8192\nM 64,168,72\nR 164,190,24576\ns_add_u32 s63, s54, 1376384\nM 64,172,88\n"
"buffer_load_dwordx4 a[152:155], v252, s[20:23], s66 offen offset:1024 sc1\nM 80,168,76\nR 166,190,28672\nM 80,172,92\nG 4\ns_barrier\n"
"M 84,176,76\nR 168,191,16384\nv_lshrrev_b32 v242, 7, v252\nM 84,180,92\nbuffer_load_dwordx4 v220, s[12:15], s63 offen lds\nv_lshlrev_b32 v242, 9, v242\n"
"M 68,176,72\nR 170,191,20480\ns_waitcnt vmcnt(12)\nv_min3_u32 v240, v1, v3, v5\nv_min3_u32 v240, v240, v7, v7\nM 68,180,88\n"
"buffer_load_dwordx4 a[156:159], v252, s[20:23], s67 offen offset:1024 sc1\nv_and_b32 v243, 0x70, v252\nv_max3_u32 v241, v1, v3, v5\nM 52,176,68\nR 172,191,24576\nv_max3_u32 v241, v241, v7, v7\n"
"v_cmp_ne_u32 s[86:87], s46, v240\nM 52,180,84\ns_and_b32 s60, s40, 1\ns_lshl_b32 s60, s60, 7\nv_add3_u32 v242, v242, v243, s60\nv_cmp_ne_u32 vcc, s46, v241\n"
"M 36,176,64\nR 174,191,28672\ns_lshl_b32 s61, s42, 13\ns_add_u32 s61, s61, 84672512\nbuffer_load_dwordx4 v[16:19], v242, s[20:23], s61 offen sc1\ns_or_b64 s[86:87], s[86:87], vcc\n"
"s_cbranch_scc1 Lq339\nLq340:\nM 36,180,80\ns_lshl_b32 s61, s42, 13\ns_add_u32 s61, s61, 84676608\nbuffer_load_dwordx4 v[20:23], v242, s[20:23], s61 offen sc1\n"
"G 4\nv_add_f32 v0, v0, v4\nM 40,160,64\nR 176,188,32768\nbuffer_load_dwordx4 v221, s[12:15], s63 offen offset:1024 lds\nv_add_f32 v2, v2, v6\n"
"M 40,164,80\nbuffer_load_dwordx4 a[160:163], v252, s[20:23], s64 offen offset:2048 sc1\ns_add_u32 s63, s54, 1605760\nv_bfe_u32 v243, v242, 2, 7\nv_add_f32_dpp v0, v0, v0 row_ror:8 row_mask:0xf bank_mask:0xf\nM 56,160,68\n"
"R 178,188,36864\ns_add_u32 m0, s50, 16384\nv_add_f32_dpp v2, v2, v2 row_ror:8 row_mask:0xf bank_mask:0xf\nM 56,164,84\nbuffer_load_dword v243, v243, s[32:35], s55 offen offset:0\nv_mov_b32 v4, v0\n"
"M 72,160,72\nR 180,188,40960\nbuffer_load_dwordx4 v220, s[12:15], s63 offen lds\nv_mov_b32 v5, v2\nM 72,164,88\nbuffer_load_dwordx4 v221, s[12:15], s63 offen offset:1024 lds\n"
"v_permlane32_swap_b32 v4, v0\nM 88,160,76\nR 182,188,45056\nv_permlane32_swap_b32 v5, v2\nv_add_f32 v0, v0, v4\nM 88,164,92\n"
"G 4\nv_add_f32 v2, v2, v5\nv_mov_b32 v4, v0\nv_mov_b32 v5, v2\nM 92,168,76\nR 160,189,32768\n"
"v_permlane16_swap_b32 v4, v0\nv_permlane16_swap_b32 v5, v2\nM 92,172,92\nbuffer_load_dwordx4 a[164:167], v252, s[20:23], s65 offen offset:2048 sc1\nv_add_f32 v0, v0, v4\nM 76,168,72\n"
"R 162,189,36864\nv_add_f32 v2, v2, v5\nv_and_b32 v8, s77, v0\nM 76,172,88\nv_sub_f32 v9, v0, v8\ns_waitcnt vmcnt(20)\n"
"v_lshlrev_b32 v10, 16, v239\nM 60,168,68\nR 164,189,40960\nv_cvt_pk_bf16_f32 v4, v8, v9\nM 60,172,84\nbuffer_load_dwordx4 a[168:171], v252, s[20:23], s66 offen offset:2048 sc1\n"
"M 44,168,64\nR 166,189,45056\nv_cvt_pk_bf16_f32 v5, v8, v10\nM 44,172,80\nG 4\nv_and_b32 v8, s77, v2\n"
"v_sub_f32 v9, v2, v8\nM 96,176,64\nR 168,190,32768\nv_and_b32 v10, s77, v239\nM 96,180,80\nv_cvt_pk_bf16_f32 v6, v8, v9\n"
"v_cvt_pk_bf16_f32 v7, v8, v10\nM 112,176,68\nR 170,190,36864\nv_and_b32 v238, 0x1ff, v238\nM 112,180,84\nbuffer_load_dwordx4 a[172:175], v252, s[20:23], s67 offen offset:2048 sc1\n"
"M 128,176,72\nR 172,190,40960\nv_add_u32 v238, 158208, v238\nM 128,180,88\ns_add_u32 m0, s50, 24576\nds_write_b128 v238, v[4:7]\n"
"M 144,176,76\nR 174,190,45056\ns_add_u32 s63, s54, 1835008\nM 144,180,92\ns_waitcnt vmcnt(15) lgkmcnt(1)\ns_barrier\n"
"M 148,160,76\nR 176,191,32768\nM 148,164,92\nbuffer_load_dwordx4 a[176:179], v252, s[20:23], s64 offen offset:3072 sc1\nM 132,160,72\nR 178,191,36864\n"
"M 132,164,88\nbuffer_load_dwordx4 v220, s[12:15], s63 offen lds\nM 116,160,68\nR 180,191,40960\nM 116,164,84\nbuffer_load_dwordx4 v221, s[12:15], s63 offen offset:1024 lds\n"
"M 100,160,64\nR 182,191,45056\ns_add_u32 m0, s50, 32768\nM 100,164,80\nG 4\nM 104,168,64\n"
"R 160,188,49152\ns_add_u32 s63, s54, 2064384\nM 104,172,80\nbuffer_load_dwordx4 a[180:183], v252, s[20:23], s65 offen offset:3072 sc1\nM 120,168,68\nR 162,188,53248\n"
"M 120,172,84\nbuffer_load_dwordx4 v220, s[12:15], s63 offen lds\nM 136,168,72\nR 164,184,0\nM 136,172,88\nbuffer_load_dwordx4 a[184:187], v252, s[20:23], s66 offen offset:3072 sc1\n"
"M 152,168,76\nR 166,184,4096\ns_waitcnt vmcnt(14)\nv_min3_u32 v244, v17, v19, v21\nv_min3_u32 v244, v244, v23, v23\nM 152,172,92\n"
"G 4\nv_max3_u32 v245, v17, v19, v21\nv_max3_u32 v245, v245, v23, v23\nv_cmp_ne_u32 s[86:87], s46, v244\nM 156,176,76\nR 168,189,49152\n"
"buffer_load_dwordx4 v221, s[12:15], s63 offen offset:1024 lds\nv_cmp_ne_u32 vcc, s46, v245\nM 156,180,92\ns_or_b64 s[86:87], s[86:87], vcc\ns_cbranch_scc1 Lq349\nLq350:\n"
"v_add_f32 v16, v16, v20\nv_add_f32 v18, v18, v22\nM 140,176,72\nR 170,189,53248\nv_add_f32_dpp v16, v16, v16 row_ror:8 row_mask:0xf bank_mask:0xf\nv_add_f32_dpp v18, v18, v18 row_ror:8 row_mask:0xf bank_mask:0xf\n"
"M 140,180,88\nbuffer_load_dwordx4 a[188:191], v252, s[20:23], s67 offen offset:3072 sc1\nv_mov_b32 v20, v16\nM 124,176,68\nR 172,185,0\nv_mov_b32 v21, v18\n"
"M 124,180,84\nv_permlane32_swap_b32 v20, v16\nv_permlane32_swap_b32 v21, v18\nM 108,176,64\nR 174,185,4096\nv_add_f32 v16, v16, v20\n"
"M 108,180,80\nG 4\nv_add_f32 v18, v18, v21\nv_mov_b32 v20, v16\nM 32,160,96\nR 176,190,49152\n"
"v_mov_b32 v21, v18\nM 32,164,112\ns_add_u32 s64, s70, 10498048\nbuffer_load_dwordx4 a[192:195], v252, s[20:23], s64 offen offset:0 sc1\nM 48,160,100\nR 178,190,53248\n"
"v_permlane16_swap_b32 v20, v16\nM 48,164,116\nv_permlane16_swap_b32 v21, v18\nv_add_f32 v16, v16, v20\nM 64,160,104\nR 180,186,0\n"
"v_add_f32 v18, v18, v21\nM 64,164,120\ns_add_u32 m0, s50, 40960\nv_and_b32 v24, s77, v16\nv_sub_f32 v25, v16, v24\nM 80,160,108\n"
"R 182,186,4096\ns_add_u32 s63, s54, 2293760\nM 80,164,124\ns_waitcnt vmcnt(12)\ns_barrier\nG 4\n"
"M 84,168,108\nR 160,191,49152\nv_lshlrev_b32 v26, 16, v243\nM 84,172,124\ns_add_u32 s65, s70, 10530816\nbuffer_load_dwordx4 a[196:199], v252, s[20:23], s65 offen offset:0 sc1\n"
"M 68,168,104\nR 162,191,53248\nv_cvt_pk_bf16_f32 v20, v24, v25\nM 68,172,120\nbuffer_load_dwordx4 v220, s[12:15], s63 offen lds\nM 52,168,100\n"
"R 164,187,0\nv_cvt_pk_bf16_f32 v21, v24, v26\nM 52,172,116\ns_add_u32 s66, s71, 10498048\nbuffer_load_dwordx4 a[200:203], v252, s[20:23], s66 offen offset:0 sc1\nM 36,168,96\n"
"R 166,187,4096\nv_and_b32 v24, s77, v18\nM 36,172,112\nG 4\nv_sub_f32 v25, v18, v24\nv_and_b32 v26, s77, v243\n"
"M 40,176,96\nR 168,184,8192\nbuffer_load_dwordx4 v221, s[12:15], s63 offen offset:1024 lds\nM 40,180,112\ns_add_u32 m0, s50, 49152\ns_add_u32 s63, s54, 2523136\n"
"buffer_load_dwordx4 v220, s[12:15], s63 offen lds\nM 56,176,100\nR 170,184,12288\nv_cvt_pk_bf16_f32 v22, v24, v25\nM 56,180,116\ns_add_u32 s67, s71, 10530816\n"
"buffer_load_dwordx4 a[204:207], v252, s[20:23], s67 offen offset:0 sc1\nM 72,176,104\nR 172,184,16384\nv_cvt_pk_bf16_f32 v23, v24, v26\nM 72,180,120\nbuffer_load_dwordx4 v221, s[12:15], s63 offen offset:1024 lds\n"
"M 88,176,108\nR 174,184,20480\nv_and_b32 v242, 0x1ff, v242\nM 88,180,124\nv_add_u32 v242, 158720, v242\nds_write_b128 v242, v[20:23]\n"
"G 5\nM 92,160,108\nR 176,185,8192\nM 92,164,124\nbuffer_load_dwordx4 a[208:211], v252, s[20:23], s64 offen offset:1024 sc1\nM 76,160,104\n"
"R 178,185,12288\nM 76,164,120\ns_lshr_b32 s84, s49, 4\nM 60,160,100\nR 180,185,16384\nLq573:\n"
"s_lshr_b32 s60, s45, 3\ns_lshl2_add_u32 s60, s60, s40\ns_lshl_b32 s60, s60, 8\nv_lshl_add_u32 v224, v252, 3, s60\nv_add_u32 v224, Lq573+4096-LqPC, v224\nv_min_u32 v224, LqE-LqPC-4, v224\n"
"s_mov_b64 exec, 3\nglobal_load_dword v224, v224, s[98:99]\ns_mov_b64 exec, -1\nM 60,164,116\ns_add_u32 s84, s84, 82313216\nM 44,160,96\n"
"R 182,185,20480\nM 44,164,112\nG 5\nM 96,168,96\nR 160,186,8192\nM 96,172,112\n"
"buffer_load_dwordx4 a[212:215], v252, s[20:23], s65 offen offset:1024 sc1\nM 112,168,100\nR 162,186,12288\nM 112,172,116\nbuffer_load_dwordx4 v[0:3], v252, s[20:23], s84 offen offset:0 sc1\nM 128,168,104\n"
"R 164,186,16384\nM 128,172,120\nbuffer_load_dwordx4 a[216:219], v252, s[20:23], s66 offen offset:1024 sc1\nM 144,168,108\nR 166,186,20480\ns_add_u32 m0, s50, 57344\n"
"M 144,172,124\nbuffer_load_dwordx4 v[4:7], v252, s[20:23], s84 offen offset:1024 sc1\ns_waitcnt vmcnt(15) lgkmcnt(8)\ns_barrier\nG 4\nM 148,176,108\n"
"R 168,187,8192\ns_add_u32 s63, s54, 2752512\nM 148,180,124\nbuffer_load_dwordx4 v[8:11], v252, s[20:23], s84 offen offset:2048 sc1\nM 132,176,104\nR 170,187,12288\n"
"M 132,180,120\nbuffer_load_dwordx4 a[220:223], v252, s[20:23], s67 offen offset:1024 sc1\nM 116,176,100\nR 172,187,16384\nbuffer_load_dwordx4 v220, s[12:15], s63 offen lds\nM 116,180,116\n"
"buffer_load_dwordx4 v[12:15], v252, s[20:23], s84 offen offset:3072 sc1\nM 100,176,96\nR 174,187,20480\nM 100,180,112\nG 4\nM 104,160,96\n"
"R 176,184,24576\nbuffer_load_dwordx4 v221, s[12:15], s63 offen offset:1024 lds\nM 104,164,112\nbuffer_load_dwordx4 a[224:227], v252, s[20:23], s64 offen offset:2048 sc1\ns_add_u32 s63, s54, 2981888\nM 120,160,100\n"
"R 178,184,28672\ns_add_u32 m0, s50, 65536\nM 120,164,116\nbuffer_load_dwordx4 v220, s[12:15], s63 offen lds\nM 136,160,104\nR 180,184,32768\n"
"M 136,164,120\nbuffer_load_dwordx4 v221, s[12:15], s63 offen offset:1024 lds\nM 152,160,108\nR 182,184,36864\nM 152,164,124\nG 4\n"
"M 156,168,108\nR 160,185,24576\nM 156,172,124\nbuffer_load_dwordx4 a[228:231], v252, s[20:23], s65 offen offset:2048 sc1\nM 140,168,104\nR 162,185,28672\n"
"M 140,172,120\nM 124,168,100\nR 164,185,32768\nM 124,172,116\nbuffer_load_dwordx4 a[232:235], v252, s[20:23], s66 offen offset:2048 sc1\nM 108,168,96\n"
"R 166,185,36864\nM 108,172,112\nG 4\nM 32,176,128\nR 168,186,24576\nM 32,180,144\n"
"M 48,176,132\nR 170,186,28672\nM 48,180,148\nbuffer_load_dwordx4 a[236:239], v252, s[20:23], s67 offen offset:2048 sc1\nM 64,176,136\nR 172,186,32768\n"
"M 64,180,152\ns_add_u32 m0, s50, 73728\nM 80,176,140\nR 174,186,36864\ns_add_u32 s63, s54, 3211264\nM 80,180,156\n"
"s_waitcnt vmcnt(17)\ns_barrier\nG 4\nM 84,160,140\nR 176,187,24576\nM 84,164,156\n"
"buffer_load_dwordx4 a[240:243], v252, s[20:23], s64 offen offset:3072 sc1\nM 68,160,136\nR 178,187,28672\nM 68,164,152\nbuffer_load_dwordx4 v220, s[12:15], s63 offen lds\nM 52,160,132\n"
"R 180,187,32768\nM 52,164,148\nbuffer_load_dwordx4 v221, s[12:15], s63 offen offset:1024 lds\nM 36,160,128\nR 182,187,36864\ns_add_u32 m0, s50, 81920\n"
"M 36,164,144\nG 4\nM 40,168,128\nR 160,184,40960\ns_add_u32 s63, s54, 3440640\nM 40,172,144\n"
"buffer_load_dwordx4 a[244:247], v252, s[20:23], s65 offen offset:3072 sc1\nM 56,168,132\nR 162,184,45056\nM 56,172,148\nbuffer_load_dwordx4 v220, s[12:15], s63 offen lds\nM 72,168,136\n"
"R 164,184,49152\nM 72,172,152\nbuffer_load_dwordx4 a[248:251], v252, s[20:23], s66 offen offset:3072 sc1\nM 88,168,140\nR 166,184,53248\ns_mov_b32 s85, 0x0c0c0400\n"
"s_waitcnt vmcnt(19)\nv_perm_b32 v16, v1, v0, s85\nM 88,172,156\nG 4\nv_perm_b32 v20, v3, v2, s85\nv_lshl_or_b32 v16, v20, 16, v16\n"
"s_waitcnt vmcnt(17)\nv_perm_b32 v17, v5, v4, s85\nM 92,176,140\nR 168,185,40960\nbuffer_load_dwordx4 v221, s[12:15], s63 offen offset:1024 lds\nv_perm_b32 v21, v7, v6, s85\n"
"M 92,180,156\nv_lshl_or_b32 v17, v21, 16, v17\ns_waitcnt vmcnt(17)\nv_perm_b32 v18, v9, v8, s85\nv_perm_b32 v22, v11, v10, s85\nM 76,176,136\n"
"R 170,185,45056\nv_lshl_or_b32 v18, v22, 16, v18\ns_waitcnt vmcnt(14)\nv_perm_b32 v19, v13, v12, s85\nM 76,180,152\nbuffer_load_dwordx4 a[252:255], v252, s[20:23], s67 offen offset:3072 sc1\n"
"v_perm_b32 v23, v15, v14, s85\nM 60,176,132\nR 172,185,49152\nv_lshl_or_b32 v19, v23, 16, v19\nv_min3_u32 v230, v16, v17, v18\nM 60,180,148\n"
"v_min3_u32 v230, v230, v19, v19\nv_max3_u32 v231, v16, v17, v18\nv_max3_u32 v231, v231, v19, v19\nM 44,176,128\nR 174,185,53248\nv_cmp_ne_u32 s[86:87], s46, v230\n"
"v_cmp_ne_u32 vcc, s46, v231\nM 44,180,144\ns_or_b64 s[86:87], s[86:87], vcc\ns_cbranch_scc1 Lq317\nLq318:\nG 4\n"
"v_and_b32 v0, 0xffffff00, v0\nM 32,160,160\nR 176,186,40960\nv_and_b32 v1, 0xffffff00, v1\nv_and_b32 v2, 0xffffff00, v2\nM 32,164,176\n"
"v_and_b32 v3, 0xffffff00, v3\nv_and_b32 v4, 0xffffff00, v4\nv_and_b32 v5, 0xffffff00, v5\nM 48,160,164\nR 178,186,45056\nv_and_b32 v6, 0xffffff00, v6\n"
"v_and_b32 v7, 0xffffff00, v7\nM 48,164,180\ns_add_u32 m0, s50, 90112\nv_and_b32 v8, 0xffffff00, v8\nv_and_b32 v9, 0xffffff00, v9\nv_and_b32 v10, 0xffffff00, v10\n"
"M 64,160,168\nR 180,186,49152\nv_and_b32 v11, 0xffffff00, v11\nv_and_b32 v12, 0xffffff00, v12\nM 64,164,184\ns_add_u32 s63, s54, 1835136\n"
"v_and_b32 v13, 0xffffff00, v13\nv_and_b32 v14, 0xffffff00, v14\nv_and_b32 v15, 0xffffff00, v15\nM 80,160,172\nR 182,186,53248\nv_add_f32 v0, v0, v8\n"
"v_add_f32 v1, v1, v9\nM 80,164,188\ns_waitcnt vmcnt(11)\ns_barrier\nG 4\nv_add_f32 v2, v2, v10\n"
"M 84,168,172\nR 160,187,40960\nv_add_f32 v3, v3, v11\nv_add_f32 v4, v4, v12\nM 84,172,188\nbuffer_load_dwordx4 v220, s[12:15], s63 offen lds\n"
"v_add_f32 v5, v5, v13\nM 68,168,168\nR 162,187,45056\nv_add_f32 v6, v6, v14\nv_add_f32 v7, v7, v15\nM 68,172,184\n"
"v_add_f32 v0, v0, v4\nv_add_f32 v1, v1, v5\nv_add_f32 v2, v2, v6\nM 52,168,164\nR 164,187,49152\nv_bfe_u32 v250, v252, 5, 1\n"
"v_add_f32 v3, v3, v7\nv_mul_u32_u24 v250, 2560, v250\nM 52,172,180\nbuffer_load_dwordx4 v221, s[12:15], s63 offen offset:1024 lds\nv_permlane32_swap_b32 v0, v2\nv_and_b32 v251, 16, v252\n"
"M 36,168,160\nR 166,187,53248\ns_add_u32 m0, s50, 98304\nv_permlane32_swap_b32 v1, v3\nv_lshl_add_u32 v250, v251, 1, v250\nM 36,172,176\n"
"s_lshl_b32 s60, s90, 6\nv_add_u32 v250, s60, v250\nG 4\nv_add_f32 v0, v0, v2\nM 40,176,160\nR 168,184,57344\n"
"s_add_u32 s63, s54, 2064512\nv_add_f32 v1, v1, v3\nM 40,180,176\nbuffer_load_dwordx4 v220, s[12:15], s63 offen lds\nv_permlane16_swap_b32 v0, v1\nM 56,176,164\n"
"R 170,184,61440\nv_add_f32 v230, v0, v1\nv_mov_b32 v232, v230\nM 56,180,180\ns_nop 0\nv_permlane16_swap_b32 v230, v232\n"
"v_mov_b32 v231, v230\nv_mov_b32 v233, v232\nM 72,176,168\nR 172,188,0\nv_permlane32_swap_b32 v230, v231\nM 72,180,184\n"
"buffer_load_dwordx4 v221, s[12:15], s63 offen offset:1024 lds\nM 88,176,172\nR 174,188,4096\ns_add_u32 m0, s50, 106496\nM 88,180,188\nG 4\n"
"v_permlane32_swap_b32 v232, v233\nv_mul_f32 v230, 0xba800000, v230\nM 92,160,172\nR 176,185,57344\ns_add_u32 s63, s54, 2293888\nLpg9:\n"
"s_cbranch_execz Lpg13\nM 92,164,188\nbuffer_load_dwordx4 v220, s[12:15], s63 offen lds\nM 76,160,168\nR 178,185,61440\nv_mul_f32 v214, v230, v230\n"
"M 76,164,184\nv_mul_f32 v231, 0x3a800000, v231\nv_sub_f32 v231, v231, v214\nM 60,160,164\nR 180,189,0\nv_max_f32 v231, 0, v231\n"
"M 60,164,180\nv_add_f32 v231, 0x3727c5ac, v231\nv_and_b32 v208, s77, v230\nM 44,160,160\nR 182,189,4096\nv_sub_f32 v230, v230, v208\n"
"M 44,164,176\nG 4\nv_rsq_f32 v234, v231\nM 32,168,192\nR 160,186,57344\nv_mul_f32 v216, v231, v234\n"
"M 32,172,208\nbuffer_load_dwordx4 v221, s[12:15], s63 offen offset:1024 lds\nM 48,168,196\nR 162,186,61440\ns_add_u32 s63, s54, 2523264\nM 48,172,212\n"
"s_add_u32 m0, s50, 114688\nv_cvt_pk_bf16_f32 v209, v230, v216\nv_cvt_pk_bf16_f32 v208, v208, v208\nM 64,168,200\nR 164,190,0\nv_mul_f32 v214, 0xc0135761, v234\n"
"M 64,172,216\nbuffer_load_dwordx4 v220, s[12:15], s63 offen lds\nM 80,168,204\nR 166,190,4096\nv_mul_f32 v215, v234, v234\nM 80,172,220\n"
"s_waitcnt vmcnt(8)\ns_barrier\nG 4\nM 84,176,204\nR 168,187,57344\nv_mul_f32 v215, v215, v214\n"
"M 84,180,220\nbuffer_load_dwordx4 v221, s[12:15], s63 offen offset:1024 lds\nM 68,176,200\nR 170,187,61440\ns_add_u32 s63, s54, 2752640\nM 68,180,216\n"
"s_add_u32 m0, s50, 0\ns_mov_b64 s[60:61], 0xffff\nv_cndmask_b32 v208, 0, v208, s[60:61]\nv_mul_f32 v215, 0x3d372713, v215\nM 52,176,196\nR 172,191,0\n"
"v_mul_f32 v232, 0xba800000, v232\nM 52,180,212\nbuffer_load_dwordx4 v220, s[12:15], s63 offen lds\nM 36,176,192\nR 174,191,4096\nv_mul_f32 v217, v232, v232\n"
"M 36,180,208\ns_mov_b64 s[60:61], 0xffff\nv_cndmask_b32 v209, 0, v209, s[60:61]\nG 4\nv_mul_f32 v233, 0x3a800000, v233\nM 40,160,192\n"
"R 176,188,8192\nv_sub_f32 v233, v233, v217\nM 40,164,208\nbuffer_load_dwordx4 v221, s[12:15], s63 offen offset:1024 lds\nM 56,160,196\nR 178,188,12288\n"
"v_max_f32 v233, 0, v233\nM 56,164,212\ns_add_u32 m0, s50, 8192\nv_add_f32 v233, 0x3727c5ac, v233\nv_and_b32 v212, s77, v232\nM 72,160,200\n"
"R 180,188,16384\nv_sub_f32 v232, v232, v212\nM 72,164,216\ns_add_u32 s63, s54, 2982016\nv_rsq_f32 v235, v233\nM 88,160,204\n"
"R 182,188,20480\nv_mul_f32 v219, v233, v235\nM 88,164,220\nG 4\nv_cvt_pk_bf16_f32 v213, v232, v219\nv_cvt_pk_bf16_f32 v212, v212, v212\n"
"M 92,168,204\nR 160,189,8192\nv_mul_f32 v217, 0xc0135761, v235\nM 92,172,220\nbuffer_load_dwordx4 v220, s[12:15], s63 offen lds\nM 76,168,200\n"
"R 162,189,12288\nv_mul_f32 v218, v235, v235\nM 76,172,216\ns_mov_b64 s[60:61], 0xffff\nv_cndmask_b32 v212, 0, v212, s[60:61]\nv_mul_f32 v218, v218, v217\n"
"M 60,168,196\nR 164,189,16384\nv_mul_f32 v218, 0x3d372713, v218\nM 60,172,212\nbuffer_load_dwordx4 v221, s[12:15], s63 offen offset:1024 lds\nM 44,168,192\n"
"R 166,189,20480\ns_add_u32 m0, s50, 16384\nM 44,172,208\ns_mov_b64 s[60:61], 0xffff\nv_cndmask_b32 v213, 0, v213, s[60:61]\nG 4\n"
"M 32,176,224\nR 168,190,8192\ns_add_u32 s63, s54, 3211392\nM 32,180,240\nbuffer_load_dwordx4 v220, s[12:15], s63 offen lds\nM 48,176,228\n"
"R 170,190,12288\nM 48,180,244\nM 64,176,232\nR 172,190,16384\nM 64,180,248\nbuffer_load_dwordx4 v221, s[12:15], s63 offen offset:1024 lds\n"
"M 80,176,236\nR 174,190,20480\ns_add_u32 m0, s50, 24576\ns_waitcnt vmcnt(14)\nM 80,180,252\ns_waitcnt vmcnt(10)\n"
"s_barrier\nG 4\nM 84,160,236\nR 176,191,8192\ns_add_u32 s63, s54, 3440768\nM 84,164,252\n"
"buffer_load_dwordx4 v220, s[12:15], s63 offen lds\nM 68,160,232\nR 178,191,12288\nM 68,164,248\nM 52,160,228\nR 180,191,16384\n"
"M 52,164,244\nM 36,160,224\nR 182,191,20480\nM 36,164,240\nG 4\nM 40,168,224\n"
"R 160,188,24576\nM 40,172,240\nbuffer_load_dwordx4 v221, s[12:15], s63 offen offset:1024 lds\nM 56,168,228\nR 162,188,28672\ns_add_u32 s63, s55, 0\n"
"M 56,172,244\ns_add_u32 m0, s50, 32768\nM 72,168,232\nR 164,188,32768\nM 72,172,248\nbuffer_load_dwordx4 v222, s[12:15], s63 offen lds\n"
"M 88,168,236\nR 166,188,36864\ns_mov_b32 s60, 157184\nv_bfe_u32 v246, v252, 4, 4\nv_lshl_add_u32 v246, v246, 3, s60\nM 88,172,252\n"
"ds_read_b64 v[238:239], v246 offset:0\nG 5\nM 92,176,236\nR 168,189,24576\nds_read_b64 v[240:241], v246 offset:128\nM 92,180,252\n"
"ds_read_b64 v[242:243], v246 offset:256\nM 76,176,232\nR 170,189,28672\nds_read_b64 v[244:245], v246 offset:384\nM 76,180,248\nds_read_b64 v[246:247], v246 offset:0\n"
"M 60,176,228\nR 172,189,32768\nM 60,180,244\nbuffer_load_dwordx4 v223, s[12:15], s63 offen offset:1024 lds\nM 44,176,224\nR 174,189,36864\n"
"M 44,180,240\ns_waitcnt vmcnt(10) lgkmcnt(2)\ns_barrier\nM 96,160,128\nR 176,190,24576\nM 96,164,144\n"
"s_mov_b32 m0, s51\nM 112,160,132\nR 178,190,28672\ns_lshl2_add_u32 s60, s45, s40\nv_lshlrev_b32 v224, 3, v252\ns_mul_i32 s60, s60, 7168\n"
"s_bfm_b64 exec, 14, 0\nbuffer_load_dword v224, v224, s[16:19], s60 offen\ns_mov_b64 exec, -1\nM 112,164,148\nds_write_addtid_b32 v64 offset:0\nM 128,160,136\n"
"R 180,190,32768\nv_mfma_f32_16x16x32_bf16 v[32:35], v[238:241], v[208:211], v[32:35]\nds_write_addtid_b32 v65 offset:256\nds_write_addtid_b32 v66 offset:512\nM 128,164,152\nds_write_addtid_b32 v67 offset:768\n"
"M 144,160,140\nR 182,190,36864\nv_mfma_f32_16x16x32_bf16 v[36:39], v[240:243], v[208:211], v[36:39]\nds_write_addtid_b32 v68 offset:1024\nds_write_addtid_b32 v69 offset:1280\nM 144,164,156\n"
"ds_write_addtid_b32 v70 offset:1536\nG 11\nM 148,168,140\nR 160,191,24576\nv_mfma_f32_16x16x32_bf16 v[40:43], v[242:245], v[208:211], v[40:43]\nds_write_addtid_b32 v71 offset:1792\n"
"ds_write_addtid_b32 v72 offset:2048\nM 148,172,156\nds_write_addtid_b32 v73 offset:2304\nM 132,168,136\nR 162,191,28672\nv_mfma_f32_16x16x32_bf16 v[44:47], v[244:247], v[208:211], v[44:47]\n"
"ds_write_addtid_b32 v74 offset:2560\nds_write_addtid_b32 v75 offset:2816\nM 132,172,152\nds_write_addtid_b32 v76 offset:3072\nM 116,168,132\nR 164,191,32768\n"
"v_mfma_f32_16x16x32_bf16 v[48:51], v[244:247], v[210:213], v[48:51]\nds_write_addtid_b32 v77 offset:3328\nds_write_addtid_b32 v78 offset:3584\nM 116,172,148\nds_write_addtid_b32 v79 offset:3840\nM 100,168,128\n"
"R 166,191,36864\nv_mfma_f32_16x16x32_bf16 v[52:55], v[238:241], v[210:213], v[52:55]\nds_write_addtid_b32 v80 offset:4096\nds_write_addtid_b32 v81 offset:4352\nM 100,172,144\nds_write_addtid_b32 v82 offset:4608\n"
"G 15\nM 104,176,128\nR 168,188,40960\nv_mfma_f32_16x16x32_bf16 v[56:59], v[240:243], v[210:213], v[56:59]\nds_write_addtid_b32 v83 offset:4864\nds_write_addtid_b32 v84 offset:5120\n"
"M 104,180,144\nds_write_addtid_b32 v85 offset:5376\nM 120,176,132\nR 170,188,45056\nv_mfma_f32_16x16x32_bf16 v[60:63], v[242:245], v[210:213], v[60:63]\nds_write_addtid_b32 v86 offset:5632\n"
"ds_write_addtid_b32 v87 offset:5888\nM 120,180,148\nds_write_addtid_b32 v88 offset:6144\nM 136,176,136\nR 172,188,49152\nM 136,180,152\n"
"ds_write_addtid_b32 v89 offset:6400\nM 152,176,140\nR 174,188,53248\nds_write_addtid_b32 v90 offset:6656\nM 152,180,156\nds_write_addtid_b32 v91 offset:6912\n"
"ds_write_addtid_b32 v92 offset:7168\nG 15\nM 156,160,140\nR 176,189,40960\nds_write_addtid_b32 v93 offset:7424\nM 156,164,156\n"
"ds_write_addtid_b32 v94 offset:7680\nds_write_addtid_b32 v95 offset:7936\nM 140,160,136\nR 178,189,45056\ns_add_u32 m0, s50, 40960\ns_add_u32 s63, s55, 458752\n"
"M 140,164,152\nbuffer_load_dwordx4 v222, s[12:15], s63 offen lds\nM 124,160,132\nR 180,189,49152\ns_waitcnt vmcnt(8) lgkmcnt(2)\ns_barrier\n"
"M 124,164,148\nds_read2st64_b32 v[64:65], v225 offset0:0 offset1:1\nds_read2st64_b32 v[66:67], v225 offset0:2 offset1:3\nM 108,160,128\nR 182,189,53248\nds_read2st64_b32 v[68:69], v225 offset0:4 offset1:5\n"
"M 108,164,144\nds_read2st64_b32 v[70:71], v225 offset0:6 offset1:7\nds_read2st64_b32 v[72:73], v225 offset0:8 offset1:9\nM 96,168,160\nR 160,190,40960\nds_read2st64_b32 v[74:75], v225 offset0:10 offset1:11\n"
"M 96,172,176\nbuffer_load_dwordx4 v223, s[12:15], s63 offen offset:1024 lds\nds_read2st64_b32 v[76:77], v225 offset0:12 offset1:13\nM 112,168,164\nR 162,190,45056\nds_read2st64_b32 v[78:79], v225 offset0:14 offset1:15\n"
"M 112,172,180\ns_add_u32 m0, s50, 49152\nds_read2st64_b32 v[80:81], v225 offset0:16 offset1:17\nds_read2st64_b32 v[82:83], v225 offset0:18 offset1:19\nM 128,168,168\nR 164,190,49152\n"
"ds_read2st64_b32 v[84:85], v225 offset0:20 offset1:21\nG 14\nv_add_f32 v32, v32, v64\nM 128,172,184\ns_add_u32 s63, s55, 917504\nds_read2st64_b32 v[86:87], v225 offset0:22 offset1:23\n"
"ds_read2st64_b32 v[88:89], v225 offset0:24 offset1:25\nM 144,168,172\nR 166,190,53248\nds_read2st64_b32 v[90:91], v225 offset0:26 offset1:27\nv_add_f32 v33, v33, v65\nM 144,172,188\n"
"ds_read2st64_b32 v[92:93], v225 offset0:28 offset1:29\nds_read2st64_b32 v[94:95], v225 offset0:30 offset1:31\nG 15\nM 148,176,172\nR 168,191,40960\nv_add_f32 v34, v34, v66\n"
"v_add_f32 v35, v35, v67\nM 148,180,188\nbuffer_load_dwordx4 v222, s[12:15], s63 offen lds\nv_add_f32 v36, v36, v68\nM 132,176,168\nR 170,191,45056\n"
"v_add_f32 v37, v37, v69\nv_add_f32 v38, v38, v70\nM 132,180,184\nv_add_f32 v39, v39, v71\nv_add_f32 v40, v40, v72\nM 116,176,164\n"
"R 172,191,49152\nv_add_f32 v41, v41, v73\nM 116,180,180\nG 15\nv_add_f32 v42, v42, v74\nv_add_f32 v43, v43, v75\n"
"M 100,176,160\nR 174,191,53248\nv_add_f32 v44, v44, v76\nM 100,180,176\nG 7\nv_add_f32 v45, v45, v77\n"
"v_add_f32 v46, v46, v78\nM 104,160,160\nR 176,184,0\nv_add_f32 v47, v47, v79\nM 104,164,176\nbuffer_load_dwordx4 v223, s[12:15], s63 offen offset:1024 lds\n"
"M 120,160,164\nR 178,184,4096\nv_add_f32 v48, v48, v80\nM 120,164,180\ns_add_u32 m0, s50, 57344\nv_add_f32 v49, v49, v81\n"
"v_add_f32 v50, v50, v82\nM 136,160,168\nR 180,184,8192\nv_add_f32 v51, v51, v83\nM 136,164,184\ns_add_u32 s63, s55, 1376256\n"
"v_add_f32 v52, v52, v84\nv_add_f32 v53, v53, v85\nM 152,160,172\nR 182,184,12288\nv_add_f32 v54, v54, v86\nM 152,164,188\n"
"G 4\nv_add_f32 v55, v55, v87\nv_add_f32 v56, v56, v88\nM 156,168,172\nR 160,185,0\nv_add_f32 v57, v57, v89\n"
"M 156,172,188\nbuffer_load_dwordx4 v222, s[12:15], s63 offen lds\nM 140,168,168\nR 162,185,4096\nv_add_f32 v58, v58, v90\nM 140,172,184\n"
"v_add_f32 v59, v59, v91\nv_add_f32 v60, v60, v92\nM 124,168,164\nR 164,185,8192\nv_add_f32 v61, v61, v93\nM 124,172,180\n"
"v_add_f32 v62, v62, v94\nv_add_f32 v63, v63, v95\nM 108,168,160\nR 166,185,12288\nv_mul_f32 v230, v32, v32\nv_mul_f32 v231, v33, v33\n"
"M 108,172,176\ns_add_u32 s84, s73, 23068672\ns_add_u32 s85, s73, 23183360\nG 4\nv_mul_f32 v232, v34, v34\nM 96,176,192\n"
"R 168,186,0\nv_mul_f32 v233, v35, v35\nv_fma_f32 v230, v230, v215, v214\nM 96,180,208\nv_fma_f32 v231, v231, v215, v214\nv_fma_f32 v232, v232, v215, v214\n"
"v_fma_f32 v233, v233, v215, v214\nM 112,176,196\nR 170,186,4096\ns_lshl2_add_u32 s60, s45, s40\ns_waitcnt vmcnt(5)\nv_lshlrev_b32 v224, 3, v252\n"
"s_mul_i32 s60, s60, 7168\ns_bfm_b64 exec, 14, 14\nbuffer_load_dword v224, v224, s[16:19], s60 offen\ns_mov_b64 exec, -1\ns_barrier\nv_mul_f32 v230, v32, v230\n"
"M 112,180,212\nv_exp_f32 v230, v230\nv_mul_f32 v231, v33, v231\nM 128,176,200\nR 172,186,8192\nv_mul_f32 v232, v34, v232\n"
"v_mul_f32 v233, v35, v233\nM 128,180,216\nv_exp_f32 v231, v231\nv_exp_f32 v232, v232\nM 144,176,204\nR 174,186,12288\n"
"v_fma_f32 v230, v230, v216, v216\nv_exp_f32 v233, v233\nM 144,180,220\nG 4\nv_rcp_f32 v230, v230\nv_fma_f32 v231, v231, v216, v216\n"
"M 148,160,204\nR 176,187,0\nv_fma_f32 v232, v232, v216, v216\nv_fma_f32 v233, v233, v216, v216\nM 148,164,220\nbuffer_load_dwordx4 v223, s[12:15], s63 offen offset:1024 lds\n"
"v_rcp_f32 v231, v231\nM 132,160,200\nR 178,187,4096\nv_mul_f32 v230, v32, v230\nv_rcp_f32 v232, v232\nM 132,164,216\n"
"s_add_u32 m0, s50, 65536\nv_rcp_f32 v233, v233\nv_mul_f32 v231, v33, v231\nM 116,160,196\nR 180,187,8192\nv_mul_f32 v232, v34, v232\n"
"v_mul_f32 v233, v35, v233\nM 116,164,212\ns_add_u32 s63, s55, 1835008\nv_cvt_pk_bf16_f32 v32, v230, v231\nv_cvt_pk_bf16_f32 v33, v232, v233\nv_mul_f32 v234, v36, v36\n"
"M 100,160,192\nR 182,187,12288\nv_mul_f32 v235, v37, v37\nv_mul_f32 v236, v38, v38\nM 100,164,208\nG 4\n"
"v_mul_f32 v237, v39, v39\nv_fma_f32 v234, v234, v215, v214\nv_fma_f32 v235, v235, v215, v214\nM 104,168,192\nR 160,184,16384\nv_fma_f32 v236, v236, v215, v214\n"
"v_fma_f32 v237, v237, v215, v214\nM 104,172,208\nbuffer_load_dwordx4 v222, s[12:15], s63 offen lds\nv_mul_f32 v234, v36, v234\nM 120,168,196\nR 162,184,20480\n"
"v_mul_f32 v235, v37, v235\nv_mul_f32 v236, v38, v236\nM 120,172,212\nv_exp_f32 v234, v234\nv_mul_f32 v237, v39, v237\nM 136,168,200\n"
"R 164,184,24576\nv_fma_f32 v234, v234, v216, v216\nv_exp_f32 v235, v235\nM 136,172,216\nv_exp_f32 v236, v236\nv_exp_f32 v237, v237\n"
"M 152,168,204\nR 166,184,28672\nv_fma_f32 v235, v235, v216, v216\nv_fma_f32 v236, v236, v216, v216\nM 152,172,220\nG 4\n"
"v_rcp_f32 v234, v234\nv_fma_f32 v237, v237, v216, v216\nM 156,176,204\nR 168,185,16384\nv_mul_f32 v234, v36, v234\nv_rcp_f32 v235, v235\n"
"M 156,180,220\nbuffer_load_dwordx4 v223, s[12:15], s63 offen offset:1024 lds\nv_rcp_f32 v236, v236\nM 140,176,200\nR 170,185,20480\nv_mul_f32 v235, v37, v235\n"
"v_rcp_f32 v237, v237\nM 140,180,216\ns_add_u32 m0, s50, 73728\nv_mul_f32 v236, v38, v236\nv_mul_f32 v237, v39, v237\nv_cvt_pk_bf16_f32 v34, v234, v235\n"
"M 124,176,196\nR 172,185,24576\nv_cvt_pk_bf16_f32 v35, v236, v237\nv_mul_f32 v230, v40, v40\nM 124,180,212\ns_add_u32 s63, s55, 2293760\n"
"v_mul_f32 v231, v41, v41\nv_mul_f32 v232, v42, v42\nv_mul_f32 v233, v43, v43\nM 108,176,192\nR 174,185,28672\nv_fma_f32 v230, v230, v215, v214\n"
"v_fma_f32 v231, v231, v215, v214\nM 108,180,208\nG 4\nv_fma_f32 v232, v232, v215, v214\nv_fma_f32 v233, v233, v215, v214\nv_mul_f32 v230, v40, v230\n"
"M 96,160,224\nR 176,186,16384\nv_mul_f32 v231, v41, v231\nv_mul_f32 v232, v42, v232\nM 96,164,240\nbuffer_load_dwordx4 v222, s[12:15], s63 offen lds\n"
"v_mul_f32 v233, v43, v233\nM 112,160,228\nR 178,186,20480\nv_mul_f32 v234, v44, v44\nv_exp_f32 v230, v230\nM 112,164,244\n"
"buffer_store_dwordx4 v[32:35], v252, s[20:23], s84 offen offset:0 sc1\nv_exp_f32 v231, v231\nM 128,160,232\nR 180,186,24576\nv_fma_f32 v230, v230, v216, v216\nv_exp_f32 v232, v232\n"
"M 128,164,248\nv_exp_f32 v233, v233\nv_fma_f32 v231, v231, v216, v216\nM 144,160,236\nR 182,186,28672\nv_fma_f32 v232, v232, v216, v216\n"
"v_fma_f32 v233, v233, v216, v216\nM 144,164,252\nG 4\nv_rcp_f32 v230, v230\nv_rcp_f32 v231, v231\nM 148,168,236\n"
"R 160,187,16384\nv_mul_f32 v230, v40, v230\nv_rcp_f32 v232, v232\nM 148,172,252\nbuffer_load_dwordx4 v223, s[12:15], s63 offen offset:1024 lds\nv_rcp_f32 v233, v233\n"
"M 132,168,232\nR 162,187,20480\nv_mul_f32 v231, v41, v231\nv_mul_f32 v232, v42, v232\nM 132,172,248\ns_add_u32 m0, s50, 81920\n"
"v_mul_f32 v233, v43, v233\nv_cvt_pk_bf16_f32 v40, v230, v231\nLpg10:\ns_cbranch_execz Lpg14\nv_cvt_pk_bf16_f32 v41, v232, v233\nM 116,168,228\n"
"R 164,187,24576\nv_mul_f32 v235, v45, v45\nv_mul_f32 v236, v46, v46\nM 116,172,244\ns_add_u32 s63, s55, 2752512\nv_mul_f32 v237, v47, v47\n"
"v_fma_f32 v234, v234, v215, v214\nv_fma_f32 v235, v235, v215, v214\nM 100,168,224\nR 166,187,28672\nv_fma_f32 v236, v236, v215, v214\nv_fma_f32 v237, v237, v215, v214\n"
"M 100,172,240\nG 4\nv_mul_f32 v234, v44, v234\nv_mul_f32 v235, v45, v235\nv_mul_f32 v236, v46, v236\nM 104,176,224\n"
"R 168,184,32768\nv_mul_f32 v237, v47, v237\nv_exp_f32 v234, v234\nM 104,180,240\nbuffer_load_dwordx4 v222, s[12:15], s63 offen lds\nv_exp_f32 v235, v235\n"
"M 120,176,228\nR 170,184,36864\nv_fma_f32 v234, v234, v216, v216\nv_exp_f32 v236, v236\nM 120,180,244\nbuffer_load_dwordx4 v223, s[12:15], s63 offen offset:1024 lds\n"
"v_exp_f32 v237, v237\nM 136,176,232\nR 172,186,32768\nv_fma_f32 v235, v235, v216, v216\nv_fma_f32 v236, v236, v216, v216\nM 136,180,248\n"
"s_add_u32 m0, s50, 90112\nv_rcp_f32 v234, v234\nv_fma_f32 v237, v237, v216, v216\nM 152,176,236\nR 174,186,36864\ns_add_u32 s63, s55, 3211264\n"
"v_rcp_f32 v235, v235\nM 152,180,252\nG 4\nv_rcp_f32 v236, v236\nv_rcp_f32 v237, v237\nM 156,160,236\n"
"R 176,185,32768\ns_mov_b32 s60, 157696\nv_bfe_u32 v246, v252, 4, 4\nv_lshl_add_u32 v246, v246, 3, s60\nv_mul_f32 v234, v44, v234\nM 156,164,252\n"
"ds_read_b64 v[238:239], v246 offset:0\nv_mul_f32 v235, v45, v235\nv_mul_f32 v236, v46, v236\nM 140,160,232\nR 178,185,36864\nds_read_b64 v[240:241], v246 offset:128\n"
"v_mul_f32 v237, v47, v237\nM 140,164,248\nds_read_b64 v[242:243], v246 offset:256\nbuffer_load_dwordx4 v222, s[12:15], s63 offen lds\nv_cvt_pk_bf16_f32 v42, v234, v235\nM 124,160,228\n"
"R 180,187,32768\nds_read_b64 v[244:245], v246 offset:384\nv_cvt_pk_bf16_f32 v43, v236, v237\nM 124,164,244\nds_read_b64 v[246:247], v246 offset:0\nv_mul_f32 v230, v48, v48\n"
"v_mul_f32 v231, v49, v49\nM 108,160,224\nR 182,187,36864\nv_mul_f32 v232, v50, v50\nv_mul_f32 v233, v51, v51\nM 108,164,240\n"
"s_waitcnt vmcnt(11) lgkmcnt(1)\ns_barrier\nv_fma_f32 v230, v230, v218, v217\nv_mfma_f32_16x16x32_bf16 v[0:3], v[168:171], a[0:3], 0\nR 160,184,40960\nbuffer_load_dwordx4 v223, s[12:15], s63 offen offset:1024 lds\n"
"v_fma_f32 v231, v231, v218, v217\nM 0,172,16\ns_mov_b32 m0, s51\nv_fma_f32 v232, v232, v218, v217\nv_fma_f32 v233, v233, v218, v217\nv_mul_f32 v230, v48, v230\n"
"v_mfma_f32_16x16x32_bf16 v[16:19], v[168:171], a[4:7], 0\nR 162,184,45056\nv_mul_f32 v231, v49, v231\nv_mul_f32 v232, v50, v232\nM 16,172,20\nbuffer_store_dwordx4 v[40:43], v252, s[20:23], s84 offen offset:1024 sc1\n"
"v_mul_f32 v233, v51, v233\nv_mfma_f32_16x16x32_bf16 v[8:11], v[168:171], a[8:11], 0\nR 164,186,40960\nv_mul_f32 v234, v52, v52\nv_exp_f32 v230, v230\nM 8,172,24\n"
"v_exp_f32 v231, v231\nv_exp_f32 v232, v232\nv_mfma_f32_16x16x32_bf16 v[24:27], v[168:171], a[12:15], 0\nR 166,186,45056\nv_mfma_f32_16x16x32_bf16 v[96:99], v[238:241], v[208:211], v[96:99]\nv_exp_f32 v233, v233\n"
"v_fma_f32 v230, v230, v219, v219\nv_fma_f32 v231, v231, v219, v219\nv_fma_f32 v232, v232, v219, v219\nM 24,172,28\nG 4\nv_rcp_f32 v230, v230\n"
"v_fma_f32 v233, v233, v219, v219\nv_mfma_f32_16x16x32_bf16 v[28:31], v[176:179], a[12:15], 0\nR 168,185,40960\nv_mfma_f32_16x16x32_bf16 v[100:103], v[240:243], v[208:211], v[100:103]\nv_rcp_f32 v231, v231\nv_rcp_f32 v232, v232\n"
"v_rcp_f32 v233, v233\nM 28,180,28\nv_mul_f32 v230, v48, v230\nv_mul_f32 v231, v49, v231\nv_mul_f32 v232, v50, v232\nv_mfma_f32_16x16x32_bf16 v[12:15], v[176:179], a[8:11], 0\n"
"R 170,185,45056\nv_mul_f32 v233, v51, v233\nv_mfma_f32_16x16x32_bf16 v[104:107], v[242:245], v[208:211], v[104:107]\nv_cvt_pk_bf16_f32 v48, v230, v231\nv_cvt_pk_bf16_f32 v49, v232, v233\nv_mul_f32 v235, v53, v53\n"
"v_mul_f32 v236, v54, v54\nM 12,180,24\nv_mul_f32 v237, v55, v55\nv_fma_f32 v234, v234, v218, v217\nv_fma_f32 v235, v235, v218, v217\nv_mfma_f32_16x16x32_bf16 v[20:23], v[176:179], a[4:7], 0\n"
"R 172,187,40960\nv_fma_f32 v236, v236, v218, v217\nv_mfma_f32_16x16x32_bf16 v[108:111], v[244:247], v[208:211], v[108:111]\nv_fma_f32 v237, v237, v218, v217\nv_mul_f32 v234, v52, v234\nv_mul_f32 v235, v53, v235\n"
"v_mul_f32 v236, v54, v236\nM 20,180,20\nds_write_addtid_b32 v128 offset:0\nv_exp_f32 v234, v234\nv_mul_f32 v237, v55, v237\nv_mfma_f32_16x16x32_bf16 v[4:7], v[176:179], a[0:3], 0\n"
"R 174,187,45056\nv_mfma_f32_16x16x32_bf16 v[112:115], v[244:247], v[210:213], v[112:115]\nds_write_addtid_b32 v129 offset:256\nv_exp_f32 v235, v235\nv_exp_f32 v236, v236\nv_exp_f32 v237, v237\n"
"M 4,180,16\nds_write_addtid_b32 v130 offset:512\nG 7\nv_fma_f32 v234, v234, v219, v219\nv_fma_f32 v235, v235, v219, v219\nv_fma_f32 v236, v236, v219, v219\n"
"M 0,160,32\nR 176,184,49152\nv_mfma_f32_16x16x32_bf16 v[116:119], v[238:241], v[210:213], v[116:119]\nds_write_addtid_b32 v131 offset:768\nv_rcp_f32 v234, v234\nv_rcp_f32 v235, v235\n"
"v_fma_f32 v237, v237, v219, v219\nM 0,164,48\nds_write_addtid_b32 v132 offset:1024\nv_rcp_f32 v236, v236\nv_rcp_f32 v237, v237\nM 16,160,36\n"
"R 178,184,53248\nv_mul_f32 v234, v52, v234\nv_mfma_f32_16x16x32_bf16 v[120:123], v[240:243], v[210:213], v[120:123]\nds_write_addtid_b32 v133 offset:1280\nv_mul_f32 v235, v53, v235\nv_mul_f32 v236, v54, v236\n"
"v_mul_f32 v237, v55, v237\nv_cvt_pk_bf16_f32 v50, v234, v235\nM 16,164,52\nds_write_addtid_b32 v134 offset:1536\nv_cvt_pk_bf16_f32 v51, v236, v237\nv_mul_f32 v230, v56, v56\n"
"v_mul_f32 v231, v57, v57\nM 8,160,40\nR 180,186,49152\nv_mul_f32 v232, v58, v58\nv_mfma_f32_16x16x32_bf16 v[124:127], v[242:245], v[210:213], v[124:127]\nbuffer_store_dwordx4 v[48:51], v252, s[20:23], s85 offen offset:0 sc1\n"
"ds_write_addtid_b32 v135 offset:1792\nv_mul_f32 v233, v59, v59\nM 8,164,56\nds_write_addtid_b32 v136 offset:2048\nv_fma_f32 v230, v230, v218, v217\nv_fma_f32 v231, v231, v218, v217\n"
"v_fma_f32 v232, v232, v218, v217\nM 24,160,44\nR 182,186,53248\nds_write_addtid_b32 v137 offset:2304\nv_fma_f32 v233, v233, v218, v217\nv_mul_f32 v230, v56, v230\n"
"M 24,164,60\nds_write_addtid_b32 v138 offset:2560\nG 14\nv_exp_f32 v230, v230\nv_mul_f32 v231, v57, v231\nM 28,168,44\n"
"R 160,185,49152\nds_write_addtid_b32 v139 offset:2816\nv_mul_f32 v232, v58, v232\nv_mul_f32 v233, v59, v233\nM 28,172,60\nds_write_addtid_b32 v140 offset:3072\n"
"ds_write_addtid_b32 v141 offset:3328\nds_write_addtid_b32 v142 offset:3584\nds_write_addtid_b32 v143 offset:3840\nds_write_addtid_b32 v144 offset:4096\nds_write_addtid_b32 v145 offset:4352\nds_write_addtid_b32 v146 offset:4608\n"
"ds_write_addtid_b32 v147 offset:4864\nds_write_addtid_b32 v148 offset:5120\nds_write_addtid_b32 v149 offset:5376\nds_write_addtid_b32 v150 offset:5632\nds_write_addtid_b32 v151 offset:5888\nds_write_addtid_b32 v152 offset:6144\n"
"ds_write_addtid_b32 v153 offset:6400\nds_write_addtid_b32 v154 offset:6656\nds_write_addtid_b32 v155 offset:6912\nds_write_addtid_b32 v156 offset:7168\nds_write_addtid_b32 v157 offset:7424\nds_write_addtid_b32 v158 offset:7680\n"
"ds_write_addtid_b32 v159 offset:7936\nv_exp_f32 v231, v231\nv_exp_f32 v232, v232\nM 12,168,40\nR 162,185,53248\ns_add_u32 m0, s50, 98304\n"
"s_add_u32 s63, s54, 256\nv_exp_f32 v233, v233\nM 12,172,56\nbuffer_load_dwordx4 v220, s[12:15], s63 offen lds\nv_fma_f32 v230, v230, v219, v219\nM 20,168,36\n"
"R 164,187,49152\ns_waitcnt vmcnt(5) lgkmcnt(2)\ns_barrier\nv_fma_f32 v231, v231, v219, v219\nM 20,172,52\nds_read2st64_b32 v[128:129], v225 offset0:0 offset1:1\n"
"v_rcp_f32 v230, v230\nv_fma_f32 v232, v232, v219, v219\nM 4,168,32\nR 166,187,53248\nds_read2st64_b32 v[130:131], v225 offset0:2 offset1:3\nv_fma_f32 v233, v233, v219, v219\n"
"v_rcp_f32 v231, v231\nM 4,172,48\nds_read2st64_b32 v[132:133], v225 offset0:4 offset1:5\nv_rcp_f32 v232, v232\nv_rcp_f32 v233, v233\nM 0,176,64\n"
"R 168,184,57344\nds_read2st64_b32 v[134:135], v225 offset0:6 offset1:7\nv_mul_f32 v230, v56, v230\nv_mul_f32 v231, v57, v231\nM 0,180,80\nbuffer_load_dwordx4 v221, s[12:15], s63 offen offset:1024 lds\n"
"ds_read2st64_b32 v[136:137], v225 offset0:8 offset1:9\nv_mul_f32 v232, v58, v232\nM 16,176,68\nR 170,184,61440\ns_add_u32 m0, s50, 106496\ns_add_u32 s63, s54, 229632\n"
"ds_read2st64_b32 v[138:139], v225 offset0:10 offset1:11\nv_mul_f32 v233, v59, v233\nM 16,180,84\nbuffer_load_dwordx4 v220, s[12:15], s63 offen lds\nds_read2st64_b32 v[140:141], v225 offset0:12 offset1:13\nv_cvt_pk_bf16_f32 v56, v230, v231\n"
"M 8,176,72\nR 172,186,57344\nds_read2st64_b32 v[142:143], v225 offset0:14 offset1:15\nv_cvt_pk_bf16_f32 v57, v232, v233\nv_mul_f32 v234, v60, v60\nM 8,180,88\n"
"ds_read2st64_b32 v[144:145], v225 offset0:16 offset1:17\nv_mul_f32 v235, v61, v61\nv_mul_f32 v236, v62, v62\nv_mul_f32 v237, v63, v63\nM 24,176,76\nR 174,186,61440\n"
"ds_read2st64_b32 v[146:147], v225 offset0:18 offset1:19\nv_fma_f32 v234, v234, v218, v217\nv_fma_f32 v235, v235, v218, v217\nM 24,180,92\nds_read2st64_b32 v[148:149], v225 offset0:20 offset1:21\nG 14\n"
"v_fma_f32 v236, v236, v218, v217\nv_fma_f32 v237, v237, v218, v217\nv_mul_f32 v234, v60, v234\nM 28,160,76\nR 176,185,57344\nds_read2st64_b32 v[150:151], v225 offset0:22 offset1:23\n"
"v_mul_f32 v235, v61, v235\nv_mul_f32 v236, v62, v236\nM 28,164,92\nbuffer_load_dwordx4 v221, s[12:15], s63 offen offset:1024 lds\nds_read2st64_b32 v[152:153], v225 offset0:24 offset1:25\nv_mul_f32 v237, v63, v237\n"
"M 12,160,72\nR 178,185,61440\ns_add_u32 s63, s54, 459008\nds_read2st64_b32 v[154:155], v225 offset0:26 offset1:27\nv_exp_f32 v234, v234\nM 12,164,88\n"
"s_add_u32 m0, s50, 114688\nds_read2st64_b32 v[156:157], v225 offset0:28 offset1:29\nds_read2st64_b32 v[158:159], v225 offset0:30 offset1:31\nv_exp_f32 v235, v235\nM 20,160,68\nR 180,187,57344\n"
"v_add_f32 v96, v96, v128\nv_add_f32 v97, v97, v129\nv_exp_f32 v236, v236\nM 20,164,84\nbuffer_load_dwordx4 v220, s[12:15], s63 offen lds\nG 15\n"
"v_add_f32 v98, v98, v130\nv_exp_f32 v237, v237\nM 4,160,64\nR 182,187,61440\nv_add_f32 v99, v99, v131\nv_add_f32 v100, v100, v132\n"
"v_fma_f32 v234, v234, v219, v219\nM 4,164,80\nG 11\nv_add_f32 v101, v101, v133\nv_add_f32 v102, v102, v134\nv_add_f32 v103, v103, v135\n"
"v_fma_f32 v235, v235, v219, v219\nM 0,168,96\nR 160,188,0\nv_add_f32 v104, v104, v136\nv_add_f32 v105, v105, v137\nM 0,172,112\n"
"buffer_load_dwordx4 v221, s[12:15], s63 offen offset:1024 lds\nv_add_f32 v106, v106, v138\nM 16,168,100\nR 162,188,4096\nv_add_f32 v107, v107, v139\nv_add_f32 v108, v108, v140\n"
"M 16,172,116\ns_add_u32 m0, s50, 0\nv_add_f32 v109, v109, v141\nv_add_f32 v110, v110, v142\nv_add_f32 v111, v111, v143\nM 8,168,104\n"
"R 164,190,0\nv_add_f32 v112, v112, v144\nv_add_f32 v113, v113, v145\nM 8,172,120\ns_add_u32 s63, s54, 688384\nG 13\n"
"v_add_f32 v114, v114, v146\nv_add_f32 v115, v115, v147\nG 12\nv_add_f32 v116, v116, v148\nM 24,168,108\nR 166,190,4096\n"
"v_add_f32 v117, v117, v149\nG 11\nv_add_f32 v118, v118, v150\nM 24,172,124\nG 4\nv_add_f32 v119, v119, v151\n"
"v_add_f32 v120, v120, v152\nv_add_f32 v121, v121, v153\nM 28,176,108\nR 168,189,0\nv_add_f32 v122, v122, v154\nv_add_f32 v123, v123, v155\n"
"M 28,180,124\nbuffer_load_dwordx4 v220, s[12:15], s63 offen lds\nM 12,176,104\nR 170,189,4096\nv_add_f32 v124, v124, v156\nM 12,180,120\n"
"v_add_f32 v125, v125, v157\nv_add_f32 v126, v126, v158\nM 20,176,100\nR 172,191,0\nv_add_f32 v127, v127, v159\nM 20,180,116\n"
"buffer_load_dwordx4 v221, s[12:15], s63 offen offset:1024 lds\nM 4,176,96\nR 174,191,4096\ns_add_u32 m0, s50, 8192\nM 4,180,112\nG 4\n"
"v_rcp_f32 v234, v234\nM 0,160,128\nR 176,188,8192\ns_add_u32 s63, s54, 917760\nM 0,164,144\nbuffer_load_dwordx4 v220, s[12:15], s63 offen lds\n"
"M 16,160,132\nR 178,188,12288\nv_fma_f32 v236, v236, v219, v219\nM 16,164,148\nv_rcp_f32 v235, v235\nM 8,160,136\n"
"R 180,190,8192\nLq575:\ns_lshr_b32 s60, s45, 3\ns_lshl2_add_u32 s60, s60, s40\ns_lshl_b32 s60, s60, 8\nv_lshl_add_u32 v224, v252, 3, s60\n"
"v_add_u32 v224, Lq575+4096-LqPC, v224\nv_min_u32 v224, LqE-LqPC-4, v224\ns_mov_b64 exec, 3\nglobal_load_dword v224, v224, s[98:99]\ns_mov_b64 exec, -1\nM 8,164,152\n"
"v_rcp_f32 v236, v236\nM 24,160,140\nR 182,190,12288\nv_fma_f32 v237, v237, v219, v219\nM 24,164,156\nG 4\n"
"v_rcp_f32 v237, v237\nM 28,168,140\nR 160,189,8192\nv_mul_f32 v234, v60, v234\nM 28,172,156\nbuffer_load_dwordx4 v221, s[12:15], s63 offen offset:1024 lds\n"
"M 12,168,136\nR 162,189,12288\nv_mul_f32 v235, v61, v235\nM 12,172,152\ns_add_u32 m0, s50, 16384\nv_mul_f32 v236, v62, v236\n"
"v_mul_f32 v237, v63, v237\nM 20,168,132\nR 164,191,8192\nv_cvt_pk_bf16_f32 v58, v234, v235\nM 20,172,148\ns_add_u32 s63, s54, 1147136\n"
"v_cvt_pk_bf16_f32 v59, v236, v237\nM 4,168,128\nR 166,191,12288\nM 4,172,144\nG 4\nM 0,176,160\n"
"R 168,188,16384\nM 0,180,176\nbuffer_load_dwordx4 v220, s[12:15], s63 offen lds\nM 16,176,164\nR 170,188,20480\nM 16,180,180\n"
"buffer_store_dwordx4 v[56:59], v252, s[20:23], s85 offen offset:1024 sc1\nM 8,176,168\nR 172,190,16384\nM 8,180,184\nbuffer_load_dwordx4 v221, s[12:15], s63 offen offset:1024 lds\nM 24,176,172\n"
"R 174,190,20480\ns_add_u32 m0, s50, 24576\nM 24,180,188\ns_add_u32 s63, s54, 1376512\nG 4\nM 28,160,172\n"
"R 176,189,16384\ns_waitcnt vmcnt(10)\ns_barrier\nv_mul_f32 v230, v96, v96\nM 28,164,188\nbuffer_load_dwordx4 v220, s[12:15], s63 offen lds\n"
"v_mul_f32 v231, v97, v97\nM 12,160,168\nR 178,189,20480\nv_mul_f32 v232, v98, v98\nv_mul_f32 v233, v99, v99\nM 12,164,184\n"
"s_add_u32 s84, s73, 23070720\ns_add_u32 s85, s73, 23185408\nv_fma_f32 v230, v230, v215, v214\nv_fma_f32 v231, v231, v215, v214\nM 20,160,164\nR 180,191,16384\n"
"v_fma_f32 v232, v232, v215, v214\nv_fma_f32 v233, v233, v215, v214\nM 20,164,180\nbuffer_load_dwordx4 v221, s[12:15], s63 offen offset:1024 lds\nv_mul_f32 v230, v96, v230\nM 4,160,160\n"
"R 182,191,20480\ns_add_u32 m0, s50, 32768\nv_mul_f32 v231, v97, v231\nM 4,164,176\nG 4\nv_exp_f32 v230, v230\n"
"v_mul_f32 v232, v98, v232\nM 0,168,192\nR 160,188,24576\ns_add_u32 s63, s54, 1605888\nv_mul_f32 v233, v99, v233\nM 0,172,208\n"
"buffer_load_dwordx4 v220, s[12:15], s63 offen lds\nv_exp_f32 v231, v231\nM 16,168,196\nR 162,188,28672\nv_fma_f32 v230, v230, v216, v216\nv_exp_f32 v232, v232\n"
"M 16,172,212\nv_exp_f32 v233, v233\nv_fma_f32 v231, v231, v216, v216\nM 8,168,200\nR 164,190,24576\nv_fma_f32 v232, v232, v216, v216\n"
"v_fma_f32 v233, v233, v216, v216\nM 8,172,216\nv_rcp_f32 v230, v230\nv_rcp_f32 v231, v231\nM 24,168,204\nR 166,190,28672\n"
"v_mul_f32 v230, v96, v230\nv_rcp_f32 v232, v232\nM 24,172,220\nG 4\nv_rcp_f32 v233, v233\nv_mul_f32 v231, v97, v231\n"
"M 28,176,204\nR 168,189,24576\nv_mul_f32 v232, v98, v232\nv_mul_f32 v233, v99, v233\nM 28,180,220\nbuffer_load_dwordx4 v221, s[12:15], s63 offen offset:1024 lds\n"
"v_cvt_pk_bf16_f32 v96, v230, v231\nM 12,176,200\nR 170,189,28672\ns_add_u32 s63, s54, 1835264\nv_cvt_pk_bf16_f32 v97, v232, v233\nM 12,180,216\n"
"s_add_u32 m0, s50, 40960\nv_mul_f32 v234, v100, v100\nv_mul_f32 v235, v101, v101\nv_mul_f32 v236, v102, v102\nM 20,176,196\nR 172,191,24576\n"
"v_mul_f32 v237, v103, v103\nv_fma_f32 v234, v234, v215, v214\nM 20,180,212\nbuffer_load_dwordx4 v220, s[12:15], s63 offen lds\nv_fma_f32 v235, v235, v215, v214\nM 4,176,192\n"
"R 174,191,28672\nv_fma_f32 v236, v236, v215, v214\nv_fma_f32 v237, v237, v215, v214\nM 4,180,208\nG 4\nv_mul_f32 v234, v100, v234\n"
"v_mul_f32 v235, v101, v235\nv_mul_f32 v236, v102, v236\nM 0,160,224\nR 176,188,32768\nv_mul_f32 v237, v103, v237\nv_exp_f32 v234, v234\n"
"M 0,164,240\nbuffer_load_dwordx4 v221, s[12:15], s63 offen offset:1024 lds\nv_exp_f32 v235, v235\nM 16,160,228\nR 178,188,36864\ns_add_u32 s63, s54, 2064640\n"
"v_exp_f32 v236, v236\nM 16,164,244\ns_add_u32 m0, s50, 49152\nv_exp_f32 v237, v237\nv_fma_f32 v234, v234, v216, v216\nM 8,160,232\n"
"R 180,188,40960\nv_fma_f32 v235, v235, v216, v216\nv_fma_f32 v236, v236, v216, v216\nM 8,164,248\nbuffer_load_dwordx4 v220, s[12:15], s63 offen lds\nv_fma_f32 v237, v237, v216, v216\n"
"M 24,160,236\nR 182,188,45056\ns_mov_b32 s60, 158720\nv_bfe_u32 v242, v252, 4, 4\nv_lshl_add_u32 v242, v242, 3, s60\nv_rcp_f32 v234, v234\n"
"M 24,164,252\nds_read_b64 v[238:239], v242 offset:0\nG 5\nv_rcp_f32 v235, v235\nM 28,168,236\nR 160,189,32768\n"
"ds_read_b64 v[240:241], v242 offset:128\nv_rcp_f32 v236, v236\nM 28,172,252\nds_read_b64 v[242:243], v242 offset:0\nv_mul_f32 v234, v100, v234\nv_rcp_f32 v237, v237\n"
"M 12,168,232\nR 162,189,36864\nv_mul_f32 v235, v101, v235\nv_mul_f32 v236, v102, v236\nM 12,172,248\nbuffer_load_dwordx4 v221, s[12:15], s63 offen offset:1024 lds\n"
"Lpg11:\ns_cbranch_execz Lpg15\nv_mul_f32 v237, v103, v237\nM 20,168,228\nR 164,189,40960\nv_cvt_pk_bf16_f32 v98, v234, v235\n"
"v_cvt_pk_bf16_f32 v99, v236, v237\nM 20,172,244\nv_mul_f32 v230, v104, v104\nv_mul_f32 v231, v105, v105\nv_mul_f32 v232, v106, v106\nM 4,168,224\n"
"R 166,189,45056\nv_mul_f32 v233, v107, v107\nv_fma_f32 v230, v230, v215, v214\nM 4,172,240\ns_waitcnt vmcnt(16) lgkmcnt(3)\ns_barrier\n"
"v_fma_f32 v231, v231, v215, v214\nv_mfma_f32_16x16x32_bf16 v[32:35], v[176:179], a[0:3], 0\nR 168,190,32768\nv_fma_f32 v232, v232, v215, v214\nv_fma_f32 v233, v233, v215, v214\nM 32,180,16\n"
"s_mov_b32 m0, s51\nv_mul_f32 v230, v104, v230\nv_mul_f32 v231, v105, v231\nv_mul_f32 v232, v106, v232\nv_mfma_f32_16x16x32_bf16 v[48:51], v[176:179], a[4:7], 0\nR 170,190,36864\n"
"v_mul_f32 v233, v107, v233\nv_exp_f32 v230, v230\nM 48,180,20\nbuffer_store_dwordx4 v[96:99], v252, s[20:23], s84 offen offset:0 sc1\nv_exp_f32 v231, v231\nv_mfma_f32_16x16x32_bf16 v[64:67], v[176:179], a[8:11], 0\n"
"R 172,190,40960\nv_mov_b32 v226, s46\nv_mfma_f32_16x16x32_bf16 v[0:3], v[238:241], v[208:211], v[0:3]\nv_exp_f32 v232, v232\nv_fma_f32 v230, v230, v216, v216\nv_exp_f32 v233, v233\n"
"M 64,180,24\nv_mov_b32 v227, s47\nv_fma_f32 v231, v231, v216, v216\nv_fma_f32 v232, v232, v216, v216\nv_mfma_f32_16x16x32_bf16 v[80:83], v[176:179], a[12:15], 0\nR 174,190,45056\n"
"v_fma_f32 v233, v233, v216, v216\nv_mfma_f32_16x16x32_bf16 v[4:7], v[240:243], v[208:211], v[4:7]\ns_add_u32 s61, s81, 385024\ns_lshl_b32 s62, s44, 5\ns_add_u32 s61, s61, s62\ns_waitcnt vmcnt(10)\n"
"s_mov_b64 exec, 1\nbuffer_atomic_swap_x2 v[226:227], off, s[20:23], s61 sc1\ns_mov_b64 exec, -1\nM 80,180,28\nds_write_addtid_b32 v8 offset:0\nG 5\n"
"v_rcp_f32 v230, v230\nv_rcp_f32 v231, v231\nv_mfma_f32_16x16x32_bf16 v[84:87], v[160:163], a[12:15], 0\nR 176,191,32768\nv_mfma_f32_16x16x32_bf16 v[16:19], v[240:243], v[210:213], v[16:19]\nds_write_addtid_b32 v9 offset:256\n"
"v_rcp_f32 v232, v232\nv_rcp_f32 v233, v233\nv_mul_f32 v230, v104, v230\nM 84,164,28\nds_write_addtid_b32 v10 offset:512\nv_mul_f32 v231, v105, v231\n"
"v_mul_f32 v232, v106, v232\nv_mul_f32 v233, v107, v233\nv_mfma_f32_16x16x32_bf16 v[68:71], v[160:163], a[8:11], 0\nR 178,191,36864\nv_cvt_pk_bf16_f32 v104, v230, v231\nv_mfma_f32_16x16x32_bf16 v[20:23], v[238:241], v[210:213], v[20:23]\n"
"ds_write_addtid_b32 v11 offset:768\nv_cvt_pk_bf16_f32 v105, v232, v233\nv_mul_f32 v234, v108, v108\nv_mul_f32 v235, v109, v109\nv_mul_f32 v236, v110, v110\nM 68,164,24\n"
"ds_write_addtid_b32 v12 offset:1024\nv_mul_f32 v237, v111, v111\nv_fma_f32 v234, v234, v215, v214\nv_fma_f32 v235, v235, v215, v214\nv_mfma_f32_16x16x32_bf16 v[52:55], v[160:163], a[4:7], 0\nR 180,191,40960\n"
"ds_write_addtid_b32 v13 offset:1280\nv_fma_f32 v236, v236, v215, v214\nv_fma_f32 v237, v237, v215, v214\nM 52,164,20\nds_write_addtid_b32 v14 offset:1536\nds_write_addtid_b32 v15 offset:1792\n"
"ds_write_addtid_b32 v24 offset:4096\nds_write_addtid_b32 v25 offset:4352\nds_write_addtid_b32 v26 offset:4608\nds_write_addtid_b32 v27 offset:4864\nds_write_addtid_b32 v28 offset:5120\nds_write_addtid_b32 v29 offset:5376\n"
"ds_write_addtid_b32 v30 offset:5632\nds_write_addtid_b32 v31 offset:5888\nv_mul_f32 v234, v108, v234\nv_mul_f32 v235, v109, v235\nv_mul_f32 v236, v110, v236\nv_mfma_f32_16x16x32_bf16 v[36:39], v[160:163], a[0:3], 0\n"
"R 182,191,45056\ns_add_u32 m0, s50, 57344\nv_mul_f32 v237, v111, v237\nM 36,164,16\ns_add_u32 s63, s54, 2294016\nG 15\n"
"v_mul_f32 v230, v112, v112\nv_exp_f32 v234, v234\nv_mfma_f32_16x16x32_bf16 v[40:43], v[168:171], a[0:3], 0\nR 160,188,49152\ns_waitcnt vmcnt(8) lgkmcnt(2)\ns_barrier\n"
"v_exp_f32 v235, v235\nM 40,172,16\nbuffer_load_dwordx4 v220, s[12:15], s63 offen lds\nds_read2st64_b32 v[8:9], v225 offset0:0 offset1:1\nv_exp_f32 v236, v236\nv_mfma_f32_16x16x32_bf16 v[56:59], v[168:171], a[4:7], 0\n"
"R 162,188,53248\nds_read2st64_b32 v[10:11], v225 offset0:2 offset1:3\nv_fma_f32 v234, v234, v216, v216\nv_exp_f32 v237, v237\nM 56,172,20\nds_read2st64_b32 v[12:13], v225 offset0:4 offset1:5\n"
"v_rcp_f32 v234, v234\nv_fma_f32 v235, v235, v216, v216\nv_mfma_f32_16x16x32_bf16 v[72:75], v[168:171], a[8:11], 0\nR 164,184,0\nds_read2st64_b32 v[14:15], v225 offset0:6 offset1:7\nv_fma_f32 v236, v236, v216, v216\n"
"v_fma_f32 v237, v237, v216, v216\nM 72,172,24\nds_read2st64_b32 v[24:25], v225 offset0:16 offset1:17\nv_rcp_f32 v235, v235\nv_rcp_f32 v236, v236\nv_mfma_f32_16x16x32_bf16 v[88:91], v[168:171], a[12:15], 0\n"
"R 166,184,4096\nds_read2st64_b32 v[26:27], v225 offset0:18 offset1:19\nv_mul_f32 v234, v108, v234\nv_rcp_f32 v237, v237\nM 88,172,28\nds_read2st64_b32 v[28:29], v225 offset0:20 offset1:21\n"
"G 11\nv_mul_f32 v235, v109, v235\nv_mul_f32 v236, v110, v236\nv_mul_f32 v237, v111, v237\nv_mfma_f32_16x16x32_bf16 v[92:95], v[176:179], a[12:15], 0\nR 168,189,49152\n"
"ds_read2st64_b32 v[30:31], v225 offset0:22 offset1:23\nv_cvt_pk_bf16_f32 v106, v234, v235\nv_cvt_pk_bf16_f32 v107, v236, v237\nM 92,180,28\nbuffer_load_dwordx4 v221, s[12:15], s63 offen offset:1024 lds\nG 11\n"
"v_add_f32 v0, v0, v8\nv_mul_f32 v231, v113, v113\nv_mfma_f32_16x16x32_bf16 v[76:79], v[176:179], a[8:11], 0\nR 170,189,53248\nv_mul_f32 v232, v114, v114\nv_add_f32 v1, v1, v9\n"
"v_mul_f32 v233, v115, v115\nM 76,180,24\nv_fma_f32 v230, v230, v218, v217\nv_fma_f32 v231, v231, v218, v217\nG 10\nv_add_f32 v2, v2, v10\n"
"v_fma_f32 v232, v232, v218, v217\nv_mfma_f32_16x16x32_bf16 v[60:63], v[176:179], a[4:7], 0\nR 172,185,0\nv_fma_f32 v233, v233, v218, v217\nv_add_f32 v3, v3, v11\nv_mul_f32 v230, v112, v230\n"
"M 60,180,20\nbuffer_store_dwordx4 v[104:107], v252, s[20:23], s84 offen offset:1024 sc1\nG 10\nv_add_f32 v4, v4, v12\nv_mul_f32 v231, v113, v231\nv_mfma_f32_16x16x32_bf16 v[44:47], v[176:179], a[0:3], 0\n"
"R 174,185,4096\nv_mul_f32 v232, v114, v232\nv_add_f32 v5, v5, v13\nv_mul_f32 v233, v115, v233\nM 44,180,16\nG 7\n"
"v_exp_f32 v230, v230\nv_add_f32 v6, v6, v14\nv_exp_f32 v231, v231\nM 32,160,32\nR 176,190,49152\nv_mov_b32 v248, 0\n"
"v_add_f32 v7, v7, v15\nv_exp_f32 v232, v232\nM 32,164,48\ns_add_u32 m0, s50, 65536\nv_mov_b32 v249, 0\nv_fma_f32 v230, v230, v219, v219\n"
"v_add_f32 v16, v16, v24\nv_exp_f32 v233, v233\nM 48,160,36\nR 178,190,53248\ns_add_u32 s61, s82, 385024\ns_mov_b64 exec, 0xf\n"
"buffer_atomic_or_x2 v[248:249], v250, s[20:23], s61 offen sc0 sc1\ns_mov_b64 exec, -1\nv_add_f32 v17, v17, v25\nv_fma_f32 v231, v231, v219, v219\nM 48,164,52\ns_add_u32 s63, s54, 2523392\n"
"G 8\nv_add_f32 v18, v18, v26\nv_add_f32 v19, v19, v27\nG 7\nv_add_f32 v20, v20, v28\nM 64,160,40\n"
"R 180,186,0\nv_add_f32 v21, v21, v29\nG 6\nv_add_f32 v22, v22, v30\nv_add_f32 v23, v23, v31\nM 64,164,56\n"
"buffer_load_dwordx4 v220, s[12:15], s63 offen lds\nM 80,160,44\nR 182,186,4096\nv_fma_f32 v232, v232, v219, v219\nM 80,164,60\nG 4\n"
"v_rcp_f32 v230, v230\nM 84,168,44\nR 160,191,49152\nv_fma_f32 v233, v233, v219, v219\nM 84,172,60\nv_rcp_f32 v231, v231\n"
"M 68,168,40\nR 162,191,53248\nv_mul_f32 v230, v112, v230\nM 68,172,56\nbuffer_load_dwordx4 v221, s[12:15], s63 offen offset:1024 lds\nM 52,168,36\n"
"R 164,187,0\ns_add_u32 s63, s54, 2752768\nM 52,172,52\ns_add_u32 m0, s50, 73728\nv_rcp_f32 v232, v232\nM 36,168,32\n"
"R 166,187,4096\nv_mul_f32 v231, v113, v231\nM 36,172,48\nG 4\nv_rcp_f32 v233, v233\nM 40,176,32\n"
"R 168,184,8192\nv_mul_f32 v232, v114, v232\nM 40,180,48\nbuffer_load_dwordx4 v220, s[12:15], s63 offen lds\nM 56,176,36\nR 170,184,12288\n"
"v_mul_f32 v233, v115, v233\nM 56,180,52\nv_cvt_pk_bf16_f32 v112, v230, v231\nv_cvt_pk_bf16_f32 v113, v232, v233\nM 72,176,40\nR 172,184,16384\n"
"v_mul_f32 v234, v116, v116\nM 72,180,56\nv_mul_f32 v235, v117, v117\nv_mul_f32 v236, v118, v118\nM 88,176,44\nR 174,184,20480\n"
"v_mul_f32 v237, v119, v119\nM 88,180,60\nG 4\nv_fma_f32 v234, v234, v218, v217\nv_fma_f32 v235, v235, v218, v217\nM 92,160,44\n"
"R 176,185,8192\nv_fma_f32 v236, v236, v218, v217\nM 92,164,60\nbuffer_load_dwordx4 v221, s[12:15], s63 offen offset:1024 lds\nM 76,160,40\nR 178,185,12288\n"
"v_fma_f32 v237, v237, v218, v217\nM 76,164,56\ns_add_u32 m0, s50, 81920\nv_mul_f32 v234, v116, v234\nv_mul_f32 v235, v117, v235\nM 60,160,36\n"
"R 180,185,16384\nv_mul_f32 v236, v118, v236\nM 60,164,52\ns_add_u32 s63, s54, 2982144\nv_exp_f32 v234, v234\nM 44,160,32\n"
"R 182,185,20480\nv_mul_f32 v237, v119, v237\nM 44,164,48\nG 4\nv_exp_f32 v235, v235\nM 32,168,64\n"
"R 160,186,8192\nv_fma_f32 v234, v234, v219, v219\nM 32,172,80\nbuffer_load_dwordx4 v220, s[12:15], s63 offen lds\nM 48,168,68\nR 162,186,12288\n"
"v_fma_f32 v235, v235, v219, v219\nM 48,172,84\nv_exp_f32 v236, v236\nM 64,168,72\nR 164,186,16384\nv_fma_f32 v236, v236, v219, v219\n"
"M 64,172,88\nbuffer_load_dwordx4 v221, s[12:15], s63 offen offset:1024 lds\nM 80,168,76\nR 166,186,20480\ns_add_u32 m0, s50, 90112\nM 80,172,92\n"
"G 4\nv_exp_f32 v237, v237\nM 84,176,76\nR 168,187,8192\ns_add_u32 s63, s54, 3211520\nM 84,180,92\n"
"buffer_load_dwordx4 v220, s[12:15], s63 offen lds\nM 68,176,72\nR 170,187,12288\nv_fma_f32 v237, v237, v219, v219\nM 68,180,88\nv_rcp_f32 v234, v234\n"
"M 52,176,68\nR 172,187,16384\nv_mul_f32 v234, v116, v234\nM 52,180,84\ns_add_u32 s64, s68, 23068672\nv_rcp_f32 v235, v235\n"
"M 36,176,64\nR 174,187,20480\nv_mul_f32 v235, v117, v235\nM 36,180,80\nG 4\nv_rcp_f32 v236, v236\n"
"M 40,160,64\nR 176,184,24576\ns_waitcnt vmcnt(13)\ns_barrier\ns_waitcnt vmcnt(7)\nv_cmp_eq_u64 s[86:87], s[46:47], v[248:249]\n"
"M 40,164,80\ns_add_u32 s65, s68, 23183360\nv_rcp_f32 v237, v237\nM 56,160,68\nR 178,184,28672\ns_and_b32 s62, s86, 0xf\n"
"s_cmp_eq_u32 s62, 0xf\ns_cbranch_scc0 Lq377\nLq378:\nM 56,164,84\nbuffer_load_dwordx4 v221, s[12:15], s63 offen offset:1024 lds\nM 72,160,72\n"
"R 180,184,32768\nv_mul_f32 v236, v118, v236\nM 72,164,88\nbuffer_load_dwordx4 a[0:3], v252, s[20:23], s64 offen sc1\nM 88,160,76\nR 182,184,36864\n"
"v_mul_f32 v237, v119, v237\nM 88,164,92\nG 4\nv_cvt_pk_bf16_f32 v114, v234, v235\nv_cvt_pk_bf16_f32 v115, v236, v237\nM 92,168,76\n"
"R 160,185,24576\nv_mul_f32 v230, v120, v120\nM 92,172,92\nbuffer_load_dwordx4 a[4:7], v252, s[20:23], s65 offen sc1\nM 76,168,72\nR 162,185,28672\n"
"v_mul_f32 v231, v121, v121\nM 76,172,88\ns_add_u32 s66, s69, 23068672\nv_mul_f32 v232, v122, v122\nv_mul_f32 v233, v123, v123\nM 60,168,68\n"
"R 164,185,32768\nv_fma_f32 v230, v230, v218, v217\nM 60,172,84\ns_add_u32 s67, s69, 23183360\nv_fma_f32 v231, v231, v218, v217\nv_fma_f32 v232, v232, v218, v217\n"
"M 44,168,64\nR 166,185,36864\nv_fma_f32 v233, v233, v218, v217\nM 44,172,80\nG 4\nv_mul_f32 v230, v120, v230\n"
"v_mul_f32 v231, v121, v231\nM 32,176,96\nR 168,186,24576\nv_mul_f32 v232, v122, v232\nM 32,180,112\nbuffer_load_dwordx4 a[8:11], v252, s[20:23], s66 offen sc1\n"
"M 48,176,100\nR 170,186,28672\nv_mul_f32 v233, v123, v233\nM 48,180,116\nbuffer_load_dwordx4 a[12:15], v252, s[20:23], s67 offen sc1\nM 64,176,104\n"
"R 172,186,32768\nv_mul_f32 v234, v124, v124\nM 64,180,120\ns_add_u32 s64, s68, 23075840\nv_exp_f32 v230, v230\nM 80,176,108\n"
"R 174,186,36864\nv_fma_f32 v230, v230, v219, v219\nM 80,180,124\nG 4\nv_exp_f32 v231, v231\nM 84,160,108\n"
"R 176,187,24576\nv_fma_f32 v231, v231, v219, v219\nM 84,164,124\nbuffer_load_dwordx4 a[16:19], v252, s[20:23], s64 offen sc1\nM 68,160,104\nR 178,187,28672\n"
"v_mul_f32 v235, v125, v125\nM 68,164,120\ns_add_u32 s65, s68, 23190528\nv_exp_f32 v232, v232\nM 52,160,100\nR 180,187,32768\n"
"v_fma_f32 v232, v232, v219, v219\nM 52,164,116\ns_add_u32 s66, s69, 23075840\nv_exp_f32 v233, v233\nM 36,160,96\nR 182,187,36864\n"
"v_fma_f32 v233, v233, v219, v219\nM 36,164,112\nG 4\nv_rcp_f32 v230, v230\nM 40,168,96\nR 160,184,40960\n"
"v_mul_f32 v230, v120, v230\nM 40,172,112\nbuffer_load_dwordx4 a[20:23], v252, s[20:23], s65 offen sc1\nM 56,168,100\nR 162,184,45056\nv_mul_f32 v236, v126, v126\n"
"M 56,172,116\nbuffer_load_dwordx4 a[24:27], v252, s[20:23], s66 offen sc1\nM 72,168,104\nR 164,184,49152\nv_mul_f32 v237, v127, v127\nM 72,172,120\n"
"s_add_u32 s67, s69, 23190528\nv_rcp_f32 v231, v231\nM 88,168,108\nR 166,184,53248\nv_mul_f32 v231, v121, v231\nM 88,172,124\n"
"G 4\nv_rcp_f32 v232, v232\nM 92,176,108\nR 168,185,40960\nv_mul_f32 v232, v122, v232\nM 92,180,124\n"
"buffer_load_dwordx4 a[28:31], v252, s[20:23], s67 offen sc1\nM 76,176,104\nR 170,185,45056\nv_cvt_pk_bf16_f32 v120, v230, v231\nM 76,180,120\ns_add_u32 s64, s68, 23069696\n"
"v_rcp_f32 v233, v233\nM 60,176,100\nR 172,185,49152\nv_mul_f32 v233, v123, v233\nM 60,180,116\ns_add_u32 s65, s68, 23184384\n"
"v_cvt_pk_bf16_f32 v121, v232, v233\nv_fma_f32 v234, v234, v218, v217\nM 44,176,96\nR 174,185,53248\nv_fma_f32 v235, v235, v218, v217\nM 44,180,112\n"
"G 4\nv_fma_f32 v236, v236, v218, v217\nv_fma_f32 v237, v237, v218, v217\nM 32,160,128\nR 176,186,40960\ns_add_u32 m0, s50, 98304\n"
"M 32,164,144\nbuffer_load_dwordx4 a[32:35], v252, s[20:23], s64 offen sc1\nM 48,160,132\nR 178,186,45056\ns_add_u32 s63, s54, 3440896\nM 48,164,148\n"
"buffer_load_dwordx4 a[36:39], v252, s[20:23], s65 offen sc1\nM 64,160,136\nR 180,186,49152\nv_mul_f32 v234, v124, v234\nM 64,164,152\ns_add_u32 s66, s69, 23069696\n"
"buffer_load_dwordx4 v220, s[12:15], s63 offen lds\nM 80,160,140\nR 182,186,53248\nv_mul_f32 v235, v125, v235\nM 80,164,156\nG 4\n"
"v_exp_f32 v234, v234\nM 84,168,140\nR 160,187,40960\nv_mul_f32 v236, v126, v236\nM 84,172,156\nbuffer_load_dwordx4 a[40:43], v252, s[20:23], s66 offen sc1\n"
"buffer_load_dwordx4 v221, s[12:15], s63 offen offset:1024 lds\nM 68,168,136\nR 162,187,45056\nv_mul_f32 v237, v127, v237\nM 68,172,152\ns_add_u32 s67, s69, 23184384\n"
"v_exp_f32 v235, v235\nM 52,168,132\nR 164,187,49152\ns_waitcnt vmcnt(15)\ns_barrier\nM 52,172,148\n"
"buffer_load_dwordx4 a[44:47], v252, s[20:23], s67 offen sc1\ns_movk_i32 s63, 2048\nv_mad_u32_u24 v220, v228, s63, v229\nv_add_u32 v221, 916480, v220\ns_add_u32 m0, s50, 106496\ns_add_u32 s63, s56, 0\n"
"buffer_load_dwordx4 v220, s[16:19], s63 offen lds\nM 36,168,128\nR 166,187,53248\nv_fma_f32 v234, v234, v219, v219\nM 36,172,144\nG 4\n"
"v_exp_f32 v236, v236\nM 40,176,128\nR 168,184,57344\nv_fma_f32 v235, v235, v219, v219\nM 40,180,144\ns_add_u32 s64, s68, 23076864\n"
"buffer_load_dwordx4 v221, s[16:19], s63 offen offset:1024 lds\nM 56,176,132\nR 170,184,61440\ns_add_u32 m0, s50, 114688\nM 56,180,148\nbuffer_load_dwordx4 a[48:51], v252, s[20:23], s64 offen sc1\n"
"M 72,176,136\nR 172,188,0\ns_add_u32 s63, s56, 524288\nM 72,180,152\ns_add_u32 s65, s68, 23191552\nbuffer_load_dwordx4 v220, s[16:19], s63 offen lds\n"
"M 88,176,140\nR 174,188,4096\nv_fma_f32 v236, v236, v219, v219\nM 88,180,156\nG 4\nv_exp_f32 v237, v237\n"
"M 92,160,140\nR 176,185,57344\nv_fma_f32 v237, v237, v219, v219\nM 92,164,156\nbuffer_load_dwordx4 a[52:55], v252, s[20:23], s65 offen sc1\nbuffer_load_dwordx4 v221, s[16:19], s63 offen offset:1024 lds\n"
"M 76,160,136\nR 178,185,61440\nM 76,164,152\ns_add_u32 s66, s69, 23076864\nv_rcp_f32 v234, v234\nM 60,160,132\n"
"R 180,189,0\nv_mul_f32 v234, v124, v234\nM 60,164,148\nbuffer_load_dwordx4 a[56:59], v252, s[20:23], s66 offen sc1\nM 44,160,128\nR 182,189,4096\n"
"M 44,164,144\nG 4\nv_rcp_f32 v235, v235\nM 32,168,160\nR 160,186,57344\nv_mul_f32 v235, v125, v235\n"
"M 32,172,176\ns_add_u32 s67, s69, 23191552\nv_rcp_f32 v236, v236\nM 48,168,164\nR 162,186,61440\nv_mul_f32 v236, v126, v236\n"
"M 48,172,180\nbuffer_load_dwordx4 a[60:63], v252, s[20:23], s67 offen sc1\nM 64,168,168\nR 164,190,0\nv_cvt_pk_bf16_f32 v122, v234, v235\nM 64,172,184\n"
"buffer_store_dwordx4 v[112:115], v252, s[20:23], s85 offen offset:0 sc1\nM 80,168,172\nLpg12:\ns_cbranch_execz LretO0n\nR 166,190,4096\nv_rcp_f32 v237, v237\n"
"M 80,172,188\nG 4\nv_mul_f32 v237, v127, v237\nv_cvt_pk_bf16_f32 v123, v236, v237\nM 84,176,172\nR 168,187,57344\n"
"M 84,180,188\ns_add_u32 m0, s50, 0\nbuffer_store_dwordx4 v[120:123], v252, s[20:23], s85 offen offset:1024 sc1\nM 68,176,168\nR 170,187,61440\nv_mul_f32 v230, v0, v0\n"
"v_mul_f32 v231, v1, v1\nM 68,180,184\ns_add_u32 s63, s56, 65536\ns_add_u32 s84, s73, 23074816\ns_add_u32 s85, s73, 23189504\nv_mul_f32 v232, v2, v2\n"
"M 52,176,164\nR 172,191,0\nv_mul_f32 v233, v3, v3\nv_fma_f32 v230, v230, v215, v214\nM 52,180,180\nbuffer_load_dwordx4 v220, s[16:19], s63 offen lds\n"
"v_fma_f32 v231, v231, v215, v214\nM 36,176,160\nR 174,191,4096\nv_fma_f32 v232, v232, v215, v214\nv_fma_f32 v233, v233, v215, v214\nM 36,180,176\n"
"G 4\nv_mul_f32 v230, v0, v230\nv_mul_f32 v231, v1, v231\nv_mul_f32 v232, v2, v232\nM 40,160,160\nR 176,188,8192\n"
"v_mul_f32 v233, v3, v233\nv_exp_f32 v230, v230\nM 40,164,176\nv_exp_f32 v231, v231\nv_exp_f32 v232, v232\nM 56,160,164\n"
"R 178,188,12288\nv_fma_f32 v230, v230, v216, v216\nv_exp_f32 v233, v233\nM 56,164,180\nbuffer_load_dwordx4 v221, s[16:19], s63 offen offset:1024 lds\nv_fma_f32 v231, v231, v216, v216\n"
"M 72,160,168\nR 180,188,16384\ns_add_u32 s63, s56, 589824\nv_fma_f32 v232, v232, v216, v216\nM 72,164,184\ns_add_u32 m0, s50, 8192\n"
"v_rcp_f32 v230, v230\nv_fma_f32 v233, v233, v216, v216\nM 88,160,172\nR 182,188,20480\nv_mul_f32 v230, v0, v230\nv_rcp_f32 v231, v231\n"
"M 88,164,188\nG 4\nv_rcp_f32 v232, v232\nv_rcp_f32 v233, v233\nM 92,168,172\nR 160,189,8192\n"
"v_mul_f32 v231, v1, v231\nv_mul_f32 v232, v2, v232\nM 92,172,188\nbuffer_load_dwordx4 v220, s[16:19], s63 offen lds\nv_mul_f32 v233, v3, v233\nM 76,168,168\n"
"R 162,189,12288\nv_cvt_pk_bf16_f32 v0, v230, v231\nv_cvt_pk_bf16_f32 v1, v232, v233\nM 76,172,184\nv_mul_f32 v234, v4, v4\nv_mul_f32 v235, v5, v5\n"
"v_mul_f32 v236, v6, v6\nM 60,168,164\nR 164,189,16384\nv_mul_f32 v237, v7, v7\nv_fma_f32 v234, v234, v215, v214\nM 60,172,180\n"
"buffer_load_dwordx4 v221, s[16:19], s63 offen offset:1024 lds\nv_fma_f32 v235, v235, v215, v214\nM 44,168,160\nR 166,189,20480\ns_add_u32 m0, s50, 16384\nv_fma_f32 v236, v236, v215, v214\n"
"M 44,172,176\nG 4\nv_fma_f32 v237, v237, v215, v214\nv_mul_f32 v234, v4, v234\nv_mul_f32 v235, v5, v235\nM 32,176,192\n"
"R 168,190,8192\ns_add_u32 s63, s57, 0\nv_mul_f32 v236, v6, v236\nM 32,180,208\nbuffer_load_dwordx4 v220, s[16:19], s63 offen lds\nv_mul_f32 v237, v7, v237\n"
"M 48,176,196\nR 170,190,12288\nv_mul_f32 v230, v16, v16\nv_exp_f32 v234, v234\nM 48,180,212\nv_exp_f32 v235, v235\n"
"v_exp_f32 v236, v236\nM 64,176,200\nR 172,190,16384\nv_fma_f32 v234, v234, v216, v216\nv_exp_f32 v237, v237\nM 64,180,216\n"
"buffer_load_dwordx4 v221, s[16:19], s63 offen offset:1024 lds\nv_fma_f32 v235, v235, v216, v216\nM 80,176,204\nR 174,190,20480\ns_add_u32 s63, s57, 524288\nv_fma_f32 v236, v236, v216, v216\n"
"M 80,180,220\nG 4\nv_rcp_f32 v234, v234\nv_fma_f32 v237, v237, v216, v216\nM 84,160,204\nR 176,191,8192\n"
"s_waitcnt vmcnt(10)\ns_barrier\nv_rcp_f32 v235, v235\nM 84,164,220\ns_add_u32 m0, s50, 24576\nv_rcp_f32 v236, v236\n"
"v_rcp_f32 v237, v237\nM 68,160,200\nR 178,191,12288\nv_mul_f32 v234, v4, v234\nv_mul_f32 v235, v5, v235\nM 68,164,216\n"
"buffer_load_dwordx4 v220, s[16:19], s63 offen lds\nv_mul_f32 v236, v6, v236\nM 52,160,196\nR 180,191,16384\nv_mul_f32 v237, v7, v237\nv_cvt_pk_bf16_f32 v2, v234, v235\n"
"M 52,164,212\nv_cvt_pk_bf16_f32 v3, v236, v237\nv_mul_f32 v231, v17, v17\nv_mul_f32 v232, v18, v18\nM 36,160,192\nR 182,191,20480\n"
"v_mul_f32 v233, v19, v19\nv_fma_f32 v230, v230, v218, v217\nM 36,164,208\nG 4\nv_fma_f32 v231, v231, v218, v217\nv_fma_f32 v232, v232, v218, v217\n"
"v_fma_f32 v233, v233, v218, v217\nM 40,168,192\nR 160,188,24576\nv_mul_f32 v230, v16, v230\nv_mul_f32 v231, v17, v231\nM 40,172,208\n"
"buffer_load_dwordx4 v221, s[16:19], s63 offen offset:1024 lds\nv_mul_f32 v232, v18, v232\nM 56,168,196\nR 162,188,28672\nv_mul_f32 v233, v19, v233\nv_exp_f32 v230, v230\n"
"M 56,172,212\nbuffer_store_dwordx4 v[0:3], v252, s[20:23], s84 offen offset:0 sc1\nv_exp_f32 v231, v231\nM 72,168,200\nR 164,188,32768\nv_fma_f32 v230, v230, v219, v219\n"
"v_exp_f32 v232, v232\nM 72,172,216\ns_add_u32 m0, s50, 32768\nv_exp_f32 v233, v233\nv_fma_f32 v231, v231, v219, v219\nM 88,168,204\n"
"R 166,188,36864\nv_mov_b32 v226, s46\nv_fma_f32 v232, v232, v219, v219\nM 88,172,220\nG 4\nv_mov_b32 v227, s47\n"
"v_fma_f32 v233, v233, v219, v219\nv_rcp_f32 v230, v230\nM 92,176,204\nR 168,189,24576\ns_add_u32 s61, s81, 385032\ns_lshl_b32 s62, s44, 5\n"
"s_add_u32 s61, s61, s62\ns_waitcnt vmcnt(9)\ns_mov_b64 exec, 1\nbuffer_atomic_swap_x2 v[226:227], off, s[20:23], s61 sc1\ns_mov_b64 exec, -1\nv_rcp_f32 v231, v231\n"
"M 92,180,220\ns_add_u32 s63, s57, 65536\nv_rcp_f32 v232, v232\nv_rcp_f32 v233, v233\nM 76,176,200\nR 170,189,28672\n"
"v_mul_f32 v230, v16, v230\nv_mul_f32 v231, v17, v231\nM 76,180,216\nbuffer_load_dwordx4 v220, s[16:19], s63 offen lds\nv_mul_f32 v232, v18, v232\nM 60,176,196\n"
"R 172,189,32768\nv_mul_f32 v233, v19, v233\nv_cvt_pk_bf16_f32 v16, v230, v231\nM 60,180,212\nv_cvt_pk_bf16_f32 v17, v232, v233\nv_mul_f32 v234, v20, v20\n"
"v_mul_f32 v235, v21, v21\nM 44,176,192\nR 174,189,36864\nv_mul_f32 v236, v22, v22\nv_mul_f32 v237, v23, v23\nM 44,180,208\n"
"G 4\nv_fma_f32 v234, v234, v218, v217\nv_fma_f32 v235, v235, v218, v217\nv_fma_f32 v236, v236, v218, v217\nM 32,160,224\nR 176,190,24576\n"
"v_fma_f32 v237, v237, v218, v217\nv_mul_f32 v234, v20, v234\nM 32,164,240\nbuffer_load_dwordx4 v221, s[16:19], s63 offen offset:1024 lds\nv_mul_f32 v235, v21, v235\nM 48,160,228\n"
"R 178,190,28672\ns_add_u32 s63, s57, 589824\nv_mul_f32 v236, v22, v236\nM 48,164,244\ns_add_u32 m0, s50, 40960\nv_exp_f32 v234, v234\n"
"v_mul_f32 v237, v23, v237\nM 64,160,232\nR 180,190,32768\nv_fma_f32 v234, v234, v219, v219\nv_exp_f32 v235, v235\nM 64,164,248\n"
"buffer_load_dwordx4 v220, s[16:19], s63 offen lds\nv_exp_f32 v236, v236\nM 80,160,236\nR 182,190,36864\nv_fma_f32 v235, v235, v219, v219\nv_exp_f32 v237, v237\n"
"M 80,164,252\nG 4\nv_rcp_f32 v234, v234\nv_fma_f32 v236, v236, v219, v219\nM 84,168,236\nR 160,191,24576\n"
"v_fma_f32 v237, v237, v219, v219\nv_rcp_f32 v235, v235\nM 84,172,252\nbuffer_load_dwordx4 v221, s[16:19], s63 offen offset:1024 lds\nv_rcp_f32 v236, v236\nM 68,168,232\n"
"R 162,191,28672\ns_add_u32 s63, s56, 131072\nv_rcp_f32 v237, v237\nM 68,172,248\ns_add_u32 m0, s50, 49152\nv_mul_f32 v234, v20, v234\n"
"v_mul_f32 v235, v21, v235\nM 52,168,228\nR 164,191,32768\nv_mul_f32 v236, v22, v236\nM 52,172,244\nbuffer_load_dwordx4 v220, s[16:19], s63 offen lds\n"
"M 36,168,224\nR 166,191,36864\nv_mul_f32 v237, v23, v237\nM 36,172,240\nG 4\nv_cvt_pk_bf16_f32 v18, v234, v235\n"
"v_cvt_pk_bf16_f32 v19, v236, v237\nM 40,176,224\nR 168,188,40960\nbuffer_store_dwordx4 v[16:19], v252, s[20:23], s85 offen offset:0 sc1\nM 40,180,240\nbuffer_load_dwordx4 v221, s[16:19], s63 offen offset:1024 lds\n"
"M 56,176,228\nR 170,188,45056\nv_mov_b32 v248, 0\nM 56,180,244\nv_mov_b32 v249, 0\nM 72,176,232\n"
"R 172,188,49152\ns_add_u32 s61, s82, 385032\ns_mov_b64 exec, 0xf\nbuffer_atomic_or_x2 v[248:249], v250, s[20:23], s61 offen sc0 sc1\ns_mov_b64 exec, -1\nM 72,180,248\n"
"M 88,176,236\nR 174,188,53248\ns_mov_b32 s60, 158208\nv_bfe_u32 v246, v252, 4, 4\nv_lshl_add_u32 v246, v246, 3, s60\nM 88,180,252\n"
"ds_read_b64 v[238:239], v246 offset:0\nG 5\nM 92,160,236\nR 176,189,40960\nds_read_b64 v[240:241], v246 offset:128\nM 92,164,252\n"
"ds_read_b64 v[242:243], v246 offset:256\ns_add_u32 m0, s50, 57344\nM 76,160,232\nR 178,189,45056\nds_read_b64 v[244:245], v246 offset:384\nM 76,164,248\n"
"ds_read_b64 v[246:247], v246 offset:0\ns_add_u32 s63, s56, 655360\nM 60,160,228\nR 180,189,49152\nM 60,164,244\nbuffer_load_dwordx4 v220, s[16:19], s63 offen lds\n"
"M 44,160,224\nR 182,189,53248\nM 44,164,240\ns_waitcnt vmcnt(15) lgkmcnt(2)\ns_barrier\nv_mfma_f32_16x16x32_bf16 v[96:99], v[168:171], a[0:3], 0\n"
"R 160,190,40960\nM 96,172,16\ns_mov_b32 m0, s51\nv_mfma_f32_16x16x32_bf16 v[112:115], v[168:171], a[4:7], 0\nR 162,190,45056\nM 112,172,20\n"
"ds_write_addtid_b32 v64 offset:0\nv_mfma_f32_16x16x32_bf16 v[128:131], v[168:171], a[8:11], 0\nR 164,190,49152\nv_mfma_f32_16x16x32_bf16 v[32:35], v[238:241], v[208:211], v[32:35]\nds_write_addtid_b32 v65 offset:256\nds_write_addtid_b32 v66 offset:512\n"
"M 128,172,24\nds_write_addtid_b32 v67 offset:768\nv_mfma_f32_16x16x32_bf16 v[144:147], v[168:171], a[12:15], 0\nR 166,190,53248\nv_mfma_f32_16x16x32_bf16 v[36:39], v[240:243], v[208:211], v[36:39]\nds_write_addtid_b32 v68 offset:1024\n"
"ds_write_addtid_b32 v69 offset:1280\nM 144,172,28\nds_write_addtid_b32 v70 offset:1536\nG 11\nv_mfma_f32_16x16x32_bf16 v[148:151], v[176:179], a[12:15], 0\nR 168,191,40960\n"
"v_mfma_f32_16x16x32_bf16 v[40:43], v[242:245], v[208:211], v[40:43]\nds_write_addtid_b32 v71 offset:1792\nds_write_addtid_b32 v72 offset:2048\nM 148,180,28\nds_write_addtid_b32 v73 offset:2304\nv_mfma_f32_16x16x32_bf16 v[132:135], v[176:179], a[8:11], 0\n"
"R 170,191,45056\nv_mfma_f32_16x16x32_bf16 v[44:47], v[244:247], v[208:211], v[44:47]\nds_write_addtid_b32 v74 offset:2560\nds_write_addtid_b32 v75 offset:2816\nM 132,180,24\nds_write_addtid_b32 v76 offset:3072\n"
"v_mfma_f32_16x16x32_bf16 v[116:119], v[176:179], a[4:7], 0\nR 172,191,49152\nv_mfma_f32_16x16x32_bf16 v[48:51], v[244:247], v[210:213], v[48:51]\nds_write_addtid_b32 v77 offset:3328\nds_write_addtid_b32 v78 offset:3584\nM 116,180,20\n"
"ds_write_addtid_b32 v79 offset:3840\nv_mfma_f32_16x16x32_bf16 v[100:103], v[176:179], a[0:3], 0\nR 174,191,53248\nv_mfma_f32_16x16x32_bf16 v[52:55], v[238:241], v[210:213], v[52:55]\nds_write_addtid_b32 v80 offset:4096\nds_write_addtid_b32 v81 offset:4352\n"
"M 100,180,16\nds_write_addtid_b32 v82 offset:4608\nG 15\nv_mfma_f32_16x16x32_bf16 v[104:107], v[160:163], a[0:3], 0\nR 176,184,0\nv_mfma_f32_16x16x32_bf16 v[56:59], v[240:243], v[210:213], v[56:59]\n"
"ds_write_addtid_b32 v83 offset:4864\nds_write_addtid_b32 v84 offset:5120\nM 104,164,16\nds_write_addtid_b32 v85 offset:5376\nv_mfma_f32_16x16x32_bf16 v[120:123], v[160:163], a[4:7], 0\nR 178,184,4096\n"
"v_mfma_f32_16x16x32_bf16 v[60:63], v[242:245], v[210:213], v[60:63]\nds_write_addtid_b32 v86 offset:5632\nds_write_addtid_b32 v87 offset:5888\nM 120,164,20\nds_write_addtid_b32 v88 offset:6144\nv_mfma_f32_16x16x32_bf16 v[136:139], v[160:163], a[8:11], 0\n"
"R 180,184,8192\nM 136,164,24\nds_write_addtid_b32 v89 offset:6400\nv_mfma_f32_16x16x32_bf16 v[152:155], v[160:163], a[12:15], 0\nR 182,184,12288\nds_write_addtid_b32 v90 offset:6656\n"
"M 152,164,28\nds_write_addtid_b32 v91 offset:6912\nds_write_addtid_b32 v92 offset:7168\nG 15\nv_mfma_f32_16x16x32_bf16 v[156:159], v[168:171], a[12:15], 0\nR 160,185,0\n"
"ds_write_addtid_b32 v93 offset:7424\nM 156,172,28\nds_write_addtid_b32 v94 offset:7680\nds_write_addtid_b32 v95 offset:7936\nv_mfma_f32_16x16x32_bf16 v[140:143], v[168:171], a[8:11], 0\nR 162,185,4096\n"
"s_add_u32 m0, s50, 57344\nM 140,172,24\nbuffer_load_dwordx4 v221, s[16:19], s63 offen offset:1024 lds\nv_mfma_f32_16x16x32_bf16 v[124:127], v[168:171], a[4:7], 0\nR 164,185,8192\ns_waitcnt vmcnt(6) lgkmcnt(2)\n"
"s_barrier\nM 124,172,20\nds_read2st64_b32 v[64:65], v225 offset0:0 offset1:1\nds_read2st64_b32 v[66:67], v225 offset0:2 offset1:3\nv_mfma_f32_16x16x32_bf16 v[108:111], v[168:171], a[0:3], 0\nR 166,185,12288\n"
"ds_read2st64_b32 v[68:69], v225 offset0:4 offset1:5\nM 108,172,16\nds_read2st64_b32 v[70:71], v225 offset0:6 offset1:7\nds_read2st64_b32 v[72:73], v225 offset0:8 offset1:9\nM 96,176,32\nR 168,186,0\n"
"ds_read2st64_b32 v[74:75], v225 offset0:10 offset1:11\nM 96,180,48\ns_add_u32 s64, s68, 23070720\nds_read2st64_b32 v[76:77], v225 offset0:12 offset1:13\nv_mov_b32 v226, s46\nM 112,176,36\n"
"R 170,186,4096\nds_read2st64_b32 v[78:79], v225 offset0:14 offset1:15\nv_mov_b32 v227, s47\ns_waitcnt vmcnt(2)\nv_cmp_eq_u64 s[86:87], s[46:47], v[248:249]\nM 112,180,52\n"
"s_add_u32 s61, s81, 385048\ns_lshl_b32 s62, s44, 5\ns_add_u32 s61, s61, s62\ns_mov_b64 exec, 1\nbuffer_atomic_swap_x2 v[226:227], off, s[20:23], s61 sc1\ns_mov_b64 exec, -1\n"
"ds_read2st64_b32 v[80:81], v225 offset0:16 offset1:17\nM 128,176,40\nR 172,186,8192\ns_and_b32 s62, s86, 0xf\ns_cmp_eq_u32 s62, 0xf\ns_cbranch_scc0 Lq412\n"
"Lq413:\nds_read2st64_b32 v[82:83], v225 offset0:18 offset1:19\nM 128,180,56\ns_add_u32 s65, s68, 23185408\nds_read2st64_b32 v[84:85], v225 offset0:20 offset1:21\nds_read2st64_b32 v[86:87], v225 offset0:22 offset1:23\n"
"M 144,176,44\nR 174,186,12288\ns_add_u32 m0, s50, 65536\nds_read2st64_b32 v[88:89], v225 offset0:24 offset1:25\nM 144,180,60\nds_read2st64_b32 v[90:91], v225 offset0:26 offset1:27\n"
"ds_read2st64_b32 v[92:93], v225 offset0:28 offset1:29\nG 15\nM 148,160,44\nR 176,187,0\ns_add_u32 s63, s56, 196608\nds_read2st64_b32 v[94:95], v225 offset0:30 offset1:31\n"
"M 148,164,60\nbuffer_load_dwordx4 a[64:67], v252, s[20:23], s64 offen sc1\nv_add_f32 v32, v32, v64\nM 132,160,40\nR 178,187,4096\nv_add_f32 v33, v33, v65\n"
"v_add_f32 v34, v34, v66\nM 132,164,56\ns_add_u32 s66, s69, 23070720\nv_add_f32 v35, v35, v67\nv_add_f32 v36, v36, v68\nv_add_f32 v37, v37, v69\n"
"M 116,160,36\nR 180,187,8192\nbuffer_load_dwordx4 v220, s[16:19], s63 offen lds\nv_add_f32 v38, v38, v70\nM 116,164,52\nbuffer_load_dwordx4 a[68:71], v252, s[20:23], s65 offen sc1\n"
"v_add_f32 v39, v39, v71\nM 100,160,32\nR 182,187,12288\nG 15\nv_add_f32 v40, v40, v72\nv_add_f32 v41, v41, v73\n"
"M 100,164,48\nG 8\nv_add_f32 v42, v42, v74\nv_add_f32 v43, v43, v75\nv_add_f32 v44, v44, v76\nM 104,168,32\n"
"R 160,184,16384\nbuffer_load_dwordx4 v221, s[16:19], s63 offen offset:1024 lds\nv_add_f32 v45, v45, v77\nM 104,172,48\ns_add_u32 s67, s69, 23185408\nv_add_f32 v46, v46, v78\n"
"v_add_f32 v47, v47, v79\nv_add_f32 v48, v48, v80\nM 120,168,36\nR 162,184,20480\nv_add_f32 v49, v49, v81\nv_add_f32 v50, v50, v82\n"
"M 120,172,52\nbuffer_load_dwordx4 a[72:75], v252, s[20:23], s66 offen sc1\nM 136,168,40\nR 164,184,24576\nv_add_f32 v51, v51, v83\nM 136,172,56\n"
"v_add_f32 v52, v52, v84\nv_add_f32 v53, v53, v85\nM 152,168,44\nR 166,184,28672\nv_add_f32 v54, v54, v86\nM 152,172,60\n"
"G 4\nv_add_f32 v55, v55, v87\nv_add_f32 v56, v56, v88\nM 156,176,44\nR 168,185,16384\nv_add_f32 v57, v57, v89\n"
"M 156,180,60\nbuffer_load_dwordx4 a[76:79], v252, s[20:23], s67 offen sc1\nM 140,176,40\nR 170,185,20480\nv_add_f32 v58, v58, v90\nM 140,180,56\n"
"s_add_u32 s64, s68, 23077888\nv_add_f32 v59, v59, v91\nv_add_f32 v60, v60, v92\nM 124,176,36\nR 172,185,24576\nv_add_f32 v61, v61, v93\n"
"M 124,180,52\ns_add_u32 s65, s68, 23192576\nv_add_f32 v62, v62, v94\nv_add_f32 v63, v63, v95\nM 108,176,32\nR 174,185,28672\n"
"v_mul_f32 v230, v32, v32\nv_mul_f32 v231, v33, v33\nM 108,180,48\ns_add_u32 s84, s73, 23072768\ns_add_u32 s85, s73, 23187456\nG 4\n"
"v_mul_f32 v232, v34, v34\nv_mfma_f32_16x16x32_bf16 v[0:3], v[160:163], a[0:3], 0\nR 176,186,16384\nLq577:\ns_lshr_b32 s60, s45, 3\ns_lshl2_add_u32 s60, s60, s40\n"
"s_lshl_b32 s60, s60, 8\nv_lshl_add_u32 v224, v252, 3, s60\nv_add_u32 v224, Lq577+4096-LqPC, v224\nv_min_u32 v224, LqE-LqPC-4, v224\ns_mov_b64 exec, 3\nglobal_load_dword v224, v224, s[98:99]\n"
"s_mov_b64 exec, -1\nv_mul_f32 v233, v35, v35\nM 0,164,16\nbuffer_load_dwordx4 a[80:83], v252, s[20:23], s64 offen sc1\nv_fma_f32 v230, v230, v215, v214\nv_mfma_f32_16x16x32_bf16 v[16:19], v[160:163], a[4:7], 0\n"
"R 178,186,20480\nv_fma_f32 v231, v231, v215, v214\nv_fma_f32 v232, v232, v215, v214\nM 16,164,20\ns_add_u32 s66, s69, 23077888\nv_fma_f32 v233, v233, v215, v214\n"
"v_mul_f32 v230, v32, v230\nv_mul_f32 v231, v33, v231\nv_mfma_f32_16x16x32_bf16 a[224:227], v[160:163], a[8:11], 0\nR 180,186,24576\nv_mul_f32 v232, v34, v232\nv_mul_f32 v233, v35, v233\n"
"v_mfma_f32_16x16x32_bf16 a[224:227], v[164:167], a[24:27], a[224:227]\nbuffer_load_dwordx4 a[84:87], v252, s[20:23], s65 offen sc1\nv_exp_f32 v230, v230\nv_mfma_f32_16x16x32_bf16 a[240:243], v[160:163], a[12:15], 0\nR 182,186,28672\nv_fma_f32 v230, v230, v216, v216\n"
"v_exp_f32 v231, v231\nv_mfma_f32_16x16x32_bf16 a[240:243], v[164:167], a[28:31], a[240:243]\nG 4\nv_exp_f32 v232, v232\nv_exp_f32 v233, v233\nv_mfma_f32_16x16x32_bf16 a[244:247], v[168:171], a[12:15], 0\n"
"R 160,187,16384\ns_add_u32 m0, s50, 73728\nLpg13:\ns_cbranch_execz LretO0n\nv_fma_f32 v231, v231, v216, v216\nv_mfma_f32_16x16x32_bf16 a[244:247], v[172:175], a[28:31], a[244:247]\n"
"s_add_u32 s67, s69, 23192576\nv_rcp_f32 v230, v230\nv_fma_f32 v232, v232, v216, v216\nv_mfma_f32_16x16x32_bf16 a[228:231], v[168:171], a[8:11], 0\nR 162,187,20480\ns_add_u32 s63, s56, 720896\n"
"v_fma_f32 v233, v233, v216, v216\nv_mfma_f32_16x16x32_bf16 a[228:231], v[172:175], a[24:27], a[228:231]\nbuffer_load_dwordx4 a[88:91], v252, s[20:23], s66 offen sc1\nv_rcp_f32 v231, v231\nv_mfma_f32_16x16x32_bf16 v[20:23], v[168:171], a[4:7], 0\nR 164,187,24576\n"
"v_mul_f32 v230, v32, v230\nv_rcp_f32 v232, v232\nM 20,172,20\nbuffer_load_dwordx4 v220, s[16:19], s63 offen lds\nv_rcp_f32 v233, v233\nv_mfma_f32_16x16x32_bf16 v[4:7], v[168:171], a[0:3], 0\n"
"R 166,187,28672\nv_mul_f32 v231, v33, v231\nv_mul_f32 v232, v34, v232\nM 4,172,16\nG 4\nv_mul_f32 v233, v35, v233\n"
"v_cvt_pk_bf16_f32 v32, v230, v231\nv_cvt_pk_bf16_f32 v33, v232, v233\nv_mfma_f32_16x16x32_bf16 v[8:11], v[176:179], a[0:3], 0\nR 168,184,32768\nv_mul_f32 v234, v36, v36\nv_mul_f32 v235, v37, v37\n"
"M 8,180,16\nbuffer_load_dwordx4 a[92:95], v252, s[20:23], s67 offen sc1\nbuffer_load_dwordx4 v221, s[16:19], s63 offen offset:1024 lds\nv_mul_f32 v236, v38, v38\nv_mfma_f32_16x16x32_bf16 v[24:27], v[176:179], a[4:7], 0\nR 170,184,36864\n"
"s_add_u32 m0, s50, 81920\nv_mul_f32 v237, v39, v39\nM 24,180,20\ns_add_u32 s64, s68, 23071744\nv_fma_f32 v234, v234, v215, v214\nv_fma_f32 v235, v235, v215, v214\n"
"v_fma_f32 v236, v236, v215, v214\nv_mfma_f32_16x16x32_bf16 a[232:235], v[176:179], a[8:11], 0\nR 172,184,40960\ns_add_u32 s63, s57, 131072\nv_fma_f32 v237, v237, v215, v214\nv_mfma_f32_16x16x32_bf16 a[232:235], v[180:183], a[24:27], a[232:235]\n"
"s_add_u32 s65, s68, 23186432\nbuffer_load_dwordx4 v220, s[16:19], s63 offen lds\nv_mul_f32 v234, v36, v234\nv_mfma_f32_16x16x32_bf16 a[248:251], v[176:179], a[12:15], 0\nR 174,184,45056\nv_mul_f32 v235, v37, v235\n"
"v_mul_f32 v236, v38, v236\nv_mfma_f32_16x16x32_bf16 a[248:251], v[180:183], a[28:31], a[248:251]\nG 4\nv_exp_f32 v234, v234\nv_mul_f32 v237, v39, v237\nv_mfma_f32_16x16x32_bf16 a[252:255], v[160:163], a[12:15], 0\n"
"R 176,185,32768\nv_fma_f32 v234, v234, v216, v216\nv_exp_f32 v235, v235\nv_mfma_f32_16x16x32_bf16 a[252:255], v[164:167], a[28:31], a[252:255]\nbuffer_load_dwordx4 a[96:99], v252, s[20:23], s64 offen sc1\nbuffer_load_dwordx4 v221, s[16:19], s63 offen offset:1024 lds\n"
"v_exp_f32 v236, v236\nv_mfma_f32_16x16x32_bf16 a[236:239], v[160:163], a[8:11], 0\nR 178,185,36864\ns_add_u32 m0, s50, 90112\nv_exp_f32 v237, v237\nv_mfma_f32_16x16x32_bf16 a[236:239], v[164:167], a[24:27], a[236:239]\n"
"s_add_u32 s66, s69, 23071744\nv_rcp_f32 v234, v234\nv_fma_f32 v235, v235, v216, v216\nv_mfma_f32_16x16x32_bf16 v[28:31], v[160:163], a[4:7], 0\nR 180,185,40960\ns_add_u32 s63, s57, 655360\n"
"v_fma_f32 v236, v236, v216, v216\nM 28,164,20\nbuffer_load_dwordx4 a[100:103], v252, s[20:23], s65 offen sc1\nbuffer_load_dwordx4 v220, s[16:19], s63 offen lds\nv_fma_f32 v237, v237, v216, v216\nv_mfma_f32_16x16x32_bf16 v[12:15], v[160:163], a[0:3], 0\n"
"R 182,185,45056\nv_mul_f32 v234, v36, v234\nv_rcp_f32 v235, v235\nM 12,164,16\ns_waitcnt vmcnt(14)\ns_barrier\n"
"G 4\nv_rcp_f32 v236, v236\nM 0,168,32\nR 160,186,32768\nv_mul_f32 v235, v37, v235\nv_rcp_f32 v237, v237\n"
"M 0,172,48\ns_add_u32 s67, s69, 23186432\nbuffer_load_dwordx4 v221, s[16:19], s63 offen offset:1024 lds\nv_mul_f32 v236, v38, v236\nM 16,168,36\nR 162,186,36864\n"
"v_mul_f32 v237, v39, v237\nv_cvt_pk_bf16_f32 v34, v234, v235\nM 16,172,52\nbuffer_load_dwordx4 a[104:107], v252, s[20:23], s66 offen sc1\nv_cvt_pk_bf16_f32 v35, v236, v237\nv_mfma_f32_16x16x32_bf16 a[224:227], v[168:171], a[40:43], a[224:227]\n"
"R 164,186,40960\nv_mul_f32 v230, v40, v40\nv_mul_f32 v231, v41, v41\nv_mfma_f32_16x16x32_bf16 a[224:227], v[172:175], a[56:59], a[224:227]\nv_mul_f32 v232, v42, v42\nv_mul_f32 v233, v43, v43\n"
"v_fma_f32 v230, v230, v215, v214\nv_mfma_f32_16x16x32_bf16 a[240:243], v[168:171], a[44:47], a[240:243]\nR 166,186,45056\nv_fma_f32 v231, v231, v215, v214\nv_fma_f32 v232, v232, v215, v214\nv_mfma_f32_16x16x32_bf16 a[240:243], v[172:175], a[60:63], a[240:243]\n"
"G 4\nv_fma_f32 v233, v233, v215, v214\nv_mul_f32 v230, v40, v230\nv_mul_f32 v231, v41, v231\nv_mfma_f32_16x16x32_bf16 a[244:247], v[176:179], a[44:47], a[244:247]\nR 168,187,32768\n"
"v_mul_f32 v232, v42, v232\nv_mul_f32 v233, v43, v233\nv_mfma_f32_16x16x32_bf16 a[244:247], v[180:183], a[60:63], a[244:247]\nbuffer_load_dwordx4 a[108:111], v252, s[20:23], s67 offen sc1\nv_exp_f32 v230, v230\nv_mfma_f32_16x16x32_bf16 a[228:231], v[176:179], a[40:43], a[228:231]\n"
"R 170,187,36864\nv_fma_f32 v230, v230, v216, v216\nv_exp_f32 v231, v231\nv_mfma_f32_16x16x32_bf16 a[228:231], v[180:183], a[56:59], a[228:231]\ns_add_u32 s64, s68, 23078912\nv_exp_f32 v232, v232\n"
"v_exp_f32 v233, v233\nM 20,176,36\nR 172,187,40960\nv_fma_f32 v231, v231, v216, v216\nv_fma_f32 v232, v232, v216, v216\nM 20,180,52\n"
"s_add_u32 s65, s68, 23193600\nv_rcp_f32 v230, v230\nv_fma_f32 v233, v233, v216, v216\nM 4,176,32\nR 174,187,45056\ns_add_u32 m0, s50, 98304\n"
"v_rcp_f32 v231, v231\nM 4,180,48\nG 4\nv_rcp_f32 v232, v232\nv_rcp_f32 v233, v233\nM 8,160,32\n"
"R 176,184,49152\ns_add_u32 s63, s57, 196608\nv_mul_f32 v230, v40, v230\nM 8,164,48\nbuffer_load_dwordx4 a[112:115], v252, s[20:23], s64 offen sc1\nv_mul_f32 v231, v41, v231\n"
"M 24,160,36\nR 178,184,53248\nv_mul_f32 v232, v42, v232\nv_mul_f32 v233, v43, v233\nM 24,164,52\ns_add_u32 s66, s69, 23078912\n"
"buffer_load_dwordx4 v220, s[16:19], s63 offen lds\nv_cvt_pk_bf16_f32 v40, v230, v231\nv_mfma_f32_16x16x32_bf16 a[232:235], v[160:163], a[40:43], a[232:235]\nR 180,184,57344\nv_cvt_pk_bf16_f32 v41, v232, v233\nv_mul_f32 v234, v44, v44\n"
"v_mfma_f32_16x16x32_bf16 a[232:235], v[164:167], a[56:59], a[232:235]\nbuffer_load_dwordx4 a[116:119], v252, s[20:23], s65 offen sc1\nv_mul_f32 v235, v45, v45\nv_mfma_f32_16x16x32_bf16 a[248:251], v[160:163], a[44:47], a[248:251]\nR 182,184,61440\nv_mul_f32 v236, v46, v46\n"
"v_mul_f32 v237, v47, v47\nv_mfma_f32_16x16x32_bf16 a[248:251], v[164:167], a[60:63], a[248:251]\nbuffer_load_dwordx4 v221, s[16:19], s63 offen offset:1024 lds\nG 4\nv_fma_f32 v234, v234, v215, v214\nv_mfma_f32_16x16x32_bf16 a[252:255], v[168:171], a[44:47], a[252:255]\n"
"R 160,185,49152\ns_add_u32 m0, s50, 106496\nv_fma_f32 v235, v235, v215, v214\nv_mfma_f32_16x16x32_bf16 a[252:255], v[172:175], a[60:63], a[252:255]\ns_add_u32 s67, s69, 23193600\ns_add_u32 s63, s57, 720896\n"
"v_fma_f32 v236, v236, v215, v214\nv_fma_f32 v237, v237, v215, v214\nv_mfma_f32_16x16x32_bf16 a[236:239], v[168:171], a[40:43], a[236:239]\nR 162,185,53248\nv_mov_b32 v248, 0\nv_mul_f32 v234, v44, v234\n"
"v_mfma_f32_16x16x32_bf16 a[236:239], v[172:175], a[56:59], a[236:239]\nbuffer_load_dwordx4 a[120:123], v252, s[20:23], s66 offen sc1\nbuffer_load_dwordx4 v220, s[16:19], s63 offen lds\nv_mov_b32 v249, 0\nv_mul_f32 v235, v45, v235\nM 28,168,36\n"
"R 164,185,57344\ns_add_u32 s61, s82, 385048\ns_mov_b64 exec, 0xf\nbuffer_atomic_or_x2 v[248:249], v250, s[20:23], s61 offen sc0 sc1\ns_mov_b64 exec, -1\nv_mul_f32 v236, v46, v236\n"
"M 28,172,52\nbuffer_load_dwordx4 v221, s[16:19], s63 offen offset:1024 lds\nv_mul_f32 v237, v47, v237\nM 12,168,32\nR 166,185,61440\ns_add_u32 m0, s50, 114688\n"
"v_exp_f32 v234, v234\nM 12,172,48\nG 4\nv_exp_f32 v235, v235\nv_exp_f32 v236, v236\nM 96,176,64\n"
"R 168,186,49152\ns_add_u32 s63, s58, 0\nv_exp_f32 v237, v237\ns_waitcnt vmcnt(21)\nM 96,180,80\nbuffer_load_dwordx4 a[124:127], v252, s[20:23], s67 offen sc1\n"
"v_fma_f32 v234, v234, v216, v216\nM 112,176,68\nR 170,186,53248\nv_fma_f32 v235, v235, v216, v216\nv_fma_f32 v236, v236, v216, v216\ns_waitcnt vmcnt(21)\n"
"M 112,180,84\nbuffer_load_dwordx4 v220, s[16:19], s63 offen lds\nv_fma_f32 v237, v237, v216, v216\nM 128,176,72\nR 172,186,57344\nv_mul_f32 v230, v48, v48\n"
"v_rcp_f32 v234, v234\ns_waitcnt vmcnt(21)\nM 128,180,88\nbuffer_load_dwordx4 v221, s[16:19], s63 offen offset:1024 lds\nv_rcp_f32 v235, v235\nM 144,176,76\n"
"R 174,186,61440\ns_add_u32 m0, s50, 0\nv_rcp_f32 v236, v236\ns_waitcnt vmcnt(20)\nM 144,180,92\nG 4\n"
"v_rcp_f32 v237, v237\nv_mul_f32 v234, v44, v234\nM 148,160,76\nR 176,187,49152\ns_add_u32 s63, s58, 524288\nv_mul_f32 v235, v45, v235\n"
"M 148,164,92\nbuffer_load_dwordx4 v220, s[16:19], s63 offen lds\nv_mul_f32 v236, v46, v236\nM 132,160,72\nR 178,187,53248\nv_mul_f32 v237, v47, v237\n"
"v_cvt_pk_bf16_f32 v42, v234, v235\nM 132,164,88\nbuffer_store_dwordx4 v[32:35], v252, s[20:23], s84 offen offset:0 sc1\nv_cvt_pk_bf16_f32 v43, v236, v237\nM 116,160,68\nR 180,187,57344\n"
"v_mul_f32 v231, v49, v49\nv_mul_f32 v232, v50, v50\nM 116,164,84\nbuffer_load_dwordx4 v221, s[16:19], s63 offen offset:1024 lds\nv_mul_f32 v233, v51, v51\nM 100,160,64\n"
"R 182,187,61440\nv_fma_f32 v230, v230, v218, v217\nv_fma_f32 v231, v231, v218, v217\nM 100,164,80\nG 4\nv_fma_f32 v232, v232, v218, v217\n"
"v_fma_f32 v233, v233, v218, v217\nv_mul_f32 v230, v48, v230\nM 104,168,64\nR 160,188,0\ns_waitcnt vmcnt(16)\ns_barrier\n"
"v_mul_f32 v231, v49, v231\nM 104,172,80\nbuffer_store_dwordx4 v[40:43], v252, s[20:23], s84 offen offset:1024 sc1\nv_mul_f32 v232, v50, v232\nM 120,168,68\nR 162,188,4096\n"
"v_mul_f32 v233, v51, v233\nv_exp_f32 v230, v230\nM 120,172,84\ns_add_u32 m0, s50, 8192\nv_exp_f32 v231, v231\nv_exp_f32 v232, v232\n"
"M 136,168,72\nR 164,188,8192\nv_fma_f32 v230, v230, v219, v219\nv_exp_f32 v233, v233\nM 136,172,88\ns_add_u32 s63, s58, 65536\n"
"v_rcp_f32 v230, v230\nv_fma_f32 v231, v231, v219, v219\nM 152,168,76\nR 166,188,12288\nv_fma_f32 v232, v232, v219, v219\nv_fma_f32 v233, v233, v219, v219\n"
"M 152,172,92\nG 4\nv_rcp_f32 v231, v231\nv_rcp_f32 v232, v232\nM 156,176,76\nR 168,189,0\n"
"v_mul_f32 v230, v48, v230\nv_rcp_f32 v233, v233\nM 156,180,92\nbuffer_load_dwordx4 v220, s[16:19], s63 offen lds\nv_mul_f32 v231, v49, v231\nM 140,176,72\n"
"R 170,189,4096\nv_mul_f32 v232, v50, v232\nv_mul_f32 v233, v51, v233\nM 140,180,88\nv_cvt_pk_bf16_f32 v48, v230, v231\nv_cvt_pk_bf16_f32 v49, v232, v233\n"
"v_mul_f32 v234, v52, v52\nM 124,176,68\nR 172,189,8192\nv_mul_f32 v235, v53, v53\nv_mul_f32 v236, v54, v54\nM 124,180,84\n"
"buffer_load_dwordx4 v221, s[16:19], s63 offen offset:1024 lds\nv_mul_f32 v237, v55, v55\nM 108,176,64\nR 174,189,12288\ns_add_u32 m0, s50, 16384\nv_fma_f32 v234, v234, v218, v217\n"
"M 108,180,80\nG 4\nv_fma_f32 v235, v235, v218, v217\nv_fma_f32 v236, v236, v218, v217\nv_fma_f32 v237, v237, v218, v217\nM 96,160,96\n"
"R 176,190,0\ns_add_u32 s63, s58, 589824\nv_mul_f32 v234, v52, v234\ns_waitcnt vmcnt(16)\nM 96,164,112\nbuffer_load_dwordx4 v220, s[16:19], s63 offen lds\n"
"v_mul_f32 v235, v53, v235\nM 112,160,100\nR 178,190,4096\nv_mul_f32 v236, v54, v236\nv_mul_f32 v237, v55, v237\ns_waitcnt vmcnt(15)\n"
"M 112,164,116\nv_exp_f32 v234, v234\nv_exp_f32 v235, v235\nM 128,160,104\nR 180,190,8192\nv_fma_f32 v234, v234, v219, v219\n"
"v_exp_f32 v236, v236\ns_waitcnt vmcnt(13)\nM 128,164,120\nv_exp_f32 v237, v237\nv_fma_f32 v235, v235, v219, v219\nM 144,160,108\n"
"R 182,190,12288\nv_fma_f32 v236, v236, v219, v219\nv_fma_f32 v237, v237, v219, v219\ns_waitcnt vmcnt(9)\nM 144,164,124\nG 4\n"
"v_rcp_f32 v234, v234\nv_rcp_f32 v235, v235\nM 148,168,108\nR 160,191,0\nv_mul_f32 v234, v52, v234\nv_rcp_f32 v236, v236\n"
"M 148,172,124\ns_add_u32 s64, s68, 23074816\nv_rcp_f32 v237, v237\nv_mul_f32 v235, v53, v235\nM 132,168,104\nR 162,191,4096\n"
"v_cmp_eq_u64 s[86:87], s[46:47], v[248:249]\nv_mul_f32 v236, v54, v236\nM 132,172,120\nbuffer_load_dwordx4 v221, s[16:19], s63 offen offset:1024 lds\nv_mul_f32 v237, v55, v237\nM 116,168,100\n"
"R 164,191,8192\ns_and_b32 s62, s86, 0xf\ns_cmp_eq_u32 s62, 0xf\ns_cbranch_scc0 Lq447\nLq448:\nM 116,172,116\n"
"s_add_u32 s65, s68, 23189504\nv_cvt_pk_bf16_f32 v50, v234, v235\nv_cvt_pk_bf16_f32 v51, v236, v237\nM 100,168,96\nR 166,191,12288\nv_mul_f32 v230, v56, v56\n"
"M 100,172,112\nG 4\nv_mul_f32 v231, v57, v57\nv_mul_f32 v232, v58, v58\nM 104,176,96\nR 168,188,16384\n"
"v_mul_f32 v233, v59, v59\nM 104,180,112\nbuffer_load_dwordx4 a[128:131], v252, s[20:23], s64 offen sc1\nM 120,176,100\nR 170,188,20480\nv_fma_f32 v230, v230, v218, v217\n"
"M 120,180,116\ns_add_u32 s66, s69, 23074816\nv_fma_f32 v231, v231, v218, v217\nv_fma_f32 v232, v232, v218, v217\nM 136,176,104\nR 172,188,24576\n"
"v_fma_f32 v233, v233, v218, v217\nM 136,180,120\nbuffer_load_dwordx4 a[132:135], v252, s[20:23], s65 offen sc1\nM 152,176,108\nR 174,188,28672\nv_mul_f32 v230, v56, v230\n"
"M 152,180,124\nG 4\nv_exp_f32 v230, v230\nM 156,160,108\nR 176,189,16384\nv_mul_f32 v231, v57, v231\n"
"M 156,164,124\ns_add_u32 s67, s69, 23189504\nv_exp_f32 v231, v231\nM 140,160,104\nR 178,189,20480\nv_mul_f32 v232, v58, v232\n"
"M 140,164,120\nbuffer_load_dwordx4 a[136:139], v252, s[20:23], s66 offen sc1\nM 124,160,100\nR 180,189,24576\nv_mul_f32 v233, v59, v233\nM 124,164,116\n"
"buffer_store_dwordx4 v[48:51], v252, s[20:23], s85 offen offset:0 sc1\nM 108,160,96\nR 182,189,28672\ns_add_u32 m0, s50, 24576\nM 108,164,112\ns_waitcnt vmcnt(9)\n"
"s_barrier\nG 4\nM 0,168,64\nR 160,190,16384\ns_add_u32 s63, s58, 131072\nM 0,172,80\n"
"buffer_load_dwordx4 a[140:143], v252, s[20:23], s67 offen sc1\nM 16,168,68\nR 162,190,20480\nv_fma_f32 v230, v230, v219, v219\nM 16,172,84\ns_add_u32 s64, s68, 23081984\n"
"v_exp_f32 v232, v232\nv_mfma_f32_16x16x32_bf16 a[224:227], v[168:171], a[72:75], a[224:227]\nR 164,190,24576\nbuffer_load_dwordx4 v220, s[16:19], s63 offen lds\nv_mfma_f32_16x16x32_bf16 a[224:227], v[172:175], a[88:91], a[224:227]\ns_add_u32 s65, s68, 23196672\n"
"v_exp_f32 v233, v233\nv_mfma_f32_16x16x32_bf16 a[240:243], v[168:171], a[76:79], a[240:243]\nR 166,190,28672\nv_fma_f32 v231, v231, v219, v219\nv_mfma_f32_16x16x32_bf16 a[240:243], v[172:175], a[92:95], a[240:243]\nG 4\n"
"v_rcp_f32 v230, v230\nv_mfma_f32_16x16x32_bf16 a[244:247], v[176:179], a[76:79], a[244:247]\nR 168,191,16384\nbuffer_load_dwordx4 v221, s[16:19], s63 offen offset:1024 lds\nv_mfma_f32_16x16x32_bf16 a[244:247], v[180:183], a[92:95], a[244:247]\nbuffer_load_dwordx4 a[144:147], v252, s[20:23], s64 offen sc1\n"
"v_mfma_f32_16x16x32_bf16 a[228:231], v[176:179], a[72:75], a[228:231]\nR 170,191,20480\ns_add_u32 m0, s50, 32768\nv_mfma_f32_16x16x32_bf16 a[228:231], v[180:183], a[88:91], a[228:231]\ns_add_u32 s66, s69, 23081984\ns_add_u32 s63, s58, 655360\n"
"v_fma_f32 v232, v232, v219, v219\nM 20,176,68\nR 172,191,24576\nbuffer_load_dwordx4 v220, s[16:19], s63 offen lds\nM 20,180,84\nbuffer_load_dwordx4 a[148:151], v252, s[20:23], s65 offen sc1\n"
"M 4,176,64\nR 174,191,28672\nv_fma_f32 v233, v233, v219, v219\nM 4,180,80\nG 4\nv_rcp_f32 v231, v231\n"
"M 8,160,64\nR 176,188,32768\nbuffer_load_dwordx4 v221, s[16:19], s63 offen offset:1024 lds\nM 8,164,80\ns_add_u32 s67, s69, 23196672\nv_rcp_f32 v232, v232\n"
"M 24,160,68\nR 178,188,36864\nv_mul_f32 v230, v56, v230\nM 24,164,84\nbuffer_load_dwordx4 a[152:155], v252, s[20:23], s66 offen sc1\nv_mfma_f32_16x16x32_bf16 a[232:235], v[160:163], a[72:75], a[232:235]\n"
"R 180,188,40960\nv_mul_f32 v231, v57, v231\nv_mfma_f32_16x16x32_bf16 a[232:235], v[164:167], a[88:91], a[232:235]\nv_rcp_f32 v233, v233\nv_mfma_f32_16x16x32_bf16 a[248:251], v[160:163], a[76:79], a[248:251]\nR 182,188,45056\n"
"v_mul_f32 v232, v58, v232\nv_mfma_f32_16x16x32_bf16 a[248:251], v[164:167], a[92:95], a[248:251]\nG 4\nv_mul_f32 v233, v59, v233\nv_cvt_pk_bf16_f32 v56, v230, v231\nv_mfma_f32_16x16x32_bf16 a[252:255], v[168:171], a[76:79], a[252:255]\n"
"R 160,189,32768\nv_cvt_pk_bf16_f32 v57, v232, v233\nv_mfma_f32_16x16x32_bf16 a[252:255], v[172:175], a[92:95], a[252:255]\nbuffer_load_dwordx4 a[156:159], v252, s[20:23], s67 offen sc1\nv_mfma_f32_16x16x32_bf16 a[236:239], v[168:171], a[72:75], a[236:239]\nR 162,189,36864\n"
"v_mul_f32 v234, v60, v60\nv_mfma_f32_16x16x32_bf16 a[236:239], v[172:175], a[88:91], a[236:239]\nv_mul_f32 v235, v61, v61\nv_mul_f32 v236, v62, v62\nM 28,168,68\nR 164,189,40960\n"
"v_mul_f32 v237, v63, v63\nM 28,172,84\ns_add_u32 m0, s50, 40960\nv_fma_f32 v234, v234, v218, v217\nv_fma_f32 v235, v235, v218, v217\nM 12,168,64\n"
"R 166,189,45056\nv_fma_f32 v236, v236, v218, v217\nM 12,172,80\nG 4\nv_fma_f32 v237, v237, v218, v217\nv_mul_f32 v234, v60, v234\n"
"M 0,176,96\nR 168,190,32768\nv_mul_f32 v235, v61, v235\nM 0,180,112\ns_add_u32 s63, s58, 196608\nv_exp_f32 v234, v234\n"
"M 16,176,100\nR 170,190,36864\nv_mul_f32 v236, v62, v236\nM 16,180,116\nbuffer_load_dwordx4 v220, s[16:19], s63 offen lds\nv_mfma_f32_16x16x32_bf16 a[224:227], v[176:179], a[104:107], a[224:227]\n"
"R 172,190,40960\nv_mul_f32 v237, v63, v237\nv_mfma_f32_16x16x32_bf16 a[224:227], v[180:183], a[120:123], a[224:227]\nv_exp_f32 v235, v235\nv_mfma_f32_16x16x32_bf16 a[240:243], v[176:179], a[108:111], a[240:243]\nR 174,190,45056\n"
"v_fma_f32 v234, v234, v219, v219\nv_mfma_f32_16x16x32_bf16 a[240:243], v[180:183], a[124:127], a[240:243]\nG 4\nv_exp_f32 v236, v236\nv_mfma_f32_16x16x32_bf16 a[244:247], v[160:163], a[108:111], a[244:247]\nR 176,191,32768\n"
"v_fma_f32 v235, v235, v219, v219\nv_mfma_f32_16x16x32_bf16 a[244:247], v[164:167], a[124:127], a[244:247]\nbuffer_load_dwordx4 v221, s[16:19], s63 offen offset:1024 lds\nv_mfma_f32_16x16x32_bf16 a[228:231], v[160:163], a[104:107], a[228:231]\nR 178,191,36864\ns_add_u32 s63, s58, 720896\n"
"v_mfma_f32_16x16x32_bf16 a[228:231], v[164:167], a[120:123], a[228:231]\ns_add_u32 m0, s50, 49152\nv_exp_f32 v237, v237\nM 20,160,100\nR 180,191,40960\nv_fma_f32 v236, v236, v219, v219\n"
"M 20,164,116\nbuffer_load_dwordx4 v220, s[16:19], s63 offen lds\nM 4,160,96\nR 182,191,45056\nv_fma_f32 v237, v237, v219, v219\nM 4,164,112\n"
"G 4\nv_rcp_f32 v234, v234\nM 8,168,96\nR 160,188,49152\nv_mul_f32 v234, v60, v234\nM 8,172,112\n"
"buffer_load_dwordx4 v221, s[16:19], s63 offen offset:1024 lds\nM 24,168,100\nR 162,188,53248\ns_add_u32 s63, s58, 393216\nLpg14:\ns_cbranch_execz LretO0n\n"
"M 24,172,116\ns_add_u32 m0, s50, 57344\nv_rcp_f32 v235, v235\nv_mfma_f32_16x16x32_bf16 a[232:235], v[168:171], a[104:107], a[232:235]\nR 164,184,0\nv_mul_f32 v235, v61, v235\n"
"v_mfma_f32_16x16x32_bf16 a[232:235], v[172:175], a[120:123], a[232:235]\nbuffer_load_dwordx4 v220, s[16:19], s63 offen lds\nv_mfma_f32_16x16x32_bf16 a[248:251], v[168:171], a[108:111], a[248:251]\nR 166,184,4096\nv_cvt_pk_bf16_f32 v58, v234, v235\nv_mfma_f32_16x16x32_bf16 a[248:251], v[172:175], a[124:127], a[248:251]\n"
"G 4\nv_rcp_f32 v236, v236\nv_mfma_f32_16x16x32_bf16 a[252:255], v[176:179], a[108:111], a[252:255]\nR 168,189,49152\nv_mul_f32 v236, v62, v236\nv_mfma_f32_16x16x32_bf16 a[252:255], v[180:183], a[124:127], a[252:255]\n"
"buffer_load_dwordx4 v221, s[16:19], s63 offen offset:1024 lds\nv_mfma_f32_16x16x32_bf16 a[236:239], v[176:179], a[104:107], a[236:239]\nR 170,189,53248\nv_rcp_f32 v237, v237\nv_mfma_f32_16x16x32_bf16 a[236:239], v[180:183], a[120:123], a[236:239]\ns_add_u32 m0, s50, 65536\n"
"v_mul_f32 v237, v63, v237\nv_cvt_pk_bf16_f32 v59, v236, v237\nM 28,176,100\nR 172,185,0\nM 28,180,116\ns_add_u32 s63, s58, 458752\n"
"buffer_store_dwordx4 v[56:59], v252, s[20:23], s85 offen offset:1024 sc1\nM 12,176,96\nR 174,185,4096\nM 12,180,112\ns_waitcnt vmcnt(9)\ns_barrier\n"
"G 4\nv_mfma_f32_16x16x32_bf16 v[32:35], v[160:163], a[0:3], 0\nR 176,190,49152\nM 32,164,16\nbuffer_load_dwordx4 v220, s[16:19], s63 offen lds\nv_mfma_f32_16x16x32_bf16 v[48:51], v[160:163], a[4:7], 0\n"
"R 178,190,53248\nM 48,164,20\nv_mfma_f32_16x16x32_bf16 v[64:67], v[160:163], a[8:11], 0\nR 180,186,0\nM 64,164,24\nv_mfma_f32_16x16x32_bf16 v[80:83], v[160:163], a[12:15], 0\n"
"R 182,186,4096\nM 80,164,28\nG 4\nv_mfma_f32_16x16x32_bf16 v[84:87], v[168:171], a[12:15], 0\nR 160,191,49152\nM 84,172,28\n"
"buffer_load_dwordx4 v221, s[16:19], s63 offen offset:1024 lds\nv_mfma_f32_16x16x32_bf16 v[68:71], v[168:171], a[8:11], 0\nR 162,191,53248\nM 68,172,24\ns_add_u32 m0, s50, 73728\nv_mfma_f32_16x16x32_bf16 v[52:55], v[168:171], a[4:7], 0\n"
"R 164,187,0\nM 52,172,20\ns_add_u32 s63, s56, 393216\nv_mfma_f32_16x16x32_bf16 v[36:39], v[168:171], a[0:3], 0\nR 166,187,4096\nM 36,172,16\n"
"G 4\nv_mfma_f32_16x16x32_bf16 v[40:43], v[176:179], a[0:3], 0\nR 168,184,8192\nM 40,180,16\nbuffer_load_dwordx4 v220, s[16:19], s63 offen lds\nv_mfma_f32_16x16x32_bf16 v[56:59], v[176:179], a[4:7], 0\n"
"R 170,184,12288\nM 56,180,20\nv_mfma_f32_16x16x32_bf16 v[72:75], v[176:179], a[8:11], 0\nR 172,184,16384\nM 72,180,24\nv_mfma_f32_16x16x32_bf16 v[88:91], v[176:179], a[12:15], 0\n"
"R 174,184,20480\nM 88,180,28\nG 4\nv_mfma_f32_16x16x32_bf16 v[92:95], v[160:163], a[12:15], 0\nR 176,185,8192\nM 92,164,28\n"
"buffer_load_dwordx4 v221, s[16:19], s63 offen offset:1024 lds\nv_mfma_f32_16x16x32_bf16 v[76:79], v[160:163], a[8:11], 0\nR 178,185,12288\nM 76,164,24\ns_add_u32 m0, s50, 81920\nv_mfma_f32_16x16x32_bf16 v[60:63], v[160:163], a[4:7], 0\n"
"R 180,185,16384\nM 60,164,20\ns_add_u32 s63, s56, 458752\nv_mfma_f32_16x16x32_bf16 v[44:47], v[160:163], a[0:3], 0\nR 182,185,20480\nM 44,164,16\n"
"G 4\nM 32,168,32\nR 160,186,8192\nM 32,172,48\nbuffer_load_dwordx4 v220, s[16:19], s63 offen lds\nM 48,168,36\n"
"R 162,186,12288\nM 48,172,52\nM 64,168,40\nR 164,186,16384\nM 64,172,56\nbuffer_load_dwordx4 v221, s[16:19], s63 offen offset:1024 lds\n"
"M 80,168,44\nR 166,186,20480\ns_add_u32 m0, s50, 90112\nM 80,172,60\nG 4\nM 84,176,44\n"
"R 168,187,8192\ns_add_u32 s63, s57, 393216\nM 84,180,60\nbuffer_load_dwordx4 v220, s[16:19], s63 offen lds\nM 68,176,40\nR 170,187,12288\n"
"v_mov_b32 v226, s46\nM 68,180,56\nv_mov_b32 v227, s47\nM 52,176,36\nR 172,187,16384\ns_add_u32 s61, s81, 385040\n"
"s_lshl_b32 s62, s44, 5\ns_add_u32 s61, s61, s62\ns_waitcnt vmcnt(7)\ns_mov_b64 exec, 1\nbuffer_atomic_swap_x2 v[226:227], off, s[20:23], s61 sc1\ns_mov_b64 exec, -1\n"
"M 52,180,52\nM 36,176,32\nR 174,187,20480\nM 36,180,48\nG 4\nM 40,160,32\n"
"R 176,184,24576\nM 40,164,48\nbuffer_load_dwordx4 v221, s[16:19], s63 offen offset:1024 lds\nM 56,160,36\nR 178,184,28672\nM 56,164,52\n"
"s_add_u32 m0, s50, 98304\nM 72,160,40\nR 180,184,32768\nM 72,164,56\ns_add_u32 s63, s57, 458752\nM 88,160,44\n"
"R 182,184,36864\nM 88,164,60\nG 4\nM 92,168,44\nR 160,185,24576\nM 92,172,60\n"
"buffer_load_dwordx4 v220, s[16:19], s63 offen lds\nM 76,168,40\nR 162,185,28672\nM 76,172,56\nM 60,168,36\nR 164,185,32768\n"
"M 60,172,52\nbuffer_load_dwordx4 v221, s[16:19], s63 offen offset:1024 lds\nM 44,168,32\nR 166,185,36864\ns_add_u32 s63, s56, 262144\nM 44,172,48\n"
"s_waitcnt vmcnt(9)\ns_barrier\nG 4\nM 32,176,64\nR 168,186,24576\nM 32,180,80\n"
"s_add_u32 m0, s50, 106496\nM 48,176,68\nR 170,186,28672\nM 48,180,84\nbuffer_load_dwordx4 v220, s[16:19], s63 offen lds\nM 64,176,72\n"
"R 172,186,32768\nM 64,180,88\nM 80,176,76\nR 174,186,36864\nM 80,180,92\nG 4\n"
"M 84,160,76\nR 176,187,24576\nM 84,164,92\nbuffer_load_dwordx4 v221, s[16:19], s63 offen offset:1024 lds\nM 68,160,72\nR 178,187,28672\n"
"M 68,164,88\ns_add_u32 m0, s50, 114688\nM 52,160,68\nR 180,187,32768\nv_mov_b32 v248, 0\nM 52,164,84\n"
"s_add_u32 s63, s56, 786432\nv_mov_b32 v249, 0\nM 36,160,64\nR 182,187,36864\ns_add_u32 s61, s82, 385040\ns_mov_b64 exec, 0xf\n"
"buffer_atomic_or_x2 v[248:249], v250, s[20:23], s61 offen sc0 sc1\ns_mov_b64 exec, -1\nM 36,164,80\nG 4\nM 40,168,64\nR 160,184,40960\n"
"M 40,172,80\nbuffer_load_dwordx4 v220, s[16:19], s63 offen lds\nM 56,168,68\nR 162,184,45056\nM 56,172,84\nM 72,168,72\n"
"R 164,184,49152\nM 72,172,88\nM 88,168,76\nR 166,184,53248\nM 88,172,92\nG 4\n"
"M 92,176,76\nR 168,185,40960\nM 92,180,92\nbuffer_load_dwordx4 v221, s[16:19], s63 offen offset:1024 lds\nM 76,176,72\nR 170,185,45056\n"
"M 76,180,88\ns_add_u32 m0, s50, 0\nM 60,176,68\nR 172,185,49152\nM 60,180,84\ns_add_u32 s63, s56, 327680\n"
"M 44,176,64\nR 174,185,53248\nM 44,180,80\nG 4\nM 32,160,96\nR 176,186,40960\n"
"M 32,164,112\nbuffer_load_dwordx4 v220, s[16:19], s63 offen lds\nM 48,160,100\nR 178,186,45056\nM 48,164,116\nM 64,160,104\n"
"R 180,186,49152\nM 64,164,120\nbuffer_load_dwordx4 v221, s[16:19], s63 offen offset:1024 lds\nM 80,160,108\nR 182,186,53248\ns_add_u32 m0, s50, 8192\n"
"M 80,164,124\nG 4\nM 84,168,108\nR 160,187,40960\ns_add_u32 s63, s56, 851968\nM 84,172,124\n"
"buffer_load_dwordx4 v220, s[16:19], s63 offen lds\nM 68,168,104\nR 162,187,45056\nM 68,172,120\nM 52,168,100\nR 164,187,49152\n"
"M 52,172,116\nbuffer_load_dwordx4 v221, s[16:19], s63 offen offset:1024 lds\nM 36,168,96\nR 166,187,53248\ns_add_u32 m0, s50, 16384\nM 36,172,112\n"
"G 4\nM 40,176,96\nR 168,184,57344\ns_add_u32 s63, s57, 262144\nM 40,180,112\nbuffer_load_dwordx4 v220, s[16:19], s63 offen lds\n"
"M 56,176,100\nR 170,184,61440\nM 56,180,116\nM 72,176,104\nR 172,188,0\nM 72,180,120\n"
"M 88,176,108\nR 174,188,4096\ns_waitcnt vmcnt(15)\ns_barrier\nM 88,180,124\nG 4\n"
"M 92,160,108\nR 176,185,57344\nM 92,164,124\ns_add_u32 s64, s68, 23072768\nM 76,160,104\nR 178,185,61440\n"
"M 76,164,120\nbuffer_load_dwordx4 v221, s[16:19], s63 offen offset:1024 lds\nM 60,160,100\nR 180,189,0\ns_waitcnt vmcnt(8)\nv_cmp_eq_u64 s[86:87], s[46:47], v[248:249]\n"
"M 60,164,116\ns_add_u32 s65, s68, 23187456\nM 44,160,96\nR 182,189,4096\ns_and_b32 s62, s86, 0xf\ns_cmp_eq_u32 s62, 0xf\n"
"s_cbranch_scc0 Lq466\nLq467:\nM 44,164,112\nG 4\nM 32,168,128\nR 160,186,57344\n"
"M 32,172,144\nbuffer_load_dwordx4 a[160:163], v252, s[20:23], s64 offen sc1\nM 48,168,132\nR 162,186,61440\nM 48,172,148\ns_add_u32 s66, s69, 23072768\n"
"M 64,168,136\nR 164,190,0\nM 64,172,152\nbuffer_load_dwordx4 a[164:167], v252, s[20:23], s65 offen sc1\nM 80,168,140\nR 166,190,4096\n"
"M 80,172,156\nG 4\nM 84,176,140\nR 168,187,57344\nM 84,180,156\ns_add_u32 s67, s69, 23187456\n"
"M 68,176,136\nR 170,187,61440\nM 68,180,152\nbuffer_load_dwordx4 a[168:171], v252, s[20:23], s66 offen sc1\nM 52,176,132\nR 172,191,0\n"
"M 52,180,148\ns_add_u32 s64, s68, 23079936\nM 36,176,128\nR 174,191,4096\nM 36,180,144\nG 4\n"
"M 40,160,128\nR 176,188,8192\nM 40,164,144\nbuffer_load_dwordx4 a[172:175], v252, s[20:23], s67 offen sc1\nM 56,160,132\nR 178,188,12288\n"
"M 56,164,148\ns_add_u32 s65, s68, 23194624\nM 72,160,136\nR 180,188,16384\nLq579:\ns_lshr_b32 s60, s45, 3\n"
"s_lshl2_add_u32 s60, s60, s40\ns_lshl_b32 s60, s60, 8\nv_lshl_add_u32 v224, v252, 3, s60\nv_add_u32 v224, Lq579+4096-LqPC, v224\nv_min_u32 v224, LqE-LqPC-4, v224\ns_mov_b64 exec, 3\n"
"global_load_dword v224, v224, s[98:99]\ns_mov_b64 exec, -1\nM 72,164,152\nbuffer_load_dwordx4 a[176:179], v252, s[20:23], s64 offen sc1\nM 88,160,140\nR 182,188,20480\n"
"s_add_u32 m0, s50, 24576\nM 88,164,156\nG 4\nM 92,168,140\nR 160,189,8192\ns_add_u32 s63, s57, 786432\n"
"M 92,172,156\ns_add_u32 s66, s69, 23079936\nM 76,168,136\nR 162,189,12288\nM 76,172,152\nbuffer_load_dwordx4 a[180:183], v252, s[20:23], s65 offen sc1\n"
"buffer_load_dwordx4 v220, s[16:19], s63 offen lds\nM 60,168,132\nR 164,189,16384\nM 60,172,148\ns_add_u32 s67, s69, 23194624\nM 44,168,128\n"
"R 166,189,20480\nM 44,172,144\nbuffer_load_dwordx4 v221, s[16:19], s63 offen offset:1024 lds\nG 4\nM 96,176,128\nR 168,190,8192\n"
"s_add_u32 m0, s50, 32768\nM 96,180,144\nbuffer_load_dwordx4 a[184:187], v252, s[20:23], s66 offen sc1\nM 112,176,132\nR 170,190,12288\ns_add_u32 s63, s57, 327680\n"
"M 112,180,148\ns_add_u32 s64, s68, 23073792\nbuffer_load_dwordx4 v220, s[16:19], s63 offen lds\nM 128,176,136\nR 172,190,16384\nM 128,180,152\n"
"buffer_load_dwordx4 a[188:191], v252, s[20:23], s67 offen sc1\nM 144,176,140\nR 174,190,20480\nM 144,180,156\nbuffer_load_dwordx4 v221, s[16:19], s63 offen offset:1024 lds\nG 4\n"
"M 148,160,140\nR 176,191,8192\ns_waitcnt vmcnt(19)\ns_barrier\nM 148,164,156\ns_add_u32 s65, s68, 23188480\n"
"s_add_u32 s63, s57, 851968\nM 132,160,136\nR 178,191,12288\ns_add_u32 m0, s50, 40960\nM 132,164,152\nbuffer_load_dwordx4 a[192:195], v252, s[20:23], s64 offen sc1\n"
"M 116,160,132\nR 180,191,16384\nbuffer_load_dwordx4 v220, s[16:19], s63 offen lds\nM 116,164,148\ns_add_u32 s66, s69, 23073792\nM 100,160,128\n"
"R 182,191,20480\nM 100,164,144\nG 4\nM 104,168,128\nR 160,188,24576\nbuffer_load_dwordx4 v221, s[16:19], s63 offen offset:1024 lds\n"
"M 104,172,144\nbuffer_load_dwordx4 a[196:199], v252, s[20:23], s65 offen sc1\nM 120,168,132\nR 162,188,28672\nM 120,172,148\ns_add_u32 s67, s69, 23188480\n"
"M 136,168,136\nR 164,188,32768\nM 136,172,152\nbuffer_load_dwordx4 a[200:203], v252, s[20:23], s66 offen sc1\nM 152,168,140\nR 166,188,36864\n"
"M 152,172,156\nG 4\nM 156,176,140\nR 168,189,24576\nM 156,180,156\nbuffer_load_dwordx4 a[204:207], v252, s[20:23], s67 offen sc1\n"
"M 140,176,136\nR 170,189,28672\nM 140,180,152\ns_add_u32 s64, s68, 23080960\nM 124,176,132\nR 172,189,32768\n"
"M 124,180,148\ns_add_u32 s65, s68, 23195648\nM 108,176,128\nR 174,189,36864\nM 108,180,144\nG 4\n"
"M 0,160,128\nR 176,190,24576\nM 0,164,144\nbuffer_load_dwordx4 a[208:211], v252, s[20:23], s64 offen sc1\nM 16,160,132\nR 178,190,28672\n"
"M 16,164,148\ns_add_u32 s66, s69, 23080960\nv_mfma_f32_16x16x32_bf16 a[224:227], v[160:163], a[136:139], a[224:227]\nR 180,190,32768\nv_mfma_f32_16x16x32_bf16 a[224:227], v[164:167], a[152:155], a[224:227]\nbuffer_load_dwordx4 a[212:215], v252, s[20:23], s65 offen sc1\n"
"v_mfma_f32_16x16x32_bf16 a[240:243], v[160:163], a[140:143], a[240:243]\nR 182,190,36864\nv_mfma_f32_16x16x32_bf16 a[240:243], v[164:167], a[156:159], a[240:243]\nG 4\nv_mfma_f32_16x16x32_bf16 a[244:247], v[168:171], a[140:143], a[244:247]\nR 160,191,24576\n"
"v_mfma_f32_16x16x32_bf16 a[244:247], v[172:175], a[156:159], a[244:247]\nbuffer_load_dwordx4 a[216:219], v252, s[20:23], s66 offen sc1\nv_mfma_f32_16x16x32_bf16 a[228:231], v[168:171], a[136:139], a[228:231]\nR 162,191,28672\nv_mfma_f32_16x16x32_bf16 a[228:231], v[172:175], a[152:155], a[228:231]\ns_add_u32 s67, s69, 23195648\n"
"M 20,168,132\nR 164,191,32768\nM 20,172,148\nM 4,168,128\nR 166,191,36864\nM 4,172,144\n"
"G 4\nM 8,176,128\nR 168,188,40960\nM 8,180,144\nbuffer_load_dwordx4 a[220:223], v252, s[20:23], s67 offen sc1\nM 24,176,132\n"
"R 170,188,45056\nM 24,180,148\ns_add_u32 m0, s50, 49152\nv_mfma_f32_16x16x32_bf16 a[232:235], v[176:179], a[136:139], a[232:235]\nR 172,188,49152\nv_mfma_f32_16x16x32_bf16 a[232:235], v[180:183], a[152:155], a[232:235]\n"
"s_add_u32 s63, s58, 262144\nv_mfma_f32_16x16x32_bf16 a[248:251], v[176:179], a[140:143], a[248:251]\nR 174,188,53248\nv_mfma_f32_16x16x32_bf16 a[248:251], v[180:183], a[156:159], a[248:251]\nG 4\nv_mfma_f32_16x16x32_bf16 a[252:255], v[160:163], a[140:143], a[252:255]\n"
"R 176,189,40960\nv_mfma_f32_16x16x32_bf16 a[252:255], v[164:167], a[156:159], a[252:255]\nbuffer_load_dwordx4 v220, s[16:19], s63 offen lds\nv_mfma_f32_16x16x32_bf16 a[236:239], v[160:163], a[136:139], a[236:239]\nR 178,189,45056\ns_add_u32 s84, s52, 2048\n"
"v_mfma_f32_16x16x32_bf16 a[236:239], v[164:167], a[152:155], a[236:239]\nv_lshrrev_b32 v244, 5, v252\nv_lshlrev_b32 v244, 5, v244\nM 28,160,132\nR 180,189,49152\nv_bfe_u32 v245, v252, 4, 1\n"
"M 28,164,148\nbuffer_load_dwordx4 v221, s[16:19], s63 offen offset:1024 lds\nM 12,160,128\nR 182,189,53248\nM 12,164,144\ns_waitcnt vmcnt(9)\n"
"s_barrier\nG 4\nM 96,168,160\nR 160,190,40960\nM 96,172,176\ns_add_u32 m0, s50, 57344\n"
"M 112,168,164\nR 162,190,45056\nM 112,172,180\ns_add_u32 s63, s58, 786432\nM 128,168,168\nR 164,190,49152\n"
"v_lshrrev_b32 v230, 8, v252\nM 128,172,184\nbuffer_load_dwordx4 v220, s[16:19], s63 offen lds\nM 144,168,172\nR 166,190,53248\nv_lshlrev_b32 v230, 3, v230\n"
"M 144,172,188\nG 4\nM 148,176,172\nLpg15:\ns_cbranch_execz LretO0n\nR 168,191,40960\n"
"M 148,180,188\ns_and_b32 s60, s41, 7\ns_lshl2_add_u32 s60, s60, s40\nv_add_u32 v244, s60, v244\nM 132,176,168\nR 170,191,45056\n"
"v_mul_u32_u24 v244, 6144, v244\nM 132,180,184\ns_sub_u32 s60, s84, s84\ns_add_u32 s60, s60, 2048\nv_mul_lo_u32 v245, v245, s60\nM 116,176,164\n"
"R 172,191,49152\nv_add_u32 v244, v244, v245\nM 116,180,180\nbuffer_load_dwordx2 v[232:233], v230, s[36:39], s53 offen offset:0\nM 100,176,160\nR 174,191,53248\n"
"s_bfm_b64 exec, 16, 0\nbuffer_load_dword v245, v244, s[4:7], s84 offen\ns_mov_b64 exec, -1\nM 100,180,176\nG 4\nM 104,160,160\n"
"R 176,184,0\nM 104,164,176\nbuffer_load_dwordx4 v221, s[16:19], s63 offen offset:1024 lds\nM 120,160,164\nR 178,184,4096\nM 120,164,180\n"
"s_add_u32 m0, s50, 65536\nM 136,160,168\nR 180,184,8192\nbuffer_load_dwordx2 v[234:235], v230, s[36:39], s53 offen offset:32\nM 136,164,184\ns_add_u32 s63, s58, 327680\n"
"buffer_load_dwordx2 v[236:237], v230, s[36:39], s53 offen offset:64\nM 152,160,172\nR 182,184,12288\nM 152,164,188\nbuffer_load_dwordx2 v[238:239], v230, s[36:39], s53 offen offset:96\nG 4\n"
"M 156,168,172\nR 160,185,0\nv_bfe_u32 v242, v252, 4, 4\nM 156,172,188\nbuffer_load_dwordx4 v220, s[16:19], s63 offen lds\nM 140,168,168\n"
"R 162,185,4096\nv_lshrrev_b32 v243, 8, v252\nM 140,172,184\nv_lshlrev_b32 v243, 2, v243\nv_sub_u32 v242, v242, v243\nM 124,168,164\n"
"R 164,185,8192\nv_lshlrev_b32 v243, 4, v242\nM 124,172,180\ns_mov_b64 s[60:61], 0x3f80\nv_lshlrev_b64 v[208:209], v243, s[60:61]\nM 108,168,160\n"
"R 166,185,12288\ns_bfm_b64 exec, 16, 16\nbuffer_load_dword v245, v244, s[4:7], s84 offen\ns_mov_b64 exec, -1\nM 108,172,176\nG 4\n"
"M 96,176,192\nR 168,186,0\ns_waitcnt vmcnt(14)\nM 96,180,208\nbuffer_load_dwordx4 v221, s[16:19], s63 offen offset:1024 lds\nM 112,176,196\n"
"R 170,186,4096\ns_waitcnt vmcnt(14)\nM 112,180,212\ns_add_u32 m0, s50, 73728\nM 128,176,200\nR 172,186,8192\n"
"s_waitcnt vmcnt(13)\nM 128,180,216\ns_add_u32 s63, s58, 851968\nM 144,176,204\nR 174,186,12288\ns_waitcnt vmcnt(12)\n"
"M 144,180,220\nG 4\nv_cmp_gt_u32 s[60:61], 4, v242\ns_nop 1\nv_cndmask_b32 v212, 0, v208, s[60:61]\nv_cndmask_b32 v208, 0, v208, s[60:61]\n"
"v_cndmask_b32 v213, 0, v209, s[60:61]\nv_cndmask_b32 v209, 0, v209, s[60:61]\nM 148,160,204\nR 176,187,0\nM 148,164,220\nbuffer_load_dwordx4 v220, s[16:19], s63 offen lds\n"
"M 132,160,200\nR 178,187,4096\nM 132,164,216\nM 116,160,196\nR 180,187,8192\nM 116,164,212\n"
"M 100,160,192\nR 182,187,12288\ns_bfm_b64 exec, 16, 32\nbuffer_load_dword v245, v244, s[4:7], s84 offen\ns_mov_b64 exec, -1\nM 100,164,208\n"
"G 4\nM 104,168,192\nR 160,184,16384\nM 104,172,208\nbuffer_load_dwordx4 v221, s[16:19], s63 offen offset:1024 lds\nM 120,168,196\n"
"R 162,184,20480\nM 120,172,212\ns_add_u32 m0, s50, 81920\nM 136,168,200\nR 164,184,24576\nM 136,172,216\n"
"s_add_u32 s63, s59, 0\nM 152,168,204\nR 166,184,28672\nM 152,172,220\nG 4\nM 156,176,204\n"
"R 168,185,16384\nM 156,180,220\nbuffer_load_dwordx4 v220, s[16:19], s63 offen lds\nM 140,176,200\nR 170,185,20480\nM 140,180,216\n"
"M 124,176,196\nR 172,185,24576\nM 124,180,212\nM 108,176,192\nR 174,185,28672\ns_bfm_b64 exec, 16, 48\n"
"buffer_load_dword v245, v244, s[4:7], s84 offen\ns_mov_b64 exec, -1\nM 108,180,208\nG 4\nM 0,160,160\nR 176,186,16384\n"
"M 0,164,176\ns_mov_b32 m0, s51\nM 16,160,164\nR 178,186,20480\nv_cvt_pk_bf16_f32 v128, v128, v129\nv_cvt_pk_bf16_f32 v129, v130, v131\n"
"M 16,164,180\nds_write_addtid_b32 v128 offset:0\nv_cvt_pk_bf16_f32 v130, v132, v133\nv_cvt_pk_bf16_f32 v131, v134, v135\nv_mfma_f32_16x16x32_bf16 a[224:227], v[160:163], a[168:171], a[224:227]\nR 180,186,24576\n"
"v_cvt_pk_bf16_f32 v132, v136, v137\nv_mfma_f32_16x16x32_bf16 a[224:227], v[164:167], a[184:187], a[224:227]\nds_write_addtid_b32 v129 offset:256\nv_cvt_pk_bf16_f32 v133, v138, v139\nv_mfma_f32_16x16x32_bf16 a[240:243], v[160:163], a[172:175], a[240:243]\nR 182,186,28672\n"
"v_cvt_pk_bf16_f32 v134, v140, v141\nv_mfma_f32_16x16x32_bf16 a[240:243], v[164:167], a[188:191], a[240:243]\nds_write_addtid_b32 v130 offset:512\nG 7\nv_cvt_pk_bf16_f32 v135, v142, v143\nv_mfma_f32_16x16x32_bf16 a[244:247], v[168:171], a[172:175], a[244:247]\n"
"R 160,187,16384\nv_cvt_pk_bf16_f32 v136, v144, v145\nv_mfma_f32_16x16x32_bf16 a[244:247], v[172:175], a[188:191], a[244:247]\nds_write_addtid_b32 v131 offset:768\nv_cvt_pk_bf16_f32 v137, v146, v147\nv_mfma_f32_16x16x32_bf16 a[228:231], v[168:171], a[168:171], a[228:231]\n"
"R 162,187,20480\nv_cvt_pk_bf16_f32 v138, v148, v149\nv_mfma_f32_16x16x32_bf16 a[228:231], v[172:175], a[184:187], a[228:231]\nds_write_addtid_b32 v132 offset:1024\nv_cvt_pk_bf16_f32 v139, v150, v151\nM 20,168,164\n"
"R 164,187,24576\nv_cvt_pk_bf16_f32 v140, v152, v153\nM 20,172,180\nds_write_addtid_b32 v133 offset:1280\nv_cvt_pk_bf16_f32 v141, v154, v155\nM 4,168,160\n"
"R 166,187,28672\nv_cvt_pk_bf16_f32 v142, v156, v157\nM 4,172,176\nds_write_addtid_b32 v134 offset:1536\nG 9\nv_cvt_pk_bf16_f32 v143, v158, v159\n"
"M 8,176,160\nR 168,184,32768\nds_write_addtid_b32 v135 offset:1792\nM 8,180,176\nds_write_addtid_b32 v136 offset:2048\nds_write_addtid_b32 v137 offset:2304\n"
"M 24,176,164\nR 170,184,36864\nds_write_addtid_b32 v138 offset:2560\nM 24,180,180\nds_write_addtid_b32 v139 offset:2816\nds_write_addtid_b32 v140 offset:3072\n"
"v_mfma_f32_16x16x32_bf16 a[232:235], v[176:179], a[168:171], a[232:235]\nR 172,184,40960\nds_write_addtid_b32 v141 offset:3328\nv_mfma_f32_16x16x32_bf16 a[232:235], v[180:183], a[184:187], a[232:235]\nds_write_addtid_b32 v142 offset:3584\nds_write_addtid_b32 v143 offset:3840\n"
"v_mfma_f32_16x16x32_bf16 a[248:251], v[176:179], a[172:175], a[248:251]\nR 174,184,45056\ns_add_u32 m0, s50, 81920\nv_mfma_f32_16x16x32_bf16 a[248:251], v[180:183], a[188:191], a[248:251]\nG 14\nv_mfma_f32_16x16x32_bf16 a[252:255], v[160:163], a[172:175], a[252:255]\n"
"R 176,185,32768\ns_waitcnt vmcnt(11) lgkmcnt(2)\ns_barrier\nv_mfma_f32_16x16x32_bf16 a[252:255], v[164:167], a[188:191], a[252:255]\nbuffer_load_dwordx4 v221, s[16:19], s63 offen offset:1024 lds\nv_mfma_f32_16x16x32_bf16 a[236:239], v[160:163], a[168:171], a[236:239]\n"
"R 178,185,36864\ns_add_u32 s63, s59, 524288\nv_mfma_f32_16x16x32_bf16 a[236:239], v[164:167], a[184:187], a[236:239]\ns_add_u32 m0, s50, 90112\nds_read2st64_b32 v[128:129], v225 offset0:0 offset1:1\nM 28,160,164\n"
"R 180,185,40960\nM 28,164,180\nbuffer_load_dwordx4 v220, s[16:19], s63 offen lds\nM 12,160,160\nR 182,185,45056\nM 12,164,176\n"
"ds_read2st64_b32 v[130:131], v225 offset0:2 offset1:3\nG 6\nM 0,168,192\nR 160,186,32768\nM 0,172,208\nbuffer_load_dwordx4 v221, s[16:19], s63 offen offset:1024 lds\n"
"ds_read2st64_b32 v[132:133], v225 offset0:4 offset1:5\nM 16,168,196\nR 162,186,36864\ns_add_u32 s63, s59, 65536\nds_read2st64_b32 v[134:135], v225 offset0:6 offset1:7\nM 16,172,212\n"
"s_add_u32 m0, s50, 98304\nds_read2st64_b32 v[136:137], v225 offset0:8 offset1:9\nv_mfma_f32_16x16x32_bf16 a[224:227], v[168:171], a[200:203], a[224:227]\nR 164,186,40960\nv_mfma_f32_16x16x32_bf16 a[224:227], v[172:175], a[216:219], a[224:227]\nbuffer_load_dwordx4 v220, s[16:19], s63 offen lds\n"
"G 6\nv_mfma_f32_16x16x32_bf16 v[96:99], v[208:211], v[128:131], v[96:99]\nds_read2st64_b32 v[138:139], v225 offset0:10 offset1:11\nv_mfma_f32_16x16x32_bf16 v[100:103], v[210:213], v[128:131], v[100:103]\nds_read2st64_b32 v[140:141], v225 offset0:12 offset1:13\nds_read2st64_b32 v[142:143], v225 offset0:14 offset1:15\n"
"v_mfma_f32_16x16x32_bf16 a[240:243], v[168:171], a[204:207], a[240:243]\nR 166,186,45056\nv_mfma_f32_16x16x32_bf16 a[240:243], v[172:175], a[220:223], a[240:243]\nv_mfma_f32_16x16x32_bf16 a[244:247], v[176:179], a[204:207], a[244:247]\nR 168,187,32768\nG 7\n"
"v_mfma_f32_16x16x32_bf16 v[104:107], v[208:211], v[132:135], v[104:107]\nv_mfma_f32_16x16x32_bf16 v[108:111], v[210:213], v[132:135], v[108:111]\nv_mfma_f32_16x16x32_bf16 a[244:247], v[180:183], a[220:223], a[244:247]\nbuffer_load_dwordx4 v221, s[16:19], s63 offen offset:1024 lds\nv_mfma_f32_16x16x32_bf16 a[228:231], v[176:179], a[200:203], a[228:231]\nR 170,187,36864\n"
"v_mfma_f32_16x16x32_bf16 a[228:231], v[180:183], a[216:219], a[228:231]\ns_add_u32 m0, s50, 106496\nG 5\nv_mfma_f32_16x16x32_bf16 v[112:115], v[208:211], v[136:139], v[112:115]\nv_mfma_f32_16x16x32_bf16 v[116:119], v[210:213], v[136:139], v[116:119]\nG 3\n"
"v_mfma_f32_16x16x32_bf16 v[120:123], v[208:211], v[140:143], v[120:123]\nv_mfma_f32_16x16x32_bf16 v[124:127], v[210:213], v[140:143], v[124:127]\nM 20,176,196\nR 172,187,40960\nM 20,180,212\ns_add_u32 s63, s59, 589824\n"
"M 4,176,192\nR 174,187,45056\nM 4,180,208\nG 4\nM 8,160,192\nR 176,184,49152\n"
"M 8,164,208\nbuffer_load_dwordx4 v220, s[16:19], s63 offen lds\nM 24,160,196\nR 178,184,53248\nM 24,164,212\nv_mfma_f32_16x16x32_bf16 a[232:235], v[160:163], a[200:203], a[232:235]\n"
"R 180,184,57344\nv_cvt_pk_bf16_f32 v120, v120, v121\nv_cvt_pk_bf16_f32 v96, v96, v97\nv_cvt_pk_bf16_f32 v112, v112, v113\nv_cvt_pk_bf16_f32 v121, v122, v123\nv_mfma_f32_16x16x32_bf16 a[232:235], v[164:167], a[216:219], a[232:235]\n"
"buffer_load_dwordx4 v221, s[16:19], s63 offen offset:1024 lds\nv_cvt_pk_bf16_f32 v97, v98, v99\nv_cvt_pk_bf16_f32 v113, v114, v115\nv_cvt_pk_bf16_f32 v122, v124, v125\nv_mfma_f32_16x16x32_bf16 a[248:251], v[160:163], a[204:207], a[248:251]\nR 182,184,61440\n"
"v_cvt_pk_bf16_f32 v123, v126, v127\nv_cvt_pk_bf16_f32 v98, v100, v101\nv_cvt_pk_bf16_f32 v114, v116, v117\nv_mfma_f32_16x16x32_bf16 a[248:251], v[164:167], a[220:223], a[248:251]\ns_bfe_u32 s100, s56, 0x20007\ns_mul_i32 s100, s100, 49152\n"
"s_add_u32 s100, s100, s72\ns_add_u32 s100, s100, 52428800\nG 4\nv_cvt_pk_bf16_f32 v99, v102, v103\nv_cvt_pk_bf16_f32 v115, v118, v119\nv_mfma_f32_16x16x32_bf16 a[252:255], v[168:171], a[204:207], a[252:255]\n"
"R 160,185,49152\ns_add_u32 s63, s59, 131072\nv_mfma_f32_16x16x32_bf16 a[252:255], v[172:175], a[220:223], a[252:255]\nbuffer_store_dwordx4 v[96:99], v252, s[20:23], s100 offen offset:0 sc1\nv_mfma_f32_16x16x32_bf16 a[236:239], v[168:171], a[200:203], a[236:239]\nR 162,185,53248\n"
"v_cvt_pk_bf16_f32 v104, v104, v105\nv_mfma_f32_16x16x32_bf16 a[236:239], v[172:175], a[216:219], a[236:239]\ns_add_u32 m0, s50, 114688\nv_cvt_pk_bf16_f32 v105, v106, v107\nv_cvt_pk_bf16_f32 v106, v108, v109\nM 28,168,196\n"
"R 164,185,57344\nv_cvt_pk_bf16_f32 v107, v110, v111\nM 28,172,212\nbuffer_load_dwordx4 v220, s[16:19], s63 offen lds\nM 12,168,192\nR 166,185,61440\n"
"M 12,172,208\nbuffer_store_dwordx4 v[104:107], v252, s[20:23], s100 offen offset:1024 sc1\ns_waitcnt vmcnt(12)\ns_barrier\nG 4\nM 32,176,160\n"
"R 168,186,49152\nM 32,180,176\nv_xor_b32 v240, 0x2000, v225\nM 48,176,164\nR 170,186,53248\nds_write2st64_b32 v240, a224, a225 offset0:0 offset1:1\n"
"M 48,180,180\nbuffer_store_dwordx4 v[112:115], v252, s[20:23], s100 offen offset:2048 sc1\nds_write2st64_b32 v240, a226, a227 offset0:2 offset1:3\nM 64,176,168\nR 172,186,57344\nds_write2st64_b32 v240, a228, a229 offset0:4 offset1:5\n"
"M 64,180,184\nds_write2st64_b32 v240, a230, a231 offset0:6 offset1:7\nM 80,176,172\nR 174,186,61440\nds_write2st64_b32 v240, a232, a233 offset0:8 offset1:9\nM 80,180,188\n"
"ds_write2st64_b32 v240, a234, a235 offset0:10 offset1:11\nG 10\nM 84,160,172\nR 176,187,49152\nds_write2st64_b32 v240, a236, a237 offset0:12 offset1:13\nM 84,164,188\n"
"buffer_store_dwordx4 v[120:123], v252, s[20:23], s100 offen offset:3072 sc1\nds_write2st64_b32 v240, a238, a239 offset0:14 offset1:15\nM 68,160,168\nR 178,187,53248\nds_write2st64_b32 v240, a240, a241 offset0:16 offset1:17\nM 68,164,184\n"
"ds_write2st64_b32 v240, a242, a243 offset0:18 offset1:19\nM 52,160,164\nR 180,187,57344\nds_write2st64_b32 v240, a244, a245 offset0:20 offset1:21\nM 52,164,180\nds_write2st64_b32 v240, a246, a247 offset0:22 offset1:23\n"
"M 36,160,160\nR 182,187,61440\nds_write2st64_b32 v240, a248, a249 offset0:24 offset1:25\nM 36,164,176\nds_write2st64_b32 v240, a250, a251 offset0:26 offset1:27\nG 14\n"
"M 40,168,160\nR 160,188,0\nds_write2st64_b32 v240, a252, a253 offset0:28 offset1:29\nM 40,172,176\nbuffer_load_dwordx4 v221, s[16:19], s63 offen offset:1024 lds\nM 56,168,164\n"
"R 162,188,4096\ns_add_u32 s63, s59, 655360\nM 56,172,180\ns_add_u32 m0, s50, 0\nds_write2st64_b32 v240, a254, a255 offset0:30 offset1:31\nM 72,168,168\n"
"R 164,188,8192\nM 72,172,184\nbuffer_load_dwordx4 v220, s[16:19], s63 offen lds\nM 88,168,172\nR 166,188,12288\nM 88,172,188\n"
"G 8\nM 92,176,172\nR 168,189,0\ns_waitcnt vmcnt(11) lgkmcnt(3)\ns_barrier\nM 92,180,188\n"
"buffer_load_dwordx4 v221, s[16:19], s63 offen offset:1024 lds\nds_read2st64_b32 v[128:129], v225 offset0:0 offset1:1\nM 76,176,168\nR 170,189,4096\ns_add_u32 s63, s59, 196608\nds_read2st64_b32 v[130:131], v225 offset0:2 offset1:3\n"
"M 76,180,184\ns_add_u32 m0, s50, 8192\nds_read2st64_b32 v[132:133], v225 offset0:4 offset1:5\nds_read2st64_b32 v[134:135], v225 offset0:6 offset1:7\nM 60,176,164\nR 172,189,8192\n"
"ds_read2st64_b32 v[136:137], v225 offset0:8 offset1:9\nM 60,180,180\nbuffer_load_dwordx4 v220, s[16:19], s63 offen lds\nds_read2st64_b32 v[138:139], v225 offset0:10 offset1:11\nM 44,176,160\nR 174,189,12288\n"
"ds_read2st64_b32 v[140:141], v225 offset0:12 offset1:13\nM 44,180,176\nds_read2st64_b32 v[142:143], v225 offset0:14 offset1:15\nds_read2st64_b32 v[144:145], v225 offset0:16 offset1:17\nG 13\nM 32,160,192\n"
"R 176,190,0\nds_read2st64_b32 v[146:147], v225 offset0:18 offset1:19\nG 13\nv_add_f32 v0, v0, v128\nM 32,164,208\nbuffer_load_dwordx4 v221, s[16:19], s63 offen offset:1024 lds\n"
"ds_read2st64_b32 v[148:149], v225 offset0:20 offset1:21\nM 48,160,196\nR 178,190,4096\nds_read2st64_b32 v[150:151], v225 offset0:22 offset1:23\nv_add_f32 v1, v1, v129\nM 48,164,212\n"
"s_add_u32 m0, s50, 16384\nds_read2st64_b32 v[152:153], v225 offset0:24 offset1:25\nds_read2st64_b32 v[154:155], v225 offset0:26 offset1:27\nM 64,160,200\nR 180,190,8192\nds_read2st64_b32 v[156:157], v225 offset0:28 offset1:29\n"
"G 15\nv_add_f32 v2, v2, v130\nM 64,164,216\ns_add_u32 s63, s59, 720896\nds_read2st64_b32 v[158:159], v225 offset0:30 offset1:31\nv_add_f32 v3, v3, v131\n"
"M 80,160,204\nR 182,190,12288\nv_add_f32 v4, v4, v132\nv_add_f32 v5, v5, v133\nM 80,164,220\nG 14\n"
"v_add_f32 v6, v6, v134\nv_add_f32 v7, v7, v135\nv_add_f32 v8, v8, v136\nM 84,168,204\nR 160,191,0\nv_add_f32 v9, v9, v137\n"
"v_add_f32 v10, v10, v138\nM 84,172,220\nbuffer_load_dwordx4 v220, s[16:19], s63 offen lds\nv_add_f32 v11, v11, v139\nM 68,168,200\nR 162,191,4096\n"
"G 15\nv_add_f32 v12, v12, v140\nv_add_f32 v13, v13, v141\nM 68,172,216\nG 14\nv_add_f32 v14, v14, v142\n"
"v_add_f32 v15, v15, v143\nG 13\nv_add_f32 v16, v16, v144\nM 52,168,196\nR 164,191,8192\nv_add_f32 v17, v17, v145\n"
"G 12\nv_add_f32 v18, v18, v146\nM 52,172,212\nbuffer_load_dwordx4 v221, s[16:19], s63 offen offset:1024 lds\nv_add_f32 v19, v19, v147\nM 36,168,192\n"
"R 166,191,12288\ns_add_u32 m0, s50, 24576\nG 12\nv_add_f32 v20, v20, v148\nM 36,172,208\nG 4\n"
"v_add_f32 v21, v21, v149\nv_add_f32 v22, v22, v150\nv_add_f32 v23, v23, v151\nM 40,176,192\nR 168,188,16384\ns_add_u32 s63, s59, 393216\n"
"v_add_f32 v24, v24, v152\nM 40,180,208\nbuffer_load_dwordx4 v220, s[16:19], s63 offen lds\nv_add_f32 v25, v25, v153\nM 56,176,196\nR 170,188,20480\n"
"v_add_f32 v26, v26, v154\nv_add_f32 v27, v27, v155\nM 56,180,212\nv_add_f32 v28, v28, v156\nv_add_f32 v29, v29, v157\nv_add_f32 v30, v30, v158\n"
"v_add_f32 v31, v31, v159\nM 72,176,200\nR 172,188,24576\nv_cvt_pk_bf16_f32 v24, v24, v25\nv_cvt_pk_bf16_f32 v0, v0, v1\nv_cvt_pk_bf16_f32 v25, v26, v27\n"
"M 72,180,216\nbuffer_load_dwordx4 v221, s[16:19], s63 offen offset:1024 lds\nv_cvt_pk_bf16_f32 v26, v28, v29\nM 88,176,204\nR 174,188,28672\nv_cvt_pk_bf16_f32 v27, v30, v31\n"
"v_cvt_pk_bf16_f32 v1, v2, v3\nM 88,180,220\nG 4\nv_cvt_pk_bf16_f32 v2, v4, v5\nv_cvt_pk_bf16_f32 v3, v6, v7\nM 92,160,204\n"
"R 176,189,16384\ns_bfe_u32 s100, s57, 0x20007\ns_mul_i32 s100, s100, 49152\ns_add_u32 s100, s100, s72\ns_add_u32 s100, s100, 52445184\nv_cvt_pk_bf16_f32 v16, v16, v17\n"
"M 92,164,220\nbuffer_store_dwordx4 v[0:3], v252, s[20:23], s100 offen offset:0 sc1\nM 76,160,200\nR 178,189,20480\nv_cvt_pk_bf16_f32 v17, v18, v19\nM 76,164,216\n"
"v_cvt_pk_bf16_f32 v8, v8, v9\nv_cvt_pk_bf16_f32 v9, v10, v11\nM 60,160,196\nR 180,189,24576\nv_cvt_pk_bf16_f32 v10, v12, v13\nM 60,164,212\n"
"v_cvt_pk_bf16_f32 v11, v14, v15\nv_cvt_pk_bf16_f32 v18, v20, v21\nM 44,160,192\nR 182,189,28672\nv_cvt_pk_bf16_f32 v19, v22, v23\nM 44,164,208\n"
"buffer_store_dwordx4 v[8:11], v252, s[20:23], s100 offen offset:1024 sc1\nLpg16:\ns_cbranch_execz LretW0z\ns_waitcnt vmcnt(16)\ns_barrier\nG 4\n"
"v_mfma_f32_16x16x32_bf16 v[96:99], v[168:171], a[0:3], 0\nR 160,190,16384\nM 96,172,16\ns_mov_b32 m0, s51\nv_mfma_f32_16x16x32_bf16 v[112:115], v[168:171], a[4:7], 0\nR 162,190,20480\n"
"v_cvt_pk_bf16_f32 v64, v64, v65\nv_cvt_pk_bf16_f32 v65, v66, v67\nM 112,172,20\nbuffer_store_dwordx4 v[16:19], v252, s[20:23], s100 offen offset:2048 sc1\nv_cvt_pk_bf16_f32 v66, v68, v69\nv_mfma_f32_16x16x32_bf16 v[128:131], v[168:171], a[8:11], 0\n"
"R 164,190,24576\nv_cvt_pk_bf16_f32 v67, v70, v71\nv_cvt_pk_bf16_f32 v68, v72, v73\nM 128,172,24\nds_write_addtid_b32 v64 offset:0\nv_cvt_pk_bf16_f32 v69, v74, v75\n"
"v_cvt_pk_bf16_f32 v70, v76, v77\nv_mfma_f32_16x16x32_bf16 v[144:147], v[168:171], a[12:15], 0\nR 166,190,28672\nv_cvt_pk_bf16_f32 v71, v78, v79\nv_cvt_pk_bf16_f32 v72, v80, v81\nM 144,172,28\n"
"ds_write_addtid_b32 v65 offset:256\nG 6\nv_cvt_pk_bf16_f32 v73, v82, v83\nv_mfma_f32_16x16x32_bf16 v[148:151], v[176:179], a[12:15], 0\nR 168,191,16384\nv_cvt_pk_bf16_f32 v74, v84, v85\n"
"v_cvt_pk_bf16_f32 v75, v86, v87\nM 148,180,28\nbuffer_store_dwordx4 v[24:27], v252, s[20:23], s100 offen offset:3072 sc1\nv_mfma_f32_16x16x32_bf16 v[132:135], v[176:179], a[8:11], 0\nR 170,191,20480\nv_cvt_pk_bf16_f32 v76, v88, v89\n"
"M 132,180,24\nds_write_addtid_b32 v66 offset:512\nv_cvt_pk_bf16_f32 v77, v90, v91\nv_mfma_f32_16x16x32_bf16 v[116:119], v[176:179], a[4:7], 0\nR 172,191,24576\nv_cvt_pk_bf16_f32 v78, v92, v93\n"
"M 116,180,20\nds_write_addtid_b32 v67 offset:768\nv_cvt_pk_bf16_f32 v79, v94, v95\nv_mfma_f32_16x16x32_bf16 v[100:103], v[176:179], a[0:3], 0\nR 174,191,28672\nds_write_addtid_b32 v68 offset:1024\n"
"M 100,180,16\nds_write_addtid_b32 v69 offset:1280\nds_write_addtid_b32 v70 offset:1536\nG 10\nv_mfma_f32_16x16x32_bf16 v[104:107], v[160:163], a[0:3], 0\nR 176,188,32768\n"
"ds_write_addtid_b32 v71 offset:1792\nM 104,164,16\nds_write_addtid_b32 v72 offset:2048\nds_write_addtid_b32 v73 offset:2304\nv_mfma_f32_16x16x32_bf16 v[120:123], v[160:163], a[4:7], 0\nR 178,188,36864\n"
"ds_write_addtid_b32 v74 offset:2560\nM 120,164,20\nds_write_addtid_b32 v75 offset:2816\nds_write_addtid_b32 v76 offset:3072\nv_mfma_f32_16x16x32_bf16 v[136:139], v[160:163], a[8:11], 0\nR 180,188,40960\n"
"ds_write_addtid_b32 v77 offset:3328\nM 136,164,24\nds_write_addtid_b32 v78 offset:3584\nds_write_addtid_b32 v79 offset:3840\nv_mfma_f32_16x16x32_bf16 v[152:155], v[160:163], a[12:15], 0\nR 182,188,45056\n"
"s_add_u32 m0, s50, 32768\nM 152,164,28\ns_add_u32 s63, s59, 458752\nG 15\nv_mfma_f32_16x16x32_bf16 v[156:159], v[168:171], a[12:15], 0\nR 160,189,32768\n"
"s_waitcnt vmcnt(10) lgkmcnt(2)\ns_barrier\nM 156,172,28\nbuffer_load_dwordx4 v220, s[16:19], s63 offen lds\nv_mfma_f32_16x16x32_bf16 v[140:143], v[168:171], a[8:11], 0\nR 162,189,36864\n"
"M 140,172,24\nds_read2st64_b32 v[64:65], v225 offset0:0 offset1:1\nv_mfma_f32_16x16x32_bf16 v[124:127], v[168:171], a[4:7], 0\nR 164,189,40960\nM 124,172,20\nbuffer_load_dwordx4 v221, s[16:19], s63 offen offset:1024 lds\n"
"v_mfma_f32_16x16x32_bf16 v[108:111], v[168:171], a[0:3], 0\nR 166,189,45056\ns_add_u32 m0, s50, 40960\nM 108,172,16\nds_read2st64_b32 v[66:67], v225 offset0:2 offset1:3\nG 6\n"
"M 96,176,32\nR 168,190,32768\ns_add_u32 s63, s59, 262144\nv_bfe_u32 v250, v252, 4, 2\nM 96,180,48\nbuffer_load_dwordx4 v220, s[16:19], s63 offen lds\n"
"ds_read2st64_b32 v[68:69], v225 offset0:4 offset1:5\nv_lshlrev_b32 v250, 3, v250\nM 112,176,36\nR 170,190,36864\nds_read2st64_b32 v[70:71], v225 offset0:6 offset1:7\ns_lshl_b32 s60, s43, 5\n"
"v_add_u32 v250, s60, v250\nM 112,180,52\nds_read2st64_b32 v[72:73], v225 offset0:8 offset1:9\nM 128,176,40\nR 172,190,40960\nM 128,180,56\n"
"buffer_load_dwordx4 v221, s[16:19], s63 offen offset:1024 lds\nG 6\nv_mfma_f32_16x16x32_bf16 v[32:35], v[208:211], v[64:67], v[32:35]\nds_read2st64_b32 v[74:75], v225 offset0:10 offset1:11\nv_mfma_f32_16x16x32_bf16 v[36:39], v[210:213], v[64:67], v[36:39]\nds_read2st64_b32 v[76:77], v225 offset0:12 offset1:13\n"
"ds_read2st64_b32 v[78:79], v225 offset0:14 offset1:15\nM 144,176,44\nR 174,190,45056\ns_add_u32 m0, s50, 49152\nM 144,180,60\nM 148,160,44\n"
"R 176,191,32768\ns_add_u32 s63, s59, 786432\nG 7\nv_mfma_f32_16x16x32_bf16 v[40:43], v[208:211], v[68:71], v[40:43]\nv_mfma_f32_16x16x32_bf16 v[44:47], v[210:213], v[68:71], v[44:47]\nM 148,164,60\n"
"buffer_load_dwordx4 v220, s[16:19], s63 offen lds\nM 132,160,40\nR 178,191,36864\nM 132,164,56\nG 5\nv_mfma_f32_16x16x32_bf16 v[48:51], v[208:211], v[72:75], v[48:51]\n"
"v_mfma_f32_16x16x32_bf16 v[52:55], v[210:213], v[72:75], v[52:55]\nG 3\nv_mfma_f32_16x16x32_bf16 v[56:59], v[208:211], v[76:79], v[56:59]\nv_mfma_f32_16x16x32_bf16 v[60:63], v[210:213], v[76:79], v[60:63]\nM 116,160,36\nR 180,191,40960\n"
"M 116,164,52\nM 100,160,32\nR 182,191,45056\nM 100,164,48\nG 4\nM 104,168,32\n"
"R 160,188,49152\nM 104,172,48\nbuffer_load_dwordx4 v221, s[16:19], s63 offen offset:1024 lds\nM 120,168,36\nR 162,188,53248\nv_mov_b32 v226, s46\n"
"M 120,172,52\nv_mov_b32 v227, s47\nM 136,168,40\nR 164,184,0\ns_bfe_u32 s62, s56, 0x20007\ns_lshl_b32 s62, s62, 5\n"
"s_add_u32 s61, s74, 548864\ns_add_u32 s61, s61, s62\ns_waitcnt vmcnt(6)\ns_mov_b64 exec, 1\nbuffer_atomic_swap_x2 v[226:227], off, s[20:23], s61 sc1\ns_mov_b64 exec, -1\n"
"v_cvt_pk_bf16_f32 v32, v32, v33\nM 136,172,56\ns_bfe_u32 s62, s57, 0x20007\ns_lshl_b32 s62, s62, 5\ns_add_u32 s61, s74, 548872\ns_add_u32 s61, s61, s62\n"
"s_mov_b64 exec, 1\nbuffer_atomic_swap_x2 v[226:227], off, s[20:23], s61 sc1\ns_mov_b64 exec, -1\nM 152,168,44\nR 166,184,4096\nv_cvt_pk_bf16_f32 v33, v34, v35\n"
"M 152,172,60\nG 4\nv_cvt_pk_bf16_f32 v34, v36, v37\nv_cvt_pk_bf16_f32 v35, v38, v39\nM 156,176,44\nR 168,189,49152\n"
"s_bfe_u32 s100, s58, 0x20007\ns_mul_i32 s100, s100, 49152\ns_add_u32 s100, s100, s72\ns_add_u32 s100, s100, 52461568\nv_cvt_pk_bf16_f32 v48, v48, v49\nM 156,180,60\n"
"buffer_store_dwordx4 v[32:35], v252, s[20:23], s100 offen offset:0 sc1\nM 140,176,40\nR 170,189,53248\nv_cvt_pk_bf16_f32 v40, v40, v41\nM 140,180,56\nv_cvt_pk_bf16_f32 v41, v42, v43\n"
"v_cvt_pk_bf16_f32 v42, v44, v45\nM 124,176,36\nR 172,185,0\nv_cvt_pk_bf16_f32 v43, v46, v47\nM 124,180,52\nv_cvt_pk_bf16_f32 v49, v50, v51\n"
"v_cvt_pk_bf16_f32 v50, v52, v53\nM 108,176,32\nR 174,185,4096\nv_cvt_pk_bf16_f32 v51, v54, v55\nM 108,180,48\nbuffer_store_dwordx4 v[40:43], v252, s[20:23], s100 offen offset:1024 sc1\n"
"s_barrier\nG 4\nM 96,160,64\nR 176,190,49152\nv_cvt_pk_bf16_f32 v56, v56, v57\nM 96,164,80\n"
"v_cvt_pk_bf16_f32 v57, v58, v59\nv_cvt_pk_bf16_f32 v58, v60, v61\nM 112,160,68\nR 178,190,53248\nv_cvt_pk_bf16_f32 v59, v62, v63\nM 112,164,84\n"
"buffer_store_dwordx4 v[48:51], v252, s[20:23], s100 offen offset:2048 sc1\nM 128,160,72\nR 180,186,0\nM 128,164,88\nM 144,160,76\nR 182,186,4096\n"
"M 144,164,92\nG 4\nM 148,168,76\nR 160,191,49152\nLq581:\ns_lshr_b32 s60, s45, 3\n"
"s_lshl2_add_u32 s60, s60, s40\ns_lshl_b32 s60, s60, 8\nv_lshl_add_u32 v224, v252, 3, s60\nv_add_u32 v224, Lq581+4096-LqPC, v224\nv_min_u32 v224, LqE-LqPC-4, v224\ns_mov_b64 exec, 3\n"
"global_load_dword v224, v224, s[98:99]\ns_mov_b64 exec, -1\nM 148,172,92\nbuffer_store_dwordx4 v[56:59], v252, s[20:23], s100 offen offset:3072 sc1\nM 132,168,72\nR 162,191,53248\n"
"M 132,172,88\nM 116,168,68\nR 164,187,0\nM 116,172,84\nM 100,168,64\nR 166,187,4096\n"
"v_mov_b32 v248, 0\nM 100,172,80\nG 4\nv_mov_b32 v249, 0\nM 104,176,64\nR 168,184,8192\n"
"s_add_u32 s61, s74, 548864\ns_mov_b64 exec, 3\nbuffer_atomic_or_x2 v[248:249], v250, s[20:23], s61 offen sc0 sc1\ns_mov_b64 exec, -1\nM 104,180,80\nM 120,176,68\n"
"R 170,184,12288\nv_mov_b32 v242, v208\nM 120,180,84\ns_add_u32 m0, s50, 57344\nv_mov_b32 v243, v209\nv_mov_b32 v244, v208\n"
"M 136,176,72\nR 172,184,16384\nv_mov_b32 v245, v209\nM 136,180,88\ns_add_u32 s63, s59, 327680\nv_mov_b32 v64, v192\n"
"v_mov_b32 v65, v193\nM 152,176,76\nR 174,184,20480\nv_mov_b32 v70, v194\nM 152,180,92\nG 4\n"
"v_mov_b32 v71, v195\nv_mov_b32 v72, v196\nM 156,160,76\nR 176,185,8192\nv_mov_b32 v73, v197\nM 156,164,92\n"
"buffer_load_dwordx4 v220, s[16:19], s63 offen lds\nM 140,160,72\nR 178,185,12288\nv_mov_b32 v78, v198\nM 140,164,88\nv_mov_b32 v79, v199\n"
"v_mov_b32 v80, v200\nM 124,160,68\nR 180,185,16384\nv_mov_b32 v81, v201\nM 124,164,84\nbuffer_load_dwordx4 v221, s[16:19], s63 offen offset:1024 lds\n"
"M 108,160,64\nR 182,185,20480\ns_add_u32 m0, s50, 65536\nM 108,164,80\nG 4\nv_mov_b32 v86, v202\n"
"v_mov_b32 v87, v203\nM 96,168,96\nR 160,186,8192\ns_add_u32 s63, s59, 851968\nM 96,172,112\nbuffer_load_dwordx4 v220, s[16:19], s63 offen lds\n"
"M 112,168,100\nR 162,186,12288\nv_mov_b32 v88, v204\nM 112,172,116\nv_mov_b32 v89, v205\nv_mov_b32 v94, v206\n"
"M 128,168,104\nR 164,186,16384\nv_mov_b32 v95, v207\nM 128,172,120\nbuffer_load_dwordx4 v221, s[16:19], s63 offen offset:1024 lds\nM 144,168,108\n"
"R 166,186,20480\nv_mov_b32 v32, v232\nM 144,172,124\nG 4\nv_mov_b32 v33, v233\nv_mov_b32 v38, v234\n"
"M 148,176,108\nR 168,187,8192\nv_mov_b32 v39, v235\nM 148,180,124\ns_movk_i32 s63, 6144\nv_mad_u32_u24 v220, v228, s63, v229\n"
"v_add_u32 v221, 3144704, v220\ns_add_u32 m0, s50, 73728\ns_add_u32 s63, s52, 2048\nbuffer_load_dwordx4 v220, s[4:7], s63 offen lds\nM 132,176,104\nR 170,187,12288\n"
"v_mov_b32 v40, v236\nM 132,180,120\nv_mov_b32 v41, v237\nv_mov_b32 v46, v238\nM 116,176,100\nR 172,187,16384\n"
"v_mov_b32 v47, v239\nM 116,180,116\nv_mov_b32 v48, v232\nv_mov_b32 v49, v233\nM 100,176,96\nR 174,187,20480\n"
"v_mov_b32 v54, v234\nM 100,180,112\nG 4\nv_mov_b32 v55, v235\nv_mov_b32 v56, v236\nM 104,160,96\n"
"R 176,184,24576\ns_waitcnt vmcnt(13)\ns_barrier\nM 104,164,112\nbuffer_load_dwordx4 v221, s[4:7], s63 offen offset:1024 lds\nM 120,160,100\n"
"R 178,184,28672\ns_add_u32 s63, s52, 198656\nM 120,164,116\ns_add_u32 m0, s50, 81920\nv_mov_b32 v57, v237\nv_mov_b32 v62, v238\n"
"M 136,160,104\nR 180,184,32768\nv_mov_b32 v63, v239\nM 136,164,120\nbuffer_load_dwordx4 v220, s[4:7], s63 offen lds\nM 152,160,108\n"
"R 182,184,36864\nM 152,164,124\nG 4\nM 156,168,108\nR 160,185,24576\nM 156,172,124\n"
"buffer_load_dwordx4 v221, s[4:7], s63 offen offset:1024 lds\nM 140,168,104\nR 162,185,28672\ns_add_u32 s63, s52, 4096\nM 140,172,120\ns_add_u32 m0, s50, 90112\n"
"M 124,168,100\nR 164,185,32768\nM 124,172,116\nbuffer_load_dwordx4 v220, s[4:7], s63 offen lds\nM 108,168,96\nR 166,185,36864\n"
"v_mov_b32 v226, s46\nM 108,172,112\nG 4\nv_mov_b32 v227, s47\nM 96,176,128\nR 168,186,24576\n"
"s_bfe_u32 s62, s58, 0x20007\ns_lshl_b32 s62, s62, 5\ns_add_u32 s61, s74, 548880\ns_add_u32 s61, s61, s62\ns_waitcnt vmcnt(10)\ns_mov_b64 exec, 1\n"
"buffer_atomic_swap_x2 v[226:227], off, s[20:23], s61 sc1\ns_mov_b64 exec, -1\nM 96,180,144\ns_mul_i32 s64, s43, 49152\nM 112,176,132\nR 170,186,28672\n"
"M 112,180,148\ns_add_u32 s64, s64, s72\nM 128,176,136\nR 172,186,32768\ns_waitcnt vmcnt(10)\nv_cmp_eq_u64 s[86:87], s[46:47], v[248:249]\n"
"s_lshr_b32 s60, s45, 3\ns_lshl2_add_u32 s60, s60, s40\ns_lshl_b32 s60, s60, 8\nv_lshl_add_u32 v224, v252, 3, s60\nv_add_u32 v224, LqM-LqPC, v224\nv_min_u32 v224, LqE-LqPC-4, v224\n"
"s_mov_b64 exec, 3\nglobal_load_dword v224, v224, s[98:99]\ns_mov_b64 exec, -1\nM 128,180,152\ns_add_u32 s64, s64, 52428800\nM 144,176,140\n"
"R 174,186,36864\ns_and_b32 s62, s86, 3\ns_cmp_eq_u32 s62, 3\ns_cbranch_scc0 Lq506\nLq507:\nM 144,180,156\n"
"G 4\nM 148,160,140\nR 176,187,24576\nM 148,164,156\nbuffer_load_dwordx4 v[2:5], v252, s[20:23], s64 offen offset:0 sc1\nM 132,160,136\n"
"R 178,187,28672\nM 132,164,152\nbuffer_load_dwordx4 v[10:13], v252, s[20:23], s64 offen offset:1024 sc1\nM 116,160,132\nR 180,187,32768\nM 116,164,148\n"
"s_mul_i32 s65, s43, 49152\nM 100,160,128\nR 182,187,36864\nM 100,164,144\nbuffer_load_dwordx4 v[18:21], v252, s[20:23], s64 offen offset:2048 sc1\nG 4\n"
"M 104,168,128\nR 160,184,40960\nM 104,172,144\nbuffer_load_dwordx4 v[26:29], v252, s[20:23], s64 offen offset:3072 sc1\nM 120,168,132\nR 162,184,45056\n"
"M 120,172,148\ns_add_u32 s65, s65, s72\nM 136,168,136\nR 164,184,49152\ns_add_u32 s65, s65, 52445184\nM 136,172,152\n"
"buffer_load_dwordx4 v[66:69], v252, s[20:23], s65 offen offset:0 sc1\nM 152,168,140\nR 166,184,53248\nM 152,172,156\nG 4\nM 156,176,140\n"
"R 168,185,40960\ns_waitcnt vmcnt(12)\ns_barrier\nM 156,180,156\nbuffer_load_dwordx4 v[74:77], v252, s[20:23], s65 offen offset:1024 sc1\nM 140,176,136\n"
"R 170,185,45056\nv_mov_b32 v246, 0\nM 140,180,152\nbuffer_load_dwordx4 v[82:85], v252, s[20:23], s65 offen offset:2048 sc1\nv_mov_b32 v247, 0\nM 124,176,132\n"
"R 172,185,49152\ns_add_u32 s61, s74, 548864\ns_mov_b64 exec, 4\nbuffer_atomic_or_x2 v[246:247], v250, s[20:23], s61 offen sc0 sc1\ns_mov_b64 exec, -1\nM 124,180,148\n"
"buffer_load_dwordx4 v[90:93], v252, s[20:23], s65 offen offset:3072 sc1\nM 108,176,128\nR 174,185,53248\nM 108,180,144\nG 4\nM 128,160,168\n"
"R 176,186,40960\nM 128,164,184\nR 178,186,45056\nM 144,160,172\nR 180,186,49152\nM 144,164,188\n"
"R 182,186,53248\nG 4\nM 148,168,172\nR 160,187,40960\nM 148,172,188\nR 162,187,45056\n"
"M 132,168,168\nR 164,187,49152\nM 132,172,184\nR 166,187,53248\nG 4\nM 136,176,168\n"
"R 168,184,57344\nM 136,180,184\nR 170,184,61440\nM 152,176,172\nR 172,188,0\nM 152,180,188\n"
"R 174,188,4096\nG 4\nM 156,160,172\nR 176,185,57344\nM 156,164,188\nR 178,185,61440\n"
"M 140,160,168\nR 180,189,0\nM 140,164,184\nR 182,189,4096\nG 4\nM 128,168,200\n"
"R 160,186,57344\nM 128,172,216\nR 162,186,61440\nM 144,168,204\nR 164,190,0\nM 144,172,220\n"
"R 166,190,4096\nG 4\nM 148,176,204\nR 168,187,57344\nM 148,180,220\nR 170,187,61440\n"
"M 132,176,200\nR 172,191,0\nM 132,180,216\nR 174,191,4096\nG 4\nM 136,160,200\n"
"R 176,184,40960\nbuffer_load_dwordx4 v221, s[4:7], s63 offen offset:1024 lds\nM 136,164,216\nR 178,184,45056\ns_add_u32 m0, s50, 98304\nM 152,160,204\n"
"R 180,184,49152\ns_add_u32 s63, s52, 200704\nM 152,164,220\nR 182,184,53248\nG 4\nM 156,168,204\n"
"R 160,185,40960\nbuffer_load_dwordx4 v220, s[4:7], s63 offen lds\nM 156,172,220\nR 162,185,45056\nM 140,168,200\nR 164,185,49152\n"
"M 140,172,216\nR 166,185,53248\nG 4\nLpg17:\ns_cbranch_execz LretW0z\nM 96,176,160\n"
"R 168,186,40960\nbuffer_load_dwordx4 v221, s[4:7], s63 offen offset:1024 lds\nM 96,180,176\nR 170,186,45056\ns_mov_b32 m0, s51\nM 112,176,164\n"
"R 172,186,49152\nv_cvt_pk_bf16_f32 v128, v128, v129\nv_cvt_pk_bf16_f32 v129, v130, v131\nM 112,180,180\nR 174,186,53248\nG 4\n"
"v_cvt_pk_bf16_f32 v130, v132, v133\nM 116,160,164\nR 176,187,40960\ns_mul_i32 s66, s43, 49152\nv_cvt_pk_bf16_f32 v131, v134, v135\nM 116,164,180\n"
"R 178,187,45056\ns_add_u32 s66, s66, s72\nv_cvt_pk_bf16_f32 v132, v136, v137\nM 100,160,160\nR 180,187,49152\ns_add_u32 s66, s66, 52461568\n"
"v_cvt_pk_bf16_f32 v133, v138, v139\nM 100,164,176\nR 182,187,53248\nG 4\nv_cvt_pk_bf16_f32 v134, v140, v141\nM 104,168,160\n"
"R 160,184,57344\ns_waitcnt vmcnt(4)\nv_cmp_eq_u64 s[86:87], s[46:47], v[246:247]\nv_cvt_pk_bf16_f32 v135, v142, v143\nM 104,172,176\nR 162,184,61440\n"
"v_cvt_pk_bf16_f32 v136, v144, v145\nv_cvt_pk_bf16_f32 v137, v146, v147\nM 120,168,164\nR 164,188,0\ns_and_b32 s62, s86, 4\ns_cmp_eq_u32 s62, 4\n"
"s_cbranch_scc0 Lq522\nLq523:\nv_cvt_pk_bf16_f32 v138, v148, v149\nM 120,172,180\nR 166,188,4096\nG 4\n"
"v_cvt_pk_bf16_f32 v139, v150, v151\nM 124,176,164\nR 168,185,57344\nbuffer_load_dwordx4 v[34:37], v252, s[20:23], s66 offen offset:0 sc1\nv_cvt_pk_bf16_f32 v140, v152, v153\nM 124,180,180\n"
"R 170,185,61440\nv_cvt_pk_bf16_f32 v141, v154, v155\nv_cvt_pk_bf16_f32 v142, v156, v157\nM 108,176,160\nR 172,189,0\nbuffer_load_dwordx4 v[42:45], v252, s[20:23], s66 offen offset:1024 sc1\n"
"ds_write_addtid_b32 v128 offset:0\nds_write_addtid_b32 v129 offset:256\nds_write_addtid_b32 v130 offset:512\nds_write_addtid_b32 v131 offset:768\nds_write_addtid_b32 v132 offset:1024\nds_write_addtid_b32 v133 offset:1280\n"
"ds_write_addtid_b32 v134 offset:1536\nds_write_addtid_b32 v135 offset:1792\nds_write_addtid_b32 v136 offset:2048\nds_write_addtid_b32 v137 offset:2304\nds_write_addtid_b32 v138 offset:2560\nds_write_addtid_b32 v139 offset:2816\n"
"ds_write_addtid_b32 v140 offset:3072\nds_write_addtid_b32 v141 offset:3328\nds_write_addtid_b32 v142 offset:3584\nv_cvt_pk_bf16_f32 v143, v158, v159\nds_write_addtid_b32 v143 offset:3840\nM 108,180,176\n"
"R 174,189,4096\nG 15\nM 96,160,192\nR 176,186,57344\nM 96,164,208\nR 178,186,61440\n"
"buffer_load_dwordx4 v[50:53], v252, s[20:23], s66 offen offset:2048 sc1\nG 3\ns_barrier\nM 112,160,196\nR 180,190,0\ns_add_u32 m0, s50, 106496\n"
"ds_read2st64_b32 v[0:1], v225 offset0:0 offset1:1\nM 112,164,212\nR 182,190,4096\nds_read2st64_b32 v[6:7], v225 offset0:2 offset1:3\nG 6\nM 116,168,196\n"
"R 160,187,57344\nbuffer_load_dwordx4 v[58:61], v252, s[20:23], s66 offen offset:3072 sc1\nds_read2st64_b32 v[8:9], v225 offset0:4 offset1:5\nM 116,172,212\nR 162,187,61440\ns_add_u32 s63, s52, 395264\n"
"ds_read2st64_b32 v[14:15], v225 offset0:6 offset1:7\nM 100,168,192\nR 164,191,0\nds_read2st64_b32 v[16:17], v225 offset0:8 offset1:9\nM 100,172,208\nR 166,191,4096\n"
"v_mfma_f32_16x16x32_bf16 v[96:99], v[242:245], v[64:67], v[96:99]\nbuffer_load_dwordx4 v220, s[4:7], s63 offen lds\nds_read2st64_b32 v[22:23], v225 offset0:10 offset1:11\nG 9\nM 104,176,192\nbuffer_load_dwordx4 v221, s[4:7], s63 offen offset:1024 lds\n"
"v_mfma_f32_16x16x32_bf16 v[100:103], v[242:245], v[68:71], v[100:103]\nds_read2st64_b32 v[24:25], v225 offset0:12 offset1:13\nds_read2st64_b32 v[30:31], v225 offset0:14 offset1:15\nM 104,180,208\ns_add_u32 m0, s50, 114688\ns_add_u32 s63, s52, 591872\n"
"v_mfma_f32_16x16x32_bf16 v[104:107], v[242:245], v[72:75], v[104:107]\nM 120,176,196\nbuffer_load_dwordx4 v220, s[4:7], s63 offen lds\nv_mfma_f32_16x16x32_bf16 v[108:111], v[242:245], v[76:79], v[108:111]\nM 120,180,212\nG 3\n"
"v_mfma_f32_16x16x32_bf16 v[112:115], v[242:245], v[80:83], v[112:115]\nM 124,160,196\nbuffer_load_dwordx4 v221, s[4:7], s63 offen offset:1024 lds\nv_mfma_f32_16x16x32_bf16 v[116:119], v[242:245], v[84:87], v[116:119]\nM 124,164,212\ns_waitcnt vmcnt(11)\n"
"v_mfma_f32_16x16x32_bf16 v[120:123], v[242:245], v[88:91], v[120:123]\nM 108,160,192\nv_mfma_f32_16x16x32_bf16 v[124:127], v[242:245], v[92:95], v[124:127]\nM 108,164,208\nG 0\ns_barrier\n"
"s_lshl_b32 s84, s75, 11\ns_add_u32 s84, s84, s49\ns_add_u32 s84, s84, 18874368\ns_add_u32 s85, s84, 32768\nv_mov_b32 v238, s46\nv_mov_b32 v239, s47\n"
"v_mfma_f32_16x16x32_bf16 v[96:99], v[242:245], v[0:3], v[96:99]\nv_mfma_f32_16x16x32_bf16 v[100:103], v[242:245], v[4:7], v[100:103]\nv_mfma_f32_16x16x32_bf16 v[104:107], v[242:245], v[8:11], v[104:107]\nv_mfma_f32_16x16x32_bf16 v[108:111], v[242:245], v[12:15], v[108:111]\nv_mfma_f32_16x16x32_bf16 v[112:115], v[242:245], v[16:19], v[112:115]\nv_mfma_f32_16x16x32_bf16 v[116:119], v[242:245], v[20:23], v[116:119]\n"
"v_mfma_f32_16x16x32_bf16 v[120:123], v[242:245], v[24:27], v[120:123]\nv_mfma_f32_16x16x32_bf16 v[124:127], v[242:245], v[28:31], v[124:127]\ns_waitcnt vmcnt(7)\nv_mfma_f32_16x16x32_bf16 v[96:99], v[242:245], v[32:35], v[96:99]\nv_mfma_f32_16x16x32_bf16 v[100:103], v[242:245], v[36:39], v[100:103]\ns_waitcnt vmcnt(6)\n"
"v_mfma_f32_16x16x32_bf16 v[104:107], v[242:245], v[40:43], v[104:107]\nv_mfma_f32_16x16x32_bf16 v[108:111], v[242:245], v[44:47], v[108:111]\ns_waitcnt vmcnt(5)\nv_mfma_f32_16x16x32_bf16 v[112:115], v[242:245], v[48:51], v[112:115]\nv_mfma_f32_16x16x32_bf16 v[116:119], v[242:245], v[52:55], v[116:119]\ns_waitcnt vmcnt(4)\n"
"v_mfma_f32_16x16x32_bf16 v[120:123], v[242:245], v[56:59], v[120:123]\nv_mfma_f32_16x16x32_bf16 v[124:127], v[242:245], v[60:63], v[124:127]\nv_cvt_pk_bf16_f32 v192, v96, v97\nv_cvt_pk_bf16_f32 v193, v98, v99\nv_cvt_pk_bf16_f32 v194, v100, v101\nv_cvt_pk_bf16_f32 v195, v102, v103\n"
"buffer_store_dwordx4 v[192:195], v252, s[20:23], s84 offen offset:0 sc1\nv_cvt_pk_bf16_f32 v196, v104, v105\nv_cvt_pk_bf16_f32 v197, v106, v107\nv_cvt_pk_bf16_f32 v198, v108, v109\nv_cvt_pk_bf16_f32 v199, v110, v111\nbuffer_store_dwordx4 v[196:199], v252, s[20:23], s84 offen offset:1024 sc1\n"
"v_cvt_pk_bf16_f32 v200, v112, v113\nv_cvt_pk_bf16_f32 v201, v114, v115\nv_cvt_pk_bf16_f32 v202, v116, v117\nv_cvt_pk_bf16_f32 v203, v118, v119\nbuffer_store_dwordx4 v[200:203], v252, s[20:23], s85 offen offset:0 sc1\nv_cvt_pk_bf16_f32 v204, v120, v121\n"
"v_cvt_pk_bf16_f32 v205, v122, v123\nv_cvt_pk_bf16_f32 v206, v124, v125\nv_cvt_pk_bf16_f32 v207, v126, v127\nbuffer_store_dwordx4 v[204:207], v252, s[20:23], s85 offen offset:1024 sc1\ns_mov_b64 exec, 0\ns_cmp_eq_u32 s40, 1\n"
"s_cbranch_scc1 Lpg19\ns_cmp_eq_u32 s40, 2\ns_cbranch_scc1 Lpg20\ns_cmp_eq_u32 s40, 3\ns_cbranch_scc1 Lpg21\ns_branch Lpg18\n"
"LretW0n:\ns_mov_b64 exec, -1\ns_branch LqM\nLq116:\ns_lshl_b32 s61, s42, 13\ns_add_u32 s61, s61, 84017152\n"
"buffer_load_dwordx4 v[128:131], v230, s[20:23], s61 offen sc1\ns_lshl_b32 s61, s42, 13\ns_add_u32 s61, s61, 84021248\nbuffer_load_dwordx4 v[132:135], v230, s[20:23], s61 offen sc1\ns_waitcnt vmcnt(0)\nv_min3_u32 v232, v129, v131, v133\n"
"v_min3_u32 v232, v232, v135, v135\nv_max3_u32 v233, v129, v131, v133\nv_max3_u32 v233, v233, v135, v135\nv_cmp_ne_u32 s[86:87], s46, v232\nv_cmp_ne_u32 vcc, s46, v233\ns_nop 0\n"
"s_or_b64 s[86:87], s[86:87], vcc\ns_cbranch_scc0 Lq117\ns_sub_u32 s48, s48, 1\ns_cmp_gt_i32 s48, 0\ns_cbranch_scc1 Lq116\ns_branch Lq117\n"
"Lq124:\ns_lshl_b32 s61, s42, 13\ns_add_u32 s61, s61, 84148224\nbuffer_load_dwordx4 v[144:147], v234, s[20:23], s61 offen sc1\ns_lshl_b32 s61, s42, 13\ns_add_u32 s61, s61, 84152320\n"
"buffer_load_dwordx4 v[148:151], v234, s[20:23], s61 offen sc1\ns_waitcnt vmcnt(0)\nv_min3_u32 v236, v145, v147, v149\nv_min3_u32 v236, v236, v151, v151\nv_max3_u32 v237, v145, v147, v149\nv_max3_u32 v237, v237, v151, v151\n"
"v_cmp_ne_u32 s[86:87], s46, v236\nv_cmp_ne_u32 vcc, s46, v237\ns_nop 0\ns_or_b64 s[86:87], s[86:87], vcc\ns_cbranch_scc0 Lq125\ns_sub_u32 s48, s48, 1\n"
"s_cmp_gt_i32 s48, 0\ns_cbranch_scc1 Lq124\ns_branch Lq125\nLq132:\ns_lshl_b32 s61, s42, 13\ns_add_u32 s61, s61, 83886080\n"
"buffer_load_dwordx4 v[128:131], v230, s[20:23], s61 offen sc1\ns_lshl_b32 s61, s42, 13\ns_add_u32 s61, s61, 83890176\nbuffer_load_dwordx4 v[132:135], v230, s[20:23], s61 offen sc1\ns_waitcnt vmcnt(0)\nv_min3_u32 v232, v129, v131, v133\n"
"v_min3_u32 v232, v232, v135, v135\nv_max3_u32 v233, v129, v131, v133\nv_max3_u32 v233, v233, v135, v135\nv_cmp_ne_u32 s[86:87], s46, v232\nv_cmp_ne_u32 vcc, s46, v233\ns_nop 0\n"
"s_or_b64 s[86:87], s[86:87], vcc\ns_cbranch_scc0 Lq133\ns_sub_u32 s48, s48, 1\ns_cmp_gt_i32 s48, 0\ns_cbranch_scc1 Lq132\ns_branch Lq133\n"
"Lq140:\ns_lshr_b32 s84, s49, 4\ns_add_u32 s84, s84, 81788928\nbuffer_load_dwordx4 v[128:131], v252, s[20:23], s84 offen offset:0 sc1\nbuffer_load_dwordx4 v[132:135], v252, s[20:23], s84 offen offset:1024 sc1\nbuffer_load_dwordx4 v[136:139], v252, s[20:23], s84 offen offset:2048 sc1\n"
"buffer_load_dwordx4 v[140:143], v252, s[20:23], s84 offen offset:3072 sc1\ns_mov_b32 s85, 0x0c0c0400\ns_waitcnt vmcnt(3)\nv_perm_b32 v144, v129, v128, s85\nv_perm_b32 v148, v131, v130, s85\nv_lshl_or_b32 v144, v148, 16, v144\n"
"s_waitcnt vmcnt(2)\nv_perm_b32 v145, v133, v132, s85\nv_perm_b32 v149, v135, v134, s85\nv_lshl_or_b32 v145, v149, 16, v145\ns_waitcnt vmcnt(1)\nv_perm_b32 v146, v137, v136, s85\n"
"v_perm_b32 v150, v139, v138, s85\nv_lshl_or_b32 v146, v150, 16, v146\ns_waitcnt vmcnt(0)\nv_perm_b32 v147, v141, v140, s85\nv_perm_b32 v151, v143, v142, s85\nv_lshl_or_b32 v147, v151, 16, v147\n"
"v_min3_u32 v230, v144, v145, v146\nv_min3_u32 v230, v230, v147, v147\nv_max3_u32 v231, v144, v145, v146\nv_max3_u32 v231, v231, v147, v147\nv_cmp_ne_u32 s[86:87], s46, v230\nv_cmp_ne_u32 vcc, s46, v231\n"
"s_nop 0\ns_or_b64 s[86:87], s[86:87], vcc\ns_cbranch_scc0 Lq141\ns_sub_u32 s48, s48, 1\ns_cmp_gt_i32 s48, 0\ns_cbranch_scc1 Lq140\n"
"s_branch Lq141\nLq187:\ns_mov_b64 exec, s[66:67]\nbuffer_load_dwordx4 v[230:233], v252, s[20:23], s65 offen offset:0 sc1\nbuffer_load_dwordx2 v[64:65], v227, s[20:23], s65 offen offset:2048 sc1\nbuffer_load_dwordx2 v[70:71], v227, s[20:23], s65 offen offset:2560 sc1\n"
"s_mov_b64 exec, -1\ns_waitcnt vmcnt(0)\nv_max3_u32 v248, v233, v65, v71\nv_cmp_eq_u32 vcc, -1, v248\ns_nop 1\ns_cbranch_vccz Lq188\n"
"s_sub_u32 s48, s48, 1\ns_cmp_gt_i32 s48, 0\ns_cbranch_scc1 Lq187\ns_branch Lq188\nLq189:\ns_mov_b64 exec, s[66:67]\n"
"buffer_load_dwordx4 v[234:237], v252, s[20:23], s65 offen offset:1024 sc1\nbuffer_load_dwordx2 v[76:77], v227, s[20:23], s65 offen offset:3072 sc1\nbuffer_load_dwordx2 v[82:83], v227, s[20:23], s65 offen offset:3584 sc1\ns_mov_b64 exec, -1\ns_waitcnt vmcnt(0)\nv_max3_u32 v248, v237, v77, v83\n"
"v_cmp_eq_u32 vcc, -1, v248\ns_nop 1\ns_cbranch_vccz Lq190\ns_sub_u32 s48, s48, 1\ns_cmp_gt_i32 s48, 0\ns_cbranch_scc1 Lq189\n"
"s_branch Lq190\nLq317:\ns_lshr_b32 s84, s49, 4\ns_add_u32 s84, s84, 82313216\nbuffer_load_dwordx4 v[0:3], v252, s[20:23], s84 offen offset:0 sc1\nbuffer_load_dwordx4 v[4:7], v252, s[20:23], s84 offen offset:1024 sc1\n"
"buffer_load_dwordx4 v[8:11], v252, s[20:23], s84 offen offset:2048 sc1\nbuffer_load_dwordx4 v[12:15], v252, s[20:23], s84 offen offset:3072 sc1\ns_mov_b32 s85, 0x0c0c0400\ns_waitcnt vmcnt(3)\nv_perm_b32 v16, v1, v0, s85\nv_perm_b32 v20, v3, v2, s85\n"
"v_lshl_or_b32 v16, v20, 16, v16\ns_waitcnt vmcnt(2)\nv_perm_b32 v17, v5, v4, s85\nv_perm_b32 v21, v7, v6, s85\nv_lshl_or_b32 v17, v21, 16, v17\ns_waitcnt vmcnt(1)\n"
"v_perm_b32 v18, v9, v8, s85\nv_perm_b32 v22, v11, v10, s85\nv_lshl_or_b32 v18, v22, 16, v18\ns_waitcnt vmcnt(0)\nv_perm_b32 v19, v13, v12, s85\nv_perm_b32 v23, v15, v14, s85\n"
"v_lshl_or_b32 v19, v23, 16, v19\nv_min3_u32 v230, v16, v17, v18\nv_min3_u32 v230, v230, v19, v19\nv_max3_u32 v231, v16, v17, v18\nv_max3_u32 v231, v231, v19, v19\nv_cmp_ne_u32 s[86:87], s46, v230\n"
"v_cmp_ne_u32 vcc, s46, v231\ns_nop 0\ns_or_b64 s[86:87], s[86:87], vcc\ns_cbranch_scc0 Lq318\ns_sub_u32 s48, s48, 1\ns_cmp_gt_i32 s48, 0\n"
"s_cbranch_scc1 Lq317\ns_branch Lq318\nLq319:\ns_lshl_b32 s61, s42, 13\ns_add_u32 s61, s61, 84279296\nbuffer_load_dwordx4 v[0:3], v238, s[20:23], s61 offen sc1\n"
"s_lshl_b32 s61, s42, 13\ns_add_u32 s61, s61, 84283392\nbuffer_load_dwordx4 v[4:7], v238, s[20:23], s61 offen sc1\ns_waitcnt vmcnt(0)\nv_min3_u32 v240, v1, v3, v5\nv_min3_u32 v240, v240, v7, v7\n"
"v_max3_u32 v241, v1, v3, v5\nv_max3_u32 v241, v241, v7, v7\nv_cmp_ne_u32 s[86:87], s46, v240\nv_cmp_ne_u32 vcc, s46, v241\ns_nop 0\ns_or_b64 s[86:87], s[86:87], vcc\n"
"s_cbranch_scc0 Lq320\ns_sub_u32 s48, s48, 1\ns_cmp_gt_i32 s48, 0\ns_cbranch_scc1 Lq319\ns_branch Lq320\nLq329:\n"
"s_lshl_b32 s61, s42, 13\ns_add_u32 s61, s61, 84410368\nbuffer_load_dwordx4 v[16:19], v242, s[20:23], s61 offen sc1\ns_lshl_b32 s61, s42, 13\ns_add_u32 s61, s61, 84414464\nbuffer_load_dwordx4 v[20:23], v242, s[20:23], s61 offen sc1\n"
"s_waitcnt vmcnt(0)\nv_min3_u32 v244, v17, v19, v21\nv_min3_u32 v244, v244, v23, v23\nv_max3_u32 v245, v17, v19, v21\nv_max3_u32 v245, v245, v23, v23\nv_cmp_ne_u32 s[86:87], s46, v244\n"
"v_cmp_ne_u32 vcc, s46, v245\ns_nop 0\ns_or_b64 s[86:87], s[86:87], vcc\ns_cbranch_scc0 Lq330\ns_sub_u32 s48, s48, 1\ns_cmp_gt_i32 s48, 0\n"
"s_cbranch_scc1 Lq329\ns_branch Lq330\nLq339:\ns_lshl_b32 s61, s42, 13\ns_add_u32 s61, s61, 84541440\nbuffer_load_dwordx4 v[0:3], v238, s[20:23], s61 offen sc1\n"
"s_lshl_b32 s61, s42, 13\ns_add_u32 s61, s61, 84545536\nbuffer_load_dwordx4 v[4:7], v238, s[20:23], s61 offen sc1\ns_waitcnt vmcnt(0)\nv_min3_u32 v240, v1, v3, v5\nv_min3_u32 v240, v240, v7, v7\n"
"v_max3_u32 v241, v1, v3, v5\nv_max3_u32 v241, v241, v7, v7\nv_cmp_ne_u32 s[86:87], s46, v240\nv_cmp_ne_u32 vcc, s46, v241\ns_nop 0\ns_or_b64 s[86:87], s[86:87], vcc\n"
"s_cbranch_scc0 Lq340\ns_sub_u32 s48, s48, 1\ns_cmp_gt_i32 s48, 0\ns_cbranch_scc1 Lq339\ns_branch Lq340\nLq349:\n"
"s_lshl_b32 s61, s42, 13\ns_add_u32 s61, s61, 84672512\nbuffer_load_dwordx4 v[16:19], v242, s[20:23], s61 offen sc1\ns_lshl_b32 s61, s42, 13\ns_add_u32 s61, s61, 84676608\nbuffer_load_dwordx4 v[20:23], v242, s[20:23], s61 offen sc1\n"
"s_waitcnt vmcnt(0)\nv_min3_u32 v244, v17, v19, v21\nv_min3_u32 v244, v244, v23, v23\nv_max3_u32 v245, v17, v19, v21\nv_max3_u32 v245, v245, v23, v23\nv_cmp_ne_u32 s[86:87], s46, v244\n"
"v_cmp_ne_u32 vcc, s46, v245\ns_nop 0\ns_or_b64 s[86:87], s[86:87], vcc\ns_cbranch_scc0 Lq350\ns_sub_u32 s48, s48, 1\ns_cmp_gt_i32 s48, 0\n"
"s_cbranch_scc1 Lq349\ns_branch Lq350\nLq377:\nv_mov_b32 v248, 0\nv_mov_b32 v249, 0\ns_add_u32 s61, s82, 385024\n"
"s_mov_b64 exec, 0xf\nbuffer_atomic_or_x2 v[248:249], v250, s[20:23], s61 offen sc0 sc1\ns_mov_b64 exec, -1\ns_waitcnt vmcnt(0)\nv_cmp_eq_u64 s[86:87], s[46:47], v[248:249]\ns_nop 1\n"
"s_and_b32 s62, s86, 0xf\ns_cmp_eq_u32 s62, 0xf\ns_cbranch_scc1 Lq378\ns_sleep 1\ns_sub_u32 s48, s48, 1\ns_cmp_gt_i32 s48, 0\n"
"s_cbranch_scc1 Lq377\ns_branch Lq378\nLq412:\nv_mov_b32 v248, 0\nv_mov_b32 v249, 0\ns_add_u32 s61, s82, 385032\n"
"s_mov_b64 exec, 0xf\nbuffer_atomic_or_x2 v[248:249], v250, s[20:23], s61 offen sc0 sc1\ns_mov_b64 exec, -1\ns_waitcnt vmcnt(0)\nv_cmp_eq_u64 s[86:87], s[46:47], v[248:249]\ns_nop 1\n"
"s_and_b32 s62, s86, 0xf\ns_cmp_eq_u32 s62, 0xf\ns_cbranch_scc1 Lq413\ns_sleep 1\ns_sub_u32 s48, s48, 1\ns_cmp_gt_i32 s48, 0\n"
"s_cbranch_scc1 Lq412\ns_branch Lq413\nLq447:\nv_mov_b32 v248, 0\nv_mov_b32 v249, 0\ns_add_u32 s61, s82, 385048\n"
"s_mov_b64 exec, 0xf\nbuffer_atomic_or_x2 v[248:249], v250, s[20:23], s61 offen sc0 sc1\ns_mov_b64 exec, -1\ns_waitcnt vmcnt(0)\nv_cmp_eq_u64 s[86:87], s[46:47], v[248:249]\ns_nop 1\n"
"s_and_b32 s62, s86, 0xf\ns_cmp_eq_u32 s62, 0xf\ns_cbranch_scc1 Lq448\ns_sleep 1\ns_sub_u32 s48, s48, 1\ns_cmp_gt_i32 s48, 0\n"
"s_cbranch_scc1 Lq447\ns_branch Lq448\nLq466:\nv_mov_b32 v248, 0\nv_mov_b32 v249, 0\ns_add_u32 s61, s82, 385040\n"
"s_mov_b64 exec, 0xf\nbuffer_atomic_or_x2 v[248:249], v250, s[20:23], s61 offen sc0 sc1\ns_mov_b64 exec, -1\ns_waitcnt vmcnt(0)\nv_cmp_eq_u64 s[86:87], s[46:47], v[248:249]\ns_nop 1\n"
"s_and_b32 s62, s86, 0xf\ns_cmp_eq_u32 s62, 0xf\ns_cbranch_scc1 Lq467\ns_sleep 1\ns_sub_u32 s48, s48, 1\ns_cmp_gt_i32 s48, 0\n"
"s_cbranch_scc1 Lq466\ns_branch Lq467\nLq506:\nv_mov_b32 v248, 0\nv_mov_b32 v249, 0\ns_add_u32 s61, s74, 548864\n"
"s_mov_b64 exec, 3\nbuffer_atomic_or_x2 v[248:249], v250, s[20:23], s61 offen sc0 sc1\ns_mov_b64 exec, -1\ns_waitcnt vmcnt(0)\nv_cmp_eq_u64 s[86:87], s[46:47], v[248:249]\ns_nop 1\n"
"s_and_b32 s62, s86, 3\ns_cmp_eq_u32 s62, 3\ns_cbranch_scc1 Lq507\ns_sleep 1\ns_sub_u32 s48, s48, 1\ns_cmp_gt_i32 s48, 0\n"
"s_cbranch_scc1 Lq506\ns_branch Lq507\nLq522:\nv_mov_b32 v246, 0\nv_mov_b32 v247, 0\ns_add_u32 s61, s74, 548864\n"
"s_mov_b64 exec, 4\nbuffer_atomic_or_x2 v[246:247], v250, s[20:23], s61 offen sc0 sc1\ns_mov_b64 exec, -1\ns_waitcnt vmcnt(0)\nv_cmp_eq_u64 s[86:87], s[46:47], v[246:247]\ns_nop 1\n"
"s_and_b32 s62, s86, 4\ns_cmp_eq_u32 s62, 4\ns_cbranch_scc1 Lq523\ns_sleep 1\ns_sub_u32 s48, s48, 1\ns_cmp_gt_i32 s48, 0\n"
"s_cbranch_scc1 Lq522\ns_branch Lq523\nLqM:\ns_add_u32 s61, s79, 88358912\ns_lshl_b32 s62, s75, 3\ns_add_u32 s61, s61, s62\n"
"s_waitcnt vmcnt(0)\ns_mov_b64 exec, 1\nbuffer_atomic_swap_x2 v[238:239], off, s[20:23], s61 sc1\ns_mov_b64 exec, -1\nLqq1:\nv_bfe_u32 v250, v252, 7, 1\n"
"v_mul_u32_u24 v250, 2176, v250\nv_bfe_u32 v251, v252, 4, 3\nv_lshl_add_u32 v250, v251, 3, v250\ns_lshl_b32 s60, s90, 6\nv_add_u32 v250, s60, v250\ns_sleep 3\n"
"v_mov_b32 v248, 0\nv_mov_b32 v249, 0\ns_add_u32 s61, s80, 88358912\ns_mov_b64 exec, 0xffff\nbuffer_atomic_or_x2 v[248:249], v250, s[20:23], s61 offen sc0 sc1\ns_mov_b64 exec, -1\n"
"s_add_u32 m0, s50, 0\ns_add_u32 s63, s52, 397312\nbuffer_load_dwordx4 v220, s[4:7], s63 offen lds\nbuffer_load_dwordx4 v221, s[4:7], s63 offen offset:1024 lds\ns_add_u32 m0, s50, 8192\ns_add_u32 s63, s52, 593920\n"
"buffer_load_dwordx4 v220, s[4:7], s63 offen lds\nbuffer_load_dwordx4 v221, s[4:7], s63 offen offset:1024 lds\ns_add_u32 m0, s50, 16384\ns_add_u32 s63, s52, 788480\nbuffer_load_dwordx4 v220, s[4:7], s63 offen lds\nbuffer_load_dwordx4 v221, s[4:7], s63 offen offset:1024 lds\n"
"s_add_u32 m0, s50, 24576\ns_add_u32 s63, s52, 985088\nbuffer_load_dwordx4 v220, s[4:7], s63 offen lds\nbuffer_load_dwordx4 v221, s[4:7], s63 offen offset:1024 lds\ns_add_u32 m0, s50, 32768\ns_add_u32 s63, s52, 790528\n"
"buffer_load_dwordx4 v220, s[4:7], s63 offen lds\nbuffer_load_dwordx4 v221, s[4:7], s63 offen offset:1024 lds\ns_waitcnt vmcnt(10)\nv_cmp_eq_u64 s[86:87], s[46:47], v[248:249]\ns_nop 1\ns_and_b32 s88, s86, 0xffff\n"
"s_cmp_eq_u32 s88, 0xffff\ns_cbranch_scc1 Lq595\nv_mov_b32 v246, 0\nv_mov_b32 v247, 0\ns_add_u32 s61, s80, 88358912\ns_mov_b64 exec, 0xffff\n"
"buffer_atomic_or_x2 v[246:247], v250, s[20:23], s61 offen sc0 sc1\ns_mov_b64 exec, -1\nLq595:\ns_add_u32 m0, s50, 40960\ns_add_u32 s63, s52, 987136\nbuffer_load_dwordx4 v220, s[4:7], s63 offen lds\n"
"buffer_load_dwordx4 v221, s[4:7], s63 offen offset:1024 lds\ns_mov_b64 exec, 0\ns_cmp_eq_u32 s40, 1\ns_cbranch_scc1 LretQ1z\ns_cmp_eq_u32 s40, 2\ns_cbranch_scc1 LretQ1z\n"
"s_cmp_eq_u32 s40, 3\ns_cbranch_scc1 LretQ1z\ns_branch LretQ1z\nLretQ1z:\ns_mov_b64 exec, -1\nv_mov_b32 v230, 0\n"
"v_mov_b32 v231, 0\nv_mov_b32 v232, 0\nv_mov_b32 v233, 0\nv_dot2c_f32_bf16 v230, s76, v192\nv_dot2c_f32_bf16 v231, v192, v192\nv_dot2c_f32_bf16 v232, s76, v200\n"
"v_dot2c_f32_bf16 v233, v200, v200\nv_dot2c_f32_bf16 v230, s76, v193\nv_dot2c_f32_bf16 v231, v193, v193\nv_dot2c_f32_bf16 v232, s76, v201\nv_dot2c_f32_bf16 v233, v201, v201\nv_dot2c_f32_bf16 v230, s76, v194\n"
"v_dot2c_f32_bf16 v231, v194, v194\nv_dot2c_f32_bf16 v232, s76, v202\nv_dot2c_f32_bf16 v233, v202, v202\nv_dot2c_f32_bf16 v230, s76, v195\nv_dot2c_f32_bf16 v231, v195, v195\nv_dot2c_f32_bf16 v232, s76, v203\n"
"v_dot2c_f32_bf16 v233, v203, v203\nv_dot2c_f32_bf16 v230, s76, v196\nv_dot2c_f32_bf16 v231, v196, v196\nv_dot2c_f32_bf16 v232, s76, v204\nv_dot2c_f32_bf16 v233, v204, v204\nv_dot2c_f32_bf16 v230, s76, v197\n"
"v_dot2c_f32_bf16 v231, v197, v197\nv_dot2c_f32_bf16 v232, s76, v205\nv_dot2c_f32_bf16 v233, v205, v205\nv_dot2c_f32_bf16 v230, s76, v198\nv_dot2c_f32_bf16 v231, v198, v198\nv_dot2c_f32_bf16 v232, s76, v206\n"
"v_dot2c_f32_bf16 v233, v206, v206\nv_dot2c_f32_bf16 v230, s76, v199\nv_dot2c_f32_bf16 v231, v199, v199\nv_dot2c_f32_bf16 v232, s76, v207\nv_dot2c_f32_bf16 v233, v207, v207\nv_lshrrev_b32 v234, 5, v252\n"
"v_and_b32 v234, 24, v234\nv_bfe_u32 v235, s46, v234, 8\nv_lshrrev_b32 v234, 1, v234\nv_and_b32 v236, 0xf0, v252\nv_add_u32 v234, v234, v236\nv_permlane32_swap_b32 v230, v231\n"
"v_permlane32_swap_b32 v232, v233\nv_add_f32 v230, v230, v231\nv_add_f32 v232, v232, v233\ns_nop 1\nv_permlane16_swap_b32 v230, v232\nv_add_f32 v230, v230, v232\n"
"s_movk_i32 s62, 0xff\nv_bfi_b32 v230, s62, v235, v230\ns_lshr_b32 s61, s49, 4\ns_lshl1_add_u32 s61, s53, s61\ns_add_u32 s61, s61, 82837504\nbuffer_store_dword v230, v234, s[20:23], s61 offen sc1\n"
"s_cmp_eq_u32 s88, 0xffff\ns_cbranch_scc1 Lq596\ns_waitcnt vmcnt(3)\nv_mov_b32 v248, v246\nv_mov_b32 v249, v247\nv_cmp_eq_u64 s[86:87], s[46:47], v[248:249]\n"
"s_nop 1\ns_and_b32 s62, s86, 0xffff\ns_cmp_eq_u32 s62, 0xffff\ns_cbranch_scc1 Lq587\nLq586:\nv_mov_b32 v248, 0\n"
"v_mov_b32 v249, 0\ns_add_u32 s61, s80, 88358912\ns_mov_b64 exec, 0xffff\nbuffer_atomic_or_x2 v[248:249], v250, s[20:23], s61 offen sc0 sc1\ns_mov_b64 exec, -1\ns_waitcnt vmcnt(0)\n"
"v_cmp_eq_u64 s[86:87], s[46:47], v[248:249]\ns_nop 1\ns_and_b32 s62, s86, 0xffff\ns_cmp_eq_u32 s62, 0xffff\nLpg18:\ns_cbranch_execz Lpg22\n"
"s_cbranch_scc1 Lq587\ns_sub_u32 s48, s48, 1\ns_cmp_gt_i32 s48, 0\ns_cbranch_scc1 Lq586\nLq587:\nLq596:\n"
"s_add_u32 s64, s70, 18874368\nbuffer_load_dwordx4 a[0:3], v252, s[20:23], s64 offen offset:0 sc1\ns_add_u32 s65, s70, 18907136\nbuffer_load_dwordx4 a[4:7], v252, s[20:23], s65 offen offset:0 sc1\ns_add_u32 s66, s71, 18874368\nbuffer_load_dwordx4 a[8:11], v252, s[20:23], s66 offen offset:0 sc1\n"
"s_add_u32 s67, s71, 18907136\nbuffer_load_dwordx4 a[12:15], v252, s[20:23], s67 offen offset:0 sc1\nbuffer_load_dwordx4 a[16:19], v252, s[20:23], s64 offen offset:1024 sc1\nbuffer_load_dwordx4 a[20:23], v252, s[20:23], s65 offen offset:1024 sc1\nbuffer_load_dwordx4 a[24:27], v252, s[20:23], s66 offen offset:1024 sc1\nbuffer_load_dwordx4 a[28:31], v252, s[20:23], s67 offen offset:1024 sc1\n"
"s_barrier\nR 160,188,8192\nR 162,188,12288\nR 164,188,16384\nR 166,188,20480\nR 168,189,8192\n"
"R 170,189,12288\nR 172,189,16384\nR 174,189,20480\nR 176,190,8192\nR 178,190,12288\nR 180,190,16384\n"
"R 182,190,20480\nbuffer_load_dwordx4 a[32:35], v252, s[20:23], s64 offen offset:2048 sc1\nbuffer_load_dwordx4 a[36:39], v252, s[20:23], s65 offen offset:2048 sc1\nbuffer_load_dwordx4 a[40:43], v252, s[20:23], s66 offen offset:2048 sc1\nbuffer_load_dwordx4 a[44:47], v252, s[20:23], s67 offen offset:2048 sc1\nbuffer_load_dwordx4 a[48:51], v252, s[20:23], s64 offen offset:3072 sc1\n"
"buffer_load_dwordx4 a[52:55], v252, s[20:23], s65 offen offset:3072 sc1\nbuffer_load_dwordx4 a[56:59], v252, s[20:23], s66 offen offset:3072 sc1\nbuffer_load_dwordx4 a[60:63], v252, s[20:23], s67 offen offset:3072 sc1\ns_waitcnt vmcnt(15) lgkmcnt(10)\nv_mfma_f32_16x16x32_bf16 v[0:3], v[160:163], a[0:3], 0\ns_add_u32 s64, s70, 18878464\n"
"buffer_load_dwordx4 a[64:67], v252, s[20:23], s64 offen offset:0 sc1\ns_waitcnt vmcnt(12) lgkmcnt(8)\nM 0,164,16\nv_mfma_f32_16x16x32_bf16 v[16:19], v[160:163], a[4:7], 0\ns_waitcnt vmcnt(11)\nM 16,164,20\n"
"v_mfma_f32_16x16x32_bf16 v[32:35], v[160:163], a[8:11], 0\ns_waitcnt vmcnt(10)\nM 32,164,24\nv_mfma_f32_16x16x32_bf16 v[48:51], v[160:163], a[12:15], 0\ns_waitcnt vmcnt(9)\nM 48,164,28\n"
"G 4\nv_mfma_f32_16x16x32_bf16 v[52:55], v[168:171], a[12:15], 0\nR 160,191,8192\nM 52,172,28\ns_add_u32 s65, s70, 18911232\nbuffer_load_dwordx4 a[68:71], v252, s[20:23], s65 offen offset:0 sc1\n"
"v_mfma_f32_16x16x32_bf16 v[36:39], v[168:171], a[8:11], 0\nR 162,191,12288\nM 36,172,24\nv_mfma_f32_16x16x32_bf16 v[20:23], v[168:171], a[4:7], 0\nR 164,191,16384\nM 20,172,20\n"
"v_mfma_f32_16x16x32_bf16 v[4:7], v[168:171], a[0:3], 0\nR 166,191,20480\nM 4,172,16\nG 4\nv_mfma_f32_16x16x32_bf16 v[8:11], v[176:179], a[0:3], 0\nR 168,188,24576\n"
"M 8,180,16\ns_add_u32 s66, s71, 18878464\nbuffer_load_dwordx4 a[72:75], v252, s[20:23], s66 offen offset:0 sc1\nv_mfma_f32_16x16x32_bf16 v[24:27], v[176:179], a[4:7], 0\nR 170,188,28672\nM 24,180,20\n"
"s_add_u32 m0, s50, 49152\nv_mfma_f32_16x16x32_bf16 v[40:43], v[176:179], a[8:11], 0\nR 172,188,32768\ns_add_u32 s63, s52, 1181696\nM 40,180,24\ns_add_u32 s67, s71, 18911232\n"
"buffer_load_dwordx4 a[76:79], v252, s[20:23], s67 offen offset:0 sc1\nv_mfma_f32_16x16x32_bf16 v[56:59], v[176:179], a[12:15], 0\nR 174,188,36864\nM 56,180,28\nG 4\nv_mfma_f32_16x16x32_bf16 v[60:63], v[160:163], a[12:15], 0\n"
"R 176,189,24576\nM 60,164,28\nbuffer_load_dwordx4 v220, s[4:7], s63 offen lds\nv_mfma_f32_16x16x32_bf16 v[44:47], v[160:163], a[8:11], 0\nR 178,189,28672\nM 44,164,24\n"
"v_mfma_f32_16x16x32_bf16 v[28:31], v[160:163], a[4:7], 0\nR 180,189,32768\nM 28,164,20\nbuffer_load_dwordx4 a[80:83], v252, s[20:23], s64 offen offset:1024 sc1\nv_mfma_f32_16x16x32_bf16 v[12:15], v[160:163], a[0:3], 0\nR 182,189,36864\n"
"M 12,164,16\nG 4\nv_mfma_f32_16x16x32_bf16 v[64:67], a[0:3], v[168:171], 0\nR 160,190,24576\nN 64,16,172\nv_mfma_f32_16x16x32_bf16 v[68:71], a[4:7], v[168:171], 0\n"
"R 162,190,28672\nN 68,20,172\nbuffer_load_dwordx4 a[84:87], v252, s[20:23], s65 offen offset:1024 sc1\nv_mfma_f32_16x16x32_bf16 v[96:99], a[8:11], v[168:171], 0\nR 164,190,32768\nN 96,24,172\n"
"v_mfma_f32_16x16x32_bf16 v[100:103], a[12:15], v[168:171], 0\nR 166,190,36864\nN 100,28,172\ns_barrier\nG 4\nv_mfma_f32_16x16x32_bf16 v[108:111], a[12:15], v[176:179], 0\n"
"R 168,191,24576\nN 108,28,180\nbuffer_load_dwordx4 v221, s[4:7], s63 offen offset:1024 lds\nv_mfma_f32_16x16x32_bf16 v[104:107], a[8:11], v[176:179], 0\nR 170,191,28672\nN 104,24,180\n"
"buffer_load_dwordx4 a[88:91], v252, s[20:23], s66 offen offset:1024 sc1\nv_mfma_f32_16x16x32_bf16 v[76:79], a[4:7], v[176:179], 0\nR 172,191,32768\nN 76,20,180\nv_mfma_f32_16x16x32_bf16 v[72:75], a[0:3], v[176:179], 0\nR 174,191,36864\n"
"N 72,16,180\nG 4\nv_mfma_f32_16x16x32_bf16 v[80:83], a[0:3], v[160:163], 0\nR 176,188,40960\ns_add_u32 s63, s52, 1378304\nN 80,16,164\n"
"buffer_load_dwordx4 a[92:95], v252, s[20:23], s67 offen offset:1024 sc1\nv_mfma_f32_16x16x32_bf16 v[84:87], a[4:7], v[160:163], 0\nR 178,188,45056\nN 84,20,164\ns_add_u32 m0, s50, 57344\nv_mfma_f32_16x16x32_bf16 v[112:115], a[8:11], v[160:163], 0\n"
"R 180,188,49152\nN 112,24,164\nbuffer_load_dwordx4 v220, s[4:7], s63 offen lds\nv_mfma_f32_16x16x32_bf16 v[116:119], a[12:15], v[160:163], 0\nR 182,188,53248\nN 116,28,164\n"
"G 4\nv_mfma_f32_16x16x32_bf16 v[124:127], a[12:15], v[168:171], 0\nR 160,189,40960\nN 124,28,172\nbuffer_load_dwordx4 a[96:99], v252, s[20:23], s64 offen offset:2048 sc1\nv_mfma_f32_16x16x32_bf16 v[120:123], a[8:11], v[168:171], 0\n"
"R 162,189,45056\nN 120,24,172\nv_mfma_f32_16x16x32_bf16 v[92:95], a[4:7], v[168:171], 0\nR 164,189,49152\nN 92,20,172\nv_mfma_f32_16x16x32_bf16 v[88:91], a[0:3], v[168:171], 0\n"
"R 166,189,53248\nN 88,16,172\nG 4\ns_waitcnt vmcnt(19)\nM 0,176,32\nR 168,190,40960\n"
"s_waitcnt vmcnt(15)\nM 0,180,48\nbuffer_load_dwordx4 a[100:103], v252, s[20:23], s65 offen offset:2048 sc1\nM 16,176,36\nR 170,190,45056\ns_waitcnt vmcnt(15)\n"
"M 16,180,52\nM 32,176,40\nR 172,190,49152\ns_waitcnt vmcnt(14)\nM 32,180,56\nbuffer_load_dwordx4 v221, s[4:7], s63 offen offset:1024 lds\n"
"M 48,176,44\nR 174,190,53248\ns_waitcnt vmcnt(14)\nM 48,180,60\ns_barrier\nG 4\n"
"M 52,160,44\nR 176,191,40960\nM 52,164,60\nbuffer_load_dwordx4 a[104:107], v252, s[20:23], s66 offen offset:2048 sc1\nM 36,160,40\nR 178,191,45056\n"
"M 36,164,56\ns_add_u32 m0, s50, 65536\nM 20,160,36\nR 180,191,49152\ns_add_u32 s63, s52, 1183744\nM 20,164,52\n"
"buffer_load_dwordx4 a[108:111], v252, s[20:23], s67 offen offset:2048 sc1\nM 4,160,32\nR 182,191,53248\nM 4,164,48\nG 4\nM 8,168,32\n"
"R 160,184,0\nM 8,172,48\nbuffer_load_dwordx4 v220, s[4:7], s63 offen lds\nM 24,168,36\nR 162,184,4096\nM 24,172,52\n"
"M 40,168,40\nR 164,184,8192\nM 40,172,56\nbuffer_load_dwordx4 a[112:115], v252, s[20:23], s64 offen offset:3072 sc1\nM 56,168,44\nR 166,184,12288\n"
"M 56,172,60\nG 4\nM 60,176,44\nR 168,185,0\nM 60,180,60\nM 44,176,40\n"
"R 170,185,4096\nM 44,180,56\nbuffer_load_dwordx4 a[116:119], v252, s[20:23], s65 offen offset:3072 sc1\nM 28,176,36\nR 172,185,8192\nM 28,180,52\n"
"M 12,176,32\nR 174,185,12288\nM 12,180,48\nG 4\nN 64,32,160\nR 176,186,0\n"
"N 64,48,164\nbuffer_load_dwordx4 v221, s[4:7], s63 offen offset:1024 lds\nN 68,36,160\nR 178,186,4096\nN 68,52,164\nbuffer_load_dwordx4 a[120:123], v252, s[20:23], s66 offen offset:3072 sc1\n"
"N 96,40,160\nR 180,186,8192\nN 96,56,164\ns_lshr_b32 s84, s49, 4\nN 100,44,160\nR 182,186,12288\n"
"s_add_u32 s84, s84, 82837504\nN 100,60,164\ns_barrier\nG 4\nN 108,44,168\nR 160,187,0\n"
"N 108,60,172\nbuffer_load_dwordx4 a[124:127], v252, s[20:23], s67 offen offset:3072 sc1\nN 104,40,168\nR 162,187,4096\nN 104,56,172\nbuffer_load_dwordx4 v[128:131], v252, s[20:23], s84 offen offset:0 sc1\n"
"N 76,36,168\nR 164,187,8192\nN 76,52,172\nN 72,32,168\nR 166,187,12288\nN 72,48,172\n"
"buffer_load_dwordx4 v[132:135], v252, s[20:23], s84 offen offset:1024 sc1\nG 4\nN 80,32,176\nR 168,184,16384\nN 80,48,180\ns_add_u32 s64, s70, 18882560\n"
"buffer_load_dwordx4 a[128:131], v252, s[20:23], s64 offen offset:0 sc1\nN 84,36,176\nR 170,184,20480\nN 84,52,180\nbuffer_load_dwordx4 v[136:139], v252, s[20:23], s84 offen offset:2048 sc1\nN 112,40,176\n"
"R 172,184,24576\nN 112,56,180\nN 116,44,176\nR 174,184,28672\nN 116,60,180\nbuffer_load_dwordx4 v[140:143], v252, s[20:23], s84 offen offset:3072 sc1\n"
"G 4\nN 124,44,160\nR 176,185,16384\nN 124,60,164\ns_add_u32 s65, s70, 18915328\nbuffer_load_dwordx4 a[132:135], v252, s[20:23], s65 offen offset:0 sc1\n"
"N 120,40,160\nR 178,185,20480\nN 120,56,164\nN 92,36,160\nR 180,185,24576\nN 92,52,164\n"
"N 88,32,160\nR 182,185,28672\nN 88,48,164\nG 4\ns_waitcnt vmcnt(27)\nM 0,168,64\n"
"R 160,186,16384\ns_waitcnt vmcnt(22)\nM 0,172,80\ns_add_u32 s66, s71, 18882560\nbuffer_load_dwordx4 a[136:139], v252, s[20:23], s66 offen offset:0 sc1\nM 16,168,68\n"
"R 162,186,20480\ns_waitcnt vmcnt(22)\nM 16,172,84\nM 32,168,72\nR 164,186,24576\ns_waitcnt vmcnt(20)\n"
"M 32,172,88\ns_add_u32 s67, s71, 18915328\nbuffer_load_dwordx4 a[140:143], v252, s[20:23], s67 offen offset:0 sc1\nM 48,168,76\nR 166,186,28672\ns_waitcnt vmcnt(20)\n"
"M 48,172,92\ns_barrier\nG 4\nM 52,176,76\nR 168,187,16384\nM 52,180,92\n"
"M 36,176,72\nR 170,187,20480\nM 36,180,88\ns_add_u32 m0, s50, 73728\nM 20,176,68\nR 172,187,24576\n"
"s_add_u32 s63, s52, 1380352\nM 20,180,84\nbuffer_load_dwordx4 a[144:147], v252, s[20:23], s64 offen offset:1024 sc1\nM 4,176,64\nR 174,187,28672\nM 4,180,80\n"
"G 4\nM 8,160,64\nR 176,184,32768\nM 8,164,80\nbuffer_load_dwordx4 v220, s[4:7], s63 offen lds\nM 24,160,68\n"
"R 178,184,36864\nM 24,164,84\nbuffer_load_dwordx4 a[148:151], v252, s[20:23], s65 offen offset:1024 sc1\nM 40,160,72\nR 180,184,40960\nM 40,164,88\n"
"buffer_load_dwordx4 v221, s[4:7], s63 offen offset:1024 lds\nM 56,160,76\nR 182,184,45056\nM 56,164,92\nG 4\nM 60,168,76\n"
"R 160,185,32768\nM 60,172,92\nM 44,168,72\nR 162,185,36864\nM 44,172,88\nbuffer_load_dwordx4 a[152:155], v252, s[20:23], s66 offen offset:1024 sc1\n"
"M 28,168,68\nR 164,185,40960\nM 28,172,84\nM 12,168,64\nR 166,185,45056\nM 12,172,80\n"
"G 4\nN 64,64,176\nR 168,186,32768\nN 64,80,180\nbuffer_load_dwordx4 a[156:159], v252, s[20:23], s67 offen offset:1024 sc1\nN 68,68,176\n"
"R 170,186,36864\nN 68,84,180\nN 96,72,176\nR 172,186,40960\ns_mov_b32 s85, 0x0c0c0400\nN 96,88,180\n"
"s_add_u32 m0, s50, 81920\ns_waitcnt vmcnt(13)\nv_perm_b32 v144, v129, v128, s85\nv_perm_b32 v148, v131, v130, s85\nN 100,76,176\nR 174,186,45056\n"
"s_add_u32 s63, s52, 1574912\nN 100,92,180\ns_barrier\nG 4\nN 108,76,160\nR 176,187,32768\n"
"v_lshl_or_b32 v144, v148, 16, v144\nN 108,92,164\nbuffer_load_dwordx4 a[160:163], v252, s[20:23], s64 offen offset:2048 sc1\nN 104,72,160\nR 178,187,36864\ns_waitcnt vmcnt(13)\n"
"v_perm_b32 v145, v133, v132, s85\nN 104,88,164\nbuffer_load_dwordx4 v220, s[4:7], s63 offen lds\nN 76,68,160\nR 180,187,40960\nv_perm_b32 v149, v135, v134, s85\n"
"N 76,84,164\nbuffer_load_dwordx4 v221, s[4:7], s63 offen offset:1024 lds\nN 72,64,160\nR 182,187,45056\ns_add_u32 m0, s50, 90112\nN 72,80,164\n"
"G 4\nv_lshl_or_b32 v145, v149, 16, v145\ns_waitcnt vmcnt(13)\nv_perm_b32 v146, v137, v136, s85\nN 80,64,168\nR 160,184,49152\n"
"s_add_u32 s63, s52, 1771520\nN 80,80,172\nbuffer_load_dwordx4 a[164:167], v252, s[20:23], s65 offen offset:2048 sc1\nN 84,68,168\nR 162,184,53248\nv_perm_b32 v150, v139, v138, s85\n"
"N 84,84,172\nbuffer_load_dwordx4 v220, s[4:7], s63 offen lds\nN 112,72,168\nR 164,184,57344\nv_lshl_or_b32 v146, v150, 16, v146\nN 112,88,172\n"
"buffer_load_dwordx4 v221, s[4:7], s63 offen offset:1024 lds\nN 116,76,168\nR 166,184,61440\ns_waitcnt vmcnt(15)\nv_perm_b32 v147, v141, v140, s85\nN 116,92,172\n"
"G 4\nv_perm_b32 v151, v143, v142, s85\nv_lshl_or_b32 v147, v151, 16, v147\nN 124,76,176\nR 168,185,49152\nv_min3_u32 v230, v144, v145, v146\n"
"N 124,92,180\nbuffer_load_dwordx4 a[168:171], v252, s[20:23], s66 offen offset:2048 sc1\nN 120,72,176\nR 170,185,53248\nv_min3_u32 v230, v230, v147, v147\nN 120,88,180\n"
"v_max3_u32 v231, v144, v145, v146\nv_max3_u32 v231, v231, v147, v147\nN 92,68,176\nR 172,185,57344\nv_cmp_ne_u32 s[86:87], s46, v230\nN 92,84,180\n"
"buffer_load_dwordx4 a[172:175], v252, s[20:23], s67 offen offset:2048 sc1\nN 88,64,176\nR 174,185,61440\nv_cmp_ne_u32 vcc, s46, v231\nN 88,80,180\ns_or_b64 s[86:87], s[86:87], vcc\n"
"s_cbranch_scc1 Lq645\nLq646:\nG 4\nM 0,160,96\nR 176,186,49152\nv_and_b32 v128, 0xffffff00, v128\n"
"M 0,164,112\nv_and_b32 v129, 0xffffff00, v129\nv_and_b32 v130, 0xffffff00, v130\nM 16,160,100\nR 178,186,53248\nv_and_b32 v131, 0xffffff00, v131\n"
"M 16,164,116\ns_add_u32 m0, s50, 98304\nv_and_b32 v132, 0xffffff00, v132\nv_and_b32 v133, 0xffffff00, v133\nM 32,160,104\nR 180,186,57344\n"
"s_add_u32 s63, s52, 1576960\nM 32,164,120\nbuffer_load_dwordx4 a[176:179], v252, s[20:23], s64 offen offset:3072 sc1\nM 48,160,108\nR 182,186,61440\nv_and_b32 v134, 0xffffff00, v134\n"
"M 48,164,124\ns_waitcnt vmcnt(11)\ns_barrier\nG 4\nM 52,168,108\nR 160,187,49152\n"
"v_and_b32 v135, 0xffffff00, v135\nM 52,172,124\nbuffer_load_dwordx4 v220, s[4:7], s63 offen lds\nM 36,168,104\nR 162,187,53248\nv_and_b32 v136, 0xffffff00, v136\n"
"M 36,172,120\nbuffer_load_dwordx4 a[180:183], v252, s[20:23], s65 offen offset:3072 sc1\nM 20,168,100\nR 164,187,57344\nv_and_b32 v137, 0xffffff00, v137\nM 20,172,116\n"
"buffer_load_dwordx4 v221, s[4:7], s63 offen offset:1024 lds\nM 4,168,96\nR 166,187,61440\ns_add_u32 m0, s50, 106496\nM 4,172,112\nG 4\n"
"v_and_b32 v138, 0xffffff00, v138\nv_and_b32 v139, 0xffffff00, v139\nM 8,176,96\nR 168,188,0\ns_add_u32 s63, s52, 1773568\nM 8,180,112\n"
"buffer_load_dwordx4 v220, s[4:7], s63 offen lds\nM 24,176,100\nR 170,188,4096\nv_and_b32 v140, 0xffffff00, v140\nM 24,180,116\nbuffer_load_dwordx4 a[184:187], v252, s[20:23], s66 offen offset:3072 sc1\n"
"M 40,176,104\nR 172,188,8192\nv_and_b32 v141, 0xffffff00, v141\nM 40,180,120\nbuffer_load_dwordx4 v221, s[4:7], s63 offen offset:1024 lds\nM 56,176,108\n"
"R 174,188,12288\nv_and_b32 v142, 0xffffff00, v142\nM 56,180,124\nG 4\nv_and_b32 v143, 0xffffff00, v143\nv_add_f32 v128, v128, v136\n"
"M 60,160,108\nR 176,189,0\nv_add_f32 v129, v129, v137\nM 60,164,124\nbuffer_load_dwordx4 a[188:191], v252, s[20:23], s67 offen offset:3072 sc1\nM 44,160,104\n"
"R 178,189,4096\nv_add_f32 v130, v130, v138\nM 44,164,120\nv_add_f32 v131, v131, v139\nv_add_f32 v132, v132, v140\nM 28,160,100\n"
"R 180,189,8192\nv_add_f32 v133, v133, v141\nM 28,164,116\nv_add_f32 v134, v134, v142\nv_add_f32 v135, v135, v143\nM 12,160,96\n"
"R 182,189,12288\nv_add_f32 v128, v128, v132\nM 12,164,112\nG 4\nv_add_f32 v129, v129, v133\nLpg19:\n"
"s_cbranch_execz LretW0n\nv_add_f32 v130, v130, v134\nN 64,96,168\nR 160,190,0\nv_add_f32 v131, v131, v135\nN 64,112,172\n"
"s_add_u32 s64, s70, 18886656\nbuffer_load_dwordx4 a[192:195], v252, s[20:23], s64 offen offset:0 sc1\nN 68,100,168\nR 162,190,4096\nv_permlane32_swap_b32 v128, v130\nN 68,116,172\n"
"v_permlane32_swap_b32 v129, v131\nv_add_f32 v128, v128, v130\nN 96,104,168\nR 164,190,8192\nv_add_f32 v129, v129, v131\nN 96,120,172\n"
"s_add_u32 m0, s50, 114688\nv_permlane16_swap_b32 v128, v129\nv_add_f32 v230, v128, v129\nN 100,108,168\nR 166,190,12288\ns_add_u32 s63, s52, 1968128\n"
"N 100,124,172\ns_waitcnt vmcnt(11)\ns_barrier\nG 4\nN 108,108,176\nR 168,191,0\n"
"v_mov_b32 v232, v230\nN 108,124,180\ns_add_u32 s65, s70, 18919424\nbuffer_load_dwordx4 a[196:199], v252, s[20:23], s65 offen offset:0 sc1\nN 104,104,176\nR 170,191,4096\n"
"v_permlane16_swap_b32 v230, v232\nN 104,120,180\nbuffer_load_dwordx4 v220, s[4:7], s63 offen lds\nN 76,100,176\nR 172,191,8192\nv_mov_b32 v231, v230\n"
"N 76,116,180\nbuffer_load_dwordx4 v221, s[4:7], s63 offen offset:1024 lds\nN 72,96,176\nR 174,191,12288\ns_add_u32 m0, s50, 0\nN 72,112,180\n"
"G 4\nv_mov_b32 v233, v232\nv_permlane32_swap_b32 v230, v231\nN 80,96,160\nR 176,188,16384\ns_add_u32 s63, s52, 2164736\n"
"N 80,112,164\ns_add_u32 s66, s71, 18886656\nbuffer_load_dwordx4 a[200:203], v252, s[20:23], s66 offen offset:0 sc1\nN 84,100,160\nR 178,188,20480\nv_permlane32_swap_b32 v232, v233\n"
"N 84,116,164\nbuffer_load_dwordx4 v220, s[4:7], s63 offen lds\nN 112,104,160\nR 180,188,24576\nv_mul_f32 v230, 0xba800000, v230\nN 112,120,164\n"
"s_add_u32 s67, s71, 18919424\nbuffer_load_dwordx4 a[204:207], v252, s[20:23], s67 offen offset:0 sc1\nN 116,108,160\nR 182,188,28672\nv_mul_f32 v214, v230, v230\nN 116,124,164\n"
"G 4\nv_mul_f32 v231, 0x3a800000, v231\nv_sub_f32 v231, v231, v214\nN 124,108,168\nR 160,189,16384\nbuffer_load_dwordx4 v221, s[4:7], s63 offen offset:1024 lds\n"
"N 124,124,172\nv_max_f32 v231, 0, v231\nv_add_f32 v231, 0x3727c5ac, v231\nN 120,104,168\nR 162,189,20480\nv_and_b32 v208, s77, v230\n"
"N 120,120,172\nv_rsq_f32 v234, v231\nN 92,100,168\nR 164,189,24576\nv_mul_f32 v216, v231, v234\nN 92,116,172\n"
"buffer_load_dwordx4 a[208:211], v252, s[20:23], s64 offen offset:1024 sc1\nN 88,96,168\nR 166,189,28672\nv_sub_f32 v230, v230, v208\nN 88,112,172\nG 4\n"
"v_cvt_pk_bf16_f32 v209, v230, v216\nv_cvt_pk_bf16_f32 v208, v208, v208\nM 0,176,128\nR 168,190,16384\nv_mov_b32 v214, v234\nM 0,180,144\n"
"s_mov_b64 s[60:61], 0xffff\nv_cndmask_b32 v208, 0, v208, s[60:61]\nv_mul_f32 v215, 0x3e8293ee, v234\nM 16,176,132\nR 170,190,20480\nv_mul_f32 v232, 0xba800000, v232\n"
"M 16,180,148\nbuffer_load_dwordx4 a[212:215], v252, s[20:23], s65 offen offset:1024 sc1\nM 32,176,136\nR 172,190,24576\ns_add_u32 s63, s52, 1970176\nM 32,180,152\n"
"s_add_u32 m0, s50, 8192\ns_mov_b64 s[60:61], 0xffff\nv_cndmask_b32 v209, 0, v209, s[60:61]\nM 48,176,140\nR 174,190,28672\nv_mul_f32 v217, v232, v232\n"
"M 48,180,156\ns_waitcnt vmcnt(11)\ns_barrier\nG 4\nM 52,160,140\nR 176,191,16384\n"
"v_mul_f32 v233, 0x3a800000, v233\nM 52,164,156\nbuffer_load_dwordx4 v220, s[4:7], s63 offen lds\nM 36,160,136\nR 178,191,20480\nv_sub_f32 v233, v233, v217\n"
"M 36,164,152\nbuffer_load_dwordx4 a[216:219], v252, s[20:23], s66 offen offset:1024 sc1\nM 20,160,132\nR 180,191,24576\nv_max_f32 v233, 0, v233\nM 20,164,148\n"
"buffer_load_dwordx4 v221, s[4:7], s63 offen offset:1024 lds\nM 4,160,128\nR 182,191,28672\ns_add_u32 m0, s50, 16384\nM 4,164,144\nG 4\n"
"v_add_f32 v233, 0x3727c5ac, v233\nv_and_b32 v212, s77, v232\nM 8,168,128\nR 160,188,32768\ns_add_u32 s63, s52, 2166784\nM 8,172,144\n"
"buffer_load_dwordx4 a[220:223], v252, s[20:23], s67 offen offset:1024 sc1\nM 24,168,132\nR 162,188,36864\nv_sub_f32 v232, v232, v212\nM 24,172,148\nbuffer_load_dwordx4 v220, s[4:7], s63 offen lds\n"
"M 40,168,136\nR 164,188,40960\nv_cvt_pk_bf16_f32 v212, v212, v212\nM 40,172,152\nbuffer_load_dwordx4 v221, s[4:7], s63 offen offset:1024 lds\nM 56,168,140\n"
"R 166,188,45056\nv_bfe_u32 v230, v252, 2, 6\nM 56,172,156\nG 4\nv_rsq_f32 v235, v233\nM 60,176,140\n"
"R 168,189,32768\nv_mul_f32 v219, v233, v235\nM 60,180,156\nbuffer_load_dwordx4 a[224:227], v252, s[20:23], s64 offen offset:2048 sc1\nM 44,176,136\nR 170,189,36864\n"
"v_cvt_pk_bf16_f32 v213, v232, v219\nM 44,180,152\ns_mov_b64 s[60:61], 0xffff\nv_cndmask_b32 v212, 0, v212, s[60:61]\nv_mov_b32 v217, v235\nM 28,176,132\n"
"R 172,189,40960\nv_mul_f32 v218, 0x3e8293ee, v235\nM 28,180,148\ns_mov_b64 s[60:61], 0xffff\nv_cndmask_b32 v213, 0, v213, s[60:61]\nM 12,176,128\n"
"R 174,189,45056\nM 12,180,144\nG 4\nN 64,128,160\nR 176,190,32768\nN 64,144,164\n"
"buffer_load_dwordx4 a[228:231], v252, s[20:23], s65 offen offset:2048 sc1\nN 68,132,160\nR 178,190,36864\nN 68,148,164\ns_lshl_b32 s60, s40, 7\ns_add_u32 s60, s60, 161280\n"
"v_add_u32 v230, s60, v230\nN 96,136,160\nR 180,190,40960\nN 96,152,164\ns_add_u32 m0, s50, 24576\nds_write_b32 v230, v214 offset:0\n"
"N 100,140,160\nR 182,190,45056\ns_add_u32 s63, s52, 2361344\nN 100,156,164\ns_waitcnt vmcnt(10)\ns_barrier\n"
"G 5\nN 108,140,168\nR 160,191,32768\nN 108,156,172\nbuffer_load_dwordx4 a[232:235], v252, s[20:23], s66 offen offset:2048 sc1\nN 104,136,168\n"
"R 162,191,36864\nN 104,152,172\nbuffer_load_dwordx4 v220, s[4:7], s63 offen lds\nN 76,132,168\nR 164,191,40960\nLq688:\n"
"s_lshr_b32 s60, s45, 3\ns_lshl2_add_u32 s60, s60, s40\ns_lshl_b32 s60, s60, 8\nv_lshl_add_u32 v224, v252, 3, s60\nv_add_u32 v224, Lq688+4096-LqPC, v224\nv_min_u32 v224, LqE-LqPC-4, v224\n"
"s_mov_b64 exec, 3\nglobal_load_dword v224, v224, s[98:99]\ns_mov_b64 exec, -1\nN 76,148,172\nbuffer_load_dwordx4 a[236:239], v252, s[20:23], s67 offen offset:2048 sc1\nN 72,128,168\n"
"R 166,191,45056\nN 72,144,172\nds_write_b32 v230, v217 offset:64\nG 5\nN 80,128,176\nR 168,188,49152\n"
"buffer_load_dwordx4 v221, s[4:7], s63 offen offset:1024 lds\nN 80,144,180\ns_add_u32 m0, s50, 32768\ns_add_u32 s63, s52, 2557952\nbuffer_load_dwordx4 v220, s[4:7], s63 offen lds\nN 84,132,176\n"
"R 170,188,53248\nN 84,148,180\nN 112,136,176\nR 172,184,0\nN 112,152,180\nbuffer_load_dwordx4 a[240:243], v252, s[20:23], s64 offen offset:3072 sc1\n"
"N 116,140,176\nR 174,184,4096\nN 116,156,180\nG 5\nN 124,140,160\nR 176,189,49152\n"
"buffer_load_dwordx4 v221, s[4:7], s63 offen offset:1024 lds\nN 124,156,164\nN 120,136,160\nR 178,189,53248\nN 120,152,164\nbuffer_load_dwordx4 a[244:247], v252, s[20:23], s65 offen offset:3072 sc1\n"
"N 92,132,160\nR 180,185,0\nN 92,148,164\nN 88,128,160\nR 182,185,4096\nN 88,144,164\n"
"G 4\nM 0,168,160\nR 160,190,49152\nM 0,172,176\nM 16,168,164\nR 162,190,53248\n"
"M 16,172,180\nbuffer_load_dwordx4 a[248:251], v252, s[20:23], s66 offen offset:3072 sc1\nM 32,168,168\nR 164,186,0\nM 32,172,184\ns_add_u32 m0, s50, 40960\n"
"M 48,168,172\nR 166,186,4096\ns_add_u32 s63, s52, 2754560\nM 48,172,188\ns_waitcnt vmcnt(12)\ns_barrier\n"
"G 4\nM 52,176,172\nR 168,191,49152\nM 52,180,188\nbuffer_load_dwordx4 a[252:255], v252, s[20:23], s67 offen offset:3072 sc1\nM 36,176,168\n"
"R 170,191,53248\nM 36,180,184\nbuffer_load_dwordx4 v220, s[4:7], s63 offen lds\nM 20,176,164\nR 172,187,0\nM 20,180,180\n"
"buffer_load_dwordx4 v221, s[4:7], s63 offen offset:1024 lds\nM 4,176,160\nR 174,187,4096\ns_add_u32 m0, s50, 49152\nM 4,180,176\nG 4\n"
"M 8,160,160\nR 176,184,8192\ns_add_u32 s63, s52, 2951168\nM 8,164,176\nbuffer_load_dwordx4 v220, s[4:7], s63 offen lds\nM 24,160,164\n"
"R 178,184,12288\nM 24,164,180\nM 40,160,168\nR 180,184,16384\nM 40,164,184\nbuffer_load_dwordx4 v221, s[4:7], s63 offen offset:1024 lds\n"
"M 56,160,172\nR 182,184,20480\nM 56,164,188\nG 4\nM 60,168,172\nR 160,185,8192\n"
"M 60,172,188\nM 44,168,168\nR 162,185,12288\nM 44,172,184\ns_add_u32 m0, s50, 57344\nM 28,168,164\n"
"R 164,185,16384\nM 28,172,180\ns_add_u32 s63, s52, 2363392\nM 12,168,160\nR 166,185,20480\nM 12,172,176\n"
"G 4\nN 64,160,176\nR 168,186,8192\nN 64,176,180\nbuffer_load_dwordx4 v220, s[4:7], s63 offen lds\nN 68,164,176\n"
"R 170,186,12288\nN 68,180,180\nN 96,168,176\nR 172,186,16384\nN 96,184,180\nbuffer_load_dwordx4 v221, s[4:7], s63 offen offset:1024 lds\n"
"N 100,172,176\nR 174,186,20480\ns_add_u32 m0, s50, 65536\nN 100,188,180\ns_waitcnt vmcnt(9)\ns_barrier\n"
"G 4\nN 108,172,160\nR 176,187,8192\ns_add_u32 s63, s52, 2560000\nN 108,188,164\nbuffer_load_dwordx4 v220, s[4:7], s63 offen lds\n"
"N 104,168,160\nR 178,187,12288\nN 104,184,164\nN 76,164,160\nR 180,187,16384\nN 76,180,164\n"
"N 72,160,160\nR 182,187,20480\nN 72,176,164\nG 4\nN 80,160,168\nR 160,184,24576\n"
"N 80,176,172\nbuffer_load_dwordx4 v221, s[4:7], s63 offen offset:1024 lds\nN 84,164,168\nR 162,184,28672\ns_add_u32 s63, s52, 2756608\nN 84,180,172\n"
"s_add_u32 m0, s50, 73728\nN 112,168,168\nR 164,184,32768\nN 112,184,172\nbuffer_load_dwordx4 v220, s[4:7], s63 offen lds\nN 116,172,168\n"
"R 166,184,36864\nN 116,188,172\nG 4\nN 124,172,176\nR 168,185,24576\nN 124,188,180\n"
"buffer_load_dwordx4 v221, s[4:7], s63 offen offset:1024 lds\nN 120,168,176\nR 170,185,28672\ns_add_u32 s63, s52, 2953216\nN 120,184,180\ns_add_u32 m0, s50, 81920\n"
"N 92,164,176\nR 172,185,32768\nN 92,180,180\nbuffer_load_dwordx4 v220, s[4:7], s63 offen lds\nN 88,160,176\nR 174,185,36864\n"
"N 88,176,180\nG 4\nM 0,160,192\nR 176,186,24576\nM 0,164,208\nbuffer_load_dwordx4 v221, s[4:7], s63 offen offset:1024 lds\n"
"M 16,160,196\nR 178,186,28672\nM 16,164,212\ns_add_u32 m0, s50, 90112\nM 32,160,200\nR 180,186,32768\n"
"M 32,164,216\ns_add_u32 s63, s52, 0\nM 48,160,204\nR 182,186,36864\nM 48,164,220\ns_waitcnt vmcnt(8)\n"
"s_barrier\nG 4\nM 52,168,204\nR 160,187,24576\nM 52,172,220\nbuffer_load_dwordx4 v220, s[4:7], s63 offen lds\n"
"M 36,168,200\nR 162,187,28672\nM 36,172,216\nM 20,168,196\nR 164,187,32768\nM 20,172,212\n"
"buffer_load_dwordx4 v221, s[4:7], s63 offen offset:1024 lds\nM 4,168,192\nR 166,187,36864\ns_add_u32 m0, s50, 98304\nM 4,172,208\nG 4\n"
"M 8,176,192\nR 168,184,40960\ns_add_u32 s63, s52, 196608\nM 8,180,208\nbuffer_load_dwordx4 v220, s[4:7], s63 offen lds\nM 24,176,196\n"
"R 170,184,45056\nM 24,180,212\nM 40,176,200\nR 172,184,49152\nM 40,180,216\nbuffer_load_dwordx4 v221, s[4:7], s63 offen offset:1024 lds\n"
"M 56,176,204\nR 174,184,53248\ns_add_u32 m0, s50, 106496\nM 56,180,220\nG 4\nM 60,160,204\n"
"R 176,185,40960\ns_add_u32 s63, s52, 393216\nM 60,164,220\nbuffer_load_dwordx4 v220, s[4:7], s63 offen lds\nM 44,160,200\nR 178,185,45056\n"
"M 44,164,216\nM 28,160,196\nR 180,185,49152\nM 28,164,212\nM 12,160,192\nR 182,185,53248\n"
"M 12,164,208\nG 4\nM 0,168,224\nR 160,186,40960\nM 0,172,240\nbuffer_load_dwordx4 v221, s[4:7], s63 offen offset:1024 lds\n"
"M 16,168,228\nR 162,186,45056\ns_add_u32 s63, s52, 589824\nM 16,172,244\ns_add_u32 m0, s50, 114688\nM 32,168,232\n"
"R 164,186,49152\nM 32,172,248\nbuffer_load_dwordx4 v220, s[4:7], s63 offen lds\nM 48,168,236\nR 166,186,53248\nM 48,172,252\n"
"s_waitcnt vmcnt(11)\ns_barrier\nG 4\nM 52,176,236\nR 168,187,40960\nM 52,180,252\n"
"buffer_load_dwordx4 v221, s[4:7], s63 offen offset:1024 lds\nM 36,176,232\nR 170,187,45056\ns_add_u32 s63, s52, 786432\nM 36,180,248\ns_add_u32 m0, s50, 0\n"
"M 20,176,228\nR 172,187,49152\nM 20,180,244\nbuffer_load_dwordx4 v220, s[4:7], s63 offen lds\nM 4,176,224\nR 174,187,53248\n"
"M 4,180,240\nG 4\nM 8,160,224\nR 176,184,57344\nM 8,164,240\nbuffer_load_dwordx4 v221, s[4:7], s63 offen offset:1024 lds\n"
"M 24,160,228\nR 178,184,61440\nM 24,164,244\nM 40,160,232\nR 180,188,0\nM 40,164,248\n"
"M 56,160,236\nR 182,188,4096\ns_mov_b32 s60, 156160\nv_bfe_u32 v246, v252, 4, 4\nv_lshl_add_u32 v246, v246, 3, s60\nM 56,164,252\n"
"ds_read_b64 v[238:239], v246 offset:0\nG 5\nM 60,168,236\nR 160,185,57344\nds_read_b64 v[240:241], v246 offset:128\nM 60,172,252\n"
"ds_read_b64 v[242:243], v246 offset:256\ns_add_u32 m0, s50, 8192\nM 44,168,232\nR 162,185,61440\nds_read_b64 v[244:245], v246 offset:384\nM 44,172,248\n"
"ds_read_b64 v[246:247], v246 offset:0\ns_add_u32 s63, s52, 983040\nM 28,168,228\nR 164,189,0\nM 28,172,244\nbuffer_load_dwordx4 v220, s[4:7], s63 offen lds\n"
"M 12,168,224\nR 166,189,4096\nM 12,172,240\ns_waitcnt vmcnt(11) lgkmcnt(2)\ns_barrier\nN 64,192,176\n"
"R 168,186,57344\nN 64,208,180\ns_mov_b32 m0, s51\nN 68,196,176\nR 170,186,61440\nN 68,212,180\n"
"ds_write_addtid_b32 v32 offset:0\nN 96,200,176\nR 172,190,0\nv_mfma_f32_16x16x32_bf16 v[0:3], v[238:241], v[208:211], v[0:3]\nLpg20:\ns_cbranch_execz LretW0n\n"
"ds_write_addtid_b32 v33 offset:256\nds_write_addtid_b32 v34 offset:512\nN 96,216,180\nds_write_addtid_b32 v35 offset:768\nN 100,204,176\nR 174,190,4096\n"
"v_mfma_f32_16x16x32_bf16 v[4:7], v[240:243], v[208:211], v[4:7]\nds_write_addtid_b32 v36 offset:1024\nds_write_addtid_b32 v37 offset:1280\nN 100,220,180\nds_write_addtid_b32 v38 offset:1536\nG 11\n"
"N 108,204,160\nR 176,187,57344\nv_mfma_f32_16x16x32_bf16 v[8:11], v[242:245], v[208:211], v[8:11]\nds_write_addtid_b32 v39 offset:1792\nds_write_addtid_b32 v40 offset:2048\nN 108,220,164\n"
"ds_write_addtid_b32 v41 offset:2304\nN 104,200,160\nR 178,187,61440\nv_mfma_f32_16x16x32_bf16 v[12:15], v[244:247], v[208:211], v[12:15]\nds_write_addtid_b32 v42 offset:2560\nds_write_addtid_b32 v43 offset:2816\n"
"N 104,216,164\nds_write_addtid_b32 v44 offset:3072\nN 76,196,160\nR 180,191,0\nv_mfma_f32_16x16x32_bf16 v[16:19], v[244:247], v[210:213], v[16:19]\nds_write_addtid_b32 v45 offset:3328\n"
"ds_write_addtid_b32 v46 offset:3584\nN 76,212,164\nds_write_addtid_b32 v47 offset:3840\nN 72,192,160\nR 182,191,4096\nv_mfma_f32_16x16x32_bf16 v[20:23], v[238:241], v[210:213], v[20:23]\n"
"ds_write_addtid_b32 v48 offset:4096\nds_write_addtid_b32 v49 offset:4352\nN 72,208,164\nds_write_addtid_b32 v50 offset:4608\nG 15\nN 80,192,168\n"
"R 160,188,8192\nv_mfma_f32_16x16x32_bf16 v[24:27], v[240:243], v[210:213], v[24:27]\nds_write_addtid_b32 v51 offset:4864\nds_write_addtid_b32 v52 offset:5120\nN 80,208,172\nds_write_addtid_b32 v53 offset:5376\n"
"N 84,196,168\nR 162,188,12288\nv_mfma_f32_16x16x32_bf16 v[28:31], v[242:245], v[210:213], v[28:31]\nds_write_addtid_b32 v54 offset:5632\nds_write_addtid_b32 v55 offset:5888\nN 84,212,172\n"
"ds_write_addtid_b32 v56 offset:6144\nds_write_addtid_b32 v57 offset:6400\nN 112,200,168\nR 164,188,16384\nds_write_addtid_b32 v58 offset:6656\nN 112,216,172\n"
"ds_write_addtid_b32 v59 offset:6912\nds_write_addtid_b32 v60 offset:7168\nN 116,204,168\nR 166,188,20480\nds_write_addtid_b32 v61 offset:7424\nN 116,220,172\n"
"ds_write_addtid_b32 v62 offset:7680\nds_write_addtid_b32 v63 offset:7936\nG 15\nN 124,204,176\nR 168,189,8192\ns_add_u32 m0, s50, 8192\n"
"N 124,220,180\nbuffer_load_dwordx4 v221, s[4:7], s63 offen offset:1024 lds\nN 120,200,176\nR 170,189,12288\ns_waitcnt vmcnt(8) lgkmcnt(2)\ns_barrier\n"
"N 120,216,180\nds_read2st64_b32 v[32:33], v225 offset0:0 offset1:1\nds_read2st64_b32 v[34:35], v225 offset0:2 offset1:3\nN 92,196,176\nR 172,189,16384\nds_read2st64_b32 v[36:37], v225 offset0:4 offset1:5\n"
"N 92,212,180\nds_read2st64_b32 v[38:39], v225 offset0:6 offset1:7\nds_read2st64_b32 v[40:41], v225 offset0:8 offset1:9\nN 88,192,176\nR 174,189,20480\nds_read2st64_b32 v[42:43], v225 offset0:10 offset1:11\n"
"N 88,208,180\nds_read2st64_b32 v[44:45], v225 offset0:12 offset1:13\nds_read2st64_b32 v[46:47], v225 offset0:14 offset1:15\nN 64,224,160\nR 176,190,8192\ns_mov_b32 s60, 156672\n"
"v_bfe_u32 v246, v252, 4, 4\nv_lshl_add_u32 v246, v246, 3, s60\nds_read2st64_b32 v[48:49], v225 offset0:16 offset1:17\nN 64,240,164\nds_read_b64 v[238:239], v246 offset:0\nds_read2st64_b32 v[50:51], v225 offset0:18 offset1:19\n"
"G 13\nv_add_f32 v0, v0, v32\nN 68,228,160\nR 178,190,12288\nds_read_b64 v[240:241], v246 offset:128\nds_read2st64_b32 v[52:53], v225 offset0:20 offset1:21\n"
"N 68,244,164\nds_read_b64 v[242:243], v246 offset:256\ns_add_u32 m0, s50, 16384\nds_read2st64_b32 v[54:55], v225 offset0:22 offset1:23\nN 96,232,160\nR 180,190,16384\n"
"ds_read_b64 v[244:245], v246 offset:384\nN 96,248,164\nds_read_b64 v[246:247], v246 offset:0\ns_add_u32 s63, s52, 1179648\nN 100,236,160\nR 182,190,20480\n"
"v_add_f32 v1, v1, v33\nN 100,252,164\nds_read2st64_b32 v[56:57], v225 offset0:24 offset1:25\nds_read2st64_b32 v[58:59], v225 offset0:26 offset1:27\nG 15\nN 108,236,168\n"
"R 160,191,8192\nv_add_f32 v2, v2, v34\nN 108,252,172\nbuffer_load_dwordx4 v220, s[4:7], s63 offen lds\nN 104,232,168\nR 162,191,12288\n"
"v_add_f32 v3, v3, v35\nN 104,248,172\nds_read2st64_b32 v[60:61], v225 offset0:28 offset1:29\nv_add_f32 v4, v4, v36\nN 76,228,168\nR 164,191,16384\n"
"v_add_f32 v5, v5, v37\nN 76,244,172\nbuffer_load_dwordx4 v221, s[4:7], s63 offen offset:1024 lds\nG 13\nv_mfma_f32_16x16x32_bf16 v[64:67], v[208:211], v[238:241], v[64:67]\nds_read2st64_b32 v[62:63], v225 offset0:30 offset1:31\n"
"v_add_f32 v6, v6, v38\nv_add_f32 v7, v7, v39\nN 72,224,168\nR 166,191,20480\ns_add_u32 m0, s50, 24576\nN 72,240,172\n"
"G 8\nv_add_f32 v8, v8, v40\nv_add_f32 v9, v9, v41\nv_mfma_f32_16x16x32_bf16 v[72:75], v[208:211], v[240:243], v[72:75]\nv_add_f32 v10, v10, v42\nv_add_f32 v11, v11, v43\n"
"N 80,224,176\nR 168,188,24576\ns_add_u32 s63, s52, 1376256\nN 80,240,180\nbuffer_load_dwordx4 v220, s[4:7], s63 offen lds\nv_mfma_f32_16x16x32_bf16 v[80:83], v[208:211], v[242:245], v[80:83]\n"
"v_add_f32 v12, v12, v44\nv_add_f32 v13, v13, v45\nv_add_f32 v14, v14, v46\nN 84,228,176\nR 170,188,28672\nv_add_f32 v15, v15, v47\n"
"N 84,244,180\nbuffer_load_dwordx4 v221, s[4:7], s63 offen offset:1024 lds\nv_mfma_f32_16x16x32_bf16 v[88:91], v[208:211], v[244:247], v[88:91]\nv_add_f32 v16, v16, v48\nv_add_f32 v17, v17, v49\nv_add_f32 v18, v18, v50\n"
"N 112,232,176\nR 172,188,32768\ns_add_u32 s63, s52, 1572864\nN 112,248,180\ns_add_u32 m0, s50, 49152\nv_add_f32 v19, v19, v51\n"
"v_add_f32 v20, v20, v52\nv_mfma_f32_16x16x32_bf16 v[68:71], v[210:213], v[244:247], v[68:71]\nv_add_f32 v21, v21, v53\nv_add_f32 v22, v22, v54\nN 116,236,176\nR 174,188,36864\n"
"v_add_f32 v23, v23, v55\nN 116,252,180\nG 4\nv_add_f32 v24, v24, v56\nv_add_f32 v25, v25, v57\nv_mfma_f32_16x16x32_bf16 v[76:79], v[210:213], v[238:241], v[76:79]\n"
"v_add_f32 v26, v26, v58\nv_add_f32 v27, v27, v59\nN 124,236,160\nR 176,189,24576\nv_add_f32 v28, v28, v60\nN 124,252,164\n"
"buffer_load_dwordx4 v220, s[4:7], s63 offen lds\nv_mfma_f32_16x16x32_bf16 v[84:87], v[210:213], v[240:243], v[84:87]\nv_add_f32 v29, v29, v61\nv_add_f32 v30, v30, v62\nv_add_f32 v31, v31, v63\nN 120,232,160\n"
"R 178,189,28672\ns_lshl4_add_u32 s64, s41, s42\nv_lshrrev_b32 v227, 1, v252\nN 120,248,164\nbuffer_load_dwordx4 v221, s[4:7], s63 offen offset:1024 lds\nv_mfma_f32_16x16x32_bf16 v[92:95], v[210:213], v[242:245], v[92:95]\n"
"s_cmp_eq_u32 s40, 3\ns_cselect_b64 s[66:67], -1, 0\ns_lshl_b32 s64, s64, 12\nv_mul_f32 v16, v16, v217\nN 92,228,160\nR 180,189,32768\n"
"s_add_u32 m0, s50, 57344\ns_add_u32 s63, s52, 1769472\nv_mul_f32 v17, v17, v217\nN 92,244,164\nbuffer_load_dwordx4 v220, s[4:7], s63 offen lds\nv_mul_f32 v18, v18, v217\n"
"N 88,224,160\nR 182,189,36864\ns_add_u32 s64, s64, 78643200\nv_mul_f32 v19, v19, v217\nN 88,240,164\ns_waitcnt vmcnt(11)\n"
"s_barrier\nG 4\nv_mul_f32 v20, v20, v217\nv_mfma_f32_16x16x32_bf16 v[128:131], v[168:171], a[0:3], 0\nR 160,190,24576\nv_mul_f32 v21, v21, v217\n"
"v_mul_f32 v22, v22, v217\nM 128,172,16\ns_mov_b32 m0, s51\nv_mul_f32 v23, v23, v217\nv_mul_f32 v24, v24, v217\nv_mul_f32 v25, v25, v217\n"
"v_mfma_f32_16x16x32_bf16 v[144:147], v[168:171], a[4:7], 0\nR 162,190,28672\nv_mul_f32 v26, v26, v217\nv_mul_f32 v27, v27, v217\nM 144,172,20\nv_mul_f32 v28, v28, v217\n"
"v_mul_f32 v29, v29, v217\nv_mul_f32 v30, v30, v217\nv_mfma_f32_16x16x32_bf16 v[32:35], v[168:171], a[8:11], 0\nR 164,190,32768\nv_mul_f32 v31, v31, v217\nv_cvt_pk_bf16_f32 v16, v16, v17\n"
"M 32,172,24\nv_cvt_pk_bf16_f32 v17, v18, v19\nv_cvt_pk_bf16_f32 v18, v20, v21\nv_cvt_pk_bf16_f32 v19, v22, v23\nv_mfma_f32_16x16x32_bf16 v[48:51], v[168:171], a[12:15], 0\nR 166,190,36864\n"
"v_cvt_pk_bf16_f32 v20, v24, v25\nv_cvt_pk_bf16_f32 v21, v26, v27\nM 48,172,28\nds_write_addtid_b32 v96 offset:0\nG 5\nv_cvt_pk_bf16_f32 v22, v28, v29\n"
"v_cvt_pk_bf16_f32 v23, v30, v31\nv_mul_f32 v0, v0, v214\nv_mfma_f32_16x16x32_bf16 v[52:55], v[176:179], a[12:15], 0\nR 168,191,24576\nds_write_addtid_b32 v97 offset:256\nv_mul_f32 v1, v1, v214\n"
"v_mul_f32 v2, v2, v214\nM 52,180,28\nds_write_addtid_b32 v98 offset:512\nv_mul_f32 v3, v3, v214\nv_mul_f32 v4, v4, v214\nv_mul_f32 v5, v5, v214\n"
"v_mfma_f32_16x16x32_bf16 v[36:39], v[176:179], a[8:11], 0\nR 170,191,28672\nds_write_addtid_b32 v99 offset:768\nv_mul_f32 v6, v6, v214\nv_mul_f32 v7, v7, v214\nM 36,180,24\n"
"ds_write_addtid_b32 v100 offset:1024\nv_mul_f32 v8, v8, v214\nv_mul_f32 v9, v9, v214\nv_mul_f32 v10, v10, v214\nv_mfma_f32_16x16x32_bf16 v[148:151], v[176:179], a[4:7], 0\nR 172,191,32768\n"
"ds_write_addtid_b32 v101 offset:1280\nv_mul_f32 v11, v11, v214\nv_mul_f32 v12, v12, v214\nM 148,180,20\nds_write_addtid_b32 v102 offset:1536\nv_mul_f32 v13, v13, v214\n"
"v_mul_f32 v14, v14, v214\nv_mul_f32 v15, v15, v214\nv_mfma_f32_16x16x32_bf16 v[132:135], v[176:179], a[0:3], 0\nR 174,191,36864\nds_write_addtid_b32 v103 offset:1792\nv_cvt_pk_bf16_f32 v0, v0, v1\n"
"v_cvt_pk_bf16_f32 v1, v2, v3\nM 132,180,16\nds_write_addtid_b32 v104 offset:2048\nG 13\nv_cvt_pk_bf16_f32 v2, v4, v5\nv_cvt_pk_bf16_f32 v3, v6, v7\n"
"v_cvt_pk_bf16_f32 v4, v8, v9\nv_mfma_f32_16x16x32_bf16 v[136:139], v[160:163], a[0:3], 0\nR 176,188,40960\nds_write_addtid_b32 v105 offset:2304\nv_cvt_pk_bf16_f32 v5, v10, v11\nv_cvt_pk_bf16_f32 v6, v12, v13\n"
"M 136,164,16\nds_write_addtid_b32 v106 offset:2560\nds_write_addtid_b32 v107 offset:2816\ns_mov_b64 exec, s[66:67]\nbuffer_store_dwordx4 v[16:19], v252, s[20:23], s64 offen offset:0 sc1\ns_mov_b64 exec, -1\n"
"v_cvt_pk_bf16_f32 v7, v14, v15\nv_mfma_f32_16x16x32_bf16 v[152:155], v[160:163], a[4:7], 0\nR 178,188,45056\nds_write_addtid_b32 v108 offset:3072\nM 152,164,20\nds_write_addtid_b32 v109 offset:3328\n"
"ds_write_addtid_b32 v110 offset:3584\nv_mfma_f32_16x16x32_bf16 v[40:43], v[160:163], a[8:11], 0\nR 180,188,49152\nds_write_addtid_b32 v111 offset:3840\nM 40,164,24\nds_write_addtid_b32 v112 offset:4096\n"
"ds_write_addtid_b32 v113 offset:4352\ns_mov_b64 exec, s[66:67]\nbuffer_store_dwordx4 v[20:23], v252, s[20:23], s64 offen offset:1024 sc1\ns_mov_b64 exec, -1\nv_mfma_f32_16x16x32_bf16 v[56:59], v[160:163], a[12:15], 0\nR 182,188,53248\n"
"ds_write_addtid_b32 v114 offset:4608\nM 56,164,28\nds_write_addtid_b32 v115 offset:4864\nds_write_addtid_b32 v116 offset:5120\nG 15\nv_mfma_f32_16x16x32_bf16 v[60:63], v[168:171], a[12:15], 0\n"
"R 160,189,40960\nds_write_addtid_b32 v117 offset:5376\nM 60,172,28\nds_write_addtid_b32 v118 offset:5632\nds_write_addtid_b32 v119 offset:5888\nv_mfma_f32_16x16x32_bf16 v[44:47], v[168:171], a[8:11], 0\n"
"R 162,189,45056\nds_write_addtid_b32 v120 offset:6144\nM 44,172,24\nds_write_addtid_b32 v121 offset:6400\nds_write_addtid_b32 v122 offset:6656\nds_write_addtid_b32 v123 offset:6912\n"
"ds_write_addtid_b32 v124 offset:7168\nds_write_addtid_b32 v125 offset:7424\nds_write_addtid_b32 v126 offset:7680\nds_write_addtid_b32 v127 offset:7936\nv_mfma_f32_16x16x32_bf16 v[156:159], v[168:171], a[4:7], 0\nR 164,189,49152\n"
"s_add_u32 m0, s50, 57344\nM 156,172,20\nbuffer_load_dwordx4 v221, s[4:7], s63 offen offset:1024 lds\nv_mfma_f32_16x16x32_bf16 v[140:143], v[168:171], a[0:3], 0\nR 166,189,53248\ns_waitcnt vmcnt(10) lgkmcnt(2)\n"
"s_barrier\nM 140,172,16\nds_read2st64_b32 v[96:97], v225 offset0:0 offset1:1\nds_read2st64_b32 v[98:99], v225 offset0:2 offset1:3\nv_lshrrev_b32 v226, 8, v252\nM 128,176,32\n"
"R 168,190,40960\ns_add_u32 s63, s52, 1966080\nds_read2st64_b32 v[100:101], v225 offset0:4 offset1:5\ns_lshl_b32 s60, s40, 7\ns_add_u32 s60, s60, 161280\nv_lshl_add_u32 v226, v226, 4, s60\n"
"M 128,180,48\ns_add_u32 m0, s50, 65536\nds_read2st64_b32 v[102:103], v225 offset0:6 offset1:7\nds_read2st64_b32 v[104:105], v225 offset0:8 offset1:9\nds_read_b128 v[230:233], v226 offset:0\nM 144,176,36\n"
"R 170,190,45056\nds_read2st64_b32 v[106:107], v225 offset0:10 offset1:11\nds_read_b128 v[234:237], v226 offset:64\nM 144,180,52\nbuffer_load_dwordx4 v220, s[4:7], s63 offen lds\nds_read2st64_b32 v[108:109], v225 offset0:12 offset1:13\n"
"M 32,176,40\nR 172,190,49152\nds_read2st64_b32 v[110:111], v225 offset0:14 offset1:15\nM 32,180,56\nds_read2st64_b32 v[112:113], v225 offset0:16 offset1:17\nds_read2st64_b32 v[114:115], v225 offset0:18 offset1:19\n"
"G 14\nv_add_f32 v64, v64, v96\nM 48,176,44\nR 174,190,53248\nds_read2st64_b32 v[116:117], v225 offset0:20 offset1:21\nv_add_f32 v65, v65, v97\n"
"M 48,180,60\nds_read2st64_b32 v[118:119], v225 offset0:22 offset1:23\nds_read2st64_b32 v[120:121], v225 offset0:24 offset1:25\nM 52,160,44\nR 176,191,40960\nds_read2st64_b32 v[122:123], v225 offset0:26 offset1:27\n"
"G 15\nv_add_f32 v66, v66, v98\nM 52,164,60\nbuffer_load_dwordx4 v221, s[4:7], s63 offen offset:1024 lds\nds_read2st64_b32 v[124:125], v225 offset0:28 offset1:29\nM 36,160,40\n"
"R 178,191,45056\nds_read2st64_b32 v[126:127], v225 offset0:30 offset1:31\nv_add_f32 v67, v67, v99\nM 36,164,56\ns_add_u32 m0, s50, 73728\nv_add_f32 v68, v68, v100\n"
"v_add_f32 v69, v69, v101\nv_add_f32 v70, v70, v102\nM 148,160,36\nR 180,191,49152\nv_add_f32 v71, v71, v103\nv_add_f32 v72, v72, v104\n"
"M 148,164,52\ns_add_u32 s63, s52, 2162688\nv_add_f32 v73, v73, v105\nG 15\nv_add_f32 v74, v74, v106\nv_add_f32 v75, v75, v107\n"
"M 132,160,32\nR 182,191,53248\nG 15\nv_add_f32 v76, v76, v108\nv_add_f32 v77, v77, v109\nM 132,164,48\n"
"G 10\nv_add_f32 v78, v78, v110\nv_add_f32 v79, v79, v111\nv_add_f32 v80, v80, v112\nM 136,168,32\nR 160,184,0\n"
"v_add_f32 v81, v81, v113\nv_add_f32 v82, v82, v114\nM 136,172,48\nbuffer_load_dwordx4 v220, s[4:7], s63 offen lds\nv_add_f32 v83, v83, v115\nM 152,168,36\n"
"R 162,184,4096\nG 11\nv_add_f32 v84, v84, v116\nM 152,172,52\nv_add_f32 v85, v85, v117\nG 10\n"
"v_add_f32 v86, v86, v118\nM 40,168,40\nR 164,184,8192\nv_add_f32 v87, v87, v119\nM 40,172,56\nbuffer_load_dwordx4 v221, s[4:7], s63 offen offset:1024 lds\n"
"M 56,168,44\nR 166,184,12288\ns_add_u32 m0, s50, 81920\nM 56,172,60\nG 4\nv_add_f32 v88, v88, v120\n"
"v_add_f32 v89, v89, v121\nM 60,176,44\nR 168,185,0\ns_add_u32 s63, s52, 2359296\nM 60,180,60\nbuffer_load_dwordx4 v220, s[4:7], s63 offen lds\n"
"M 44,176,40\nR 170,185,4096\nv_add_f32 v90, v90, v122\nM 44,180,56\nv_add_f32 v91, v91, v123\nv_add_f32 v92, v92, v124\n"
"M 156,176,36\nR 172,185,8192\nv_add_f32 v93, v93, v125\nM 156,180,52\nbuffer_load_dwordx4 v221, s[4:7], s63 offen offset:1024 lds\nM 140,176,32\n"
"R 174,185,12288\ns_add_u32 m0, s50, 90112\nM 140,180,48\nG 4\nv_add_f32 v94, v94, v126\nv_add_f32 v95, v95, v127\n"
"M 128,160,64\nR 176,186,0\ns_add_u32 s63, s52, 2555904\nv_mul_f32 v64, v64, v230\nM 128,164,80\nbuffer_load_dwordx4 v220, s[4:7], s63 offen lds\n"
"v_mul_f32 v65, v65, v231\nM 144,160,68\nR 178,186,4096\nv_mul_f32 v66, v66, v232\nv_mul_f32 v67, v67, v233\nM 144,164,84\n"
"v_mul_f32 v68, v68, v234\nv_mul_f32 v69, v69, v235\nv_mul_f32 v70, v70, v236\nM 32,160,72\nR 180,186,8192\nv_mul_f32 v71, v71, v237\n"
"v_cvt_pk_bf16_f32 v67, v66, v67\nM 32,164,88\nv_cvt_pk_bf16_f32 v66, v64, v65\nv_cvt_pk_bf16_f32 v68, v68, v69\nv_cvt_pk_bf16_f32 v69, v70, v71\nM 48,160,76\n"
"R 182,186,12288\nv_mul_f32 v72, v72, v230\nv_mul_f32 v73, v73, v231\nM 48,164,92\nG 4\nv_mul_f32 v74, v74, v232\n"
"v_mul_f32 v75, v75, v233\nv_mul_f32 v76, v76, v234\nM 52,168,76\nR 160,187,0\nv_mul_f32 v77, v77, v235\nv_mul_f32 v78, v78, v236\n"
"M 52,172,92\nbuffer_load_dwordx4 v221, s[4:7], s63 offen offset:1024 lds\nv_mul_f32 v79, v79, v237\nM 36,168,72\nR 162,187,4096\ns_add_u32 s63, s52, 2752512\n"
"v_cvt_pk_bf16_f32 v72, v72, v73\nM 36,172,88\ns_add_u32 m0, s50, 98304\nv_cvt_pk_bf16_f32 v73, v74, v75\nv_cvt_pk_bf16_f32 v74, v76, v77\nv_cvt_pk_bf16_f32 v75, v78, v79\n"
"M 148,168,68\nR 164,187,8192\nv_mul_f32 v80, v80, v230\nv_mul_f32 v81, v81, v231\nM 148,172,84\nbuffer_load_dwordx4 v220, s[4:7], s63 offen lds\n"
"v_mul_f32 v82, v82, v232\nM 132,168,64\nR 166,187,12288\ns_waitcnt vmcnt(9)\ns_barrier\nv_mul_f32 v83, v83, v233\n"
"M 132,172,80\nG 4\nv_mul_f32 v84, v84, v234\nv_mul_f32 v85, v85, v235\nM 136,176,64\nR 168,184,16384\n"
"v_mul_f32 v86, v86, v236\nM 136,180,80\nv_mul_f32 v87, v87, v237\nv_cvt_pk_bf16_f32 v78, v80, v81\nM 152,176,68\nR 170,184,20480\n"
"v_cvt_pk_bf16_f32 v79, v82, v83\nM 152,180,84\nv_cvt_pk_bf16_f32 v80, v84, v85\nv_cvt_pk_bf16_f32 v81, v86, v87\nM 40,176,72\nR 172,184,24576\n"
"s_mov_b32 s60, 155648\nv_bfe_u32 v246, v252, 4, 4\nv_lshl_add_u32 v246, v246, 3, s60\nM 40,180,88\nbuffer_load_dwordx4 v221, s[4:7], s63 offen offset:1024 lds\nM 56,176,76\n"
"R 174,184,28672\nds_read_b64 v[238:239], v246 offset:0\nM 56,180,92\nG 5\nv_mul_f32 v88, v88, v230\nv_mul_f32 v89, v89, v231\n"
"M 60,160,76\nR 176,185,16384\nds_read_b64 v[240:241], v246 offset:128\nM 60,164,92\ns_add_u32 m0, s50, 106496\nv_mul_f32 v90, v90, v232\n"
"v_mul_f32 v91, v91, v233\nM 44,160,72\nR 178,185,20480\nds_read_b64 v[242:243], v246 offset:256\nM 44,164,88\ns_add_u32 s63, s52, 2949120\n"
"v_mul_f32 v92, v92, v234\nv_mul_f32 v93, v93, v235\nLpg21:\ns_cbranch_execz LretW0n\nM 156,160,68\nR 180,185,24576\n"
"ds_read_b64 v[244:245], v246 offset:384\nM 156,164,84\nbuffer_load_dwordx4 v220, s[4:7], s63 offen lds\nM 140,160,64\nR 182,185,28672\nds_read_b64 v[246:247], v246 offset:0\n"
"M 140,164,80\nG 9\nv_mul_f32 v94, v94, v236\nv_mul_f32 v95, v95, v237\nM 128,168,96\nR 160,186,16384\n"
"v_cvt_pk_bf16_f32 v84, v88, v89\nM 128,172,112\nv_cvt_pk_bf16_f32 v85, v90, v91\nv_cvt_pk_bf16_f32 v86, v92, v93\nM 144,168,100\nR 162,186,20480\n"
"v_cvt_pk_bf16_f32 v87, v94, v95\nM 144,172,116\nv_add_u32 v108, 161792, v252\nds_read_b128 v[112:115], v108\ns_mov_b64 exec, s[66:67]\nbuffer_store_dwordx2 v[68:69], v227, s[20:23], s64 offen offset:2048 sc1\n"
"s_mov_b64 exec, -1\nM 32,168,104\nR 164,186,24576\nG 10\nv_mfma_f32_16x16x32_bf16 v[128:131], v[238:241], v[208:211], v[128:131]\ns_lshl_b32 s60, s40, 12\n"
"s_add_u32 s60, s60, 69632\nv_add_u32 v230, s60, v252\nv_add_u32 v231, s60, v227\nM 32,172,120\nbuffer_load_dwordx4 v221, s[4:7], s63 offen offset:1024 lds\nM 48,168,108\n"
"R 166,186,28672\nG 9\nv_mfma_f32_16x16x32_bf16 v[132:135], v[240:243], v[208:211], v[132:135]\nds_read_b128 v[104:107], v108 offset:1024\nM 48,172,124\nG 7\n"
"M 52,176,108\nR 168,187,16384\nG 7\nv_mfma_f32_16x16x32_bf16 v[144:147], v[244:247], v[210:213], v[144:147]\nM 52,180,124\ns_mov_b64 exec, s[66:67]\n"
"buffer_store_dwordx2 v[74:75], v227, s[20:23], s64 offen offset:2560 sc1\ns_mov_b64 exec, -1\nM 36,176,104\nR 170,187,20480\nv_mfma_f32_16x16x32_bf16 v[148:151], v[238:241], v[210:213], v[148:151]\nM 36,180,120\n"
"s_movk_i32 s63, 2048\nv_mad_u32_u24 v220, v228, s63, v229\nv_add_u32 v221, 1047552, v220\ns_add_u32 m0, s50, 114688\ns_add_u32 s63, s53, 0\nbuffer_load_dwordx4 v220, s[8:11], s63 offen lds\n"
"M 148,176,100\nR 172,187,24576\nG 3\nv_lshlrev_b32 v100, 16, v104\nv_mfma_f32_16x16x32_bf16 v[136:139], v[242:245], v[208:211], v[136:139]\nv_and_b32 v101, s77, v104\n"
"v_lshlrev_b32 v102, 16, v105\nv_and_b32 v103, s77, v105\nM 148,180,116\nv_lshlrev_b32 v96, 16, v106\nv_and_b32 v97, s77, v106\nM 132,176,96\n"
"R 174,187,28672\nv_lshlrev_b32 v98, 16, v107\nv_mfma_f32_16x16x32_bf16 v[140:143], v[244:247], v[208:211], v[140:143]\nv_and_b32 v99, s77, v107\nv_add_f32 v104, s78, v100\nv_add_f32 v105, s78, v101\n"
"M 132,180,112\ns_mov_b64 exec, s[66:67]\nbuffer_store_dwordx2 v[80:81], v227, s[20:23], s64 offen offset:3072 sc1\ns_mov_b64 exec, -1\nv_add_f32 v106, s78, v102\nv_add_f32 v107, s78, v103\n"
"M 136,160,96\nR 176,184,49152\nv_mfma_f32_16x16x32_bf16 v[152:155], v[240:243], v[210:213], v[152:155]\nM 136,164,112\nbuffer_load_dwordx4 v221, s[8:11], s63 offen offset:1024 lds\nM 152,160,100\n"
"R 178,184,53248\nv_mfma_f32_16x16x32_bf16 v[156:159], v[242:245], v[210:213], v[156:159]\nv_lshrrev_b32 v248, 5, v252\nv_lshlrev_b32 v248, 5, v248\nv_bfe_u32 v249, v252, 4, 1\ns_and_b32 s60, s41, 7\n"
"s_lshl2_add_u32 s60, s60, s40\nv_add_u32 v248, s60, v248\nM 152,164,116\ns_sub_u32 s60, s54, s54\ns_add_u32 s60, s60, 128\nv_mul_lo_u32 v249, v249, s60\n"
"v_mul_u32_u24 v248, 7168, v248\nM 40,160,104\nR 180,184,57344\nv_add_u32 v248, v248, v249\nM 40,164,120\ns_mov_b64 exec, s[66:67]\n"
"buffer_store_dwordx2 v[86:87], v227, s[20:23], s64 offen offset:3584 sc1\ns_mov_b64 exec, -1\nM 56,160,108\nR 182,184,61440\ns_not_b64 exec, s[66:67]\nds_write_b128 v230, v[16:19] offset:0\n"
"ds_write_b128 v230, v[20:23] offset:1024\ns_mov_b64 exec, -1\nM 56,164,124\ns_not_b64 exec, s[66:67]\nds_write_b64 v231, v[68:69] offset:2048\nds_write_b64 v231, v[74:75] offset:2560\n"
"s_mov_b64 exec, -1\nG 8\nM 60,168,108\nR 160,185,49152\ns_not_b64 exec, s[66:67]\nds_write_b64 v231, v[80:81] offset:3072\n"
"ds_write_b64 v231, v[86:87] offset:3584\ns_mov_b64 exec, -1\nM 60,172,124\nM 44,168,104\nR 162,185,53248\nM 44,172,120\n"
"s_add_u32 m0, s50, 0\nM 156,168,100\nR 164,185,57344\nM 156,172,116\ns_add_u32 s63, s53, 65536\nM 140,168,96\n"
"R 166,185,61440\nM 140,172,112\nG 10\nM 128,176,128\nR 168,186,49152\ns_waitcnt vmcnt(12) lgkmcnt(4)\n"
"s_barrier\nM 128,180,144\nbuffer_load_dwordx4 v220, s[8:11], s63 offen lds\nM 144,176,132\nR 170,186,53248\nM 144,180,148\n"
"s_cmp_eq_u32 s78, 0\ns_cbranch_scc1 Lq656\nv_mov_b32 v230, 0\nv_mov_b32 v231, 0\nv_mov_b32 v232, 0\nv_mov_b32 v233, 0\n"
"v_mov_b32 v64, 0\nv_mov_b32 v65, 0\nv_mov_b32 v70, 0\nv_mov_b32 v71, 0\nv_mov_b32 v234, 0\nv_mov_b32 v235, 0\n"
"v_mov_b32 v236, 0\nv_mov_b32 v237, 0\nv_mov_b32 v76, 0\nv_mov_b32 v77, 0\nv_mov_b32 v82, 0\nv_mov_b32 v83, 0\n"
"Lq656:\ns_lshl4_add_u32 s65, s41, s42\nM 32,176,136\nR 172,186,57344\ns_lshl_b32 s65, s65, 12\nM 32,180,152\n"
"s_add_u32 s65, s65, 78577664\nM 48,176,140\nR 174,186,61440\ns_bfm_b64 exec, 16, 0\nbuffer_load_dword v249, v248, s[12:15], s54 offen\ns_mov_b64 exec, -1\n"
"M 48,180,156\nG 4\nM 52,160,140\nR 176,187,49152\nM 52,164,156\nbuffer_load_dwordx4 v221, s[8:11], s63 offen offset:1024 lds\n"
"M 36,160,136\nR 178,187,53248\nM 36,164,152\ns_add_u32 m0, s50, 8192\nM 148,160,132\nR 180,187,57344\n"
"M 148,164,148\ns_add_u32 s63, s53, 131072\nM 132,160,128\nR 182,187,61440\nM 132,164,144\nG 4\n"
"M 136,168,128\nR 160,188,0\nM 136,172,144\nbuffer_load_dwordx4 v220, s[8:11], s63 offen lds\nM 152,168,132\nR 162,188,4096\n"
"M 152,172,148\nM 40,168,136\nR 164,188,8192\nM 40,172,152\nM 56,168,140\nR 166,188,12288\n"
"s_bfm_b64 exec, 16, 16\nbuffer_load_dword v249, v248, s[12:15], s54 offen\ns_mov_b64 exec, -1\nM 56,172,156\nG 4\nM 60,176,140\n"
"R 168,189,0\nM 60,180,156\nbuffer_load_dwordx4 v221, s[8:11], s63 offen offset:1024 lds\nM 44,176,136\nR 170,189,4096\nM 44,180,152\n"
"s_add_u32 m0, s50, 16384\nM 156,176,132\nR 172,189,8192\nM 156,180,148\ns_add_u32 s63, s53, 196608\nM 140,176,128\n"
"R 174,189,12288\nM 140,180,144\nG 4\nM 128,160,160\nR 176,190,0\nM 128,164,176\n"
"buffer_load_dwordx4 v220, s[8:11], s63 offen lds\nM 144,160,164\nR 178,190,4096\nM 144,164,180\nM 32,160,168\nR 180,190,8192\n"
"M 32,164,184\nM 48,160,172\nR 182,190,12288\ns_bfm_b64 exec, 16, 32\nbuffer_load_dword v249, v248, s[12:15], s54 offen\ns_mov_b64 exec, -1\n"
"M 48,164,188\nG 4\nM 52,168,172\nR 160,191,0\nM 52,172,188\nbuffer_load_dwordx4 v221, s[8:11], s63 offen offset:1024 lds\n"
"M 36,168,168\nR 162,191,4096\ns_add_u32 s63, s53, 262144\nM 36,172,184\ns_add_u32 m0, s50, 24576\ns_lshl_b32 s60, s40, 12\n"
"s_add_u32 s60, s60, 65536\nv_add_u32 v226, s60, v252\nv_add_u32 v111, s60, v227\nM 148,168,164\nR 164,191,8192\nM 148,172,180\n"
"buffer_load_dwordx4 v220, s[8:11], s63 offen lds\nM 132,168,160\nR 166,191,12288\ns_cmp_eq_u32 s40, 0\ns_cselect_b64 s[86:87], 0, -1\ns_cselect_b32 s60, s41, 0\n"
"s_cmp_lg_u32 s60, 0\ns_cselect_b64 s[66:67], -1, 0\nM 132,172,176\nG 4\nM 136,176,160\nR 168,188,16384\n"
"M 136,180,176\ns_mov_b64 exec, s[86:87]\nds_read_b128 v[230:233], v226 offset:0\nds_read_b64 v[64:65], v111 offset:2048\ns_mov_b64 exec, -1\nM 152,176,164\n"
"R 170,188,20480\ns_waitcnt vmcnt(15) lgkmcnt(1)\ns_barrier\nM 152,180,180\nM 40,176,168\nR 172,188,24576\n"
"M 40,180,184\ns_mov_b64 exec, s[86:87]\nds_read_b64 v[70:71], v111 offset:2560\nds_read_b128 v[234:237], v226 offset:1024\ns_mov_b64 exec, -1\nM 56,176,172\n"
"R 174,188,28672\ns_bfm_b64 exec, 16, 48\nbuffer_load_dword v249, v248, s[12:15], s54 offen\ns_mov_b64 exec, -1\nM 56,180,188\nM 60,160,172\n"
"R 176,189,16384\nM 60,164,188\ns_mov_b64 exec, s[86:87]\nds_read_b64 v[76:77], v111 offset:3072\nds_read_b64 v[82:83], v111 offset:3584\ns_mov_b64 exec, -1\n"
"M 44,160,168\nR 178,189,20480\nM 44,164,184\nbuffer_load_dwordx4 v221, s[8:11], s63 offen offset:1024 lds\nM 156,160,164\nR 180,189,24576\n"
"M 156,164,180\ns_mov_b64 exec, s[66:67]\nbuffer_load_dwordx4 v[230:233], v252, s[20:23], s65 offen offset:0 sc1\ns_mov_b64 exec, -1\nM 140,160,160\nR 182,189,28672\n"
"M 140,164,176\nG 6\nM 32,168,200\nR 160,190,16384\ns_mov_b64 exec, s[66:67]\nbuffer_load_dwordx2 v[64:65], v227, s[20:23], s65 offen offset:2048 sc1\n"
"s_mov_b64 exec, -1\nM 32,172,216\nR 162,190,20480\nM 48,168,204\nR 164,190,24576\nM 48,172,220\n"
"R 166,190,28672\nG 4\nM 52,176,204\nR 168,191,16384\ns_mov_b64 exec, s[66:67]\nbuffer_load_dwordx2 v[70:71], v227, s[20:23], s65 offen offset:2560 sc1\n"
"s_mov_b64 exec, -1\nM 52,180,220\nR 170,191,20480\nM 36,176,200\nR 172,191,24576\nM 36,180,216\n"
"R 174,191,28672\nG 4\nM 40,160,200\nR 176,188,32768\ns_mov_b64 exec, s[66:67]\nbuffer_load_dwordx4 v[234:237], v252, s[20:23], s65 offen offset:1024 sc1\n"
"s_mov_b64 exec, -1\nM 40,164,216\nR 178,188,36864\nM 56,160,204\nR 180,188,40960\nM 56,164,220\n"
"R 182,188,45056\nG 4\nM 60,168,204\nR 160,189,32768\ns_mov_b64 exec, s[66:67]\nbuffer_load_dwordx2 v[76:77], v227, s[20:23], s65 offen offset:3072 sc1\n"
"s_mov_b64 exec, -1\nM 60,172,220\nR 162,189,36864\nM 44,168,200\nR 164,189,40960\nM 44,172,216\n"
"R 166,189,45056\nG 4\nM 32,176,232\nR 168,190,32768\ns_mov_b64 exec, s[66:67]\nbuffer_load_dwordx2 v[82:83], v227, s[20:23], s65 offen offset:3584 sc1\n"
"s_mov_b64 exec, -1\nLq690:\ns_lshr_b32 s60, s45, 3\ns_lshl2_add_u32 s60, s60, s40\ns_lshl_b32 s60, s60, 8\nv_lshl_add_u32 v224, v252, 3, s60\n"
"v_add_u32 v224, Lq690+4096-LqPC, v224\nv_min_u32 v224, LqE-LqPC-4, v224\ns_mov_b64 exec, 3\nglobal_load_dword v224, v224, s[98:99]\ns_mov_b64 exec, -1\nM 32,180,248\n"
"R 170,190,36864\nM 48,176,236\nR 172,190,40960\nM 48,180,252\nR 174,190,45056\nG 4\n"
"M 52,160,236\nR 176,191,32768\nM 52,164,252\nR 178,191,36864\nM 36,160,232\nR 180,191,40960\n"
"M 36,164,248\nR 182,191,45056\nG 4\nM 40,168,232\nR 160,188,16384\nM 40,172,248\n"
"R 162,188,20480\nM 56,168,236\nR 164,188,24576\nM 56,172,252\nR 166,188,28672\nG 4\n"
"M 60,176,236\nR 168,189,16384\nM 60,180,252\nR 170,189,20480\nM 44,176,232\nR 172,189,24576\n"
"M 44,180,248\nR 174,189,28672\nG 4\nM 128,160,192\nR 176,190,16384\nM 128,164,208\n"
"R 178,190,20480\ns_mov_b32 m0, s51\nM 144,160,196\nR 180,190,24576\nds_write_addtid_b32 v32 offset:0\nM 144,164,212\n"
"R 182,190,28672\nds_write_addtid_b32 v33 offset:256\nG 6\nM 148,168,196\nR 160,191,16384\nds_write_addtid_b32 v34 offset:512\n"
"M 148,172,212\nR 162,191,20480\nds_write_addtid_b32 v35 offset:768\nM 132,168,192\nR 164,191,24576\nds_write_addtid_b32 v36 offset:1024\n"
"M 132,172,208\nR 166,191,28672\nds_write_addtid_b32 v37 offset:1280\nG 9\nM 136,176,192\nR 168,188,32768\n"
"ds_write_addtid_b32 v38 offset:1536\nM 136,180,208\nR 170,188,36864\nds_write_addtid_b32 v39 offset:1792\nM 152,176,196\nR 172,188,40960\n"
"ds_write_addtid_b32 v40 offset:2048\nM 152,180,212\nR 174,188,45056\nds_write_addtid_b32 v41 offset:2304\nG 9\nM 156,160,196\n"
"R 176,189,32768\nds_write_addtid_b32 v42 offset:2560\nM 156,164,212\nR 178,189,36864\nds_write_addtid_b32 v43 offset:2816\nM 140,160,192\n"
"R 180,189,40960\nds_write_addtid_b32 v44 offset:3072\nds_write_addtid_b32 v45 offset:3328\nds_write_addtid_b32 v46 offset:3584\nds_write_addtid_b32 v47 offset:3840\nds_write_addtid_b32 v48 offset:4096\n"
"ds_write_addtid_b32 v49 offset:4352\nds_write_addtid_b32 v50 offset:4608\nds_write_addtid_b32 v51 offset:4864\nds_write_addtid_b32 v52 offset:5120\nds_write_addtid_b32 v53 offset:5376\nds_write_addtid_b32 v54 offset:5632\n"
"ds_write_addtid_b32 v55 offset:5888\nds_write_addtid_b32 v56 offset:6144\nds_write_addtid_b32 v57 offset:6400\nds_write_addtid_b32 v58 offset:6656\nds_write_addtid_b32 v59 offset:6912\nds_write_addtid_b32 v60 offset:7168\n"
"ds_write_addtid_b32 v61 offset:7424\nds_write_addtid_b32 v62 offset:7680\nds_write_addtid_b32 v63 offset:7936\nM 140,164,208\nR 182,189,45056\nG 15\n"
"M 128,168,224\nR 160,190,32768\ns_add_u32 m0, s50, 32768\nM 128,172,240\nR 162,190,36864\nG 3\n"
"s_barrier\ns_add_u32 s63, s53, 327680\nM 144,168,228\nR 164,190,40960\nbuffer_load_dwordx4 v220, s[8:11], s63 offen lds\nds_read2st64_b32 v[32:33], v225 offset0:0 offset1:1\n"
"M 144,172,244\nR 166,190,45056\nbuffer_load_dwordx4 v221, s[8:11], s63 offen offset:1024 lds\nds_read2st64_b32 v[34:35], v225 offset0:2 offset1:3\nG 6\nM 148,176,228\n"
"R 168,191,32768\ns_add_u32 m0, s50, 40960\nds_read2st64_b32 v[36:37], v225 offset0:4 offset1:5\nM 148,180,244\nR 170,191,36864\ns_add_u32 s63, s53, 393216\n"
"ds_read2st64_b32 v[38:39], v225 offset0:6 offset1:7\nM 132,176,224\nR 172,191,40960\nds_read2st64_b32 v[40:41], v225 offset0:8 offset1:9\nM 132,180,240\nR 174,191,45056\n"
"ds_read2st64_b32 v[42:43], v225 offset0:10 offset1:11\nG 9\nM 136,160,224\nbuffer_load_dwordx4 v220, s[8:11], s63 offen lds\nds_read2st64_b32 v[44:45], v225 offset0:12 offset1:13\nM 136,164,240\n"
"ds_read2st64_b32 v[46:47], v225 offset0:14 offset1:15\nds_read2st64_b32 v[48:49], v225 offset0:16 offset1:17\nM 152,160,228\nbuffer_load_dwordx4 v221, s[8:11], s63 offen offset:1024 lds\nds_read2st64_b32 v[50:51], v225 offset0:18 offset1:19\nM 152,164,244\n"
"ds_read2st64_b32 v[52:53], v225 offset0:20 offset1:21\nds_read2st64_b32 v[54:55], v225 offset0:22 offset1:23\nG 7\nM 156,168,228\nds_read2st64_b32 v[56:57], v225 offset0:24 offset1:25\nds_read2st64_b32 v[58:59], v225 offset0:26 offset1:27\n"
"M 156,172,244\nds_read2st64_b32 v[60:61], v225 offset0:28 offset1:29\nds_read2st64_b32 v[62:63], v225 offset0:30 offset1:31\nM 140,168,224\nM 140,172,240\nG 7\n"
"v_pk_add_f32 v[144:145], v[144:145], v[48:49]\nG 6\nv_pk_add_f32 v[146:147], v[146:147], v[50:51]\nG 5\nv_pk_add_f32 v[148:149], v[148:149], v[52:53]\nG 4\n"
"v_pk_add_f32 v[150:151], v[150:151], v[54:55]\nG 3\nv_pk_add_f32 v[152:153], v[152:153], v[56:57]\nG 2\nv_pk_add_f32 v[154:155], v[154:155], v[58:59]\nG 1\n"
"v_pk_add_f32 v[156:157], v[156:157], v[60:61]\nG 0\nv_pk_add_f32 v[158:159], v[158:159], v[62:63]\nv_pk_add_f32 v[128:129], v[128:129], v[32:33]\nv_pk_add_f32 v[130:131], v[130:131], v[34:35]\nv_pk_add_f32 v[132:133], v[132:133], v[36:37]\n"
"v_pk_add_f32 v[134:135], v[134:135], v[38:39]\nv_pk_add_f32 v[136:137], v[136:137], v[40:41]\nv_pk_add_f32 v[138:139], v[138:139], v[42:43]\nv_pk_add_f32 v[140:141], v[140:141], v[44:45]\nv_pk_add_f32 v[142:143], v[142:143], v[46:47]\nv_cvt_pk_bf16_f32 v144, v144, v145\n"
"v_cvt_pk_bf16_f32 v145, v146, v147\nv_cvt_pk_bf16_f32 v146, v148, v149\nv_cvt_pk_bf16_f32 v147, v150, v151\nv_cvt_pk_bf16_f32 v152, v152, v153\nv_cvt_pk_bf16_f32 v153, v154, v155\nv_cvt_pk_bf16_f32 v154, v156, v157\n"
"v_cvt_pk_bf16_f32 v155, v158, v159\nv_cvt_pk_bf16_f32 v128, v128, v129\nv_cvt_pk_bf16_f32 v129, v130, v131\nv_cvt_pk_bf16_f32 v130, v132, v133\nv_cvt_pk_bf16_f32 v131, v134, v135\nv_cvt_pk_bf16_f32 v136, v136, v137\n"
"v_cvt_pk_bf16_f32 v137, v138, v139\nv_cvt_pk_bf16_f32 v138, v140, v141\nv_cvt_pk_bf16_f32 v139, v142, v143\nv_mfma_f32_16x16x32_bf16 v[52:55], v[16:19], v[144:147], v[96:99]\nv_mfma_f32_16x16x32_bf16 v[48:51], v[0:3], v[144:147], v[100:103]\nv_mfma_f32_16x16x32_bf16 v[60:63], v[20:23], v[152:155], v[96:99]\n"
"v_mfma_f32_16x16x32_bf16 v[56:59], v[4:7], v[152:155], v[100:103]\ns_waitcnt vmcnt(8)\nv_max3_u32 v248, v233, v65, v71\nLpg22:\ns_cbranch_execz LretW0n\nv_cmp_eq_u32 vcc, -1, v248\n"
"s_nop 0\ns_cbranch_vccnz Lq692\nLq693:\ns_waitcnt vmcnt(5)\nv_max3_u32 v248, v237, v77, v83\nv_cmp_eq_u32 vcc, -1, v248\n"
"s_nop 0\ns_cbranch_vccnz Lq694\nLq695:\nv_mfma_f32_16x16x32_bf16 v[36:39], v[0:3], v[128:131], v[96:99]\nv_mfma_f32_16x16x32_bf16 v[32:35], v[230:233], v[128:131], v[104:107]\nv_mfma_f32_16x16x32_bf16 v[44:47], v[4:7], v[136:139], v[96:99]\n"
"v_mfma_f32_16x16x32_bf16 v[40:43], v[234:237], v[136:139], v[104:107]\ns_lshl_b32 s84, s42, 11\ns_add_u32 s84, s84, s49\ns_add_u32 s84, s84, 6291456\ns_add_u32 s85, s84, 32768\nv_mov_b32 v238, s46\n"
"v_mov_b32 v239, s47\nv_max_f32 v52, v52, v53\nv_max_f32 v54, v54, v55\nv_max_f32 v60, v60, v61\nv_max_f32 v62, v62, v63\nv_max3_f32 v52, v52, v48, v49\n"
"v_max3_f32 v54, v54, v50, v51\nv_max3_f32 v60, v60, v56, v57\nv_max3_f32 v62, v62, v58, v59\nv_max_f32 v36, v36, v37\nv_max_f32 v38, v38, v39\nv_max_f32 v44, v44, v45\n"
"v_max_f32 v46, v46, v47\nv_max3_f32 v36, v36, v32, v33\nv_max3_f32 v38, v38, v34, v35\nv_max3_f32 v44, v44, v40, v41\nv_max3_f32 v46, v46, v42, v43\nv_max_f32 v118, v52, v54\n"
"v_max_f32 v119, v60, v62\nv_max_f32 v116, v36, v38\nv_max_f32 v117, v44, v46\ns_nop 0\nv_permlane32_swap_b32 v116, v118\nv_permlane32_swap_b32 v117, v119\n"
"v_max_f32 v116, v116, v118\nv_max_f32 v117, v117, v119\ns_nop 1\nv_permlane16_swap_b32 v116, v117\nv_max_f32 v116, v116, v117\nv_mov_b32 v117, v116\n"
"s_nop 1\nv_permlane16_swap_b32 v116, v117\nv_mov_b32 v118, v116\nv_mov_b32 v119, v117\ns_nop 0\nv_permlane32_swap_b32 v116, v118\n"
"v_permlane32_swap_b32 v117, v119\nv_mul_f32 v116, v116, v215\nv_mul_f32 v118, v118, v218\nv_mul_f32 v117, v117, v215\nv_mul_f32 v119, v119, v218\nv_fma_f32 v52, v52, v218, -v118\n"
"v_fma_f32 v54, v54, v218, -v118\nv_fma_f32 v60, v60, v218, -v119\nv_fma_f32 v62, v62, v218, -v119\nv_fma_f32 v36, v36, v215, -v116\nv_fma_f32 v38, v38, v215, -v116\nv_fma_f32 v44, v44, v215, -v117\n"
"v_fma_f32 v46, v46, v215, -v117\nv_exp_f32 v52, v52\nv_exp_f32 v54, v54\nv_exp_f32 v60, v60\nv_exp_f32 v62, v62\nv_exp_f32 v36, v36\n"
"v_exp_f32 v38, v38\nv_exp_f32 v44, v44\nv_exp_f32 v46, v46\nv_add_f32 v118, v52, v54\nv_add_f32 v119, v60, v62\nv_add_f32 v116, v36, v38\n"
"v_add_f32 v117, v44, v46\ns_nop 0\nv_permlane32_swap_b32 v116, v118\nv_permlane32_swap_b32 v117, v119\nv_add_f32 v116, v116, v118\nv_add_f32 v117, v117, v119\n"
"s_nop 1\nv_permlane16_swap_b32 v116, v117\nv_add_f32 v116, v116, v117\ns_barrier\nv_rcp_f32 v116, v116\ns_nop 0\n"
"v_mov_b32 v117, v116\ns_nop 1\nv_permlane16_swap_b32 v116, v117\nv_mov_b32 v118, v116\nv_mov_b32 v119, v117\ns_nop 0\n"
"v_permlane32_swap_b32 v116, v118\nv_permlane32_swap_b32 v117, v119\nv_mul_f32 v52, v52, v118\nv_mul_f32 v54, v54, v118\nv_mul_f32 v60, v60, v119\nv_mul_f32 v62, v62, v119\n"
"v_mul_f32 v36, v36, v116\nv_mul_f32 v38, v38, v116\nv_mul_f32 v44, v44, v117\nv_mul_f32 v46, v46, v117\nv_cvt_pk_bf16_f32 v52, v52, v52\nv_cvt_pk_bf16_f32 v54, v54, v54\n"
"v_cvt_pk_bf16_f32 v60, v60, v60\nv_cvt_pk_bf16_f32 v62, v62, v62\nv_cvt_pk_bf16_f32 v36, v36, v36\nv_cvt_pk_bf16_f32 v38, v38, v38\nv_cvt_pk_bf16_f32 v44, v44, v44\nv_cvt_pk_bf16_f32 v46, v46, v46\n"
"v_and_b32 v48, v52, v112\nv_and_b32 v49, v54, v113\nv_and_b32 v50, v52, v114\nv_and_b32 v51, v54, v115\nv_and_b32 v56, v60, v112\nv_and_b32 v57, v62, v113\n"
"v_and_b32 v58, v60, v114\nv_and_b32 v59, v62, v115\nv_mfma_f32_16x16x32_bf16 v[144:147], v[66:69], v[48:51], 0\nv_mfma_f32_16x16x32_bf16 v[148:151], v[72:75], v[48:51], 0\nv_and_b32 v32, v36, v112\nv_and_b32 v33, v38, v113\n"
"v_and_b32 v34, v36, v114\nv_and_b32 v35, v38, v115\nv_mfma_f32_16x16x32_bf16 v[152:155], v[78:81], v[56:59], 0\nv_mfma_f32_16x16x32_bf16 v[156:159], v[84:87], v[56:59], 0\nv_and_b32 v40, v44, v112\nv_and_b32 v41, v46, v113\n"
"v_and_b32 v42, v44, v114\nv_and_b32 v43, v46, v115\nv_mfma_f32_16x16x32_bf16 v[128:131], v[64:67], v[32:35], 0\nv_mfma_f32_16x16x32_bf16 v[132:135], v[70:73], v[32:35], 0\nv_mfma_f32_16x16x32_bf16 v[136:139], v[76:79], v[40:43], 0\nv_mfma_f32_16x16x32_bf16 v[140:143], v[82:85], v[40:43], 0\n"
"v_cvt_pk_bf16_f32 v144, v144, v145\nv_cvt_pk_bf16_f32 v145, v146, v147\nv_cvt_pk_bf16_f32 v146, v148, v149\nv_cvt_pk_bf16_f32 v147, v150, v151\nbuffer_store_dwordx4 v[144:147], v252, s[20:23], s85 offen offset:0 sc1\nv_cvt_pk_bf16_f32 v152, v152, v153\n"
"v_cvt_pk_bf16_f32 v153, v154, v155\nv_cvt_pk_bf16_f32 v154, v156, v157\nv_cvt_pk_bf16_f32 v155, v158, v159\nbuffer_store_dwordx4 v[152:155], v252, s[20:23], s85 offen offset:1024 sc1\nv_cvt_pk_bf16_f32 v128, v128, v129\nv_cvt_pk_bf16_f32 v129, v130, v131\n"
"v_cvt_pk_bf16_f32 v130, v132, v133\nv_cvt_pk_bf16_f32 v131, v134, v135\nbuffer_store_dwordx4 v[128:131], v252, s[20:23], s84 offen offset:0 sc1\nv_cvt_pk_bf16_f32 v136, v136, v137\nv_cvt_pk_bf16_f32 v137, v138, v139\nv_cvt_pk_bf16_f32 v138, v140, v141\n"
"v_cvt_pk_bf16_f32 v139, v142, v143\nbuffer_store_dwordx4 v[136:139], v252, s[20:23], s84 offen offset:1024 sc1\ns_mov_b64 exec, 0\ns_cmp_eq_u32 s40, 1\ns_cbranch_scc1 LretO1\ns_cmp_eq_u32 s40, 2\n"
"s_cbranch_scc1 LretO1\ns_cmp_eq_u32 s40, 3\ns_cbranch_scc1 LretO1\ns_branch Lpg23\nLretO1:\ns_mov_b64 exec, -1\n"
"s_add_u32 s61, s79, 88498176\ns_lshl_b32 s62, s42, 3\ns_add_u32 s61, s61, s62\ns_waitcnt vmcnt(0)\ns_mov_b64 exec, 1\nbuffer_atomic_swap_x2 v[238:239], off, s[20:23], s61 sc1\n"
"s_mov_b64 exec, -1\nv_bfe_u32 v250, v252, 7, 1\nv_mul_u32_u24 v250, 2176, v250\nv_bfe_u32 v251, v252, 4, 3\nv_lshl_add_u32 v250, v251, 3, v250\ns_lshl_b32 s60, s90, 6\n"
"v_add_u32 v250, s60, v250\ns_sleep 3\nv_mov_b32 v248, 0\nv_mov_b32 v249, 0\ns_add_u32 s61, s80, 88498176\ns_mov_b64 exec, 0xffff\n"
"buffer_atomic_or_x2 v[248:249], v250, s[20:23], s61 offen sc0 sc1\ns_mov_b64 exec, -1\ns_add_u32 m0, s50, 49152\ns_add_u32 s63, s53, 458752\nbuffer_load_dwordx4 v220, s[8:11], s63 offen lds\nbuffer_load_dwordx4 v221, s[8:11], s63 offen offset:1024 lds\n"
"s_add_u32 m0, s50, 57344\ns_add_u32 s63, s53, 524288\nbuffer_load_dwordx4 v220, s[8:11], s63 offen lds\nbuffer_load_dwordx4 v221, s[8:11], s63 offen offset:1024 lds\ns_add_u32 m0, s50, 65536\ns_add_u32 s63, s53, 589824\n"
"buffer_load_dwordx4 v220, s[8:11], s63 offen lds\nbuffer_load_dwordx4 v221, s[8:11], s63 offen offset:1024 lds\ns_add_u32 m0, s50, 73728\ns_add_u32 s63, s53, 655360\nbuffer_load_dwordx4 v220, s[8:11], s63 offen lds\nbuffer_load_dwordx4 v221, s[8:11], s63 offen offset:1024 lds\n"
"s_add_u32 m0, s50, 81920\ns_add_u32 s63, s53, 720896\nbuffer_load_dwordx4 v220, s[8:11], s63 offen lds\nbuffer_load_dwordx4 v221, s[8:11], s63 offen offset:1024 lds\ns_waitcnt vmcnt(10)\nv_cmp_eq_u64 s[86:87], s[46:47], v[248:249]\n"
"s_nop 1\ns_and_b32 s88, s86, 0xffff\ns_cmp_eq_u32 s88, 0xffff\ns_cbranch_scc1 Lq706\nv_mov_b32 v246, 0\nv_mov_b32 v247, 0\n"
"s_add_u32 s61, s80, 88498176\ns_mov_b64 exec, 0xffff\nbuffer_atomic_or_x2 v[246:247], v250, s[20:23], s61 offen sc0 sc1\ns_mov_b64 exec, -1\nLq706:\ns_add_u32 m0, s50, 90112\n"
"s_add_u32 s63, s53, 786432\nbuffer_load_dwordx4 v220, s[8:11], s63 offen lds\nbuffer_load_dwordx4 v221, s[8:11], s63 offen offset:1024 lds\ns_add_u32 m0, s50, 98304\ns_add_u32 s63, s53, 851968\nbuffer_load_dwordx4 v220, s[8:11], s63 offen lds\n"
"buffer_load_dwordx4 v221, s[8:11], s63 offen offset:1024 lds\ns_add_u32 m0, s50, 106496\ns_add_u32 s63, s53, 917504\nbuffer_load_dwordx4 v220, s[8:11], s63 offen lds\nbuffer_load_dwordx4 v221, s[8:11], s63 offen offset:1024 lds\nv_bfe_u32 v234, v252, 4, 4\n"
"v_lshrrev_b32 v235, 8, v252\nv_lshlrev_b32 v235, 2, v235\nv_sub_u32 v234, v234, v235\nv_lshlrev_b32 v235, 4, v234\ns_mov_b64 s[60:61], 0x3f80\nv_lshlrev_b64 v[208:209], v235, s[60:61]\n"
"v_cmp_gt_u32 s[60:61], 4, v234\ns_nop 1\nv_cndmask_b32 v212, 0, v208, s[60:61]\nv_cndmask_b32 v208, 0, v208, s[60:61]\nv_cndmask_b32 v213, 0, v209, s[60:61]\nv_cndmask_b32 v209, 0, v209, s[60:61]\n"
"s_nop 1\nv_mfma_f32_16x16x32_bf16 v[0:3], v[208:211], v[192:195], 0\nv_mfma_f32_16x16x32_bf16 v[4:7], v[210:213], v[192:195], 0\nv_mfma_f32_16x16x32_bf16 v[8:11], v[208:211], v[196:199], 0\nv_mfma_f32_16x16x32_bf16 v[12:15], v[210:213], v[196:199], 0\nv_mfma_f32_16x16x32_bf16 v[16:19], v[208:211], v[200:203], 0\n"
"v_mfma_f32_16x16x32_bf16 v[20:23], v[210:213], v[200:203], 0\nv_mfma_f32_16x16x32_bf16 v[24:27], v[208:211], v[204:207], 0\nv_mfma_f32_16x16x32_bf16 v[28:31], v[210:213], v[204:207], 0\ns_cmp_eq_u32 s88, 0xffff\ns_cbranch_scc1 Lq707\ns_waitcnt vmcnt(6)\n"
"v_mov_b32 v248, v246\nv_mov_b32 v249, v247\nv_cmp_eq_u64 s[86:87], s[46:47], v[248:249]\ns_nop 1\ns_and_b32 s62, s86, 0xffff\ns_cmp_eq_u32 s62, 0xffff\n"
"s_cbranch_scc1 Lq698\nLq697:\nv_mov_b32 v248, 0\nv_mov_b32 v249, 0\ns_add_u32 s61, s80, 88498176\ns_mov_b64 exec, 0xffff\n"
"buffer_atomic_or_x2 v[248:249], v250, s[20:23], s61 offen sc0 sc1\ns_mov_b64 exec, -1\ns_waitcnt vmcnt(0)\nv_cmp_eq_u64 s[86:87], s[46:47], v[248:249]\ns_nop 1\ns_and_b32 s62, s86, 0xffff\n"
"s_cmp_eq_u32 s62, 0xffff\ns_cbranch_scc1 Lq698\ns_sub_u32 s48, s48, 1\ns_cmp_gt_i32 s48, 0\ns_cbranch_scc1 Lq697\nLq698:\n"
"Lq707:\ns_add_u32 s64, s70, 6291456\nbuffer_load_dwordx4 a[0:3], v252, s[20:23], s64 offen offset:0 sc1\ns_add_u32 s65, s70, 6324224\nbuffer_load_dwordx4 a[4:7], v252, s[20:23], s65 offen offset:0 sc1\ns_add_u32 s66, s71, 6291456\n"
"buffer_load_dwordx4 a[8:11], v252, s[20:23], s66 offen offset:0 sc1\ns_add_u32 s67, s71, 6324224\nbuffer_load_dwordx4 a[12:15], v252, s[20:23], s67 offen offset:0 sc1\nbuffer_load_dwordx4 a[16:19], v252, s[20:23], s64 offen offset:1024 sc1\nbuffer_load_dwordx4 a[20:23], v252, s[20:23], s65 offen offset:1024 sc1\nbuffer_load_dwordx4 a[24:27], v252, s[20:23], s66 offen offset:1024 sc1\n"
"buffer_load_dwordx4 a[28:31], v252, s[20:23], s67 offen offset:1024 sc1\ns_barrier\nR 160,188,49152\nR 162,188,53248\nR 164,184,0\nR 166,184,4096\n"
"R 168,189,49152\nR 170,189,53248\nR 172,185,0\nR 174,185,4096\nR 176,190,49152\nR 178,190,53248\n"
"R 180,186,0\nR 182,186,4096\nbuffer_load_dwordx4 a[32:35], v252, s[20:23], s64 offen offset:2048 sc1\nbuffer_load_dwordx4 a[36:39], v252, s[20:23], s65 offen offset:2048 sc1\nbuffer_load_dwordx4 a[40:43], v252, s[20:23], s66 offen offset:2048 sc1\nbuffer_load_dwordx4 a[44:47], v252, s[20:23], s67 offen offset:2048 sc1\n"
"buffer_load_dwordx4 a[48:51], v252, s[20:23], s64 offen offset:3072 sc1\nbuffer_load_dwordx4 a[52:55], v252, s[20:23], s65 offen offset:3072 sc1\nbuffer_load_dwordx4 a[56:59], v252, s[20:23], s66 offen offset:3072 sc1\nbuffer_load_dwordx4 a[60:63], v252, s[20:23], s67 offen offset:3072 sc1\ns_add_u32 s64, s70, 6295552\nbuffer_load_dwordx4 a[64:67], v252, s[20:23], s64 offen offset:0 sc1\n"
"s_add_u32 s65, s70, 6328320\nbuffer_load_dwordx4 a[68:71], v252, s[20:23], s65 offen offset:0 sc1\ns_add_u32 s66, s71, 6295552\nbuffer_load_dwordx4 a[72:75], v252, s[20:23], s66 offen offset:0 sc1\ns_add_u32 s67, s71, 6328320\nbuffer_load_dwordx4 a[76:79], v252, s[20:23], s67 offen offset:0 sc1\n"
"s_waitcnt vmcnt(19) lgkmcnt(10)\nM 0,160,0\nbuffer_load_dwordx4 a[80:83], v252, s[20:23], s64 offen offset:1024 sc1\ns_waitcnt vmcnt(16) lgkmcnt(8)\nM 0,164,16\nM 16,160,4\n"
"s_waitcnt vmcnt(15)\nM 16,164,20\nv_mfma_f32_16x16x32_bf16 v[32:35], v[160:163], a[8:11], 0\nbuffer_load_dwordx4 a[84:87], v252, s[20:23], s65 offen offset:1024 sc1\ns_waitcnt vmcnt(15)\nM 32,164,24\n"
"v_mfma_f32_16x16x32_bf16 v[48:51], v[160:163], a[12:15], 0\ns_waitcnt vmcnt(14)\nM 48,164,28\nG 4\nv_mfma_f32_16x16x32_bf16 v[52:55], v[168:171], a[12:15], 0\nR 160,191,49152\n"
"M 52,172,28\nbuffer_load_dwordx4 a[88:91], v252, s[20:23], s66 offen offset:1024 sc1\nv_mfma_f32_16x16x32_bf16 v[36:39], v[168:171], a[8:11], 0\nR 162,191,53248\nM 36,172,24\nM 20,168,4\n"
"R 164,187,0\nM 20,172,20\nbuffer_load_dwordx4 a[92:95], v252, s[20:23], s67 offen offset:1024 sc1\nM 4,168,0\nR 166,187,4096\nM 4,172,16\n"
"G 4\nM 8,176,0\nR 168,184,8192\nM 8,180,16\nbuffer_load_dwordx4 a[96:99], v252, s[20:23], s64 offen offset:2048 sc1\nM 24,176,4\n"
"R 170,184,12288\nM 24,180,20\nv_mfma_f32_16x16x32_bf16 v[40:43], v[176:179], a[8:11], 0\nR 172,184,16384\nM 40,180,24\nbuffer_load_dwordx4 a[100:103], v252, s[20:23], s65 offen offset:2048 sc1\n"
"v_mfma_f32_16x16x32_bf16 v[56:59], v[176:179], a[12:15], 0\nR 174,184,20480\nM 56,180,28\nG 4\nv_mfma_f32_16x16x32_bf16 v[60:63], v[160:163], a[12:15], 0\nR 176,185,8192\n"
"M 60,164,28\nbuffer_load_dwordx4 a[104:107], v252, s[20:23], s66 offen offset:2048 sc1\nv_mfma_f32_16x16x32_bf16 v[44:47], v[160:163], a[8:11], 0\nR 178,185,12288\nM 44,164,24\nM 28,160,4\n"
"R 180,185,16384\nM 28,164,20\nbuffer_load_dwordx4 a[108:111], v252, s[20:23], s67 offen offset:2048 sc1\nM 12,160,0\nR 182,185,20480\nM 12,164,16\n"
"G 4\ns_waitcnt vmcnt(19)\nM 0,168,32\nR 160,186,8192\ns_waitcnt vmcnt(15)\nM 0,172,48\n"
"buffer_load_dwordx4 a[112:115], v252, s[20:23], s64 offen offset:3072 sc1\nM 16,168,36\nR 162,186,12288\ns_waitcnt vmcnt(15)\nM 16,172,52\nM 32,168,40\n"
"R 164,186,16384\ns_waitcnt vmcnt(14)\nM 32,172,56\nbuffer_load_dwordx4 a[116:119], v252, s[20:23], s65 offen offset:3072 sc1\nM 48,168,44\nR 166,186,20480\n"
"s_waitcnt vmcnt(14)\nM 48,172,60\nG 4\nM 52,176,44\nR 168,187,8192\nM 52,180,60\n"
"buffer_load_dwordx4 a[120:123], v252, s[20:23], s66 offen offset:3072 sc1\nM 36,176,40\nR 170,187,12288\nM 36,180,56\nM 20,176,36\nR 172,187,16384\n"
"M 20,180,52\nbuffer_load_dwordx4 a[124:127], v252, s[20:23], s67 offen offset:3072 sc1\nM 4,176,32\nR 174,187,20480\nM 4,180,48\nG 4\n"
"M 8,160,32\nR 176,184,24576\nM 8,164,48\ns_add_u32 s64, s70, 6299648\nbuffer_load_dwordx4 a[128:131], v252, s[20:23], s64 offen offset:0 sc1\nM 24,160,36\n"
"R 178,184,28672\nM 24,164,52\nM 40,160,40\nR 180,184,32768\nM 40,164,56\ns_add_u32 s65, s70, 6332416\n"
"buffer_load_dwordx4 a[132:135], v252, s[20:23], s65 offen offset:0 sc1\nM 56,160,44\nR 182,184,36864\nM 56,164,60\nG 4\nM 60,168,44\n"
"R 160,185,24576\nM 60,172,60\ns_add_u32 s66, s71, 6299648\nbuffer_load_dwordx4 a[136:139], v252, s[20:23], s66 offen offset:0 sc1\nM 44,168,40\nR 162,185,28672\n"
"M 44,172,56\nM 28,168,36\nR 164,185,32768\nM 28,172,52\ns_add_u32 s67, s71, 6332416\nbuffer_load_dwordx4 a[140:143], v252, s[20:23], s67 offen offset:0 sc1\n"
"M 12,168,32\nR 166,185,36864\nM 12,172,48\ns_barrier\nG 4\ns_waitcnt vmcnt(19)\n"
"M 0,176,64\nR 168,186,24576\ns_waitcnt vmcnt(15)\nM 0,180,80\nbuffer_load_dwordx4 a[144:147], v252, s[20:23], s64 offen offset:1024 sc1\nM 16,176,68\n"
"R 170,186,28672\ns_waitcnt vmcnt(15)\nM 16,180,84\nM 32,176,72\nR 172,186,32768\ns_waitcnt vmcnt(14)\n"
"M 32,180,88\nbuffer_load_dwordx4 a[148:151], v252, s[20:23], s65 offen offset:1024 sc1\nM 48,176,76\nR 174,186,36864\ns_waitcnt vmcnt(14)\nM 48,180,92\n"
"G 4\nM 52,160,76\nR 176,187,24576\nM 52,164,92\nbuffer_load_dwordx4 a[152:155], v252, s[20:23], s66 offen offset:1024 sc1\nM 36,160,72\n"
"R 178,187,28672\nM 36,164,88\nM 20,160,68\nR 180,187,32768\nM 20,164,84\nbuffer_load_dwordx4 a[156:159], v252, s[20:23], s67 offen offset:1024 sc1\n"
"M 4,160,64\nR 182,187,36864\nM 4,164,80\nG 4\nM 8,168,64\nR 160,184,40960\n"
"M 8,172,80\nbuffer_load_dwordx4 a[160:163], v252, s[20:23], s64 offen offset:2048 sc1\nM 24,168,68\nR 162,184,45056\nM 24,172,84\nM 40,168,72\n"
"R 164,184,49152\nM 40,172,88\nbuffer_load_dwordx4 a[164:167], v252, s[20:23], s65 offen offset:2048 sc1\nM 56,168,76\nR 166,184,53248\nM 56,172,92\n"
"G 4\nM 60,176,76\nR 168,185,40960\nM 60,180,92\nbuffer_load_dwordx4 a[168:171], v252, s[20:23], s66 offen offset:2048 sc1\nM 44,176,72\n"
"R 170,185,45056\nM 44,180,88\nM 28,176,68\nR 172,185,49152\nM 28,180,84\nbuffer_load_dwordx4 a[172:175], v252, s[20:23], s67 offen offset:2048 sc1\n"
"M 12,176,64\nR 174,185,53248\nM 12,180,80\nG 4\ns_waitcnt vmcnt(19)\nM 0,160,96\n"
"R 176,186,40960\ns_waitcnt vmcnt(15)\nM 0,164,112\nbuffer_load_dwordx4 a[176:179], v252, s[20:23], s64 offen offset:3072 sc1\nM 16,160,100\nR 178,186,45056\n"
"s_waitcnt vmcnt(15)\nM 16,164,116\nM 32,160,104\nR 180,186,49152\ns_lshr_b32 s60, s45, 3\ns_lshl2_add_u32 s60, s60, s40\n"
"s_lshl_b32 s60, s60, 8\nv_lshl_add_u32 v224, v252, 3, s60\nv_add_u32 v224, Lqw1+24576-LqPC, v224\nv_min_u32 v224, LqE-LqPC-4, v224\ns_mov_b64 exec, 3\nglobal_load_dword v224, v224, s[98:99]\n"
"s_mov_b64 exec, -1\ns_waitcnt vmcnt(15)\nM 32,164,120\nbuffer_load_dwordx4 a[180:183], v252, s[20:23], s65 offen offset:3072 sc1\nM 48,160,108\nR 182,186,53248\n"
"s_waitcnt vmcnt(15)\nM 48,164,124\nG 4\nM 52,168,108\nR 160,187,40960\nM 52,172,124\n"
"buffer_load_dwordx4 a[184:187], v252, s[20:23], s66 offen offset:3072 sc1\nM 36,168,104\nR 162,187,45056\nM 36,172,120\nM 20,168,100\nR 164,187,49152\n"
"M 20,172,116\nbuffer_load_dwordx4 a[188:191], v252, s[20:23], s67 offen offset:3072 sc1\nM 4,168,96\nR 166,187,53248\nM 4,172,112\nG 4\n"
"M 8,176,96\nR 168,184,57344\nM 8,180,112\ns_add_u32 s64, s70, 6303744\nbuffer_load_dwordx4 a[192:195], v252, s[20:23], s64 offen offset:0 sc1\nM 24,176,100\n"
"R 170,184,61440\ns_add_u32 s63, s53, 983040\nM 24,180,116\ns_add_u32 m0, s50, 114688\nM 40,176,104\nR 172,188,0\n"
"M 40,180,120\ns_add_u32 s65, s70, 6336512\nbuffer_load_dwordx4 a[196:199], v252, s[20:23], s65 offen offset:0 sc1\nM 56,176,108\nR 174,188,4096\nM 56,180,124\n"
"buffer_load_dwordx4 v220, s[8:11], s63 offen lds\nG 4\nM 60,160,108\nR 176,185,57344\nM 60,164,124\ns_add_u32 s66, s71, 6303744\n"
"buffer_load_dwordx4 a[200:203], v252, s[20:23], s66 offen offset:0 sc1\nM 44,160,104\nR 178,185,61440\nM 44,164,120\nbuffer_load_dwordx4 v221, s[8:11], s63 offen offset:1024 lds\nM 28,160,100\n"
"R 180,189,0\nM 28,164,116\ns_add_u32 s67, s71, 6336512\nbuffer_load_dwordx4 a[204:207], v252, s[20:23], s67 offen offset:0 sc1\nM 12,160,96\nR 182,189,4096\n"
"M 12,164,112\ns_barrier\nG 4\ns_waitcnt vmcnt(22)\nM 0,168,128\nR 160,186,57344\n"
"s_waitcnt vmcnt(18)\nM 0,172,144\nbuffer_load_dwordx4 a[208:211], v252, s[20:23], s64 offen offset:1024 sc1\nM 16,168,132\nR 162,186,61440\ns_waitcnt vmcnt(18)\n"
"M 16,172,148\nLpg23:\ns_cbranch_execz LretO1\nM 32,168,136\nR 164,190,0\ns_waitcnt vmcnt(17)\n"
"M 32,172,152\nbuffer_load_dwordx4 a[212:215], v252, s[20:23], s65 offen offset:1024 sc1\nM 48,168,140\nR 166,190,4096\ns_waitcnt vmcnt(17)\nM 48,172,156\n"
"G 4\nM 52,176,140\nR 168,187,57344\nM 52,180,156\nbuffer_load_dwordx4 a[216:219], v252, s[20:23], s66 offen offset:1024 sc1\nM 36,176,136\n"
"R 170,187,61440\nM 36,180,152\nM 20,176,132\nR 172,191,0\nM 20,180,148\nbuffer_load_dwordx4 a[220:223], v252, s[20:23], s67 offen offset:1024 sc1\n"
"M 4,176,128\nR 174,191,4096\nM 4,180,144\nG 4\nM 8,160,128\nR 176,188,8192\n"
"M 8,164,144\nbuffer_load_dwordx4 a[224:227], v252, s[20:23], s64 offen offset:2048 sc1\nM 24,160,132\nR 178,188,12288\nM 24,164,148\nM 40,160,136\n"
"R 180,188,16384\nM 40,164,152\nbuffer_load_dwordx4 a[228:231], v252, s[20:23], s65 offen offset:2048 sc1\nM 56,160,140\nR 182,188,20480\nM 56,164,156\n"
"G 4\nM 60,168,140\nR 160,189,8192\nM 60,172,156\nbuffer_load_dwordx4 a[232:235], v252, s[20:23], s66 offen offset:2048 sc1\nM 44,168,136\n"
"R 162,189,12288\nM 44,172,152\nM 28,168,132\nR 164,189,16384\nM 28,172,148\nbuffer_load_dwordx4 a[236:239], v252, s[20:23], s67 offen offset:2048 sc1\n"
"M 12,168,128\nR 166,189,20480\nM 12,172,144\nG 4\ns_waitcnt vmcnt(22)\nM 0,176,160\n"
"R 168,190,8192\ns_waitcnt vmcnt(18)\nM 0,180,176\nbuffer_load_dwordx4 a[240:243], v252, s[20:23], s64 offen offset:3072 sc1\nM 16,176,164\nR 170,190,12288\n"
"s_waitcnt vmcnt(17)\nM 16,180,180\nM 32,176,168\nR 172,190,16384\ns_waitcnt vmcnt(16)\nM 32,180,184\n"
"buffer_load_dwordx4 a[244:247], v252, s[20:23], s65 offen offset:3072 sc1\nM 48,176,172\nR 174,190,20480\ns_waitcnt vmcnt(16)\nM 48,180,188\nG 4\n"
"M 52,160,172\nR 176,191,8192\nM 52,164,188\nbuffer_load_dwordx4 a[248:251], v252, s[20:23], s66 offen offset:3072 sc1\nM 36,160,168\nR 178,191,12288\n"
"s_waitcnt vmcnt(12)\ns_barrier\nM 36,164,184\nM 20,160,164\nR 180,191,16384\nM 20,164,180\n"
"buffer_load_dwordx4 a[252:255], v252, s[20:23], s67 offen offset:3072 sc1\nM 4,160,160\nR 182,191,20480\nM 4,164,176\nG 4\nM 8,168,160\n"
"R 160,188,24576\nM 8,172,176\nM 24,168,164\nR 162,188,28672\nM 24,172,180\nM 40,168,168\n"
"R 164,188,32768\nM 40,172,184\nM 56,168,172\nR 166,188,36864\nM 56,172,188\nG 4\n"
"M 60,176,172\nR 168,189,24576\nM 60,180,188\ns_movk_i32 s63, 7168\nv_mad_u32_u24 v220, v228, s63, v229\nv_add_u32 v221, 3668992, v220\n"
"s_add_u32 m0, s50, 0\ns_add_u32 s63, s54, 0\nbuffer_load_dwordx4 v220, s[12:15], s63 offen lds\nM 44,176,168\nR 170,189,28672\nM 44,180,184\n"
"M 28,176,164\nR 172,189,32768\nM 28,180,180\nbuffer_load_dwordx4 v221, s[12:15], s63 offen offset:1024 lds\nM 12,176,160\nR 174,189,36864\n"
"s_add_u32 m0, s50, 8192\nM 12,180,176\nG 4\nM 32,160,200\nR 176,190,24576\ns_add_u32 s63, s54, 229376\n"
"s_waitcnt vmcnt(11)\nM 32,164,216\nR 178,190,28672\nM 48,160,204\nR 180,190,32768\ns_waitcnt vmcnt(10)\n"
"M 48,164,220\nR 182,190,36864\nG 4\nM 52,168,204\nR 160,191,24576\nbuffer_load_dwordx4 v220, s[12:15], s63 offen lds\n"
"M 52,172,220\nR 162,191,28672\nM 36,168,200\nR 164,191,32768\nM 36,172,216\nR 166,191,36864\n"
"G 4\nM 40,176,200\nR 168,188,40960\nbuffer_load_dwordx4 v221, s[12:15], s63 offen offset:1024 lds\nM 40,180,216\nR 170,188,45056\n"
"s_add_u32 m0, s50, 16384\nM 56,176,204\nR 172,188,49152\ns_add_u32 s63, s54, 128\nM 56,180,220\nR 174,188,53248\n"
"G 4\nM 60,160,204\nR 176,189,40960\nbuffer_load_dwordx4 v220, s[12:15], s63 offen lds\nM 60,164,220\nR 178,189,45056\n"
"M 44,160,200\nR 180,189,49152\nM 44,164,216\nR 182,189,53248\nG 4\ns_waitcnt vmcnt(10)\n"
"M 32,168,232\nR 160,190,40960\nbuffer_load_dwordx4 v221, s[12:15], s63 offen offset:1024 lds\ns_waitcnt vmcnt(7)\nM 32,172,248\nR 162,190,45056\n"
"s_add_u32 m0, s50, 24576\nM 48,168,236\nR 164,190,49152\ns_add_u32 s63, s54, 229504\ns_waitcnt vmcnt(6)\nM 48,172,252\n"
"R 166,190,53248\nG 4\nM 52,176,236\nR 168,191,40960\nbuffer_load_dwordx4 v220, s[12:15], s63 offen lds\nM 52,180,252\n"
"R 170,191,45056\nM 36,176,232\nR 172,191,49152\nM 36,180,248\nR 174,191,53248\nG 4\n"
"M 40,160,232\nR 176,188,24576\nbuffer_load_dwordx4 v221, s[12:15], s63 offen offset:1024 lds\nM 40,164,248\nR 178,188,28672\nM 56,160,236\n"
"R 180,188,32768\nM 56,164,252\nR 182,188,36864\nG 4\nM 60,168,236\nR 160,189,24576\n"
"M 60,172,252\nR 162,189,28672\nM 44,168,232\nR 164,189,32768\nM 44,172,248\nR 166,189,36864\n"
"G 4\nM 0,176,192\nR 168,190,24576\nM 0,180,208\nR 170,190,28672\ns_mov_b32 m0, s51\n"
"M 16,176,196\nR 172,190,32768\nv_cvt_pk_bf16_f32 v32, v32, v33\nv_cvt_pk_bf16_f32 v33, v34, v35\nM 16,180,212\nR 174,190,36864\n"
"G 4\nv_cvt_pk_bf16_f32 v34, v36, v37\nM 20,160,196\nR 176,191,24576\nv_cvt_pk_bf16_f32 v35, v38, v39\nv_cvt_pk_bf16_f32 v36, v40, v41\n"
"M 20,164,212\nR 178,191,28672\nv_cvt_pk_bf16_f32 v37, v42, v43\nv_cvt_pk_bf16_f32 v38, v44, v45\nM 4,160,192\nR 180,191,32768\n"
"v_cvt_pk_bf16_f32 v39, v46, v47\nv_cvt_pk_bf16_f32 v40, v48, v49\nM 4,164,208\nR 182,191,36864\nG 4\nv_cvt_pk_bf16_f32 v41, v50, v51\n"
"M 8,168,192\nR 160,188,40960\nv_cvt_pk_bf16_f32 v42, v52, v53\nv_cvt_pk_bf16_f32 v43, v54, v55\nM 8,172,208\nR 162,188,45056\n"
"v_cvt_pk_bf16_f32 v44, v56, v57\nv_cvt_pk_bf16_f32 v45, v58, v59\nM 24,168,196\nR 164,188,49152\nv_cvt_pk_bf16_f32 v46, v60, v61\nv_cvt_pk_bf16_f32 v47, v62, v63\n"
"M 24,172,212\nR 166,188,53248\nds_write_addtid_b32 v32 offset:0\nG 5\nM 28,176,196\nR 168,189,40960\n"
"ds_write_addtid_b32 v33 offset:256\nM 28,180,212\nR 170,189,45056\nds_write_addtid_b32 v34 offset:512\nM 12,176,192\nR 172,189,49152\n"
"ds_write_addtid_b32 v35 offset:768\nds_write_addtid_b32 v36 offset:1024\nds_write_addtid_b32 v37 offset:1280\nds_write_addtid_b32 v38 offset:1536\nds_write_addtid_b32 v39 offset:1792\nds_write_addtid_b32 v40 offset:2048\n"
"ds_write_addtid_b32 v41 offset:2304\nds_write_addtid_b32 v42 offset:2560\nds_write_addtid_b32 v43 offset:2816\nds_write_addtid_b32 v44 offset:3072\nds_write_addtid_b32 v45 offset:3328\nds_write_addtid_b32 v46 offset:3584\n"
"ds_write_addtid_b32 v47 offset:3840\nM 12,180,208\nR 174,189,53248\nG 15\nM 0,160,224\nR 176,190,40960\n"
"s_add_u32 m0, s50, 32768\nM 0,164,240\nR 178,190,45056\nG 3\ns_barrier\ns_add_u32 s63, s54, 458752\n"
"M 16,160,228\nR 180,190,49152\nbuffer_load_dwordx4 v220, s[12:15], s63 offen lds\nds_read2st64_b32 v[32:33], v225 offset0:0 offset1:1\nM 16,164,244\nR 182,190,53248\n"
"ds_read2st64_b32 v[34:35], v225 offset0:2 offset1:3\nG 6\nM 20,168,228\nR 160,191,40960\nds_read2st64_b32 v[36:37], v225 offset0:4 offset1:5\nM 20,172,244\n"
"R 162,191,45056\nds_read2st64_b32 v[38:39], v225 offset0:6 offset1:7\nM 4,168,224\nR 164,191,49152\nbuffer_load_dwordx4 v221, s[12:15], s63 offen offset:1024 lds\nds_read2st64_b32 v[40:41], v225 offset0:8 offset1:9\n"
"M 4,172,240\nR 166,191,53248\ns_add_u32 m0, s50, 40960\ns_add_u32 s63, s54, 688128\nds_read2st64_b32 v[42:43], v225 offset0:10 offset1:11\nG 9\n"
"M 8,176,224\nbuffer_load_dwordx4 v220, s[12:15], s63 offen lds\nds_read2st64_b32 v[44:45], v225 offset0:12 offset1:13\nM 8,180,240\nds_read2st64_b32 v[46:47], v225 offset0:14 offset1:15\nM 24,176,228\n"
"M 24,180,244\nG 3\nM 28,160,228\nbuffer_load_dwordx4 v221, s[12:15], s63 offen offset:1024 lds\nM 28,164,244\nM 12,160,224\n"
"M 12,164,240\nG 0\ns_barrier\nv_mfma_f32_16x16x32_bf16 v[0:3], v[208:211], v[32:35], v[0:3]\nv_mfma_f32_16x16x32_bf16 v[4:7], v[210:213], v[32:35], v[4:7]\nv_mfma_f32_16x16x32_bf16 v[8:11], v[208:211], v[36:39], v[8:11]\n"
"v_mfma_f32_16x16x32_bf16 v[12:15], v[210:213], v[36:39], v[12:15]\nv_mfma_f32_16x16x32_bf16 v[16:19], v[208:211], v[40:43], v[16:19]\nv_mfma_f32_16x16x32_bf16 v[20:23], v[210:213], v[40:43], v[20:23]\nv_mfma_f32_16x16x32_bf16 v[24:27], v[208:211], v[44:47], v[24:27]\nv_mfma_f32_16x16x32_bf16 v[28:31], v[210:213], v[44:47], v[28:31]\ns_lshl_b32 s84, s75, 11\n"
"s_add_u32 s84, s84, s49\ns_add_u32 s84, s84, 14680064\ns_add_u32 s85, s84, 32768\nv_mov_b32 v238, s46\nv_mov_b32 v239, s47\nv_cvt_pk_bf16_f32 v192, v0, v1\n"
"v_cvt_pk_bf16_f32 v193, v2, v3\nv_cvt_pk_bf16_f32 v194, v4, v5\nv_cvt_pk_bf16_f32 v195, v6, v7\nbuffer_store_dwordx4 v[192:195], v252, s[20:23], s84 offen offset:0 sc1\nv_cvt_pk_bf16_f32 v196, v8, v9\nv_cvt_pk_bf16_f32 v197, v10, v11\n"
"v_cvt_pk_bf16_f32 v198, v12, v13\nv_cvt_pk_bf16_f32 v199, v14, v15\nbuffer_store_dwordx4 v[196:199], v252, s[20:23], s84 offen offset:1024 sc1\nv_cvt_pk_bf16_f32 v200, v16, v17\nv_cvt_pk_bf16_f32 v201, v18, v19\nv_cvt_pk_bf16_f32 v202, v20, v21\n"
"v_cvt_pk_bf16_f32 v203, v22, v23\nbuffer_store_dwordx4 v[200:203], v252, s[20:23], s85 offen offset:0 sc1\nv_cvt_pk_bf16_f32 v204, v24, v25\nv_cvt_pk_bf16_f32 v205, v26, v27\nv_cvt_pk_bf16_f32 v206, v28, v29\nv_cvt_pk_bf16_f32 v207, v30, v31\n"
"buffer_store_dwordx4 v[204:207], v252, s[20:23], s85 offen offset:1024 sc1\ns_mov_b64 exec, 0\ns_cmp_eq_u32 s40, 1\ns_cbranch_scc1 Lpg25\ns_cmp_eq_u32 s40, 2\ns_cbranch_scc1 Lpg26\n"
"s_cmp_eq_u32 s40, 3\ns_cbranch_scc1 Lpg27\ns_branch Lpg24\nLretO1n:\ns_mov_b64 exec, -1\ns_add_u32 s61, s79, 88637440\n"
"s_lshl_b32 s62, s75, 3\ns_add_u32 s61, s61, s62\ns_waitcnt vmcnt(0)\ns_mov_b64 exec, 1\nbuffer_atomic_swap_x2 v[238:239], off, s[20:23], s61 sc1\ns_mov_b64 exec, -1\n"
"Lqw1:\nv_bfe_u32 v250, v252, 7, 1\nv_mul_u32_u24 v250, 2176, v250\nv_bfe_u32 v251, v252, 4, 3\nv_lshl_add_u32 v250, v251, 3, v250\ns_lshl_b32 s60, s90, 6\n"
"v_add_u32 v250, s60, v250\ns_sleep 3\nv_mov_b32 v248, 0\nv_mov_b32 v249, 0\ns_add_u32 s61, s80, 88637440\ns_mov_b64 exec, 0xffff\n"
"buffer_atomic_or_x2 v[248:249], v250, s[20:23], s61 offen sc0 sc1\ns_mov_b64 exec, -1\ns_add_u32 m0, s50, 49152\ns_add_u32 s63, s54, 458880\nbuffer_load_dwordx4 v220, s[12:15], s63 offen lds\nbuffer_load_dwordx4 v221, s[12:15], s63 offen offset:1024 lds\n"
"s_add_u32 m0, s50, 57344\ns_add_u32 s63, s54, 688256\nbuffer_load_dwordx4 v220, s[12:15], s63 offen lds\nbuffer_load_dwordx4 v221, s[12:15], s63 offen offset:1024 lds\ns_add_u32 m0, s50, 65536\ns_add_u32 s63, s54, 917504\n"
"buffer_load_dwordx4 v220, s[12:15], s63 offen lds\nbuffer_load_dwordx4 v221, s[12:15], s63 offen offset:1024 lds\ns_add_u32 m0, s50, 73728\ns_add_u32 s63, s54, 1146880\nbuffer_load_dwordx4 v220, s[12:15], s63 offen lds\nbuffer_load_dwordx4 v221, s[12:15], s63 offen offset:1024 lds\n"
"s_add_u32 m0, s50, 81920\ns_add_u32 s63, s54, 917632\nbuffer_load_dwordx4 v220, s[12:15], s63 offen lds\nbuffer_load_dwordx4 v221, s[12:15], s63 offen offset:1024 lds\ns_waitcnt vmcnt(10)\nv_cmp_eq_u64 s[86:87], s[46:47], v[248:249]\n"
"s_nop 1\ns_and_b32 s88, s86, 0xffff\ns_cmp_eq_u32 s88, 0xffff\ns_cbranch_scc1 Lq768\nv_mov_b32 v246, 0\nv_mov_b32 v247, 0\n"
"s_add_u32 s61, s80, 88637440\ns_mov_b64 exec, 0xffff\nbuffer_atomic_or_x2 v[246:247], v250, s[20:23], s61 offen sc0 sc1\ns_mov_b64 exec, -1\nLq768:\ns_add_u32 m0, s50, 90112\n"
"s_add_u32 s63, s54, 1147008\nbuffer_load_dwordx4 v220, s[12:15], s63 offen lds\nbuffer_load_dwordx4 v221, s[12:15], s63 offen offset:1024 lds\ns_mov_b64 exec, 0\ns_cmp_eq_u32 s40, 1\ns_cbranch_scc1 Lpg33\n"
"s_cmp_eq_u32 s40, 2\ns_cbranch_scc1 LretW1z\ns_cmp_eq_u32 s40, 3\ns_cbranch_scc1 LretW1z\ns_branch Lpg32\nLretW1z:\n"
"s_mov_b64 exec, -1\nv_mov_b32 v230, 0\nv_mov_b32 v231, 0\nv_mov_b32 v232, 0\nv_mov_b32 v233, 0\nv_dot2c_f32_bf16 v230, s76, v192\n"
"v_dot2c_f32_bf16 v231, v192, v192\nv_dot2c_f32_bf16 v232, s76, v200\nv_dot2c_f32_bf16 v233, v200, v200\nv_dot2c_f32_bf16 v230, s76, v193\nv_dot2c_f32_bf16 v231, v193, v193\nv_dot2c_f32_bf16 v232, s76, v201\n"
"v_dot2c_f32_bf16 v233, v201, v201\nv_dot2c_f32_bf16 v230, s76, v194\nv_dot2c_f32_bf16 v231, v194, v194\nv_dot2c_f32_bf16 v232, s76, v202\nv_dot2c_f32_bf16 v233, v202, v202\nv_dot2c_f32_bf16 v230, s76, v195\n"
"v_dot2c_f32_bf16 v231, v195, v195\nv_dot2c_f32_bf16 v232, s76, v203\nv_dot2c_f32_bf16 v233, v203, v203\nv_dot2c_f32_bf16 v230, s76, v196\nv_dot2c_f32_bf16 v231, v196, v196\nv_dot2c_f32_bf16 v232, s76, v204\n"
"v_dot2c_f32_bf16 v233, v204, v204\nv_dot2c_f32_bf16 v230, s76, v197\nv_dot2c_f32_bf16 v231, v197, v197\nv_dot2c_f32_bf16 v232, s76, v205\nv_dot2c_f32_bf16 v233, v205, v205\nv_dot2c_f32_bf16 v230, s76, v198\n"
"v_dot2c_f32_bf16 v231, v198, v198\nv_dot2c_f32_bf16 v232, s76, v206\nv_dot2c_f32_bf16 v233, v206, v206\nv_dot2c_f32_bf16 v230, s76, v199\nv_dot2c_f32_bf16 v231, v199, v199\nv_dot2c_f32_bf16 v232, s76, v207\n"
"v_dot2c_f32_bf16 v233, v207, v207\nv_lshrrev_b32 v234, 5, v252\nv_and_b32 v234, 24, v234\nv_bfe_u32 v235, s46, v234, 8\nv_lshrrev_b32 v234, 1, v234\nv_and_b32 v236, 0xf0, v252\n"
"v_add_u32 v234, v234, v236\nv_permlane32_swap_b32 v230, v231\nv_permlane32_swap_b32 v232, v233\nv_add_f32 v230, v230, v231\nv_add_f32 v232, v232, v233\ns_nop 1\n"
"v_permlane16_swap_b32 v230, v232\nv_add_f32 v230, v230, v232\ns_movk_i32 s62, 0xff\nv_bfi_b32 v230, s62, v235, v230\ns_lshr_b32 s61, s49, 4\ns_lshl1_add_u32 s61, s53, s61\n"
"s_add_u32 s61, s61, 83361792\nbuffer_store_dword v230, v234, s[20:23], s61 offen sc1\ns_cmp_eq_u32 s88, 0xffff\ns_cbranch_scc1 Lq769\ns_waitcnt vmcnt(3)\nv_mov_b32 v248, v246\n"
"v_mov_b32 v249, v247\nv_cmp_eq_u64 s[86:87], s[46:47], v[248:249]\ns_nop 1\ns_and_b32 s62, s86, 0xffff\ns_cmp_eq_u32 s62, 0xffff\ns_cbranch_scc1 Lq760\n"
"Lq759:\nv_mov_b32 v248, 0\nv_mov_b32 v249, 0\ns_add_u32 s61, s80, 88637440\ns_mov_b64 exec, 0xffff\nbuffer_atomic_or_x2 v[248:249], v250, s[20:23], s61 offen sc0 sc1\n"
"s_mov_b64 exec, -1\ns_waitcnt vmcnt(0)\nv_cmp_eq_u64 s[86:87], s[46:47], v[248:249]\ns_nop 1\ns_and_b32 s62, s86, 0xffff\ns_cmp_eq_u32 s62, 0xffff\n"
"s_cbranch_scc1 Lq760\ns_sub_u32 s48, s48, 1\ns_cmp_gt_i32 s48, 0\ns_cbranch_scc1 Lq759\nLq760:\nLq769:\n"
"s_add_u32 s64, s70, 14680064\nbuffer_load_dwordx4 a[0:3], v252, s[20:23], s64 offen offset:0 sc1\ns_add_u32 s65, s70, 14712832\nbuffer_load_dwordx4 a[4:7], v252, s[20:23], s65 offen offset:0 sc1\ns_add_u32 s66, s71, 14680064\nbuffer_load_dwordx4 a[8:11], v252, s[20:23], s66 offen offset:0 sc1\n"
"s_add_u32 s67, s71, 14712832\nbuffer_load_dwordx4 a[12:15], v252, s[20:23], s67 offen offset:0 sc1\nbuffer_load_dwordx4 a[16:19], v252, s[20:23], s64 offen offset:1024 sc1\nbuffer_load_dwordx4 a[20:23], v252, s[20:23], s65 offen offset:1024 sc1\nbuffer_load_dwordx4 a[24:27], v252, s[20:23], s66 offen offset:1024 sc1\nbuffer_load_dwordx4 a[28:31], v252, s[20:23], s67 offen offset:1024 sc1\n"
"s_barrier\nR 160,184,0\nR 162,184,4096\nR 164,184,8192\nR 166,184,12288\nR 168,185,0\n"
"R 170,185,4096\nR 172,185,8192\nR 174,185,12288\nR 176,186,0\nR 178,186,4096\nR 180,186,8192\n"
"R 182,186,12288\nbuffer_load_dwordx4 a[32:35], v252, s[20:23], s64 offen offset:2048 sc1\nbuffer_load_dwordx4 a[36:39], v252, s[20:23], s65 offen offset:2048 sc1\nbuffer_load_dwordx4 a[40:43], v252, s[20:23], s66 offen offset:2048 sc1\nbuffer_load_dwordx4 a[44:47], v252, s[20:23], s67 offen offset:2048 sc1\nbuffer_load_dwordx4 a[48:51], v252, s[20:23], s64 offen offset:3072 sc1\n"
"buffer_load_dwordx4 a[52:55], v252, s[20:23], s65 offen offset:3072 sc1\nbuffer_load_dwordx4 a[56:59], v252, s[20:23], s66 offen offset:3072 sc1\nbuffer_load_dwordx4 a[60:63], v252, s[20:23], s67 offen offset:3072 sc1\ns_waitcnt vmcnt(15) lgkmcnt(10)\nv_mfma_f32_16x16x32_bf16 v[32:35], v[160:163], a[0:3], 0\ns_add_u32 s64, s70, 14684160\n"
"buffer_load_dwordx4 a[64:67], v252, s[20:23], s64 offen offset:0 sc1\ns_waitcnt vmcnt(12) lgkmcnt(8)\nM 32,164,16\nv_mfma_f32_16x16x32_bf16 v[48:51], v[160:163], a[4:7], 0\ns_waitcnt vmcnt(11)\nM 48,164,20\n"
"v_mfma_f32_16x16x32_bf16 v[64:67], v[160:163], a[8:11], 0\ns_waitcnt vmcnt(10)\nM 64,164,24\nv_mfma_f32_16x16x32_bf16 v[80:83], v[160:163], a[12:15], 0\ns_add_u32 s65, s70, 14716928\nbuffer_load_dwordx4 a[68:71], v252, s[20:23], s65 offen offset:0 sc1\n"
"s_waitcnt vmcnt(10)\nM 80,164,28\nG 4\nv_mfma_f32_16x16x32_bf16 v[84:87], v[168:171], a[12:15], 0\nR 160,187,0\nM 84,172,28\n"
"v_mfma_f32_16x16x32_bf16 v[68:71], v[168:171], a[8:11], 0\nR 162,187,4096\nM 68,172,24\nv_mfma_f32_16x16x32_bf16 v[52:55], v[168:171], a[4:7], 0\nR 164,187,8192\nM 52,172,20\n"
"s_add_u32 s66, s71, 14684160\nbuffer_load_dwordx4 a[72:75], v252, s[20:23], s66 offen offset:0 sc1\nv_mfma_f32_16x16x32_bf16 v[36:39], v[168:171], a[0:3], 0\nR 166,187,12288\nM 36,172,16\nG 4\n"
"v_mfma_f32_16x16x32_bf16 v[40:43], v[176:179], a[0:3], 0\nR 168,184,16384\nM 40,180,16\nv_mfma_f32_16x16x32_bf16 v[56:59], v[176:179], a[4:7], 0\nR 170,184,20480\nM 56,180,20\n"
"Lpg24:\ns_cbranch_execz Lpg28\ns_add_u32 s67, s71, 14716928\nbuffer_load_dwordx4 a[76:79], v252, s[20:23], s67 offen offset:0 sc1\nv_mfma_f32_16x16x32_bf16 v[72:75], v[176:179], a[8:11], 0\nR 172,184,24576\n"
"M 72,180,24\nv_mfma_f32_16x16x32_bf16 v[88:91], v[176:179], a[12:15], 0\nR 174,184,28672\nM 88,180,28\nG 4\nv_mfma_f32_16x16x32_bf16 v[92:95], v[160:163], a[12:15], 0\n"
"R 176,185,16384\ns_add_u32 s63, s54, 1376256\nM 92,164,28\nbuffer_load_dwordx4 a[80:83], v252, s[20:23], s64 offen offset:1024 sc1\nv_mfma_f32_16x16x32_bf16 v[76:79], v[160:163], a[8:11], 0\nR 178,185,20480\n"
"M 76,164,24\ns_add_u32 m0, s50, 98304\nv_mfma_f32_16x16x32_bf16 v[60:63], v[160:163], a[4:7], 0\nR 180,185,24576\nM 60,164,20\nbuffer_load_dwordx4 v220, s[12:15], s63 offen lds\n"
"v_mfma_f32_16x16x32_bf16 v[44:47], v[160:163], a[0:3], 0\nR 182,185,28672\nM 44,164,16\nG 4\nv_mfma_f32_16x16x32_bf16 v[96:99], v[168:171], a[0:3], 0\nR 160,186,16384\n"
"M 96,172,16\nbuffer_load_dwordx4 a[84:87], v252, s[20:23], s65 offen offset:1024 sc1\nv_mfma_f32_16x16x32_bf16 v[112:115], v[168:171], a[4:7], 0\nR 162,186,20480\nM 112,172,20\nv_mfma_f32_16x16x32_bf16 v[128:131], v[168:171], a[8:11], 0\n"
"R 164,186,24576\nM 128,172,24\nbuffer_load_dwordx4 a[88:91], v252, s[20:23], s66 offen offset:1024 sc1\nv_mfma_f32_16x16x32_bf16 v[144:147], v[168:171], a[12:15], 0\nR 166,186,28672\nM 144,172,28\n"
"s_barrier\nG 4\nv_mfma_f32_16x16x32_bf16 v[148:151], v[176:179], a[12:15], 0\nR 168,187,16384\nM 148,180,28\nv_mfma_f32_16x16x32_bf16 v[132:135], v[176:179], a[8:11], 0\n"
"R 170,187,20480\nM 132,180,24\nbuffer_load_dwordx4 a[92:95], v252, s[20:23], s67 offen offset:1024 sc1\nv_mfma_f32_16x16x32_bf16 v[116:119], v[176:179], a[4:7], 0\nR 172,187,24576\nM 116,180,20\n"
"v_mfma_f32_16x16x32_bf16 v[100:103], v[176:179], a[0:3], 0\nR 174,187,28672\nM 100,180,16\nG 4\nv_mfma_f32_16x16x32_bf16 v[104:107], v[160:163], a[0:3], 0\nR 176,184,32768\n"
"M 104,164,16\nbuffer_load_dwordx4 a[96:99], v252, s[20:23], s64 offen offset:2048 sc1\nv_mfma_f32_16x16x32_bf16 v[120:123], v[160:163], a[4:7], 0\nR 178,184,36864\nM 120,164,20\nv_mfma_f32_16x16x32_bf16 v[136:139], v[160:163], a[8:11], 0\n"
"R 180,184,40960\nM 136,164,24\nbuffer_load_dwordx4 v221, s[12:15], s63 offen offset:1024 lds\nv_mfma_f32_16x16x32_bf16 v[152:155], v[160:163], a[12:15], 0\nR 182,184,45056\nM 152,164,28\n"
"G 4\nv_mfma_f32_16x16x32_bf16 v[156:159], v[168:171], a[12:15], 0\nR 160,185,32768\nM 156,172,28\nbuffer_load_dwordx4 a[100:103], v252, s[20:23], s65 offen offset:2048 sc1\nv_mfma_f32_16x16x32_bf16 v[140:143], v[168:171], a[8:11], 0\n"
"R 162,185,36864\nM 140,172,24\ns_add_u32 m0, s50, 106496\nv_mfma_f32_16x16x32_bf16 v[124:127], v[168:171], a[4:7], 0\nR 164,185,40960\ns_add_u32 s63, s54, 1605632\n"
"M 124,172,20\nbuffer_load_dwordx4 a[104:107], v252, s[20:23], s66 offen offset:2048 sc1\nv_mfma_f32_16x16x32_bf16 v[108:111], v[168:171], a[0:3], 0\nR 166,185,45056\nM 108,172,16\nG 4\n"
"s_waitcnt vmcnt(20)\nM 32,176,32\nR 168,186,32768\ns_waitcnt vmcnt(16)\nM 32,180,48\nbuffer_load_dwordx4 v220, s[12:15], s63 offen lds\n"
"M 48,176,36\nR 170,186,36864\ns_waitcnt vmcnt(16)\nM 48,180,52\nbuffer_load_dwordx4 a[108:111], v252, s[20:23], s67 offen offset:2048 sc1\nM 64,176,40\n"
"R 172,186,40960\ns_waitcnt vmcnt(16)\nM 64,180,56\nM 80,176,44\nR 174,186,45056\ns_waitcnt vmcnt(15)\n"
"M 80,180,60\ns_barrier\nG 4\nM 84,160,44\nR 176,187,32768\nM 84,164,60\n"
"buffer_load_dwordx4 a[112:115], v252, s[20:23], s64 offen offset:3072 sc1\nM 68,160,40\nR 178,187,36864\nM 68,164,56\nM 52,160,36\nR 180,187,40960\n"
"M 52,164,52\nM 36,160,32\nR 182,187,45056\nM 36,164,48\nG 4\nM 40,168,32\n"
"R 160,184,49152\nM 40,172,48\nbuffer_load_dwordx4 a[116:119], v252, s[20:23], s65 offen offset:3072 sc1\nM 56,168,36\nR 162,184,53248\nM 56,172,52\n"
"M 72,168,40\nR 164,184,57344\nM 72,172,56\nbuffer_load_dwordx4 a[120:123], v252, s[20:23], s66 offen offset:3072 sc1\nM 88,168,44\nR 166,184,61440\n"
"M 88,172,60\nG 4\nM 92,176,44\nR 168,185,49152\nM 92,180,60\nbuffer_load_dwordx4 v221, s[12:15], s63 offen offset:1024 lds\n"
"M 76,176,40\nR 170,185,53248\nM 76,180,56\nbuffer_load_dwordx4 a[124:127], v252, s[20:23], s67 offen offset:3072 sc1\nM 60,176,36\nR 172,185,57344\n"
"M 60,180,52\nM 44,176,32\nR 174,185,61440\nM 44,180,48\nG 4\nM 96,160,32\n"
"R 176,186,49152\nM 96,164,48\ns_add_u32 s64, s70, 14688256\nbuffer_load_dwordx4 a[128:131], v252, s[20:23], s64 offen offset:0 sc1\nM 112,160,36\nR 178,186,53248\n"
"M 112,164,52\ns_lshr_b32 s84, s49, 4\nM 128,160,40\nR 180,186,57344\nM 128,164,56\ns_add_u32 s84, s84, 83361792\n"
"M 144,160,44\nR 182,186,61440\nM 144,164,60\ns_barrier\nG 4\nM 148,168,44\n"
"R 160,187,49152\nM 148,172,60\ns_add_u32 s65, s70, 14721024\nbuffer_load_dwordx4 a[132:135], v252, s[20:23], s65 offen offset:0 sc1\nM 132,168,40\nR 162,187,53248\n"
"M 132,172,56\nbuffer_load_dwordx4 v[0:3], v252, s[20:23], s84 offen offset:0 sc1\nM 116,168,36\nR 164,187,57344\nM 116,172,52\ns_add_u32 s66, s71, 14688256\n"
"buffer_load_dwordx4 a[136:139], v252, s[20:23], s66 offen offset:0 sc1\nM 100,168,32\nR 166,187,61440\nM 100,172,48\nG 4\nM 104,176,32\n"
"R 168,188,0\nM 104,180,48\nbuffer_load_dwordx4 v[4:7], v252, s[20:23], s84 offen offset:1024 sc1\nM 120,176,36\nR 170,188,4096\nM 120,180,52\n"
"s_add_u32 s67, s71, 14721024\nbuffer_load_dwordx4 a[140:143], v252, s[20:23], s67 offen offset:0 sc1\nM 136,176,40\nR 172,188,8192\nM 136,180,56\nbuffer_load_dwordx4 v[8:11], v252, s[20:23], s84 offen offset:2048 sc1\n"
"M 152,176,44\nR 174,188,12288\nM 152,180,60\nbuffer_load_dwordx4 v[12:15], v252, s[20:23], s84 offen offset:3072 sc1\nG 4\nM 156,160,44\n"
"R 176,189,0\nM 156,164,60\nbuffer_load_dwordx4 a[144:147], v252, s[20:23], s64 offen offset:1024 sc1\nM 140,160,40\nR 178,189,4096\nM 140,164,56\n"
"M 124,160,36\nR 180,189,8192\nM 124,164,52\nM 108,160,32\nR 182,189,12288\nM 108,164,48\n"
"G 4\ns_waitcnt vmcnt(28)\nM 32,168,64\nR 160,190,0\ns_waitcnt vmcnt(24)\nM 32,172,80\n"
"buffer_load_dwordx4 a[148:151], v252, s[20:23], s65 offen offset:1024 sc1\nM 48,168,68\nR 162,190,4096\ns_waitcnt vmcnt(23)\nM 48,172,84\ns_add_u32 m0, s50, 114688\n"
"M 64,168,72\nR 164,190,8192\ns_add_u32 s63, s54, 1376384\ns_waitcnt vmcnt(22)\nM 64,172,88\nbuffer_load_dwordx4 a[152:155], v252, s[20:23], s66 offen offset:1024 sc1\n"
"M 80,168,76\nR 166,190,12288\ns_waitcnt vmcnt(22)\nM 80,172,92\ns_barrier\nG 4\n"
"M 84,176,76\nR 168,191,0\nM 84,180,92\nbuffer_load_dwordx4 v220, s[12:15], s63 offen lds\nM 68,176,72\nR 170,191,4096\n"
"M 68,180,88\nbuffer_load_dwordx4 a[156:159], v252, s[20:23], s67 offen offset:1024 sc1\nM 52,176,68\nR 172,191,8192\nM 52,180,84\nbuffer_load_dwordx4 v221, s[12:15], s63 offen offset:1024 lds\n"
"M 36,176,64\nR 174,191,12288\ns_add_u32 m0, s50, 0\nM 36,180,80\nG 4\nM 40,160,64\n"
"R 176,188,16384\ns_add_u32 s63, s54, 1605760\nM 40,164,80\nbuffer_load_dwordx4 a[160:163], v252, s[20:23], s64 offen offset:2048 sc1\nM 56,160,68\nR 178,188,20480\n"
"M 56,164,84\nbuffer_load_dwordx4 v220, s[12:15], s63 offen lds\nM 72,160,72\nR 180,188,24576\nM 72,164,88\nbuffer_load_dwordx4 v221, s[12:15], s63 offen offset:1024 lds\n"
"M 88,160,76\nR 182,188,28672\nM 88,164,92\nG 4\nM 92,168,76\nR 160,189,16384\n"
"M 92,172,92\nbuffer_load_dwordx4 a[164:167], v252, s[20:23], s65 offen offset:2048 sc1\nM 76,168,72\nR 162,189,20480\nM 76,172,88\nM 60,168,68\n"
"R 164,189,24576\nM 60,172,84\nbuffer_load_dwordx4 a[168:171], v252, s[20:23], s66 offen offset:2048 sc1\nM 44,168,64\nR 166,189,28672\nM 44,172,80\n"
"G 4\nM 96,176,64\nR 168,190,16384\ns_mov_b32 s85, 0x0c0c0400\nM 96,180,80\ns_waitcnt vmcnt(16)\n"
"v_perm_b32 v16, v1, v0, s85\nv_perm_b32 v20, v3, v2, s85\nM 112,176,68\nR 170,190,20480\nv_lshl_or_b32 v16, v20, 16, v16\nM 112,180,84\n"
"buffer_load_dwordx4 a[172:175], v252, s[20:23], s67 offen offset:2048 sc1\nM 128,176,72\nR 172,190,24576\ns_waitcnt vmcnt(15)\nv_perm_b32 v17, v5, v4, s85\nM 128,180,88\n"
"s_add_u32 m0, s50, 8192\nv_perm_b32 v21, v7, v6, s85\nv_lshl_or_b32 v17, v21, 16, v17\nM 144,176,76\nR 174,190,28672\ns_add_u32 s63, s54, 1835008\n"
"M 144,180,92\ns_barrier\nG 4\nM 148,160,76\nR 176,191,16384\ns_waitcnt vmcnt(13)\n"
"v_perm_b32 v18, v9, v8, s85\nM 148,164,92\nbuffer_load_dwordx4 a[176:179], v252, s[20:23], s64 offen offset:3072 sc1\nM 132,160,72\nR 178,191,20480\nv_perm_b32 v22, v11, v10, s85\n"
"M 132,164,88\nbuffer_load_dwordx4 v220, s[12:15], s63 offen lds\nM 116,160,68\nR 180,191,24576\nv_lshl_or_b32 v18, v22, 16, v18\nM 116,164,84\n"
"buffer_load_dwordx4 v221, s[12:15], s63 offen offset:1024 lds\nM 100,160,64\nR 182,191,28672\ns_add_u32 m0, s50, 16384\nM 100,164,80\nG 4\n"
"s_waitcnt vmcnt(15)\nv_perm_b32 v19, v13, v12, s85\nv_perm_b32 v23, v15, v14, s85\nM 104,168,64\nR 160,188,32768\ns_add_u32 s63, s54, 2064384\n"
"M 104,172,80\nbuffer_load_dwordx4 a[180:183], v252, s[20:23], s65 offen offset:3072 sc1\nM 120,168,68\nR 162,188,36864\nv_lshl_or_b32 v19, v23, 16, v19\nM 120,172,84\n"
"buffer_load_dwordx4 v220, s[12:15], s63 offen lds\nM 136,168,72\nR 164,188,40960\nv_min3_u32 v230, v16, v17, v18\nM 136,172,88\nbuffer_load_dwordx4 a[184:187], v252, s[20:23], s66 offen offset:3072 sc1\n"
"M 152,168,76\nR 166,188,45056\nv_min3_u32 v230, v230, v19, v19\nM 152,172,92\nG 4\nv_max3_u32 v231, v16, v17, v18\n"
"v_max3_u32 v231, v231, v19, v19\nM 156,176,76\nR 168,189,32768\nbuffer_load_dwordx4 v221, s[12:15], s63 offen offset:1024 lds\nM 156,180,92\nv_cmp_ne_u32 s[86:87], s46, v230\n"
"v_cmp_ne_u32 vcc, s46, v231\nM 140,176,72\nR 170,189,36864\nM 140,180,88\nbuffer_load_dwordx4 a[188:191], v252, s[20:23], s67 offen offset:3072 sc1\nM 124,176,68\n"
"R 172,189,40960\nM 124,180,84\ns_or_b64 s[86:87], s[86:87], vcc\ns_cbranch_scc1 Lq818\nLq819:\nv_and_b32 v0, 0xffffff00, v0\n"
"M 108,176,64\nR 174,189,45056\nv_and_b32 v1, 0xffffff00, v1\nM 108,180,80\nG 4\nv_and_b32 v2, 0xffffff00, v2\n"
"v_and_b32 v3, 0xffffff00, v3\nM 32,160,96\nR 176,190,32768\nv_and_b32 v4, 0xffffff00, v4\nM 32,164,112\ns_add_u32 s64, s70, 14692352\n"
"buffer_load_dwordx4 a[192:195], v252, s[20:23], s64 offen offset:0 sc1\nM 48,160,100\nR 178,190,36864\nv_and_b32 v5, 0xffffff00, v5\nM 48,164,116\nv_and_b32 v6, 0xffffff00, v6\n"
"v_and_b32 v7, 0xffffff00, v7\nM 64,160,104\nR 180,190,40960\nv_and_b32 v8, 0xffffff00, v8\nM 64,164,120\ns_add_u32 m0, s50, 24576\n"
"v_and_b32 v9, 0xffffff00, v9\nv_and_b32 v10, 0xffffff00, v10\nM 80,160,108\nR 182,190,45056\ns_add_u32 s63, s54, 2293760\nM 80,164,124\n"
"s_waitcnt vmcnt(12)\ns_barrier\nG 4\nM 84,168,108\nR 160,191,32768\nv_and_b32 v11, 0xffffff00, v11\n"
"M 84,172,124\ns_add_u32 s65, s70, 14725120\nbuffer_load_dwordx4 a[196:199], v252, s[20:23], s65 offen offset:0 sc1\nM 68,168,104\nR 162,191,36864\nv_and_b32 v12, 0xffffff00, v12\n"
"M 68,172,120\nbuffer_load_dwordx4 v220, s[12:15], s63 offen lds\nM 52,168,100\nR 164,191,40960\nv_and_b32 v13, 0xffffff00, v13\nM 52,172,116\n"
"s_add_u32 s66, s71, 14692352\nbuffer_load_dwordx4 a[200:203], v252, s[20:23], s66 offen offset:0 sc1\nM 36,168,96\nR 166,191,45056\nv_and_b32 v14, 0xffffff00, v14\nM 36,172,112\n"
"G 4\nv_and_b32 v15, 0xffffff00, v15\nv_add_f32 v0, v0, v8\nM 40,176,96\nR 168,188,49152\nbuffer_load_dwordx4 v221, s[12:15], s63 offen offset:1024 lds\n"
"M 40,180,112\ns_add_u32 m0, s50, 32768\ns_add_u32 s63, s54, 2523136\nbuffer_load_dwordx4 v220, s[12:15], s63 offen lds\nM 56,176,100\nR 170,188,53248\n"
"v_add_f32 v1, v1, v9\nM 56,180,116\ns_add_u32 s67, s71, 14725120\nbuffer_load_dwordx4 a[204:207], v252, s[20:23], s67 offen offset:0 sc1\nM 72,176,104\nR 172,184,0\n"
"v_add_f32 v2, v2, v10\nM 72,180,120\nbuffer_load_dwordx4 v221, s[12:15], s63 offen offset:1024 lds\nM 88,176,108\nR 174,184,4096\nv_add_f32 v3, v3, v11\n"
"M 88,180,124\nG 4\nv_add_f32 v4, v4, v12\nv_add_f32 v5, v5, v13\nM 92,160,108\nR 176,189,49152\n"
"v_add_f32 v6, v6, v14\nM 92,164,124\nbuffer_load_dwordx4 a[208:211], v252, s[20:23], s64 offen offset:1024 sc1\nM 76,160,104\nR 178,189,53248\nv_add_f32 v7, v7, v15\n"
"M 76,164,120\nv_add_f32 v0, v0, v4\nv_add_f32 v1, v1, v5\nM 60,160,100\nR 180,185,0\nLq1034:\n"
"s_lshr_b32 s60, s45, 3\ns_lshl2_add_u32 s60, s60, s40\ns_lshl_b32 s60, s60, 8\nv_lshl_add_u32 v224, v252, 3, s60\nv_add_u32 v224, Lq1034+4096-LqPC, v224\nv_min_u32 v224, LqE-LqPC-4, v224\n"
"s_mov_b64 exec, 3\nglobal_load_dword v224, v224, s[98:99]\ns_mov_b64 exec, -1\nM 60,164,116\nv_add_f32 v2, v2, v6\nv_add_f32 v3, v3, v7\n"
"M 44,160,96\nR 182,185,4096\nv_permlane32_swap_b32 v0, v2\nM 44,164,112\nG 4\nv_permlane32_swap_b32 v1, v3\n"
"v_add_f32 v0, v0, v2\nM 96,168,96\nR 160,190,49152\nv_add_f32 v1, v1, v3\nM 96,172,112\nbuffer_load_dwordx4 a[212:215], v252, s[20:23], s65 offen offset:1024 sc1\n"
"M 112,168,100\nR 162,190,53248\nv_permlane16_swap_b32 v0, v1\nM 112,172,116\ns_add_u32 m0, s50, 40960\nv_add_f32 v230, v0, v1\n"
"v_mov_b32 v232, v230\nM 128,168,104\nR 164,186,0\ns_add_u32 s63, s54, 2752512\nM 128,172,120\nbuffer_load_dwordx4 a[216:219], v252, s[20:23], s66 offen offset:1024 sc1\n"
"M 144,168,108\nR 166,186,4096\nv_permlane16_swap_b32 v230, v232\nM 144,172,124\ns_waitcnt vmcnt(13)\ns_barrier\n"
"G 4\nM 148,176,108\nR 168,191,49152\nv_mov_b32 v231, v230\nM 148,180,124\nbuffer_load_dwordx4 v220, s[12:15], s63 offen lds\n"
"M 132,176,104\nR 170,191,53248\nv_mov_b32 v233, v232\nM 132,180,120\nbuffer_load_dwordx4 a[220:223], v252, s[20:23], s67 offen offset:1024 sc1\nM 116,176,100\n"
"R 172,187,0\nv_permlane32_swap_b32 v230, v231\nM 116,180,116\nbuffer_load_dwordx4 v221, s[12:15], s63 offen offset:1024 lds\nM 100,176,96\nR 174,187,4096\n"
"s_add_u32 m0, s50, 49152\nM 100,180,112\nG 4\nv_permlane32_swap_b32 v232, v233\nv_mul_f32 v230, 0xba800000, v230\nM 104,160,96\n"
"R 176,184,8192\ns_add_u32 s63, s54, 2981888\nM 104,164,112\nbuffer_load_dwordx4 a[224:227], v252, s[20:23], s64 offen offset:2048 sc1\nM 120,160,100\nR 178,184,12288\n"
"v_mul_f32 v214, v230, v230\nM 120,164,116\nbuffer_load_dwordx4 v220, s[12:15], s63 offen lds\nM 136,160,104\nR 180,184,16384\nv_mul_f32 v231, 0x3a800000, v231\n"
"M 136,164,120\nLpg25:\ns_cbranch_execz Lpg29\nbuffer_load_dwordx4 v221, s[12:15], s63 offen offset:1024 lds\nM 152,160,108\nR 182,184,20480\n"
"v_sub_f32 v231, v231, v214\nM 152,164,124\nG 4\nv_max_f32 v231, 0, v231\nv_add_f32 v231, 0x3727c5ac, v231\nM 156,168,108\n"
"R 160,185,8192\nv_and_b32 v208, s77, v230\nM 156,172,124\nbuffer_load_dwordx4 a[228:231], v252, s[20:23], s65 offen offset:2048 sc1\nM 140,168,104\nR 162,185,12288\n"
"v_sub_f32 v230, v230, v208\nM 140,172,120\nv_rsq_f32 v234, v231\nM 124,168,100\nR 164,185,16384\nv_mul_f32 v216, v231, v234\n"
"M 124,172,116\nbuffer_load_dwordx4 a[232:235], v252, s[20:23], s66 offen offset:2048 sc1\nM 108,168,96\nR 166,185,20480\nv_cvt_pk_bf16_f32 v209, v230, v216\nM 108,172,112\n"
"G 4\nv_cvt_pk_bf16_f32 v208, v208, v208\nv_mul_f32 v214, 0xc0135761, v234\nM 32,176,128\nR 168,186,8192\nv_mul_f32 v215, v234, v234\n"
"M 32,180,144\ns_mov_b64 s[60:61], 0xffff\nv_cndmask_b32 v208, 0, v208, s[60:61]\nv_mul_f32 v215, v215, v214\nM 48,176,132\nR 170,186,12288\n"
"v_mul_f32 v215, 0x3d372713, v215\nM 48,180,148\nbuffer_load_dwordx4 a[236:239], v252, s[20:23], s67 offen offset:2048 sc1\nM 64,176,136\nR 172,186,16384\nv_mul_f32 v232, 0xba800000, v232\n"
"M 64,180,152\ns_add_u32 m0, s50, 57344\ns_mov_b64 s[60:61], 0xffff\nv_cndmask_b32 v209, 0, v209, s[60:61]\nM 80,176,140\nR 174,186,20480\n"
"s_add_u32 s63, s54, 3211264\nM 80,180,156\ns_waitcnt vmcnt(13)\ns_barrier\nG 4\nM 84,160,140\n"
"R 176,187,8192\nv_mul_f32 v217, v232, v232\nM 84,164,156\nbuffer_load_dwordx4 a[240:243], v252, s[20:23], s64 offen offset:3072 sc1\nM 68,160,136\nR 178,187,12288\n"
"v_mul_f32 v233, 0x3a800000, v233\nM 68,164,152\nbuffer_load_dwordx4 v220, s[12:15], s63 offen lds\nM 52,160,132\nR 180,187,16384\nv_sub_f32 v233, v233, v217\n"
"M 52,164,148\nbuffer_load_dwordx4 v221, s[12:15], s63 offen offset:1024 lds\nM 36,160,128\nR 182,187,20480\ns_add_u32 m0, s50, 65536\nM 36,164,144\n"
"G 4\nv_max_f32 v233, 0, v233\nv_add_f32 v233, 0x3727c5ac, v233\nM 40,168,128\nR 160,184,24576\ns_add_u32 s63, s54, 3440640\n"
"M 40,172,144\nbuffer_load_dwordx4 a[244:247], v252, s[20:23], s65 offen offset:3072 sc1\nM 56,168,132\nR 162,184,28672\nv_and_b32 v212, s77, v232\nM 56,172,148\n"
"buffer_load_dwordx4 v220, s[12:15], s63 offen lds\nM 72,168,136\nR 164,184,32768\nv_sub_f32 v232, v232, v212\nM 72,172,152\nbuffer_load_dwordx4 a[248:251], v252, s[20:23], s66 offen offset:3072 sc1\n"
"M 88,168,140\nR 166,184,36864\nv_cvt_pk_bf16_f32 v212, v212, v212\nM 88,172,156\nG 4\nv_rsq_f32 v235, v233\n"
"M 92,176,140\nR 168,185,24576\nbuffer_load_dwordx4 v221, s[12:15], s63 offen offset:1024 lds\nM 92,180,156\ns_mov_b64 s[60:61], 0xffff\nv_cndmask_b32 v212, 0, v212, s[60:61]\n"
"v_mul_f32 v219, v233, v235\nM 76,176,136\nR 170,185,28672\nv_cvt_pk_bf16_f32 v213, v232, v219\nM 76,180,152\nbuffer_load_dwordx4 a[252:255], v252, s[20:23], s67 offen offset:3072 sc1\n"
"M 60,176,132\nR 172,185,32768\nv_mul_f32 v217, 0xc0135761, v235\nM 60,180,148\ns_mov_b64 s[60:61], 0xffff\nv_cndmask_b32 v213, 0, v213, s[60:61]\n"
"v_mul_f32 v218, v235, v235\nM 44,176,128\nR 174,185,36864\nv_mul_f32 v218, v218, v217\nM 44,180,144\nG 4\n"
"v_mul_f32 v218, 0x3d372713, v218\nM 32,160,160\nR 176,186,24576\nM 32,164,176\nM 48,160,164\nR 178,186,28672\n"
"M 48,164,180\ns_add_u32 m0, s50, 73728\nM 64,160,168\nR 180,186,32768\nM 64,164,184\ns_add_u32 s63, s54, 1835136\n"
"M 80,160,172\nR 182,186,36864\nM 80,164,188\ns_waitcnt vmcnt(11)\ns_barrier\nG 4\n"
"M 84,168,172\nR 160,187,24576\nM 84,172,188\nbuffer_load_dwordx4 v220, s[12:15], s63 offen lds\nM 68,168,168\nR 162,187,28672\n"
"M 68,172,184\nM 52,168,164\nR 164,187,32768\nv_bfe_u32 v250, v252, 5, 1\nv_mul_u32_u24 v250, 2560, v250\nM 52,172,180\n"
"buffer_load_dwordx4 v221, s[12:15], s63 offen offset:1024 lds\nv_and_b32 v251, 16, v252\nM 36,168,160\nR 166,187,36864\ns_add_u32 m0, s50, 81920\nv_lshl_add_u32 v250, v251, 1, v250\n"
"M 36,172,176\ns_lshl_b32 s60, s90, 6\nv_add_u32 v250, s60, v250\nG 4\nM 40,176,160\nR 168,184,40960\n"
"s_add_u32 s63, s54, 2064512\nM 40,180,176\nbuffer_load_dwordx4 v220, s[12:15], s63 offen lds\nM 56,176,164\nR 170,184,45056\nM 56,180,180\n"
"M 72,176,168\nR 172,184,49152\nM 72,180,184\nbuffer_load_dwordx4 v221, s[12:15], s63 offen offset:1024 lds\nM 88,176,172\nR 174,184,53248\n"
"s_add_u32 m0, s50, 90112\nM 88,180,188\nG 4\nM 92,160,172\nR 176,185,40960\ns_add_u32 s63, s54, 2293888\n"
"M 92,164,188\nbuffer_load_dwordx4 v220, s[12:15], s63 offen lds\nM 76,160,168\nR 178,185,45056\nM 76,164,184\nM 60,160,164\n"
"R 180,185,49152\nM 60,164,180\nbuffer_load_dwordx4 v221, s[12:15], s63 offen offset:1024 lds\nM 44,160,160\nR 182,185,53248\ns_add_u32 s63, s54, 2523264\n"
"M 44,164,176\nG 4\nM 32,168,192\nR 160,186,40960\nM 32,172,208\ns_add_u32 m0, s50, 98304\n"
"M 48,168,196\nR 162,186,45056\nM 48,172,212\nbuffer_load_dwordx4 v220, s[12:15], s63 offen lds\nM 64,168,200\nR 164,186,49152\n"
"M 64,172,216\nM 80,168,204\nR 166,186,53248\nM 80,172,220\ns_waitcnt vmcnt(8)\ns_barrier\n"
"G 4\nM 84,176,204\nR 168,187,40960\nM 84,180,220\nbuffer_load_dwordx4 v221, s[12:15], s63 offen offset:1024 lds\nM 68,176,200\n"
"R 170,187,45056\ns_add_u32 s63, s54, 2752640\nM 68,180,216\ns_add_u32 m0, s50, 106496\nM 52,176,196\nR 172,187,49152\n"
"M 52,180,212\nbuffer_load_dwordx4 v220, s[12:15], s63 offen lds\nM 36,176,192\nR 174,187,53248\nM 36,180,208\nG 4\n"
"M 40,160,192\nR 176,184,57344\nM 40,164,208\nbuffer_load_dwordx4 v221, s[12:15], s63 offen offset:1024 lds\nM 56,160,196\nR 178,184,61440\n"
"s_add_u32 s63, s54, 2982016\nM 56,164,212\ns_add_u32 m0, s50, 114688\nM 72,160,200\nR 180,188,0\nM 72,164,216\n"
"buffer_load_dwordx4 v220, s[12:15], s63 offen lds\nM 88,160,204\nR 182,188,4096\nM 88,164,220\nG 4\nM 92,168,204\n"
"R 160,185,57344\nM 92,172,220\nbuffer_load_dwordx4 v221, s[12:15], s63 offen offset:1024 lds\nM 76,168,200\nR 162,185,61440\ns_add_u32 s63, s54, 3211392\n"
"M 76,172,216\ns_add_u32 m0, s50, 0\nM 60,168,196\nR 164,189,0\nM 60,172,212\nbuffer_load_dwordx4 v220, s[12:15], s63 offen lds\n"
"M 44,168,192\nR 166,189,4096\nM 44,172,208\nG 4\nM 32,176,224\nR 168,186,57344\n"
"M 32,180,240\nM 48,176,228\nR 170,186,61440\nM 48,180,244\nbuffer_load_dwordx4 v221, s[12:15], s63 offen offset:1024 lds\nM 64,176,232\n"
"R 172,190,0\ns_add_u32 s63, s54, 3440768\nM 64,180,248\ns_add_u32 m0, s50, 8192\nM 80,176,236\nR 174,190,4096\n"
"s_waitcnt vmcnt(14)\nM 80,180,252\ns_waitcnt vmcnt(10)\ns_barrier\nG 4\nM 84,160,236\n"
"R 176,187,57344\nM 84,164,252\nbuffer_load_dwordx4 v220, s[12:15], s63 offen lds\nM 68,160,232\nR 178,187,61440\nM 68,164,248\n"
"M 52,160,228\nR 180,191,0\nM 52,164,244\nbuffer_load_dwordx4 v221, s[12:15], s63 offen offset:1024 lds\nM 36,160,224\nR 182,191,4096\n"
"s_add_u32 m0, s50, 16384\nM 36,164,240\nG 4\nM 40,168,224\nR 160,188,8192\ns_add_u32 s63, s55, 0\n"
"M 40,172,240\nbuffer_load_dwordx4 v222, s[12:15], s63 offen lds\nM 56,168,228\nR 162,188,12288\nM 56,172,244\nM 72,168,232\n"
"R 164,188,16384\nM 72,172,248\nM 88,168,236\nR 166,188,20480\ns_mov_b32 s60, 157184\nv_bfe_u32 v246, v252, 4, 4\n"
"v_lshl_add_u32 v246, v246, 3, s60\nM 88,172,252\nds_read_b64 v[238:239], v246 offset:0\nG 5\nM 92,176,236\nR 168,189,8192\n"
"ds_read_b64 v[240:241], v246 offset:128\nM 92,180,252\nds_read_b64 v[242:243], v246 offset:256\nM 76,176,232\nR 170,189,12288\nds_read_b64 v[244:245], v246 offset:384\n"
"M 76,180,248\nds_read_b64 v[246:247], v246 offset:0\nM 60,176,228\nR 172,189,16384\nM 60,180,244\nbuffer_load_dwordx4 v223, s[12:15], s63 offen offset:1024 lds\n"
"M 44,176,224\nR 174,189,20480\nM 44,180,240\ns_waitcnt vmcnt(10) lgkmcnt(2)\ns_barrier\nM 96,160,128\n"
"R 176,190,8192\nM 96,164,144\ns_mov_b32 m0, s51\nM 112,160,132\nR 178,190,12288\nM 112,164,148\n"
"ds_write_addtid_b32 v64 offset:0\nM 128,160,136\nR 180,190,16384\nv_mfma_f32_16x16x32_bf16 v[32:35], v[238:241], v[208:211], v[32:35]\nds_write_addtid_b32 v65 offset:256\nds_write_addtid_b32 v66 offset:512\n"
"M 128,164,152\nds_write_addtid_b32 v67 offset:768\nM 144,160,140\nR 182,190,20480\nv_mfma_f32_16x16x32_bf16 v[36:39], v[240:243], v[208:211], v[36:39]\nds_write_addtid_b32 v68 offset:1024\n"
"ds_write_addtid_b32 v69 offset:1280\nM 144,164,156\nds_write_addtid_b32 v70 offset:1536\nG 11\nM 148,168,140\nR 160,191,8192\n"
"v_mfma_f32_16x16x32_bf16 v[40:43], v[242:245], v[208:211], v[40:43]\nds_write_addtid_b32 v71 offset:1792\nds_write_addtid_b32 v72 offset:2048\nM 148,172,156\nds_write_addtid_b32 v73 offset:2304\nM 132,168,136\n"
"R 162,191,12288\nv_mfma_f32_16x16x32_bf16 v[44:47], v[244:247], v[208:211], v[44:47]\nds_write_addtid_b32 v74 offset:2560\nds_write_addtid_b32 v75 offset:2816\nM 132,172,152\nds_write_addtid_b32 v76 offset:3072\n"
"M 116,168,132\nR 164,191,16384\nv_mfma_f32_16x16x32_bf16 v[48:51], v[244:247], v[210:213], v[48:51]\nds_write_addtid_b32 v77 offset:3328\nds_write_addtid_b32 v78 offset:3584\nM 116,172,148\n"
"ds_write_addtid_b32 v79 offset:3840\nM 100,168,128\nR 166,191,20480\nv_mfma_f32_16x16x32_bf16 v[52:55], v[238:241], v[210:213], v[52:55]\nds_write_addtid_b32 v80 offset:4096\nds_write_addtid_b32 v81 offset:4352\n"
"M 100,172,144\nds_write_addtid_b32 v82 offset:4608\nG 15\nM 104,176,128\nR 168,188,24576\nv_mfma_f32_16x16x32_bf16 v[56:59], v[240:243], v[210:213], v[56:59]\n"
"ds_write_addtid_b32 v83 offset:4864\nds_write_addtid_b32 v84 offset:5120\nM 104,180,144\nds_write_addtid_b32 v85 offset:5376\nM 120,176,132\nR 170,188,28672\n"
"v_mfma_f32_16x16x32_bf16 v[60:63], v[242:245], v[210:213], v[60:63]\nds_write_addtid_b32 v86 offset:5632\nds_write_addtid_b32 v87 offset:5888\nM 120,180,148\nds_write_addtid_b32 v88 offset:6144\nM 136,176,136\n"
"R 172,188,32768\nM 136,180,152\nds_write_addtid_b32 v89 offset:6400\nM 152,176,140\nR 174,188,36864\nds_write_addtid_b32 v90 offset:6656\n"
"M 152,180,156\nds_write_addtid_b32 v91 offset:6912\nds_write_addtid_b32 v92 offset:7168\nG 15\nM 156,160,140\nR 176,189,24576\n"
"ds_write_addtid_b32 v93 offset:7424\nM 156,164,156\nds_write_addtid_b32 v94 offset:7680\nds_write_addtid_b32 v95 offset:7936\nM 140,160,136\nR 178,189,28672\n"
"s_add_u32 m0, s50, 24576\ns_add_u32 s63, s55, 458752\nM 140,164,152\nbuffer_load_dwordx4 v222, s[12:15], s63 offen lds\nM 124,160,132\nR 180,189,32768\n"
"s_waitcnt vmcnt(7) lgkmcnt(2)\ns_barrier\nM 124,164,148\nds_read2st64_b32 v[64:65], v225 offset0:0 offset1:1\nds_read2st64_b32 v[66:67], v225 offset0:2 offset1:3\nM 108,160,128\n"
"R 182,189,36864\nds_read2st64_b32 v[68:69], v225 offset0:4 offset1:5\nM 108,164,144\nds_read2st64_b32 v[70:71], v225 offset0:6 offset1:7\nds_read2st64_b32 v[72:73], v225 offset0:8 offset1:9\nM 96,168,160\n"
"R 160,190,24576\nds_read2st64_b32 v[74:75], v225 offset0:10 offset1:11\nM 96,172,176\nbuffer_load_dwordx4 v223, s[12:15], s63 offen offset:1024 lds\nds_read2st64_b32 v[76:77], v225 offset0:12 offset1:13\nM 112,168,164\n"
"R 162,190,28672\nds_read2st64_b32 v[78:79], v225 offset0:14 offset1:15\nM 112,172,180\ns_add_u32 m0, s50, 32768\nds_read2st64_b32 v[80:81], v225 offset0:16 offset1:17\nds_read2st64_b32 v[82:83], v225 offset0:18 offset1:19\n"
"M 128,168,168\nR 164,190,32768\nds_read2st64_b32 v[84:85], v225 offset0:20 offset1:21\nG 14\nv_add_f32 v32, v32, v64\nM 128,172,184\n"
"s_add_u32 s63, s55, 917504\nds_read2st64_b32 v[86:87], v225 offset0:22 offset1:23\nds_read2st64_b32 v[88:89], v225 offset0:24 offset1:25\nM 144,168,172\nR 166,190,36864\nds_read2st64_b32 v[90:91], v225 offset0:26 offset1:27\n"
"v_add_f32 v33, v33, v65\nM 144,172,188\nds_read2st64_b32 v[92:93], v225 offset0:28 offset1:29\nds_read2st64_b32 v[94:95], v225 offset0:30 offset1:31\nG 15\nM 148,176,172\n"
"R 168,191,24576\nv_add_f32 v34, v34, v66\nv_add_f32 v35, v35, v67\nM 148,180,188\nbuffer_load_dwordx4 v222, s[12:15], s63 offen lds\nv_add_f32 v36, v36, v68\n"
"M 132,176,168\nR 170,191,28672\nv_add_f32 v37, v37, v69\nv_add_f32 v38, v38, v70\nM 132,180,184\nv_add_f32 v39, v39, v71\n"
"v_add_f32 v40, v40, v72\nM 116,176,164\nR 172,191,32768\nv_add_f32 v41, v41, v73\nM 116,180,180\nG 15\n"
"v_add_f32 v42, v42, v74\nv_add_f32 v43, v43, v75\nM 100,176,160\nR 174,191,36864\nv_add_f32 v44, v44, v76\nM 100,180,176\n"
"G 7\nv_add_f32 v45, v45, v77\nv_add_f32 v46, v46, v78\nM 104,160,160\nR 176,188,40960\nv_add_f32 v47, v47, v79\n"
"M 104,164,176\nbuffer_load_dwordx4 v223, s[12:15], s63 offen offset:1024 lds\nM 120,160,164\nR 178,188,45056\nv_add_f32 v48, v48, v80\nM 120,164,180\n"
"s_add_u32 m0, s50, 40960\nv_add_f32 v49, v49, v81\nv_add_f32 v50, v50, v82\nM 136,160,168\nR 180,188,49152\nv_add_f32 v51, v51, v83\n"
"M 136,164,184\ns_add_u32 s63, s55, 1376256\nv_add_f32 v52, v52, v84\nv_add_f32 v53, v53, v85\nM 152,160,172\nR 182,188,53248\n"
"v_add_f32 v54, v54, v86\nM 152,164,188\nG 4\nv_add_f32 v55, v55, v87\nv_add_f32 v56, v56, v88\nM 156,168,172\n"
"R 160,189,40960\nv_add_f32 v57, v57, v89\nM 156,172,188\nbuffer_load_dwordx4 v222, s[12:15], s63 offen lds\nM 140,168,168\nR 162,189,45056\n"
"v_add_f32 v58, v58, v90\nM 140,172,184\nv_add_f32 v59, v59, v91\nv_add_f32 v60, v60, v92\nM 124,168,164\nR 164,189,49152\n"
"v_add_f32 v61, v61, v93\nM 124,172,180\nv_add_f32 v62, v62, v94\nv_add_f32 v63, v63, v95\nM 108,168,160\nR 166,189,53248\n"
"v_mul_f32 v230, v32, v32\nv_mul_f32 v231, v33, v33\nM 108,172,176\ns_add_u32 s84, s73, 37748736\ns_add_u32 s85, s73, 37863424\nG 4\n"
"v_mul_f32 v232, v34, v34\nM 96,176,192\nR 168,190,40960\nv_mul_f32 v233, v35, v35\nv_fma_f32 v230, v230, v215, v214\nM 96,180,208\n"
"buffer_load_dwordx4 v223, s[12:15], s63 offen offset:1024 lds\nv_fma_f32 v231, v231, v215, v214\nM 112,176,196\nR 170,190,45056\ns_waitcnt vmcnt(6)\ns_barrier\n"
"Lpg26:\ns_cbranch_execz Lpg30\nv_fma_f32 v232, v232, v215, v214\nM 112,180,212\ns_add_u32 m0, s50, 49152\nv_fma_f32 v233, v233, v215, v214\n"
"v_mul_f32 v230, v32, v230\nv_mul_f32 v231, v33, v231\nM 128,176,200\nR 172,190,49152\nv_mul_f32 v232, v34, v232\nv_mul_f32 v233, v35, v233\n"
"M 128,180,216\ns_add_u32 s63, s55, 1835008\nv_exp_f32 v230, v230\nv_exp_f32 v231, v231\nM 144,176,204\nR 174,190,53248\n"
"v_fma_f32 v230, v230, v216, v216\nv_exp_f32 v232, v232\nM 144,180,220\nG 4\nv_exp_f32 v233, v233\nv_fma_f32 v231, v231, v216, v216\n"
"M 148,160,204\nR 176,191,40960\nv_fma_f32 v232, v232, v216, v216\nv_fma_f32 v233, v233, v216, v216\nM 148,164,220\nbuffer_load_dwordx4 v222, s[12:15], s63 offen lds\n"
"v_rcp_f32 v230, v230\nM 132,160,200\nR 178,191,45056\nv_mul_f32 v230, v32, v230\nv_rcp_f32 v231, v231\nM 132,164,216\n"
"v_rcp_f32 v232, v232\nv_rcp_f32 v233, v233\nM 116,160,196\nR 180,191,49152\nv_mul_f32 v231, v33, v231\nv_mul_f32 v232, v34, v232\n"
"M 116,164,212\nv_mul_f32 v233, v35, v233\nv_cvt_pk_bf16_f32 v32, v230, v231\nv_cvt_pk_bf16_f32 v33, v232, v233\nM 100,160,192\nR 182,191,53248\n"
"v_mul_f32 v234, v36, v36\nv_mul_f32 v235, v37, v37\nM 100,164,208\nG 4\nv_mul_f32 v236, v38, v38\nv_mul_f32 v237, v39, v39\n"
"v_fma_f32 v234, v234, v215, v214\nM 104,168,192\nR 160,184,0\nv_fma_f32 v235, v235, v215, v214\nv_fma_f32 v236, v236, v215, v214\nM 104,172,208\n"
"buffer_load_dwordx4 v223, s[12:15], s63 offen offset:1024 lds\nv_fma_f32 v237, v237, v215, v214\nM 120,168,196\nR 162,184,4096\nv_mul_f32 v234, v36, v234\nv_mul_f32 v235, v37, v235\n"
"M 120,172,212\ns_add_u32 m0, s50, 57344\nv_exp_f32 v234, v234\nv_mul_f32 v236, v38, v236\nM 136,168,200\nR 164,184,8192\n"
"v_mul_f32 v237, v39, v237\nv_exp_f32 v235, v235\nM 136,172,216\ns_add_u32 s63, s55, 2293760\nv_exp_f32 v236, v236\nv_exp_f32 v237, v237\n"
"M 152,168,204\nR 166,184,12288\nv_fma_f32 v234, v234, v216, v216\nv_fma_f32 v235, v235, v216, v216\nM 152,172,220\nG 4\n"
"v_rcp_f32 v234, v234\nv_fma_f32 v236, v236, v216, v216\nM 156,176,204\nR 168,185,0\nv_fma_f32 v237, v237, v216, v216\nv_rcp_f32 v235, v235\n"
"M 156,180,220\nbuffer_load_dwordx4 v222, s[12:15], s63 offen lds\nv_rcp_f32 v236, v236\nM 140,176,200\nR 170,185,4096\nv_mul_f32 v234, v36, v234\n"
"v_rcp_f32 v237, v237\nM 140,180,216\nv_mul_f32 v235, v37, v235\nv_mul_f32 v236, v38, v236\nv_mul_f32 v237, v39, v237\nM 124,176,196\n"
"R 172,185,8192\nv_cvt_pk_bf16_f32 v34, v234, v235\nv_cvt_pk_bf16_f32 v35, v236, v237\nM 124,180,212\nbuffer_store_dwordx4 v[32:35], v252, s[20:23], s84 offen offset:0 sc1\nv_mul_f32 v230, v40, v40\n"
"M 108,176,192\nR 174,185,12288\nv_mul_f32 v231, v41, v41\nv_mul_f32 v232, v42, v42\nM 108,180,208\nG 4\n"
"v_mul_f32 v233, v43, v43\nv_fma_f32 v230, v230, v215, v214\nv_fma_f32 v231, v231, v215, v214\nM 96,160,224\nR 176,186,0\nv_fma_f32 v232, v232, v215, v214\n"
"v_fma_f32 v233, v233, v215, v214\nM 96,164,240\nbuffer_load_dwordx4 v223, s[12:15], s63 offen offset:1024 lds\nv_mul_f32 v230, v40, v230\nM 112,160,228\nR 178,186,4096\n"
"v_mul_f32 v231, v41, v231\nv_mul_f32 v232, v42, v232\nM 112,164,244\ns_add_u32 m0, s50, 65536\nv_exp_f32 v230, v230\nv_mul_f32 v233, v43, v233\n"
"M 128,160,232\nR 180,186,8192\nv_fma_f32 v230, v230, v216, v216\nv_exp_f32 v231, v231\nM 128,164,248\ns_add_u32 s63, s55, 2752512\n"
"v_exp_f32 v232, v232\nv_exp_f32 v233, v233\nM 144,160,236\nR 182,186,12288\nv_fma_f32 v231, v231, v216, v216\nv_fma_f32 v232, v232, v216, v216\n"
"M 144,164,252\nG 4\nv_rcp_f32 v230, v230\nv_fma_f32 v233, v233, v216, v216\nM 148,168,236\nR 160,187,0\n"
"v_mul_f32 v230, v40, v230\nv_rcp_f32 v231, v231\nM 148,172,252\nbuffer_load_dwordx4 v222, s[12:15], s63 offen lds\nv_rcp_f32 v232, v232\nM 132,168,232\n"
"R 162,187,4096\nv_mul_f32 v231, v41, v231\nv_rcp_f32 v233, v233\nM 132,172,248\nv_mul_f32 v232, v42, v232\nv_mul_f32 v233, v43, v233\n"
"v_cvt_pk_bf16_f32 v40, v230, v231\nM 116,168,228\nR 164,187,8192\nv_cvt_pk_bf16_f32 v41, v232, v233\nv_mul_f32 v234, v44, v44\nM 116,172,244\n"
"v_mul_f32 v235, v45, v45\nv_mul_f32 v236, v46, v46\nv_mul_f32 v237, v47, v47\nM 100,168,224\nR 166,187,12288\nv_fma_f32 v234, v234, v215, v214\n"
"v_fma_f32 v235, v235, v215, v214\nM 100,172,240\nG 4\nv_fma_f32 v236, v236, v215, v214\nv_fma_f32 v237, v237, v215, v214\nv_mul_f32 v234, v44, v234\n"
"M 104,176,224\nR 168,184,16384\nv_mul_f32 v235, v45, v235\nv_mul_f32 v236, v46, v236\nM 104,180,240\nbuffer_load_dwordx4 v223, s[12:15], s63 offen offset:1024 lds\n"
"v_mul_f32 v237, v47, v237\nM 120,176,228\nR 170,184,20480\nv_mul_f32 v230, v48, v48\nv_exp_f32 v234, v234\nM 120,180,244\n"
"v_exp_f32 v235, v235\nv_exp_f32 v236, v236\nM 136,176,232\nR 172,186,16384\nv_fma_f32 v234, v234, v216, v216\nv_exp_f32 v237, v237\n"
"M 136,180,248\ns_add_u32 m0, s50, 73728\nv_rcp_f32 v234, v234\nv_fma_f32 v235, v235, v216, v216\nM 152,176,236\nR 174,186,20480\n"
"s_add_u32 s63, s55, 3211264\nv_fma_f32 v236, v236, v216, v216\nM 152,180,252\nG 4\nv_rcp_f32 v235, v235\nv_fma_f32 v237, v237, v216, v216\n"
"M 156,160,236\nR 176,185,16384\ns_mov_b32 s60, 157696\nv_bfe_u32 v246, v252, 4, 4\nv_lshl_add_u32 v246, v246, 3, s60\nv_rcp_f32 v236, v236\n"
"M 156,164,252\nds_read_b64 v[238:239], v246 offset:0\nv_mul_f32 v234, v44, v234\nv_rcp_f32 v237, v237\nM 140,160,232\nR 178,185,20480\n"
"ds_read_b64 v[240:241], v246 offset:128\nv_mul_f32 v235, v45, v235\nM 140,164,248\nds_read_b64 v[242:243], v246 offset:256\nbuffer_load_dwordx4 v222, s[12:15], s63 offen lds\nv_mul_f32 v236, v46, v236\n"
"M 124,160,228\nR 180,187,16384\nds_read_b64 v[244:245], v246 offset:384\nv_mul_f32 v237, v47, v237\nM 124,164,244\nds_read_b64 v[246:247], v246 offset:0\n"
"v_cvt_pk_bf16_f32 v42, v234, v235\nv_cvt_pk_bf16_f32 v43, v236, v237\nM 108,160,224\nR 182,187,20480\nv_mul_f32 v231, v49, v49\nv_mul_f32 v232, v50, v50\n"
"M 108,164,240\ns_waitcnt vmcnt(10) lgkmcnt(1)\ns_barrier\nv_mul_f32 v233, v51, v51\nv_mfma_f32_16x16x32_bf16 v[0:3], v[168:171], a[0:3], 0\nR 160,184,24576\n"
"buffer_load_dwordx4 v223, s[12:15], s63 offen offset:1024 lds\nv_fma_f32 v230, v230, v218, v217\nM 0,172,16\ns_mov_b32 m0, s51\nv_fma_f32 v231, v231, v218, v217\nv_fma_f32 v232, v232, v218, v217\n"
"v_fma_f32 v233, v233, v218, v217\nv_mfma_f32_16x16x32_bf16 v[16:19], v[168:171], a[4:7], 0\nR 162,184,28672\nv_mul_f32 v230, v48, v230\nv_mul_f32 v231, v49, v231\nM 16,172,20\n"
"buffer_store_dwordx4 v[40:43], v252, s[20:23], s84 offen offset:1024 sc1\nv_mul_f32 v232, v50, v232\nv_mfma_f32_16x16x32_bf16 v[8:11], v[168:171], a[8:11], 0\nR 164,186,24576\nv_mul_f32 v233, v51, v233\nv_exp_f32 v230, v230\n"
"M 8,172,24\nv_exp_f32 v231, v231\nv_exp_f32 v232, v232\nv_mfma_f32_16x16x32_bf16 v[24:27], v[168:171], a[12:15], 0\nR 166,186,28672\nv_mfma_f32_16x16x32_bf16 v[96:99], v[238:241], v[208:211], v[96:99]\n"
"v_exp_f32 v233, v233\nv_fma_f32 v230, v230, v219, v219\nv_fma_f32 v231, v231, v219, v219\nv_fma_f32 v232, v232, v219, v219\nM 24,172,28\nG 4\n"
"v_rcp_f32 v230, v230\nv_fma_f32 v233, v233, v219, v219\nv_mfma_f32_16x16x32_bf16 v[28:31], v[176:179], a[12:15], 0\nR 168,185,24576\nv_mfma_f32_16x16x32_bf16 v[100:103], v[240:243], v[208:211], v[100:103]\nv_rcp_f32 v231, v231\n"
"v_rcp_f32 v232, v232\nv_rcp_f32 v233, v233\nM 28,180,28\nv_mul_f32 v230, v48, v230\nv_mul_f32 v231, v49, v231\nv_mul_f32 v232, v50, v232\n"
"v_mfma_f32_16x16x32_bf16 v[12:15], v[176:179], a[8:11], 0\nR 170,185,28672\nv_mul_f32 v233, v51, v233\nv_mfma_f32_16x16x32_bf16 v[104:107], v[242:245], v[208:211], v[104:107]\nv_cvt_pk_bf16_f32 v48, v230, v231\nv_cvt_pk_bf16_f32 v49, v232, v233\n"
"v_mul_f32 v234, v52, v52\nv_mul_f32 v235, v53, v53\nM 12,180,24\nv_mul_f32 v236, v54, v54\nv_mul_f32 v237, v55, v55\nv_fma_f32 v234, v234, v218, v217\n"
"v_mfma_f32_16x16x32_bf16 v[20:23], v[176:179], a[4:7], 0\nR 172,187,24576\nv_fma_f32 v235, v235, v218, v217\nv_mfma_f32_16x16x32_bf16 v[108:111], v[244:247], v[208:211], v[108:111]\nv_fma_f32 v236, v236, v218, v217\nv_fma_f32 v237, v237, v218, v217\n"
"v_mul_f32 v234, v52, v234\nv_mul_f32 v235, v53, v235\nM 20,180,20\nds_write_addtid_b32 v128 offset:0\nv_exp_f32 v234, v234\nv_mul_f32 v236, v54, v236\n"
"v_mfma_f32_16x16x32_bf16 v[4:7], v[176:179], a[0:3], 0\nR 174,187,28672\nv_mfma_f32_16x16x32_bf16 v[112:115], v[244:247], v[210:213], v[112:115]\nds_write_addtid_b32 v129 offset:256\nv_exp_f32 v235, v235\nv_exp_f32 v236, v236\n"
"v_mul_f32 v237, v55, v237\nM 4,180,16\nds_write_addtid_b32 v130 offset:512\nG 7\nv_exp_f32 v237, v237\nv_fma_f32 v234, v234, v219, v219\n"
"M 0,160,32\nR 176,184,32768\nv_mfma_f32_16x16x32_bf16 v[116:119], v[238:241], v[210:213], v[116:119]\nds_write_addtid_b32 v131 offset:768\nv_rcp_f32 v234, v234\nv_fma_f32 v235, v235, v219, v219\n"
"v_fma_f32 v236, v236, v219, v219\nv_fma_f32 v237, v237, v219, v219\nM 0,164,48\nds_write_addtid_b32 v132 offset:1024\nv_rcp_f32 v235, v235\nv_rcp_f32 v236, v236\n"
"M 16,160,36\nR 178,184,36864\nv_mfma_f32_16x16x32_bf16 v[120:123], v[240:243], v[210:213], v[120:123]\nds_write_addtid_b32 v133 offset:1280\nv_rcp_f32 v237, v237\nv_mul_f32 v234, v52, v234\n"
"v_mul_f32 v235, v53, v235\nv_mul_f32 v236, v54, v236\nM 16,164,52\nds_write_addtid_b32 v134 offset:1536\nv_mul_f32 v237, v55, v237\nv_cvt_pk_bf16_f32 v50, v234, v235\n"
"v_cvt_pk_bf16_f32 v51, v236, v237\nM 8,160,40\nR 180,186,32768\nv_mul_f32 v230, v56, v56\nv_mfma_f32_16x16x32_bf16 v[124:127], v[242:245], v[210:213], v[124:127]\nbuffer_store_dwordx4 v[48:51], v252, s[20:23], s85 offen offset:0 sc1\n"
"ds_write_addtid_b32 v135 offset:1792\nv_mul_f32 v231, v57, v57\nM 8,164,56\nds_write_addtid_b32 v136 offset:2048\nv_mul_f32 v232, v58, v58\nv_mul_f32 v233, v59, v59\n"
"v_fma_f32 v230, v230, v218, v217\nM 24,160,44\nR 182,186,36864\nds_write_addtid_b32 v137 offset:2304\nv_fma_f32 v231, v231, v218, v217\nv_fma_f32 v232, v232, v218, v217\n"
"M 24,164,60\nds_write_addtid_b32 v138 offset:2560\nG 14\nv_fma_f32 v233, v233, v218, v217\nv_mul_f32 v230, v56, v230\nv_mul_f32 v231, v57, v231\n"
"M 28,168,44\nR 160,185,32768\nds_write_addtid_b32 v139 offset:2816\nv_mul_f32 v232, v58, v232\nv_mul_f32 v233, v59, v233\nM 28,172,60\n"
"ds_write_addtid_b32 v140 offset:3072\nds_write_addtid_b32 v141 offset:3328\nds_write_addtid_b32 v142 offset:3584\nds_write_addtid_b32 v143 offset:3840\nds_write_addtid_b32 v144 offset:4096\nds_write_addtid_b32 v145 offset:4352\n"
"ds_write_addtid_b32 v146 offset:4608\nds_write_addtid_b32 v147 offset:4864\nds_write_addtid_b32 v148 offset:5120\nds_write_addtid_b32 v149 offset:5376\nds_write_addtid_b32 v150 offset:5632\nds_write_addtid_b32 v151 offset:5888\n"
"ds_write_addtid_b32 v152 offset:6144\nds_write_addtid_b32 v153 offset:6400\nds_write_addtid_b32 v154 offset:6656\nds_write_addtid_b32 v155 offset:6912\nds_write_addtid_b32 v156 offset:7168\nds_write_addtid_b32 v157 offset:7424\n"
"ds_write_addtid_b32 v158 offset:7680\nds_write_addtid_b32 v159 offset:7936\nv_exp_f32 v230, v230\nv_exp_f32 v231, v231\nM 12,168,40\nR 162,185,36864\n"
"s_add_u32 m0, s50, 81920\ns_add_u32 s63, s54, 256\nv_exp_f32 v232, v232\nM 12,172,56\nbuffer_load_dwordx4 v220, s[12:15], s63 offen lds\nv_exp_f32 v233, v233\n"
"M 20,168,36\nR 164,187,32768\ns_waitcnt vmcnt(5) lgkmcnt(2)\ns_barrier\nv_fma_f32 v230, v230, v219, v219\nM 20,172,52\n"
"ds_read2st64_b32 v[128:129], v225 offset0:0 offset1:1\nv_rcp_f32 v230, v230\nv_fma_f32 v231, v231, v219, v219\nM 4,168,32\nR 166,187,36864\nds_read2st64_b32 v[130:131], v225 offset0:2 offset1:3\n"
"v_fma_f32 v232, v232, v219, v219\nv_fma_f32 v233, v233, v219, v219\nM 4,172,48\nds_read2st64_b32 v[132:133], v225 offset0:4 offset1:5\nv_rcp_f32 v231, v231\nv_rcp_f32 v232, v232\n"
"M 0,176,64\nR 168,184,40960\nds_read2st64_b32 v[134:135], v225 offset0:6 offset1:7\nv_mul_f32 v230, v56, v230\nv_rcp_f32 v233, v233\nM 0,180,80\n"
"buffer_load_dwordx4 v221, s[12:15], s63 offen offset:1024 lds\nds_read2st64_b32 v[136:137], v225 offset0:8 offset1:9\nv_mul_f32 v231, v57, v231\nM 16,176,68\nR 170,184,45056\ns_add_u32 m0, s50, 90112\n"
"s_add_u32 s63, s54, 229632\nds_read2st64_b32 v[138:139], v225 offset0:10 offset1:11\nv_mul_f32 v232, v58, v232\nM 16,180,84\nbuffer_load_dwordx4 v220, s[12:15], s63 offen lds\nds_read2st64_b32 v[140:141], v225 offset0:12 offset1:13\n"
"v_mul_f32 v233, v59, v233\nM 8,176,72\nR 172,186,40960\nds_read2st64_b32 v[142:143], v225 offset0:14 offset1:15\nv_cvt_pk_bf16_f32 v56, v230, v231\nv_cvt_pk_bf16_f32 v57, v232, v233\n"
"M 8,180,88\nds_read2st64_b32 v[144:145], v225 offset0:16 offset1:17\nv_mul_f32 v234, v60, v60\nv_mul_f32 v235, v61, v61\nv_mul_f32 v236, v62, v62\nM 24,176,76\n"
"R 174,186,45056\nds_read2st64_b32 v[146:147], v225 offset0:18 offset1:19\nv_mul_f32 v237, v63, v63\nv_fma_f32 v234, v234, v218, v217\nM 24,180,92\nds_read2st64_b32 v[148:149], v225 offset0:20 offset1:21\n"
"G 14\nv_fma_f32 v235, v235, v218, v217\nv_fma_f32 v236, v236, v218, v217\nv_fma_f32 v237, v237, v218, v217\nM 28,160,76\nR 176,185,40960\n"
"ds_read2st64_b32 v[150:151], v225 offset0:22 offset1:23\nv_mul_f32 v234, v60, v234\nv_mul_f32 v235, v61, v235\nM 28,164,92\nbuffer_load_dwordx4 v221, s[12:15], s63 offen offset:1024 lds\nds_read2st64_b32 v[152:153], v225 offset0:24 offset1:25\n"
"v_mul_f32 v236, v62, v236\nM 12,160,72\nR 178,185,45056\ns_add_u32 s63, s54, 459008\nds_read2st64_b32 v[154:155], v225 offset0:26 offset1:27\nv_mul_f32 v237, v63, v237\n"
"M 12,164,88\ns_add_u32 m0, s50, 98304\nds_read2st64_b32 v[156:157], v225 offset0:28 offset1:29\nv_exp_f32 v234, v234\nv_exp_f32 v235, v235\nM 20,160,68\n"
"R 180,187,40960\nds_read2st64_b32 v[158:159], v225 offset0:30 offset1:31\nv_add_f32 v96, v96, v128\nv_exp_f32 v236, v236\nM 20,164,84\nbuffer_load_dwordx4 v220, s[12:15], s63 offen lds\n"
"v_add_f32 v97, v97, v129\nv_exp_f32 v237, v237\nM 4,160,64\nR 182,187,45056\nG 15\nv_add_f32 v98, v98, v130\n"
"v_add_f32 v99, v99, v131\nv_fma_f32 v234, v234, v219, v219\nM 4,164,80\nG 11\nv_add_f32 v100, v100, v132\nv_add_f32 v101, v101, v133\n"
"v_add_f32 v102, v102, v134\nv_fma_f32 v235, v235, v219, v219\nM 0,168,96\nR 160,184,49152\nv_add_f32 v103, v103, v135\nv_add_f32 v104, v104, v136\n"
"v_fma_f32 v236, v236, v219, v219\nM 0,172,112\nbuffer_load_dwordx4 v221, s[12:15], s63 offen offset:1024 lds\nv_add_f32 v105, v105, v137\nv_fma_f32 v237, v237, v219, v219\nM 16,168,100\n"
"R 162,184,53248\ns_add_u32 s63, s54, 688384\nv_add_f32 v106, v106, v138\nM 16,172,116\ns_add_u32 m0, s50, 106496\nv_add_f32 v107, v107, v139\n"
"v_add_f32 v108, v108, v140\nv_add_f32 v109, v109, v141\nM 8,168,104\nR 164,186,49152\nv_add_f32 v110, v110, v142\nv_add_f32 v111, v111, v143\n"
"M 8,172,120\nbuffer_load_dwordx4 v220, s[12:15], s63 offen lds\nv_add_f32 v112, v112, v144\nM 24,168,108\nR 166,186,53248\nv_add_f32 v113, v113, v145\n"
"G 14\nv_add_f32 v114, v114, v146\nM 24,172,124\nG 4\nv_add_f32 v115, v115, v147\nv_add_f32 v116, v116, v148\n"
"v_add_f32 v117, v117, v149\nM 28,176,108\nR 168,185,49152\nv_add_f32 v118, v118, v150\nv_add_f32 v119, v119, v151\nM 28,180,124\n"
"v_add_f32 v120, v120, v152\nv_add_f32 v121, v121, v153\nv_add_f32 v122, v122, v154\nM 12,176,104\nR 170,185,53248\nv_add_f32 v123, v123, v155\n"
"v_add_f32 v124, v124, v156\nM 12,180,120\nbuffer_load_dwordx4 v221, s[12:15], s63 offen offset:1024 lds\nv_add_f32 v125, v125, v157\nM 20,176,100\nR 172,187,49152\n"
"s_add_u32 s63, s54, 917760\nM 20,180,116\ns_add_u32 m0, s50, 114688\nv_add_f32 v126, v126, v158\nv_add_f32 v127, v127, v159\nM 4,176,96\n"
"R 174,187,53248\nM 4,180,112\nG 4\nv_rcp_f32 v234, v234\nM 0,160,128\nR 176,184,57344\n"
"v_mul_f32 v234, v60, v234\nM 0,164,144\nbuffer_load_dwordx4 v220, s[12:15], s63 offen lds\nM 16,160,132\nR 178,184,61440\nM 16,164,148\n"
"v_rcp_f32 v235, v235\nM 8,160,136\nR 180,186,57344\nLq1036:\ns_lshr_b32 s60, s45, 3\ns_lshl2_add_u32 s60, s60, s40\n"
"s_lshl_b32 s60, s60, 8\nv_lshl_add_u32 v224, v252, 3, s60\nv_add_u32 v224, Lq1036+4096-LqPC, v224\nv_min_u32 v224, LqE-LqPC-4, v224\ns_mov_b64 exec, 3\nglobal_load_dword v224, v224, s[98:99]\n"
"s_mov_b64 exec, -1\nM 8,164,152\nv_rcp_f32 v236, v236\nM 24,160,140\nR 182,186,61440\nv_mul_f32 v235, v61, v235\n"
"M 24,164,156\nG 4\nv_rcp_f32 v237, v237\nM 28,168,140\nR 160,185,57344\nv_mul_f32 v236, v62, v236\n"
"M 28,172,156\nbuffer_load_dwordx4 v221, s[12:15], s63 offen offset:1024 lds\nM 12,168,136\nR 162,185,61440\ns_add_u32 s63, s54, 1147136\nM 12,172,152\n"
"s_add_u32 m0, s50, 0\nv_mul_f32 v237, v63, v237\nv_cvt_pk_bf16_f32 v58, v234, v235\nM 20,168,132\nR 164,187,57344\nv_cvt_pk_bf16_f32 v59, v236, v237\n"
"M 20,172,148\nbuffer_load_dwordx4 v220, s[12:15], s63 offen lds\nM 4,168,128\nR 166,187,61440\nM 4,172,144\nG 4\n"
"M 0,176,160\nR 168,188,0\nM 0,180,176\nbuffer_load_dwordx4 v221, s[12:15], s63 offen offset:1024 lds\nM 16,176,164\nLpg27:\n"
"s_cbranch_execz Lpg31\nR 170,188,4096\ns_add_u32 s63, s54, 1376512\nM 16,180,180\ns_add_u32 m0, s50, 8192\nM 8,176,168\n"
"R 172,190,0\nM 8,180,184\nbuffer_load_dwordx4 v220, s[12:15], s63 offen lds\nM 24,176,172\nR 174,190,4096\nM 24,180,188\n"
"buffer_store_dwordx4 v[56:59], v252, s[20:23], s85 offen offset:1024 sc1\nG 4\nM 28,160,172\nR 176,189,0\ns_waitcnt vmcnt(11)\ns_barrier\n"
"v_mul_f32 v230, v96, v96\nM 28,164,188\ns_add_u32 s84, s73, 37750784\ns_add_u32 s85, s73, 37865472\nv_mul_f32 v231, v97, v97\nv_mul_f32 v232, v98, v98\n"
"M 12,160,168\nR 178,189,4096\nv_mul_f32 v233, v99, v99\nv_fma_f32 v230, v230, v215, v214\nM 12,164,184\nbuffer_load_dwordx4 v221, s[12:15], s63 offen offset:1024 lds\n"
"v_fma_f32 v231, v231, v215, v214\nM 20,160,164\nR 180,191,0\ns_add_u32 s63, s54, 1605888\nv_fma_f32 v232, v232, v215, v214\nM 20,164,180\n"
"s_add_u32 m0, s50, 16384\nv_fma_f32 v233, v233, v215, v214\nv_mul_f32 v230, v96, v230\nv_mul_f32 v231, v97, v231\nM 4,160,160\nR 182,191,4096\n"
"v_mul_f32 v232, v98, v232\nv_mul_f32 v233, v99, v233\nM 4,164,176\nG 4\nv_exp_f32 v230, v230\nv_exp_f32 v231, v231\n"
"M 0,168,192\nR 160,188,8192\nv_fma_f32 v230, v230, v216, v216\nv_exp_f32 v232, v232\nM 0,172,208\nbuffer_load_dwordx4 v220, s[12:15], s63 offen lds\n"
"v_exp_f32 v233, v233\nM 16,168,196\nR 162,188,12288\nv_fma_f32 v231, v231, v216, v216\nv_fma_f32 v232, v232, v216, v216\nM 16,172,212\n"
"v_rcp_f32 v230, v230\nv_fma_f32 v233, v233, v216, v216\nM 8,168,200\nR 164,190,8192\nv_mul_f32 v230, v96, v230\nv_rcp_f32 v231, v231\n"
"M 8,172,216\nbuffer_load_dwordx4 v221, s[12:15], s63 offen offset:1024 lds\nv_rcp_f32 v232, v232\nM 24,168,204\nR 166,190,12288\ns_add_u32 m0, s50, 24576\n"
"v_rcp_f32 v233, v233\nM 24,172,220\nG 4\nv_mul_f32 v231, v97, v231\nv_mul_f32 v232, v98, v232\nv_mul_f32 v233, v99, v233\n"
"M 28,176,204\nR 168,189,8192\ns_add_u32 s63, s54, 1835264\nv_cvt_pk_bf16_f32 v96, v230, v231\nM 28,180,220\nbuffer_load_dwordx4 v220, s[12:15], s63 offen lds\n"
"v_cvt_pk_bf16_f32 v97, v232, v233\nM 12,176,200\nR 170,189,12288\nv_mul_f32 v234, v100, v100\nv_mul_f32 v235, v101, v101\nM 12,180,216\n"
"v_mul_f32 v236, v102, v102\nv_mul_f32 v237, v103, v103\nv_fma_f32 v234, v234, v215, v214\nM 20,176,196\nR 172,191,8192\nv_fma_f32 v235, v235, v215, v214\n"
"v_fma_f32 v236, v236, v215, v214\nM 20,180,212\nbuffer_load_dwordx4 v221, s[12:15], s63 offen offset:1024 lds\nv_fma_f32 v237, v237, v215, v214\nM 4,176,192\nR 174,191,12288\n"
"s_add_u32 m0, s50, 32768\nv_mul_f32 v234, v100, v234\nM 4,180,208\nG 4\nv_exp_f32 v234, v234\nv_mul_f32 v235, v101, v235\n"
"M 0,160,224\nR 176,188,16384\ns_add_u32 s63, s54, 2064640\nv_mul_f32 v236, v102, v236\nM 0,164,240\nbuffer_load_dwordx4 v220, s[12:15], s63 offen lds\n"
"v_mul_f32 v237, v103, v237\nM 16,160,228\nR 178,188,20480\nv_fma_f32 v234, v234, v216, v216\nv_exp_f32 v235, v235\nM 16,164,244\n"
"v_exp_f32 v236, v236\nv_exp_f32 v237, v237\nM 8,160,232\nR 180,188,24576\nv_fma_f32 v235, v235, v216, v216\nv_fma_f32 v236, v236, v216, v216\n"
"M 8,164,248\nbuffer_load_dwordx4 v221, s[12:15], s63 offen offset:1024 lds\nv_fma_f32 v237, v237, v216, v216\nM 24,160,236\nR 182,188,28672\ns_mov_b32 s60, 158720\n"
"v_bfe_u32 v242, v252, 4, 4\nv_lshl_add_u32 v242, v242, 3, s60\nv_rcp_f32 v234, v234\nM 24,164,252\nds_read_b64 v[238:239], v242 offset:0\nG 5\n"
"v_rcp_f32 v235, v235\nM 28,168,236\nR 160,189,16384\nds_read_b64 v[240:241], v242 offset:128\nv_rcp_f32 v236, v236\nM 28,172,252\n"
"ds_read_b64 v[242:243], v242 offset:0\ns_add_u32 m0, s50, 40960\nv_rcp_f32 v237, v237\nM 12,168,232\nR 162,189,20480\ns_add_u32 s63, s54, 2294016\n"
"v_mul_f32 v234, v100, v234\nM 12,172,248\nbuffer_load_dwordx4 v220, s[12:15], s63 offen lds\nv_mul_f32 v235, v101, v235\nM 20,168,228\nR 164,189,24576\n"
"v_mul_f32 v236, v102, v236\nv_mul_f32 v237, v103, v237\nM 20,172,244\nv_cvt_pk_bf16_f32 v98, v234, v235\nv_cvt_pk_bf16_f32 v99, v236, v237\nv_mul_f32 v230, v104, v104\n"
"M 4,168,224\nR 166,189,28672\nv_mul_f32 v231, v105, v105\nv_mul_f32 v232, v106, v106\nM 4,172,240\ns_waitcnt vmcnt(17) lgkmcnt(3)\n"
"s_barrier\nv_mul_f32 v233, v107, v107\nv_mfma_f32_16x16x32_bf16 v[32:35], v[176:179], a[0:3], 0\nR 168,190,16384\nv_fma_f32 v230, v230, v215, v214\nv_fma_f32 v231, v231, v215, v214\n"
"M 32,180,16\ns_mov_b32 m0, s51\nv_fma_f32 v232, v232, v215, v214\nv_fma_f32 v233, v233, v215, v214\nv_mul_f32 v230, v104, v230\nv_mfma_f32_16x16x32_bf16 v[48:51], v[176:179], a[4:7], 0\n"
"R 170,190,20480\nv_mul_f32 v231, v105, v231\nv_mul_f32 v232, v106, v232\nM 48,180,20\nbuffer_store_dwordx4 v[96:99], v252, s[20:23], s84 offen offset:0 sc1\nv_mul_f32 v233, v107, v233\n"
"v_mfma_f32_16x16x32_bf16 v[64:67], v[176:179], a[8:11], 0\nR 172,190,24576\nv_mov_b32 v226, s46\nv_mfma_f32_16x16x32_bf16 v[0:3], v[238:241], v[208:211], v[0:3]\nv_exp_f32 v230, v230\ns_nop 0\n"
"v_fma_f32 v230, v230, v216, v216\nv_exp_f32 v231, v231\nM 64,180,24\nv_mov_b32 v227, s47\nv_fma_f32 v231, v231, v216, v216\nv_exp_f32 v232, v232\n"
"v_mfma_f32_16x16x32_bf16 v[80:83], v[176:179], a[12:15], 0\nR 174,190,28672\nv_mfma_f32_16x16x32_bf16 v[4:7], v[240:243], v[208:211], v[4:7]\ns_add_u32 s61, s81, 845824\ns_lshl_b32 s62, s44, 5\ns_add_u32 s61, s61, s62\n"
"s_waitcnt vmcnt(9)\ns_mov_b64 exec, 1\nbuffer_atomic_swap_x2 v[226:227], off, s[20:23], s61 sc1\ns_mov_b64 exec, -1\nv_exp_f32 v233, v233\nM 80,180,28\n"
"ds_write_addtid_b32 v8 offset:0\nG 5\nv_rcp_f32 v230, v230\nv_fma_f32 v232, v232, v216, v216\nv_mfma_f32_16x16x32_bf16 v[84:87], v[160:163], a[12:15], 0\nR 176,191,16384\n"
"v_mfma_f32_16x16x32_bf16 v[16:19], v[240:243], v[210:213], v[16:19]\nds_write_addtid_b32 v9 offset:256\nv_rcp_f32 v231, v231\nv_rcp_f32 v232, v232\nv_fma_f32 v233, v233, v216, v216\nM 84,164,28\n"
"ds_write_addtid_b32 v10 offset:512\nv_rcp_f32 v233, v233\nv_mul_f32 v230, v104, v230\nv_mfma_f32_16x16x32_bf16 v[68:71], v[160:163], a[8:11], 0\nR 178,191,20480\nv_mul_f32 v231, v105, v231\n"
"v_mfma_f32_16x16x32_bf16 v[20:23], v[238:241], v[210:213], v[20:23]\nds_write_addtid_b32 v11 offset:768\nv_mul_f32 v232, v106, v232\nv_mul_f32 v233, v107, v233\nv_cvt_pk_bf16_f32 v104, v230, v231\nv_cvt_pk_bf16_f32 v105, v232, v233\n"
"M 68,164,24\nds_write_addtid_b32 v12 offset:1024\nv_mul_f32 v234, v108, v108\nv_mul_f32 v235, v109, v109\nv_mul_f32 v236, v110, v110\nv_mfma_f32_16x16x32_bf16 v[52:55], v[160:163], a[4:7], 0\n"
"R 180,191,24576\nds_write_addtid_b32 v13 offset:1280\nv_mul_f32 v237, v111, v111\nv_fma_f32 v234, v234, v215, v214\nM 52,164,20\nds_write_addtid_b32 v14 offset:1536\n"
"ds_write_addtid_b32 v15 offset:1792\nds_write_addtid_b32 v24 offset:4096\nds_write_addtid_b32 v25 offset:4352\nds_write_addtid_b32 v26 offset:4608\nds_write_addtid_b32 v27 offset:4864\nds_write_addtid_b32 v28 offset:5120\n"
"ds_write_addtid_b32 v29 offset:5376\nds_write_addtid_b32 v30 offset:5632\nds_write_addtid_b32 v31 offset:5888\nv_fma_f32 v235, v235, v215, v214\nv_fma_f32 v236, v236, v215, v214\nv_fma_f32 v237, v237, v215, v214\n"
"v_mfma_f32_16x16x32_bf16 v[36:39], v[160:163], a[0:3], 0\nR 182,191,28672\ns_add_u32 m0, s50, 40960\nv_mul_f32 v234, v108, v234\nM 36,164,16\nG 15\n"
"v_exp_f32 v234, v234\nv_mul_f32 v235, v109, v235\nv_mfma_f32_16x16x32_bf16 v[40:43], v[168:171], a[0:3], 0\nR 160,188,32768\ns_waitcnt vmcnt(9) lgkmcnt(2)\ns_barrier\n"
"v_mul_f32 v236, v110, v236\nM 40,172,16\nbuffer_load_dwordx4 v221, s[12:15], s63 offen offset:1024 lds\nds_read2st64_b32 v[8:9], v225 offset0:0 offset1:1\nv_mul_f32 v237, v111, v237\nv_mfma_f32_16x16x32_bf16 v[56:59], v[168:171], a[4:7], 0\n"
"R 162,188,36864\nds_read2st64_b32 v[10:11], v225 offset0:2 offset1:3\nv_fma_f32 v234, v234, v216, v216\nv_exp_f32 v235, v235\nM 56,172,20\ns_add_u32 m0, s50, 49152\n"
"ds_read2st64_b32 v[12:13], v225 offset0:4 offset1:5\nv_exp_f32 v236, v236\nv_exp_f32 v237, v237\nv_mfma_f32_16x16x32_bf16 v[72:75], v[168:171], a[8:11], 0\nR 164,188,40960\nds_read2st64_b32 v[14:15], v225 offset0:6 offset1:7\n"
"v_fma_f32 v235, v235, v216, v216\nv_fma_f32 v236, v236, v216, v216\nM 72,172,24\ns_add_u32 s63, s54, 2523392\nds_read2st64_b32 v[24:25], v225 offset0:16 offset1:17\nv_rcp_f32 v234, v234\n"
"v_fma_f32 v237, v237, v216, v216\nv_mfma_f32_16x16x32_bf16 v[88:91], v[168:171], a[12:15], 0\nR 166,188,45056\nds_read2st64_b32 v[26:27], v225 offset0:18 offset1:19\nv_mul_f32 v234, v108, v234\nv_rcp_f32 v235, v235\n"
"M 88,172,28\nds_read2st64_b32 v[28:29], v225 offset0:20 offset1:21\nG 11\nv_rcp_f32 v236, v236\nv_rcp_f32 v237, v237\nv_mfma_f32_16x16x32_bf16 v[92:95], v[176:179], a[12:15], 0\n"
"R 168,189,32768\nds_read2st64_b32 v[30:31], v225 offset0:22 offset1:23\nv_mul_f32 v235, v109, v235\nv_mul_f32 v236, v110, v236\nM 92,180,28\nbuffer_load_dwordx4 v220, s[12:15], s63 offen lds\n"
"G 11\nv_add_f32 v0, v0, v8\nv_mul_f32 v237, v111, v237\nv_mfma_f32_16x16x32_bf16 v[76:79], v[176:179], a[8:11], 0\nR 170,189,36864\nv_cvt_pk_bf16_f32 v106, v234, v235\n"
"v_add_f32 v1, v1, v9\nv_cvt_pk_bf16_f32 v107, v236, v237\nM 76,180,24\nv_mul_f32 v230, v112, v112\nv_mul_f32 v231, v113, v113\nG 10\n"
"v_add_f32 v2, v2, v10\nv_mul_f32 v232, v114, v114\nv_mfma_f32_16x16x32_bf16 v[60:63], v[176:179], a[4:7], 0\nR 172,189,40960\nv_mul_f32 v233, v115, v115\nv_add_f32 v3, v3, v11\n"
"v_fma_f32 v230, v230, v218, v217\nM 60,180,20\nbuffer_store_dwordx4 v[104:107], v252, s[20:23], s84 offen offset:1024 sc1\nG 10\nv_add_f32 v4, v4, v12\nv_fma_f32 v231, v231, v218, v217\n"
"v_mfma_f32_16x16x32_bf16 v[44:47], v[176:179], a[0:3], 0\nR 174,189,45056\nv_fma_f32 v232, v232, v218, v217\nv_add_f32 v5, v5, v13\nv_fma_f32 v233, v233, v218, v217\nM 44,180,16\n"
"G 7\nv_mul_f32 v230, v112, v230\nv_mul_f32 v231, v113, v231\nv_add_f32 v6, v6, v14\nv_mul_f32 v232, v114, v232\nM 32,160,32\n"
"R 176,190,32768\nv_mov_b32 v248, 0\nv_add_f32 v7, v7, v15\nv_mul_f32 v233, v115, v233\nM 32,164,48\nv_mov_b32 v249, 0\n"
"v_mul_f32 v234, v116, v116\nv_add_f32 v16, v16, v24\nv_exp_f32 v230, v230\nM 48,160,36\nR 178,190,36864\ns_add_u32 s61, s82, 845824\n"
"s_mov_b64 exec, 0xf\nbuffer_atomic_or_x2 v[248:249], v250, s[20:23], s61 offen sc0 sc1\ns_mov_b64 exec, -1\nv_add_f32 v17, v17, v25\nv_exp_f32 v231, v231\nM 48,164,52\n"
"G 8\nv_add_f32 v18, v18, v26\nv_add_f32 v19, v19, v27\nG 7\nv_add_f32 v20, v20, v28\nv_exp_f32 v232, v232\n"
"M 64,160,40\nR 180,190,40960\nv_add_f32 v21, v21, v29\nG 6\nv_add_f32 v22, v22, v30\nv_add_f32 v23, v23, v31\n"
"M 64,164,56\nbuffer_load_dwordx4 v221, s[12:15], s63 offen offset:1024 lds\nM 80,160,44\nR 182,190,45056\nv_fma_f32 v230, v230, v219, v219\nM 80,164,60\n"
"G 4\nv_exp_f32 v233, v233\nM 84,168,44\nR 160,191,32768\nv_fma_f32 v231, v231, v219, v219\nM 84,172,60\n"
"s_add_u32 m0, s50, 57344\nv_rcp_f32 v230, v230\nM 68,168,40\nR 162,191,36864\nv_fma_f32 v232, v232, v219, v219\nM 68,172,56\n"
"s_add_u32 s63, s54, 2752768\nv_rcp_f32 v231, v231\nM 52,168,36\nR 164,191,40960\nv_fma_f32 v233, v233, v219, v219\nM 52,172,52\n"
"buffer_load_dwordx4 v220, s[12:15], s63 offen lds\nM 36,168,32\nR 166,191,45056\nv_mul_f32 v230, v112, v230\nM 36,172,48\nG 4\n"
"v_rcp_f32 v232, v232\nM 40,176,32\nR 168,188,49152\nv_mul_f32 v231, v113, v231\nM 40,180,48\nv_rcp_f32 v233, v233\n"
"M 56,176,36\nR 170,188,53248\nv_mul_f32 v232, v114, v232\nM 56,180,52\nbuffer_load_dwordx4 v221, s[12:15], s63 offen offset:1024 lds\nM 72,176,40\n"
"R 172,184,0\ns_add_u32 s63, s54, 2982144\nM 72,180,56\ns_add_u32 m0, s50, 65536\nv_mul_f32 v233, v115, v233\nv_cvt_pk_bf16_f32 v112, v230, v231\n"
"M 88,176,44\nR 174,184,4096\nv_cvt_pk_bf16_f32 v113, v232, v233\nM 88,180,60\nG 4\nv_mul_f32 v235, v117, v117\n"
"v_mul_f32 v236, v118, v118\nM 92,160,44\nR 176,189,49152\nv_mul_f32 v237, v119, v119\nM 92,164,60\nbuffer_load_dwordx4 v220, s[12:15], s63 offen lds\n"
"M 76,160,40\nR 178,189,53248\nv_fma_f32 v234, v234, v218, v217\nM 76,164,56\nv_fma_f32 v235, v235, v218, v217\nv_fma_f32 v236, v236, v218, v217\n"
"M 60,160,36\nR 180,185,0\nv_fma_f32 v237, v237, v218, v217\nM 60,164,52\nv_mul_f32 v234, v116, v234\nv_mul_f32 v235, v117, v235\n"
"M 44,160,32\nR 182,185,4096\nv_mul_f32 v236, v118, v236\nM 44,164,48\nG 4\nv_exp_f32 v234, v234\n"
"M 32,168,64\nR 160,190,49152\nv_mul_f32 v237, v119, v237\nM 32,172,80\nbuffer_load_dwordx4 v221, s[12:15], s63 offen offset:1024 lds\nM 48,168,68\n"
"R 162,190,53248\nv_fma_f32 v234, v234, v219, v219\nM 48,172,84\ns_add_u32 m0, s50, 73728\nv_exp_f32 v235, v235\nM 64,168,72\n"
"R 164,186,0\nv_fma_f32 v235, v235, v219, v219\nM 64,172,88\ns_add_u32 s63, s54, 3211520\nv_exp_f32 v236, v236\nM 80,168,76\n"
"R 166,186,4096\nv_fma_f32 v236, v236, v219, v219\nM 80,172,92\nG 4\nv_exp_f32 v237, v237\nM 84,176,76\n"
"R 168,191,49152\nv_fma_f32 v237, v237, v219, v219\nM 84,180,92\nbuffer_load_dwordx4 v220, s[12:15], s63 offen lds\nM 68,176,72\nR 170,191,53248\n"
"v_mul_f32 v230, v120, v120\nM 68,180,88\nv_rcp_f32 v234, v234\nM 52,176,68\nR 172,187,0\nv_mul_f32 v234, v116, v234\n"
"M 52,180,84\ns_add_u32 s64, s68, 37748736\nv_rcp_f32 v235, v235\nM 36,176,64\nR 174,187,4096\nv_mul_f32 v235, v117, v235\n"
"M 36,180,80\nG 4\nv_rcp_f32 v236, v236\nM 40,160,64\nR 176,184,8192\ns_waitcnt vmcnt(13)\n"
"s_barrier\ns_waitcnt vmcnt(6)\nv_cmp_eq_u64 s[86:87], s[46:47], v[248:249]\nM 40,164,80\ns_add_u32 s65, s68, 37863424\nv_rcp_f32 v237, v237\n"
"M 56,160,68\nR 178,184,12288\ns_and_b32 s62, s86, 0xf\ns_cmp_eq_u32 s62, 0xf\ns_cbranch_scc0 Lq838\nLq839:\n"
"M 56,164,84\nbuffer_load_dwordx4 v221, s[12:15], s63 offen offset:1024 lds\nM 72,160,72\nR 180,184,16384\nv_mul_f32 v236, v118, v236\nM 72,164,88\n"
"buffer_load_dwordx4 a[0:3], v252, s[20:23], s64 offen sc1\nM 88,160,76\nR 182,184,20480\nv_mul_f32 v237, v119, v237\nM 88,164,92\nG 4\n"
"v_cvt_pk_bf16_f32 v114, v234, v235\nv_cvt_pk_bf16_f32 v115, v236, v237\nM 92,168,76\nR 160,185,8192\nv_mul_f32 v231, v121, v121\nM 92,172,92\n"
"buffer_load_dwordx4 a[4:7], v252, s[20:23], s65 offen sc1\nM 76,168,72\nR 162,185,12288\nv_mul_f32 v232, v122, v122\nM 76,172,88\ns_add_u32 s66, s69, 37748736\n"
"v_mul_f32 v233, v123, v123\nv_fma_f32 v230, v230, v218, v217\nM 60,168,68\nR 164,185,16384\nv_fma_f32 v231, v231, v218, v217\nM 60,172,84\n"
"s_add_u32 s67, s69, 37863424\nv_fma_f32 v232, v232, v218, v217\nv_fma_f32 v233, v233, v218, v217\nM 44,168,64\nR 166,185,20480\nv_mul_f32 v230, v120, v230\n"
"M 44,172,80\nG 4\nv_exp_f32 v230, v230\nM 32,176,96\nR 168,186,8192\nv_mul_f32 v231, v121, v231\n"
"M 32,180,112\nbuffer_load_dwordx4 a[8:11], v252, s[20:23], s66 offen sc1\nM 48,176,100\nR 170,186,12288\nv_mul_f32 v232, v122, v232\nM 48,180,116\n"
"buffer_load_dwordx4 a[12:15], v252, s[20:23], s67 offen sc1\nM 64,176,104\nR 172,186,16384\nv_mul_f32 v233, v123, v233\nM 64,180,120\ns_add_u32 s64, s68, 37755904\n"
"v_exp_f32 v231, v231\nM 80,176,108\nR 174,186,20480\nv_fma_f32 v230, v230, v219, v219\nM 80,180,124\nG 4\n"
"v_exp_f32 v232, v232\nM 84,160,108\nR 176,187,8192\nv_fma_f32 v231, v231, v219, v219\nM 84,164,124\nbuffer_load_dwordx4 a[16:19], v252, s[20:23], s64 offen sc1\n"
"M 68,160,104\nR 178,187,12288\nv_fma_f32 v232, v232, v219, v219\nM 68,164,120\ns_add_u32 s65, s68, 37870592\nv_exp_f32 v233, v233\n"
"M 52,160,100\nR 180,187,16384\nv_fma_f32 v233, v233, v219, v219\nM 52,164,116\ns_add_u32 s66, s69, 37755904\nv_rcp_f32 v230, v230\n"
"M 36,160,96\nR 182,187,20480\nv_mul_f32 v230, v120, v230\nM 36,164,112\nG 4\nv_rcp_f32 v231, v231\n"
"M 40,168,96\nR 160,184,24576\nv_mul_f32 v231, v121, v231\nM 40,172,112\nbuffer_load_dwordx4 a[20:23], v252, s[20:23], s65 offen sc1\nM 56,168,100\n"
"R 162,184,28672\nv_cvt_pk_bf16_f32 v120, v230, v231\nM 56,172,116\nbuffer_load_dwordx4 a[24:27], v252, s[20:23], s66 offen sc1\nM 72,168,104\nR 164,184,32768\n"
"v_mul_f32 v234, v124, v124\nM 72,172,120\ns_add_u32 s67, s69, 37870592\nLpg28:\ns_cbranch_execz LretO1n\nv_rcp_f32 v232, v232\n"
"M 88,168,108\nR 166,184,36864\nv_mul_f32 v232, v122, v232\nM 88,172,124\nG 4\nv_rcp_f32 v233, v233\n"
"M 92,176,108\nR 168,185,24576\nv_mul_f32 v233, v123, v233\nM 92,180,124\nbuffer_load_dwordx4 a[28:31], v252, s[20:23], s67 offen sc1\nM 76,176,104\n"
"R 170,185,28672\nv_cvt_pk_bf16_f32 v121, v232, v233\nM 76,180,120\ns_add_u32 s64, s68, 37749760\nv_mul_f32 v235, v125, v125\nv_mul_f32 v236, v126, v126\n"
"M 60,176,100\nR 172,185,32768\nv_mul_f32 v237, v127, v127\nM 60,180,116\ns_add_u32 s65, s68, 37864448\nv_fma_f32 v234, v234, v218, v217\n"
"v_fma_f32 v235, v235, v218, v217\nM 44,176,96\nR 174,185,36864\nv_fma_f32 v236, v236, v218, v217\nM 44,180,112\nG 4\n"
"v_fma_f32 v237, v237, v218, v217\nv_mul_f32 v234, v124, v234\nM 32,160,128\nR 176,186,24576\ns_add_u32 m0, s50, 81920\nM 32,164,144\n"
"buffer_load_dwordx4 a[32:35], v252, s[20:23], s64 offen sc1\nM 48,160,132\nR 178,186,28672\ns_add_u32 s63, s54, 3440896\nM 48,164,148\nbuffer_load_dwordx4 a[36:39], v252, s[20:23], s65 offen sc1\n"
"M 64,160,136\nR 180,186,32768\nv_mul_f32 v235, v125, v235\nM 64,164,152\ns_add_u32 s66, s69, 37749760\nbuffer_load_dwordx4 v220, s[12:15], s63 offen lds\n"
"M 80,160,140\nR 182,186,36864\nv_mul_f32 v236, v126, v236\nM 80,164,156\nG 4\nv_exp_f32 v234, v234\n"
"M 84,168,140\nR 160,187,24576\nv_mul_f32 v237, v127, v237\nM 84,172,156\nbuffer_load_dwordx4 a[40:43], v252, s[20:23], s66 offen sc1\nbuffer_load_dwordx4 v221, s[12:15], s63 offen offset:1024 lds\n"
"M 68,168,136\nR 162,187,28672\nv_fma_f32 v234, v234, v219, v219\nM 68,172,152\ns_add_u32 s67, s69, 37864448\nv_exp_f32 v235, v235\n"
"M 52,168,132\nR 164,187,32768\ns_waitcnt vmcnt(15)\ns_barrier\nM 52,172,148\nbuffer_load_dwordx4 a[44:47], v252, s[20:23], s67 offen sc1\n"
"s_movk_i32 s63, 2048\nv_mad_u32_u24 v220, v228, s63, v229\nv_add_u32 v221, 916480, v220\ns_add_u32 m0, s50, 90112\ns_add_u32 s63, s56, 0\nbuffer_load_dwordx4 v220, s[16:19], s63 offen lds\n"
"M 36,168,128\nR 166,187,36864\nv_fma_f32 v235, v235, v219, v219\nM 36,172,144\nG 4\nv_exp_f32 v236, v236\n"
"M 40,176,128\nR 168,184,40960\nv_fma_f32 v236, v236, v219, v219\nM 40,180,144\ns_add_u32 s64, s68, 37756928\nbuffer_load_dwordx4 v221, s[16:19], s63 offen offset:1024 lds\n"
"M 56,176,132\nR 170,184,45056\ns_add_u32 m0, s50, 98304\nM 56,180,148\nbuffer_load_dwordx4 a[48:51], v252, s[20:23], s64 offen sc1\nM 72,176,136\n"
"R 172,184,49152\ns_add_u32 s63, s56, 524288\nM 72,180,152\ns_add_u32 s65, s68, 37871616\nbuffer_load_dwordx4 v220, s[16:19], s63 offen lds\nM 88,176,140\n"
"R 174,184,53248\nM 88,180,156\nG 4\nv_exp_f32 v237, v237\nM 92,160,140\nR 176,185,40960\n"
"v_fma_f32 v237, v237, v219, v219\nM 92,164,156\nbuffer_load_dwordx4 a[52:55], v252, s[20:23], s65 offen sc1\nbuffer_load_dwordx4 v221, s[16:19], s63 offen offset:1024 lds\nM 76,160,136\nR 178,185,45056\n"
"M 76,164,152\ns_add_u32 s66, s69, 37756928\nv_rcp_f32 v234, v234\nM 60,160,132\nR 180,185,49152\nv_mul_f32 v234, v124, v234\n"
"M 60,164,148\nbuffer_load_dwordx4 a[56:59], v252, s[20:23], s66 offen sc1\nM 44,160,128\nR 182,185,53248\nM 44,164,144\nG 4\n"
"v_rcp_f32 v235, v235\nM 32,168,160\nR 160,186,40960\nv_mul_f32 v235, v125, v235\nM 32,172,176\ns_add_u32 s67, s69, 37871616\n"
"v_rcp_f32 v236, v236\nM 48,168,164\nR 162,186,45056\nv_mul_f32 v236, v126, v236\nM 48,172,180\nbuffer_load_dwordx4 a[60:63], v252, s[20:23], s67 offen sc1\n"
"M 64,168,168\nR 164,186,49152\nv_cvt_pk_bf16_f32 v122, v234, v235\nM 64,172,184\nbuffer_store_dwordx4 v[112:115], v252, s[20:23], s85 offen offset:0 sc1\nM 80,168,172\n"
"R 166,186,53248\nv_rcp_f32 v237, v237\nM 80,172,188\nG 4\nv_mul_f32 v237, v127, v237\nv_cvt_pk_bf16_f32 v123, v236, v237\n"
"M 84,176,172\nR 168,187,40960\nM 84,180,188\ns_add_u32 m0, s50, 106496\nbuffer_store_dwordx4 v[120:123], v252, s[20:23], s85 offen offset:1024 sc1\nM 68,176,168\n"
"R 170,187,45056\nv_mul_f32 v230, v0, v0\nv_mul_f32 v231, v1, v1\nM 68,180,184\ns_add_u32 s63, s56, 65536\ns_add_u32 s84, s73, 37754880\n"
"s_add_u32 s85, s73, 37869568\nv_mul_f32 v232, v2, v2\nM 52,176,164\nR 172,187,49152\nv_mul_f32 v233, v3, v3\nv_fma_f32 v230, v230, v215, v214\n"
"M 52,180,180\nbuffer_load_dwordx4 v220, s[16:19], s63 offen lds\nv_fma_f32 v231, v231, v215, v214\nM 36,176,160\nR 174,187,53248\nv_fma_f32 v232, v232, v215, v214\n"
"v_fma_f32 v233, v233, v215, v214\nM 36,180,176\nG 4\nv_mul_f32 v230, v0, v230\nv_mul_f32 v231, v1, v231\nv_mul_f32 v232, v2, v232\n"
"M 40,160,160\nR 176,184,57344\nv_mul_f32 v233, v3, v233\nv_exp_f32 v230, v230\nM 40,164,176\nbuffer_load_dwordx4 v221, s[16:19], s63 offen offset:1024 lds\n"
"v_exp_f32 v231, v231\nM 56,160,164\nR 178,184,61440\nv_fma_f32 v230, v230, v216, v216\nv_exp_f32 v232, v232\nM 56,164,180\n"
"s_add_u32 m0, s50, 114688\nv_exp_f32 v233, v233\nv_fma_f32 v231, v231, v216, v216\nM 72,160,168\nR 180,188,0\nv_fma_f32 v232, v232, v216, v216\n"
"v_fma_f32 v233, v233, v216, v216\nM 72,164,184\ns_add_u32 s63, s56, 589824\nv_rcp_f32 v230, v230\nv_rcp_f32 v231, v231\nM 88,160,172\n"
"R 182,188,4096\nv_mul_f32 v230, v0, v230\nv_rcp_f32 v232, v232\nM 88,164,188\nG 4\nv_rcp_f32 v233, v233\n"
"v_mul_f32 v231, v1, v231\nM 92,168,172\nR 160,185,57344\nv_mul_f32 v232, v2, v232\nv_mul_f32 v233, v3, v233\nM 92,172,188\n"
"buffer_load_dwordx4 v220, s[16:19], s63 offen lds\nv_cvt_pk_bf16_f32 v0, v230, v231\nM 76,168,168\nR 162,185,61440\nv_cvt_pk_bf16_f32 v1, v232, v233\nv_mul_f32 v234, v4, v4\n"
"M 76,172,184\nv_mul_f32 v235, v5, v5\nv_mul_f32 v236, v6, v6\nv_mul_f32 v237, v7, v7\nM 60,168,164\nR 164,189,0\n"
"v_fma_f32 v234, v234, v215, v214\nv_fma_f32 v235, v235, v215, v214\nM 60,172,180\nbuffer_load_dwordx4 v221, s[16:19], s63 offen offset:1024 lds\nv_fma_f32 v236, v236, v215, v214\nM 44,168,160\n"
"R 166,189,4096\ns_add_u32 m0, s50, 0\nv_fma_f32 v237, v237, v215, v214\nM 44,172,176\nG 4\nv_mul_f32 v234, v4, v234\n"
"v_mul_f32 v235, v5, v235\nv_mul_f32 v236, v6, v236\nM 32,176,192\nR 168,186,57344\ns_add_u32 s63, s57, 0\nv_mul_f32 v237, v7, v237\n"
"M 32,180,208\nbuffer_load_dwordx4 v220, s[16:19], s63 offen lds\nv_exp_f32 v234, v234\nM 48,176,196\nR 170,186,61440\nv_fma_f32 v234, v234, v216, v216\n"
"v_exp_f32 v235, v235\nM 48,180,212\nv_exp_f32 v236, v236\nv_exp_f32 v237, v237\nM 64,176,200\nR 172,190,0\n"
"v_fma_f32 v235, v235, v216, v216\nv_fma_f32 v236, v236, v216, v216\nM 64,180,216\nbuffer_load_dwordx4 v221, s[16:19], s63 offen offset:1024 lds\nv_fma_f32 v237, v237, v216, v216\nM 80,176,204\n"
"R 174,190,4096\ns_add_u32 s63, s57, 524288\nv_rcp_f32 v234, v234\nM 80,180,220\nG 4\nv_rcp_f32 v235, v235\n"
"v_rcp_f32 v236, v236\nM 84,160,204\nR 176,187,57344\ns_waitcnt vmcnt(10)\ns_barrier\nv_rcp_f32 v237, v237\n"
"M 84,164,220\ns_add_u32 m0, s50, 8192\nv_mul_f32 v234, v4, v234\nv_mul_f32 v235, v5, v235\nv_mul_f32 v236, v6, v236\nM 68,160,200\n"
"R 178,187,61440\nv_mul_f32 v237, v7, v237\nv_cvt_pk_bf16_f32 v2, v234, v235\nM 68,164,216\nbuffer_load_dwordx4 v220, s[16:19], s63 offen lds\nv_cvt_pk_bf16_f32 v3, v236, v237\n"
"M 52,160,196\nR 180,191,0\nv_mul_f32 v230, v16, v16\nv_mul_f32 v231, v17, v17\nM 52,164,212\nv_mul_f32 v232, v18, v18\n"
"v_mul_f32 v233, v19, v19\nv_fma_f32 v230, v230, v218, v217\nM 36,160,192\nR 182,191,4096\nv_fma_f32 v231, v231, v218, v217\nv_fma_f32 v232, v232, v218, v217\n"
"M 36,164,208\nG 4\nv_fma_f32 v233, v233, v218, v217\nv_mul_f32 v230, v16, v230\nv_mul_f32 v231, v17, v231\nM 40,168,192\n"
"R 160,188,8192\nv_mul_f32 v232, v18, v232\nv_mul_f32 v233, v19, v233\nM 40,172,208\nbuffer_load_dwordx4 v221, s[16:19], s63 offen offset:1024 lds\nv_exp_f32 v230, v230\n"
"M 56,168,196\nR 162,188,12288\nv_fma_f32 v230, v230, v219, v219\nv_exp_f32 v231, v231\nM 56,172,212\nbuffer_store_dwordx4 v[0:3], v252, s[20:23], s84 offen offset:0 sc1\n"
"v_exp_f32 v232, v232\nM 72,168,200\nR 164,188,16384\nv_fma_f32 v231, v231, v219, v219\nv_exp_f32 v233, v233\nM 72,172,216\n"
"s_add_u32 m0, s50, 16384\nv_rcp_f32 v230, v230\nv_fma_f32 v232, v232, v219, v219\nM 88,168,204\nR 166,188,20480\nv_mov_b32 v226, s46\n"
"v_fma_f32 v233, v233, v219, v219\nM 88,172,220\nG 4\nv_mov_b32 v227, s47\nv_mul_f32 v230, v16, v230\nv_rcp_f32 v231, v231\n"
"M 92,176,204\nR 168,189,8192\ns_add_u32 s61, s81, 845832\ns_lshl_b32 s62, s44, 5\ns_add_u32 s61, s61, s62\ns_waitcnt vmcnt(9)\n"
"s_mov_b64 exec, 1\nbuffer_atomic_swap_x2 v[226:227], off, s[20:23], s61 sc1\ns_mov_b64 exec, -1\nv_rcp_f32 v232, v232\nM 92,180,220\ns_add_u32 s63, s57, 65536\n"
"v_rcp_f32 v233, v233\nv_mul_f32 v231, v17, v231\nM 76,176,200\nR 170,189,12288\nv_mul_f32 v232, v18, v232\nv_mul_f32 v233, v19, v233\n"
"M 76,180,216\nbuffer_load_dwordx4 v220, s[16:19], s63 offen lds\nv_cvt_pk_bf16_f32 v16, v230, v231\nM 60,176,196\nR 172,189,16384\nv_cvt_pk_bf16_f32 v17, v232, v233\n"
"v_mul_f32 v234, v20, v20\nM 60,180,212\nv_mul_f32 v235, v21, v21\nv_mul_f32 v236, v22, v22\nv_mul_f32 v237, v23, v23\nM 44,176,192\n"
"R 174,189,20480\nv_fma_f32 v234, v234, v218, v217\nv_fma_f32 v235, v235, v218, v217\nM 44,180,208\nG 4\nv_fma_f32 v236, v236, v218, v217\n"
"v_fma_f32 v237, v237, v218, v217\nv_mul_f32 v234, v20, v234\nM 32,160,224\nR 176,190,8192\nv_mul_f32 v235, v21, v235\nv_mul_f32 v236, v22, v236\n"
"M 32,164,240\nbuffer_load_dwordx4 v221, s[16:19], s63 offen offset:1024 lds\nv_mul_f32 v237, v23, v237\nM 48,160,228\nR 178,190,12288\ns_add_u32 s63, s57, 589824\n"
"v_exp_f32 v234, v234\nM 48,164,244\ns_add_u32 m0, s50, 24576\nv_exp_f32 v235, v235\nv_exp_f32 v236, v236\nM 64,160,232\n"
"R 180,190,16384\nv_fma_f32 v234, v234, v219, v219\nv_exp_f32 v237, v237\nM 64,164,248\nbuffer_load_dwordx4 v220, s[16:19], s63 offen lds\nv_fma_f32 v235, v235, v219, v219\n"
"M 80,160,236\nR 182,190,20480\nv_fma_f32 v236, v236, v219, v219\nv_fma_f32 v237, v237, v219, v219\nM 80,164,252\nG 4\n"
"v_rcp_f32 v234, v234\nv_rcp_f32 v235, v235\nM 84,168,236\nR 160,191,8192\nv_mul_f32 v234, v20, v234\nv_rcp_f32 v236, v236\n"
"M 84,172,252\nbuffer_load_dwordx4 v221, s[16:19], s63 offen offset:1024 lds\nv_rcp_f32 v237, v237\nM 68,168,232\nR 162,191,12288\ns_add_u32 s63, s56, 131072\n"
"M 68,172,248\ns_add_u32 m0, s50, 32768\nv_mul_f32 v235, v21, v235\nv_mul_f32 v236, v22, v236\nM 52,168,228\nR 164,191,16384\n"
"v_mul_f32 v237, v23, v237\nM 52,172,244\nbuffer_load_dwordx4 v220, s[16:19], s63 offen lds\nM 36,168,224\nR 166,191,20480\nv_cvt_pk_bf16_f32 v18, v234, v235\n"
"M 36,172,240\nG 4\nv_cvt_pk_bf16_f32 v19, v236, v237\nM 40,176,224\nR 168,188,24576\nbuffer_store_dwordx4 v[16:19], v252, s[20:23], s85 offen offset:0 sc1\n"
"M 40,180,240\nbuffer_load_dwordx4 v221, s[16:19], s63 offen offset:1024 lds\nM 56,176,228\nR 170,188,28672\nv_mov_b32 v248, 0\nM 56,180,244\n"
"v_mov_b32 v249, 0\nM 72,176,232\nR 172,188,32768\ns_add_u32 s61, s82, 845832\ns_mov_b64 exec, 0xf\nbuffer_atomic_or_x2 v[248:249], v250, s[20:23], s61 offen sc0 sc1\n"
"s_mov_b64 exec, -1\nM 72,180,248\nM 88,176,236\nR 174,188,36864\ns_mov_b32 s60, 158208\nv_bfe_u32 v246, v252, 4, 4\n"
"v_lshl_add_u32 v246, v246, 3, s60\nM 88,180,252\nds_read_b64 v[238:239], v246 offset:0\nG 5\nM 92,160,236\nR 176,189,24576\n"
"ds_read_b64 v[240:241], v246 offset:128\nM 92,164,252\nds_read_b64 v[242:243], v246 offset:256\ns_add_u32 m0, s50, 40960\nM 76,160,232\nR 178,189,28672\n"
"ds_read_b64 v[244:245], v246 offset:384\nM 76,164,248\nds_read_b64 v[246:247], v246 offset:0\ns_add_u32 s63, s56, 655360\nM 60,160,228\nR 180,189,32768\n"
"M 60,164,244\nbuffer_load_dwordx4 v220, s[16:19], s63 offen lds\nM 44,160,224\nR 182,189,36864\nM 44,164,240\ns_waitcnt vmcnt(15) lgkmcnt(2)\n"
"s_barrier\nv_mfma_f32_16x16x32_bf16 v[96:99], v[168:171], a[0:3], 0\nR 160,190,24576\nM 96,172,16\ns_mov_b32 m0, s51\nv_mfma_f32_16x16x32_bf16 v[112:115], v[168:171], a[4:7], 0\n"
"R 162,190,28672\nM 112,172,20\nds_write_addtid_b32 v64 offset:0\nv_mfma_f32_16x16x32_bf16 v[128:131], v[168:171], a[8:11], 0\nR 164,190,32768\nv_mfma_f32_16x16x32_bf16 v[32:35], v[238:241], v[208:211], v[32:35]\n"
"ds_write_addtid_b32 v65 offset:256\nds_write_addtid_b32 v66 offset:512\nM 128,172,24\nds_write_addtid_b32 v67 offset:768\nv_mfma_f32_16x16x32_bf16 v[144:147], v[168:171], a[12:15], 0\nR 166,190,36864\n"
"v_mfma_f32_16x16x32_bf16 v[36:39], v[240:243], v[208:211], v[36:39]\nds_write_addtid_b32 v68 offset:1024\nds_write_addtid_b32 v69 offset:1280\nM 144,172,28\nds_write_addtid_b32 v70 offset:1536\nG 11\n"
"v_mfma_f32_16x16x32_bf16 v[148:151], v[176:179], a[12:15], 0\nR 168,191,24576\nv_mfma_f32_16x16x32_bf16 v[40:43], v[242:245], v[208:211], v[40:43]\nds_write_addtid_b32 v71 offset:1792\nds_write_addtid_b32 v72 offset:2048\nM 148,180,28\n"
"ds_write_addtid_b32 v73 offset:2304\nv_mfma_f32_16x16x32_bf16 v[132:135], v[176:179], a[8:11], 0\nR 170,191,28672\nv_mfma_f32_16x16x32_bf16 v[44:47], v[244:247], v[208:211], v[44:47]\nds_write_addtid_b32 v74 offset:2560\nds_write_addtid_b32 v75 offset:2816\n"
"M 132,180,24\nds_write_addtid_b32 v76 offset:3072\nv_mfma_f32_16x16x32_bf16 v[116:119], v[176:179], a[4:7], 0\nR 172,191,32768\nv_mfma_f32_16x16x32_bf16 v[48:51], v[244:247], v[210:213], v[48:51]\nds_write_addtid_b32 v77 offset:3328\n"
"ds_write_addtid_b32 v78 offset:3584\nM 116,180,20\nds_write_addtid_b32 v79 offset:3840\nv_mfma_f32_16x16x32_bf16 v[100:103], v[176:179], a[0:3], 0\nR 174,191,36864\nv_mfma_f32_16x16x32_bf16 v[52:55], v[238:241], v[210:213], v[52:55]\n"
"ds_write_addtid_b32 v80 offset:4096\nds_write_addtid_b32 v81 offset:4352\nM 100,180,16\nds_write_addtid_b32 v82 offset:4608\nG 15\nv_mfma_f32_16x16x32_bf16 v[104:107], v[160:163], a[0:3], 0\n"
"R 176,188,40960\nv_mfma_f32_16x16x32_bf16 v[56:59], v[240:243], v[210:213], v[56:59]\nds_write_addtid_b32 v83 offset:4864\nds_write_addtid_b32 v84 offset:5120\nM 104,164,16\nds_write_addtid_b32 v85 offset:5376\n"
"v_mfma_f32_16x16x32_bf16 v[120:123], v[160:163], a[4:7], 0\nR 178,188,45056\nv_mfma_f32_16x16x32_bf16 v[60:63], v[242:245], v[210:213], v[60:63]\nds_write_addtid_b32 v86 offset:5632\nds_write_addtid_b32 v87 offset:5888\nM 120,164,20\n"
"ds_write_addtid_b32 v88 offset:6144\nv_mfma_f32_16x16x32_bf16 v[136:139], v[160:163], a[8:11], 0\nR 180,188,49152\nM 136,164,24\nds_write_addtid_b32 v89 offset:6400\nv_mfma_f32_16x16x32_bf16 v[152:155], v[160:163], a[12:15], 0\n"
"R 182,188,53248\nds_write_addtid_b32 v90 offset:6656\nM 152,164,28\nds_write_addtid_b32 v91 offset:6912\nds_write_addtid_b32 v92 offset:7168\nG 15\n"
"v_mfma_f32_16x16x32_bf16 v[156:159], v[168:171], a[12:15], 0\nR 160,189,40960\nds_write_addtid_b32 v93 offset:7424\nM 156,172,28\nds_write_addtid_b32 v94 offset:7680\nds_write_addtid_b32 v95 offset:7936\n"
"v_mfma_f32_16x16x32_bf16 v[140:143], v[168:171], a[8:11], 0\nR 162,189,45056\ns_add_u32 m0, s50, 40960\nM 140,172,24\nbuffer_load_dwordx4 v221, s[16:19], s63 offen offset:1024 lds\nv_mfma_f32_16x16x32_bf16 v[124:127], v[168:171], a[4:7], 0\n"
"R 164,189,49152\ns_waitcnt vmcnt(6) lgkmcnt(2)\ns_barrier\nM 124,172,20\nds_read2st64_b32 v[64:65], v225 offset0:0 offset1:1\nds_read2st64_b32 v[66:67], v225 offset0:2 offset1:3\n"
"v_mfma_f32_16x16x32_bf16 v[108:111], v[168:171], a[0:3], 0\nR 166,189,53248\nds_read2st64_b32 v[68:69], v225 offset0:4 offset1:5\nM 108,172,16\nds_read2st64_b32 v[70:71], v225 offset0:6 offset1:7\nds_read2st64_b32 v[72:73], v225 offset0:8 offset1:9\n"
"M 96,176,32\nR 168,190,40960\nds_read2st64_b32 v[74:75], v225 offset0:10 offset1:11\nM 96,180,48\ns_add_u32 s64, s68, 37750784\nds_read2st64_b32 v[76:77], v225 offset0:12 offset1:13\n"
"v_mov_b32 v226, s46\nM 112,176,36\nR 170,190,45056\nLpg29:\ns_cbranch_execz LretO1n\nds_read2st64_b32 v[78:79], v225 offset0:14 offset1:15\n"
"v_mov_b32 v227, s47\ns_waitcnt vmcnt(2)\nv_cmp_eq_u64 s[86:87], s[46:47], v[248:249]\nM 112,180,52\ns_add_u32 s61, s81, 845848\ns_lshl_b32 s62, s44, 5\n"
"s_add_u32 s61, s61, s62\ns_mov_b64 exec, 1\nbuffer_atomic_swap_x2 v[226:227], off, s[20:23], s61 sc1\ns_mov_b64 exec, -1\nds_read2st64_b32 v[80:81], v225 offset0:16 offset1:17\nM 128,176,40\n"
"R 172,190,49152\ns_and_b32 s62, s86, 0xf\ns_cmp_eq_u32 s62, 0xf\ns_cbranch_scc0 Lq873\nLq874:\nds_read2st64_b32 v[82:83], v225 offset0:18 offset1:19\n"
"M 128,180,56\ns_add_u32 s65, s68, 37865472\nds_read2st64_b32 v[84:85], v225 offset0:20 offset1:21\nds_read2st64_b32 v[86:87], v225 offset0:22 offset1:23\nM 144,176,44\nR 174,190,53248\n"
"s_add_u32 m0, s50, 49152\nds_read2st64_b32 v[88:89], v225 offset0:24 offset1:25\nM 144,180,60\nds_read2st64_b32 v[90:91], v225 offset0:26 offset1:27\nds_read2st64_b32 v[92:93], v225 offset0:28 offset1:29\nG 15\n"
"M 148,160,44\nR 176,191,40960\ns_add_u32 s63, s56, 196608\nds_read2st64_b32 v[94:95], v225 offset0:30 offset1:31\nM 148,164,60\nbuffer_load_dwordx4 a[64:67], v252, s[20:23], s64 offen sc1\n"
"v_add_f32 v32, v32, v64\nM 132,160,40\nR 178,191,45056\nv_add_f32 v33, v33, v65\nv_add_f32 v34, v34, v66\nM 132,164,56\n"
"s_add_u32 s66, s69, 37750784\nv_add_f32 v35, v35, v67\nv_add_f32 v36, v36, v68\nv_add_f32 v37, v37, v69\nM 116,160,36\nR 180,191,49152\n"
"buffer_load_dwordx4 v220, s[16:19], s63 offen lds\nv_add_f32 v38, v38, v70\nM 116,164,52\nbuffer_load_dwordx4 a[68:71], v252, s[20:23], s65 offen sc1\nv_add_f32 v39, v39, v71\nM 100,160,32\n"
"R 182,191,53248\nG 15\nv_add_f32 v40, v40, v72\nv_add_f32 v41, v41, v73\nM 100,164,48\nG 8\n"
"v_add_f32 v42, v42, v74\nv_add_f32 v43, v43, v75\nv_add_f32 v44, v44, v76\nM 104,168,32\nR 160,184,0\nbuffer_load_dwordx4 v221, s[16:19], s63 offen offset:1024 lds\n"
"v_add_f32 v45, v45, v77\nM 104,172,48\ns_add_u32 s67, s69, 37865472\nv_add_f32 v46, v46, v78\nv_add_f32 v47, v47, v79\nv_add_f32 v48, v48, v80\n"
"M 120,168,36\nR 162,184,4096\nv_add_f32 v49, v49, v81\nv_add_f32 v50, v50, v82\nM 120,172,52\nbuffer_load_dwordx4 a[72:75], v252, s[20:23], s66 offen sc1\n"
"M 136,168,40\nR 164,184,8192\nv_add_f32 v51, v51, v83\nM 136,172,56\nv_add_f32 v52, v52, v84\nv_add_f32 v53, v53, v85\n"
"M 152,168,44\nR 166,184,12288\nv_add_f32 v54, v54, v86\nM 152,172,60\nG 4\nv_add_f32 v55, v55, v87\n"
"v_add_f32 v56, v56, v88\nM 156,176,44\nR 168,185,0\nv_add_f32 v57, v57, v89\nM 156,180,60\nbuffer_load_dwordx4 a[76:79], v252, s[20:23], s67 offen sc1\n"
"M 140,176,40\nR 170,185,4096\nv_add_f32 v58, v58, v90\nM 140,180,56\ns_add_u32 s64, s68, 37757952\nv_add_f32 v59, v59, v91\n"
"v_add_f32 v60, v60, v92\nM 124,176,36\nR 172,185,8192\nv_add_f32 v61, v61, v93\nM 124,180,52\ns_add_u32 s65, s68, 37872640\n"
"v_add_f32 v62, v62, v94\nv_add_f32 v63, v63, v95\nM 108,176,32\nR 174,185,12288\nv_mul_f32 v230, v32, v32\nv_mul_f32 v231, v33, v33\n"
"M 108,180,48\ns_add_u32 s84, s73, 37752832\ns_add_u32 s85, s73, 37867520\nG 4\nv_mul_f32 v232, v34, v34\nv_mfma_f32_16x16x32_bf16 v[0:3], v[160:163], a[0:3], 0\n"
"R 176,186,0\ns_lshr_b32 s60, s45, 3\ns_lshl2_add_u32 s60, s60, s40\ns_lshl_b32 s60, s60, 8\nv_lshl_add_u32 v224, v252, 3, s60\nv_add_u32 v224, LqF-32768-LqPC, v224\n"
"v_min_u32 v224, LqE-LqPC-4, v224\ns_mov_b64 exec, 3\nglobal_load_dword v224, v224, s[98:99]\ns_mov_b64 exec, -1\nv_mul_f32 v233, v35, v35\nM 0,164,16\n"
"buffer_load_dwordx4 a[80:83], v252, s[20:23], s64 offen sc1\nv_fma_f32 v230, v230, v215, v214\nv_mfma_f32_16x16x32_bf16 v[16:19], v[160:163], a[4:7], 0\nR 178,186,4096\nv_fma_f32 v231, v231, v215, v214\nv_fma_f32 v232, v232, v215, v214\n"
"M 16,164,20\ns_add_u32 s66, s69, 37757952\nv_fma_f32 v233, v233, v215, v214\nv_mul_f32 v230, v32, v230\nv_mul_f32 v231, v33, v231\nv_mfma_f32_16x16x32_bf16 a[224:227], v[160:163], a[8:11], 0\n"
"R 180,186,8192\nv_mul_f32 v232, v34, v232\nv_mul_f32 v233, v35, v233\nv_mfma_f32_16x16x32_bf16 a[224:227], v[164:167], a[24:27], a[224:227]\nbuffer_load_dwordx4 a[84:87], v252, s[20:23], s65 offen sc1\nv_exp_f32 v230, v230\n"
"v_mfma_f32_16x16x32_bf16 a[240:243], v[160:163], a[12:15], 0\nR 182,186,12288\nv_fma_f32 v230, v230, v216, v216\nv_exp_f32 v231, v231\nv_mfma_f32_16x16x32_bf16 a[240:243], v[164:167], a[28:31], a[240:243]\nG 4\n"
"v_exp_f32 v232, v232\nv_exp_f32 v233, v233\nv_mfma_f32_16x16x32_bf16 a[244:247], v[168:171], a[12:15], 0\nR 160,187,0\ns_add_u32 m0, s50, 57344\nv_fma_f32 v231, v231, v216, v216\n"
"v_mfma_f32_16x16x32_bf16 a[244:247], v[172:175], a[28:31], a[244:247]\ns_add_u32 s67, s69, 37872640\nv_rcp_f32 v230, v230\nv_fma_f32 v232, v232, v216, v216\nv_mfma_f32_16x16x32_bf16 a[228:231], v[168:171], a[8:11], 0\nR 162,187,4096\n"
"s_add_u32 s63, s56, 720896\nv_fma_f32 v233, v233, v216, v216\nv_mfma_f32_16x16x32_bf16 a[228:231], v[172:175], a[24:27], a[228:231]\nbuffer_load_dwordx4 a[88:91], v252, s[20:23], s66 offen sc1\nv_rcp_f32 v231, v231\nv_mfma_f32_16x16x32_bf16 v[20:23], v[168:171], a[4:7], 0\n"
"R 164,187,8192\nv_mul_f32 v230, v32, v230\nv_rcp_f32 v232, v232\nM 20,172,20\nbuffer_load_dwordx4 v220, s[16:19], s63 offen lds\nv_rcp_f32 v233, v233\n"
"v_mfma_f32_16x16x32_bf16 v[4:7], v[168:171], a[0:3], 0\nR 166,187,12288\nv_mul_f32 v231, v33, v231\nv_mul_f32 v232, v34, v232\nM 4,172,16\nG 4\n"
"v_mul_f32 v233, v35, v233\nv_cvt_pk_bf16_f32 v32, v230, v231\nv_cvt_pk_bf16_f32 v33, v232, v233\nv_mfma_f32_16x16x32_bf16 v[8:11], v[176:179], a[0:3], 0\nR 168,184,16384\nv_mul_f32 v234, v36, v36\n"
"v_mul_f32 v235, v37, v37\nM 8,180,16\nbuffer_load_dwordx4 a[92:95], v252, s[20:23], s67 offen sc1\nbuffer_load_dwordx4 v221, s[16:19], s63 offen offset:1024 lds\nv_mul_f32 v236, v38, v38\nv_mfma_f32_16x16x32_bf16 v[24:27], v[176:179], a[4:7], 0\n"
"R 170,184,20480\ns_add_u32 m0, s50, 65536\nv_mul_f32 v237, v39, v39\nM 24,180,20\ns_add_u32 s64, s68, 37751808\nv_fma_f32 v234, v234, v215, v214\n"
"v_fma_f32 v235, v235, v215, v214\nv_fma_f32 v236, v236, v215, v214\nv_mfma_f32_16x16x32_bf16 a[232:235], v[176:179], a[8:11], 0\nR 172,184,24576\ns_add_u32 s63, s57, 131072\nv_fma_f32 v237, v237, v215, v214\n"
"v_mfma_f32_16x16x32_bf16 a[232:235], v[180:183], a[24:27], a[232:235]\ns_add_u32 s65, s68, 37866496\nbuffer_load_dwordx4 v220, s[16:19], s63 offen lds\nv_mul_f32 v234, v36, v234\nv_mfma_f32_16x16x32_bf16 a[248:251], v[176:179], a[12:15], 0\nR 174,184,28672\n"
"v_mul_f32 v235, v37, v235\nv_mul_f32 v236, v38, v236\nv_mfma_f32_16x16x32_bf16 a[248:251], v[180:183], a[28:31], a[248:251]\nG 4\nv_exp_f32 v234, v234\nv_mul_f32 v237, v39, v237\n"
"v_mfma_f32_16x16x32_bf16 a[252:255], v[160:163], a[12:15], 0\nR 176,185,16384\nv_fma_f32 v234, v234, v216, v216\nv_exp_f32 v235, v235\nv_mfma_f32_16x16x32_bf16 a[252:255], v[164:167], a[28:31], a[252:255]\nbuffer_load_dwordx4 a[96:99], v252, s[20:23], s64 offen sc1\n"
"buffer_load_dwordx4 v221, s[16:19], s63 offen offset:1024 lds\nv_exp_f32 v236, v236\nv_mfma_f32_16x16x32_bf16 a[236:239], v[160:163], a[8:11], 0\nR 178,185,20480\ns_add_u32 m0, s50, 73728\nv_exp_f32 v237, v237\n"
"v_mfma_f32_16x16x32_bf16 a[236:239], v[164:167], a[24:27], a[236:239]\ns_add_u32 s66, s69, 37751808\nv_rcp_f32 v234, v234\nv_fma_f32 v235, v235, v216, v216\nv_mfma_f32_16x16x32_bf16 v[28:31], v[160:163], a[4:7], 0\nR 180,185,24576\n"
"s_add_u32 s63, s57, 655360\nv_fma_f32 v236, v236, v216, v216\nM 28,164,20\nbuffer_load_dwordx4 a[100:103], v252, s[20:23], s65 offen sc1\nbuffer_load_dwordx4 v220, s[16:19], s63 offen lds\nv_fma_f32 v237, v237, v216, v216\n"
"v_mfma_f32_16x16x32_bf16 v[12:15], v[160:163], a[0:3], 0\nR 182,185,28672\nv_mul_f32 v234, v36, v234\nv_rcp_f32 v235, v235\nM 12,164,16\ns_waitcnt vmcnt(14)\n"
"s_barrier\nG 4\nv_rcp_f32 v236, v236\nM 0,168,32\nR 160,186,16384\nv_mul_f32 v235, v37, v235\n"
"v_rcp_f32 v237, v237\nM 0,172,48\ns_add_u32 s67, s69, 37866496\nbuffer_load_dwordx4 v221, s[16:19], s63 offen offset:1024 lds\nv_mul_f32 v236, v38, v236\nM 16,168,36\n"
"R 162,186,20480\nv_mul_f32 v237, v39, v237\nv_cvt_pk_bf16_f32 v34, v234, v235\nM 16,172,52\nbuffer_load_dwordx4 a[104:107], v252, s[20:23], s66 offen sc1\nv_cvt_pk_bf16_f32 v35, v236, v237\n"
"v_mfma_f32_16x16x32_bf16 a[224:227], v[168:171], a[40:43], a[224:227]\nR 164,186,24576\nv_mul_f32 v230, v40, v40\nv_mul_f32 v231, v41, v41\nv_mfma_f32_16x16x32_bf16 a[224:227], v[172:175], a[56:59], a[224:227]\nv_mul_f32 v232, v42, v42\n"
"v_mul_f32 v233, v43, v43\nv_fma_f32 v230, v230, v215, v214\nv_mfma_f32_16x16x32_bf16 a[240:243], v[168:171], a[44:47], a[240:243]\nR 166,186,28672\nv_fma_f32 v231, v231, v215, v214\nv_fma_f32 v232, v232, v215, v214\n"
"v_mfma_f32_16x16x32_bf16 a[240:243], v[172:175], a[60:63], a[240:243]\nG 4\nv_fma_f32 v233, v233, v215, v214\nv_mul_f32 v230, v40, v230\nv_mul_f32 v231, v41, v231\nv_mfma_f32_16x16x32_bf16 a[244:247], v[176:179], a[44:47], a[244:247]\n"
"R 168,187,16384\nv_mul_f32 v232, v42, v232\nv_mul_f32 v233, v43, v233\nv_mfma_f32_16x16x32_bf16 a[244:247], v[180:183], a[60:63], a[244:247]\nbuffer_load_dwordx4 a[108:111], v252, s[20:23], s67 offen sc1\nv_exp_f32 v230, v230\n"
"v_mfma_f32_16x16x32_bf16 a[228:231], v[176:179], a[40:43], a[228:231]\nR 170,187,20480\nv_fma_f32 v230, v230, v216, v216\nv_exp_f32 v231, v231\nv_mfma_f32_16x16x32_bf16 a[228:231], v[180:183], a[56:59], a[228:231]\ns_add_u32 s64, s68, 37758976\n"
"v_exp_f32 v232, v232\nv_exp_f32 v233, v233\nM 20,176,36\nR 172,187,24576\nv_fma_f32 v231, v231, v216, v216\nv_fma_f32 v232, v232, v216, v216\n"
"M 20,180,52\ns_add_u32 s65, s68, 37873664\nv_rcp_f32 v230, v230\nv_fma_f32 v233, v233, v216, v216\nM 4,176,32\nR 174,187,28672\n"
"s_add_u32 m0, s50, 81920\nv_rcp_f32 v231, v231\nM 4,180,48\nG 4\nv_rcp_f32 v232, v232\nv_rcp_f32 v233, v233\n"
"M 8,160,32\nR 176,184,32768\ns_add_u32 s63, s57, 196608\nv_mul_f32 v230, v40, v230\nM 8,164,48\nbuffer_load_dwordx4 a[112:115], v252, s[20:23], s64 offen sc1\n"
"v_mul_f32 v231, v41, v231\nM 24,160,36\nR 178,184,36864\nv_mul_f32 v232, v42, v232\nv_mul_f32 v233, v43, v233\nM 24,164,52\n"
"s_add_u32 s66, s69, 37758976\nbuffer_load_dwordx4 v220, s[16:19], s63 offen lds\nv_cvt_pk_bf16_f32 v40, v230, v231\nv_mfma_f32_16x16x32_bf16 a[232:235], v[160:163], a[40:43], a[232:235]\nR 180,184,40960\nv_cvt_pk_bf16_f32 v41, v232, v233\n"
"v_mul_f32 v234, v44, v44\nv_mfma_f32_16x16x32_bf16 a[232:235], v[164:167], a[56:59], a[232:235]\nbuffer_load_dwordx4 a[116:119], v252, s[20:23], s65 offen sc1\nv_mul_f32 v235, v45, v45\nv_mfma_f32_16x16x32_bf16 a[248:251], v[160:163], a[44:47], a[248:251]\nR 182,184,45056\n"
"v_mul_f32 v236, v46, v46\nv_mul_f32 v237, v47, v47\nv_mfma_f32_16x16x32_bf16 a[248:251], v[164:167], a[60:63], a[248:251]\nbuffer_load_dwordx4 v221, s[16:19], s63 offen offset:1024 lds\nG 4\nv_fma_f32 v234, v234, v215, v214\n"
"v_mfma_f32_16x16x32_bf16 a[252:255], v[168:171], a[44:47], a[252:255]\nR 160,185,32768\ns_add_u32 m0, s50, 90112\nv_fma_f32 v235, v235, v215, v214\nv_mfma_f32_16x16x32_bf16 a[252:255], v[172:175], a[60:63], a[252:255]\ns_add_u32 s67, s69, 37873664\n"
"s_add_u32 s63, s57, 720896\nv_fma_f32 v236, v236, v215, v214\nv_fma_f32 v237, v237, v215, v214\nv_mfma_f32_16x16x32_bf16 a[236:239], v[168:171], a[40:43], a[236:239]\nR 162,185,36864\nv_mov_b32 v248, 0\n"
"v_mul_f32 v234, v44, v234\nv_mfma_f32_16x16x32_bf16 a[236:239], v[172:175], a[56:59], a[236:239]\nbuffer_load_dwordx4 a[120:123], v252, s[20:23], s66 offen sc1\nbuffer_load_dwordx4 v220, s[16:19], s63 offen lds\nv_mov_b32 v249, 0\nv_mul_f32 v235, v45, v235\n"
"M 28,168,36\nR 164,185,40960\ns_add_u32 s61, s82, 845848\ns_mov_b64 exec, 0xf\nbuffer_atomic_or_x2 v[248:249], v250, s[20:23], s61 offen sc0 sc1\ns_mov_b64 exec, -1\n"
"v_mul_f32 v236, v46, v236\nM 28,172,52\nbuffer_load_dwordx4 v221, s[16:19], s63 offen offset:1024 lds\nv_mul_f32 v237, v47, v237\nM 12,168,32\nR 166,185,45056\n"
"s_add_u32 m0, s50, 98304\nv_exp_f32 v234, v234\nM 12,172,48\nG 4\nv_exp_f32 v235, v235\nv_exp_f32 v236, v236\n"
"M 96,176,64\nR 168,186,32768\ns_add_u32 s63, s58, 0\nv_exp_f32 v237, v237\ns_waitcnt vmcnt(21)\nM 96,180,80\n"
"buffer_load_dwordx4 a[124:127], v252, s[20:23], s67 offen sc1\nv_fma_f32 v234, v234, v216, v216\nM 112,176,68\nR 170,186,36864\nv_fma_f32 v235, v235, v216, v216\nv_fma_f32 v236, v236, v216, v216\n"
"s_waitcnt vmcnt(21)\nM 112,180,84\nbuffer_load_dwordx4 v220, s[16:19], s63 offen lds\nv_fma_f32 v237, v237, v216, v216\nM 128,176,72\nR 172,186,40960\n"
"v_mul_f32 v230, v48, v48\nv_rcp_f32 v234, v234\ns_waitcnt vmcnt(21)\nM 128,180,88\nbuffer_load_dwordx4 v221, s[16:19], s63 offen offset:1024 lds\nv_rcp_f32 v235, v235\n"
"M 144,176,76\nR 174,186,45056\ns_add_u32 m0, s50, 106496\nv_rcp_f32 v236, v236\ns_waitcnt vmcnt(20)\nM 144,180,92\n"
"G 4\nv_rcp_f32 v237, v237\nv_mul_f32 v234, v44, v234\nM 148,160,76\nR 176,187,32768\ns_add_u32 s63, s58, 524288\n"
"v_mul_f32 v235, v45, v235\nM 148,164,92\nbuffer_load_dwordx4 v220, s[16:19], s63 offen lds\nv_mul_f32 v236, v46, v236\nM 132,160,72\nR 178,187,36864\n"
"v_mul_f32 v237, v47, v237\nv_cvt_pk_bf16_f32 v42, v234, v235\nM 132,164,88\nbuffer_store_dwordx4 v[32:35], v252, s[20:23], s84 offen offset:0 sc1\nv_cvt_pk_bf16_f32 v43, v236, v237\nM 116,160,68\n"
"R 180,187,40960\nv_mul_f32 v231, v49, v49\nv_mul_f32 v232, v50, v50\nM 116,164,84\nbuffer_load_dwordx4 v221, s[16:19], s63 offen offset:1024 lds\nv_mul_f32 v233, v51, v51\n"
"M 100,160,64\nR 182,187,45056\nv_fma_f32 v230, v230, v218, v217\nv_fma_f32 v231, v231, v218, v217\nM 100,164,80\nG 4\n"
"v_fma_f32 v232, v232, v218, v217\nv_fma_f32 v233, v233, v218, v217\nv_mul_f32 v230, v48, v230\nM 104,168,64\nR 160,184,49152\ns_waitcnt vmcnt(16)\n"
"s_barrier\nv_mul_f32 v231, v49, v231\nM 104,172,80\nbuffer_store_dwordx4 v[40:43], v252, s[20:23], s84 offen offset:1024 sc1\nv_mul_f32 v232, v50, v232\nM 120,168,68\n"
"R 162,184,53248\nv_mul_f32 v233, v51, v233\nv_exp_f32 v230, v230\nM 120,172,84\ns_add_u32 m0, s50, 114688\nv_exp_f32 v231, v231\n"
"v_exp_f32 v232, v232\nM 136,168,72\nR 164,184,57344\nv_fma_f32 v230, v230, v219, v219\nv_exp_f32 v233, v233\nM 136,172,88\n"
"s_add_u32 s63, s58, 65536\nv_rcp_f32 v230, v230\nv_fma_f32 v231, v231, v219, v219\nM 152,168,76\nR 166,184,61440\nv_fma_f32 v232, v232, v219, v219\n"
"v_fma_f32 v233, v233, v219, v219\nM 152,172,92\nG 4\nv_rcp_f32 v231, v231\nv_rcp_f32 v232, v232\nM 156,176,76\n"
"R 168,185,49152\nv_mul_f32 v230, v48, v230\nv_rcp_f32 v233, v233\nM 156,180,92\nbuffer_load_dwordx4 v220, s[16:19], s63 offen lds\nv_mul_f32 v231, v49, v231\n"
"M 140,176,72\nR 170,185,53248\nv_mul_f32 v232, v50, v232\nv_mul_f32 v233, v51, v233\nM 140,180,88\nv_cvt_pk_bf16_f32 v48, v230, v231\n"
"v_cvt_pk_bf16_f32 v49, v232, v233\nv_mul_f32 v234, v52, v52\nM 124,176,68\nR 172,185,57344\nv_mul_f32 v235, v53, v53\nv_mul_f32 v236, v54, v54\n"
"M 124,180,84\nbuffer_load_dwordx4 v221, s[16:19], s63 offen offset:1024 lds\nv_mul_f32 v237, v55, v55\nM 108,176,64\nR 174,185,61440\ns_add_u32 m0, s50, 0\n"
"v_fma_f32 v234, v234, v218, v217\nM 108,180,80\nG 4\nv_fma_f32 v235, v235, v218, v217\nv_fma_f32 v236, v236, v218, v217\nv_fma_f32 v237, v237, v218, v217\n"
"M 96,160,96\nR 176,186,49152\ns_add_u32 s63, s58, 589824\nv_mul_f32 v234, v52, v234\ns_waitcnt vmcnt(16)\nM 96,164,112\n"
"buffer_load_dwordx4 v220, s[16:19], s63 offen lds\nv_mul_f32 v235, v53, v235\nM 112,160,100\nR 178,186,53248\nv_mul_f32 v236, v54, v236\nv_mul_f32 v237, v55, v237\n"
"s_waitcnt vmcnt(15)\nM 112,164,116\nv_exp_f32 v234, v234\nv_exp_f32 v235, v235\nM 128,160,104\nR 180,186,57344\n"
"v_fma_f32 v234, v234, v219, v219\nv_exp_f32 v236, v236\ns_waitcnt vmcnt(13)\nM 128,164,120\nv_exp_f32 v237, v237\nv_fma_f32 v235, v235, v219, v219\n"
"M 144,160,108\nR 182,186,61440\nv_fma_f32 v236, v236, v219, v219\nv_fma_f32 v237, v237, v219, v219\ns_waitcnt vmcnt(9)\nM 144,164,124\n"
"G 4\nv_rcp_f32 v234, v234\nv_rcp_f32 v235, v235\nM 148,168,108\nR 160,187,49152\nv_mul_f32 v234, v52, v234\n"
"v_rcp_f32 v236, v236\nM 148,172,124\ns_add_u32 s64, s68, 37754880\nv_rcp_f32 v237, v237\nv_mul_f32 v235, v53, v235\nM 132,168,104\n"
"R 162,187,53248\nv_cmp_eq_u64 s[86:87], s[46:47], v[248:249]\nv_mul_f32 v236, v54, v236\nM 132,172,120\nbuffer_load_dwordx4 v221, s[16:19], s63 offen offset:1024 lds\nv_mul_f32 v237, v55, v237\n"
"M 116,168,100\nR 164,187,57344\ns_and_b32 s62, s86, 0xf\ns_cmp_eq_u32 s62, 0xf\ns_cbranch_scc0 Lq908\nLq909:\n"
"v_cvt_pk_bf16_f32 v50, v234, v235\nM 116,172,116\ns_add_u32 s65, s68, 37869568\nv_cvt_pk_bf16_f32 v51, v236, v237\nv_mul_f32 v230, v56, v56\nv_mul_f32 v231, v57, v57\n"
"M 100,168,96\nR 166,187,61440\nv_mul_f32 v232, v58, v58\nv_mul_f32 v233, v59, v59\nM 100,172,112\nG 4\n"
"v_fma_f32 v230, v230, v218, v217\nv_fma_f32 v231, v231, v218, v217\nM 104,176,96\nR 168,188,0\nv_fma_f32 v232, v232, v218, v217\nM 104,180,112\n"
"buffer_load_dwordx4 a[128:131], v252, s[20:23], s64 offen sc1\nM 120,176,100\nR 170,188,4096\nv_fma_f32 v233, v233, v218, v217\nM 120,180,116\ns_add_u32 s66, s69, 37754880\n"
"v_mul_f32 v230, v56, v230\nv_mul_f32 v231, v57, v231\nM 136,176,104\nR 172,188,8192\nv_mul_f32 v232, v58, v232\nM 136,180,120\n"
"buffer_load_dwordx4 a[132:135], v252, s[20:23], s65 offen sc1\nM 152,176,108\nR 174,188,12288\nv_mul_f32 v233, v59, v233\nM 152,180,124\nG 4\n"
"v_exp_f32 v230, v230\nM 156,160,108\nR 176,189,0\nv_fma_f32 v230, v230, v219, v219\nM 156,164,124\ns_add_u32 s67, s69, 37869568\n"
"v_exp_f32 v231, v231\nM 140,160,104\nR 178,189,4096\nv_fma_f32 v231, v231, v219, v219\nM 140,164,120\nbuffer_load_dwordx4 a[136:139], v252, s[20:23], s66 offen sc1\n"
"M 124,160,100\nR 180,189,8192\nv_mul_f32 v234, v60, v60\nM 124,164,116\nbuffer_store_dwordx4 v[48:51], v252, s[20:23], s85 offen offset:0 sc1\nM 108,160,96\n"
"R 182,189,12288\ns_add_u32 m0, s50, 8192\nM 108,164,112\nLpg30:\ns_cbranch_execz LretO1n\ns_waitcnt vmcnt(9)\n"
"s_barrier\nG 4\nM 0,168,64\nR 160,190,0\ns_add_u32 s63, s58, 131072\nM 0,172,80\n"
"buffer_load_dwordx4 a[140:143], v252, s[20:23], s67 offen sc1\nM 16,168,68\nR 162,190,4096\nv_mul_f32 v235, v61, v61\nM 16,172,84\ns_add_u32 s64, s68, 37762048\n"
"v_exp_f32 v232, v232\nv_mfma_f32_16x16x32_bf16 a[224:227], v[168:171], a[72:75], a[224:227]\nR 164,190,8192\nbuffer_load_dwordx4 v220, s[16:19], s63 offen lds\nv_mfma_f32_16x16x32_bf16 a[224:227], v[172:175], a[88:91], a[224:227]\ns_add_u32 s65, s68, 37876736\n"
"v_exp_f32 v233, v233\nv_mfma_f32_16x16x32_bf16 a[240:243], v[168:171], a[76:79], a[240:243]\nR 166,190,12288\nv_fma_f32 v232, v232, v219, v219\nv_mfma_f32_16x16x32_bf16 a[240:243], v[172:175], a[92:95], a[240:243]\nG 4\n"
"v_rcp_f32 v230, v230\nv_mfma_f32_16x16x32_bf16 a[244:247], v[176:179], a[76:79], a[244:247]\nR 168,191,0\nbuffer_load_dwordx4 v221, s[16:19], s63 offen offset:1024 lds\nv_mfma_f32_16x16x32_bf16 a[244:247], v[180:183], a[92:95], a[244:247]\nbuffer_load_dwordx4 a[144:147], v252, s[20:23], s64 offen sc1\n"
"v_mfma_f32_16x16x32_bf16 a[228:231], v[176:179], a[72:75], a[228:231]\nR 170,191,4096\ns_add_u32 m0, s50, 16384\nv_mfma_f32_16x16x32_bf16 a[228:231], v[180:183], a[88:91], a[228:231]\ns_add_u32 s66, s69, 37762048\ns_add_u32 s63, s58, 655360\n"
"v_fma_f32 v233, v233, v219, v219\nM 20,176,68\nR 172,191,8192\nbuffer_load_dwordx4 v220, s[16:19], s63 offen lds\nM 20,180,84\nbuffer_load_dwordx4 a[148:151], v252, s[20:23], s65 offen sc1\n"
"M 4,176,64\nR 174,191,12288\nv_mul_f32 v230, v56, v230\nM 4,180,80\nG 4\nv_rcp_f32 v231, v231\n"
"M 8,160,64\nR 176,188,16384\nbuffer_load_dwordx4 v221, s[16:19], s63 offen offset:1024 lds\nM 8,164,80\ns_add_u32 s67, s69, 37876736\nv_rcp_f32 v232, v232\n"
"M 24,160,68\nR 178,188,20480\nv_mul_f32 v231, v57, v231\nM 24,164,84\nbuffer_load_dwordx4 a[152:155], v252, s[20:23], s66 offen sc1\nv_mfma_f32_16x16x32_bf16 a[232:235], v[160:163], a[72:75], a[232:235]\n"
"R 180,188,24576\nv_mul_f32 v232, v58, v232\nv_mfma_f32_16x16x32_bf16 a[232:235], v[164:167], a[88:91], a[232:235]\nv_rcp_f32 v233, v233\nv_mfma_f32_16x16x32_bf16 a[248:251], v[160:163], a[76:79], a[248:251]\nR 182,188,28672\n"
"v_mul_f32 v233, v59, v233\nv_mfma_f32_16x16x32_bf16 a[248:251], v[164:167], a[92:95], a[248:251]\nG 4\nv_cvt_pk_bf16_f32 v56, v230, v231\nv_cvt_pk_bf16_f32 v57, v232, v233\nv_mfma_f32_16x16x32_bf16 a[252:255], v[168:171], a[76:79], a[252:255]\n"
"R 160,189,16384\nv_mul_f32 v236, v62, v62\nv_mfma_f32_16x16x32_bf16 a[252:255], v[172:175], a[92:95], a[252:255]\nbuffer_load_dwordx4 a[156:159], v252, s[20:23], s67 offen sc1\nv_mfma_f32_16x16x32_bf16 a[236:239], v[168:171], a[72:75], a[236:239]\nR 162,189,20480\n"
"v_mul_f32 v237, v63, v63\nv_mfma_f32_16x16x32_bf16 a[236:239], v[172:175], a[88:91], a[236:239]\nv_fma_f32 v234, v234, v218, v217\nv_fma_f32 v235, v235, v218, v217\nM 28,168,68\nR 164,189,24576\n"
"v_fma_f32 v236, v236, v218, v217\nM 28,172,84\ns_add_u32 m0, s50, 24576\nv_fma_f32 v237, v237, v218, v217\nv_mul_f32 v234, v60, v234\nM 12,168,64\n"
"R 166,189,28672\nv_mul_f32 v235, v61, v235\nM 12,172,80\nG 4\nv_exp_f32 v234, v234\nM 0,176,96\n"
"R 168,190,16384\nv_mul_f32 v236, v62, v236\nM 0,180,112\ns_add_u32 s63, s58, 196608\nv_exp_f32 v235, v235\nM 16,176,100\n"
"R 170,190,20480\nv_mul_f32 v237, v63, v237\nM 16,180,116\nbuffer_load_dwordx4 v220, s[16:19], s63 offen lds\nv_mfma_f32_16x16x32_bf16 a[224:227], v[176:179], a[104:107], a[224:227]\nR 172,190,24576\n"
"v_fma_f32 v234, v234, v219, v219\nv_mfma_f32_16x16x32_bf16 a[224:227], v[180:183], a[120:123], a[224:227]\nv_exp_f32 v236, v236\nv_mfma_f32_16x16x32_bf16 a[240:243], v[176:179], a[108:111], a[240:243]\nR 174,190,28672\nv_fma_f32 v235, v235, v219, v219\n"
"v_mfma_f32_16x16x32_bf16 a[240:243], v[180:183], a[124:127], a[240:243]\nG 4\nv_exp_f32 v237, v237\nv_mfma_f32_16x16x32_bf16 a[244:247], v[160:163], a[108:111], a[244:247]\nR 176,191,16384\nv_fma_f32 v236, v236, v219, v219\n"
"v_mfma_f32_16x16x32_bf16 a[244:247], v[164:167], a[124:127], a[244:247]\nbuffer_load_dwordx4 v221, s[16:19], s63 offen offset:1024 lds\nv_mfma_f32_16x16x32_bf16 a[228:231], v[160:163], a[104:107], a[228:231]\nR 178,191,20480\ns_add_u32 s63, s58, 720896\nv_mfma_f32_16x16x32_bf16 a[228:231], v[164:167], a[120:123], a[228:231]\n"
"s_add_u32 m0, s50, 32768\nv_rcp_f32 v234, v234\nM 20,160,100\nR 180,191,24576\nv_fma_f32 v237, v237, v219, v219\nM 20,164,116\n"
"buffer_load_dwordx4 v220, s[16:19], s63 offen lds\nM 4,160,96\nR 182,191,28672\nv_mul_f32 v234, v60, v234\nM 4,164,112\nG 4\n"
"v_rcp_f32 v235, v235\nM 8,168,96\nR 160,188,32768\nv_mul_f32 v235, v61, v235\nM 8,172,112\nbuffer_load_dwordx4 v221, s[16:19], s63 offen offset:1024 lds\n"
"M 24,168,100\nR 162,188,36864\ns_add_u32 s63, s58, 393216\nM 24,172,116\ns_add_u32 m0, s50, 40960\nv_rcp_f32 v236, v236\n"
"v_mfma_f32_16x16x32_bf16 a[232:235], v[168:171], a[104:107], a[232:235]\nR 164,188,40960\nv_mul_f32 v236, v62, v236\nv_mfma_f32_16x16x32_bf16 a[232:235], v[172:175], a[120:123], a[232:235]\nbuffer_load_dwordx4 v220, s[16:19], s63 offen lds\nv_mfma_f32_16x16x32_bf16 a[248:251], v[168:171], a[108:111], a[248:251]\n"
"R 166,188,45056\nv_cvt_pk_bf16_f32 v58, v234, v235\nv_mfma_f32_16x16x32_bf16 a[248:251], v[172:175], a[124:127], a[248:251]\nG 4\nv_rcp_f32 v237, v237\nv_mfma_f32_16x16x32_bf16 a[252:255], v[176:179], a[108:111], a[252:255]\n"
"R 168,189,32768\nv_mul_f32 v237, v63, v237\nv_mfma_f32_16x16x32_bf16 a[252:255], v[180:183], a[124:127], a[252:255]\nbuffer_load_dwordx4 v221, s[16:19], s63 offen offset:1024 lds\nv_mfma_f32_16x16x32_bf16 a[236:239], v[176:179], a[104:107], a[236:239]\nR 170,189,36864\n"
"s_add_u32 s63, s58, 458752\nv_mfma_f32_16x16x32_bf16 a[236:239], v[180:183], a[120:123], a[236:239]\ns_add_u32 m0, s50, 49152\nv_cvt_pk_bf16_f32 v59, v236, v237\nbuffer_store_dwordx4 v[56:59], v252, s[20:23], s85 offen offset:1024 sc1\nM 28,176,100\n"
"R 172,189,40960\nM 28,180,116\nbuffer_load_dwordx4 v220, s[16:19], s63 offen lds\nM 12,176,96\nR 174,189,45056\nM 12,180,112\n"
"s_waitcnt vmcnt(10)\ns_barrier\nG 4\nv_mfma_f32_16x16x32_bf16 v[32:35], v[160:163], a[0:3], 0\nR 176,190,32768\nM 32,164,16\n"
"v_mfma_f32_16x16x32_bf16 v[48:51], v[160:163], a[4:7], 0\nR 178,190,36864\nM 48,164,20\nbuffer_load_dwordx4 v221, s[16:19], s63 offen offset:1024 lds\nv_mfma_f32_16x16x32_bf16 v[64:67], v[160:163], a[8:11], 0\nR 180,190,40960\n"
"s_add_u32 s63, s56, 393216\nM 64,164,24\ns_add_u32 m0, s50, 57344\nv_mfma_f32_16x16x32_bf16 v[80:83], v[160:163], a[12:15], 0\nR 182,190,45056\nM 80,164,28\n"
"G 4\nv_mfma_f32_16x16x32_bf16 v[84:87], v[168:171], a[12:15], 0\nR 160,191,32768\nM 84,172,28\nbuffer_load_dwordx4 v220, s[16:19], s63 offen lds\nv_mfma_f32_16x16x32_bf16 v[68:71], v[168:171], a[8:11], 0\n"
"R 162,191,36864\nM 68,172,24\nv_mfma_f32_16x16x32_bf16 v[52:55], v[168:171], a[4:7], 0\nR 164,191,40960\nM 52,172,20\nv_mfma_f32_16x16x32_bf16 v[36:39], v[168:171], a[0:3], 0\n"
"R 166,191,45056\nM 36,172,16\nG 4\nv_mfma_f32_16x16x32_bf16 v[40:43], v[176:179], a[0:3], 0\nR 168,188,49152\nM 40,180,16\n"
"buffer_load_dwordx4 v221, s[16:19], s63 offen offset:1024 lds\nv_mfma_f32_16x16x32_bf16 v[56:59], v[176:179], a[4:7], 0\nR 170,188,53248\nM 56,180,20\ns_add_u32 m0, s50, 65536\nv_mfma_f32_16x16x32_bf16 v[72:75], v[176:179], a[8:11], 0\n"
"R 172,184,0\nM 72,180,24\ns_add_u32 s63, s56, 458752\nv_mfma_f32_16x16x32_bf16 v[88:91], v[176:179], a[12:15], 0\nR 174,184,4096\nM 88,180,28\n"
"G 4\nv_mfma_f32_16x16x32_bf16 v[92:95], v[160:163], a[12:15], 0\nR 176,189,49152\nM 92,164,28\nbuffer_load_dwordx4 v220, s[16:19], s63 offen lds\nv_mfma_f32_16x16x32_bf16 v[76:79], v[160:163], a[8:11], 0\n"
"R 178,189,53248\nM 76,164,24\nv_mfma_f32_16x16x32_bf16 v[60:63], v[160:163], a[4:7], 0\nR 180,185,0\nM 60,164,20\nv_mfma_f32_16x16x32_bf16 v[44:47], v[160:163], a[0:3], 0\n"
"R 182,185,4096\nM 44,164,16\nG 4\nM 32,168,32\nR 160,190,49152\nM 32,172,48\n"
"buffer_load_dwordx4 v221, s[16:19], s63 offen offset:1024 lds\nM 48,168,36\nR 162,190,53248\nM 48,172,52\ns_add_u32 m0, s50, 73728\nM 64,168,40\n"
"R 164,186,0\nM 64,172,56\ns_add_u32 s63, s57, 393216\nM 80,168,44\nR 166,186,4096\nM 80,172,60\n"
"G 4\nM 84,176,44\nR 168,191,49152\nM 84,180,60\nbuffer_load_dwordx4 v220, s[16:19], s63 offen lds\nM 68,176,40\n"
"R 170,191,53248\nv_mov_b32 v226, s46\nM 68,180,56\nv_mov_b32 v227, s47\nM 52,176,36\nR 172,187,0\n"
"s_add_u32 s61, s81, 845840\ns_lshl_b32 s62, s44, 5\ns_add_u32 s61, s61, s62\ns_waitcnt vmcnt(7)\ns_mov_b64 exec, 1\nbuffer_atomic_swap_x2 v[226:227], off, s[20:23], s61 sc1\n"
"s_mov_b64 exec, -1\nM 52,180,52\nM 36,176,32\nR 174,187,4096\nM 36,180,48\nG 4\n"
"M 40,160,32\nR 176,184,8192\nM 40,164,48\nbuffer_load_dwordx4 v221, s[16:19], s63 offen offset:1024 lds\nM 56,160,36\nR 178,184,12288\n"
"s_add_u32 s63, s57, 458752\nM 56,164,52\ns_add_u32 m0, s50, 81920\nM 72,160,40\nR 180,184,16384\nM 72,164,56\n"
"buffer_load_dwordx4 v220, s[16:19], s63 offen lds\nM 88,160,44\nR 182,184,20480\nM 88,164,60\nG 4\nM 92,168,44\n"
"R 160,185,8192\nM 92,172,60\nbuffer_load_dwordx4 v221, s[16:19], s63 offen offset:1024 lds\nM 76,168,40\nR 162,185,12288\ns_add_u32 s63, s56, 262144\n"
"M 76,172,56\ns_add_u32 m0, s50, 90112\nM 60,168,36\nR 164,185,16384\nM 60,172,52\nbuffer_load_dwordx4 v220, s[16:19], s63 offen lds\n"
"M 44,168,32\nR 166,185,20480\nM 44,172,48\ns_waitcnt vmcnt(10)\ns_barrier\nG 4\n"
"M 32,176,64\nR 168,186,8192\nM 32,180,80\nM 48,176,68\nR 170,186,12288\nM 48,180,84\n"
"buffer_load_dwordx4 v221, s[16:19], s63 offen offset:1024 lds\nM 64,176,72\nR 172,186,16384\ns_add_u32 s63, s56, 786432\nM 64,180,88\ns_add_u32 m0, s50, 98304\n"
"M 80,176,76\nR 174,186,20480\nM 80,180,92\nG 4\nM 84,160,76\nR 176,187,8192\n"
"M 84,164,92\nbuffer_load_dwordx4 v220, s[16:19], s63 offen lds\nM 68,160,72\nR 178,187,12288\nM 68,164,88\nM 52,160,68\n"
"R 180,187,16384\nv_mov_b32 v248, 0\nM 52,164,84\nv_mov_b32 v249, 0\nM 36,160,64\nR 182,187,20480\n"
"s_add_u32 s61, s82, 845840\ns_mov_b64 exec, 0xf\nbuffer_atomic_or_x2 v[248:249], v250, s[20:23], s61 offen sc0 sc1\ns_mov_b64 exec, -1\nM 36,164,80\nG 4\n"
"M 40,168,64\nR 160,184,24576\nM 40,172,80\nbuffer_load_dwordx4 v221, s[16:19], s63 offen offset:1024 lds\nM 56,168,68\nR 162,184,28672\n"
"M 56,172,84\ns_add_u32 m0, s50, 106496\nM 72,168,72\nR 164,184,32768\nM 72,172,88\ns_add_u32 s63, s56, 327680\n"
"M 88,168,76\nR 166,184,36864\nM 88,172,92\nG 4\nM 92,176,76\nR 168,185,24576\n"
"M 92,180,92\nbuffer_load_dwordx4 v220, s[16:19], s63 offen lds\nM 76,176,72\nR 170,185,28672\nM 76,180,88\nM 60,176,68\n"
"R 172,185,32768\nM 60,180,84\nM 44,176,64\nR 174,185,36864\nM 44,180,80\nG 4\n"
"M 32,160,96\nR 176,186,24576\nM 32,164,112\nbuffer_load_dwordx4 v221, s[16:19], s63 offen offset:1024 lds\nM 48,160,100\nR 178,186,28672\n"
"M 48,164,116\ns_add_u32 m0, s50, 114688\nM 64,160,104\nR 180,186,32768\nM 64,164,120\ns_add_u32 s63, s56, 851968\n"
"M 80,160,108\nR 182,186,36864\nM 80,164,124\nG 4\nM 84,168,108\nR 160,187,24576\n"
"M 84,172,124\nbuffer_load_dwordx4 v220, s[16:19], s63 offen lds\nM 68,168,104\nR 162,187,28672\nM 68,172,120\nM 52,168,100\n"
"R 164,187,32768\nM 52,172,116\nbuffer_load_dwordx4 v221, s[16:19], s63 offen offset:1024 lds\nM 36,168,96\nR 166,187,36864\ns_add_u32 m0, s50, 0\n"
"M 36,172,112\nG 4\nM 40,176,96\nR 168,184,40960\ns_add_u32 s63, s57, 262144\nM 40,180,112\n"
"buffer_load_dwordx4 v220, s[16:19], s63 offen lds\nM 56,176,100\nR 170,184,45056\nM 56,180,116\nM 72,176,104\nR 172,184,49152\n"
"M 72,180,120\nbuffer_load_dwordx4 v221, s[16:19], s63 offen offset:1024 lds\nM 88,176,108\nR 174,184,53248\ns_waitcnt vmcnt(16)\ns_barrier\n"
"M 88,180,124\nG 4\nM 92,160,108\nR 176,185,40960\nM 92,164,124\ns_add_u32 s64, s68, 37752832\n"
"M 76,160,104\nR 178,185,45056\nM 76,164,120\nM 60,160,100\nR 180,185,49152\ns_waitcnt vmcnt(7)\n"
"v_cmp_eq_u64 s[86:87], s[46:47], v[248:249]\nM 60,164,116\ns_add_u32 s65, s68, 37867520\nM 44,160,96\nR 182,185,53248\ns_and_b32 s62, s86, 0xf\n"
"s_cmp_eq_u32 s62, 0xf\ns_cbranch_scc0 Lq927\nLq928:\nM 44,164,112\nG 4\nM 32,168,128\n"
"R 160,186,40960\nM 32,172,144\nbuffer_load_dwordx4 a[160:163], v252, s[20:23], s64 offen sc1\nM 48,168,132\nR 162,186,45056\nM 48,172,148\n"
"s_add_u32 s66, s69, 37752832\nM 64,168,136\nR 164,186,49152\nM 64,172,152\nbuffer_load_dwordx4 a[164:167], v252, s[20:23], s65 offen sc1\nM 80,168,140\n"
"R 166,186,53248\nM 80,172,156\nG 4\nM 84,176,140\nR 168,187,40960\nM 84,180,156\n"
"s_add_u32 s67, s69, 37867520\nM 68,176,136\nR 170,187,45056\nM 68,180,152\nbuffer_load_dwordx4 a[168:171], v252, s[20:23], s66 offen sc1\nM 52,176,132\n"
"R 172,187,49152\nM 52,180,148\ns_add_u32 s64, s68, 37760000\nM 36,176,128\nR 174,187,53248\nM 36,180,144\n"
"G 4\nM 40,160,128\nR 176,184,57344\nM 40,164,144\nbuffer_load_dwordx4 a[172:175], v252, s[20:23], s67 offen sc1\nM 56,160,132\n"
"R 178,184,61440\nM 56,164,148\ns_add_u32 s65, s68, 37874688\nM 72,160,136\nR 180,188,0\nM 72,164,152\n"
"buffer_load_dwordx4 a[176:179], v252, s[20:23], s64 offen sc1\nM 88,160,140\nR 182,188,4096\ns_add_u32 m0, s50, 8192\nM 88,164,156\nG 4\n"
"M 92,168,140\nR 160,185,57344\ns_add_u32 s63, s57, 786432\nM 92,172,156\ns_add_u32 s66, s69, 37760000\nM 76,168,136\n"
"R 162,185,61440\nM 76,172,152\nbuffer_load_dwordx4 a[180:183], v252, s[20:23], s65 offen sc1\nbuffer_load_dwordx4 v220, s[16:19], s63 offen lds\nM 60,168,132\nR 164,189,0\n"
"M 60,172,148\ns_add_u32 s67, s69, 37874688\nM 44,168,128\nR 166,189,4096\nM 44,172,144\nbuffer_load_dwordx4 v221, s[16:19], s63 offen offset:1024 lds\n"
"G 4\nM 96,176,128\nR 168,186,57344\ns_add_u32 m0, s50, 16384\nM 96,180,144\nbuffer_load_dwordx4 a[184:187], v252, s[20:23], s66 offen sc1\n"
"M 112,176,132\nR 170,186,61440\ns_add_u32 s63, s57, 327680\nM 112,180,148\ns_add_u32 s64, s68, 37753856\nbuffer_load_dwordx4 v220, s[16:19], s63 offen lds\n"
"M 128,176,136\nR 172,190,0\nM 128,180,152\nbuffer_load_dwordx4 a[188:191], v252, s[20:23], s67 offen sc1\nM 144,176,140\nR 174,190,4096\n"
"M 144,180,156\nbuffer_load_dwordx4 v221, s[16:19], s63 offen offset:1024 lds\nG 4\nM 148,160,140\nR 176,187,57344\ns_waitcnt vmcnt(18)\n"
"s_barrier\nM 148,164,156\ns_add_u32 s65, s68, 37868544\ns_add_u32 s63, s57, 851968\nM 132,160,136\nR 178,187,61440\n"
"s_add_u32 m0, s50, 24576\nM 132,164,152\nLpg31:\ns_cbranch_execz LretO1n\nbuffer_load_dwordx4 a[192:195], v252, s[20:23], s64 offen sc1\nM 116,160,132\n"
"R 180,191,0\nbuffer_load_dwordx4 v220, s[16:19], s63 offen lds\nM 116,164,148\ns_add_u32 s66, s69, 37753856\nM 100,160,128\nR 182,191,4096\n"
"M 100,164,144\nG 4\nM 104,168,128\nR 160,188,8192\nbuffer_load_dwordx4 v221, s[16:19], s63 offen offset:1024 lds\nM 104,172,144\n"
"buffer_load_dwordx4 a[196:199], v252, s[20:23], s65 offen sc1\nM 120,168,132\nR 162,188,12288\nM 120,172,148\ns_add_u32 s67, s69, 37868544\nM 136,168,136\n"
"R 164,188,16384\nM 136,172,152\nbuffer_load_dwordx4 a[200:203], v252, s[20:23], s66 offen sc1\nM 152,168,140\nR 166,188,20480\nM 152,172,156\n"
"G 4\nM 156,176,140\nR 168,189,8192\nM 156,180,156\nbuffer_load_dwordx4 a[204:207], v252, s[20:23], s67 offen sc1\nM 140,176,136\n"
"R 170,189,12288\nM 140,180,152\ns_add_u32 s64, s68, 37761024\nM 124,176,132\nR 172,189,16384\nM 124,180,148\n"
"s_add_u32 s65, s68, 37875712\nM 108,176,128\nR 174,189,20480\nM 108,180,144\nG 4\nM 0,160,128\n"
"R 176,190,8192\nM 0,164,144\nbuffer_load_dwordx4 a[208:211], v252, s[20:23], s64 offen sc1\nM 16,160,132\nR 178,190,12288\nM 16,164,148\n"
"s_add_u32 s66, s69, 37761024\nv_mfma_f32_16x16x32_bf16 a[224:227], v[160:163], a[136:139], a[224:227]\nR 180,190,16384\nv_mfma_f32_16x16x32_bf16 a[224:227], v[164:167], a[152:155], a[224:227]\nbuffer_load_dwordx4 a[212:215], v252, s[20:23], s65 offen sc1\nv_mfma_f32_16x16x32_bf16 a[240:243], v[160:163], a[140:143], a[240:243]\n"
"R 182,190,20480\nv_mfma_f32_16x16x32_bf16 a[240:243], v[164:167], a[156:159], a[240:243]\nG 4\nv_mfma_f32_16x16x32_bf16 a[244:247], v[168:171], a[140:143], a[244:247]\nR 160,191,8192\nv_mfma_f32_16x16x32_bf16 a[244:247], v[172:175], a[156:159], a[244:247]\n"
"buffer_load_dwordx4 a[216:219], v252, s[20:23], s66 offen sc1\nv_mfma_f32_16x16x32_bf16 a[228:231], v[168:171], a[136:139], a[228:231]\nR 162,191,12288\nv_mfma_f32_16x16x32_bf16 a[228:231], v[172:175], a[152:155], a[228:231]\ns_add_u32 s67, s69, 37875712\nM 20,168,132\n"
"R 164,191,16384\nM 20,172,148\nM 4,168,128\nR 166,191,20480\nM 4,172,144\nG 4\n"
"M 8,176,128\nR 168,188,24576\nM 8,180,144\nbuffer_load_dwordx4 a[220:223], v252, s[20:23], s67 offen sc1\nM 24,176,132\nR 170,188,28672\n"
"M 24,180,148\ns_add_u32 m0, s50, 32768\nv_mfma_f32_16x16x32_bf16 a[232:235], v[176:179], a[136:139], a[232:235]\nR 172,188,32768\nv_mfma_f32_16x16x32_bf16 a[232:235], v[180:183], a[152:155], a[232:235]\ns_add_u32 s63, s58, 262144\n"
"v_mfma_f32_16x16x32_bf16 a[248:251], v[176:179], a[140:143], a[248:251]\nR 174,188,36864\nv_mfma_f32_16x16x32_bf16 a[248:251], v[180:183], a[156:159], a[248:251]\nG 4\nv_mfma_f32_16x16x32_bf16 a[252:255], v[160:163], a[140:143], a[252:255]\nR 176,189,24576\n"
"v_mfma_f32_16x16x32_bf16 a[252:255], v[164:167], a[156:159], a[252:255]\nbuffer_load_dwordx4 v220, s[16:19], s63 offen lds\nv_mfma_f32_16x16x32_bf16 a[236:239], v[160:163], a[136:139], a[236:239]\nR 178,189,28672\nv_mfma_f32_16x16x32_bf16 a[236:239], v[164:167], a[152:155], a[236:239]\nM 28,160,132\n"
"R 180,189,32768\nM 28,164,148\nbuffer_load_dwordx4 v221, s[16:19], s63 offen offset:1024 lds\nM 12,160,128\nR 182,189,36864\nM 12,164,144\n"
"s_waitcnt vmcnt(9)\ns_barrier\nG 4\nM 96,168,160\nR 160,190,24576\nM 96,172,176\n"
"s_add_u32 m0, s50, 40960\nM 112,168,164\nR 162,190,28672\nM 112,172,180\ns_add_u32 s63, s58, 786432\nM 128,168,168\n"
"R 164,190,32768\nv_lshrrev_b32 v230, 8, v252\nM 128,172,184\nbuffer_load_dwordx4 v220, s[16:19], s63 offen lds\nM 144,168,172\nR 166,190,36864\n"
"v_lshlrev_b32 v230, 3, v230\nM 144,172,188\nG 4\nM 148,176,172\nR 168,191,24576\nM 148,180,188\n"
"buffer_load_dwordx2 v[232:233], v230, s[36:39], s53 offen offset:0\nM 132,176,168\nR 170,191,28672\nM 132,180,184\nbuffer_load_dwordx2 v[234:235], v230, s[36:39], s53 offen offset:32\nM 116,176,164\n"
"R 172,191,32768\nM 116,180,180\nbuffer_load_dwordx4 v221, s[16:19], s63 offen offset:1024 lds\nM 100,176,160\nR 174,191,36864\nM 100,180,176\n"
"G 4\nM 104,160,160\nR 176,188,40960\nM 104,164,176\ns_add_u32 m0, s50, 49152\nM 120,160,164\n"
"R 178,188,45056\nM 120,164,180\ns_add_u32 s63, s58, 327680\nM 136,160,168\nR 180,188,49152\nbuffer_load_dwordx2 v[236:237], v230, s[36:39], s53 offen offset:64\n"
"M 136,164,184\nbuffer_load_dwordx4 v220, s[16:19], s63 offen lds\nM 152,160,172\nR 182,188,53248\nM 152,164,188\nbuffer_load_dwordx2 v[238:239], v230, s[36:39], s53 offen offset:96\n"
"G 4\nM 156,168,172\nR 160,189,40960\nv_bfe_u32 v242, v252, 4, 4\nM 156,172,188\nv_lshrrev_b32 v243, 8, v252\n"
"v_lshlrev_b32 v243, 2, v243\nM 140,168,168\nR 162,189,45056\nv_sub_u32 v242, v242, v243\nM 140,172,184\nv_lshlrev_b32 v243, 4, v242\n"
"s_mov_b64 s[60:61], 0x3f80\nv_lshlrev_b64 v[208:209], v243, s[60:61]\nM 124,168,164\nR 164,189,49152\nM 124,172,180\nbuffer_load_dwordx4 v221, s[16:19], s63 offen offset:1024 lds\n"
"M 108,168,160\nR 166,189,53248\nM 108,172,176\nG 4\nM 96,176,192\nR 168,190,40960\n"
"s_waitcnt vmcnt(13)\nM 96,180,208\ns_add_u32 m0, s50, 57344\nM 112,176,196\nR 170,190,45056\ns_waitcnt vmcnt(12)\n"
"M 112,180,212\ns_add_u32 s63, s58, 851968\nM 128,176,200\nR 172,190,49152\ns_waitcnt vmcnt(11)\nM 128,180,216\n"
"buffer_load_dwordx4 v220, s[16:19], s63 offen lds\nM 144,176,204\nR 174,190,53248\ns_waitcnt vmcnt(11)\nM 144,180,220\nG 4\n"
"M 148,160,204\nR 176,191,40960\nM 148,164,220\nv_cmp_gt_u32 s[60:61], 4, v242\ns_nop 1\nv_cndmask_b32 v212, 0, v208, s[60:61]\n"
"v_cndmask_b32 v208, 0, v208, s[60:61]\nv_cndmask_b32 v213, 0, v209, s[60:61]\nv_cndmask_b32 v209, 0, v209, s[60:61]\nM 132,160,200\nR 178,191,45056\nM 132,164,216\n"
"M 116,160,196\nR 180,191,49152\nM 116,164,212\nbuffer_load_dwordx4 v221, s[16:19], s63 offen offset:1024 lds\nM 100,160,192\nR 182,191,53248\n"
"M 100,164,208\nG 4\nM 104,168,192\nR 160,184,0\nM 104,172,208\ns_add_u32 m0, s50, 65536\n"
"M 120,168,196\nR 162,184,4096\nM 120,172,212\ns_add_u32 s63, s59, 0\nM 136,168,200\nR 164,184,8192\n"
"M 136,172,216\nbuffer_load_dwordx4 v220, s[16:19], s63 offen lds\nM 152,168,204\nR 166,184,12288\nM 152,172,220\nG 4\n"
"M 156,176,204\nR 168,185,0\nM 156,180,220\nM 140,176,200\nR 170,185,4096\nM 140,180,216\n"
"buffer_load_dwordx4 v221, s[16:19], s63 offen offset:1024 lds\nM 124,176,196\nR 172,185,8192\nM 124,180,212\nM 108,176,192\nR 174,185,12288\n"
"M 108,180,208\nG 4\nM 0,160,160\nR 176,186,0\nM 0,164,176\ns_mov_b32 m0, s51\n"
"M 16,160,164\nR 178,186,4096\nv_cvt_pk_bf16_f32 v128, v128, v129\nv_cvt_pk_bf16_f32 v129, v130, v131\nM 16,164,180\nds_write_addtid_b32 v128 offset:0\n"
"v_cvt_pk_bf16_f32 v130, v132, v133\nv_cvt_pk_bf16_f32 v131, v134, v135\nv_mfma_f32_16x16x32_bf16 a[224:227], v[160:163], a[168:171], a[224:227]\nR 180,186,8192\nv_cvt_pk_bf16_f32 v132, v136, v137\nv_mfma_f32_16x16x32_bf16 a[224:227], v[164:167], a[184:187], a[224:227]\n"
"ds_write_addtid_b32 v129 offset:256\nv_cvt_pk_bf16_f32 v133, v138, v139\nv_mfma_f32_16x16x32_bf16 a[240:243], v[160:163], a[172:175], a[240:243]\nR 182,186,12288\nv_cvt_pk_bf16_f32 v134, v140, v141\nv_mfma_f32_16x16x32_bf16 a[240:243], v[164:167], a[188:191], a[240:243]\n"
"ds_write_addtid_b32 v130 offset:512\nG 7\nv_cvt_pk_bf16_f32 v135, v142, v143\nv_mfma_f32_16x16x32_bf16 a[244:247], v[168:171], a[172:175], a[244:247]\nR 160,187,0\nv_cvt_pk_bf16_f32 v136, v144, v145\n"
"v_mfma_f32_16x16x32_bf16 a[244:247], v[172:175], a[188:191], a[244:247]\nds_write_addtid_b32 v131 offset:768\nv_cvt_pk_bf16_f32 v137, v146, v147\nv_mfma_f32_16x16x32_bf16 a[228:231], v[168:171], a[168:171], a[228:231]\nR 162,187,4096\nv_cvt_pk_bf16_f32 v138, v148, v149\n"
"v_mfma_f32_16x16x32_bf16 a[228:231], v[172:175], a[184:187], a[228:231]\nds_write_addtid_b32 v132 offset:1024\nv_cvt_pk_bf16_f32 v139, v150, v151\nM 20,168,164\nR 164,187,8192\nv_cvt_pk_bf16_f32 v140, v152, v153\n"
"M 20,172,180\nds_write_addtid_b32 v133 offset:1280\nv_cvt_pk_bf16_f32 v141, v154, v155\nM 4,168,160\nR 166,187,12288\nv_cvt_pk_bf16_f32 v142, v156, v157\n"
"M 4,172,176\nds_write_addtid_b32 v134 offset:1536\nG 9\nv_cvt_pk_bf16_f32 v143, v158, v159\nM 8,176,160\nR 168,184,16384\n"
"ds_write_addtid_b32 v135 offset:1792\nM 8,180,176\nds_write_addtid_b32 v136 offset:2048\nds_write_addtid_b32 v137 offset:2304\nM 24,176,164\nR 170,184,20480\n"
"ds_write_addtid_b32 v138 offset:2560\nM 24,180,180\nds_write_addtid_b32 v139 offset:2816\nds_write_addtid_b32 v140 offset:3072\nv_mfma_f32_16x16x32_bf16 a[232:235], v[176:179], a[168:171], a[232:235]\nR 172,184,24576\n"
"ds_write_addtid_b32 v141 offset:3328\nv_mfma_f32_16x16x32_bf16 a[232:235], v[180:183], a[184:187], a[232:235]\nds_write_addtid_b32 v142 offset:3584\nds_write_addtid_b32 v143 offset:3840\nv_mfma_f32_16x16x32_bf16 a[248:251], v[176:179], a[172:175], a[248:251]\nR 174,184,28672\n"
"s_add_u32 m0, s50, 73728\nv_mfma_f32_16x16x32_bf16 a[248:251], v[180:183], a[188:191], a[248:251]\ns_add_u32 s63, s59, 524288\nG 14\nv_mfma_f32_16x16x32_bf16 a[252:255], v[160:163], a[172:175], a[252:255]\nR 176,185,16384\n"
"s_waitcnt vmcnt(8) lgkmcnt(2)\ns_barrier\nv_mfma_f32_16x16x32_bf16 a[252:255], v[164:167], a[188:191], a[252:255]\nbuffer_load_dwordx4 v220, s[16:19], s63 offen lds\nv_mfma_f32_16x16x32_bf16 a[236:239], v[160:163], a[168:171], a[236:239]\nR 178,185,20480\n"
"v_mfma_f32_16x16x32_bf16 a[236:239], v[164:167], a[184:187], a[236:239]\nds_read2st64_b32 v[128:129], v225 offset0:0 offset1:1\nM 28,160,164\nR 180,185,24576\nM 28,164,180\nbuffer_load_dwordx4 v221, s[16:19], s63 offen offset:1024 lds\n"
"M 12,160,160\nR 182,185,28672\ns_add_u32 m0, s50, 81920\nM 12,164,176\nds_read2st64_b32 v[130:131], v225 offset0:2 offset1:3\nG 6\n"
"M 0,168,192\nR 160,186,16384\ns_add_u32 s63, s59, 65536\nM 0,172,208\nbuffer_load_dwordx4 v220, s[16:19], s63 offen lds\nds_read2st64_b32 v[132:133], v225 offset0:4 offset1:5\n"
"M 16,168,196\nR 162,186,20480\nds_read2st64_b32 v[134:135], v225 offset0:6 offset1:7\nM 16,172,212\nds_read2st64_b32 v[136:137], v225 offset0:8 offset1:9\nv_mfma_f32_16x16x32_bf16 a[224:227], v[168:171], a[200:203], a[224:227]\n"
"R 164,186,24576\nv_mfma_f32_16x16x32_bf16 a[224:227], v[172:175], a[216:219], a[224:227]\nbuffer_load_dwordx4 v221, s[16:19], s63 offen offset:1024 lds\nG 6\nv_mfma_f32_16x16x32_bf16 v[96:99], v[208:211], v[128:131], v[96:99]\nds_read2st64_b32 v[138:139], v225 offset0:10 offset1:11\n"
"v_mfma_f32_16x16x32_bf16 v[100:103], v[210:213], v[128:131], v[100:103]\nds_read2st64_b32 v[140:141], v225 offset0:12 offset1:13\nds_read2st64_b32 v[142:143], v225 offset0:14 offset1:15\nv_mfma_f32_16x16x32_bf16 a[240:243], v[168:171], a[204:207], a[240:243]\nR 166,186,28672\ns_add_u32 m0, s50, 90112\n"
"v_mfma_f32_16x16x32_bf16 a[240:243], v[172:175], a[220:223], a[240:243]\nv_mfma_f32_16x16x32_bf16 a[244:247], v[176:179], a[204:207], a[244:247]\nR 168,187,16384\ns_add_u32 s63, s59, 589824\nG 7\nv_mfma_f32_16x16x32_bf16 v[104:107], v[208:211], v[132:135], v[104:107]\n"
"v_mfma_f32_16x16x32_bf16 v[108:111], v[210:213], v[132:135], v[108:111]\nv_mfma_f32_16x16x32_bf16 a[244:247], v[180:183], a[220:223], a[244:247]\nbuffer_load_dwordx4 v220, s[16:19], s63 offen lds\nv_mfma_f32_16x16x32_bf16 a[228:231], v[176:179], a[200:203], a[228:231]\nR 170,187,20480\nv_mfma_f32_16x16x32_bf16 a[228:231], v[180:183], a[216:219], a[228:231]\n"
"G 5\nv_mfma_f32_16x16x32_bf16 v[112:115], v[208:211], v[136:139], v[112:115]\nv_mfma_f32_16x16x32_bf16 v[116:119], v[210:213], v[136:139], v[116:119]\nG 3\nv_mfma_f32_16x16x32_bf16 v[120:123], v[208:211], v[140:143], v[120:123]\nv_mfma_f32_16x16x32_bf16 v[124:127], v[210:213], v[140:143], v[124:127]\n"
"M 20,176,196\nR 172,187,24576\nM 20,180,212\nbuffer_load_dwordx4 v221, s[16:19], s63 offen offset:1024 lds\nM 4,176,192\nR 174,187,28672\n"
"s_add_u32 s63, s59, 131072\nM 4,180,208\nG 4\nM 8,160,192\nR 176,184,32768\nM 8,164,208\n"
"s_add_u32 m0, s50, 98304\nM 24,160,196\nR 178,184,36864\nM 24,164,212\nbuffer_load_dwordx4 v220, s[16:19], s63 offen lds\nv_mfma_f32_16x16x32_bf16 a[232:235], v[160:163], a[200:203], a[232:235]\n"
"R 180,184,40960\nv_cvt_pk_bf16_f32 v120, v120, v121\nv_cvt_pk_bf16_f32 v112, v112, v113\nv_cvt_pk_bf16_f32 v121, v122, v123\nv_mfma_f32_16x16x32_bf16 a[232:235], v[164:167], a[216:219], a[232:235]\nv_cvt_pk_bf16_f32 v122, v124, v125\n"
"v_cvt_pk_bf16_f32 v123, v126, v127\nv_cvt_pk_bf16_f32 v96, v96, v97\nv_cvt_pk_bf16_f32 v113, v114, v115\nv_mfma_f32_16x16x32_bf16 a[248:251], v[160:163], a[204:207], a[248:251]\nR 182,184,45056\nv_cvt_pk_bf16_f32 v114, v116, v117\n"
"v_cvt_pk_bf16_f32 v97, v98, v99\nv_mfma_f32_16x16x32_bf16 a[248:251], v[164:167], a[220:223], a[248:251]\nG 4\nv_cvt_pk_bf16_f32 v98, v100, v101\nv_cvt_pk_bf16_f32 v99, v102, v103\nv_mfma_f32_16x16x32_bf16 a[252:255], v[168:171], a[204:207], a[252:255]\n"
"R 160,185,32768\ns_bfe_u32 s100, s56, 0x20007\ns_mul_i32 s100, s100, 49152\ns_add_u32 s100, s100, s72\ns_add_u32 s100, s100, 65011712\nv_cvt_pk_bf16_f32 v115, v118, v119\n"
"v_mfma_f32_16x16x32_bf16 a[252:255], v[172:175], a[220:223], a[252:255]\nbuffer_store_dwordx4 v[96:99], v252, s[20:23], s100 offen offset:0 sc1\nv_mfma_f32_16x16x32_bf16 a[236:239], v[168:171], a[200:203], a[236:239]\nR 162,185,36864\nv_cvt_pk_bf16_f32 v104, v104, v105\nv_mfma_f32_16x16x32_bf16 a[236:239], v[172:175], a[216:219], a[236:239]\n"
"v_cvt_pk_bf16_f32 v105, v106, v107\nv_cvt_pk_bf16_f32 v106, v108, v109\nM 28,168,196\nR 164,185,40960\nv_cvt_pk_bf16_f32 v107, v110, v111\nM 28,172,212\n"
"buffer_load_dwordx4 v221, s[16:19], s63 offen offset:1024 lds\nM 12,168,192\nR 166,185,45056\nM 12,172,208\nbuffer_store_dwordx4 v[104:107], v252, s[20:23], s100 offen offset:1024 sc1\ns_waitcnt vmcnt(12)\n"
"s_barrier\nG 4\nM 32,176,160\nR 168,186,32768\nM 32,180,176\nv_xor_b32 v240, 0x2000, v225\n"
"M 48,176,164\nR 170,186,36864\nds_write2st64_b32 v240, a224, a225 offset0:0 offset1:1\nM 48,180,180\nbuffer_store_dwordx4 v[112:115], v252, s[20:23], s100 offen offset:2048 sc1\nds_write2st64_b32 v240, a226, a227 offset0:2 offset1:3\n"
"M 64,176,168\nR 172,186,40960\nds_write2st64_b32 v240, a228, a229 offset0:4 offset1:5\nM 64,180,184\nds_write2st64_b32 v240, a230, a231 offset0:6 offset1:7\nM 80,176,172\n"
"R 174,186,45056\nds_write2st64_b32 v240, a232, a233 offset0:8 offset1:9\nM 80,180,188\nds_write2st64_b32 v240, a234, a235 offset0:10 offset1:11\nG 10\nM 84,160,172\n"
"R 176,187,32768\ns_add_u32 s63, s59, 655360\nds_write2st64_b32 v240, a236, a237 offset0:12 offset1:13\nM 84,164,188\nbuffer_store_dwordx4 v[120:123], v252, s[20:23], s100 offen offset:3072 sc1\nds_write2st64_b32 v240, a238, a239 offset0:14 offset1:15\n"
"M 68,160,168\nR 178,187,36864\nds_write2st64_b32 v240, a240, a241 offset0:16 offset1:17\nM 68,164,184\ns_add_u32 m0, s50, 106496\nds_write2st64_b32 v240, a242, a243 offset0:18 offset1:19\n"
"M 52,160,164\nR 180,187,40960\nds_write2st64_b32 v240, a244, a245 offset0:20 offset1:21\nM 52,164,180\nbuffer_load_dwordx4 v220, s[16:19], s63 offen lds\nds_write2st64_b32 v240, a246, a247 offset0:22 offset1:23\n"
"M 36,160,160\nR 182,187,45056\nds_write2st64_b32 v240, a248, a249 offset0:24 offset1:25\nM 36,164,176\nds_write2st64_b32 v240, a250, a251 offset0:26 offset1:27\nG 14\n"
"M 40,168,160\nR 160,184,49152\nds_write2st64_b32 v240, a252, a253 offset0:28 offset1:29\nM 40,172,176\nM 56,168,164\nR 162,184,53248\n"
"ds_write2st64_b32 v240, a254, a255 offset0:30 offset1:31\nM 56,172,180\nbuffer_load_dwordx4 v221, s[16:19], s63 offen offset:1024 lds\nM 72,168,168\nR 164,184,57344\ns_add_u32 s63, s59, 196608\n"
"M 72,172,184\ns_add_u32 m0, s50, 114688\nM 88,168,172\nR 166,184,61440\nM 88,172,188\nG 8\n"
"M 92,176,172\nR 168,185,49152\ns_waitcnt vmcnt(12) lgkmcnt(3)\ns_barrier\nM 92,180,188\nbuffer_load_dwordx4 v220, s[16:19], s63 offen lds\n"
"ds_read2st64_b32 v[128:129], v225 offset0:0 offset1:1\nM 76,176,168\nR 170,185,53248\nds_read2st64_b32 v[130:131], v225 offset0:2 offset1:3\nM 76,180,184\nds_read2st64_b32 v[132:133], v225 offset0:4 offset1:5\n"
"ds_read2st64_b32 v[134:135], v225 offset0:6 offset1:7\nM 60,176,164\nR 172,185,57344\nds_read2st64_b32 v[136:137], v225 offset0:8 offset1:9\nM 60,180,180\nbuffer_load_dwordx4 v221, s[16:19], s63 offen offset:1024 lds\n"
"ds_read2st64_b32 v[138:139], v225 offset0:10 offset1:11\nM 44,176,160\nR 174,185,61440\ns_add_u32 m0, s50, 0\nds_read2st64_b32 v[140:141], v225 offset0:12 offset1:13\nM 44,180,176\n"
"ds_read2st64_b32 v[142:143], v225 offset0:14 offset1:15\nds_read2st64_b32 v[144:145], v225 offset0:16 offset1:17\nLpg32:\ns_cbranch_execz LretW1z\nG 13\nM 32,160,192\n"
"R 176,186,49152\ns_add_u32 s63, s59, 720896\nds_read2st64_b32 v[146:147], v225 offset0:18 offset1:19\nM 32,164,208\nbuffer_load_dwordx4 v220, s[16:19], s63 offen lds\nds_read2st64_b32 v[148:149], v225 offset0:20 offset1:21\n"
"M 48,160,196\nR 178,186,53248\nds_read2st64_b32 v[150:151], v225 offset0:22 offset1:23\nG 15\nv_add_f32 v0, v0, v128\nM 48,164,212\n"
"ds_read2st64_b32 v[152:153], v225 offset0:24 offset1:25\nds_read2st64_b32 v[154:155], v225 offset0:26 offset1:27\nv_add_f32 v1, v1, v129\nM 64,160,200\nR 180,186,57344\nds_read2st64_b32 v[156:157], v225 offset0:28 offset1:29\n"
"G 15\nv_add_f32 v2, v2, v130\nM 64,164,216\nbuffer_load_dwordx4 v221, s[16:19], s63 offen offset:1024 lds\nds_read2st64_b32 v[158:159], v225 offset0:30 offset1:31\nM 80,160,204\n"
"R 182,186,61440\ns_add_u32 m0, s50, 8192\nv_add_f32 v3, v3, v131\nM 80,164,220\nG 14\nv_add_f32 v4, v4, v132\n"
"v_add_f32 v5, v5, v133\nv_add_f32 v6, v6, v134\nM 84,168,204\nR 160,187,49152\ns_add_u32 s63, s59, 393216\nv_add_f32 v7, v7, v135\n"
"M 84,172,220\nbuffer_load_dwordx4 v220, s[16:19], s63 offen lds\nv_add_f32 v8, v8, v136\nM 68,168,200\nR 162,187,53248\nv_add_f32 v9, v9, v137\n"
"v_add_f32 v10, v10, v138\nM 68,172,216\nv_add_f32 v11, v11, v139\nG 15\nv_add_f32 v12, v12, v140\nv_add_f32 v13, v13, v141\n"
"M 52,168,196\nR 164,187,57344\nG 15\nv_add_f32 v14, v14, v142\nv_add_f32 v15, v15, v143\nM 52,172,212\n"
"buffer_load_dwordx4 v221, s[16:19], s63 offen offset:1024 lds\nG 14\nv_add_f32 v16, v16, v144\nM 36,168,192\nR 166,187,61440\ns_add_u32 s63, s59, 458752\n"
"v_add_f32 v17, v17, v145\nM 36,172,208\nG 4\nv_add_f32 v18, v18, v146\nv_add_f32 v19, v19, v147\nv_add_f32 v20, v20, v148\n"
"M 40,176,192\nR 168,188,0\nv_add_f32 v21, v21, v149\nv_add_f32 v22, v22, v150\nM 40,180,208\ns_add_u32 m0, s50, 16384\n"
"v_add_f32 v23, v23, v151\nv_add_f32 v24, v24, v152\nv_add_f32 v25, v25, v153\nM 56,176,196\nR 170,188,4096\nv_add_f32 v26, v26, v154\n"
"v_add_f32 v27, v27, v155\nM 56,180,212\nbuffer_load_dwordx4 v220, s[16:19], s63 offen lds\nv_add_f32 v28, v28, v156\nv_add_f32 v29, v29, v157\nv_add_f32 v30, v30, v158\n"
"v_add_f32 v31, v31, v159\nM 72,176,200\nR 172,188,8192\nv_cvt_pk_bf16_f32 v24, v24, v25\nv_cvt_pk_bf16_f32 v25, v26, v27\nM 72,180,216\n"
"v_cvt_pk_bf16_f32 v26, v28, v29\nv_cvt_pk_bf16_f32 v27, v30, v31\nv_cvt_pk_bf16_f32 v0, v0, v1\nv_cvt_pk_bf16_f32 v16, v16, v17\nM 88,176,204\nR 174,188,12288\n"
"v_cvt_pk_bf16_f32 v1, v2, v3\nM 88,180,220\nG 4\nv_cvt_pk_bf16_f32 v2, v4, v5\nv_cvt_pk_bf16_f32 v3, v6, v7\nM 92,160,204\n"
"R 176,189,0\ns_bfe_u32 s100, s57, 0x20007\ns_mul_i32 s100, s100, 49152\ns_add_u32 s100, s100, s72\ns_add_u32 s100, s100, 65028096\nv_cvt_pk_bf16_f32 v17, v18, v19\n"
"M 92,164,220\nbuffer_store_dwordx4 v[0:3], v252, s[20:23], s100 offen offset:0 sc1\nM 76,160,200\nR 178,189,4096\nv_cvt_pk_bf16_f32 v8, v8, v9\nM 76,164,216\n"
"v_cvt_pk_bf16_f32 v9, v10, v11\nv_cvt_pk_bf16_f32 v10, v12, v13\nM 60,160,196\nR 180,189,8192\nv_cvt_pk_bf16_f32 v11, v14, v15\nM 60,164,212\n"
"buffer_load_dwordx4 v221, s[16:19], s63 offen offset:1024 lds\nM 44,160,192\nR 182,189,12288\nv_cvt_pk_bf16_f32 v18, v20, v21\nM 44,164,208\nbuffer_store_dwordx4 v[8:11], v252, s[20:23], s100 offen offset:1024 sc1\n"
"s_waitcnt vmcnt(18)\ns_barrier\nG 4\nv_mfma_f32_16x16x32_bf16 v[96:99], v[168:171], a[0:3], 0\nR 160,190,0\nv_cvt_pk_bf16_f32 v19, v22, v23\n"
"M 96,172,16\ns_mov_b32 m0, s51\nv_mfma_f32_16x16x32_bf16 v[112:115], v[168:171], a[4:7], 0\nR 162,190,4096\nv_cvt_pk_bf16_f32 v64, v64, v65\nv_cvt_pk_bf16_f32 v65, v66, v67\n"
"M 112,172,20\nbuffer_store_dwordx4 v[16:19], v252, s[20:23], s100 offen offset:2048 sc1\nv_cvt_pk_bf16_f32 v66, v68, v69\nv_mfma_f32_16x16x32_bf16 v[128:131], v[168:171], a[8:11], 0\nR 164,190,8192\nv_cvt_pk_bf16_f32 v67, v70, v71\n"
"v_cvt_pk_bf16_f32 v68, v72, v73\nM 128,172,24\nds_write_addtid_b32 v64 offset:0\nv_cvt_pk_bf16_f32 v69, v74, v75\nv_cvt_pk_bf16_f32 v70, v76, v77\nv_mfma_f32_16x16x32_bf16 v[144:147], v[168:171], a[12:15], 0\n"
"R 166,190,12288\nv_cvt_pk_bf16_f32 v71, v78, v79\nv_cvt_pk_bf16_f32 v72, v80, v81\nM 144,172,28\nds_write_addtid_b32 v65 offset:256\nG 6\n"
"v_cvt_pk_bf16_f32 v73, v82, v83\nv_mfma_f32_16x16x32_bf16 v[148:151], v[176:179], a[12:15], 0\nR 168,191,0\nv_cvt_pk_bf16_f32 v74, v84, v85\nv_cvt_pk_bf16_f32 v75, v86, v87\nM 148,180,28\n"
"buffer_store_dwordx4 v[24:27], v252, s[20:23], s100 offen offset:3072 sc1\nv_mfma_f32_16x16x32_bf16 v[132:135], v[176:179], a[8:11], 0\nR 170,191,4096\nv_cvt_pk_bf16_f32 v76, v88, v89\nM 132,180,24\nds_write_addtid_b32 v66 offset:512\n"
"v_cvt_pk_bf16_f32 v77, v90, v91\nv_mfma_f32_16x16x32_bf16 v[116:119], v[176:179], a[4:7], 0\nR 172,191,8192\nv_cvt_pk_bf16_f32 v78, v92, v93\nM 116,180,20\nds_write_addtid_b32 v67 offset:768\n"
"v_cvt_pk_bf16_f32 v79, v94, v95\nv_mfma_f32_16x16x32_bf16 v[100:103], v[176:179], a[0:3], 0\nR 174,191,12288\nds_write_addtid_b32 v68 offset:1024\nM 100,180,16\nds_write_addtid_b32 v69 offset:1280\n"
"ds_write_addtid_b32 v70 offset:1536\nG 10\nv_mfma_f32_16x16x32_bf16 v[104:107], v[160:163], a[0:3], 0\nR 176,188,16384\nds_write_addtid_b32 v71 offset:1792\nM 104,164,16\n"
"ds_write_addtid_b32 v72 offset:2048\nds_write_addtid_b32 v73 offset:2304\nv_mfma_f32_16x16x32_bf16 v[120:123], v[160:163], a[4:7], 0\nR 178,188,20480\nds_write_addtid_b32 v74 offset:2560\nM 120,164,20\n"
"ds_write_addtid_b32 v75 offset:2816\nds_write_addtid_b32 v76 offset:3072\nv_mfma_f32_16x16x32_bf16 v[136:139], v[160:163], a[8:11], 0\nR 180,188,24576\nds_write_addtid_b32 v77 offset:3328\nM 136,164,24\n"
"ds_write_addtid_b32 v78 offset:3584\nds_write_addtid_b32 v79 offset:3840\nv_mfma_f32_16x16x32_bf16 v[152:155], v[160:163], a[12:15], 0\nR 182,188,28672\ns_add_u32 m0, s50, 24576\nM 152,164,28\n"
"s_add_u32 s63, s59, 262144\nG 15\nv_mfma_f32_16x16x32_bf16 v[156:159], v[168:171], a[12:15], 0\nR 160,189,16384\ns_waitcnt vmcnt(12) lgkmcnt(2)\ns_barrier\n"
"M 156,172,28\nbuffer_load_dwordx4 v220, s[16:19], s63 offen lds\nv_mfma_f32_16x16x32_bf16 v[140:143], v[168:171], a[8:11], 0\nR 162,189,20480\nM 140,172,24\nds_read2st64_b32 v[64:65], v225 offset0:0 offset1:1\n"
"v_mfma_f32_16x16x32_bf16 v[124:127], v[168:171], a[4:7], 0\nR 164,189,24576\nM 124,172,20\nds_read2st64_b32 v[66:67], v225 offset0:2 offset1:3\nv_mfma_f32_16x16x32_bf16 v[108:111], v[168:171], a[0:3], 0\nR 166,189,28672\n"
"M 108,172,16\nds_read2st64_b32 v[68:69], v225 offset0:4 offset1:5\nG 7\nM 96,176,32\nR 168,190,16384\nv_bfe_u32 v250, v252, 4, 2\n"
"v_lshlrev_b32 v250, 3, v250\nM 96,180,48\nbuffer_load_dwordx4 v221, s[16:19], s63 offen offset:1024 lds\ns_lshl_b32 s60, s43, 5\nv_add_u32 v250, s60, v250\nM 112,176,36\n"
"R 170,190,20480\nds_read2st64_b32 v[70:71], v225 offset0:6 offset1:7\nM 112,180,52\ns_add_u32 m0, s50, 32768\nds_read2st64_b32 v[72:73], v225 offset0:8 offset1:9\nG 6\n"
"v_mfma_f32_16x16x32_bf16 v[32:35], v[208:211], v[64:67], v[32:35]\nds_read2st64_b32 v[74:75], v225 offset0:10 offset1:11\nv_mfma_f32_16x16x32_bf16 v[36:39], v[210:213], v[64:67], v[36:39]\nds_read2st64_b32 v[76:77], v225 offset0:12 offset1:13\nds_read2st64_b32 v[78:79], v225 offset0:14 offset1:15\nM 128,176,40\n"
"R 172,190,24576\nM 128,180,56\ns_add_u32 s63, s59, 786432\nM 144,176,44\nR 174,190,28672\nM 144,180,60\n"
"G 10\nM 148,160,44\nR 176,191,16384\nG 7\nv_mfma_f32_16x16x32_bf16 v[40:43], v[208:211], v[68:71], v[40:43]\nv_mfma_f32_16x16x32_bf16 v[44:47], v[210:213], v[68:71], v[44:47]\n"
"M 148,164,60\nbuffer_load_dwordx4 v220, s[16:19], s63 offen lds\nG 5\nv_mfma_f32_16x16x32_bf16 v[48:51], v[208:211], v[72:75], v[48:51]\nv_mfma_f32_16x16x32_bf16 v[52:55], v[210:213], v[72:75], v[52:55]\nG 3\n"
"v_mfma_f32_16x16x32_bf16 v[56:59], v[208:211], v[76:79], v[56:59]\nv_mfma_f32_16x16x32_bf16 v[60:63], v[210:213], v[76:79], v[60:63]\nM 132,160,40\nR 178,191,20480\nM 132,164,56\nM 116,160,36\n"
"R 180,191,24576\nM 116,164,52\nbuffer_load_dwordx4 v221, s[16:19], s63 offen offset:1024 lds\nM 100,160,32\nR 182,191,28672\ns_add_u32 m0, s50, 40960\n"
"M 100,164,48\nG 4\nM 104,168,32\nR 160,188,32768\ns_add_u32 s63, s59, 327680\nM 104,172,48\n"
"buffer_load_dwordx4 v220, s[16:19], s63 offen lds\nM 120,168,36\nR 162,188,36864\nv_mov_b32 v226, s46\nM 120,172,52\nv_mov_b32 v227, s47\n"
"M 136,168,40\nR 164,188,40960\ns_bfe_u32 s62, s56, 0x20007\ns_lshl_b32 s62, s62, 5\ns_add_u32 s61, s74, 1009664\ns_add_u32 s61, s61, s62\n"
"s_waitcnt vmcnt(5)\ns_mov_b64 exec, 1\nbuffer_atomic_swap_x2 v[226:227], off, s[20:23], s61 sc1\ns_mov_b64 exec, -1\nv_cvt_pk_bf16_f32 v32, v32, v33\nM 136,172,56\n"
"s_bfe_u32 s62, s57, 0x20007\ns_lshl_b32 s62, s62, 5\ns_add_u32 s61, s74, 1009672\ns_add_u32 s61, s61, s62\ns_mov_b64 exec, 1\nbuffer_atomic_swap_x2 v[226:227], off, s[20:23], s61 sc1\n"
"s_mov_b64 exec, -1\nM 152,168,44\nR 166,188,45056\nv_cvt_pk_bf16_f32 v33, v34, v35\nM 152,172,60\nG 4\n"
"v_cvt_pk_bf16_f32 v34, v36, v37\nv_cvt_pk_bf16_f32 v35, v38, v39\nM 156,176,44\nR 168,189,32768\ns_bfe_u32 s100, s58, 0x20007\ns_mul_i32 s100, s100, 49152\n"
"s_add_u32 s100, s100, s72\ns_add_u32 s100, s100, 65044480\nv_cvt_pk_bf16_f32 v48, v48, v49\nM 156,180,60\nbuffer_store_dwordx4 v[32:35], v252, s[20:23], s100 offen offset:0 sc1\nM 140,176,40\n"
"R 170,189,36864\nv_cvt_pk_bf16_f32 v40, v40, v41\nM 140,180,56\nv_cvt_pk_bf16_f32 v41, v42, v43\nv_cvt_pk_bf16_f32 v42, v44, v45\nM 124,176,36\n"
"R 172,189,40960\nv_cvt_pk_bf16_f32 v43, v46, v47\nM 124,180,52\nv_cvt_pk_bf16_f32 v49, v50, v51\nv_cvt_pk_bf16_f32 v50, v52, v53\nM 108,176,32\n"
"R 174,189,45056\nv_cvt_pk_bf16_f32 v51, v54, v55\nM 108,180,48\nbuffer_store_dwordx4 v[40:43], v252, s[20:23], s100 offen offset:1024 sc1\ns_barrier\nG 4\n"
"M 96,160,64\nR 176,190,32768\nv_cvt_pk_bf16_f32 v56, v56, v57\nM 96,164,80\nv_cvt_pk_bf16_f32 v57, v58, v59\nv_cvt_pk_bf16_f32 v58, v60, v61\n"
"M 112,160,68\nR 178,190,36864\nv_cvt_pk_bf16_f32 v59, v62, v63\nM 112,164,84\nbuffer_store_dwordx4 v[48:51], v252, s[20:23], s100 offen offset:2048 sc1\nM 128,160,72\n"
"R 180,190,40960\nM 128,164,88\nM 144,160,76\nR 182,190,45056\nM 144,164,92\nG 4\n"
"M 148,168,76\nR 160,191,32768\nM 148,172,92\nbuffer_store_dwordx4 v[56:59], v252, s[20:23], s100 offen offset:3072 sc1\nM 132,168,72\nR 162,191,36864\n"
"M 132,172,88\nM 116,168,68\nR 164,191,40960\nM 116,172,84\nM 100,168,64\nR 166,191,45056\n"
"v_mov_b32 v248, 0\nM 100,172,80\nG 4\nv_mov_b32 v249, 0\nM 104,176,64\nR 168,188,49152\n"
"s_add_u32 s61, s74, 1009664\ns_mov_b64 exec, 3\nbuffer_atomic_or_x2 v[248:249], v250, s[20:23], s61 offen sc0 sc1\ns_mov_b64 exec, -1\nM 104,180,80\nM 120,176,68\n"
"R 170,188,53248\nv_mov_b32 v242, v208\nM 120,180,84\nbuffer_load_dwordx4 v221, s[16:19], s63 offen offset:1024 lds\nM 136,176,72\nR 172,184,0\n"
"v_mov_b32 v243, v209\nM 136,180,88\ns_add_u32 m0, s50, 49152\nv_mov_b32 v244, v208\nv_mov_b32 v245, v209\nM 152,176,76\n"
"R 174,184,4096\nv_mov_b32 v64, v192\nM 152,180,92\nG 4\nv_mov_b32 v65, v193\nv_mov_b32 v70, v194\n"
"M 156,160,76\nR 176,189,49152\nv_mov_b32 v71, v195\nM 156,164,92\ns_add_u32 s63, s59, 851968\nv_mov_b32 v72, v196\n"
"v_mov_b32 v73, v197\nM 140,160,72\nR 178,189,53248\nv_mov_b32 v78, v198\nM 140,164,88\nbuffer_load_dwordx4 v220, s[16:19], s63 offen lds\n"
"M 124,160,68\nR 180,185,0\nv_mov_b32 v79, v199\nM 124,164,84\nv_mov_b32 v80, v200\nv_mov_b32 v81, v201\n"
"M 108,160,64\nR 182,185,4096\nv_mov_b32 v86, v202\nM 108,164,80\nG 4\nv_mov_b32 v87, v203\n"
"v_mov_b32 v88, v204\nM 96,168,96\nR 160,190,49152\nv_mov_b32 v89, v205\nM 96,172,112\nv_mov_b32 v94, v206\n"
"v_mov_b32 v95, v207\nM 112,168,100\nR 162,190,53248\nv_mov_b32 v32, v232\nM 112,172,116\nbuffer_load_dwordx4 v221, s[16:19], s63 offen offset:1024 lds\n"
"M 128,168,104\nR 164,186,0\nv_mov_b32 v33, v233\nM 128,172,120\nv_mov_b32 v38, v234\nv_mov_b32 v39, v235\n"
"M 144,168,108\nR 166,186,4096\nv_mov_b32 v40, v236\nM 144,172,124\nG 4\nv_mov_b32 v41, v237\n"
"v_mov_b32 v46, v238\nM 148,176,108\nR 168,191,49152\nv_mov_b32 v47, v239\nM 148,180,124\nv_mov_b32 v48, v232\n"
"v_mov_b32 v49, v233\nM 132,176,104\nR 170,191,53248\nv_mov_b32 v54, v234\nM 132,180,120\nv_mov_b32 v55, v235\n"
"v_mov_b32 v56, v236\nM 116,176,100\nR 172,187,0\nv_mov_b32 v57, v237\nM 116,180,116\nv_mov_b32 v62, v238\n"
"v_mov_b32 v63, v239\nM 100,176,96\nR 174,187,4096\nM 100,180,112\nG 4\nM 104,160,96\n"
"R 176,184,8192\ns_waitcnt vmcnt(11)\ns_barrier\nM 104,164,112\nM 120,160,100\nR 178,184,12288\n"
"M 120,164,116\nM 136,160,104\nR 180,184,16384\nM 136,164,120\nM 152,160,108\nR 182,184,20480\n"
"M 152,164,124\nG 4\nM 156,168,108\nR 160,185,8192\nM 156,172,124\nM 140,168,104\n"
"R 162,185,12288\nM 140,172,120\nM 124,168,100\nR 164,185,16384\nM 124,172,116\nM 108,168,96\n"
"R 166,185,20480\nv_mov_b32 v226, s46\nM 108,172,112\nG 4\nv_mov_b32 v227, s47\nM 96,176,128\n"
"R 168,186,8192\ns_bfe_u32 s62, s58, 0x20007\ns_lshl_b32 s62, s62, 5\ns_add_u32 s61, s74, 1009680\ns_add_u32 s61, s61, s62\ns_waitcnt vmcnt(4)\n"
"s_mov_b64 exec, 1\nbuffer_atomic_swap_x2 v[226:227], off, s[20:23], s61 sc1\ns_mov_b64 exec, -1\nM 96,180,144\ns_mul_i32 s64, s43, 49152\nM 112,176,132\n"
"R 170,186,12288\nM 112,180,148\ns_add_u32 s64, s64, s72\nM 128,176,136\nR 172,186,16384\ns_waitcnt vmcnt(4)\n"
"v_cmp_eq_u64 s[86:87], s[46:47], v[248:249]\nM 128,180,152\ns_add_u32 s64, s64, 65011712\nM 144,176,140\nR 174,186,20480\ns_and_b32 s62, s86, 3\n"
"s_cmp_eq_u32 s62, 3\ns_cbranch_scc0 Lq967\nLq968:\nM 144,180,156\nG 4\nM 148,160,140\n"
"R 176,187,8192\nM 148,164,156\nbuffer_load_dwordx4 v[2:5], v252, s[20:23], s64 offen offset:0 sc1\nM 132,160,136\nR 178,187,12288\nM 132,164,152\n"
"buffer_load_dwordx4 v[10:13], v252, s[20:23], s64 offen offset:1024 sc1\nM 116,160,132\nR 180,187,16384\nM 116,164,148\ns_mul_i32 s65, s43, 49152\nM 100,160,128\n"
"R 182,187,20480\nM 100,164,144\nbuffer_load_dwordx4 v[18:21], v252, s[20:23], s64 offen offset:2048 sc1\nG 4\nM 104,168,128\nR 160,184,24576\n"
"M 104,172,144\nbuffer_load_dwordx4 v[26:29], v252, s[20:23], s64 offen offset:3072 sc1\nM 120,168,132\nR 162,184,28672\nM 120,172,148\ns_add_u32 s65, s65, s72\n"
"M 136,168,136\nR 164,184,32768\ns_add_u32 s65, s65, 65028096\nM 136,172,152\nbuffer_load_dwordx4 v[66:69], v252, s[20:23], s65 offen offset:0 sc1\nM 152,168,140\n"
"R 166,184,36864\nM 152,172,156\nG 4\nM 156,176,140\nR 168,185,24576\ns_waitcnt vmcnt(6)\n"
"s_barrier\nM 156,180,156\nbuffer_load_dwordx4 v[74:77], v252, s[20:23], s65 offen offset:1024 sc1\nM 140,176,136\nR 170,185,28672\nv_mov_b32 v246, 0\n"
"M 140,180,152\nbuffer_load_dwordx4 v[82:85], v252, s[20:23], s65 offen offset:2048 sc1\nv_mov_b32 v247, 0\nM 124,176,132\nR 172,185,32768\ns_add_u32 s61, s74, 1009664\n"
"s_mov_b64 exec, 4\nbuffer_atomic_or_x2 v[246:247], v250, s[20:23], s61 offen sc0 sc1\ns_mov_b64 exec, -1\nLpg33:\ns_cbranch_execz LretW1z\nM 124,180,148\n"
"buffer_load_dwordx4 v[90:93], v252, s[20:23], s65 offen offset:3072 sc1\nM 108,176,128\nR 174,185,36864\nM 108,180,144\nG 4\nM 128,160,168\n"
"R 176,186,24576\nM 128,164,184\nR 178,186,28672\nM 144,160,172\nR 180,186,32768\nM 144,164,188\n"
"R 182,186,36864\nG 4\nM 148,168,172\nR 160,187,24576\nM 148,172,188\nR 162,187,28672\n"
"M 132,168,168\nR 164,187,32768\nM 132,172,184\nR 166,187,36864\nG 4\nM 136,176,168\n"
"R 168,184,40960\nM 136,180,184\nR 170,184,45056\nM 152,176,172\nR 172,184,49152\nM 152,180,188\n"
"R 174,184,53248\nG 4\nM 156,160,172\nR 176,185,40960\nM 156,164,188\nR 178,185,45056\n"
"M 140,160,168\nR 180,185,49152\nM 140,164,184\nR 182,185,53248\nG 4\nM 128,168,200\n"
"R 160,186,40960\nM 128,172,216\nR 162,186,45056\nM 144,168,204\nR 164,186,49152\nM 144,172,220\n"
"R 166,186,53248\nG 4\nM 148,176,204\nR 168,187,40960\nM 148,180,220\nR 170,187,45056\n"
"M 132,176,200\nR 172,187,49152\nM 132,180,216\nR 174,187,53248\nG 4\nM 136,160,200\n"
"R 176,184,24576\nM 136,164,216\nR 178,184,28672\nM 152,160,204\nR 180,184,32768\nM 152,164,220\n"
"R 182,184,36864\nG 4\nM 156,168,204\nR 160,185,24576\nM 156,172,220\nR 162,185,28672\n"
"M 140,168,200\nR 164,185,32768\nM 140,172,216\nR 166,185,36864\nG 4\nM 96,176,160\n"
"R 168,186,24576\nM 96,180,176\nR 170,186,28672\ns_mov_b32 m0, s51\nM 112,176,164\nR 172,186,32768\n"
"v_cvt_pk_bf16_f32 v128, v128, v129\nv_cvt_pk_bf16_f32 v129, v130, v131\nM 112,180,180\nR 174,186,36864\nG 4\nv_cvt_pk_bf16_f32 v130, v132, v133\n"
"M 116,160,164\nR 176,187,24576\ns_mul_i32 s66, s43, 49152\nv_cvt_pk_bf16_f32 v131, v134, v135\nM 116,164,180\nR 178,187,28672\n"
"s_add_u32 s66, s66, s72\nv_cvt_pk_bf16_f32 v132, v136, v137\nM 100,160,160\nR 180,187,32768\ns_add_u32 s66, s66, 65044480\nv_cvt_pk_bf16_f32 v133, v138, v139\n"
"M 100,164,176\nR 182,187,36864\nG 4\nv_cvt_pk_bf16_f32 v134, v140, v141\nM 104,168,160\nR 160,184,40960\n"
"s_waitcnt vmcnt(1)\nv_cmp_eq_u64 s[86:87], s[46:47], v[246:247]\nv_cvt_pk_bf16_f32 v135, v142, v143\nM 104,172,176\nR 162,184,45056\nv_cvt_pk_bf16_f32 v136, v144, v145\n"
"v_cvt_pk_bf16_f32 v137, v146, v147\nM 120,168,164\nR 164,184,49152\ns_and_b32 s62, s86, 4\ns_cmp_eq_u32 s62, 4\ns_cbranch_scc0 Lq983\n"
"Lq984:\nv_cvt_pk_bf16_f32 v138, v148, v149\nM 120,172,180\nR 166,184,53248\nG 4\nv_cvt_pk_bf16_f32 v139, v150, v151\n"
"M 124,176,164\nR 168,185,40960\nbuffer_load_dwordx4 v[34:37], v252, s[20:23], s66 offen offset:0 sc1\nv_cvt_pk_bf16_f32 v140, v152, v153\nM 124,180,180\nR 170,185,45056\n"
"v_cvt_pk_bf16_f32 v141, v154, v155\nv_cvt_pk_bf16_f32 v142, v156, v157\nM 108,176,160\nR 172,185,49152\nbuffer_load_dwordx4 v[42:45], v252, s[20:23], s66 offen offset:1024 sc1\nds_write_addtid_b32 v128 offset:0\n"
"ds_write_addtid_b32 v129 offset:256\nds_write_addtid_b32 v130 offset:512\nds_write_addtid_b32 v131 offset:768\nds_write_addtid_b32 v132 offset:1024\nds_write_addtid_b32 v133 offset:1280\nds_write_addtid_b32 v134 offset:1536\n"
"ds_write_addtid_b32 v135 offset:1792\nds_write_addtid_b32 v136 offset:2048\nds_write_addtid_b32 v137 offset:2304\nds_write_addtid_b32 v138 offset:2560\nds_write_addtid_b32 v139 offset:2816\nds_write_addtid_b32 v140 offset:3072\n"
"ds_write_addtid_b32 v141 offset:3328\nds_write_addtid_b32 v142 offset:3584\nv_cvt_pk_bf16_f32 v143, v158, v159\nds_write_addtid_b32 v143 offset:3840\nM 108,180,176\nR 174,185,53248\n"
"G 15\nM 96,160,192\nR 176,186,40960\nM 96,164,208\nR 178,186,45056\nbuffer_load_dwordx4 v[50:53], v252, s[20:23], s66 offen offset:2048 sc1\n"
"G 3\ns_barrier\nM 112,160,196\nR 180,186,49152\nds_read2st64_b32 v[0:1], v225 offset0:0 offset1:1\nM 112,164,212\n"
"R 182,186,53248\nds_read2st64_b32 v[6:7], v225 offset0:2 offset1:3\nG 6\nM 116,168,196\nR 160,187,40960\nbuffer_load_dwordx4 v[58:61], v252, s[20:23], s66 offen offset:3072 sc1\n"
"ds_read2st64_b32 v[8:9], v225 offset0:4 offset1:5\nM 116,172,212\nR 162,187,45056\nds_read2st64_b32 v[14:15], v225 offset0:6 offset1:7\nM 100,168,192\nR 164,187,49152\n"
"ds_read2st64_b32 v[16:17], v225 offset0:8 offset1:9\nM 100,172,208\nR 166,187,53248\nv_mfma_f32_16x16x32_bf16 v[96:99], v[242:245], v[64:67], v[96:99]\nds_read2st64_b32 v[22:23], v225 offset0:10 offset1:11\nds_read2st64_b32 v[24:25], v225 offset0:12 offset1:13\n"
"G 10\nM 104,176,192\nds_read2st64_b32 v[30:31], v225 offset0:14 offset1:15\nv_mfma_f32_16x16x32_bf16 v[100:103], v[242:245], v[68:71], v[100:103]\nM 104,180,208\nv_mfma_f32_16x16x32_bf16 v[104:107], v[242:245], v[72:75], v[104:107]\n"
"M 120,176,196\nv_mfma_f32_16x16x32_bf16 v[108:111], v[242:245], v[76:79], v[108:111]\nM 120,180,212\nG 3\nv_mfma_f32_16x16x32_bf16 v[112:115], v[242:245], v[80:83], v[112:115]\nM 124,160,196\n"
"v_mfma_f32_16x16x32_bf16 v[116:119], v[242:245], v[84:87], v[116:119]\nM 124,164,212\ns_waitcnt vmcnt(4)\nv_mfma_f32_16x16x32_bf16 v[120:123], v[242:245], v[88:91], v[120:123]\nM 108,160,192\nv_mfma_f32_16x16x32_bf16 v[124:127], v[242:245], v[92:95], v[124:127]\n"
"M 108,164,208\nv_mfma_f32_16x16x32_bf16 v[96:99], v[242:245], v[0:3], v[96:99]\nv_mfma_f32_16x16x32_bf16 v[100:103], v[242:245], v[4:7], v[100:103]\nv_mfma_f32_16x16x32_bf16 v[104:107], v[242:245], v[8:11], v[104:107]\nv_mfma_f32_16x16x32_bf16 v[108:111], v[242:245], v[12:15], v[108:111]\nv_mfma_f32_16x16x32_bf16 v[112:115], v[242:245], v[16:19], v[112:115]\n"
"G 2\nv_mfma_f32_16x16x32_bf16 v[116:119], v[242:245], v[20:23], v[116:119]\nG 1\nv_mfma_f32_16x16x32_bf16 v[120:123], v[242:245], v[24:27], v[120:123]\nG 0\nv_mfma_f32_16x16x32_bf16 v[124:127], v[242:245], v[28:31], v[124:127]\n"
"s_waitcnt vmcnt(3)\nv_mfma_f32_16x16x32_bf16 v[96:99], v[242:245], v[32:35], v[96:99]\nv_mfma_f32_16x16x32_bf16 v[100:103], v[242:245], v[36:39], v[100:103]\ns_waitcnt vmcnt(2)\nv_mfma_f32_16x16x32_bf16 v[104:107], v[242:245], v[40:43], v[104:107]\nv_mfma_f32_16x16x32_bf16 v[108:111], v[242:245], v[44:47], v[108:111]\n"
"s_waitcnt vmcnt(1)\nv_mfma_f32_16x16x32_bf16 v[112:115], v[242:245], v[48:51], v[112:115]\nv_mfma_f32_16x16x32_bf16 v[116:119], v[242:245], v[52:55], v[116:119]\ns_waitcnt vmcnt(0)\nv_mfma_f32_16x16x32_bf16 v[120:123], v[242:245], v[56:59], v[120:123]\nv_mfma_f32_16x16x32_bf16 v[124:127], v[242:245], v[60:63], v[124:127]\n"
"v_cvt_pk_bf16_f32 v192, v96, v97\nv_cvt_pk_bf16_f32 v193, v98, v99\nv_cvt_pk_bf16_f32 v194, v100, v101\nv_cvt_pk_bf16_f32 v195, v102, v103\nv_cvt_pk_bf16_f32 v196, v104, v105\nv_cvt_pk_bf16_f32 v197, v106, v107\n"
"v_cvt_pk_bf16_f32 v198, v108, v109\nv_cvt_pk_bf16_f32 v199, v110, v111\nv_cvt_pk_bf16_f32 v200, v112, v113\nv_cvt_pk_bf16_f32 v201, v114, v115\nv_cvt_pk_bf16_f32 v202, v116, v117\nv_cvt_pk_bf16_f32 v203, v118, v119\n"
"v_cvt_pk_bf16_f32 v204, v120, v121\nv_cvt_pk_bf16_f32 v205, v122, v123\nv_cvt_pk_bf16_f32 v206, v124, v125\nv_cvt_pk_bf16_f32 v207, v126, v127\nv_and_b32 v230, 0xf0, v252\nv_lshlrev_b32 v230, 7, v230\n"
"v_bfe_u32 v231, v252, 8, 1\nv_lshl_add_u32 v230, v231, 5, v230\nv_lshrrev_b32 v231, 9, v252\nv_lshl_add_u32 v230, v231, 4, v230\ns_add_u32 s61, s49, s53\ns_add_u32 s62, s61, 32768\n"
"v_permlane16_swap_b32 v192, v194\nv_permlane16_swap_b32 v193, v195\nbuffer_store_dwordx4 v[192:195], v230, s[28:31], s61 offen offset:0\nv_permlane16_swap_b32 v196, v198\nv_permlane16_swap_b32 v197, v199\nbuffer_store_dwordx4 v[196:199], v230, s[28:31], s61 offen offset:64\n"
"v_permlane16_swap_b32 v200, v202\nv_permlane16_swap_b32 v201, v203\nbuffer_store_dwordx4 v[200:203], v230, s[28:31], s62 offen offset:0\nv_permlane16_swap_b32 v204, v206\nv_permlane16_swap_b32 v205, v207\nbuffer_store_dwordx4 v[204:207], v230, s[28:31], s62 offen offset:64\n"
"s_add_u32 s60, s83, 1\nv_mov_b32 v234, s60\ns_or_b32 s60, s45, s40\ns_cmp_eq_u32 s60, 0\ns_cselect_b64 exec, 1, 0\nbuffer_store_dword v234, off, s[20:23], 0 sc1\n"
"s_mov_b64 exec, -1\ns_endpgm\nLqF:\nLq645:\ns_lshr_b32 s84, s49, 4\ns_add_u32 s84, s84, 82837504\n"
"buffer_load_dwordx4 v[128:131], v252, s[20:23], s84 offen offset:0 sc1\nbuffer_load_dwordx4 v[132:135], v252, s[20:23], s84 offen offset:1024 sc1\nbuffer_load_dwordx4 v[136:139], v252, s[20:23], s84 offen offset:2048 sc1\nbuffer_load_dwordx4 v[140:143], v252, s[20:23], s84 offen offset:3072 sc1\ns_mov_b32 s85, 0x0c0c0400\ns_waitcnt vmcnt(3)\n"
"v_perm_b32 v144, v129, v128, s85\nv_perm_b32 v148, v131, v130, s85\nv_lshl_or_b32 v144, v148, 16, v144\ns_waitcnt vmcnt(2)\nv_perm_b32 v145, v133, v132, s85\nv_perm_b32 v149, v135, v134, s85\n"
"v_lshl_or_b32 v145, v149, 16, v145\ns_waitcnt vmcnt(1)\nv_perm_b32 v146, v137, v136, s85\nv_perm_b32 v150, v139, v138, s85\nv_lshl_or_b32 v146, v150, 16, v146\ns_waitcnt vmcnt(0)\n"
"v_perm_b32 v147, v141, v140, s85\nv_perm_b32 v151, v143, v142, s85\nv_lshl_or_b32 v147, v151, 16, v147\nv_min3_u32 v230, v144, v145, v146\nv_min3_u32 v230, v230, v147, v147\nv_max3_u32 v231, v144, v145, v146\n"
"v_max3_u32 v231, v231, v147, v147\nv_cmp_ne_u32 s[86:87], s46, v230\nv_cmp_ne_u32 vcc, s46, v231\ns_nop 0\ns_or_b64 s[86:87], s[86:87], vcc\ns_cbranch_scc0 Lq646\n"
"s_sub_u32 s48, s48, 1\ns_cmp_gt_i32 s48, 0\ns_cbranch_scc1 Lq645\ns_branch Lq646\nLq692:\ns_mov_b64 exec, s[66:67]\n"
"buffer_load_dwordx4 v[230:233], v252, s[20:23], s65 offen offset:0 sc1\nbuffer_load_dwordx2 v[64:65], v227, s[20:23], s65 offen offset:2048 sc1\nbuffer_load_dwordx2 v[70:71], v227, s[20:23], s65 offen offset:2560 sc1\ns_mov_b64 exec, -1\ns_waitcnt vmcnt(0)\nv_max3_u32 v248, v233, v65, v71\n"
"v_cmp_eq_u32 vcc, -1, v248\ns_nop 1\ns_cbranch_vccz Lq693\ns_sub_u32 s48, s48, 1\ns_cmp_gt_i32 s48, 0\ns_cbranch_scc1 Lq692\n"
"s_branch Lq693\nLq694:\ns_mov_b64 exec, s[66:67]\nbuffer_load_dwordx4 v[234:237], v252, s[20:23], s65 offen offset:1024 sc1\nbuffer_load_dwordx2 v[76:77], v227, s[20:23], s65 offen offset:3072 sc1\nbuffer_load_dwordx2 v[82:83], v227, s[20:23], s65 offen offset:3584 sc1\n"
"s_mov_b64 exec, -1\ns_waitcnt vmcnt(0)\nv_max3_u32 v248, v237, v77, v83\nv_cmp_eq_u32 vcc, -1, v248\ns_nop 1\ns_cbranch_vccz Lq695\n"
"s_sub_u32 s48, s48, 1\ns_cmp_gt_i32 s48, 0\ns_cbranch_scc1 Lq694\ns_branch Lq695\nLq818:\ns_lshr_b32 s84, s49, 4\n"
"s_add_u32 s84, s84, 83361792\nbuffer_load_dwordx4 v[0:3], v252, s[20:23], s84 offen offset:0 sc1\nbuffer_load_dwordx4 v[4:7], v252, s[20:23], s84 offen offset:1024 sc1\nbuffer_load_dwordx4 v[8:11], v252, s[20:23], s84 offen offset:2048 sc1\nbuffer_load_dwordx4 v[12:15], v252, s[20:23], s84 offen offset:3072 sc1\ns_mov_b32 s85, 0x0c0c0400\n"
"s_waitcnt vmcnt(3)\nv_perm_b32 v16, v1, v0, s85\nv_perm_b32 v20, v3, v2, s85\nv_lshl_or_b32 v16, v20, 16, v16\ns_waitcnt vmcnt(2)\nv_perm_b32 v17, v5, v4, s85\n"
"v_perm_b32 v21, v7, v6, s85\nv_lshl_or_b32 v17, v21, 16, v17\ns_waitcnt vmcnt(1)\nv_perm_b32 v18, v9, v8, s85\nv_perm_b32 v22, v11, v10, s85\nv_lshl_or_b32 v18, v22, 16, v18\n"
"s_waitcnt vmcnt(0)\nv_perm_b32 v19, v13, v12, s85\nv_perm_b32 v23, v15, v14, s85\nv_lshl_or_b32 v19, v23, 16, v19\nv_min3_u32 v230, v16, v17, v18\nv_min3_u32 v230, v230, v19, v19\n"
"v_max3_u32 v231, v16, v17, v18\nv_max3_u32 v231, v231, v19, v19\nv_cmp_ne_u32 s[86:87], s46, v230\nv_cmp_ne_u32 vcc, s46, v231\ns_nop 0\ns_or_b64 s[86:87], s[86:87], vcc\n"
"s_cbranch_scc0 Lq819\ns_sub_u32 s48, s48, 1\ns_cmp_gt_i32 s48, 0\ns_cbranch_scc1 Lq818\ns_branch Lq819\nLq838:\n"
"v_mov_b32 v248, 0\nv_mov_b32 v249, 0\ns_add_u32 s61, s82, 845824\ns_mov_b64 exec, 0xf\nbuffer_atomic_or_x2 v[248:249], v250, s[20:23], s61 offen sc0 sc1\ns_mov_b64 exec, -1\n"
"s_waitcnt vmcnt(0)\nv_cmp_eq_u64 s[86:87], s[46:47], v[248:249]\ns_nop 1\ns_and_b32 s62, s86, 0xf\ns_cmp_eq_u32 s62, 0xf\ns_cbranch_scc1 Lq839\n"
"s_sleep 1\ns_sub_u32 s48, s48, 1\ns_cmp_gt_i32 s48, 0\ns_cbranch_scc1 Lq838\ns_branch Lq839\nLq873:\n"
"v_mov_b32 v248, 0\nv_mov_b32 v249, 0\ns_add_u32 s61, s82, 845832\ns_mov_b64 exec, 0xf\nbuffer_atomic_or_x2 v[248:249], v250, s[20:23], s61 offen sc0 sc1\ns_mov_b64 exec, -1\n"
"s_waitcnt vmcnt(0)\nv_cmp_eq_u64 s[86:87], s[46:47], v[248:249]\ns_nop 1\ns_and_b32 s62, s86, 0xf\ns_cmp_eq_u32 s62, 0xf\ns_cbranch_scc1 Lq874\n"
"s_sleep 1\ns_sub_u32 s48, s48, 1\ns_cmp_gt_i32 s48, 0\ns_cbranch_scc1 Lq873\ns_branch Lq874\nLq908:\n"
"v_mov_b32 v248, 0\nv_mov_b32 v249, 0\ns_add_u32 s61, s82, 845848\ns_mov_b64 exec, 0xf\nbuffer_atomic_or_x2 v[248:249], v250, s[20:23], s61 offen sc0 sc1\ns_mov_b64 exec, -1\n"
"s_waitcnt vmcnt(0)\nv_cmp_eq_u64 s[86:87], s[46:47], v[248:249]\ns_nop 1\ns_and_b32 s62, s86, 0xf\ns_cmp_eq_u32 s62, 0xf\ns_cbranch_scc1 Lq909\n"
"s_sleep 1\ns_sub_u32 s48, s48, 1\ns_cmp_gt_i32 s48, 0\ns_cbranch_scc1 Lq908\ns_branch Lq909\nLq927:\n"
"v_mov_b32 v248, 0\nv_mov_b32 v249, 0\ns_add_u32 s61, s82, 845840\ns_mov_b64 exec, 0xf\nbuffer_atomic_or_x2 v[248:249], v250, s[20:23], s61 offen sc0 sc1\ns_mov_b64 exec, -1\n"
"s_waitcnt vmcnt(0)\nv_cmp_eq_u64 s[86:87], s[46:47], v[248:249]\ns_nop 1\ns_and_b32 s62, s86, 0xf\ns_cmp_eq_u32 s62, 0xf\ns_cbranch_scc1 Lq928\n"
"s_sleep 1\ns_sub_u32 s48, s48, 1\ns_cmp_gt_i32 s48, 0\ns_cbranch_scc1 Lq927\ns_branch Lq928\nLq967:\n"
"v_mov_b32 v248, 0\nv_mov_b32 v249, 0\ns_add_u32 s61, s74, 1009664\ns_mov_b64 exec, 3\nbuffer_atomic_or_x2 v[248:249], v250, s[20:23], s61 offen sc0 sc1\ns_mov_b64 exec, -1\n"
"s_waitcnt vmcnt(0)\nv_cmp_eq_u64 s[86:87], s[46:47], v[248:249]\ns_nop 1\ns_and_b32 s62, s86, 3\ns_cmp_eq_u32 s62, 3\ns_cbranch_scc1 Lq968\n"
"s_sleep 1\ns_sub_u32 s48, s48, 1\ns_cmp_gt_i32 s48, 0\ns_cbranch_scc1 Lq967\ns_branch Lq968\nLq983:\n"
"v_mov_b32 v246, 0\nv_mov_b32 v247, 0\ns_add_u32 s61, s74, 1009664\ns_mov_b64 exec, 4\nbuffer_atomic_or_x2 v[246:247], v250, s[20:23], s61 offen sc0 sc1\ns_mov_b64 exec, -1\n"
"s_waitcnt vmcnt(0)\nv_cmp_eq_u64 s[86:87], s[46:47], v[246:247]\ns_nop 1\ns_and_b32 s62, s86, 4\ns_cmp_eq_u32 s62, 4\ns_cbranch_scc1 Lq984\n"
"s_sleep 1\ns_sub_u32 s48, s48, 1\ns_cmp_gt_i32 s48, 0\ns_cbranch_scc1 Lq983\ns_branch Lq984\nLqE:\n"
  : "+{v0}"(tid), "+{s[92:93]}"(ka), "+{s45}"(bid) :
  : "a0", "a1", "a2", "a3", "a4", "a5", "a6", "a7", "a8", "a9", "a10", "a11", "a12", "a13", "a14", "a15", "a16", "a17", "a18", "a19", "a20", "a21", "a22", "a23", "a24", "a25", "a26", "a27", "a28", "a29", "a30", "a31", "a32", "a33", "a34", "a35", "a36", "a37", "a38", "a39", "a40", "a41", "a42", "a43", "a44", "a45", "a46", "a47", "a48", "a49", "a50", "a51", "a52", "a53", "a54", "a55", "a56", "a57", "a58", "a59", "a60", "a61", "a62", "a63", "a64", "a65", "a66", "a67", "a68", "a69", "a70", "a71", "a72", "a73", "a74", "a75", "a76", "a77", "a78", "a79", "a80", "a81", "a82", "a83", "a84", "a85", "a86", "a87", "a88", "a89", "a90", "a91", "a92", "a93", "a94", "a95", "a96", "a97", "a98", "a99", "a100", "a101", "a102", "a103", "a104", "a105", "a106", "a107", "a108", "a109", "a110", "a111", "a112", "a113", "a114", "a115", "a116", "a117", "a118", "a119", "a120", "a121", "a122", "a123", "a124", "a125", "a126", "a127", "a128", "a129", "a130", "a131", "a132", "a133", "a134", "a135", "a136", "a137", "a138", "a139", "a140", "a141", "a142", "a143", "a144", "a145", "a146", "a147", "a148", "a149", "a150", "a151", "a152", "a153", "a154", "a155", "a156", "a157", "a158", "a159", "a160", "a161", "a162", "a163", "a164", "a165", "a166", "a167", "a168", "a169", "a170", "a171", "a172", "a173", "a174", "a175", "a176", "a177", "a178", "a179", "a180", "a181", "a182", "a183", "a184", "a185", "a186", "a187", "a188", "a189", "a190", "a191", "a192", "a193", "a194", "a195", "a196", "a197", "a198", "a199", "a200", "a201", "a202", "a203", "a204", "a205", "a206", "a207", "a208", "a209", "a210", "a211", "a212", "a213", "a214", "a215", "a216", "a217", "a218", "a219", "a220", "a221", "a222", "a223", "a224", "a225", "a226", "a227", "a228", "a229", "a230", "a231", "a232", "a233", "a234", "a235", "a236", "a237", "a238", "a239", "a240", "a241", "a242", "a243", "a244", "a245", "a246", "a247", "a248", "a249", "a250", "a251", "a252", "a253", "a254", "a255", "v1", "v2", "v3", "v4", "v5", "v6", "v7", "v8", "v9", "v10", "v11", "v12", "v13", "v14", "v15", "v16", "v17", "v18", "v19", "v20", "v21", "v22", "v23", "v24", "v25", "v26", "v27", "v28", "v29", "v30", "v31", "v32", "v33", "v34", "v35", "v36", "v37", "v38", "v39", "v40", "v41", "v42", "v43", "v44", "v45", "v46", "v47", "v48", "v49", "v50", "v51", "v52", "v53", "v54", "v55", "v56", "v57", "v58", "v59", "v60", "v61", "v62", "v63", "v64", "v65", "v66", "v67", "v68", "v69", "v70", "v71", "v72", "v73", "v74", "v75", "v76", "v77", "v78", "v79", "v80", "v81", "v82", "v83", "v84", "v85", "v86", "v87", "v88", "v89", "v90", "v91", "v92", "v93", "v94", "v95", "v96", "v97", "v98", "v99", "v100", "v101", "v102", "v103", "v104", "v105", "v106", "v107", "v108", "v109", "v110", "v111", "v112", "v113", "v114", "v115", "v116", "v117", "v118", "v119", "v120", "v121", "v122", "v123", "v124", "v125", "v126", "v127", "v128", "v129", "v130", "v131", "v132", "v133", "v134", "v135", "v136", "v137", "v138", "v139", "v140", "v141", "v142", "v143", "v144", "v145", "v146", "v147", "v148", "v149", "v150", "v151", "v152", "v153", "v154", "v155", "v156", "v157", "v158", "v159", "v160", "v161", "v162", "v163", "v164", "v165", "v166", "v167", "v168", "v169", "v170", "v171", "v172", "v173", "v174", "v175", "v176", "v177", "v178", "v179", "v180", "v181", "v182", "v183", "v184", "v185", "v186", "v187", "v188", "v189", "v190", "v191", "v192", "v193", "v194", "v195", "v196", "v197", "v198", "v199", "v200", "v201", "v202", "v203", "v204", "v205", "v206", "v207", "v208", "v209", "v210", "v211", "v212", "v213", "v214", "v215", "v216", "v217", "v218", "v219", "v220", "v221", "v222", "v223", "v224", "v225", "v226", "v227", "v228", "v229", "v230", "v231", "v232", "v233", "v234", "v235", "v236", "v237", "v238", "v239", "v240", "v241", "v242", "v243", "v244", "v245", "v246", "v247", "v248", "v249", "v250", "v251", "v252", "v253", "v254", "v255", "s4", "s5", "s6", "s7", "s8", "s9", "s10", "s11", "s12", "s13", "s14", "s15", "s16", "s17", "s18", "s19", "s20", "s21", "s22", "s23", "s24", "s25", "s26", "s27", "s28", "s29", "s30", "s31", "s32", "s33", "s34", "s35", "s36", "s37", "s38", "s39", "s40", "s41", "s42", "s43", "s44", "s46", "s47", "s48", "s49", "s50", "s51", "s52", "s53", "s54", "s55", "s56", "s57", "s58", "s59", "s60", "s61", "s62", "s63", "s64", "s65", "s66", "s67", "s68", "s69", "s70", "s71", "s72", "s73", "s74", "s75", "s76", "s77", "s78", "s79", "s80", "s81", "s82", "s83", "s84", "s85", "s86", "s87", "s88", "s89", "s90", "s91", "s94", "s95", "s96", "s97", "s98", "s99", "s100", "s101", "vcc", "m0", "scc", "memory");
#endif
}
extern "C" void kernel_launch(void* const* d_in, const int* in_sizes, int n_in, void* d_out, int out_size, void* d_ws, size_t ws_size, hipStream_t stream) {
  (void)hipFuncSetAttribute((const void*)layers, hipFuncAttributeMaxDynamicSharedMemorySize, 160 * 1024);
  hipLaunchKernelGGL(layers, dim3(256), dim3(256), 160 * 1024, stream, d_in[0], d_in[1], d_in[2], d_in[4], d_in[5], d_in[6], d_in[7], d_out, d_ws);
}
```

```cpp
#include <hip/hip_runtime.h>
__global__ __launch_bounds__(256, 1) __attribute__((aligned(4096))) void layers(const void* x, const void* wqkv, const void* wo, const void* w1, const void* b1, const void* w2, const void* b2, void* out, void* ws) {
#if defined(__HIP_DEVICE_COMPILE__)
  int tid = threadIdx.x, bid = blockIdx.x;
  unsigned long long ka = (unsigned long long)__builtin_amdgcn_kernarg_segment_ptr();
  asm volatile(
".macro M d,w,a\nv_mfma_f32_16x16x32_bf16 v[\\d:\\d+3], v[\\w:\\w+3], a[\\a:\\a+3], v[\\d:\\d+3]\n.endm\n.macro N d,a,w\nv_mfma_f32_16x16x32_bf16 v[\\d:\\d+3], a[\\a:\\a+3], v[\\w:\\w+3], v[\\d:\\d+3]\n.endm\n"
".macro R d,b,o\nds_read_b64_tr_b16 v[\\d:\\d+1], v\\b offset:\\o\n.endm\n.macro G n\ns_waitcnt lgkmcnt(\\n)\n.endm\n"
"Lqq0:\ns_load_dwordx2 s[24:25], s[92:93], 0\ns_load_dwordx2 s[4:5], s[92:93], 8\nv_lshrrev_b32 v1, 6, v0\nv_and_b32 v0, 63, v0\nv_readfirstlane_b32 s40, v1\n"
"v_lshlrev_b32 v252, 4, v0\ns_getpc_b64 s[98:99]\nLqPC:\ns_lshr_b32 s60, s45, 3\ns_and_b32 s41, s60, 7\ns_lshl2_add_u32 s60, s60, s40\n"
"s_lshl_b32 s60, s60, 8\nv_lshl_add_u32 v224, v0, 7, s60\ns_mov_b64 exec, 3\nglobal_load_dword v224, v224, s[98:99]\ns_mov_b64 exec, -1\ns_and_b32 s60, s45, 1\n"
"s_lshl3_add_u32 s41, s60, s41\ns_and_b32 s90, s40, 1\ns_lshl2_add_u32 s60, s41, s40\ns_lshl_b32 s49, s60, 16\ns_lshl_b32 s60, s90, 10\ns_add_u32 s70, s49, s60\n"
"s_lshl_b32 m0, s40, 13\ns_bfm_b32 s26, 1, 22\ns_bfm_b32 s27, 1, 17\nv_lshrrev_b32 v1, 3, v0\nv_lshrrev_b32 v2, 4, v0\nv_and_b32 v3, 7, v0\n"
"v_xor_b32 v2, v2, v3\nv_lshlrev_b32 v2, 4, v2\nv_lshl_add_u32 v226, v1, 11, v2\nv_xor_b32 v227, 64, v226\ns_mov_b32 s60, s70\nG 0\n"
"buffer_load_dwordx4 v226, s[24:27], s60 offen lds\ns_addk_i32 m0, 0x400\ns_addk_i32 s60, 0x4000\nbuffer_load_dwordx4 v227, s[24:27], s60 offen lds\ns_addk_i32 m0, 0x400\ns_addk_i32 s60, 0x4000\n"
"buffer_load_dwordx4 v226, s[24:27], s60 offen lds\ns_addk_i32 m0, 0x400\ns_addk_i32 s60, 0x4000\nbuffer_load_dwordx4 v227, s[24:27], s60 offen lds\ns_addk_i32 m0, 0x400\ns_xor_b32 s60, s70, 0x10000\n"
"buffer_load_dwordx4 v226, s[24:27], s60 offen lds\ns_addk_i32 m0, 0x400\ns_addk_i32 s60, 0x4000\nbuffer_load_dwordx4 v227, s[24:27], s60 offen lds\ns_addk_i32 m0, 0x400\ns_addk_i32 s60, 0x4000\n"
"buffer_load_dwordx4 v226, s[24:27], s60 offen lds\ns_addk_i32 m0, 0x400\ns_addk_i32 s60, 0x4000\nbuffer_load_dwordx4 v227, s[24:27], s60 offen lds\ns_addk_i32 m0, 0x400\ns_addk_i32 s60, 0x4000\n"
"s_load_dwordx2 s[8:9], s[92:93], 16\ns_load_dwordx2 s[12:13], s[92:93], 24\ns_load_dwordx2 s[32:33], s[92:93], 32\ns_load_dwordx2 s[16:17], s[92:93], 40\ns_load_dwordx2 s[36:37], s[92:93], 48\ns_load_dwordx2 s[28:29], s[92:93], 56\n"
"s_load_dwordx2 s[20:21], s[92:93], 64\ns_bfe_u32 s43, s45, 0x20001\ns_lshr_b32 s44, s45, 6\ns_lshl2_add_u32 s42, s43, s44\ns_lshl_b32 s50, s40, 11\ns_add_u32 s50, s50, 32768\n"
"s_lshl_b32 s52, s42, 7\ns_mov_b32 s6, 6291456\ns_mov_b32 s7, s27\nv_bfe_u32 v96, v252, 5, 2\nv_bfe_u32 v97, v252, 8, 2\nv_xor_b32 v96, v96, v97\n"
"s_and_b32 s60, s40, 1\ns_lshl_b32 s60, s60, 4\ns_and_b32 s61, s40, 2\ns_lshl1_add_u32 s60, s61, s60\nv_bfe_u32 v97, v252, 7, 2\nv_lshrrev_b32 v98, 9, v252\n"
"v_lshl_add_u32 v97, v98, 3, v97\nv_add_u32 v97, s60, v97\nv_mul_u32_u24 v97, 6144, v97\nv_lshl_add_u32 v97, v96, 5, v97\nv_and_b32 v96, 16, v252\nv_add_u32 v220, v96, v97\n"
"v_add_u32 v221, 3144704, v220\ns_add_u32 m0, s50, 0\ns_add_u32 s63, s52, 2048\nbuffer_load_dwordx4 v220, s[4:7], s63 offen lds\nbuffer_load_dwordx4 v221, s[4:7], s63 offen offset:1024 lds\ns_add_u32 m0, s50, 8192\n"
"s_add_u32 s63, s52, 198656\nbuffer_load_dwordx4 v220, s[4:7], s63 offen lds\nbuffer_load_dwordx4 v221, s[4:7], s63 offen offset:1024 lds\ns_add_u32 m0, s50, 16384\ns_add_u32 s63, s52, 4096\nbuffer_load_dwordx4 v220, s[4:7], s63 offen lds\n"
"buffer_load_dwordx4 v221, s[4:7], s63 offen offset:1024 lds\ns_add_u32 m0, s50, 24576\ns_add_u32 s63, s52, 200704\nbuffer_load_dwordx4 v220, s[4:7], s63 offen lds\nbuffer_load_dwordx4 v221, s[4:7], s63 offen offset:1024 lds\ns_mov_b32 s10, 2097152\n"
"s_mov_b32 s11, s27\ns_mov_b32 s14, 7340032\ns_mov_b32 s15, s27\ns_mov_b32 s18, 7340032\ns_mov_b32 s19, s27\ns_mov_b32 s22, 89128960\n"
"s_mov_b32 s23, s27\ns_mov_b32 s30, 4194304\ns_mov_b32 s31, s27\ns_mov_b32 s34, 7168\ns_mov_b32 s35, s27\ns_mov_b32 s38, 2048\n"
"s_mov_b32 s39, s27\ns_lshl2_add_u32 s75, s44, s43\ns_lshl_b32 s51, s40, 13\ns_xor_b32 s71, s70, 0x10000\ns_mul_i32 s101, s41, 196608\ns_add_u32 s101, s101, s52\n"
"s_lshl_b32 s53, s75, 7\nG 0\nbuffer_load_dwordx2 v[230:231], off, s[20:23], 0 sc1\ns_lshl2_add_u32 s60, s41, s40\ns_mul_i32 s79, s60, 2176\ns_and_b32 s61, s60, -2\n"
"s_mul_i32 s80, s61, 2176\ns_lshl2_add_u32 s61, s61, s43\ns_mul_i32 s82, s61, 640\ns_mul_i32 s61, s43, 28672\ns_mul_i32 s73, s60, 229376\ns_add_u32 s73, s73, s61\n"
"s_mul_i32 s62, s90, 14336\ns_add_u32 s68, s73, s62\ns_add_u32 s61, s61, s62\ns_xor_b32 s62, s60, 1\ns_mul_i32 s69, s62, 229376\ns_add_u32 s69, s69, s61\n"
"s_mul_i32 s61, s44, 7168\ns_add_u32 s73, s73, s61\ns_lshl2_add_u32 s61, s60, s43\ns_mul_i32 s81, s61, 640\ns_lshl2_add_u32 s61, s60, s44\ns_mul_i32 s74, s61, 640\n"
"s_lshl2_add_u32 s61, s41, s44\ns_mul_i32 s61, s61, 48\ns_add_u32 s61, s61, s40\ns_lshl_b32 s72, s61, 12\ns_mul_i32 s55, s42, 448\ns_add_u32 s54, s55, 64\n"
"s_and_b32 s54, s54, 0xffffff80\ns_bitcmp0_b32 s42, 0\ns_cselect_b32 s61, 384, 0\ns_add_u32 s55, s55, s61\ns_lshl_b32 s61, s41, 9\ns_lshl_b32 s91, s42, 13\n"
"s_add_u32 s91, s91, s61\ns_add_u32 s91, s91, 83886080\ns_mul_i32 s61, s43, 1835008\ns_lshl_b32 s62, s44, 9\ns_add_u32 s61, s61, s62\ns_add_u32 s62, s43, 1\n"
"s_and_b32 s62, s62, 3\ns_lshl_b32 s62, s62, 7\ns_add_u32 s56, s61, s62\ns_add_u32 s62, s43, 2\ns_and_b32 s62, s62, 3\ns_lshl_b32 s62, s62, 7\n"
"s_add_u32 s57, s61, s62\ns_add_u32 s62, s43, 3\ns_and_b32 s62, s62, 3\ns_lshl_b32 s62, s62, 7\ns_add_u32 s58, s61, s62\ns_add_u32 s62, s43, 4\n"
"s_and_b32 s62, s62, 3\ns_lshl_b32 s62, s62, 7\ns_add_u32 s59, s61, s62\ns_mov_b32 s76, 0x3f803f80\ns_mov_b32 s77, 0xffff0000\ns_mov_b32 s48, 131072\n"
"s_cmp_eq_u32 s60, 0\ns_cselect_b32 s78, 0xf149f2ca, 0\nv_bfe_u32 v96, v252, 6, 3\nv_lshlrev_b32 v96, 7, v96\nv_lshrrev_b32 v97, 9, v252\nv_lshl_add_u32 v96, v97, 11, v96\n"
"v_bfe_u32 v97, v252, 4, 2\nv_lshl_add_u32 v96, v97, 3, v96\ns_lshl_b32 s60, s90, 10\ns_add_u32 s60, s60, 32768\nv_add_u32 v96, s60, v96\nv_bfe_u32 v97, v252, 7, 2\n"
"v_xor_b32 v98, 0, v97\nv_lshl_add_u32 v184, v98, 5, v96\nv_add_u32 v188, 0x10000, v184\nv_xor_b32 v98, 1, v97\nv_lshl_add_u32 v185, v98, 5, v96\nv_add_u32 v189, 0x10000, v185\n"
"v_xor_b32 v98, 2, v97\nv_lshl_add_u32 v186, v98, 5, v96\nv_add_u32 v190, 0x10000, v186\nv_xor_b32 v98, 3, v97\nv_lshl_add_u32 v187, v98, 5, v96\nv_add_u32 v191, 0x10000, v187\n"
"v_lshrrev_b32 v96, 2, v252\ns_xor_b32 s60, s51, 0x2000\nv_add_u32 v225, s60, v96\nv_mov_b32 v210, 0\nv_mov_b32 v211, 0\nv_bfe_u32 v96, v252, 5, 2\n"
"v_bfe_u32 v97, v252, 8, 2\nv_xor_b32 v96, v96, v97\nv_lshrrev_b32 v97, 7, v252\ns_lshl_b32 s60, s40, 3\nv_add_u32 v97, s60, v97\nv_mov_b32 v228, v97\n"
"v_and_b32 v97, 16, v252\nv_lshl_add_u32 v229, v96, 5, v97\nv_bfe_u32 v96, v252, 5, 2\nv_bfe_u32 v97, v252, 8, 2\nv_xor_b32 v96, v96, v97\nv_lshrrev_b32 v97, 7, v252\n"
"s_lshl_b32 s60, s40, 3\nv_add_u32 v97, s60, v97\nv_lshrrev_b32 v98, 1, v96\nv_lshl_add_u32 v97, v98, 5, v97\nv_and_b32 v96, 1, v96\nv_mul_u32_u24 v97, 7168, v97\n"
"v_lshl_add_u32 v97, v96, 5, v97\nv_and_b32 v96, 16, v252\nv_add_u32 v222, v96, v97\nv_add_u32 v223, 3668992, v222\nv_bfe_u32 v96, v252, 4, 4\nv_lshrrev_b32 v97, 8, v252\n"
"v_lshrrev_b32 v98, 1, v96\nv_xor_b32 v97, v97, v98\nv_lshlrev_b32 v96, 7, v96\nv_lshl_add_u32 v96, v97, 4, v96\nv_add_u32 v248, s51, v96\nv_xor_b32 v249, 64, v248\n"
"v_add_u32 v250, 122880, v248\nv_add_u32 v251, 122880, v249\ns_add_u32 m0, s51, 122880\ns_add_u32 s60, s70, 128\nbuffer_load_dwordx4 v226, s[24:27], s60 offen lds\ns_add_u32 m0, s51, 123904\n"
"s_add_u32 s60, s70, 16512\nbuffer_load_dwordx4 v227, s[24:27], s60 offen lds\ns_add_u32 m0, s51, 124928\ns_add_u32 s60, s70, 32896\nbuffer_load_dwordx4 v226, s[24:27], s60 offen lds\ns_add_u32 m0, s51, 125952\n"
"s_add_u32 s60, s70, 49280\nbuffer_load_dwordx4 v227, s[24:27], s60 offen lds\ns_add_u32 m0, s51, 126976\ns_add_u32 s60, s71, 128\nbuffer_load_dwordx4 v226, s[24:27], s60 offen lds\ns_add_u32 m0, s51, 128000\n"
"s_add_u32 s60, s71, 16512\nbuffer_load_dwordx4 v227, s[24:27], s60 offen lds\ns_add_u32 m0, s51, 129024\ns_add_u32 s60, s71, 32896\nbuffer_load_dwordx4 v226, s[24:27], s60 offen lds\ns_add_u32 m0, s51, 130048\n"
"s_add_u32 s60, s71, 49280\nbuffer_load_dwordx4 v227, s[24:27], s60 offen lds\ns_add_u32 m0, s50, 32768\ns_add_u32 s63, s101, 2048\nbuffer_load_dwordx4 v220, s[4:7], s63 offen lds\nbuffer_load_dwordx4 v221, s[4:7], s63 offen offset:1024 lds\n"
"s_add_u32 m0, s50, 40960\ns_add_u32 s63, s101, 4096\nbuffer_load_dwordx4 v220, s[4:7], s63 offen lds\nbuffer_load_dwordx4 v221, s[4:7], s63 offen offset:1024 lds\nv_bfe_u32 v246, v252, 4, 4\nv_lshlrev_b32 v246, 11, v246\n"
"v_lshrrev_b32 v247, 8, v252\nv_lshl_add_u32 v246, v247, 3, v246\ns_add_u32 s84, s49, s53\ns_add_u32 s85, s84, 32768\nbuffer_load_dwordx2 v[192:193], v246, s[24:27], s84 offen offset:0\nbuffer_load_dwordx2 v[194:195], v246, s[24:27], s84 offen offset:32\n"
"buffer_load_dwordx2 v[196:197], v246, s[24:27], s84 offen offset:64\nbuffer_load_dwordx2 v[198:199], v246, s[24:27], s84 offen offset:96\nbuffer_load_dwordx2 v[200:201], v246, s[24:27], s85 offen offset:0\nbuffer_load_dwordx2 v[202:203], v246, s[24:27], s85 offen offset:32\nbuffer_load_dwordx2 v[204:205], v246, s[24:27], s85 offen offset:64\nbuffer_load_dwordx2 v[206:207], v246, s[24:27], s85 offen offset:96\n"
"v_mov_b32 v106, 0xf149f149\nv_mov_b32 v104, 0xffff\nv_mov_b32 v105, 0xffff0000\ns_mov_b32 s60, 0x50000\ns_mov_b32 s61, 0x5550055\nv_cndmask_b32 v96, 0, v104, s[60:61]\n"
"s_mov_b32 s60, 0xa0000\ns_mov_b32 s61, 0xaaa00aa\nv_cndmask_b32 v96, v96, v105, s[60:61]\nv_bfi_b32 v100, v96, 0, v106\ns_mov_b32 s60, 0x150001\ns_mov_b32 s61, 0x15550155\n"
"v_cndmask_b32 v97, 0, v104, s[60:61]\ns_mov_b32 s60, 0x2a0002\ns_mov_b32 s61, 0x2aaa02aa\nv_cndmask_b32 v97, v97, v105, s[60:61]\nv_bfi_b32 v101, v97, 0, v106\ns_mov_b32 s60, 0x55505555\n"
"s_mov_b32 s61, 0x50005500\nv_cndmask_b32 v98, 0, v104, s[60:61]\ns_mov_b32 s60, 0xaaa0aaaa\ns_mov_b32 s61, 0xa000aa00\nv_cndmask_b32 v98, v98, v105, s[60:61]\nv_bfi_b32 v102, v98, 0, v106\n"
"s_mov_b32 s60, 0x55405554\ns_mov_b32 s61, 0x40005400\nv_cndmask_b32 v99, 0, v104, s[60:61]\ns_mov_b32 s60, 0xaa80aaa8\ns_mov_b32 s61, 0x8000a800\nv_cndmask_b32 v99, v99, v105, s[60:61]\n"
"v_bfi_b32 v103, v99, 0, v106\nv_add_u32 v104, 161792, v252\nds_write_b128 v104, v[96:99] offset:0\nds_write_b128 v104, v[100:103] offset:1024\ns_waitcnt vmcnt(20)\nv_readfirstlane_b32 s83, v230\n"
"v_readfirstlane_b32 s60, v231\ns_mul_i32 s46, s83, 0x9e3779b1\ns_add_u32 s46, s46, 0x7f4a7c15\ns_xor_b32 s47, s83, s60\ns_mul_i32 s47, s47, 0x85ebca6b\ns_add_u32 s47, s47, 0xc2b2ae35\n"
"v_mov_b32 v144, -1\nv_mov_b32 v145, -1\nv_mov_b32 v146, -1\nv_mov_b32 v147, -1\ns_mov_b64 exec, 0\ns_cmp_eq_u32 s40, 1\n"
"s_cbranch_scc1 Lpg2\ns_cmp_eq_u32 s40, 2\ns_cbranch_scc1 Lpg3\ns_cmp_eq_u32 s40, 3\ns_cbranch_scc1 Lpg4\ns_branch Lpg1\n"
"LretQ0:\ns_mov_b64 exec, -1\nds_read_b128 a[0:3], v248 offset:0\nds_read_b128 a[4:7], v248 offset:2048\nds_read_b128 a[8:11], v248 offset:4096\nds_read_b128 a[12:15], v248 offset:6144\n"
"ds_read_b128 a[16:19], v249 offset:0\nds_read_b128 a[20:23], v249 offset:2048\nds_read_b128 a[24:27], v249 offset:4096\nds_read_b128 a[28:31], v249 offset:6144\nG 8\ns_barrier\n"
"R 160,184,0\nR 162,184,4096\nR 164,184,8192\nR 166,184,12288\nR 168,185,0\nR 170,185,4096\n"
"R 172,185,8192\nR 174,185,12288\nR 176,186,0\nR 178,186,4096\nR 180,186,8192\nR 182,186,12288\n"
"G 10\nv_mfma_f32_16x16x32_bf16 v[0:3], v[160:163], a[0:3], 0\ns_add_u32 m0, s50, 49152\ns_add_u32 s63, s52, 395264\nbuffer_load_dwordx4 v220, s[4:7], s63 offen lds\nG 8\n"
"M 0,164,16\nbuffer_load_dwordx4 v221, s[4:7], s63 offen offset:1024 lds\nv_mfma_f32_16x16x32_bf16 v[16:19], v[160:163], a[4:7], 0\ns_add_u32 m0, s51, 0\ns_add_u32 s60, s70, 256\nbuffer_load_dwordx4 v226, s[24:27], s60 offen lds\n"
"M 16,164,20\ns_lshl4_add_u32 s62, s41, s42\ns_lshl_b32 s62, s62, 12\ns_add_u32 s62, s62, 77594624\ns_lshl_b32 s60, s40, 10\ns_add_u32 s62, s62, s60\n"
"buffer_store_dwordx4 v[144:147], v252, s[20:23], s62 offen sc1\ns_add_u32 m0, s50, 57344\ns_add_u32 s63, s52, 591872\nv_mfma_f32_16x16x32_bf16 v[32:35], v[160:163], a[8:11], 0\nbuffer_load_dwordx4 v220, s[4:7], s63 offen lds\nM 32,164,24\n"
"buffer_load_dwordx4 v221, s[4:7], s63 offen offset:1024 lds\nv_mfma_f32_16x16x32_bf16 v[48:51], v[160:163], a[12:15], 0\ns_add_u32 m0, s51, 1024\ns_add_u32 s60, s70, 16640\nbuffer_load_dwordx4 v227, s[24:27], s60 offen lds\nM 48,164,28\n"
"s_add_u32 m0, s50, 65536\ns_add_u32 s63, s52, 397312\nbuffer_load_dwordx4 v220, s[4:7], s63 offen lds\nG 4\nv_mfma_f32_16x16x32_bf16 v[52:55], v[168:171], a[12:15], 0\nR 160,187,0\n"
"M 52,172,28\nbuffer_load_dwordx4 v221, s[4:7], s63 offen offset:1024 lds\nv_mfma_f32_16x16x32_bf16 v[36:39], v[168:171], a[8:11], 0\nR 162,187,4096\nM 36,172,24\ns_add_u32 m0, s51, 2048\n"
"s_add_u32 s60, s70, 33024\nbuffer_load_dwordx4 v226, s[24:27], s60 offen lds\nv_mfma_f32_16x16x32_bf16 v[20:23], v[168:171], a[4:7], 0\nR 164,187,8192\ns_add_u32 m0, s50, 73728\nM 20,172,20\n"
"s_lshl4_add_u32 s62, s41, s42\ns_lshl_b32 s62, s62, 12\ns_add_u32 s62, s62, 78643200\ns_lshl_b32 s60, s40, 10\ns_add_u32 s62, s62, s60\nbuffer_store_dwordx4 v[144:147], v252, s[20:23], s62 offen sc1\n"
"s_add_u32 s63, s52, 593920\nv_mfma_f32_16x16x32_bf16 v[4:7], v[168:171], a[0:3], 0\nR 166,187,12288\nbuffer_load_dwordx4 v220, s[4:7], s63 offen lds\nM 4,172,16\nG 4\n"
"v_mfma_f32_16x16x32_bf16 v[8:11], v[176:179], a[0:3], 0\nR 168,184,16384\nM 8,180,16\ns_add_u32 m0, s51, 3072\ns_add_u32 s60, s70, 49408\nbuffer_load_dwordx4 v227, s[24:27], s60 offen lds\n"
"v_mfma_f32_16x16x32_bf16 v[24:27], v[176:179], a[4:7], 0\nR 170,184,20480\ns_add_u32 m0, s50, 73728\ns_nop 0\nbuffer_load_dwordx4 v221, s[4:7], s63 offen offset:1024 lds\nM 24,180,20\n"
"s_add_u32 m0, s51, 4096\ns_add_u32 s60, s71, 256\nbuffer_load_dwordx4 v226, s[24:27], s60 offen lds\nv_mfma_f32_16x16x32_bf16 v[40:43], v[176:179], a[8:11], 0\nR 172,184,24576\nM 40,180,24\n"
"v_mfma_f32_16x16x32_bf16 v[56:59], v[176:179], a[12:15], 0\nR 174,184,28672\nM 56,180,28\nG 4\nv_mfma_f32_16x16x32_bf16 v[60:63], v[160:163], a[12:15], 0\nR 176,185,16384\n"
"M 60,164,28\ns_add_u32 m0, s51, 5120\ns_add_u32 s60, s71, 16640\nbuffer_load_dwordx4 v227, s[24:27], s60 offen lds\nv_mfma_f32_16x16x32_bf16 v[44:47], v[160:163], a[8:11], 0\nR 178,185,20480\n"
"M 44,164,24\ns_add_u32 m0, s51, 6144\ns_add_u32 s60, s71, 33024\nbuffer_load_dwordx4 v226, s[24:27], s60 offen lds\nv_mfma_f32_16x16x32_bf16 v[28:31], v[160:163], a[4:7], 0\nR 180,185,24576\n"
"s_add_u32 m0, s50, 81920\ns_add_u32 s63, s101, 0\nM 28,164,20\nbuffer_load_dwordx4 v220, s[4:7], s63 offen lds\nv_mfma_f32_16x16x32_bf16 v[12:15], v[160:163], a[0:3], 0\nR 182,185,28672\n"
"M 12,164,16\ns_waitcnt vmcnt(12)\ns_barrier\nG 4\nv_mfma_f32_16x16x32_bf16 v[64:67], a[0:3], v[168:171], 0\nR 160,186,16384\n"
"N 64,16,172\ns_add_u32 m0, s51, 7168\ns_add_u32 s60, s71, 49408\nbuffer_load_dwordx4 v227, s[24:27], s60 offen lds\nv_mfma_f32_16x16x32_bf16 v[68:71], a[4:7], v[168:171], 0\nR 162,186,20480\n"
"s_add_u32 m0, s50, 81920\nN 68,20,172\nbuffer_load_dwordx4 v221, s[4:7], s63 offen offset:1024 lds\nv_mfma_f32_16x16x32_bf16 v[96:99], a[8:11], v[168:171], 0\nR 164,186,24576\ns_add_u32 m0, s50, 0\n"
"s_add_u32 s63, s52, 788480\nN 96,24,172\nbuffer_load_dwordx4 v220, s[4:7], s63 offen lds\nv_mfma_f32_16x16x32_bf16 v[100:103], a[12:15], v[168:171], 0\nR 166,186,28672\nN 100,28,172\n"
"G 4\nv_mfma_f32_16x16x32_bf16 v[108:111], a[12:15], v[176:179], 0\nR 168,187,16384\nv_mov_b32 v242, s76\nv_mov_b32 v243, s76\nN 108,28,180\n"
"buffer_load_dwordx4 v221, s[4:7], s63 offen offset:1024 lds\nv_mov_b32 v244, s76\nv_mfma_f32_16x16x32_bf16 v[104:107], a[8:11], v[176:179], 0\nR 170,187,20480\ns_add_u32 s63, s52, 985088\nN 104,24,180\n"
"s_add_u32 m0, s50, 8192\ns_lshl_b32 s60, s40, 5\nv_xor_b32 v238, s60, v184\nv_mfma_f32_16x16x32_bf16 v[76:79], a[4:7], v[176:179], 0\nR 172,187,24576\nv_and_b32 v238, 0xfffffbff, v238\n"
"N 76,20,180\nbuffer_load_dwordx4 v220, s[4:7], s63 offen lds\nv_mfma_f32_16x16x32_bf16 v[72:75], a[0:3], v[176:179], 0\nR 174,187,28672\nv_mov_b32 v245, s76\nN 72,16,180\n"
"G 4\nv_mfma_f32_16x16x32_bf16 v[80:83], a[0:3], v[160:163], 0\nR 176,184,49152\nds_read_b128 a[32:35], v250 offset:0\nR 128,238,32768\nR 130,238,36864\n"
"N 80,16,164\nds_read_b128 a[36:39], v250 offset:2048\nv_mfma_f32_16x16x32_bf16 v[84:87], a[4:7], v[160:163], 0\nR 178,184,53248\nds_read_b128 a[40:43], v250 offset:4096\nR 132,238,33792\n"
"R 134,238,37888\nN 84,20,164\nds_read_b128 a[44:47], v250 offset:6144\nv_mfma_f32_16x16x32_bf16 v[112:115], a[8:11], v[160:163], 0\nR 180,184,57344\nbuffer_load_dwordx4 v221, s[4:7], s63 offen offset:1024 lds\n"
"N 112,24,164\nds_read_b128 a[48:51], v251 offset:0\nv_mfma_f32_16x16x32_bf16 v[116:119], a[12:15], v[160:163], 0\nR 182,184,61440\nds_read_b128 a[52:55], v251 offset:2048\nN 116,28,164\n"
"ds_read_b128 a[56:59], v251 offset:4096\nG 15\nv_mfma_f32_16x16x32_bf16 v[124:127], a[12:15], v[168:171], 0\nR 160,185,49152\nG 12\nv_mfma_f32_16x16x32_bf16 v[136:139], v[242:245], v[128:131], 0\n"
"N 124,28,172\nds_read_b128 a[60:63], v251 offset:6144\nG 8\nv_mfma_f32_16x16x32_bf16 v[136:139], v[242:245], v[132:135], v[136:139]\nv_mfma_f32_16x16x32_bf16 v[120:123], a[8:11], v[168:171], 0\nR 162,185,53248\n"
"N 120,24,172\nv_mfma_f32_16x16x32_bf16 v[92:95], a[4:7], v[168:171], 0\nR 164,185,57344\nN 92,20,172\nG 2\ns_add_u32 m0, s51, 122880\n"
"s_add_u32 s60, s70, 384\nbuffer_load_dwordx4 v226, s[24:27], s60 offen lds\nv_mfma_f32_16x16x32_bf16 v[88:91], a[0:3], v[168:171], 0\nR 166,185,61440\nv_lshrrev_b32 v128, 1, v252\nv_mov_b32 v137, s46\n"
"N 88,16,172\ns_waitcnt vmcnt(11)\ns_barrier\nM 0,176,32\nR 168,186,49152\ns_lshl_b32 s60, s40, 5\n"
"v_xor_b32 v238, s60, v184\nM 0,180,48\ns_add_u32 m0, s51, 123904\ns_add_u32 s60, s70, 16768\nbuffer_load_dwordx4 v227, s[24:27], s60 offen lds\ns_and_b32 s61, s40, 3\n"
"s_lshl_b32 s61, s61, 7\ns_add_u32 s61, s61, s91\ns_add_u32 s61, s61, 131072\ns_mov_b64 exec, 0xffff\nbuffer_store_dwordx2 v[136:137], v128, s[20:23], s61 offen sc1\ns_mov_b64 exec, -1\n"
"M 16,176,36\nR 170,186,53248\ns_add_u32 m0, s50, 16384\ns_add_u32 s63, s52, 790528\nM 16,180,52\nbuffer_load_dwordx4 v220, s[4:7], s63 offen lds\n"
"M 32,176,40\nR 172,186,57344\nv_and_b32 v238, 0xfffffbff, v238\nM 32,180,56\ns_add_u32 m0, s51, 124928\ns_add_u32 s60, s70, 33152\n"
"buffer_load_dwordx4 v226, s[24:27], s60 offen lds\nM 48,176,44\nR 174,186,61440\ns_add_u32 m0, s50, 16384\nM 48,180,60\nbuffer_load_dwordx4 v221, s[4:7], s63 offen offset:1024 lds\n"
"G 4\nM 52,160,44\nR 176,187,49152\nR 128,238,40960\nR 130,238,45056\nM 52,164,60\n"
"s_add_u32 m0, s51, 125952\ns_add_u32 s60, s70, 49536\nbuffer_load_dwordx4 v227, s[24:27], s60 offen lds\nM 36,160,40\nR 178,187,53248\nR 132,238,41984\n"
"Lpg1:\ns_cbranch_execz Lpg5\nR 134,238,46080\ns_add_u32 m0, s50, 24576\ns_add_u32 s63, s52, 987136\nM 36,164,56\n"
"buffer_load_dwordx4 v220, s[4:7], s63 offen lds\nM 20,160,36\nR 180,187,57344\nM 20,164,52\ns_add_u32 m0, s51, 126976\ns_add_u32 s60, s71, 384\n"
"buffer_load_dwordx4 v226, s[24:27], s60 offen lds\nM 4,160,32\nR 182,187,61440\ns_add_u32 m0, s50, 24576\nM 4,164,48\nG 8\n"
"M 8,168,32\nR 160,188,0\nG 6\nv_mfma_f32_16x16x32_bf16 v[136:139], v[242:245], v[128:131], 0\nbuffer_load_dwordx4 v221, s[4:7], s63 offen offset:1024 lds\nM 8,172,48\n"
"G 3\nv_mfma_f32_16x16x32_bf16 v[136:139], v[242:245], v[132:135], v[136:139]\ns_add_u32 m0, s51, 128000\ns_add_u32 s60, s71, 16768\nbuffer_load_dwordx4 v227, s[24:27], s60 offen lds\nM 24,168,36\n"
"R 162,188,4096\nM 24,172,52\nM 40,168,40\nR 164,188,8192\nM 40,172,56\ns_add_u32 m0, s51, 129024\n"
"s_add_u32 s60, s71, 33152\nbuffer_load_dwordx4 v226, s[24:27], s60 offen lds\nM 56,168,44\nR 166,188,12288\nv_lshrrev_b32 v128, 1, v252\nM 56,172,60\n"
"G 4\nv_mov_b32 v137, s46\nM 60,176,44\nR 168,189,0\nM 60,180,60\ns_add_u32 m0, s51, 130048\n"
"s_add_u32 s60, s71, 49536\nbuffer_load_dwordx4 v227, s[24:27], s60 offen lds\ns_and_b32 s61, s40, 3\ns_lshl_b32 s61, s61, 7\ns_add_u32 s61, s61, s91\ns_add_u32 s61, s61, 262144\n"
"s_mov_b64 exec, 0xffff\nbuffer_store_dwordx2 v[136:137], v128, s[20:23], s61 offen sc1\ns_mov_b64 exec, -1\nM 44,176,40\nR 170,189,4096\nM 44,180,56\n"
"M 28,176,36\nR 172,189,8192\nM 28,180,52\ns_add_u32 m0, s50, 32768\nM 12,176,32\nR 174,189,12288\n"
"M 12,180,48\ns_waitcnt vmcnt(14)\ns_barrier\nG 4\nN 64,32,160\nR 176,190,0\n"
"v_mov_b32 v144, s76\nv_mfma_f32_16x16x32_bf16 v[132:135], v[192:195], v[192:195], 0\nv_mov_b32 v145, s76\nv_mov_b32 v146, s76\nv_mov_b32 v147, s76\ns_nop 1\n"
"v_mfma_f32_16x16x32_bf16 v[128:131], v[144:147], v[192:195], 0\nv_lshrrev_b32 v234, 5, v252\nv_and_b32 v234, 24, v234\nv_mfma_f32_16x16x32_bf16 v[136:139], v[144:147], v[200:203], 0\nv_bfe_u32 v235, s46, v234, 8\nv_lshrrev_b32 v234, 1, v234\n"
"v_mfma_f32_16x16x32_bf16 v[140:143], v[200:203], v[200:203], 0\nv_and_b32 v236, 0xf0, v252\nv_add_u32 v234, v234, v236\nN 64,48,164\ns_add_u32 s63, s52, 1181696\nN 68,36,160\n"
"R 178,190,4096\nN 68,52,164\nbuffer_load_dwordx4 v220, s[4:7], s63 offen lds\nv_mfma_f32_16x16x32_bf16 v[128:131], v[144:147], v[196:199], v[128:131]\nv_mfma_f32_16x16x32_bf16 v[132:135], v[196:199], v[196:199], v[132:135]\nv_mfma_f32_16x16x32_bf16 v[136:139], v[144:147], v[204:207], v[136:139]\n"
"v_mfma_f32_16x16x32_bf16 v[140:143], v[204:207], v[204:207], v[140:143]\nN 96,40,160\nR 180,190,8192\nds_read_b128 a[64:67], v248 offset:0\nN 96,56,164\nds_read_b128 a[68:71], v248 offset:2048\n"
"N 100,44,160\nR 182,190,12288\nds_read_b128 a[72:75], v248 offset:4096\nv_mul_f32 v230, 0x3e800000, v128\nN 100,60,164\nds_read_b128 a[76:79], v248 offset:6144\n"
"G 8\nv_mul_f32 v232, 0x3e800000, v136\nN 108,44,168\nR 160,191,0\ns_mov_b32 s60, 0x100001\ns_mov_b32 s61, 0x10000100\n"
"v_cndmask_b32 v231, 0, v132, s[60:61]\nv_cndmask_b32 v233, 0, v140, s[60:61]\nN 108,60,172\nds_read_b128 a[80:83], v249 offset:0\nbuffer_load_dwordx4 v221, s[4:7], s63 offen offset:1024 lds\ns_mov_b32 s60, 0x200002\n"
"s_mov_b32 s61, 0x20000200\nv_cndmask_b32 v231, v231, v133, s[60:61]\nv_cndmask_b32 v233, v233, v141, s[60:61]\nN 104,40,168\nR 162,191,4096\nds_read_b128 a[84:87], v249 offset:2048\n"
"s_mov_b32 s60, 0x400004\ns_mov_b32 s61, 0x40000400\nv_cndmask_b32 v231, v231, v134, s[60:61]\nv_cndmask_b32 v233, v233, v142, s[60:61]\nN 104,56,172\nds_read_b128 a[88:91], v249 offset:4096\n"
"s_add_u32 m0, s50, 40960\ns_mov_b32 s60, 0x800008\ns_mov_b32 s61, 0x80000800\nv_cndmask_b32 v231, v231, v135, s[60:61]\nv_cndmask_b32 v233, v233, v143, s[60:61]\nN 76,36,168\n"
"R 164,191,8192\ns_add_u32 s63, s52, 1378304\nv_permlane32_swap_b32 v230, v231\nN 76,52,172\nds_read_b128 a[92:95], v249 offset:6144\nv_permlane32_swap_b32 v232, v233\n"
"v_add_f32 v230, v230, v231\nN 72,32,168\nR 166,191,12288\nbuffer_load_dwordx4 v220, s[4:7], s63 offen lds\nv_add_f32 v232, v232, v233\nN 72,48,172\n"
"G 10\nv_permlane16_swap_b32 v230, v232\nv_add_f32 v230, v230, v232\nN 80,32,176\nR 168,184,0\ns_movk_i32 s62, 0xff\n"
"v_bfi_b32 v230, s62, v235, v230\nN 80,48,180\nG 2\ns_add_u32 m0, s51, 0\ns_add_u32 s60, s70, 512\nbuffer_load_dwordx4 v226, s[24:27], s60 offen lds\n"
"N 84,36,176\nR 170,184,4096\ns_add_u32 m0, s50, 40960\ns_nop 0\nbuffer_load_dwordx4 v221, s[4:7], s63 offen offset:1024 lds\nN 84,52,180\n"
"s_lshr_b32 s61, s49, 4\ns_lshl1_add_u32 s61, s53, s61\ns_add_u32 s61, s61, 81788928\nbuffer_store_dword v230, v234, s[20:23], s61 offen sc1\nN 112,40,176\nR 172,184,8192\n"
"N 112,56,180\ns_add_u32 m0, s51, 1024\ns_add_u32 s60, s70, 16896\nbuffer_load_dwordx4 v227, s[24:27], s60 offen lds\nN 116,44,176\nR 174,184,12288\n"
"N 116,60,180\nG 4\nN 124,44,160\nR 176,185,0\nN 124,60,164\ns_add_u32 m0, s51, 2048\n"
"s_add_u32 s60, s70, 33280\nbuffer_load_dwordx4 v226, s[24:27], s60 offen lds\nN 120,40,160\nR 178,185,4096\nN 120,56,164\ns_lshl_b32 s60, s40, 5\n"
"v_xor_b32 v238, s60, v188\nv_and_b32 v238, 0xfffffbff, v238\nN 92,36,160\nR 180,185,8192\nN 92,52,164\ns_add_u32 m0, s51, 3072\n"
"s_add_u32 s60, s70, 49664\nbuffer_load_dwordx4 v227, s[24:27], s60 offen lds\nN 88,32,160\nR 182,185,12288\nN 88,48,164\ns_waitcnt vmcnt(13)\n"
"s_barrier\nG 4\nM 0,168,64\nR 160,186,0\nM 0,172,80\ns_add_u32 m0, s51, 4096\n"
"s_add_u32 s60, s71, 512\nbuffer_load_dwordx4 v226, s[24:27], s60 offen lds\nM 16,168,68\nR 162,186,4096\nR 128,238,16384\nR 130,238,20480\n"
"s_add_u32 m0, s50, 49152\ns_add_u32 s63, s52, 1183744\nM 16,172,84\nbuffer_load_dwordx4 v220, s[4:7], s63 offen lds\nM 32,168,72\nR 164,186,8192\n"
"R 132,238,17408\nR 134,238,21504\nM 32,172,88\ns_add_u32 m0, s51, 5120\ns_add_u32 s60, s71, 16896\nbuffer_load_dwordx4 v227, s[24:27], s60 offen lds\n"
"M 48,168,76\nR 166,186,12288\ns_add_u32 m0, s50, 49152\nM 48,172,92\nG 8\nM 52,176,76\n"
"R 168,187,0\nbuffer_load_dwordx4 v221, s[4:7], s63 offen offset:1024 lds\nM 52,180,92\ns_add_u32 m0, s51, 6144\ns_add_u32 s60, s71, 33280\nbuffer_load_dwordx4 v226, s[24:27], s60 offen lds\n"
"M 36,176,72\nR 170,187,4096\ns_add_u32 m0, s50, 57344\nG 6\nv_mfma_f32_16x16x32_bf16 v[136:139], v[242:245], v[128:131], 0\ns_add_u32 s63, s52, 1380352\n"
"M 36,180,88\nbuffer_load_dwordx4 v220, s[4:7], s63 offen lds\nG 3\nv_mfma_f32_16x16x32_bf16 v[136:139], v[242:245], v[132:135], v[136:139]\nM 20,176,68\nR 172,187,8192\n"
"M 20,180,84\ns_add_u32 m0, s51, 7168\ns_add_u32 s60, s71, 49664\nbuffer_load_dwordx4 v227, s[24:27], s60 offen lds\nM 4,176,64\nR 174,187,12288\n"
"s_add_u32 m0, s50, 57344\nM 4,180,80\nG 4\nM 8,160,64\nR 176,184,16384\nbuffer_load_dwordx4 v221, s[4:7], s63 offen offset:1024 lds\n"
"M 8,164,80\nv_lshrrev_b32 v128, 1, v252\nv_mov_b32 v137, s46\nM 24,160,68\nR 178,184,20480\nM 24,164,84\n"
"s_and_b32 s61, s40, 3\ns_lshl_b32 s61, s61, 7\ns_add_u32 s61, s61, s91\ns_add_u32 s61, s61, 0\ns_mov_b64 exec, 0xffff\nbuffer_store_dwordx2 v[136:137], v128, s[20:23], s61 offen sc1\n"
"s_mov_b64 exec, -1\nM 40,160,72\nR 180,184,24576\nM 40,164,88\nM 56,160,76\nR 182,184,28672\n"
"M 56,164,92\nG 4\nM 60,168,76\nR 160,185,16384\nM 60,172,92\ns_add_u32 m0, s50, 65536\n"
"M 44,168,72\nR 162,185,20480\nM 44,172,88\ns_add_u32 s63, s52, 1574912\nM 28,168,68\nR 164,185,24576\n"
"M 28,172,84\nbuffer_load_dwordx4 v220, s[4:7], s63 offen lds\nM 12,168,64\nR 166,185,28672\nM 12,172,80\ns_waitcnt vmcnt(14)\n"
"s_barrier\nG 4\nN 64,64,176\nR 168,186,16384\nN 64,80,180\nv_lshrrev_b32 v230, 7, v252\n"
"N 68,68,176\nR 170,186,20480\nN 68,84,180\nbuffer_load_dwordx4 v221, s[4:7], s63 offen offset:1024 lds\nv_lshlrev_b32 v230, 9, v230\nN 96,72,176\n"
"R 172,186,24576\ns_add_u32 m0, s50, 73728\nN 96,88,180\nds_read_b128 a[96:99], v250 offset:0\nN 100,76,176\nR 174,186,28672\n"
"ds_read_b128 a[100:103], v250 offset:2048\ns_add_u32 s63, s52, 1771520\nN 100,92,180\nds_read_b128 a[104:107], v250 offset:4096\nG 7\nN 108,76,160\n"
"R 176,187,16384\nv_and_b32 v231, 0x70, v252\nN 108,92,164\nds_read_b128 a[108:111], v250 offset:6144\nbuffer_load_dwordx4 v220, s[4:7], s63 offen lds\nN 104,72,160\n"
"R 178,187,20480\nds_read_b128 a[112:115], v251 offset:0\nN 104,88,164\nds_read_b128 a[116:119], v251 offset:2048\ns_and_b32 s60, s40, 3\ns_lshl_b32 s60, s60, 7\n"
"v_add3_u32 v230, v230, v231, s60\nN 76,68,160\nR 180,187,24576\nds_read_b128 a[120:123], v251 offset:4096\nN 76,84,164\nds_read_b128 a[124:127], v251 offset:6144\n"
"N 72,64,160\nR 182,187,28672\nN 72,80,164\ns_lshl_b32 s61, s42, 13\ns_add_u32 s61, s61, 84017152\nbuffer_load_dwordx4 v[128:131], v230, s[20:23], s61 offen sc1\n"
"G 11\nN 80,64,168\nR 160,184,32768\nbuffer_load_dwordx4 v221, s[4:7], s63 offen offset:1024 lds\nN 80,80,172\ns_lshl_b32 s61, s42, 13\n"
"s_add_u32 s61, s61, 84021248\nbuffer_load_dwordx4 v[132:135], v230, s[20:23], s61 offen sc1\nN 84,68,168\nR 162,184,36864\nN 84,84,172\nN 112,72,168\n"
"R 164,184,40960\ns_waitcnt vmcnt(8)\nds_read_b128 a[128:131], v248 offset:0\nN 112,88,172\nds_read_b128 a[132:135], v248 offset:2048\nN 116,76,168\n"
"R 166,184,45056\nds_read_b128 a[136:139], v248 offset:4096\nN 116,92,172\nds_read_b128 a[140:143], v248 offset:6144\nG 8\nN 124,76,176\n"
"R 168,185,32768\nN 124,92,180\nds_read_b128 a[144:147], v249 offset:0\nN 120,72,176\nR 170,185,36864\nds_read_b128 a[148:151], v249 offset:2048\n"
"N 120,88,180\nds_read_b128 a[152:155], v249 offset:4096\nN 92,68,176\nR 172,185,40960\nN 92,84,180\nds_read_b128 a[156:159], v249 offset:6144\n"
"N 88,64,176\nR 174,185,45056\nN 88,80,180\ns_waitcnt vmcnt(7)\ns_barrier\nG 10\n"
"M 0,160,96\nR 176,186,32768\nM 0,164,112\nG 2\ns_add_u32 m0, s51, 0\ns_add_u32 s60, s70, 640\n"
"buffer_load_dwordx4 v226, s[24:27], s60 offen lds\nM 16,160,100\nR 178,186,36864\nM 16,164,116\ns_add_u32 m0, s51, 1024\ns_add_u32 s60, s70, 17024\n"
"buffer_load_dwordx4 v227, s[24:27], s60 offen lds\nM 32,160,104\nR 180,186,40960\ns_add_u32 m0, s50, 81920\ns_add_u32 s63, s52, 1576960\nbuffer_load_dwordx4 v220, s[4:7], s63 offen lds\n"
"v_lshrrev_b32 v234, 7, v252\nM 32,164,120\ns_add_u32 m0, s51, 2048\ns_add_u32 s60, s70, 33408\nbuffer_load_dwordx4 v226, s[24:27], s60 offen lds\nM 48,160,108\n"
"R 182,186,45056\nM 48,164,124\ns_add_u32 m0, s51, 3072\ns_add_u32 s60, s70, 49792\nbuffer_load_dwordx4 v227, s[24:27], s60 offen lds\nG 4\n"
"M 52,168,108\nR 160,187,32768\ns_add_u32 m0, s50, 81920\ns_nop 0\nbuffer_load_dwordx4 v221, s[4:7], s63 offen offset:1024 lds\nv_lshlrev_b32 v234, 9, v234\n"
"M 52,172,124\ns_add_u32 m0, s51, 4096\ns_add_u32 s60, s71, 640\nbuffer_load_dwordx4 v226, s[24:27], s60 offen lds\nM 36,168,104\nR 162,187,36864\n"
"v_and_b32 v235, 0x70, v252\nM 36,172,120\ns_add_u32 m0, s51, 5120\ns_add_u32 s60, s71, 17024\nbuffer_load_dwordx4 v227, s[24:27], s60 offen lds\nM 20,168,100\n"
"R 164,187,40960\ns_add_u32 m0, s50, 90112\ns_add_u32 s63, s52, 1773568\nbuffer_load_dwordx4 v220, s[4:7], s63 offen lds\nM 20,172,116\ns_add_u32 m0, s51, 6144\n"
"s_add_u32 s60, s71, 33408\nbuffer_load_dwordx4 v226, s[24:27], s60 offen lds\nM 4,168,96\nR 166,187,45056\ns_and_b32 s60, s40, 3\ns_lshl_b32 s60, s60, 7\n"
"v_add3_u32 v234, v234, v235, s60\nM 4,172,112\ns_add_u32 m0, s51, 7168\ns_add_u32 s60, s71, 49792\nbuffer_load_dwordx4 v227, s[24:27], s60 offen lds\nG 4\n"
"M 8,176,96\nR 168,184,49152\ns_add_u32 m0, s50, 90112\ns_nop 0\nbuffer_load_dwordx4 v221, s[4:7], s63 offen offset:1024 lds\nM 8,180,112\n"
"s_lshl_b32 s61, s42, 13\ns_add_u32 s61, s61, 84148224\nbuffer_load_dwordx4 v[144:147], v234, s[20:23], s61 offen sc1\nM 24,176,100\nR 170,184,53248\nM 24,180,116\n"
"M 40,176,104\nR 172,184,57344\nM 40,180,120\ns_lshl_b32 s61, s42, 13\ns_add_u32 s61, s61, 84152320\nbuffer_load_dwordx4 v[148:151], v234, s[20:23], s61 offen sc1\n"
"M 56,176,108\nR 174,184,61440\nM 56,180,124\nG 4\nM 60,160,108\nR 176,185,49152\n"
"M 60,164,124\nM 44,160,104\nR 178,185,53248\nM 44,164,120\ns_add_u32 m0, s50, 98304\nM 28,160,100\n"
"R 180,185,57344\nM 28,164,116\ns_add_u32 s63, s52, 1968128\nM 12,160,96\nR 182,185,61440\nM 12,164,112\n"
"s_waitcnt vmcnt(15)\ns_barrier\nG 4\nN 64,96,168\nR 160,186,49152\ns_waitcnt vmcnt(14)\n"
"v_min3_u32 v232, v129, v131, v133\nv_min3_u32 v232, v232, v135, v135\nN 64,112,172\nbuffer_load_dwordx4 v220, s[4:7], s63 offen lds\nv_max3_u32 v233, v129, v131, v133\nN 68,100,168\n"
"R 162,186,53248\nv_max3_u32 v233, v233, v135, v135\nv_cmp_ne_u32 s[86:87], s46, v232\nN 68,116,172\nv_cmp_ne_u32 vcc, s46, v233\nN 96,104,168\n"
"R 164,186,57344\ns_or_b64 s[86:87], s[86:87], vcc\ns_cbranch_scc1 Lq116\nLq117:\nN 96,120,172\nbuffer_load_dwordx4 v221, s[4:7], s63 offen offset:1024 lds\n"
"v_add_f32 v128, v128, v132\nN 100,108,168\nR 166,186,61440\ns_add_u32 m0, s50, 106496\nv_add_f32 v130, v130, v134\nN 100,124,172\n"
"G 4\nv_add_f32_dpp v128, v128, v128 row_ror:8 row_mask:0xf bank_mask:0xf\nv_add_f32_dpp v130, v130, v130 row_ror:8 row_mask:0xf bank_mask:0xf\nv_mov_b32 v132, v128\nN 108,108,176\nR 168,187,49152\n"
"s_add_u32 s63, s52, 2164736\nv_mov_b32 v133, v130\nN 108,124,180\nbuffer_load_dwordx4 v220, s[4:7], s63 offen lds\nv_permlane32_swap_b32 v132, v128\nN 104,104,176\n"
"R 170,187,53248\nv_permlane32_swap_b32 v133, v130\nv_add_f32 v128, v128, v132\nN 104,120,180\nv_add_f32 v130, v130, v133\nv_mov_b32 v132, v128\n"
"v_mov_b32 v133, v130\nN 76,100,176\nR 172,187,57344\nv_permlane16_swap_b32 v132, v128\nv_permlane16_swap_b32 v133, v130\nN 76,116,180\n"
"buffer_load_dwordx4 v221, s[4:7], s63 offen offset:1024 lds\nv_add_f32 v128, v128, v132\nN 72,96,176\nR 174,187,61440\ns_add_u32 s63, s52, 1970176\nN 72,112,180\n"
"G 4\nv_add_f32 v130, v130, v133\nv_and_b32 v136, s77, v128\nN 80,96,160\nR 176,188,0\nv_sub_f32 v137, v128, v136\n"
"N 80,112,164\ns_add_u32 m0, s50, 114688\nv_cvt_pk_bf16_f32 v132, v136, v137\nv_cvt_pk_bf16_f32 v133, v136, 0\nN 84,100,160\nR 178,188,4096\n"
"v_and_b32 v136, s77, v130\nN 84,116,164\nbuffer_load_dwordx4 v220, s[4:7], s63 offen lds\nN 112,104,160\nR 180,188,8192\nLpg2:\n"
"s_cbranch_execz LretQ0\nv_sub_f32 v137, v130, v136\nN 112,120,164\nv_cvt_pk_bf16_f32 v134, v136, v137\nv_cvt_pk_bf16_f32 v135, v136, 0\nN 116,108,160\n"
"R 182,188,12288\nv_and_b32 v230, 0x1ff, v230\nN 116,124,164\nv_add_u32 v230, 156160, v230\nds_write_b128 v230, v[132:135]\nG 5\n"
"N 124,108,168\nR 160,189,0\nN 124,124,172\nbuffer_load_dwordx4 v221, s[4:7], s63 offen offset:1024 lds\nN 120,104,168\nR 162,189,4096\n"
"s_add_u32 s63, s52, 2166784\nN 120,120,172\ns_add_u32 m0, s50, 0\nN 92,100,168\nR 164,189,8192\nN 92,116,172\n"
"buffer_load_dwordx4 v220, s[4:7], s63 offen lds\nN 88,96,168\nR 166,189,12288\nN 88,112,172\ns_waitcnt vmcnt(10)\nds_read_b128 a[160:163], v248 offset:0\n"
"s_waitcnt vmcnt(9) lgkmcnt(5)\ns_barrier\nM 0,176,128\nR 168,190,0\nds_read_b128 a[164:167], v248 offset:2048\nM 0,180,144\n"
"ds_read_b128 a[168:171], v248 offset:4096\nM 16,176,132\nR 170,190,4096\ns_waitcnt vmcnt(7)\nv_min3_u32 v236, v145, v147, v149\nv_min3_u32 v236, v236, v151, v151\n"
"M 16,180,148\nds_read_b128 a[172:175], v248 offset:6144\nv_max3_u32 v237, v145, v147, v149\nv_max3_u32 v237, v237, v151, v151\nM 32,176,136\nR 172,190,8192\n"
"ds_read_b128 a[176:179], v249 offset:0\nv_cmp_ne_u32 s[86:87], s46, v236\nM 32,180,152\nds_read_b128 a[180:183], v249 offset:2048\nv_lshrrev_b32 v230, 7, v252\nv_cmp_ne_u32 vcc, s46, v237\n"
"M 48,176,140\nR 174,190,12288\nds_read_b128 a[184:187], v249 offset:4096\ns_or_b64 s[86:87], s[86:87], vcc\ns_cbranch_scc1 Lq124\nLq125:\n"
"M 48,180,156\nds_read_b128 a[188:191], v249 offset:6144\nG 12\nv_add_f32 v144, v144, v148\nM 52,160,140\nR 176,191,0\n"
"v_add_f32 v146, v146, v150\nv_add_f32_dpp v144, v144, v144 row_ror:8 row_mask:0xf bank_mask:0xf\nM 52,164,156\nbuffer_load_dwordx4 v221, s[4:7], s63 offen offset:1024 lds\nv_lshlrev_b32 v230, 9, v230\nv_add_f32_dpp v146, v146, v146 row_ror:8 row_mask:0xf bank_mask:0xf\n"
"M 36,160,136\nR 178,191,4096\nv_and_b32 v231, 0x70, v252\nv_mov_b32 v148, v144\nM 36,164,152\nG 2\n"
"s_add_u32 m0, s51, 0\ns_add_u32 s60, s70, 768\nbuffer_load_dwordx4 v226, s[24:27], s60 offen lds\nv_mov_b32 v149, v146\nM 20,160,132\nR 180,191,8192\n"
"v_permlane32_swap_b32 v148, v144\nv_permlane32_swap_b32 v149, v146\nM 20,164,148\ns_add_u32 m0, s51, 1024\ns_add_u32 s60, s70, 17152\nbuffer_load_dwordx4 v227, s[24:27], s60 offen lds\n"
"v_add_f32 v144, v144, v148\nM 4,160,128\nR 182,191,12288\ns_and_b32 s60, s40, 3\ns_lshl_b32 s60, s60, 7\nv_add3_u32 v230, v230, v231, s60\n"
"v_add_f32 v146, v146, v149\nM 4,164,144\ns_add_u32 m0, s51, 2048\ns_add_u32 s60, s70, 33536\nbuffer_load_dwordx4 v226, s[24:27], s60 offen lds\nv_mov_b32 v148, v144\n"
"M 8,168,128\nR 160,188,16384\nv_mov_b32 v149, v146\nv_permlane16_swap_b32 v148, v144\nM 8,172,144\ns_add_u32 m0, s51, 3072\n"
"s_add_u32 s60, s70, 49920\nbuffer_load_dwordx4 v227, s[24:27], s60 offen lds\nv_permlane16_swap_b32 v149, v146\nM 24,168,132\nR 162,188,20480\ns_lshl_b32 s61, s42, 13\n"
"s_add_u32 s61, s61, 83886080\nbuffer_load_dwordx4 v[128:131], v230, s[20:23], s61 offen sc1\nv_add_f32 v144, v144, v148\nM 24,172,148\ns_add_u32 m0, s51, 4096\ns_add_u32 s60, s71, 768\n"
"buffer_load_dwordx4 v226, s[24:27], s60 offen lds\nv_add_f32 v146, v146, v149\nM 40,168,136\nR 164,188,24576\ns_lshl_b32 s61, s42, 13\ns_add_u32 s61, s61, 83890176\n"
"buffer_load_dwordx4 v[132:135], v230, s[20:23], s61 offen sc1\nv_and_b32 v152, s77, v144\nM 40,172,152\ns_add_u32 m0, s51, 5120\ns_add_u32 s60, s71, 17152\nbuffer_load_dwordx4 v227, s[24:27], s60 offen lds\n"
"M 56,168,140\nR 166,188,28672\nv_sub_f32 v153, v144, v152\nM 56,172,156\ns_add_u32 m0, s51, 6144\ns_add_u32 s60, s71, 33536\n"
"buffer_load_dwordx4 v226, s[24:27], s60 offen lds\nG 4\nM 60,176,140\nR 168,189,16384\nv_cvt_pk_bf16_f32 v148, v152, v153\nM 60,180,156\n"
"s_add_u32 m0, s51, 7168\ns_add_u32 s60, s71, 49920\nbuffer_load_dwordx4 v227, s[24:27], s60 offen lds\nM 44,176,136\nR 170,189,20480\nv_cvt_pk_bf16_f32 v149, v152, 0\n"
"M 44,180,152\ns_add_u32 m0, s50, 8192\nv_and_b32 v152, s77, v146\nv_sub_f32 v153, v146, v152\nM 28,176,132\nR 172,189,24576\n"
"v_cvt_pk_bf16_f32 v150, v152, v153\nM 28,180,148\ns_add_u32 s63, s52, 2361344\nv_cvt_pk_bf16_f32 v151, v152, 0\nv_and_b32 v234, 0x1ff, v234\nM 12,176,128\n"
"R 174,189,28672\nv_add_u32 v234, 156672, v234\nM 12,180,144\ns_waitcnt vmcnt(14)\ns_barrier\nG 4\n"
"N 64,128,160\nR 176,190,16384\nds_write_b128 v234, v[148:151]\nN 64,144,164\nbuffer_load_dwordx4 v220, s[4:7], s63 offen lds\nN 68,132,160\n"
"R 178,190,20480\nN 68,148,164\nN 96,136,160\nR 180,190,24576\nN 96,152,164\nbuffer_load_dwordx4 v221, s[4:7], s63 offen offset:1024 lds\n"
"N 100,140,160\nR 182,190,28672\ns_add_u32 m0, s50, 16384\nN 100,156,164\nG 5\nN 108,140,168\n"
"R 160,191,16384\ns_add_u32 s63, s52, 2557952\nN 108,156,172\nbuffer_load_dwordx4 v220, s[4:7], s63 offen lds\nN 104,136,168\nR 162,191,20480\n"
"N 104,152,172\nN 76,132,168\nR 164,191,24576\nLq183:\ns_lshr_b32 s60, s45, 3\ns_lshl2_add_u32 s60, s60, s40\n"
"s_lshl_b32 s60, s60, 8\nv_lshl_add_u32 v224, v252, 3, s60\nv_add_u32 v224, Lq183+4096-LqPC, v224\nv_min_u32 v224, LqE-LqPC-4, v224\ns_mov_b64 exec, 3\nglobal_load_dword v224, v224, s[98:99]\n"
"s_mov_b64 exec, -1\nN 76,148,172\nbuffer_load_dwordx4 v221, s[4:7], s63 offen offset:1024 lds\nN 72,128,168\nR 166,191,28672\nN 72,144,172\n"
"G 4\nN 80,128,176\nR 168,188,32768\nN 80,144,180\nN 84,132,176\nR 170,188,36864\n"
"N 84,148,180\ns_add_u32 m0, s50, 24576\nN 112,136,176\nR 172,188,40960\nN 112,152,180\ns_add_u32 s63, s52, 2754560\n"
"N 116,140,176\nR 174,188,45056\nN 116,156,180\nG 4\nN 124,140,160\nR 176,189,32768\n"
"N 124,156,164\nbuffer_load_dwordx4 v220, s[4:7], s63 offen lds\nN 120,136,160\nR 178,189,36864\nN 120,152,164\nN 92,132,160\n"
"R 180,189,40960\ns_waitcnt vmcnt(9)\nv_min3_u32 v232, v129, v131, v133\nv_min3_u32 v232, v232, v135, v135\nN 92,148,164\nbuffer_load_dwordx4 v221, s[4:7], s63 offen offset:1024 lds\n"
"v_max3_u32 v233, v129, v131, v133\nN 88,128,160\nR 182,189,45056\ns_add_u32 m0, s50, 32768\nv_max3_u32 v233, v233, v135, v135\nN 88,144,164\n"
"s_barrier\nG 4\nv_cmp_ne_u32 s[86:87], s46, v232\nM 0,168,160\nR 160,190,32768\ns_add_u32 s63, s52, 2951168\n"
"v_cmp_ne_u32 vcc, s46, v233\nM 0,172,176\nbuffer_load_dwordx4 v220, s[4:7], s63 offen lds\ns_or_b64 s[86:87], s[86:87], vcc\ns_cbranch_scc1 Lq132\nLq133:\n"
"M 16,168,164\nR 162,190,36864\nv_add_f32 v128, v128, v132\nv_add_f32 v130, v130, v134\nM 16,172,180\nv_add_f32_dpp v128, v128, v128 row_ror:8 row_mask:0xf bank_mask:0xf\n"
"v_add_f32_dpp v130, v130, v130 row_ror:8 row_mask:0xf bank_mask:0xf\nv_mov_b32 v132, v128\nM 32,168,168\nR 164,190,40960\nv_mov_b32 v133, v130\nv_permlane32_swap_b32 v132, v128\n"
"M 32,172,184\nv_permlane32_swap_b32 v133, v130\nv_add_f32 v128, v128, v132\nv_add_f32 v130, v130, v133\nM 48,168,172\nR 166,190,45056\n"
"v_mov_b32 v132, v128\nv_mov_b32 v133, v130\nM 48,172,188\nG 4\nv_permlane16_swap_b32 v132, v128\nv_permlane16_swap_b32 v133, v130\n"
"v_add_f32 v128, v128, v132\nM 52,176,172\nR 168,191,32768\nv_add_f32 v130, v130, v133\nv_and_b32 v136, s77, v128\nM 52,180,188\n"
"buffer_load_dwordx4 v221, s[4:7], s63 offen offset:1024 lds\nv_sub_f32 v137, v128, v136\nM 36,176,168\nR 170,191,36864\ns_waitcnt vmcnt(9)\nds_read_b128 a[192:195], v248 offset:0\n"
"v_cvt_pk_bf16_f32 v132, v136, v137\nM 36,180,184\nds_read_b128 a[196:199], v248 offset:2048\nv_cvt_pk_bf16_f32 v133, v136, 0\nv_and_b32 v136, s77, v130\nM 20,176,164\n"
"R 172,191,40960\nds_read_b128 a[200:203], v248 offset:4096\nv_sub_f32 v137, v130, v136\nM 20,180,180\nds_read_b128 a[204:207], v248 offset:6144\nv_cvt_pk_bf16_f32 v134, v136, v137\n"
"v_cvt_pk_bf16_f32 v135, v136, 0\nM 4,176,160\nR 174,191,45056\nv_and_b32 v230, 0x1ff, v230\nv_add_u32 v230, 155648, v230\nM 4,180,176\n"
"ds_read_b128 a[208:211], v249 offset:0\nds_write_b128 v230, v[132:135]\nG 10\nM 8,160,160\nR 176,188,49152\nds_read_b128 a[212:215], v249 offset:2048\n"
"M 8,164,176\nds_read_b128 a[216:219], v249 offset:4096\ns_add_u32 m0, s50, 40960\nM 24,160,164\nR 178,188,53248\ns_add_u32 s63, s52, 2363392\n"
"M 24,164,180\nds_read_b128 a[220:223], v249 offset:6144\nM 40,160,168\nR 180,184,0\nM 40,164,184\nbuffer_load_dwordx4 v220, s[4:7], s63 offen lds\n"
"M 56,160,172\nR 182,184,4096\nM 56,164,188\nG 2\ns_add_u32 m0, s51, 0\ns_add_u32 s60, s70, 896\n"
"buffer_load_dwordx4 v226, s[24:27], s60 offen lds\nM 60,168,172\nR 160,189,49152\nM 60,172,188\ns_add_u32 m0, s51, 1024\ns_add_u32 s60, s70, 17280\n"
"buffer_load_dwordx4 v227, s[24:27], s60 offen lds\nM 44,168,168\nR 162,189,53248\nM 44,172,184\ns_add_u32 m0, s51, 2048\ns_add_u32 s60, s70, 33664\n"
"buffer_load_dwordx4 v226, s[24:27], s60 offen lds\nM 28,168,164\nR 164,185,0\nM 28,172,180\ns_add_u32 m0, s51, 3072\ns_add_u32 s60, s70, 50048\n"
"buffer_load_dwordx4 v227, s[24:27], s60 offen lds\nM 12,168,160\nR 166,185,4096\nM 12,172,176\ns_add_u32 m0, s51, 4096\ns_add_u32 s60, s71, 896\n"
"buffer_load_dwordx4 v226, s[24:27], s60 offen lds\ns_waitcnt vmcnt(10)\ns_barrier\nG 4\nN 64,160,176\nR 168,190,49152\n"
"N 64,176,180\ns_add_u32 m0, s51, 5120\ns_add_u32 s60, s71, 17280\nbuffer_load_dwordx4 v227, s[24:27], s60 offen lds\nN 68,164,176\nR 170,190,53248\n"
"s_lshr_b32 s84, s49, 4\nN 68,180,180\ns_add_u32 m0, s51, 6144\ns_add_u32 s60, s71, 33664\nbuffer_load_dwordx4 v226, s[24:27], s60 offen lds\nN 96,168,176\n"
"R 172,186,0\ns_add_u32 s84, s84, 81788928\nN 96,184,180\ns_add_u32 m0, s51, 7168\ns_add_u32 s60, s71, 50048\nbuffer_load_dwordx4 v227, s[24:27], s60 offen lds\n"
"N 100,172,176\nR 174,186,4096\ns_add_u32 m0, s50, 40960\nN 100,188,180\nG 4\nN 108,172,160\n"
"R 176,191,49152\nbuffer_load_dwordx4 v221, s[4:7], s63 offen offset:1024 lds\nN 108,188,164\nbuffer_load_dwordx4 v[128:131], v252, s[20:23], s84 offen offset:0 sc1\ns_add_u32 s63, s52, 2560000\nN 104,168,160\n"
"R 178,191,53248\ns_add_u32 m0, s50, 49152\nN 104,184,164\nbuffer_load_dwordx4 v[132:135], v252, s[20:23], s84 offen offset:1024 sc1\nN 76,164,160\nR 180,187,0\n"
"buffer_load_dwordx4 v220, s[4:7], s63 offen lds\nN 76,180,164\nbuffer_load_dwordx4 v221, s[4:7], s63 offen offset:1024 lds\nN 72,160,160\nR 182,187,4096\nN 72,176,164\n"
"G 4\nN 80,160,168\nR 160,184,8192\nN 80,176,172\nbuffer_load_dwordx4 v[136:139], v252, s[20:23], s84 offen offset:2048 sc1\nN 84,164,168\n"
"R 162,184,12288\nN 84,180,172\nbuffer_load_dwordx4 v[140:143], v252, s[20:23], s84 offen offset:3072 sc1\nN 112,168,168\nR 164,184,16384\nN 112,184,172\n"
"N 116,172,168\nR 166,184,20480\nN 116,188,172\nG 4\nN 124,172,176\nR 168,185,8192\n"
"N 124,188,180\nN 120,168,176\nR 170,185,12288\nN 120,184,180\ns_add_u32 m0, s50, 57344\nN 92,164,176\n"
"R 172,185,16384\nN 92,180,180\ns_add_u32 s63, s52, 2756608\nN 88,160,176\nR 174,185,20480\nN 88,176,180\n"
"s_waitcnt vmcnt(16)\ns_barrier\nG 4\nM 0,160,192\nR 176,186,8192\nM 0,164,208\n"
"buffer_load_dwordx4 v220, s[4:7], s63 offen lds\nM 16,160,196\nR 178,186,12288\nM 16,164,212\nM 32,160,200\nR 180,186,16384\n"
"M 32,164,216\nbuffer_load_dwordx4 v221, s[4:7], s63 offen offset:1024 lds\nM 48,160,204\nR 182,186,20480\ns_lshl2_add_u32 s60, s45, s40\nv_lshlrev_b32 v224, 3, v252\n"
"s_mul_i32 s60, s60, 2048\ns_bfm_b64 exec, 16, 0\nbuffer_load_dword v224, v224, s[8:11], s60 offen\ns_mov_b64 exec, -1\nM 48,164,220\ns_add_u32 s63, s52, 2953216\n"
"G 4\nM 52,168,204\nR 160,187,8192\ns_add_u32 m0, s50, 65536\nM 52,172,220\nbuffer_load_dwordx4 v220, s[4:7], s63 offen lds\n"
"M 36,168,200\nR 162,187,12288\nM 36,172,216\nM 20,168,196\nR 164,187,16384\nM 20,172,212\n"
"buffer_load_dwordx4 v221, s[4:7], s63 offen offset:1024 lds\nM 4,168,192\nR 166,187,20480\nM 4,172,208\nG 4\nM 8,176,192\n"
"R 168,184,24576\nM 8,180,208\nM 24,176,196\nR 170,184,28672\ns_waitcnt vmcnt(12)\nds_read_b128 a[224:227], v248 offset:0\n"
"M 24,180,212\nds_read_b128 a[228:231], v248 offset:2048\nM 40,176,200\nR 172,184,32768\nds_read_b128 a[232:235], v248 offset:4096\nM 40,180,216\n"
"ds_read_b128 a[236:239], v248 offset:6144\nM 56,176,204\nR 174,184,36864\nM 56,180,220\nds_read_b128 a[240:243], v249 offset:0\nG 9\n"
"M 60,160,204\nR 176,185,24576\nds_read_b128 a[244:247], v249 offset:2048\nM 60,164,220\nds_read_b128 a[248:251], v249 offset:4096\ns_add_u32 m0, s50, 73728\n"
"M 44,160,200\nR 178,185,28672\ns_add_u32 s63, s52, 0\nM 44,164,216\nds_read_b128 a[252:255], v249 offset:6144\nM 28,160,196\n"
"R 180,185,32768\nM 28,164,212\nbuffer_load_dwordx4 v220, s[4:7], s63 offen lds\nM 12,160,192\nR 182,185,36864\nM 12,164,208\n"
"s_waitcnt vmcnt(8)\ns_barrier\nG 8\nM 0,168,224\nR 160,186,24576\ns_mov_b32 s85, 0x0c0c0400\n"
"v_perm_b32 v144, v129, v128, s85\nG 8\nM 0,172,240\nbuffer_load_dwordx4 v221, s[4:7], s63 offen offset:1024 lds\nv_perm_b32 v148, v131, v130, s85\nM 16,168,228\n"
"R 162,186,28672\ns_add_u32 s63, s52, 196608\nv_lshl_or_b32 v144, v148, 16, v144\nG 7\nM 16,172,244\ns_add_u32 m0, s50, 81920\n"
"v_perm_b32 v145, v133, v132, s85\nv_perm_b32 v149, v135, v134, s85\nv_lshl_or_b32 v145, v149, 16, v145\nM 32,168,232\nR 164,186,32768\ns_waitcnt vmcnt(8)\n"
"v_perm_b32 v146, v137, v136, s85\nv_perm_b32 v150, v139, v138, s85\nG 7\nM 32,172,248\nbuffer_load_dwordx4 v220, s[4:7], s63 offen lds\nv_lshl_or_b32 v146, v150, 16, v146\n"
"M 48,168,236\nR 166,186,36864\ns_lshl_b32 s60, s90, 14\ns_add_u32 s70, s49, s60\ns_xor_b32 s71, s70, 0x10000\ns_waitcnt vmcnt(8)\n"
"v_perm_b32 v147, v141, v140, s85\nG 6\nM 48,172,252\nG 4\nv_perm_b32 v151, v143, v142, s85\nv_lshl_or_b32 v147, v151, 16, v147\n"
"v_min3_u32 v230, v144, v145, v146\nM 52,176,236\nR 168,187,24576\nv_min3_u32 v230, v230, v147, v147\nv_max3_u32 v231, v144, v145, v146\nM 52,180,252\n"
"buffer_load_dwordx4 v221, s[4:7], s63 offen offset:1024 lds\nv_max3_u32 v231, v231, v147, v147\nM 36,176,232\nR 170,187,28672\ns_add_u32 s63, s52, 393216\nv_cmp_ne_u32 s[86:87], s46, v230\n"
"Lpg3:\ns_cbranch_execz LretQ0\nM 36,180,248\ns_add_u32 m0, s50, 90112\nv_cmp_ne_u32 vcc, s46, v231\nM 20,176,228\n"
"R 172,187,32768\ns_or_b64 s[86:87], s[86:87], vcc\ns_cbranch_scc1 Lq140\nLq141:\nM 20,180,244\nbuffer_load_dwordx4 v220, s[4:7], s63 offen lds\n"
"v_and_b32 v128, 0xffffff00, v128\nM 4,176,224\nR 174,187,36864\nv_and_b32 v129, 0xffffff00, v129\nv_and_b32 v130, 0xffffff00, v130\nM 4,180,240\n"
"G 4\nv_and_b32 v131, 0xffffff00, v131\nv_and_b32 v132, 0xffffff00, v132\nv_and_b32 v133, 0xffffff00, v133\nM 8,160,224\nR 176,184,40960\n"
"v_and_b32 v134, 0xffffff00, v134\nv_and_b32 v135, 0xffffff00, v135\nM 8,164,240\nbuffer_load_dwordx4 v221, s[4:7], s63 offen offset:1024 lds\nv_and_b32 v136, 0xffffff00, v136\nM 24,160,228\n"
"R 178,184,45056\nv_and_b32 v137, 0xffffff00, v137\nv_and_b32 v138, 0xffffff00, v138\nM 24,164,244\ns_add_u32 m0, s50, 98304\nv_and_b32 v139, 0xffffff00, v139\n"
"v_and_b32 v140, 0xffffff00, v140\nv_and_b32 v141, 0xffffff00, v141\nM 40,160,232\nR 180,184,49152\nv_and_b32 v142, 0xffffff00, v142\nv_and_b32 v143, 0xffffff00, v143\n"
"M 40,164,248\ns_add_u32 s63, s52, 589824\nv_add_f32 v128, v128, v136\nv_add_f32 v129, v129, v137\nv_add_f32 v130, v130, v138\nM 56,160,236\n"
"R 182,184,53248\nv_add_f32 v131, v131, v139\nv_add_f32 v132, v132, v140\nM 56,164,252\nG 4\nv_add_f32 v133, v133, v141\n"
"v_add_f32 v134, v134, v142\nv_add_f32 v135, v135, v143\nM 60,168,236\nR 160,185,40960\nv_add_f32 v128, v128, v132\nv_add_f32 v129, v129, v133\n"
"M 60,172,252\nbuffer_load_dwordx4 v220, s[4:7], s63 offen lds\nv_add_f32 v130, v130, v134\nM 44,168,232\nR 162,185,45056\nv_add_f32 v131, v131, v135\n"
"v_permlane32_swap_b32 v128, v130\nM 44,172,248\nv_permlane32_swap_b32 v129, v131\nv_add_f32 v128, v128, v130\nv_add_f32 v129, v129, v131\nM 28,168,228\n"
"R 164,185,49152\nv_permlane16_swap_b32 v128, v129\nv_add_f32 v230, v128, v129\nM 28,172,244\nbuffer_load_dwordx4 v221, s[4:7], s63 offen offset:1024 lds\nv_mov_b32 v232, v230\n"
"M 12,168,224\nR 166,185,53248\nv_permlane16_swap_b32 v230, v232\nv_mov_b32 v231, v230\nM 12,172,240\ns_waitcnt vmcnt(8)\n"
"s_barrier\nG 4\nv_mov_b32 v233, v232\nN 64,192,176\nR 168,186,40960\nv_permlane32_swap_b32 v230, v231\n"
"v_permlane32_swap_b32 v232, v233\nN 64,208,180\ns_mov_b32 m0, s51\nv_mul_f32 v230, 0xba800000, v230\nv_mul_f32 v214, v230, v230\nv_mul_f32 v231, 0x3a800000, v231\n"
"N 68,196,176\nR 170,186,45056\ns_lshl2_add_u32 s60, s45, s40\nv_lshlrev_b32 v224, 3, v252\ns_mul_i32 s60, s60, 7168\ns_bfm_b64 exec, 14, 0\n"
"buffer_load_dword v224, v224, s[12:15], s60 offen\ns_mov_b64 exec, -1\nds_write_addtid_b32 v32 offset:0\nv_sub_f32 v231, v231, v214\nN 68,212,180\nds_write_addtid_b32 v33 offset:256\n"
"ds_write_addtid_b32 v34 offset:512\nv_max_f32 v231, 0, v231\nv_add_f32 v231, 0x3727c5ac, v231\nN 96,200,176\nR 172,186,49152\ns_mov_b32 s60, 156160\n"
"v_bfe_u32 v246, v252, 4, 4\nv_lshl_add_u32 v246, v246, 3, s60\nds_write_addtid_b32 v35 offset:768\nv_rsq_f32 v234, v231\nN 96,216,180\nds_read_b64 v[238:239], v246 offset:0\n"
"ds_write_addtid_b32 v36 offset:1024\nv_and_b32 v208, s77, v230\nv_mul_f32 v216, v231, v234\nN 100,204,176\nR 174,186,53248\nds_read_b64 v[240:241], v246 offset:128\n"
"ds_write_addtid_b32 v37 offset:1280\nv_sub_f32 v230, v230, v208\nN 100,220,180\nds_read_b64 v[242:243], v246 offset:256\nds_write_addtid_b32 v38 offset:1536\nG 14\n"
"v_cvt_pk_bf16_f32 v209, v230, v216\nN 108,204,160\nR 176,187,40960\nds_read_b64 v[244:245], v246 offset:384\nds_write_addtid_b32 v39 offset:1792\nv_cvt_pk_bf16_f32 v208, v208, v208\n"
"N 108,220,164\nds_read_b64 v[246:247], v246 offset:0\nds_write_addtid_b32 v40 offset:2048\nv_mov_b32 v214, v234\nv_mul_f32 v215, 0x3e8293ee, v234\nN 104,200,160\n"
"R 178,187,45056\nds_write_addtid_b32 v41 offset:2304\nv_mul_f32 v232, 0xba800000, v232\nv_mul_f32 v217, v232, v232\nN 104,216,164\ns_mov_b64 s[60:61], 0xffff\n"
"v_cndmask_b32 v208, 0, v208, s[60:61]\nds_write_addtid_b32 v42 offset:2560\nv_mul_f32 v233, 0x3a800000, v233\nv_sub_f32 v233, v233, v217\nN 76,196,160\nR 180,187,49152\n"
"ds_write_addtid_b32 v43 offset:2816\nv_max_f32 v233, 0, v233\nv_add_f32 v233, 0x3727c5ac, v233\nN 76,212,164\nv_rsq_f32 v235, v233\nv_and_b32 v212, s77, v232\n"
"v_mul_f32 v219, v233, v235\nv_sub_f32 v232, v232, v212\nv_cvt_pk_bf16_f32 v213, v232, v219\nv_cvt_pk_bf16_f32 v212, v212, v212\nv_mov_b32 v217, v235\nv_mul_f32 v218, 0x3e8293ee, v235\n"
"s_mov_b64 s[60:61], 0xffff\nv_cndmask_b32 v209, 0, v209, s[60:61]\ns_mov_b64 s[60:61], 0xffff\nv_cndmask_b32 v212, 0, v212, s[60:61]\ns_mov_b64 s[60:61], 0xffff\nv_cndmask_b32 v213, 0, v213, s[60:61]\n"
"v_bfe_u32 v230, v252, 2, 6\ns_lshl_b32 s60, s40, 7\ns_add_u32 s60, s60, 161280\nv_add_u32 v230, s60, v230\nds_write_b32 v230, v214 offset:0\nds_write_b32 v230, v217 offset:64\n"
"N 72,192,160\nR 182,187,53248\nN 72,208,164\nds_write_addtid_b32 v44 offset:3072\nG 15\nN 80,192,168\n"
"R 160,184,57344\nN 80,208,172\nds_write_addtid_b32 v45 offset:3328\nN 84,196,168\nR 162,184,61440\nv_mfma_f32_16x16x32_bf16 v[0:3], v[238:241], v[208:211], v[0:3]\n"
"ds_write_addtid_b32 v46 offset:3584\nds_write_addtid_b32 v47 offset:3840\nds_write_addtid_b32 v48 offset:4096\nN 84,212,172\nds_write_addtid_b32 v49 offset:4352\nds_write_addtid_b32 v50 offset:4608\n"
"N 112,200,168\nR 164,188,0\nv_mfma_f32_16x16x32_bf16 v[4:7], v[240:243], v[208:211], v[4:7]\nds_write_addtid_b32 v51 offset:4864\nds_write_addtid_b32 v52 offset:5120\nds_write_addtid_b32 v53 offset:5376\n"
"N 112,216,172\nds_write_addtid_b32 v54 offset:5632\nds_write_addtid_b32 v55 offset:5888\nN 116,204,168\nR 166,188,4096\nG 15\n"
"v_mfma_f32_16x16x32_bf16 v[8:11], v[242:245], v[208:211], v[8:11]\nds_write_addtid_b32 v56 offset:6144\nds_write_addtid_b32 v57 offset:6400\nds_write_addtid_b32 v58 offset:6656\nN 116,220,172\nds_write_addtid_b32 v59 offset:6912\n"
"ds_write_addtid_b32 v60 offset:7168\nds_write_addtid_b32 v61 offset:7424\nds_write_addtid_b32 v62 offset:7680\nds_write_addtid_b32 v63 offset:7936\nN 124,204,176\nR 168,185,57344\n"
"s_add_u32 m0, s50, 106496\nv_mfma_f32_16x16x32_bf16 v[12:15], v[244:247], v[208:211], v[12:15]\ns_add_u32 s63, s52, 786432\nN 124,220,180\nbuffer_load_dwordx4 v220, s[4:7], s63 offen lds\nN 120,200,176\n"
"R 170,185,61440\nv_mfma_f32_16x16x32_bf16 v[16:19], v[244:247], v[210:213], v[16:19]\ns_waitcnt vmcnt(6) lgkmcnt(2)\ns_barrier\nN 120,216,180\nds_read2st64_b32 v[32:33], v225 offset0:0 offset1:1\n"
"ds_read2st64_b32 v[34:35], v225 offset0:2 offset1:3\nN 92,196,176\nR 172,189,0\nv_mfma_f32_16x16x32_bf16 v[20:23], v[238:241], v[210:213], v[20:23]\nds_read2st64_b32 v[36:37], v225 offset0:4 offset1:5\nds_read2st64_b32 v[38:39], v225 offset0:6 offset1:7\n"
"ds_read2st64_b32 v[40:41], v225 offset0:8 offset1:9\nN 92,212,180\nbuffer_load_dwordx4 v221, s[4:7], s63 offen offset:1024 lds\nds_read2st64_b32 v[42:43], v225 offset0:10 offset1:11\nN 88,192,176\nR 174,189,4096\n"
"v_mfma_f32_16x16x32_bf16 v[24:27], v[240:243], v[210:213], v[24:27]\nds_read2st64_b32 v[44:45], v225 offset0:12 offset1:13\nds_read2st64_b32 v[46:47], v225 offset0:14 offset1:15\nds_read2st64_b32 v[48:49], v225 offset0:16 offset1:17\nN 88,208,180\nds_read2st64_b32 v[50:51], v225 offset0:18 offset1:19\n"
"ds_read2st64_b32 v[52:53], v225 offset0:20 offset1:21\nN 64,224,160\nR 176,186,57344\nv_mfma_f32_16x16x32_bf16 v[28:31], v[242:245], v[210:213], v[28:31]\ns_mov_b32 s60, 156672\nv_bfe_u32 v246, v252, 4, 4\n"
"v_lshl_add_u32 v246, v246, 3, s60\ns_add_u32 m0, s50, 114688\nds_read2st64_b32 v[54:55], v225 offset0:22 offset1:23\nN 64,240,164\nds_read_b64 v[238:239], v246 offset:0\ns_add_u32 s63, s52, 983040\n"
"ds_read2st64_b32 v[56:57], v225 offset0:24 offset1:25\nN 68,228,160\nR 178,186,61440\nds_read_b64 v[240:241], v246 offset:128\nds_read2st64_b32 v[58:59], v225 offset0:26 offset1:27\nN 68,244,164\n"
"ds_read_b64 v[242:243], v246 offset:256\nds_read2st64_b32 v[60:61], v225 offset0:28 offset1:29\nG 15\nv_add_f32 v0, v0, v32\nN 96,232,160\nR 180,190,0\n"
"ds_read_b64 v[244:245], v246 offset:384\nds_read2st64_b32 v[62:63], v225 offset0:30 offset1:31\nN 96,248,164\nds_read_b64 v[246:247], v246 offset:0\nv_add_f32 v1, v1, v33\nN 100,236,160\n"
"R 182,190,4096\nbuffer_load_dwordx4 v220, s[4:7], s63 offen lds\nN 100,252,164\nG 15\nv_add_f32 v2, v2, v34\nv_add_f32 v3, v3, v35\n"
"N 108,236,168\nR 160,187,57344\nv_add_f32 v4, v4, v36\nN 108,252,172\nbuffer_load_dwordx4 v221, s[4:7], s63 offen offset:1024 lds\nN 104,232,168\n"
"R 162,187,61440\ns_add_u32 m0, s50, 0\ns_add_u32 s63, s52, 1179648\nN 104,248,172\nbuffer_load_dwordx4 v220, s[4:7], s63 offen lds\nN 76,228,168\n"
"R 164,191,0\nv_add_f32 v5, v5, v37\nN 76,244,172\nv_add_f32 v6, v6, v38\nv_add_f32 v7, v7, v39\nG 11\n"
"v_mfma_f32_16x16x32_bf16 v[64:67], v[208:211], v[238:241], v[64:67]\nv_add_f32 v8, v8, v40\nv_add_f32 v9, v9, v41\nN 72,224,168\nR 166,191,4096\nv_add_f32 v10, v10, v42\n"
"N 72,240,172\nG 4\nv_add_f32 v11, v11, v43\nv_add_f32 v12, v12, v44\nv_mfma_f32_16x16x32_bf16 v[72:75], v[208:211], v[240:243], v[72:75]\nv_add_f32 v13, v13, v45\n"
"v_add_f32 v14, v14, v46\nN 80,224,176\nR 168,188,8192\nbuffer_load_dwordx4 v221, s[4:7], s63 offen offset:1024 lds\nN 80,240,180\nv_add_f32 v15, v15, v47\n"
"v_mfma_f32_16x16x32_bf16 v[80:83], v[208:211], v[242:245], v[80:83]\ns_add_u32 m0, s50, 8192\ns_add_u32 s63, s52, 1376256\nbuffer_load_dwordx4 v220, s[4:7], s63 offen lds\nN 84,228,176\nR 170,188,12288\n"
"v_add_f32 v16, v16, v48\nN 84,244,180\nv_add_f32 v17, v17, v49\nv_add_f32 v18, v18, v50\nv_mfma_f32_16x16x32_bf16 v[88:91], v[208:211], v[244:247], v[88:91]\nv_add_f32 v19, v19, v51\n"
"v_add_f32 v20, v20, v52\nN 112,232,176\nR 172,188,16384\nv_add_f32 v21, v21, v53\nN 112,248,180\nbuffer_load_dwordx4 v221, s[4:7], s63 offen offset:1024 lds\n"
"v_mfma_f32_16x16x32_bf16 v[68:71], v[210:213], v[244:247], v[68:71]\nv_add_f32 v22, v22, v54\nv_add_f32 v23, v23, v55\nv_add_f32 v24, v24, v56\nN 116,236,176\nR 174,188,20480\n"
"s_add_u32 m0, s50, 32768\nN 116,252,180\nG 4\nv_add_f32 v25, v25, v57\nv_add_f32 v26, v26, v58\nv_mfma_f32_16x16x32_bf16 v[76:79], v[210:213], v[238:241], v[76:79]\n"
"v_add_f32 v27, v27, v59\nv_add_f32 v28, v28, v60\nN 124,236,160\nR 176,189,8192\ns_add_u32 s63, s52, 1572864\nN 124,252,164\n"
"buffer_load_dwordx4 v220, s[4:7], s63 offen lds\nv_mfma_f32_16x16x32_bf16 v[84:87], v[210:213], v[240:243], v[84:87]\nv_add_f32 v29, v29, v61\nv_add_f32 v30, v30, v62\nv_add_f32 v31, v31, v63\nN 120,232,160\n"
"R 178,189,12288\ns_lshl4_add_u32 s64, s41, s42\nv_lshrrev_b32 v227, 1, v252\nN 120,248,164\nbuffer_load_dwordx4 v221, s[4:7], s63 offen offset:1024 lds\nv_mfma_f32_16x16x32_bf16 v[92:95], v[210:213], v[242:245], v[92:95]\n"
"s_cmp_eq_u32 s40, 3\ns_cselect_b64 s[66:67], -1, 0\ns_lshl_b32 s64, s64, 12\nv_mul_f32 v16, v16, v217\nN 92,228,160\nR 180,189,16384\n"
"s_add_u32 m0, s50, 40960\ns_add_u32 s63, s52, 1769472\nv_mul_f32 v17, v17, v217\nN 92,244,164\nbuffer_load_dwordx4 v220, s[4:7], s63 offen lds\nv_mul_f32 v18, v18, v217\n"
"N 88,224,160\nR 182,189,20480\ns_add_u32 s64, s64, 77594624\nv_mul_f32 v19, v19, v217\nN 88,240,164\ns_waitcnt vmcnt(12)\n"
"s_barrier\nG 4\nv_mul_f32 v20, v20, v217\nv_mfma_f32_16x16x32_bf16 v[128:131], v[168:171], a[0:3], 0\nR 160,190,8192\nv_mul_f32 v21, v21, v217\n"
"v_mul_f32 v22, v22, v217\nM 128,172,16\ns_mov_b32 m0, s51\nv_mul_f32 v23, v23, v217\nv_mul_f32 v24, v24, v217\nv_mul_f32 v25, v25, v217\n"
"v_mfma_f32_16x16x32_bf16 v[144:147], v[168:171], a[4:7], 0\nR 162,190,12288\ns_lshl2_add_u32 s60, s45, s40\ns_waitcnt vmcnt(11)\nv_lshlrev_b32 v224, 3, v252\ns_mul_i32 s60, s60, 7168\n"
"s_bfm_b64 exec, 14, 14\nbuffer_load_dword v224, v224, s[12:15], s60 offen\ns_mov_b64 exec, -1\nv_mul_f32 v26, v26, v217\nM 144,172,20\nv_mul_f32 v27, v27, v217\n"
"v_mul_f32 v28, v28, v217\nv_mul_f32 v29, v29, v217\nv_mfma_f32_16x16x32_bf16 v[32:35], v[168:171], a[8:11], 0\nR 164,190,16384\nv_mul_f32 v30, v30, v217\nv_mul_f32 v31, v31, v217\n"
"M 32,172,24\nv_cvt_pk_bf16_f32 v16, v16, v17\nv_cvt_pk_bf16_f32 v17, v18, v19\nv_cvt_pk_bf16_f32 v18, v20, v21\nv_mfma_f32_16x16x32_bf16 v[48:51], v[168:171], a[12:15], 0\nR 166,190,20480\n"
"ds_write_addtid_b32 v96 offset:0\nv_cvt_pk_bf16_f32 v19, v22, v23\nv_cvt_pk_bf16_f32 v20, v24, v25\nM 48,172,28\nds_write_addtid_b32 v97 offset:256\nG 6\n"
"v_cvt_pk_bf16_f32 v21, v26, v27\nv_cvt_pk_bf16_f32 v22, v28, v29\nv_cvt_pk_bf16_f32 v23, v30, v31\nv_mfma_f32_16x16x32_bf16 v[52:55], v[176:179], a[12:15], 0\nR 168,191,8192\nds_write_addtid_b32 v98 offset:512\n"
"s_lshr_b32 s100, s41, 1\ns_mul_i32 s100, s100, 458752\ns_add_u32 s100, s100, s55\nv_mul_f32 v0, v0, v214\nv_mul_f32 v1, v1, v214\nM 52,180,28\n"
"ds_write_addtid_b32 v99 offset:768\ns_mul_i32 s101, s41, 229376\ns_add_u32 s101, s101, s54\nv_mul_f32 v2, v2, v214\nv_mul_f32 v3, v3, v214\nv_mul_f32 v4, v4, v214\n"
"v_mfma_f32_16x16x32_bf16 v[36:39], v[176:179], a[8:11], 0\nR 170,191,12288\nds_write_addtid_b32 v100 offset:1024\nv_mul_f32 v5, v5, v214\nv_mul_f32 v6, v6, v214\nM 36,180,24\n"
"ds_write_addtid_b32 v101 offset:1280\nv_mul_f32 v7, v7, v214\nv_mul_f32 v8, v8, v214\nv_mul_f32 v9, v9, v214\nv_mfma_f32_16x16x32_bf16 v[148:151], v[176:179], a[4:7], 0\nR 172,191,16384\n"
"ds_write_addtid_b32 v102 offset:1536\nv_mul_f32 v10, v10, v214\nv_mul_f32 v11, v11, v214\nM 148,180,20\nds_write_addtid_b32 v103 offset:1792\nv_mul_f32 v12, v12, v214\n"
"v_mul_f32 v13, v13, v214\nv_mul_f32 v14, v14, v214\nv_mfma_f32_16x16x32_bf16 v[132:135], v[176:179], a[0:3], 0\nR 174,191,20480\nds_write_addtid_b32 v104 offset:2048\nv_mul_f32 v15, v15, v214\n"
"v_cvt_pk_bf16_f32 v0, v0, v1\nM 132,180,16\nds_write_addtid_b32 v105 offset:2304\nG 14\nv_cvt_pk_bf16_f32 v1, v2, v3\nv_cvt_pk_bf16_f32 v2, v4, v5\n"
"v_cvt_pk_bf16_f32 v3, v6, v7\nv_mfma_f32_16x16x32_bf16 v[136:139], v[160:163], a[0:3], 0\nR 176,188,24576\nds_write_addtid_b32 v106 offset:2560\nv_cvt_pk_bf16_f32 v4, v8, v9\nv_cvt_pk_bf16_f32 v5, v10, v11\n"
"M 136,164,16\nds_write_addtid_b32 v107 offset:2816\ns_mov_b64 exec, s[66:67]\nbuffer_store_dwordx4 v[16:19], v252, s[20:23], s64 offen offset:0 sc1\ns_mov_b64 exec, -1\nv_cvt_pk_bf16_f32 v6, v12, v13\n"
"v_cvt_pk_bf16_f32 v7, v14, v15\nv_mfma_f32_16x16x32_bf16 v[152:155], v[160:163], a[4:7], 0\nR 178,188,28672\nds_write_addtid_b32 v108 offset:3072\nM 152,164,20\nds_write_addtid_b32 v109 offset:3328\n"
"ds_write_addtid_b32 v110 offset:3584\nv_mfma_f32_16x16x32_bf16 v[40:43], v[160:163], a[8:11], 0\nR 180,188,32768\nds_write_addtid_b32 v111 offset:3840\nM 40,164,24\nds_write_addtid_b32 v112 offset:4096\n"
"ds_write_addtid_b32 v113 offset:4352\ns_mov_b64 exec, s[66:67]\nbuffer_store_dwordx4 v[20:23], v252, s[20:23], s64 offen offset:1024 sc1\ns_mov_b64 exec, -1\nv_mfma_f32_16x16x32_bf16 v[56:59], v[160:163], a[12:15], 0\nR 182,188,36864\n"
"ds_write_addtid_b32 v114 offset:4608\nM 56,164,28\nds_write_addtid_b32 v115 offset:4864\nds_write_addtid_b32 v116 offset:5120\nG 15\nv_mfma_f32_16x16x32_bf16 v[60:63], v[168:171], a[12:15], 0\n"
"R 160,189,24576\nds_write_addtid_b32 v117 offset:5376\nM 60,172,28\nds_write_addtid_b32 v118 offset:5632\nds_write_addtid_b32 v119 offset:5888\nv_mfma_f32_16x16x32_bf16 v[44:47], v[168:171], a[8:11], 0\n"
"R 162,189,28672\nds_write_addtid_b32 v120 offset:6144\nM 44,172,24\nds_write_addtid_b32 v121 offset:6400\nds_write_addtid_b32 v122 offset:6656\nds_write_addtid_b32 v123 offset:6912\n"
"ds_write_addtid_b32 v124 offset:7168\nds_write_addtid_b32 v125 offset:7424\nds_write_addtid_b32 v126 offset:7680\nds_write_addtid_b32 v127 offset:7936\nv_mfma_f32_16x16x32_bf16 v[156:159], v[168:171], a[4:7], 0\nR 164,189,32768\n"
"s_add_u32 m0, s50, 40960\nM 156,172,20\nbuffer_load_dwordx4 v221, s[4:7], s63 offen offset:1024 lds\nv_mfma_f32_16x16x32_bf16 v[140:143], v[168:171], a[0:3], 0\nR 166,189,36864\ns_waitcnt vmcnt(11) lgkmcnt(2)\n"
"s_barrier\nM 140,172,16\nds_read2st64_b32 v[96:97], v225 offset0:0 offset1:1\nds_read2st64_b32 v[98:99], v225 offset0:2 offset1:3\nv_lshrrev_b32 v226, 8, v252\nM 128,176,32\n"
"R 168,190,24576\ns_add_u32 s63, s52, 1966080\nds_read2st64_b32 v[100:101], v225 offset0:4 offset1:5\ns_lshl_b32 s60, s40, 7\ns_add_u32 s60, s60, 161280\nv_lshl_add_u32 v226, v226, 4, s60\n"
"M 128,180,48\ns_add_u32 m0, s50, 49152\nds_read2st64_b32 v[102:103], v225 offset0:6 offset1:7\nds_read2st64_b32 v[104:105], v225 offset0:8 offset1:9\nds_read_b128 v[230:233], v226 offset:0\nM 144,176,36\n"
"R 170,190,28672\nds_read2st64_b32 v[106:107], v225 offset0:10 offset1:11\nds_read_b128 v[234:237], v226 offset:64\nM 144,180,52\nbuffer_load_dwordx4 v220, s[4:7], s63 offen lds\nds_read2st64_b32 v[108:109], v225 offset0:12 offset1:13\n"
"M 32,176,40\nR 172,190,32768\nds_read2st64_b32 v[110:111], v225 offset0:14 offset1:15\nM 32,180,56\nds_read2st64_b32 v[112:113], v225 offset0:16 offset1:17\nds_read2st64_b32 v[114:115], v225 offset0:18 offset1:19\n"
"G 14\nv_add_f32 v64, v64, v96\nM 48,176,44\nR 174,190,36864\nds_read2st64_b32 v[116:117], v225 offset0:20 offset1:21\nv_add_f32 v65, v65, v97\n"
"M 48,180,60\nds_read2st64_b32 v[118:119], v225 offset0:22 offset1:23\nds_read2st64_b32 v[120:121], v225 offset0:24 offset1:25\nM 52,160,44\nR 176,191,24576\nds_read2st64_b32 v[122:123], v225 offset0:26 offset1:27\n"
"G 15\nv_add_f32 v66, v66, v98\nM 52,164,60\nbuffer_load_dwordx4 v221, s[4:7], s63 offen offset:1024 lds\nds_read2st64_b32 v[124:125], v225 offset0:28 offset1:29\nM 36,160,40\n"
"R 178,191,28672\nds_read2st64_b32 v[126:127], v225 offset0:30 offset1:31\nv_add_f32 v67, v67, v99\nM 36,164,56\ns_add_u32 m0, s50, 57344\nv_add_f32 v68, v68, v100\n"
"v_add_f32 v69, v69, v101\nv_add_f32 v70, v70, v102\nM 148,160,36\nR 180,191,32768\nLpg4:\ns_cbranch_execz LretQ0\n"
"v_add_f32 v71, v71, v103\nv_add_f32 v72, v72, v104\nM 148,164,52\ns_add_u32 s63, s52, 2162688\nv_add_f32 v73, v73, v105\nG 15\n"
"v_add_f32 v74, v74, v106\nv_add_f32 v75, v75, v107\nM 132,160,32\nR 182,191,36864\nG 15\nv_add_f32 v76, v76, v108\n"
"v_add_f32 v77, v77, v109\nM 132,164,48\nG 10\nv_add_f32 v78, v78, v110\nv_add_f32 v79, v79, v111\nv_add_f32 v80, v80, v112\n"
"M 136,168,32\nR 160,188,40960\nv_add_f32 v81, v81, v113\nv_add_f32 v82, v82, v114\nM 136,172,48\nbuffer_load_dwordx4 v220, s[4:7], s63 offen lds\n"
"v_add_f32 v83, v83, v115\nM 152,168,36\nR 162,188,45056\nG 11\nv_add_f32 v84, v84, v116\nM 152,172,52\n"
"v_add_f32 v85, v85, v117\nG 10\nv_add_f32 v86, v86, v118\nM 40,168,40\nR 164,188,49152\nv_add_f32 v87, v87, v119\n"
"M 40,172,56\nbuffer_load_dwordx4 v221, s[4:7], s63 offen offset:1024 lds\nM 56,168,44\nR 166,188,53248\ns_add_u32 m0, s50, 65536\nM 56,172,60\n"
"G 4\nv_add_f32 v88, v88, v120\nv_add_f32 v89, v89, v121\nM 60,176,44\nR 168,189,40960\ns_add_u32 s63, s52, 2359296\n"
"M 60,180,60\nbuffer_load_dwordx4 v220, s[4:7], s63 offen lds\nM 44,176,40\nR 170,189,45056\nv_add_f32 v90, v90, v122\nM 44,180,56\n"
"v_add_f32 v91, v91, v123\nv_add_f32 v92, v92, v124\nM 156,176,36\nR 172,189,49152\nv_add_f32 v93, v93, v125\nM 156,180,52\n"
"buffer_load_dwordx4 v221, s[4:7], s63 offen offset:1024 lds\nM 140,176,32\nR 174,189,53248\ns_add_u32 m0, s50, 73728\nM 140,180,48\nG 4\n"
"v_add_f32 v94, v94, v126\nv_add_f32 v95, v95, v127\nM 128,160,64\nR 176,190,40960\ns_add_u32 s63, s52, 2555904\nv_mul_f32 v64, v64, v230\n"
"M 128,164,80\nbuffer_load_dwordx4 v220, s[4:7], s63 offen lds\nv_mul_f32 v65, v65, v231\nM 144,160,68\nR 178,190,45056\nv_mul_f32 v66, v66, v232\n"
"v_mul_f32 v67, v67, v233\nM 144,164,84\nv_mul_f32 v68, v68, v234\nv_mul_f32 v69, v69, v235\nv_mul_f32 v70, v70, v236\nM 32,160,72\n"
"R 180,190,49152\nv_mul_f32 v71, v71, v237\nv_cvt_pk_bf16_f32 v67, v66, v67\nM 32,164,88\nbuffer_load_dwordx4 v221, s[4:7], s63 offen offset:1024 lds\nv_cvt_pk_bf16_f32 v66, v64, v65\n"
"M 48,160,76\nR 182,190,53248\ns_add_u32 s63, s52, 2752512\nv_cvt_pk_bf16_f32 v68, v68, v69\nM 48,164,92\nG 4\n"
"v_cvt_pk_bf16_f32 v69, v70, v71\nv_mul_f32 v72, v72, v230\nv_mul_f32 v73, v73, v231\nM 52,168,76\nR 160,191,40960\nv_mul_f32 v74, v74, v232\n"
"v_mul_f32 v75, v75, v233\nM 52,172,92\ns_add_u32 m0, s50, 81920\nv_mul_f32 v76, v76, v234\nv_mul_f32 v77, v77, v235\nv_mul_f32 v78, v78, v236\n"
"M 36,168,72\nR 162,191,45056\nv_mul_f32 v79, v79, v237\nv_cvt_pk_bf16_f32 v72, v72, v73\nM 36,172,88\nbuffer_load_dwordx4 v220, s[4:7], s63 offen lds\n"
"M 148,168,68\nR 164,191,49152\nv_cvt_pk_bf16_f32 v73, v74, v75\nM 148,172,84\nv_cvt_pk_bf16_f32 v74, v76, v77\nv_cvt_pk_bf16_f32 v75, v78, v79\n"
"M 132,168,64\nR 166,191,53248\ns_waitcnt vmcnt(9)\ns_barrier\nM 132,172,80\nG 4\n"
"v_mul_f32 v80, v80, v230\nv_mul_f32 v81, v81, v231\nM 136,176,64\nR 168,184,0\ns_lshl2_add_u32 s60, s45, s40\nv_lshlrev_b32 v224, 3, v252\n"
"s_mul_i32 s60, s60, 7168\ns_bfm_b64 exec, 14, 28\nbuffer_load_dword v224, v224, s[12:15], s60 offen\ns_mov_b64 exec, -1\nM 136,180,80\nv_mul_f32 v82, v82, v232\n"
"v_mul_f32 v83, v83, v233\nM 152,176,68\nR 170,184,4096\nv_mul_f32 v84, v84, v234\nM 152,180,84\nbuffer_load_dwordx4 v221, s[4:7], s63 offen offset:1024 lds\n"
"M 40,176,72\nR 172,184,8192\ns_mov_b32 s60, 155648\nv_bfe_u32 v246, v252, 4, 4\nv_lshl_add_u32 v246, v246, 3, s60\nM 40,180,88\n"
"s_add_u32 m0, s50, 90112\nv_mul_f32 v85, v85, v235\nv_mul_f32 v86, v86, v236\nM 56,176,76\nR 174,184,12288\nds_read_b64 v[238:239], v246 offset:0\n"
"M 56,180,92\nG 5\nv_mul_f32 v87, v87, v237\nv_cvt_pk_bf16_f32 v78, v80, v81\nM 60,160,76\nR 176,185,0\n"
"ds_read_b64 v[240:241], v246 offset:128\nM 60,164,92\ns_add_u32 s63, s52, 2949120\nv_cvt_pk_bf16_f32 v79, v82, v83\nv_cvt_pk_bf16_f32 v80, v84, v85\nM 44,160,72\n"
"R 178,185,4096\nds_read_b64 v[242:243], v246 offset:256\nM 44,164,88\nbuffer_load_dwordx4 v220, s[4:7], s63 offen lds\nM 156,160,68\nR 180,185,8192\n"
"ds_read_b64 v[244:245], v246 offset:384\nM 156,164,84\nv_cvt_pk_bf16_f32 v81, v86, v87\nv_mul_f32 v88, v88, v230\nM 140,160,64\nR 182,185,12288\n"
"ds_read_b64 v[246:247], v246 offset:0\nM 140,164,80\nG 9\nv_mul_f32 v89, v89, v231\nv_mul_f32 v90, v90, v232\nM 128,168,96\n"
"R 160,186,0\nv_mul_f32 v91, v91, v233\nM 128,172,112\nv_mul_f32 v92, v92, v234\nv_mul_f32 v93, v93, v235\nM 144,168,100\n"
"R 162,186,4096\nv_mul_f32 v94, v94, v236\nM 144,172,116\nbuffer_load_dwordx4 v221, s[4:7], s63 offen offset:1024 lds\nM 32,168,104\nR 164,186,8192\n"
"v_mul_f32 v95, v95, v237\nG 9\nv_mfma_f32_16x16x32_bf16 v[128:131], v[238:241], v[208:211], v[128:131]\ns_lshl_b32 s60, s40, 12\ns_add_u32 s60, s60, 53248\nv_add_u32 v230, s60, v252\n"
"v_add_u32 v231, s60, v227\nM 32,172,120\nv_cvt_pk_bf16_f32 v84, v88, v89\nv_cvt_pk_bf16_f32 v85, v90, v91\nM 48,168,108\nR 166,186,12288\n"
"v_cvt_pk_bf16_f32 v86, v92, v93\nG 8\nv_mfma_f32_16x16x32_bf16 v[132:135], v[240:243], v[208:211], v[132:135]\ns_mov_b64 exec, s[66:67]\nbuffer_store_dwordx2 v[68:69], v227, s[20:23], s64 offen offset:2048 sc1\ns_mov_b64 exec, -1\n"
"v_cvt_pk_bf16_f32 v87, v94, v95\nv_add_u32 v108, 161792, v252\nM 48,172,124\nds_read_b128 v[112:115], v108\nG 6\nM 52,176,108\n"
"R 168,187,0\nG 6\nv_mfma_f32_16x16x32_bf16 v[144:147], v[244:247], v[210:213], v[144:147]\nds_read_b128 v[104:107], v108 offset:1024\nM 52,180,124\ns_movk_i32 s63, 7168\n"
"v_mad_u32_u24 v220, v228, s63, v229\nv_add_u32 v221, 3668992, v220\ns_add_u32 m0, s50, 98304\ns_add_u32 s63, s101, 0\nbuffer_load_dwordx4 v220, s[12:15], s63 offen lds\nM 36,176,104\n"
"R 170,187,4096\nv_mfma_f32_16x16x32_bf16 v[148:151], v[238:241], v[210:213], v[148:151]\nM 36,180,120\nM 148,176,100\nR 172,187,8192\nv_mfma_f32_16x16x32_bf16 v[136:139], v[242:245], v[208:211], v[136:139]\n"
"s_mov_b64 exec, s[66:67]\nbuffer_store_dwordx2 v[74:75], v227, s[20:23], s64 offen offset:2560 sc1\ns_mov_b64 exec, -1\nM 148,180,116\nM 132,176,96\nR 174,187,12288\n"
"G 3\nv_lshlrev_b32 v100, 16, v104\nv_mfma_f32_16x16x32_bf16 v[140:143], v[244:247], v[208:211], v[140:143]\nv_and_b32 v101, s77, v104\nv_lshlrev_b32 v102, 16, v105\nv_and_b32 v103, s77, v105\n"
"M 132,180,112\nv_lshlrev_b32 v96, 16, v106\nv_and_b32 v97, s77, v106\nM 136,160,96\nR 176,184,32768\nv_lshlrev_b32 v98, 16, v107\n"
"v_mfma_f32_16x16x32_bf16 v[152:155], v[240:243], v[210:213], v[152:155]\nv_and_b32 v99, s77, v107\nv_add_f32 v104, s78, v100\nv_add_f32 v105, s78, v101\nM 136,164,112\nbuffer_load_dwordx4 v221, s[12:15], s63 offen offset:1024 lds\n"
"M 152,160,100\nR 178,184,36864\nv_add_f32 v106, s78, v102\nv_mfma_f32_16x16x32_bf16 v[156:159], v[242:245], v[210:213], v[156:159]\nv_add_f32 v107, s78, v103\nM 152,164,116\n"
"s_mov_b64 exec, s[66:67]\nbuffer_store_dwordx2 v[80:81], v227, s[20:23], s64 offen offset:3072 sc1\ns_mov_b64 exec, -1\nM 40,160,104\nR 180,184,40960\nM 40,164,120\n"
"M 56,160,108\nR 182,184,45056\nM 56,164,124\ns_mov_b64 exec, s[66:67]\nbuffer_store_dwordx2 v[86:87], v227, s[20:23], s64 offen offset:3584 sc1\ns_mov_b64 exec, -1\n"
"G 4\nM 60,168,108\nR 160,185,32768\ns_not_b64 exec, s[66:67]\nds_write_b128 v230, v[16:19] offset:0\nds_write_b128 v230, v[20:23] offset:1024\n"
"s_mov_b64 exec, -1\nM 60,172,124\ns_not_b64 exec, s[66:67]\nds_write_b64 v231, v[68:69] offset:2048\nds_write_b64 v231, v[74:75] offset:2560\ns_mov_b64 exec, -1\n"
"M 44,168,104\nR 162,185,36864\ns_not_b64 exec, s[66:67]\nds_write_b64 v231, v[80:81] offset:3072\nds_write_b64 v231, v[86:87] offset:3584\ns_mov_b64 exec, -1\n"
"M 44,172,120\ns_add_u32 m0, s50, 106496\nM 156,168,100\nR 164,185,40960\nM 156,172,116\ns_add_u32 s63, s101, 128\n"
"M 140,168,96\nR 166,185,45056\nM 140,172,112\nG 10\nM 128,176,128\nR 168,186,32768\n"
"s_waitcnt vmcnt(13) lgkmcnt(3)\ns_barrier\nM 128,180,144\nbuffer_load_dwordx4 v220, s[12:15], s63 offen lds\nM 144,176,132\nR 170,186,36864\n"
"M 144,180,148\ns_cmp_eq_u32 s78, 0\ns_cbranch_scc1 Lq151\nv_mov_b32 v230, 0\nv_mov_b32 v231, 0\nv_mov_b32 v232, 0\n"
"v_mov_b32 v233, 0\nv_mov_b32 v64, 0\nv_mov_b32 v65, 0\nv_mov_b32 v70, 0\nv_mov_b32 v71, 0\nv_mov_b32 v234, 0\n"
"v_mov_b32 v235, 0\nv_mov_b32 v236, 0\nv_mov_b32 v237, 0\nv_mov_b32 v76, 0\nv_mov_b32 v77, 0\nv_mov_b32 v82, 0\n"
"v_mov_b32 v83, 0\nLq151:\ns_lshl4_add_u32 s65, s41, s42\nM 32,176,136\nR 172,186,40960\ns_lshl_b32 s65, s65, 12\n"
"M 32,180,152\ns_add_u32 s65, s65, 77529088\nM 48,176,140\nR 174,186,45056\nM 48,180,156\nG 4\n"
"M 52,160,140\nR 176,187,32768\nM 52,164,156\nbuffer_load_dwordx4 v221, s[12:15], s63 offen offset:1024 lds\nM 36,160,136\nR 178,187,36864\n"
"M 36,164,152\ns_add_u32 m0, s50, 114688\nM 148,160,132\nR 180,187,40960\nM 148,164,148\ns_add_u32 s63, s101, 256\n"
"M 132,160,128\nR 182,187,45056\nM 132,164,144\nG 4\nM 136,168,128\nR 160,184,49152\n"
"M 136,172,144\nbuffer_load_dwordx4 v220, s[12:15], s63 offen lds\nM 152,168,132\nR 162,184,53248\nM 152,172,148\nM 40,168,136\n"
"R 164,184,57344\ns_lshl2_add_u32 s60, s45, s40\ns_waitcnt vmcnt(12)\nv_lshlrev_b32 v224, 3, v252\ns_mul_i32 s60, s60, 7168\ns_bfm_b64 exec, 14, 42\n"
"buffer_load_dword v224, v224, s[12:15], s60 offen\ns_mov_b64 exec, -1\nM 40,172,152\nM 56,168,140\nR 166,184,61440\nM 56,172,156\n"
"G 4\nM 60,176,140\nR 168,185,49152\nM 60,180,156\nbuffer_load_dwordx4 v221, s[12:15], s63 offen offset:1024 lds\nM 44,176,136\n"
"R 170,185,53248\nM 44,180,152\ns_add_u32 m0, s50, 0\nM 156,176,132\nR 172,185,57344\nM 156,180,148\n"
"s_add_u32 s63, s100, 0\nM 140,176,128\nR 174,185,61440\nM 140,180,144\nG 4\nM 128,160,160\n"
"R 176,186,49152\nM 128,164,176\nbuffer_load_dwordx4 v222, s[12:15], s63 offen lds\nM 144,160,164\nR 178,186,53248\nM 144,164,180\n"
"M 32,160,168\nR 180,186,57344\nM 32,164,184\nM 48,160,172\nR 182,186,61440\nM 48,164,188\n"
"G 4\nM 52,168,172\nR 160,187,49152\nM 52,172,188\nbuffer_load_dwordx4 v223, s[12:15], s63 offen offset:1024 lds\nM 36,168,168\n"
"R 162,187,53248\nM 36,172,184\nM 148,168,164\nR 164,187,57344\ns_lshl_b32 s60, s40, 12\ns_add_u32 s60, s60, 49152\n"
"v_add_u32 v226, s60, v252\nv_add_u32 v111, s60, v227\nM 148,172,180\nM 132,168,160\nR 166,187,61440\ns_cmp_eq_u32 s40, 0\n"
"s_cselect_b64 s[86:87], 0, -1\ns_cselect_b32 s60, s41, 0\ns_cmp_lg_u32 s60, 0\ns_cselect_b64 s[66:67], -1, 0\nM 132,172,176\nG 4\n"
"M 136,176,160\nR 168,188,0\nM 136,180,176\ns_mov_b64 exec, s[86:87]\nds_read_b128 v[230:233], v226 offset:0\nds_read_b64 v[64:65], v111 offset:2048\n"
"s_mov_b64 exec, -1\nM 152,176,164\nR 170,188,4096\ns_waitcnt vmcnt(13) lgkmcnt(1)\ns_barrier\nM 152,180,180\n"
"s_movk_i32 s63, 2048\nv_mad_u32_u24 v220, v228, s63, v229\nv_add_u32 v221, 1047552, v220\ns_add_u32 m0, s50, 8192\ns_add_u32 s63, s53, 0\nbuffer_load_dwordx4 v220, s[8:11], s63 offen lds\n"
"M 40,176,168\nR 172,188,8192\nM 40,180,184\ns_mov_b64 exec, s[86:87]\nds_read_b64 v[70:71], v111 offset:2560\nds_read_b128 v[234:237], v226 offset:1024\n"
"s_mov_b64 exec, -1\nM 56,176,172\nR 174,188,12288\nM 56,180,188\nM 60,160,172\nR 176,189,0\n"
"M 60,164,188\ns_mov_b64 exec, s[86:87]\nds_read_b64 v[76:77], v111 offset:3072\nds_read_b64 v[82:83], v111 offset:3584\ns_mov_b64 exec, -1\nM 44,160,168\n"
"R 178,189,4096\nM 44,164,184\nbuffer_load_dwordx4 v221, s[8:11], s63 offen offset:1024 lds\nM 156,160,164\nR 180,189,8192\nM 156,164,180\n"
"s_mov_b64 exec, s[66:67]\nbuffer_load_dwordx4 v[230:233], v252, s[20:23], s65 offen offset:0 sc1\ns_mov_b64 exec, -1\nM 140,160,160\nR 182,189,12288\nM 140,164,176\n"
"G 6\nM 32,168,200\nR 160,190,0\ns_mov_b64 exec, s[66:67]\nbuffer_load_dwordx2 v[64:65], v227, s[20:23], s65 offen offset:2048 sc1\ns_mov_b64 exec, -1\n"
"M 32,172,216\nR 162,190,4096\nM 48,168,204\nR 164,190,8192\nM 48,172,220\nR 166,190,12288\n"
"G 4\nM 52,176,204\nR 168,191,0\ns_mov_b64 exec, s[66:67]\nbuffer_load_dwordx2 v[70:71], v227, s[20:23], s65 offen offset:2560 sc1\ns_mov_b64 exec, -1\n"
"M 52,180,220\nR 170,191,4096\nM 36,176,200\nR 172,191,8192\nM 36,180,216\nR 174,191,12288\n"
"G 4\nM 40,160,200\nR 176,188,16384\ns_mov_b64 exec, s[66:67]\nbuffer_load_dwordx4 v[234:237], v252, s[20:23], s65 offen offset:1024 sc1\ns_mov_b64 exec, -1\n"
"M 40,164,216\nR 178,188,20480\nM 56,160,204\nR 180,188,24576\nM 56,164,220\nR 182,188,28672\n"
"G 4\nM 60,168,204\nR 160,189,16384\ns_mov_b64 exec, s[66:67]\nbuffer_load_dwordx2 v[76:77], v227, s[20:23], s65 offen offset:3072 sc1\ns_mov_b64 exec, -1\n"
"M 60,172,220\nR 162,189,20480\nM 44,168,200\nR 164,189,24576\nM 44,172,216\nR 166,189,28672\n"
"G 4\nM 32,176,232\nR 168,190,16384\ns_mov_b64 exec, s[66:67]\nbuffer_load_dwordx2 v[82:83], v227, s[20:23], s65 offen offset:3584 sc1\ns_mov_b64 exec, -1\n"
"Lq185:\ns_lshr_b32 s60, s45, 3\ns_lshl2_add_u32 s60, s60, s40\ns_lshl_b32 s60, s60, 8\ns_waitcnt vmcnt(11)\nv_lshl_add_u32 v224, v252, 3, s60\n"
"v_add_u32 v224, Lq185+4096-LqPC, v224\nv_min_u32 v224, LqE-LqPC-4, v224\ns_mov_b64 exec, 3\nglobal_load_dword v224, v224, s[98:99]\ns_mov_b64 exec, -1\nM 32,180,248\n"
"R 170,190,20480\nM 48,176,236\nR 172,190,24576\nM 48,180,252\nR 174,190,28672\nG 4\n"
"M 52,160,236\nR 176,191,16384\nM 52,164,252\nR 178,191,20480\nM 36,160,232\nR 180,191,24576\n"
"M 36,164,248\nR 182,191,28672\nG 4\nM 40,168,232\nR 160,188,0\nM 40,172,248\n"
"R 162,188,4096\nM 56,168,236\nR 164,188,8192\nM 56,172,252\nR 166,188,12288\nG 4\n"
"M 60,176,236\nR 168,189,0\nM 60,180,252\nR 170,189,4096\nM 44,176,232\nR 172,189,8192\n"
"M 44,180,248\nR 174,189,12288\nG 4\nM 128,160,192\nR 176,190,0\nM 128,164,208\n"
"R 178,190,4096\ns_mov_b32 m0, s51\nM 144,160,196\nR 180,190,8192\nds_write_addtid_b32 v32 offset:0\nM 144,164,212\n"
"R 182,190,12288\nds_write_addtid_b32 v33 offset:256\nG 6\nM 148,168,196\nR 160,191,0\nds_write_addtid_b32 v34 offset:512\n"
"M 148,172,212\nR 162,191,4096\nds_write_addtid_b32 v35 offset:768\nM 132,168,192\nR 164,191,8192\nds_write_addtid_b32 v36 offset:1024\n"
"M 132,172,208\nR 166,191,12288\nds_write_addtid_b32 v37 offset:1280\nG 9\nM 136,176,192\nLpg5:\n"
"s_cbranch_execz LretQ0\nR 168,188,16384\nds_write_addtid_b32 v38 offset:1536\nM 136,180,208\nR 170,188,20480\nds_write_addtid_b32 v39 offset:1792\n"
"M 152,176,196\nR 172,188,24576\nds_write_addtid_b32 v40 offset:2048\nM 152,180,212\nR 174,188,28672\nds_write_addtid_b32 v41 offset:2304\n"
"G 9\nM 156,160,196\nR 176,189,16384\nds_write_addtid_b32 v42 offset:2560\nM 156,164,212\nR 178,189,20480\n"
"ds_write_addtid_b32 v43 offset:2816\nM 140,160,192\nR 180,189,24576\nds_write_addtid_b32 v44 offset:3072\nds_write_addtid_b32 v45 offset:3328\nds_write_addtid_b32 v46 offset:3584\n"
"ds_write_addtid_b32 v47 offset:3840\nds_write_addtid_b32 v48 offset:4096\nds_write_addtid_b32 v49 offset:4352\nds_write_addtid_b32 v50 offset:4608\nds_write_addtid_b32 v51 offset:4864\nds_write_addtid_b32 v52 offset:5120\n"
"ds_write_addtid_b32 v53 offset:5376\nds_write_addtid_b32 v54 offset:5632\nds_write_addtid_b32 v55 offset:5888\nds_write_addtid_b32 v56 offset:6144\nds_write_addtid_b32 v57 offset:6400\nds_write_addtid_b32 v58 offset:6656\n"
"ds_write_addtid_b32 v59 offset:6912\nds_write_addtid_b32 v60 offset:7168\nds_write_addtid_b32 v61 offset:7424\nds_write_addtid_b32 v62 offset:7680\nds_write_addtid_b32 v63 offset:7936\nM 140,164,208\n"
"R 182,189,28672\nG 15\nM 128,168,224\nR 160,190,16384\ns_add_u32 m0, s50, 16384\nM 128,172,240\n"
"R 162,190,20480\nG 3\ns_barrier\ns_add_u32 s63, s53, 65536\nM 144,168,228\nR 164,190,24576\n"
"buffer_load_dwordx4 v220, s[8:11], s63 offen lds\nds_read2st64_b32 v[32:33], v225 offset0:0 offset1:1\nM 144,172,244\nR 166,190,28672\nbuffer_load_dwordx4 v221, s[8:11], s63 offen offset:1024 lds\nds_read2st64_b32 v[34:35], v225 offset0:2 offset1:3\n"
"G 6\nM 148,176,228\nR 168,191,16384\ns_add_u32 m0, s50, 24576\ns_add_u32 s63, s53, 131072\nbuffer_load_dwordx4 v220, s[8:11], s63 offen lds\n"
"ds_read2st64_b32 v[36:37], v225 offset0:4 offset1:5\nM 148,180,244\nR 170,191,20480\nbuffer_load_dwordx4 v221, s[8:11], s63 offen offset:1024 lds\nds_read2st64_b32 v[38:39], v225 offset0:6 offset1:7\nM 132,176,224\n"
"R 172,191,24576\ns_add_u32 m0, s50, 32768\ns_add_u32 s63, s53, 196608\nbuffer_load_dwordx4 v220, s[8:11], s63 offen lds\nds_read2st64_b32 v[40:41], v225 offset0:8 offset1:9\nM 132,180,240\n"
"R 174,191,28672\nbuffer_load_dwordx4 v221, s[8:11], s63 offen offset:1024 lds\nds_read2st64_b32 v[42:43], v225 offset0:10 offset1:11\nG 9\nM 136,160,224\ns_add_u32 m0, s50, 40960\n"
"s_add_u32 s63, s53, 262144\nbuffer_load_dwordx4 v220, s[8:11], s63 offen lds\nds_read2st64_b32 v[44:45], v225 offset0:12 offset1:13\nM 136,164,240\nbuffer_load_dwordx4 v221, s[8:11], s63 offen offset:1024 lds\nds_read2st64_b32 v[46:47], v225 offset0:14 offset1:15\n"
"M 152,160,228\ns_add_u32 m0, s50, 49152\ns_add_u32 s63, s53, 327680\nbuffer_load_dwordx4 v220, s[8:11], s63 offen lds\nds_read2st64_b32 v[48:49], v225 offset0:16 offset1:17\nM 152,164,244\n"
"ds_read2st64_b32 v[50:51], v225 offset0:18 offset1:19\nds_read2st64_b32 v[52:53], v225 offset0:20 offset1:21\nG 6\nM 156,168,228\nds_read2st64_b32 v[54:55], v225 offset0:22 offset1:23\nds_read2st64_b32 v[56:57], v225 offset0:24 offset1:25\n"
"M 156,172,244\nds_read2st64_b32 v[58:59], v225 offset0:26 offset1:27\nds_read2st64_b32 v[60:61], v225 offset0:28 offset1:29\nM 140,168,224\nds_read2st64_b32 v[62:63], v225 offset0:30 offset1:31\nM 140,172,240\n"
"G 7\nv_pk_add_f32 v[144:145], v[144:145], v[48:49]\nG 6\nv_pk_add_f32 v[146:147], v[146:147], v[50:51]\nG 5\nv_pk_add_f32 v[148:149], v[148:149], v[52:53]\n"
"G 4\nv_pk_add_f32 v[150:151], v[150:151], v[54:55]\nG 3\nv_pk_add_f32 v[152:153], v[152:153], v[56:57]\nG 2\nv_pk_add_f32 v[154:155], v[154:155], v[58:59]\n"
"G 1\nv_pk_add_f32 v[156:157], v[156:157], v[60:61]\nG 0\nv_pk_add_f32 v[158:159], v[158:159], v[62:63]\nv_pk_add_f32 v[128:129], v[128:129], v[32:33]\nv_pk_add_f32 v[130:131], v[130:131], v[34:35]\n"
"v_pk_add_f32 v[132:133], v[132:133], v[36:37]\nv_pk_add_f32 v[134:135], v[134:135], v[38:39]\nv_pk_add_f32 v[136:137], v[136:137], v[40:41]\nv_pk_add_f32 v[138:139], v[138:139], v[42:43]\nv_pk_add_f32 v[140:141], v[140:141], v[44:45]\nv_pk_add_f32 v[142:143], v[142:143], v[46:47]\n"
"v_cvt_pk_bf16_f32 v144, v144, v145\nv_cvt_pk_bf16_f32 v145, v146, v147\nv_cvt_pk_bf16_f32 v146, v148, v149\nv_cvt_pk_bf16_f32 v147, v150, v151\nv_cvt_pk_bf16_f32 v152, v152, v153\nv_cvt_pk_bf16_f32 v153, v154, v155\n"
"v_cvt_pk_bf16_f32 v154, v156, v157\nv_cvt_pk_bf16_f32 v155, v158, v159\nv_cvt_pk_bf16_f32 v128, v128, v129\nv_cvt_pk_bf16_f32 v129, v130, v131\nv_cvt_pk_bf16_f32 v130, v132, v133\nv_cvt_pk_bf16_f32 v131, v134, v135\n"
"v_cvt_pk_bf16_f32 v136, v136, v137\nv_cvt_pk_bf16_f32 v137, v138, v139\nv_cvt_pk_bf16_f32 v138, v140, v141\nv_cvt_pk_bf16_f32 v139, v142, v143\nv_mfma_f32_16x16x32_bf16 v[52:55], v[16:19], v[144:147], v[96:99]\nv_mfma_f32_16x16x32_bf16 v[48:51], v[0:3], v[144:147], v[100:103]\n"
"v_mfma_f32_16x16x32_bf16 v[60:63], v[20:23], v[152:155], v[96:99]\nv_mfma_f32_16x16x32_bf16 v[56:59], v[4:7], v[152:155], v[100:103]\ns_waitcnt vmcnt(13)\nv_max3_u32 v248, v233, v65, v71\nv_cmp_eq_u32 vcc, -1, v248\ns_nop 0\n"
"s_cbranch_vccnz Lq187\nLq188:\ns_waitcnt vmcnt(10)\nv_max3_u32 v248, v237, v77, v83\nv_cmp_eq_u32 vcc, -1, v248\ns_nop 0\n"
"s_cbranch_vccnz Lq189\nLq190:\nv_mfma_f32_16x16x32_bf16 v[36:39], v[0:3], v[128:131], v[96:99]\nv_mfma_f32_16x16x32_bf16 v[32:35], v[230:233], v[128:131], v[104:107]\nv_mfma_f32_16x16x32_bf16 v[44:47], v[4:7], v[136:139], v[96:99]\nv_mfma_f32_16x16x32_bf16 v[40:43], v[234:237], v[136:139], v[104:107]\n"
"s_lshl_b32 s84, s42, 11\ns_add_u32 s84, s84, s49\ns_add_u32 s84, s84, 2097152\ns_add_u32 s85, s84, 32768\ns_lshl2_add_u32 s60, s45, s40\ns_waitcnt vmcnt(9)\n"
"v_lshlrev_b32 v224, 3, v252\ns_mul_i32 s60, s60, 7168\ns_bfm_b64 exec, 28, 28\nbuffer_load_dword v224, v224, s[16:19], s60 offen\ns_mov_b64 exec, -1\nv_mov_b32 v238, s46\n"
"v_mov_b32 v239, s47\nv_max_f32 v52, v52, v53\nv_max_f32 v54, v54, v55\nv_max_f32 v60, v60, v61\nv_max_f32 v62, v62, v63\nv_max3_f32 v52, v52, v48, v49\n"
"v_max3_f32 v54, v54, v50, v51\nv_max3_f32 v60, v60, v56, v57\nv_max3_f32 v62, v62, v58, v59\nv_max_f32 v36, v36, v37\nv_max_f32 v38, v38, v39\nv_max_f32 v44, v44, v45\n"
"v_max_f32 v46, v46, v47\nv_max3_f32 v36, v36, v32, v33\nv_max3_f32 v38, v38, v34, v35\nv_max3_f32 v44, v44, v40, v41\nv_max3_f32 v46, v46, v42, v43\nv_max_f32 v118, v52, v54\n"
"v_max_f32 v119, v60, v62\nv_max_f32 v116, v36, v38\nv_max_f32 v117, v44, v46\ns_nop 0\nv_permlane32_swap_b32 v116, v118\nv_permlane32_swap_b32 v117, v119\n"
"v_max_f32 v116, v116, v118\nv_max_f32 v117, v117, v119\ns_nop 1\nv_permlane16_swap_b32 v116, v117\nv_max_f32 v116, v116, v117\nv_mov_b32 v117, v116\n"
"s_nop 1\nv_permlane16_swap_b32 v116, v117\nv_mov_b32 v118, v116\nv_mov_b32 v119, v117\ns_nop 0\nv_permlane32_swap_b32 v116, v118\n"
"v_permlane32_swap_b32 v117, v119\nv_mul_f32 v116, v116, v215\nv_mul_f32 v118, v118, v218\nv_mul_f32 v117, v117, v215\nv_mul_f32 v119, v119, v218\nv_fma_f32 v52, v52, v218, -v118\n"
"v_fma_f32 v54, v54, v218, -v118\nv_fma_f32 v60, v60, v218, -v119\nv_fma_f32 v62, v62, v218, -v119\nv_fma_f32 v36, v36, v215, -v116\nv_fma_f32 v38, v38, v215, -v116\nv_fma_f32 v44, v44, v215, -v117\n"
"v_fma_f32 v46, v46, v215, -v117\nv_exp_f32 v52, v52\nv_exp_f32 v54, v54\nv_exp_f32 v60, v60\nv_exp_f32 v62, v62\nv_exp_f32 v36, v36\n"
"v_exp_f32 v38, v38\nv_exp_f32 v44, v44\nv_exp_f32 v46, v46\nv_add_f32 v118, v52, v54\nv_add_f32 v119, v60, v62\nv_add_f32 v116, v36, v38\n"
"v_add_f32 v117, v44, v46\ns_nop 0\nv_permlane32_swap_b32 v116, v118\nv_permlane32_swap_b32 v117, v119\nv_add_f32 v116, v116, v118\nv_add_f32 v117, v117, v119\n"
"s_nop 1\nv_permlane16_swap_b32 v116, v117\nv_add_f32 v116, v116, v117\ns_barrier\nv_rcp_f32 v116, v116\ns_nop 0\n"
"v_mov_b32 v117, v116\ns_nop 1\nv_permlane16_swap_b32 v116, v117\nv_mov_b32 v118, v116\nv_mov_b32 v119, v117\ns_nop 0\n"
"v_permlane32_swap_b32 v116, v118\nv_permlane32_swap_b32 v117, v119\nv_mul_f32 v52, v52, v118\nv_mul_f32 v54, v54, v118\nv_mul_f32 v60, v60, v119\nv_mul_f32 v62, v62, v119\n"
"v_mul_f32 v36, v36, v116\nv_mul_f32 v38, v38, v116\nv_mul_f32 v44, v44, v117\nv_mul_f32 v46, v46, v117\nv_cvt_pk_bf16_f32 v52, v52, v52\nv_cvt_pk_bf16_f32 v54, v54, v54\n"
"v_cvt_pk_bf16_f32 v60, v60, v60\nv_cvt_pk_bf16_f32 v62, v62, v62\nv_cvt_pk_bf16_f32 v36, v36, v36\nv_cvt_pk_bf16_f32 v38, v38, v38\nv_cvt_pk_bf16_f32 v44, v44, v44\nv_cvt_pk_bf16_f32 v46, v46, v46\n"
"v_and_b32 v48, v52, v112\nv_and_b32 v49, v54, v113\nv_and_b32 v50, v52, v114\nv_and_b32 v51, v54, v115\nv_and_b32 v56, v60, v112\nv_and_b32 v57, v62, v113\n"
"v_and_b32 v58, v60, v114\nv_and_b32 v59, v62, v115\nv_mfma_f32_16x16x32_bf16 v[144:147], v[66:69], v[48:51], 0\nv_mfma_f32_16x16x32_bf16 v[148:151], v[72:75], v[48:51], 0\nv_and_b32 v32, v36, v112\nv_and_b32 v33, v38, v113\n"
"v_and_b32 v34, v36, v114\nv_and_b32 v35, v38, v115\nv_mfma_f32_16x16x32_bf16 v[152:155], v[78:81], v[56:59], 0\nv_mfma_f32_16x16x32_bf16 v[156:159], v[84:87], v[56:59], 0\nv_and_b32 v40, v44, v112\nv_and_b32 v41, v46, v113\n"
"v_and_b32 v42, v44, v114\nv_and_b32 v43, v46, v115\nv_mfma_f32_16x16x32_bf16 v[128:131], v[64:67], v[32:35], 0\nv_mfma_f32_16x16x32_bf16 v[132:135], v[70:73], v[32:35], 0\nv_mfma_f32_16x16x32_bf16 v[136:139], v[76:79], v[40:43], 0\nv_mfma_f32_16x16x32_bf16 v[140:143], v[82:85], v[40:43], 0\n"
"v_cvt_pk_bf16_f32 v144, v144, v145\nv_cvt_pk_bf16_f32 v145, v146, v147\nv_cvt_pk_bf16_f32 v146, v148, v149\nv_cvt_pk_bf16_f32 v147, v150, v151\nbuffer_store_dwordx4 v[144:147], v252, s[20:23], s85 offen offset:0 sc1\nv_cvt_pk_bf16_f32 v152, v152, v153\n"
"v_cvt_pk_bf16_f32 v153, v154, v155\nv_cvt_pk_bf16_f32 v154, v156, v157\nv_cvt_pk_bf16_f32 v155, v158, v159\nbuffer_store_dwordx4 v[152:155], v252, s[20:23], s85 offen offset:1024 sc1\nv_cvt_pk_bf16_f32 v128, v128, v129\nv_cvt_pk_bf16_f32 v129, v130, v131\n"
"v_cvt_pk_bf16_f32 v130, v132, v133\nv_cvt_pk_bf16_f32 v131, v134, v135\nbuffer_store_dwordx4 v[128:131], v252, s[20:23], s84 offen offset:0 sc1\nv_cvt_pk_bf16_f32 v136, v136, v137\nv_cvt_pk_bf16_f32 v137, v138, v139\nv_cvt_pk_bf16_f32 v138, v140, v141\n"
"v_cvt_pk_bf16_f32 v139, v142, v143\nbuffer_store_dwordx4 v[136:139], v252, s[20:23], s84 offen offset:1024 sc1\ns_mov_b64 exec, 0\ns_cmp_eq_u32 s40, 1\ns_cbranch_scc1 Lpg7\ns_cmp_eq_u32 s40, 2\n"
"s_cbranch_scc1 LretO0\ns_cmp_eq_u32 s40, 3\ns_cbranch_scc1 LretO0\ns_branch Lpg6\nLretO0:\ns_mov_b64 exec, -1\n"
"s_add_u32 s61, s79, 88080384\ns_lshl_b32 s62, s42, 3\ns_add_u32 s61, s61, s62\ns_waitcnt vmcnt(0)\ns_mov_b64 exec, 1\nbuffer_atomic_swap_x2 v[238:239], off, s[20:23], s61 sc1\n"
"s_mov_b64 exec, -1\nv_bfe_u32 v250, v252, 7, 1\nv_mul_u32_u24 v250, 2176, v250\nv_bfe_u32 v251, v252, 4, 3\nv_lshl_add_u32 v250, v251, 3, v250\ns_lshl_b32 s60, s90, 6\n"
"v_add_u32 v250, s60, v250\ns_sleep 3\nv_mov_b32 v248, 0\nv_mov_b32 v249, 0\ns_add_u32 s61, s80, 88080384\ns_mov_b64 exec, 0xffff\n"
"buffer_atomic_or_x2 v[248:249], v250, s[20:23], s61 offen sc0 sc1\ns_mov_b64 exec, -1\nbuffer_load_dwordx4 v221, s[8:11], s63 offen offset:1024 lds\ns_add_u32 m0, s50, 57344\ns_add_u32 s63, s53, 393216\nbuffer_load_dwordx4 v220, s[8:11], s63 offen lds\n"
"buffer_load_dwordx4 v221, s[8:11], s63 offen offset:1024 lds\ns_add_u32 m0, s50, 65536\ns_add_u32 s63, s53, 458752\nbuffer_load_dwordx4 v220, s[8:11], s63 offen lds\nbuffer_load_dwordx4 v221, s[8:11], s63 offen offset:1024 lds\ns_add_u32 m0, s50, 73728\n"
"s_add_u32 s63, s53, 524288\nbuffer_load_dwordx4 v220, s[8:11], s63 offen lds\nbuffer_load_dwordx4 v221, s[8:11], s63 offen offset:1024 lds\ns_add_u32 m0, s50, 81920\ns_add_u32 s63, s53, 589824\nbuffer_load_dwordx4 v220, s[8:11], s63 offen lds\n"
"buffer_load_dwordx4 v221, s[8:11], s63 offen offset:1024 lds\ns_add_u32 m0, s50, 90112\ns_add_u32 s63, s53, 655360\nbuffer_load_dwordx4 v220, s[8:11], s63 offen lds\ns_waitcnt vmcnt(10)\nv_cmp_eq_u64 s[86:87], s[46:47], v[248:249]\n"
"s_nop 1\ns_and_b32 s88, s86, 0xffff\ns_cmp_eq_u32 s88, 0xffff\ns_cbranch_scc1 Lq201\nv_mov_b32 v246, 0\nv_mov_b32 v247, 0\n"
"s_add_u32 s61, s80, 88080384\ns_mov_b64 exec, 0xffff\nbuffer_atomic_or_x2 v[246:247], v250, s[20:23], s61 offen sc0 sc1\ns_mov_b64 exec, -1\nLq201:\nbuffer_load_dwordx4 v221, s[8:11], s63 offen offset:1024 lds\n"
"v_bfe_u32 v234, v252, 4, 4\nv_lshrrev_b32 v235, 8, v252\nv_lshlrev_b32 v235, 2, v235\nv_sub_u32 v234, v234, v235\nv_lshlrev_b32 v235, 4, v234\ns_mov_b64 s[60:61], 0x3f80\n"
"v_lshlrev_b64 v[208:209], v235, s[60:61]\nv_cmp_gt_u32 s[60:61], 4, v234\ns_nop 1\nv_cndmask_b32 v212, 0, v208, s[60:61]\nv_cndmask_b32 v208, 0, v208, s[60:61]\nv_cndmask_b32 v213, 0, v209, s[60:61]\n"
"v_cndmask_b32 v209, 0, v209, s[60:61]\ns_nop 1\nv_mfma_f32_16x16x32_bf16 v[0:3], v[208:211], v[192:195], 0\nv_mfma_f32_16x16x32_bf16 v[4:7], v[210:213], v[192:195], 0\nv_mfma_f32_16x16x32_bf16 v[8:11], v[208:211], v[196:199], 0\nv_mfma_f32_16x16x32_bf16 v[12:15], v[210:213], v[196:199], 0\n"
"v_mfma_f32_16x16x32_bf16 v[16:19], v[208:211], v[200:203], 0\nv_mfma_f32_16x16x32_bf16 v[20:23], v[210:213], v[200:203], 0\nv_mfma_f32_16x16x32_bf16 v[24:27], v[208:211], v[204:207], 0\nv_mfma_f32_16x16x32_bf16 v[28:31], v[210:213], v[204:207], 0\ns_cmp_eq_u32 s88, 0xffff\ns_cbranch_scc1 Lq202\n"
"s_waitcnt vmcnt(1)\nv_mov_b32 v248, v246\nv_mov_b32 v249, v247\nv_cmp_eq_u64 s[86:87], s[46:47], v[248:249]\ns_nop 1\ns_and_b32 s62, s86, 0xffff\n"
"s_cmp_eq_u32 s62, 0xffff\ns_cbranch_scc1 Lq193\nLq192:\nv_mov_b32 v248, 0\nv_mov_b32 v249, 0\ns_add_u32 s61, s80, 88080384\n"
"s_mov_b64 exec, 0xffff\nbuffer_atomic_or_x2 v[248:249], v250, s[20:23], s61 offen sc0 sc1\ns_mov_b64 exec, -1\ns_waitcnt vmcnt(0)\nv_cmp_eq_u64 s[86:87], s[46:47], v[248:249]\ns_nop 1\n"
"s_and_b32 s62, s86, 0xffff\ns_cmp_eq_u32 s62, 0xffff\ns_cbranch_scc1 Lq193\ns_sub_u32 s48, s48, 1\ns_cmp_gt_i32 s48, 0\ns_cbranch_scc1 Lq192\n"
"Lq193:\nLq202:\ns_add_u32 s64, s70, 2097152\nbuffer_load_dwordx4 a[0:3], v252, s[20:23], s64 offen offset:0 sc1\ns_add_u32 s65, s70, 2129920\nbuffer_load_dwordx4 a[4:7], v252, s[20:23], s65 offen offset:0 sc1\n"
"s_add_u32 s66, s71, 2097152\nbuffer_load_dwordx4 a[8:11], v252, s[20:23], s66 offen offset:0 sc1\ns_add_u32 s67, s71, 2129920\nbuffer_load_dwordx4 a[12:15], v252, s[20:23], s67 offen offset:0 sc1\nbuffer_load_dwordx4 a[16:19], v252, s[20:23], s64 offen offset:1024 sc1\nbuffer_load_dwordx4 a[20:23], v252, s[20:23], s65 offen offset:1024 sc1\n"
"buffer_load_dwordx4 a[24:27], v252, s[20:23], s66 offen offset:1024 sc1\nbuffer_load_dwordx4 a[28:31], v252, s[20:23], s67 offen offset:1024 sc1\ns_barrier\nR 160,184,8192\nR 162,184,12288\nR 164,184,16384\n"
"R 166,184,20480\nR 168,185,8192\nR 170,185,12288\nR 172,185,16384\nR 174,185,20480\nR 176,186,8192\n"
"R 178,186,12288\nR 180,186,16384\nR 182,186,20480\nbuffer_load_dwordx4 a[32:35], v252, s[20:23], s64 offen offset:2048 sc1\nbuffer_load_dwordx4 a[36:39], v252, s[20:23], s65 offen offset:2048 sc1\nbuffer_load_dwordx4 a[40:43], v252, s[20:23], s66 offen offset:2048 sc1\n"
"buffer_load_dwordx4 a[44:47], v252, s[20:23], s67 offen offset:2048 sc1\nbuffer_load_dwordx4 a[48:51], v252, s[20:23], s64 offen offset:3072 sc1\nbuffer_load_dwordx4 a[52:55], v252, s[20:23], s65 offen offset:3072 sc1\nbuffer_load_dwordx4 a[56:59], v252, s[20:23], s66 offen offset:3072 sc1\nbuffer_load_dwordx4 a[60:63], v252, s[20:23], s67 offen offset:3072 sc1\ns_add_u32 s64, s70, 2101248\n"
"buffer_load_dwordx4 a[64:67], v252, s[20:23], s64 offen offset:0 sc1\ns_add_u32 s65, s70, 2134016\nbuffer_load_dwordx4 a[68:71], v252, s[20:23], s65 offen offset:0 sc1\ns_add_u32 s66, s71, 2101248\nbuffer_load_dwordx4 a[72:75], v252, s[20:23], s66 offen offset:0 sc1\ns_add_u32 s67, s71, 2134016\n"
"buffer_load_dwordx4 a[76:79], v252, s[20:23], s67 offen offset:0 sc1\ns_waitcnt vmcnt(19) lgkmcnt(10)\nM 0,160,0\nbuffer_load_dwordx4 a[80:83], v252, s[20:23], s64 offen offset:1024 sc1\ns_waitcnt vmcnt(16) lgkmcnt(8)\nM 0,164,16\n"
"M 16,160,4\ns_lshl_b32 s60, s40, 5\nv_xor_b32 v238, s60, v188\nv_and_b32 v238, 0xfffffbff, v238\ns_waitcnt vmcnt(15)\nM 16,164,20\n"
"v_mov_b32 v242, s76\nv_mov_b32 v243, s76\nv_mfma_f32_16x16x32_bf16 v[32:35], v[160:163], a[8:11], 0\nbuffer_load_dwordx4 a[84:87], v252, s[20:23], s65 offen offset:1024 sc1\ns_waitcnt vmcnt(15)\nM 32,164,24\n"
"v_mov_b32 v244, s76\nv_mov_b32 v245, s76\nv_mfma_f32_16x16x32_bf16 v[48:51], v[160:163], a[12:15], 0\ns_waitcnt vmcnt(14)\nM 48,164,28\nG 4\n"
"v_mfma_f32_16x16x32_bf16 v[52:55], v[168:171], a[12:15], 0\nR 160,187,8192\nM 52,172,28\nbuffer_load_dwordx4 a[88:91], v252, s[20:23], s66 offen offset:1024 sc1\nv_mfma_f32_16x16x32_bf16 v[36:39], v[168:171], a[8:11], 0\nR 162,187,12288\n"
"M 36,172,24\nM 20,168,4\nR 164,187,16384\nR 64,238,32768\nR 66,238,36864\nM 20,172,20\n"
"buffer_load_dwordx4 a[92:95], v252, s[20:23], s67 offen offset:1024 sc1\nM 4,168,0\nR 166,187,20480\nR 68,238,33792\nR 70,238,37888\nM 4,172,16\n"
"G 8\nM 8,176,0\nR 168,184,24576\nM 8,180,16\nbuffer_load_dwordx4 a[96:99], v252, s[20:23], s64 offen offset:2048 sc1\nM 24,176,4\n"
"R 170,184,28672\nM 24,180,20\nv_mfma_f32_16x16x32_bf16 v[40:43], v[176:179], a[8:11], 0\nR 172,184,32768\nG 6\nv_mfma_f32_16x16x32_bf16 v[72:75], v[242:245], v[64:67], 0\n"
"M 40,180,24\nbuffer_load_dwordx4 a[100:103], v252, s[20:23], s65 offen offset:2048 sc1\nG 3\nv_mfma_f32_16x16x32_bf16 v[72:75], v[242:245], v[68:71], v[72:75]\nv_mfma_f32_16x16x32_bf16 v[56:59], v[176:179], a[12:15], 0\nR 174,184,36864\n"
"M 56,180,28\nv_mfma_f32_16x16x32_bf16 v[60:63], v[160:163], a[12:15], 0\nR 176,185,24576\nM 60,164,28\nbuffer_load_dwordx4 a[104:107], v252, s[20:23], s66 offen offset:2048 sc1\nv_lshrrev_b32 v64, 1, v252\n"
"v_mfma_f32_16x16x32_bf16 v[44:47], v[160:163], a[8:11], 0\nR 178,185,28672\nv_mov_b32 v73, s46\ns_and_b32 s61, s40, 3\ns_lshl_b32 s61, s61, 7\ns_add_u32 s61, s61, s91\n"
"s_add_u32 s61, s61, 393216\ns_mov_b64 exec, 0xffff\nbuffer_store_dwordx2 v[72:73], v64, s[20:23], s61 offen sc1\ns_mov_b64 exec, -1\nM 44,164,24\nM 28,160,4\n"
"R 180,185,32768\nM 28,164,20\nbuffer_load_dwordx4 a[108:111], v252, s[20:23], s67 offen offset:2048 sc1\nM 12,160,0\nR 182,185,36864\nM 12,164,16\n"
"s_lshl_b32 s60, s40, 5\nv_xor_b32 v238, s60, v188\nG 4\ns_waitcnt vmcnt(20)\nM 0,168,32\nR 160,186,24576\n"
"v_and_b32 v238, 0xfffffbff, v238\ns_waitcnt vmcnt(16)\nM 0,172,48\nbuffer_load_dwordx4 a[112:115], v252, s[20:23], s64 offen offset:3072 sc1\nM 16,168,36\nR 162,186,28672\n"
"s_waitcnt vmcnt(16)\nM 16,172,52\nM 32,168,40\nR 164,186,32768\ns_waitcnt vmcnt(15)\nM 32,172,56\n"
"buffer_load_dwordx4 a[116:119], v252, s[20:23], s65 offen offset:3072 sc1\ns_barrier\nM 48,168,44\nR 166,186,36864\nR 64,238,40960\nR 66,238,45056\n"
"s_waitcnt vmcnt(15)\nM 48,172,60\nG 6\nM 52,176,44\nR 168,187,24576\nR 68,238,41984\n"
"R 70,238,46080\nM 52,180,60\nbuffer_load_dwordx4 a[120:123], v252, s[20:23], s66 offen offset:3072 sc1\nM 36,176,40\nR 170,187,28672\nM 36,180,56\n"
"M 20,176,36\nR 172,187,32768\nM 20,180,52\nbuffer_load_dwordx4 a[124:127], v252, s[20:23], s67 offen offset:3072 sc1\nM 4,176,32\nR 174,187,36864\n"
"G 6\nv_mfma_f32_16x16x32_bf16 v[72:75], v[242:245], v[64:67], 0\nM 4,180,48\nG 3\nv_mfma_f32_16x16x32_bf16 v[72:75], v[242:245], v[68:71], v[72:75]\nM 8,160,32\n"
"R 176,184,40960\nM 8,164,48\ns_add_u32 s64, s70, 2105344\nbuffer_load_dwordx4 a[128:131], v252, s[20:23], s64 offen offset:0 sc1\nM 24,160,36\nR 178,184,45056\n"
"M 24,164,52\nv_lshrrev_b32 v64, 1, v252\nv_mov_b32 v73, s46\ns_and_b32 s61, s40, 3\ns_lshl_b32 s61, s61, 7\ns_add_u32 s61, s61, s91\n"
"s_add_u32 s61, s61, 524288\ns_mov_b64 exec, 0xffff\nbuffer_store_dwordx2 v[72:73], v64, s[20:23], s61 offen sc1\ns_mov_b64 exec, -1\nM 40,160,40\nLpg6:\n"
"s_cbranch_execz LretO0\nR 180,184,49152\nM 40,164,56\ns_add_u32 s65, s70, 2138112\nbuffer_load_dwordx4 a[132:135], v252, s[20:23], s65 offen offset:0 sc1\nM 56,160,44\n"
"R 182,184,53248\nM 56,164,60\ns_lshl_b32 s60, s40, 5\nv_xor_b32 v238, s60, v188\nG 4\nM 60,168,44\n"
"R 160,185,40960\nv_and_b32 v238, 0xfffffbff, v238\nM 60,172,60\ns_add_u32 s66, s71, 2105344\nbuffer_load_dwordx4 a[136:139], v252, s[20:23], s66 offen offset:0 sc1\nM 44,168,40\n"
"R 162,185,45056\nM 44,172,56\nM 28,168,36\nR 164,185,49152\nM 28,172,52\ns_add_u32 s67, s71, 2138112\n"
"buffer_load_dwordx4 a[140:143], v252, s[20:23], s67 offen offset:0 sc1\nM 12,168,32\nR 166,185,53248\nM 12,172,48\ns_barrier\nG 4\n"
"s_waitcnt vmcnt(21)\nM 0,176,64\nR 168,186,40960\nR 64,238,49152\nR 66,238,53248\ns_waitcnt vmcnt(17)\n"
"M 0,180,80\nbuffer_load_dwordx4 a[144:147], v252, s[20:23], s64 offen offset:1024 sc1\nM 16,176,68\nR 170,186,45056\nR 68,238,50176\nR 70,238,54272\n"
"s_waitcnt vmcnt(17)\nM 16,180,84\nM 32,176,72\nR 172,186,49152\ns_waitcnt vmcnt(16)\nM 32,180,88\n"
"buffer_load_dwordx4 a[148:151], v252, s[20:23], s65 offen offset:1024 sc1\nM 48,176,76\nR 174,186,53248\ns_waitcnt vmcnt(16)\nM 48,180,92\nG 8\n"
"M 52,160,76\nR 176,187,40960\nG 6\nv_mfma_f32_16x16x32_bf16 v[72:75], v[242:245], v[64:67], 0\nM 52,164,92\nbuffer_load_dwordx4 a[152:155], v252, s[20:23], s66 offen offset:1024 sc1\n"
"G 3\nv_mfma_f32_16x16x32_bf16 v[72:75], v[242:245], v[68:71], v[72:75]\ns_add_u32 m0, s50, 98304\ns_add_u32 s63, s53, 720896\nM 36,160,72\nR 178,187,45056\n"
"M 36,164,88\nbuffer_load_dwordx4 v220, s[8:11], s63 offen lds\nM 20,160,68\nR 180,187,49152\nM 20,164,84\nbuffer_load_dwordx4 a[156:159], v252, s[20:23], s67 offen offset:1024 sc1\n"
"v_lshrrev_b32 v64, 1, v252\nM 4,160,64\nR 182,187,53248\nv_mov_b32 v73, s46\ns_and_b32 s61, s40, 3\ns_lshl_b32 s61, s61, 7\n"
"s_add_u32 s61, s61, s91\ns_add_u32 s61, s61, 655360\ns_mov_b64 exec, 0xffff\nbuffer_store_dwordx2 v[72:73], v64, s[20:23], s61 offen sc1\ns_mov_b64 exec, -1\nM 4,164,80\n"
"G 4\nM 8,168,64\nR 160,184,57344\nM 8,172,80\nbuffer_load_dwordx4 a[160:163], v252, s[20:23], s64 offen offset:2048 sc1\nM 24,168,68\n"
"R 162,184,61440\nbuffer_load_dwordx4 v221, s[8:11], s63 offen offset:1024 lds\nM 24,172,84\nM 40,168,72\nR 164,188,0\nM 40,172,88\n"
"buffer_load_dwordx4 a[164:167], v252, s[20:23], s65 offen offset:2048 sc1\nM 56,168,76\nR 166,188,4096\nM 56,172,92\ns_and_b32 s60, s41, 1\ns_lshl1_add_u32 s60, s60, s90\n"
"s_lshl_b32 s60, s60, 5\nv_xor_b32 v238, s60, v184\nG 4\nM 60,176,76\nR 168,185,57344\nv_and_b32 v238, 0xfffffbff, v238\n"
"M 60,180,92\nbuffer_load_dwordx4 a[168:171], v252, s[20:23], s66 offen offset:2048 sc1\nM 44,176,72\nR 170,185,61440\nR 64,238,0\nR 66,238,4096\n"
"M 44,180,88\ns_add_u32 m0, s50, 106496\nM 28,176,68\nR 172,189,0\nR 68,238,1024\nR 70,238,5120\n"
"M 28,180,84\nbuffer_load_dwordx4 a[172:175], v252, s[20:23], s67 offen offset:2048 sc1\nM 12,176,64\nR 174,189,4096\ns_add_u32 s63, s53, 786432\nM 12,180,80\n"
"s_barrier\nG 8\ns_waitcnt vmcnt(24)\nM 0,160,96\nR 176,186,57344\ns_waitcnt vmcnt(19)\n"
"M 0,164,112\nbuffer_load_dwordx4 a[176:179], v252, s[20:23], s64 offen offset:3072 sc1\nbuffer_load_dwordx4 v220, s[8:11], s63 offen lds\nM 16,160,100\nR 178,186,61440\nG 6\n"
"v_mfma_f32_16x16x32_bf16 v[72:75], v[242:245], v[64:67], 0\ns_waitcnt vmcnt(20)\nM 16,164,116\nbuffer_load_dwordx4 v221, s[8:11], s63 offen offset:1024 lds\nG 3\nv_mfma_f32_16x16x32_bf16 v[72:75], v[242:245], v[68:71], v[72:75]\n"
"M 32,160,104\nR 180,190,0\ns_lshr_b32 s60, s45, 3\ns_lshl2_add_u32 s60, s60, s40\ns_lshl_b32 s60, s60, 8\nv_lshl_add_u32 v224, v252, 3, s60\n"
"v_add_u32 v224, Lqw0+24576-LqPC, v224\nv_min_u32 v224, LqE-LqPC-4, v224\ns_mov_b64 exec, 3\nglobal_load_dword v224, v224, s[98:99]\ns_mov_b64 exec, -1\ns_add_u32 s63, s53, 851968\n"
"s_waitcnt vmcnt(21)\nM 32,164,120\nbuffer_load_dwordx4 a[180:183], v252, s[20:23], s65 offen offset:3072 sc1\nM 48,160,108\nR 182,190,4096\ns_add_u32 m0, s50, 114688\n"
"s_waitcnt vmcnt(21)\nM 48,164,124\nbuffer_load_dwordx4 v220, s[8:11], s63 offen lds\nG 4\nv_lshrrev_b32 v64, 1, v252\nM 52,168,108\n"
"R 160,187,57344\nv_mov_b32 v73, s46\ns_and_b32 s61, s40, 1\ns_lshl_b32 s61, s61, 7\ns_add_u32 s61, s61, s91\ns_add_u32 s61, s61, 786432\n"
"s_mov_b64 exec, 0xffff\nbuffer_store_dwordx2 v[72:73], v64, s[20:23], s61 offen sc1\ns_mov_b64 exec, -1\nM 52,172,124\nbuffer_load_dwordx4 a[184:187], v252, s[20:23], s66 offen offset:3072 sc1\nM 36,168,104\n"
"R 162,187,61440\nM 36,172,120\nbuffer_load_dwordx4 v221, s[8:11], s63 offen offset:1024 lds\nM 20,168,100\nR 164,191,0\nM 20,172,116\n"
"buffer_load_dwordx4 a[188:191], v252, s[20:23], s67 offen offset:3072 sc1\nM 4,168,96\nR 166,191,4096\nM 4,172,112\nG 4\nM 8,176,96\n"
"R 168,188,8192\nM 8,180,112\ns_add_u32 s64, s70, 2109440\nbuffer_load_dwordx4 a[192:195], v252, s[20:23], s64 offen offset:0 sc1\nM 24,176,100\nR 170,188,12288\n"
"M 24,180,116\nM 40,176,104\nR 172,188,16384\nM 40,180,120\ns_add_u32 s65, s70, 2142208\nbuffer_load_dwordx4 a[196:199], v252, s[20:23], s65 offen offset:0 sc1\n"
"M 56,176,108\nR 174,188,20480\nM 56,180,124\nG 4\nM 60,160,108\nR 176,189,8192\n"
"M 60,164,124\ns_add_u32 s66, s71, 2109440\nbuffer_load_dwordx4 a[200:203], v252, s[20:23], s66 offen offset:0 sc1\nM 44,160,104\nR 178,189,12288\ns_add_u32 s63, s53, 917504\n"
"M 44,164,120\ns_add_u32 m0, s50, 0\nM 28,160,100\nR 180,189,16384\nM 28,164,116\ns_add_u32 s67, s71, 2142208\n"
"buffer_load_dwordx4 a[204:207], v252, s[20:23], s67 offen offset:0 sc1\nM 12,160,96\nR 182,189,20480\nM 12,164,112\ns_waitcnt vmcnt(17)\ns_barrier\n"
"buffer_load_dwordx4 v220, s[8:11], s63 offen lds\nG 4\nM 0,168,128\nR 160,190,8192\nbuffer_load_dwordx4 v221, s[8:11], s63 offen offset:1024 lds\nM 0,172,144\n"
"buffer_load_dwordx4 a[208:211], v252, s[20:23], s64 offen offset:1024 sc1\ns_add_u32 m0, s50, 8192\ns_add_u32 s63, s53, 983040\nbuffer_load_dwordx4 v220, s[8:11], s63 offen lds\nM 16,168,132\nR 162,190,12288\n"
"buffer_load_dwordx4 v221, s[8:11], s63 offen offset:1024 lds\nM 16,172,148\nM 32,168,136\nR 164,190,16384\nM 32,172,152\nbuffer_load_dwordx4 a[212:215], v252, s[20:23], s65 offen offset:1024 sc1\n"
"M 48,168,140\nR 166,190,20480\nM 48,172,156\nG 4\nM 52,176,140\nR 168,191,8192\n"
"M 52,180,156\nbuffer_load_dwordx4 a[216:219], v252, s[20:23], s66 offen offset:1024 sc1\nM 36,176,136\nR 170,191,12288\nM 36,180,152\nM 20,176,132\n"
"R 172,191,16384\nM 20,180,148\nbuffer_load_dwordx4 a[220:223], v252, s[20:23], s67 offen offset:1024 sc1\nM 4,176,128\nR 174,191,20480\nM 4,180,144\n"
"G 4\nM 8,160,128\nR 176,188,24576\nM 8,164,144\nbuffer_load_dwordx4 a[224:227], v252, s[20:23], s64 offen offset:2048 sc1\nM 24,160,132\n"
"R 178,188,28672\nM 24,164,148\nM 40,160,136\nR 180,188,32768\nM 40,164,152\nbuffer_load_dwordx4 a[228:231], v252, s[20:23], s65 offen offset:2048 sc1\n"
"M 56,160,140\nR 182,188,36864\nM 56,164,156\nG 4\nM 60,168,140\nR 160,189,24576\n"
"M 60,172,156\nbuffer_load_dwordx4 a[232:235], v252, s[20:23], s66 offen offset:2048 sc1\nM 44,168,136\nR 162,189,28672\nM 44,172,152\nM 28,168,132\n"
"R 164,189,32768\nM 28,172,148\nbuffer_load_dwordx4 a[236:239], v252, s[20:23], s67 offen offset:2048 sc1\nM 12,168,128\nR 166,189,36864\nM 12,172,144\n"
"G 4\nM 0,176,160\nR 168,190,24576\ns_waitcnt vmcnt(25)\nM 0,180,176\nbuffer_load_dwordx4 a[240:243], v252, s[20:23], s64 offen offset:3072 sc1\n"
"M 16,176,164\nR 170,190,28672\ns_waitcnt vmcnt(22)\nM 16,180,180\nM 32,176,168\nR 172,190,32768\n"
"s_waitcnt vmcnt(19)\nM 32,180,184\nbuffer_load_dwordx4 a[244:247], v252, s[20:23], s65 offen offset:3072 sc1\nM 48,176,172\nR 174,190,36864\ns_waitcnt vmcnt(18)\n"
"M 48,180,188\nG 4\nM 52,160,172\nR 176,191,24576\nM 52,164,188\nbuffer_load_dwordx4 a[248:251], v252, s[20:23], s66 offen offset:3072 sc1\n"
"M 36,160,168\nR 178,191,28672\ns_waitcnt vmcnt(10)\ns_barrier\nM 36,164,184\nM 20,160,164\n"
"R 180,191,32768\nM 20,164,180\nbuffer_load_dwordx4 a[252:255], v252, s[20:23], s67 offen offset:3072 sc1\nM 4,160,160\nR 182,191,36864\nM 4,164,176\n"
"G 4\nM 8,168,160\nR 160,188,40960\nM 8,172,176\nM 24,168,164\nR 162,188,45056\n"
"M 24,172,180\nM 40,168,168\nR 164,188,49152\nM 40,172,184\nM 56,168,172\nR 166,188,53248\n"
"M 56,172,188\nG 4\nM 60,176,172\nR 168,189,40960\nM 60,180,188\ns_movk_i32 s63, 7168\n"
"v_mad_u32_u24 v220, v228, s63, v229\nv_add_u32 v221, 3668992, v220\ns_add_u32 m0, s50, 16384\ns_add_u32 s63, s54, 0\nbuffer_load_dwordx4 v220, s[12:15], s63 offen lds\nM 44,176,168\n"
"R 170,189,45056\nM 44,180,184\nM 28,176,164\nR 172,189,49152\nM 28,180,180\nbuffer_load_dwordx4 v221, s[12:15], s63 offen offset:1024 lds\n"
"M 12,176,160\nR 174,189,53248\ns_add_u32 m0, s50, 24576\nM 12,180,176\nG 4\nM 32,160,200\n"
"R 176,190,40960\ns_add_u32 s63, s54, 229376\ns_waitcnt vmcnt(11)\nM 32,164,216\nR 178,190,45056\nM 48,160,204\n"
"R 180,190,49152\ns_waitcnt vmcnt(10)\nM 48,164,220\nR 182,190,53248\nG 4\nM 52,168,204\n"
"R 160,191,40960\nbuffer_load_dwordx4 v220, s[12:15], s63 offen lds\nM 52,172,220\nR 162,191,45056\nM 36,168,200\nR 164,191,49152\n"
"M 36,172,216\nR 166,191,53248\nG 4\nM 40,176,200\nR 168,184,0\nbuffer_load_dwordx4 v221, s[12:15], s63 offen offset:1024 lds\n"
"M 40,180,216\nR 170,184,4096\ns_add_u32 m0, s50, 32768\nM 56,176,204\nR 172,184,8192\ns_add_u32 s63, s54, 128\n"
"M 56,180,220\nR 174,184,12288\nG 4\nM 60,160,204\nR 176,185,0\nbuffer_load_dwordx4 v220, s[12:15], s63 offen lds\n"
"M 60,164,220\nR 178,185,4096\nM 44,160,200\nR 180,185,8192\nM 44,164,216\nR 182,185,12288\n"
"G 4\ns_waitcnt vmcnt(10)\nM 32,168,232\nR 160,186,0\nbuffer_load_dwordx4 v221, s[12:15], s63 offen offset:1024 lds\ns_waitcnt vmcnt(7)\n"
"M 32,172,248\nR 162,186,4096\ns_add_u32 m0, s50, 40960\nM 48,168,236\nR 164,186,8192\ns_add_u32 s63, s54, 229504\n"
"s_waitcnt vmcnt(6)\nM 48,172,252\nR 166,186,12288\nG 4\nM 52,176,236\nR 168,187,0\n"
"buffer_load_dwordx4 v220, s[12:15], s63 offen lds\nM 52,180,252\nR 170,187,4096\nM 36,176,232\nR 172,187,8192\nM 36,180,248\n"
"R 174,187,12288\nG 4\nM 40,160,232\nR 176,188,40960\nbuffer_load_dwordx4 v221, s[12:15], s63 offen offset:1024 lds\nM 40,164,248\n"
"R 178,188,45056\nM 56,160,236\nR 180,188,49152\nM 56,164,252\nR 182,188,53248\nG 4\n"
"M 60,168,236\nR 160,189,40960\nM 60,172,252\nR 162,189,45056\nM 44,168,232\nR 164,189,49152\n"
"M 44,172,248\nR 166,189,53248\nG 4\nM 0,176,192\nR 168,190,40960\nM 0,180,208\n"
"R 170,190,45056\ns_mov_b32 m0, s51\nM 16,176,196\nR 172,190,49152\nv_cvt_pk_bf16_f32 v32, v32, v33\nv_cvt_pk_bf16_f32 v33, v34, v35\n"
"M 16,180,212\nR 174,190,53248\nG 4\nv_cvt_pk_bf16_f32 v34, v36, v37\nM 20,160,196\nR 176,191,40960\n"
"v_cvt_pk_bf16_f32 v35, v38, v39\nv_cvt_pk_bf16_f32 v36, v40, v41\nM 20,164,212\nR 178,191,45056\nv_cvt_pk_bf16_f32 v37, v42, v43\nv_cvt_pk_bf16_f32 v38, v44, v45\n"
"M 4,160,192\nR 180,191,49152\nv_cvt_pk_bf16_f32 v39, v46, v47\nv_cvt_pk_bf16_f32 v40, v48, v49\nM 4,164,208\nR 182,191,53248\n"
"G 4\nv_cvt_pk_bf16_f32 v41, v50, v51\nM 8,168,192\nR 160,184,0\nv_cvt_pk_bf16_f32 v42, v52, v53\nv_cvt_pk_bf16_f32 v43, v54, v55\n"
"M 8,172,208\nR 162,184,4096\nv_cvt_pk_bf16_f32 v44, v56, v57\nv_cvt_pk_bf16_f32 v45, v58, v59\nM 24,168,196\nR 164,184,8192\n"
"v_cvt_pk_bf16_f32 v46, v60, v61\nv_cvt_pk_bf16_f32 v47, v62, v63\nM 24,172,212\nR 166,184,12288\nds_write_addtid_b32 v32 offset:0\nG 5\n"
"M 28,176,196\nR 168,185,0\nds_write_addtid_b32 v33 offset:256\nM 28,180,212\nR 170,185,4096\nds_write_addtid_b32 v34 offset:512\n"
"M 12,176,192\nR 172,185,8192\nds_write_addtid_b32 v35 offset:768\nds_write_addtid_b32 v36 offset:1024\nds_write_addtid_b32 v37 offset:1280\nds_write_addtid_b32 v38 offset:1536\n"
"ds_write_addtid_b32 v39 offset:1792\nds_write_addtid_b32 v40 offset:2048\nds_write_addtid_b32 v41 offset:2304\nds_write_addtid_b32 v42 offset:2560\nds_write_addtid_b32 v43 offset:2816\nds_write_addtid_b32 v44 offset:3072\n"
"ds_write_addtid_b32 v45 offset:3328\nds_write_addtid_b32 v46 offset:3584\nds_write_addtid_b32 v47 offset:3840\nM 12,180,208\nR 174,185,12288\nG 15\n"
"M 0,160,224\nR 176,186,0\ns_add_u32 m0, s50, 49152\nM 0,164,240\nR 178,186,4096\nG 3\n"
"s_barrier\ns_add_u32 s63, s54, 458752\nM 16,160,228\nR 180,186,8192\nbuffer_load_dwordx4 v220, s[12:15], s63 offen lds\nds_read2st64_b32 v[32:33], v225 offset0:0 offset1:1\n"
"M 16,164,244\nR 182,186,12288\nds_read2st64_b32 v[34:35], v225 offset0:2 offset1:3\nG 6\nM 20,168,228\nR 160,187,0\n"
"ds_read2st64_b32 v[36:37], v225 offset0:4 offset1:5\nM 20,172,244\nR 162,187,4096\nds_read2st64_b32 v[38:39], v225 offset0:6 offset1:7\nM 4,168,224\nR 164,187,8192\n"
"buffer_load_dwordx4 v221, s[12:15], s63 offen offset:1024 lds\nds_read2st64_b32 v[40:41], v225 offset0:8 offset1:9\nM 4,172,240\nR 166,187,12288\ns_add_u32 m0, s50, 57344\ns_add_u32 s63, s54, 688128\n"
"ds_read2st64_b32 v[42:43], v225 offset0:10 offset1:11\nG 9\nM 8,176,224\nbuffer_load_dwordx4 v220, s[12:15], s63 offen lds\nds_read2st64_b32 v[44:45], v225 offset0:12 offset1:13\nLpg7:\n"
"s_cbranch_execz LretO0\nM 8,180,240\nds_read2st64_b32 v[46:47], v225 offset0:14 offset1:15\nM 24,176,228\nM 24,180,244\nG 3\n"
"M 28,160,228\nbuffer_load_dwordx4 v221, s[12:15], s63 offen offset:1024 lds\nM 28,164,244\nM 12,160,224\nM 12,164,240\nG 0\n"
"s_barrier\nv_mfma_f32_16x16x32_bf16 v[0:3], v[208:211], v[32:35], v[0:3]\nv_mfma_f32_16x16x32_bf16 v[4:7], v[210:213], v[32:35], v[4:7]\nv_mfma_f32_16x16x32_bf16 v[8:11], v[208:211], v[36:39], v[8:11]\nv_mfma_f32_16x16x32_bf16 v[12:15], v[210:213], v[36:39], v[12:15]\nv_mfma_f32_16x16x32_bf16 v[16:19], v[208:211], v[40:43], v[16:19]\n"
"v_mfma_f32_16x16x32_bf16 v[20:23], v[210:213], v[40:43], v[20:23]\nv_mfma_f32_16x16x32_bf16 v[24:27], v[208:211], v[44:47], v[24:27]\nv_mfma_f32_16x16x32_bf16 v[28:31], v[210:213], v[44:47], v[28:31]\ns_lshl_b32 s84, s75, 11\ns_add_u32 s84, s84, s49\ns_add_u32 s84, s84, 10485760\n"
"s_add_u32 s85, s84, 32768\nv_mov_b32 v238, s46\nv_mov_b32 v239, s47\nv_cvt_pk_bf16_f32 v192, v0, v1\nv_cvt_pk_bf16_f32 v193, v2, v3\nv_cvt_pk_bf16_f32 v194, v4, v5\n"
"v_cvt_pk_bf16_f32 v195, v6, v7\nbuffer_store_dwordx4 v[192:195], v252, s[20:23], s84 offen offset:0 sc1\nv_cvt_pk_bf16_f32 v196, v8, v9\nv_cvt_pk_bf16_f32 v197, v10, v11\nv_cvt_pk_bf16_f32 v198, v12, v13\nv_cvt_pk_bf16_f32 v199, v14, v15\n"
"buffer_store_dwordx4 v[196:199], v252, s[20:23], s84 offen offset:1024 sc1\nv_cvt_pk_bf16_f32 v200, v16, v17\nv_cvt_pk_bf16_f32 v201, v18, v19\nv_cvt_pk_bf16_f32 v202, v20, v21\nv_cvt_pk_bf16_f32 v203, v22, v23\nbuffer_store_dwordx4 v[200:203], v252, s[20:23], s85 offen offset:0 sc1\n"
"v_cvt_pk_bf16_f32 v204, v24, v25\nv_cvt_pk_bf16_f32 v205, v26, v27\nv_cvt_pk_bf16_f32 v206, v28, v29\nv_cvt_pk_bf16_f32 v207, v30, v31\nbuffer_store_dwordx4 v[204:207], v252, s[20:23], s85 offen offset:1024 sc1\ns_mov_b64 exec, 0\n"
"s_cmp_eq_u32 s40, 1\ns_cbranch_scc1 Lpg9\ns_cmp_eq_u32 s40, 2\ns_cbranch_scc1 Lpg10\ns_cmp_eq_u32 s40, 3\ns_cbranch_scc1 Lpg11\n"
"s_branch Lpg8\nLretO0n:\ns_mov_b64 exec, -1\ns_add_u32 s61, s79, 88219648\ns_lshl_b32 s62, s75, 3\ns_add_u32 s61, s61, s62\n"
"s_waitcnt vmcnt(0)\ns_mov_b64 exec, 1\nbuffer_atomic_swap_x2 v[238:239], off, s[20:23], s61 sc1\ns_mov_b64 exec, -1\nLqw0:\nv_bfe_u32 v250, v252, 7, 1\n"
"v_mul_u32_u24 v250, 2176, v250\nv_bfe_u32 v251, v252, 4, 3\nv_lshl_add_u32 v250, v251, 3, v250\ns_lshl_b32 s60, s90, 6\nv_add_u32 v250, s60, v250\ns_sleep 3\n"
"v_mov_b32 v248, 0\nv_mov_b32 v249, 0\ns_add_u32 s61, s80, 88219648\ns_mov_b64 exec, 0xffff\nbuffer_atomic_or_x2 v[248:249], v250, s[20:23], s61 offen sc0 sc1\ns_mov_b64 exec, -1\n"
"s_add_u32 m0, s50, 65536\ns_add_u32 s63, s54, 458880\nbuffer_load_dwordx4 v220, s[12:15], s63 offen lds\nbuffer_load_dwordx4 v221, s[12:15], s63 offen offset:1024 lds\ns_add_u32 m0, s50, 73728\ns_add_u32 s63, s54, 688256\n"
"buffer_load_dwordx4 v220, s[12:15], s63 offen lds\nbuffer_load_dwordx4 v221, s[12:15], s63 offen offset:1024 lds\ns_add_u32 m0, s50, 81920\ns_add_u32 s63, s54, 917504\nbuffer_load_dwordx4 v220, s[12:15], s63 offen lds\nbuffer_load_dwordx4 v221, s[12:15], s63 offen offset:1024 lds\n"
"s_add_u32 m0, s50, 90112\ns_add_u32 s63, s54, 1146880\nbuffer_load_dwordx4 v220, s[12:15], s63 offen lds\nbuffer_load_dwordx4 v221, s[12:15], s63 offen offset:1024 lds\ns_add_u32 m0, s50, 98304\ns_add_u32 s63, s54, 917632\n"
"buffer_load_dwordx4 v220, s[12:15], s63 offen lds\nbuffer_load_dwordx4 v221, s[12:15], s63 offen offset:1024 lds\ns_waitcnt vmcnt(10)\nv_cmp_eq_u64 s[86:87], s[46:47], v[248:249]\ns_nop 1\ns_and_b32 s88, s86, 0xffff\n"
"s_cmp_eq_u32 s88, 0xffff\ns_cbranch_scc1 Lq265\nv_mov_b32 v246, 0\nv_mov_b32 v247, 0\ns_add_u32 s61, s80, 88219648\ns_mov_b64 exec, 0xffff\n"
"buffer_atomic_or_x2 v[246:247], v250, s[20:23], s61 offen sc0 sc1\ns_mov_b64 exec, -1\nLq265:\ns_add_u32 m0, s50, 106496\ns_add_u32 s63, s54, 1147008\nbuffer_load_dwordx4 v220, s[12:15], s63 offen lds\n"
"buffer_load_dwordx4 v221, s[12:15], s63 offen offset:1024 lds\ns_mov_b64 exec, 0\ns_cmp_eq_u32 s40, 1\ns_cbranch_scc1 Lpg17\ns_cmp_eq_u32 s40, 2\ns_cbranch_scc1 LretW0z\n"
"s_cmp_eq_u32 s40, 3\ns_cbranch_scc1 LretW0z\ns_branch Lpg16\nLretW0z:\ns_mov_b64 exec, -1\nv_mov_b32 v230, 0\n"
"v_mov_b32 v231, 0\nv_mov_b32 v232, 0\nv_mov_b32 v233, 0\nv_dot2c_f32_bf16 v230, s76, v192\nv_dot2c_f32_bf16 v231, v192, v192\nv_dot2c_f32_bf16 v232, s76, v200\n"
"v_dot2c_f32_bf16 v233, v200, v200\nv_dot2c_f32_bf16 v230, s76, v193\nv_dot2c_f32_bf16 v231, v193, v193\nv_dot2c_f32_bf16 v232, s76, v201\nv_dot2c_f32_bf16 v233, v201, v201\nv_dot2c_f32_bf16 v230, s76, v194\n"
"v_dot2c_f32_bf16 v231, v194, v194\nv_dot2c_f32_bf16 v232, s76, v202\nv_dot2c_f32_bf16 v233, v202, v202\nv_dot2c_f32_bf16 v230, s76, v195\nv_dot2c_f32_bf16 v231, v195, v195\nv_dot2c_f32_bf16 v232, s76, v203\n"
"v_dot2c_f32_bf16 v233, v203, v203\nv_dot2c_f32_bf16 v230, s76, v196\nv_dot2c_f32_bf16 v231, v196, v196\nv_dot2c_f32_bf16 v232, s76, v204\nv_dot2c_f32_bf16 v233, v204, v204\nv_dot2c_f32_bf16 v230, s76, v197\n"
"v_dot2c_f32_bf16 v231, v197, v197\nv_dot2c_f32_bf16 v232, s76, v205\nv_dot2c_f32_bf16 v233, v205, v205\nv_dot2c_f32_bf16 v230, s76, v198\nv_dot2c_f32_bf16 v231, v198, v198\nv_dot2c_f32_bf16 v232, s76, v206\n"
"v_dot2c_f32_bf16 v233, v206, v206\nv_dot2c_f32_bf16 v230, s76, v199\nv_dot2c_f32_bf16 v231, v199, v199\nv_dot2c_f32_bf16 v232, s76, v207\nv_dot2c_f32_bf16 v233, v207, v207\nv_lshrrev_b32 v234, 5, v252\n"
"v_and_b32 v234, 24, v234\nv_bfe_u32 v235, s46, v234, 8\nv_lshrrev_b32 v234, 1, v234\nv_and_b32 v236, 0xf0, v252\nv_add_u32 v234, v234, v236\nv_permlane32_swap_b32 v230, v231\n"
"v_permlane32_swap_b32 v232, v233\nv_add_f32 v230, v230, v231\nv_add_f32 v232, v232, v233\ns_nop 1\nv_permlane16_swap_b32 v230, v232\nv_add_f32 v230, v230, v232\n"
"s_movk_i32 s62, 0xff\nv_bfi_b32 v230, s62, v235, v230\ns_lshr_b32 s61, s49, 4\ns_lshl1_add_u32 s61, s53, s61\ns_add_u32 s61, s61, 82313216\nbuffer_store_dword v230, v234, s[20:23], s61 offen sc1\n"
"s_cmp_eq_u32 s88, 0xffff\ns_cbranch_scc1 Lq266\ns_waitcnt vmcnt(3)\nv_mov_b32 v248, v246\nv_mov_b32 v249, v247\nv_cmp_eq_u64 s[86:87], s[46:47], v[248:249]\n"
"s_nop 1\ns_and_b32 s62, s86, 0xffff\ns_cmp_eq_u32 s62, 0xffff\ns_cbranch_scc1 Lq257\nLq256:\nv_mov_b32 v248, 0\n"
"v_mov_b32 v249, 0\ns_add_u32 s61, s80, 88219648\ns_mov_b64 exec, 0xffff\nbuffer_atomic_or_x2 v[248:249], v250, s[20:23], s61 offen sc0 sc1\ns_mov_b64 exec, -1\ns_waitcnt vmcnt(0)\n"
"v_cmp_eq_u64 s[86:87], s[46:47], v[248:249]\ns_nop 1\ns_and_b32 s62, s86, 0xffff\ns_cmp_eq_u32 s62, 0xffff\ns_cbranch_scc1 Lq257\ns_sub_u32 s48, s48, 1\n"
"s_cmp_gt_i32 s48, 0\ns_cbranch_scc1 Lq256\nLq257:\nLq266:\ns_add_u32 s64, s70, 10485760\nbuffer_load_dwordx4 a[0:3], v252, s[20:23], s64 offen offset:0 sc1\n"
"s_add_u32 s65, s70, 10518528\nbuffer_load_dwordx4 a[4:7], v252, s[20:23], s65 offen offset:0 sc1\ns_add_u32 s66, s71, 10485760\nbuffer_load_dwordx4 a[8:11], v252, s[20:23], s66 offen offset:0 sc1\ns_add_u32 s67, s71, 10518528\nbuffer_load_dwordx4 a[12:15], v252, s[20:23], s67 offen offset:0 sc1\n"
"buffer_load_dwordx4 a[16:19], v252, s[20:23], s64 offen offset:1024 sc1\nbuffer_load_dwordx4 a[20:23], v252, s[20:23], s65 offen offset:1024 sc1\nbuffer_load_dwordx4 a[24:27], v252, s[20:23], s66 offen offset:1024 sc1\nbuffer_load_dwordx4 a[28:31], v252, s[20:23], s67 offen offset:1024 sc1\ns_barrier\nR 160,184,16384\n"
"R 162,184,20480\nR 164,184,24576\nR 166,184,28672\nR 168,185,16384\nR 170,185,20480\nR 172,185,24576\n"
"R 174,185,28672\nR 176,186,16384\nR 178,186,20480\nR 180,186,24576\nR 182,186,28672\nbuffer_load_dwordx4 a[32:35], v252, s[20:23], s64 offen offset:2048 sc1\n"
"buffer_load_dwordx4 a[36:39], v252, s[20:23], s65 offen offset:2048 sc1\nbuffer_load_dwordx4 a[40:43], v252, s[20:23], s66 offen offset:2048 sc1\nbuffer_load_dwordx4 a[44:47], v252, s[20:23], s67 offen offset:2048 sc1\nbuffer_load_dwordx4 a[48:51], v252, s[20:23], s64 offen offset:3072 sc1\nbuffer_load_dwordx4 a[52:55], v252, s[20:23], s65 offen offset:3072 sc1\nbuffer_load_dwordx4 a[56:59], v252, s[20:23], s66 offen offset:3072 sc1\n"
"buffer_load_dwordx4 a[60:63], v252, s[20:23], s67 offen offset:3072 sc1\ns_waitcnt vmcnt(15) lgkmcnt(10)\nv_mfma_f32_16x16x32_bf16 v[32:35], v[160:163], a[0:3], 0\ns_add_u32 s64, s70, 10489856\nbuffer_load_dwordx4 a[64:67], v252, s[20:23], s64 offen offset:0 sc1\ns_waitcnt vmcnt(12) lgkmcnt(8)\n"
"M 32,164,16\nv_mfma_f32_16x16x32_bf16 v[48:51], v[160:163], a[4:7], 0\ns_waitcnt vmcnt(11)\nM 48,164,20\nv_mfma_f32_16x16x32_bf16 v[64:67], v[160:163], a[8:11], 0\nv_lshrrev_b32 v238, 7, v252\n"
"s_waitcnt vmcnt(10)\nM 64,164,24\nv_lshlrev_b32 v238, 9, v238\nv_mfma_f32_16x16x32_bf16 v[80:83], v[160:163], a[12:15], 0\ns_add_u32 s65, s70, 10522624\nbuffer_load_dwordx4 a[68:71], v252, s[20:23], s65 offen offset:0 sc1\n"
"s_waitcnt vmcnt(10)\nM 80,164,28\nG 4\nv_and_b32 v239, 0x70, v252\nv_mfma_f32_16x16x32_bf16 v[84:87], v[168:171], a[12:15], 0\nR 160,187,16384\n"
"M 84,172,28\ns_and_b32 s60, s40, 3\ns_lshl_b32 s60, s60, 7\nv_add3_u32 v238, v238, v239, s60\nv_mfma_f32_16x16x32_bf16 v[68:71], v[168:171], a[8:11], 0\nR 162,187,20480\n"
"s_lshl_b32 s61, s42, 13\ns_add_u32 s61, s61, 84279296\nbuffer_load_dwordx4 v[0:3], v238, s[20:23], s61 offen sc1\nM 68,172,24\ns_lshl_b32 s61, s42, 13\ns_add_u32 s61, s61, 84283392\n"
"buffer_load_dwordx4 v[4:7], v238, s[20:23], s61 offen sc1\nv_mfma_f32_16x16x32_bf16 v[52:55], v[168:171], a[4:7], 0\nR 164,187,24576\nM 52,172,20\ns_add_u32 s66, s71, 10489856\nbuffer_load_dwordx4 a[72:75], v252, s[20:23], s66 offen offset:0 sc1\n"
"v_bfe_u32 v239, v238, 2, 7\nv_mfma_f32_16x16x32_bf16 v[36:39], v[168:171], a[0:3], 0\nR 166,187,28672\nM 36,172,16\nbuffer_load_dword v239, v239, s[32:35], s54 offen offset:0\nG 4\n"
"v_mfma_f32_16x16x32_bf16 v[40:43], v[176:179], a[0:3], 0\nR 168,184,32768\nM 40,180,16\nv_mfma_f32_16x16x32_bf16 v[56:59], v[176:179], a[4:7], 0\nR 170,184,36864\nM 56,180,20\n"
"s_add_u32 s67, s71, 10522624\nbuffer_load_dwordx4 a[76:79], v252, s[20:23], s67 offen offset:0 sc1\nv_mfma_f32_16x16x32_bf16 v[72:75], v[176:179], a[8:11], 0\nR 172,184,40960\nM 72,180,24\nv_mfma_f32_16x16x32_bf16 v[88:91], v[176:179], a[12:15], 0\n"
"R 174,184,45056\nM 88,180,28\nG 4\nv_mfma_f32_16x16x32_bf16 v[92:95], v[160:163], a[12:15], 0\nR 176,185,32768\nM 92,164,28\n"
"buffer_load_dwordx4 a[80:83], v252, s[20:23], s64 offen offset:1024 sc1\nv_mfma_f32_16x16x32_bf16 v[76:79], v[160:163], a[8:11], 0\nR 178,185,36864\nM 76,164,24\nv_mfma_f32_16x16x32_bf16 v[60:63], v[160:163], a[4:7], 0\nR 180,185,40960\n"
"M 60,164,20\nv_mfma_f32_16x16x32_bf16 v[44:47], v[160:163], a[0:3], 0\nR 182,185,45056\nM 44,164,16\nG 4\nv_mfma_f32_16x16x32_bf16 v[96:99], v[168:171], a[0:3], 0\n"
"R 160,186,32768\nM 96,172,16\nbuffer_load_dwordx4 a[84:87], v252, s[20:23], s65 offen offset:1024 sc1\nv_mfma_f32_16x16x32_bf16 v[112:115], v[168:171], a[4:7], 0\nR 162,186,36864\nM 112,172,20\n"
"v_mfma_f32_16x16x32_bf16 v[128:131], v[168:171], a[8:11], 0\nR 164,186,40960\nM 128,172,24\nbuffer_load_dwordx4 a[88:91], v252, s[20:23], s66 offen offset:1024 sc1\nv_mfma_f32_16x16x32_bf16 v[144:147], v[168:171], a[12:15], 0\nR 166,186,45056\n"
"M 144,172,28\ns_barrier\nG 4\nv_mfma_f32_16x16x32_bf16 v[148:151], v[176:179], a[12:15], 0\nR 168,187,32768\nM 148,180,28\n"
"v_mfma_f32_16x16x32_bf16 v[132:135], v[176:179], a[8:11], 0\nR 170,187,36864\nM 132,180,24\nbuffer_load_dwordx4 a[92:95], v252, s[20:23], s67 offen offset:1024 sc1\nv_mfma_f32_16x16x32_bf16 v[116:119], v[176:179], a[4:7], 0\nR 172,187,40960\n"
"M 116,180,20\nv_mfma_f32_16x16x32_bf16 v[100:103], v[176:179], a[0:3], 0\nR 174,187,45056\nM 100,180,16\nG 4\nv_mfma_f32_16x16x32_bf16 v[104:107], v[160:163], a[0:3], 0\n"
"R 176,184,49152\nv_lshrrev_b32 v242, 7, v252\nM 104,164,16\nbuffer_load_dwordx4 a[96:99], v252, s[20:23], s64 offen offset:2048 sc1\nv_mfma_f32_16x16x32_bf16 v[120:123], v[160:163], a[4:7], 0\nR 178,184,53248\n"
"v_lshlrev_b32 v242, 9, v242\ns_waitcnt vmcnt(8)\nv_min3_u32 v240, v1, v3, v5\nM 120,164,20\nv_and_b32 v243, 0x70, v252\nv_min3_u32 v240, v240, v7, v7\n"
"v_max3_u32 v241, v1, v3, v5\nv_mfma_f32_16x16x32_bf16 v[136:139], v[160:163], a[8:11], 0\nR 180,184,57344\nv_max3_u32 v241, v241, v7, v7\nv_cmp_ne_u32 s[86:87], s46, v240\nM 136,164,24\n"
"s_and_b32 s60, s40, 3\ns_lshl_b32 s60, s60, 7\nv_add3_u32 v242, v242, v243, s60\nv_cmp_ne_u32 vcc, s46, v241\nv_mfma_f32_16x16x32_bf16 v[152:155], v[160:163], a[12:15], 0\nR 182,184,61440\n"
"s_lshl_b32 s61, s42, 13\ns_add_u32 s61, s61, 84410368\nbuffer_load_dwordx4 v[16:19], v242, s[20:23], s61 offen sc1\ns_or_b64 s[86:87], s[86:87], vcc\ns_cbranch_scc1 Lq319\nLq320:\n"
"M 152,164,28\ns_lshl_b32 s61, s42, 13\ns_add_u32 s61, s61, 84414464\nbuffer_load_dwordx4 v[20:23], v242, s[20:23], s61 offen sc1\nG 4\nv_mfma_f32_16x16x32_bf16 v[156:159], v[168:171], a[12:15], 0\n"
"R 160,185,49152\nv_add_f32 v0, v0, v4\nM 156,172,28\nbuffer_load_dwordx4 a[100:103], v252, s[20:23], s65 offen offset:2048 sc1\nv_bfe_u32 v243, v242, 2, 7\nv_mfma_f32_16x16x32_bf16 v[140:143], v[168:171], a[8:11], 0\n"
"R 162,185,53248\nv_add_f32 v2, v2, v6\nM 140,172,24\nbuffer_load_dword v243, v243, s[32:35], s54 offen offset:128\nv_mfma_f32_16x16x32_bf16 v[124:127], v[168:171], a[4:7], 0\nR 164,185,57344\n"
"v_add_f32_dpp v0, v0, v0 row_ror:8 row_mask:0xf bank_mask:0xf\nM 124,172,20\nbuffer_load_dwordx4 a[104:107], v252, s[20:23], s66 offen offset:2048 sc1\nv_mfma_f32_16x16x32_bf16 v[108:111], v[168:171], a[0:3], 0\nR 166,185,61440\nv_add_f32_dpp v2, v2, v2 row_ror:8 row_mask:0xf bank_mask:0xf\n"
"M 108,172,16\nG 4\nv_mov_b32 v4, v0\nv_mov_b32 v5, v2\nM 32,176,32\nR 168,186,49152\n"
"v_permlane32_swap_b32 v4, v0\nM 32,180,48\nv_permlane32_swap_b32 v5, v2\nv_add_f32 v0, v0, v4\nM 48,176,36\nR 170,186,53248\n"
"v_add_f32 v2, v2, v5\nM 48,180,52\nbuffer_load_dwordx4 a[108:111], v252, s[20:23], s67 offen offset:2048 sc1\nM 64,176,40\nR 172,186,57344\nv_mov_b32 v4, v0\n"
"M 64,180,56\nv_mov_b32 v5, v2\nv_permlane16_swap_b32 v4, v0\nM 80,176,44\nR 174,186,61440\nv_permlane16_swap_b32 v5, v2\n"
"M 80,180,60\ns_barrier\nG 4\nM 84,160,44\nR 176,187,49152\nv_add_f32 v0, v0, v4\n"
"M 84,164,60\nbuffer_load_dwordx4 a[112:115], v252, s[20:23], s64 offen offset:3072 sc1\nM 68,160,40\nR 178,187,53248\nv_add_f32 v2, v2, v5\nM 68,164,56\n"
"v_and_b32 v8, s77, v0\nv_sub_f32 v9, v0, v8\nM 52,160,36\nR 180,187,57344\ns_waitcnt vmcnt(13)\nv_lshlrev_b32 v10, 16, v239\n"
"M 52,164,52\nv_cvt_pk_bf16_f32 v4, v8, v9\nv_cvt_pk_bf16_f32 v5, v8, v10\nM 36,160,32\nR 182,187,61440\nv_and_b32 v8, s77, v2\n"
"M 36,164,48\nG 4\nv_sub_f32 v9, v2, v8\nv_and_b32 v10, s77, v239\nM 40,168,32\nR 160,188,0\n"
"v_cvt_pk_bf16_f32 v6, v8, v9\nM 40,172,48\nbuffer_load_dwordx4 a[116:119], v252, s[20:23], s65 offen offset:3072 sc1\nM 56,168,36\nR 162,188,4096\nv_cvt_pk_bf16_f32 v7, v8, v10\n"
"M 56,172,52\nv_and_b32 v238, 0x1ff, v238\nv_add_u32 v238, 157184, v238\nM 72,168,40\nR 164,188,8192\nds_write_b128 v238, v[4:7]\n"
"M 72,172,56\nbuffer_load_dwordx4 a[120:123], v252, s[20:23], s66 offen offset:3072 sc1\nM 88,168,44\nR 166,188,12288\nM 88,172,60\nG 5\n"
"M 92,176,44\nR 168,189,0\nM 92,180,60\nM 76,176,40\nR 170,189,4096\nM 76,180,56\n"
"buffer_load_dwordx4 a[124:127], v252, s[20:23], s67 offen offset:3072 sc1\nM 60,176,36\nR 172,189,8192\nv_lshrrev_b32 v238, 7, v252\nM 60,180,52\nv_lshlrev_b32 v238, 9, v238\n"
"M 44,176,32\nR 174,189,12288\ns_waitcnt vmcnt(8)\nv_min3_u32 v244, v17, v19, v21\nv_min3_u32 v244, v244, v23, v23\nM 44,180,48\n"
"G 4\nv_and_b32 v239, 0x70, v252\nv_max3_u32 v245, v17, v19, v21\nv_max3_u32 v245, v245, v23, v23\nM 96,160,32\nR 176,190,0\n"
"s_and_b32 s60, s40, 3\ns_lshl_b32 s60, s60, 7\nv_add3_u32 v238, v238, v239, s60\nv_cmp_ne_u32 s[86:87], s46, v244\nM 96,164,48\ns_add_u32 s64, s70, 10493952\n"
"buffer_load_dwordx4 a[128:131], v252, s[20:23], s64 offen offset:0 sc1\nv_cmp_ne_u32 vcc, s46, v245\nM 112,160,36\nR 178,190,4096\ns_lshl_b32 s61, s42, 13\ns_add_u32 s61, s61, 84541440\n"
"buffer_load_dwordx4 v[0:3], v238, s[20:23], s61 offen sc1\ns_or_b64 s[86:87], s[86:87], vcc\ns_cbranch_scc1 Lq329\nLq330:\nM 112,164,52\ns_lshl_b32 s61, s42, 13\n"
"s_add_u32 s61, s61, 84545536\nbuffer_load_dwordx4 v[4:7], v238, s[20:23], s61 offen sc1\nv_add_f32 v16, v16, v20\nM 128,160,40\nR 180,190,8192\nv_add_f32 v18, v18, v22\n"
"v_add_f32_dpp v16, v16, v16 row_ror:8 row_mask:0xf bank_mask:0xf\nM 128,164,56\ns_add_u32 m0, s50, 114688\nv_bfe_u32 v239, v238, 2, 7\nv_add_f32_dpp v18, v18, v18 row_ror:8 row_mask:0xf bank_mask:0xf\nv_mov_b32 v20, v16\n"
"M 144,160,44\nR 182,190,12288\ns_add_u32 s63, s54, 1376256\nv_mov_b32 v21, v18\nM 144,164,60\nbuffer_load_dword v239, v239, s[32:35], s54 offen offset:256\n"
"s_barrier\nG 4\nv_permlane32_swap_b32 v20, v16\nM 148,168,44\nR 160,191,0\nv_permlane32_swap_b32 v21, v18\n"
"v_add_f32 v16, v16, v20\nM 148,172,60\ns_add_u32 s65, s70, 10526720\nbuffer_load_dwordx4 a[132:135], v252, s[20:23], s65 offen offset:0 sc1\nv_add_f32 v18, v18, v21\nM 132,168,40\n"
"R 162,191,4096\nv_mov_b32 v20, v16\nv_mov_b32 v21, v18\nM 132,172,56\nbuffer_load_dwordx4 v220, s[12:15], s63 offen lds\nM 116,168,36\n"
"R 164,191,8192\nv_permlane16_swap_b32 v20, v16\nM 116,172,52\ns_add_u32 s66, s71, 10493952\nbuffer_load_dwordx4 a[136:139], v252, s[20:23], s66 offen offset:0 sc1\nM 100,168,32\n"
"R 166,191,12288\nv_permlane16_swap_b32 v21, v18\nM 100,172,48\nG 4\nv_add_f32 v16, v16, v20\nv_add_f32 v18, v18, v21\n"
"M 104,176,32\nR 168,188,16384\nbuffer_load_dwordx4 v221, s[12:15], s63 offen offset:1024 lds\nM 104,180,48\ns_add_u32 m0, s50, 0\ns_add_u32 s63, s54, 1605632\n"
"buffer_load_dwordx4 v220, s[12:15], s63 offen lds\nM 120,176,36\nR 170,188,20480\nv_and_b32 v24, s77, v16\nM 120,180,52\ns_add_u32 s67, s71, 10526720\n"
"buffer_load_dwordx4 a[140:143], v252, s[20:23], s67 offen offset:0 sc1\nM 136,176,40\nR 172,188,24576\nv_sub_f32 v25, v16, v24\nM 136,180,56\nbuffer_load_dwordx4 v221, s[12:15], s63 offen offset:1024 lds\n"
"M 152,176,44\nR 174,188,28672\ns_waitcnt vmcnt(17)\nv_lshlrev_b32 v26, 16, v243\nM 152,180,60\nG 4\n"
"v_cvt_pk_bf16_f32 v20, v24, v25\nv_cvt_pk_bf16_f32 v21, v24, v26\nM 156,160,44\nR 176,189,16384\nv_and_b32 v24, s77, v18\nM 156,164,60\n"
"buffer_load_dwordx4 a[144:147], v252, s[20:23], s64 offen offset:1024 sc1\nM 140,160,40\nR 178,189,20480\nv_sub_f32 v25, v18, v24\nM 140,164,56\nv_and_b32 v26, s77, v243\n"
"v_cvt_pk_bf16_f32 v22, v24, v25\nM 124,160,36\nR 180,189,24576\nv_cvt_pk_bf16_f32 v23, v24, v26\nM 124,164,52\nv_and_b32 v242, 0x1ff, v242\n"
"v_add_u32 v242, 157696, v242\nM 108,160,32\nR 182,189,28672\nds_write_b128 v242, v[20:23]\nM 108,164,48\nG 5\n"
"M 32,168,64\nR 160,190,16384\nM 32,172,80\nbuffer_load_dwordx4 a[148:151], v252, s[20:23], s65 offen offset:1024 sc1\nM 48,168,68\nR 162,190,20480\n"
"M 48,172,84\ns_add_u32 m0, s50, 8192\nM 64,168,72\nR 164,190,24576\nLpg8:\ns_cbranch_execz Lpg12\n"
"s_add_u32 s63, s54, 1376384\nM 64,172,88\nbuffer_load_dwordx4 a[152:155], v252, s[20:23], s66 offen offset:1024 sc1\nM 80,168,76\nR 166,190,28672\nM 80,172,92\n"
"G 4\ns_barrier\nM 84,176,76\nR 168,191,16384\nv_lshrrev_b32 v242, 7, v252\nM 84,180,92\n"
"buffer_load_dwordx4 v220, s[12:15], s63 offen lds\nv_lshlrev_b32 v242, 9, v242\nM 68,176,72\nR 170,191,20480\ns_waitcnt vmcnt(12)\nv_min3_u32 v240, v1, v3, v5\n"
"v_min3_u32 v240, v240, v7, v7\nM 68,180,88\nbuffer_load_dwordx4 a[156:159], v252, s[20:23], s67 offen offset:1024 sc1\nv_and_b32 v243, 0x70, v252\nv_max3_u32 v241, v1, v3, v5\nM 52,176,68\n"
"R 172,191,24576\nv_max3_u32 v241, v241, v7, v7\nv_cmp_ne_u32 s[86:87], s46, v240\nM 52,180,84\ns_and_b32 s60, s40, 1\ns_lshl_b32 s60, s60, 7\n"
"v_add3_u32 v242, v242, v243, s60\nv_cmp_ne_u32 vcc, s46, v241\nM 36,176,64\nR 174,191,28672\ns_lshl_b32 s61, s42, 13\ns_add_u32 s61, s61, 84672512\n"
"buffer_load_dwordx4 v[16:19], v242, s[20:23], s61 offen sc1\ns_or_b64 s[86:87], s[86:87], vcc\ns_cbranch_scc1 Lq339\nLq340:\nM 36,180,80\ns_lshl_b32 s61, s42, 13\n"
"s_add_u32 s61, s61, 84676608\nbuffer_load_dwordx4 v[20:23], v242, s[20:23], s61 offen sc1\nG 4\nv_add_f32 v0, v0, v4\nM 40,160,64\nR 176,188,32768\n"
"buffer_load_dwordx4 v221, s[12:15], s63 offen offset:1024 lds\nv_add_f32 v2, v2, v6\nM 40,164,80\nbuffer_load_dwordx4 a[160:163], v252, s[20:23], s64 offen offset:2048 sc1\ns_add_u32 s63, s54, 1605760\nv_bfe_u32 v243, v242, 2, 7\n"
"v_add_f32_dpp v0, v0, v0 row_ror:8 row_mask:0xf bank_mask:0xf\nM 56,160,68\nR 178,188,36864\ns_add_u32 m0, s50, 16384\nv_add_f32_dpp v2, v2, v2 row_ror:8 row_mask:0xf bank_mask:0xf\nM 56,164,84\n"
"buffer_load_dword v243, v243, s[32:35], s55 offen offset:0\nv_mov_b32 v4, v0\nM 72,160,72\nR 180,188,40960\nbuffer_load_dwordx4 v220, s[12:15], s63 offen lds\nv_mov_b32 v5, v2\n"
"M 72,164,88\nbuffer_load_dwordx4 v221, s[12:15], s63 offen offset:1024 lds\nv_permlane32_swap_b32 v4, v0\nM 88,160,76\nR 182,188,45056\nv_permlane32_swap_b32 v5, v2\n"
"v_add_f32 v0, v0, v4\nM 88,164,92\nG 4\nv_add_f32 v2, v2, v5\nv_mov_b32 v4, v0\nv_mov_b32 v5, v2\n"
"M 92,168,76\nR 160,189,32768\nv_permlane16_swap_b32 v4, v0\nv_permlane16_swap_b32 v5, v2\nM 92,172,92\nbuffer_load_dwordx4 a[164:167], v252, s[20:23], s65 offen offset:2048 sc1\n"
"v_add_f32 v0, v0, v4\nM 76,168,72\nR 162,189,36864\nv_add_f32 v2, v2, v5\nv_and_b32 v8, s77, v0\nM 76,172,88\n"
"v_sub_f32 v9, v0, v8\ns_waitcnt vmcnt(20)\nv_lshlrev_b32 v10, 16, v239\nM 60,168,68\nR 164,189,40960\nv_cvt_pk_bf16_f32 v4, v8, v9\n"
"M 60,172,84\nbuffer_load_dwordx4 a[168:171], v252, s[20:23], s66 offen offset:2048 sc1\nM 44,168,64\nR 166,189,45056\nv_cvt_pk_bf16_f32 v5, v8, v10\nM 44,172,80\n"
"G 4\nv_and_b32 v8, s77, v2\nv_sub_f32 v9, v2, v8\nM 96,176,64\nR 168,190,32768\nv_and_b32 v10, s77, v239\n"
"M 96,180,80\nv_cvt_pk_bf16_f32 v6, v8, v9\nv_cvt_pk_bf16_f32 v7, v8, v10\nM 112,176,68\nR 170,190,36864\nv_and_b32 v238, 0x1ff, v238\n"
"M 112,180,84\nbuffer_load_dwordx4 a[172:175], v252, s[20:23], s67 offen offset:2048 sc1\nM 128,176,72\nR 172,190,40960\nv_add_u32 v238, 158208, v238\nM 128,180,88\n"
"s_add_u32 m0, s50, 24576\nds_write_b128 v238, v[4:7]\nM 144,176,76\nR 174,190,45056\ns_add_u32 s63, s54, 1835008\nM 144,180,92\n"
"s_waitcnt vmcnt(15) lgkmcnt(1)\ns_barrier\nM 148,160,76\nR 176,191,32768\nM 148,164,92\nbuffer_load_dwordx4 a[176:179], v252, s[20:23], s64 offen offset:3072 sc1\n"
"M 132,160,72\nR 178,191,36864\nM 132,164,88\nbuffer_load_dwordx4 v220, s[12:15], s63 offen lds\nM 116,160,68\nR 180,191,40960\n"
"M 116,164,84\nbuffer_load_dwordx4 v221, s[12:15], s63 offen offset:1024 lds\nM 100,160,64\nR 182,191,45056\ns_add_u32 m0, s50, 32768\nM 100,164,80\n"
"G 4\nM 104,168,64\nR 160,188,49152\ns_add_u32 s63, s54, 2064384\nM 104,172,80\nbuffer_load_dwordx4 a[180:183], v252, s[20:23], s65 offen offset:3072 sc1\n"
"M 120,168,68\nR 162,188,53248\nM 120,172,84\nbuffer_load_dwordx4 v220, s[12:15], s63 offen lds\nM 136,168,72\nR 164,184,0\n"
"M 136,172,88\nbuffer_load_dwordx4 a[184:187], v252, s[20:23], s66 offen offset:3072 sc1\nM 152,168,76\nR 166,184,4096\ns_waitcnt vmcnt(14)\nv_min3_u32 v244, v17, v19, v21\n"
"v_min3_u32 v244, v244, v23, v23\nM 152,172,92\nG 4\nv_max3_u32 v245, v17, v19, v21\nv_max3_u32 v245, v245, v23, v23\nv_cmp_ne_u32 s[86:87], s46, v244\n"
"M 156,176,76\nR 168,189,49152\nbuffer_load_dwordx4 v221, s[12:15], s63 offen offset:1024 lds\nv_cmp_ne_u32 vcc, s46, v245\nM 156,180,92\ns_or_b64 s[86:87], s[86:87], vcc\n"
"s_cbranch_scc1 Lq349\nLq350:\nv_add_f32 v16, v16, v20\nv_add_f32 v18, v18, v22\nM 140,176,72\nR 170,189,53248\n"
"v_add_f32_dpp v16, v16, v16 row_ror:8 row_mask:0xf bank_mask:0xf\nv_add_f32_dpp v18, v18, v18 row_ror:8 row_mask:0xf bank_mask:0xf\nM 140,180,88\nbuffer_load_dwordx4 a[188:191], v252, s[20:23], s67 offen offset:3072 sc1\nv_mov_b32 v20, v16\nM 124,176,68\n"
"R 172,185,0\nv_mov_b32 v21, v18\nM 124,180,84\nv_permlane32_swap_b32 v20, v16\nv_permlane32_swap_b32 v21, v18\nM 108,176,64\n"
"R 174,185,4096\nv_add_f32 v16, v16, v20\nM 108,180,80\nG 4\nv_add_f32 v18, v18, v21\nv_mov_b32 v20, v16\n"
"M 32,160,96\nR 176,190,49152\nv_mov_b32 v21, v18\nM 32,164,112\ns_add_u32 s64, s70, 10498048\nbuffer_load_dwordx4 a[192:195], v252, s[20:23], s64 offen offset:0 sc1\n"
"M 48,160,100\nR 178,190,53248\nv_permlane16_swap_b32 v20, v16\nM 48,164,116\nv_permlane16_swap_b32 v21, v18\nv_add_f32 v16, v16, v20\n"
"M 64,160,104\nR 180,186,0\nv_add_f32 v18, v18, v21\nM 64,164,120\ns_add_u32 m0, s50, 40960\nv_and_b32 v24, s77, v16\n"
"v_sub_f32 v25, v16, v24\nM 80,160,108\nR 182,186,4096\ns_add_u32 s63, s54, 2293760\nM 80,164,124\ns_waitcnt vmcnt(12)\n"
"s_barrier\nG 4\nM 84,168,108\nR 160,191,49152\nv_lshlrev_b32 v26, 16, v243\nM 84,172,124\n"
"s_add_u32 s65, s70, 10530816\nbuffer_load_dwordx4 a[196:199], v252, s[20:23], s65 offen offset:0 sc1\nM 68,168,104\nR 162,191,53248\nv_cvt_pk_bf16_f32 v20, v24, v25\nM 68,172,120\n"
"buffer_load_dwordx4 v220, s[12:15], s63 offen lds\nM 52,168,100\nR 164,187,0\nv_cvt_pk_bf16_f32 v21, v24, v26\nM 52,172,116\ns_add_u32 s66, s71, 10498048\n"
"buffer_load_dwordx4 a[200:203], v252, s[20:23], s66 offen offset:0 sc1\nM 36,168,96\nR 166,187,4096\nv_and_b32 v24, s77, v18\nM 36,172,112\nG 4\n"
"v_sub_f32 v25, v18, v24\nv_and_b32 v26, s77, v243\nM 40,176,96\nR 168,184,8192\nbuffer_load_dwordx4 v221, s[12:15], s63 offen offset:1024 lds\nM 40,180,112\n"
"s_add_u32 m0, s50, 49152\ns_add_u32 s63, s54, 2523136\nbuffer_load_dwordx4 v220, s[12:15], s63 offen lds\nM 56,176,100\nR 170,184,12288\nv_cvt_pk_bf16_f32 v22, v24, v25\n"
"M 56,180,116\ns_add_u32 s67, s71, 10530816\nbuffer_load_dwordx4 a[204:207], v252, s[20:23], s67 offen offset:0 sc1\nM 72,176,104\nR 172,184,16384\nv_cvt_pk_bf16_f32 v23, v24, v26\n"
"M 72,180,120\nbuffer_load_dwordx4 v221, s[12:15], s63 offen offset:1024 lds\nM 88,176,108\nR 174,184,20480\nv_and_b32 v242, 0x1ff, v242\nM 88,180,124\n"
"v_add_u32 v242, 158720, v242\nds_write_b128 v242, v[20:23]\nG 5\nM 92,160,108\nR 176,185,8192\nM 92,164,124\n"
"buffer_load_dwordx4 a[208:211], v252, s[20:23], s64 offen offset:1024 sc1\nM 76,160,104\nR 178,185,12288\nM 76,164,120\ns_lshr_b32 s84, s49, 4\nM 60,160,100\n"
"R 180,185,16384\nLq573:\ns_lshr_b32 s60, s45, 3\ns_lshl2_add_u32 s60, s60, s40\ns_lshl_b32 s60, s60, 8\nv_lshl_add_u32 v224, v252, 3, s60\n"
"v_add_u32 v224, Lq573+4096-LqPC, v224\nv_min_u32 v224, LqE-LqPC-4, v224\ns_mov_b64 exec, 3\nglobal_load_dword v224, v224, s[98:99]\ns_mov_b64 exec, -1\nM 60,164,116\n"
"s_add_u32 s84, s84, 82313216\nM 44,160,96\nR 182,185,20480\nM 44,164,112\nG 5\nM 96,168,96\n"
"R 160,186,8192\nM 96,172,112\nbuffer_load_dwordx4 a[212:215], v252, s[20:23], s65 offen offset:1024 sc1\nM 112,168,100\nR 162,186,12288\nM 112,172,116\n"
"buffer_load_dwordx4 v[0:3], v252, s[20:23], s84 offen offset:0 sc1\nM 128,168,104\nR 164,186,16384\nM 128,172,120\nbuffer_load_dwordx4 a[216:219], v252, s[20:23], s66 offen offset:1024 sc1\nM 144,168,108\n"
"R 166,186,20480\ns_add_u32 m0, s50, 57344\nM 144,172,124\nbuffer_load_dwordx4 v[4:7], v252, s[20:23], s84 offen offset:1024 sc1\ns_waitcnt vmcnt(15) lgkmcnt(8)\ns_barrier\n"
"G 4\nM 148,176,108\nR 168,187,8192\ns_add_u32 s63, s54, 2752512\nM 148,180,124\nbuffer_load_dwordx4 v[8:11], v252, s[20:23], s84 offen offset:2048 sc1\n"
"M 132,176,104\nR 170,187,12288\nM 132,180,120\nbuffer_load_dwordx4 a[220:223], v252, s[20:23], s67 offen offset:1024 sc1\nM 116,176,100\nR 172,187,16384\n"
"buffer_load_dwordx4 v220, s[12:15], s63 offen lds\nM 116,180,116\nbuffer_load_dwordx4 v[12:15], v252, s[20:23], s84 offen offset:3072 sc1\nM 100,176,96\nR 174,187,20480\nM 100,180,112\n"
"G 4\nM 104,160,96\nR 176,184,24576\nbuffer_load_dwordx4 v221, s[12:15], s63 offen offset:1024 lds\nM 104,164,112\nbuffer_load_dwordx4 a[224:227], v252, s[20:23], s64 offen offset:2048 sc1\n"
"s_add_u32 s63, s54, 2981888\nM 120,160,100\nR 178,184,28672\ns_add_u32 m0, s50, 65536\nM 120,164,116\nbuffer_load_dwordx4 v220, s[12:15], s63 offen lds\n"
"M 136,160,104\nR 180,184,32768\nM 136,164,120\nbuffer_load_dwordx4 v221, s[12:15], s63 offen offset:1024 lds\nM 152,160,108\nR 182,184,36864\n"
"M 152,164,124\nG 4\nM 156,168,108\nR 160,185,24576\nM 156,172,124\nbuffer_load_dwordx4 a[228:231], v252, s[20:23], s65 offen offset:2048 sc1\n"
"M 140,168,104\nR 162,185,28672\nM 140,172,120\nM 124,168,100\nR 164,185,32768\nM 124,172,116\n"
"buffer_load_dwordx4 a[232:235], v252, s[20:23], s66 offen offset:2048 sc1\nM 108,168,96\nR 166,185,36864\nM 108,172,112\nG 4\nM 32,176,128\n"
"R 168,186,24576\nM 32,180,144\nM 48,176,132\nR 170,186,28672\nM 48,180,148\nbuffer_load_dwordx4 a[236:239], v252, s[20:23], s67 offen offset:2048 sc1\n"
"M 64,176,136\nR 172,186,32768\nM 64,180,152\ns_add_u32 m0, s50, 73728\nM 80,176,140\nR 174,186,36864\n"
"s_add_u32 s63, s54, 3211264\nM 80,180,156\ns_waitcnt vmcnt(17)\ns_barrier\nG 4\nM 84,160,140\n"
"R 176,187,24576\nM 84,164,156\nbuffer_load_dwordx4 a[240:243], v252, s[20:23], s64 offen offset:3072 sc1\nM 68,160,136\nR 178,187,28672\nM 68,164,152\n"
"buffer_load_dwordx4 v220, s[12:15], s63 offen lds\nM 52,160,132\nR 180,187,32768\nM 52,164,148\nbuffer_load_dwordx4 v221, s[12:15], s63 offen offset:1024 lds\nM 36,160,128\n"
"R 182,187,36864\ns_add_u32 m0, s50, 81920\nM 36,164,144\nG 4\nM 40,168,128\nR 160,184,40960\n"
"s_add_u32 s63, s54, 3440640\nM 40,172,144\nbuffer_load_dwordx4 a[244:247], v252, s[20:23], s65 offen offset:3072 sc1\nM 56,168,132\nR 162,184,45056\nM 56,172,148\n"
"buffer_load_dwordx4 v220, s[12:15], s63 offen lds\nM 72,168,136\nR 164,184,49152\nM 72,172,152\nbuffer_load_dwordx4 a[248:251], v252, s[20:23], s66 offen offset:3072 sc1\nM 88,168,140\n"
"R 166,184,53248\ns_mov_b32 s85, 0x0c0c0400\ns_waitcnt vmcnt(19)\nv_perm_b32 v16, v1, v0, s85\nM 88,172,156\nG 4\n"
"v_perm_b32 v20, v3, v2, s85\nv_lshl_or_b32 v16, v20, 16, v16\ns_waitcnt vmcnt(17)\nv_perm_b32 v17, v5, v4, s85\nM 92,176,140\nR 168,185,40960\n"
"buffer_load_dwordx4 v221, s[12:15], s63 offen offset:1024 lds\nv_perm_b32 v21, v7, v6, s85\nM 92,180,156\nv_lshl_or_b32 v17, v21, 16, v17\ns_waitcnt vmcnt(17)\nv_perm_b32 v18, v9, v8, s85\n"
"v_perm_b32 v22, v11, v10, s85\nM 76,176,136\nR 170,185,45056\nv_lshl_or_b32 v18, v22, 16, v18\ns_waitcnt vmcnt(14)\nv_perm_b32 v19, v13, v12, s85\n"
"M 76,180,152\nbuffer_load_dwordx4 a[252:255], v252, s[20:23], s67 offen offset:3072 sc1\nv_perm_b32 v23, v15, v14, s85\nM 60,176,132\nR 172,185,49152\nv_lshl_or_b32 v19, v23, 16, v19\n"
"v_min3_u32 v230, v16, v17, v18\nM 60,180,148\nv_min3_u32 v230, v230, v19, v19\nv_max3_u32 v231, v16, v17, v18\nv_max3_u32 v231, v231, v19, v19\nM 44,176,128\n"
"R 174,185,53248\nv_cmp_ne_u32 s[86:87], s46, v230\nv_cmp_ne_u32 vcc, s46, v231\nM 44,180,144\ns_or_b64 s[86:87], s[86:87], vcc\ns_cbranch_scc1 Lq317\n"
"Lq318:\nG 4\nv_and_b32 v0, 0xffffff00, v0\nM 32,160,160\nR 176,186,40960\nv_and_b32 v1, 0xffffff00, v1\n"
"v_and_b32 v2, 0xffffff00, v2\nM 32,164,176\nv_and_b32 v3, 0xffffff00, v3\nv_and_b32 v4, 0xffffff00, v4\nv_and_b32 v5, 0xffffff00, v5\nM 48,160,164\n"
"R 178,186,45056\nv_and_b32 v6, 0xffffff00, v6\nv_and_b32 v7, 0xffffff00, v7\nM 48,164,180\ns_add_u32 m0, s50, 90112\nv_and_b32 v8, 0xffffff00, v8\n"
"v_and_b32 v9, 0xffffff00, v9\nv_and_b32 v10, 0xffffff00, v10\nM 64,160,168\nR 180,186,49152\nv_and_b32 v11, 0xffffff00, v11\nv_and_b32 v12, 0xffffff00, v12\n"
"M 64,164,184\ns_add_u32 s63, s54, 1835136\nv_and_b32 v13, 0xffffff00, v13\nv_and_b32 v14, 0xffffff00, v14\nv_and_b32 v15, 0xffffff00, v15\nM 80,160,172\n"
"R 182,186,53248\nv_add_f32 v0, v0, v8\nv_add_f32 v1, v1, v9\nM 80,164,188\ns_waitcnt vmcnt(11)\ns_barrier\n"
"G 4\nv_add_f32 v2, v2, v10\nM 84,168,172\nR 160,187,40960\nv_add_f32 v3, v3, v11\nv_add_f32 v4, v4, v12\n"
"M 84,172,188\nbuffer_load_dwordx4 v220, s[12:15], s63 offen lds\nv_add_f32 v5, v5, v13\nM 68,168,168\nR 162,187,45056\nv_add_f32 v6, v6, v14\n"
"v_add_f32 v7, v7, v15\nM 68,172,184\nv_add_f32 v0, v0, v4\nv_add_f32 v1, v1, v5\nv_add_f32 v2, v2, v6\nM 52,168,164\n"
"R 164,187,49152\nv_bfe_u32 v250, v252, 5, 1\nv_add_f32 v3, v3, v7\nv_mul_u32_u24 v250, 2560, v250\nM 52,172,180\nbuffer_load_dwordx4 v221, s[12:15], s63 offen offset:1024 lds\n"
"v_permlane32_swap_b32 v0, v2\nv_and_b32 v251, 16, v252\nM 36,168,160\nR 166,187,53248\ns_add_u32 m0, s50, 98304\nv_permlane32_swap_b32 v1, v3\n"
"v_lshl_add_u32 v250, v251, 1, v250\nM 36,172,176\ns_lshl_b32 s60, s90, 6\nv_add_u32 v250, s60, v250\nG 4\nv_add_f32 v0, v0, v2\n"
"M 40,176,160\nR 168,184,57344\ns_add_u32 s63, s54, 2064512\nv_add_f32 v1, v1, v3\nM 40,180,176\nbuffer_load_dwordx4 v220, s[12:15], s63 offen lds\n"
"v_permlane16_swap_b32 v0, v1\nM 56,176,164\nR 170,184,61440\nv_add_f32 v230, v0, v1\nv_mov_b32 v232, v230\nM 56,180,180\n"
"s_nop 0\nv_permlane16_swap_b32 v230, v232\nv_mov_b32 v231, v230\nv_mov_b32 v233, v232\nM 72,176,168\nR 172,188,0\n"
"v_permlane32_swap_b32 v230, v231\nM 72,180,184\nbuffer_load_dwordx4 v221, s[12:15], s63 offen offset:1024 lds\nM 88,176,172\nR 174,188,4096\ns_add_u32 m0, s50, 106496\n"
"M 88,180,188\nG 4\nv_permlane32_swap_b32 v232, v233\nv_mul_f32 v230, 0xba800000, v230\nM 92,160,172\nR 176,185,57344\n"
"s_add_u32 s63, s54, 2293888\nM 92,164,188\nbuffer_load_dwordx4 v220, s[12:15], s63 offen lds\nM 76,160,168\nR 178,185,61440\nv_mul_f32 v214, v230, v230\n"
"M 76,164,184\nv_mul_f32 v231, 0x3a800000, v231\nv_sub_f32 v231, v231, v214\nM 60,160,164\nR 180,189,0\nv_max_f32 v231, 0, v231\n"
"M 60,164,180\nv_add_f32 v231, 0x3727c5ac, v231\nv_and_b32 v208, s77, v230\nM 44,160,160\nR 182,189,4096\nv_sub_f32 v230, v230, v208\n"
"M 44,164,176\nLpg9:\ns_cbranch_execz Lpg13\nG 4\nv_rsq_f32 v234, v231\nM 32,168,192\n"
"R 160,186,57344\nv_mul_f32 v216, v231, v234\nM 32,172,208\nbuffer_load_dwordx4 v221, s[12:15], s63 offen offset:1024 lds\nM 48,168,196\nR 162,186,61440\n"
"s_add_u32 s63, s54, 2523264\nM 48,172,212\ns_add_u32 m0, s50, 114688\nv_cvt_pk_bf16_f32 v209, v230, v216\nv_cvt_pk_bf16_f32 v208, v208, v208\nM 64,168,200\n"
"R 164,190,0\nv_mul_f32 v214, 0xc0135761, v234\nM 64,172,216\nbuffer_load_dwordx4 v220, s[12:15], s63 offen lds\nM 80,168,204\nR 166,190,4096\n"
"v_mul_f32 v215, v234, v234\nM 80,172,220\ns_waitcnt vmcnt(8)\ns_barrier\nG 4\nM 84,176,204\n"
"R 168,187,57344\nv_mul_f32 v215, v215, v214\nM 84,180,220\nbuffer_load_dwordx4 v221, s[12:15], s63 offen offset:1024 lds\nM 68,176,200\nR 170,187,61440\n"
"s_add_u32 s63, s54, 2752640\nM 68,180,216\ns_add_u32 m0, s50, 0\ns_mov_b64 s[60:61], 0xffff\nv_cndmask_b32 v208, 0, v208, s[60:61]\nv_mul_f32 v215, 0x3d372713, v215\n"
"M 52,176,196\nR 172,191,0\nv_mul_f32 v232, 0xba800000, v232\nM 52,180,212\nbuffer_load_dwordx4 v220, s[12:15], s63 offen lds\nM 36,176,192\n"
"R 174,191,4096\nv_mul_f32 v217, v232, v232\nM 36,180,208\ns_mov_b64 s[60:61], 0xffff\nv_cndmask_b32 v209, 0, v209, s[60:61]\nG 4\n"
"v_mul_f32 v233, 0x3a800000, v233\nM 40,160,192\nR 176,188,8192\nv_sub_f32 v233, v233, v217\nM 40,164,208\nbuffer_load_dwordx4 v221, s[12:15], s63 offen offset:1024 lds\n"
"M 56,160,196\nR 178,188,12288\nv_max_f32 v233, 0, v233\nM 56,164,212\ns_add_u32 m0, s50, 8192\nv_add_f32 v233, 0x3727c5ac, v233\n"
"v_and_b32 v212, s77, v232\nM 72,160,200\nR 180,188,16384\nv_sub_f32 v232, v232, v212\nM 72,164,216\ns_add_u32 s63, s54, 2982016\n"
"v_rsq_f32 v235, v233\nM 88,160,204\nR 182,188,20480\nv_mul_f32 v219, v233, v235\nM 88,164,220\nG 4\n"
"v_cvt_pk_bf16_f32 v213, v232, v219\nv_cvt_pk_bf16_f32 v212, v212, v212\nM 92,168,204\nR 160,189,8192\nv_mul_f32 v217, 0xc0135761, v235\nM 92,172,220\n"
"buffer_load_dwordx4 v220, s[12:15], s63 offen lds\nM 76,168,200\nR 162,189,12288\nv_mul_f32 v218, v235, v235\nM 76,172,216\ns_mov_b64 s[60:61], 0xffff\n"
"v_cndmask_b32 v212, 0, v212, s[60:61]\nv_mul_f32 v218, v218, v217\nM 60,168,196\nR 164,189,16384\nv_mul_f32 v218, 0x3d372713, v218\nM 60,172,212\n"
"buffer_load_dwordx4 v221, s[12:15], s63 offen offset:1024 lds\nM 44,168,192\nR 166,189,20480\ns_add_u32 m0, s50, 16384\nM 44,172,208\ns_mov_b64 s[60:61], 0xffff\n"
"v_cndmask_b32 v213, 0, v213, s[60:61]\nG 4\nM 32,176,224\nR 168,190,8192\ns_add_u32 s63, s54, 3211392\nM 32,180,240\n"
"buffer_load_dwordx4 v220, s[12:15], s63 offen lds\nM 48,176,228\nR 170,190,12288\nM 48,180,244\nM 64,176,232\nR 172,190,16384\n"
"M 64,180,248\nbuffer_load_dwordx4 v221, s[12:15], s63 offen offset:1024 lds\nM 80,176,236\nR 174,190,20480\ns_add_u32 m0, s50, 24576\ns_waitcnt vmcnt(14)\n"
"M 80,180,252\ns_waitcnt vmcnt(10)\ns_barrier\nG 4\nM 84,160,236\nR 176,191,8192\n"
"s_add_u32 s63, s54, 3440768\nM 84,164,252\nbuffer_load_dwordx4 v220, s[12:15], s63 offen lds\nM 68,160,232\nR 178,191,12288\nM 68,164,248\n"
"M 52,160,228\nR 180,191,16384\nM 52,164,244\nM 36,160,224\nR 182,191,20480\nM 36,164,240\n"
"G 4\nM 40,168,224\nR 160,188,24576\nM 40,172,240\nbuffer_load_dwordx4 v221, s[12:15], s63 offen offset:1024 lds\nM 56,168,228\n"
"R 162,188,28672\ns_add_u32 s63, s55, 0\nM 56,172,244\ns_add_u32 m0, s50, 32768\nM 72,168,232\nR 164,188,32768\n"
"M 72,172,248\nbuffer_load_dwordx4 v222, s[12:15], s63 offen lds\nM 88,168,236\nR 166,188,36864\ns_mov_b32 s60, 157184\nv_bfe_u32 v246, v252, 4, 4\n"
"v_lshl_add_u32 v246, v246, 3, s60\nM 88,172,252\nds_read_b64 v[238:239], v246 offset:0\nG 5\nM 92,176,236\nR 168,189,24576\n"
"ds_read_b64 v[240:241], v246 offset:128\nM 92,180,252\nds_read_b64 v[242:243], v246 offset:256\nM 76,176,232\nR 170,189,28672\nds_read_b64 v[244:245], v246 offset:384\n"
"M 76,180,248\nds_read_b64 v[246:247], v246 offset:0\nM 60,176,228\nR 172,189,32768\nM 60,180,244\nbuffer_load_dwordx4 v223, s[12:15], s63 offen offset:1024 lds\n"
"M 44,176,224\nR 174,189,36864\nM 44,180,240\ns_waitcnt vmcnt(10) lgkmcnt(2)\ns_barrier\nM 96,160,128\n"
"R 176,190,24576\nM 96,164,144\ns_mov_b32 m0, s51\nM 112,160,132\nR 178,190,28672\ns_lshl2_add_u32 s60, s45, s40\n"
"v_lshlrev_b32 v224, 3, v252\ns_mul_i32 s60, s60, 7168\ns_bfm_b64 exec, 14, 0\nbuffer_load_dword v224, v224, s[16:19], s60 offen\ns_mov_b64 exec, -1\nM 112,164,148\n"
"ds_write_addtid_b32 v64 offset:0\nM 128,160,136\nR 180,190,32768\nv_mfma_f32_16x16x32_bf16 v[32:35], v[238:241], v[208:211], v[32:35]\nds_write_addtid_b32 v65 offset:256\nds_write_addtid_b32 v66 offset:512\n"
"M 128,164,152\nds_write_addtid_b32 v67 offset:768\nM 144,160,140\nR 182,190,36864\nv_mfma_f32_16x16x32_bf16 v[36:39], v[240:243], v[208:211], v[36:39]\nds_write_addtid_b32 v68 offset:1024\n"
"ds_write_addtid_b32 v69 offset:1280\nM 144,164,156\nds_write_addtid_b32 v70 offset:1536\nG 11\nM 148,168,140\nR 160,191,24576\n"
"v_mfma_f32_16x16x32_bf16 v[40:43], v[242:245], v[208:211], v[40:43]\nds_write_addtid_b32 v71 offset:1792\nds_write_addtid_b32 v72 offset:2048\nM 148,172,156\nds_write_addtid_b32 v73 offset:2304\nM 132,168,136\n"
"R 162,191,28672\nv_mfma_f32_16x16x32_bf16 v[44:47], v[244:247], v[208:211], v[44:47]\nds_write_addtid_b32 v74 offset:2560\nds_write_addtid_b32 v75 offset:2816\nM 132,172,152\nds_write_addtid_b32 v76 offset:3072\n"
"M 116,168,132\nR 164,191,32768\nv_mfma_f32_16x16x32_bf16 v[48:51], v[244:247], v[210:213], v[48:51]\nds_write_addtid_b32 v77 offset:3328\nds_write_addtid_b32 v78 offset:3584\nM 116,172,148\n"
"ds_write_addtid_b32 v79 offset:3840\nM 100,168,128\nR 166,191,36864\nv_mfma_f32_16x16x32_bf16 v[52:55], v[238:241], v[210:213], v[52:55]\nds_write_addtid_b32 v80 offset:4096\nds_write_addtid_b32 v81 offset:4352\n"
"M 100,172,144\nds_write_addtid_b32 v82 offset:4608\nG 15\nM 104,176,128\nR 168,188,40960\nv_mfma_f32_16x16x32_bf16 v[56:59], v[240:243], v[210:213], v[56:59]\n"
"ds_write_addtid_b32 v83 offset:4864\nds_write_addtid_b32 v84 offset:5120\nM 104,180,144\nds_write_addtid_b32 v85 offset:5376\nM 120,176,132\nR 170,188,45056\n"
"v_mfma_f32_16x16x32_bf16 v[60:63], v[242:245], v[210:213], v[60:63]\nds_write_addtid_b32 v86 offset:5632\nds_write_addtid_b32 v87 offset:5888\nM 120,180,148\nds_write_addtid_b32 v88 offset:6144\nM 136,176,136\n"
"R 172,188,49152\nM 136,180,152\nds_write_addtid_b32 v89 offset:6400\nM 152,176,140\nR 174,188,53248\nds_write_addtid_b32 v90 offset:6656\n"
"M 152,180,156\nds_write_addtid_b32 v91 offset:6912\nds_write_addtid_b32 v92 offset:7168\nG 15\nM 156,160,140\nR 176,189,40960\n"
"ds_write_addtid_b32 v93 offset:7424\nM 156,164,156\nds_write_addtid_b32 v94 offset:7680\nds_write_addtid_b32 v95 offset:7936\nM 140,160,136\nR 178,189,45056\n"
"s_add_u32 m0, s50, 40960\ns_add_u32 s63, s55, 458752\nM 140,164,152\nbuffer_load_dwordx4 v222, s[12:15], s63 offen lds\nM 124,160,132\nR 180,189,49152\n"
"s_waitcnt vmcnt(8) lgkmcnt(2)\ns_barrier\nM 124,164,148\nds_read2st64_b32 v[64:65], v225 offset0:0 offset1:1\nds_read2st64_b32 v[66:67], v225 offset0:2 offset1:3\nM 108,160,128\n"
"R 182,189,53248\nds_read2st64_b32 v[68:69], v225 offset0:4 offset1:5\nM 108,164,144\nds_read2st64_b32 v[70:71], v225 offset0:6 offset1:7\nds_read2st64_b32 v[72:73], v225 offset0:8 offset1:9\nM 96,168,160\n"
"R 160,190,40960\nds_read2st64_b32 v[74:75], v225 offset0:10 offset1:11\nM 96,172,176\nbuffer_load_dwordx4 v223, s[12:15], s63 offen offset:1024 lds\nds_read2st64_b32 v[76:77], v225 offset0:12 offset1:13\nM 112,168,164\n"
"R 162,190,45056\nds_read2st64_b32 v[78:79], v225 offset0:14 offset1:15\nM 112,172,180\ns_add_u32 m0, s50, 49152\nds_read2st64_b32 v[80:81], v225 offset0:16 offset1:17\nds_read2st64_b32 v[82:83], v225 offset0:18 offset1:19\n"
"M 128,168,168\nR 164,190,49152\nds_read2st64_b32 v[84:85], v225 offset0:20 offset1:21\nG 14\nv_add_f32 v32, v32, v64\nM 128,172,184\n"
"s_add_u32 s63, s55, 917504\nds_read2st64_b32 v[86:87], v225 offset0:22 offset1:23\nds_read2st64_b32 v[88:89], v225 offset0:24 offset1:25\nM 144,168,172\nR 166,190,53248\nds_read2st64_b32 v[90:91], v225 offset0:26 offset1:27\n"
"v_add_f32 v33, v33, v65\nM 144,172,188\nds_read2st64_b32 v[92:93], v225 offset0:28 offset1:29\nds_read2st64_b32 v[94:95], v225 offset0:30 offset1:31\nG 15\nM 148,176,172\n"
"R 168,191,40960\nv_add_f32 v34, v34, v66\nv_add_f32 v35, v35, v67\nM 148,180,188\nbuffer_load_dwordx4 v222, s[12:15], s63 offen lds\nv_add_f32 v36, v36, v68\n"
"M 132,176,168\nR 170,191,45056\nv_add_f32 v37, v37, v69\nv_add_f32 v38, v38, v70\nM 132,180,184\nv_add_f32 v39, v39, v71\n"
"v_add_f32 v40, v40, v72\nM 116,176,164\nR 172,191,49152\nv_add_f32 v41, v41, v73\nM 116,180,180\nG 15\n"
"v_add_f32 v42, v42, v74\nv_add_f32 v43, v43, v75\nM 100,176,160\nR 174,191,53248\nv_add_f32 v44, v44, v76\nM 100,180,176\n"
"G 7\nv_add_f32 v45, v45, v77\nv_add_f32 v46, v46, v78\nM 104,160,160\nR 176,184,0\nv_add_f32 v47, v47, v79\n"
"M 104,164,176\nbuffer_load_dwordx4 v223, s[12:15], s63 offen offset:1024 lds\nM 120,160,164\nR 178,184,4096\nv_add_f32 v48, v48, v80\nM 120,164,180\n"
"s_add_u32 m0, s50, 57344\nv_add_f32 v49, v49, v81\nv_add_f32 v50, v50, v82\nM 136,160,168\nR 180,184,8192\nv_add_f32 v51, v51, v83\n"
"M 136,164,184\ns_add_u32 s63, s55, 1376256\nv_add_f32 v52, v52, v84\nv_add_f32 v53, v53, v85\nM 152,160,172\nR 182,184,12288\n"
"v_add_f32 v54, v54, v86\nM 152,164,188\nG 4\nv_add_f32 v55, v55, v87\nv_add_f32 v56, v56, v88\nM 156,168,172\n"
"R 160,185,0\nv_add_f32 v57, v57, v89\nM 156,172,188\nbuffer_load_dwordx4 v222, s[12:15], s63 offen lds\nM 140,168,168\nR 162,185,4096\n"
"v_add_f32 v58, v58, v90\nM 140,172,184\nv_add_f32 v59, v59, v91\nv_add_f32 v60, v60, v92\nM 124,168,164\nR 164,185,8192\n"
"v_add_f32 v61, v61, v93\nM 124,172,180\nv_add_f32 v62, v62, v94\nv_add_f32 v63, v63, v95\nM 108,168,160\nR 166,185,12288\n"
"v_mul_f32 v230, v32, v32\nv_mul_f32 v231, v33, v33\nM 108,172,176\ns_add_u32 s84, s73, 23068672\ns_add_u32 s85, s73, 23183360\nG 4\n"
"v_mul_f32 v232, v34, v34\nM 96,176,192\nR 168,186,0\nv_mul_f32 v233, v35, v35\nv_fma_f32 v230, v230, v215, v214\nM 96,180,208\n"
"v_fma_f32 v231, v231, v215, v214\nv_fma_f32 v232, v232, v215, v214\nv_fma_f32 v233, v233, v215, v214\nM 112,176,196\nR 170,186,4096\ns_lshl2_add_u32 s60, s45, s40\n"
"s_waitcnt vmcnt(5)\nv_lshlrev_b32 v224, 3, v252\ns_mul_i32 s60, s60, 7168\ns_bfm_b64 exec, 14, 14\nbuffer_load_dword v224, v224, s[16:19], s60 offen\ns_mov_b64 exec, -1\n"
"s_barrier\nv_mul_f32 v230, v32, v230\nM 112,180,212\nv_exp_f32 v230, v230\nv_mul_f32 v231, v33, v231\nM 128,176,200\n"
"R 172,186,8192\nv_mul_f32 v232, v34, v232\nv_mul_f32 v233, v35, v233\nM 128,180,216\nv_exp_f32 v231, v231\nv_exp_f32 v232, v232\n"
"M 144,176,204\nR 174,186,12288\nv_fma_f32 v230, v230, v216, v216\nv_exp_f32 v233, v233\nM 144,180,220\nG 4\n"
"v_rcp_f32 v230, v230\nv_fma_f32 v231, v231, v216, v216\nM 148,160,204\nR 176,187,0\nv_fma_f32 v232, v232, v216, v216\nv_fma_f32 v233, v233, v216, v216\n"
"M 148,164,220\nbuffer_load_dwordx4 v223, s[12:15], s63 offen offset:1024 lds\nv_rcp_f32 v231, v231\nM 132,160,200\nR 178,187,4096\nv_mul_f32 v230, v32, v230\n"
"v_rcp_f32 v232, v232\nM 132,164,216\ns_add_u32 m0, s50, 65536\nv_rcp_f32 v233, v233\nv_mul_f32 v231, v33, v231\nM 116,160,196\n"
"R 180,187,8192\nv_mul_f32 v232, v34, v232\nv_mul_f32 v233, v35, v233\nM 116,164,212\ns_add_u32 s63, s55, 1835008\nv_cvt_pk_bf16_f32 v32, v230, v231\n"
"v_cvt_pk_bf16_f32 v33, v232, v233\nv_mul_f32 v234, v36, v36\nM 100,160,192\nR 182,187,12288\nv_mul_f32 v235, v37, v37\nv_mul_f32 v236, v38, v38\n"
"M 100,164,208\nG 4\nv_mul_f32 v237, v39, v39\nv_fma_f32 v234, v234, v215, v214\nv_fma_f32 v235, v235, v215, v214\nM 104,168,192\n"
"R 160,184,16384\nv_fma_f32 v236, v236, v215, v214\nv_fma_f32 v237, v237, v215, v214\nM 104,172,208\nbuffer_load_dwordx4 v222, s[12:15], s63 offen lds\nv_mul_f32 v234, v36, v234\n"
"M 120,168,196\nR 162,184,20480\nv_mul_f32 v235, v37, v235\nv_mul_f32 v236, v38, v236\nM 120,172,212\nv_exp_f32 v234, v234\n"
"v_mul_f32 v237, v39, v237\nM 136,168,200\nR 164,184,24576\nv_fma_f32 v234, v234, v216, v216\nv_exp_f32 v235, v235\nM 136,172,216\n"
"v_exp_f32 v236, v236\nv_exp_f32 v237, v237\nM 152,168,204\nR 166,184,28672\nv_fma_f32 v235, v235, v216, v216\nv_fma_f32 v236, v236, v216, v216\n"
"M 152,172,220\nG 4\nv_rcp_f32 v234, v234\nv_fma_f32 v237, v237, v216, v216\nM 156,176,204\nR 168,185,16384\n"
"v_mul_f32 v234, v36, v234\nv_rcp_f32 v235, v235\nM 156,180,220\nbuffer_load_dwordx4 v223, s[12:15], s63 offen offset:1024 lds\nv_rcp_f32 v236, v236\nM 140,176,200\n"
"R 170,185,20480\nv_mul_f32 v235, v37, v235\nv_rcp_f32 v237, v237\nM 140,180,216\ns_add_u32 m0, s50, 73728\nv_mul_f32 v236, v38, v236\n"
"v_mul_f32 v237, v39, v237\nv_cvt_pk_bf16_f32 v34, v234, v235\nM 124,176,196\nR 172,185,24576\nv_cvt_pk_bf16_f32 v35, v236, v237\nv_mul_f32 v230, v40, v40\n"
"M 124,180,212\ns_add_u32 s63, s55, 2293760\nv_mul_f32 v231, v41, v41\nv_mul_f32 v232, v42, v42\nv_mul_f32 v233, v43, v43\nM 108,176,192\n"
"R 174,185,28672\nv_fma_f32 v230, v230, v215, v214\nv_fma_f32 v231, v231, v215, v214\nM 108,180,208\nG 4\nv_fma_f32 v232, v232, v215, v214\n"
"v_fma_f32 v233, v233, v215, v214\nv_mul_f32 v230, v40, v230\nM 96,160,224\nR 176,186,16384\nv_mul_f32 v231, v41, v231\nv_mul_f32 v232, v42, v232\n"
"M 96,164,240\nbuffer_load_dwordx4 v222, s[12:15], s63 offen lds\nv_mul_f32 v233, v43, v233\nM 112,160,228\nR 178,186,20480\nv_mul_f32 v234, v44, v44\n"
"v_exp_f32 v230, v230\nM 112,164,244\nbuffer_store_dwordx4 v[32:35], v252, s[20:23], s84 offen offset:0 sc1\nv_exp_f32 v231, v231\nM 128,160,232\nR 180,186,24576\n"
"v_fma_f32 v230, v230, v216, v216\nv_exp_f32 v232, v232\nM 128,164,248\nv_exp_f32 v233, v233\nv_fma_f32 v231, v231, v216, v216\nM 144,160,236\n"
"R 182,186,28672\nv_fma_f32 v232, v232, v216, v216\nv_fma_f32 v233, v233, v216, v216\nM 144,164,252\nG 4\nv_rcp_f32 v230, v230\n"
"v_rcp_f32 v231, v231\nM 148,168,236\nR 160,187,16384\nv_mul_f32 v230, v40, v230\nv_rcp_f32 v232, v232\nM 148,172,252\n"
"buffer_load_dwordx4 v223, s[12:15], s63 offen offset:1024 lds\nv_rcp_f32 v233, v233\nM 132,168,232\nR 162,187,20480\nv_mul_f32 v231, v41, v231\nv_mul_f32 v232, v42, v232\n"
"M 132,172,248\ns_add_u32 m0, s50, 81920\nv_mul_f32 v233, v43, v233\nv_cvt_pk_bf16_f32 v40, v230, v231\nv_cvt_pk_bf16_f32 v41, v232, v233\nM 116,168,228\n"
"R 164,187,24576\nv_mul_f32 v235, v45, v45\nv_mul_f32 v236, v46, v46\nM 116,172,244\ns_add_u32 s63, s55, 2752512\nv_mul_f32 v237, v47, v47\n"
"v_fma_f32 v234, v234, v215, v214\nv_fma_f32 v235, v235, v215, v214\nM 100,168,224\nR 166,187,28672\nv_fma_f32 v236, v236, v215, v214\nv_fma_f32 v237, v237, v215, v214\n"
"M 100,172,240\nG 4\nv_mul_f32 v234, v44, v234\nv_mul_f32 v235, v45, v235\nLpg10:\ns_cbranch_execz Lpg14\n"
"v_mul_f32 v236, v46, v236\nM 104,176,224\nR 168,184,32768\nv_mul_f32 v237, v47, v237\nv_exp_f32 v234, v234\nM 104,180,240\n"
"buffer_load_dwordx4 v222, s[12:15], s63 offen lds\nv_exp_f32 v235, v235\nM 120,176,228\nR 170,184,36864\nv_fma_f32 v234, v234, v216, v216\nv_exp_f32 v236, v236\n"
"M 120,180,244\nbuffer_load_dwordx4 v223, s[12:15], s63 offen offset:1024 lds\nv_exp_f32 v237, v237\nM 136,176,232\nR 172,186,32768\nv_fma_f32 v235, v235, v216, v216\n"
"v_fma_f32 v236, v236, v216, v216\nM 136,180,248\ns_add_u32 m0, s50, 90112\nv_rcp_f32 v234, v234\nv_fma_f32 v237, v237, v216, v216\nM 152,176,236\n"
"R 174,186,36864\ns_add_u32 s63, s55, 3211264\nv_rcp_f32 v235, v235\nM 152,180,252\nG 4\nv_rcp_f32 v236, v236\n"
"v_rcp_f32 v237, v237\nM 156,160,236\nR 176,185,32768\ns_mov_b32 s60, 157696\nv_bfe_u32 v246, v252, 4, 4\nv_lshl_add_u32 v246, v246, 3, s60\n"
"v_mul_f32 v234, v44, v234\nM 156,164,252\nds_read_b64 v[238:239], v246 offset:0\nv_mul_f32 v235, v45, v235\nv_mul_f32 v236, v46, v236\nM 140,160,232\n"
"R 178,185,36864\nds_read_b64 v[240:241], v246 offset:128\nv_mul_f32 v237, v47, v237\nM 140,164,248\nds_read_b64 v[242:243], v246 offset:256\nbuffer_load_dwordx4 v222, s[12:15], s63 offen lds\n"
"v_cvt_pk_bf16_f32 v42, v234, v235\nM 124,160,228\nR 180,187,32768\nds_read_b64 v[244:245], v246 offset:384\nv_cvt_pk_bf16_f32 v43, v236, v237\nM 124,164,244\n"
"ds_read_b64 v[246:247], v246 offset:0\nv_mul_f32 v230, v48, v48\nv_mul_f32 v231, v49, v49\nM 108,160,224\nR 182,187,36864\nv_mul_f32 v232, v50, v50\n"
"v_mul_f32 v233, v51, v51\nM 108,164,240\ns_waitcnt vmcnt(11) lgkmcnt(1)\ns_barrier\nv_fma_f32 v230, v230, v218, v217\nv_mfma_f32_16x16x32_bf16 v[0:3], v[168:171], a[0:3], 0\n"
"R 160,184,40960\nbuffer_load_dwordx4 v223, s[12:15], s63 offen offset:1024 lds\nv_fma_f32 v231, v231, v218, v217\nM 0,172,16\ns_mov_b32 m0, s51\nv_fma_f32 v232, v232, v218, v217\n"
"v_fma_f32 v233, v233, v218, v217\nv_mul_f32 v230, v48, v230\nv_mfma_f32_16x16x32_bf16 v[16:19], v[168:171], a[4:7], 0\nR 162,184,45056\nv_mul_f32 v231, v49, v231\nv_mul_f32 v232, v50, v232\n"
"M 16,172,20\nbuffer_store_dwordx4 v[40:43], v252, s[20:23], s84 offen offset:1024 sc1\nv_mul_f32 v233, v51, v233\nv_mfma_f32_16x16x32_bf16 v[8:11], v[168:171], a[8:11], 0\nR 164,186,40960\nv_mul_f32 v234, v52, v52\n"
"v_exp_f32 v230, v230\nM 8,172,24\nv_exp_f32 v231, v231\nv_exp_f32 v232, v232\nv_mfma_f32_16x16x32_bf16 v[24:27], v[168:171], a[12:15], 0\nR 166,186,45056\n"
"v_mfma_f32_16x16x32_bf16 v[96:99], v[238:241], v[208:211], v[96:99]\nv_exp_f32 v233, v233\nv_fma_f32 v230, v230, v219, v219\nv_fma_f32 v231, v231, v219, v219\nv_fma_f32 v232, v232, v219, v219\nM 24,172,28\n"
"G 4\nv_rcp_f32 v230, v230\nv_fma_f32 v233, v233, v219, v219\nv_mfma_f32_16x16x32_bf16 v[28:31], v[176:179], a[12:15], 0\nR 168,185,40960\nv_mfma_f32_16x16x32_bf16 v[100:103], v[240:243], v[208:211], v[100:103]\n"
"v_rcp_f32 v231, v231\nv_rcp_f32 v232, v232\nv_rcp_f32 v233, v233\nM 28,180,28\nv_mul_f32 v230, v48, v230\nv_mul_f32 v231, v49, v231\n"
"v_mul_f32 v232, v50, v232\nv_mfma_f32_16x16x32_bf16 v[12:15], v[176:179], a[8:11], 0\nR 170,185,45056\nv_mul_f32 v233, v51, v233\nv_mfma_f32_16x16x32_bf16 v[104:107], v[242:245], v[208:211], v[104:107]\nv_cvt_pk_bf16_f32 v48, v230, v231\n"
"v_cvt_pk_bf16_f32 v49, v232, v233\nv_mul_f32 v235, v53, v53\nv_mul_f32 v236, v54, v54\nM 12,180,24\nv_mul_f32 v237, v55, v55\nv_fma_f32 v234, v234, v218, v217\n"
"v_fma_f32 v235, v235, v218, v217\nv_mfma_f32_16x16x32_bf16 v[20:23], v[176:179], a[4:7], 0\nR 172,187,40960\nv_fma_f32 v236, v236, v218, v217\nv_mfma_f32_16x16x32_bf16 v[108:111], v[244:247], v[208:211], v[108:111]\nv_fma_f32 v237, v237, v218, v217\n"
"v_mul_f32 v234, v52, v234\nv_mul_f32 v235, v53, v235\nv_mul_f32 v236, v54, v236\nM 20,180,20\nds_write_addtid_b32 v128 offset:0\nv_exp_f32 v234, v234\n"
"v_mul_f32 v237, v55, v237\nv_mfma_f32_16x16x32_bf16 v[4:7], v[176:179], a[0:3], 0\nR 174,187,45056\nv_mfma_f32_16x16x32_bf16 v[112:115], v[244:247], v[210:213], v[112:115]\nds_write_addtid_b32 v129 offset:256\nv_exp_f32 v235, v235\n"
"v_exp_f32 v236, v236\nv_exp_f32 v237, v237\nM 4,180,16\nds_write_addtid_b32 v130 offset:512\nG 7\nv_fma_f32 v234, v234, v219, v219\n"
"v_fma_f32 v235, v235, v219, v219\nv_fma_f32 v236, v236, v219, v219\nM 0,160,32\nR 176,184,49152\nv_mfma_f32_16x16x32_bf16 v[116:119], v[238:241], v[210:213], v[116:119]\nds_write_addtid_b32 v131 offset:768\n"
"v_rcp_f32 v234, v234\nv_rcp_f32 v235, v235\nv_fma_f32 v237, v237, v219, v219\nM 0,164,48\nds_write_addtid_b32 v132 offset:1024\nv_rcp_f32 v236, v236\n"
"v_rcp_f32 v237, v237\nM 16,160,36\nR 178,184,53248\nv_mul_f32 v234, v52, v234\nv_mfma_f32_16x16x32_bf16 v[120:123], v[240:243], v[210:213], v[120:123]\nds_write_addtid_b32 v133 offset:1280\n"
"v_mul_f32 v235, v53, v235\nv_mul_f32 v236, v54, v236\nv_mul_f32 v237, v55, v237\nv_cvt_pk_bf16_f32 v50, v234, v235\nM 16,164,52\nds_write_addtid_b32 v134 offset:1536\n"
"v_cvt_pk_bf16_f32 v51, v236, v237\nv_mul_f32 v230, v56, v56\nv_mul_f32 v231, v57, v57\nM 8,160,40\nR 180,186,49152\nv_mul_f32 v232, v58, v58\n"
"v_mfma_f32_16x16x32_bf16 v[124:127], v[242:245], v[210:213], v[124:127]\nbuffer_store_dwordx4 v[48:51], v252, s[20:23], s85 offen offset:0 sc1\nds_write_addtid_b32 v135 offset:1792\nv_mul_f32 v233, v59, v59\nM 8,164,56\nds_write_addtid_b32 v136 offset:2048\n"
"v_fma_f32 v230, v230, v218, v217\nv_fma_f32 v231, v231, v218, v217\nv_fma_f32 v232, v232, v218, v217\nM 24,160,44\nR 182,186,53248\nds_write_addtid_b32 v137 offset:2304\n"
"v_fma_f32 v233, v233, v218, v217\nv_mul_f32 v230, v56, v230\nM 24,164,60\nds_write_addtid_b32 v138 offset:2560\nG 14\nv_exp_f32 v230, v230\n"
"v_mul_f32 v231, v57, v231\nM 28,168,44\nR 160,185,49152\nds_write_addtid_b32 v139 offset:2816\nv_mul_f32 v232, v58, v232\nv_mul_f32 v233, v59, v233\n"
"M 28,172,60\nds_write_addtid_b32 v140 offset:3072\nds_write_addtid_b32 v141 offset:3328\nds_write_addtid_b32 v142 offset:3584\nds_write_addtid_b32 v143 offset:3840\nds_write_addtid_b32 v144 offset:4096\n"
"ds_write_addtid_b32 v145 offset:4352\nds_write_addtid_b32 v146 offset:4608\nds_write_addtid_b32 v147 offset:4864\nds_write_addtid_b32 v148 offset:5120\nds_write_addtid_b32 v149 offset:5376\nds_write_addtid_b32 v150 offset:5632\n"
"ds_write_addtid_b32 v151 offset:5888\nds_write_addtid_b32 v152 offset:6144\nds_write_addtid_b32 v153 offset:6400\nds_write_addtid_b32 v154 offset:6656\nds_write_addtid_b32 v155 offset:6912\nds_write_addtid_b32 v156 offset:7168\n"
"ds_write_addtid_b32 v157 offset:7424\nds_write_addtid_b32 v158 offset:7680\nds_write_addtid_b32 v159 offset:7936\nv_exp_f32 v231, v231\nv_exp_f32 v232, v232\nM 12,168,40\n"
"R 162,185,53248\ns_add_u32 m0, s50, 98304\ns_add_u32 s63, s54, 256\nv_exp_f32 v233, v233\nM 12,172,56\nbuffer_load_dwordx4 v220, s[12:15], s63 offen lds\n"
"v_fma_f32 v230, v230, v219, v219\nM 20,168,36\nR 164,187,49152\ns_waitcnt vmcnt(5) lgkmcnt(2)\ns_barrier\nv_fma_f32 v231, v231, v219, v219\n"
"M 20,172,52\nds_read2st64_b32 v[128:129], v225 offset0:0 offset1:1\nv_rcp_f32 v230, v230\nv_fma_f32 v232, v232, v219, v219\nM 4,168,32\nR 166,187,53248\n"
"ds_read2st64_b32 v[130:131], v225 offset0:2 offset1:3\nv_fma_f32 v233, v233, v219, v219\nv_rcp_f32 v231, v231\nM 4,172,48\nds_read2st64_b32 v[132:133], v225 offset0:4 offset1:5\nv_rcp_f32 v232, v232\n"
"v_rcp_f32 v233, v233\nM 0,176,64\nR 168,184,57344\nds_read2st64_b32 v[134:135], v225 offset0:6 offset1:7\nv_mul_f32 v230, v56, v230\nv_mul_f32 v231, v57, v231\n"
"M 0,180,80\nbuffer_load_dwordx4 v221, s[12:15], s63 offen offset:1024 lds\nds_read2st64_b32 v[136:137], v225 offset0:8 offset1:9\nv_mul_f32 v232, v58, v232\nM 16,176,68\nR 170,184,61440\n"
"s_add_u32 m0, s50, 106496\ns_add_u32 s63, s54, 229632\nds_read2st64_b32 v[138:139], v225 offset0:10 offset1:11\nv_mul_f32 v233, v59, v233\nM 16,180,84\nbuffer_load_dwordx4 v220, s[12:15], s63 offen lds\n"
"ds_read2st64_b32 v[140:141], v225 offset0:12 offset1:13\nv_cvt_pk_bf16_f32 v56, v230, v231\nM 8,176,72\nR 172,186,57344\nds_read2st64_b32 v[142:143], v225 offset0:14 offset1:15\nv_cvt_pk_bf16_f32 v57, v232, v233\n"
"v_mul_f32 v234, v60, v60\nM 8,180,88\nds_read2st64_b32 v[144:145], v225 offset0:16 offset1:17\nv_mul_f32 v235, v61, v61\nv_mul_f32 v236, v62, v62\nv_mul_f32 v237, v63, v63\n"
"M 24,176,76\nR 174,186,61440\nds_read2st64_b32 v[146:147], v225 offset0:18 offset1:19\nv_fma_f32 v234, v234, v218, v217\nv_fma_f32 v235, v235, v218, v217\nM 24,180,92\n"
"ds_read2st64_b32 v[148:149], v225 offset0:20 offset1:21\nG 14\nv_fma_f32 v236, v236, v218, v217\nv_fma_f32 v237, v237, v218, v217\nv_mul_f32 v234, v60, v234\nM 28,160,76\n"
"R 176,185,57344\nds_read2st64_b32 v[150:151], v225 offset0:22 offset1:23\nv_mul_f32 v235, v61, v235\nv_mul_f32 v236, v62, v236\nM 28,164,92\nbuffer_load_dwordx4 v221, s[12:15], s63 offen offset:1024 lds\n"
"ds_read2st64_b32 v[152:153], v225 offset0:24 offset1:25\nv_mul_f32 v237, v63, v237\nM 12,160,72\nR 178,185,61440\ns_add_u32 s63, s54, 459008\nds_read2st64_b32 v[154:155], v225 offset0:26 offset1:27\n"
"v_exp_f32 v234, v234\nM 12,164,88\ns_add_u32 m0, s50, 114688\nds_read2st64_b32 v[156:157], v225 offset0:28 offset1:29\nds_read2st64_b32 v[158:159], v225 offset0:30 offset1:31\nv_exp_f32 v235, v235\n"
"M 20,160,68\nR 180,187,57344\nv_add_f32 v96, v96, v128\nv_add_f32 v97, v97, v129\nv_exp_f32 v236, v236\nM 20,164,84\n"
"buffer_load_dwordx4 v220, s[12:15], s63 offen lds\nG 15\nv_add_f32 v98, v98, v130\nv_exp_f32 v237, v237\nM 4,160,64\nR 182,187,61440\n"
"v_add_f32 v99, v99, v131\nv_add_f32 v100, v100, v132\nv_fma_f32 v234, v234, v219, v219\nM 4,164,80\nG 11\nv_add_f32 v101, v101, v133\n"
"v_add_f32 v102, v102, v134\nv_add_f32 v103, v103, v135\nv_fma_f32 v235, v235, v219, v219\nM 0,168,96\nR 160,188,0\nv_add_f32 v104, v104, v136\n"
"v_add_f32 v105, v105, v137\nM 0,172,112\nbuffer_load_dwordx4 v221, s[12:15], s63 offen offset:1024 lds\nv_add_f32 v106, v106, v138\nM 16,168,100\nR 162,188,4096\n"
"v_add_f32 v107, v107, v139\nv_add_f32 v108, v108, v140\nM 16,172,116\ns_add_u32 m0, s50, 0\nv_add_f32 v109, v109, v141\nv_add_f32 v110, v110, v142\n"
"v_add_f32 v111, v111, v143\nM 8,168,104\nR 164,190,0\nv_add_f32 v112, v112, v144\nv_add_f32 v113, v113, v145\nM 8,172,120\n"
"s_add_u32 s63, s54, 688384\nG 13\nv_add_f32 v114, v114, v146\nv_add_f32 v115, v115, v147\nG 12\nv_add_f32 v116, v116, v148\n"
"M 24,168,108\nR 166,190,4096\nv_add_f32 v117, v117, v149\nG 11\nv_add_f32 v118, v118, v150\nM 24,172,124\n"
"G 4\nv_add_f32 v119, v119, v151\nv_add_f32 v120, v120, v152\nv_add_f32 v121, v121, v153\nM 28,176,108\nR 168,189,0\n"
"v_add_f32 v122, v122, v154\nv_add_f32 v123, v123, v155\nM 28,180,124\nbuffer_load_dwordx4 v220, s[12:15], s63 offen lds\nM 12,176,104\nR 170,189,4096\n"
"v_add_f32 v124, v124, v156\nM 12,180,120\nv_add_f32 v125, v125, v157\nv_add_f32 v126, v126, v158\nM 20,176,100\nR 172,191,0\n"
"v_add_f32 v127, v127, v159\nM 20,180,116\nbuffer_load_dwordx4 v221, s[12:15], s63 offen offset:1024 lds\nM 4,176,96\nR 174,191,4096\ns_add_u32 m0, s50, 8192\n"
"M 4,180,112\nG 4\nv_rcp_f32 v234, v234\nM 0,160,128\nR 176,188,8192\ns_add_u32 s63, s54, 917760\n"
"M 0,164,144\nbuffer_load_dwordx4 v220, s[12:15], s63 offen lds\nM 16,160,132\nR 178,188,12288\nv_fma_f32 v236, v236, v219, v219\nM 16,164,148\n"
"v_rcp_f32 v235, v235\nM 8,160,136\nR 180,190,8192\nLq575:\ns_lshr_b32 s60, s45, 3\ns_lshl2_add_u32 s60, s60, s40\n"
"s_lshl_b32 s60, s60, 8\nv_lshl_add_u32 v224, v252, 3, s60\nv_add_u32 v224, Lq575+4096-LqPC, v224\nv_min_u32 v224, LqE-LqPC-4, v224\ns_mov_b64 exec, 3\nglobal_load_dword v224, v224, s[98:99]\n"
"s_mov_b64 exec, -1\nM 8,164,152\nv_rcp_f32 v236, v236\nM 24,160,140\nR 182,190,12288\nv_fma_f32 v237, v237, v219, v219\n"
"M 24,164,156\nG 4\nv_rcp_f32 v237, v237\nM 28,168,140\nR 160,189,8192\nv_mul_f32 v234, v60, v234\n"
"M 28,172,156\nbuffer_load_dwordx4 v221, s[12:15], s63 offen offset:1024 lds\nM 12,168,136\nR 162,189,12288\nv_mul_f32 v235, v61, v235\nM 12,172,152\n"
"s_add_u32 m0, s50, 16384\nv_mul_f32 v236, v62, v236\nv_mul_f32 v237, v63, v237\nM 20,168,132\nR 164,191,8192\nv_cvt_pk_bf16_f32 v58, v234, v235\n"
"M 20,172,148\ns_add_u32 s63, s54, 1147136\nv_cvt_pk_bf16_f32 v59, v236, v237\nM 4,168,128\nR 166,191,12288\nM 4,172,144\n"
"G 4\nM 0,176,160\nR 168,188,16384\nM 0,180,176\nbuffer_load_dwordx4 v220, s[12:15], s63 offen lds\nM 16,176,164\n"
"R 170,188,20480\nM 16,180,180\nbuffer_store_dwordx4 v[56:59], v252, s[20:23], s85 offen offset:1024 sc1\nM 8,176,168\nR 172,190,16384\nM 8,180,184\n"
"buffer_load_dwordx4 v221, s[12:15], s63 offen offset:1024 lds\nM 24,176,172\nR 174,190,20480\ns_add_u32 m0, s50, 24576\nM 24,180,188\ns_add_u32 s63, s54, 1376512\n"
"G 4\nM 28,160,172\nR 176,189,16384\ns_waitcnt vmcnt(10)\ns_barrier\nv_mul_f32 v230, v96, v96\n"
"M 28,164,188\nbuffer_load_dwordx4 v220, s[12:15], s63 offen lds\nv_mul_f32 v231, v97, v97\nM 12,160,168\nR 178,189,20480\nv_mul_f32 v232, v98, v98\n"
"v_mul_f32 v233, v99, v99\nM 12,164,184\ns_add_u32 s84, s73, 23070720\ns_add_u32 s85, s73, 23185408\nv_fma_f32 v230, v230, v215, v214\nv_fma_f32 v231, v231, v215, v214\n"
"M 20,160,164\nR 180,191,16384\nv_fma_f32 v232, v232, v215, v214\nv_fma_f32 v233, v233, v215, v214\nM 20,164,180\nbuffer_load_dwordx4 v221, s[12:15], s63 offen offset:1024 lds\n"
"v_mul_f32 v230, v96, v230\nM 4,160,160\nR 182,191,20480\ns_add_u32 m0, s50, 32768\nv_mul_f32 v231, v97, v231\nM 4,164,176\n"
"G 4\nv_exp_f32 v230, v230\nv_mul_f32 v232, v98, v232\nM 0,168,192\nR 160,188,24576\ns_add_u32 s63, s54, 1605888\n"
"v_mul_f32 v233, v99, v233\nM 0,172,208\nbuffer_load_dwordx4 v220, s[12:15], s63 offen lds\nv_exp_f32 v231, v231\nM 16,168,196\nR 162,188,28672\n"
"v_fma_f32 v230, v230, v216, v216\nv_exp_f32 v232, v232\nM 16,172,212\nv_exp_f32 v233, v233\nv_fma_f32 v231, v231, v216, v216\nM 8,168,200\n"
"R 164,190,24576\nv_fma_f32 v232, v232, v216, v216\nv_fma_f32 v233, v233, v216, v216\nM 8,172,216\nv_rcp_f32 v230, v230\nv_rcp_f32 v231, v231\n"
"M 24,168,204\nR 166,190,28672\nv_mul_f32 v230, v96, v230\nv_rcp_f32 v232, v232\nM 24,172,220\nG 4\n"
"v_rcp_f32 v233, v233\nv_mul_f32 v231, v97, v231\nM 28,176,204\nR 168,189,24576\nv_mul_f32 v232, v98, v232\nv_mul_f32 v233, v99, v233\n"
"M 28,180,220\nbuffer_load_dwordx4 v221, s[12:15], s63 offen offset:1024 lds\nv_cvt_pk_bf16_f32 v96, v230, v231\nM 12,176,200\nR 170,189,28672\ns_add_u32 s63, s54, 1835264\n"
"v_cvt_pk_bf16_f32 v97, v232, v233\nM 12,180,216\ns_add_u32 m0, s50, 40960\nv_mul_f32 v234, v100, v100\nv_mul_f32 v235, v101, v101\nv_mul_f32 v236, v102, v102\n"
"M 20,176,196\nR 172,191,24576\nv_mul_f32 v237, v103, v103\nv_fma_f32 v234, v234, v215, v214\nM 20,180,212\nbuffer_load_dwordx4 v220, s[12:15], s63 offen lds\n"
"v_fma_f32 v235, v235, v215, v214\nM 4,176,192\nR 174,191,28672\nv_fma_f32 v236, v236, v215, v214\nv_fma_f32 v237, v237, v215, v214\nM 4,180,208\n"
"G 4\nv_mul_f32 v234, v100, v234\nv_mul_f32 v235, v101, v235\nv_mul_f32 v236, v102, v236\nM 0,160,224\nR 176,188,32768\n"
"v_mul_f32 v237, v103, v237\nv_exp_f32 v234, v234\nM 0,164,240\nbuffer_load_dwordx4 v221, s[12:15], s63 offen offset:1024 lds\nv_exp_f32 v235, v235\nM 16,160,228\n"
"R 178,188,36864\ns_add_u32 s63, s54, 2064640\nv_exp_f32 v236, v236\nM 16,164,244\ns_add_u32 m0, s50, 49152\nv_exp_f32 v237, v237\n"
"v_fma_f32 v234, v234, v216, v216\nM 8,160,232\nR 180,188,40960\nv_fma_f32 v235, v235, v216, v216\nv_fma_f32 v236, v236, v216, v216\nM 8,164,248\n"
"buffer_load_dwordx4 v220, s[12:15], s63 offen lds\nv_fma_f32 v237, v237, v216, v216\nM 24,160,236\nR 182,188,45056\ns_mov_b32 s60, 158720\nv_bfe_u32 v242, v252, 4, 4\n"
"v_lshl_add_u32 v242, v242, 3, s60\nv_rcp_f32 v234, v234\nM 24,164,252\nds_read_b64 v[238:239], v242 offset:0\nG 5\nv_rcp_f32 v235, v235\n"
"M 28,168,236\nR 160,189,32768\nds_read_b64 v[240:241], v242 offset:128\nv_rcp_f32 v236, v236\nM 28,172,252\nds_read_b64 v[242:243], v242 offset:0\n"
"v_mul_f32 v234, v100, v234\nv_rcp_f32 v237, v237\nM 12,168,232\nR 162,189,36864\nv_mul_f32 v235, v101, v235\nv_mul_f32 v236, v102, v236\n"
"M 12,172,248\nbuffer_load_dwordx4 v221, s[12:15], s63 offen offset:1024 lds\nv_mul_f32 v237, v103, v237\nM 20,168,228\nR 164,189,40960\nv_cvt_pk_bf16_f32 v98, v234, v235\n"
"v_cvt_pk_bf16_f32 v99, v236, v237\nM 20,172,244\nv_mul_f32 v230, v104, v104\nv_mul_f32 v231, v105, v105\nv_mul_f32 v232, v106, v106\nM 4,168,224\n"
"R 166,189,45056\nv_mul_f32 v233, v107, v107\nv_fma_f32 v230, v230, v215, v214\nM 4,172,240\ns_waitcnt vmcnt(16) lgkmcnt(3)\ns_barrier\n"
"v_fma_f32 v231, v231, v215, v214\nv_mfma_f32_16x16x32_bf16 v[32:35], v[176:179], a[0:3], 0\nR 168,190,32768\nLpg11:\ns_cbranch_execz Lpg15\nv_fma_f32 v232, v232, v215, v214\n"
"v_fma_f32 v233, v233, v215, v214\nM 32,180,16\ns_mov_b32 m0, s51\nv_mul_f32 v230, v104, v230\nv_mul_f32 v231, v105, v231\nv_mul_f32 v232, v106, v232\n"
"v_mfma_f32_16x16x32_bf16 v[48:51], v[176:179], a[4:7], 0\nR 170,190,36864\nv_mul_f32 v233, v107, v233\nv_exp_f32 v230, v230\nM 48,180,20\nbuffer_store_dwordx4 v[96:99], v252, s[20:23], s84 offen offset:0 sc1\n"
"v_exp_f32 v231, v231\nv_mfma_f32_16x16x32_bf16 v[64:67], v[176:179], a[8:11], 0\nR 172,190,40960\nv_mov_b32 v226, s46\nv_mfma_f32_16x16x32_bf16 v[0:3], v[238:241], v[208:211], v[0:3]\nv_exp_f32 v232, v232\n"
"v_fma_f32 v230, v230, v216, v216\nv_exp_f32 v233, v233\nM 64,180,24\nv_mov_b32 v227, s47\nv_fma_f32 v231, v231, v216, v216\nv_fma_f32 v232, v232, v216, v216\n"
"v_mfma_f32_16x16x32_bf16 v[80:83], v[176:179], a[12:15], 0\nR 174,190,45056\nv_fma_f32 v233, v233, v216, v216\nv_mfma_f32_16x16x32_bf16 v[4:7], v[240:243], v[208:211], v[4:7]\ns_add_u32 s61, s81, 385024\ns_lshl_b32 s62, s44, 5\n"
"s_add_u32 s61, s61, s62\ns_waitcnt vmcnt(10)\ns_mov_b64 exec, 1\nbuffer_atomic_swap_x2 v[226:227], off, s[20:23], s61 sc1\ns_mov_b64 exec, -1\nM 80,180,28\n"
"ds_write_addtid_b32 v8 offset:0\nG 5\nv_rcp_f32 v230, v230\nv_rcp_f32 v231, v231\nv_mfma_f32_16x16x32_bf16 v[84:87], v[160:163], a[12:15], 0\nR 176,191,32768\n"
"v_mfma_f32_16x16x32_bf16 v[16:19], v[240:243], v[210:213], v[16:19]\nds_write_addtid_b32 v9 offset:256\nv_rcp_f32 v232, v232\nv_rcp_f32 v233, v233\nv_mul_f32 v230, v104, v230\nM 84,164,28\n"
"ds_write_addtid_b32 v10 offset:512\nv_mul_f32 v231, v105, v231\nv_mul_f32 v232, v106, v232\nv_mul_f32 v233, v107, v233\nv_mfma_f32_16x16x32_bf16 v[68:71], v[160:163], a[8:11], 0\nR 178,191,36864\n"
"v_cvt_pk_bf16_f32 v104, v230, v231\nv_mfma_f32_16x16x32_bf16 v[20:23], v[238:241], v[210:213], v[20:23]\nds_write_addtid_b32 v11 offset:768\nv_cvt_pk_bf16_f32 v105, v232, v233\nv_mul_f32 v234, v108, v108\nv_mul_f32 v235, v109, v109\n"
"v_mul_f32 v236, v110, v110\nM 68,164,24\nds_write_addtid_b32 v12 offset:1024\nv_mul_f32 v237, v111, v111\nv_fma_f32 v234, v234, v215, v214\nv_fma_f32 v235, v235, v215, v214\n"
"v_mfma_f32_16x16x32_bf16 v[52:55], v[160:163], a[4:7], 0\nR 180,191,40960\nds_write_addtid_b32 v13 offset:1280\nv_fma_f32 v236, v236, v215, v214\nv_fma_f32 v237, v237, v215, v214\nM 52,164,20\n"
"ds_write_addtid_b32 v14 offset:1536\nds_write_addtid_b32 v15 offset:1792\nds_write_addtid_b32 v24 offset:4096\nds_write_addtid_b32 v25 offset:4352\nds_write_addtid_b32 v26 offset:4608\nds_write_addtid_b32 v27 offset:4864\n"
"ds_write_addtid_b32 v28 offset:5120\nds_write_addtid_b32 v29 offset:5376\nds_write_addtid_b32 v30 offset:5632\nds_write_addtid_b32 v31 offset:5888\nv_mul_f32 v234, v108, v234\nv_mul_f32 v235, v109, v235\n"
"v_mul_f32 v236, v110, v236\nv_mfma_f32_16x16x32_bf16 v[36:39], v[160:163], a[0:3], 0\nR 182,191,45056\ns_add_u32 m0, s50, 57344\nv_mul_f32 v237, v111, v237\nM 36,164,16\n"
"s_add_u32 s63, s54, 2294016\nG 15\nv_mul_f32 v230, v112, v112\nv_exp_f32 v234, v234\nv_mfma_f32_16x16x32_bf16 v[40:43], v[168:171], a[0:3], 0\nR 160,188,49152\n"
"s_waitcnt vmcnt(8) lgkmcnt(2)\ns_barrier\nv_exp_f32 v235, v235\nM 40,172,16\nbuffer_load_dwordx4 v220, s[12:15], s63 offen lds\nds_read2st64_b32 v[8:9], v225 offset0:0 offset1:1\n"
"v_exp_f32 v236, v236\nv_mfma_f32_16x16x32_bf16 v[56:59], v[168:171], a[4:7], 0\nR 162,188,53248\nds_read2st64_b32 v[10:11], v225 offset0:2 offset1:3\nv_fma_f32 v234, v234, v216, v216\nv_exp_f32 v237, v237\n"
"M 56,172,20\nds_read2st64_b32 v[12:13], v225 offset0:4 offset1:5\nv_rcp_f32 v234, v234\nv_fma_f32 v235, v235, v216, v216\nv_mfma_f32_16x16x32_bf16 v[72:75], v[168:171], a[8:11], 0\nR 164,184,0\n"
"ds_read2st64_b32 v[14:15], v225 offset0:6 offset1:7\nv_fma_f32 v236, v236, v216, v216\nv_fma_f32 v237, v237, v216, v216\nM 72,172,24\nds_read2st64_b32 v[24:25], v225 offset0:16 offset1:17\nv_rcp_f32 v235, v235\n"
"v_rcp_f32 v236, v236\nv_mfma_f32_16x16x32_bf16 v[88:91], v[168:171], a[12:15], 0\nR 166,184,4096\nds_read2st64_b32 v[26:27], v225 offset0:18 offset1:19\nv_mul_f32 v234, v108, v234\nv_rcp_f32 v237, v237\n"
"M 88,172,28\nds_read2st64_b32 v[28:29], v225 offset0:20 offset1:21\nG 11\nv_mul_f32 v235, v109, v235\nv_mul_f32 v236, v110, v236\nv_mul_f32 v237, v111, v237\n"
"v_mfma_f32_16x16x32_bf16 v[92:95], v[176:179], a[12:15], 0\nR 168,189,49152\nds_read2st64_b32 v[30:31], v225 offset0:22 offset1:23\nv_cvt_pk_bf16_f32 v106, v234, v235\nv_cvt_pk_bf16_f32 v107, v236, v237\nM 92,180,28\n"
"buffer_load_dwordx4 v221, s[12:15], s63 offen offset:1024 lds\nG 11\nv_add_f32 v0, v0, v8\nv_mul_f32 v231, v113, v113\nv_mfma_f32_16x16x32_bf16 v[76:79], v[176:179], a[8:11], 0\nR 170,189,53248\n"
"v_mul_f32 v232, v114, v114\nv_add_f32 v1, v1, v9\nv_mul_f32 v233, v115, v115\nM 76,180,24\nv_fma_f32 v230, v230, v218, v217\nv_fma_f32 v231, v231, v218, v217\n"
"G 10\nv_add_f32 v2, v2, v10\nv_fma_f32 v232, v232, v218, v217\nv_mfma_f32_16x16x32_bf16 v[60:63], v[176:179], a[4:7], 0\nR 172,185,0\nv_fma_f32 v233, v233, v218, v217\n"
"v_add_f32 v3, v3, v11\nv_mul_f32 v230, v112, v230\nM 60,180,20\nbuffer_store_dwordx4 v[104:107], v252, s[20:23], s84 offen offset:1024 sc1\nG 10\nv_add_f32 v4, v4, v12\n"
"v_mul_f32 v231, v113, v231\nv_mfma_f32_16x16x32_bf16 v[44:47], v[176:179], a[0:3], 0\nR 174,185,4096\nv_mul_f32 v232, v114, v232\nv_add_f32 v5, v5, v13\nv_mul_f32 v233, v115, v233\n"
"M 44,180,16\nG 7\nv_exp_f32 v230, v230\nv_add_f32 v6, v6, v14\nv_exp_f32 v231, v231\nM 32,160,32\n"
"R 176,190,49152\nv_mov_b32 v248, 0\nv_add_f32 v7, v7, v15\nv_exp_f32 v232, v232\nM 32,164,48\ns_add_u32 m0, s50, 65536\n"
"v_mov_b32 v249, 0\nv_fma_f32 v230, v230, v219, v219\nv_add_f32 v16, v16, v24\nv_exp_f32 v233, v233\nM 48,160,36\nR 178,190,53248\n"
"s_add_u32 s61, s82, 385024\ns_mov_b64 exec, 0xf\nbuffer_atomic_or_x2 v[248:249], v250, s[20:23], s61 offen sc0 sc1\ns_mov_b64 exec, -1\nv_add_f32 v17, v17, v25\nv_fma_f32 v231, v231, v219, v219\n"
"M 48,164,52\ns_add_u32 s63, s54, 2523392\nG 8\nv_add_f32 v18, v18, v26\nv_add_f32 v19, v19, v27\nG 7\n"
"v_add_f32 v20, v20, v28\nM 64,160,40\nR 180,186,0\nv_add_f32 v21, v21, v29\nG 6\nv_add_f32 v22, v22, v30\n"
"v_add_f32 v23, v23, v31\nM 64,164,56\nbuffer_load_dwordx4 v220, s[12:15], s63 offen lds\nM 80,160,44\nR 182,186,4096\nv_fma_f32 v232, v232, v219, v219\n"
"M 80,164,60\nG 4\nv_rcp_f32 v230, v230\nM 84,168,44\nR 160,191,49152\nv_fma_f32 v233, v233, v219, v219\n"
"M 84,172,60\nv_rcp_f32 v231, v231\nM 68,168,40\nR 162,191,53248\nv_mul_f32 v230, v112, v230\nM 68,172,56\n"
"buffer_load_dwordx4 v221, s[12:15], s63 offen offset:1024 lds\nM 52,168,36\nR 164,187,0\ns_add_u32 s63, s54, 2752768\nM 52,172,52\ns_add_u32 m0, s50, 73728\n"
"v_rcp_f32 v232, v232\nM 36,168,32\nR 166,187,4096\nv_mul_f32 v231, v113, v231\nM 36,172,48\nG 4\n"
"v_rcp_f32 v233, v233\nM 40,176,32\nR 168,184,8192\nv_mul_f32 v232, v114, v232\nM 40,180,48\nbuffer_load_dwordx4 v220, s[12:15], s63 offen lds\n"
"M 56,176,36\nR 170,184,12288\nv_mul_f32 v233, v115, v233\nM 56,180,52\nv_cvt_pk_bf16_f32 v112, v230, v231\nv_cvt_pk_bf16_f32 v113, v232, v233\n"
"M 72,176,40\nR 172,184,16384\nv_mul_f32 v234, v116, v116\nM 72,180,56\nv_mul_f32 v235, v117, v117\nv_mul_f32 v236, v118, v118\n"
"M 88,176,44\nR 174,184,20480\nv_mul_f32 v237, v119, v119\nM 88,180,60\nG 4\nv_fma_f32 v234, v234, v218, v217\n"
"v_fma_f32 v235, v235, v218, v217\nM 92,160,44\nR 176,185,8192\nv_fma_f32 v236, v236, v218, v217\nM 92,164,60\nbuffer_load_dwordx4 v221, s[12:15], s63 offen offset:1024 lds\n"
"M 76,160,40\nR 178,185,12288\nv_fma_f32 v237, v237, v218, v217\nM 76,164,56\ns_add_u32 m0, s50, 81920\nv_mul_f32 v234, v116, v234\n"
"v_mul_f32 v235, v117, v235\nM 60,160,36\nR 180,185,16384\nv_mul_f32 v236, v118, v236\nM 60,164,52\ns_add_u32 s63, s54, 2982144\n"
"v_exp_f32 v234, v234\nM 44,160,32\nR 182,185,20480\nv_mul_f32 v237, v119, v237\nM 44,164,48\nG 4\n"
"v_exp_f32 v235, v235\nM 32,168,64\nR 160,186,8192\nv_fma_f32 v234, v234, v219, v219\nM 32,172,80\nbuffer_load_dwordx4 v220, s[12:15], s63 offen lds\n"
"M 48,168,68\nR 162,186,12288\nv_fma_f32 v235, v235, v219, v219\nM 48,172,84\nv_exp_f32 v236, v236\nM 64,168,72\n"
"R 164,186,16384\nv_fma_f32 v236, v236, v219, v219\nM 64,172,88\nbuffer_load_dwordx4 v221, s[12:15], s63 offen offset:1024 lds\nM 80,168,76\nR 166,186,20480\n"
"s_add_u32 m0, s50, 90112\nM 80,172,92\nG 4\nv_exp_f32 v237, v237\nM 84,176,76\nR 168,187,8192\n"
"s_add_u32 s63, s54, 3211520\nM 84,180,92\nbuffer_load_dwordx4 v220, s[12:15], s63 offen lds\nM 68,176,72\nR 170,187,12288\nv_fma_f32 v237, v237, v219, v219\n"
"M 68,180,88\nv_rcp_f32 v234, v234\nM 52,176,68\nR 172,187,16384\nv_mul_f32 v234, v116, v234\nM 52,180,84\n"
"s_add_u32 s64, s68, 23068672\nv_rcp_f32 v235, v235\nM 36,176,64\nR 174,187,20480\nv_mul_f32 v235, v117, v235\nM 36,180,80\n"
"G 4\nv_rcp_f32 v236, v236\nM 40,160,64\nR 176,184,24576\ns_waitcnt vmcnt(13)\ns_barrier\n"
"s_waitcnt vmcnt(7)\nv_cmp_eq_u64 s[86:87], s[46:47], v[248:249]\nM 40,164,80\ns_add_u32 s65, s68, 23183360\nv_rcp_f32 v237, v237\nM 56,160,68\n"
"R 178,184,28672\ns_and_b32 s62, s86, 0xf\ns_cmp_eq_u32 s62, 0xf\ns_cbranch_scc0 Lq377\nLq378:\nM 56,164,84\n"
"buffer_load_dwordx4 v221, s[12:15], s63 offen offset:1024 lds\nM 72,160,72\nR 180,184,32768\nv_mul_f32 v236, v118, v236\nM 72,164,88\nbuffer_load_dwordx4 a[0:3], v252, s[20:23], s64 offen sc1\n"
"M 88,160,76\nR 182,184,36864\nv_mul_f32 v237, v119, v237\nM 88,164,92\nG 4\nv_cvt_pk_bf16_f32 v114, v234, v235\n"
"v_cvt_pk_bf16_f32 v115, v236, v237\nM 92,168,76\nR 160,185,24576\nv_mul_f32 v230, v120, v120\nM 92,172,92\nbuffer_load_dwordx4 a[4:7], v252, s[20:23], s65 offen sc1\n"
"M 76,168,72\nR 162,185,28672\nv_mul_f32 v231, v121, v121\nM 76,172,88\ns_add_u32 s66, s69, 23068672\nv_mul_f32 v232, v122, v122\n"
"v_mul_f32 v233, v123, v123\nM 60,168,68\nR 164,185,32768\nv_fma_f32 v230, v230, v218, v217\nM 60,172,84\ns_add_u32 s67, s69, 23183360\n"
"v_fma_f32 v231, v231, v218, v217\nv_fma_f32 v232, v232, v218, v217\nM 44,168,64\nR 166,185,36864\nv_fma_f32 v233, v233, v218, v217\nM 44,172,80\n"
"G 4\nv_mul_f32 v230, v120, v230\nv_mul_f32 v231, v121, v231\nM 32,176,96\nR 168,186,24576\nv_mul_f32 v232, v122, v232\n"
"M 32,180,112\nbuffer_load_dwordx4 a[8:11], v252, s[20:23], s66 offen sc1\nM 48,176,100\nR 170,186,28672\nv_mul_f32 v233, v123, v233\nM 48,180,116\n"
"buffer_load_dwordx4 a[12:15], v252, s[20:23], s67 offen sc1\nM 64,176,104\nR 172,186,32768\nv_mul_f32 v234, v124, v124\nM 64,180,120\ns_add_u32 s64, s68, 23075840\n"
"v_exp_f32 v230, v230\nM 80,176,108\nR 174,186,36864\nv_fma_f32 v230, v230, v219, v219\nM 80,180,124\nG 4\n"
"v_exp_f32 v231, v231\nM 84,160,108\nR 176,187,24576\nv_fma_f32 v231, v231, v219, v219\nM 84,164,124\nbuffer_load_dwordx4 a[16:19], v252, s[20:23], s64 offen sc1\n"
"M 68,160,104\nR 178,187,28672\nv_mul_f32 v235, v125, v125\nM 68,164,120\ns_add_u32 s65, s68, 23190528\nv_exp_f32 v232, v232\n"
"M 52,160,100\nR 180,187,32768\nv_fma_f32 v232, v232, v219, v219\nM 52,164,116\ns_add_u32 s66, s69, 23075840\nv_exp_f32 v233, v233\n"
"M 36,160,96\nR 182,187,36864\nv_fma_f32 v233, v233, v219, v219\nM 36,164,112\nG 4\nv_rcp_f32 v230, v230\n"
"M 40,168,96\nR 160,184,40960\nv_mul_f32 v230, v120, v230\nM 40,172,112\nbuffer_load_dwordx4 a[20:23], v252, s[20:23], s65 offen sc1\nM 56,168,100\n"
"R 162,184,45056\nv_mul_f32 v236, v126, v126\nM 56,172,116\nbuffer_load_dwordx4 a[24:27], v252, s[20:23], s66 offen sc1\nM 72,168,104\nR 164,184,49152\n"
"v_mul_f32 v237, v127, v127\nM 72,172,120\ns_add_u32 s67, s69, 23190528\nv_rcp_f32 v231, v231\nM 88,168,108\nR 166,184,53248\n"
"v_mul_f32 v231, v121, v231\nM 88,172,124\nG 4\nv_rcp_f32 v232, v232\nM 92,176,108\nR 168,185,40960\n"
"v_mul_f32 v232, v122, v232\nM 92,180,124\nbuffer_load_dwordx4 a[28:31], v252, s[20:23], s67 offen sc1\nM 76,176,104\nR 170,185,45056\nv_cvt_pk_bf16_f32 v120, v230, v231\n"
"M 76,180,120\ns_add_u32 s64, s68, 23069696\nv_rcp_f32 v233, v233\nM 60,176,100\nR 172,185,49152\nv_mul_f32 v233, v123, v233\n"
"M 60,180,116\ns_add_u32 s65, s68, 23184384\nv_cvt_pk_bf16_f32 v121, v232, v233\nv_fma_f32 v234, v234, v218, v217\nM 44,176,96\nR 174,185,53248\n"
"v_fma_f32 v235, v235, v218, v217\nM 44,180,112\nG 4\nv_fma_f32 v236, v236, v218, v217\nv_fma_f32 v237, v237, v218, v217\nM 32,160,128\n"
"R 176,186,40960\ns_add_u32 m0, s50, 98304\nM 32,164,144\nbuffer_load_dwordx4 a[32:35], v252, s[20:23], s64 offen sc1\nM 48,160,132\nR 178,186,45056\n"
"s_add_u32 s63, s54, 3440896\nM 48,164,148\nbuffer_load_dwordx4 a[36:39], v252, s[20:23], s65 offen sc1\nM 64,160,136\nR 180,186,49152\nv_mul_f32 v234, v124, v234\n"
"M 64,164,152\ns_add_u32 s66, s69, 23069696\nbuffer_load_dwordx4 v220, s[12:15], s63 offen lds\nM 80,160,140\nR 182,186,53248\nv_mul_f32 v235, v125, v235\n"
"M 80,164,156\nG 4\nv_exp_f32 v234, v234\nM 84,168,140\nR 160,187,40960\nv_mul_f32 v236, v126, v236\n"
"M 84,172,156\nbuffer_load_dwordx4 a[40:43], v252, s[20:23], s66 offen sc1\nbuffer_load_dwordx4 v221, s[12:15], s63 offen offset:1024 lds\nM 68,168,136\nR 162,187,45056\nv_mul_f32 v237, v127, v237\n"
"M 68,172,152\ns_add_u32 s67, s69, 23184384\nv_exp_f32 v235, v235\nM 52,168,132\nR 164,187,49152\ns_waitcnt vmcnt(15)\n"
"s_barrier\nM 52,172,148\nbuffer_load_dwordx4 a[44:47], v252, s[20:23], s67 offen sc1\ns_movk_i32 s63, 2048\nv_mad_u32_u24 v220, v228, s63, v229\nv_add_u32 v221, 916480, v220\n"
"s_add_u32 m0, s50, 106496\ns_add_u32 s63, s56, 0\nbuffer_load_dwordx4 v220, s[16:19], s63 offen lds\nM 36,168,128\nR 166,187,53248\nv_fma_f32 v234, v234, v219, v219\n"
"M 36,172,144\nG 4\nv_exp_f32 v236, v236\nM 40,176,128\nR 168,184,57344\nv_fma_f32 v235, v235, v219, v219\n"
"M 40,180,144\ns_add_u32 s64, s68, 23076864\nbuffer_load_dwordx4 v221, s[16:19], s63 offen offset:1024 lds\nM 56,176,132\nR 170,184,61440\ns_add_u32 m0, s50, 114688\n"
"M 56,180,148\nbuffer_load_dwordx4 a[48:51], v252, s[20:23], s64 offen sc1\nM 72,176,136\nR 172,188,0\ns_add_u32 s63, s56, 524288\nM 72,180,152\n"
"s_add_u32 s65, s68, 23191552\nbuffer_load_dwordx4 v220, s[16:19], s63 offen lds\nM 88,176,140\nR 174,188,4096\nv_fma_f32 v236, v236, v219, v219\nM 88,180,156\n"
"G 4\nv_exp_f32 v237, v237\nM 92,160,140\nR 176,185,57344\nv_fma_f32 v237, v237, v219, v219\nM 92,164,156\n"
"buffer_load_dwordx4 a[52:55], v252, s[20:23], s65 offen sc1\nbuffer_load_dwordx4 v221, s[16:19], s63 offen offset:1024 lds\nM 76,160,136\nR 178,185,61440\nM 76,164,152\ns_add_u32 s66, s69, 23076864\n"
"v_rcp_f32 v234, v234\nM 60,160,132\nR 180,189,0\nv_mul_f32 v234, v124, v234\nM 60,164,148\nbuffer_load_dwordx4 a[56:59], v252, s[20:23], s66 offen sc1\n"
"M 44,160,128\nR 182,189,4096\nM 44,164,144\nG 4\nv_rcp_f32 v235, v235\nM 32,168,160\n"
"R 160,186,57344\nv_mul_f32 v235, v125, v235\nM 32,172,176\ns_add_u32 s67, s69, 23191552\nv_rcp_f32 v236, v236\nM 48,168,164\n"
"R 162,186,61440\nv_mul_f32 v236, v126, v236\nM 48,172,180\nbuffer_load_dwordx4 a[60:63], v252, s[20:23], s67 offen sc1\nM 64,168,168\nR 164,190,0\n"
"v_cvt_pk_bf16_f32 v122, v234, v235\nM 64,172,184\nbuffer_store_dwordx4 v[112:115], v252, s[20:23], s85 offen offset:0 sc1\nM 80,168,172\nR 166,190,4096\nv_rcp_f32 v237, v237\n"
"M 80,172,188\nG 4\nv_mul_f32 v237, v127, v237\nv_cvt_pk_bf16_f32 v123, v236, v237\nM 84,176,172\nR 168,187,57344\n"
"M 84,180,188\ns_add_u32 m0, s50, 0\nbuffer_store_dwordx4 v[120:123], v252, s[20:23], s85 offen offset:1024 sc1\nM 68,176,168\nR 170,187,61440\nv_mul_f32 v230, v0, v0\n"
"v_mul_f32 v231, v1, v1\nM 68,180,184\ns_add_u32 s63, s56, 65536\ns_add_u32 s84, s73, 23074816\nLpg12:\ns_cbranch_execz LretO0n\n"
"s_add_u32 s85, s73, 23189504\nv_mul_f32 v232, v2, v2\nM 52,176,164\nR 172,191,0\nv_mul_f32 v233, v3, v3\nv_fma_f32 v230, v230, v215, v214\n"
"M 52,180,180\nbuffer_load_dwordx4 v220, s[16:19], s63 offen lds\nv_fma_f32 v231, v231, v215, v214\nM 36,176,160\nR 174,191,4096\nv_fma_f32 v232, v232, v215, v214\n"
"v_fma_f32 v233, v233, v215, v214\nM 36,180,176\nG 4\nv_mul_f32 v230, v0, v230\nv_mul_f32 v231, v1, v231\nv_mul_f32 v232, v2, v232\n"
"M 40,160,160\nR 176,188,8192\nv_mul_f32 v233, v3, v233\nv_exp_f32 v230, v230\nM 40,164,176\nv_exp_f32 v231, v231\n"
"v_exp_f32 v232, v232\nM 56,160,164\nR 178,188,12288\nv_fma_f32 v230, v230, v216, v216\nv_exp_f32 v233, v233\nM 56,164,180\n"
"buffer_load_dwordx4 v221, s[16:19], s63 offen offset:1024 lds\nv_fma_f32 v231, v231, v216, v216\nM 72,160,168\nR 180,188,16384\ns_add_u32 s63, s56, 589824\nv_fma_f32 v232, v232, v216, v216\n"
"M 72,164,184\ns_add_u32 m0, s50, 8192\nv_rcp_f32 v230, v230\nv_fma_f32 v233, v233, v216, v216\nM 88,160,172\nR 182,188,20480\n"
"v_mul_f32 v230, v0, v230\nv_rcp_f32 v231, v231\nM 88,164,188\nG 4\nv_rcp_f32 v232, v232\nv_rcp_f32 v233, v233\n"
"M 92,168,172\nR 160,189,8192\nv_mul_f32 v231, v1, v231\nv_mul_f32 v232, v2, v232\nM 92,172,188\nbuffer_load_dwordx4 v220, s[16:19], s63 offen lds\n"
"v_mul_f32 v233, v3, v233\nM 76,168,168\nR 162,189,12288\nv_cvt_pk_bf16_f32 v0, v230, v231\nv_cvt_pk_bf16_f32 v1, v232, v233\nM 76,172,184\n"
"v_mul_f32 v234, v4, v4\nv_mul_f32 v235, v5, v5\nv_mul_f32 v236, v6, v6\nM 60,168,164\nR 164,189,16384\nv_mul_f32 v237, v7, v7\n"
"v_fma_f32 v234, v234, v215, v214\nM 60,172,180\nbuffer_load_dwordx4 v221, s[16:19], s63 offen offset:1024 lds\nv_fma_f32 v235, v235, v215, v214\nM 44,168,160\nR 166,189,20480\n"
"s_add_u32 m0, s50, 16384\nv_fma_f32 v236, v236, v215, v214\nM 44,172,176\nG 4\nv_fma_f32 v237, v237, v215, v214\nv_mul_f32 v234, v4, v234\n"
"v_mul_f32 v235, v5, v235\nM 32,176,192\nR 168,190,8192\ns_add_u32 s63, s57, 0\nv_mul_f32 v236, v6, v236\nM 32,180,208\n"
"buffer_load_dwordx4 v220, s[16:19], s63 offen lds\nv_mul_f32 v237, v7, v237\nM 48,176,196\nR 170,190,12288\nv_mul_f32 v230, v16, v16\nv_exp_f32 v234, v234\n"
"M 48,180,212\nv_exp_f32 v235, v235\nv_exp_f32 v236, v236\nM 64,176,200\nR 172,190,16384\nv_fma_f32 v234, v234, v216, v216\n"
"v_exp_f32 v237, v237\nM 64,180,216\nbuffer_load_dwordx4 v221, s[16:19], s63 offen offset:1024 lds\nv_fma_f32 v235, v235, v216, v216\nM 80,176,204\nR 174,190,20480\n"
"s_add_u32 s63, s57, 524288\nv_fma_f32 v236, v236, v216, v216\nM 80,180,220\nG 4\nv_rcp_f32 v234, v234\nv_fma_f32 v237, v237, v216, v216\n"
"M 84,160,204\nR 176,191,8192\ns_waitcnt vmcnt(10)\ns_barrier\nv_rcp_f32 v235, v235\nM 84,164,220\n"
"s_add_u32 m0, s50, 24576\nv_rcp_f32 v236, v236\nv_rcp_f32 v237, v237\nM 68,160,200\nR 178,191,12288\nv_mul_f32 v234, v4, v234\n"
"v_mul_f32 v235, v5, v235\nM 68,164,216\nbuffer_load_dwordx4 v220, s[16:19], s63 offen lds\nv_mul_f32 v236, v6, v236\nM 52,160,196\nR 180,191,16384\n"
"v_mul_f32 v237, v7, v237\nv_cvt_pk_bf16_f32 v2, v234, v235\nM 52,164,212\nv_cvt_pk_bf16_f32 v3, v236, v237\nv_mul_f32 v231, v17, v17\nv_mul_f32 v232, v18, v18\n"
"M 36,160,192\nR 182,191,20480\nv_mul_f32 v233, v19, v19\nv_fma_f32 v230, v230, v218, v217\nM 36,164,208\nG 4\n"
"v_fma_f32 v231, v231, v218, v217\nv_fma_f32 v232, v232, v218, v217\nv_fma_f32 v233, v233, v218, v217\nM 40,168,192\nR 160,188,24576\nv_mul_f32 v230, v16, v230\n"
"v_mul_f32 v231, v17, v231\nM 40,172,208\nbuffer_load_dwordx4 v221, s[16:19], s63 offen offset:1024 lds\nv_mul_f32 v232, v18, v232\nM 56,168,196\nR 162,188,28672\n"
"v_mul_f32 v233, v19, v233\nv_exp_f32 v230, v230\nM 56,172,212\nbuffer_store_dwordx4 v[0:3], v252, s[20:23], s84 offen offset:0 sc1\nv_exp_f32 v231, v231\nM 72,168,200\n"
"R 164,188,32768\nv_fma_f32 v230, v230, v219, v219\nv_exp_f32 v232, v232\nM 72,172,216\ns_add_u32 m0, s50, 32768\nv_exp_f32 v233, v233\n"
"v_fma_f32 v231, v231, v219, v219\nM 88,168,204\nR 166,188,36864\nv_mov_b32 v226, s46\nv_fma_f32 v232, v232, v219, v219\nM 88,172,220\n"
"G 4\nv_mov_b32 v227, s47\nv_fma_f32 v233, v233, v219, v219\nv_rcp_f32 v230, v230\nM 92,176,204\nR 168,189,24576\n"
"s_add_u32 s61, s81, 385032\ns_lshl_b32 s62, s44, 5\ns_add_u32 s61, s61, s62\ns_waitcnt vmcnt(9)\ns_mov_b64 exec, 1\nbuffer_atomic_swap_x2 v[226:227], off, s[20:23], s61 sc1\n"
"s_mov_b64 exec, -1\nv_rcp_f32 v231, v231\nM 92,180,220\ns_add_u32 s63, s57, 65536\nv_rcp_f32 v232, v232\nv_rcp_f32 v233, v233\n"
"M 76,176,200\nR 170,189,28672\nv_mul_f32 v230, v16, v230\nv_mul_f32 v231, v17, v231\nM 76,180,216\nbuffer_load_dwordx4 v220, s[16:19], s63 offen lds\n"
"v_mul_f32 v232, v18, v232\nM 60,176,196\nR 172,189,32768\nv_mul_f32 v233, v19, v233\nv_cvt_pk_bf16_f32 v16, v230, v231\nM 60,180,212\n"
"v_cvt_pk_bf16_f32 v17, v232, v233\nv_mul_f32 v234, v20, v20\nv_mul_f32 v235, v21, v21\nM 44,176,192\nR 174,189,36864\nv_mul_f32 v236, v22, v22\n"
"v_mul_f32 v237, v23, v23\nM 44,180,208\nG 4\nv_fma_f32 v234, v234, v218, v217\nv_fma_f32 v235, v235, v218, v217\nv_fma_f32 v236, v236, v218, v217\n"
"M 32,160,224\nR 176,190,24576\nv_fma_f32 v237, v237, v218, v217\nv_mul_f32 v234, v20, v234\nM 32,164,240\nbuffer_load_dwordx4 v221, s[16:19], s63 offen offset:1024 lds\n"
"v_mul_f32 v235, v21, v235\nM 48,160,228\nR 178,190,28672\ns_add_u32 s63, s57, 589824\nv_mul_f32 v236, v22, v236\nM 48,164,244\n"
"s_add_u32 m0, s50, 40960\nv_exp_f32 v234, v234\nv_mul_f32 v237, v23, v237\nM 64,160,232\nR 180,190,32768\nv_fma_f32 v234, v234, v219, v219\n"
"v_exp_f32 v235, v235\nM 64,164,248\nbuffer_load_dwordx4 v220, s[16:19], s63 offen lds\nv_exp_f32 v236, v236\nM 80,160,236\nR 182,190,36864\n"
"v_fma_f32 v235, v235, v219, v219\nv_exp_f32 v237, v237\nM 80,164,252\nG 4\nv_rcp_f32 v234, v234\nv_fma_f32 v236, v236, v219, v219\n"
"M 84,168,236\nR 160,191,24576\nv_fma_f32 v237, v237, v219, v219\nv_rcp_f32 v235, v235\nM 84,172,252\nbuffer_load_dwordx4 v221, s[16:19], s63 offen offset:1024 lds\n"
"v_rcp_f32 v236, v236\nM 68,168,232\nR 162,191,28672\ns_add_u32 s63, s56, 131072\nv_rcp_f32 v237, v237\nM 68,172,248\n"
"s_add_u32 m0, s50, 49152\nv_mul_f32 v234, v20, v234\nv_mul_f32 v235, v21, v235\nM 52,168,228\nR 164,191,32768\nv_mul_f32 v236, v22, v236\n"
"M 52,172,244\nbuffer_load_dwordx4 v220, s[16:19], s63 offen lds\nM 36,168,224\nR 166,191,36864\nv_mul_f32 v237, v23, v237\nM 36,172,240\n"
"G 4\nv_cvt_pk_bf16_f32 v18, v234, v235\nv_cvt_pk_bf16_f32 v19, v236, v237\nM 40,176,224\nR 168,188,40960\nbuffer_store_dwordx4 v[16:19], v252, s[20:23], s85 offen offset:0 sc1\n"
"M 40,180,240\nbuffer_load_dwordx4 v221, s[16:19], s63 offen offset:1024 lds\nM 56,176,228\nR 170,188,45056\nv_mov_b32 v248, 0\nM 56,180,244\n"
"v_mov_b32 v249, 0\nM 72,176,232\nR 172,188,49152\ns_add_u32 s61, s82, 385032\ns_mov_b64 exec, 0xf\nbuffer_atomic_or_x2 v[248:249], v250, s[20:23], s61 offen sc0 sc1\n"
"s_mov_b64 exec, -1\nM 72,180,248\nM 88,176,236\nR 174,188,53248\ns_mov_b32 s60, 158208\nv_bfe_u32 v246, v252, 4, 4\n"
"v_lshl_add_u32 v246, v246, 3, s60\nM 88,180,252\nds_read_b64 v[238:239], v246 offset:0\nG 5\nM 92,160,236\nR 176,189,40960\n"
"ds_read_b64 v[240:241], v246 offset:128\nM 92,164,252\nds_read_b64 v[242:243], v246 offset:256\ns_add_u32 m0, s50, 57344\nM 76,160,232\nR 178,189,45056\n"
"ds_read_b64 v[244:245], v246 offset:384\nM 76,164,248\nds_read_b64 v[246:247], v246 offset:0\ns_add_u32 s63, s56, 655360\nM 60,160,228\nR 180,189,49152\n"
"M 60,164,244\nbuffer_load_dwordx4 v220, s[16:19], s63 offen lds\nM 44,160,224\nR 182,189,53248\nM 44,164,240\ns_waitcnt vmcnt(15) lgkmcnt(2)\n"
"s_barrier\nv_mfma_f32_16x16x32_bf16 v[96:99], v[168:171], a[0:3], 0\nR 160,190,40960\nM 96,172,16\ns_mov_b32 m0, s51\nv_mfma_f32_16x16x32_bf16 v[112:115], v[168:171], a[4:7], 0\n"
"R 162,190,45056\nM 112,172,20\nds_write_addtid_b32 v64 offset:0\nv_mfma_f32_16x16x32_bf16 v[128:131], v[168:171], a[8:11], 0\nR 164,190,49152\nv_mfma_f32_16x16x32_bf16 v[32:35], v[238:241], v[208:211], v[32:35]\n"
"ds_write_addtid_b32 v65 offset:256\nds_write_addtid_b32 v66 offset:512\nM 128,172,24\nds_write_addtid_b32 v67 offset:768\nv_mfma_f32_16x16x32_bf16 v[144:147], v[168:171], a[12:15], 0\nR 166,190,53248\n"
"v_mfma_f32_16x16x32_bf16 v[36:39], v[240:243], v[208:211], v[36:39]\nds_write_addtid_b32 v68 offset:1024\nds_write_addtid_b32 v69 offset:1280\nM 144,172,28\nds_write_addtid_b32 v70 offset:1536\nG 11\n"
"v_mfma_f32_16x16x32_bf16 v[148:151], v[176:179], a[12:15], 0\nR 168,191,40960\nv_mfma_f32_16x16x32_bf16 v[40:43], v[242:245], v[208:211], v[40:43]\nds_write_addtid_b32 v71 offset:1792\nds_write_addtid_b32 v72 offset:2048\nM 148,180,28\n"
"ds_write_addtid_b32 v73 offset:2304\nv_mfma_f32_16x16x32_bf16 v[132:135], v[176:179], a[8:11], 0\nR 170,191,45056\nv_mfma_f32_16x16x32_bf16 v[44:47], v[244:247], v[208:211], v[44:47]\nds_write_addtid_b32 v74 offset:2560\nds_write_addtid_b32 v75 offset:2816\n"
"M 132,180,24\nds_write_addtid_b32 v76 offset:3072\nv_mfma_f32_16x16x32_bf16 v[116:119], v[176:179], a[4:7], 0\nR 172,191,49152\nv_mfma_f32_16x16x32_bf16 v[48:51], v[244:247], v[210:213], v[48:51]\nds_write_addtid_b32 v77 offset:3328\n"
"ds_write_addtid_b32 v78 offset:3584\nM 116,180,20\nds_write_addtid_b32 v79 offset:3840\nv_mfma_f32_16x16x32_bf16 v[100:103], v[176:179], a[0:3], 0\nR 174,191,53248\nv_mfma_f32_16x16x32_bf16 v[52:55], v[238:241], v[210:213], v[52:55]\n"
"ds_write_addtid_b32 v80 offset:4096\nds_write_addtid_b32 v81 offset:4352\nM 100,180,16\nds_write_addtid_b32 v82 offset:4608\nG 15\nv_mfma_f32_16x16x32_bf16 v[104:107], v[160:163], a[0:3], 0\n"
"R 176,184,0\nv_mfma_f32_16x16x32_bf16 v[56:59], v[240:243], v[210:213], v[56:59]\nds_write_addtid_b32 v83 offset:4864\nds_write_addtid_b32 v84 offset:5120\nM 104,164,16\nds_write_addtid_b32 v85 offset:5376\n"
"v_mfma_f32_16x16x32_bf16 v[120:123], v[160:163], a[4:7], 0\nR 178,184,4096\nv_mfma_f32_16x16x32_bf16 v[60:63], v[242:245], v[210:213], v[60:63]\nds_write_addtid_b32 v86 offset:5632\nds_write_addtid_b32 v87 offset:5888\nM 120,164,20\n"
"ds_write_addtid_b32 v88 offset:6144\nv_mfma_f32_16x16x32_bf16 v[136:139], v[160:163], a[8:11], 0\nR 180,184,8192\nM 136,164,24\nds_write_addtid_b32 v89 offset:6400\nv_mfma_f32_16x16x32_bf16 v[152:155], v[160:163], a[12:15], 0\n"
"R 182,184,12288\nds_write_addtid_b32 v90 offset:6656\nM 152,164,28\nds_write_addtid_b32 v91 offset:6912\nds_write_addtid_b32 v92 offset:7168\nG 15\n"
"v_mfma_f32_16x16x32_bf16 v[156:159], v[168:171], a[12:15], 0\nR 160,185,0\nds_write_addtid_b32 v93 offset:7424\nM 156,172,28\nds_write_addtid_b32 v94 offset:7680\nds_write_addtid_b32 v95 offset:7936\n"
"v_mfma_f32_16x16x32_bf16 v[140:143], v[168:171], a[8:11], 0\nR 162,185,4096\ns_add_u32 m0, s50, 57344\nM 140,172,24\nbuffer_load_dwordx4 v221, s[16:19], s63 offen offset:1024 lds\nv_mfma_f32_16x16x32_bf16 v[124:127], v[168:171], a[4:7], 0\n"
"R 164,185,8192\ns_waitcnt vmcnt(6) lgkmcnt(2)\ns_barrier\nM 124,172,20\nds_read2st64_b32 v[64:65], v225 offset0:0 offset1:1\nds_read2st64_b32 v[66:67], v225 offset0:2 offset1:3\n"
"v_mfma_f32_16x16x32_bf16 v[108:111], v[168:171], a[0:3], 0\nR 166,185,12288\nds_read2st64_b32 v[68:69], v225 offset0:4 offset1:5\nM 108,172,16\nds_read2st64_b32 v[70:71], v225 offset0:6 offset1:7\nds_read2st64_b32 v[72:73], v225 offset0:8 offset1:9\n"
"M 96,176,32\nR 168,186,0\nds_read2st64_b32 v[74:75], v225 offset0:10 offset1:11\nM 96,180,48\ns_add_u32 s64, s68, 23070720\nds_read2st64_b32 v[76:77], v225 offset0:12 offset1:13\n"
"v_mov_b32 v226, s46\nM 112,176,36\nR 170,186,4096\nds_read2st64_b32 v[78:79], v225 offset0:14 offset1:15\nv_mov_b32 v227, s47\ns_waitcnt vmcnt(2)\n"
"v_cmp_eq_u64 s[86:87], s[46:47], v[248:249]\nM 112,180,52\ns_add_u32 s61, s81, 385048\ns_lshl_b32 s62, s44, 5\ns_add_u32 s61, s61, s62\ns_mov_b64 exec, 1\n"
"buffer_atomic_swap_x2 v[226:227], off, s[20:23], s61 sc1\ns_mov_b64 exec, -1\nds_read2st64_b32 v[80:81], v225 offset0:16 offset1:17\nM 128,176,40\nR 172,186,8192\ns_and_b32 s62, s86, 0xf\n"
"s_cmp_eq_u32 s62, 0xf\ns_cbranch_scc0 Lq412\nLq413:\nds_read2st64_b32 v[82:83], v225 offset0:18 offset1:19\nM 128,180,56\ns_add_u32 s65, s68, 23185408\n"
"ds_read2st64_b32 v[84:85], v225 offset0:20 offset1:21\nds_read2st64_b32 v[86:87], v225 offset0:22 offset1:23\nM 144,176,44\nR 174,186,12288\ns_add_u32 m0, s50, 65536\nds_read2st64_b32 v[88:89], v225 offset0:24 offset1:25\n"
"M 144,180,60\nds_read2st64_b32 v[90:91], v225 offset0:26 offset1:27\nds_read2st64_b32 v[92:93], v225 offset0:28 offset1:29\nG 15\nM 148,160,44\nR 176,187,0\n"
"s_add_u32 s63, s56, 196608\nds_read2st64_b32 v[94:95], v225 offset0:30 offset1:31\nM 148,164,60\nbuffer_load_dwordx4 a[64:67], v252, s[20:23], s64 offen sc1\nv_add_f32 v32, v32, v64\nM 132,160,40\n"
"R 178,187,4096\nv_add_f32 v33, v33, v65\nv_add_f32 v34, v34, v66\nM 132,164,56\ns_add_u32 s66, s69, 23070720\nv_add_f32 v35, v35, v67\n"
"v_add_f32 v36, v36, v68\nv_add_f32 v37, v37, v69\nM 116,160,36\nR 180,187,8192\nbuffer_load_dwordx4 v220, s[16:19], s63 offen lds\nv_add_f32 v38, v38, v70\n"
"M 116,164,52\nbuffer_load_dwordx4 a[68:71], v252, s[20:23], s65 offen sc1\nv_add_f32 v39, v39, v71\nM 100,160,32\nR 182,187,12288\nG 15\n"
"v_add_f32 v40, v40, v72\nv_add_f32 v41, v41, v73\nM 100,164,48\nG 8\nv_add_f32 v42, v42, v74\nv_add_f32 v43, v43, v75\n"
"v_add_f32 v44, v44, v76\nM 104,168,32\nR 160,184,16384\nbuffer_load_dwordx4 v221, s[16:19], s63 offen offset:1024 lds\nv_add_f32 v45, v45, v77\nM 104,172,48\n"
"s_add_u32 s67, s69, 23185408\nv_add_f32 v46, v46, v78\nv_add_f32 v47, v47, v79\nv_add_f32 v48, v48, v80\nM 120,168,36\nR 162,184,20480\n"
"v_add_f32 v49, v49, v81\nv_add_f32 v50, v50, v82\nM 120,172,52\nbuffer_load_dwordx4 a[72:75], v252, s[20:23], s66 offen sc1\nM 136,168,40\nR 164,184,24576\n"
"v_add_f32 v51, v51, v83\nM 136,172,56\nv_add_f32 v52, v52, v84\nv_add_f32 v53, v53, v85\nM 152,168,44\nR 166,184,28672\n"
"v_add_f32 v54, v54, v86\nM 152,172,60\nG 4\nv_add_f32 v55, v55, v87\nv_add_f32 v56, v56, v88\nM 156,176,44\n"
"R 168,185,16384\nv_add_f32 v57, v57, v89\nM 156,180,60\nbuffer_load_dwordx4 a[76:79], v252, s[20:23], s67 offen sc1\nM 140,176,40\nR 170,185,20480\n"
"v_add_f32 v58, v58, v90\nM 140,180,56\ns_add_u32 s64, s68, 23077888\nv_add_f32 v59, v59, v91\nv_add_f32 v60, v60, v92\nM 124,176,36\n"
"R 172,185,24576\nv_add_f32 v61, v61, v93\nM 124,180,52\ns_add_u32 s65, s68, 23192576\nv_add_f32 v62, v62, v94\nv_add_f32 v63, v63, v95\n"
"M 108,176,32\nR 174,185,28672\nv_mul_f32 v230, v32, v32\nv_mul_f32 v231, v33, v33\nM 108,180,48\ns_add_u32 s84, s73, 23072768\n"
"s_add_u32 s85, s73, 23187456\nG 4\nv_mul_f32 v232, v34, v34\nv_mfma_f32_16x16x32_bf16 v[0:3], v[160:163], a[0:3], 0\nR 176,186,16384\nLq577:\n"
"s_lshr_b32 s60, s45, 3\ns_lshl2_add_u32 s60, s60, s40\ns_lshl_b32 s60, s60, 8\nv_lshl_add_u32 v224, v252, 3, s60\nv_add_u32 v224, Lq577+4096-LqPC, v224\nv_min_u32 v224, LqE-LqPC-4, v224\n"
"s_mov_b64 exec, 3\nglobal_load_dword v224, v224, s[98:99]\ns_mov_b64 exec, -1\nv_mul_f32 v233, v35, v35\nM 0,164,16\nbuffer_load_dwordx4 a[80:83], v252, s[20:23], s64 offen sc1\n"
"v_fma_f32 v230, v230, v215, v214\nv_mfma_f32_16x16x32_bf16 v[16:19], v[160:163], a[4:7], 0\nR 178,186,20480\nv_fma_f32 v231, v231, v215, v214\nv_fma_f32 v232, v232, v215, v214\nM 16,164,20\n"
"s_add_u32 s66, s69, 23077888\nv_fma_f32 v233, v233, v215, v214\nv_mul_f32 v230, v32, v230\nv_mul_f32 v231, v33, v231\nv_mfma_f32_16x16x32_bf16 a[224:227], v[160:163], a[8:11], 0\nR 180,186,24576\n"
"v_mul_f32 v232, v34, v232\nv_mul_f32 v233, v35, v233\nv_mfma_f32_16x16x32_bf16 a[224:227], v[164:167], a[24:27], a[224:227]\nbuffer_load_dwordx4 a[84:87], v252, s[20:23], s65 offen sc1\nv_exp_f32 v230, v230\nv_mfma_f32_16x16x32_bf16 a[240:243], v[160:163], a[12:15], 0\n"
"R 182,186,28672\nv_fma_f32 v230, v230, v216, v216\nv_exp_f32 v231, v231\nv_mfma_f32_16x16x32_bf16 a[240:243], v[164:167], a[28:31], a[240:243]\nG 4\nv_exp_f32 v232, v232\n"
"v_exp_f32 v233, v233\nv_mfma_f32_16x16x32_bf16 a[244:247], v[168:171], a[12:15], 0\nR 160,187,16384\ns_add_u32 m0, s50, 73728\nv_fma_f32 v231, v231, v216, v216\nv_mfma_f32_16x16x32_bf16 a[244:247], v[172:175], a[28:31], a[244:247]\n"
"s_add_u32 s67, s69, 23192576\nv_rcp_f32 v230, v230\nv_fma_f32 v232, v232, v216, v216\nv_mfma_f32_16x16x32_bf16 a[228:231], v[168:171], a[8:11], 0\nR 162,187,20480\ns_add_u32 s63, s56, 720896\n"
"v_fma_f32 v233, v233, v216, v216\nv_mfma_f32_16x16x32_bf16 a[228:231], v[172:175], a[24:27], a[228:231]\nbuffer_load_dwordx4 a[88:91], v252, s[20:23], s66 offen sc1\nv_rcp_f32 v231, v231\nv_mfma_f32_16x16x32_bf16 v[20:23], v[168:171], a[4:7], 0\nR 164,187,24576\n"
"v_mul_f32 v230, v32, v230\nv_rcp_f32 v232, v232\nM 20,172,20\nLpg13:\ns_cbranch_execz LretO0n\nbuffer_load_dwordx4 v220, s[16:19], s63 offen lds\n"
"v_rcp_f32 v233, v233\nv_mfma_f32_16x16x32_bf16 v[4:7], v[168:171], a[0:3], 0\nR 166,187,28672\nv_mul_f32 v231, v33, v231\nv_mul_f32 v232, v34, v232\nM 4,172,16\n"
"G 4\nv_mul_f32 v233, v35, v233\nv_cvt_pk_bf16_f32 v32, v230, v231\nv_cvt_pk_bf16_f32 v33, v232, v233\nv_mfma_f32_16x16x32_bf16 v[8:11], v[176:179], a[0:3], 0\nR 168,184,32768\n"
"v_mul_f32 v234, v36, v36\nv_mul_f32 v235, v37, v37\nM 8,180,16\nbuffer_load_dwordx4 a[92:95], v252, s[20:23], s67 offen sc1\nbuffer_load_dwordx4 v221, s[16:19], s63 offen offset:1024 lds\nv_mul_f32 v236, v38, v38\n"
"v_mfma_f32_16x16x32_bf16 v[24:27], v[176:179], a[4:7], 0\nR 170,184,36864\ns_add_u32 m0, s50, 81920\nv_mul_f32 v237, v39, v39\nM 24,180,20\ns_add_u32 s64, s68, 23071744\n"
"v_fma_f32 v234, v234, v215, v214\nv_fma_f32 v235, v235, v215, v214\nv_fma_f32 v236, v236, v215, v214\nv_mfma_f32_16x16x32_bf16 a[232:235], v[176:179], a[8:11], 0\nR 172,184,40960\ns_add_u32 s63, s57, 131072\n"
"v_fma_f32 v237, v237, v215, v214\nv_mfma_f32_16x16x32_bf16 a[232:235], v[180:183], a[24:27], a[232:235]\ns_add_u32 s65, s68, 23186432\nbuffer_load_dwordx4 v220, s[16:19], s63 offen lds\nv_mul_f32 v234, v36, v234\nv_mfma_f32_16x16x32_bf16 a[248:251], v[176:179], a[12:15], 0\n"
"R 174,184,45056\nv_mul_f32 v235, v37, v235\nv_mul_f32 v236, v38, v236\nv_mfma_f32_16x16x32_bf16 a[248:251], v[180:183], a[28:31], a[248:251]\nG 4\nv_exp_f32 v234, v234\n"
"v_mul_f32 v237, v39, v237\nv_mfma_f32_16x16x32_bf16 a[252:255], v[160:163], a[12:15], 0\nR 176,185,32768\nv_fma_f32 v234, v234, v216, v216\nv_exp_f32 v235, v235\nv_mfma_f32_16x16x32_bf16 a[252:255], v[164:167], a[28:31], a[252:255]\n"
"buffer_load_dwordx4 a[96:99], v252, s[20:23], s64 offen sc1\nbuffer_load_dwordx4 v221, s[16:19], s63 offen offset:1024 lds\nv_exp_f32 v236, v236\nv_mfma_f32_16x16x32_bf16 a[236:239], v[160:163], a[8:11], 0\nR 178,185,36864\ns_add_u32 m0, s50, 90112\n"
"v_exp_f32 v237, v237\nv_mfma_f32_16x16x32_bf16 a[236:239], v[164:167], a[24:27], a[236:239]\ns_add_u32 s66, s69, 23071744\nv_rcp_f32 v234, v234\nv_fma_f32 v235, v235, v216, v216\nv_mfma_f32_16x16x32_bf16 v[28:31], v[160:163], a[4:7], 0\n"
"R 180,185,40960\ns_add_u32 s63, s57, 655360\nv_fma_f32 v236, v236, v216, v216\nM 28,164,20\nbuffer_load_dwordx4 a[100:103], v252, s[20:23], s65 offen sc1\nbuffer_load_dwordx4 v220, s[16:19], s63 offen lds\n"
"v_fma_f32 v237, v237, v216, v216\nv_mfma_f32_16x16x32_bf16 v[12:15], v[160:163], a[0:3], 0\nR 182,185,45056\nv_mul_f32 v234, v36, v234\nv_rcp_f32 v235, v235\nM 12,164,16\n"
"s_waitcnt vmcnt(14)\ns_barrier\nG 4\nv_rcp_f32 v236, v236\nM 0,168,32\nR 160,186,32768\n"
"v_mul_f32 v235, v37, v235\nv_rcp_f32 v237, v237\nM 0,172,48\ns_add_u32 s67, s69, 23186432\nbuffer_load_dwordx4 v221, s[16:19], s63 offen offset:1024 lds\nv_mul_f32 v236, v38, v236\n"
"M 16,168,36\nR 162,186,36864\nv_mul_f32 v237, v39, v237\nv_cvt_pk_bf16_f32 v34, v234, v235\nM 16,172,52\nbuffer_load_dwordx4 a[104:107], v252, s[20:23], s66 offen sc1\n"
"v_cvt_pk_bf16_f32 v35, v236, v237\nv_mfma_f32_16x16x32_bf16 a[224:227], v[168:171], a[40:43], a[224:227]\nR 164,186,40960\nv_mul_f32 v230, v40, v40\nv_mul_f32 v231, v41, v41\nv_mfma_f32_16x16x32_bf16 a[224:227], v[172:175], a[56:59], a[224:227]\n"
"v_mul_f32 v232, v42, v42\nv_mul_f32 v233, v43, v43\nv_fma_f32 v230, v230, v215, v214\nv_mfma_f32_16x16x32_bf16 a[240:243], v[168:171], a[44:47], a[240:243]\nR 166,186,45056\nv_fma_f32 v231, v231, v215, v214\n"
"v_fma_f32 v232, v232, v215, v214\nv_mfma_f32_16x16x32_bf16 a[240:243], v[172:175], a[60:63], a[240:243]\nG 4\nv_fma_f32 v233, v233, v215, v214\nv_mul_f32 v230, v40, v230\nv_mul_f32 v231, v41, v231\n"
"v_mfma_f32_16x16x32_bf16 a[244:247], v[176:179], a[44:47], a[244:247]\nR 168,187,32768\nv_mul_f32 v232, v42, v232\nv_mul_f32 v233, v43, v233\nv_mfma_f32_16x16x32_bf16 a[244:247], v[180:183], a[60:63], a[244:247]\nbuffer_load_dwordx4 a[108:111], v252, s[20:23], s67 offen sc1\n"
"v_exp_f32 v230, v230\nv_mfma_f32_16x16x32_bf16 a[228:231], v[176:179], a[40:43], a[228:231]\nR 170,187,36864\nv_fma_f32 v230, v230, v216, v216\nv_exp_f32 v231, v231\nv_mfma_f32_16x16x32_bf16 a[228:231], v[180:183], a[56:59], a[228:231]\n"
"s_add_u32 s64, s68, 23078912\nv_exp_f32 v232, v232\nv_exp_f32 v233, v233\nM 20,176,36\nR 172,187,40960\nv_fma_f32 v231, v231, v216, v216\n"
"v_fma_f32 v232, v232, v216, v216\nM 20,180,52\ns_add_u32 s65, s68, 23193600\nv_rcp_f32 v230, v230\nv_fma_f32 v233, v233, v216, v216\nM 4,176,32\n"
"R 174,187,45056\ns_add_u32 m0, s50, 98304\nv_rcp_f32 v231, v231\nM 4,180,48\nG 4\nv_rcp_f32 v232, v232\n"
"v_rcp_f32 v233, v233\nM 8,160,32\nR 176,184,49152\ns_add_u32 s63, s57, 196608\nv_mul_f32 v230, v40, v230\nM 8,164,48\n"
"buffer_load_dwordx4 a[112:115], v252, s[20:23], s64 offen sc1\nv_mul_f32 v231, v41, v231\nM 24,160,36\nR 178,184,53248\nv_mul_f32 v232, v42, v232\nv_mul_f32 v233, v43, v233\n"
"M 24,164,52\ns_add_u32 s66, s69, 23078912\nbuffer_load_dwordx4 v220, s[16:19], s63 offen lds\nv_cvt_pk_bf16_f32 v40, v230, v231\nv_mfma_f32_16x16x32_bf16 a[232:235], v[160:163], a[40:43], a[232:235]\nR 180,184,57344\n"
"v_cvt_pk_bf16_f32 v41, v232, v233\nv_mul_f32 v234, v44, v44\nv_mfma_f32_16x16x32_bf16 a[232:235], v[164:167], a[56:59], a[232:235]\nbuffer_load_dwordx4 a[116:119], v252, s[20:23], s65 offen sc1\nv_mul_f32 v235, v45, v45\nv_mfma_f32_16x16x32_bf16 a[248:251], v[160:163], a[44:47], a[248:251]\n"
"R 182,184,61440\nv_mul_f32 v236, v46, v46\nv_mul_f32 v237, v47, v47\nv_mfma_f32_16x16x32_bf16 a[248:251], v[164:167], a[60:63], a[248:251]\nbuffer_load_dwordx4 v221, s[16:19], s63 offen offset:1024 lds\nG 4\n"
"v_fma_f32 v234, v234, v215, v214\nv_mfma_f32_16x16x32_bf16 a[252:255], v[168:171], a[44:47], a[252:255]\nR 160,185,49152\ns_add_u32 m0, s50, 106496\nv_fma_f32 v235, v235, v215, v214\nv_mfma_f32_16x16x32_bf16 a[252:255], v[172:175], a[60:63], a[252:255]\n"
"s_add_u32 s67, s69, 23193600\ns_add_u32 s63, s57, 720896\nv_fma_f32 v236, v236, v215, v214\nv_fma_f32 v237, v237, v215, v214\nv_mfma_f32_16x16x32_bf16 a[236:239], v[168:171], a[40:43], a[236:239]\nR 162,185,53248\n"
"v_mov_b32 v248, 0\nv_mul_f32 v234, v44, v234\nv_mfma_f32_16x16x32_bf16 a[236:239], v[172:175], a[56:59], a[236:239]\nbuffer_load_dwordx4 a[120:123], v252, s[20:23], s66 offen sc1\nbuffer_load_dwordx4 v220, s[16:19], s63 offen lds\nv_mov_b32 v249, 0\n"
"v_mul_f32 v235, v45, v235\nM 28,168,36\nR 164,185,57344\ns_add_u32 s61, s82, 385048\ns_mov_b64 exec, 0xf\nbuffer_atomic_or_x2 v[248:249], v250, s[20:23], s61 offen sc0 sc1\n"
"s_mov_b64 exec, -1\nv_mul_f32 v236, v46, v236\nM 28,172,52\nbuffer_load_dwordx4 v221, s[16:19], s63 offen offset:1024 lds\nv_mul_f32 v237, v47, v237\nM 12,168,32\n"
"R 166,185,61440\ns_add_u32 m0, s50, 114688\nv_exp_f32 v234, v234\nM 12,172,48\nG 4\nv_exp_f32 v235, v235\n"
"v_exp_f32 v236, v236\nM 96,176,64\nR 168,186,49152\ns_add_u32 s63, s58, 0\nv_exp_f32 v237, v237\ns_waitcnt vmcnt(21)\n"
"M 96,180,80\nbuffer_load_dwordx4 a[124:127], v252, s[20:23], s67 offen sc1\nv_fma_f32 v234, v234, v216, v216\nM 112,176,68\nR 170,186,53248\nv_fma_f32 v235, v235, v216, v216\n"
"v_fma_f32 v236, v236, v216, v216\ns_waitcnt vmcnt(21)\nM 112,180,84\nbuffer_load_dwordx4 v220, s[16:19], s63 offen lds\nv_fma_f32 v237, v237, v216, v216\nM 128,176,72\n"
"R 172,186,57344\nv_mul_f32 v230, v48, v48\nv_rcp_f32 v234, v234\ns_waitcnt vmcnt(21)\nM 128,180,88\nbuffer_load_dwordx4 v221, s[16:19], s63 offen offset:1024 lds\n"
"v_rcp_f32 v235, v235\nM 144,176,76\nR 174,186,61440\ns_add_u32 m0, s50, 0\nv_rcp_f32 v236, v236\ns_waitcnt vmcnt(20)\n"
"M 144,180,92\nG 4\nv_rcp_f32 v237, v237\nv_mul_f32 v234, v44, v234\nM 148,160,76\nR 176,187,49152\n"
"s_add_u32 s63, s58, 524288\nv_mul_f32 v235, v45, v235\nM 148,164,92\nbuffer_load_dwordx4 v220, s[16:19], s63 offen lds\nv_mul_f32 v236, v46, v236\nM 132,160,72\n"
"R 178,187,53248\nv_mul_f32 v237, v47, v237\nv_cvt_pk_bf16_f32 v42, v234, v235\nM 132,164,88\nbuffer_store_dwordx4 v[32:35], v252, s[20:23], s84 offen offset:0 sc1\nv_cvt_pk_bf16_f32 v43, v236, v237\n"
"M 116,160,68\nR 180,187,57344\nv_mul_f32 v231, v49, v49\nv_mul_f32 v232, v50, v50\nM 116,164,84\nbuffer_load_dwordx4 v221, s[16:19], s63 offen offset:1024 lds\n"
"v_mul_f32 v233, v51, v51\nM 100,160,64\nR 182,187,61440\nv_fma_f32 v230, v230, v218, v217\nv_fma_f32 v231, v231, v218, v217\nM 100,164,80\n"
"G 4\nv_fma_f32 v232, v232, v218, v217\nv_fma_f32 v233, v233, v218, v217\nv_mul_f32 v230, v48, v230\nM 104,168,64\nR 160,188,0\n"
"s_waitcnt vmcnt(16)\ns_barrier\nv_mul_f32 v231, v49, v231\nM 104,172,80\nbuffer_store_dwordx4 v[40:43], v252, s[20:23], s84 offen offset:1024 sc1\nv_mul_f32 v232, v50, v232\n"
"M 120,168,68\nR 162,188,4096\nv_mul_f32 v233, v51, v233\nv_exp_f32 v230, v230\nM 120,172,84\ns_add_u32 m0, s50, 8192\n"
"v_exp_f32 v231, v231\nv_exp_f32 v232, v232\nM 136,168,72\nR 164,188,8192\nv_fma_f32 v230, v230, v219, v219\nv_exp_f32 v233, v233\n"
"M 136,172,88\ns_add_u32 s63, s58, 65536\nv_rcp_f32 v230, v230\nv_fma_f32 v231, v231, v219, v219\nM 152,168,76\nR 166,188,12288\n"
"v_fma_f32 v232, v232, v219, v219\nv_fma_f32 v233, v233, v219, v219\nM 152,172,92\nG 4\nv_rcp_f32 v231, v231\nv_rcp_f32 v232, v232\n"
"M 156,176,76\nR 168,189,0\nv_mul_f32 v230, v48, v230\nv_rcp_f32 v233, v233\nM 156,180,92\nbuffer_load_dwordx4 v220, s[16:19], s63 offen lds\n"
"v_mul_f32 v231, v49, v231\nM 140,176,72\nR 170,189,4096\nv_mul_f32 v232, v50, v232\nv_mul_f32 v233, v51, v233\nM 140,180,88\n"
"v_cvt_pk_bf16_f32 v48, v230, v231\nv_cvt_pk_bf16_f32 v49, v232, v233\nv_mul_f32 v234, v52, v52\nM 124,176,68\nR 172,189,8192\nv_mul_f32 v235, v53, v53\n"
"v_mul_f32 v236, v54, v54\nM 124,180,84\nbuffer_load_dwordx4 v221, s[16:19], s63 offen offset:1024 lds\nv_mul_f32 v237, v55, v55\nM 108,176,64\nR 174,189,12288\n"
"s_add_u32 m0, s50, 16384\nv_fma_f32 v234, v234, v218, v217\nM 108,180,80\nG 4\nv_fma_f32 v235, v235, v218, v217\nv_fma_f32 v236, v236, v218, v217\n"
"v_fma_f32 v237, v237, v218, v217\nM 96,160,96\nR 176,190,0\ns_add_u32 s63, s58, 589824\nv_mul_f32 v234, v52, v234\ns_waitcnt vmcnt(16)\n"
"M 96,164,112\nbuffer_load_dwordx4 v220, s[16:19], s63 offen lds\nv_mul_f32 v235, v53, v235\nM 112,160,100\nR 178,190,4096\nv_mul_f32 v236, v54, v236\n"
"v_mul_f32 v237, v55, v237\ns_waitcnt vmcnt(15)\nM 112,164,116\nv_exp_f32 v234, v234\nv_exp_f32 v235, v235\nM 128,160,104\n"
"R 180,190,8192\nv_fma_f32 v234, v234, v219, v219\nv_exp_f32 v236, v236\ns_waitcnt vmcnt(13)\nM 128,164,120\nv_exp_f32 v237, v237\n"
"v_fma_f32 v235, v235, v219, v219\nM 144,160,108\nR 182,190,12288\nv_fma_f32 v236, v236, v219, v219\nv_fma_f32 v237, v237, v219, v219\ns_waitcnt vmcnt(9)\n"
"M 144,164,124\nG 4\nv_rcp_f32 v234, v234\nv_rcp_f32 v235, v235\nM 148,168,108\nR 160,191,0\n"
"v_mul_f32 v234, v52, v234\nv_rcp_f32 v236, v236\nM 148,172,124\ns_add_u32 s64, s68, 23074816\nv_rcp_f32 v237, v237\nv_mul_f32 v235, v53, v235\n"
"M 132,168,104\nR 162,191,4096\nv_cmp_eq_u64 s[86:87], s[46:47], v[248:249]\nv_mul_f32 v236, v54, v236\nM 132,172,120\nbuffer_load_dwordx4 v221, s[16:19], s63 offen offset:1024 lds\n"
"v_mul_f32 v237, v55, v237\nM 116,168,100\nR 164,191,8192\ns_and_b32 s62, s86, 0xf\ns_cmp_eq_u32 s62, 0xf\ns_cbranch_scc0 Lq447\n"
"Lq448:\nM 116,172,116\ns_add_u32 s65, s68, 23189504\nv_cvt_pk_bf16_f32 v50, v234, v235\nv_cvt_pk_bf16_f32 v51, v236, v237\nM 100,168,96\n"
"R 166,191,12288\nv_mul_f32 v230, v56, v56\nM 100,172,112\nG 4\nv_mul_f32 v231, v57, v57\nv_mul_f32 v232, v58, v58\n"
"M 104,176,96\nR 168,188,16384\nv_mul_f32 v233, v59, v59\nM 104,180,112\nbuffer_load_dwordx4 a[128:131], v252, s[20:23], s64 offen sc1\nM 120,176,100\n"
"R 170,188,20480\nv_fma_f32 v230, v230, v218, v217\nM 120,180,116\ns_add_u32 s66, s69, 23074816\nv_fma_f32 v231, v231, v218, v217\nv_fma_f32 v232, v232, v218, v217\n"
"M 136,176,104\nR 172,188,24576\nv_fma_f32 v233, v233, v218, v217\nM 136,180,120\nbuffer_load_dwordx4 a[132:135], v252, s[20:23], s65 offen sc1\nM 152,176,108\n"
"R 174,188,28672\nv_mul_f32 v230, v56, v230\nM 152,180,124\nG 4\nv_exp_f32 v230, v230\nM 156,160,108\n"
"R 176,189,16384\nv_mul_f32 v231, v57, v231\nM 156,164,124\ns_add_u32 s67, s69, 23189504\nv_exp_f32 v231, v231\nM 140,160,104\n"
"R 178,189,20480\nv_mul_f32 v232, v58, v232\nM 140,164,120\nbuffer_load_dwordx4 a[136:139], v252, s[20:23], s66 offen sc1\nM 124,160,100\nR 180,189,24576\n"
"v_mul_f32 v233, v59, v233\nM 124,164,116\nbuffer_store_dwordx4 v[48:51], v252, s[20:23], s85 offen offset:0 sc1\nM 108,160,96\nR 182,189,28672\ns_add_u32 m0, s50, 24576\n"
"M 108,164,112\ns_waitcnt vmcnt(9)\ns_barrier\nG 4\nM 0,168,64\nR 160,190,16384\n"
"s_add_u32 s63, s58, 131072\nM 0,172,80\nbuffer_load_dwordx4 a[140:143], v252, s[20:23], s67 offen sc1\nM 16,168,68\nR 162,190,20480\nv_fma_f32 v230, v230, v219, v219\n"
"M 16,172,84\ns_add_u32 s64, s68, 23081984\nv_exp_f32 v232, v232\nv_mfma_f32_16x16x32_bf16 a[224:227], v[168:171], a[72:75], a[224:227]\nR 164,190,24576\nbuffer_load_dwordx4 v220, s[16:19], s63 offen lds\n"
"v_mfma_f32_16x16x32_bf16 a[224:227], v[172:175], a[88:91], a[224:227]\ns_add_u32 s65, s68, 23196672\nv_exp_f32 v233, v233\nv_mfma_f32_16x16x32_bf16 a[240:243], v[168:171], a[76:79], a[240:243]\nR 166,190,28672\nv_fma_f32 v231, v231, v219, v219\n"
"v_mfma_f32_16x16x32_bf16 a[240:243], v[172:175], a[92:95], a[240:243]\nG 4\nv_rcp_f32 v230, v230\nv_mfma_f32_16x16x32_bf16 a[244:247], v[176:179], a[76:79], a[244:247]\nR 168,191,16384\nbuffer_load_dwordx4 v221, s[16:19], s63 offen offset:1024 lds\n"
"v_mfma_f32_16x16x32_bf16 a[244:247], v[180:183], a[92:95], a[244:247]\nbuffer_load_dwordx4 a[144:147], v252, s[20:23], s64 offen sc1\nv_mfma_f32_16x16x32_bf16 a[228:231], v[176:179], a[72:75], a[228:231]\nR 170,191,20480\ns_add_u32 m0, s50, 32768\nv_mfma_f32_16x16x32_bf16 a[228:231], v[180:183], a[88:91], a[228:231]\n"
"s_add_u32 s66, s69, 23081984\ns_add_u32 s63, s58, 655360\nv_fma_f32 v232, v232, v219, v219\nM 20,176,68\nR 172,191,24576\nbuffer_load_dwordx4 v220, s[16:19], s63 offen lds\n"
"M 20,180,84\nbuffer_load_dwordx4 a[148:151], v252, s[20:23], s65 offen sc1\nM 4,176,64\nR 174,191,28672\nv_fma_f32 v233, v233, v219, v219\nM 4,180,80\n"
"G 4\nv_rcp_f32 v231, v231\nM 8,160,64\nR 176,188,32768\nbuffer_load_dwordx4 v221, s[16:19], s63 offen offset:1024 lds\nM 8,164,80\n"
"s_add_u32 s67, s69, 23196672\nv_rcp_f32 v232, v232\nM 24,160,68\nR 178,188,36864\nv_mul_f32 v230, v56, v230\nM 24,164,84\n"
"buffer_load_dwordx4 a[152:155], v252, s[20:23], s66 offen sc1\nv_mfma_f32_16x16x32_bf16 a[232:235], v[160:163], a[72:75], a[232:235]\nR 180,188,40960\nv_mul_f32 v231, v57, v231\nv_mfma_f32_16x16x32_bf16 a[232:235], v[164:167], a[88:91], a[232:235]\nv_rcp_f32 v233, v233\n"
"v_mfma_f32_16x16x32_bf16 a[248:251], v[160:163], a[76:79], a[248:251]\nR 182,188,45056\nv_mul_f32 v232, v58, v232\nv_mfma_f32_16x16x32_bf16 a[248:251], v[164:167], a[92:95], a[248:251]\nG 4\nv_mul_f32 v233, v59, v233\n"
"v_cvt_pk_bf16_f32 v56, v230, v231\nv_mfma_f32_16x16x32_bf16 a[252:255], v[168:171], a[76:79], a[252:255]\nR 160,189,32768\nv_cvt_pk_bf16_f32 v57, v232, v233\nv_mfma_f32_16x16x32_bf16 a[252:255], v[172:175], a[92:95], a[252:255]\nbuffer_load_dwordx4 a[156:159], v252, s[20:23], s67 offen sc1\n"
"v_mfma_f32_16x16x32_bf16 a[236:239], v[168:171], a[72:75], a[236:239]\nR 162,189,36864\nv_mul_f32 v234, v60, v60\nv_mfma_f32_16x16x32_bf16 a[236:239], v[172:175], a[88:91], a[236:239]\nv_mul_f32 v235, v61, v61\nv_mul_f32 v236, v62, v62\n"
"M 28,168,68\nR 164,189,40960\nv_mul_f32 v237, v63, v63\nM 28,172,84\ns_add_u32 m0, s50, 40960\nv_fma_f32 v234, v234, v218, v217\n"
"v_fma_f32 v235, v235, v218, v217\nM 12,168,64\nR 166,189,45056\nv_fma_f32 v236, v236, v218, v217\nM 12,172,80\nG 4\n"
"v_fma_f32 v237, v237, v218, v217\nv_mul_f32 v234, v60, v234\nM 0,176,96\nR 168,190,32768\nv_mul_f32 v235, v61, v235\nM 0,180,112\n"
"s_add_u32 s63, s58, 196608\nv_exp_f32 v234, v234\nM 16,176,100\nR 170,190,36864\nv_mul_f32 v236, v62, v236\nM 16,180,116\n"
"buffer_load_dwordx4 v220, s[16:19], s63 offen lds\nv_mfma_f32_16x16x32_bf16 a[224:227], v[176:179], a[104:107], a[224:227]\nR 172,190,40960\nv_mul_f32 v237, v63, v237\nv_mfma_f32_16x16x32_bf16 a[224:227], v[180:183], a[120:123], a[224:227]\nv_exp_f32 v235, v235\n"
"v_mfma_f32_16x16x32_bf16 a[240:243], v[176:179], a[108:111], a[240:243]\nR 174,190,45056\nv_fma_f32 v234, v234, v219, v219\nv_mfma_f32_16x16x32_bf16 a[240:243], v[180:183], a[124:127], a[240:243]\nG 4\nv_exp_f32 v236, v236\n"
"v_mfma_f32_16x16x32_bf16 a[244:247], v[160:163], a[108:111], a[244:247]\nR 176,191,32768\nv_fma_f32 v235, v235, v219, v219\nv_mfma_f32_16x16x32_bf16 a[244:247], v[164:167], a[124:127], a[244:247]\nbuffer_load_dwordx4 v221, s[16:19], s63 offen offset:1024 lds\nv_mfma_f32_16x16x32_bf16 a[228:231], v[160:163], a[104:107], a[228:231]\n"
"R 178,191,36864\ns_add_u32 s63, s58, 720896\nv_mfma_f32_16x16x32_bf16 a[228:231], v[164:167], a[120:123], a[228:231]\ns_add_u32 m0, s50, 49152\nv_exp_f32 v237, v237\nM 20,160,100\n"
"R 180,191,40960\nv_fma_f32 v236, v236, v219, v219\nM 20,164,116\nbuffer_load_dwordx4 v220, s[16:19], s63 offen lds\nM 4,160,96\nR 182,191,45056\n"
"v_fma_f32 v237, v237, v219, v219\nM 4,164,112\nG 4\nv_rcp_f32 v234, v234\nM 8,168,96\nR 160,188,49152\n"
"v_mul_f32 v234, v60, v234\nM 8,172,112\nbuffer_load_dwordx4 v221, s[16:19], s63 offen offset:1024 lds\nM 24,168,100\nR 162,188,53248\ns_add_u32 s63, s58, 393216\n"
"M 24,172,116\ns_add_u32 m0, s50, 57344\nv_rcp_f32 v235, v235\nv_mfma_f32_16x16x32_bf16 a[232:235], v[168:171], a[104:107], a[232:235]\nR 164,184,0\nv_mul_f32 v235, v61, v235\n"
"v_mfma_f32_16x16x32_bf16 a[232:235], v[172:175], a[120:123], a[232:235]\nbuffer_load_dwordx4 v220, s[16:19], s63 offen lds\nv_mfma_f32_16x16x32_bf16 a[248:251], v[168:171], a[108:111], a[248:251]\nR 166,184,4096\nv_cvt_pk_bf16_f32 v58, v234, v235\nv_mfma_f32_16x16x32_bf16 a[248:251], v[172:175], a[124:127], a[248:251]\n"
"G 4\nv_rcp_f32 v236, v236\nv_mfma_f32_16x16x32_bf16 a[252:255], v[176:179], a[108:111], a[252:255]\nR 168,189,49152\nv_mul_f32 v236, v62, v236\nLpg14:\n"
"s_cbranch_execz LretO0n\nv_mfma_f32_16x16x32_bf16 a[252:255], v[180:183], a[124:127], a[252:255]\nbuffer_load_dwordx4 v221, s[16:19], s63 offen offset:1024 lds\nv_mfma_f32_16x16x32_bf16 a[236:239], v[176:179], a[104:107], a[236:239]\nR 170,189,53248\nv_rcp_f32 v237, v237\n"
"v_mfma_f32_16x16x32_bf16 a[236:239], v[180:183], a[120:123], a[236:239]\ns_add_u32 m0, s50, 65536\nv_mul_f32 v237, v63, v237\nv_cvt_pk_bf16_f32 v59, v236, v237\nM 28,176,100\nR 172,185,0\n"
"M 28,180,116\ns_add_u32 s63, s58, 458752\nbuffer_store_dwordx4 v[56:59], v252, s[20:23], s85 offen offset:1024 sc1\nM 12,176,96\nR 174,185,4096\nM 12,180,112\n"
"s_waitcnt vmcnt(9)\ns_barrier\nG 4\nv_mfma_f32_16x16x32_bf16 v[32:35], v[160:163], a[0:3], 0\nR 176,190,49152\nM 32,164,16\n"
"buffer_load_dwordx4 v220, s[16:19], s63 offen lds\nv_mfma_f32_16x16x32_bf16 v[48:51], v[160:163], a[4:7], 0\nR 178,190,53248\nM 48,164,20\nv_mfma_f32_16x16x32_bf16 v[64:67], v[160:163], a[8:11], 0\nR 180,186,0\n"
"M 64,164,24\nv_mfma_f32_16x16x32_bf16 v[80:83], v[160:163], a[12:15], 0\nR 182,186,4096\nM 80,164,28\nG 4\nv_mfma_f32_16x16x32_bf16 v[84:87], v[168:171], a[12:15], 0\n"
"R 160,191,49152\nM 84,172,28\nbuffer_load_dwordx4 v221, s[16:19], s63 offen offset:1024 lds\nv_mfma_f32_16x16x32_bf16 v[68:71], v[168:171], a[8:11], 0\nR 162,191,53248\nM 68,172,24\n"
"s_add_u32 m0, s50, 73728\nv_mfma_f32_16x16x32_bf16 v[52:55], v[168:171], a[4:7], 0\nR 164,187,0\nM 52,172,20\ns_add_u32 s63, s56, 393216\nv_mfma_f32_16x16x32_bf16 v[36:39], v[168:171], a[0:3], 0\n"
"R 166,187,4096\nM 36,172,16\nG 4\nv_mfma_f32_16x16x32_bf16 v[40:43], v[176:179], a[0:3], 0\nR 168,184,8192\nM 40,180,16\n"
"buffer_load_dwordx4 v220, s[16:19], s63 offen lds\nv_mfma_f32_16x16x32_bf16 v[56:59], v[176:179], a[4:7], 0\nR 170,184,12288\nM 56,180,20\nv_mfma_f32_16x16x32_bf16 v[72:75], v[176:179], a[8:11], 0\nR 172,184,16384\n"
"M 72,180,24\nv_mfma_f32_16x16x32_bf16 v[88:91], v[176:179], a[12:15], 0\nR 174,184,20480\nM 88,180,28\nG 4\nv_mfma_f32_16x16x32_bf16 v[92:95], v[160:163], a[12:15], 0\n"
"R 176,185,8192\nM 92,164,28\nbuffer_load_dwordx4 v221, s[16:19], s63 offen offset:1024 lds\nv_mfma_f32_16x16x32_bf16 v[76:79], v[160:163], a[8:11], 0\nR 178,185,12288\nM 76,164,24\n"
"s_add_u32 m0, s50, 81920\nv_mfma_f32_16x16x32_bf16 v[60:63], v[160:163], a[4:7], 0\nR 180,185,16384\nM 60,164,20\ns_add_u32 s63, s56, 458752\nv_mfma_f32_16x16x32_bf16 v[44:47], v[160:163], a[0:3], 0\n"
"R 182,185,20480\nM 44,164,16\nG 4\nM 32,168,32\nR 160,186,8192\nM 32,172,48\n"
"buffer_load_dwordx4 v220, s[16:19], s63 offen lds\nM 48,168,36\nR 162,186,12288\nM 48,172,52\nM 64,168,40\nR 164,186,16384\n"
"M 64,172,56\nbuffer_load_dwordx4 v221, s[16:19], s63 offen offset:1024 lds\nM 80,168,44\nR 166,186,20480\ns_add_u32 m0, s50, 90112\nM 80,172,60\n"
"G 4\nM 84,176,44\nR 168,187,8192\ns_add_u32 s63, s57, 393216\nM 84,180,60\nbuffer_load_dwordx4 v220, s[16:19], s63 offen lds\n"
"M 68,176,40\nR 170,187,12288\nv_mov_b32 v226, s46\nM 68,180,56\nv_mov_b32 v227, s47\nM 52,176,36\n"
"R 172,187,16384\ns_add_u32 s61, s81, 385040\ns_lshl_b32 s62, s44, 5\ns_add_u32 s61, s61, s62\ns_waitcnt vmcnt(7)\ns_mov_b64 exec, 1\n"
"buffer_atomic_swap_x2 v[226:227], off, s[20:23], s61 sc1\ns_mov_b64 exec, -1\nM 52,180,52\nM 36,176,32\nR 174,187,20480\nM 36,180,48\n"
"G 4\nM 40,160,32\nR 176,184,24576\nM 40,164,48\nbuffer_load_dwordx4 v221, s[16:19], s63 offen offset:1024 lds\nM 56,160,36\n"
"R 178,184,28672\nM 56,164,52\ns_add_u32 m0, s50, 98304\nM 72,160,40\nR 180,184,32768\nM 72,164,56\n"
"s_add_u32 s63, s57, 458752\nM 88,160,44\nR 182,184,36864\nM 88,164,60\nG 4\nM 92,168,44\n"
"R 160,185,24576\nM 92,172,60\nbuffer_load_dwordx4 v220, s[16:19], s63 offen lds\nM 76,168,40\nR 162,185,28672\nM 76,172,56\n"
"M 60,168,36\nR 164,185,32768\nM 60,172,52\nbuffer_load_dwordx4 v221, s[16:19], s63 offen offset:1024 lds\nM 44,168,32\nR 166,185,36864\n"
"s_add_u32 s63, s56, 262144\nM 44,172,48\ns_waitcnt vmcnt(9)\ns_barrier\nG 4\nM 32,176,64\n"
"R 168,186,24576\nM 32,180,80\ns_add_u32 m0, s50, 106496\nM 48,176,68\nR 170,186,28672\nM 48,180,84\n"
"buffer_load_dwordx4 v220, s[16:19], s63 offen lds\nM 64,176,72\nR 172,186,32768\nM 64,180,88\nM 80,176,76\nR 174,186,36864\n"
"M 80,180,92\nG 4\nM 84,160,76\nR 176,187,24576\nM 84,164,92\nbuffer_load_dwordx4 v221, s[16:19], s63 offen offset:1024 lds\n"
"M 68,160,72\nR 178,187,28672\nM 68,164,88\ns_add_u32 m0, s50, 114688\nM 52,160,68\nR 180,187,32768\n"
"v_mov_b32 v248, 0\nM 52,164,84\ns_add_u32 s63, s56, 786432\nv_mov_b32 v249, 0\nM 36,160,64\nR 182,187,36864\n"
"s_add_u32 s61, s82, 385040\ns_mov_b64 exec, 0xf\nbuffer_atomic_or_x2 v[248:249], v250, s[20:23], s61 offen sc0 sc1\ns_mov_b64 exec, -1\nM 36,164,80\nG 4\n"
"M 40,168,64\nR 160,184,40960\nM 40,172,80\nbuffer_load_dwordx4 v220, s[16:19], s63 offen lds\nM 56,168,68\nR 162,184,45056\n"
"M 56,172,84\nM 72,168,72\nR 164,184,49152\nM 72,172,88\nM 88,168,76\nR 166,184,53248\n"
"M 88,172,92\nG 4\nM 92,176,76\nR 168,185,40960\nM 92,180,92\nbuffer_load_dwordx4 v221, s[16:19], s63 offen offset:1024 lds\n"
"M 76,176,72\nR 170,185,45056\nM 76,180,88\ns_add_u32 m0, s50, 0\nM 60,176,68\nR 172,185,49152\n"
"M 60,180,84\ns_add_u32 s63, s56, 327680\nM 44,176,64\nR 174,185,53248\nM 44,180,80\nG 4\n"
"M 32,160,96\nR 176,186,40960\nM 32,164,112\nbuffer_load_dwordx4 v220, s[16:19], s63 offen lds\nM 48,160,100\nR 178,186,45056\n"
"M 48,164,116\nM 64,160,104\nR 180,186,49152\nM 64,164,120\nbuffer_load_dwordx4 v221, s[16:19], s63 offen offset:1024 lds\nM 80,160,108\n"
"R 182,186,53248\ns_add_u32 m0, s50, 8192\nM 80,164,124\nG 4\nM 84,168,108\nR 160,187,40960\n"
"s_add_u32 s63, s56, 851968\nM 84,172,124\nbuffer_load_dwordx4 v220, s[16:19], s63 offen lds\nM 68,168,104\nR 162,187,45056\nM 68,172,120\n"
"M 52,168,100\nR 164,187,49152\nM 52,172,116\nbuffer_load_dwordx4 v221, s[16:19], s63 offen offset:1024 lds\nM 36,168,96\nR 166,187,53248\n"
"s_add_u32 m0, s50, 16384\nM 36,172,112\nG 4\nM 40,176,96\nR 168,184,57344\ns_add_u32 s63, s57, 262144\n"
"M 40,180,112\nbuffer_load_dwordx4 v220, s[16:19], s63 offen lds\nM 56,176,100\nR 170,184,61440\nM 56,180,116\nM 72,176,104\n"
"R 172,188,0\nM 72,180,120\nM 88,176,108\nR 174,188,4096\ns_waitcnt vmcnt(15)\ns_barrier\n"
"M 88,180,124\nG 4\nM 92,160,108\nR 176,185,57344\nM 92,164,124\ns_add_u32 s64, s68, 23072768\n"
"M 76,160,104\nR 178,185,61440\nM 76,164,120\nbuffer_load_dwordx4 v221, s[16:19], s63 offen offset:1024 lds\nM 60,160,100\nR 180,189,0\n"
"s_waitcnt vmcnt(8)\nv_cmp_eq_u64 s[86:87], s[46:47], v[248:249]\nM 60,164,116\ns_add_u32 s65, s68, 23187456\nM 44,160,96\nR 182,189,4096\n"
"s_and_b32 s62, s86, 0xf\ns_cmp_eq_u32 s62, 0xf\ns_cbranch_scc0 Lq466\nLq467:\nM 44,164,112\nG 4\n"
"M 32,168,128\nR 160,186,57344\nM 32,172,144\nbuffer_load_dwordx4 a[160:163], v252, s[20:23], s64 offen sc1\nM 48,168,132\nR 162,186,61440\n"
"M 48,172,148\ns_add_u32 s66, s69, 23072768\nM 64,168,136\nR 164,190,0\nM 64,172,152\nbuffer_load_dwordx4 a[164:167], v252, s[20:23], s65 offen sc1\n"
"M 80,168,140\nR 166,190,4096\nM 80,172,156\nG 4\nM 84,176,140\nR 168,187,57344\n"
"M 84,180,156\ns_add_u32 s67, s69, 23187456\nM 68,176,136\nR 170,187,61440\nM 68,180,152\nbuffer_load_dwordx4 a[168:171], v252, s[20:23], s66 offen sc1\n"
"M 52,176,132\nR 172,191,0\nM 52,180,148\ns_add_u32 s64, s68, 23079936\nM 36,176,128\nR 174,191,4096\n"
"M 36,180,144\nG 4\nM 40,160,128\nR 176,188,8192\nM 40,164,144\nbuffer_load_dwordx4 a[172:175], v252, s[20:23], s67 offen sc1\n"
"M 56,160,132\nR 178,188,12288\nM 56,164,148\ns_add_u32 s65, s68, 23194624\nM 72,160,136\nR 180,188,16384\n"
"Lq579:\ns_lshr_b32 s60, s45, 3\ns_lshl2_add_u32 s60, s60, s40\ns_lshl_b32 s60, s60, 8\nv_lshl_add_u32 v224, v252, 3, s60\nv_add_u32 v224, Lq579+4096-LqPC, v224\n"
"v_min_u32 v224, LqE-LqPC-4, v224\ns_mov_b64 exec, 3\nglobal_load_dword v224, v224, s[98:99]\ns_mov_b64 exec, -1\nM 72,164,152\nbuffer_load_dwordx4 a[176:179], v252, s[20:23], s64 offen sc1\n"
"M 88,160,140\nR 182,188,20480\ns_add_u32 m0, s50, 24576\nM 88,164,156\nG 4\nM 92,168,140\n"
"R 160,189,8192\ns_add_u32 s63, s57, 786432\nM 92,172,156\ns_add_u32 s66, s69, 23079936\nM 76,168,136\nR 162,189,12288\n"
"M 76,172,152\nbuffer_load_dwordx4 a[180:183], v252, s[20:23], s65 offen sc1\nbuffer_load_dwordx4 v220, s[16:19], s63 offen lds\nM 60,168,132\nR 164,189,16384\nM 60,172,148\n"
"s_add_u32 s67, s69, 23194624\nM 44,168,128\nR 166,189,20480\nM 44,172,144\nbuffer_load_dwordx4 v221, s[16:19], s63 offen offset:1024 lds\nG 4\n"
"M 96,176,128\nR 168,190,8192\ns_add_u32 m0, s50, 32768\nM 96,180,144\nbuffer_load_dwordx4 a[184:187], v252, s[20:23], s66 offen sc1\nM 112,176,132\n"
"R 170,190,12288\ns_add_u32 s63, s57, 327680\nM 112,180,148\ns_add_u32 s64, s68, 23073792\nbuffer_load_dwordx4 v220, s[16:19], s63 offen lds\nM 128,176,136\n"
"R 172,190,16384\nM 128,180,152\nbuffer_load_dwordx4 a[188:191], v252, s[20:23], s67 offen sc1\nM 144,176,140\nR 174,190,20480\nM 144,180,156\n"
"buffer_load_dwordx4 v221, s[16:19], s63 offen offset:1024 lds\nG 4\nM 148,160,140\nR 176,191,8192\ns_waitcnt vmcnt(19)\ns_barrier\n"
"M 148,164,156\ns_add_u32 s65, s68, 23188480\ns_add_u32 s63, s57, 851968\nM 132,160,136\nR 178,191,12288\ns_add_u32 m0, s50, 40960\n"
"M 132,164,152\nbuffer_load_dwordx4 a[192:195], v252, s[20:23], s64 offen sc1\nM 116,160,132\nR 180,191,16384\nbuffer_load_dwordx4 v220, s[16:19], s63 offen lds\nM 116,164,148\n"
"s_add_u32 s66, s69, 23073792\nM 100,160,128\nR 182,191,20480\nM 100,164,144\nG 4\nM 104,168,128\n"
"R 160,188,24576\nbuffer_load_dwordx4 v221, s[16:19], s63 offen offset:1024 lds\nM 104,172,144\nbuffer_load_dwordx4 a[196:199], v252, s[20:23], s65 offen sc1\nM 120,168,132\nR 162,188,28672\n"
"M 120,172,148\ns_add_u32 s67, s69, 23188480\nM 136,168,136\nR 164,188,32768\nM 136,172,152\nbuffer_load_dwordx4 a[200:203], v252, s[20:23], s66 offen sc1\n"
"M 152,168,140\nR 166,188,36864\nM 152,172,156\nG 4\nM 156,176,140\nR 168,189,24576\n"
"M 156,180,156\nbuffer_load_dwordx4 a[204:207], v252, s[20:23], s67 offen sc1\nM 140,176,136\nR 170,189,28672\nM 140,180,152\ns_add_u32 s64, s68, 23080960\n"
"M 124,176,132\nR 172,189,32768\nM 124,180,148\ns_add_u32 s65, s68, 23195648\nM 108,176,128\nR 174,189,36864\n"
"M 108,180,144\nG 4\nM 0,160,128\nR 176,190,24576\nM 0,164,144\nbuffer_load_dwordx4 a[208:211], v252, s[20:23], s64 offen sc1\n"
"M 16,160,132\nR 178,190,28672\nM 16,164,148\ns_add_u32 s66, s69, 23080960\nv_mfma_f32_16x16x32_bf16 a[224:227], v[160:163], a[136:139], a[224:227]\nR 180,190,32768\n"
"v_mfma_f32_16x16x32_bf16 a[224:227], v[164:167], a[152:155], a[224:227]\nbuffer_load_dwordx4 a[212:215], v252, s[20:23], s65 offen sc1\nv_mfma_f32_16x16x32_bf16 a[240:243], v[160:163], a[140:143], a[240:243]\nR 182,190,36864\nv_mfma_f32_16x16x32_bf16 a[240:243], v[164:167], a[156:159], a[240:243]\nG 4\n"
"v_mfma_f32_16x16x32_bf16 a[244:247], v[168:171], a[140:143], a[244:247]\nR 160,191,24576\nv_mfma_f32_16x16x32_bf16 a[244:247], v[172:175], a[156:159], a[244:247]\nbuffer_load_dwordx4 a[216:219], v252, s[20:23], s66 offen sc1\nv_mfma_f32_16x16x32_bf16 a[228:231], v[168:171], a[136:139], a[228:231]\nR 162,191,28672\n"
"v_mfma_f32_16x16x32_bf16 a[228:231], v[172:175], a[152:155], a[228:231]\ns_add_u32 s67, s69, 23195648\nM 20,168,132\nR 164,191,32768\nM 20,172,148\nM 4,168,128\n"
"R 166,191,36864\nM 4,172,144\nG 4\nM 8,176,128\nR 168,188,40960\nM 8,180,144\n"
"buffer_load_dwordx4 a[220:223], v252, s[20:23], s67 offen sc1\nM 24,176,132\nR 170,188,45056\nM 24,180,148\ns_add_u32 m0, s50, 49152\nv_mfma_f32_16x16x32_bf16 a[232:235], v[176:179], a[136:139], a[232:235]\n"
"R 172,188,49152\nv_mfma_f32_16x16x32_bf16 a[232:235], v[180:183], a[152:155], a[232:235]\ns_add_u32 s63, s58, 262144\nv_mfma_f32_16x16x32_bf16 a[248:251], v[176:179], a[140:143], a[248:251]\nR 174,188,53248\nv_mfma_f32_16x16x32_bf16 a[248:251], v[180:183], a[156:159], a[248:251]\n"
"G 4\nv_mfma_f32_16x16x32_bf16 a[252:255], v[160:163], a[140:143], a[252:255]\nR 176,189,40960\nv_mfma_f32_16x16x32_bf16 a[252:255], v[164:167], a[156:159], a[252:255]\nbuffer_load_dwordx4 v220, s[16:19], s63 offen lds\nv_mfma_f32_16x16x32_bf16 a[236:239], v[160:163], a[136:139], a[236:239]\n"
"R 178,189,45056\ns_add_u32 s84, s52, 2048\nv_mfma_f32_16x16x32_bf16 a[236:239], v[164:167], a[152:155], a[236:239]\nv_lshrrev_b32 v244, 5, v252\nv_lshlrev_b32 v244, 5, v244\nM 28,160,132\n"
"R 180,189,49152\nv_bfe_u32 v245, v252, 4, 1\nM 28,164,148\nbuffer_load_dwordx4 v221, s[16:19], s63 offen offset:1024 lds\nM 12,160,128\nR 182,189,53248\n"
"M 12,164,144\ns_waitcnt vmcnt(9)\ns_barrier\nG 4\nM 96,168,160\nR 160,190,40960\n"
"M 96,172,176\ns_add_u32 m0, s50, 57344\nM 112,168,164\nR 162,190,45056\nM 112,172,180\ns_add_u32 s63, s58, 786432\n"
"M 128,168,168\nR 164,190,49152\nv_lshrrev_b32 v230, 8, v252\nM 128,172,184\nbuffer_load_dwordx4 v220, s[16:19], s63 offen lds\nM 144,168,172\n"
"R 166,190,53248\nv_lshlrev_b32 v230, 3, v230\nM 144,172,188\nG 4\nM 148,176,172\nR 168,191,40960\n"
"M 148,180,188\ns_and_b32 s60, s41, 7\ns_lshl2_add_u32 s60, s60, s40\nv_add_u32 v244, s60, v244\nM 132,176,168\nR 170,191,45056\n"
"v_mul_u32_u24 v244, 6144, v244\nM 132,180,184\ns_sub_u32 s60, s84, s84\ns_add_u32 s60, s60, 2048\nv_mul_lo_u32 v245, v245, s60\nM 116,176,164\n"
"R 172,191,49152\nv_add_u32 v244, v244, v245\nM 116,180,180\nbuffer_load_dwordx2 v[232:233], v230, s[36:39], s53 offen offset:0\nM 100,176,160\nLpg15:\n"
"s_cbranch_execz LretO0n\nR 174,191,53248\ns_bfm_b64 exec, 16, 0\nbuffer_load_dword v245, v244, s[4:7], s84 offen\ns_mov_b64 exec, -1\nM 100,180,176\n"
"G 4\nM 104,160,160\nR 176,184,0\nM 104,164,176\nbuffer_load_dwordx4 v221, s[16:19], s63 offen offset:1024 lds\nM 120,160,164\n"
"R 178,184,4096\nM 120,164,180\ns_add_u32 m0, s50, 65536\nM 136,160,168\nR 180,184,8192\nbuffer_load_dwordx2 v[234:235], v230, s[36:39], s53 offen offset:32\n"
"M 136,164,184\ns_add_u32 s63, s58, 327680\nbuffer_load_dwordx2 v[236:237], v230, s[36:39], s53 offen offset:64\nM 152,160,172\nR 182,184,12288\nM 152,164,188\n"
"buffer_load_dwordx2 v[238:239], v230, s[36:39], s53 offen offset:96\nG 4\nM 156,168,172\nR 160,185,0\nv_bfe_u32 v242, v252, 4, 4\nM 156,172,188\n"
"buffer_load_dwordx4 v220, s[16:19], s63 offen lds\nM 140,168,168\nR 162,185,4096\nv_lshrrev_b32 v243, 8, v252\nM 140,172,184\nv_lshlrev_b32 v243, 2, v243\n"
"v_sub_u32 v242, v242, v243\nM 124,168,164\nR 164,185,8192\nv_lshlrev_b32 v243, 4, v242\nM 124,172,180\ns_mov_b64 s[60:61], 0x3f80\n"
"v_lshlrev_b64 v[208:209], v243, s[60:61]\nM 108,168,160\nR 166,185,12288\ns_bfm_b64 exec, 16, 16\nbuffer_load_dword v245, v244, s[4:7], s84 offen\ns_mov_b64 exec, -1\n"
"M 108,172,176\nG 4\nM 96,176,192\nR 168,186,0\ns_waitcnt vmcnt(14)\nM 96,180,208\n"
"buffer_load_dwordx4 v221, s[16:19], s63 offen offset:1024 lds\nM 112,176,196\nR 170,186,4096\ns_waitcnt vmcnt(14)\nM 112,180,212\ns_add_u32 m0, s50, 73728\n"
"M 128,176,200\nR 172,186,8192\ns_waitcnt vmcnt(13)\nM 128,180,216\ns_add_u32 s63, s58, 851968\nM 144,176,204\n"
"R 174,186,12288\ns_waitcnt vmcnt(12)\nM 144,180,220\nG 4\nv_cmp_gt_u32 s[60:61], 4, v242\ns_nop 1\n"
"v_cndmask_b32 v212, 0, v208, s[60:61]\nv_cndmask_b32 v208, 0, v208, s[60:61]\nv_cndmask_b32 v213, 0, v209, s[60:61]\nv_cndmask_b32 v209, 0, v209, s[60:61]\nM 148,160,204\nR 176,187,0\n"
"M 148,164,220\nbuffer_load_dwordx4 v220, s[16:19], s63 offen lds\nM 132,160,200\nR 178,187,4096\nM 132,164,216\nM 116,160,196\n"
"R 180,187,8192\nM 116,164,212\nM 100,160,192\nR 182,187,12288\ns_bfm_b64 exec, 16, 32\nbuffer_load_dword v245, v244, s[4:7], s84 offen\n"
"s_mov_b64 exec, -1\nM 100,164,208\nG 4\nM 104,168,192\nR 160,184,16384\nM 104,172,208\n"
"buffer_load_dwordx4 v221, s[16:19], s63 offen offset:1024 lds\nM 120,168,196\nR 162,184,20480\nM 120,172,212\ns_add_u32 m0, s50, 81920\nM 136,168,200\n"
"R 164,184,24576\nM 136,172,216\ns_add_u32 s63, s59, 0\nM 152,168,204\nR 166,184,28672\nM 152,172,220\n"
"G 4\nM 156,176,204\nR 168,185,16384\nM 156,180,220\nbuffer_load_dwordx4 v220, s[16:19], s63 offen lds\nM 140,176,200\n"
"R 170,185,20480\nM 140,180,216\nM 124,176,196\nR 172,185,24576\nM 124,180,212\nM 108,176,192\n"
"R 174,185,28672\ns_bfm_b64 exec, 16, 48\nbuffer_load_dword v245, v244, s[4:7], s84 offen\ns_mov_b64 exec, -1\nM 108,180,208\nG 4\n"
"M 0,160,160\nR 176,186,16384\nM 0,164,176\ns_mov_b32 m0, s51\nM 16,160,164\nR 178,186,20480\n"
"v_cvt_pk_bf16_f32 v128, v128, v129\nv_cvt_pk_bf16_f32 v129, v130, v131\nM 16,164,180\nds_write_addtid_b32 v128 offset:0\nv_cvt_pk_bf16_f32 v130, v132, v133\nv_cvt_pk_bf16_f32 v131, v134, v135\n"
"v_mfma_f32_16x16x32_bf16 a[224:227], v[160:163], a[168:171], a[224:227]\nR 180,186,24576\nv_cvt_pk_bf16_f32 v132, v136, v137\nv_mfma_f32_16x16x32_bf16 a[224:227], v[164:167], a[184:187], a[224:227]\nds_write_addtid_b32 v129 offset:256\nv_cvt_pk_bf16_f32 v133, v138, v139\n"
"v_mfma_f32_16x16x32_bf16 a[240:243], v[160:163], a[172:175], a[240:243]\nR 182,186,28672\nv_cvt_pk_bf16_f32 v134, v140, v141\nv_mfma_f32_16x16x32_bf16 a[240:243], v[164:167], a[188:191], a[240:243]\nds_write_addtid_b32 v130 offset:512\nG 7\n"
"v_cvt_pk_bf16_f32 v135, v142, v143\nv_mfma_f32_16x16x32_bf16 a[244:247], v[168:171], a[172:175], a[244:247]\nR 160,187,16384\nv_cvt_pk_bf16_f32 v136, v144, v145\nv_mfma_f32_16x16x32_bf16 a[244:247], v[172:175], a[188:191], a[244:247]\nds_write_addtid_b32 v131 offset:768\n"
"v_cvt_pk_bf16_f32 v137, v146, v147\nv_mfma_f32_16x16x32_bf16 a[228:231], v[168:171], a[168:171], a[228:231]\nR 162,187,20480\nv_cvt_pk_bf16_f32 v138, v148, v149\nv_mfma_f32_16x16x32_bf16 a[228:231], v[172:175], a[184:187], a[228:231]\nds_write_addtid_b32 v132 offset:1024\n"
"v_cvt_pk_bf16_f32 v139, v150, v151\nM 20,168,164\nR 164,187,24576\nv_cvt_pk_bf16_f32 v140, v152, v153\nM 20,172,180\nds_write_addtid_b32 v133 offset:1280\n"
"v_cvt_pk_bf16_f32 v141, v154, v155\nM 4,168,160\nR 166,187,28672\nv_cvt_pk_bf16_f32 v142, v156, v157\nM 4,172,176\nds_write_addtid_b32 v134 offset:1536\n"
"G 9\nv_cvt_pk_bf16_f32 v143, v158, v159\nM 8,176,160\nR 168,184,32768\nds_write_addtid_b32 v135 offset:1792\nM 8,180,176\n"
"ds_write_addtid_b32 v136 offset:2048\nds_write_addtid_b32 v137 offset:2304\nM 24,176,164\nR 170,184,36864\nds_write_addtid_b32 v138 offset:2560\nM 24,180,180\n"
"ds_write_addtid_b32 v139 offset:2816\nds_write_addtid_b32 v140 offset:3072\nv_mfma_f32_16x16x32_bf16 a[232:235], v[176:179], a[168:171], a[232:235]\nR 172,184,40960\nds_write_addtid_b32 v141 offset:3328\nv_mfma_f32_16x16x32_bf16 a[232:235], v[180:183], a[184:187], a[232:235]\n"
"ds_write_addtid_b32 v142 offset:3584\nds_write_addtid_b32 v143 offset:3840\nv_mfma_f32_16x16x32_bf16 a[248:251], v[176:179], a[172:175], a[248:251]\nR 174,184,45056\ns_add_u32 m0, s50, 81920\nv_mfma_f32_16x16x32_bf16 a[248:251], v[180:183], a[188:191], a[248:251]\n"
"G 14\nv_mfma_f32_16x16x32_bf16 a[252:255], v[160:163], a[172:175], a[252:255]\nR 176,185,32768\ns_waitcnt vmcnt(11) lgkmcnt(2)\ns_barrier\nv_mfma_f32_16x16x32_bf16 a[252:255], v[164:167], a[188:191], a[252:255]\n"
"buffer_load_dwordx4 v221, s[16:19], s63 offen offset:1024 lds\nv_mfma_f32_16x16x32_bf16 a[236:239], v[160:163], a[168:171], a[236:239]\nR 178,185,36864\ns_add_u32 s63, s59, 524288\nv_mfma_f32_16x16x32_bf16 a[236:239], v[164:167], a[184:187], a[236:239]\ns_add_u32 m0, s50, 90112\n"
"ds_read2st64_b32 v[128:129], v225 offset0:0 offset1:1\nM 28,160,164\nR 180,185,40960\nM 28,164,180\nbuffer_load_dwordx4 v220, s[16:19], s63 offen lds\nM 12,160,160\n"
"R 182,185,45056\nM 12,164,176\nds_read2st64_b32 v[130:131], v225 offset0:2 offset1:3\nG 6\nM 0,168,192\nR 160,186,32768\n"
"M 0,172,208\nbuffer_load_dwordx4 v221, s[16:19], s63 offen offset:1024 lds\nds_read2st64_b32 v[132:133], v225 offset0:4 offset1:5\nM 16,168,196\nR 162,186,36864\ns_add_u32 s63, s59, 65536\n"
"ds_read2st64_b32 v[134:135], v225 offset0:6 offset1:7\nM 16,172,212\ns_add_u32 m0, s50, 98304\nds_read2st64_b32 v[136:137], v225 offset0:8 offset1:9\nv_mfma_f32_16x16x32_bf16 a[224:227], v[168:171], a[200:203], a[224:227]\nR 164,186,40960\n"
"v_mfma_f32_16x16x32_bf16 a[224:227], v[172:175], a[216:219], a[224:227]\nbuffer_load_dwordx4 v220, s[16:19], s63 offen lds\nG 6\nv_mfma_f32_16x16x32_bf16 v[96:99], v[208:211], v[128:131], v[96:99]\nds_read2st64_b32 v[138:139], v225 offset0:10 offset1:11\nv_mfma_f32_16x16x32_bf16 v[100:103], v[210:213], v[128:131], v[100:103]\n"
"ds_read2st64_b32 v[140:141], v225 offset0:12 offset1:13\nds_read2st64_b32 v[142:143], v225 offset0:14 offset1:15\nv_mfma_f32_16x16x32_bf16 a[240:243], v[168:171], a[204:207], a[240:243]\nR 166,186,45056\nv_mfma_f32_16x16x32_bf16 a[240:243], v[172:175], a[220:223], a[240:243]\nv_mfma_f32_16x16x32_bf16 a[244:247], v[176:179], a[204:207], a[244:247]\n"
"R 168,187,32768\nG 7\nv_mfma_f32_16x16x32_bf16 v[104:107], v[208:211], v[132:135], v[104:107]\nv_mfma_f32_16x16x32_bf16 v[108:111], v[210:213], v[132:135], v[108:111]\nv_mfma_f32_16x16x32_bf16 a[244:247], v[180:183], a[220:223], a[244:247]\nbuffer_load_dwordx4 v221, s[16:19], s63 offen offset:1024 lds\n"
"v_mfma_f32_16x16x32_bf16 a[228:231], v[176:179], a[200:203], a[228:231]\nR 170,187,36864\nv_mfma_f32_16x16x32_bf16 a[228:231], v[180:183], a[216:219], a[228:231]\ns_add_u32 m0, s50, 106496\nG 5\nv_mfma_f32_16x16x32_bf16 v[112:115], v[208:211], v[136:139], v[112:115]\n"
"v_mfma_f32_16x16x32_bf16 v[116:119], v[210:213], v[136:139], v[116:119]\nG 3\nv_mfma_f32_16x16x32_bf16 v[120:123], v[208:211], v[140:143], v[120:123]\nv_mfma_f32_16x16x32_bf16 v[124:127], v[210:213], v[140:143], v[124:127]\nM 20,176,196\nR 172,187,40960\n"
"M 20,180,212\ns_add_u32 s63, s59, 589824\nM 4,176,192\nR 174,187,45056\nM 4,180,208\nG 4\n"
"M 8,160,192\nR 176,184,49152\nM 8,164,208\nbuffer_load_dwordx4 v220, s[16:19], s63 offen lds\nM 24,160,196\nR 178,184,53248\n"
"M 24,164,212\nv_mfma_f32_16x16x32_bf16 a[232:235], v[160:163], a[200:203], a[232:235]\nR 180,184,57344\nv_cvt_pk_bf16_f32 v120, v120, v121\nv_cvt_pk_bf16_f32 v96, v96, v97\nv_cvt_pk_bf16_f32 v112, v112, v113\n"
"v_cvt_pk_bf16_f32 v121, v122, v123\nv_mfma_f32_16x16x32_bf16 a[232:235], v[164:167], a[216:219], a[232:235]\nbuffer_load_dwordx4 v221, s[16:19], s63 offen offset:1024 lds\nv_cvt_pk_bf16_f32 v97, v98, v99\nv_cvt_pk_bf16_f32 v113, v114, v115\nv_cvt_pk_bf16_f32 v122, v124, v125\n"
"v_mfma_f32_16x16x32_bf16 a[248:251], v[160:163], a[204:207], a[248:251]\nR 182,184,61440\nv_cvt_pk_bf16_f32 v123, v126, v127\nv_cvt_pk_bf16_f32 v98, v100, v101\nv_cvt_pk_bf16_f32 v114, v116, v117\nv_mfma_f32_16x16x32_bf16 a[248:251], v[164:167], a[220:223], a[248:251]\n"
"s_bfe_u32 s100, s56, 0x20007\ns_mul_i32 s100, s100, 49152\ns_add_u32 s100, s100, s72\ns_add_u32 s100, s100, 52428800\nG 4\nv_cvt_pk_bf16_f32 v99, v102, v103\n"
"v_cvt_pk_bf16_f32 v115, v118, v119\nv_mfma_f32_16x16x32_bf16 a[252:255], v[168:171], a[204:207], a[252:255]\nR 160,185,49152\ns_add_u32 s63, s59, 131072\nv_mfma_f32_16x16x32_bf16 a[252:255], v[172:175], a[220:223], a[252:255]\nbuffer_store_dwordx4 v[96:99], v252, s[20:23], s100 offen offset:0 sc1\n"
"v_mfma_f32_16x16x32_bf16 a[236:239], v[168:171], a[200:203], a[236:239]\nR 162,185,53248\nv_cvt_pk_bf16_f32 v104, v104, v105\nv_mfma_f32_16x16x32_bf16 a[236:239], v[172:175], a[216:219], a[236:239]\ns_add_u32 m0, s50, 114688\nv_cvt_pk_bf16_f32 v105, v106, v107\n"
"v_cvt_pk_bf16_f32 v106, v108, v109\nM 28,168,196\nR 164,185,57344\nv_cvt_pk_bf16_f32 v107, v110, v111\nM 28,172,212\nbuffer_load_dwordx4 v220, s[16:19], s63 offen lds\n"
"M 12,168,192\nR 166,185,61440\nM 12,172,208\nbuffer_store_dwordx4 v[104:107], v252, s[20:23], s100 offen offset:1024 sc1\ns_waitcnt vmcnt(12)\ns_barrier\n"
"G 4\nM 32,176,160\nR 168,186,49152\nM 32,180,176\nv_xor_b32 v240, 0x2000, v225\nM 48,176,164\n"
"R 170,186,53248\nds_write2st64_b32 v240, a224, a225 offset0:0 offset1:1\nM 48,180,180\nbuffer_store_dwordx4 v[112:115], v252, s[20:23], s100 offen offset:2048 sc1\nds_write2st64_b32 v240, a226, a227 offset0:2 offset1:3\nM 64,176,168\n"
"R 172,186,57344\nds_write2st64_b32 v240, a228, a229 offset0:4 offset1:5\nM 64,180,184\nds_write2st64_b32 v240, a230, a231 offset0:6 offset1:7\nM 80,176,172\nR 174,186,61440\n"
"ds_write2st64_b32 v240, a232, a233 offset0:8 offset1:9\nM 80,180,188\nds_write2st64_b32 v240, a234, a235 offset0:10 offset1:11\nG 10\nM 84,160,172\nR 176,187,49152\n"
"ds_write2st64_b32 v240, a236, a237 offset0:12 offset1:13\nM 84,164,188\nbuffer_store_dwordx4 v[120:123], v252, s[20:23], s100 offen offset:3072 sc1\nds_write2st64_b32 v240, a238, a239 offset0:14 offset1:15\nM 68,160,168\nR 178,187,53248\n"
"ds_write2st64_b32 v240, a240, a241 offset0:16 offset1:17\nM 68,164,184\nds_write2st64_b32 v240, a242, a243 offset0:18 offset1:19\nM 52,160,164\nR 180,187,57344\nds_write2st64_b32 v240, a244, a245 offset0:20 offset1:21\n"
"M 52,164,180\nds_write2st64_b32 v240, a246, a247 offset0:22 offset1:23\nM 36,160,160\nR 182,187,61440\nds_write2st64_b32 v240, a248, a249 offset0:24 offset1:25\nM 36,164,176\n"
"ds_write2st64_b32 v240, a250, a251 offset0:26 offset1:27\nG 14\nM 40,168,160\nR 160,188,0\nds_write2st64_b32 v240, a252, a253 offset0:28 offset1:29\nM 40,172,176\n"
"buffer_load_dwordx4 v221, s[16:19], s63 offen offset:1024 lds\nM 56,168,164\nR 162,188,4096\ns_add_u32 s63, s59, 655360\nM 56,172,180\ns_add_u32 m0, s50, 0\n"
"ds_write2st64_b32 v240, a254, a255 offset0:30 offset1:31\nM 72,168,168\nR 164,188,8192\nM 72,172,184\nbuffer_load_dwordx4 v220, s[16:19], s63 offen lds\nM 88,168,172\n"
"R 166,188,12288\nM 88,172,188\nG 8\nM 92,176,172\nR 168,189,0\ns_waitcnt vmcnt(11) lgkmcnt(3)\n"
"s_barrier\nM 92,180,188\nbuffer_load_dwordx4 v221, s[16:19], s63 offen offset:1024 lds\nds_read2st64_b32 v[128:129], v225 offset0:0 offset1:1\nM 76,176,168\nR 170,189,4096\n"
"s_add_u32 s63, s59, 196608\nds_read2st64_b32 v[130:131], v225 offset0:2 offset1:3\nM 76,180,184\ns_add_u32 m0, s50, 8192\nds_read2st64_b32 v[132:133], v225 offset0:4 offset1:5\nds_read2st64_b32 v[134:135], v225 offset0:6 offset1:7\n"
"M 60,176,164\nR 172,189,8192\nds_read2st64_b32 v[136:137], v225 offset0:8 offset1:9\nM 60,180,180\nbuffer_load_dwordx4 v220, s[16:19], s63 offen lds\nds_read2st64_b32 v[138:139], v225 offset0:10 offset1:11\n"
"M 44,176,160\nR 174,189,12288\nds_read2st64_b32 v[140:141], v225 offset0:12 offset1:13\nM 44,180,176\nds_read2st64_b32 v[142:143], v225 offset0:14 offset1:15\nds_read2st64_b32 v[144:145], v225 offset0:16 offset1:17\n"
"G 13\nM 32,160,192\nR 176,190,0\nds_read2st64_b32 v[146:147], v225 offset0:18 offset1:19\nG 13\nv_add_f32 v0, v0, v128\n"
"M 32,164,208\nbuffer_load_dwordx4 v221, s[16:19], s63 offen offset:1024 lds\nds_read2st64_b32 v[148:149], v225 offset0:20 offset1:21\nM 48,160,196\nR 178,190,4096\nds_read2st64_b32 v[150:151], v225 offset0:22 offset1:23\n"
"v_add_f32 v1, v1, v129\nM 48,164,212\ns_add_u32 m0, s50, 16384\nds_read2st64_b32 v[152:153], v225 offset0:24 offset1:25\nds_read2st64_b32 v[154:155], v225 offset0:26 offset1:27\nM 64,160,200\n"
"R 180,190,8192\nds_read2st64_b32 v[156:157], v225 offset0:28 offset1:29\nG 15\nv_add_f32 v2, v2, v130\nM 64,164,216\ns_add_u32 s63, s59, 720896\n"
"ds_read2st64_b32 v[158:159], v225 offset0:30 offset1:31\nv_add_f32 v3, v3, v131\nM 80,160,204\nR 182,190,12288\nv_add_f32 v4, v4, v132\nv_add_f32 v5, v5, v133\n"
"M 80,164,220\nG 14\nv_add_f32 v6, v6, v134\nv_add_f32 v7, v7, v135\nv_add_f32 v8, v8, v136\nM 84,168,204\n"
"R 160,191,0\nv_add_f32 v9, v9, v137\nv_add_f32 v10, v10, v138\nM 84,172,220\nbuffer_load_dwordx4 v220, s[16:19], s63 offen lds\nv_add_f32 v11, v11, v139\n"
"M 68,168,200\nR 162,191,4096\nG 15\nv_add_f32 v12, v12, v140\nv_add_f32 v13, v13, v141\nM 68,172,216\n"
"G 14\nv_add_f32 v14, v14, v142\nv_add_f32 v15, v15, v143\nG 13\nv_add_f32 v16, v16, v144\nM 52,168,196\n"
"R 164,191,8192\nv_add_f32 v17, v17, v145\nG 12\nv_add_f32 v18, v18, v146\nM 52,172,212\nbuffer_load_dwordx4 v221, s[16:19], s63 offen offset:1024 lds\n"
"v_add_f32 v19, v19, v147\nM 36,168,192\nR 166,191,12288\ns_add_u32 m0, s50, 24576\nG 12\nv_add_f32 v20, v20, v148\n"
"M 36,172,208\nG 4\nv_add_f32 v21, v21, v149\nv_add_f32 v22, v22, v150\nv_add_f32 v23, v23, v151\nM 40,176,192\n"
"R 168,188,16384\ns_add_u32 s63, s59, 393216\nv_add_f32 v24, v24, v152\nM 40,180,208\nbuffer_load_dwordx4 v220, s[16:19], s63 offen lds\nv_add_f32 v25, v25, v153\n"
"M 56,176,196\nR 170,188,20480\nv_add_f32 v26, v26, v154\nv_add_f32 v27, v27, v155\nM 56,180,212\nv_add_f32 v28, v28, v156\n"
"v_add_f32 v29, v29, v157\nv_add_f32 v30, v30, v158\nv_add_f32 v31, v31, v159\nM 72,176,200\nR 172,188,24576\nv_cvt_pk_bf16_f32 v24, v24, v25\n"
"v_cvt_pk_bf16_f32 v0, v0, v1\nv_cvt_pk_bf16_f32 v25, v26, v27\nM 72,180,216\nbuffer_load_dwordx4 v221, s[16:19], s63 offen offset:1024 lds\nv_cvt_pk_bf16_f32 v26, v28, v29\nM 88,176,204\n"
"R 174,188,28672\nv_cvt_pk_bf16_f32 v27, v30, v31\nv_cvt_pk_bf16_f32 v1, v2, v3\nM 88,180,220\nG 4\nv_cvt_pk_bf16_f32 v2, v4, v5\n"
"v_cvt_pk_bf16_f32 v3, v6, v7\nM 92,160,204\nR 176,189,16384\ns_bfe_u32 s100, s57, 0x20007\ns_mul_i32 s100, s100, 49152\ns_add_u32 s100, s100, s72\n"
"s_add_u32 s100, s100, 52445184\nv_cvt_pk_bf16_f32 v16, v16, v17\nM 92,164,220\nbuffer_store_dwordx4 v[0:3], v252, s[20:23], s100 offen offset:0 sc1\nM 76,160,200\nR 178,189,20480\n"
"v_cvt_pk_bf16_f32 v17, v18, v19\nM 76,164,216\nv_cvt_pk_bf16_f32 v8, v8, v9\nv_cvt_pk_bf16_f32 v9, v10, v11\nM 60,160,196\nR 180,189,24576\n"
"v_cvt_pk_bf16_f32 v10, v12, v13\nM 60,164,212\nv_cvt_pk_bf16_f32 v11, v14, v15\nv_cvt_pk_bf16_f32 v18, v20, v21\nM 44,160,192\nR 182,189,28672\n"
"v_cvt_pk_bf16_f32 v19, v22, v23\nM 44,164,208\nbuffer_store_dwordx4 v[8:11], v252, s[20:23], s100 offen offset:1024 sc1\ns_waitcnt vmcnt(16)\ns_barrier\nG 4\n"
"v_mfma_f32_16x16x32_bf16 v[96:99], v[168:171], a[0:3], 0\nR 160,190,16384\nM 96,172,16\ns_mov_b32 m0, s51\nv_mfma_f32_16x16x32_bf16 v[112:115], v[168:171], a[4:7], 0\nR 162,190,20480\n"
"v_cvt_pk_bf16_f32 v64, v64, v65\nv_cvt_pk_bf16_f32 v65, v66, v67\nM 112,172,20\nbuffer_store_dwordx4 v[16:19], v252, s[20:23], s100 offen offset:2048 sc1\nv_cvt_pk_bf16_f32 v66, v68, v69\nv_mfma_f32_16x16x32_bf16 v[128:131], v[168:171], a[8:11], 0\n"
"R 164,190,24576\nv_cvt_pk_bf16_f32 v67, v70, v71\nLpg16:\ns_cbranch_execz LretW0z\nv_cvt_pk_bf16_f32 v68, v72, v73\nM 128,172,24\n"
"ds_write_addtid_b32 v64 offset:0\nv_cvt_pk_bf16_f32 v69, v74, v75\nv_cvt_pk_bf16_f32 v70, v76, v77\nv_mfma_f32_16x16x32_bf16 v[144:147], v[168:171], a[12:15], 0\nR 166,190,28672\nv_cvt_pk_bf16_f32 v71, v78, v79\n"
"v_cvt_pk_bf16_f32 v72, v80, v81\nM 144,172,28\nds_write_addtid_b32 v65 offset:256\nG 6\nv_cvt_pk_bf16_f32 v73, v82, v83\nv_mfma_f32_16x16x32_bf16 v[148:151], v[176:179], a[12:15], 0\n"
"R 168,191,16384\nv_cvt_pk_bf16_f32 v74, v84, v85\nv_cvt_pk_bf16_f32 v75, v86, v87\nM 148,180,28\nbuffer_store_dwordx4 v[24:27], v252, s[20:23], s100 offen offset:3072 sc1\nv_mfma_f32_16x16x32_bf16 v[132:135], v[176:179], a[8:11], 0\n"
"R 170,191,20480\nv_cvt_pk_bf16_f32 v76, v88, v89\nM 132,180,24\nds_write_addtid_b32 v66 offset:512\nv_cvt_pk_bf16_f32 v77, v90, v91\nv_mfma_f32_16x16x32_bf16 v[116:119], v[176:179], a[4:7], 0\n"
"R 172,191,24576\nv_cvt_pk_bf16_f32 v78, v92, v93\nM 116,180,20\nds_write_addtid_b32 v67 offset:768\nv_cvt_pk_bf16_f32 v79, v94, v95\nv_mfma_f32_16x16x32_bf16 v[100:103], v[176:179], a[0:3], 0\n"
"R 174,191,28672\nds_write_addtid_b32 v68 offset:1024\nM 100,180,16\nds_write_addtid_b32 v69 offset:1280\nds_write_addtid_b32 v70 offset:1536\nG 10\n"
"v_mfma_f32_16x16x32_bf16 v[104:107], v[160:163], a[0:3], 0\nR 176,188,32768\nds_write_addtid_b32 v71 offset:1792\nM 104,164,16\nds_write_addtid_b32 v72 offset:2048\nds_write_addtid_b32 v73 offset:2304\n"
"v_mfma_f32_16x16x32_bf16 v[120:123], v[160:163], a[4:7], 0\nR 178,188,36864\nds_write_addtid_b32 v74 offset:2560\nM 120,164,20\nds_write_addtid_b32 v75 offset:2816\nds_write_addtid_b32 v76 offset:3072\n"
"v_mfma_f32_16x16x32_bf16 v[136:139], v[160:163], a[8:11], 0\nR 180,188,40960\nds_write_addtid_b32 v77 offset:3328\nM 136,164,24\nds_write_addtid_b32 v78 offset:3584\nds_write_addtid_b32 v79 offset:3840\n"
"v_mfma_f32_16x16x32_bf16 v[152:155], v[160:163], a[12:15], 0\nR 182,188,45056\ns_add_u32 m0, s50, 32768\nM 152,164,28\ns_add_u32 s63, s59, 458752\nG 15\n"
"v_mfma_f32_16x16x32_bf16 v[156:159], v[168:171], a[12:15], 0\nR 160,189,32768\ns_waitcnt vmcnt(10) lgkmcnt(2)\ns_barrier\nM 156,172,28\nbuffer_load_dwordx4 v220, s[16:19], s63 offen lds\n"
"v_mfma_f32_16x16x32_bf16 v[140:143], v[168:171], a[8:11], 0\nR 162,189,36864\nM 140,172,24\nds_read2st64_b32 v[64:65], v225 offset0:0 offset1:1\nv_mfma_f32_16x16x32_bf16 v[124:127], v[168:171], a[4:7], 0\nR 164,189,40960\n"
"M 124,172,20\nbuffer_load_dwordx4 v221, s[16:19], s63 offen offset:1024 lds\nv_mfma_f32_16x16x32_bf16 v[108:111], v[168:171], a[0:3], 0\nR 166,189,45056\ns_add_u32 m0, s50, 40960\nM 108,172,16\n"
"ds_read2st64_b32 v[66:67], v225 offset0:2 offset1:3\nG 6\nM 96,176,32\nR 168,190,32768\ns_add_u32 s63, s59, 262144\nv_bfe_u32 v250, v252, 4, 2\n"
"M 96,180,48\nbuffer_load_dwordx4 v220, s[16:19], s63 offen lds\nds_read2st64_b32 v[68:69], v225 offset0:4 offset1:5\nv_lshlrev_b32 v250, 3, v250\nM 112,176,36\nR 170,190,36864\n"
"ds_read2st64_b32 v[70:71], v225 offset0:6 offset1:7\ns_lshl_b32 s60, s43, 5\nv_add_u32 v250, s60, v250\nM 112,180,52\nds_read2st64_b32 v[72:73], v225 offset0:8 offset1:9\nM 128,176,40\n"
"R 172,190,40960\nM 128,180,56\nbuffer_load_dwordx4 v221, s[16:19], s63 offen offset:1024 lds\nG 6\nv_mfma_f32_16x16x32_bf16 v[32:35], v[208:211], v[64:67], v[32:35]\nds_read2st64_b32 v[74:75], v225 offset0:10 offset1:11\n"
"v_mfma_f32_16x16x32_bf16 v[36:39], v[210:213], v[64:67], v[36:39]\nds_read2st64_b32 v[76:77], v225 offset0:12 offset1:13\nds_read2st64_b32 v[78:79], v225 offset0:14 offset1:15\nM 144,176,44\nR 174,190,45056\ns_add_u32 m0, s50, 49152\n"
"M 144,180,60\nM 148,160,44\nR 176,191,32768\ns_add_u32 s63, s59, 786432\nG 7\nv_mfma_f32_16x16x32_bf16 v[40:43], v[208:211], v[68:71], v[40:43]\n"
"v_mfma_f32_16x16x32_bf16 v[44:47], v[210:213], v[68:71], v[44:47]\nM 148,164,60\nbuffer_load_dwordx4 v220, s[16:19], s63 offen lds\nM 132,160,40\nR 178,191,36864\nM 132,164,56\n"
"G 5\nv_mfma_f32_16x16x32_bf16 v[48:51], v[208:211], v[72:75], v[48:51]\nv_mfma_f32_16x16x32_bf16 v[52:55], v[210:213], v[72:75], v[52:55]\nG 3\nv_mfma_f32_16x16x32_bf16 v[56:59], v[208:211], v[76:79], v[56:59]\nv_mfma_f32_16x16x32_bf16 v[60:63], v[210:213], v[76:79], v[60:63]\n"
"M 116,160,36\nR 180,191,40960\nM 116,164,52\nM 100,160,32\nR 182,191,45056\nM 100,164,48\n"
"G 4\nM 104,168,32\nR 160,188,49152\nM 104,172,48\nbuffer_load_dwordx4 v221, s[16:19], s63 offen offset:1024 lds\nM 120,168,36\n"
"R 162,188,53248\nv_mov_b32 v226, s46\nM 120,172,52\nv_mov_b32 v227, s47\nM 136,168,40\nR 164,184,0\n"
"s_bfe_u32 s62, s56, 0x20007\ns_lshl_b32 s62, s62, 5\ns_add_u32 s61, s74, 548864\ns_add_u32 s61, s61, s62\ns_waitcnt vmcnt(6)\ns_mov_b64 exec, 1\n"
"buffer_atomic_swap_x2 v[226:227], off, s[20:23], s61 sc1\ns_mov_b64 exec, -1\nv_cvt_pk_bf16_f32 v32, v32, v33\nM 136,172,56\ns_bfe_u32 s62, s57, 0x20007\ns_lshl_b32 s62, s62, 5\n"
"s_add_u32 s61, s74, 548872\ns_add_u32 s61, s61, s62\ns_mov_b64 exec, 1\nbuffer_atomic_swap_x2 v[226:227], off, s[20:23], s61 sc1\ns_mov_b64 exec, -1\nM 152,168,44\n"
"R 166,184,4096\nv_cvt_pk_bf16_f32 v33, v34, v35\nM 152,172,60\nG 4\nv_cvt_pk_bf16_f32 v34, v36, v37\nv_cvt_pk_bf16_f32 v35, v38, v39\n"
"M 156,176,44\nR 168,189,49152\ns_bfe_u32 s100, s58, 0x20007\ns_mul_i32 s100, s100, 49152\ns_add_u32 s100, s100, s72\ns_add_u32 s100, s100, 52461568\n"
"v_cvt_pk_bf16_f32 v48, v48, v49\nM 156,180,60\nbuffer_store_dwordx4 v[32:35], v252, s[20:23], s100 offen offset:0 sc1\nM 140,176,40\nR 170,189,53248\nv_cvt_pk_bf16_f32 v40, v40, v41\n"
"M 140,180,56\nv_cvt_pk_bf16_f32 v41, v42, v43\nv_cvt_pk_bf16_f32 v42, v44, v45\nM 124,176,36\nR 172,185,0\nv_cvt_pk_bf16_f32 v43, v46, v47\n"
"M 124,180,52\nv_cvt_pk_bf16_f32 v49, v50, v51\nv_cvt_pk_bf16_f32 v50, v52, v53\nM 108,176,32\nR 174,185,4096\nv_cvt_pk_bf16_f32 v51, v54, v55\n"
"M 108,180,48\nbuffer_store_dwordx4 v[40:43], v252, s[20:23], s100 offen offset:1024 sc1\ns_barrier\nG 4\nM 96,160,64\nR 176,190,49152\n"
"v_cvt_pk_bf16_f32 v56, v56, v57\nM 96,164,80\nv_cvt_pk_bf16_f32 v57, v58, v59\nv_cvt_pk_bf16_f32 v58, v60, v61\nM 112,160,68\nR 178,190,53248\n"
"v_cvt_pk_bf16_f32 v59, v62, v63\nM 112,164,84\nbuffer_store_dwordx4 v[48:51], v252, s[20:23], s100 offen offset:2048 sc1\nM 128,160,72\nR 180,186,0\nM 128,164,88\n"
"M 144,160,76\nR 182,186,4096\nM 144,164,92\nG 4\nM 148,168,76\nR 160,191,49152\n"
"Lq581:\ns_lshr_b32 s60, s45, 3\ns_lshl2_add_u32 s60, s60, s40\ns_lshl_b32 s60, s60, 8\nv_lshl_add_u32 v224, v252, 3, s60\nv_add_u32 v224, Lq581+4096-LqPC, v224\n"
"v_min_u32 v224, LqE-LqPC-4, v224\ns_mov_b64 exec, 3\nglobal_load_dword v224, v224, s[98:99]\ns_mov_b64 exec, -1\nM 148,172,92\nbuffer_store_dwordx4 v[56:59], v252, s[20:23], s100 offen offset:3072 sc1\n"
"M 132,168,72\nR 162,191,53248\nM 132,172,88\nM 116,168,68\nR 164,187,0\nM 116,172,84\n"
"M 100,168,64\nR 166,187,4096\nv_mov_b32 v248, 0\nM 100,172,80\nG 4\nv_mov_b32 v249, 0\n"
"M 104,176,64\nR 168,184,8192\ns_add_u32 s61, s74, 548864\ns_mov_b64 exec, 3\nbuffer_atomic_or_x2 v[248:249], v250, s[20:23], s61 offen sc0 sc1\ns_mov_b64 exec, -1\n"
"M 104,180,80\nM 120,176,68\nR 170,184,12288\nv_mov_b32 v242, v208\nM 120,180,84\ns_add_u32 m0, s50, 57344\n"
"v_mov_b32 v243, v209\nv_mov_b32 v244, v208\nM 136,176,72\nR 172,184,16384\nv_mov_b32 v245, v209\nM 136,180,88\n"
"s_add_u32 s63, s59, 327680\nv_mov_b32 v64, v192\nv_mov_b32 v65, v193\nM 152,176,76\nR 174,184,20480\nv_mov_b32 v70, v194\n"
"M 152,180,92\nG 4\nv_mov_b32 v71, v195\nv_mov_b32 v72, v196\nM 156,160,76\nR 176,185,8192\n"
"v_mov_b32 v73, v197\nM 156,164,92\nbuffer_load_dwordx4 v220, s[16:19], s63 offen lds\nM 140,160,72\nR 178,185,12288\nv_mov_b32 v78, v198\n"
"M 140,164,88\nv_mov_b32 v79, v199\nv_mov_b32 v80, v200\nM 124,160,68\nR 180,185,16384\nv_mov_b32 v81, v201\n"
"M 124,164,84\nbuffer_load_dwordx4 v221, s[16:19], s63 offen offset:1024 lds\nM 108,160,64\nR 182,185,20480\ns_add_u32 m0, s50, 65536\nM 108,164,80\n"
"G 4\nv_mov_b32 v86, v202\nv_mov_b32 v87, v203\nM 96,168,96\nR 160,186,8192\ns_add_u32 s63, s59, 851968\n"
"M 96,172,112\nbuffer_load_dwordx4 v220, s[16:19], s63 offen lds\nM 112,168,100\nR 162,186,12288\nv_mov_b32 v88, v204\nM 112,172,116\n"
"v_mov_b32 v89, v205\nv_mov_b32 v94, v206\nM 128,168,104\nR 164,186,16384\nv_mov_b32 v95, v207\nM 128,172,120\n"
"buffer_load_dwordx4 v221, s[16:19], s63 offen offset:1024 lds\nM 144,168,108\nR 166,186,20480\nv_mov_b32 v32, v232\nM 144,172,124\nG 4\n"
"v_mov_b32 v33, v233\nv_mov_b32 v38, v234\nM 148,176,108\nR 168,187,8192\nv_mov_b32 v39, v235\nM 148,180,124\n"
"s_movk_i32 s63, 6144\nv_mad_u32_u24 v220, v228, s63, v229\nv_add_u32 v221, 3144704, v220\ns_add_u32 m0, s50, 73728\ns_add_u32 s63, s52, 2048\nbuffer_load_dwordx4 v220, s[4:7], s63 offen lds\n"
"M 132,176,104\nR 170,187,12288\nv_mov_b32 v40, v236\nM 132,180,120\nv_mov_b32 v41, v237\nv_mov_b32 v46, v238\n"
"M 116,176,100\nR 172,187,16384\nv_mov_b32 v47, v239\nM 116,180,116\nv_mov_b32 v48, v232\nv_mov_b32 v49, v233\n"
"M 100,176,96\nR 174,187,20480\nv_mov_b32 v54, v234\nM 100,180,112\nG 4\nv_mov_b32 v55, v235\n"
"v_mov_b32 v56, v236\nM 104,160,96\nR 176,184,24576\ns_waitcnt vmcnt(13)\ns_barrier\nM 104,164,112\n"
"buffer_load_dwordx4 v221, s[4:7], s63 offen offset:1024 lds\nM 120,160,100\nR 178,184,28672\ns_add_u32 s63, s52, 198656\nM 120,164,116\ns_add_u32 m0, s50, 81920\n"
"v_mov_b32 v57, v237\nv_mov_b32 v62, v238\nM 136,160,104\nR 180,184,32768\nv_mov_b32 v63, v239\nM 136,164,120\n"
"buffer_load_dwordx4 v220, s[4:7], s63 offen lds\nM 152,160,108\nR 182,184,36864\nM 152,164,124\nG 4\nM 156,168,108\n"
"R 160,185,24576\nM 156,172,124\nbuffer_load_dwordx4 v221, s[4:7], s63 offen offset:1024 lds\nM 140,168,104\nR 162,185,28672\ns_add_u32 s63, s52, 4096\n"
"M 140,172,120\ns_add_u32 m0, s50, 90112\nM 124,168,100\nR 164,185,32768\nM 124,172,116\nbuffer_load_dwordx4 v220, s[4:7], s63 offen lds\n"
"M 108,168,96\nR 166,185,36864\nv_mov_b32 v226, s46\nM 108,172,112\nG 4\nv_mov_b32 v227, s47\n"
"M 96,176,128\nR 168,186,24576\ns_bfe_u32 s62, s58, 0x20007\ns_lshl_b32 s62, s62, 5\ns_add_u32 s61, s74, 548880\ns_add_u32 s61, s61, s62\n"
"s_waitcnt vmcnt(10)\ns_mov_b64 exec, 1\nbuffer_atomic_swap_x2 v[226:227], off, s[20:23], s61 sc1\ns_mov_b64 exec, -1\nM 96,180,144\ns_mul_i32 s64, s43, 49152\n"
"M 112,176,132\nR 170,186,28672\nM 112,180,148\ns_add_u32 s64, s64, s72\nM 128,176,136\nR 172,186,32768\n"
"s_waitcnt vmcnt(10)\nv_cmp_eq_u64 s[86:87], s[46:47], v[248:249]\ns_lshr_b32 s60, s45, 3\ns_lshl2_add_u32 s60, s60, s40\ns_lshl_b32 s60, s60, 8\nv_lshl_add_u32 v224, v252, 3, s60\n"
"v_add_u32 v224, LqM-LqPC, v224\nv_min_u32 v224, LqE-LqPC-4, v224\ns_mov_b64 exec, 3\nglobal_load_dword v224, v224, s[98:99]\ns_mov_b64 exec, -1\nM 128,180,152\n"
"s_add_u32 s64, s64, 52428800\nM 144,176,140\nR 174,186,36864\ns_and_b32 s62, s86, 3\ns_cmp_eq_u32 s62, 3\ns_cbranch_scc0 Lq506\n"
"Lq507:\nM 144,180,156\nG 4\nM 148,160,140\nR 176,187,24576\nM 148,164,156\n"
"buffer_load_dwordx4 v[2:5], v252, s[20:23], s64 offen offset:0 sc1\nM 132,160,136\nR 178,187,28672\nM 132,164,152\nbuffer_load_dwordx4 v[10:13], v252, s[20:23], s64 offen offset:1024 sc1\nM 116,160,132\n"
"R 180,187,32768\nM 116,164,148\ns_mul_i32 s65, s43, 49152\nM 100,160,128\nR 182,187,36864\nM 100,164,144\n"
"buffer_load_dwordx4 v[18:21], v252, s[20:23], s64 offen offset:2048 sc1\nG 4\nM 104,168,128\nR 160,184,40960\nM 104,172,144\nbuffer_load_dwordx4 v[26:29], v252, s[20:23], s64 offen offset:3072 sc1\n"
"M 120,168,132\nR 162,184,45056\nM 120,172,148\ns_add_u32 s65, s65, s72\nM 136,168,136\nR 164,184,49152\n"
"s_add_u32 s65, s65, 52445184\nM 136,172,152\nbuffer_load_dwordx4 v[66:69], v252, s[20:23], s65 offen offset:0 sc1\nM 152,168,140\nR 166,184,53248\nM 152,172,156\n"
"G 4\nM 156,176,140\nR 168,185,40960\ns_waitcnt vmcnt(12)\ns_barrier\nM 156,180,156\n"
"buffer_load_dwordx4 v[74:77], v252, s[20:23], s65 offen offset:1024 sc1\nM 140,176,136\nR 170,185,45056\nv_mov_b32 v246, 0\nM 140,180,152\nbuffer_load_dwordx4 v[82:85], v252, s[20:23], s65 offen offset:2048 sc1\n"
"v_mov_b32 v247, 0\nM 124,176,132\nR 172,185,49152\ns_add_u32 s61, s74, 548864\ns_mov_b64 exec, 4\nbuffer_atomic_or_x2 v[246:247], v250, s[20:23], s61 offen sc0 sc1\n"
"s_mov_b64 exec, -1\nM 124,180,148\nbuffer_load_dwordx4 v[90:93], v252, s[20:23], s65 offen offset:3072 sc1\nM 108,176,128\nR 174,185,53248\nM 108,180,144\n"
"G 4\nM 128,160,168\nR 176,186,40960\nM 128,164,184\nR 178,186,45056\nM 144,160,172\n"
"R 180,186,49152\nM 144,164,188\nR 182,186,53248\nG 4\nM 148,168,172\nR 160,187,40960\n"
"M 148,172,188\nR 162,187,45056\nM 132,168,168\nR 164,187,49152\nM 132,172,184\nR 166,187,53248\n"
"G 4\nM 136,176,168\nR 168,184,57344\nM 136,180,184\nR 170,184,61440\nM 152,176,172\n"
"R 172,188,0\nM 152,180,188\nR 174,188,4096\nG 4\nM 156,160,172\nR 176,185,57344\n"
"M 156,164,188\nR 178,185,61440\nM 140,160,168\nR 180,189,0\nM 140,164,184\nR 182,189,4096\n"
"G 4\nM 128,168,200\nR 160,186,57344\nM 128,172,216\nR 162,186,61440\nM 144,168,204\n"
"R 164,190,0\nM 144,172,220\nR 166,190,4096\nG 4\nM 148,176,204\nR 168,187,57344\n"
"M 148,180,220\nR 170,187,61440\nM 132,176,200\nR 172,191,0\nM 132,180,216\nR 174,191,4096\n"
"G 4\nM 136,160,200\nR 176,184,40960\nbuffer_load_dwordx4 v221, s[4:7], s63 offen offset:1024 lds\nM 136,164,216\nR 178,184,45056\n"
"s_add_u32 m0, s50, 98304\nM 152,160,204\nR 180,184,49152\ns_add_u32 s63, s52, 200704\nM 152,164,220\nR 182,184,53248\n"
"G 4\nM 156,168,204\nR 160,185,40960\nbuffer_load_dwordx4 v220, s[4:7], s63 offen lds\nM 156,172,220\nR 162,185,45056\n"
"M 140,168,200\nR 164,185,49152\nM 140,172,216\nR 166,185,53248\nG 4\nM 96,176,160\n"
"R 168,186,40960\nbuffer_load_dwordx4 v221, s[4:7], s63 offen offset:1024 lds\nM 96,180,176\nR 170,186,45056\ns_mov_b32 m0, s51\nM 112,176,164\n"
"R 172,186,49152\nv_cvt_pk_bf16_f32 v128, v128, v129\nv_cvt_pk_bf16_f32 v129, v130, v131\nM 112,180,180\nR 174,186,53248\nG 4\n"
"v_cvt_pk_bf16_f32 v130, v132, v133\nM 116,160,164\nR 176,187,40960\nLpg17:\ns_cbranch_execz LretW0z\ns_mul_i32 s66, s43, 49152\n"
"v_cvt_pk_bf16_f32 v131, v134, v135\nM 116,164,180\nR 178,187,45056\ns_add_u32 s66, s66, s72\nv_cvt_pk_bf16_f32 v132, v136, v137\nM 100,160,160\n"
"R 180,187,49152\ns_add_u32 s66, s66, 52461568\nv_cvt_pk_bf16_f32 v133, v138, v139\nM 100,164,176\nR 182,187,53248\nG 4\n"
"v_cvt_pk_bf16_f32 v134, v140, v141\nM 104,168,160\nR 160,184,57344\ns_waitcnt vmcnt(4)\nv_cmp_eq_u64 s[86:87], s[46:47], v[246:247]\nv_cvt_pk_bf16_f32 v135, v142, v143\n"
"M 104,172,176\nR 162,184,61440\nv_cvt_pk_bf16_f32 v136, v144, v145\nv_cvt_pk_bf16_f32 v137, v146, v147\nM 120,168,164\nR 164,188,0\n"
"s_and_b32 s62, s86, 4\ns_cmp_eq_u32 s62, 4\ns_cbranch_scc0 Lq522\nLq523:\nv_cvt_pk_bf16_f32 v138, v148, v149\nM 120,172,180\n"
"R 166,188,4096\nG 4\nv_cvt_pk_bf16_f32 v139, v150, v151\nM 124,176,164\nR 168,185,57344\nbuffer_load_dwordx4 v[34:37], v252, s[20:23], s66 offen offset:0 sc1\n"
"v_cvt_pk_bf16_f32 v140, v152, v153\nM 124,180,180\nR 170,185,61440\nv_cvt_pk_bf16_f32 v141, v154, v155\nv_cvt_pk_bf16_f32 v142, v156, v157\nM 108,176,160\n"
"R 172,189,0\nbuffer_load_dwordx4 v[42:45], v252, s[20:23], s66 offen offset:1024 sc1\nds_write_addtid_b32 v128 offset:0\nds_write_addtid_b32 v129 offset:256\nds_write_addtid_b32 v130 offset:512\nds_write_addtid_b32 v131 offset:768\n"
"ds_write_addtid_b32 v132 offset:1024\nds_write_addtid_b32 v133 offset:1280\nds_write_addtid_b32 v134 offset:1536\nds_write_addtid_b32 v135 offset:1792\nds_write_addtid_b32 v136 offset:2048\nds_write_addtid_b32 v137 offset:2304\n"
"ds_write_addtid_b32 v138 offset:2560\nds_write_addtid_b32 v139 offset:2816\nds_write_addtid_b32 v140 offset:3072\nds_write_addtid_b32 v141 offset:3328\nds_write_addtid_b32 v142 offset:3584\nv_cvt_pk_bf16_f32 v143, v158, v159\n"
"ds_write_addtid_b32 v143 offset:3840\nM 108,180,176\nR 174,189,4096\nG 15\nM 96,160,192\nR 176,186,57344\n"
"M 96,164,208\nR 178,186,61440\nbuffer_load_dwordx4 v[50:53], v252, s[20:23], s66 offen offset:2048 sc1\nG 3\ns_barrier\nM 112,160,196\n"
"R 180,190,0\ns_add_u32 m0, s50, 106496\nds_read2st64_b32 v[0:1], v225 offset0:0 offset1:1\nM 112,164,212\nR 182,190,4096\nds_read2st64_b32 v[6:7], v225 offset0:2 offset1:3\n"
"G 6\nM 116,168,196\nR 160,187,57344\nbuffer_load_dwordx4 v[58:61], v252, s[20:23], s66 offen offset:3072 sc1\nds_read2st64_b32 v[8:9], v225 offset0:4 offset1:5\nM 116,172,212\n"
"R 162,187,61440\ns_add_u32 s63, s52, 395264\nds_read2st64_b32 v[14:15], v225 offset0:6 offset1:7\nM 100,168,192\nR 164,191,0\nds_read2st64_b32 v[16:17], v225 offset0:8 offset1:9\n"
"M 100,172,208\nR 166,191,4096\nv_mfma_f32_16x16x32_bf16 v[96:99], v[242:245], v[64:67], v[96:99]\nbuffer_load_dwordx4 v220, s[4:7], s63 offen lds\nds_read2st64_b32 v[22:23], v225 offset0:10 offset1:11\nG 9\n"
"M 104,176,192\nbuffer_load_dwordx4 v221, s[4:7], s63 offen offset:1024 lds\nv_mfma_f32_16x16x32_bf16 v[100:103], v[242:245], v[68:71], v[100:103]\nds_read2st64_b32 v[24:25], v225 offset0:12 offset1:13\nds_read2st64_b32 v[30:31], v225 offset0:14 offset1:15\nM 104,180,208\n"
"s_add_u32 m0, s50, 114688\ns_add_u32 s63, s52, 591872\nv_mfma_f32_16x16x32_bf16 v[104:107], v[242:245], v[72:75], v[104:107]\nM 120,176,196\nbuffer_load_dwordx4 v220, s[4:7], s63 offen lds\nv_mfma_f32_16x16x32_bf16 v[108:111], v[242:245], v[76:79], v[108:111]\n"
"M 120,180,212\nG 3\nv_mfma_f32_16x16x32_bf16 v[112:115], v[242:245], v[80:83], v[112:115]\nM 124,160,196\nbuffer_load_dwordx4 v221, s[4:7], s63 offen offset:1024 lds\nv_mfma_f32_16x16x32_bf16 v[116:119], v[242:245], v[84:87], v[116:119]\n"
"M 124,164,212\ns_waitcnt vmcnt(11)\nv_mfma_f32_16x16x32_bf16 v[120:123], v[242:245], v[88:91], v[120:123]\nM 108,160,192\nv_mfma_f32_16x16x32_bf16 v[124:127], v[242:245], v[92:95], v[124:127]\nM 108,164,208\n"
"G 0\ns_barrier\ns_lshl_b32 s84, s75, 11\ns_add_u32 s84, s84, s49\ns_add_u32 s84, s84, 18874368\ns_add_u32 s85, s84, 32768\n"
"v_mov_b32 v238, s46\nv_mov_b32 v239, s47\nv_mfma_f32_16x16x32_bf16 v[96:99], v[242:245], v[0:3], v[96:99]\nv_mfma_f32_16x16x32_bf16 v[100:103], v[242:245], v[4:7], v[100:103]\nv_mfma_f32_16x16x32_bf16 v[104:107], v[242:245], v[8:11], v[104:107]\nv_mfma_f32_16x16x32_bf16 v[108:111], v[242:245], v[12:15], v[108:111]\n"
"v_mfma_f32_16x16x32_bf16 v[112:115], v[242:245], v[16:19], v[112:115]\nv_mfma_f32_16x16x32_bf16 v[116:119], v[242:245], v[20:23], v[116:119]\nv_mfma_f32_16x16x32_bf16 v[120:123], v[242:245], v[24:27], v[120:123]\nv_mfma_f32_16x16x32_bf16 v[124:127], v[242:245], v[28:31], v[124:127]\ns_waitcnt vmcnt(7)\nv_mfma_f32_16x16x32_bf16 v[96:99], v[242:245], v[32:35], v[96:99]\n"
"v_mfma_f32_16x16x32_bf16 v[100:103], v[242:245], v[36:39], v[100:103]\ns_waitcnt vmcnt(6)\nv_mfma_f32_16x16x32_bf16 v[104:107], v[242:245], v[40:43], v[104:107]\nv_mfma_f32_16x16x32_bf16 v[108:111], v[242:245], v[44:47], v[108:111]\ns_waitcnt vmcnt(5)\nv_mfma_f32_16x16x32_bf16 v[112:115], v[242:245], v[48:51], v[112:115]\n"
"v_mfma_f32_16x16x32_bf16 v[116:119], v[242:245], v[52:55], v[116:119]\ns_waitcnt vmcnt(4)\nv_mfma_f32_16x16x32_bf16 v[120:123], v[242:245], v[56:59], v[120:123]\nv_mfma_f32_16x16x32_bf16 v[124:127], v[242:245], v[60:63], v[124:127]\nv_cvt_pk_bf16_f32 v192, v96, v97\nv_cvt_pk_bf16_f32 v193, v98, v99\n"
"v_cvt_pk_bf16_f32 v194, v100, v101\nv_cvt_pk_bf16_f32 v195, v102, v103\nbuffer_store_dwordx4 v[192:195], v252, s[20:23], s84 offen offset:0 sc1\nv_cvt_pk_bf16_f32 v196, v104, v105\nv_cvt_pk_bf16_f32 v197, v106, v107\nv_cvt_pk_bf16_f32 v198, v108, v109\n"
"v_cvt_pk_bf16_f32 v199, v110, v111\nbuffer_store_dwordx4 v[196:199], v252, s[20:23], s84 offen offset:1024 sc1\nv_cvt_pk_bf16_f32 v200, v112, v113\nv_cvt_pk_bf16_f32 v201, v114, v115\nv_cvt_pk_bf16_f32 v202, v116, v117\nv_cvt_pk_bf16_f32 v203, v118, v119\n"
"buffer_store_dwordx4 v[200:203], v252, s[20:23], s85 offen offset:0 sc1\nv_cvt_pk_bf16_f32 v204, v120, v121\nv_cvt_pk_bf16_f32 v205, v122, v123\nv_cvt_pk_bf16_f32 v206, v124, v125\nv_cvt_pk_bf16_f32 v207, v126, v127\nbuffer_store_dwordx4 v[204:207], v252, s[20:23], s85 offen offset:1024 sc1\n"
"s_mov_b64 exec, 0\ns_cmp_eq_u32 s40, 1\ns_cbranch_scc1 Lpg19\ns_cmp_eq_u32 s40, 2\ns_cbranch_scc1 Lpg20\ns_cmp_eq_u32 s40, 3\n"
"s_cbranch_scc1 Lpg21\ns_branch Lpg18\nLretW0n:\ns_mov_b64 exec, -1\ns_branch LqM\nLq116:\n"
"s_lshl_b32 s61, s42, 13\ns_add_u32 s61, s61, 84017152\nbuffer_load_dwordx4 v[128:131], v230, s[20:23], s61 offen sc1\ns_lshl_b32 s61, s42, 13\ns_add_u32 s61, s61, 84021248\nbuffer_load_dwordx4 v[132:135], v230, s[20:23], s61 offen sc1\n"
"s_waitcnt vmcnt(0)\nv_min3_u32 v232, v129, v131, v133\nv_min3_u32 v232, v232, v135, v135\nv_max3_u32 v233, v129, v131, v133\nv_max3_u32 v233, v233, v135, v135\nv_cmp_ne_u32 s[86:87], s46, v232\n"
"v_cmp_ne_u32 vcc, s46, v233\ns_nop 0\ns_or_b64 s[86:87], s[86:87], vcc\ns_cbranch_scc0 Lq117\ns_sub_u32 s48, s48, 1\ns_cmp_gt_i32 s48, 0\n"
"s_cbranch_scc1 Lq116\ns_branch Lq117\nLq124:\ns_lshl_b32 s61, s42, 13\ns_add_u32 s61, s61, 84148224\nbuffer_load_dwordx4 v[144:147], v234, s[20:23], s61 offen sc1\n"
"s_lshl_b32 s61, s42, 13\ns_add_u32 s61, s61, 84152320\nbuffer_load_dwordx4 v[148:151], v234, s[20:23], s61 offen sc1\ns_waitcnt vmcnt(0)\nv_min3_u32 v236, v145, v147, v149\nv_min3_u32 v236, v236, v151, v151\n"
"v_max3_u32 v237, v145, v147, v149\nv_max3_u32 v237, v237, v151, v151\nv_cmp_ne_u32 s[86:87], s46, v236\nv_cmp_ne_u32 vcc, s46, v237\ns_nop 0\ns_or_b64 s[86:87], s[86:87], vcc\n"
"s_cbranch_scc0 Lq125\ns_sub_u32 s48, s48, 1\ns_cmp_gt_i32 s48, 0\ns_cbranch_scc1 Lq124\ns_branch Lq125\nLq132:\n"
"s_lshl_b32 s61, s42, 13\ns_add_u32 s61, s61, 83886080\nbuffer_load_dwordx4 v[128:131], v230, s[20:23], s61 offen sc1\ns_lshl_b32 s61, s42, 13\ns_add_u32 s61, s61, 83890176\nbuffer_load_dwordx4 v[132:135], v230, s[20:23], s61 offen sc1\n"
"s_waitcnt vmcnt(0)\nv_min3_u32 v232, v129, v131, v133\nv_min3_u32 v232, v232, v135, v135\nv_max3_u32 v233, v129, v131, v133\nv_max3_u32 v233, v233, v135, v135\nv_cmp_ne_u32 s[86:87], s46, v232\n"
"v_cmp_ne_u32 vcc, s46, v233\ns_nop 0\ns_or_b64 s[86:87], s[86:87], vcc\ns_cbranch_scc0 Lq133\ns_sub_u32 s48, s48, 1\ns_cmp_gt_i32 s48, 0\n"
"s_cbranch_scc1 Lq132\ns_branch Lq133\nLq140:\ns_lshr_b32 s84, s49, 4\ns_add_u32 s84, s84, 81788928\nbuffer_load_dwordx4 v[128:131], v252, s[20:23], s84 offen offset:0 sc1\n"
"buffer_load_dwordx4 v[132:135], v252, s[20:23], s84 offen offset:1024 sc1\nbuffer_load_dwordx4 v[136:139], v252, s[20:23], s84 offen offset:2048 sc1\nbuffer_load_dwordx4 v[140:143], v252, s[20:23], s84 offen offset:3072 sc1\ns_mov_b32 s85, 0x0c0c0400\ns_waitcnt vmcnt(3)\nv_perm_b32 v144, v129, v128, s85\n"
"v_perm_b32 v148, v131, v130, s85\nv_lshl_or_b32 v144, v148, 16, v144\ns_waitcnt vmcnt(2)\nv_perm_b32 v145, v133, v132, s85\nv_perm_b32 v149, v135, v134, s85\nv_lshl_or_b32 v145, v149, 16, v145\n"
"s_waitcnt vmcnt(1)\nv_perm_b32 v146, v137, v136, s85\nv_perm_b32 v150, v139, v138, s85\nv_lshl_or_b32 v146, v150, 16, v146\ns_waitcnt vmcnt(0)\nv_perm_b32 v147, v141, v140, s85\n"
"v_perm_b32 v151, v143, v142, s85\nv_lshl_or_b32 v147, v151, 16, v147\nv_min3_u32 v230, v144, v145, v146\nv_min3_u32 v230, v230, v147, v147\nv_max3_u32 v231, v144, v145, v146\nv_max3_u32 v231, v231, v147, v147\n"
"v_cmp_ne_u32 s[86:87], s46, v230\nv_cmp_ne_u32 vcc, s46, v231\ns_nop 0\ns_or_b64 s[86:87], s[86:87], vcc\ns_cbranch_scc0 Lq141\ns_sub_u32 s48, s48, 1\n"
"s_cmp_gt_i32 s48, 0\ns_cbranch_scc1 Lq140\ns_branch Lq141\nLq187:\ns_mov_b64 exec, s[66:67]\nbuffer_load_dwordx4 v[230:233], v252, s[20:23], s65 offen offset:0 sc1\n"
"buffer_load_dwordx2 v[64:65], v227, s[20:23], s65 offen offset:2048 sc1\nbuffer_load_dwordx2 v[70:71], v227, s[20:23], s65 offen offset:2560 sc1\ns_mov_b64 exec, -1\ns_waitcnt vmcnt(0)\nv_max3_u32 v248, v233, v65, v71\nv_cmp_eq_u32 vcc, -1, v248\n"
"s_nop 1\ns_cbranch_vccz Lq188\ns_sub_u32 s48, s48, 1\ns_cmp_gt_i32 s48, 0\ns_cbranch_scc1 Lq187\ns_branch Lq188\n"
"Lq189:\ns_mov_b64 exec, s[66:67]\nbuffer_load_dwordx4 v[234:237], v252, s[20:23], s65 offen offset:1024 sc1\nbuffer_load_dwordx2 v[76:77], v227, s[20:23], s65 offen offset:3072 sc1\nbuffer_load_dwordx2 v[82:83], v227, s[20:23], s65 offen offset:3584 sc1\ns_mov_b64 exec, -1\n"
"s_waitcnt vmcnt(0)\nv_max3_u32 v248, v237, v77, v83\nv_cmp_eq_u32 vcc, -1, v248\ns_nop 1\ns_cbranch_vccz Lq190\ns_sub_u32 s48, s48, 1\n"
"s_cmp_gt_i32 s48, 0\ns_cbranch_scc1 Lq189\ns_branch Lq190\nLq317:\ns_lshr_b32 s84, s49, 4\ns_add_u32 s84, s84, 82313216\n"
"buffer_load_dwordx4 v[0:3], v252, s[20:23], s84 offen offset:0 sc1\nbuffer_load_dwordx4 v[4:7], v252, s[20:23], s84 offen offset:1024 sc1\nbuffer_load_dwordx4 v[8:11], v252, s[20:23], s84 offen offset:2048 sc1\nbuffer_load_dwordx4 v[12:15], v252, s[20:23], s84 offen offset:3072 sc1\ns_mov_b32 s85, 0x0c0c0400\ns_waitcnt vmcnt(3)\n"
"v_perm_b32 v16, v1, v0, s85\nv_perm_b32 v20, v3, v2, s85\nv_lshl_or_b32 v16, v20, 16, v16\ns_waitcnt vmcnt(2)\nv_perm_b32 v17, v5, v4, s85\nv_perm_b32 v21, v7, v6, s85\n"
"v_lshl_or_b32 v17, v21, 16, v17\ns_waitcnt vmcnt(1)\nv_perm_b32 v18, v9, v8, s85\nv_perm_b32 v22, v11, v10, s85\nv_lshl_or_b32 v18, v22, 16, v18\ns_waitcnt vmcnt(0)\n"
"v_perm_b32 v19, v13, v12, s85\nv_perm_b32 v23, v15, v14, s85\nv_lshl_or_b32 v19, v23, 16, v19\nv_min3_u32 v230, v16, v17, v18\nv_min3_u32 v230, v230, v19, v19\nv_max3_u32 v231, v16, v17, v18\n"
"v_max3_u32 v231, v231, v19, v19\nv_cmp_ne_u32 s[86:87], s46, v230\nv_cmp_ne_u32 vcc, s46, v231\ns_nop 0\ns_or_b64 s[86:87], s[86:87], vcc\ns_cbranch_scc0 Lq318\n"
"s_sub_u32 s48, s48, 1\ns_cmp_gt_i32 s48, 0\ns_cbranch_scc1 Lq317\ns_branch Lq318\nLq319:\ns_lshl_b32 s61, s42, 13\n"
"s_add_u32 s61, s61, 84279296\nbuffer_load_dwordx4 v[0:3], v238, s[20:23], s61 offen sc1\ns_lshl_b32 s61, s42, 13\ns_add_u32 s61, s61, 84283392\nbuffer_load_dwordx4 v[4:7], v238, s[20:23], s61 offen sc1\ns_waitcnt vmcnt(0)\n"
"v_min3_u32 v240, v1, v3, v5\nv_min3_u32 v240, v240, v7, v7\nv_max3_u32 v241, v1, v3, v5\nv_max3_u32 v241, v241, v7, v7\nv_cmp_ne_u32 s[86:87], s46, v240\nv_cmp_ne_u32 vcc, s46, v241\n"
"s_nop 0\ns_or_b64 s[86:87], s[86:87], vcc\ns_cbranch_scc0 Lq320\ns_sub_u32 s48, s48, 1\ns_cmp_gt_i32 s48, 0\ns_cbranch_scc1 Lq319\n"
"s_branch Lq320\nLq329:\ns_lshl_b32 s61, s42, 13\ns_add_u32 s61, s61, 84410368\nbuffer_load_dwordx4 v[16:19], v242, s[20:23], s61 offen sc1\ns_lshl_b32 s61, s42, 13\n"
"s_add_u32 s61, s61, 84414464\nbuffer_load_dwordx4 v[20:23], v242, s[20:23], s61 offen sc1\ns_waitcnt vmcnt(0)\nv_min3_u32 v244, v17, v19, v21\nv_min3_u32 v244, v244, v23, v23\nv_max3_u32 v245, v17, v19, v21\n"
"v_max3_u32 v245, v245, v23, v23\nv_cmp_ne_u32 s[86:87], s46, v244\nv_cmp_ne_u32 vcc, s46, v245\ns_nop 0\ns_or_b64 s[86:87], s[86:87], vcc\ns_cbranch_scc0 Lq330\n"
"s_sub_u32 s48, s48, 1\ns_cmp_gt_i32 s48, 0\ns_cbranch_scc1 Lq329\ns_branch Lq330\nLq339:\ns_lshl_b32 s61, s42, 13\n"
"s_add_u32 s61, s61, 84541440\nbuffer_load_dwordx4 v[0:3], v238, s[20:23], s61 offen sc1\ns_lshl_b32 s61, s42, 13\ns_add_u32 s61, s61, 84545536\nbuffer_load_dwordx4 v[4:7], v238, s[20:23], s61 offen sc1\ns_waitcnt vmcnt(0)\n"
"v_min3_u32 v240, v1, v3, v5\nv_min3_u32 v240, v240, v7, v7\nv_max3_u32 v241, v1, v3, v5\nv_max3_u32 v241, v241, v7, v7\nv_cmp_ne_u32 s[86:87], s46, v240\nv_cmp_ne_u32 vcc, s46, v241\n"
"s_nop 0\ns_or_b64 s[86:87], s[86:87], vcc\ns_cbranch_scc0 Lq340\ns_sub_u32 s48, s48, 1\ns_cmp_gt_i32 s48, 0\ns_cbranch_scc1 Lq339\n"
"s_branch Lq340\nLq349:\ns_lshl_b32 s61, s42, 13\ns_add_u32 s61, s61, 84672512\nbuffer_load_dwordx4 v[16:19], v242, s[20:23], s61 offen sc1\ns_lshl_b32 s61, s42, 13\n"
"s_add_u32 s61, s61, 84676608\nbuffer_load_dwordx4 v[20:23], v242, s[20:23], s61 offen sc1\ns_waitcnt vmcnt(0)\nv_min3_u32 v244, v17, v19, v21\nv_min3_u32 v244, v244, v23, v23\nv_max3_u32 v245, v17, v19, v21\n"
"v_max3_u32 v245, v245, v23, v23\nv_cmp_ne_u32 s[86:87], s46, v244\nv_cmp_ne_u32 vcc, s46, v245\ns_nop 0\ns_or_b64 s[86:87], s[86:87], vcc\ns_cbranch_scc0 Lq350\n"
"s_sub_u32 s48, s48, 1\ns_cmp_gt_i32 s48, 0\ns_cbranch_scc1 Lq349\ns_branch Lq350\nLq377:\nv_mov_b32 v248, 0\n"
"v_mov_b32 v249, 0\ns_add_u32 s61, s82, 385024\ns_mov_b64 exec, 0xf\nbuffer_atomic_or_x2 v[248:249], v250, s[20:23], s61 offen sc0 sc1\ns_mov_b64 exec, -1\ns_waitcnt vmcnt(0)\n"
"v_cmp_eq_u64 s[86:87], s[46:47], v[248:249]\ns_nop 1\ns_and_b32 s62, s86, 0xf\ns_cmp_eq_u32 s62, 0xf\ns_cbranch_scc1 Lq378\ns_sleep 1\n"
"s_sub_u32 s48, s48, 1\ns_cmp_gt_i32 s48, 0\ns_cbranch_scc1 Lq377\ns_branch Lq378\nLq412:\nv_mov_b32 v248, 0\n"
"v_mov_b32 v249, 0\ns_add_u32 s61, s82, 385032\ns_mov_b64 exec, 0xf\nbuffer_atomic_or_x2 v[248:249], v250, s[20:23], s61 offen sc0 sc1\ns_mov_b64 exec, -1\ns_waitcnt vmcnt(0)\n"
"v_cmp_eq_u64 s[86:87], s[46:47], v[248:249]\ns_nop 1\ns_and_b32 s62, s86, 0xf\ns_cmp_eq_u32 s62, 0xf\ns_cbranch_scc1 Lq413\ns_sleep 1\n"
"s_sub_u32 s48, s48, 1\ns_cmp_gt_i32 s48, 0\ns_cbranch_scc1 Lq412\ns_branch Lq413\nLq447:\nv_mov_b32 v248, 0\n"
"v_mov_b32 v249, 0\ns_add_u32 s61, s82, 385048\ns_mov_b64 exec, 0xf\nbuffer_atomic_or_x2 v[248:249], v250, s[20:23], s61 offen sc0 sc1\ns_mov_b64 exec, -1\ns_waitcnt vmcnt(0)\n"
"v_cmp_eq_u64 s[86:87], s[46:47], v[248:249]\ns_nop 1\ns_and_b32 s62, s86, 0xf\ns_cmp_eq_u32 s62, 0xf\ns_cbranch_scc1 Lq448\ns_sleep 1\n"
"s_sub_u32 s48, s48, 1\ns_cmp_gt_i32 s48, 0\ns_cbranch_scc1 Lq447\ns_branch Lq448\nLq466:\nv_mov_b32 v248, 0\n"
"v_mov_b32 v249, 0\ns_add_u32 s61, s82, 385040\ns_mov_b64 exec, 0xf\nbuffer_atomic_or_x2 v[248:249], v250, s[20:23], s61 offen sc0 sc1\ns_mov_b64 exec, -1\ns_waitcnt vmcnt(0)\n"
"v_cmp_eq_u64 s[86:87], s[46:47], v[248:249]\ns_nop 1\ns_and_b32 s62, s86, 0xf\ns_cmp_eq_u32 s62, 0xf\ns_cbranch_scc1 Lq467\ns_sleep 1\n"
"s_sub_u32 s48, s48, 1\ns_cmp_gt_i32 s48, 0\ns_cbranch_scc1 Lq466\ns_branch Lq467\nLq506:\nv_mov_b32 v248, 0\n"
"v_mov_b32 v249, 0\ns_add_u32 s61, s74, 548864\ns_mov_b64 exec, 3\nbuffer_atomic_or_x2 v[248:249], v250, s[20:23], s61 offen sc0 sc1\ns_mov_b64 exec, -1\ns_waitcnt vmcnt(0)\n"
"v_cmp_eq_u64 s[86:87], s[46:47], v[248:249]\ns_nop 1\ns_and_b32 s62, s86, 3\ns_cmp_eq_u32 s62, 3\ns_cbranch_scc1 Lq507\ns_sleep 1\n"
"s_sub_u32 s48, s48, 1\ns_cmp_gt_i32 s48, 0\ns_cbranch_scc1 Lq506\ns_branch Lq507\nLq522:\nv_mov_b32 v246, 0\n"
"v_mov_b32 v247, 0\ns_add_u32 s61, s74, 548864\ns_mov_b64 exec, 4\nbuffer_atomic_or_x2 v[246:247], v250, s[20:23], s61 offen sc0 sc1\ns_mov_b64 exec, -1\ns_waitcnt vmcnt(0)\n"
"v_cmp_eq_u64 s[86:87], s[46:47], v[246:247]\ns_nop 1\ns_and_b32 s62, s86, 4\ns_cmp_eq_u32 s62, 4\ns_cbranch_scc1 Lq523\ns_sleep 1\n"
"s_sub_u32 s48, s48, 1\ns_cmp_gt_i32 s48, 0\ns_cbranch_scc1 Lq522\ns_branch Lq523\nLqM:\ns_add_u32 s61, s79, 88358912\n"
"s_lshl_b32 s62, s75, 3\ns_add_u32 s61, s61, s62\ns_waitcnt vmcnt(0)\ns_mov_b64 exec, 1\nbuffer_atomic_swap_x2 v[238:239], off, s[20:23], s61 sc1\ns_mov_b64 exec, -1\n"
"Lqq1:\nv_bfe_u32 v250, v252, 7, 1\nv_mul_u32_u24 v250, 2176, v250\nv_bfe_u32 v251, v252, 4, 3\nv_lshl_add_u32 v250, v251, 3, v250\ns_lshl_b32 s60, s90, 6\n"
"v_add_u32 v250, s60, v250\ns_sleep 3\nv_mov_b32 v248, 0\nv_mov_b32 v249, 0\ns_add_u32 s61, s80, 88358912\ns_mov_b64 exec, 0xffff\n"
"buffer_atomic_or_x2 v[248:249], v250, s[20:23], s61 offen sc0 sc1\ns_mov_b64 exec, -1\ns_add_u32 m0, s50, 0\ns_add_u32 s63, s52, 397312\nbuffer_load_dwordx4 v220, s[4:7], s63 offen lds\nbuffer_load_dwordx4 v221, s[4:7], s63 offen offset:1024 lds\n"
"s_add_u32 m0, s50, 8192\ns_add_u32 s63, s52, 593920\nbuffer_load_dwordx4 v220, s[4:7], s63 offen lds\nbuffer_load_dwordx4 v221, s[4:7], s63 offen offset:1024 lds\ns_add_u32 m0, s50, 16384\ns_add_u32 s63, s52, 788480\n"
"buffer_load_dwordx4 v220, s[4:7], s63 offen lds\nbuffer_load_dwordx4 v221, s[4:7], s63 offen offset:1024 lds\ns_add_u32 m0, s50, 24576\ns_add_u32 s63, s52, 985088\nbuffer_load_dwordx4 v220, s[4:7], s63 offen lds\nbuffer_load_dwordx4 v221, s[4:7], s63 offen offset:1024 lds\n"
"s_add_u32 m0, s50, 32768\ns_add_u32 s63, s52, 790528\nbuffer_load_dwordx4 v220, s[4:7], s63 offen lds\nbuffer_load_dwordx4 v221, s[4:7], s63 offen offset:1024 lds\ns_waitcnt vmcnt(10)\nv_cmp_eq_u64 s[86:87], s[46:47], v[248:249]\n"
"s_nop 1\ns_and_b32 s88, s86, 0xffff\ns_cmp_eq_u32 s88, 0xffff\ns_cbranch_scc1 Lq595\nv_mov_b32 v246, 0\nv_mov_b32 v247, 0\n"
"s_add_u32 s61, s80, 88358912\ns_mov_b64 exec, 0xffff\nbuffer_atomic_or_x2 v[246:247], v250, s[20:23], s61 offen sc0 sc1\ns_mov_b64 exec, -1\nLq595:\ns_add_u32 m0, s50, 40960\n"
"s_add_u32 s63, s52, 987136\nbuffer_load_dwordx4 v220, s[4:7], s63 offen lds\nbuffer_load_dwordx4 v221, s[4:7], s63 offen offset:1024 lds\ns_mov_b64 exec, 0\ns_cmp_eq_u32 s40, 1\ns_cbranch_scc1 LretQ1z\n"
"s_cmp_eq_u32 s40, 2\ns_cbranch_scc1 LretQ1z\ns_cmp_eq_u32 s40, 3\ns_cbranch_scc1 LretQ1z\ns_branch LretQ1z\nLretQ1z:\n"
"s_mov_b64 exec, -1\nv_mov_b32 v230, 0\nv_mov_b32 v231, 0\nv_mov_b32 v232, 0\nv_mov_b32 v233, 0\nv_dot2c_f32_bf16 v230, s76, v192\n"
"v_dot2c_f32_bf16 v231, v192, v192\nv_dot2c_f32_bf16 v232, s76, v200\nv_dot2c_f32_bf16 v233, v200, v200\nv_dot2c_f32_bf16 v230, s76, v193\nv_dot2c_f32_bf16 v231, v193, v193\nv_dot2c_f32_bf16 v232, s76, v201\n"
"v_dot2c_f32_bf16 v233, v201, v201\nv_dot2c_f32_bf16 v230, s76, v194\nv_dot2c_f32_bf16 v231, v194, v194\nv_dot2c_f32_bf16 v232, s76, v202\nv_dot2c_f32_bf16 v233, v202, v202\nv_dot2c_f32_bf16 v230, s76, v195\n"
"v_dot2c_f32_bf16 v231, v195, v195\nv_dot2c_f32_bf16 v232, s76, v203\nv_dot2c_f32_bf16 v233, v203, v203\nv_dot2c_f32_bf16 v230, s76, v196\nv_dot2c_f32_bf16 v231, v196, v196\nv_dot2c_f32_bf16 v232, s76, v204\n"
"v_dot2c_f32_bf16 v233, v204, v204\nv_dot2c_f32_bf16 v230, s76, v197\nv_dot2c_f32_bf16 v231, v197, v197\nv_dot2c_f32_bf16 v232, s76, v205\nv_dot2c_f32_bf16 v233, v205, v205\nv_dot2c_f32_bf16 v230, s76, v198\n"
"v_dot2c_f32_bf16 v231, v198, v198\nv_dot2c_f32_bf16 v232, s76, v206\nv_dot2c_f32_bf16 v233, v206, v206\nv_dot2c_f32_bf16 v230, s76, v199\nv_dot2c_f32_bf16 v231, v199, v199\nv_dot2c_f32_bf16 v232, s76, v207\n"
"v_dot2c_f32_bf16 v233, v207, v207\nv_lshrrev_b32 v234, 5, v252\nv_and_b32 v234, 24, v234\nv_bfe_u32 v235, s46, v234, 8\nv_lshrrev_b32 v234, 1, v234\nv_and_b32 v236, 0xf0, v252\n"
"v_add_u32 v234, v234, v236\nv_permlane32_swap_b32 v230, v231\nv_permlane32_swap_b32 v232, v233\nv_add_f32 v230, v230, v231\nv_add_f32 v232, v232, v233\ns_nop 1\n"
"v_permlane16_swap_b32 v230, v232\nv_add_f32 v230, v230, v232\ns_movk_i32 s62, 0xff\nv_bfi_b32 v230, s62, v235, v230\ns_lshr_b32 s61, s49, 4\ns_lshl1_add_u32 s61, s53, s61\n"
"s_add_u32 s61, s61, 82837504\nbuffer_store_dword v230, v234, s[20:23], s61 offen sc1\ns_cmp_eq_u32 s88, 0xffff\ns_cbranch_scc1 Lq596\ns_waitcnt vmcnt(3)\nv_mov_b32 v248, v246\n"
"v_mov_b32 v249, v247\nv_cmp_eq_u64 s[86:87], s[46:47], v[248:249]\ns_nop 1\ns_and_b32 s62, s86, 0xffff\ns_cmp_eq_u32 s62, 0xffff\ns_cbranch_scc1 Lq587\n"
"Lq586:\nv_mov_b32 v248, 0\nv_mov_b32 v249, 0\ns_add_u32 s61, s80, 88358912\ns_mov_b64 exec, 0xffff\nbuffer_atomic_or_x2 v[248:249], v250, s[20:23], s61 offen sc0 sc1\n"
"s_mov_b64 exec, -1\ns_waitcnt vmcnt(0)\nv_cmp_eq_u64 s[86:87], s[46:47], v[248:249]\ns_nop 1\ns_and_b32 s62, s86, 0xffff\ns_cmp_eq_u32 s62, 0xffff\n"
"s_cbranch_scc1 Lq587\ns_sub_u32 s48, s48, 1\ns_cmp_gt_i32 s48, 0\ns_cbranch_scc1 Lq586\nLq587:\nLq596:\n"
"s_add_u32 s64, s70, 18874368\nbuffer_load_dwordx4 a[0:3], v252, s[20:23], s64 offen offset:0 sc1\ns_add_u32 s65, s70, 18907136\nbuffer_load_dwordx4 a[4:7], v252, s[20:23], s65 offen offset:0 sc1\ns_add_u32 s66, s71, 18874368\nbuffer_load_dwordx4 a[8:11], v252, s[20:23], s66 offen offset:0 sc1\n"
"s_add_u32 s67, s71, 18907136\nbuffer_load_dwordx4 a[12:15], v252, s[20:23], s67 offen offset:0 sc1\nbuffer_load_dwordx4 a[16:19], v252, s[20:23], s64 offen offset:1024 sc1\nbuffer_load_dwordx4 a[20:23], v252, s[20:23], s65 offen offset:1024 sc1\nbuffer_load_dwordx4 a[24:27], v252, s[20:23], s66 offen offset:1024 sc1\nbuffer_load_dwordx4 a[28:31], v252, s[20:23], s67 offen offset:1024 sc1\n"
"s_barrier\nR 160,188,8192\nLpg18:\ns_cbranch_execz Lpg22\nR 162,188,12288\nR 164,188,16384\n"
"R 166,188,20480\nR 168,189,8192\nR 170,189,12288\nR 172,189,16384\nR 174,189,20480\nR 176,190,8192\n"
"R 178,190,12288\nR 180,190,16384\nR 182,190,20480\nbuffer_load_dwordx4 a[32:35], v252, s[20:23], s64 offen offset:2048 sc1\nbuffer_load_dwordx4 a[36:39], v252, s[20:23], s65 offen offset:2048 sc1\nbuffer_load_dwordx4 a[40:43], v252, s[20:23], s66 offen offset:2048 sc1\n"
"buffer_load_dwordx4 a[44:47], v252, s[20:23], s67 offen offset:2048 sc1\nbuffer_load_dwordx4 a[48:51], v252, s[20:23], s64 offen offset:3072 sc1\nbuffer_load_dwordx4 a[52:55], v252, s[20:23], s65 offen offset:3072 sc1\nbuffer_load_dwordx4 a[56:59], v252, s[20:23], s66 offen offset:3072 sc1\nbuffer_load_dwordx4 a[60:63], v252, s[20:23], s67 offen offset:3072 sc1\ns_waitcnt vmcnt(15) lgkmcnt(10)\n"
"v_mfma_f32_16x16x32_bf16 v[0:3], v[160:163], a[0:3], 0\ns_add_u32 s64, s70, 18878464\nbuffer_load_dwordx4 a[64:67], v252, s[20:23], s64 offen offset:0 sc1\ns_waitcnt vmcnt(12) lgkmcnt(8)\nM 0,164,16\nv_mfma_f32_16x16x32_bf16 v[16:19], v[160:163], a[4:7], 0\n"
"s_waitcnt vmcnt(11)\nM 16,164,20\nv_mfma_f32_16x16x32_bf16 v[32:35], v[160:163], a[8:11], 0\ns_waitcnt vmcnt(10)\nM 32,164,24\nv_mfma_f32_16x16x32_bf16 v[48:51], v[160:163], a[12:15], 0\n"
"s_waitcnt vmcnt(9)\nM 48,164,28\nG 4\nv_mfma_f32_16x16x32_bf16 v[52:55], v[168:171], a[12:15], 0\nR 160,191,8192\nM 52,172,28\n"
"s_add_u32 s65, s70, 18911232\nbuffer_load_dwordx4 a[68:71], v252, s[20:23], s65 offen offset:0 sc1\nv_mfma_f32_16x16x32_bf16 v[36:39], v[168:171], a[8:11], 0\nR 162,191,12288\nM 36,172,24\nv_mfma_f32_16x16x32_bf16 v[20:23], v[168:171], a[4:7], 0\n"
"R 164,191,16384\nM 20,172,20\nv_mfma_f32_16x16x32_bf16 v[4:7], v[168:171], a[0:3], 0\nR 166,191,20480\nM 4,172,16\nG 4\n"
"v_mfma_f32_16x16x32_bf16 v[8:11], v[176:179], a[0:3], 0\nR 168,188,24576\nM 8,180,16\ns_add_u32 s66, s71, 18878464\nbuffer_load_dwordx4 a[72:75], v252, s[20:23], s66 offen offset:0 sc1\nv_mfma_f32_16x16x32_bf16 v[24:27], v[176:179], a[4:7], 0\n"
"R 170,188,28672\nM 24,180,20\ns_add_u32 m0, s50, 49152\nv_mfma_f32_16x16x32_bf16 v[40:43], v[176:179], a[8:11], 0\nR 172,188,32768\ns_add_u32 s63, s52, 1181696\n"
"M 40,180,24\ns_add_u32 s67, s71, 18911232\nbuffer_load_dwordx4 a[76:79], v252, s[20:23], s67 offen offset:0 sc1\nv_mfma_f32_16x16x32_bf16 v[56:59], v[176:179], a[12:15], 0\nR 174,188,36864\nM 56,180,28\n"
"G 4\nv_mfma_f32_16x16x32_bf16 v[60:63], v[160:163], a[12:15], 0\nR 176,189,24576\nM 60,164,28\nbuffer_load_dwordx4 v220, s[4:7], s63 offen lds\nv_mfma_f32_16x16x32_bf16 v[44:47], v[160:163], a[8:11], 0\n"
"R 178,189,28672\nM 44,164,24\nv_mfma_f32_16x16x32_bf16 v[28:31], v[160:163], a[4:7], 0\nR 180,189,32768\nM 28,164,20\nbuffer_load_dwordx4 a[80:83], v252, s[20:23], s64 offen offset:1024 sc1\n"
"v_mfma_f32_16x16x32_bf16 v[12:15], v[160:163], a[0:3], 0\nR 182,189,36864\nM 12,164,16\nG 4\nv_mfma_f32_16x16x32_bf16 v[64:67], a[0:3], v[168:171], 0\nR 160,190,24576\n"
"N 64,16,172\nv_mfma_f32_16x16x32_bf16 v[68:71], a[4:7], v[168:171], 0\nR 162,190,28672\nN 68,20,172\nbuffer_load_dwordx4 a[84:87], v252, s[20:23], s65 offen offset:1024 sc1\nv_mfma_f32_16x16x32_bf16 v[96:99], a[8:11], v[168:171], 0\n"
"R 164,190,32768\nN 96,24,172\nv_mfma_f32_16x16x32_bf16 v[100:103], a[12:15], v[168:171], 0\nR 166,190,36864\nN 100,28,172\ns_barrier\n"
"G 4\nv_mfma_f32_16x16x32_bf16 v[108:111], a[12:15], v[176:179], 0\nR 168,191,24576\nN 108,28,180\nbuffer_load_dwordx4 v221, s[4:7], s63 offen offset:1024 lds\nv_mfma_f32_16x16x32_bf16 v[104:107], a[8:11], v[176:179], 0\n"
"R 170,191,28672\nN 104,24,180\nbuffer_load_dwordx4 a[88:91], v252, s[20:23], s66 offen offset:1024 sc1\nv_mfma_f32_16x16x32_bf16 v[76:79], a[4:7], v[176:179], 0\nR 172,191,32768\nN 76,20,180\n"
"v_mfma_f32_16x16x32_bf16 v[72:75], a[0:3], v[176:179], 0\nR 174,191,36864\nN 72,16,180\nG 4\nv_mfma_f32_16x16x32_bf16 v[80:83], a[0:3], v[160:163], 0\nR 176,188,40960\n"
"s_add_u32 s63, s52, 1378304\nN 80,16,164\nbuffer_load_dwordx4 a[92:95], v252, s[20:23], s67 offen offset:1024 sc1\nv_mfma_f32_16x16x32_bf16 v[84:87], a[4:7], v[160:163], 0\nR 178,188,45056\nN 84,20,164\n"
"s_add_u32 m0, s50, 57344\nv_mfma_f32_16x16x32_bf16 v[112:115], a[8:11], v[160:163], 0\nR 180,188,49152\nN 112,24,164\nbuffer_load_dwordx4 v220, s[4:7], s63 offen lds\nv_mfma_f32_16x16x32_bf16 v[116:119], a[12:15], v[160:163], 0\n"
"R 182,188,53248\nN 116,28,164\nG 4\nv_mfma_f32_16x16x32_bf16 v[124:127], a[12:15], v[168:171], 0\nR 160,189,40960\nN 124,28,172\n"
"buffer_load_dwordx4 a[96:99], v252, s[20:23], s64 offen offset:2048 sc1\nv_mfma_f32_16x16x32_bf16 v[120:123], a[8:11], v[168:171], 0\nR 162,189,45056\nN 120,24,172\nv_mfma_f32_16x16x32_bf16 v[92:95], a[4:7], v[168:171], 0\nR 164,189,49152\n"
"N 92,20,172\nv_mfma_f32_16x16x32_bf16 v[88:91], a[0:3], v[168:171], 0\nR 166,189,53248\nN 88,16,172\nG 4\ns_waitcnt vmcnt(19)\n"
"M 0,176,32\nR 168,190,40960\ns_waitcnt vmcnt(15)\nM 0,180,48\nbuffer_load_dwordx4 a[100:103], v252, s[20:23], s65 offen offset:2048 sc1\nM 16,176,36\n"
"R 170,190,45056\ns_waitcnt vmcnt(15)\nM 16,180,52\nM 32,176,40\nR 172,190,49152\ns_waitcnt vmcnt(14)\n"
"M 32,180,56\nbuffer_load_dwordx4 v221, s[4:7], s63 offen offset:1024 lds\nM 48,176,44\nR 174,190,53248\ns_waitcnt vmcnt(14)\nM 48,180,60\n"
"s_barrier\nG 4\nM 52,160,44\nR 176,191,40960\nM 52,164,60\nbuffer_load_dwordx4 a[104:107], v252, s[20:23], s66 offen offset:2048 sc1\n"
"M 36,160,40\nR 178,191,45056\nM 36,164,56\ns_add_u32 m0, s50, 65536\nM 20,160,36\nR 180,191,49152\n"
"s_add_u32 s63, s52, 1183744\nM 20,164,52\nbuffer_load_dwordx4 a[108:111], v252, s[20:23], s67 offen offset:2048 sc1\nM 4,160,32\nR 182,191,53248\nM 4,164,48\n"
"G 4\nM 8,168,32\nR 160,184,0\nM 8,172,48\nbuffer_load_dwordx4 v220, s[4:7], s63 offen lds\nM 24,168,36\n"
"R 162,184,4096\nM 24,172,52\nM 40,168,40\nR 164,184,8192\nM 40,172,56\nbuffer_load_dwordx4 a[112:115], v252, s[20:23], s64 offen offset:3072 sc1\n"
"M 56,168,44\nR 166,184,12288\nM 56,172,60\nG 4\nM 60,176,44\nR 168,185,0\n"
"M 60,180,60\nM 44,176,40\nR 170,185,4096\nM 44,180,56\nbuffer_load_dwordx4 a[116:119], v252, s[20:23], s65 offen offset:3072 sc1\nM 28,176,36\n"
"R 172,185,8192\nM 28,180,52\nM 12,176,32\nR 174,185,12288\nM 12,180,48\nG 4\n"
"N 64,32,160\nR 176,186,0\nN 64,48,164\nbuffer_load_dwordx4 v221, s[4:7], s63 offen offset:1024 lds\nN 68,36,160\nR 178,186,4096\n"
"N 68,52,164\nbuffer_load_dwordx4 a[120:123], v252, s[20:23], s66 offen offset:3072 sc1\nN 96,40,160\nR 180,186,8192\nN 96,56,164\ns_lshr_b32 s84, s49, 4\n"
"N 100,44,160\nR 182,186,12288\ns_add_u32 s84, s84, 82837504\nN 100,60,164\ns_barrier\nG 4\n"
"N 108,44,168\nR 160,187,0\nN 108,60,172\nbuffer_load_dwordx4 a[124:127], v252, s[20:23], s67 offen offset:3072 sc1\nN 104,40,168\nR 162,187,4096\n"
"N 104,56,172\nbuffer_load_dwordx4 v[128:131], v252, s[20:23], s84 offen offset:0 sc1\nN 76,36,168\nR 164,187,8192\nN 76,52,172\nN 72,32,168\n"
"R 166,187,12288\nN 72,48,172\nbuffer_load_dwordx4 v[132:135], v252, s[20:23], s84 offen offset:1024 sc1\nG 4\nN 80,32,176\nR 168,184,16384\n"
"N 80,48,180\ns_add_u32 s64, s70, 18882560\nbuffer_load_dwordx4 a[128:131], v252, s[20:23], s64 offen offset:0 sc1\nN 84,36,176\nR 170,184,20480\nN 84,52,180\n"
"buffer_load_dwordx4 v[136:139], v252, s[20:23], s84 offen offset:2048 sc1\nN 112,40,176\nR 172,184,24576\nN 112,56,180\nN 116,44,176\nR 174,184,28672\n"
"N 116,60,180\nbuffer_load_dwordx4 v[140:143], v252, s[20:23], s84 offen offset:3072 sc1\nG 4\nN 124,44,160\nR 176,185,16384\nN 124,60,164\n"
"s_add_u32 s65, s70, 18915328\nbuffer_load_dwordx4 a[132:135], v252, s[20:23], s65 offen offset:0 sc1\nN 120,40,160\nR 178,185,20480\nN 120,56,164\nN 92,36,160\n"
"R 180,185,24576\nN 92,52,164\nN 88,32,160\nR 182,185,28672\nN 88,48,164\nG 4\n"
"s_waitcnt vmcnt(27)\nM 0,168,64\nR 160,186,16384\ns_waitcnt vmcnt(22)\nM 0,172,80\ns_add_u32 s66, s71, 18882560\n"
"buffer_load_dwordx4 a[136:139], v252, s[20:23], s66 offen offset:0 sc1\nM 16,168,68\nR 162,186,20480\ns_waitcnt vmcnt(22)\nM 16,172,84\nM 32,168,72\n"
"R 164,186,24576\ns_waitcnt vmcnt(20)\nM 32,172,88\ns_add_u32 s67, s71, 18915328\nbuffer_load_dwordx4 a[140:143], v252, s[20:23], s67 offen offset:0 sc1\nM 48,168,76\n"
"R 166,186,28672\ns_waitcnt vmcnt(20)\nM 48,172,92\ns_barrier\nG 4\nM 52,176,76\n"
"R 168,187,16384\nM 52,180,92\nM 36,176,72\nR 170,187,20480\nM 36,180,88\ns_add_u32 m0, s50, 73728\n"
"M 20,176,68\nR 172,187,24576\ns_add_u32 s63, s52, 1380352\nM 20,180,84\nbuffer_load_dwordx4 a[144:147], v252, s[20:23], s64 offen offset:1024 sc1\nM 4,176,64\n"
"R 174,187,28672\nM 4,180,80\nG 4\nM 8,160,64\nR 176,184,32768\nM 8,164,80\n"
"buffer_load_dwordx4 v220, s[4:7], s63 offen lds\nM 24,160,68\nR 178,184,36864\nM 24,164,84\nbuffer_load_dwordx4 a[148:151], v252, s[20:23], s65 offen offset:1024 sc1\nM 40,160,72\n"
"R 180,184,40960\nM 40,164,88\nbuffer_load_dwordx4 v221, s[4:7], s63 offen offset:1024 lds\nM 56,160,76\nR 182,184,45056\nM 56,164,92\n"
"G 4\nM 60,168,76\nR 160,185,32768\nM 60,172,92\nM 44,168,72\nR 162,185,36864\n"
"M 44,172,88\nbuffer_load_dwordx4 a[152:155], v252, s[20:23], s66 offen offset:1024 sc1\nM 28,168,68\nR 164,185,40960\nM 28,172,84\nM 12,168,64\n"
"R 166,185,45056\nM 12,172,80\nG 4\nN 64,64,176\nR 168,186,32768\nN 64,80,180\n"
"buffer_load_dwordx4 a[156:159], v252, s[20:23], s67 offen offset:1024 sc1\nN 68,68,176\nR 170,186,36864\nN 68,84,180\nN 96,72,176\nR 172,186,40960\n"
"s_mov_b32 s85, 0x0c0c0400\nN 96,88,180\ns_add_u32 m0, s50, 81920\ns_waitcnt vmcnt(13)\nv_perm_b32 v144, v129, v128, s85\nv_perm_b32 v148, v131, v130, s85\n"
"N 100,76,176\nR 174,186,45056\ns_add_u32 s63, s52, 1574912\nN 100,92,180\ns_barrier\nG 4\n"
"N 108,76,160\nR 176,187,32768\nv_lshl_or_b32 v144, v148, 16, v144\nN 108,92,164\nbuffer_load_dwordx4 a[160:163], v252, s[20:23], s64 offen offset:2048 sc1\nN 104,72,160\n"
"R 178,187,36864\ns_waitcnt vmcnt(13)\nv_perm_b32 v145, v133, v132, s85\nN 104,88,164\nbuffer_load_dwordx4 v220, s[4:7], s63 offen lds\nN 76,68,160\n"
"R 180,187,40960\nv_perm_b32 v149, v135, v134, s85\nN 76,84,164\nbuffer_load_dwordx4 v221, s[4:7], s63 offen offset:1024 lds\nN 72,64,160\nR 182,187,45056\n"
"s_add_u32 m0, s50, 90112\nN 72,80,164\nG 4\nv_lshl_or_b32 v145, v149, 16, v145\ns_waitcnt vmcnt(13)\nv_perm_b32 v146, v137, v136, s85\n"
"N 80,64,168\nR 160,184,49152\ns_add_u32 s63, s52, 1771520\nN 80,80,172\nbuffer_load_dwordx4 a[164:167], v252, s[20:23], s65 offen offset:2048 sc1\nN 84,68,168\n"
"R 162,184,53248\nv_perm_b32 v150, v139, v138, s85\nN 84,84,172\nbuffer_load_dwordx4 v220, s[4:7], s63 offen lds\nN 112,72,168\nR 164,184,57344\n"
"v_lshl_or_b32 v146, v150, 16, v146\nN 112,88,172\nbuffer_load_dwordx4 v221, s[4:7], s63 offen offset:1024 lds\nN 116,76,168\nR 166,184,61440\ns_waitcnt vmcnt(15)\n"
"v_perm_b32 v147, v141, v140, s85\nN 116,92,172\nG 4\nv_perm_b32 v151, v143, v142, s85\nv_lshl_or_b32 v147, v151, 16, v147\nN 124,76,176\n"
"R 168,185,49152\nv_min3_u32 v230, v144, v145, v146\nN 124,92,180\nbuffer_load_dwordx4 a[168:171], v252, s[20:23], s66 offen offset:2048 sc1\nN 120,72,176\nR 170,185,53248\n"
"v_min3_u32 v230, v230, v147, v147\nN 120,88,180\nv_max3_u32 v231, v144, v145, v146\nv_max3_u32 v231, v231, v147, v147\nN 92,68,176\nR 172,185,57344\n"
"v_cmp_ne_u32 s[86:87], s46, v230\nN 92,84,180\nbuffer_load_dwordx4 a[172:175], v252, s[20:23], s67 offen offset:2048 sc1\nN 88,64,176\nR 174,185,61440\nv_cmp_ne_u32 vcc, s46, v231\n"
"N 88,80,180\ns_or_b64 s[86:87], s[86:87], vcc\ns_cbranch_scc1 Lq645\nLq646:\nG 4\nM 0,160,96\n"
"R 176,186,49152\nv_and_b32 v128, 0xffffff00, v128\nM 0,164,112\nv_and_b32 v129, 0xffffff00, v129\nv_and_b32 v130, 0xffffff00, v130\nM 16,160,100\n"
"R 178,186,53248\nv_and_b32 v131, 0xffffff00, v131\nM 16,164,116\ns_add_u32 m0, s50, 98304\nv_and_b32 v132, 0xffffff00, v132\nv_and_b32 v133, 0xffffff00, v133\n"
"M 32,160,104\nR 180,186,57344\ns_add_u32 s63, s52, 1576960\nM 32,164,120\nbuffer_load_dwordx4 a[176:179], v252, s[20:23], s64 offen offset:3072 sc1\nM 48,160,108\n"
"R 182,186,61440\nv_and_b32 v134, 0xffffff00, v134\nM 48,164,124\ns_waitcnt vmcnt(11)\ns_barrier\nG 4\n"
"M 52,168,108\nR 160,187,49152\nv_and_b32 v135, 0xffffff00, v135\nM 52,172,124\nbuffer_load_dwordx4 v220, s[4:7], s63 offen lds\nM 36,168,104\n"
"R 162,187,53248\nv_and_b32 v136, 0xffffff00, v136\nM 36,172,120\nbuffer_load_dwordx4 a[180:183], v252, s[20:23], s65 offen offset:3072 sc1\nM 20,168,100\nR 164,187,57344\n"
"v_and_b32 v137, 0xffffff00, v137\nM 20,172,116\nbuffer_load_dwordx4 v221, s[4:7], s63 offen offset:1024 lds\nM 4,168,96\nR 166,187,61440\ns_add_u32 m0, s50, 106496\n"
"M 4,172,112\nG 4\nv_and_b32 v138, 0xffffff00, v138\nv_and_b32 v139, 0xffffff00, v139\nM 8,176,96\nR 168,188,0\n"
"s_add_u32 s63, s52, 1773568\nM 8,180,112\nbuffer_load_dwordx4 v220, s[4:7], s63 offen lds\nM 24,176,100\nR 170,188,4096\nv_and_b32 v140, 0xffffff00, v140\n"
"M 24,180,116\nbuffer_load_dwordx4 a[184:187], v252, s[20:23], s66 offen offset:3072 sc1\nM 40,176,104\nR 172,188,8192\nv_and_b32 v141, 0xffffff00, v141\nM 40,180,120\n"
"buffer_load_dwordx4 v221, s[4:7], s63 offen offset:1024 lds\nM 56,176,108\nR 174,188,12288\nv_and_b32 v142, 0xffffff00, v142\nM 56,180,124\nG 4\n"
"v_and_b32 v143, 0xffffff00, v143\nv_add_f32 v128, v128, v136\nM 60,160,108\nR 176,189,0\nv_add_f32 v129, v129, v137\nM 60,164,124\n"
"buffer_load_dwordx4 a[188:191], v252, s[20:23], s67 offen offset:3072 sc1\nM 44,160,104\nR 178,189,4096\nv_add_f32 v130, v130, v138\nM 44,164,120\nv_add_f32 v131, v131, v139\n"
"v_add_f32 v132, v132, v140\nM 28,160,100\nR 180,189,8192\nv_add_f32 v133, v133, v141\nM 28,164,116\nv_add_f32 v134, v134, v142\n"
"v_add_f32 v135, v135, v143\nM 12,160,96\nR 182,189,12288\nv_add_f32 v128, v128, v132\nM 12,164,112\nG 4\n"
"v_add_f32 v129, v129, v133\nv_add_f32 v130, v130, v134\nN 64,96,168\nR 160,190,0\nv_add_f32 v131, v131, v135\nN 64,112,172\n"
"s_add_u32 s64, s70, 18886656\nbuffer_load_dwordx4 a[192:195], v252, s[20:23], s64 offen offset:0 sc1\nN 68,100,168\nR 162,190,4096\nv_permlane32_swap_b32 v128, v130\nN 68,116,172\n"
"v_permlane32_swap_b32 v129, v131\nv_add_f32 v128, v128, v130\nN 96,104,168\nR 164,190,8192\nv_add_f32 v129, v129, v131\nN 96,120,172\n"
"s_add_u32 m0, s50, 114688\nLpg19:\ns_cbranch_execz LretW0n\nv_permlane16_swap_b32 v128, v129\nv_add_f32 v230, v128, v129\nN 100,108,168\n"
"R 166,190,12288\ns_add_u32 s63, s52, 1968128\nN 100,124,172\ns_waitcnt vmcnt(11)\ns_barrier\nG 4\n"
"N 108,108,176\nR 168,191,0\nv_mov_b32 v232, v230\nN 108,124,180\ns_add_u32 s65, s70, 18919424\nbuffer_load_dwordx4 a[196:199], v252, s[20:23], s65 offen offset:0 sc1\n"
"N 104,104,176\nR 170,191,4096\nv_permlane16_swap_b32 v230, v232\nN 104,120,180\nbuffer_load_dwordx4 v220, s[4:7], s63 offen lds\nN 76,100,176\n"
"R 172,191,8192\nv_mov_b32 v231, v230\nN 76,116,180\nbuffer_load_dwordx4 v221, s[4:7], s63 offen offset:1024 lds\nN 72,96,176\nR 174,191,12288\n"
"s_add_u32 m0, s50, 0\nN 72,112,180\nG 4\nv_mov_b32 v233, v232\nv_permlane32_swap_b32 v230, v231\nN 80,96,160\n"
"R 176,188,16384\ns_add_u32 s63, s52, 2164736\nN 80,112,164\ns_add_u32 s66, s71, 18886656\nbuffer_load_dwordx4 a[200:203], v252, s[20:23], s66 offen offset:0 sc1\nN 84,100,160\n"
"R 178,188,20480\nv_permlane32_swap_b32 v232, v233\nN 84,116,164\nbuffer_load_dwordx4 v220, s[4:7], s63 offen lds\nN 112,104,160\nR 180,188,24576\n"
"v_mul_f32 v230, 0xba800000, v230\nN 112,120,164\ns_add_u32 s67, s71, 18919424\nbuffer_load_dwordx4 a[204:207], v252, s[20:23], s67 offen offset:0 sc1\nN 116,108,160\nR 182,188,28672\n"
"v_mul_f32 v214, v230, v230\nN 116,124,164\nG 4\nv_mul_f32 v231, 0x3a800000, v231\nv_sub_f32 v231, v231, v214\nN 124,108,168\n"
"R 160,189,16384\nbuffer_load_dwordx4 v221, s[4:7], s63 offen offset:1024 lds\nN 124,124,172\nv_max_f32 v231, 0, v231\nv_add_f32 v231, 0x3727c5ac, v231\nN 120,104,168\n"
"R 162,189,20480\nv_and_b32 v208, s77, v230\nN 120,120,172\nv_rsq_f32 v234, v231\nN 92,100,168\nR 164,189,24576\n"
"v_mul_f32 v216, v231, v234\nN 92,116,172\nbuffer_load_dwordx4 a[208:211], v252, s[20:23], s64 offen offset:1024 sc1\nN 88,96,168\nR 166,189,28672\nv_sub_f32 v230, v230, v208\n"
"N 88,112,172\nG 4\nv_cvt_pk_bf16_f32 v209, v230, v216\nv_cvt_pk_bf16_f32 v208, v208, v208\nM 0,176,128\nR 168,190,16384\n"
"v_mov_b32 v214, v234\nM 0,180,144\ns_mov_b64 s[60:61], 0xffff\nv_cndmask_b32 v208, 0, v208, s[60:61]\nv_mul_f32 v215, 0x3e8293ee, v234\nM 16,176,132\n"
"R 170,190,20480\nv_mul_f32 v232, 0xba800000, v232\nM 16,180,148\nbuffer_load_dwordx4 a[212:215], v252, s[20:23], s65 offen offset:1024 sc1\nM 32,176,136\nR 172,190,24576\n"
"s_add_u32 s63, s52, 1970176\nM 32,180,152\ns_add_u32 m0, s50, 8192\ns_mov_b64 s[60:61], 0xffff\nv_cndmask_b32 v209, 0, v209, s[60:61]\nM 48,176,140\n"
"R 174,190,28672\nv_mul_f32 v217, v232, v232\nM 48,180,156\ns_waitcnt vmcnt(11)\ns_barrier\nG 4\n"
"M 52,160,140\nR 176,191,16384\nv_mul_f32 v233, 0x3a800000, v233\nM 52,164,156\nbuffer_load_dwordx4 v220, s[4:7], s63 offen lds\nM 36,160,136\n"
"R 178,191,20480\nv_sub_f32 v233, v233, v217\nM 36,164,152\nbuffer_load_dwordx4 a[216:219], v252, s[20:23], s66 offen offset:1024 sc1\nM 20,160,132\nR 180,191,24576\n"
"v_max_f32 v233, 0, v233\nM 20,164,148\nbuffer_load_dwordx4 v221, s[4:7], s63 offen offset:1024 lds\nM 4,160,128\nR 182,191,28672\ns_add_u32 m0, s50, 16384\n"
"M 4,164,144\nG 4\nv_add_f32 v233, 0x3727c5ac, v233\nv_and_b32 v212, s77, v232\nM 8,168,128\nR 160,188,32768\n"
"s_add_u32 s63, s52, 2166784\nM 8,172,144\nbuffer_load_dwordx4 a[220:223], v252, s[20:23], s67 offen offset:1024 sc1\nM 24,168,132\nR 162,188,36864\nv_sub_f32 v232, v232, v212\n"
"M 24,172,148\nbuffer_load_dwordx4 v220, s[4:7], s63 offen lds\nM 40,168,136\nR 164,188,40960\nv_cvt_pk_bf16_f32 v212, v212, v212\nM 40,172,152\n"
"buffer_load_dwordx4 v221, s[4:7], s63 offen offset:1024 lds\nM 56,168,140\nR 166,188,45056\nv_bfe_u32 v230, v252, 2, 6\nM 56,172,156\nG 4\n"
"v_rsq_f32 v235, v233\nM 60,176,140\nR 168,189,32768\nv_mul_f32 v219, v233, v235\nM 60,180,156\nbuffer_load_dwordx4 a[224:227], v252, s[20:23], s64 offen offset:2048 sc1\n"
"M 44,176,136\nR 170,189,36864\nv_cvt_pk_bf16_f32 v213, v232, v219\nM 44,180,152\ns_mov_b64 s[60:61], 0xffff\nv_cndmask_b32 v212, 0, v212, s[60:61]\n"
"v_mov_b32 v217, v235\nM 28,176,132\nR 172,189,40960\nv_mul_f32 v218, 0x3e8293ee, v235\nM 28,180,148\ns_mov_b64 s[60:61], 0xffff\n"
"v_cndmask_b32 v213, 0, v213, s[60:61]\nM 12,176,128\nR 174,189,45056\nM 12,180,144\nG 4\nN 64,128,160\n"
"R 176,190,32768\nN 64,144,164\nbuffer_load_dwordx4 a[228:231], v252, s[20:23], s65 offen offset:2048 sc1\nN 68,132,160\nR 178,190,36864\nN 68,148,164\n"
"s_lshl_b32 s60, s40, 7\ns_add_u32 s60, s60, 161280\nv_add_u32 v230, s60, v230\nN 96,136,160\nR 180,190,40960\nN 96,152,164\n"
"s_add_u32 m0, s50, 24576\nds_write_b32 v230, v214 offset:0\nN 100,140,160\nR 182,190,45056\ns_add_u32 s63, s52, 2361344\nN 100,156,164\n"
"s_waitcnt vmcnt(10)\ns_barrier\nG 5\nN 108,140,168\nR 160,191,32768\nN 108,156,172\n"
"buffer_load_dwordx4 a[232:235], v252, s[20:23], s66 offen offset:2048 sc1\nN 104,136,168\nR 162,191,36864\nN 104,152,172\nbuffer_load_dwordx4 v220, s[4:7], s63 offen lds\nN 76,132,168\n"
"R 164,191,40960\nLq688:\ns_lshr_b32 s60, s45, 3\ns_lshl2_add_u32 s60, s60, s40\ns_lshl_b32 s60, s60, 8\nv_lshl_add_u32 v224, v252, 3, s60\n"
"v_add_u32 v224, Lq688+4096-LqPC, v224\nv_min_u32 v224, LqE-LqPC-4, v224\ns_mov_b64 exec, 3\nglobal_load_dword v224, v224, s[98:99]\ns_mov_b64 exec, -1\nN 76,148,172\n"
"buffer_load_dwordx4 a[236:239], v252, s[20:23], s67 offen offset:2048 sc1\nN 72,128,168\nR 166,191,45056\nN 72,144,172\nds_write_b32 v230, v217 offset:64\nG 5\n"
"N 80,128,176\nR 168,188,49152\nbuffer_load_dwordx4 v221, s[4:7], s63 offen offset:1024 lds\nN 80,144,180\ns_add_u32 m0, s50, 32768\ns_add_u32 s63, s52, 2557952\n"
"buffer_load_dwordx4 v220, s[4:7], s63 offen lds\nN 84,132,176\nR 170,188,53248\nN 84,148,180\nN 112,136,176\nR 172,184,0\n"
"N 112,152,180\nbuffer_load_dwordx4 a[240:243], v252, s[20:23], s64 offen offset:3072 sc1\nN 116,140,176\nR 174,184,4096\nN 116,156,180\nG 5\n"
"N 124,140,160\nR 176,189,49152\nbuffer_load_dwordx4 v221, s[4:7], s63 offen offset:1024 lds\nN 124,156,164\nN 120,136,160\nR 178,189,53248\n"
"N 120,152,164\nbuffer_load_dwordx4 a[244:247], v252, s[20:23], s65 offen offset:3072 sc1\nN 92,132,160\nR 180,185,0\nN 92,148,164\nN 88,128,160\n"
"R 182,185,4096\nN 88,144,164\nG 4\nM 0,168,160\nR 160,190,49152\nM 0,172,176\n"
"M 16,168,164\nR 162,190,53248\nM 16,172,180\nbuffer_load_dwordx4 a[248:251], v252, s[20:23], s66 offen offset:3072 sc1\nM 32,168,168\nR 164,186,0\n"
"M 32,172,184\ns_add_u32 m0, s50, 40960\nM 48,168,172\nR 166,186,4096\ns_add_u32 s63, s52, 2754560\nM 48,172,188\n"
"s_waitcnt vmcnt(12)\ns_barrier\nG 4\nM 52,176,172\nR 168,191,49152\nM 52,180,188\n"
"buffer_load_dwordx4 a[252:255], v252, s[20:23], s67 offen offset:3072 sc1\nM 36,176,168\nR 170,191,53248\nM 36,180,184\nbuffer_load_dwordx4 v220, s[4:7], s63 offen lds\nM 20,176,164\n"
"R 172,187,0\nM 20,180,180\nbuffer_load_dwordx4 v221, s[4:7], s63 offen offset:1024 lds\nM 4,176,160\nR 174,187,4096\ns_add_u32 m0, s50, 49152\n"
"M 4,180,176\nG 4\nM 8,160,160\nR 176,184,8192\ns_add_u32 s63, s52, 2951168\nM 8,164,176\n"
"buffer_load_dwordx4 v220, s[4:7], s63 offen lds\nM 24,160,164\nR 178,184,12288\nM 24,164,180\nM 40,160,168\nR 180,184,16384\n"
"M 40,164,184\nbuffer_load_dwordx4 v221, s[4:7], s63 offen offset:1024 lds\nM 56,160,172\nR 182,184,20480\nM 56,164,188\nG 4\n"
"M 60,168,172\nR 160,185,8192\nM 60,172,188\nM 44,168,168\nR 162,185,12288\nM 44,172,184\n"
"s_add_u32 m0, s50, 57344\nM 28,168,164\nR 164,185,16384\nM 28,172,180\ns_add_u32 s63, s52, 2363392\nM 12,168,160\n"
"R 166,185,20480\nM 12,172,176\nG 4\nN 64,160,176\nR 168,186,8192\nN 64,176,180\n"
"buffer_load_dwordx4 v220, s[4:7], s63 offen lds\nN 68,164,176\nR 170,186,12288\nN 68,180,180\nN 96,168,176\nR 172,186,16384\n"
"N 96,184,180\nbuffer_load_dwordx4 v221, s[4:7], s63 offen offset:1024 lds\nN 100,172,176\nR 174,186,20480\ns_add_u32 m0, s50, 65536\nN 100,188,180\n"
"s_waitcnt vmcnt(9)\ns_barrier\nG 4\nN 108,172,160\nR 176,187,8192\ns_add_u32 s63, s52, 2560000\n"
"N 108,188,164\nbuffer_load_dwordx4 v220, s[4:7], s63 offen lds\nN 104,168,160\nR 178,187,12288\nN 104,184,164\nN 76,164,160\n"
"R 180,187,16384\nN 76,180,164\nN 72,160,160\nR 182,187,20480\nN 72,176,164\nG 4\n"
"N 80,160,168\nR 160,184,24576\nN 80,176,172\nbuffer_load_dwordx4 v221, s[4:7], s63 offen offset:1024 lds\nN 84,164,168\nR 162,184,28672\n"
"s_add_u32 s63, s52, 2756608\nN 84,180,172\ns_add_u32 m0, s50, 73728\nN 112,168,168\nR 164,184,32768\nN 112,184,172\n"
"buffer_load_dwordx4 v220, s[4:7], s63 offen lds\nN 116,172,168\nR 166,184,36864\nN 116,188,172\nG 4\nN 124,172,176\n"
"R 168,185,24576\nN 124,188,180\nbuffer_load_dwordx4 v221, s[4:7], s63 offen offset:1024 lds\nN 120,168,176\nR 170,185,28672\ns_add_u32 s63, s52, 2953216\n"
"N 120,184,180\ns_add_u32 m0, s50, 81920\nN 92,164,176\nR 172,185,32768\nN 92,180,180\nbuffer_load_dwordx4 v220, s[4:7], s63 offen lds\n"
"N 88,160,176\nR 174,185,36864\nN 88,176,180\nG 4\nM 0,160,192\nR 176,186,24576\n"
"M 0,164,208\nbuffer_load_dwordx4 v221, s[4:7], s63 offen offset:1024 lds\nM 16,160,196\nR 178,186,28672\nM 16,164,212\ns_add_u32 m0, s50, 90112\n"
"M 32,160,200\nR 180,186,32768\nM 32,164,216\ns_add_u32 s63, s52, 0\nM 48,160,204\nR 182,186,36864\n"
"M 48,164,220\ns_waitcnt vmcnt(8)\ns_barrier\nG 4\nM 52,168,204\nR 160,187,24576\n"
"M 52,172,220\nbuffer_load_dwordx4 v220, s[4:7], s63 offen lds\nM 36,168,200\nR 162,187,28672\nM 36,172,216\nM 20,168,196\n"
"R 164,187,32768\nM 20,172,212\nbuffer_load_dwordx4 v221, s[4:7], s63 offen offset:1024 lds\nM 4,168,192\nR 166,187,36864\ns_add_u32 m0, s50, 98304\n"
"M 4,172,208\nG 4\nM 8,176,192\nR 168,184,40960\ns_add_u32 s63, s52, 196608\nM 8,180,208\n"
"buffer_load_dwordx4 v220, s[4:7], s63 offen lds\nM 24,176,196\nR 170,184,45056\nM 24,180,212\nM 40,176,200\nR 172,184,49152\n"
"M 40,180,216\nbuffer_load_dwordx4 v221, s[4:7], s63 offen offset:1024 lds\nM 56,176,204\nR 174,184,53248\ns_add_u32 m0, s50, 106496\nM 56,180,220\n"
"G 4\nM 60,160,204\nR 176,185,40960\ns_add_u32 s63, s52, 393216\nM 60,164,220\nbuffer_load_dwordx4 v220, s[4:7], s63 offen lds\n"
"M 44,160,200\nR 178,185,45056\nM 44,164,216\nM 28,160,196\nR 180,185,49152\nM 28,164,212\n"
"M 12,160,192\nR 182,185,53248\nM 12,164,208\nG 4\nM 0,168,224\nR 160,186,40960\n"
"M 0,172,240\nbuffer_load_dwordx4 v221, s[4:7], s63 offen offset:1024 lds\nM 16,168,228\nR 162,186,45056\ns_add_u32 s63, s52, 589824\nM 16,172,244\n"
"s_add_u32 m0, s50, 114688\nM 32,168,232\nR 164,186,49152\nM 32,172,248\nbuffer_load_dwordx4 v220, s[4:7], s63 offen lds\nM 48,168,236\n"
"R 166,186,53248\nM 48,172,252\ns_waitcnt vmcnt(11)\ns_barrier\nG 4\nM 52,176,236\n"
"R 168,187,40960\nM 52,180,252\nbuffer_load_dwordx4 v221, s[4:7], s63 offen offset:1024 lds\nM 36,176,232\nR 170,187,45056\ns_add_u32 s63, s52, 786432\n"
"M 36,180,248\ns_add_u32 m0, s50, 0\nM 20,176,228\nR 172,187,49152\nM 20,180,244\nbuffer_load_dwordx4 v220, s[4:7], s63 offen lds\n"
"M 4,176,224\nR 174,187,53248\nM 4,180,240\nG 4\nM 8,160,224\nR 176,184,57344\n"
"M 8,164,240\nbuffer_load_dwordx4 v221, s[4:7], s63 offen offset:1024 lds\nM 24,160,228\nR 178,184,61440\nM 24,164,244\nM 40,160,232\n"
"R 180,188,0\nM 40,164,248\nM 56,160,236\nR 182,188,4096\ns_mov_b32 s60, 156160\nv_bfe_u32 v246, v252, 4, 4\n"
"v_lshl_add_u32 v246, v246, 3, s60\nM 56,164,252\nds_read_b64 v[238:239], v246 offset:0\nG 5\nM 60,168,236\nR 160,185,57344\n"
"ds_read_b64 v[240:241], v246 offset:128\nM 60,172,252\nds_read_b64 v[242:243], v246 offset:256\ns_add_u32 m0, s50, 8192\nM 44,168,232\nR 162,185,61440\n"
"ds_read_b64 v[244:245], v246 offset:384\nM 44,172,248\nds_read_b64 v[246:247], v246 offset:0\ns_add_u32 s63, s52, 983040\nM 28,168,228\nR 164,189,0\n"
"M 28,172,244\nbuffer_load_dwordx4 v220, s[4:7], s63 offen lds\nM 12,168,224\nR 166,189,4096\nM 12,172,240\ns_waitcnt vmcnt(11) lgkmcnt(2)\n"
"s_barrier\nN 64,192,176\nR 168,186,57344\nN 64,208,180\ns_mov_b32 m0, s51\nN 68,196,176\n"
"R 170,186,61440\nN 68,212,180\nds_write_addtid_b32 v32 offset:0\nN 96,200,176\nR 172,190,0\nv_mfma_f32_16x16x32_bf16 v[0:3], v[238:241], v[208:211], v[0:3]\n"
"ds_write_addtid_b32 v33 offset:256\nds_write_addtid_b32 v34 offset:512\nN 96,216,180\nds_write_addtid_b32 v35 offset:768\nN 100,204,176\nR 174,190,4096\n"
"v_mfma_f32_16x16x32_bf16 v[4:7], v[240:243], v[208:211], v[4:7]\nds_write_addtid_b32 v36 offset:1024\nds_write_addtid_b32 v37 offset:1280\nN 100,220,180\nds_write_addtid_b32 v38 offset:1536\nG 11\n"
"N 108,204,160\nR 176,187,57344\nv_mfma_f32_16x16x32_bf16 v[8:11], v[242:245], v[208:211], v[8:11]\nLpg20:\ns_cbranch_execz LretW0n\nds_write_addtid_b32 v39 offset:1792\n"
"ds_write_addtid_b32 v40 offset:2048\nN 108,220,164\nds_write_addtid_b32 v41 offset:2304\nN 104,200,160\nR 178,187,61440\nv_mfma_f32_16x16x32_bf16 v[12:15], v[244:247], v[208:211], v[12:15]\n"
"ds_write_addtid_b32 v42 offset:2560\nds_write_addtid_b32 v43 offset:2816\nN 104,216,164\nds_write_addtid_b32 v44 offset:3072\nN 76,196,160\nR 180,191,0\n"
"v_mfma_f32_16x16x32_bf16 v[16:19], v[244:247], v[210:213], v[16:19]\nds_write_addtid_b32 v45 offset:3328\nds_write_addtid_b32 v46 offset:3584\nN 76,212,164\nds_write_addtid_b32 v47 offset:3840\nN 72,192,160\n"
"R 182,191,4096\nv_mfma_f32_16x16x32_bf16 v[20:23], v[238:241], v[210:213], v[20:23]\nds_write_addtid_b32 v48 offset:4096\nds_write_addtid_b32 v49 offset:4352\nN 72,208,164\nds_write_addtid_b32 v50 offset:4608\n"
"G 15\nN 80,192,168\nR 160,188,8192\nv_mfma_f32_16x16x32_bf16 v[24:27], v[240:243], v[210:213], v[24:27]\nds_write_addtid_b32 v51 offset:4864\nds_write_addtid_b32 v52 offset:5120\n"
"N 80,208,172\nds_write_addtid_b32 v53 offset:5376\nN 84,196,168\nR 162,188,12288\nv_mfma_f32_16x16x32_bf16 v[28:31], v[242:245], v[210:213], v[28:31]\nds_write_addtid_b32 v54 offset:5632\n"
"ds_write_addtid_b32 v55 offset:5888\nN 84,212,172\nds_write_addtid_b32 v56 offset:6144\nds_write_addtid_b32 v57 offset:6400\nN 112,200,168\nR 164,188,16384\n"
"ds_write_addtid_b32 v58 offset:6656\nN 112,216,172\nds_write_addtid_b32 v59 offset:6912\nds_write_addtid_b32 v60 offset:7168\nN 116,204,168\nR 166,188,20480\n"
"ds_write_addtid_b32 v61 offset:7424\nN 116,220,172\nds_write_addtid_b32 v62 offset:7680\nds_write_addtid_b32 v63 offset:7936\nG 15\nN 124,204,176\n"
"R 168,189,8192\ns_add_u32 m0, s50, 8192\nN 124,220,180\nbuffer_load_dwordx4 v221, s[4:7], s63 offen offset:1024 lds\nN 120,200,176\nR 170,189,12288\n"
"s_waitcnt vmcnt(8) lgkmcnt(2)\ns_barrier\nN 120,216,180\nds_read2st64_b32 v[32:33], v225 offset0:0 offset1:1\nds_read2st64_b32 v[34:35], v225 offset0:2 offset1:3\nN 92,196,176\n"
"R 172,189,16384\nds_read2st64_b32 v[36:37], v225 offset0:4 offset1:5\nN 92,212,180\nds_read2st64_b32 v[38:39], v225 offset0:6 offset1:7\nds_read2st64_b32 v[40:41], v225 offset0:8 offset1:9\nN 88,192,176\n"
"R 174,189,20480\nds_read2st64_b32 v[42:43], v225 offset0:10 offset1:11\nN 88,208,180\nds_read2st64_b32 v[44:45], v225 offset0:12 offset1:13\nds_read2st64_b32 v[46:47], v225 offset0:14 offset1:15\nN 64,224,160\n"
"R 176,190,8192\ns_mov_b32 s60, 156672\nv_bfe_u32 v246, v252, 4, 4\nv_lshl_add_u32 v246, v246, 3, s60\nds_read2st64_b32 v[48:49], v225 offset0:16 offset1:17\nN 64,240,164\n"
"ds_read_b64 v[238:239], v246 offset:0\nds_read2st64_b32 v[50:51], v225 offset0:18 offset1:19\nG 13\nv_add_f32 v0, v0, v32\nN 68,228,160\nR 178,190,12288\n"
"ds_read_b64 v[240:241], v246 offset:128\nds_read2st64_b32 v[52:53], v225 offset0:20 offset1:21\nN 68,244,164\nds_read_b64 v[242:243], v246 offset:256\ns_add_u32 m0, s50, 16384\nds_read2st64_b32 v[54:55], v225 offset0:22 offset1:23\n"
"N 96,232,160\nR 180,190,16384\nds_read_b64 v[244:245], v246 offset:384\nN 96,248,164\nds_read_b64 v[246:247], v246 offset:0\ns_add_u32 s63, s52, 1179648\n"
"N 100,236,160\nR 182,190,20480\nv_add_f32 v1, v1, v33\nN 100,252,164\nds_read2st64_b32 v[56:57], v225 offset0:24 offset1:25\nds_read2st64_b32 v[58:59], v225 offset0:26 offset1:27\n"
"G 15\nN 108,236,168\nR 160,191,8192\nv_add_f32 v2, v2, v34\nN 108,252,172\nbuffer_load_dwordx4 v220, s[4:7], s63 offen lds\n"
"N 104,232,168\nR 162,191,12288\nv_add_f32 v3, v3, v35\nN 104,248,172\nds_read2st64_b32 v[60:61], v225 offset0:28 offset1:29\nv_add_f32 v4, v4, v36\n"
"N 76,228,168\nR 164,191,16384\nv_add_f32 v5, v5, v37\nN 76,244,172\nbuffer_load_dwordx4 v221, s[4:7], s63 offen offset:1024 lds\nG 13\n"
"v_mfma_f32_16x16x32_bf16 v[64:67], v[208:211], v[238:241], v[64:67]\nds_read2st64_b32 v[62:63], v225 offset0:30 offset1:31\nv_add_f32 v6, v6, v38\nv_add_f32 v7, v7, v39\nN 72,224,168\nR 166,191,20480\n"
"s_add_u32 m0, s50, 24576\nN 72,240,172\nG 8\nv_add_f32 v8, v8, v40\nv_add_f32 v9, v9, v41\nv_mfma_f32_16x16x32_bf16 v[72:75], v[208:211], v[240:243], v[72:75]\n"
"v_add_f32 v10, v10, v42\nv_add_f32 v11, v11, v43\nN 80,224,176\nR 168,188,24576\ns_add_u32 s63, s52, 1376256\nN 80,240,180\n"
"buffer_load_dwordx4 v220, s[4:7], s63 offen lds\nv_mfma_f32_16x16x32_bf16 v[80:83], v[208:211], v[242:245], v[80:83]\nv_add_f32 v12, v12, v44\nv_add_f32 v13, v13, v45\nv_add_f32 v14, v14, v46\nN 84,228,176\n"
"R 170,188,28672\nv_add_f32 v15, v15, v47\nN 84,244,180\nbuffer_load_dwordx4 v221, s[4:7], s63 offen offset:1024 lds\nv_mfma_f32_16x16x32_bf16 v[88:91], v[208:211], v[244:247], v[88:91]\nv_add_f32 v16, v16, v48\n"
"v_add_f32 v17, v17, v49\nv_add_f32 v18, v18, v50\nN 112,232,176\nR 172,188,32768\ns_add_u32 s63, s52, 1572864\nN 112,248,180\n"
"s_add_u32 m0, s50, 49152\nv_add_f32 v19, v19, v51\nv_add_f32 v20, v20, v52\nv_mfma_f32_16x16x32_bf16 v[68:71], v[210:213], v[244:247], v[68:71]\nv_add_f32 v21, v21, v53\nv_add_f32 v22, v22, v54\n"
"N 116,236,176\nR 174,188,36864\nv_add_f32 v23, v23, v55\nN 116,252,180\nG 4\nv_add_f32 v24, v24, v56\n"
"v_add_f32 v25, v25, v57\nv_mfma_f32_16x16x32_bf16 v[76:79], v[210:213], v[238:241], v[76:79]\nv_add_f32 v26, v26, v58\nv_add_f32 v27, v27, v59\nN 124,236,160\nR 176,189,24576\n"
"v_add_f32 v28, v28, v60\nN 124,252,164\nbuffer_load_dwordx4 v220, s[4:7], s63 offen lds\nv_mfma_f32_16x16x32_bf16 v[84:87], v[210:213], v[240:243], v[84:87]\nv_add_f32 v29, v29, v61\nv_add_f32 v30, v30, v62\n"
"v_add_f32 v31, v31, v63\nN 120,232,160\nR 178,189,28672\ns_lshl4_add_u32 s64, s41, s42\nv_lshrrev_b32 v227, 1, v252\nN 120,248,164\n"
"buffer_load_dwordx4 v221, s[4:7], s63 offen offset:1024 lds\nv_mfma_f32_16x16x32_bf16 v[92:95], v[210:213], v[242:245], v[92:95]\ns_cmp_eq_u32 s40, 3\ns_cselect_b64 s[66:67], -1, 0\ns_lshl_b32 s64, s64, 12\nv_mul_f32 v16, v16, v217\n"
"N 92,228,160\nR 180,189,32768\ns_add_u32 m0, s50, 57344\ns_add_u32 s63, s52, 1769472\nv_mul_f32 v17, v17, v217\nN 92,244,164\n"
"buffer_load_dwordx4 v220, s[4:7], s63 offen lds\nv_mul_f32 v18, v18, v217\nN 88,224,160\nR 182,189,36864\ns_add_u32 s64, s64, 78643200\nv_mul_f32 v19, v19, v217\n"
"N 88,240,164\ns_waitcnt vmcnt(11)\ns_barrier\nG 4\nv_mul_f32 v20, v20, v217\nv_mfma_f32_16x16x32_bf16 v[128:131], v[168:171], a[0:3], 0\n"
"R 160,190,24576\nv_mul_f32 v21, v21, v217\nv_mul_f32 v22, v22, v217\nM 128,172,16\ns_mov_b32 m0, s51\nv_mul_f32 v23, v23, v217\n"
"v_mul_f32 v24, v24, v217\nv_mul_f32 v25, v25, v217\nv_mfma_f32_16x16x32_bf16 v[144:147], v[168:171], a[4:7], 0\nR 162,190,28672\nv_mul_f32 v26, v26, v217\nv_mul_f32 v27, v27, v217\n"
"M 144,172,20\nv_mul_f32 v28, v28, v217\nv_mul_f32 v29, v29, v217\nv_mul_f32 v30, v30, v217\nv_mfma_f32_16x16x32_bf16 v[32:35], v[168:171], a[8:11], 0\nR 164,190,32768\n"
"v_mul_f32 v31, v31, v217\nv_cvt_pk_bf16_f32 v16, v16, v17\nM 32,172,24\nv_cvt_pk_bf16_f32 v17, v18, v19\nv_cvt_pk_bf16_f32 v18, v20, v21\nv_cvt_pk_bf16_f32 v19, v22, v23\n"
"v_mfma_f32_16x16x32_bf16 v[48:51], v[168:171], a[12:15], 0\nR 166,190,36864\nv_cvt_pk_bf16_f32 v20, v24, v25\nv_cvt_pk_bf16_f32 v21, v26, v27\nM 48,172,28\nds_write_addtid_b32 v96 offset:0\n"
"G 5\nv_cvt_pk_bf16_f32 v22, v28, v29\nv_cvt_pk_bf16_f32 v23, v30, v31\nv_mul_f32 v0, v0, v214\nv_mfma_f32_16x16x32_bf16 v[52:55], v[176:179], a[12:15], 0\nR 168,191,24576\n"
"ds_write_addtid_b32 v97 offset:256\nv_mul_f32 v1, v1, v214\nv_mul_f32 v2, v2, v214\nM 52,180,28\nds_write_addtid_b32 v98 offset:512\nv_mul_f32 v3, v3, v214\n"
"v_mul_f32 v4, v4, v214\nv_mul_f32 v5, v5, v214\nv_mfma_f32_16x16x32_bf16 v[36:39], v[176:179], a[8:11], 0\nR 170,191,28672\nds_write_addtid_b32 v99 offset:768\nv_mul_f32 v6, v6, v214\n"
"v_mul_f32 v7, v7, v214\nM 36,180,24\nds_write_addtid_b32 v100 offset:1024\nv_mul_f32 v8, v8, v214\nv_mul_f32 v9, v9, v214\nv_mul_f32 v10, v10, v214\n"
"v_mfma_f32_16x16x32_bf16 v[148:151], v[176:179], a[4:7], 0\nR 172,191,32768\nds_write_addtid_b32 v101 offset:1280\nv_mul_f32 v11, v11, v214\nv_mul_f32 v12, v12, v214\nM 148,180,20\n"
"ds_write_addtid_b32 v102 offset:1536\nv_mul_f32 v13, v13, v214\nv_mul_f32 v14, v14, v214\nv_mul_f32 v15, v15, v214\nv_mfma_f32_16x16x32_bf16 v[132:135], v[176:179], a[0:3], 0\nR 174,191,36864\n"
"ds_write_addtid_b32 v103 offset:1792\nv_cvt_pk_bf16_f32 v0, v0, v1\nv_cvt_pk_bf16_f32 v1, v2, v3\nM 132,180,16\nds_write_addtid_b32 v104 offset:2048\nG 13\n"
"v_cvt_pk_bf16_f32 v2, v4, v5\nv_cvt_pk_bf16_f32 v3, v6, v7\nv_cvt_pk_bf16_f32 v4, v8, v9\nv_mfma_f32_16x16x32_bf16 v[136:139], v[160:163], a[0:3], 0\nR 176,188,40960\nds_write_addtid_b32 v105 offset:2304\n"
"v_cvt_pk_bf16_f32 v5, v10, v11\nv_cvt_pk_bf16_f32 v6, v12, v13\nM 136,164,16\nds_write_addtid_b32 v106 offset:2560\nds_write_addtid_b32 v107 offset:2816\ns_mov_b64 exec, s[66:67]\n"
"buffer_store_dwordx4 v[16:19], v252, s[20:23], s64 offen offset:0 sc1\ns_mov_b64 exec, -1\nv_cvt_pk_bf16_f32 v7, v14, v15\nv_mfma_f32_16x16x32_bf16 v[152:155], v[160:163], a[4:7], 0\nR 178,188,45056\nds_write_addtid_b32 v108 offset:3072\n"
"M 152,164,20\nds_write_addtid_b32 v109 offset:3328\nds_write_addtid_b32 v110 offset:3584\nv_mfma_f32_16x16x32_bf16 v[40:43], v[160:163], a[8:11], 0\nR 180,188,49152\nds_write_addtid_b32 v111 offset:3840\n"
"M 40,164,24\nds_write_addtid_b32 v112 offset:4096\nds_write_addtid_b32 v113 offset:4352\ns_mov_b64 exec, s[66:67]\nbuffer_store_dwordx4 v[20:23], v252, s[20:23], s64 offen offset:1024 sc1\ns_mov_b64 exec, -1\n"
"v_mfma_f32_16x16x32_bf16 v[56:59], v[160:163], a[12:15], 0\nR 182,188,53248\nds_write_addtid_b32 v114 offset:4608\nM 56,164,28\nds_write_addtid_b32 v115 offset:4864\nds_write_addtid_b32 v116 offset:5120\n"
"G 15\nv_mfma_f32_16x16x32_bf16 v[60:63], v[168:171], a[12:15], 0\nR 160,189,40960\nds_write_addtid_b32 v117 offset:5376\nM 60,172,28\nds_write_addtid_b32 v118 offset:5632\n"
"ds_write_addtid_b32 v119 offset:5888\nv_mfma_f32_16x16x32_bf16 v[44:47], v[168:171], a[8:11], 0\nR 162,189,45056\nds_write_addtid_b32 v120 offset:6144\nM 44,172,24\nds_write_addtid_b32 v121 offset:6400\n"
"ds_write_addtid_b32 v122 offset:6656\nds_write_addtid_b32 v123 offset:6912\nds_write_addtid_b32 v124 offset:7168\nds_write_addtid_b32 v125 offset:7424\nds_write_addtid_b32 v126 offset:7680\nds_write_addtid_b32 v127 offset:7936\n"
"v_mfma_f32_16x16x32_bf16 v[156:159], v[168:171], a[4:7], 0\nR 164,189,49152\ns_add_u32 m0, s50, 57344\nM 156,172,20\nbuffer_load_dwordx4 v221, s[4:7], s63 offen offset:1024 lds\nv_mfma_f32_16x16x32_bf16 v[140:143], v[168:171], a[0:3], 0\n"
"R 166,189,53248\ns_waitcnt vmcnt(10) lgkmcnt(2)\ns_barrier\nM 140,172,16\nds_read2st64_b32 v[96:97], v225 offset0:0 offset1:1\nds_read2st64_b32 v[98:99], v225 offset0:2 offset1:3\n"
"v_lshrrev_b32 v226, 8, v252\nM 128,176,32\nR 168,190,40960\ns_add_u32 s63, s52, 1966080\nds_read2st64_b32 v[100:101], v225 offset0:4 offset1:5\ns_lshl_b32 s60, s40, 7\n"
"s_add_u32 s60, s60, 161280\nv_lshl_add_u32 v226, v226, 4, s60\nM 128,180,48\ns_add_u32 m0, s50, 65536\nds_read2st64_b32 v[102:103], v225 offset0:6 offset1:7\nds_read2st64_b32 v[104:105], v225 offset0:8 offset1:9\n"
"ds_read_b128 v[230:233], v226 offset:0\nM 144,176,36\nR 170,190,45056\nds_read2st64_b32 v[106:107], v225 offset0:10 offset1:11\nds_read_b128 v[234:237], v226 offset:64\nM 144,180,52\n"
"buffer_load_dwordx4 v220, s[4:7], s63 offen lds\nds_read2st64_b32 v[108:109], v225 offset0:12 offset1:13\nM 32,176,40\nR 172,190,49152\nds_read2st64_b32 v[110:111], v225 offset0:14 offset1:15\nM 32,180,56\n"
"ds_read2st64_b32 v[112:113], v225 offset0:16 offset1:17\nds_read2st64_b32 v[114:115], v225 offset0:18 offset1:19\nG 14\nv_add_f32 v64, v64, v96\nM 48,176,44\nR 174,190,53248\n"
"ds_read2st64_b32 v[116:117], v225 offset0:20 offset1:21\nv_add_f32 v65, v65, v97\nM 48,180,60\nds_read2st64_b32 v[118:119], v225 offset0:22 offset1:23\nds_read2st64_b32 v[120:121], v225 offset0:24 offset1:25\nM 52,160,44\n"
"R 176,191,40960\nds_read2st64_b32 v[122:123], v225 offset0:26 offset1:27\nG 15\nv_add_f32 v66, v66, v98\nM 52,164,60\nbuffer_load_dwordx4 v221, s[4:7], s63 offen offset:1024 lds\n"
"ds_read2st64_b32 v[124:125], v225 offset0:28 offset1:29\nM 36,160,40\nR 178,191,45056\nds_read2st64_b32 v[126:127], v225 offset0:30 offset1:31\nv_add_f32 v67, v67, v99\nM 36,164,56\n"
"s_add_u32 m0, s50, 73728\nv_add_f32 v68, v68, v100\nv_add_f32 v69, v69, v101\nv_add_f32 v70, v70, v102\nM 148,160,36\nR 180,191,49152\n"
"v_add_f32 v71, v71, v103\nv_add_f32 v72, v72, v104\nM 148,164,52\ns_add_u32 s63, s52, 2162688\nv_add_f32 v73, v73, v105\nG 15\n"
"v_add_f32 v74, v74, v106\nv_add_f32 v75, v75, v107\nM 132,160,32\nR 182,191,53248\nG 15\nv_add_f32 v76, v76, v108\n"
"v_add_f32 v77, v77, v109\nM 132,164,48\nG 10\nv_add_f32 v78, v78, v110\nv_add_f32 v79, v79, v111\nv_add_f32 v80, v80, v112\n"
"M 136,168,32\nR 160,184,0\nv_add_f32 v81, v81, v113\nv_add_f32 v82, v82, v114\nM 136,172,48\nbuffer_load_dwordx4 v220, s[4:7], s63 offen lds\n"
"v_add_f32 v83, v83, v115\nM 152,168,36\nR 162,184,4096\nG 11\nv_add_f32 v84, v84, v116\nM 152,172,52\n"
"v_add_f32 v85, v85, v117\nG 10\nv_add_f32 v86, v86, v118\nM 40,168,40\nR 164,184,8192\nv_add_f32 v87, v87, v119\n"
"M 40,172,56\nbuffer_load_dwordx4 v221, s[4:7], s63 offen offset:1024 lds\nM 56,168,44\nR 166,184,12288\ns_add_u32 m0, s50, 81920\nM 56,172,60\n"
"G 4\nv_add_f32 v88, v88, v120\nv_add_f32 v89, v89, v121\nM 60,176,44\nR 168,185,0\ns_add_u32 s63, s52, 2359296\n"
"M 60,180,60\nbuffer_load_dwordx4 v220, s[4:7], s63 offen lds\nM 44,176,40\nR 170,185,4096\nv_add_f32 v90, v90, v122\nM 44,180,56\n"
"v_add_f32 v91, v91, v123\nv_add_f32 v92, v92, v124\nM 156,176,36\nR 172,185,8192\nv_add_f32 v93, v93, v125\nM 156,180,52\n"
"buffer_load_dwordx4 v221, s[4:7], s63 offen offset:1024 lds\nM 140,176,32\nR 174,185,12288\ns_add_u32 m0, s50, 90112\nM 140,180,48\nG 4\n"
"v_add_f32 v94, v94, v126\nv_add_f32 v95, v95, v127\nM 128,160,64\nR 176,186,0\ns_add_u32 s63, s52, 2555904\nv_mul_f32 v64, v64, v230\n"
"M 128,164,80\nbuffer_load_dwordx4 v220, s[4:7], s63 offen lds\nv_mul_f32 v65, v65, v231\nM 144,160,68\nR 178,186,4096\nv_mul_f32 v66, v66, v232\n"
"v_mul_f32 v67, v67, v233\nM 144,164,84\nv_mul_f32 v68, v68, v234\nv_mul_f32 v69, v69, v235\nv_mul_f32 v70, v70, v236\nM 32,160,72\n"
"R 180,186,8192\nv_mul_f32 v71, v71, v237\nv_cvt_pk_bf16_f32 v67, v66, v67\nM 32,164,88\nv_cvt_pk_bf16_f32 v66, v64, v65\nv_cvt_pk_bf16_f32 v68, v68, v69\n"
"v_cvt_pk_bf16_f32 v69, v70, v71\nM 48,160,76\nR 182,186,12288\nv_mul_f32 v72, v72, v230\nv_mul_f32 v73, v73, v231\nM 48,164,92\n"
"G 4\nv_mul_f32 v74, v74, v232\nv_mul_f32 v75, v75, v233\nv_mul_f32 v76, v76, v234\nM 52,168,76\nR 160,187,0\n"
"v_mul_f32 v77, v77, v235\nv_mul_f32 v78, v78, v236\nM 52,172,92\nbuffer_load_dwordx4 v221, s[4:7], s63 offen offset:1024 lds\nv_mul_f32 v79, v79, v237\nM 36,168,72\n"
"R 162,187,4096\ns_add_u32 s63, s52, 2752512\nv_cvt_pk_bf16_f32 v72, v72, v73\nM 36,172,88\ns_add_u32 m0, s50, 98304\nv_cvt_pk_bf16_f32 v73, v74, v75\n"
"v_cvt_pk_bf16_f32 v74, v76, v77\nv_cvt_pk_bf16_f32 v75, v78, v79\nM 148,168,68\nR 164,187,8192\nv_mul_f32 v80, v80, v230\nv_mul_f32 v81, v81, v231\n"
"M 148,172,84\nbuffer_load_dwordx4 v220, s[4:7], s63 offen lds\nv_mul_f32 v82, v82, v232\nM 132,168,64\nR 166,187,12288\ns_waitcnt vmcnt(9)\n"
"s_barrier\nv_mul_f32 v83, v83, v233\nM 132,172,80\nG 4\nv_mul_f32 v84, v84, v234\nv_mul_f32 v85, v85, v235\n"
"M 136,176,64\nR 168,184,16384\nv_mul_f32 v86, v86, v236\nM 136,180,80\nv_mul_f32 v87, v87, v237\nv_cvt_pk_bf16_f32 v78, v80, v81\n"
"M 152,176,68\nR 170,184,20480\nv_cvt_pk_bf16_f32 v79, v82, v83\nM 152,180,84\nv_cvt_pk_bf16_f32 v80, v84, v85\nv_cvt_pk_bf16_f32 v81, v86, v87\n"
"M 40,176,72\nR 172,184,24576\ns_mov_b32 s60, 155648\nv_bfe_u32 v246, v252, 4, 4\nv_lshl_add_u32 v246, v246, 3, s60\nM 40,180,88\n"
"buffer_load_dwordx4 v221, s[4:7], s63 offen offset:1024 lds\nM 56,176,76\nR 174,184,28672\nds_read_b64 v[238:239], v246 offset:0\nM 56,180,92\nG 5\n"
"v_mul_f32 v88, v88, v230\nv_mul_f32 v89, v89, v231\nM 60,160,76\nR 176,185,16384\nds_read_b64 v[240:241], v246 offset:128\nM 60,164,92\n"
"s_add_u32 m0, s50, 106496\nv_mul_f32 v90, v90, v232\nv_mul_f32 v91, v91, v233\nM 44,160,72\nR 178,185,20480\nds_read_b64 v[242:243], v246 offset:256\n"
"M 44,164,88\ns_add_u32 s63, s52, 2949120\nv_mul_f32 v92, v92, v234\nv_mul_f32 v93, v93, v235\nM 156,160,68\nR 180,185,24576\n"
"ds_read_b64 v[244:245], v246 offset:384\nM 156,164,84\nbuffer_load_dwordx4 v220, s[4:7], s63 offen lds\nM 140,160,64\nR 182,185,28672\nds_read_b64 v[246:247], v246 offset:0\n"
"M 140,164,80\nG 9\nv_mul_f32 v94, v94, v236\nv_mul_f32 v95, v95, v237\nM 128,168,96\nR 160,186,16384\n"
"v_cvt_pk_bf16_f32 v84, v88, v89\nM 128,172,112\nv_cvt_pk_bf16_f32 v85, v90, v91\nLpg21:\ns_cbranch_execz LretW0n\nv_cvt_pk_bf16_f32 v86, v92, v93\n"
"M 144,168,100\nR 162,186,20480\nv_cvt_pk_bf16_f32 v87, v94, v95\nM 144,172,116\nv_add_u32 v108, 161792, v252\nds_read_b128 v[112:115], v108\n"
"s_mov_b64 exec, s[66:67]\nbuffer_store_dwordx2 v[68:69], v227, s[20:23], s64 offen offset:2048 sc1\ns_mov_b64 exec, -1\nM 32,168,104\nR 164,186,24576\nG 10\n"
"v_mfma_f32_16x16x32_bf16 v[128:131], v[238:241], v[208:211], v[128:131]\ns_lshl_b32 s60, s40, 12\ns_add_u32 s60, s60, 69632\nv_add_u32 v230, s60, v252\nv_add_u32 v231, s60, v227\nM 32,172,120\n"
"buffer_load_dwordx4 v221, s[4:7], s63 offen offset:1024 lds\nM 48,168,108\nR 166,186,28672\nG 9\nv_mfma_f32_16x16x32_bf16 v[132:135], v[240:243], v[208:211], v[132:135]\nds_read_b128 v[104:107], v108 offset:1024\n"
"M 48,172,124\nG 7\nM 52,176,108\nR 168,187,16384\nG 7\nv_mfma_f32_16x16x32_bf16 v[144:147], v[244:247], v[210:213], v[144:147]\n"
"M 52,180,124\ns_mov_b64 exec, s[66:67]\nbuffer_store_dwordx2 v[74:75], v227, s[20:23], s64 offen offset:2560 sc1\ns_mov_b64 exec, -1\nM 36,176,104\nR 170,187,20480\n"
"v_mfma_f32_16x16x32_bf16 v[148:151], v[238:241], v[210:213], v[148:151]\nM 36,180,120\ns_movk_i32 s63, 2048\nv_mad_u32_u24 v220, v228, s63, v229\nv_add_u32 v221, 1047552, v220\ns_add_u32 m0, s50, 114688\n"
"s_add_u32 s63, s53, 0\nbuffer_load_dwordx4 v220, s[8:11], s63 offen lds\nM 148,176,100\nR 172,187,24576\nG 3\nv_lshlrev_b32 v100, 16, v104\n"
"v_mfma_f32_16x16x32_bf16 v[136:139], v[242:245], v[208:211], v[136:139]\nv_and_b32 v101, s77, v104\nv_lshlrev_b32 v102, 16, v105\nv_and_b32 v103, s77, v105\nM 148,180,116\nv_lshlrev_b32 v96, 16, v106\n"
"v_and_b32 v97, s77, v106\nM 132,176,96\nR 174,187,28672\nv_lshlrev_b32 v98, 16, v107\nv_mfma_f32_16x16x32_bf16 v[140:143], v[244:247], v[208:211], v[140:143]\nv_and_b32 v99, s77, v107\n"
"v_add_f32 v104, s78, v100\nv_add_f32 v105, s78, v101\nM 132,180,112\ns_mov_b64 exec, s[66:67]\nbuffer_store_dwordx2 v[80:81], v227, s[20:23], s64 offen offset:3072 sc1\ns_mov_b64 exec, -1\n"
"v_add_f32 v106, s78, v102\nv_add_f32 v107, s78, v103\nM 136,160,96\nR 176,184,49152\nv_mfma_f32_16x16x32_bf16 v[152:155], v[240:243], v[210:213], v[152:155]\nM 136,164,112\n"
"buffer_load_dwordx4 v221, s[8:11], s63 offen offset:1024 lds\nM 152,160,100\nR 178,184,53248\nv_mfma_f32_16x16x32_bf16 v[156:159], v[242:245], v[210:213], v[156:159]\nv_lshrrev_b32 v248, 5, v252\nv_lshlrev_b32 v248, 5, v248\n"
"v_bfe_u32 v249, v252, 4, 1\ns_and_b32 s60, s41, 7\ns_lshl2_add_u32 s60, s60, s40\nv_add_u32 v248, s60, v248\nM 152,164,116\ns_sub_u32 s60, s54, s54\n"
"s_add_u32 s60, s60, 128\nv_mul_lo_u32 v249, v249, s60\nv_mul_u32_u24 v248, 7168, v248\nM 40,160,104\nR 180,184,57344\nv_add_u32 v248, v248, v249\n"
"M 40,164,120\ns_mov_b64 exec, s[66:67]\nbuffer_store_dwordx2 v[86:87], v227, s[20:23], s64 offen offset:3584 sc1\ns_mov_b64 exec, -1\nM 56,160,108\nR 182,184,61440\n"
"s_not_b64 exec, s[66:67]\nds_write_b128 v230, v[16:19] offset:0\nds_write_b128 v230, v[20:23] offset:1024\ns_mov_b64 exec, -1\nM 56,164,124\ns_not_b64 exec, s[66:67]\n"
"ds_write_b64 v231, v[68:69] offset:2048\nds_write_b64 v231, v[74:75] offset:2560\ns_mov_b64 exec, -1\nG 8\nM 60,168,108\nR 160,185,49152\n"
"s_not_b64 exec, s[66:67]\nds_write_b64 v231, v[80:81] offset:3072\nds_write_b64 v231, v[86:87] offset:3584\ns_mov_b64 exec, -1\nM 60,172,124\nM 44,168,104\n"
"R 162,185,53248\nM 44,172,120\ns_add_u32 m0, s50, 0\nM 156,168,100\nR 164,185,57344\nM 156,172,116\n"
"s_add_u32 s63, s53, 65536\nM 140,168,96\nR 166,185,61440\nM 140,172,112\nG 10\nM 128,176,128\n"
"R 168,186,49152\ns_waitcnt vmcnt(12) lgkmcnt(4)\ns_barrier\nM 128,180,144\nbuffer_load_dwordx4 v220, s[8:11], s63 offen lds\nM 144,176,132\n"
"R 170,186,53248\nM 144,180,148\ns_cmp_eq_u32 s78, 0\ns_cbranch_scc1 Lq656\nv_mov_b32 v230, 0\nv_mov_b32 v231, 0\n"
"v_mov_b32 v232, 0\nv_mov_b32 v233, 0\nv_mov_b32 v64, 0\nv_mov_b32 v65, 0\nv_mov_b32 v70, 0\nv_mov_b32 v71, 0\n"
"v_mov_b32 v234, 0\nv_mov_b32 v235, 0\nv_mov_b32 v236, 0\nv_mov_b32 v237, 0\nv_mov_b32 v76, 0\nv_mov_b32 v77, 0\n"
"v_mov_b32 v82, 0\nv_mov_b32 v83, 0\nLq656:\ns_lshl4_add_u32 s65, s41, s42\nM 32,176,136\nR 172,186,57344\n"
"s_lshl_b32 s65, s65, 12\nM 32,180,152\ns_add_u32 s65, s65, 78577664\nM 48,176,140\nR 174,186,61440\ns_bfm_b64 exec, 16, 0\n"
"buffer_load_dword v249, v248, s[12:15], s54 offen\ns_mov_b64 exec, -1\nM 48,180,156\nG 4\nM 52,160,140\nR 176,187,49152\n"
"M 52,164,156\nbuffer_load_dwordx4 v221, s[8:11], s63 offen offset:1024 lds\nM 36,160,136\nR 178,187,53248\nM 36,164,152\ns_add_u32 m0, s50, 8192\n"
"M 148,160,132\nR 180,187,57344\nM 148,164,148\ns_add_u32 s63, s53, 131072\nM 132,160,128\nR 182,187,61440\n"
"M 132,164,144\nG 4\nM 136,168,128\nR 160,188,0\nM 136,172,144\nbuffer_load_dwordx4 v220, s[8:11], s63 offen lds\n"
"M 152,168,132\nR 162,188,4096\nM 152,172,148\nM 40,168,136\nR 164,188,8192\nM 40,172,152\n"
"M 56,168,140\nR 166,188,12288\ns_bfm_b64 exec, 16, 16\nbuffer_load_dword v249, v248, s[12:15], s54 offen\ns_mov_b64 exec, -1\nM 56,172,156\n"
"G 4\nM 60,176,140\nR 168,189,0\nM 60,180,156\nbuffer_load_dwordx4 v221, s[8:11], s63 offen offset:1024 lds\nM 44,176,136\n"
"R 170,189,4096\nM 44,180,152\ns_add_u32 m0, s50, 16384\nM 156,176,132\nR 172,189,8192\nM 156,180,148\n"
"s_add_u32 s63, s53, 196608\nM 140,176,128\nR 174,189,12288\nM 140,180,144\nG 4\nM 128,160,160\n"
"R 176,190,0\nM 128,164,176\nbuffer_load_dwordx4 v220, s[8:11], s63 offen lds\nM 144,160,164\nR 178,190,4096\nM 144,164,180\n"
"M 32,160,168\nR 180,190,8192\nM 32,164,184\nM 48,160,172\nR 182,190,12288\ns_bfm_b64 exec, 16, 32\n"
"buffer_load_dword v249, v248, s[12:15], s54 offen\ns_mov_b64 exec, -1\nM 48,164,188\nG 4\nM 52,168,172\nR 160,191,0\n"
"M 52,172,188\nbuffer_load_dwordx4 v221, s[8:11], s63 offen offset:1024 lds\nM 36,168,168\nR 162,191,4096\ns_add_u32 s63, s53, 262144\nM 36,172,184\n"
"s_add_u32 m0, s50, 24576\ns_lshl_b32 s60, s40, 12\ns_add_u32 s60, s60, 65536\nv_add_u32 v226, s60, v252\nv_add_u32 v111, s60, v227\nM 148,168,164\n"
"R 164,191,8192\nM 148,172,180\nbuffer_load_dwordx4 v220, s[8:11], s63 offen lds\nM 132,168,160\nR 166,191,12288\ns_cmp_eq_u32 s40, 0\n"
"s_cselect_b64 s[86:87], 0, -1\ns_cselect_b32 s60, s41, 0\ns_cmp_lg_u32 s60, 0\ns_cselect_b64 s[66:67], -1, 0\nM 132,172,176\nG 4\n"
"M 136,176,160\nR 168,188,16384\nM 136,180,176\ns_mov_b64 exec, s[86:87]\nds_read_b128 v[230:233], v226 offset:0\nds_read_b64 v[64:65], v111 offset:2048\n"
"s_mov_b64 exec, -1\nM 152,176,164\nR 170,188,20480\ns_waitcnt vmcnt(15) lgkmcnt(1)\ns_barrier\nM 152,180,180\n"
"M 40,176,168\nR 172,188,24576\nM 40,180,184\ns_mov_b64 exec, s[86:87]\nds_read_b64 v[70:71], v111 offset:2560\nds_read_b128 v[234:237], v226 offset:1024\n"
"s_mov_b64 exec, -1\nM 56,176,172\nR 174,188,28672\ns_bfm_b64 exec, 16, 48\nbuffer_load_dword v249, v248, s[12:15], s54 offen\ns_mov_b64 exec, -1\n"
"M 56,180,188\nM 60,160,172\nR 176,189,16384\nM 60,164,188\ns_mov_b64 exec, s[86:87]\nds_read_b64 v[76:77], v111 offset:3072\n"
"ds_read_b64 v[82:83], v111 offset:3584\ns_mov_b64 exec, -1\nM 44,160,168\nR 178,189,20480\nM 44,164,184\nbuffer_load_dwordx4 v221, s[8:11], s63 offen offset:1024 lds\n"
"M 156,160,164\nR 180,189,24576\nM 156,164,180\ns_mov_b64 exec, s[66:67]\nbuffer_load_dwordx4 v[230:233], v252, s[20:23], s65 offen offset:0 sc1\ns_mov_b64 exec, -1\n"
"M 140,160,160\nR 182,189,28672\nM 140,164,176\nG 6\nM 32,168,200\nR 160,190,16384\n"
"s_mov_b64 exec, s[66:67]\nbuffer_load_dwordx2 v[64:65], v227, s[20:23], s65 offen offset:2048 sc1\ns_mov_b64 exec, -1\nM 32,172,216\nR 162,190,20480\nM 48,168,204\n"
"R 164,190,24576\nM 48,172,220\nR 166,190,28672\nG 4\nM 52,176,204\nR 168,191,16384\n"
"s_mov_b64 exec, s[66:67]\nbuffer_load_dwordx2 v[70:71], v227, s[20:23], s65 offen offset:2560 sc1\ns_mov_b64 exec, -1\nM 52,180,220\nR 170,191,20480\nM 36,176,200\n"
"R 172,191,24576\nM 36,180,216\nR 174,191,28672\nG 4\nM 40,160,200\nR 176,188,32768\n"
"s_mov_b64 exec, s[66:67]\nbuffer_load_dwordx4 v[234:237], v252, s[20:23], s65 offen offset:1024 sc1\ns_mov_b64 exec, -1\nM 40,164,216\nR 178,188,36864\nM 56,160,204\n"
"R 180,188,40960\nM 56,164,220\nR 182,188,45056\nG 4\nM 60,168,204\nR 160,189,32768\n"
"s_mov_b64 exec, s[66:67]\nbuffer_load_dwordx2 v[76:77], v227, s[20:23], s65 offen offset:3072 sc1\ns_mov_b64 exec, -1\nM 60,172,220\nR 162,189,36864\nM 44,168,200\n"
"R 164,189,40960\nM 44,172,216\nR 166,189,45056\nG 4\nM 32,176,232\nR 168,190,32768\n"
"s_mov_b64 exec, s[66:67]\nbuffer_load_dwordx2 v[82:83], v227, s[20:23], s65 offen offset:3584 sc1\ns_mov_b64 exec, -1\nLq690:\ns_lshr_b32 s60, s45, 3\ns_lshl2_add_u32 s60, s60, s40\n"
"s_lshl_b32 s60, s60, 8\nv_lshl_add_u32 v224, v252, 3, s60\nv_add_u32 v224, Lq690+4096-LqPC, v224\nv_min_u32 v224, LqE-LqPC-4, v224\ns_mov_b64 exec, 3\nglobal_load_dword v224, v224, s[98:99]\n"
"s_mov_b64 exec, -1\nM 32,180,248\nR 170,190,36864\nM 48,176,236\nR 172,190,40960\nM 48,180,252\n"
"R 174,190,45056\nG 4\nM 52,160,236\nR 176,191,32768\nM 52,164,252\nR 178,191,36864\n"
"M 36,160,232\nR 180,191,40960\nM 36,164,248\nR 182,191,45056\nG 4\nM 40,168,232\n"
"R 160,188,16384\nM 40,172,248\nR 162,188,20480\nM 56,168,236\nR 164,188,24576\nM 56,172,252\n"
"R 166,188,28672\nG 4\nM 60,176,236\nR 168,189,16384\nM 60,180,252\nR 170,189,20480\n"
"M 44,176,232\nR 172,189,24576\nM 44,180,248\nR 174,189,28672\nG 4\nM 128,160,192\n"
"R 176,190,16384\nM 128,164,208\nR 178,190,20480\ns_mov_b32 m0, s51\nM 144,160,196\nR 180,190,24576\n"
"ds_write_addtid_b32 v32 offset:0\nM 144,164,212\nR 182,190,28672\nds_write_addtid_b32 v33 offset:256\nG 6\nM 148,168,196\n"
"R 160,191,16384\nds_write_addtid_b32 v34 offset:512\nM 148,172,212\nR 162,191,20480\nds_write_addtid_b32 v35 offset:768\nM 132,168,192\n"
"R 164,191,24576\nds_write_addtid_b32 v36 offset:1024\nM 132,172,208\nR 166,191,28672\nds_write_addtid_b32 v37 offset:1280\nG 9\n"
"M 136,176,192\nR 168,188,32768\nds_write_addtid_b32 v38 offset:1536\nM 136,180,208\nR 170,188,36864\nds_write_addtid_b32 v39 offset:1792\n"
"M 152,176,196\nR 172,188,40960\nds_write_addtid_b32 v40 offset:2048\nM 152,180,212\nR 174,188,45056\nds_write_addtid_b32 v41 offset:2304\n"
"G 9\nM 156,160,196\nR 176,189,32768\nds_write_addtid_b32 v42 offset:2560\nM 156,164,212\nR 178,189,36864\n"
"ds_write_addtid_b32 v43 offset:2816\nM 140,160,192\nR 180,189,40960\nds_write_addtid_b32 v44 offset:3072\nds_write_addtid_b32 v45 offset:3328\nds_write_addtid_b32 v46 offset:3584\n"
"ds_write_addtid_b32 v47 offset:3840\nds_write_addtid_b32 v48 offset:4096\nds_write_addtid_b32 v49 offset:4352\nds_write_addtid_b32 v50 offset:4608\nds_write_addtid_b32 v51 offset:4864\nds_write_addtid_b32 v52 offset:5120\n"
"ds_write_addtid_b32 v53 offset:5376\nds_write_addtid_b32 v54 offset:5632\nds_write_addtid_b32 v55 offset:5888\nds_write_addtid_b32 v56 offset:6144\nds_write_addtid_b32 v57 offset:6400\nds_write_addtid_b32 v58 offset:6656\n"
"ds_write_addtid_b32 v59 offset:6912\nds_write_addtid_b32 v60 offset:7168\nds_write_addtid_b32 v61 offset:7424\nds_write_addtid_b32 v62 offset:7680\nds_write_addtid_b32 v63 offset:7936\nM 140,164,208\n"
"R 182,189,45056\nG 15\nM 128,168,224\nR 160,190,32768\ns_add_u32 m0, s50, 32768\nM 128,172,240\n"
"R 162,190,36864\nG 3\ns_barrier\ns_add_u32 s63, s53, 327680\nM 144,168,228\nR 164,190,40960\n"
"buffer_load_dwordx4 v220, s[8:11], s63 offen lds\nds_read2st64_b32 v[32:33], v225 offset0:0 offset1:1\nM 144,172,244\nR 166,190,45056\nbuffer_load_dwordx4 v221, s[8:11], s63 offen offset:1024 lds\nds_read2st64_b32 v[34:35], v225 offset0:2 offset1:3\n"
"G 6\nM 148,176,228\nR 168,191,32768\ns_add_u32 m0, s50, 40960\nds_read2st64_b32 v[36:37], v225 offset0:4 offset1:5\nM 148,180,244\n"
"R 170,191,36864\ns_add_u32 s63, s53, 393216\nds_read2st64_b32 v[38:39], v225 offset0:6 offset1:7\nM 132,176,224\nR 172,191,40960\nds_read2st64_b32 v[40:41], v225 offset0:8 offset1:9\n"
"M 132,180,240\nR 174,191,45056\nds_read2st64_b32 v[42:43], v225 offset0:10 offset1:11\nG 9\nM 136,160,224\nbuffer_load_dwordx4 v220, s[8:11], s63 offen lds\n"
"ds_read2st64_b32 v[44:45], v225 offset0:12 offset1:13\nM 136,164,240\nds_read2st64_b32 v[46:47], v225 offset0:14 offset1:15\nds_read2st64_b32 v[48:49], v225 offset0:16 offset1:17\nM 152,160,228\nbuffer_load_dwordx4 v221, s[8:11], s63 offen offset:1024 lds\n"
"ds_read2st64_b32 v[50:51], v225 offset0:18 offset1:19\nM 152,164,244\nds_read2st64_b32 v[52:53], v225 offset0:20 offset1:21\nds_read2st64_b32 v[54:55], v225 offset0:22 offset1:23\nG 7\nM 156,168,228\n"
"ds_read2st64_b32 v[56:57], v225 offset0:24 offset1:25\nds_read2st64_b32 v[58:59], v225 offset0:26 offset1:27\nM 156,172,244\nds_read2st64_b32 v[60:61], v225 offset0:28 offset1:29\nds_read2st64_b32 v[62:63], v225 offset0:30 offset1:31\nM 140,168,224\n"
"M 140,172,240\nG 7\nv_pk_add_f32 v[144:145], v[144:145], v[48:49]\nG 6\nv_pk_add_f32 v[146:147], v[146:147], v[50:51]\nG 5\n"
"v_pk_add_f32 v[148:149], v[148:149], v[52:53]\nG 4\nv_pk_add_f32 v[150:151], v[150:151], v[54:55]\nG 3\nv_pk_add_f32 v[152:153], v[152:153], v[56:57]\nG 2\n"
"v_pk_add_f32 v[154:155], v[154:155], v[58:59]\nG 1\nv_pk_add_f32 v[156:157], v[156:157], v[60:61]\nG 0\nv_pk_add_f32 v[158:159], v[158:159], v[62:63]\nv_pk_add_f32 v[128:129], v[128:129], v[32:33]\n"
"v_pk_add_f32 v[130:131], v[130:131], v[34:35]\nv_pk_add_f32 v[132:133], v[132:133], v[36:37]\nv_pk_add_f32 v[134:135], v[134:135], v[38:39]\nv_pk_add_f32 v[136:137], v[136:137], v[40:41]\nv_pk_add_f32 v[138:139], v[138:139], v[42:43]\nv_pk_add_f32 v[140:141], v[140:141], v[44:45]\n"
"v_pk_add_f32 v[142:143], v[142:143], v[46:47]\nv_cvt_pk_bf16_f32 v144, v144, v145\nv_cvt_pk_bf16_f32 v145, v146, v147\nv_cvt_pk_bf16_f32 v146, v148, v149\nv_cvt_pk_bf16_f32 v147, v150, v151\nv_cvt_pk_bf16_f32 v152, v152, v153\n"
"v_cvt_pk_bf16_f32 v153, v154, v155\nv_cvt_pk_bf16_f32 v154, v156, v157\nv_cvt_pk_bf16_f32 v155, v158, v159\nv_cvt_pk_bf16_f32 v128, v128, v129\nv_cvt_pk_bf16_f32 v129, v130, v131\nv_cvt_pk_bf16_f32 v130, v132, v133\n"
"v_cvt_pk_bf16_f32 v131, v134, v135\nv_cvt_pk_bf16_f32 v136, v136, v137\nv_cvt_pk_bf16_f32 v137, v138, v139\nv_cvt_pk_bf16_f32 v138, v140, v141\nv_cvt_pk_bf16_f32 v139, v142, v143\nv_mfma_f32_16x16x32_bf16 v[52:55], v[16:19], v[144:147], v[96:99]\n"
"v_mfma_f32_16x16x32_bf16 v[48:51], v[0:3], v[144:147], v[100:103]\nv_mfma_f32_16x16x32_bf16 v[60:63], v[20:23], v[152:155], v[96:99]\nv_mfma_f32_16x16x32_bf16 v[56:59], v[4:7], v[152:155], v[100:103]\ns_waitcnt vmcnt(8)\nv_max3_u32 v248, v233, v65, v71\nv_cmp_eq_u32 vcc, -1, v248\n"
"s_nop 0\ns_cbranch_vccnz Lq692\nLq693:\ns_waitcnt vmcnt(5)\nv_max3_u32 v248, v237, v77, v83\nv_cmp_eq_u32 vcc, -1, v248\n"
"s_nop 0\ns_cbranch_vccnz Lq694\nLq695:\nv_mfma_f32_16x16x32_bf16 v[36:39], v[0:3], v[128:131], v[96:99]\nv_mfma_f32_16x16x32_bf16 v[32:35], v[230:233], v[128:131], v[104:107]\nv_mfma_f32_16x16x32_bf16 v[44:47], v[4:7], v[136:139], v[96:99]\n"
"v_mfma_f32_16x16x32_bf16 v[40:43], v[234:237], v[136:139], v[104:107]\ns_lshl_b32 s84, s42, 11\ns_add_u32 s84, s84, s49\ns_add_u32 s84, s84, 6291456\ns_add_u32 s85, s84, 32768\nv_mov_b32 v238, s46\n"
"v_mov_b32 v239, s47\nv_max_f32 v52, v52, v53\nv_max_f32 v54, v54, v55\nv_max_f32 v60, v60, v61\nv_max_f32 v62, v62, v63\nLpg22:\n"
"s_cbranch_execz LretW0n\nv_max3_f32 v52, v52, v48, v49\nv_max3_f32 v54, v54, v50, v51\nv_max3_f32 v60, v60, v56, v57\nv_max3_f32 v62, v62, v58, v59\nv_max_f32 v36, v36, v37\n"
"v_max_f32 v38, v38, v39\nv_max_f32 v44, v44, v45\nv_max_f32 v46, v46, v47\nv_max3_f32 v36, v36, v32, v33\nv_max3_f32 v38, v38, v34, v35\nv_max3_f32 v44, v44, v40, v41\n"
"v_max3_f32 v46, v46, v42, v43\nv_max_f32 v118, v52, v54\nv_max_f32 v119, v60, v62\nv_max_f32 v116, v36, v38\nv_max_f32 v117, v44, v46\ns_nop 0\n"
"v_permlane32_swap_b32 v116, v118\nv_permlane32_swap_b32 v117, v119\nv_max_f32 v116, v116, v118\nv_max_f32 v117, v117, v119\ns_nop 1\nv_permlane16_swap_b32 v116, v117\n"
"v_max_f32 v116, v116, v117\nv_mov_b32 v117, v116\ns_nop 1\nv_permlane16_swap_b32 v116, v117\nv_mov_b32 v118, v116\nv_mov_b32 v119, v117\n"
"s_nop 0\nv_permlane32_swap_b32 v116, v118\nv_permlane32_swap_b32 v117, v119\nv_mul_f32 v116, v116, v215\nv_mul_f32 v118, v118, v218\nv_mul_f32 v117, v117, v215\n"
"v_mul_f32 v119, v119, v218\nv_fma_f32 v52, v52, v218, -v118\nv_fma_f32 v54, v54, v218, -v118\nv_fma_f32 v60, v60, v218, -v119\nv_fma_f32 v62, v62, v218, -v119\nv_fma_f32 v36, v36, v215, -v116\n"
"v_fma_f32 v38, v38, v215, -v116\nv_fma_f32 v44, v44, v215, -v117\nv_fma_f32 v46, v46, v215, -v117\nv_exp_f32 v52, v52\nv_exp_f32 v54, v54\nv_exp_f32 v60, v60\n"
"v_exp_f32 v62, v62\nv_exp_f32 v36, v36\nv_exp_f32 v38, v38\nv_exp_f32 v44, v44\nv_exp_f32 v46, v46\nv_add_f32 v118, v52, v54\n"
"v_add_f32 v119, v60, v62\nv_add_f32 v116, v36, v38\nv_add_f32 v117, v44, v46\ns_nop 0\nv_permlane32_swap_b32 v116, v118\nv_permlane32_swap_b32 v117, v119\n"
"v_add_f32 v116, v116, v118\nv_add_f32 v117, v117, v119\ns_nop 1\nv_permlane16_swap_b32 v116, v117\nv_add_f32 v116, v116, v117\ns_barrier\n"
"v_rcp_f32 v116, v116\ns_nop 0\nv_mov_b32 v117, v116\ns_nop 1\nv_permlane16_swap_b32 v116, v117\nv_mov_b32 v118, v116\n"
"v_mov_b32 v119, v117\ns_nop 0\nv_permlane32_swap_b32 v116, v118\nv_permlane32_swap_b32 v117, v119\nv_mul_f32 v52, v52, v118\nv_mul_f32 v54, v54, v118\n"
"v_mul_f32 v60, v60, v119\nv_mul_f32 v62, v62, v119\nv_mul_f32 v36, v36, v116\nv_mul_f32 v38, v38, v116\nv_mul_f32 v44, v44, v117\nv_mul_f32 v46, v46, v117\n"
"v_cvt_pk_bf16_f32 v52, v52, v52\nv_cvt_pk_bf16_f32 v54, v54, v54\nv_cvt_pk_bf16_f32 v60, v60, v60\nv_cvt_pk_bf16_f32 v62, v62, v62\nv_cvt_pk_bf16_f32 v36, v36, v36\nv_cvt_pk_bf16_f32 v38, v38, v38\n"
"v_cvt_pk_bf16_f32 v44, v44, v44\nv_cvt_pk_bf16_f32 v46, v46, v46\nv_and_b32 v48, v52, v112\nv_and_b32 v49, v54, v113\nv_and_b32 v50, v52, v114\nv_and_b32 v51, v54, v115\n"
"v_and_b32 v56, v60, v112\nv_and_b32 v57, v62, v113\nv_and_b32 v58, v60, v114\nv_and_b32 v59, v62, v115\nv_mfma_f32_16x16x32_bf16 v[144:147], v[66:69], v[48:51], 0\nv_mfma_f32_16x16x32_bf16 v[148:151], v[72:75], v[48:51], 0\n"
"v_and_b32 v32, v36, v112\nv_and_b32 v33, v38, v113\nv_and_b32 v34, v36, v114\nv_and_b32 v35, v38, v115\nv_mfma_f32_16x16x32_bf16 v[152:155], v[78:81], v[56:59], 0\nv_mfma_f32_16x16x32_bf16 v[156:159], v[84:87], v[56:59], 0\n"
"v_and_b32 v40, v44, v112\nv_and_b32 v41, v46, v113\nv_and_b32 v42, v44, v114\nv_and_b32 v43, v46, v115\nv_mfma_f32_16x16x32_bf16 v[128:131], v[64:67], v[32:35], 0\nv_mfma_f32_16x16x32_bf16 v[132:135], v[70:73], v[32:35], 0\n"
"v_mfma_f32_16x16x32_bf16 v[136:139], v[76:79], v[40:43], 0\nv_mfma_f32_16x16x32_bf16 v[140:143], v[82:85], v[40:43], 0\nv_cvt_pk_bf16_f32 v144, v144, v145\nv_cvt_pk_bf16_f32 v145, v146, v147\nv_cvt_pk_bf16_f32 v146, v148, v149\nv_cvt_pk_bf16_f32 v147, v150, v151\n"
"buffer_store_dwordx4 v[144:147], v252, s[20:23], s85 offen offset:0 sc1\nv_cvt_pk_bf16_f32 v152, v152, v153\nv_cvt_pk_bf16_f32 v153, v154, v155\nv_cvt_pk_bf16_f32 v154, v156, v157\nv_cvt_pk_bf16_f32 v155, v158, v159\nbuffer_store_dwordx4 v[152:155], v252, s[20:23], s85 offen offset:1024 sc1\n"
"v_cvt_pk_bf16_f32 v128, v128, v129\nv_cvt_pk_bf16_f32 v129, v130, v131\nv_cvt_pk_bf16_f32 v130, v132, v133\nv_cvt_pk_bf16_f32 v131, v134, v135\nbuffer_store_dwordx4 v[128:131], v252, s[20:23], s84 offen offset:0 sc1\nv_cvt_pk_bf16_f32 v136, v136, v137\n"
"v_cvt_pk_bf16_f32 v137, v138, v139\nv_cvt_pk_bf16_f32 v138, v140, v141\nv_cvt_pk_bf16_f32 v139, v142, v143\nbuffer_store_dwordx4 v[136:139], v252, s[20:23], s84 offen offset:1024 sc1\ns_mov_b64 exec, 0\ns_cmp_eq_u32 s40, 1\n"
"s_cbranch_scc1 LretO1\ns_cmp_eq_u32 s40, 2\ns_cbranch_scc1 LretO1\ns_cmp_eq_u32 s40, 3\ns_cbranch_scc1 LretO1\ns_branch Lpg23\n"
"LretO1:\ns_mov_b64 exec, -1\ns_add_u32 s61, s79, 88498176\ns_lshl_b32 s62, s42, 3\ns_add_u32 s61, s61, s62\ns_waitcnt vmcnt(0)\n"
"s_mov_b64 exec, 1\nbuffer_atomic_swap_x2 v[238:239], off, s[20:23], s61 sc1\ns_mov_b64 exec, -1\nv_bfe_u32 v250, v252, 7, 1\nv_mul_u32_u24 v250, 2176, v250\nv_bfe_u32 v251, v252, 4, 3\n"
"v_lshl_add_u32 v250, v251, 3, v250\ns_lshl_b32 s60, s90, 6\nv_add_u32 v250, s60, v250\ns_sleep 3\nv_mov_b32 v248, 0\nv_mov_b32 v249, 0\n"
"s_add_u32 s61, s80, 88498176\ns_mov_b64 exec, 0xffff\nbuffer_atomic_or_x2 v[248:249], v250, s[20:23], s61 offen sc0 sc1\ns_mov_b64 exec, -1\ns_add_u32 m0, s50, 49152\ns_add_u32 s63, s53, 458752\n"
"buffer_load_dwordx4 v220, s[8:11], s63 offen lds\nbuffer_load_dwordx4 v221, s[8:11], s63 offen offset:1024 lds\ns_add_u32 m0, s50, 57344\ns_add_u32 s63, s53, 524288\nbuffer_load_dwordx4 v220, s[8:11], s63 offen lds\nbuffer_load_dwordx4 v221, s[8:11], s63 offen offset:1024 lds\n"
"s_add_u32 m0, s50, 65536\ns_add_u32 s63, s53, 589824\nbuffer_load_dwordx4 v220, s[8:11], s63 offen lds\nbuffer_load_dwordx4 v221, s[8:11], s63 offen offset:1024 lds\ns_add_u32 m0, s50, 73728\ns_add_u32 s63, s53, 655360\n"
"buffer_load_dwordx4 v220, s[8:11], s63 offen lds\nbuffer_load_dwordx4 v221, s[8:11], s63 offen offset:1024 lds\ns_add_u32 m0, s50, 81920\ns_add_u32 s63, s53, 720896\nbuffer_load_dwordx4 v220, s[8:11], s63 offen lds\nbuffer_load_dwordx4 v221, s[8:11], s63 offen offset:1024 lds\n"
"s_waitcnt vmcnt(10)\nv_cmp_eq_u64 s[86:87], s[46:47], v[248:249]\ns_nop 1\ns_and_b32 s88, s86, 0xffff\ns_cmp_eq_u32 s88, 0xffff\ns_cbranch_scc1 Lq706\n"
"v_mov_b32 v246, 0\nv_mov_b32 v247, 0\ns_add_u32 s61, s80, 88498176\ns_mov_b64 exec, 0xffff\nbuffer_atomic_or_x2 v[246:247], v250, s[20:23], s61 offen sc0 sc1\ns_mov_b64 exec, -1\n"
"Lq706:\ns_add_u32 m0, s50, 90112\ns_add_u32 s63, s53, 786432\nbuffer_load_dwordx4 v220, s[8:11], s63 offen lds\nbuffer_load_dwordx4 v221, s[8:11], s63 offen offset:1024 lds\ns_add_u32 m0, s50, 98304\n"
"s_add_u32 s63, s53, 851968\nbuffer_load_dwordx4 v220, s[8:11], s63 offen lds\nbuffer_load_dwordx4 v221, s[8:11], s63 offen offset:1024 lds\ns_add_u32 m0, s50, 106496\ns_add_u32 s63, s53, 917504\nbuffer_load_dwordx4 v220, s[8:11], s63 offen lds\n"
"buffer_load_dwordx4 v221, s[8:11], s63 offen offset:1024 lds\nv_bfe_u32 v234, v252, 4, 4\nv_lshrrev_b32 v235, 8, v252\nv_lshlrev_b32 v235, 2, v235\nv_sub_u32 v234, v234, v235\nv_lshlrev_b32 v235, 4, v234\n"
"s_mov_b64 s[60:61], 0x3f80\nv_lshlrev_b64 v[208:209], v235, s[60:61]\nv_cmp_gt_u32 s[60:61], 4, v234\ns_nop 1\nv_cndmask_b32 v212, 0, v208, s[60:61]\nv_cndmask_b32 v208, 0, v208, s[60:61]\n"
"v_cndmask_b32 v213, 0, v209, s[60:61]\nv_cndmask_b32 v209, 0, v209, s[60:61]\ns_nop 1\nv_mfma_f32_16x16x32_bf16 v[0:3], v[208:211], v[192:195], 0\nv_mfma_f32_16x16x32_bf16 v[4:7], v[210:213], v[192:195], 0\nv_mfma_f32_16x16x32_bf16 v[8:11], v[208:211], v[196:199], 0\n"
"v_mfma_f32_16x16x32_bf16 v[12:15], v[210:213], v[196:199], 0\nv_mfma_f32_16x16x32_bf16 v[16:19], v[208:211], v[200:203], 0\nv_mfma_f32_16x16x32_bf16 v[20:23], v[210:213], v[200:203], 0\nv_mfma_f32_16x16x32_bf16 v[24:27], v[208:211], v[204:207], 0\nv_mfma_f32_16x16x32_bf16 v[28:31], v[210:213], v[204:207], 0\ns_cmp_eq_u32 s88, 0xffff\n"
"s_cbranch_scc1 Lq707\ns_waitcnt vmcnt(6)\nv_mov_b32 v248, v246\nv_mov_b32 v249, v247\nv_cmp_eq_u64 s[86:87], s[46:47], v[248:249]\ns_nop 1\n"
"s_and_b32 s62, s86, 0xffff\ns_cmp_eq_u32 s62, 0xffff\ns_cbranch_scc1 Lq698\nLq697:\nv_mov_b32 v248, 0\nv_mov_b32 v249, 0\n"
"s_add_u32 s61, s80, 88498176\ns_mov_b64 exec, 0xffff\nbuffer_atomic_or_x2 v[248:249], v250, s[20:23], s61 offen sc0 sc1\ns_mov_b64 exec, -1\ns_waitcnt vmcnt(0)\nv_cmp_eq_u64 s[86:87], s[46:47], v[248:249]\n"
"s_nop 1\ns_and_b32 s62, s86, 0xffff\ns_cmp_eq_u32 s62, 0xffff\ns_cbranch_scc1 Lq698\ns_sub_u32 s48, s48, 1\ns_cmp_gt_i32 s48, 0\n"
"s_cbranch_scc1 Lq697\nLq698:\nLq707:\ns_add_u32 s64, s70, 6291456\nbuffer_load_dwordx4 a[0:3], v252, s[20:23], s64 offen offset:0 sc1\ns_add_u32 s65, s70, 6324224\n"
"buffer_load_dwordx4 a[4:7], v252, s[20:23], s65 offen offset:0 sc1\ns_add_u32 s66, s71, 6291456\nbuffer_load_dwordx4 a[8:11], v252, s[20:23], s66 offen offset:0 sc1\ns_add_u32 s67, s71, 6324224\nbuffer_load_dwordx4 a[12:15], v252, s[20:23], s67 offen offset:0 sc1\nbuffer_load_dwordx4 a[16:19], v252, s[20:23], s64 offen offset:1024 sc1\n"
"buffer_load_dwordx4 a[20:23], v252, s[20:23], s65 offen offset:1024 sc1\nbuffer_load_dwordx4 a[24:27], v252, s[20:23], s66 offen offset:1024 sc1\nbuffer_load_dwordx4 a[28:31], v252, s[20:23], s67 offen offset:1024 sc1\ns_barrier\nR 160,188,49152\nR 162,188,53248\n"
"R 164,184,0\nR 166,184,4096\nR 168,189,49152\nR 170,189,53248\nR 172,185,0\nR 174,185,4096\n"
"R 176,190,49152\nR 178,190,53248\nR 180,186,0\nR 182,186,4096\nbuffer_load_dwordx4 a[32:35], v252, s[20:23], s64 offen offset:2048 sc1\nbuffer_load_dwordx4 a[36:39], v252, s[20:23], s65 offen offset:2048 sc1\n"
"buffer_load_dwordx4 a[40:43], v252, s[20:23], s66 offen offset:2048 sc1\nbuffer_load_dwordx4 a[44:47], v252, s[20:23], s67 offen offset:2048 sc1\nbuffer_load_dwordx4 a[48:51], v252, s[20:23], s64 offen offset:3072 sc1\nbuffer_load_dwordx4 a[52:55], v252, s[20:23], s65 offen offset:3072 sc1\nbuffer_load_dwordx4 a[56:59], v252, s[20:23], s66 offen offset:3072 sc1\nbuffer_load_dwordx4 a[60:63], v252, s[20:23], s67 offen offset:3072 sc1\n"
"s_add_u32 s64, s70, 6295552\nbuffer_load_dwordx4 a[64:67], v252, s[20:23], s64 offen offset:0 sc1\ns_add_u32 s65, s70, 6328320\nbuffer_load_dwordx4 a[68:71], v252, s[20:23], s65 offen offset:0 sc1\ns_add_u32 s66, s71, 6295552\nbuffer_load_dwordx4 a[72:75], v252, s[20:23], s66 offen offset:0 sc1\n"
"s_add_u32 s67, s71, 6328320\nbuffer_load_dwordx4 a[76:79], v252, s[20:23], s67 offen offset:0 sc1\ns_waitcnt vmcnt(19) lgkmcnt(10)\nM 0,160,0\nbuffer_load_dwordx4 a[80:83], v252, s[20:23], s64 offen offset:1024 sc1\ns_waitcnt vmcnt(16) lgkmcnt(8)\n"
"M 0,164,16\nM 16,160,4\ns_waitcnt vmcnt(15)\nM 16,164,20\nv_mfma_f32_16x16x32_bf16 v[32:35], v[160:163], a[8:11], 0\nbuffer_load_dwordx4 a[84:87], v252, s[20:23], s65 offen offset:1024 sc1\n"
"s_waitcnt vmcnt(15)\nM 32,164,24\nv_mfma_f32_16x16x32_bf16 v[48:51], v[160:163], a[12:15], 0\ns_waitcnt vmcnt(14)\nM 48,164,28\nG 4\n"
"v_mfma_f32_16x16x32_bf16 v[52:55], v[168:171], a[12:15], 0\nR 160,191,49152\nM 52,172,28\nbuffer_load_dwordx4 a[88:91], v252, s[20:23], s66 offen offset:1024 sc1\nv_mfma_f32_16x16x32_bf16 v[36:39], v[168:171], a[8:11], 0\nR 162,191,53248\n"
"M 36,172,24\nM 20,168,4\nR 164,187,0\nM 20,172,20\nbuffer_load_dwordx4 a[92:95], v252, s[20:23], s67 offen offset:1024 sc1\nM 4,168,0\n"
"R 166,187,4096\nM 4,172,16\nG 4\nM 8,176,0\nR 168,184,8192\nM 8,180,16\n"
"buffer_load_dwordx4 a[96:99], v252, s[20:23], s64 offen offset:2048 sc1\nM 24,176,4\nR 170,184,12288\nM 24,180,20\nv_mfma_f32_16x16x32_bf16 v[40:43], v[176:179], a[8:11], 0\nR 172,184,16384\n"
"M 40,180,24\nbuffer_load_dwordx4 a[100:103], v252, s[20:23], s65 offen offset:2048 sc1\nv_mfma_f32_16x16x32_bf16 v[56:59], v[176:179], a[12:15], 0\nR 174,184,20480\nM 56,180,28\nG 4\n"
"v_mfma_f32_16x16x32_bf16 v[60:63], v[160:163], a[12:15], 0\nR 176,185,8192\nM 60,164,28\nbuffer_load_dwordx4 a[104:107], v252, s[20:23], s66 offen offset:2048 sc1\nv_mfma_f32_16x16x32_bf16 v[44:47], v[160:163], a[8:11], 0\nR 178,185,12288\n"
"M 44,164,24\nM 28,160,4\nR 180,185,16384\nM 28,164,20\nbuffer_load_dwordx4 a[108:111], v252, s[20:23], s67 offen offset:2048 sc1\nM 12,160,0\n"
"R 182,185,20480\nM 12,164,16\nG 4\ns_waitcnt vmcnt(19)\nM 0,168,32\nR 160,186,8192\n"
"s_waitcnt vmcnt(15)\nM 0,172,48\nbuffer_load_dwordx4 a[112:115], v252, s[20:23], s64 offen offset:3072 sc1\nM 16,168,36\nR 162,186,12288\ns_waitcnt vmcnt(15)\n"
"M 16,172,52\nM 32,168,40\nR 164,186,16384\ns_waitcnt vmcnt(14)\nM 32,172,56\nbuffer_load_dwordx4 a[116:119], v252, s[20:23], s65 offen offset:3072 sc1\n"
"M 48,168,44\nR 166,186,20480\ns_waitcnt vmcnt(14)\nM 48,172,60\nG 4\nM 52,176,44\n"
"R 168,187,8192\nM 52,180,60\nbuffer_load_dwordx4 a[120:123], v252, s[20:23], s66 offen offset:3072 sc1\nM 36,176,40\nR 170,187,12288\nM 36,180,56\n"
"M 20,176,36\nR 172,187,16384\nM 20,180,52\nbuffer_load_dwordx4 a[124:127], v252, s[20:23], s67 offen offset:3072 sc1\nM 4,176,32\nR 174,187,20480\n"
"M 4,180,48\nG 4\nM 8,160,32\nR 176,184,24576\nM 8,164,48\ns_add_u32 s64, s70, 6299648\n"
"buffer_load_dwordx4 a[128:131], v252, s[20:23], s64 offen offset:0 sc1\nM 24,160,36\nR 178,184,28672\nM 24,164,52\nM 40,160,40\nR 180,184,32768\n"
"M 40,164,56\ns_add_u32 s65, s70, 6332416\nbuffer_load_dwordx4 a[132:135], v252, s[20:23], s65 offen offset:0 sc1\nM 56,160,44\nR 182,184,36864\nM 56,164,60\n"
"G 4\nM 60,168,44\nR 160,185,24576\nM 60,172,60\ns_add_u32 s66, s71, 6299648\nbuffer_load_dwordx4 a[136:139], v252, s[20:23], s66 offen offset:0 sc1\n"
"M 44,168,40\nR 162,185,28672\nM 44,172,56\nM 28,168,36\nR 164,185,32768\nM 28,172,52\n"
"s_add_u32 s67, s71, 6332416\nbuffer_load_dwordx4 a[140:143], v252, s[20:23], s67 offen offset:0 sc1\nM 12,168,32\nR 166,185,36864\nM 12,172,48\ns_barrier\n"
"G 4\ns_waitcnt vmcnt(19)\nM 0,176,64\nR 168,186,24576\ns_waitcnt vmcnt(15)\nM 0,180,80\n"
"buffer_load_dwordx4 a[144:147], v252, s[20:23], s64 offen offset:1024 sc1\nM 16,176,68\nR 170,186,28672\ns_waitcnt vmcnt(15)\nM 16,180,84\nM 32,176,72\n"
"R 172,186,32768\ns_waitcnt vmcnt(14)\nM 32,180,88\nbuffer_load_dwordx4 a[148:151], v252, s[20:23], s65 offen offset:1024 sc1\nM 48,176,76\nR 174,186,36864\n"
"s_waitcnt vmcnt(14)\nM 48,180,92\nG 4\nM 52,160,76\nR 176,187,24576\nM 52,164,92\n"
"buffer_load_dwordx4 a[152:155], v252, s[20:23], s66 offen offset:1024 sc1\nM 36,160,72\nR 178,187,28672\nM 36,164,88\nM 20,160,68\nR 180,187,32768\n"
"M 20,164,84\nbuffer_load_dwordx4 a[156:159], v252, s[20:23], s67 offen offset:1024 sc1\nM 4,160,64\nR 182,187,36864\nM 4,164,80\nG 4\n"
"M 8,168,64\nR 160,184,40960\nM 8,172,80\nbuffer_load_dwordx4 a[160:163], v252, s[20:23], s64 offen offset:2048 sc1\nM 24,168,68\nR 162,184,45056\n"
"M 24,172,84\nM 40,168,72\nR 164,184,49152\nM 40,172,88\nbuffer_load_dwordx4 a[164:167], v252, s[20:23], s65 offen offset:2048 sc1\nM 56,168,76\n"
"R 166,184,53248\nM 56,172,92\nG 4\nM 60,176,76\nR 168,185,40960\nM 60,180,92\n"
"buffer_load_dwordx4 a[168:171], v252, s[20:23], s66 offen offset:2048 sc1\nM 44,176,72\nR 170,185,45056\nM 44,180,88\nM 28,176,68\nR 172,185,49152\n"
"M 28,180,84\nbuffer_load_dwordx4 a[172:175], v252, s[20:23], s67 offen offset:2048 sc1\nM 12,176,64\nR 174,185,53248\nM 12,180,80\nG 4\n"
"s_waitcnt vmcnt(19)\nM 0,160,96\nR 176,186,40960\ns_waitcnt vmcnt(15)\nM 0,164,112\nbuffer_load_dwordx4 a[176:179], v252, s[20:23], s64 offen offset:3072 sc1\n"
"M 16,160,100\nR 178,186,45056\ns_waitcnt vmcnt(15)\nM 16,164,116\nM 32,160,104\nR 180,186,49152\n"
"s_lshr_b32 s60, s45, 3\ns_lshl2_add_u32 s60, s60, s40\ns_lshl_b32 s60, s60, 8\nv_lshl_add_u32 v224, v252, 3, s60\nv_add_u32 v224, Lqw1+24576-LqPC, v224\nv_min_u32 v224, LqE-LqPC-4, v224\n"
"s_mov_b64 exec, 3\nglobal_load_dword v224, v224, s[98:99]\ns_mov_b64 exec, -1\ns_waitcnt vmcnt(15)\nM 32,164,120\nbuffer_load_dwordx4 a[180:183], v252, s[20:23], s65 offen offset:3072 sc1\n"
"M 48,160,108\nR 182,186,53248\ns_waitcnt vmcnt(15)\nM 48,164,124\nG 4\nM 52,168,108\n"
"R 160,187,40960\nM 52,172,124\nbuffer_load_dwordx4 a[184:187], v252, s[20:23], s66 offen offset:3072 sc1\nM 36,168,104\nR 162,187,45056\nM 36,172,120\n"
"M 20,168,100\nR 164,187,49152\nM 20,172,116\nbuffer_load_dwordx4 a[188:191], v252, s[20:23], s67 offen offset:3072 sc1\nM 4,168,96\nR 166,187,53248\n"
"M 4,172,112\nG 4\nM 8,176,96\nR 168,184,57344\nM 8,180,112\ns_add_u32 s64, s70, 6303744\n"
"buffer_load_dwordx4 a[192:195], v252, s[20:23], s64 offen offset:0 sc1\nM 24,176,100\nR 170,184,61440\ns_add_u32 s63, s53, 983040\nM 24,180,116\ns_add_u32 m0, s50, 114688\n"
"M 40,176,104\nR 172,188,0\nM 40,180,120\ns_add_u32 s65, s70, 6336512\nbuffer_load_dwordx4 a[196:199], v252, s[20:23], s65 offen offset:0 sc1\nM 56,176,108\n"
"R 174,188,4096\nM 56,180,124\nbuffer_load_dwordx4 v220, s[8:11], s63 offen lds\nG 4\nM 60,160,108\nR 176,185,57344\n"
"M 60,164,124\ns_add_u32 s66, s71, 6303744\nbuffer_load_dwordx4 a[200:203], v252, s[20:23], s66 offen offset:0 sc1\nM 44,160,104\nR 178,185,61440\nM 44,164,120\n"
"buffer_load_dwordx4 v221, s[8:11], s63 offen offset:1024 lds\nM 28,160,100\nR 180,189,0\nM 28,164,116\ns_add_u32 s67, s71, 6336512\nbuffer_load_dwordx4 a[204:207], v252, s[20:23], s67 offen offset:0 sc1\n"
"M 12,160,96\nR 182,189,4096\nM 12,164,112\ns_barrier\nG 4\ns_waitcnt vmcnt(22)\n"
"M 0,168,128\nR 160,186,57344\ns_waitcnt vmcnt(18)\nM 0,172,144\nbuffer_load_dwordx4 a[208:211], v252, s[20:23], s64 offen offset:1024 sc1\nM 16,168,132\n"
"R 162,186,61440\ns_waitcnt vmcnt(18)\nM 16,172,148\nM 32,168,136\nR 164,190,0\ns_waitcnt vmcnt(17)\n"
"M 32,172,152\nbuffer_load_dwordx4 a[212:215], v252, s[20:23], s65 offen offset:1024 sc1\nM 48,168,140\nR 166,190,4096\ns_waitcnt vmcnt(17)\nM 48,172,156\n"
"G 4\nM 52,176,140\nR 168,187,57344\nM 52,180,156\nbuffer_load_dwordx4 a[216:219], v252, s[20:23], s66 offen offset:1024 sc1\nM 36,176,136\n"
"R 170,187,61440\nM 36,180,152\nLpg23:\ns_cbranch_execz LretO1\nM 20,176,132\nR 172,191,0\n"
"M 20,180,148\nbuffer_load_dwordx4 a[220:223], v252, s[20:23], s67 offen offset:1024 sc1\nM 4,176,128\nR 174,191,4096\nM 4,180,144\nG 4\n"
"M 8,160,128\nR 176,188,8192\nM 8,164,144\nbuffer_load_dwordx4 a[224:227], v252, s[20:23], s64 offen offset:2048 sc1\nM 24,160,132\nR 178,188,12288\n"
"M 24,164,148\nM 40,160,136\nR 180,188,16384\nM 40,164,152\nbuffer_load_dwordx4 a[228:231], v252, s[20:23], s65 offen offset:2048 sc1\nM 56,160,140\n"
"R 182,188,20480\nM 56,164,156\nG 4\nM 60,168,140\nR 160,189,8192\nM 60,172,156\n"
"buffer_load_dwordx4 a[232:235], v252, s[20:23], s66 offen offset:2048 sc1\nM 44,168,136\nR 162,189,12288\nM 44,172,152\nM 28,168,132\nR 164,189,16384\n"
"M 28,172,148\nbuffer_load_dwordx4 a[236:239], v252, s[20:23], s67 offen offset:2048 sc1\nM 12,168,128\nR 166,189,20480\nM 12,172,144\nG 4\n"
"s_waitcnt vmcnt(22)\nM 0,176,160\nR 168,190,8192\ns_waitcnt vmcnt(18)\nM 0,180,176\nbuffer_load_dwordx4 a[240:243], v252, s[20:23], s64 offen offset:3072 sc1\n"
"M 16,176,164\nR 170,190,12288\ns_waitcnt vmcnt(17)\nM 16,180,180\nM 32,176,168\nR 172,190,16384\n"
"s_waitcnt vmcnt(16)\nM 32,180,184\nbuffer_load_dwordx4 a[244:247], v252, s[20:23], s65 offen offset:3072 sc1\nM 48,176,172\nR 174,190,20480\ns_waitcnt vmcnt(16)\n"
"M 48,180,188\nG 4\nM 52,160,172\nR 176,191,8192\nM 52,164,188\nbuffer_load_dwordx4 a[248:251], v252, s[20:23], s66 offen offset:3072 sc1\n"
"M 36,160,168\nR 178,191,12288\ns_waitcnt vmcnt(12)\ns_barrier\nM 36,164,184\nM 20,160,164\n"
"R 180,191,16384\nM 20,164,180\nbuffer_load_dwordx4 a[252:255], v252, s[20:23], s67 offen offset:3072 sc1\nM 4,160,160\nR 182,191,20480\nM 4,164,176\n"
"G 4\nM 8,168,160\nR 160,188,24576\nM 8,172,176\nM 24,168,164\nR 162,188,28672\n"
"M 24,172,180\nM 40,168,168\nR 164,188,32768\nM 40,172,184\nM 56,168,172\nR 166,188,36864\n"
"M 56,172,188\nG 4\nM 60,176,172\nR 168,189,24576\nM 60,180,188\ns_movk_i32 s63, 7168\n"
"v_mad_u32_u24 v220, v228, s63, v229\nv_add_u32 v221, 3668992, v220\ns_add_u32 m0, s50, 0\ns_add_u32 s63, s54, 0\nbuffer_load_dwordx4 v220, s[12:15], s63 offen lds\nM 44,176,168\n"
"R 170,189,28672\nM 44,180,184\nM 28,176,164\nR 172,189,32768\nM 28,180,180\nbuffer_load_dwordx4 v221, s[12:15], s63 offen offset:1024 lds\n"
"M 12,176,160\nR 174,189,36864\ns_add_u32 m0, s50, 8192\nM 12,180,176\nG 4\nM 32,160,200\n"
"R 176,190,24576\ns_add_u32 s63, s54, 229376\ns_waitcnt vmcnt(11)\nM 32,164,216\nR 178,190,28672\nM 48,160,204\n"
"R 180,190,32768\ns_waitcnt vmcnt(10)\nM 48,164,220\nR 182,190,36864\nG 4\nM 52,168,204\n"
"R 160,191,24576\nbuffer_load_dwordx4 v220, s[12:15], s63 offen lds\nM 52,172,220\nR 162,191,28672\nM 36,168,200\nR 164,191,32768\n"
"M 36,172,216\nR 166,191,36864\nG 4\nM 40,176,200\nR 168,188,40960\nbuffer_load_dwordx4 v221, s[12:15], s63 offen offset:1024 lds\n"
"M 40,180,216\nR 170,188,45056\ns_add_u32 m0, s50, 16384\nM 56,176,204\nR 172,188,49152\ns_add_u32 s63, s54, 128\n"
"M 56,180,220\nR 174,188,53248\nG 4\nM 60,160,204\nR 176,189,40960\nbuffer_load_dwordx4 v220, s[12:15], s63 offen lds\n"
"M 60,164,220\nR 178,189,45056\nM 44,160,200\nR 180,189,49152\nM 44,164,216\nR 182,189,53248\n"
"G 4\ns_waitcnt vmcnt(10)\nM 32,168,232\nR 160,190,40960\nbuffer_load_dwordx4 v221, s[12:15], s63 offen offset:1024 lds\ns_waitcnt vmcnt(7)\n"
"M 32,172,248\nR 162,190,45056\ns_add_u32 m0, s50, 24576\nM 48,168,236\nR 164,190,49152\ns_add_u32 s63, s54, 229504\n"
"s_waitcnt vmcnt(6)\nM 48,172,252\nR 166,190,53248\nG 4\nM 52,176,236\nR 168,191,40960\n"
"buffer_load_dwordx4 v220, s[12:15], s63 offen lds\nM 52,180,252\nR 170,191,45056\nM 36,176,232\nR 172,191,49152\nM 36,180,248\n"
"R 174,191,53248\nG 4\nM 40,160,232\nR 176,188,24576\nbuffer_load_dwordx4 v221, s[12:15], s63 offen offset:1024 lds\nM 40,164,248\n"
"R 178,188,28672\nM 56,160,236\nR 180,188,32768\nM 56,164,252\nR 182,188,36864\nG 4\n"
"M 60,168,236\nR 160,189,24576\nM 60,172,252\nR 162,189,28672\nM 44,168,232\nR 164,189,32768\n"
"M 44,172,248\nR 166,189,36864\nG 4\nM 0,176,192\nR 168,190,24576\nM 0,180,208\n"
"R 170,190,28672\ns_mov_b32 m0, s51\nM 16,176,196\nR 172,190,32768\nv_cvt_pk_bf16_f32 v32, v32, v33\nv_cvt_pk_bf16_f32 v33, v34, v35\n"
"M 16,180,212\nR 174,190,36864\nG 4\nv_cvt_pk_bf16_f32 v34, v36, v37\nM 20,160,196\nR 176,191,24576\n"
"v_cvt_pk_bf16_f32 v35, v38, v39\nv_cvt_pk_bf16_f32 v36, v40, v41\nM 20,164,212\nR 178,191,28672\nv_cvt_pk_bf16_f32 v37, v42, v43\nv_cvt_pk_bf16_f32 v38, v44, v45\n"
"M 4,160,192\nR 180,191,32768\nv_cvt_pk_bf16_f32 v39, v46, v47\nv_cvt_pk_bf16_f32 v40, v48, v49\nM 4,164,208\nR 182,191,36864\n"
"G 4\nv_cvt_pk_bf16_f32 v41, v50, v51\nM 8,168,192\nR 160,188,40960\nv_cvt_pk_bf16_f32 v42, v52, v53\nv_cvt_pk_bf16_f32 v43, v54, v55\n"
"M 8,172,208\nR 162,188,45056\nv_cvt_pk_bf16_f32 v44, v56, v57\nv_cvt_pk_bf16_f32 v45, v58, v59\nM 24,168,196\nR 164,188,49152\n"
"v_cvt_pk_bf16_f32 v46, v60, v61\nv_cvt_pk_bf16_f32 v47, v62, v63\nM 24,172,212\nR 166,188,53248\nds_write_addtid_b32 v32 offset:0\nG 5\n"
"M 28,176,196\nR 168,189,40960\nds_write_addtid_b32 v33 offset:256\nM 28,180,212\nR 170,189,45056\nds_write_addtid_b32 v34 offset:512\n"
"M 12,176,192\nR 172,189,49152\nds_write_addtid_b32 v35 offset:768\nds_write_addtid_b32 v36 offset:1024\nds_write_addtid_b32 v37 offset:1280\nds_write_addtid_b32 v38 offset:1536\n"
"ds_write_addtid_b32 v39 offset:1792\nds_write_addtid_b32 v40 offset:2048\nds_write_addtid_b32 v41 offset:2304\nds_write_addtid_b32 v42 offset:2560\nds_write_addtid_b32 v43 offset:2816\nds_write_addtid_b32 v44 offset:3072\n"
"ds_write_addtid_b32 v45 offset:3328\nds_write_addtid_b32 v46 offset:3584\nds_write_addtid_b32 v47 offset:3840\nM 12,180,208\nR 174,189,53248\nG 15\n"
"M 0,160,224\nR 176,190,40960\ns_add_u32 m0, s50, 32768\nM 0,164,240\nR 178,190,45056\nG 3\n"
"s_barrier\ns_add_u32 s63, s54, 458752\nM 16,160,228\nR 180,190,49152\nbuffer_load_dwordx4 v220, s[12:15], s63 offen lds\nds_read2st64_b32 v[32:33], v225 offset0:0 offset1:1\n"
"M 16,164,244\nR 182,190,53248\nds_read2st64_b32 v[34:35], v225 offset0:2 offset1:3\nG 6\nM 20,168,228\nR 160,191,40960\n"
"ds_read2st64_b32 v[36:37], v225 offset0:4 offset1:5\nM 20,172,244\nR 162,191,45056\nds_read2st64_b32 v[38:39], v225 offset0:6 offset1:7\nM 4,168,224\nR 164,191,49152\n"
"buffer_load_dwordx4 v221, s[12:15], s63 offen offset:1024 lds\nds_read2st64_b32 v[40:41], v225 offset0:8 offset1:9\nM 4,172,240\nR 166,191,53248\ns_add_u32 m0, s50, 40960\ns_add_u32 s63, s54, 688128\n"
"ds_read2st64_b32 v[42:43], v225 offset0:10 offset1:11\nG 9\nM 8,176,224\nbuffer_load_dwordx4 v220, s[12:15], s63 offen lds\nds_read2st64_b32 v[44:45], v225 offset0:12 offset1:13\nM 8,180,240\n"
"ds_read2st64_b32 v[46:47], v225 offset0:14 offset1:15\nM 24,176,228\nM 24,180,244\nG 3\nM 28,160,228\nbuffer_load_dwordx4 v221, s[12:15], s63 offen offset:1024 lds\n"
"M 28,164,244\nM 12,160,224\nM 12,164,240\nG 0\ns_barrier\nv_mfma_f32_16x16x32_bf16 v[0:3], v[208:211], v[32:35], v[0:3]\n"
"v_mfma_f32_16x16x32_bf16 v[4:7], v[210:213], v[32:35], v[4:7]\nv_mfma_f32_16x16x32_bf16 v[8:11], v[208:211], v[36:39], v[8:11]\nv_mfma_f32_16x16x32_bf16 v[12:15], v[210:213], v[36:39], v[12:15]\nv_mfma_f32_16x16x32_bf16 v[16:19], v[208:211], v[40:43], v[16:19]\nv_mfma_f32_16x16x32_bf16 v[20:23], v[210:213], v[40:43], v[20:23]\nv_mfma_f32_16x16x32_bf16 v[24:27], v[208:211], v[44:47], v[24:27]\n"
"v_mfma_f32_16x16x32_bf16 v[28:31], v[210:213], v[44:47], v[28:31]\ns_lshl_b32 s84, s75, 11\ns_add_u32 s84, s84, s49\ns_add_u32 s84, s84, 14680064\ns_add_u32 s85, s84, 32768\nv_mov_b32 v238, s46\n"
"v_mov_b32 v239, s47\nv_cvt_pk_bf16_f32 v192, v0, v1\nv_cvt_pk_bf16_f32 v193, v2, v3\nv_cvt_pk_bf16_f32 v194, v4, v5\nv_cvt_pk_bf16_f32 v195, v6, v7\nbuffer_store_dwordx4 v[192:195], v252, s[20:23], s84 offen offset:0 sc1\n"
"v_cvt_pk_bf16_f32 v196, v8, v9\nv_cvt_pk_bf16_f32 v197, v10, v11\nv_cvt_pk_bf16_f32 v198, v12, v13\nv_cvt_pk_bf16_f32 v199, v14, v15\nbuffer_store_dwordx4 v[196:199], v252, s[20:23], s84 offen offset:1024 sc1\nv_cvt_pk_bf16_f32 v200, v16, v17\n"
"v_cvt_pk_bf16_f32 v201, v18, v19\nv_cvt_pk_bf16_f32 v202, v20, v21\nv_cvt_pk_bf16_f32 v203, v22, v23\nbuffer_store_dwordx4 v[200:203], v252, s[20:23], s85 offen offset:0 sc1\nv_cvt_pk_bf16_f32 v204, v24, v25\nv_cvt_pk_bf16_f32 v205, v26, v27\n"
"v_cvt_pk_bf16_f32 v206, v28, v29\nv_cvt_pk_bf16_f32 v207, v30, v31\nbuffer_store_dwordx4 v[204:207], v252, s[20:23], s85 offen offset:1024 sc1\ns_mov_b64 exec, 0\ns_cmp_eq_u32 s40, 1\ns_cbranch_scc1 Lpg25\n"
"s_cmp_eq_u32 s40, 2\ns_cbranch_scc1 Lpg26\ns_cmp_eq_u32 s40, 3\ns_cbranch_scc1 Lpg27\ns_branch Lpg24\nLretO1n:\n"
"s_mov_b64 exec, -1\ns_add_u32 s61, s79, 88637440\ns_lshl_b32 s62, s75, 3\ns_add_u32 s61, s61, s62\ns_waitcnt vmcnt(0)\ns_mov_b64 exec, 1\n"
"buffer_atomic_swap_x2 v[238:239], off, s[20:23], s61 sc1\ns_mov_b64 exec, -1\nLqw1:\nv_bfe_u32 v250, v252, 7, 1\nv_mul_u32_u24 v250, 2176, v250\nv_bfe_u32 v251, v252, 4, 3\n"
"v_lshl_add_u32 v250, v251, 3, v250\ns_lshl_b32 s60, s90, 6\nv_add_u32 v250, s60, v250\ns_sleep 3\nv_mov_b32 v248, 0\nv_mov_b32 v249, 0\n"
"s_add_u32 s61, s80, 88637440\ns_mov_b64 exec, 0xffff\nbuffer_atomic_or_x2 v[248:249], v250, s[20:23], s61 offen sc0 sc1\ns_mov_b64 exec, -1\ns_add_u32 m0, s50, 49152\ns_add_u32 s63, s54, 458880\n"
"buffer_load_dwordx4 v220, s[12:15], s63 offen lds\nbuffer_load_dwordx4 v221, s[12:15], s63 offen offset:1024 lds\ns_add_u32 m0, s50, 57344\ns_add_u32 s63, s54, 688256\nbuffer_load_dwordx4 v220, s[12:15], s63 offen lds\nbuffer_load_dwordx4 v221, s[12:15], s63 offen offset:1024 lds\n"
"s_add_u32 m0, s50, 65536\ns_add_u32 s63, s54, 917504\nbuffer_load_dwordx4 v220, s[12:15], s63 offen lds\nbuffer_load_dwordx4 v221, s[12:15], s63 offen offset:1024 lds\ns_add_u32 m0, s50, 73728\ns_add_u32 s63, s54, 1146880\n"
"buffer_load_dwordx4 v220, s[12:15], s63 offen lds\nbuffer_load_dwordx4 v221, s[12:15], s63 offen offset:1024 lds\ns_add_u32 m0, s50, 81920\ns_add_u32 s63, s54, 917632\nbuffer_load_dwordx4 v220, s[12:15], s63 offen lds\nbuffer_load_dwordx4 v221, s[12:15], s63 offen offset:1024 lds\n"
"s_waitcnt vmcnt(10)\nv_cmp_eq_u64 s[86:87], s[46:47], v[248:249]\ns_nop 1\ns_and_b32 s88, s86, 0xffff\ns_cmp_eq_u32 s88, 0xffff\ns_cbranch_scc1 Lq768\n"
"v_mov_b32 v246, 0\nv_mov_b32 v247, 0\ns_add_u32 s61, s80, 88637440\ns_mov_b64 exec, 0xffff\nbuffer_atomic_or_x2 v[246:247], v250, s[20:23], s61 offen sc0 sc1\ns_mov_b64 exec, -1\n"
"Lq768:\ns_add_u32 m0, s50, 90112\ns_add_u32 s63, s54, 1147008\nbuffer_load_dwordx4 v220, s[12:15], s63 offen lds\nbuffer_load_dwordx4 v221, s[12:15], s63 offen offset:1024 lds\ns_mov_b64 exec, 0\n"
"s_cmp_eq_u32 s40, 1\ns_cbranch_scc1 Lpg33\ns_cmp_eq_u32 s40, 2\ns_cbranch_scc1 LretW1z\ns_cmp_eq_u32 s40, 3\ns_cbranch_scc1 LretW1z\n"
"s_branch Lpg32\nLretW1z:\ns_mov_b64 exec, -1\nv_mov_b32 v230, 0\nv_mov_b32 v231, 0\nv_mov_b32 v232, 0\n"
"v_mov_b32 v233, 0\nv_dot2c_f32_bf16 v230, s76, v192\nv_dot2c_f32_bf16 v231, v192, v192\nv_dot2c_f32_bf16 v232, s76, v200\nv_dot2c_f32_bf16 v233, v200, v200\nv_dot2c_f32_bf16 v230, s76, v193\n"
"v_dot2c_f32_bf16 v231, v193, v193\nv_dot2c_f32_bf16 v232, s76, v201\nv_dot2c_f32_bf16 v233, v201, v201\nv_dot2c_f32_bf16 v230, s76, v194\nv_dot2c_f32_bf16 v231, v194, v194\nv_dot2c_f32_bf16 v232, s76, v202\n"
"v_dot2c_f32_bf16 v233, v202, v202\nv_dot2c_f32_bf16 v230, s76, v195\nv_dot2c_f32_bf16 v231, v195, v195\nv_dot2c_f32_bf16 v232, s76, v203\nv_dot2c_f32_bf16 v233, v203, v203\nv_dot2c_f32_bf16 v230, s76, v196\n"
"v_dot2c_f32_bf16 v231, v196, v196\nv_dot2c_f32_bf16 v232, s76, v204\nv_dot2c_f32_bf16 v233, v204, v204\nv_dot2c_f32_bf16 v230, s76, v197\nv_dot2c_f32_bf16 v231, v197, v197\nv_dot2c_f32_bf16 v232, s76, v205\n"
"v_dot2c_f32_bf16 v233, v205, v205\nv_dot2c_f32_bf16 v230, s76, v198\nv_dot2c_f32_bf16 v231, v198, v198\nv_dot2c_f32_bf16 v232, s76, v206\nv_dot2c_f32_bf16 v233, v206, v206\nv_dot2c_f32_bf16 v230, s76, v199\n"
"v_dot2c_f32_bf16 v231, v199, v199\nv_dot2c_f32_bf16 v232, s76, v207\nv_dot2c_f32_bf16 v233, v207, v207\nv_lshrrev_b32 v234, 5, v252\nv_and_b32 v234, 24, v234\nv_bfe_u32 v235, s46, v234, 8\n"
"v_lshrrev_b32 v234, 1, v234\nv_and_b32 v236, 0xf0, v252\nv_add_u32 v234, v234, v236\nv_permlane32_swap_b32 v230, v231\nv_permlane32_swap_b32 v232, v233\nv_add_f32 v230, v230, v231\n"
"v_add_f32 v232, v232, v233\ns_nop 1\nv_permlane16_swap_b32 v230, v232\nv_add_f32 v230, v230, v232\ns_movk_i32 s62, 0xff\nv_bfi_b32 v230, s62, v235, v230\n"
"s_lshr_b32 s61, s49, 4\ns_lshl1_add_u32 s61, s53, s61\ns_add_u32 s61, s61, 83361792\nbuffer_store_dword v230, v234, s[20:23], s61 offen sc1\ns_cmp_eq_u32 s88, 0xffff\ns_cbranch_scc1 Lq769\n"
"s_waitcnt vmcnt(3)\nv_mov_b32 v248, v246\nv_mov_b32 v249, v247\nv_cmp_eq_u64 s[86:87], s[46:47], v[248:249]\ns_nop 1\ns_and_b32 s62, s86, 0xffff\n"
"s_cmp_eq_u32 s62, 0xffff\ns_cbranch_scc1 Lq760\nLq759:\nv_mov_b32 v248, 0\nv_mov_b32 v249, 0\ns_add_u32 s61, s80, 88637440\n"
"s_mov_b64 exec, 0xffff\nbuffer_atomic_or_x2 v[248:249], v250, s[20:23], s61 offen sc0 sc1\ns_mov_b64 exec, -1\ns_waitcnt vmcnt(0)\nv_cmp_eq_u64 s[86:87], s[46:47], v[248:249]\ns_nop 1\n"
"s_and_b32 s62, s86, 0xffff\ns_cmp_eq_u32 s62, 0xffff\ns_cbranch_scc1 Lq760\ns_sub_u32 s48, s48, 1\ns_cmp_gt_i32 s48, 0\ns_cbranch_scc1 Lq759\n"
"Lq760:\nLq769:\ns_add_u32 s64, s70, 14680064\nbuffer_load_dwordx4 a[0:3], v252, s[20:23], s64 offen offset:0 sc1\ns_add_u32 s65, s70, 14712832\nbuffer_load_dwordx4 a[4:7], v252, s[20:23], s65 offen offset:0 sc1\n"
"s_add_u32 s66, s71, 14680064\nbuffer_load_dwordx4 a[8:11], v252, s[20:23], s66 offen offset:0 sc1\ns_add_u32 s67, s71, 14712832\nbuffer_load_dwordx4 a[12:15], v252, s[20:23], s67 offen offset:0 sc1\nbuffer_load_dwordx4 a[16:19], v252, s[20:23], s64 offen offset:1024 sc1\nbuffer_load_dwordx4 a[20:23], v252, s[20:23], s65 offen offset:1024 sc1\n"
"buffer_load_dwordx4 a[24:27], v252, s[20:23], s66 offen offset:1024 sc1\nbuffer_load_dwordx4 a[28:31], v252, s[20:23], s67 offen offset:1024 sc1\ns_barrier\nR 160,184,0\nR 162,184,4096\nR 164,184,8192\n"
"R 166,184,12288\nR 168,185,0\nR 170,185,4096\nR 172,185,8192\nR 174,185,12288\nR 176,186,0\n"
"R 178,186,4096\nR 180,186,8192\nR 182,186,12288\nbuffer_load_dwordx4 a[32:35], v252, s[20:23], s64 offen offset:2048 sc1\nbuffer_load_dwordx4 a[36:39], v252, s[20:23], s65 offen offset:2048 sc1\nbuffer_load_dwordx4 a[40:43], v252, s[20:23], s66 offen offset:2048 sc1\n"
"buffer_load_dwordx4 a[44:47], v252, s[20:23], s67 offen offset:2048 sc1\nbuffer_load_dwordx4 a[48:51], v252, s[20:23], s64 offen offset:3072 sc1\nbuffer_load_dwordx4 a[52:55], v252, s[20:23], s65 offen offset:3072 sc1\nbuffer_load_dwordx4 a[56:59], v252, s[20:23], s66 offen offset:3072 sc1\nbuffer_load_dwordx4 a[60:63], v252, s[20:23], s67 offen offset:3072 sc1\ns_waitcnt vmcnt(15) lgkmcnt(10)\n"
"v_mfma_f32_16x16x32_bf16 v[32:35], v[160:163], a[0:3], 0\ns_add_u32 s64, s70, 14684160\nbuffer_load_dwordx4 a[64:67], v252, s[20:23], s64 offen offset:0 sc1\ns_waitcnt vmcnt(12) lgkmcnt(8)\nM 32,164,16\nv_mfma_f32_16x16x32_bf16 v[48:51], v[160:163], a[4:7], 0\n"
"s_waitcnt vmcnt(11)\nM 48,164,20\nv_mfma_f32_16x16x32_bf16 v[64:67], v[160:163], a[8:11], 0\ns_waitcnt vmcnt(10)\nM 64,164,24\nv_mfma_f32_16x16x32_bf16 v[80:83], v[160:163], a[12:15], 0\n"
"s_add_u32 s65, s70, 14716928\nbuffer_load_dwordx4 a[68:71], v252, s[20:23], s65 offen offset:0 sc1\ns_waitcnt vmcnt(10)\nM 80,164,28\nG 4\nv_mfma_f32_16x16x32_bf16 v[84:87], v[168:171], a[12:15], 0\n"
"R 160,187,0\nM 84,172,28\nv_mfma_f32_16x16x32_bf16 v[68:71], v[168:171], a[8:11], 0\nR 162,187,4096\nM 68,172,24\nv_mfma_f32_16x16x32_bf16 v[52:55], v[168:171], a[4:7], 0\n"
"R 164,187,8192\nM 52,172,20\ns_add_u32 s66, s71, 14684160\nbuffer_load_dwordx4 a[72:75], v252, s[20:23], s66 offen offset:0 sc1\nv_mfma_f32_16x16x32_bf16 v[36:39], v[168:171], a[0:3], 0\nR 166,187,12288\n"
"M 36,172,16\nG 4\nv_mfma_f32_16x16x32_bf16 v[40:43], v[176:179], a[0:3], 0\nR 168,184,16384\nM 40,180,16\nv_mfma_f32_16x16x32_bf16 v[56:59], v[176:179], a[4:7], 0\n"
"R 170,184,20480\nM 56,180,20\ns_add_u32 s67, s71, 14716928\nbuffer_load_dwordx4 a[76:79], v252, s[20:23], s67 offen offset:0 sc1\nv_mfma_f32_16x16x32_bf16 v[72:75], v[176:179], a[8:11], 0\nR 172,184,24576\n"
"M 72,180,24\nv_mfma_f32_16x16x32_bf16 v[88:91], v[176:179], a[12:15], 0\nR 174,184,28672\nM 88,180,28\nG 4\nv_mfma_f32_16x16x32_bf16 v[92:95], v[160:163], a[12:15], 0\n"
"R 176,185,16384\ns_add_u32 s63, s54, 1376256\nM 92,164,28\nbuffer_load_dwordx4 a[80:83], v252, s[20:23], s64 offen offset:1024 sc1\nv_mfma_f32_16x16x32_bf16 v[76:79], v[160:163], a[8:11], 0\nR 178,185,20480\n"
"Lpg24:\ns_cbranch_execz Lpg28\nM 76,164,24\ns_add_u32 m0, s50, 98304\nv_mfma_f32_16x16x32_bf16 v[60:63], v[160:163], a[4:7], 0\nR 180,185,24576\n"
"M 60,164,20\nbuffer_load_dwordx4 v220, s[12:15], s63 offen lds\nv_mfma_f32_16x16x32_bf16 v[44:47], v[160:163], a[0:3], 0\nR 182,185,28672\nM 44,164,16\nG 4\n"
"v_mfma_f32_16x16x32_bf16 v[96:99], v[168:171], a[0:3], 0\nR 160,186,16384\nM 96,172,16\nbuffer_load_dwordx4 a[84:87], v252, s[20:23], s65 offen offset:1024 sc1\nv_mfma_f32_16x16x32_bf16 v[112:115], v[168:171], a[4:7], 0\nR 162,186,20480\n"
"M 112,172,20\nv_mfma_f32_16x16x32_bf16 v[128:131], v[168:171], a[8:11], 0\nR 164,186,24576\nM 128,172,24\nbuffer_load_dwordx4 a[88:91], v252, s[20:23], s66 offen offset:1024 sc1\nv_mfma_f32_16x16x32_bf16 v[144:147], v[168:171], a[12:15], 0\n"
"R 166,186,28672\nM 144,172,28\ns_barrier\nG 4\nv_mfma_f32_16x16x32_bf16 v[148:151], v[176:179], a[12:15], 0\nR 168,187,16384\n"
"M 148,180,28\nv_mfma_f32_16x16x32_bf16 v[132:135], v[176:179], a[8:11], 0\nR 170,187,20480\nM 132,180,24\nbuffer_load_dwordx4 a[92:95], v252, s[20:23], s67 offen offset:1024 sc1\nv_mfma_f32_16x16x32_bf16 v[116:119], v[176:179], a[4:7], 0\n"
"R 172,187,24576\nM 116,180,20\nv_mfma_f32_16x16x32_bf16 v[100:103], v[176:179], a[0:3], 0\nR 174,187,28672\nM 100,180,16\nG 4\n"
"v_mfma_f32_16x16x32_bf16 v[104:107], v[160:163], a[0:3], 0\nR 176,184,32768\nM 104,164,16\nbuffer_load_dwordx4 a[96:99], v252, s[20:23], s64 offen offset:2048 sc1\nv_mfma_f32_16x16x32_bf16 v[120:123], v[160:163], a[4:7], 0\nR 178,184,36864\n"
"M 120,164,20\nv_mfma_f32_16x16x32_bf16 v[136:139], v[160:163], a[8:11], 0\nR 180,184,40960\nM 136,164,24\nbuffer_load_dwordx4 v221, s[12:15], s63 offen offset:1024 lds\nv_mfma_f32_16x16x32_bf16 v[152:155], v[160:163], a[12:15], 0\n"
"R 182,184,45056\nM 152,164,28\nG 4\nv_mfma_f32_16x16x32_bf16 v[156:159], v[168:171], a[12:15], 0\nR 160,185,32768\nM 156,172,28\n"
"buffer_load_dwordx4 a[100:103], v252, s[20:23], s65 offen offset:2048 sc1\nv_mfma_f32_16x16x32_bf16 v[140:143], v[168:171], a[8:11], 0\nR 162,185,36864\nM 140,172,24\ns_add_u32 m0, s50, 106496\nv_mfma_f32_16x16x32_bf16 v[124:127], v[168:171], a[4:7], 0\n"
"R 164,185,40960\ns_add_u32 s63, s54, 1605632\nM 124,172,20\nbuffer_load_dwordx4 a[104:107], v252, s[20:23], s66 offen offset:2048 sc1\nv_mfma_f32_16x16x32_bf16 v[108:111], v[168:171], a[0:3], 0\nR 166,185,45056\n"
"M 108,172,16\nG 4\ns_waitcnt vmcnt(20)\nM 32,176,32\nR 168,186,32768\ns_waitcnt vmcnt(16)\n"
"M 32,180,48\nbuffer_load_dwordx4 v220, s[12:15], s63 offen lds\nM 48,176,36\nR 170,186,36864\ns_waitcnt vmcnt(16)\nM 48,180,52\n"
"buffer_load_dwordx4 a[108:111], v252, s[20:23], s67 offen offset:2048 sc1\nM 64,176,40\nR 172,186,40960\ns_waitcnt vmcnt(16)\nM 64,180,56\nM 80,176,44\n"
"R 174,186,45056\ns_waitcnt vmcnt(15)\nM 80,180,60\ns_barrier\nG 4\nM 84,160,44\n"
"R 176,187,32768\nM 84,164,60\nbuffer_load_dwordx4 a[112:115], v252, s[20:23], s64 offen offset:3072 sc1\nM 68,160,40\nR 178,187,36864\nM 68,164,56\n"
"M 52,160,36\nR 180,187,40960\nM 52,164,52\nM 36,160,32\nR 182,187,45056\nM 36,164,48\n"
"G 4\nM 40,168,32\nR 160,184,49152\nM 40,172,48\nbuffer_load_dwordx4 a[116:119], v252, s[20:23], s65 offen offset:3072 sc1\nM 56,168,36\n"
"R 162,184,53248\nM 56,172,52\nM 72,168,40\nR 164,184,57344\nM 72,172,56\nbuffer_load_dwordx4 a[120:123], v252, s[20:23], s66 offen offset:3072 sc1\n"
"M 88,168,44\nR 166,184,61440\nM 88,172,60\nG 4\nM 92,176,44\nR 168,185,49152\n"
"M 92,180,60\nbuffer_load_dwordx4 v221, s[12:15], s63 offen offset:1024 lds\nM 76,176,40\nR 170,185,53248\nM 76,180,56\nbuffer_load_dwordx4 a[124:127], v252, s[20:23], s67 offen offset:3072 sc1\n"
"M 60,176,36\nR 172,185,57344\nM 60,180,52\nM 44,176,32\nR 174,185,61440\nM 44,180,48\n"
"G 4\nM 96,160,32\nR 176,186,49152\nM 96,164,48\ns_add_u32 s64, s70, 14688256\nbuffer_load_dwordx4 a[128:131], v252, s[20:23], s64 offen offset:0 sc1\n"
"M 112,160,36\nR 178,186,53248\nM 112,164,52\ns_lshr_b32 s84, s49, 4\nM 128,160,40\nR 180,186,57344\n"
"M 128,164,56\ns_add_u32 s84, s84, 83361792\nM 144,160,44\nR 182,186,61440\nM 144,164,60\ns_barrier\n"
"G 4\nM 148,168,44\nR 160,187,49152\nM 148,172,60\ns_add_u32 s65, s70, 14721024\nbuffer_load_dwordx4 a[132:135], v252, s[20:23], s65 offen offset:0 sc1\n"
"M 132,168,40\nR 162,187,53248\nM 132,172,56\nbuffer_load_dwordx4 v[0:3], v252, s[20:23], s84 offen offset:0 sc1\nM 116,168,36\nR 164,187,57344\n"
"M 116,172,52\ns_add_u32 s66, s71, 14688256\nbuffer_load_dwordx4 a[136:139], v252, s[20:23], s66 offen offset:0 sc1\nM 100,168,32\nR 166,187,61440\nM 100,172,48\n"
"G 4\nM 104,176,32\nR 168,188,0\nM 104,180,48\nbuffer_load_dwordx4 v[4:7], v252, s[20:23], s84 offen offset:1024 sc1\nM 120,176,36\n"
"R 170,188,4096\nM 120,180,52\ns_add_u32 s67, s71, 14721024\nbuffer_load_dwordx4 a[140:143], v252, s[20:23], s67 offen offset:0 sc1\nM 136,176,40\nR 172,188,8192\n"
"M 136,180,56\nbuffer_load_dwordx4 v[8:11], v252, s[20:23], s84 offen offset:2048 sc1\nM 152,176,44\nR 174,188,12288\nM 152,180,60\nbuffer_load_dwordx4 v[12:15], v252, s[20:23], s84 offen offset:3072 sc1\n"
"G 4\nM 156,160,44\nR 176,189,0\nM 156,164,60\nbuffer_load_dwordx4 a[144:147], v252, s[20:23], s64 offen offset:1024 sc1\nM 140,160,40\n"
"R 178,189,4096\nM 140,164,56\nM 124,160,36\nR 180,189,8192\nM 124,164,52\nM 108,160,32\n"
"R 182,189,12288\nM 108,164,48\nG 4\ns_waitcnt vmcnt(28)\nM 32,168,64\nR 160,190,0\n"
"s_waitcnt vmcnt(24)\nM 32,172,80\nbuffer_load_dwordx4 a[148:151], v252, s[20:23], s65 offen offset:1024 sc1\nM 48,168,68\nR 162,190,4096\ns_waitcnt vmcnt(23)\n"
"M 48,172,84\ns_add_u32 m0, s50, 114688\nM 64,168,72\nR 164,190,8192\ns_add_u32 s63, s54, 1376384\ns_waitcnt vmcnt(22)\n"
"M 64,172,88\nbuffer_load_dwordx4 a[152:155], v252, s[20:23], s66 offen offset:1024 sc1\nM 80,168,76\nR 166,190,12288\ns_waitcnt vmcnt(22)\nM 80,172,92\n"
"s_barrier\nG 4\nM 84,176,76\nR 168,191,0\nM 84,180,92\nbuffer_load_dwordx4 v220, s[12:15], s63 offen lds\n"
"M 68,176,72\nR 170,191,4096\nM 68,180,88\nbuffer_load_dwordx4 a[156:159], v252, s[20:23], s67 offen offset:1024 sc1\nM 52,176,68\nR 172,191,8192\n"
"M 52,180,84\nbuffer_load_dwordx4 v221, s[12:15], s63 offen offset:1024 lds\nM 36,176,64\nR 174,191,12288\ns_add_u32 m0, s50, 0\nM 36,180,80\n"
"G 4\nM 40,160,64\nR 176,188,16384\ns_add_u32 s63, s54, 1605760\nM 40,164,80\nbuffer_load_dwordx4 a[160:163], v252, s[20:23], s64 offen offset:2048 sc1\n"
"M 56,160,68\nR 178,188,20480\nM 56,164,84\nbuffer_load_dwordx4 v220, s[12:15], s63 offen lds\nM 72,160,72\nR 180,188,24576\n"
"M 72,164,88\nbuffer_load_dwordx4 v221, s[12:15], s63 offen offset:1024 lds\nM 88,160,76\nR 182,188,28672\nM 88,164,92\nG 4\n"
"M 92,168,76\nR 160,189,16384\nM 92,172,92\nbuffer_load_dwordx4 a[164:167], v252, s[20:23], s65 offen offset:2048 sc1\nM 76,168,72\nR 162,189,20480\n"
"M 76,172,88\nM 60,168,68\nR 164,189,24576\nM 60,172,84\nbuffer_load_dwordx4 a[168:171], v252, s[20:23], s66 offen offset:2048 sc1\nM 44,168,64\n"
"R 166,189,28672\nM 44,172,80\nG 4\nM 96,176,64\nR 168,190,16384\ns_mov_b32 s85, 0x0c0c0400\n"
"M 96,180,80\ns_waitcnt vmcnt(16)\nv_perm_b32 v16, v1, v0, s85\nv_perm_b32 v20, v3, v2, s85\nM 112,176,68\nR 170,190,20480\n"
"v_lshl_or_b32 v16, v20, 16, v16\nM 112,180,84\nbuffer_load_dwordx4 a[172:175], v252, s[20:23], s67 offen offset:2048 sc1\nM 128,176,72\nR 172,190,24576\ns_waitcnt vmcnt(15)\n"
"v_perm_b32 v17, v5, v4, s85\nM 128,180,88\ns_add_u32 m0, s50, 8192\nv_perm_b32 v21, v7, v6, s85\nv_lshl_or_b32 v17, v21, 16, v17\nM 144,176,76\n"
"R 174,190,28672\ns_add_u32 s63, s54, 1835008\nM 144,180,92\ns_barrier\nG 4\nM 148,160,76\n"
"R 176,191,16384\ns_waitcnt vmcnt(13)\nv_perm_b32 v18, v9, v8, s85\nM 148,164,92\nbuffer_load_dwordx4 a[176:179], v252, s[20:23], s64 offen offset:3072 sc1\nM 132,160,72\n"
"R 178,191,20480\nv_perm_b32 v22, v11, v10, s85\nM 132,164,88\nbuffer_load_dwordx4 v220, s[12:15], s63 offen lds\nM 116,160,68\nR 180,191,24576\n"
"v_lshl_or_b32 v18, v22, 16, v18\nM 116,164,84\nbuffer_load_dwordx4 v221, s[12:15], s63 offen offset:1024 lds\nM 100,160,64\nR 182,191,28672\ns_add_u32 m0, s50, 16384\n"
"M 100,164,80\nG 4\ns_waitcnt vmcnt(15)\nv_perm_b32 v19, v13, v12, s85\nv_perm_b32 v23, v15, v14, s85\nM 104,168,64\n"
"R 160,188,32768\ns_add_u32 s63, s54, 2064384\nM 104,172,80\nbuffer_load_dwordx4 a[180:183], v252, s[20:23], s65 offen offset:3072 sc1\nM 120,168,68\nR 162,188,36864\n"
"v_lshl_or_b32 v19, v23, 16, v19\nM 120,172,84\nbuffer_load_dwordx4 v220, s[12:15], s63 offen lds\nM 136,168,72\nR 164,188,40960\nv_min3_u32 v230, v16, v17, v18\n"
"M 136,172,88\nbuffer_load_dwordx4 a[184:187], v252, s[20:23], s66 offen offset:3072 sc1\nM 152,168,76\nR 166,188,45056\nv_min3_u32 v230, v230, v19, v19\nM 152,172,92\n"
"G 4\nv_max3_u32 v231, v16, v17, v18\nv_max3_u32 v231, v231, v19, v19\nM 156,176,76\nR 168,189,32768\nbuffer_load_dwordx4 v221, s[12:15], s63 offen offset:1024 lds\n"
"M 156,180,92\nv_cmp_ne_u32 s[86:87], s46, v230\nv_cmp_ne_u32 vcc, s46, v231\nM 140,176,72\nR 170,189,36864\nM 140,180,88\n"
"buffer_load_dwordx4 a[188:191], v252, s[20:23], s67 offen offset:3072 sc1\nM 124,176,68\nR 172,189,40960\nM 124,180,84\ns_or_b64 s[86:87], s[86:87], vcc\ns_cbranch_scc1 Lq818\n"
"Lq819:\nv_and_b32 v0, 0xffffff00, v0\nM 108,176,64\nR 174,189,45056\nv_and_b32 v1, 0xffffff00, v1\nM 108,180,80\n"
"G 4\nv_and_b32 v2, 0xffffff00, v2\nv_and_b32 v3, 0xffffff00, v3\nM 32,160,96\nR 176,190,32768\nv_and_b32 v4, 0xffffff00, v4\n"
"M 32,164,112\ns_add_u32 s64, s70, 14692352\nbuffer_load_dwordx4 a[192:195], v252, s[20:23], s64 offen offset:0 sc1\nM 48,160,100\nR 178,190,36864\nv_and_b32 v5, 0xffffff00, v5\n"
"M 48,164,116\nv_and_b32 v6, 0xffffff00, v6\nv_and_b32 v7, 0xffffff00, v7\nM 64,160,104\nR 180,190,40960\nv_and_b32 v8, 0xffffff00, v8\n"
"M 64,164,120\ns_add_u32 m0, s50, 24576\nv_and_b32 v9, 0xffffff00, v9\nv_and_b32 v10, 0xffffff00, v10\nM 80,160,108\nR 182,190,45056\n"
"s_add_u32 s63, s54, 2293760\nM 80,164,124\ns_waitcnt vmcnt(12)\ns_barrier\nG 4\nM 84,168,108\n"
"R 160,191,32768\nv_and_b32 v11, 0xffffff00, v11\nM 84,172,124\ns_add_u32 s65, s70, 14725120\nbuffer_load_dwordx4 a[196:199], v252, s[20:23], s65 offen offset:0 sc1\nM 68,168,104\n"
"R 162,191,36864\nv_and_b32 v12, 0xffffff00, v12\nM 68,172,120\nbuffer_load_dwordx4 v220, s[12:15], s63 offen lds\nM 52,168,100\nR 164,191,40960\n"
"v_and_b32 v13, 0xffffff00, v13\nM 52,172,116\ns_add_u32 s66, s71, 14692352\nbuffer_load_dwordx4 a[200:203], v252, s[20:23], s66 offen offset:0 sc1\nM 36,168,96\nR 166,191,45056\n"
"v_and_b32 v14, 0xffffff00, v14\nM 36,172,112\nG 4\nv_and_b32 v15, 0xffffff00, v15\nv_add_f32 v0, v0, v8\nM 40,176,96\n"
"R 168,188,49152\nbuffer_load_dwordx4 v221, s[12:15], s63 offen offset:1024 lds\nM 40,180,112\ns_add_u32 m0, s50, 32768\ns_add_u32 s63, s54, 2523136\nbuffer_load_dwordx4 v220, s[12:15], s63 offen lds\n"
"M 56,176,100\nR 170,188,53248\nv_add_f32 v1, v1, v9\nM 56,180,116\ns_add_u32 s67, s71, 14725120\nbuffer_load_dwordx4 a[204:207], v252, s[20:23], s67 offen offset:0 sc1\n"
"M 72,176,104\nR 172,184,0\nv_add_f32 v2, v2, v10\nM 72,180,120\nbuffer_load_dwordx4 v221, s[12:15], s63 offen offset:1024 lds\nM 88,176,108\n"
"R 174,184,4096\nv_add_f32 v3, v3, v11\nM 88,180,124\nG 4\nv_add_f32 v4, v4, v12\nv_add_f32 v5, v5, v13\n"
"M 92,160,108\nR 176,189,49152\nv_add_f32 v6, v6, v14\nM 92,164,124\nbuffer_load_dwordx4 a[208:211], v252, s[20:23], s64 offen offset:1024 sc1\nM 76,160,104\n"
"R 178,189,53248\nv_add_f32 v7, v7, v15\nM 76,164,120\nv_add_f32 v0, v0, v4\nv_add_f32 v1, v1, v5\nM 60,160,100\n"
"R 180,185,0\nLq1034:\ns_lshr_b32 s60, s45, 3\ns_lshl2_add_u32 s60, s60, s40\ns_lshl_b32 s60, s60, 8\nv_lshl_add_u32 v224, v252, 3, s60\n"
"v_add_u32 v224, Lq1034+4096-LqPC, v224\nv_min_u32 v224, LqE-LqPC-4, v224\ns_mov_b64 exec, 3\nglobal_load_dword v224, v224, s[98:99]\ns_mov_b64 exec, -1\nM 60,164,116\n"
"v_add_f32 v2, v2, v6\nv_add_f32 v3, v3, v7\nM 44,160,96\nR 182,185,4096\nv_permlane32_swap_b32 v0, v2\nM 44,164,112\n"
"G 4\nv_permlane32_swap_b32 v1, v3\nv_add_f32 v0, v0, v2\nM 96,168,96\nR 160,190,49152\nv_add_f32 v1, v1, v3\n"
"M 96,172,112\nbuffer_load_dwordx4 a[212:215], v252, s[20:23], s65 offen offset:1024 sc1\nM 112,168,100\nR 162,190,53248\nv_permlane16_swap_b32 v0, v1\nM 112,172,116\n"
"s_add_u32 m0, s50, 40960\nv_add_f32 v230, v0, v1\nv_mov_b32 v232, v230\nM 128,168,104\nR 164,186,0\ns_add_u32 s63, s54, 2752512\n"
"M 128,172,120\nbuffer_load_dwordx4 a[216:219], v252, s[20:23], s66 offen offset:1024 sc1\nM 144,168,108\nR 166,186,4096\nv_permlane16_swap_b32 v230, v232\nM 144,172,124\n"
"s_waitcnt vmcnt(13)\ns_barrier\nG 4\nM 148,176,108\nR 168,191,49152\nv_mov_b32 v231, v230\n"
"M 148,180,124\nbuffer_load_dwordx4 v220, s[12:15], s63 offen lds\nM 132,176,104\nR 170,191,53248\nv_mov_b32 v233, v232\nM 132,180,120\n"
"buffer_load_dwordx4 a[220:223], v252, s[20:23], s67 offen offset:1024 sc1\nM 116,176,100\nR 172,187,0\nv_permlane32_swap_b32 v230, v231\nM 116,180,116\nbuffer_load_dwordx4 v221, s[12:15], s63 offen offset:1024 lds\n"
"M 100,176,96\nR 174,187,4096\ns_add_u32 m0, s50, 49152\nM 100,180,112\nG 4\nv_permlane32_swap_b32 v232, v233\n"
"v_mul_f32 v230, 0xba800000, v230\nM 104,160,96\nR 176,184,8192\ns_add_u32 s63, s54, 2981888\nM 104,164,112\nbuffer_load_dwordx4 a[224:227], v252, s[20:23], s64 offen offset:2048 sc1\n"
"M 120,160,100\nR 178,184,12288\nv_mul_f32 v214, v230, v230\nM 120,164,116\nbuffer_load_dwordx4 v220, s[12:15], s63 offen lds\nM 136,160,104\n"
"R 180,184,16384\nv_mul_f32 v231, 0x3a800000, v231\nM 136,164,120\nbuffer_load_dwordx4 v221, s[12:15], s63 offen offset:1024 lds\nM 152,160,108\nR 182,184,20480\n"
"v_sub_f32 v231, v231, v214\nM 152,164,124\nG 4\nv_max_f32 v231, 0, v231\nv_add_f32 v231, 0x3727c5ac, v231\nM 156,168,108\n"
"R 160,185,8192\nv_and_b32 v208, s77, v230\nM 156,172,124\nbuffer_load_dwordx4 a[228:231], v252, s[20:23], s65 offen offset:2048 sc1\nM 140,168,104\nR 162,185,12288\n"
"v_sub_f32 v230, v230, v208\nM 140,172,120\nLpg25:\ns_cbranch_execz Lpg29\nv_rsq_f32 v234, v231\nM 124,168,100\n"
"R 164,185,16384\nv_mul_f32 v216, v231, v234\nM 124,172,116\nbuffer_load_dwordx4 a[232:235], v252, s[20:23], s66 offen offset:2048 sc1\nM 108,168,96\nR 166,185,20480\n"
"v_cvt_pk_bf16_f32 v209, v230, v216\nM 108,172,112\nG 4\nv_cvt_pk_bf16_f32 v208, v208, v208\nv_mul_f32 v214, 0xc0135761, v234\nM 32,176,128\n"
"R 168,186,8192\nv_mul_f32 v215, v234, v234\nM 32,180,144\ns_mov_b64 s[60:61], 0xffff\nv_cndmask_b32 v208, 0, v208, s[60:61]\nv_mul_f32 v215, v215, v214\n"
"M 48,176,132\nR 170,186,12288\nv_mul_f32 v215, 0x3d372713, v215\nM 48,180,148\nbuffer_load_dwordx4 a[236:239], v252, s[20:23], s67 offen offset:2048 sc1\nM 64,176,136\n"
"R 172,186,16384\nv_mul_f32 v232, 0xba800000, v232\nM 64,180,152\ns_add_u32 m0, s50, 57344\ns_mov_b64 s[60:61], 0xffff\nv_cndmask_b32 v209, 0, v209, s[60:61]\n"
"M 80,176,140\nR 174,186,20480\ns_add_u32 s63, s54, 3211264\nM 80,180,156\ns_waitcnt vmcnt(13)\ns_barrier\n"
"G 4\nM 84,160,140\nR 176,187,8192\nv_mul_f32 v217, v232, v232\nM 84,164,156\nbuffer_load_dwordx4 a[240:243], v252, s[20:23], s64 offen offset:3072 sc1\n"
"M 68,160,136\nR 178,187,12288\nv_mul_f32 v233, 0x3a800000, v233\nM 68,164,152\nbuffer_load_dwordx4 v220, s[12:15], s63 offen lds\nM 52,160,132\n"
"R 180,187,16384\nv_sub_f32 v233, v233, v217\nM 52,164,148\nbuffer_load_dwordx4 v221, s[12:15], s63 offen offset:1024 lds\nM 36,160,128\nR 182,187,20480\n"
"s_add_u32 m0, s50, 65536\nM 36,164,144\nG 4\nv_max_f32 v233, 0, v233\nv_add_f32 v233, 0x3727c5ac, v233\nM 40,168,128\n"
"R 160,184,24576\ns_add_u32 s63, s54, 3440640\nM 40,172,144\nbuffer_load_dwordx4 a[244:247], v252, s[20:23], s65 offen offset:3072 sc1\nM 56,168,132\nR 162,184,28672\n"
"v_and_b32 v212, s77, v232\nM 56,172,148\nbuffer_load_dwordx4 v220, s[12:15], s63 offen lds\nM 72,168,136\nR 164,184,32768\nv_sub_f32 v232, v232, v212\n"
"M 72,172,152\nbuffer_load_dwordx4 a[248:251], v252, s[20:23], s66 offen offset:3072 sc1\nM 88,168,140\nR 166,184,36864\nv_cvt_pk_bf16_f32 v212, v212, v212\nM 88,172,156\n"
"G 4\nv_rsq_f32 v235, v233\nM 92,176,140\nR 168,185,24576\nbuffer_load_dwordx4 v221, s[12:15], s63 offen offset:1024 lds\nM 92,180,156\n"
"s_mov_b64 s[60:61], 0xffff\nv_cndmask_b32 v212, 0, v212, s[60:61]\nv_mul_f32 v219, v233, v235\nM 76,176,136\nR 170,185,28672\nv_cvt_pk_bf16_f32 v213, v232, v219\n"
"M 76,180,152\nbuffer_load_dwordx4 a[252:255], v252, s[20:23], s67 offen offset:3072 sc1\nM 60,176,132\nR 172,185,32768\nv_mul_f32 v217, 0xc0135761, v235\nM 60,180,148\n"
"s_mov_b64 s[60:61], 0xffff\nv_cndmask_b32 v213, 0, v213, s[60:61]\nv_mul_f32 v218, v235, v235\nM 44,176,128\nR 174,185,36864\nv_mul_f32 v218, v218, v217\n"
"M 44,180,144\nG 4\nv_mul_f32 v218, 0x3d372713, v218\nM 32,160,160\nR 176,186,24576\nM 32,164,176\n"
"M 48,160,164\nR 178,186,28672\nM 48,164,180\ns_add_u32 m0, s50, 73728\nM 64,160,168\nR 180,186,32768\n"
"M 64,164,184\ns_add_u32 s63, s54, 1835136\nM 80,160,172\nR 182,186,36864\nM 80,164,188\ns_waitcnt vmcnt(11)\n"
"s_barrier\nG 4\nM 84,168,172\nR 160,187,24576\nM 84,172,188\nbuffer_load_dwordx4 v220, s[12:15], s63 offen lds\n"
"M 68,168,168\nR 162,187,28672\nM 68,172,184\nM 52,168,164\nR 164,187,32768\nv_bfe_u32 v250, v252, 5, 1\n"
"v_mul_u32_u24 v250, 2560, v250\nM 52,172,180\nbuffer_load_dwordx4 v221, s[12:15], s63 offen offset:1024 lds\nv_and_b32 v251, 16, v252\nM 36,168,160\nR 166,187,36864\n"
"s_add_u32 m0, s50, 81920\nv_lshl_add_u32 v250, v251, 1, v250\nM 36,172,176\ns_lshl_b32 s60, s90, 6\nv_add_u32 v250, s60, v250\nG 4\n"
"M 40,176,160\nR 168,184,40960\ns_add_u32 s63, s54, 2064512\nM 40,180,176\nbuffer_load_dwordx4 v220, s[12:15], s63 offen lds\nM 56,176,164\n"
"R 170,184,45056\nM 56,180,180\nM 72,176,168\nR 172,184,49152\nM 72,180,184\nbuffer_load_dwordx4 v221, s[12:15], s63 offen offset:1024 lds\n"
"M 88,176,172\nR 174,184,53248\ns_add_u32 m0, s50, 90112\nM 88,180,188\nG 4\nM 92,160,172\n"
"R 176,185,40960\ns_add_u32 s63, s54, 2293888\nM 92,164,188\nbuffer_load_dwordx4 v220, s[12:15], s63 offen lds\nM 76,160,168\nR 178,185,45056\n"
"M 76,164,184\nM 60,160,164\nR 180,185,49152\nM 60,164,180\nbuffer_load_dwordx4 v221, s[12:15], s63 offen offset:1024 lds\nM 44,160,160\n"
"R 182,185,53248\ns_add_u32 s63, s54, 2523264\nM 44,164,176\nG 4\nM 32,168,192\nR 160,186,40960\n"
"M 32,172,208\ns_add_u32 m0, s50, 98304\nM 48,168,196\nR 162,186,45056\nM 48,172,212\nbuffer_load_dwordx4 v220, s[12:15], s63 offen lds\n"
"M 64,168,200\nR 164,186,49152\nM 64,172,216\nM 80,168,204\nR 166,186,53248\nM 80,172,220\n"
"s_waitcnt vmcnt(8)\ns_barrier\nG 4\nM 84,176,204\nR 168,187,40960\nM 84,180,220\n"
"buffer_load_dwordx4 v221, s[12:15], s63 offen offset:1024 lds\nM 68,176,200\nR 170,187,45056\ns_add_u32 s63, s54, 2752640\nM 68,180,216\ns_add_u32 m0, s50, 106496\n"
"M 52,176,196\nR 172,187,49152\nM 52,180,212\nbuffer_load_dwordx4 v220, s[12:15], s63 offen lds\nM 36,176,192\nR 174,187,53248\n"
"M 36,180,208\nG 4\nM 40,160,192\nR 176,184,57344\nM 40,164,208\nbuffer_load_dwordx4 v221, s[12:15], s63 offen offset:1024 lds\n"
"M 56,160,196\nR 178,184,61440\ns_add_u32 s63, s54, 2982016\nM 56,164,212\ns_add_u32 m0, s50, 114688\nM 72,160,200\n"
"R 180,188,0\nM 72,164,216\nbuffer_load_dwordx4 v220, s[12:15], s63 offen lds\nM 88,160,204\nR 182,188,4096\nM 88,164,220\n"
"G 4\nM 92,168,204\nR 160,185,57344\nM 92,172,220\nbuffer_load_dwordx4 v221, s[12:15], s63 offen offset:1024 lds\nM 76,168,200\n"
"R 162,185,61440\ns_add_u32 s63, s54, 3211392\nM 76,172,216\ns_add_u32 m0, s50, 0\nM 60,168,196\nR 164,189,0\n"
"M 60,172,212\nbuffer_load_dwordx4 v220, s[12:15], s63 offen lds\nM 44,168,192\nR 166,189,4096\nM 44,172,208\nG 4\n"
"M 32,176,224\nR 168,186,57344\nM 32,180,240\nM 48,176,228\nR 170,186,61440\nM 48,180,244\n"
"buffer_load_dwordx4 v221, s[12:15], s63 offen offset:1024 lds\nM 64,176,232\nR 172,190,0\ns_add_u32 s63, s54, 3440768\nM 64,180,248\ns_add_u32 m0, s50, 8192\n"
"M 80,176,236\nR 174,190,4096\ns_waitcnt vmcnt(14)\nM 80,180,252\ns_waitcnt vmcnt(10)\ns_barrier\n"
"G 4\nM 84,160,236\nR 176,187,57344\nM 84,164,252\nbuffer_load_dwordx4 v220, s[12:15], s63 offen lds\nM 68,160,232\n"
"R 178,187,61440\nM 68,164,248\nM 52,160,228\nR 180,191,0\nM 52,164,244\nbuffer_load_dwordx4 v221, s[12:15], s63 offen offset:1024 lds\n"
"M 36,160,224\nR 182,191,4096\ns_add_u32 m0, s50, 16384\nM 36,164,240\nG 4\nM 40,168,224\n"
"R 160,188,8192\ns_add_u32 s63, s55, 0\nM 40,172,240\nbuffer_load_dwordx4 v222, s[12:15], s63 offen lds\nM 56,168,228\nR 162,188,12288\n"
"M 56,172,244\nM 72,168,232\nR 164,188,16384\nM 72,172,248\nM 88,168,236\nR 166,188,20480\n"
"s_mov_b32 s60, 157184\nv_bfe_u32 v246, v252, 4, 4\nv_lshl_add_u32 v246, v246, 3, s60\nM 88,172,252\nds_read_b64 v[238:239], v246 offset:0\nG 5\n"
"M 92,176,236\nR 168,189,8192\nds_read_b64 v[240:241], v246 offset:128\nM 92,180,252\nds_read_b64 v[242:243], v246 offset:256\nM 76,176,232\n"
"R 170,189,12288\nds_read_b64 v[244:245], v246 offset:384\nM 76,180,248\nds_read_b64 v[246:247], v246 offset:0\nM 60,176,228\nR 172,189,16384\n"
"M 60,180,244\nbuffer_load_dwordx4 v223, s[12:15], s63 offen offset:1024 lds\nM 44,176,224\nR 174,189,20480\nM 44,180,240\ns_waitcnt vmcnt(10) lgkmcnt(2)\n"
"s_barrier\nM 96,160,128\nR 176,190,8192\nM 96,164,144\ns_mov_b32 m0, s51\nM 112,160,132\n"
"R 178,190,12288\nM 112,164,148\nds_write_addtid_b32 v64 offset:0\nM 128,160,136\nR 180,190,16384\nv_mfma_f32_16x16x32_bf16 v[32:35], v[238:241], v[208:211], v[32:35]\n"
"ds_write_addtid_b32 v65 offset:256\nds_write_addtid_b32 v66 offset:512\nM 128,164,152\nds_write_addtid_b32 v67 offset:768\nM 144,160,140\nR 182,190,20480\n"
"v_mfma_f32_16x16x32_bf16 v[36:39], v[240:243], v[208:211], v[36:39]\nds_write_addtid_b32 v68 offset:1024\nds_write_addtid_b32 v69 offset:1280\nM 144,164,156\nds_write_addtid_b32 v70 offset:1536\nG 11\n"
"M 148,168,140\nR 160,191,8192\nv_mfma_f32_16x16x32_bf16 v[40:43], v[242:245], v[208:211], v[40:43]\nds_write_addtid_b32 v71 offset:1792\nds_write_addtid_b32 v72 offset:2048\nM 148,172,156\n"
"ds_write_addtid_b32 v73 offset:2304\nM 132,168,136\nR 162,191,12288\nv_mfma_f32_16x16x32_bf16 v[44:47], v[244:247], v[208:211], v[44:47]\nds_write_addtid_b32 v74 offset:2560\nds_write_addtid_b32 v75 offset:2816\n"
"M 132,172,152\nds_write_addtid_b32 v76 offset:3072\nM 116,168,132\nR 164,191,16384\nv_mfma_f32_16x16x32_bf16 v[48:51], v[244:247], v[210:213], v[48:51]\nds_write_addtid_b32 v77 offset:3328\n"
"ds_write_addtid_b32 v78 offset:3584\nM 116,172,148\nds_write_addtid_b32 v79 offset:3840\nM 100,168,128\nR 166,191,20480\nv_mfma_f32_16x16x32_bf16 v[52:55], v[238:241], v[210:213], v[52:55]\n"
"ds_write_addtid_b32 v80 offset:4096\nds_write_addtid_b32 v81 offset:4352\nM 100,172,144\nds_write_addtid_b32 v82 offset:4608\nG 15\nM 104,176,128\n"
"R 168,188,24576\nv_mfma_f32_16x16x32_bf16 v[56:59], v[240:243], v[210:213], v[56:59]\nds_write_addtid_b32 v83 offset:4864\nds_write_addtid_b32 v84 offset:5120\nM 104,180,144\nds_write_addtid_b32 v85 offset:5376\n"
"M 120,176,132\nR 170,188,28672\nv_mfma_f32_16x16x32_bf16 v[60:63], v[242:245], v[210:213], v[60:63]\nds_write_addtid_b32 v86 offset:5632\nds_write_addtid_b32 v87 offset:5888\nM 120,180,148\n"
"ds_write_addtid_b32 v88 offset:6144\nM 136,176,136\nR 172,188,32768\nM 136,180,152\nds_write_addtid_b32 v89 offset:6400\nM 152,176,140\n"
"R 174,188,36864\nds_write_addtid_b32 v90 offset:6656\nM 152,180,156\nds_write_addtid_b32 v91 offset:6912\nds_write_addtid_b32 v92 offset:7168\nG 15\n"
"M 156,160,140\nR 176,189,24576\nds_write_addtid_b32 v93 offset:7424\nM 156,164,156\nds_write_addtid_b32 v94 offset:7680\nds_write_addtid_b32 v95 offset:7936\n"
"M 140,160,136\nR 178,189,28672\ns_add_u32 m0, s50, 24576\ns_add_u32 s63, s55, 458752\nM 140,164,152\nbuffer_load_dwordx4 v222, s[12:15], s63 offen lds\n"
"M 124,160,132\nR 180,189,32768\ns_waitcnt vmcnt(7) lgkmcnt(2)\ns_barrier\nM 124,164,148\nds_read2st64_b32 v[64:65], v225 offset0:0 offset1:1\n"
"ds_read2st64_b32 v[66:67], v225 offset0:2 offset1:3\nM 108,160,128\nR 182,189,36864\nds_read2st64_b32 v[68:69], v225 offset0:4 offset1:5\nM 108,164,144\nds_read2st64_b32 v[70:71], v225 offset0:6 offset1:7\n"
"ds_read2st64_b32 v[72:73], v225 offset0:8 offset1:9\nM 96,168,160\nR 160,190,24576\nds_read2st64_b32 v[74:75], v225 offset0:10 offset1:11\nM 96,172,176\nbuffer_load_dwordx4 v223, s[12:15], s63 offen offset:1024 lds\n"
"ds_read2st64_b32 v[76:77], v225 offset0:12 offset1:13\nM 112,168,164\nR 162,190,28672\nds_read2st64_b32 v[78:79], v225 offset0:14 offset1:15\nM 112,172,180\ns_add_u32 m0, s50, 32768\n"
"ds_read2st64_b32 v[80:81], v225 offset0:16 offset1:17\nds_read2st64_b32 v[82:83], v225 offset0:18 offset1:19\nM 128,168,168\nR 164,190,32768\nds_read2st64_b32 v[84:85], v225 offset0:20 offset1:21\nG 14\n"
"v_add_f32 v32, v32, v64\nM 128,172,184\ns_add_u32 s63, s55, 917504\nds_read2st64_b32 v[86:87], v225 offset0:22 offset1:23\nds_read2st64_b32 v[88:89], v225 offset0:24 offset1:25\nM 144,168,172\n"
"R 166,190,36864\nds_read2st64_b32 v[90:91], v225 offset0:26 offset1:27\nv_add_f32 v33, v33, v65\nM 144,172,188\nds_read2st64_b32 v[92:93], v225 offset0:28 offset1:29\nds_read2st64_b32 v[94:95], v225 offset0:30 offset1:31\n"
"G 15\nM 148,176,172\nR 168,191,24576\nv_add_f32 v34, v34, v66\nv_add_f32 v35, v35, v67\nM 148,180,188\n"
"buffer_load_dwordx4 v222, s[12:15], s63 offen lds\nv_add_f32 v36, v36, v68\nM 132,176,168\nR 170,191,28672\nv_add_f32 v37, v37, v69\nv_add_f32 v38, v38, v70\n"
"M 132,180,184\nv_add_f32 v39, v39, v71\nv_add_f32 v40, v40, v72\nM 116,176,164\nR 172,191,32768\nv_add_f32 v41, v41, v73\n"
"M 116,180,180\nG 15\nv_add_f32 v42, v42, v74\nv_add_f32 v43, v43, v75\nM 100,176,160\nR 174,191,36864\n"
"v_add_f32 v44, v44, v76\nM 100,180,176\nG 7\nv_add_f32 v45, v45, v77\nv_add_f32 v46, v46, v78\nM 104,160,160\n"
"R 176,188,40960\nv_add_f32 v47, v47, v79\nM 104,164,176\nbuffer_load_dwordx4 v223, s[12:15], s63 offen offset:1024 lds\nM 120,160,164\nR 178,188,45056\n"
"v_add_f32 v48, v48, v80\nM 120,164,180\ns_add_u32 m0, s50, 40960\nv_add_f32 v49, v49, v81\nv_add_f32 v50, v50, v82\nM 136,160,168\n"
"R 180,188,49152\nv_add_f32 v51, v51, v83\nM 136,164,184\ns_add_u32 s63, s55, 1376256\nv_add_f32 v52, v52, v84\nv_add_f32 v53, v53, v85\n"
"M 152,160,172\nR 182,188,53248\nv_add_f32 v54, v54, v86\nM 152,164,188\nG 4\nv_add_f32 v55, v55, v87\n"
"v_add_f32 v56, v56, v88\nM 156,168,172\nR 160,189,40960\nv_add_f32 v57, v57, v89\nM 156,172,188\nbuffer_load_dwordx4 v222, s[12:15], s63 offen lds\n"
"M 140,168,168\nR 162,189,45056\nv_add_f32 v58, v58, v90\nM 140,172,184\nv_add_f32 v59, v59, v91\nv_add_f32 v60, v60, v92\n"
"M 124,168,164\nR 164,189,49152\nv_add_f32 v61, v61, v93\nM 124,172,180\nv_add_f32 v62, v62, v94\nv_add_f32 v63, v63, v95\n"
"M 108,168,160\nR 166,189,53248\nv_mul_f32 v230, v32, v32\nv_mul_f32 v231, v33, v33\nM 108,172,176\ns_add_u32 s84, s73, 37748736\n"
"s_add_u32 s85, s73, 37863424\nG 4\nv_mul_f32 v232, v34, v34\nM 96,176,192\nR 168,190,40960\nv_mul_f32 v233, v35, v35\n"
"v_fma_f32 v230, v230, v215, v214\nM 96,180,208\nbuffer_load_dwordx4 v223, s[12:15], s63 offen offset:1024 lds\nv_fma_f32 v231, v231, v215, v214\nM 112,176,196\nR 170,190,45056\n"
"s_waitcnt vmcnt(6)\ns_barrier\nv_fma_f32 v232, v232, v215, v214\nM 112,180,212\ns_add_u32 m0, s50, 49152\nv_fma_f32 v233, v233, v215, v214\n"
"v_mul_f32 v230, v32, v230\nv_mul_f32 v231, v33, v231\nM 128,176,200\nR 172,190,49152\nv_mul_f32 v232, v34, v232\nv_mul_f32 v233, v35, v233\n"
"M 128,180,216\ns_add_u32 s63, s55, 1835008\nv_exp_f32 v230, v230\nv_exp_f32 v231, v231\nM 144,176,204\nR 174,190,53248\n"
"v_fma_f32 v230, v230, v216, v216\nv_exp_f32 v232, v232\nM 144,180,220\nLpg26:\ns_cbranch_execz Lpg30\nG 4\n"
"v_exp_f32 v233, v233\nv_fma_f32 v231, v231, v216, v216\nM 148,160,204\nR 176,191,40960\nv_fma_f32 v232, v232, v216, v216\nv_fma_f32 v233, v233, v216, v216\n"
"M 148,164,220\nbuffer_load_dwordx4 v222, s[12:15], s63 offen lds\nv_rcp_f32 v230, v230\nM 132,160,200\nR 178,191,45056\nv_mul_f32 v230, v32, v230\n"
"v_rcp_f32 v231, v231\nM 132,164,216\nv_rcp_f32 v232, v232\nv_rcp_f32 v233, v233\nM 116,160,196\nR 180,191,49152\n"
"v_mul_f32 v231, v33, v231\nv_mul_f32 v232, v34, v232\nM 116,164,212\nv_mul_f32 v233, v35, v233\nv_cvt_pk_bf16_f32 v32, v230, v231\nv_cvt_pk_bf16_f32 v33, v232, v233\n"
"M 100,160,192\nR 182,191,53248\nv_mul_f32 v234, v36, v36\nv_mul_f32 v235, v37, v37\nM 100,164,208\nG 4\n"
"v_mul_f32 v236, v38, v38\nv_mul_f32 v237, v39, v39\nv_fma_f32 v234, v234, v215, v214\nM 104,168,192\nR 160,184,0\nv_fma_f32 v235, v235, v215, v214\n"
"v_fma_f32 v236, v236, v215, v214\nM 104,172,208\nbuffer_load_dwordx4 v223, s[12:15], s63 offen offset:1024 lds\nv_fma_f32 v237, v237, v215, v214\nM 120,168,196\nR 162,184,4096\n"
"v_mul_f32 v234, v36, v234\nv_mul_f32 v235, v37, v235\nM 120,172,212\ns_add_u32 m0, s50, 57344\nv_exp_f32 v234, v234\nv_mul_f32 v236, v38, v236\n"
"M 136,168,200\nR 164,184,8192\nv_mul_f32 v237, v39, v237\nv_exp_f32 v235, v235\nM 136,172,216\ns_add_u32 s63, s55, 2293760\n"
"v_exp_f32 v236, v236\nv_exp_f32 v237, v237\nM 152,168,204\nR 166,184,12288\nv_fma_f32 v234, v234, v216, v216\nv_fma_f32 v235, v235, v216, v216\n"
"M 152,172,220\nG 4\nv_rcp_f32 v234, v234\nv_fma_f32 v236, v236, v216, v216\nM 156,176,204\nR 168,185,0\n"
"v_fma_f32 v237, v237, v216, v216\nv_rcp_f32 v235, v235\nM 156,180,220\nbuffer_load_dwordx4 v222, s[12:15], s63 offen lds\nv_rcp_f32 v236, v236\nM 140,176,200\n"
"R 170,185,4096\nv_mul_f32 v234, v36, v234\nv_rcp_f32 v237, v237\nM 140,180,216\nv_mul_f32 v235, v37, v235\nv_mul_f32 v236, v38, v236\n"
"v_mul_f32 v237, v39, v237\nM 124,176,196\nR 172,185,8192\nv_cvt_pk_bf16_f32 v34, v234, v235\nv_cvt_pk_bf16_f32 v35, v236, v237\nM 124,180,212\n"
"buffer_store_dwordx4 v[32:35], v252, s[20:23], s84 offen offset:0 sc1\nv_mul_f32 v230, v40, v40\nM 108,176,192\nR 174,185,12288\nv_mul_f32 v231, v41, v41\nv_mul_f32 v232, v42, v42\n"
"M 108,180,208\nG 4\nv_mul_f32 v233, v43, v43\nv_fma_f32 v230, v230, v215, v214\nv_fma_f32 v231, v231, v215, v214\nM 96,160,224\n"
"R 176,186,0\nv_fma_f32 v232, v232, v215, v214\nv_fma_f32 v233, v233, v215, v214\nM 96,164,240\nbuffer_load_dwordx4 v223, s[12:15], s63 offen offset:1024 lds\nv_mul_f32 v230, v40, v230\n"
"M 112,160,228\nR 178,186,4096\nv_mul_f32 v231, v41, v231\nv_mul_f32 v232, v42, v232\nM 112,164,244\ns_add_u32 m0, s50, 65536\n"
"v_exp_f32 v230, v230\nv_mul_f32 v233, v43, v233\nM 128,160,232\nR 180,186,8192\nv_fma_f32 v230, v230, v216, v216\nv_exp_f32 v231, v231\n"
"M 128,164,248\ns_add_u32 s63, s55, 2752512\nv_exp_f32 v232, v232\nv_exp_f32 v233, v233\nM 144,160,236\nR 182,186,12288\n"
"v_fma_f32 v231, v231, v216, v216\nv_fma_f32 v232, v232, v216, v216\nM 144,164,252\nG 4\nv_rcp_f32 v230, v230\nv_fma_f32 v233, v233, v216, v216\n"
"M 148,168,236\nR 160,187,0\nv_mul_f32 v230, v40, v230\nv_rcp_f32 v231, v231\nM 148,172,252\nbuffer_load_dwordx4 v222, s[12:15], s63 offen lds\n"
"v_rcp_f32 v232, v232\nM 132,168,232\nR 162,187,4096\nv_mul_f32 v231, v41, v231\nv_rcp_f32 v233, v233\nM 132,172,248\n"
"v_mul_f32 v232, v42, v232\nv_mul_f32 v233, v43, v233\nv_cvt_pk_bf16_f32 v40, v230, v231\nM 116,168,228\nR 164,187,8192\nv_cvt_pk_bf16_f32 v41, v232, v233\n"
"v_mul_f32 v234, v44, v44\nM 116,172,244\nv_mul_f32 v235, v45, v45\nv_mul_f32 v236, v46, v46\nv_mul_f32 v237, v47, v47\nM 100,168,224\n"
"R 166,187,12288\nv_fma_f32 v234, v234, v215, v214\nv_fma_f32 v235, v235, v215, v214\nM 100,172,240\nG 4\nv_fma_f32 v236, v236, v215, v214\n"
"v_fma_f32 v237, v237, v215, v214\nv_mul_f32 v234, v44, v234\nM 104,176,224\nR 168,184,16384\nv_mul_f32 v235, v45, v235\nv_mul_f32 v236, v46, v236\n"
"M 104,180,240\nbuffer_load_dwordx4 v223, s[12:15], s63 offen offset:1024 lds\nv_mul_f32 v237, v47, v237\nM 120,176,228\nR 170,184,20480\nv_mul_f32 v230, v48, v48\n"
"v_exp_f32 v234, v234\nM 120,180,244\nv_exp_f32 v235, v235\nv_exp_f32 v236, v236\nM 136,176,232\nR 172,186,16384\n"
"v_fma_f32 v234, v234, v216, v216\nv_exp_f32 v237, v237\nM 136,180,248\ns_add_u32 m0, s50, 73728\nv_rcp_f32 v234, v234\nv_fma_f32 v235, v235, v216, v216\n"
"M 152,176,236\nR 174,186,20480\ns_add_u32 s63, s55, 3211264\nv_fma_f32 v236, v236, v216, v216\nM 152,180,252\nG 4\n"
"v_rcp_f32 v235, v235\nv_fma_f32 v237, v237, v216, v216\nM 156,160,236\nR 176,185,16384\ns_mov_b32 s60, 157696\nv_bfe_u32 v246, v252, 4, 4\n"
"v_lshl_add_u32 v246, v246, 3, s60\nv_rcp_f32 v236, v236\nM 156,164,252\nds_read_b64 v[238:239], v246 offset:0\nv_mul_f32 v234, v44, v234\nv_rcp_f32 v237, v237\n"
"M 140,160,232\nR 178,185,20480\nds_read_b64 v[240:241], v246 offset:128\nv_mul_f32 v235, v45, v235\nM 140,164,248\nds_read_b64 v[242:243], v246 offset:256\n"
"buffer_load_dwordx4 v222, s[12:15], s63 offen lds\nv_mul_f32 v236, v46, v236\nM 124,160,228\nR 180,187,16384\nds_read_b64 v[244:245], v246 offset:384\nv_mul_f32 v237, v47, v237\n"
"M 124,164,244\nds_read_b64 v[246:247], v246 offset:0\nv_cvt_pk_bf16_f32 v42, v234, v235\nv_cvt_pk_bf16_f32 v43, v236, v237\nM 108,160,224\nR 182,187,20480\n"
"v_mul_f32 v231, v49, v49\nv_mul_f32 v232, v50, v50\nM 108,164,240\ns_waitcnt vmcnt(10) lgkmcnt(1)\ns_barrier\nv_mul_f32 v233, v51, v51\n"
"v_mfma_f32_16x16x32_bf16 v[0:3], v[168:171], a[0:3], 0\nR 160,184,24576\nbuffer_load_dwordx4 v223, s[12:15], s63 offen offset:1024 lds\nv_fma_f32 v230, v230, v218, v217\nM 0,172,16\ns_mov_b32 m0, s51\n"
"v_fma_f32 v231, v231, v218, v217\nv_fma_f32 v232, v232, v218, v217\nv_fma_f32 v233, v233, v218, v217\nv_mfma_f32_16x16x32_bf16 v[16:19], v[168:171], a[4:7], 0\nR 162,184,28672\nv_mul_f32 v230, v48, v230\n"
"v_mul_f32 v231, v49, v231\nM 16,172,20\nbuffer_store_dwordx4 v[40:43], v252, s[20:23], s84 offen offset:1024 sc1\nv_mul_f32 v232, v50, v232\nv_mfma_f32_16x16x32_bf16 v[8:11], v[168:171], a[8:11], 0\nR 164,186,24576\n"
"v_mul_f32 v233, v51, v233\nv_exp_f32 v230, v230\nM 8,172,24\nv_exp_f32 v231, v231\nv_exp_f32 v232, v232\nv_mfma_f32_16x16x32_bf16 v[24:27], v[168:171], a[12:15], 0\n"
"R 166,186,28672\nv_mfma_f32_16x16x32_bf16 v[96:99], v[238:241], v[208:211], v[96:99]\nv_exp_f32 v233, v233\nv_fma_f32 v230, v230, v219, v219\nv_fma_f32 v231, v231, v219, v219\nv_fma_f32 v232, v232, v219, v219\n"
"M 24,172,28\nG 4\nv_rcp_f32 v230, v230\nv_fma_f32 v233, v233, v219, v219\nv_mfma_f32_16x16x32_bf16 v[28:31], v[176:179], a[12:15], 0\nR 168,185,24576\n"
"v_mfma_f32_16x16x32_bf16 v[100:103], v[240:243], v[208:211], v[100:103]\nv_rcp_f32 v231, v231\nv_rcp_f32 v232, v232\nv_rcp_f32 v233, v233\nM 28,180,28\nv_mul_f32 v230, v48, v230\n"
"v_mul_f32 v231, v49, v231\nv_mul_f32 v232, v50, v232\nv_mfma_f32_16x16x32_bf16 v[12:15], v[176:179], a[8:11], 0\nR 170,185,28672\nv_mul_f32 v233, v51, v233\nv_mfma_f32_16x16x32_bf16 v[104:107], v[242:245], v[208:211], v[104:107]\n"
"v_cvt_pk_bf16_f32 v48, v230, v231\nv_cvt_pk_bf16_f32 v49, v232, v233\nv_mul_f32 v234, v52, v52\nv_mul_f32 v235, v53, v53\nM 12,180,24\nv_mul_f32 v236, v54, v54\n"
"v_mul_f32 v237, v55, v55\nv_fma_f32 v234, v234, v218, v217\nv_mfma_f32_16x16x32_bf16 v[20:23], v[176:179], a[4:7], 0\nR 172,187,24576\nv_fma_f32 v235, v235, v218, v217\nv_mfma_f32_16x16x32_bf16 v[108:111], v[244:247], v[208:211], v[108:111]\n"
"v_fma_f32 v236, v236, v218, v217\nv_fma_f32 v237, v237, v218, v217\nv_mul_f32 v234, v52, v234\nv_mul_f32 v235, v53, v235\nM 20,180,20\nds_write_addtid_b32 v128 offset:0\n"
"v_exp_f32 v234, v234\nv_mul_f32 v236, v54, v236\nv_mfma_f32_16x16x32_bf16 v[4:7], v[176:179], a[0:3], 0\nR 174,187,28672\nv_mfma_f32_16x16x32_bf16 v[112:115], v[244:247], v[210:213], v[112:115]\nds_write_addtid_b32 v129 offset:256\n"
"v_exp_f32 v235, v235\nv_exp_f32 v236, v236\nv_mul_f32 v237, v55, v237\nM 4,180,16\nds_write_addtid_b32 v130 offset:512\nG 7\n"
"v_exp_f32 v237, v237\nv_fma_f32 v234, v234, v219, v219\nM 0,160,32\nR 176,184,32768\nv_mfma_f32_16x16x32_bf16 v[116:119], v[238:241], v[210:213], v[116:119]\nds_write_addtid_b32 v131 offset:768\n"
"v_rcp_f32 v234, v234\nv_fma_f32 v235, v235, v219, v219\nv_fma_f32 v236, v236, v219, v219\nv_fma_f32 v237, v237, v219, v219\nM 0,164,48\nds_write_addtid_b32 v132 offset:1024\n"
"v_rcp_f32 v235, v235\nv_rcp_f32 v236, v236\nM 16,160,36\nR 178,184,36864\nv_mfma_f32_16x16x32_bf16 v[120:123], v[240:243], v[210:213], v[120:123]\nds_write_addtid_b32 v133 offset:1280\n"
"v_rcp_f32 v237, v237\nv_mul_f32 v234, v52, v234\nv_mul_f32 v235, v53, v235\nv_mul_f32 v236, v54, v236\nM 16,164,52\nds_write_addtid_b32 v134 offset:1536\n"
"v_mul_f32 v237, v55, v237\nv_cvt_pk_bf16_f32 v50, v234, v235\nv_cvt_pk_bf16_f32 v51, v236, v237\nM 8,160,40\nR 180,186,32768\nv_mul_f32 v230, v56, v56\n"
"v_mfma_f32_16x16x32_bf16 v[124:127], v[242:245], v[210:213], v[124:127]\nbuffer_store_dwordx4 v[48:51], v252, s[20:23], s85 offen offset:0 sc1\nds_write_addtid_b32 v135 offset:1792\nv_mul_f32 v231, v57, v57\nM 8,164,56\nds_write_addtid_b32 v136 offset:2048\n"
"v_mul_f32 v232, v58, v58\nv_mul_f32 v233, v59, v59\nv_fma_f32 v230, v230, v218, v217\nM 24,160,44\nR 182,186,36864\nds_write_addtid_b32 v137 offset:2304\n"
"v_fma_f32 v231, v231, v218, v217\nv_fma_f32 v232, v232, v218, v217\nM 24,164,60\nds_write_addtid_b32 v138 offset:2560\nG 14\nv_fma_f32 v233, v233, v218, v217\n"
"v_mul_f32 v230, v56, v230\nv_mul_f32 v231, v57, v231\nM 28,168,44\nR 160,185,32768\nds_write_addtid_b32 v139 offset:2816\nv_mul_f32 v232, v58, v232\n"
"v_mul_f32 v233, v59, v233\nM 28,172,60\nds_write_addtid_b32 v140 offset:3072\nds_write_addtid_b32 v141 offset:3328\nds_write_addtid_b32 v142 offset:3584\nds_write_addtid_b32 v143 offset:3840\n"
"ds_write_addtid_b32 v144 offset:4096\nds_write_addtid_b32 v145 offset:4352\nds_write_addtid_b32 v146 offset:4608\nds_write_addtid_b32 v147 offset:4864\nds_write_addtid_b32 v148 offset:5120\nds_write_addtid_b32 v149 offset:5376\n"
"ds_write_addtid_b32 v150 offset:5632\nds_write_addtid_b32 v151 offset:5888\nds_write_addtid_b32 v152 offset:6144\nds_write_addtid_b32 v153 offset:6400\nds_write_addtid_b32 v154 offset:6656\nds_write_addtid_b32 v155 offset:6912\n"
"ds_write_addtid_b32 v156 offset:7168\nds_write_addtid_b32 v157 offset:7424\nds_write_addtid_b32 v158 offset:7680\nds_write_addtid_b32 v159 offset:7936\nv_exp_f32 v230, v230\nv_exp_f32 v231, v231\n"
"M 12,168,40\nR 162,185,36864\ns_add_u32 m0, s50, 81920\ns_add_u32 s63, s54, 256\nv_exp_f32 v232, v232\nM 12,172,56\n"
"buffer_load_dwordx4 v220, s[12:15], s63 offen lds\nv_exp_f32 v233, v233\nM 20,168,36\nR 164,187,32768\ns_waitcnt vmcnt(5) lgkmcnt(2)\ns_barrier\n"
"v_fma_f32 v230, v230, v219, v219\nM 20,172,52\nds_read2st64_b32 v[128:129], v225 offset0:0 offset1:1\nv_rcp_f32 v230, v230\nv_fma_f32 v231, v231, v219, v219\nM 4,168,32\n"
"R 166,187,36864\nds_read2st64_b32 v[130:131], v225 offset0:2 offset1:3\nv_fma_f32 v232, v232, v219, v219\nv_fma_f32 v233, v233, v219, v219\nM 4,172,48\nds_read2st64_b32 v[132:133], v225 offset0:4 offset1:5\n"
"v_rcp_f32 v231, v231\nv_rcp_f32 v232, v232\nM 0,176,64\nR 168,184,40960\nds_read2st64_b32 v[134:135], v225 offset0:6 offset1:7\nv_mul_f32 v230, v56, v230\n"
"v_rcp_f32 v233, v233\nM 0,180,80\nbuffer_load_dwordx4 v221, s[12:15], s63 offen offset:1024 lds\nds_read2st64_b32 v[136:137], v225 offset0:8 offset1:9\nv_mul_f32 v231, v57, v231\nM 16,176,68\n"
"R 170,184,45056\ns_add_u32 m0, s50, 90112\ns_add_u32 s63, s54, 229632\nds_read2st64_b32 v[138:139], v225 offset0:10 offset1:11\nv_mul_f32 v232, v58, v232\nM 16,180,84\n"
"buffer_load_dwordx4 v220, s[12:15], s63 offen lds\nds_read2st64_b32 v[140:141], v225 offset0:12 offset1:13\nv_mul_f32 v233, v59, v233\nM 8,176,72\nR 172,186,40960\nds_read2st64_b32 v[142:143], v225 offset0:14 offset1:15\n"
"v_cvt_pk_bf16_f32 v56, v230, v231\nv_cvt_pk_bf16_f32 v57, v232, v233\nM 8,180,88\nds_read2st64_b32 v[144:145], v225 offset0:16 offset1:17\nv_mul_f32 v234, v60, v60\nv_mul_f32 v235, v61, v61\n"
"v_mul_f32 v236, v62, v62\nM 24,176,76\nR 174,186,45056\nds_read2st64_b32 v[146:147], v225 offset0:18 offset1:19\nv_mul_f32 v237, v63, v63\nv_fma_f32 v234, v234, v218, v217\n"
"M 24,180,92\nds_read2st64_b32 v[148:149], v225 offset0:20 offset1:21\nG 14\nv_fma_f32 v235, v235, v218, v217\nv_fma_f32 v236, v236, v218, v217\nv_fma_f32 v237, v237, v218, v217\n"
"M 28,160,76\nR 176,185,40960\nds_read2st64_b32 v[150:151], v225 offset0:22 offset1:23\nv_mul_f32 v234, v60, v234\nv_mul_f32 v235, v61, v235\nM 28,164,92\n"
"buffer_load_dwordx4 v221, s[12:15], s63 offen offset:1024 lds\nds_read2st64_b32 v[152:153], v225 offset0:24 offset1:25\nv_mul_f32 v236, v62, v236\nM 12,160,72\nR 178,185,45056\ns_add_u32 s63, s54, 459008\n"
"ds_read2st64_b32 v[154:155], v225 offset0:26 offset1:27\nv_mul_f32 v237, v63, v237\nM 12,164,88\ns_add_u32 m0, s50, 98304\nds_read2st64_b32 v[156:157], v225 offset0:28 offset1:29\nv_exp_f32 v234, v234\n"
"v_exp_f32 v235, v235\nM 20,160,68\nR 180,187,40960\nds_read2st64_b32 v[158:159], v225 offset0:30 offset1:31\nv_add_f32 v96, v96, v128\nv_exp_f32 v236, v236\n"
"M 20,164,84\nbuffer_load_dwordx4 v220, s[12:15], s63 offen lds\nv_add_f32 v97, v97, v129\nv_exp_f32 v237, v237\nM 4,160,64\nR 182,187,45056\n"
"G 15\nv_add_f32 v98, v98, v130\nv_add_f32 v99, v99, v131\nv_fma_f32 v234, v234, v219, v219\nM 4,164,80\nG 11\n"
"v_add_f32 v100, v100, v132\nv_add_f32 v101, v101, v133\nv_add_f32 v102, v102, v134\nv_fma_f32 v235, v235, v219, v219\nM 0,168,96\nR 160,184,49152\n"
"v_add_f32 v103, v103, v135\nv_add_f32 v104, v104, v136\nv_fma_f32 v236, v236, v219, v219\nM 0,172,112\nbuffer_load_dwordx4 v221, s[12:15], s63 offen offset:1024 lds\nv_add_f32 v105, v105, v137\n"
"v_fma_f32 v237, v237, v219, v219\nM 16,168,100\nR 162,184,53248\ns_add_u32 s63, s54, 688384\nv_add_f32 v106, v106, v138\nM 16,172,116\n"
"s_add_u32 m0, s50, 106496\nv_add_f32 v107, v107, v139\nv_add_f32 v108, v108, v140\nv_add_f32 v109, v109, v141\nM 8,168,104\nR 164,186,49152\n"
"v_add_f32 v110, v110, v142\nv_add_f32 v111, v111, v143\nM 8,172,120\nbuffer_load_dwordx4 v220, s[12:15], s63 offen lds\nv_add_f32 v112, v112, v144\nM 24,168,108\n"
"R 166,186,53248\nv_add_f32 v113, v113, v145\nG 14\nv_add_f32 v114, v114, v146\nM 24,172,124\nG 4\n"
"v_add_f32 v115, v115, v147\nv_add_f32 v116, v116, v148\nv_add_f32 v117, v117, v149\nM 28,176,108\nR 168,185,49152\nv_add_f32 v118, v118, v150\n"
"v_add_f32 v119, v119, v151\nM 28,180,124\nv_add_f32 v120, v120, v152\nv_add_f32 v121, v121, v153\nv_add_f32 v122, v122, v154\nM 12,176,104\n"
"R 170,185,53248\nv_add_f32 v123, v123, v155\nv_add_f32 v124, v124, v156\nM 12,180,120\nbuffer_load_dwordx4 v221, s[12:15], s63 offen offset:1024 lds\nv_add_f32 v125, v125, v157\n"
"M 20,176,100\nR 172,187,49152\ns_add_u32 s63, s54, 917760\nM 20,180,116\ns_add_u32 m0, s50, 114688\nv_add_f32 v126, v126, v158\n"
"v_add_f32 v127, v127, v159\nM 4,176,96\nR 174,187,53248\nM 4,180,112\nG 4\nv_rcp_f32 v234, v234\n"
"M 0,160,128\nR 176,184,57344\nv_mul_f32 v234, v60, v234\nM 0,164,144\nbuffer_load_dwordx4 v220, s[12:15], s63 offen lds\nM 16,160,132\n"
"R 178,184,61440\nM 16,164,148\nv_rcp_f32 v235, v235\nM 8,160,136\nR 180,186,57344\nLq1036:\n"
"s_lshr_b32 s60, s45, 3\ns_lshl2_add_u32 s60, s60, s40\ns_lshl_b32 s60, s60, 8\nv_lshl_add_u32 v224, v252, 3, s60\nv_add_u32 v224, Lq1036+4096-LqPC, v224\nv_min_u32 v224, LqE-LqPC-4, v224\n"
"s_mov_b64 exec, 3\nglobal_load_dword v224, v224, s[98:99]\ns_mov_b64 exec, -1\nM 8,164,152\nv_rcp_f32 v236, v236\nM 24,160,140\n"
"R 182,186,61440\nv_mul_f32 v235, v61, v235\nM 24,164,156\nG 4\nv_rcp_f32 v237, v237\nM 28,168,140\n"
"R 160,185,57344\nv_mul_f32 v236, v62, v236\nM 28,172,156\nbuffer_load_dwordx4 v221, s[12:15], s63 offen offset:1024 lds\nM 12,168,136\nR 162,185,61440\n"
"s_add_u32 s63, s54, 1147136\nM 12,172,152\ns_add_u32 m0, s50, 0\nv_mul_f32 v237, v63, v237\nv_cvt_pk_bf16_f32 v58, v234, v235\nM 20,168,132\n"
"R 164,187,57344\nv_cvt_pk_bf16_f32 v59, v236, v237\nM 20,172,148\nbuffer_load_dwordx4 v220, s[12:15], s63 offen lds\nM 4,168,128\nR 166,187,61440\n"
"M 4,172,144\nG 4\nM 0,176,160\nR 168,188,0\nM 0,180,176\nbuffer_load_dwordx4 v221, s[12:15], s63 offen offset:1024 lds\n"
"M 16,176,164\nR 170,188,4096\ns_add_u32 s63, s54, 1376512\nM 16,180,180\ns_add_u32 m0, s50, 8192\nM 8,176,168\n"
"R 172,190,0\nM 8,180,184\nbuffer_load_dwordx4 v220, s[12:15], s63 offen lds\nM 24,176,172\nR 174,190,4096\nM 24,180,188\n"
"buffer_store_dwordx4 v[56:59], v252, s[20:23], s85 offen offset:1024 sc1\nG 4\nM 28,160,172\nR 176,189,0\ns_waitcnt vmcnt(11)\nLpg27:\n"
"s_cbranch_execz Lpg31\ns_barrier\nv_mul_f32 v230, v96, v96\nM 28,164,188\ns_add_u32 s84, s73, 37750784\ns_add_u32 s85, s73, 37865472\n"
"v_mul_f32 v231, v97, v97\nv_mul_f32 v232, v98, v98\nM 12,160,168\nR 178,189,4096\nv_mul_f32 v233, v99, v99\nv_fma_f32 v230, v230, v215, v214\n"
"M 12,164,184\nbuffer_load_dwordx4 v221, s[12:15], s63 offen offset:1024 lds\nv_fma_f32 v231, v231, v215, v214\nM 20,160,164\nR 180,191,0\ns_add_u32 s63, s54, 1605888\n"
"v_fma_f32 v232, v232, v215, v214\nM 20,164,180\ns_add_u32 m0, s50, 16384\nv_fma_f32 v233, v233, v215, v214\nv_mul_f32 v230, v96, v230\nv_mul_f32 v231, v97, v231\n"
"M 4,160,160\nR 182,191,4096\nv_mul_f32 v232, v98, v232\nv_mul_f32 v233, v99, v233\nM 4,164,176\nG 4\n"
"v_exp_f32 v230, v230\nv_exp_f32 v231, v231\nM 0,168,192\nR 160,188,8192\nv_fma_f32 v230, v230, v216, v216\nv_exp_f32 v232, v232\n"
"M 0,172,208\nbuffer_load_dwordx4 v220, s[12:15], s63 offen lds\nv_exp_f32 v233, v233\nM 16,168,196\nR 162,188,12288\nv_fma_f32 v231, v231, v216, v216\n"
"v_fma_f32 v232, v232, v216, v216\nM 16,172,212\nv_rcp_f32 v230, v230\nv_fma_f32 v233, v233, v216, v216\nM 8,168,200\nR 164,190,8192\n"
"v_mul_f32 v230, v96, v230\nv_rcp_f32 v231, v231\nM 8,172,216\nbuffer_load_dwordx4 v221, s[12:15], s63 offen offset:1024 lds\nv_rcp_f32 v232, v232\nM 24,168,204\n"
"R 166,190,12288\ns_add_u32 m0, s50, 24576\nv_rcp_f32 v233, v233\nM 24,172,220\nG 4\nv_mul_f32 v231, v97, v231\n"
"v_mul_f32 v232, v98, v232\nv_mul_f32 v233, v99, v233\nM 28,176,204\nR 168,189,8192\ns_add_u32 s63, s54, 1835264\nv_cvt_pk_bf16_f32 v96, v230, v231\n"
"M 28,180,220\nbuffer_load_dwordx4 v220, s[12:15], s63 offen lds\nv_cvt_pk_bf16_f32 v97, v232, v233\nM 12,176,200\nR 170,189,12288\nv_mul_f32 v234, v100, v100\n"
"v_mul_f32 v235, v101, v101\nM 12,180,216\nv_mul_f32 v236, v102, v102\nv_mul_f32 v237, v103, v103\nv_fma_f32 v234, v234, v215, v214\nM 20,176,196\n"
"R 172,191,8192\nv_fma_f32 v235, v235, v215, v214\nv_fma_f32 v236, v236, v215, v214\nM 20,180,212\nbuffer_load_dwordx4 v221, s[12:15], s63 offen offset:1024 lds\nv_fma_f32 v237, v237, v215, v214\n"
"M 4,176,192\nR 174,191,12288\ns_add_u32 m0, s50, 32768\nv_mul_f32 v234, v100, v234\nM 4,180,208\nG 4\n"
"v_exp_f32 v234, v234\nv_mul_f32 v235, v101, v235\nM 0,160,224\nR 176,188,16384\ns_add_u32 s63, s54, 2064640\nv_mul_f32 v236, v102, v236\n"
"M 0,164,240\nbuffer_load_dwordx4 v220, s[12:15], s63 offen lds\nv_mul_f32 v237, v103, v237\nM 16,160,228\nR 178,188,20480\nv_fma_f32 v234, v234, v216, v216\n"
"v_exp_f32 v235, v235\nM 16,164,244\nv_exp_f32 v236, v236\nv_exp_f32 v237, v237\nM 8,160,232\nR 180,188,24576\n"
"v_fma_f32 v235, v235, v216, v216\nv_fma_f32 v236, v236, v216, v216\nM 8,164,248\nbuffer_load_dwordx4 v221, s[12:15], s63 offen offset:1024 lds\nv_fma_f32 v237, v237, v216, v216\nM 24,160,236\n"
"R 182,188,28672\ns_mov_b32 s60, 158720\nv_bfe_u32 v242, v252, 4, 4\nv_lshl_add_u32 v242, v242, 3, s60\nv_rcp_f32 v234, v234\nM 24,164,252\n"
"ds_read_b64 v[238:239], v242 offset:0\nG 5\nv_rcp_f32 v235, v235\nM 28,168,236\nR 160,189,16384\nds_read_b64 v[240:241], v242 offset:128\n"
"v_rcp_f32 v236, v236\nM 28,172,252\nds_read_b64 v[242:243], v242 offset:0\ns_add_u32 m0, s50, 40960\nv_rcp_f32 v237, v237\nM 12,168,232\n"
"R 162,189,20480\ns_add_u32 s63, s54, 2294016\nv_mul_f32 v234, v100, v234\nM 12,172,248\nbuffer_load_dwordx4 v220, s[12:15], s63 offen lds\nv_mul_f32 v235, v101, v235\n"
"M 20,168,228\nR 164,189,24576\nv_mul_f32 v236, v102, v236\nv_mul_f32 v237, v103, v237\nM 20,172,244\nv_cvt_pk_bf16_f32 v98, v234, v235\n"
"v_cvt_pk_bf16_f32 v99, v236, v237\nv_mul_f32 v230, v104, v104\nM 4,168,224\nR 166,189,28672\nv_mul_f32 v231, v105, v105\nv_mul_f32 v232, v106, v106\n"
"M 4,172,240\ns_waitcnt vmcnt(17) lgkmcnt(3)\ns_barrier\nv_mul_f32 v233, v107, v107\nv_mfma_f32_16x16x32_bf16 v[32:35], v[176:179], a[0:3], 0\nR 168,190,16384\n"
"v_fma_f32 v230, v230, v215, v214\nv_fma_f32 v231, v231, v215, v214\nM 32,180,16\ns_mov_b32 m0, s51\nv_fma_f32 v232, v232, v215, v214\nv_fma_f32 v233, v233, v215, v214\n"
"v_mul_f32 v230, v104, v230\nv_mfma_f32_16x16x32_bf16 v[48:51], v[176:179], a[4:7], 0\nR 170,190,20480\nv_mul_f32 v231, v105, v231\nv_mul_f32 v232, v106, v232\nM 48,180,20\n"
"buffer_store_dwordx4 v[96:99], v252, s[20:23], s84 offen offset:0 sc1\nv_mul_f32 v233, v107, v233\nv_mfma_f32_16x16x32_bf16 v[64:67], v[176:179], a[8:11], 0\nR 172,190,24576\nv_mov_b32 v226, s46\nv_mfma_f32_16x16x32_bf16 v[0:3], v[238:241], v[208:211], v[0:3]\n"
"v_exp_f32 v230, v230\ns_nop 0\nv_fma_f32 v230, v230, v216, v216\nv_exp_f32 v231, v231\nM 64,180,24\nv_mov_b32 v227, s47\n"
"v_fma_f32 v231, v231, v216, v216\nv_exp_f32 v232, v232\nv_mfma_f32_16x16x32_bf16 v[80:83], v[176:179], a[12:15], 0\nR 174,190,28672\nv_mfma_f32_16x16x32_bf16 v[4:7], v[240:243], v[208:211], v[4:7]\ns_add_u32 s61, s81, 845824\n"
"s_lshl_b32 s62, s44, 5\ns_add_u32 s61, s61, s62\ns_waitcnt vmcnt(9)\ns_mov_b64 exec, 1\nbuffer_atomic_swap_x2 v[226:227], off, s[20:23], s61 sc1\ns_mov_b64 exec, -1\n"
"v_exp_f32 v233, v233\nM 80,180,28\nds_write_addtid_b32 v8 offset:0\nG 5\nv_rcp_f32 v230, v230\nv_fma_f32 v232, v232, v216, v216\n"
"v_mfma_f32_16x16x32_bf16 v[84:87], v[160:163], a[12:15], 0\nR 176,191,16384\nv_mfma_f32_16x16x32_bf16 v[16:19], v[240:243], v[210:213], v[16:19]\nds_write_addtid_b32 v9 offset:256\nv_rcp_f32 v231, v231\nv_rcp_f32 v232, v232\n"
"v_fma_f32 v233, v233, v216, v216\nM 84,164,28\nds_write_addtid_b32 v10 offset:512\nv_rcp_f32 v233, v233\nv_mul_f32 v230, v104, v230\nv_mfma_f32_16x16x32_bf16 v[68:71], v[160:163], a[8:11], 0\n"
"R 178,191,20480\nv_mul_f32 v231, v105, v231\nv_mfma_f32_16x16x32_bf16 v[20:23], v[238:241], v[210:213], v[20:23]\nds_write_addtid_b32 v11 offset:768\nv_mul_f32 v232, v106, v232\nv_mul_f32 v233, v107, v233\n"
"v_cvt_pk_bf16_f32 v104, v230, v231\nv_cvt_pk_bf16_f32 v105, v232, v233\nM 68,164,24\nds_write_addtid_b32 v12 offset:1024\nv_mul_f32 v234, v108, v108\nv_mul_f32 v235, v109, v109\n"
"v_mul_f32 v236, v110, v110\nv_mfma_f32_16x16x32_bf16 v[52:55], v[160:163], a[4:7], 0\nR 180,191,24576\nds_write_addtid_b32 v13 offset:1280\nv_mul_f32 v237, v111, v111\nv_fma_f32 v234, v234, v215, v214\n"
"M 52,164,20\nds_write_addtid_b32 v14 offset:1536\nds_write_addtid_b32 v15 offset:1792\nds_write_addtid_b32 v24 offset:4096\nds_write_addtid_b32 v25 offset:4352\nds_write_addtid_b32 v26 offset:4608\n"
"ds_write_addtid_b32 v27 offset:4864\nds_write_addtid_b32 v28 offset:5120\nds_write_addtid_b32 v29 offset:5376\nds_write_addtid_b32 v30 offset:5632\nds_write_addtid_b32 v31 offset:5888\nv_fma_f32 v235, v235, v215, v214\n"
"v_fma_f32 v236, v236, v215, v214\nv_fma_f32 v237, v237, v215, v214\nv_mfma_f32_16x16x32_bf16 v[36:39], v[160:163], a[0:3], 0\nR 182,191,28672\ns_add_u32 m0, s50, 40960\nv_mul_f32 v234, v108, v234\n"
"M 36,164,16\nG 15\nv_exp_f32 v234, v234\nv_mul_f32 v235, v109, v235\nv_mfma_f32_16x16x32_bf16 v[40:43], v[168:171], a[0:3], 0\nR 160,188,32768\n"
"s_waitcnt vmcnt(9) lgkmcnt(2)\ns_barrier\nv_mul_f32 v236, v110, v236\nM 40,172,16\nbuffer_load_dwordx4 v221, s[12:15], s63 offen offset:1024 lds\nds_read2st64_b32 v[8:9], v225 offset0:0 offset1:1\n"
"v_mul_f32 v237, v111, v237\nv_mfma_f32_16x16x32_bf16 v[56:59], v[168:171], a[4:7], 0\nR 162,188,36864\nds_read2st64_b32 v[10:11], v225 offset0:2 offset1:3\nv_fma_f32 v234, v234, v216, v216\nv_exp_f32 v235, v235\n"
"M 56,172,20\ns_add_u32 m0, s50, 49152\nds_read2st64_b32 v[12:13], v225 offset0:4 offset1:5\nv_exp_f32 v236, v236\nv_exp_f32 v237, v237\nv_mfma_f32_16x16x32_bf16 v[72:75], v[168:171], a[8:11], 0\n"
"R 164,188,40960\nds_read2st64_b32 v[14:15], v225 offset0:6 offset1:7\nv_fma_f32 v235, v235, v216, v216\nv_fma_f32 v236, v236, v216, v216\nM 72,172,24\ns_add_u32 s63, s54, 2523392\n"
"ds_read2st64_b32 v[24:25], v225 offset0:16 offset1:17\nv_rcp_f32 v234, v234\nv_fma_f32 v237, v237, v216, v216\nv_mfma_f32_16x16x32_bf16 v[88:91], v[168:171], a[12:15], 0\nR 166,188,45056\nds_read2st64_b32 v[26:27], v225 offset0:18 offset1:19\n"
"v_mul_f32 v234, v108, v234\nv_rcp_f32 v235, v235\nM 88,172,28\nds_read2st64_b32 v[28:29], v225 offset0:20 offset1:21\nG 11\nv_rcp_f32 v236, v236\n"
"v_rcp_f32 v237, v237\nv_mfma_f32_16x16x32_bf16 v[92:95], v[176:179], a[12:15], 0\nR 168,189,32768\nds_read2st64_b32 v[30:31], v225 offset0:22 offset1:23\nv_mul_f32 v235, v109, v235\nv_mul_f32 v236, v110, v236\n"
"M 92,180,28\nbuffer_load_dwordx4 v220, s[12:15], s63 offen lds\nG 11\nv_add_f32 v0, v0, v8\nv_mul_f32 v237, v111, v237\nv_mfma_f32_16x16x32_bf16 v[76:79], v[176:179], a[8:11], 0\n"
"R 170,189,36864\nv_cvt_pk_bf16_f32 v106, v234, v235\nv_add_f32 v1, v1, v9\nv_cvt_pk_bf16_f32 v107, v236, v237\nM 76,180,24\nv_mul_f32 v230, v112, v112\n"
"v_mul_f32 v231, v113, v113\nG 10\nv_add_f32 v2, v2, v10\nv_mul_f32 v232, v114, v114\nv_mfma_f32_16x16x32_bf16 v[60:63], v[176:179], a[4:7], 0\nR 172,189,40960\n"
"v_mul_f32 v233, v115, v115\nv_add_f32 v3, v3, v11\nv_fma_f32 v230, v230, v218, v217\nM 60,180,20\nbuffer_store_dwordx4 v[104:107], v252, s[20:23], s84 offen offset:1024 sc1\nG 10\n"
"v_add_f32 v4, v4, v12\nv_fma_f32 v231, v231, v218, v217\nv_mfma_f32_16x16x32_bf16 v[44:47], v[176:179], a[0:3], 0\nR 174,189,45056\nv_fma_f32 v232, v232, v218, v217\nv_add_f32 v5, v5, v13\n"
"v_fma_f32 v233, v233, v218, v217\nM 44,180,16\nG 7\nv_mul_f32 v230, v112, v230\nv_mul_f32 v231, v113, v231\nv_add_f32 v6, v6, v14\n"
"v_mul_f32 v232, v114, v232\nM 32,160,32\nR 176,190,32768\nv_mov_b32 v248, 0\nv_add_f32 v7, v7, v15\nv_mul_f32 v233, v115, v233\n"
"M 32,164,48\nv_mov_b32 v249, 0\nv_mul_f32 v234, v116, v116\nv_add_f32 v16, v16, v24\nv_exp_f32 v230, v230\nM 48,160,36\n"
"R 178,190,36864\ns_add_u32 s61, s82, 845824\ns_mov_b64 exec, 0xf\nbuffer_atomic_or_x2 v[248:249], v250, s[20:23], s61 offen sc0 sc1\ns_mov_b64 exec, -1\nv_add_f32 v17, v17, v25\n"
"v_exp_f32 v231, v231\nM 48,164,52\nG 8\nv_add_f32 v18, v18, v26\nv_add_f32 v19, v19, v27\nG 7\n"
"v_add_f32 v20, v20, v28\nv_exp_f32 v232, v232\nM 64,160,40\nR 180,190,40960\nv_add_f32 v21, v21, v29\nG 6\n"
"v_add_f32 v22, v22, v30\nv_add_f32 v23, v23, v31\nM 64,164,56\nbuffer_load_dwordx4 v221, s[12:15], s63 offen offset:1024 lds\nM 80,160,44\nR 182,190,45056\n"
"v_fma_f32 v230, v230, v219, v219\nM 80,164,60\nG 4\nv_exp_f32 v233, v233\nM 84,168,44\nR 160,191,32768\n"
"v_fma_f32 v231, v231, v219, v219\nM 84,172,60\ns_add_u32 m0, s50, 57344\nv_rcp_f32 v230, v230\nM 68,168,40\nR 162,191,36864\n"
"v_fma_f32 v232, v232, v219, v219\nM 68,172,56\ns_add_u32 s63, s54, 2752768\nv_rcp_f32 v231, v231\nM 52,168,36\nR 164,191,40960\n"
"v_fma_f32 v233, v233, v219, v219\nM 52,172,52\nbuffer_load_dwordx4 v220, s[12:15], s63 offen lds\nM 36,168,32\nR 166,191,45056\nv_mul_f32 v230, v112, v230\n"
"M 36,172,48\nG 4\nv_rcp_f32 v232, v232\nM 40,176,32\nR 168,188,49152\nv_mul_f32 v231, v113, v231\n"
"M 40,180,48\nv_rcp_f32 v233, v233\nM 56,176,36\nR 170,188,53248\nv_mul_f32 v232, v114, v232\nM 56,180,52\n"
"buffer_load_dwordx4 v221, s[12:15], s63 offen offset:1024 lds\nM 72,176,40\nR 172,184,0\ns_add_u32 s63, s54, 2982144\nM 72,180,56\ns_add_u32 m0, s50, 65536\n"
"v_mul_f32 v233, v115, v233\nv_cvt_pk_bf16_f32 v112, v230, v231\nM 88,176,44\nR 174,184,4096\nv_cvt_pk_bf16_f32 v113, v232, v233\nM 88,180,60\n"
"G 4\nv_mul_f32 v235, v117, v117\nv_mul_f32 v236, v118, v118\nM 92,160,44\nR 176,189,49152\nv_mul_f32 v237, v119, v119\n"
"M 92,164,60\nbuffer_load_dwordx4 v220, s[12:15], s63 offen lds\nM 76,160,40\nR 178,189,53248\nv_fma_f32 v234, v234, v218, v217\nM 76,164,56\n"
"v_fma_f32 v235, v235, v218, v217\nv_fma_f32 v236, v236, v218, v217\nM 60,160,36\nR 180,185,0\nv_fma_f32 v237, v237, v218, v217\nM 60,164,52\n"
"v_mul_f32 v234, v116, v234\nv_mul_f32 v235, v117, v235\nM 44,160,32\nR 182,185,4096\nv_mul_f32 v236, v118, v236\nM 44,164,48\n"
"G 4\nv_exp_f32 v234, v234\nM 32,168,64\nR 160,190,49152\nv_mul_f32 v237, v119, v237\nM 32,172,80\n"
"buffer_load_dwordx4 v221, s[12:15], s63 offen offset:1024 lds\nM 48,168,68\nR 162,190,53248\nv_fma_f32 v234, v234, v219, v219\nM 48,172,84\ns_add_u32 m0, s50, 73728\n"
"v_exp_f32 v235, v235\nM 64,168,72\nR 164,186,0\nv_fma_f32 v235, v235, v219, v219\nM 64,172,88\ns_add_u32 s63, s54, 3211520\n"
"v_exp_f32 v236, v236\nM 80,168,76\nR 166,186,4096\nv_fma_f32 v236, v236, v219, v219\nM 80,172,92\nG 4\n"
"v_exp_f32 v237, v237\nM 84,176,76\nR 168,191,49152\nv_fma_f32 v237, v237, v219, v219\nM 84,180,92\nbuffer_load_dwordx4 v220, s[12:15], s63 offen lds\n"
"M 68,176,72\nR 170,191,53248\nv_mul_f32 v230, v120, v120\nM 68,180,88\nv_rcp_f32 v234, v234\nM 52,176,68\n"
"R 172,187,0\nv_mul_f32 v234, v116, v234\nM 52,180,84\ns_add_u32 s64, s68, 37748736\nv_rcp_f32 v235, v235\nM 36,176,64\n"
"R 174,187,4096\nv_mul_f32 v235, v117, v235\nM 36,180,80\nG 4\nv_rcp_f32 v236, v236\nM 40,160,64\n"
"R 176,184,8192\ns_waitcnt vmcnt(13)\ns_barrier\ns_waitcnt vmcnt(6)\nv_cmp_eq_u64 s[86:87], s[46:47], v[248:249]\nM 40,164,80\n"
"s_add_u32 s65, s68, 37863424\nv_rcp_f32 v237, v237\nM 56,160,68\nR 178,184,12288\ns_and_b32 s62, s86, 0xf\ns_cmp_eq_u32 s62, 0xf\n"
"s_cbranch_scc0 Lq838\nLq839:\nM 56,164,84\nbuffer_load_dwordx4 v221, s[12:15], s63 offen offset:1024 lds\nM 72,160,72\nR 180,184,16384\n"
"v_mul_f32 v236, v118, v236\nM 72,164,88\nbuffer_load_dwordx4 a[0:3], v252, s[20:23], s64 offen sc1\nM 88,160,76\nR 182,184,20480\nv_mul_f32 v237, v119, v237\n"
"M 88,164,92\nG 4\nv_cvt_pk_bf16_f32 v114, v234, v235\nv_cvt_pk_bf16_f32 v115, v236, v237\nM 92,168,76\nR 160,185,8192\n"
"v_mul_f32 v231, v121, v121\nM 92,172,92\nbuffer_load_dwordx4 a[4:7], v252, s[20:23], s65 offen sc1\nM 76,168,72\nR 162,185,12288\nv_mul_f32 v232, v122, v122\n"
"M 76,172,88\ns_add_u32 s66, s69, 37748736\nv_mul_f32 v233, v123, v123\nv_fma_f32 v230, v230, v218, v217\nM 60,168,68\nR 164,185,16384\n"
"v_fma_f32 v231, v231, v218, v217\nM 60,172,84\ns_add_u32 s67, s69, 37863424\nv_fma_f32 v232, v232, v218, v217\nv_fma_f32 v233, v233, v218, v217\nM 44,168,64\n"
"R 166,185,20480\nv_mul_f32 v230, v120, v230\nM 44,172,80\nG 4\nv_exp_f32 v230, v230\nM 32,176,96\n"
"R 168,186,8192\nv_mul_f32 v231, v121, v231\nM 32,180,112\nbuffer_load_dwordx4 a[8:11], v252, s[20:23], s66 offen sc1\nM 48,176,100\nR 170,186,12288\n"
"v_mul_f32 v232, v122, v232\nM 48,180,116\nbuffer_load_dwordx4 a[12:15], v252, s[20:23], s67 offen sc1\nM 64,176,104\nR 172,186,16384\nv_mul_f32 v233, v123, v233\n"
"M 64,180,120\ns_add_u32 s64, s68, 37755904\nv_exp_f32 v231, v231\nM 80,176,108\nR 174,186,20480\nv_fma_f32 v230, v230, v219, v219\n"
"M 80,180,124\nG 4\nv_exp_f32 v232, v232\nM 84,160,108\nR 176,187,8192\nv_fma_f32 v231, v231, v219, v219\n"
"M 84,164,124\nbuffer_load_dwordx4 a[16:19], v252, s[20:23], s64 offen sc1\nM 68,160,104\nR 178,187,12288\nv_fma_f32 v232, v232, v219, v219\nM 68,164,120\n"
"s_add_u32 s65, s68, 37870592\nv_exp_f32 v233, v233\nM 52,160,100\nR 180,187,16384\nv_fma_f32 v233, v233, v219, v219\nM 52,164,116\n"
"s_add_u32 s66, s69, 37755904\nv_rcp_f32 v230, v230\nM 36,160,96\nR 182,187,20480\nv_mul_f32 v230, v120, v230\nM 36,164,112\n"
"G 4\nv_rcp_f32 v231, v231\nM 40,168,96\nR 160,184,24576\nv_mul_f32 v231, v121, v231\nM 40,172,112\n"
"buffer_load_dwordx4 a[20:23], v252, s[20:23], s65 offen sc1\nM 56,168,100\nR 162,184,28672\nv_cvt_pk_bf16_f32 v120, v230, v231\nM 56,172,116\nbuffer_load_dwordx4 a[24:27], v252, s[20:23], s66 offen sc1\n"
"M 72,168,104\nR 164,184,32768\nv_mul_f32 v234, v124, v124\nM 72,172,120\ns_add_u32 s67, s69, 37870592\nv_rcp_f32 v232, v232\n"
"M 88,168,108\nR 166,184,36864\nv_mul_f32 v232, v122, v232\nM 88,172,124\nG 4\nv_rcp_f32 v233, v233\n"
"M 92,176,108\nR 168,185,24576\nv_mul_f32 v233, v123, v233\nM 92,180,124\nbuffer_load_dwordx4 a[28:31], v252, s[20:23], s67 offen sc1\nM 76,176,104\n"
"R 170,185,28672\nv_cvt_pk_bf16_f32 v121, v232, v233\nM 76,180,120\ns_add_u32 s64, s68, 37749760\nv_mul_f32 v235, v125, v125\nLpg28:\n"
"s_cbranch_execz LretO1n\nv_mul_f32 v236, v126, v126\nM 60,176,100\nR 172,185,32768\nv_mul_f32 v237, v127, v127\nM 60,180,116\n"
"s_add_u32 s65, s68, 37864448\nv_fma_f32 v234, v234, v218, v217\nv_fma_f32 v235, v235, v218, v217\nM 44,176,96\nR 174,185,36864\nv_fma_f32 v236, v236, v218, v217\n"
"M 44,180,112\nG 4\nv_fma_f32 v237, v237, v218, v217\nv_mul_f32 v234, v124, v234\nM 32,160,128\nR 176,186,24576\n"
"s_add_u32 m0, s50, 81920\nM 32,164,144\nbuffer_load_dwordx4 a[32:35], v252, s[20:23], s64 offen sc1\nM 48,160,132\nR 178,186,28672\ns_add_u32 s63, s54, 3440896\n"
"M 48,164,148\nbuffer_load_dwordx4 a[36:39], v252, s[20:23], s65 offen sc1\nM 64,160,136\nR 180,186,32768\nv_mul_f32 v235, v125, v235\nM 64,164,152\n"
"s_add_u32 s66, s69, 37749760\nbuffer_load_dwordx4 v220, s[12:15], s63 offen lds\nM 80,160,140\nR 182,186,36864\nv_mul_f32 v236, v126, v236\nM 80,164,156\n"
"G 4\nv_exp_f32 v234, v234\nM 84,168,140\nR 160,187,24576\nv_mul_f32 v237, v127, v237\nM 84,172,156\n"
"buffer_load_dwordx4 a[40:43], v252, s[20:23], s66 offen sc1\nbuffer_load_dwordx4 v221, s[12:15], s63 offen offset:1024 lds\nM 68,168,136\nR 162,187,28672\nv_fma_f32 v234, v234, v219, v219\nM 68,172,152\n"
"s_add_u32 s67, s69, 37864448\nv_exp_f32 v235, v235\nM 52,168,132\nR 164,187,32768\ns_waitcnt vmcnt(15)\ns_barrier\n"
"M 52,172,148\nbuffer_load_dwordx4 a[44:47], v252, s[20:23], s67 offen sc1\ns_movk_i32 s63, 2048\nv_mad_u32_u24 v220, v228, s63, v229\nv_add_u32 v221, 916480, v220\ns_add_u32 m0, s50, 90112\n"
"s_add_u32 s63, s56, 0\nbuffer_load_dwordx4 v220, s[16:19], s63 offen lds\nM 36,168,128\nR 166,187,36864\nv_fma_f32 v235, v235, v219, v219\nM 36,172,144\n"
"G 4\nv_exp_f32 v236, v236\nM 40,176,128\nR 168,184,40960\nv_fma_f32 v236, v236, v219, v219\nM 40,180,144\n"
"s_add_u32 s64, s68, 37756928\nbuffer_load_dwordx4 v221, s[16:19], s63 offen offset:1024 lds\nM 56,176,132\nR 170,184,45056\ns_add_u32 m0, s50, 98304\nM 56,180,148\n"
"buffer_load_dwordx4 a[48:51], v252, s[20:23], s64 offen sc1\nM 72,176,136\nR 172,184,49152\ns_add_u32 s63, s56, 524288\nM 72,180,152\ns_add_u32 s65, s68, 37871616\n"
"buffer_load_dwordx4 v220, s[16:19], s63 offen lds\nM 88,176,140\nR 174,184,53248\nM 88,180,156\nG 4\nv_exp_f32 v237, v237\n"
"M 92,160,140\nR 176,185,40960\nv_fma_f32 v237, v237, v219, v219\nM 92,164,156\nbuffer_load_dwordx4 a[52:55], v252, s[20:23], s65 offen sc1\nbuffer_load_dwordx4 v221, s[16:19], s63 offen offset:1024 lds\n"
"M 76,160,136\nR 178,185,45056\nM 76,164,152\ns_add_u32 s66, s69, 37756928\nv_rcp_f32 v234, v234\nM 60,160,132\n"
"R 180,185,49152\nv_mul_f32 v234, v124, v234\nM 60,164,148\nbuffer_load_dwordx4 a[56:59], v252, s[20:23], s66 offen sc1\nM 44,160,128\nR 182,185,53248\n"
"M 44,164,144\nG 4\nv_rcp_f32 v235, v235\nM 32,168,160\nR 160,186,40960\nv_mul_f32 v235, v125, v235\n"
"M 32,172,176\ns_add_u32 s67, s69, 37871616\nv_rcp_f32 v236, v236\nM 48,168,164\nR 162,186,45056\nv_mul_f32 v236, v126, v236\n"
"M 48,172,180\nbuffer_load_dwordx4 a[60:63], v252, s[20:23], s67 offen sc1\nM 64,168,168\nR 164,186,49152\nv_cvt_pk_bf16_f32 v122, v234, v235\nM 64,172,184\n"
"buffer_store_dwordx4 v[112:115], v252, s[20:23], s85 offen offset:0 sc1\nM 80,168,172\nR 166,186,53248\nv_rcp_f32 v237, v237\nM 80,172,188\nG 4\n"
"v_mul_f32 v237, v127, v237\nv_cvt_pk_bf16_f32 v123, v236, v237\nM 84,176,172\nR 168,187,40960\nM 84,180,188\ns_add_u32 m0, s50, 106496\n"
"buffer_store_dwordx4 v[120:123], v252, s[20:23], s85 offen offset:1024 sc1\nM 68,176,168\nR 170,187,45056\nv_mul_f32 v230, v0, v0\nv_mul_f32 v231, v1, v1\nM 68,180,184\n"
"s_add_u32 s63, s56, 65536\ns_add_u32 s84, s73, 37754880\ns_add_u32 s85, s73, 37869568\nv_mul_f32 v232, v2, v2\nM 52,176,164\nR 172,187,49152\n"
"v_mul_f32 v233, v3, v3\nv_fma_f32 v230, v230, v215, v214\nM 52,180,180\nbuffer_load_dwordx4 v220, s[16:19], s63 offen lds\nv_fma_f32 v231, v231, v215, v214\nM 36,176,160\n"
"R 174,187,53248\nv_fma_f32 v232, v232, v215, v214\nv_fma_f32 v233, v233, v215, v214\nM 36,180,176\nG 4\nv_mul_f32 v230, v0, v230\n"
"v_mul_f32 v231, v1, v231\nv_mul_f32 v232, v2, v232\nM 40,160,160\nR 176,184,57344\nv_mul_f32 v233, v3, v233\nv_exp_f32 v230, v230\n"
"M 40,164,176\nbuffer_load_dwordx4 v221, s[16:19], s63 offen offset:1024 lds\nv_exp_f32 v231, v231\nM 56,160,164\nR 178,184,61440\nv_fma_f32 v230, v230, v216, v216\n"
"v_exp_f32 v232, v232\nM 56,164,180\ns_add_u32 m0, s50, 114688\nv_exp_f32 v233, v233\nv_fma_f32 v231, v231, v216, v216\nM 72,160,168\n"
"R 180,188,0\nv_fma_f32 v232, v232, v216, v216\nv_fma_f32 v233, v233, v216, v216\nM 72,164,184\ns_add_u32 s63, s56, 589824\nv_rcp_f32 v230, v230\n"
"v_rcp_f32 v231, v231\nM 88,160,172\nR 182,188,4096\nv_mul_f32 v230, v0, v230\nv_rcp_f32 v232, v232\nM 88,164,188\n"
"G 4\nv_rcp_f32 v233, v233\nv_mul_f32 v231, v1, v231\nM 92,168,172\nR 160,185,57344\nv_mul_f32 v232, v2, v232\n"
"v_mul_f32 v233, v3, v233\nM 92,172,188\nbuffer_load_dwordx4 v220, s[16:19], s63 offen lds\nv_cvt_pk_bf16_f32 v0, v230, v231\nM 76,168,168\nR 162,185,61440\n"
"v_cvt_pk_bf16_f32 v1, v232, v233\nv_mul_f32 v234, v4, v4\nM 76,172,184\nv_mul_f32 v235, v5, v5\nv_mul_f32 v236, v6, v6\nv_mul_f32 v237, v7, v7\n"
"M 60,168,164\nR 164,189,0\nv_fma_f32 v234, v234, v215, v214\nv_fma_f32 v235, v235, v215, v214\nM 60,172,180\nbuffer_load_dwordx4 v221, s[16:19], s63 offen offset:1024 lds\n"
"v_fma_f32 v236, v236, v215, v214\nM 44,168,160\nR 166,189,4096\ns_add_u32 m0, s50, 0\nv_fma_f32 v237, v237, v215, v214\nM 44,172,176\n"
"G 4\nv_mul_f32 v234, v4, v234\nv_mul_f32 v235, v5, v235\nv_mul_f32 v236, v6, v236\nM 32,176,192\nR 168,186,57344\n"
"s_add_u32 s63, s57, 0\nv_mul_f32 v237, v7, v237\nM 32,180,208\nbuffer_load_dwordx4 v220, s[16:19], s63 offen lds\nv_exp_f32 v234, v234\nM 48,176,196\n"
"R 170,186,61440\nv_fma_f32 v234, v234, v216, v216\nv_exp_f32 v235, v235\nM 48,180,212\nv_exp_f32 v236, v236\nv_exp_f32 v237, v237\n"
"M 64,176,200\nR 172,190,0\nv_fma_f32 v235, v235, v216, v216\nv_fma_f32 v236, v236, v216, v216\nM 64,180,216\nbuffer_load_dwordx4 v221, s[16:19], s63 offen offset:1024 lds\n"
"v_fma_f32 v237, v237, v216, v216\nM 80,176,204\nR 174,190,4096\ns_add_u32 s63, s57, 524288\nv_rcp_f32 v234, v234\nM 80,180,220\n"
"G 4\nv_rcp_f32 v235, v235\nv_rcp_f32 v236, v236\nM 84,160,204\nR 176,187,57344\ns_waitcnt vmcnt(10)\n"
"s_barrier\nv_rcp_f32 v237, v237\nM 84,164,220\ns_add_u32 m0, s50, 8192\nv_mul_f32 v234, v4, v234\nv_mul_f32 v235, v5, v235\n"
"v_mul_f32 v236, v6, v236\nM 68,160,200\nR 178,187,61440\nv_mul_f32 v237, v7, v237\nv_cvt_pk_bf16_f32 v2, v234, v235\nM 68,164,216\n"
"buffer_load_dwordx4 v220, s[16:19], s63 offen lds\nv_cvt_pk_bf16_f32 v3, v236, v237\nM 52,160,196\nR 180,191,0\nv_mul_f32 v230, v16, v16\nv_mul_f32 v231, v17, v17\n"
"M 52,164,212\nv_mul_f32 v232, v18, v18\nv_mul_f32 v233, v19, v19\nv_fma_f32 v230, v230, v218, v217\nM 36,160,192\nR 182,191,4096\n"
"v_fma_f32 v231, v231, v218, v217\nv_fma_f32 v232, v232, v218, v217\nM 36,164,208\nG 4\nv_fma_f32 v233, v233, v218, v217\nv_mul_f32 v230, v16, v230\n"
"v_mul_f32 v231, v17, v231\nM 40,168,192\nR 160,188,8192\nv_mul_f32 v232, v18, v232\nv_mul_f32 v233, v19, v233\nM 40,172,208\n"
"buffer_load_dwordx4 v221, s[16:19], s63 offen offset:1024 lds\nv_exp_f32 v230, v230\nM 56,168,196\nR 162,188,12288\nv_fma_f32 v230, v230, v219, v219\nv_exp_f32 v231, v231\n"
"M 56,172,212\nbuffer_store_dwordx4 v[0:3], v252, s[20:23], s84 offen offset:0 sc1\nv_exp_f32 v232, v232\nM 72,168,200\nR 164,188,16384\nv_fma_f32 v231, v231, v219, v219\n"
"v_exp_f32 v233, v233\nM 72,172,216\ns_add_u32 m0, s50, 16384\nv_rcp_f32 v230, v230\nv_fma_f32 v232, v232, v219, v219\nM 88,168,204\n"
"R 166,188,20480\nv_mov_b32 v226, s46\nv_fma_f32 v233, v233, v219, v219\nM 88,172,220\nG 4\nv_mov_b32 v227, s47\n"
"v_mul_f32 v230, v16, v230\nv_rcp_f32 v231, v231\nM 92,176,204\nR 168,189,8192\ns_add_u32 s61, s81, 845832\ns_lshl_b32 s62, s44, 5\n"
"s_add_u32 s61, s61, s62\ns_waitcnt vmcnt(9)\ns_mov_b64 exec, 1\nbuffer_atomic_swap_x2 v[226:227], off, s[20:23], s61 sc1\ns_mov_b64 exec, -1\nv_rcp_f32 v232, v232\n"
"M 92,180,220\ns_add_u32 s63, s57, 65536\nv_rcp_f32 v233, v233\nv_mul_f32 v231, v17, v231\nM 76,176,200\nR 170,189,12288\n"
"v_mul_f32 v232, v18, v232\nv_mul_f32 v233, v19, v233\nM 76,180,216\nbuffer_load_dwordx4 v220, s[16:19], s63 offen lds\nv_cvt_pk_bf16_f32 v16, v230, v231\nM 60,176,196\n"
"R 172,189,16384\nv_cvt_pk_bf16_f32 v17, v232, v233\nv_mul_f32 v234, v20, v20\nM 60,180,212\nv_mul_f32 v235, v21, v21\nv_mul_f32 v236, v22, v22\n"
"v_mul_f32 v237, v23, v23\nM 44,176,192\nR 174,189,20480\nv_fma_f32 v234, v234, v218, v217\nv_fma_f32 v235, v235, v218, v217\nM 44,180,208\n"
"G 4\nv_fma_f32 v236, v236, v218, v217\nv_fma_f32 v237, v237, v218, v217\nv_mul_f32 v234, v20, v234\nM 32,160,224\nR 176,190,8192\n"
"v_mul_f32 v235, v21, v235\nv_mul_f32 v236, v22, v236\nM 32,164,240\nbuffer_load_dwordx4 v221, s[16:19], s63 offen offset:1024 lds\nv_mul_f32 v237, v23, v237\nM 48,160,228\n"
"R 178,190,12288\ns_add_u32 s63, s57, 589824\nv_exp_f32 v234, v234\nM 48,164,244\ns_add_u32 m0, s50, 24576\nv_exp_f32 v235, v235\n"
"v_exp_f32 v236, v236\nM 64,160,232\nR 180,190,16384\nv_fma_f32 v234, v234, v219, v219\nv_exp_f32 v237, v237\nM 64,164,248\n"
"buffer_load_dwordx4 v220, s[16:19], s63 offen lds\nv_fma_f32 v235, v235, v219, v219\nM 80,160,236\nR 182,190,20480\nv_fma_f32 v236, v236, v219, v219\nv_fma_f32 v237, v237, v219, v219\n"
"M 80,164,252\nG 4\nv_rcp_f32 v234, v234\nv_rcp_f32 v235, v235\nM 84,168,236\nR 160,191,8192\n"
"v_mul_f32 v234, v20, v234\nv_rcp_f32 v236, v236\nM 84,172,252\nbuffer_load_dwordx4 v221, s[16:19], s63 offen offset:1024 lds\nv_rcp_f32 v237, v237\nM 68,168,232\n"
"R 162,191,12288\ns_add_u32 s63, s56, 131072\nM 68,172,248\ns_add_u32 m0, s50, 32768\nv_mul_f32 v235, v21, v235\nv_mul_f32 v236, v22, v236\n"
"M 52,168,228\nR 164,191,16384\nv_mul_f32 v237, v23, v237\nM 52,172,244\nbuffer_load_dwordx4 v220, s[16:19], s63 offen lds\nM 36,168,224\n"
"R 166,191,20480\nv_cvt_pk_bf16_f32 v18, v234, v235\nM 36,172,240\nG 4\nv_cvt_pk_bf16_f32 v19, v236, v237\nM 40,176,224\n"
"R 168,188,24576\nbuffer_store_dwordx4 v[16:19], v252, s[20:23], s85 offen offset:0 sc1\nM 40,180,240\nbuffer_load_dwordx4 v221, s[16:19], s63 offen offset:1024 lds\nM 56,176,228\nR 170,188,28672\n"
"v_mov_b32 v248, 0\nM 56,180,244\nv_mov_b32 v249, 0\nM 72,176,232\nR 172,188,32768\ns_add_u32 s61, s82, 845832\n"
"s_mov_b64 exec, 0xf\nbuffer_atomic_or_x2 v[248:249], v250, s[20:23], s61 offen sc0 sc1\ns_mov_b64 exec, -1\nM 72,180,248\nM 88,176,236\nR 174,188,36864\n"
"s_mov_b32 s60, 158208\nv_bfe_u32 v246, v252, 4, 4\nv_lshl_add_u32 v246, v246, 3, s60\nM 88,180,252\nds_read_b64 v[238:239], v246 offset:0\nG 5\n"
"M 92,160,236\nR 176,189,24576\nds_read_b64 v[240:241], v246 offset:128\nM 92,164,252\nds_read_b64 v[242:243], v246 offset:256\ns_add_u32 m0, s50, 40960\n"
"M 76,160,232\nR 178,189,28672\nds_read_b64 v[244:245], v246 offset:384\nM 76,164,248\nds_read_b64 v[246:247], v246 offset:0\ns_add_u32 s63, s56, 655360\n"
"M 60,160,228\nR 180,189,32768\nM 60,164,244\nbuffer_load_dwordx4 v220, s[16:19], s63 offen lds\nM 44,160,224\nR 182,189,36864\n"
"M 44,164,240\ns_waitcnt vmcnt(15) lgkmcnt(2)\ns_barrier\nv_mfma_f32_16x16x32_bf16 v[96:99], v[168:171], a[0:3], 0\nR 160,190,24576\nM 96,172,16\n"
"s_mov_b32 m0, s51\nv_mfma_f32_16x16x32_bf16 v[112:115], v[168:171], a[4:7], 0\nR 162,190,28672\nM 112,172,20\nds_write_addtid_b32 v64 offset:0\nv_mfma_f32_16x16x32_bf16 v[128:131], v[168:171], a[8:11], 0\n"
"R 164,190,32768\nv_mfma_f32_16x16x32_bf16 v[32:35], v[238:241], v[208:211], v[32:35]\nds_write_addtid_b32 v65 offset:256\nds_write_addtid_b32 v66 offset:512\nM 128,172,24\nds_write_addtid_b32 v67 offset:768\n"
"v_mfma_f32_16x16x32_bf16 v[144:147], v[168:171], a[12:15], 0\nR 166,190,36864\nv_mfma_f32_16x16x32_bf16 v[36:39], v[240:243], v[208:211], v[36:39]\nds_write_addtid_b32 v68 offset:1024\nds_write_addtid_b32 v69 offset:1280\nM 144,172,28\n"
"ds_write_addtid_b32 v70 offset:1536\nG 11\nv_mfma_f32_16x16x32_bf16 v[148:151], v[176:179], a[12:15], 0\nR 168,191,24576\nv_mfma_f32_16x16x32_bf16 v[40:43], v[242:245], v[208:211], v[40:43]\nds_write_addtid_b32 v71 offset:1792\n"
"ds_write_addtid_b32 v72 offset:2048\nM 148,180,28\nds_write_addtid_b32 v73 offset:2304\nv_mfma_f32_16x16x32_bf16 v[132:135], v[176:179], a[8:11], 0\nR 170,191,28672\nv_mfma_f32_16x16x32_bf16 v[44:47], v[244:247], v[208:211], v[44:47]\n"
"ds_write_addtid_b32 v74 offset:2560\nds_write_addtid_b32 v75 offset:2816\nM 132,180,24\nds_write_addtid_b32 v76 offset:3072\nv_mfma_f32_16x16x32_bf16 v[116:119], v[176:179], a[4:7], 0\nR 172,191,32768\n"
"v_mfma_f32_16x16x32_bf16 v[48:51], v[244:247], v[210:213], v[48:51]\nds_write_addtid_b32 v77 offset:3328\nds_write_addtid_b32 v78 offset:3584\nM 116,180,20\nds_write_addtid_b32 v79 offset:3840\nv_mfma_f32_16x16x32_bf16 v[100:103], v[176:179], a[0:3], 0\n"
"R 174,191,36864\nv_mfma_f32_16x16x32_bf16 v[52:55], v[238:241], v[210:213], v[52:55]\nds_write_addtid_b32 v80 offset:4096\nds_write_addtid_b32 v81 offset:4352\nM 100,180,16\nds_write_addtid_b32 v82 offset:4608\n"
"G 15\nv_mfma_f32_16x16x32_bf16 v[104:107], v[160:163], a[0:3], 0\nR 176,188,40960\nv_mfma_f32_16x16x32_bf16 v[56:59], v[240:243], v[210:213], v[56:59]\nds_write_addtid_b32 v83 offset:4864\nds_write_addtid_b32 v84 offset:5120\n"
"M 104,164,16\nds_write_addtid_b32 v85 offset:5376\nv_mfma_f32_16x16x32_bf16 v[120:123], v[160:163], a[4:7], 0\nR 178,188,45056\nv_mfma_f32_16x16x32_bf16 v[60:63], v[242:245], v[210:213], v[60:63]\nds_write_addtid_b32 v86 offset:5632\n"
"ds_write_addtid_b32 v87 offset:5888\nM 120,164,20\nds_write_addtid_b32 v88 offset:6144\nv_mfma_f32_16x16x32_bf16 v[136:139], v[160:163], a[8:11], 0\nR 180,188,49152\nM 136,164,24\n"
"ds_write_addtid_b32 v89 offset:6400\nv_mfma_f32_16x16x32_bf16 v[152:155], v[160:163], a[12:15], 0\nR 182,188,53248\nds_write_addtid_b32 v90 offset:6656\nM 152,164,28\nds_write_addtid_b32 v91 offset:6912\n"
"ds_write_addtid_b32 v92 offset:7168\nG 15\nv_mfma_f32_16x16x32_bf16 v[156:159], v[168:171], a[12:15], 0\nR 160,189,40960\nds_write_addtid_b32 v93 offset:7424\nM 156,172,28\n"
"ds_write_addtid_b32 v94 offset:7680\nds_write_addtid_b32 v95 offset:7936\nv_mfma_f32_16x16x32_bf16 v[140:143], v[168:171], a[8:11], 0\nR 162,189,45056\ns_add_u32 m0, s50, 40960\nM 140,172,24\n"
"buffer_load_dwordx4 v221, s[16:19], s63 offen offset:1024 lds\nv_mfma_f32_16x16x32_bf16 v[124:127], v[168:171], a[4:7], 0\nR 164,189,49152\ns_waitcnt vmcnt(6) lgkmcnt(2)\ns_barrier\nM 124,172,20\n"
"ds_read2st64_b32 v[64:65], v225 offset0:0 offset1:1\nds_read2st64_b32 v[66:67], v225 offset0:2 offset1:3\nv_mfma_f32_16x16x32_bf16 v[108:111], v[168:171], a[0:3], 0\nR 166,189,53248\nds_read2st64_b32 v[68:69], v225 offset0:4 offset1:5\nM 108,172,16\n"
"ds_read2st64_b32 v[70:71], v225 offset0:6 offset1:7\nds_read2st64_b32 v[72:73], v225 offset0:8 offset1:9\nM 96,176,32\nR 168,190,40960\nds_read2st64_b32 v[74:75], v225 offset0:10 offset1:11\nM 96,180,48\n"
"s_add_u32 s64, s68, 37750784\nds_read2st64_b32 v[76:77], v225 offset0:12 offset1:13\nv_mov_b32 v226, s46\nM 112,176,36\nR 170,190,45056\nds_read2st64_b32 v[78:79], v225 offset0:14 offset1:15\n"
"v_mov_b32 v227, s47\ns_waitcnt vmcnt(2)\nv_cmp_eq_u64 s[86:87], s[46:47], v[248:249]\nM 112,180,52\ns_add_u32 s61, s81, 845848\ns_lshl_b32 s62, s44, 5\n"
"s_add_u32 s61, s61, s62\ns_mov_b64 exec, 1\nbuffer_atomic_swap_x2 v[226:227], off, s[20:23], s61 sc1\ns_mov_b64 exec, -1\nds_read2st64_b32 v[80:81], v225 offset0:16 offset1:17\nM 128,176,40\n"
"R 172,190,49152\ns_and_b32 s62, s86, 0xf\ns_cmp_eq_u32 s62, 0xf\ns_cbranch_scc0 Lq873\nLq874:\nds_read2st64_b32 v[82:83], v225 offset0:18 offset1:19\n"
"M 128,180,56\nLpg29:\ns_cbranch_execz LretO1n\ns_add_u32 s65, s68, 37865472\nds_read2st64_b32 v[84:85], v225 offset0:20 offset1:21\nds_read2st64_b32 v[86:87], v225 offset0:22 offset1:23\n"
"M 144,176,44\nR 174,190,53248\ns_add_u32 m0, s50, 49152\nds_read2st64_b32 v[88:89], v225 offset0:24 offset1:25\nM 144,180,60\nds_read2st64_b32 v[90:91], v225 offset0:26 offset1:27\n"
"ds_read2st64_b32 v[92:93], v225 offset0:28 offset1:29\nG 15\nM 148,160,44\nR 176,191,40960\ns_add_u32 s63, s56, 196608\nds_read2st64_b32 v[94:95], v225 offset0:30 offset1:31\n"
"M 148,164,60\nbuffer_load_dwordx4 a[64:67], v252, s[20:23], s64 offen sc1\nv_add_f32 v32, v32, v64\nM 132,160,40\nR 178,191,45056\nv_add_f32 v33, v33, v65\n"
"v_add_f32 v34, v34, v66\nM 132,164,56\ns_add_u32 s66, s69, 37750784\nv_add_f32 v35, v35, v67\nv_add_f32 v36, v36, v68\nv_add_f32 v37, v37, v69\n"
"M 116,160,36\nR 180,191,49152\nbuffer_load_dwordx4 v220, s[16:19], s63 offen lds\nv_add_f32 v38, v38, v70\nM 116,164,52\nbuffer_load_dwordx4 a[68:71], v252, s[20:23], s65 offen sc1\n"
"v_add_f32 v39, v39, v71\nM 100,160,32\nR 182,191,53248\nG 15\nv_add_f32 v40, v40, v72\nv_add_f32 v41, v41, v73\n"
"M 100,164,48\nG 8\nv_add_f32 v42, v42, v74\nv_add_f32 v43, v43, v75\nv_add_f32 v44, v44, v76\nM 104,168,32\n"
"R 160,184,0\nbuffer_load_dwordx4 v221, s[16:19], s63 offen offset:1024 lds\nv_add_f32 v45, v45, v77\nM 104,172,48\ns_add_u32 s67, s69, 37865472\nv_add_f32 v46, v46, v78\n"
"v_add_f32 v47, v47, v79\nv_add_f32 v48, v48, v80\nM 120,168,36\nR 162,184,4096\nv_add_f32 v49, v49, v81\nv_add_f32 v50, v50, v82\n"
"M 120,172,52\nbuffer_load_dwordx4 a[72:75], v252, s[20:23], s66 offen sc1\nM 136,168,40\nR 164,184,8192\nv_add_f32 v51, v51, v83\nM 136,172,56\n"
"v_add_f32 v52, v52, v84\nv_add_f32 v53, v53, v85\nM 152,168,44\nR 166,184,12288\nv_add_f32 v54, v54, v86\nM 152,172,60\n"
"G 4\nv_add_f32 v55, v55, v87\nv_add_f32 v56, v56, v88\nM 156,176,44\nR 168,185,0\nv_add_f32 v57, v57, v89\n"
"M 156,180,60\nbuffer_load_dwordx4 a[76:79], v252, s[20:23], s67 offen sc1\nM 140,176,40\nR 170,185,4096\nv_add_f32 v58, v58, v90\nM 140,180,56\n"
"s_add_u32 s64, s68, 37757952\nv_add_f32 v59, v59, v91\nv_add_f32 v60, v60, v92\nM 124,176,36\nR 172,185,8192\nv_add_f32 v61, v61, v93\n"
"M 124,180,52\ns_add_u32 s65, s68, 37872640\nv_add_f32 v62, v62, v94\nv_add_f32 v63, v63, v95\nM 108,176,32\nR 174,185,12288\n"
"v_mul_f32 v230, v32, v32\nv_mul_f32 v231, v33, v33\nM 108,180,48\ns_add_u32 s84, s73, 37752832\ns_add_u32 s85, s73, 37867520\nG 4\n"
"v_mul_f32 v232, v34, v34\nv_mfma_f32_16x16x32_bf16 v[0:3], v[160:163], a[0:3], 0\nR 176,186,0\ns_lshr_b32 s60, s45, 3\ns_lshl2_add_u32 s60, s60, s40\ns_lshl_b32 s60, s60, 8\n"
"v_lshl_add_u32 v224, v252, 3, s60\nv_add_u32 v224, LqF-32768-LqPC, v224\nv_min_u32 v224, LqE-LqPC-4, v224\ns_mov_b64 exec, 3\nglobal_load_dword v224, v224, s[98:99]\ns_mov_b64 exec, -1\n"
"v_mul_f32 v233, v35, v35\nM 0,164,16\nbuffer_load_dwordx4 a[80:83], v252, s[20:23], s64 offen sc1\nv_fma_f32 v230, v230, v215, v214\nv_mfma_f32_16x16x32_bf16 v[16:19], v[160:163], a[4:7], 0\nR 178,186,4096\n"
"v_fma_f32 v231, v231, v215, v214\nv_fma_f32 v232, v232, v215, v214\nM 16,164,20\ns_add_u32 s66, s69, 37757952\nv_fma_f32 v233, v233, v215, v214\nv_mul_f32 v230, v32, v230\n"
"v_mul_f32 v231, v33, v231\nv_mfma_f32_16x16x32_bf16 a[224:227], v[160:163], a[8:11], 0\nR 180,186,8192\nv_mul_f32 v232, v34, v232\nv_mul_f32 v233, v35, v233\nv_mfma_f32_16x16x32_bf16 a[224:227], v[164:167], a[24:27], a[224:227]\n"
"buffer_load_dwordx4 a[84:87], v252, s[20:23], s65 offen sc1\nv_exp_f32 v230, v230\nv_mfma_f32_16x16x32_bf16 a[240:243], v[160:163], a[12:15], 0\nR 182,186,12288\nv_fma_f32 v230, v230, v216, v216\nv_exp_f32 v231, v231\n"
"v_mfma_f32_16x16x32_bf16 a[240:243], v[164:167], a[28:31], a[240:243]\nG 4\nv_exp_f32 v232, v232\nv_exp_f32 v233, v233\nv_mfma_f32_16x16x32_bf16 a[244:247], v[168:171], a[12:15], 0\nR 160,187,0\n"
"s_add_u32 m0, s50, 57344\nv_fma_f32 v231, v231, v216, v216\nv_mfma_f32_16x16x32_bf16 a[244:247], v[172:175], a[28:31], a[244:247]\ns_add_u32 s67, s69, 37872640\nv_rcp_f32 v230, v230\nv_fma_f32 v232, v232, v216, v216\n"
"v_mfma_f32_16x16x32_bf16 a[228:231], v[168:171], a[8:11], 0\nR 162,187,4096\ns_add_u32 s63, s56, 720896\nv_fma_f32 v233, v233, v216, v216\nv_mfma_f32_16x16x32_bf16 a[228:231], v[172:175], a[24:27], a[228:231]\nbuffer_load_dwordx4 a[88:91], v252, s[20:23], s66 offen sc1\n"
"v_rcp_f32 v231, v231\nv_mfma_f32_16x16x32_bf16 v[20:23], v[168:171], a[4:7], 0\nR 164,187,8192\nv_mul_f32 v230, v32, v230\nv_rcp_f32 v232, v232\nM 20,172,20\n"
"buffer_load_dwordx4 v220, s[16:19], s63 offen lds\nv_rcp_f32 v233, v233\nv_mfma_f32_16x16x32_bf16 v[4:7], v[168:171], a[0:3], 0\nR 166,187,12288\nv_mul_f32 v231, v33, v231\nv_mul_f32 v232, v34, v232\n"
"M 4,172,16\nG 4\nv_mul_f32 v233, v35, v233\nv_cvt_pk_bf16_f32 v32, v230, v231\nv_cvt_pk_bf16_f32 v33, v232, v233\nv_mfma_f32_16x16x32_bf16 v[8:11], v[176:179], a[0:3], 0\n"
"R 168,184,16384\nv_mul_f32 v234, v36, v36\nv_mul_f32 v235, v37, v37\nM 8,180,16\nbuffer_load_dwordx4 a[92:95], v252, s[20:23], s67 offen sc1\nbuffer_load_dwordx4 v221, s[16:19], s63 offen offset:1024 lds\n"
"v_mul_f32 v236, v38, v38\nv_mfma_f32_16x16x32_bf16 v[24:27], v[176:179], a[4:7], 0\nR 170,184,20480\ns_add_u32 m0, s50, 65536\nv_mul_f32 v237, v39, v39\nM 24,180,20\n"
"s_add_u32 s64, s68, 37751808\nv_fma_f32 v234, v234, v215, v214\nv_fma_f32 v235, v235, v215, v214\nv_fma_f32 v236, v236, v215, v214\nv_mfma_f32_16x16x32_bf16 a[232:235], v[176:179], a[8:11], 0\nR 172,184,24576\n"
"s_add_u32 s63, s57, 131072\nv_fma_f32 v237, v237, v215, v214\nv_mfma_f32_16x16x32_bf16 a[232:235], v[180:183], a[24:27], a[232:235]\ns_add_u32 s65, s68, 37866496\nbuffer_load_dwordx4 v220, s[16:19], s63 offen lds\nv_mul_f32 v234, v36, v234\n"
"v_mfma_f32_16x16x32_bf16 a[248:251], v[176:179], a[12:15], 0\nR 174,184,28672\nv_mul_f32 v235, v37, v235\nv_mul_f32 v236, v38, v236\nv_mfma_f32_16x16x32_bf16 a[248:251], v[180:183], a[28:31], a[248:251]\nG 4\n"
"v_exp_f32 v234, v234\nv_mul_f32 v237, v39, v237\nv_mfma_f32_16x16x32_bf16 a[252:255], v[160:163], a[12:15], 0\nR 176,185,16384\nv_fma_f32 v234, v234, v216, v216\nv_exp_f32 v235, v235\n"
"v_mfma_f32_16x16x32_bf16 a[252:255], v[164:167], a[28:31], a[252:255]\nbuffer_load_dwordx4 a[96:99], v252, s[20:23], s64 offen sc1\nbuffer_load_dwordx4 v221, s[16:19], s63 offen offset:1024 lds\nv_exp_f32 v236, v236\nv_mfma_f32_16x16x32_bf16 a[236:239], v[160:163], a[8:11], 0\nR 178,185,20480\n"
"s_add_u32 m0, s50, 73728\nv_exp_f32 v237, v237\nv_mfma_f32_16x16x32_bf16 a[236:239], v[164:167], a[24:27], a[236:239]\ns_add_u32 s66, s69, 37751808\nv_rcp_f32 v234, v234\nv_fma_f32 v235, v235, v216, v216\n"
"v_mfma_f32_16x16x32_bf16 v[28:31], v[160:163], a[4:7], 0\nR 180,185,24576\ns_add_u32 s63, s57, 655360\nv_fma_f32 v236, v236, v216, v216\nM 28,164,20\nbuffer_load_dwordx4 a[100:103], v252, s[20:23], s65 offen sc1\n"
"buffer_load_dwordx4 v220, s[16:19], s63 offen lds\nv_fma_f32 v237, v237, v216, v216\nv_mfma_f32_16x16x32_bf16 v[12:15], v[160:163], a[0:3], 0\nR 182,185,28672\nv_mul_f32 v234, v36, v234\nv_rcp_f32 v235, v235\n"
"M 12,164,16\ns_waitcnt vmcnt(14)\ns_barrier\nG 4\nv_rcp_f32 v236, v236\nM 0,168,32\n"
"R 160,186,16384\nv_mul_f32 v235, v37, v235\nv_rcp_f32 v237, v237\nM 0,172,48\ns_add_u32 s67, s69, 37866496\nbuffer_load_dwordx4 v221, s[16:19], s63 offen offset:1024 lds\n"
"v_mul_f32 v236, v38, v236\nM 16,168,36\nR 162,186,20480\nv_mul_f32 v237, v39, v237\nv_cvt_pk_bf16_f32 v34, v234, v235\nM 16,172,52\n"
"buffer_load_dwordx4 a[104:107], v252, s[20:23], s66 offen sc1\nv_cvt_pk_bf16_f32 v35, v236, v237\nv_mfma_f32_16x16x32_bf16 a[224:227], v[168:171], a[40:43], a[224:227]\nR 164,186,24576\nv_mul_f32 v230, v40, v40\nv_mul_f32 v231, v41, v41\n"
"v_mfma_f32_16x16x32_bf16 a[224:227], v[172:175], a[56:59], a[224:227]\nv_mul_f32 v232, v42, v42\nv_mul_f32 v233, v43, v43\nv_fma_f32 v230, v230, v215, v214\nv_mfma_f32_16x16x32_bf16 a[240:243], v[168:171], a[44:47], a[240:243]\nR 166,186,28672\n"
"v_fma_f32 v231, v231, v215, v214\nv_fma_f32 v232, v232, v215, v214\nv_mfma_f32_16x16x32_bf16 a[240:243], v[172:175], a[60:63], a[240:243]\nG 4\nv_fma_f32 v233, v233, v215, v214\nv_mul_f32 v230, v40, v230\n"
"v_mul_f32 v231, v41, v231\nv_mfma_f32_16x16x32_bf16 a[244:247], v[176:179], a[44:47], a[244:247]\nR 168,187,16384\nv_mul_f32 v232, v42, v232\nv_mul_f32 v233, v43, v233\nv_mfma_f32_16x16x32_bf16 a[244:247], v[180:183], a[60:63], a[244:247]\n"
"buffer_load_dwordx4 a[108:111], v252, s[20:23], s67 offen sc1\nv_exp_f32 v230, v230\nv_mfma_f32_16x16x32_bf16 a[228:231], v[176:179], a[40:43], a[228:231]\nR 170,187,20480\nv_fma_f32 v230, v230, v216, v216\nv_exp_f32 v231, v231\n"
"v_mfma_f32_16x16x32_bf16 a[228:231], v[180:183], a[56:59], a[228:231]\ns_add_u32 s64, s68, 37758976\nv_exp_f32 v232, v232\nv_exp_f32 v233, v233\nM 20,176,36\nR 172,187,24576\n"
"v_fma_f32 v231, v231, v216, v216\nv_fma_f32 v232, v232, v216, v216\nM 20,180,52\ns_add_u32 s65, s68, 37873664\nv_rcp_f32 v230, v230\nv_fma_f32 v233, v233, v216, v216\n"
"M 4,176,32\nR 174,187,28672\ns_add_u32 m0, s50, 81920\nv_rcp_f32 v231, v231\nM 4,180,48\nG 4\n"
"v_rcp_f32 v232, v232\nv_rcp_f32 v233, v233\nM 8,160,32\nR 176,184,32768\ns_add_u32 s63, s57, 196608\nv_mul_f32 v230, v40, v230\n"
"M 8,164,48\nbuffer_load_dwordx4 a[112:115], v252, s[20:23], s64 offen sc1\nv_mul_f32 v231, v41, v231\nM 24,160,36\nR 178,184,36864\nv_mul_f32 v232, v42, v232\n"
"v_mul_f32 v233, v43, v233\nM 24,164,52\ns_add_u32 s66, s69, 37758976\nbuffer_load_dwordx4 v220, s[16:19], s63 offen lds\nv_cvt_pk_bf16_f32 v40, v230, v231\nv_mfma_f32_16x16x32_bf16 a[232:235], v[160:163], a[40:43], a[232:235]\n"
"R 180,184,40960\nv_cvt_pk_bf16_f32 v41, v232, v233\nv_mul_f32 v234, v44, v44\nv_mfma_f32_16x16x32_bf16 a[232:235], v[164:167], a[56:59], a[232:235]\nbuffer_load_dwordx4 a[116:119], v252, s[20:23], s65 offen sc1\nv_mul_f32 v235, v45, v45\n"
"v_mfma_f32_16x16x32_bf16 a[248:251], v[160:163], a[44:47], a[248:251]\nR 182,184,45056\nv_mul_f32 v236, v46, v46\nv_mul_f32 v237, v47, v47\nv_mfma_f32_16x16x32_bf16 a[248:251], v[164:167], a[60:63], a[248:251]\nbuffer_load_dwordx4 v221, s[16:19], s63 offen offset:1024 lds\n"
"G 4\nv_fma_f32 v234, v234, v215, v214\nv_mfma_f32_16x16x32_bf16 a[252:255], v[168:171], a[44:47], a[252:255]\nR 160,185,32768\ns_add_u32 m0, s50, 90112\nv_fma_f32 v235, v235, v215, v214\n"
"v_mfma_f32_16x16x32_bf16 a[252:255], v[172:175], a[60:63], a[252:255]\ns_add_u32 s67, s69, 37873664\ns_add_u32 s63, s57, 720896\nv_fma_f32 v236, v236, v215, v214\nv_fma_f32 v237, v237, v215, v214\nv_mfma_f32_16x16x32_bf16 a[236:239], v[168:171], a[40:43], a[236:239]\n"
"R 162,185,36864\nv_mov_b32 v248, 0\nv_mul_f32 v234, v44, v234\nv_mfma_f32_16x16x32_bf16 a[236:239], v[172:175], a[56:59], a[236:239]\nbuffer_load_dwordx4 a[120:123], v252, s[20:23], s66 offen sc1\nbuffer_load_dwordx4 v220, s[16:19], s63 offen lds\n"
"v_mov_b32 v249, 0\nv_mul_f32 v235, v45, v235\nM 28,168,36\nR 164,185,40960\ns_add_u32 s61, s82, 845848\ns_mov_b64 exec, 0xf\n"
"buffer_atomic_or_x2 v[248:249], v250, s[20:23], s61 offen sc0 sc1\ns_mov_b64 exec, -1\nv_mul_f32 v236, v46, v236\nM 28,172,52\nbuffer_load_dwordx4 v221, s[16:19], s63 offen offset:1024 lds\nv_mul_f32 v237, v47, v237\n"
"M 12,168,32\nR 166,185,45056\ns_add_u32 m0, s50, 98304\nv_exp_f32 v234, v234\nM 12,172,48\nG 4\n"
"v_exp_f32 v235, v235\nv_exp_f32 v236, v236\nM 96,176,64\nR 168,186,32768\ns_add_u32 s63, s58, 0\nv_exp_f32 v237, v237\n"
"s_waitcnt vmcnt(21)\nM 96,180,80\nbuffer_load_dwordx4 a[124:127], v252, s[20:23], s67 offen sc1\nv_fma_f32 v234, v234, v216, v216\nM 112,176,68\nR 170,186,36864\n"
"v_fma_f32 v235, v235, v216, v216\nv_fma_f32 v236, v236, v216, v216\ns_waitcnt vmcnt(21)\nM 112,180,84\nbuffer_load_dwordx4 v220, s[16:19], s63 offen lds\nv_fma_f32 v237, v237, v216, v216\n"
"M 128,176,72\nR 172,186,40960\nv_mul_f32 v230, v48, v48\nv_rcp_f32 v234, v234\ns_waitcnt vmcnt(21)\nM 128,180,88\n"
"buffer_load_dwordx4 v221, s[16:19], s63 offen offset:1024 lds\nv_rcp_f32 v235, v235\nM 144,176,76\nR 174,186,45056\ns_add_u32 m0, s50, 106496\nv_rcp_f32 v236, v236\n"
"s_waitcnt vmcnt(20)\nM 144,180,92\nG 4\nv_rcp_f32 v237, v237\nv_mul_f32 v234, v44, v234\nM 148,160,76\n"
"R 176,187,32768\ns_add_u32 s63, s58, 524288\nv_mul_f32 v235, v45, v235\nM 148,164,92\nbuffer_load_dwordx4 v220, s[16:19], s63 offen lds\nv_mul_f32 v236, v46, v236\n"
"M 132,160,72\nR 178,187,36864\nv_mul_f32 v237, v47, v237\nv_cvt_pk_bf16_f32 v42, v234, v235\nM 132,164,88\nbuffer_store_dwordx4 v[32:35], v252, s[20:23], s84 offen offset:0 sc1\n"
"v_cvt_pk_bf16_f32 v43, v236, v237\nM 116,160,68\nR 180,187,40960\nv_mul_f32 v231, v49, v49\nv_mul_f32 v232, v50, v50\nM 116,164,84\n"
"buffer_load_dwordx4 v221, s[16:19], s63 offen offset:1024 lds\nv_mul_f32 v233, v51, v51\nM 100,160,64\nR 182,187,45056\nv_fma_f32 v230, v230, v218, v217\nv_fma_f32 v231, v231, v218, v217\n"
"M 100,164,80\nG 4\nv_fma_f32 v232, v232, v218, v217\nv_fma_f32 v233, v233, v218, v217\nv_mul_f32 v230, v48, v230\nM 104,168,64\n"
"R 160,184,49152\ns_waitcnt vmcnt(16)\ns_barrier\nv_mul_f32 v231, v49, v231\nM 104,172,80\nbuffer_store_dwordx4 v[40:43], v252, s[20:23], s84 offen offset:1024 sc1\n"
"v_mul_f32 v232, v50, v232\nM 120,168,68\nR 162,184,53248\nv_mul_f32 v233, v51, v233\nv_exp_f32 v230, v230\nM 120,172,84\n"
"s_add_u32 m0, s50, 114688\nv_exp_f32 v231, v231\nv_exp_f32 v232, v232\nM 136,168,72\nR 164,184,57344\nv_fma_f32 v230, v230, v219, v219\n"
"v_exp_f32 v233, v233\nM 136,172,88\ns_add_u32 s63, s58, 65536\nv_rcp_f32 v230, v230\nv_fma_f32 v231, v231, v219, v219\nM 152,168,76\n"
"R 166,184,61440\nv_fma_f32 v232, v232, v219, v219\nv_fma_f32 v233, v233, v219, v219\nM 152,172,92\nG 4\nv_rcp_f32 v231, v231\n"
"v_rcp_f32 v232, v232\nM 156,176,76\nR 168,185,49152\nv_mul_f32 v230, v48, v230\nv_rcp_f32 v233, v233\nM 156,180,92\n"
"buffer_load_dwordx4 v220, s[16:19], s63 offen lds\nv_mul_f32 v231, v49, v231\nM 140,176,72\nR 170,185,53248\nv_mul_f32 v232, v50, v232\nv_mul_f32 v233, v51, v233\n"
"M 140,180,88\nv_cvt_pk_bf16_f32 v48, v230, v231\nv_cvt_pk_bf16_f32 v49, v232, v233\nv_mul_f32 v234, v52, v52\nM 124,176,68\nR 172,185,57344\n"
"v_mul_f32 v235, v53, v53\nv_mul_f32 v236, v54, v54\nM 124,180,84\nbuffer_load_dwordx4 v221, s[16:19], s63 offen offset:1024 lds\nv_mul_f32 v237, v55, v55\nM 108,176,64\n"
"R 174,185,61440\ns_add_u32 m0, s50, 0\nv_fma_f32 v234, v234, v218, v217\nM 108,180,80\nG 4\nv_fma_f32 v235, v235, v218, v217\n"
"v_fma_f32 v236, v236, v218, v217\nv_fma_f32 v237, v237, v218, v217\nM 96,160,96\nR 176,186,49152\ns_add_u32 s63, s58, 589824\nv_mul_f32 v234, v52, v234\n"
"s_waitcnt vmcnt(16)\nM 96,164,112\nbuffer_load_dwordx4 v220, s[16:19], s63 offen lds\nv_mul_f32 v235, v53, v235\nM 112,160,100\nR 178,186,53248\n"
"v_mul_f32 v236, v54, v236\nv_mul_f32 v237, v55, v237\ns_waitcnt vmcnt(15)\nM 112,164,116\nv_exp_f32 v234, v234\nv_exp_f32 v235, v235\n"
"M 128,160,104\nR 180,186,57344\nv_fma_f32 v234, v234, v219, v219\nv_exp_f32 v236, v236\ns_waitcnt vmcnt(13)\nM 128,164,120\n"
"v_exp_f32 v237, v237\nv_fma_f32 v235, v235, v219, v219\nM 144,160,108\nR 182,186,61440\nv_fma_f32 v236, v236, v219, v219\nv_fma_f32 v237, v237, v219, v219\n"
"s_waitcnt vmcnt(9)\nM 144,164,124\nG 4\nv_rcp_f32 v234, v234\nv_rcp_f32 v235, v235\nM 148,168,108\n"
"R 160,187,49152\nv_mul_f32 v234, v52, v234\nv_rcp_f32 v236, v236\nM 148,172,124\ns_add_u32 s64, s68, 37754880\nv_rcp_f32 v237, v237\n"
"v_mul_f32 v235, v53, v235\nM 132,168,104\nR 162,187,53248\nv_cmp_eq_u64 s[86:87], s[46:47], v[248:249]\nv_mul_f32 v236, v54, v236\nM 132,172,120\n"
"buffer_load_dwordx4 v221, s[16:19], s63 offen offset:1024 lds\nv_mul_f32 v237, v55, v237\nM 116,168,100\nR 164,187,57344\ns_and_b32 s62, s86, 0xf\ns_cmp_eq_u32 s62, 0xf\n"
"s_cbranch_scc0 Lq908\nLq909:\nv_cvt_pk_bf16_f32 v50, v234, v235\nM 116,172,116\ns_add_u32 s65, s68, 37869568\nv_cvt_pk_bf16_f32 v51, v236, v237\n"
"v_mul_f32 v230, v56, v56\nv_mul_f32 v231, v57, v57\nM 100,168,96\nR 166,187,61440\nv_mul_f32 v232, v58, v58\nv_mul_f32 v233, v59, v59\n"
"M 100,172,112\nG 4\nv_fma_f32 v230, v230, v218, v217\nv_fma_f32 v231, v231, v218, v217\nM 104,176,96\nR 168,188,0\n"
"v_fma_f32 v232, v232, v218, v217\nM 104,180,112\nbuffer_load_dwordx4 a[128:131], v252, s[20:23], s64 offen sc1\nM 120,176,100\nR 170,188,4096\nv_fma_f32 v233, v233, v218, v217\n"
"M 120,180,116\ns_add_u32 s66, s69, 37754880\nv_mul_f32 v230, v56, v230\nv_mul_f32 v231, v57, v231\nM 136,176,104\nR 172,188,8192\n"
"v_mul_f32 v232, v58, v232\nM 136,180,120\nbuffer_load_dwordx4 a[132:135], v252, s[20:23], s65 offen sc1\nM 152,176,108\nR 174,188,12288\nv_mul_f32 v233, v59, v233\n"
"M 152,180,124\nG 4\nv_exp_f32 v230, v230\nM 156,160,108\nR 176,189,0\nv_fma_f32 v230, v230, v219, v219\n"
"M 156,164,124\ns_add_u32 s67, s69, 37869568\nv_exp_f32 v231, v231\nM 140,160,104\nR 178,189,4096\nv_fma_f32 v231, v231, v219, v219\n"
"M 140,164,120\nbuffer_load_dwordx4 a[136:139], v252, s[20:23], s66 offen sc1\nM 124,160,100\nR 180,189,8192\nv_mul_f32 v234, v60, v60\nM 124,164,116\n"
"buffer_store_dwordx4 v[48:51], v252, s[20:23], s85 offen offset:0 sc1\nM 108,160,96\nR 182,189,12288\ns_add_u32 m0, s50, 8192\nM 108,164,112\ns_waitcnt vmcnt(9)\n"
"s_barrier\nG 4\nM 0,168,64\nR 160,190,0\ns_add_u32 s63, s58, 131072\nM 0,172,80\n"
"buffer_load_dwordx4 a[140:143], v252, s[20:23], s67 offen sc1\nM 16,168,68\nR 162,190,4096\nv_mul_f32 v235, v61, v61\nM 16,172,84\ns_add_u32 s64, s68, 37762048\n"
"v_exp_f32 v232, v232\nv_mfma_f32_16x16x32_bf16 a[224:227], v[168:171], a[72:75], a[224:227]\nR 164,190,8192\nbuffer_load_dwordx4 v220, s[16:19], s63 offen lds\nv_mfma_f32_16x16x32_bf16 a[224:227], v[172:175], a[88:91], a[224:227]\nLpg30:\n"
"s_cbranch_execz LretO1n\ns_add_u32 s65, s68, 37876736\nv_exp_f32 v233, v233\nv_mfma_f32_16x16x32_bf16 a[240:243], v[168:171], a[76:79], a[240:243]\nR 166,190,12288\nv_fma_f32 v232, v232, v219, v219\n"
"v_mfma_f32_16x16x32_bf16 a[240:243], v[172:175], a[92:95], a[240:243]\nG 4\nv_rcp_f32 v230, v230\nv_mfma_f32_16x16x32_bf16 a[244:247], v[176:179], a[76:79], a[244:247]\nR 168,191,0\nbuffer_load_dwordx4 v221, s[16:19], s63 offen offset:1024 lds\n"
"v_mfma_f32_16x16x32_bf16 a[244:247], v[180:183], a[92:95], a[244:247]\nbuffer_load_dwordx4 a[144:147], v252, s[20:23], s64 offen sc1\nv_mfma_f32_16x16x32_bf16 a[228:231], v[176:179], a[72:75], a[228:231]\nR 170,191,4096\ns_add_u32 m0, s50, 16384\nv_mfma_f32_16x16x32_bf16 a[228:231], v[180:183], a[88:91], a[228:231]\n"
"s_add_u32 s66, s69, 37762048\ns_add_u32 s63, s58, 655360\nv_fma_f32 v233, v233, v219, v219\nM 20,176,68\nR 172,191,8192\nbuffer_load_dwordx4 v220, s[16:19], s63 offen lds\n"
"M 20,180,84\nbuffer_load_dwordx4 a[148:151], v252, s[20:23], s65 offen sc1\nM 4,176,64\nR 174,191,12288\nv_mul_f32 v230, v56, v230\nM 4,180,80\n"
"G 4\nv_rcp_f32 v231, v231\nM 8,160,64\nR 176,188,16384\nbuffer_load_dwordx4 v221, s[16:19], s63 offen offset:1024 lds\nM 8,164,80\n"
"s_add_u32 s67, s69, 37876736\nv_rcp_f32 v232, v232\nM 24,160,68\nR 178,188,20480\nv_mul_f32 v231, v57, v231\nM 24,164,84\n"
"buffer_load_dwordx4 a[152:155], v252, s[20:23], s66 offen sc1\nv_mfma_f32_16x16x32_bf16 a[232:235], v[160:163], a[72:75], a[232:235]\nR 180,188,24576\nv_mul_f32 v232, v58, v232\nv_mfma_f32_16x16x32_bf16 a[232:235], v[164:167], a[88:91], a[232:235]\nv_rcp_f32 v233, v233\n"
"v_mfma_f32_16x16x32_bf16 a[248:251], v[160:163], a[76:79], a[248:251]\nR 182,188,28672\nv_mul_f32 v233, v59, v233\nv_mfma_f32_16x16x32_bf16 a[248:251], v[164:167], a[92:95], a[248:251]\nG 4\nv_cvt_pk_bf16_f32 v56, v230, v231\n"
"v_cvt_pk_bf16_f32 v57, v232, v233\nv_mfma_f32_16x16x32_bf16 a[252:255], v[168:171], a[76:79], a[252:255]\nR 160,189,16384\nv_mul_f32 v236, v62, v62\nv_mfma_f32_16x16x32_bf16 a[252:255], v[172:175], a[92:95], a[252:255]\nbuffer_load_dwordx4 a[156:159], v252, s[20:23], s67 offen sc1\n"
"v_mfma_f32_16x16x32_bf16 a[236:239], v[168:171], a[72:75], a[236:239]\nR 162,189,20480\nv_mul_f32 v237, v63, v63\nv_mfma_f32_16x16x32_bf16 a[236:239], v[172:175], a[88:91], a[236:239]\nv_fma_f32 v234, v234, v218, v217\nv_fma_f32 v235, v235, v218, v217\n"
"M 28,168,68\nR 164,189,24576\nv_fma_f32 v236, v236, v218, v217\nM 28,172,84\ns_add_u32 m0, s50, 24576\nv_fma_f32 v237, v237, v218, v217\n"
"v_mul_f32 v234, v60, v234\nM 12,168,64\nR 166,189,28672\nv_mul_f32 v235, v61, v235\nM 12,172,80\nG 4\n"
"v_exp_f32 v234, v234\nM 0,176,96\nR 168,190,16384\nv_mul_f32 v236, v62, v236\nM 0,180,112\ns_add_u32 s63, s58, 196608\n"
"v_exp_f32 v235, v235\nM 16,176,100\nR 170,190,20480\nv_mul_f32 v237, v63, v237\nM 16,180,116\nbuffer_load_dwordx4 v220, s[16:19], s63 offen lds\n"
"v_mfma_f32_16x16x32_bf16 a[224:227], v[176:179], a[104:107], a[224:227]\nR 172,190,24576\nv_fma_f32 v234, v234, v219, v219\nv_mfma_f32_16x16x32_bf16 a[224:227], v[180:183], a[120:123], a[224:227]\nv_exp_f32 v236, v236\nv_mfma_f32_16x16x32_bf16 a[240:243], v[176:179], a[108:111], a[240:243]\n"
"R 174,190,28672\nv_fma_f32 v235, v235, v219, v219\nv_mfma_f32_16x16x32_bf16 a[240:243], v[180:183], a[124:127], a[240:243]\nG 4\nv_exp_f32 v237, v237\nv_mfma_f32_16x16x32_bf16 a[244:247], v[160:163], a[108:111], a[244:247]\n"
"R 176,191,16384\nv_fma_f32 v236, v236, v219, v219\nv_mfma_f32_16x16x32_bf16 a[244:247], v[164:167], a[124:127], a[244:247]\nbuffer_load_dwordx4 v221, s[16:19], s63 offen offset:1024 lds\nv_mfma_f32_16x16x32_bf16 a[228:231], v[160:163], a[104:107], a[228:231]\nR 178,191,20480\n"
"s_add_u32 s63, s58, 720896\nv_mfma_f32_16x16x32_bf16 a[228:231], v[164:167], a[120:123], a[228:231]\ns_add_u32 m0, s50, 32768\nv_rcp_f32 v234, v234\nM 20,160,100\nR 180,191,24576\n"
"v_fma_f32 v237, v237, v219, v219\nM 20,164,116\nbuffer_load_dwordx4 v220, s[16:19], s63 offen lds\nM 4,160,96\nR 182,191,28672\nv_mul_f32 v234, v60, v234\n"
"M 4,164,112\nG 4\nv_rcp_f32 v235, v235\nM 8,168,96\nR 160,188,32768\nv_mul_f32 v235, v61, v235\n"
"M 8,172,112\nbuffer_load_dwordx4 v221, s[16:19], s63 offen offset:1024 lds\nM 24,168,100\nR 162,188,36864\ns_add_u32 s63, s58, 393216\nM 24,172,116\n"
"s_add_u32 m0, s50, 40960\nv_rcp_f32 v236, v236\nv_mfma_f32_16x16x32_bf16 a[232:235], v[168:171], a[104:107], a[232:235]\nR 164,188,40960\nv_mul_f32 v236, v62, v236\nv_mfma_f32_16x16x32_bf16 a[232:235], v[172:175], a[120:123], a[232:235]\n"
"buffer_load_dwordx4 v220, s[16:19], s63 offen lds\nv_mfma_f32_16x16x32_bf16 a[248:251], v[168:171], a[108:111], a[248:251]\nR 166,188,45056\nv_cvt_pk_bf16_f32 v58, v234, v235\nv_mfma_f32_16x16x32_bf16 a[248:251], v[172:175], a[124:127], a[248:251]\nG 4\n"
"v_rcp_f32 v237, v237\nv_mfma_f32_16x16x32_bf16 a[252:255], v[176:179], a[108:111], a[252:255]\nR 168,189,32768\nv_mul_f32 v237, v63, v237\nv_mfma_f32_16x16x32_bf16 a[252:255], v[180:183], a[124:127], a[252:255]\nbuffer_load_dwordx4 v221, s[16:19], s63 offen offset:1024 lds\n"
"v_mfma_f32_16x16x32_bf16 a[236:239], v[176:179], a[104:107], a[236:239]\nR 170,189,36864\ns_add_u32 s63, s58, 458752\nv_mfma_f32_16x16x32_bf16 a[236:239], v[180:183], a[120:123], a[236:239]\ns_add_u32 m0, s50, 49152\nv_cvt_pk_bf16_f32 v59, v236, v237\n"
"buffer_store_dwordx4 v[56:59], v252, s[20:23], s85 offen offset:1024 sc1\nM 28,176,100\nR 172,189,40960\nM 28,180,116\nbuffer_load_dwordx4 v220, s[16:19], s63 offen lds\nM 12,176,96\n"
"R 174,189,45056\nM 12,180,112\ns_waitcnt vmcnt(10)\ns_barrier\nG 4\nv_mfma_f32_16x16x32_bf16 v[32:35], v[160:163], a[0:3], 0\n"
"R 176,190,32768\nM 32,164,16\nv_mfma_f32_16x16x32_bf16 v[48:51], v[160:163], a[4:7], 0\nR 178,190,36864\nM 48,164,20\nbuffer_load_dwordx4 v221, s[16:19], s63 offen offset:1024 lds\n"
"v_mfma_f32_16x16x32_bf16 v[64:67], v[160:163], a[8:11], 0\nR 180,190,40960\ns_add_u32 s63, s56, 393216\nM 64,164,24\ns_add_u32 m0, s50, 57344\nv_mfma_f32_16x16x32_bf16 v[80:83], v[160:163], a[12:15], 0\n"
"R 182,190,45056\nM 80,164,28\nG 4\nv_mfma_f32_16x16x32_bf16 v[84:87], v[168:171], a[12:15], 0\nR 160,191,32768\nM 84,172,28\n"
"buffer_load_dwordx4 v220, s[16:19], s63 offen lds\nv_mfma_f32_16x16x32_bf16 v[68:71], v[168:171], a[8:11], 0\nR 162,191,36864\nM 68,172,24\nv_mfma_f32_16x16x32_bf16 v[52:55], v[168:171], a[4:7], 0\nR 164,191,40960\n"
"M 52,172,20\nv_mfma_f32_16x16x32_bf16 v[36:39], v[168:171], a[0:3], 0\nR 166,191,45056\nM 36,172,16\nG 4\nv_mfma_f32_16x16x32_bf16 v[40:43], v[176:179], a[0:3], 0\n"
"R 168,188,49152\nM 40,180,16\nbuffer_load_dwordx4 v221, s[16:19], s63 offen offset:1024 lds\nv_mfma_f32_16x16x32_bf16 v[56:59], v[176:179], a[4:7], 0\nR 170,188,53248\nM 56,180,20\n"
"s_add_u32 m0, s50, 65536\nv_mfma_f32_16x16x32_bf16 v[72:75], v[176:179], a[8:11], 0\nR 172,184,0\nM 72,180,24\ns_add_u32 s63, s56, 458752\nv_mfma_f32_16x16x32_bf16 v[88:91], v[176:179], a[12:15], 0\n"
"R 174,184,4096\nM 88,180,28\nG 4\nv_mfma_f32_16x16x32_bf16 v[92:95], v[160:163], a[12:15], 0\nR 176,189,49152\nM 92,164,28\n"
"buffer_load_dwordx4 v220, s[16:19], s63 offen lds\nv_mfma_f32_16x16x32_bf16 v[76:79], v[160:163], a[8:11], 0\nR 178,189,53248\nM 76,164,24\nv_mfma_f32_16x16x32_bf16 v[60:63], v[160:163], a[4:7], 0\nR 180,185,0\n"
"M 60,164,20\nv_mfma_f32_16x16x32_bf16 v[44:47], v[160:163], a[0:3], 0\nR 182,185,4096\nM 44,164,16\nG 4\nM 32,168,32\n"
"R 160,190,49152\nM 32,172,48\nbuffer_load_dwordx4 v221, s[16:19], s63 offen offset:1024 lds\nM 48,168,36\nR 162,190,53248\nM 48,172,52\n"
"s_add_u32 m0, s50, 73728\nM 64,168,40\nR 164,186,0\nM 64,172,56\ns_add_u32 s63, s57, 393216\nM 80,168,44\n"
"R 166,186,4096\nM 80,172,60\nG 4\nM 84,176,44\nR 168,191,49152\nM 84,180,60\n"
"buffer_load_dwordx4 v220, s[16:19], s63 offen lds\nM 68,176,40\nR 170,191,53248\nv_mov_b32 v226, s46\nM 68,180,56\nv_mov_b32 v227, s47\n"
"M 52,176,36\nR 172,187,0\ns_add_u32 s61, s81, 845840\ns_lshl_b32 s62, s44, 5\ns_add_u32 s61, s61, s62\ns_waitcnt vmcnt(7)\n"
"s_mov_b64 exec, 1\nbuffer_atomic_swap_x2 v[226:227], off, s[20:23], s61 sc1\ns_mov_b64 exec, -1\nM 52,180,52\nM 36,176,32\nR 174,187,4096\n"
"M 36,180,48\nG 4\nM 40,160,32\nR 176,184,8192\nM 40,164,48\nbuffer_load_dwordx4 v221, s[16:19], s63 offen offset:1024 lds\n"
"M 56,160,36\nR 178,184,12288\ns_add_u32 s63, s57, 458752\nM 56,164,52\ns_add_u32 m0, s50, 81920\nM 72,160,40\n"
"R 180,184,16384\nM 72,164,56\nbuffer_load_dwordx4 v220, s[16:19], s63 offen lds\nM 88,160,44\nR 182,184,20480\nM 88,164,60\n"
"G 4\nM 92,168,44\nR 160,185,8192\nM 92,172,60\nbuffer_load_dwordx4 v221, s[16:19], s63 offen offset:1024 lds\nM 76,168,40\n"
"R 162,185,12288\ns_add_u32 s63, s56, 262144\nM 76,172,56\ns_add_u32 m0, s50, 90112\nM 60,168,36\nR 164,185,16384\n"
"M 60,172,52\nbuffer_load_dwordx4 v220, s[16:19], s63 offen lds\nM 44,168,32\nR 166,185,20480\nM 44,172,48\ns_waitcnt vmcnt(10)\n"
"s_barrier\nG 4\nM 32,176,64\nR 168,186,8192\nM 32,180,80\nM 48,176,68\n"
"R 170,186,12288\nM 48,180,84\nbuffer_load_dwordx4 v221, s[16:19], s63 offen offset:1024 lds\nM 64,176,72\nR 172,186,16384\ns_add_u32 s63, s56, 786432\n"
"M 64,180,88\ns_add_u32 m0, s50, 98304\nM 80,176,76\nR 174,186,20480\nM 80,180,92\nG 4\n"
"M 84,160,76\nR 176,187,8192\nM 84,164,92\nbuffer_load_dwordx4 v220, s[16:19], s63 offen lds\nM 68,160,72\nR 178,187,12288\n"
"M 68,164,88\nM 52,160,68\nR 180,187,16384\nv_mov_b32 v248, 0\nM 52,164,84\nv_mov_b32 v249, 0\n"
"M 36,160,64\nR 182,187,20480\ns_add_u32 s61, s82, 845840\ns_mov_b64 exec, 0xf\nbuffer_atomic_or_x2 v[248:249], v250, s[20:23], s61 offen sc0 sc1\ns_mov_b64 exec, -1\n"
"M 36,164,80\nG 4\nM 40,168,64\nR 160,184,24576\nM 40,172,80\nbuffer_load_dwordx4 v221, s[16:19], s63 offen offset:1024 lds\n"
"M 56,168,68\nR 162,184,28672\nM 56,172,84\ns_add_u32 m0, s50, 106496\nM 72,168,72\nR 164,184,32768\n"
"M 72,172,88\ns_add_u32 s63, s56, 327680\nM 88,168,76\nR 166,184,36864\nM 88,172,92\nG 4\n"
"M 92,176,76\nR 168,185,24576\nM 92,180,92\nbuffer_load_dwordx4 v220, s[16:19], s63 offen lds\nM 76,176,72\nR 170,185,28672\n"
"M 76,180,88\nM 60,176,68\nR 172,185,32768\nM 60,180,84\nM 44,176,64\nR 174,185,36864\n"
"M 44,180,80\nG 4\nM 32,160,96\nR 176,186,24576\nM 32,164,112\nbuffer_load_dwordx4 v221, s[16:19], s63 offen offset:1024 lds\n"
"M 48,160,100\nR 178,186,28672\nM 48,164,116\ns_add_u32 m0, s50, 114688\nM 64,160,104\nR 180,186,32768\n"
"M 64,164,120\ns_add_u32 s63, s56, 851968\nM 80,160,108\nR 182,186,36864\nM 80,164,124\nG 4\n"
"M 84,168,108\nR 160,187,24576\nM 84,172,124\nbuffer_load_dwordx4 v220, s[16:19], s63 offen lds\nM 68,168,104\nR 162,187,28672\n"
"M 68,172,120\nM 52,168,100\nR 164,187,32768\nM 52,172,116\nbuffer_load_dwordx4 v221, s[16:19], s63 offen offset:1024 lds\nM 36,168,96\n"
"R 166,187,36864\ns_add_u32 m0, s50, 0\nM 36,172,112\nG 4\nM 40,176,96\nR 168,184,40960\n"
"s_add_u32 s63, s57, 262144\nM 40,180,112\nbuffer_load_dwordx4 v220, s[16:19], s63 offen lds\nM 56,176,100\nR 170,184,45056\nM 56,180,116\n"
"M 72,176,104\nR 172,184,49152\nM 72,180,120\nbuffer_load_dwordx4 v221, s[16:19], s63 offen offset:1024 lds\nM 88,176,108\nR 174,184,53248\n"
"s_waitcnt vmcnt(16)\ns_barrier\nM 88,180,124\nG 4\nM 92,160,108\nR 176,185,40960\n"
"M 92,164,124\ns_add_u32 s64, s68, 37752832\nM 76,160,104\nR 178,185,45056\nM 76,164,120\nM 60,160,100\n"
"R 180,185,49152\ns_waitcnt vmcnt(7)\nv_cmp_eq_u64 s[86:87], s[46:47], v[248:249]\nM 60,164,116\ns_add_u32 s65, s68, 37867520\nM 44,160,96\n"
"R 182,185,53248\ns_and_b32 s62, s86, 0xf\ns_cmp_eq_u32 s62, 0xf\ns_cbranch_scc0 Lq927\nLq928:\nM 44,164,112\n"
"G 4\nM 32,168,128\nR 160,186,40960\nM 32,172,144\nbuffer_load_dwordx4 a[160:163], v252, s[20:23], s64 offen sc1\nM 48,168,132\n"
"R 162,186,45056\nM 48,172,148\ns_add_u32 s66, s69, 37752832\nM 64,168,136\nR 164,186,49152\nM 64,172,152\n"
"buffer_load_dwordx4 a[164:167], v252, s[20:23], s65 offen sc1\nM 80,168,140\nR 166,186,53248\nM 80,172,156\nG 4\nM 84,176,140\n"
"R 168,187,40960\nM 84,180,156\ns_add_u32 s67, s69, 37867520\nM 68,176,136\nR 170,187,45056\nM 68,180,152\n"
"buffer_load_dwordx4 a[168:171], v252, s[20:23], s66 offen sc1\nM 52,176,132\nR 172,187,49152\nM 52,180,148\ns_add_u32 s64, s68, 37760000\nM 36,176,128\n"
"R 174,187,53248\nM 36,180,144\nG 4\nM 40,160,128\nR 176,184,57344\nM 40,164,144\n"
"buffer_load_dwordx4 a[172:175], v252, s[20:23], s67 offen sc1\nM 56,160,132\nR 178,184,61440\nM 56,164,148\ns_add_u32 s65, s68, 37874688\nM 72,160,136\n"
"R 180,188,0\nM 72,164,152\nbuffer_load_dwordx4 a[176:179], v252, s[20:23], s64 offen sc1\nM 88,160,140\nR 182,188,4096\ns_add_u32 m0, s50, 8192\n"
"M 88,164,156\nG 4\nM 92,168,140\nR 160,185,57344\ns_add_u32 s63, s57, 786432\nM 92,172,156\n"
"s_add_u32 s66, s69, 37760000\nM 76,168,136\nR 162,185,61440\nM 76,172,152\nbuffer_load_dwordx4 a[180:183], v252, s[20:23], s65 offen sc1\nbuffer_load_dwordx4 v220, s[16:19], s63 offen lds\n"
"M 60,168,132\nR 164,189,0\nM 60,172,148\ns_add_u32 s67, s69, 37874688\nM 44,168,128\nR 166,189,4096\n"
"M 44,172,144\nbuffer_load_dwordx4 v221, s[16:19], s63 offen offset:1024 lds\nG 4\nM 96,176,128\nR 168,186,57344\ns_add_u32 m0, s50, 16384\n"
"M 96,180,144\nbuffer_load_dwordx4 a[184:187], v252, s[20:23], s66 offen sc1\nM 112,176,132\nR 170,186,61440\ns_add_u32 s63, s57, 327680\nM 112,180,148\n"
"s_add_u32 s64, s68, 37753856\nbuffer_load_dwordx4 v220, s[16:19], s63 offen lds\nM 128,176,136\nR 172,190,0\nM 128,180,152\nbuffer_load_dwordx4 a[188:191], v252, s[20:23], s67 offen sc1\n"
"M 144,176,140\nR 174,190,4096\nM 144,180,156\nbuffer_load_dwordx4 v221, s[16:19], s63 offen offset:1024 lds\nG 4\nM 148,160,140\n"
"R 176,187,57344\ns_waitcnt vmcnt(18)\ns_barrier\nM 148,164,156\ns_add_u32 s65, s68, 37868544\ns_add_u32 s63, s57, 851968\n"
"M 132,160,136\nR 178,187,61440\ns_add_u32 m0, s50, 24576\nM 132,164,152\nbuffer_load_dwordx4 a[192:195], v252, s[20:23], s64 offen sc1\nM 116,160,132\n"
"R 180,191,0\nbuffer_load_dwordx4 v220, s[16:19], s63 offen lds\nM 116,164,148\ns_add_u32 s66, s69, 37753856\nM 100,160,128\nR 182,191,4096\n"
"M 100,164,144\nG 4\nM 104,168,128\nR 160,188,8192\nbuffer_load_dwordx4 v221, s[16:19], s63 offen offset:1024 lds\nM 104,172,144\n"
"buffer_load_dwordx4 a[196:199], v252, s[20:23], s65 offen sc1\nM 120,168,132\nLpg31:\ns_cbranch_execz LretO1n\nR 162,188,12288\nM 120,172,148\n"
"s_add_u32 s67, s69, 37868544\nM 136,168,136\nR 164,188,16384\nM 136,172,152\nbuffer_load_dwordx4 a[200:203], v252, s[20:23], s66 offen sc1\nM 152,168,140\n"
"R 166,188,20480\nM 152,172,156\nG 4\nM 156,176,140\nR 168,189,8192\nM 156,180,156\n"
"buffer_load_dwordx4 a[204:207], v252, s[20:23], s67 offen sc1\nM 140,176,136\nR 170,189,12288\nM 140,180,152\ns_add_u32 s64, s68, 37761024\nM 124,176,132\n"
"R 172,189,16384\nM 124,180,148\ns_add_u32 s65, s68, 37875712\nM 108,176,128\nR 174,189,20480\nM 108,180,144\n"
"G 4\nM 0,160,128\nR 176,190,8192\nM 0,164,144\nbuffer_load_dwordx4 a[208:211], v252, s[20:23], s64 offen sc1\nM 16,160,132\n"
"R 178,190,12288\nM 16,164,148\ns_add_u32 s66, s69, 37761024\nv_mfma_f32_16x16x32_bf16 a[224:227], v[160:163], a[136:139], a[224:227]\nR 180,190,16384\nv_mfma_f32_16x16x32_bf16 a[224:227], v[164:167], a[152:155], a[224:227]\n"
"buffer_load_dwordx4 a[212:215], v252, s[20:23], s65 offen sc1\nv_mfma_f32_16x16x32_bf16 a[240:243], v[160:163], a[140:143], a[240:243]\nR 182,190,20480\nv_mfma_f32_16x16x32_bf16 a[240:243], v[164:167], a[156:159], a[240:243]\nG 4\nv_mfma_f32_16x16x32_bf16 a[244:247], v[168:171], a[140:143], a[244:247]\n"
"R 160,191,8192\nv_mfma_f32_16x16x32_bf16 a[244:247], v[172:175], a[156:159], a[244:247]\nbuffer_load_dwordx4 a[216:219], v252, s[20:23], s66 offen sc1\nv_mfma_f32_16x16x32_bf16 a[228:231], v[168:171], a[136:139], a[228:231]\nR 162,191,12288\nv_mfma_f32_16x16x32_bf16 a[228:231], v[172:175], a[152:155], a[228:231]\n"
"s_add_u32 s67, s69, 37875712\nM 20,168,132\nR 164,191,16384\nM 20,172,148\nM 4,168,128\nR 166,191,20480\n"
"M 4,172,144\nG 4\nM 8,176,128\nR 168,188,24576\nM 8,180,144\nbuffer_load_dwordx4 a[220:223], v252, s[20:23], s67 offen sc1\n"
"M 24,176,132\nR 170,188,28672\nM 24,180,148\ns_add_u32 m0, s50, 32768\nv_mfma_f32_16x16x32_bf16 a[232:235], v[176:179], a[136:139], a[232:235]\nR 172,188,32768\n"
"v_mfma_f32_16x16x32_bf16 a[232:235], v[180:183], a[152:155], a[232:235]\ns_add_u32 s63, s58, 262144\nv_mfma_f32_16x16x32_bf16 a[248:251], v[176:179], a[140:143], a[248:251]\nR 174,188,36864\nv_mfma_f32_16x16x32_bf16 a[248:251], v[180:183], a[156:159], a[248:251]\nG 4\n"
"v_mfma_f32_16x16x32_bf16 a[252:255], v[160:163], a[140:143], a[252:255]\nR 176,189,24576\nv_mfma_f32_16x16x32_bf16 a[252:255], v[164:167], a[156:159], a[252:255]\nbuffer_load_dwordx4 v220, s[16:19], s63 offen lds\nv_mfma_f32_16x16x32_bf16 a[236:239], v[160:163], a[136:139], a[236:239]\nR 178,189,28672\n"
"v_mfma_f32_16x16x32_bf16 a[236:239], v[164:167], a[152:155], a[236:239]\nM 28,160,132\nR 180,189,32768\nM 28,164,148\nbuffer_load_dwordx4 v221, s[16:19], s63 offen offset:1024 lds\nM 12,160,128\n"
"R 182,189,36864\nM 12,164,144\ns_waitcnt vmcnt(9)\ns_barrier\nG 4\nM 96,168,160\n"
"R 160,190,24576\nM 96,172,176\ns_add_u32 m0, s50, 40960\nM 112,168,164\nR 162,190,28672\nM 112,172,180\n"
"s_add_u32 s63, s58, 786432\nM 128,168,168\nR 164,190,32768\nv_lshrrev_b32 v230, 8, v252\nM 128,172,184\nbuffer_load_dwordx4 v220, s[16:19], s63 offen lds\n"
"M 144,168,172\nR 166,190,36864\nv_lshlrev_b32 v230, 3, v230\nM 144,172,188\nG 4\nM 148,176,172\n"
"R 168,191,24576\nM 148,180,188\nbuffer_load_dwordx2 v[232:233], v230, s[36:39], s53 offen offset:0\nM 132,176,168\nR 170,191,28672\nM 132,180,184\n"
"buffer_load_dwordx2 v[234:235], v230, s[36:39], s53 offen offset:32\nM 116,176,164\nR 172,191,32768\nM 116,180,180\nbuffer_load_dwordx4 v221, s[16:19], s63 offen offset:1024 lds\nM 100,176,160\n"
"R 174,191,36864\nM 100,180,176\nG 4\nM 104,160,160\nR 176,188,40960\nM 104,164,176\n"
"s_add_u32 m0, s50, 49152\nM 120,160,164\nR 178,188,45056\nM 120,164,180\ns_add_u32 s63, s58, 327680\nM 136,160,168\n"
"R 180,188,49152\nbuffer_load_dwordx2 v[236:237], v230, s[36:39], s53 offen offset:64\nM 136,164,184\nbuffer_load_dwordx4 v220, s[16:19], s63 offen lds\nM 152,160,172\nR 182,188,53248\n"
"M 152,164,188\nbuffer_load_dwordx2 v[238:239], v230, s[36:39], s53 offen offset:96\nG 4\nM 156,168,172\nR 160,189,40960\nv_bfe_u32 v242, v252, 4, 4\n"
"M 156,172,188\nv_lshrrev_b32 v243, 8, v252\nv_lshlrev_b32 v243, 2, v243\nM 140,168,168\nR 162,189,45056\nv_sub_u32 v242, v242, v243\n"
"M 140,172,184\nv_lshlrev_b32 v243, 4, v242\ns_mov_b64 s[60:61], 0x3f80\nv_lshlrev_b64 v[208:209], v243, s[60:61]\nM 124,168,164\nR 164,189,49152\n"
"M 124,172,180\nbuffer_load_dwordx4 v221, s[16:19], s63 offen offset:1024 lds\nM 108,168,160\nR 166,189,53248\nM 108,172,176\nG 4\n"
"M 96,176,192\nR 168,190,40960\ns_waitcnt vmcnt(13)\nM 96,180,208\ns_add_u32 m0, s50, 57344\nM 112,176,196\n"
"R 170,190,45056\ns_waitcnt vmcnt(12)\nM 112,180,212\ns_add_u32 s63, s58, 851968\nM 128,176,200\nR 172,190,49152\n"
"s_waitcnt vmcnt(11)\nM 128,180,216\nbuffer_load_dwordx4 v220, s[16:19], s63 offen lds\nM 144,176,204\nR 174,190,53248\ns_waitcnt vmcnt(11)\n"
"M 144,180,220\nG 4\nM 148,160,204\nR 176,191,40960\nM 148,164,220\nv_cmp_gt_u32 s[60:61], 4, v242\n"
"s_nop 1\nv_cndmask_b32 v212, 0, v208, s[60:61]\nv_cndmask_b32 v208, 0, v208, s[60:61]\nv_cndmask_b32 v213, 0, v209, s[60:61]\nv_cndmask_b32 v209, 0, v209, s[60:61]\nM 132,160,200\n"
"R 178,191,45056\nM 132,164,216\nM 116,160,196\nR 180,191,49152\nM 116,164,212\nbuffer_load_dwordx4 v221, s[16:19], s63 offen offset:1024 lds\n"
"M 100,160,192\nR 182,191,53248\nM 100,164,208\nG 4\nM 104,168,192\nR 160,184,0\n"
"M 104,172,208\ns_add_u32 m0, s50, 65536\nM 120,168,196\nR 162,184,4096\nM 120,172,212\ns_add_u32 s63, s59, 0\n"
"M 136,168,200\nR 164,184,8192\nM 136,172,216\nbuffer_load_dwordx4 v220, s[16:19], s63 offen lds\nM 152,168,204\nR 166,184,12288\n"
"M 152,172,220\nG 4\nM 156,176,204\nR 168,185,0\nM 156,180,220\nM 140,176,200\n"
"R 170,185,4096\nM 140,180,216\nbuffer_load_dwordx4 v221, s[16:19], s63 offen offset:1024 lds\nM 124,176,196\nR 172,185,8192\nM 124,180,212\n"
"M 108,176,192\nR 174,185,12288\nM 108,180,208\nG 4\nM 0,160,160\nR 176,186,0\n"
"M 0,164,176\ns_mov_b32 m0, s51\nM 16,160,164\nR 178,186,4096\nv_cvt_pk_bf16_f32 v128, v128, v129\nv_cvt_pk_bf16_f32 v129, v130, v131\n"
"M 16,164,180\nds_write_addtid_b32 v128 offset:0\nv_cvt_pk_bf16_f32 v130, v132, v133\nv_cvt_pk_bf16_f32 v131, v134, v135\nv_mfma_f32_16x16x32_bf16 a[224:227], v[160:163], a[168:171], a[224:227]\nR 180,186,8192\n"
"v_cvt_pk_bf16_f32 v132, v136, v137\nv_mfma_f32_16x16x32_bf16 a[224:227], v[164:167], a[184:187], a[224:227]\nds_write_addtid_b32 v129 offset:256\nv_cvt_pk_bf16_f32 v133, v138, v139\nv_mfma_f32_16x16x32_bf16 a[240:243], v[160:163], a[172:175], a[240:243]\nR 182,186,12288\n"
"v_cvt_pk_bf16_f32 v134, v140, v141\nv_mfma_f32_16x16x32_bf16 a[240:243], v[164:167], a[188:191], a[240:243]\nds_write_addtid_b32 v130 offset:512\nG 7\nv_cvt_pk_bf16_f32 v135, v142, v143\nv_mfma_f32_16x16x32_bf16 a[244:247], v[168:171], a[172:175], a[244:247]\n"
"R 160,187,0\nv_cvt_pk_bf16_f32 v136, v144, v145\nv_mfma_f32_16x16x32_bf16 a[244:247], v[172:175], a[188:191], a[244:247]\nds_write_addtid_b32 v131 offset:768\nv_cvt_pk_bf16_f32 v137, v146, v147\nv_mfma_f32_16x16x32_bf16 a[228:231], v[168:171], a[168:171], a[228:231]\n"
"R 162,187,4096\nv_cvt_pk_bf16_f32 v138, v148, v149\nv_mfma_f32_16x16x32_bf16 a[228:231], v[172:175], a[184:187], a[228:231]\nds_write_addtid_b32 v132 offset:1024\nv_cvt_pk_bf16_f32 v139, v150, v151\nM 20,168,164\n"
"R 164,187,8192\nv_cvt_pk_bf16_f32 v140, v152, v153\nM 20,172,180\nds_write_addtid_b32 v133 offset:1280\nv_cvt_pk_bf16_f32 v141, v154, v155\nM 4,168,160\n"
"R 166,187,12288\nv_cvt_pk_bf16_f32 v142, v156, v157\nM 4,172,176\nds_write_addtid_b32 v134 offset:1536\nG 9\nv_cvt_pk_bf16_f32 v143, v158, v159\n"
"M 8,176,160\nR 168,184,16384\nds_write_addtid_b32 v135 offset:1792\nM 8,180,176\nds_write_addtid_b32 v136 offset:2048\nds_write_addtid_b32 v137 offset:2304\n"
"M 24,176,164\nR 170,184,20480\nds_write_addtid_b32 v138 offset:2560\nM 24,180,180\nds_write_addtid_b32 v139 offset:2816\nds_write_addtid_b32 v140 offset:3072\n"
"v_mfma_f32_16x16x32_bf16 a[232:235], v[176:179], a[168:171], a[232:235]\nR 172,184,24576\nds_write_addtid_b32 v141 offset:3328\nv_mfma_f32_16x16x32_bf16 a[232:235], v[180:183], a[184:187], a[232:235]\nds_write_addtid_b32 v142 offset:3584\nds_write_addtid_b32 v143 offset:3840\n"
"v_mfma_f32_16x16x32_bf16 a[248:251], v[176:179], a[172:175], a[248:251]\nR 174,184,28672\ns_add_u32 m0, s50, 73728\nv_mfma_f32_16x16x32_bf16 a[248:251], v[180:183], a[188:191], a[248:251]\ns_add_u32 s63, s59, 524288\nG 14\n"
"v_mfma_f32_16x16x32_bf16 a[252:255], v[160:163], a[172:175], a[252:255]\nR 176,185,16384\ns_waitcnt vmcnt(8) lgkmcnt(2)\ns_barrier\nv_mfma_f32_16x16x32_bf16 a[252:255], v[164:167], a[188:191], a[252:255]\nbuffer_load_dwordx4 v220, s[16:19], s63 offen lds\n"
"v_mfma_f32_16x16x32_bf16 a[236:239], v[160:163], a[168:171], a[236:239]\nR 178,185,20480\nv_mfma_f32_16x16x32_bf16 a[236:239], v[164:167], a[184:187], a[236:239]\nds_read2st64_b32 v[128:129], v225 offset0:0 offset1:1\nM 28,160,164\nR 180,185,24576\n"
"M 28,164,180\nbuffer_load_dwordx4 v221, s[16:19], s63 offen offset:1024 lds\nM 12,160,160\nR 182,185,28672\ns_add_u32 m0, s50, 81920\nM 12,164,176\n"
"ds_read2st64_b32 v[130:131], v225 offset0:2 offset1:3\nG 6\nM 0,168,192\nR 160,186,16384\ns_add_u32 s63, s59, 65536\nM 0,172,208\n"
"buffer_load_dwordx4 v220, s[16:19], s63 offen lds\nds_read2st64_b32 v[132:133], v225 offset0:4 offset1:5\nM 16,168,196\nR 162,186,20480\nds_read2st64_b32 v[134:135], v225 offset0:6 offset1:7\nM 16,172,212\n"
"ds_read2st64_b32 v[136:137], v225 offset0:8 offset1:9\nv_mfma_f32_16x16x32_bf16 a[224:227], v[168:171], a[200:203], a[224:227]\nR 164,186,24576\nv_mfma_f32_16x16x32_bf16 a[224:227], v[172:175], a[216:219], a[224:227]\nbuffer_load_dwordx4 v221, s[16:19], s63 offen offset:1024 lds\nG 6\n"
"v_mfma_f32_16x16x32_bf16 v[96:99], v[208:211], v[128:131], v[96:99]\nds_read2st64_b32 v[138:139], v225 offset0:10 offset1:11\nv_mfma_f32_16x16x32_bf16 v[100:103], v[210:213], v[128:131], v[100:103]\nds_read2st64_b32 v[140:141], v225 offset0:12 offset1:13\nds_read2st64_b32 v[142:143], v225 offset0:14 offset1:15\nv_mfma_f32_16x16x32_bf16 a[240:243], v[168:171], a[204:207], a[240:243]\n"
"R 166,186,28672\ns_add_u32 m0, s50, 90112\nv_mfma_f32_16x16x32_bf16 a[240:243], v[172:175], a[220:223], a[240:243]\nv_mfma_f32_16x16x32_bf16 a[244:247], v[176:179], a[204:207], a[244:247]\nR 168,187,16384\ns_add_u32 s63, s59, 589824\n"
"G 7\nv_mfma_f32_16x16x32_bf16 v[104:107], v[208:211], v[132:135], v[104:107]\nv_mfma_f32_16x16x32_bf16 v[108:111], v[210:213], v[132:135], v[108:111]\nv_mfma_f32_16x16x32_bf16 a[244:247], v[180:183], a[220:223], a[244:247]\nbuffer_load_dwordx4 v220, s[16:19], s63 offen lds\nv_mfma_f32_16x16x32_bf16 a[228:231], v[176:179], a[200:203], a[228:231]\n"
"R 170,187,20480\nv_mfma_f32_16x16x32_bf16 a[228:231], v[180:183], a[216:219], a[228:231]\nG 5\nv_mfma_f32_16x16x32_bf16 v[112:115], v[208:211], v[136:139], v[112:115]\nv_mfma_f32_16x16x32_bf16 v[116:119], v[210:213], v[136:139], v[116:119]\nG 3\n"
"v_mfma_f32_16x16x32_bf16 v[120:123], v[208:211], v[140:143], v[120:123]\nv_mfma_f32_16x16x32_bf16 v[124:127], v[210:213], v[140:143], v[124:127]\nM 20,176,196\nR 172,187,24576\nM 20,180,212\nbuffer_load_dwordx4 v221, s[16:19], s63 offen offset:1024 lds\n"
"M 4,176,192\nR 174,187,28672\ns_add_u32 s63, s59, 131072\nM 4,180,208\nG 4\nM 8,160,192\n"
"R 176,184,32768\nM 8,164,208\ns_add_u32 m0, s50, 98304\nM 24,160,196\nR 178,184,36864\nM 24,164,212\n"
"buffer_load_dwordx4 v220, s[16:19], s63 offen lds\nv_mfma_f32_16x16x32_bf16 a[232:235], v[160:163], a[200:203], a[232:235]\nR 180,184,40960\nv_cvt_pk_bf16_f32 v120, v120, v121\nv_cvt_pk_bf16_f32 v112, v112, v113\nv_cvt_pk_bf16_f32 v121, v122, v123\n"
"v_mfma_f32_16x16x32_bf16 a[232:235], v[164:167], a[216:219], a[232:235]\nv_cvt_pk_bf16_f32 v122, v124, v125\nv_cvt_pk_bf16_f32 v123, v126, v127\nv_cvt_pk_bf16_f32 v96, v96, v97\nv_cvt_pk_bf16_f32 v113, v114, v115\nv_mfma_f32_16x16x32_bf16 a[248:251], v[160:163], a[204:207], a[248:251]\n"
"R 182,184,45056\nv_cvt_pk_bf16_f32 v114, v116, v117\nv_cvt_pk_bf16_f32 v97, v98, v99\nv_mfma_f32_16x16x32_bf16 a[248:251], v[164:167], a[220:223], a[248:251]\nG 4\nv_cvt_pk_bf16_f32 v98, v100, v101\n"
"v_cvt_pk_bf16_f32 v99, v102, v103\nv_mfma_f32_16x16x32_bf16 a[252:255], v[168:171], a[204:207], a[252:255]\nR 160,185,32768\ns_bfe_u32 s100, s56, 0x20007\ns_mul_i32 s100, s100, 49152\ns_add_u32 s100, s100, s72\n"
"s_add_u32 s100, s100, 65011712\nv_cvt_pk_bf16_f32 v115, v118, v119\nv_mfma_f32_16x16x32_bf16 a[252:255], v[172:175], a[220:223], a[252:255]\nbuffer_store_dwordx4 v[96:99], v252, s[20:23], s100 offen offset:0 sc1\nv_mfma_f32_16x16x32_bf16 a[236:239], v[168:171], a[200:203], a[236:239]\nR 162,185,36864\n"
"v_cvt_pk_bf16_f32 v104, v104, v105\nv_mfma_f32_16x16x32_bf16 a[236:239], v[172:175], a[216:219], a[236:239]\nv_cvt_pk_bf16_f32 v105, v106, v107\nv_cvt_pk_bf16_f32 v106, v108, v109\nM 28,168,196\nR 164,185,40960\n"
"v_cvt_pk_bf16_f32 v107, v110, v111\nM 28,172,212\nbuffer_load_dwordx4 v221, s[16:19], s63 offen offset:1024 lds\nM 12,168,192\nR 166,185,45056\nM 12,172,208\n"
"buffer_store_dwordx4 v[104:107], v252, s[20:23], s100 offen offset:1024 sc1\ns_waitcnt vmcnt(12)\ns_barrier\nG 4\nM 32,176,160\nR 168,186,32768\n"
"M 32,180,176\nv_xor_b32 v240, 0x2000, v225\nM 48,176,164\nR 170,186,36864\nds_write2st64_b32 v240, a224, a225 offset0:0 offset1:1\nM 48,180,180\n"
"buffer_store_dwordx4 v[112:115], v252, s[20:23], s100 offen offset:2048 sc1\nds_write2st64_b32 v240, a226, a227 offset0:2 offset1:3\nM 64,176,168\nR 172,186,40960\nds_write2st64_b32 v240, a228, a229 offset0:4 offset1:5\nM 64,180,184\n"
"ds_write2st64_b32 v240, a230, a231 offset0:6 offset1:7\nM 80,176,172\nR 174,186,45056\nds_write2st64_b32 v240, a232, a233 offset0:8 offset1:9\nM 80,180,188\nds_write2st64_b32 v240, a234, a235 offset0:10 offset1:11\n"
"G 10\nM 84,160,172\nR 176,187,32768\ns_add_u32 s63, s59, 655360\nds_write2st64_b32 v240, a236, a237 offset0:12 offset1:13\nM 84,164,188\n"
"buffer_store_dwordx4 v[120:123], v252, s[20:23], s100 offen offset:3072 sc1\nds_write2st64_b32 v240, a238, a239 offset0:14 offset1:15\nM 68,160,168\nR 178,187,36864\nds_write2st64_b32 v240, a240, a241 offset0:16 offset1:17\nM 68,164,184\n"
"s_add_u32 m0, s50, 106496\nds_write2st64_b32 v240, a242, a243 offset0:18 offset1:19\nM 52,160,164\nR 180,187,40960\nds_write2st64_b32 v240, a244, a245 offset0:20 offset1:21\nM 52,164,180\n"
"buffer_load_dwordx4 v220, s[16:19], s63 offen lds\nds_write2st64_b32 v240, a246, a247 offset0:22 offset1:23\nM 36,160,160\nR 182,187,45056\nds_write2st64_b32 v240, a248, a249 offset0:24 offset1:25\nM 36,164,176\n"
"ds_write2st64_b32 v240, a250, a251 offset0:26 offset1:27\nG 14\nM 40,168,160\nR 160,184,49152\nds_write2st64_b32 v240, a252, a253 offset0:28 offset1:29\nM 40,172,176\n"
"M 56,168,164\nR 162,184,53248\nds_write2st64_b32 v240, a254, a255 offset0:30 offset1:31\nM 56,172,180\nbuffer_load_dwordx4 v221, s[16:19], s63 offen offset:1024 lds\nM 72,168,168\n"
"R 164,184,57344\ns_add_u32 s63, s59, 196608\nM 72,172,184\ns_add_u32 m0, s50, 114688\nM 88,168,172\nR 166,184,61440\n"
"M 88,172,188\nG 8\nM 92,176,172\nR 168,185,49152\ns_waitcnt vmcnt(12) lgkmcnt(3)\ns_barrier\n"
"M 92,180,188\nbuffer_load_dwordx4 v220, s[16:19], s63 offen lds\nds_read2st64_b32 v[128:129], v225 offset0:0 offset1:1\nM 76,176,168\nR 170,185,53248\nds_read2st64_b32 v[130:131], v225 offset0:2 offset1:3\n"
"M 76,180,184\nds_read2st64_b32 v[132:133], v225 offset0:4 offset1:5\nds_read2st64_b32 v[134:135], v225 offset0:6 offset1:7\nM 60,176,164\nR 172,185,57344\nds_read2st64_b32 v[136:137], v225 offset0:8 offset1:9\n"
"M 60,180,180\nbuffer_load_dwordx4 v221, s[16:19], s63 offen offset:1024 lds\nds_read2st64_b32 v[138:139], v225 offset0:10 offset1:11\nM 44,176,160\nR 174,185,61440\ns_add_u32 m0, s50, 0\n"
"ds_read2st64_b32 v[140:141], v225 offset0:12 offset1:13\nM 44,180,176\nds_read2st64_b32 v[142:143], v225 offset0:14 offset1:15\nds_read2st64_b32 v[144:145], v225 offset0:16 offset1:17\nG 13\nM 32,160,192\n"
"R 176,186,49152\ns_add_u32 s63, s59, 720896\nds_read2st64_b32 v[146:147], v225 offset0:18 offset1:19\nM 32,164,208\nbuffer_load_dwordx4 v220, s[16:19], s63 offen lds\nds_read2st64_b32 v[148:149], v225 offset0:20 offset1:21\n"
"M 48,160,196\nR 178,186,53248\nds_read2st64_b32 v[150:151], v225 offset0:22 offset1:23\nG 15\nv_add_f32 v0, v0, v128\nM 48,164,212\n"
"ds_read2st64_b32 v[152:153], v225 offset0:24 offset1:25\nds_read2st64_b32 v[154:155], v225 offset0:26 offset1:27\nLpg32:\ns_cbranch_execz LretW1z\nv_add_f32 v1, v1, v129\nM 64,160,200\n"
"R 180,186,57344\nds_read2st64_b32 v[156:157], v225 offset0:28 offset1:29\nG 15\nv_add_f32 v2, v2, v130\nM 64,164,216\nbuffer_load_dwordx4 v221, s[16:19], s63 offen offset:1024 lds\n"
"ds_read2st64_b32 v[158:159], v225 offset0:30 offset1:31\nM 80,160,204\nR 182,186,61440\ns_add_u32 m0, s50, 8192\nv_add_f32 v3, v3, v131\nM 80,164,220\n"
"G 14\nv_add_f32 v4, v4, v132\nv_add_f32 v5, v5, v133\nv_add_f32 v6, v6, v134\nM 84,168,204\nR 160,187,49152\n"
"s_add_u32 s63, s59, 393216\nv_add_f32 v7, v7, v135\nM 84,172,220\nbuffer_load_dwordx4 v220, s[16:19], s63 offen lds\nv_add_f32 v8, v8, v136\nM 68,168,200\n"
"R 162,187,53248\nv_add_f32 v9, v9, v137\nv_add_f32 v10, v10, v138\nM 68,172,216\nv_add_f32 v11, v11, v139\nG 15\n"
"v_add_f32 v12, v12, v140\nv_add_f32 v13, v13, v141\nM 52,168,196\nR 164,187,57344\nG 15\nv_add_f32 v14, v14, v142\n"
"v_add_f32 v15, v15, v143\nM 52,172,212\nbuffer_load_dwordx4 v221, s[16:19], s63 offen offset:1024 lds\nG 14\nv_add_f32 v16, v16, v144\nM 36,168,192\n"
"R 166,187,61440\ns_add_u32 s63, s59, 458752\nv_add_f32 v17, v17, v145\nM 36,172,208\nG 4\nv_add_f32 v18, v18, v146\n"
"v_add_f32 v19, v19, v147\nv_add_f32 v20, v20, v148\nM 40,176,192\nR 168,188,0\nv_add_f32 v21, v21, v149\nv_add_f32 v22, v22, v150\n"
"M 40,180,208\ns_add_u32 m0, s50, 16384\nv_add_f32 v23, v23, v151\nv_add_f32 v24, v24, v152\nv_add_f32 v25, v25, v153\nM 56,176,196\n"
"R 170,188,4096\nv_add_f32 v26, v26, v154\nv_add_f32 v27, v27, v155\nM 56,180,212\nbuffer_load_dwordx4 v220, s[16:19], s63 offen lds\nv_add_f32 v28, v28, v156\n"
"v_add_f32 v29, v29, v157\nv_add_f32 v30, v30, v158\nv_add_f32 v31, v31, v159\nM 72,176,200\nR 172,188,8192\nv_cvt_pk_bf16_f32 v24, v24, v25\n"
"v_cvt_pk_bf16_f32 v25, v26, v27\nM 72,180,216\nv_cvt_pk_bf16_f32 v26, v28, v29\nv_cvt_pk_bf16_f32 v27, v30, v31\nv_cvt_pk_bf16_f32 v0, v0, v1\nv_cvt_pk_bf16_f32 v16, v16, v17\n"
"M 88,176,204\nR 174,188,12288\nv_cvt_pk_bf16_f32 v1, v2, v3\nM 88,180,220\nG 4\nv_cvt_pk_bf16_f32 v2, v4, v5\n"
"v_cvt_pk_bf16_f32 v3, v6, v7\nM 92,160,204\nR 176,189,0\ns_bfe_u32 s100, s57, 0x20007\ns_mul_i32 s100, s100, 49152\ns_add_u32 s100, s100, s72\n"
"s_add_u32 s100, s100, 65028096\nv_cvt_pk_bf16_f32 v17, v18, v19\nM 92,164,220\nbuffer_store_dwordx4 v[0:3], v252, s[20:23], s100 offen offset:0 sc1\nM 76,160,200\nR 178,189,4096\n"
"v_cvt_pk_bf16_f32 v8, v8, v9\nM 76,164,216\nv_cvt_pk_bf16_f32 v9, v10, v11\nv_cvt_pk_bf16_f32 v10, v12, v13\nM 60,160,196\nR 180,189,8192\n"
"v_cvt_pk_bf16_f32 v11, v14, v15\nM 60,164,212\nbuffer_load_dwordx4 v221, s[16:19], s63 offen offset:1024 lds\nM 44,160,192\nR 182,189,12288\nv_cvt_pk_bf16_f32 v18, v20, v21\n"
"M 44,164,208\nbuffer_store_dwordx4 v[8:11], v252, s[20:23], s100 offen offset:1024 sc1\ns_waitcnt vmcnt(18)\ns_barrier\nG 4\nv_mfma_f32_16x16x32_bf16 v[96:99], v[168:171], a[0:3], 0\n"
"R 160,190,0\nv_cvt_pk_bf16_f32 v19, v22, v23\nM 96,172,16\ns_mov_b32 m0, s51\nv_mfma_f32_16x16x32_bf16 v[112:115], v[168:171], a[4:7], 0\nR 162,190,4096\n"
"v_cvt_pk_bf16_f32 v64, v64, v65\nv_cvt_pk_bf16_f32 v65, v66, v67\nM 112,172,20\nbuffer_store_dwordx4 v[16:19], v252, s[20:23], s100 offen offset:2048 sc1\nv_cvt_pk_bf16_f32 v66, v68, v69\nv_mfma_f32_16x16x32_bf16 v[128:131], v[168:171], a[8:11], 0\n"
"R 164,190,8192\nv_cvt_pk_bf16_f32 v67, v70, v71\nv_cvt_pk_bf16_f32 v68, v72, v73\nM 128,172,24\nds_write_addtid_b32 v64 offset:0\nv_cvt_pk_bf16_f32 v69, v74, v75\n"
"v_cvt_pk_bf16_f32 v70, v76, v77\nv_mfma_f32_16x16x32_bf16 v[144:147], v[168:171], a[12:15], 0\nR 166,190,12288\nv_cvt_pk_bf16_f32 v71, v78, v79\nv_cvt_pk_bf16_f32 v72, v80, v81\nM 144,172,28\n"
"ds_write_addtid_b32 v65 offset:256\nG 6\nv_cvt_pk_bf16_f32 v73, v82, v83\nv_mfma_f32_16x16x32_bf16 v[148:151], v[176:179], a[12:15], 0\nR 168,191,0\nv_cvt_pk_bf16_f32 v74, v84, v85\n"
"v_cvt_pk_bf16_f32 v75, v86, v87\nM 148,180,28\nbuffer_store_dwordx4 v[24:27], v252, s[20:23], s100 offen offset:3072 sc1\nv_mfma_f32_16x16x32_bf16 v[132:135], v[176:179], a[8:11], 0\nR 170,191,4096\nv_cvt_pk_bf16_f32 v76, v88, v89\n"
"M 132,180,24\nds_write_addtid_b32 v66 offset:512\nv_cvt_pk_bf16_f32 v77, v90, v91\nv_mfma_f32_16x16x32_bf16 v[116:119], v[176:179], a[4:7], 0\nR 172,191,8192\nv_cvt_pk_bf16_f32 v78, v92, v93\n"
"M 116,180,20\nds_write_addtid_b32 v67 offset:768\nv_cvt_pk_bf16_f32 v79, v94, v95\nv_mfma_f32_16x16x32_bf16 v[100:103], v[176:179], a[0:3], 0\nR 174,191,12288\nds_write_addtid_b32 v68 offset:1024\n"
"M 100,180,16\nds_write_addtid_b32 v69 offset:1280\nds_write_addtid_b32 v70 offset:1536\nG 10\nv_mfma_f32_16x16x32_bf16 v[104:107], v[160:163], a[0:3], 0\nR 176,188,16384\n"
"ds_write_addtid_b32 v71 offset:1792\nM 104,164,16\nds_write_addtid_b32 v72 offset:2048\nds_write_addtid_b32 v73 offset:2304\nv_mfma_f32_16x16x32_bf16 v[120:123], v[160:163], a[4:7], 0\nR 178,188,20480\n"
"ds_write_addtid_b32 v74 offset:2560\nM 120,164,20\nds_write_addtid_b32 v75 offset:2816\nds_write_addtid_b32 v76 offset:3072\nv_mfma_f32_16x16x32_bf16 v[136:139], v[160:163], a[8:11], 0\nR 180,188,24576\n"
"ds_write_addtid_b32 v77 offset:3328\nM 136,164,24\nds_write_addtid_b32 v78 offset:3584\nds_write_addtid_b32 v79 offset:3840\nv_mfma_f32_16x16x32_bf16 v[152:155], v[160:163], a[12:15], 0\nR 182,188,28672\n"
"s_add_u32 m0, s50, 24576\nM 152,164,28\ns_add_u32 s63, s59, 262144\nG 15\nv_mfma_f32_16x16x32_bf16 v[156:159], v[168:171], a[12:15], 0\nR 160,189,16384\n"
"s_waitcnt vmcnt(12) lgkmcnt(2)\ns_barrier\nM 156,172,28\nbuffer_load_dwordx4 v220, s[16:19], s63 offen lds\nv_mfma_f32_16x16x32_bf16 v[140:143], v[168:171], a[8:11], 0\nR 162,189,20480\n"
"M 140,172,24\nds_read2st64_b32 v[64:65], v225 offset0:0 offset1:1\nv_mfma_f32_16x16x32_bf16 v[124:127], v[168:171], a[4:7], 0\nR 164,189,24576\nM 124,172,20\nds_read2st64_b32 v[66:67], v225 offset0:2 offset1:3\n"
"v_mfma_f32_16x16x32_bf16 v[108:111], v[168:171], a[0:3], 0\nR 166,189,28672\nM 108,172,16\nds_read2st64_b32 v[68:69], v225 offset0:4 offset1:5\nG 7\nM 96,176,32\n"
"R 168,190,16384\nv_bfe_u32 v250, v252, 4, 2\nv_lshlrev_b32 v250, 3, v250\nM 96,180,48\nbuffer_load_dwordx4 v221, s[16:19], s63 offen offset:1024 lds\ns_lshl_b32 s60, s43, 5\n"
"v_add_u32 v250, s60, v250\nM 112,176,36\nR 170,190,20480\nds_read2st64_b32 v[70:71], v225 offset0:6 offset1:7\nM 112,180,52\ns_add_u32 m0, s50, 32768\n"
"ds_read2st64_b32 v[72:73], v225 offset0:8 offset1:9\nG 6\nv_mfma_f32_16x16x32_bf16 v[32:35], v[208:211], v[64:67], v[32:35]\nds_read2st64_b32 v[74:75], v225 offset0:10 offset1:11\nv_mfma_f32_16x16x32_bf16 v[36:39], v[210:213], v[64:67], v[36:39]\nds_read2st64_b32 v[76:77], v225 offset0:12 offset1:13\n"
"ds_read2st64_b32 v[78:79], v225 offset0:14 offset1:15\nM 128,176,40\nR 172,190,24576\nM 128,180,56\ns_add_u32 s63, s59, 786432\nM 144,176,44\n"
"R 174,190,28672\nM 144,180,60\nG 10\nM 148,160,44\nR 176,191,16384\nG 7\n"
"v_mfma_f32_16x16x32_bf16 v[40:43], v[208:211], v[68:71], v[40:43]\nv_mfma_f32_16x16x32_bf16 v[44:47], v[210:213], v[68:71], v[44:47]\nM 148,164,60\nbuffer_load_dwordx4 v220, s[16:19], s63 offen lds\nG 5\nv_mfma_f32_16x16x32_bf16 v[48:51], v[208:211], v[72:75], v[48:51]\n"
"v_mfma_f32_16x16x32_bf16 v[52:55], v[210:213], v[72:75], v[52:55]\nG 3\nv_mfma_f32_16x16x32_bf16 v[56:59], v[208:211], v[76:79], v[56:59]\nv_mfma_f32_16x16x32_bf16 v[60:63], v[210:213], v[76:79], v[60:63]\nM 132,160,40\nR 178,191,20480\n"
"M 132,164,56\nM 116,160,36\nR 180,191,24576\nM 116,164,52\nbuffer_load_dwordx4 v221, s[16:19], s63 offen offset:1024 lds\nM 100,160,32\n"
"R 182,191,28672\ns_add_u32 m0, s50, 40960\nM 100,164,48\nG 4\nM 104,168,32\nR 160,188,32768\n"
"s_add_u32 s63, s59, 327680\nM 104,172,48\nbuffer_load_dwordx4 v220, s[16:19], s63 offen lds\nM 120,168,36\nR 162,188,36864\nv_mov_b32 v226, s46\n"
"M 120,172,52\nv_mov_b32 v227, s47\nM 136,168,40\nR 164,188,40960\ns_bfe_u32 s62, s56, 0x20007\ns_lshl_b32 s62, s62, 5\n"
"s_add_u32 s61, s74, 1009664\ns_add_u32 s61, s61, s62\ns_waitcnt vmcnt(5)\ns_mov_b64 exec, 1\nbuffer_atomic_swap_x2 v[226:227], off, s[20:23], s61 sc1\ns_mov_b64 exec, -1\n"
"v_cvt_pk_bf16_f32 v32, v32, v33\nM 136,172,56\ns_bfe_u32 s62, s57, 0x20007\ns_lshl_b32 s62, s62, 5\ns_add_u32 s61, s74, 1009672\ns_add_u32 s61, s61, s62\n"
"s_mov_b64 exec, 1\nbuffer_atomic_swap_x2 v[226:227], off, s[20:23], s61 sc1\ns_mov_b64 exec, -1\nM 152,168,44\nR 166,188,45056\nv_cvt_pk_bf16_f32 v33, v34, v35\n"
"M 152,172,60\nG 4\nv_cvt_pk_bf16_f32 v34, v36, v37\nv_cvt_pk_bf16_f32 v35, v38, v39\nM 156,176,44\nR 168,189,32768\n"
"s_bfe_u32 s100, s58, 0x20007\ns_mul_i32 s100, s100, 49152\ns_add_u32 s100, s100, s72\ns_add_u32 s100, s100, 65044480\nv_cvt_pk_bf16_f32 v48, v48, v49\nM 156,180,60\n"
"buffer_store_dwordx4 v[32:35], v252, s[20:23], s100 offen offset:0 sc1\nM 140,176,40\nR 170,189,36864\nv_cvt_pk_bf16_f32 v40, v40, v41\nM 140,180,56\nv_cvt_pk_bf16_f32 v41, v42, v43\n"
"v_cvt_pk_bf16_f32 v42, v44, v45\nM 124,176,36\nR 172,189,40960\nv_cvt_pk_bf16_f32 v43, v46, v47\nM 124,180,52\nv_cvt_pk_bf16_f32 v49, v50, v51\n"
"v_cvt_pk_bf16_f32 v50, v52, v53\nM 108,176,32\nR 174,189,45056\nv_cvt_pk_bf16_f32 v51, v54, v55\nM 108,180,48\nbuffer_store_dwordx4 v[40:43], v252, s[20:23], s100 offen offset:1024 sc1\n"
"s_barrier\nG 4\nM 96,160,64\nR 176,190,32768\nv_cvt_pk_bf16_f32 v56, v56, v57\nM 96,164,80\n"
"v_cvt_pk_bf16_f32 v57, v58, v59\nv_cvt_pk_bf16_f32 v58, v60, v61\nM 112,160,68\nR 178,190,36864\nv_cvt_pk_bf16_f32 v59, v62, v63\nM 112,164,84\n"
"buffer_store_dwordx4 v[48:51], v252, s[20:23], s100 offen offset:2048 sc1\nM 128,160,72\nR 180,190,40960\nM 128,164,88\nM 144,160,76\nR 182,190,45056\n"
"M 144,164,92\nG 4\nM 148,168,76\nR 160,191,32768\nM 148,172,92\nbuffer_store_dwordx4 v[56:59], v252, s[20:23], s100 offen offset:3072 sc1\n"
"M 132,168,72\nR 162,191,36864\nM 132,172,88\nM 116,168,68\nR 164,191,40960\nM 116,172,84\n"
"M 100,168,64\nR 166,191,45056\nv_mov_b32 v248, 0\nM 100,172,80\nG 4\nv_mov_b32 v249, 0\n"
"M 104,176,64\nR 168,188,49152\ns_add_u32 s61, s74, 1009664\ns_mov_b64 exec, 3\nbuffer_atomic_or_x2 v[248:249], v250, s[20:23], s61 offen sc0 sc1\ns_mov_b64 exec, -1\n"
"M 104,180,80\nM 120,176,68\nR 170,188,53248\nv_mov_b32 v242, v208\nM 120,180,84\nbuffer_load_dwordx4 v221, s[16:19], s63 offen offset:1024 lds\n"
"M 136,176,72\nR 172,184,0\nv_mov_b32 v243, v209\nM 136,180,88\ns_add_u32 m0, s50, 49152\nv_mov_b32 v244, v208\n"
"v_mov_b32 v245, v209\nM 152,176,76\nR 174,184,4096\nv_mov_b32 v64, v192\nM 152,180,92\nG 4\n"
"v_mov_b32 v65, v193\nv_mov_b32 v70, v194\nM 156,160,76\nR 176,189,49152\nv_mov_b32 v71, v195\nM 156,164,92\n"
"s_add_u32 s63, s59, 851968\nv_mov_b32 v72, v196\nv_mov_b32 v73, v197\nM 140,160,72\nR 178,189,53248\nv_mov_b32 v78, v198\n"
"M 140,164,88\nbuffer_load_dwordx4 v220, s[16:19], s63 offen lds\nM 124,160,68\nR 180,185,0\nv_mov_b32 v79, v199\nM 124,164,84\n"
"v_mov_b32 v80, v200\nv_mov_b32 v81, v201\nM 108,160,64\nR 182,185,4096\nv_mov_b32 v86, v202\nM 108,164,80\n"
"G 4\nv_mov_b32 v87, v203\nv_mov_b32 v88, v204\nM 96,168,96\nR 160,190,49152\nv_mov_b32 v89, v205\n"
"M 96,172,112\nv_mov_b32 v94, v206\nv_mov_b32 v95, v207\nM 112,168,100\nR 162,190,53248\nv_mov_b32 v32, v232\n"
"M 112,172,116\nbuffer_load_dwordx4 v221, s[16:19], s63 offen offset:1024 lds\nM 128,168,104\nR 164,186,0\nv_mov_b32 v33, v233\nM 128,172,120\n"
"v_mov_b32 v38, v234\nv_mov_b32 v39, v235\nM 144,168,108\nR 166,186,4096\nv_mov_b32 v40, v236\nM 144,172,124\n"
"G 4\nv_mov_b32 v41, v237\nv_mov_b32 v46, v238\nM 148,176,108\nR 168,191,49152\nv_mov_b32 v47, v239\n"
"M 148,180,124\nv_mov_b32 v48, v232\nv_mov_b32 v49, v233\nM 132,176,104\nR 170,191,53248\nv_mov_b32 v54, v234\n"
"M 132,180,120\nv_mov_b32 v55, v235\nv_mov_b32 v56, v236\nM 116,176,100\nR 172,187,0\nv_mov_b32 v57, v237\n"
"M 116,180,116\nv_mov_b32 v62, v238\nv_mov_b32 v63, v239\nM 100,176,96\nR 174,187,4096\nM 100,180,112\n"
"G 4\nM 104,160,96\nR 176,184,8192\ns_waitcnt vmcnt(11)\ns_barrier\nM 104,164,112\n"
"M 120,160,100\nR 178,184,12288\nM 120,164,116\nM 136,160,104\nR 180,184,16384\nM 136,164,120\n"
"M 152,160,108\nR 182,184,20480\nM 152,164,124\nG 4\nM 156,168,108\nR 160,185,8192\n"
"M 156,172,124\nM 140,168,104\nR 162,185,12288\nM 140,172,120\nM 124,168,100\nR 164,185,16384\n"
"M 124,172,116\nM 108,168,96\nR 166,185,20480\nv_mov_b32 v226, s46\nM 108,172,112\nG 4\n"
"v_mov_b32 v227, s47\nM 96,176,128\nR 168,186,8192\ns_bfe_u32 s62, s58, 0x20007\ns_lshl_b32 s62, s62, 5\ns_add_u32 s61, s74, 1009680\n"
"s_add_u32 s61, s61, s62\ns_waitcnt vmcnt(4)\ns_mov_b64 exec, 1\nbuffer_atomic_swap_x2 v[226:227], off, s[20:23], s61 sc1\ns_mov_b64 exec, -1\nM 96,180,144\n"
"s_mul_i32 s64, s43, 49152\nM 112,176,132\nR 170,186,12288\nM 112,180,148\ns_add_u32 s64, s64, s72\nM 128,176,136\n"
"R 172,186,16384\ns_waitcnt vmcnt(4)\nv_cmp_eq_u64 s[86:87], s[46:47], v[248:249]\nM 128,180,152\ns_add_u32 s64, s64, 65011712\nM 144,176,140\n"
"R 174,186,20480\ns_and_b32 s62, s86, 3\ns_cmp_eq_u32 s62, 3\ns_cbranch_scc0 Lq967\nLq968:\nM 144,180,156\n"
"G 4\nM 148,160,140\nR 176,187,8192\nM 148,164,156\nbuffer_load_dwordx4 v[2:5], v252, s[20:23], s64 offen offset:0 sc1\nM 132,160,136\n"
"R 178,187,12288\nM 132,164,152\nbuffer_load_dwordx4 v[10:13], v252, s[20:23], s64 offen offset:1024 sc1\nM 116,160,132\nR 180,187,16384\nM 116,164,148\n"
"s_mul_i32 s65, s43, 49152\nM 100,160,128\nR 182,187,20480\nM 100,164,144\nbuffer_load_dwordx4 v[18:21], v252, s[20:23], s64 offen offset:2048 sc1\nG 4\n"
"M 104,168,128\nR 160,184,24576\nM 104,172,144\nbuffer_load_dwordx4 v[26:29], v252, s[20:23], s64 offen offset:3072 sc1\nM 120,168,132\nR 162,184,28672\n"
"M 120,172,148\ns_add_u32 s65, s65, s72\nM 136,168,136\nR 164,184,32768\ns_add_u32 s65, s65, 65028096\nM 136,172,152\n"
"buffer_load_dwordx4 v[66:69], v252, s[20:23], s65 offen offset:0 sc1\nM 152,168,140\nR 166,184,36864\nM 152,172,156\nG 4\nM 156,176,140\n"
"R 168,185,24576\ns_waitcnt vmcnt(6)\ns_barrier\nM 156,180,156\nbuffer_load_dwordx4 v[74:77], v252, s[20:23], s65 offen offset:1024 sc1\nM 140,176,136\n"
"R 170,185,28672\nv_mov_b32 v246, 0\nM 140,180,152\nbuffer_load_dwordx4 v[82:85], v252, s[20:23], s65 offen offset:2048 sc1\nv_mov_b32 v247, 0\nM 124,176,132\n"
"R 172,185,32768\ns_add_u32 s61, s74, 1009664\ns_mov_b64 exec, 4\nbuffer_atomic_or_x2 v[246:247], v250, s[20:23], s61 offen sc0 sc1\ns_mov_b64 exec, -1\nM 124,180,148\n"
"buffer_load_dwordx4 v[90:93], v252, s[20:23], s65 offen offset:3072 sc1\nM 108,176,128\nR 174,185,36864\nM 108,180,144\nG 4\nM 128,160,168\n"
"R 176,186,24576\nM 128,164,184\nR 178,186,28672\nM 144,160,172\nR 180,186,32768\nM 144,164,188\n"
"R 182,186,36864\nG 4\nLpg33:\ns_cbranch_execz LretW1z\nM 148,168,172\nR 160,187,24576\n"
"M 148,172,188\nR 162,187,28672\nM 132,168,168\nR 164,187,32768\nM 132,172,184\nR 166,187,36864\n"
"G 4\nM 136,176,168\nR 168,184,40960\nM 136,180,184\nR 170,184,45056\nM 152,176,172\n"
"R 172,184,49152\nM 152,180,188\nR 174,184,53248\nG 4\nM 156,160,172\nR 176,185,40960\n"
"M 156,164,188\nR 178,185,45056\nM 140,160,168\nR 180,185,49152\nM 140,164,184\nR 182,185,53248\n"
"G 4\nM 128,168,200\nR 160,186,40960\nM 128,172,216\nR 162,186,45056\nM 144,168,204\n"
"R 164,186,49152\nM 144,172,220\nR 166,186,53248\nG 4\nM 148,176,204\nR 168,187,40960\n"
"M 148,180,220\nR 170,187,45056\nM 132,176,200\nR 172,187,49152\nM 132,180,216\nR 174,187,53248\n"
"G 4\nM 136,160,200\nR 176,184,24576\nM 136,164,216\nR 178,184,28672\nM 152,160,204\n"
"R 180,184,32768\nM 152,164,220\nR 182,184,36864\nG 4\nM 156,168,204\nR 160,185,24576\n"
"M 156,172,220\nR 162,185,28672\nM 140,168,200\nR 164,185,32768\nM 140,172,216\nR 166,185,36864\n"
"G 4\nM 96,176,160\nR 168,186,24576\nM 96,180,176\nR 170,186,28672\ns_mov_b32 m0, s51\n"
"M 112,176,164\nR 172,186,32768\nv_cvt_pk_bf16_f32 v128, v128, v129\nv_cvt_pk_bf16_f32 v129, v130, v131\nM 112,180,180\nR 174,186,36864\n"
"G 4\nv_cvt_pk_bf16_f32 v130, v132, v133\nM 116,160,164\nR 176,187,24576\ns_mul_i32 s66, s43, 49152\nv_cvt_pk_bf16_f32 v131, v134, v135\n"
"M 116,164,180\nR 178,187,28672\ns_add_u32 s66, s66, s72\nv_cvt_pk_bf16_f32 v132, v136, v137\nM 100,160,160\nR 180,187,32768\n"
"s_add_u32 s66, s66, 65044480\nv_cvt_pk_bf16_f32 v133, v138, v139\nM 100,164,176\nR 182,187,36864\nG 4\nv_cvt_pk_bf16_f32 v134, v140, v141\n"
"M 104,168,160\nR 160,184,40960\ns_waitcnt vmcnt(1)\nv_cmp_eq_u64 s[86:87], s[46:47], v[246:247]\nv_cvt_pk_bf16_f32 v135, v142, v143\nM 104,172,176\n"
"R 162,184,45056\nv_cvt_pk_bf16_f32 v136, v144, v145\nv_cvt_pk_bf16_f32 v137, v146, v147\nM 120,168,164\nR 164,184,49152\ns_and_b32 s62, s86, 4\n"
"s_cmp_eq_u32 s62, 4\ns_cbranch_scc0 Lq983\nLq984:\nv_cvt_pk_bf16_f32 v138, v148, v149\nM 120,172,180\nR 166,184,53248\n"
"G 4\nv_cvt_pk_bf16_f32 v139, v150, v151\nM 124,176,164\nR 168,185,40960\nbuffer_load_dwordx4 v[34:37], v252, s[20:23], s66 offen offset:0 sc1\nv_cvt_pk_bf16_f32 v140, v152, v153\n"
"M 124,180,180\nR 170,185,45056\nv_cvt_pk_bf16_f32 v141, v154, v155\nv_cvt_pk_bf16_f32 v142, v156, v157\nM 108,176,160\nR 172,185,49152\n"
"buffer_load_dwordx4 v[42:45], v252, s[20:23], s66 offen offset:1024 sc1\nds_write_addtid_b32 v128 offset:0\nds_write_addtid_b32 v129 offset:256\nds_write_addtid_b32 v130 offset:512\nds_write_addtid_b32 v131 offset:768\nds_write_addtid_b32 v132 offset:1024\n"
"ds_write_addtid_b32 v133 offset:1280\nds_write_addtid_b32 v134 offset:1536\nds_write_addtid_b32 v135 offset:1792\nds_write_addtid_b32 v136 offset:2048\nds_write_addtid_b32 v137 offset:2304\nds_write_addtid_b32 v138 offset:2560\n"
"ds_write_addtid_b32 v139 offset:2816\nds_write_addtid_b32 v140 offset:3072\nds_write_addtid_b32 v141 offset:3328\nds_write_addtid_b32 v142 offset:3584\nv_cvt_pk_bf16_f32 v143, v158, v159\nds_write_addtid_b32 v143 offset:3840\n"
"M 108,180,176\nR 174,185,53248\nG 15\nM 96,160,192\nR 176,186,40960\nM 96,164,208\n"
"R 178,186,45056\nbuffer_load_dwordx4 v[50:53], v252, s[20:23], s66 offen offset:2048 sc1\nG 3\ns_barrier\nM 112,160,196\nR 180,186,49152\n"
"ds_read2st64_b32 v[0:1], v225 offset0:0 offset1:1\nM 112,164,212\nR 182,186,53248\nds_read2st64_b32 v[6:7], v225 offset0:2 offset1:3\nG 6\nM 116,168,196\n"
"R 160,187,40960\nbuffer_load_dwordx4 v[58:61], v252, s[20:23], s66 offen offset:3072 sc1\nds_read2st64_b32 v[8:9], v225 offset0:4 offset1:5\nM 116,172,212\nR 162,187,45056\nds_read2st64_b32 v[14:15], v225 offset0:6 offset1:7\n"
"M 100,168,192\nR 164,187,49152\nds_read2st64_b32 v[16:17], v225 offset0:8 offset1:9\nM 100,172,208\nR 166,187,53248\nv_mfma_f32_16x16x32_bf16 v[96:99], v[242:245], v[64:67], v[96:99]\n"
"ds_read2st64_b32 v[22:23], v225 offset0:10 offset1:11\nds_read2st64_b32 v[24:25], v225 offset0:12 offset1:13\nG 10\nM 104,176,192\nds_read2st64_b32 v[30:31], v225 offset0:14 offset1:15\nv_mfma_f32_16x16x32_bf16 v[100:103], v[242:245], v[68:71], v[100:103]\n"
"M 104,180,208\nv_mfma_f32_16x16x32_bf16 v[104:107], v[242:245], v[72:75], v[104:107]\nM 120,176,196\nv_mfma_f32_16x16x32_bf16 v[108:111], v[242:245], v[76:79], v[108:111]\nM 120,180,212\nG 3\n"
"v_mfma_f32_16x16x32_bf16 v[112:115], v[242:245], v[80:83], v[112:115]\nM 124,160,196\nv_mfma_f32_16x16x32_bf16 v[116:119], v[242:245], v[84:87], v[116:119]\nM 124,164,212\ns_waitcnt vmcnt(4)\nv_mfma_f32_16x16x32_bf16 v[120:123], v[242:245], v[88:91], v[120:123]\n"
"M 108,160,192\nv_mfma_f32_16x16x32_bf16 v[124:127], v[242:245], v[92:95], v[124:127]\nM 108,164,208\nv_mfma_f32_16x16x32_bf16 v[96:99], v[242:245], v[0:3], v[96:99]\nv_mfma_f32_16x16x32_bf16 v[100:103], v[242:245], v[4:7], v[100:103]\nv_mfma_f32_16x16x32_bf16 v[104:107], v[242:245], v[8:11], v[104:107]\n"
"v_mfma_f32_16x16x32_bf16 v[108:111], v[242:245], v[12:15], v[108:111]\nv_mfma_f32_16x16x32_bf16 v[112:115], v[242:245], v[16:19], v[112:115]\nG 2\nv_mfma_f32_16x16x32_bf16 v[116:119], v[242:245], v[20:23], v[116:119]\nG 1\nv_mfma_f32_16x16x32_bf16 v[120:123], v[242:245], v[24:27], v[120:123]\n"
"G 0\nv_mfma_f32_16x16x32_bf16 v[124:127], v[242:245], v[28:31], v[124:127]\ns_waitcnt vmcnt(3)\nv_mfma_f32_16x16x32_bf16 v[96:99], v[242:245], v[32:35], v[96:99]\nv_mfma_f32_16x16x32_bf16 v[100:103], v[242:245], v[36:39], v[100:103]\ns_waitcnt vmcnt(2)\n"
"v_mfma_f32_16x16x32_bf16 v[104:107], v[242:245], v[40:43], v[104:107]\nv_mfma_f32_16x16x32_bf16 v[108:111], v[242:245], v[44:47], v[108:111]\ns_waitcnt vmcnt(1)\nv_mfma_f32_16x16x32_bf16 v[112:115], v[242:245], v[48:51], v[112:115]\nv_mfma_f32_16x16x32_bf16 v[116:119], v[242:245], v[52:55], v[116:119]\ns_waitcnt vmcnt(0)\n"
"v_mfma_f32_16x16x32_bf16 v[120:123], v[242:245], v[56:59], v[120:123]\nv_mfma_f32_16x16x32_bf16 v[124:127], v[242:245], v[60:63], v[124:127]\nv_cvt_pk_bf16_f32 v192, v96, v97\nv_cvt_pk_bf16_f32 v193, v98, v99\nv_cvt_pk_bf16_f32 v194, v100, v101\nv_cvt_pk_bf16_f32 v195, v102, v103\n"
"v_cvt_pk_bf16_f32 v196, v104, v105\nv_cvt_pk_bf16_f32 v197, v106, v107\nv_cvt_pk_bf16_f32 v198, v108, v109\nv_cvt_pk_bf16_f32 v199, v110, v111\nv_cvt_pk_bf16_f32 v200, v112, v113\nv_cvt_pk_bf16_f32 v201, v114, v115\n"
"v_cvt_pk_bf16_f32 v202, v116, v117\nv_cvt_pk_bf16_f32 v203, v118, v119\nv_cvt_pk_bf16_f32 v204, v120, v121\nv_cvt_pk_bf16_f32 v205, v122, v123\nv_cvt_pk_bf16_f32 v206, v124, v125\nv_cvt_pk_bf16_f32 v207, v126, v127\n"
"v_and_b32 v230, 0xf0, v252\nv_lshlrev_b32 v230, 7, v230\nv_bfe_u32 v231, v252, 8, 1\nv_lshl_add_u32 v230, v231, 5, v230\nv_lshrrev_b32 v231, 9, v252\nv_lshl_add_u32 v230, v231, 4, v230\n"
"s_add_u32 s61, s49, s53\ns_add_u32 s62, s61, 32768\nv_permlane16_swap_b32 v192, v194\nv_permlane16_swap_b32 v193, v195\nbuffer_store_dwordx4 v[192:195], v230, s[28:31], s61 offen offset:0\nv_permlane16_swap_b32 v196, v198\n"
"v_permlane16_swap_b32 v197, v199\nbuffer_store_dwordx4 v[196:199], v230, s[28:31], s61 offen offset:64\nv_permlane16_swap_b32 v200, v202\nv_permlane16_swap_b32 v201, v203\nbuffer_store_dwordx4 v[200:203], v230, s[28:31], s62 offen offset:0\nv_permlane16_swap_b32 v204, v206\n"
"v_permlane16_swap_b32 v205, v207\nbuffer_store_dwordx4 v[204:207], v230, s[28:31], s62 offen offset:64\ns_add_u32 s60, s83, 1\nv_mov_b32 v234, s60\ns_or_b32 s60, s45, s40\ns_cmp_eq_u32 s60, 0\n"
"s_cselect_b64 exec, 1, 0\nbuffer_store_dword v234, off, s[20:23], 0 sc1\ns_mov_b64 exec, -1\ns_endpgm\nLqF:\nLq645:\n"
"s_lshr_b32 s84, s49, 4\ns_add_u32 s84, s84, 82837504\nbuffer_load_dwordx4 v[128:131], v252, s[20:23], s84 offen offset:0 sc1\nbuffer_load_dwordx4 v[132:135], v252, s[20:23], s84 offen offset:1024 sc1\nbuffer_load_dwordx4 v[136:139], v252, s[20:23], s84 offen offset:2048 sc1\nbuffer_load_dwordx4 v[140:143], v252, s[20:23], s84 offen offset:3072 sc1\n"
"s_mov_b32 s85, 0x0c0c0400\ns_waitcnt vmcnt(3)\nv_perm_b32 v144, v129, v128, s85\nv_perm_b32 v148, v131, v130, s85\nv_lshl_or_b32 v144, v148, 16, v144\ns_waitcnt vmcnt(2)\n"
"v_perm_b32 v145, v133, v132, s85\nv_perm_b32 v149, v135, v134, s85\nv_lshl_or_b32 v145, v149, 16, v145\ns_waitcnt vmcnt(1)\nv_perm_b32 v146, v137, v136, s85\nv_perm_b32 v150, v139, v138, s85\n"
"v_lshl_or_b32 v146, v150, 16, v146\ns_waitcnt vmcnt(0)\nv_perm_b32 v147, v141, v140, s85\nv_perm_b32 v151, v143, v142, s85\nv_lshl_or_b32 v147, v151, 16, v147\nv_min3_u32 v230, v144, v145, v146\n"
"v_min3_u32 v230, v230, v147, v147\nv_max3_u32 v231, v144, v145, v146\nv_max3_u32 v231, v231, v147, v147\nv_cmp_ne_u32 s[86:87], s46, v230\nv_cmp_ne_u32 vcc, s46, v231\ns_nop 0\n"
"s_or_b64 s[86:87], s[86:87], vcc\ns_cbranch_scc0 Lq646\ns_sub_u32 s48, s48, 1\ns_cmp_gt_i32 s48, 0\ns_cbranch_scc1 Lq645\ns_branch Lq646\n"
"Lq692:\ns_mov_b64 exec, s[66:67]\nbuffer_load_dwordx4 v[230:233], v252, s[20:23], s65 offen offset:0 sc1\nbuffer_load_dwordx2 v[64:65], v227, s[20:23], s65 offen offset:2048 sc1\nbuffer_load_dwordx2 v[70:71], v227, s[20:23], s65 offen offset:2560 sc1\ns_mov_b64 exec, -1\n"
"s_waitcnt vmcnt(0)\nv_max3_u32 v248, v233, v65, v71\nv_cmp_eq_u32 vcc, -1, v248\ns_nop 1\ns_cbranch_vccz Lq693\ns_sub_u32 s48, s48, 1\n"
"s_cmp_gt_i32 s48, 0\ns_cbranch_scc1 Lq692\ns_branch Lq693\nLq694:\ns_mov_b64 exec, s[66:67]\nbuffer_load_dwordx4 v[234:237], v252, s[20:23], s65 offen offset:1024 sc1\n"
"buffer_load_dwordx2 v[76:77], v227, s[20:23], s65 offen offset:3072 sc1\nbuffer_load_dwordx2 v[82:83], v227, s[20:23], s65 offen offset:3584 sc1\ns_mov_b64 exec, -1\ns_waitcnt vmcnt(0)\nv_max3_u32 v248, v237, v77, v83\nv_cmp_eq_u32 vcc, -1, v248\n"
"s_nop 1\ns_cbranch_vccz Lq695\ns_sub_u32 s48, s48, 1\ns_cmp_gt_i32 s48, 0\ns_cbranch_scc1 Lq694\ns_branch Lq695\n"
"Lq818:\ns_lshr_b32 s84, s49, 4\ns_add_u32 s84, s84, 83361792\nbuffer_load_dwordx4 v[0:3], v252, s[20:23], s84 offen offset:0 sc1\nbuffer_load_dwordx4 v[4:7], v252, s[20:23], s84 offen offset:1024 sc1\nbuffer_load_dwordx4 v[8:11], v252, s[20:23], s84 offen offset:2048 sc1\n"
"buffer_load_dwordx4 v[12:15], v252, s[20:23], s84 offen offset:3072 sc1\ns_mov_b32 s85, 0x0c0c0400\ns_waitcnt vmcnt(3)\nv_perm_b32 v16, v1, v0, s85\nv_perm_b32 v20, v3, v2, s85\nv_lshl_or_b32 v16, v20, 16, v16\n"
"s_waitcnt vmcnt(2)\nv_perm_b32 v17, v5, v4, s85\nv_perm_b32 v21, v7, v6, s85\nv_lshl_or_b32 v17, v21, 16, v17\ns_waitcnt vmcnt(1)\nv_perm_b32 v18, v9, v8, s85\n"
"v_perm_b32 v22, v11, v10, s85\nv_lshl_or_b32 v18, v22, 16, v18\ns_waitcnt vmcnt(0)\nv_perm_b32 v19, v13, v12, s85\nv_perm_b32 v23, v15, v14, s85\nv_lshl_or_b32 v19, v23, 16, v19\n"
"v_min3_u32 v230, v16, v17, v18\nv_min3_u32 v230, v230, v19, v19\nv_max3_u32 v231, v16, v17, v18\nv_max3_u32 v231, v231, v19, v19\nv_cmp_ne_u32 s[86:87], s46, v230\nv_cmp_ne_u32 vcc, s46, v231\n"
"s_nop 0\ns_or_b64 s[86:87], s[86:87], vcc\ns_cbranch_scc0 Lq819\ns_sub_u32 s48, s48, 1\ns_cmp_gt_i32 s48, 0\ns_cbranch_scc1 Lq818\n"
"s_branch Lq819\nLq838:\nv_mov_b32 v248, 0\nv_mov_b32 v249, 0\ns_add_u32 s61, s82, 845824\ns_mov_b64 exec, 0xf\n"
"buffer_atomic_or_x2 v[248:249], v250, s[20:23], s61 offen sc0 sc1\ns_mov_b64 exec, -1\ns_waitcnt vmcnt(0)\nv_cmp_eq_u64 s[86:87], s[46:47], v[248:249]\ns_nop 1\ns_and_b32 s62, s86, 0xf\n"
"s_cmp_eq_u32 s62, 0xf\ns_cbranch_scc1 Lq839\ns_sleep 1\ns_sub_u32 s48, s48, 1\ns_cmp_gt_i32 s48, 0\ns_cbranch_scc1 Lq838\n"
"s_branch Lq839\nLq873:\nv_mov_b32 v248, 0\nv_mov_b32 v249, 0\ns_add_u32 s61, s82, 845832\ns_mov_b64 exec, 0xf\n"
"buffer_atomic_or_x2 v[248:249], v250, s[20:23], s61 offen sc0 sc1\ns_mov_b64 exec, -1\ns_waitcnt vmcnt(0)\nv_cmp_eq_u64 s[86:87], s[46:47], v[248:249]\ns_nop 1\ns_and_b32 s62, s86, 0xf\n"
"s_cmp_eq_u32 s62, 0xf\ns_cbranch_scc1 Lq874\ns_sleep 1\ns_sub_u32 s48, s48, 1\ns_cmp_gt_i32 s48, 0\ns_cbranch_scc1 Lq873\n"
"s_branch Lq874\nLq908:\nv_mov_b32 v248, 0\nv_mov_b32 v249, 0\ns_add_u32 s61, s82, 845848\ns_mov_b64 exec, 0xf\n"
"buffer_atomic_or_x2 v[248:249], v250, s[20:23], s61 offen sc0 sc1\ns_mov_b64 exec, -1\ns_waitcnt vmcnt(0)\nv_cmp_eq_u64 s[86:87], s[46:47], v[248:249]\ns_nop 1\ns_and_b32 s62, s86, 0xf\n"
"s_cmp_eq_u32 s62, 0xf\ns_cbranch_scc1 Lq909\ns_sleep 1\ns_sub_u32 s48, s48, 1\ns_cmp_gt_i32 s48, 0\ns_cbranch_scc1 Lq908\n"
"s_branch Lq909\nLq927:\nv_mov_b32 v248, 0\nv_mov_b32 v249, 0\ns_add_u32 s61, s82, 845840\ns_mov_b64 exec, 0xf\n"
"buffer_atomic_or_x2 v[248:249], v250, s[20:23], s61 offen sc0 sc1\ns_mov_b64 exec, -1\ns_waitcnt vmcnt(0)\nv_cmp_eq_u64 s[86:87], s[46:47], v[248:249]\ns_nop 1\ns_and_b32 s62, s86, 0xf\n"
"s_cmp_eq_u32 s62, 0xf\ns_cbranch_scc1 Lq928\ns_sleep 1\ns_sub_u32 s48, s48, 1\ns_cmp_gt_i32 s48, 0\ns_cbranch_scc1 Lq927\n"
"s_branch Lq928\nLq967:\nv_mov_b32 v248, 0\nv_mov_b32 v249, 0\ns_add_u32 s61, s74, 1009664\ns_mov_b64 exec, 3\n"
"buffer_atomic_or_x2 v[248:249], v250, s[20:23], s61 offen sc0 sc1\ns_mov_b64 exec, -1\ns_waitcnt vmcnt(0)\nv_cmp_eq_u64 s[86:87], s[46:47], v[248:249]\ns_nop 1\ns_and_b32 s62, s86, 3\n"
"s_cmp_eq_u32 s62, 3\ns_cbranch_scc1 Lq968\ns_sleep 1\ns_sub_u32 s48, s48, 1\ns_cmp_gt_i32 s48, 0\ns_cbranch_scc1 Lq967\n"
"s_branch Lq968\nLq983:\nv_mov_b32 v246, 0\nv_mov_b32 v247, 0\ns_add_u32 s61, s74, 1009664\ns_mov_b64 exec, 4\n"
"buffer_atomic_or_x2 v[246:247], v250, s[20:23], s61 offen sc0 sc1\ns_mov_b64 exec, -1\ns_waitcnt vmcnt(0)\nv_cmp_eq_u64 s[86:87], s[46:47], v[246:247]\ns_nop 1\ns_and_b32 s62, s86, 4\n"
"s_cmp_eq_u32 s62, 4\ns_cbranch_scc1 Lq984\ns_sleep 1\ns_sub_u32 s48, s48, 1\ns_cmp_gt_i32 s48, 0\ns_cbranch_scc1 Lq983\n"
"s_branch Lq984\nLqE:\n"
  : "+{v0}"(tid), "+{s[92:93]}"(ka), "+{s45}"(bid) :
  : "a0", "a1", "a2", "a3", "a4", "a5", "a6", "a7", "a8", "a9", "a10", "a11", "a12", "a13", "a14", "a15", "a16", "a17", "a18", "a19", "a20", "a21", "a22", "a23", "a24", "a25", "a26", "a27", "a28", "a29", "a30", "a31", "a32", "a33", "a34", "a35", "a36", "a37", "a38", "a39", "a40", "a41", "a42", "a43", "a44", "a45", "a46", "a47", "a48", "a49", "a50", "a51", "a52", "a53", "a54", "a55", "a56", "a57", "a58", "a59", "a60", "a61", "a62", "a63", "a64", "a65", "a66", "a67", "a68", "a69", "a70", "a71", "a72", "a73", "a74", "a75", "a76", "a77", "a78", "a79", "a80", "a81", "a82", "a83", "a84", "a85", "a86", "a87", "a88", "a89", "a90", "a91", "a92", "a93", "a94", "a95", "a96", "a97", "a98", "a99", "a100", "a101", "a102", "a103", "a104", "a105", "a106", "a107", "a108", "a109", "a110", "a111", "a112", "a113", "a114", "a115", "a116", "a117", "a118", "a119", "a120", "a121", "a122", "a123", "a124", "a125", "a126", "a127", "a128", "a129", "a130", "a131", "a132", "a133", "a134", "a135", "a136", "a137", "a138", "a139", "a140", "a141", "a142", "a143", "a144", "a145", "a146", "a147", "a148", "a149", "a150", "a151", "a152", "a153", "a154", "a155", "a156", "a157", "a158", "a159", "a160", "a161", "a162", "a163", "a164", "a165", "a166", "a167", "a168", "a169", "a170", "a171", "a172", "a173", "a174", "a175", "a176", "a177", "a178", "a179", "a180", "a181", "a182", "a183", "a184", "a185", "a186", "a187", "a188", "a189", "a190", "a191", "a192", "a193", "a194", "a195", "a196", "a197", "a198", "a199", "a200", "a201", "a202", "a203", "a204", "a205", "a206", "a207", "a208", "a209", "a210", "a211", "a212", "a213", "a214", "a215", "a216", "a217", "a218", "a219", "a220", "a221", "a222", "a223", "a224", "a225", "a226", "a227", "a228", "a229", "a230", "a231", "a232", "a233", "a234", "a235", "a236", "a237", "a238", "a239", "a240", "a241", "a242", "a243", "a244", "a245", "a246", "a247", "a248", "a249", "a250", "a251", "a252", "a253", "a254", "a255", "v1", "v2", "v3", "v4", "v5", "v6", "v7", "v8", "v9", "v10", "v11", "v12", "v13", "v14", "v15", "v16", "v17", "v18", "v19", "v20", "v21", "v22", "v23", "v24", "v25", "v26", "v27", "v28", "v29", "v30", "v31", "v32", "v33", "v34", "v35", "v36", "v37", "v38", "v39", "v40", "v41", "v42", "v43", "v44", "v45", "v46", "v47", "v48", "v49", "v50", "v51", "v52", "v53", "v54", "v55", "v56", "v57", "v58", "v59", "v60", "v61", "v62", "v63", "v64", "v65", "v66", "v67", "v68", "v69", "v70", "v71", "v72", "v73", "v74", "v75", "v76", "v77", "v78", "v79", "v80", "v81", "v82", "v83", "v84", "v85", "v86", "v87", "v88", "v89", "v90", "v91", "v92", "v93", "v94", "v95", "v96", "v97", "v98", "v99", "v100", "v101", "v102", "v103", "v104", "v105", "v106", "v107", "v108", "v109", "v110", "v111", "v112", "v113", "v114", "v115", "v116", "v117", "v118", "v119", "v120", "v121", "v122", "v123", "v124", "v125", "v126", "v127", "v128", "v129", "v130", "v131", "v132", "v133", "v134", "v135", "v136", "v137", "v138", "v139", "v140", "v141", "v142", "v143", "v144", "v145", "v146", "v147", "v148", "v149", "v150", "v151", "v152", "v153", "v154", "v155", "v156", "v157", "v158", "v159", "v160", "v161", "v162", "v163", "v164", "v165", "v166", "v167", "v168", "v169", "v170", "v171", "v172", "v173", "v174", "v175", "v176", "v177", "v178", "v179", "v180", "v181", "v182", "v183", "v184", "v185", "v186", "v187", "v188", "v189", "v190", "v191", "v192", "v193", "v194", "v195", "v196", "v197", "v198", "v199", "v200", "v201", "v202", "v203", "v204", "v205", "v206", "v207", "v208", "v209", "v210", "v211", "v212", "v213", "v214", "v215", "v216", "v217", "v218", "v219", "v220", "v221", "v222", "v223", "v224", "v225", "v226", "v227", "v228", "v229", "v230", "v231", "v232", "v233", "v234", "v235", "v236", "v237", "v238", "v239", "v240", "v241", "v242", "v243", "v244", "v245", "v246", "v247", "v248", "v249", "v250", "v251", "v252", "v253", "v254", "v255", "s4", "s5", "s6", "s7", "s8", "s9", "s10", "s11", "s12", "s13", "s14", "s15", "s16", "s17", "s18", "s19", "s20", "s21", "s22", "s23", "s24", "s25", "s26", "s27", "s28", "s29", "s30", "s31", "s32", "s33", "s34", "s35", "s36", "s37", "s38", "s39", "s40", "s41", "s42", "s43", "s44", "s46", "s47", "s48", "s49", "s50", "s51", "s52", "s53", "s54", "s55", "s56", "s57", "s58", "s59", "s60", "s61", "s62", "s63", "s64", "s65", "s66", "s67", "s68", "s69", "s70", "s71", "s72", "s73", "s74", "s75", "s76", "s77", "s78", "s79", "s80", "s81", "s82", "s83", "s84", "s85", "s86", "s87", "s88", "s89", "s90", "s91", "s94", "s95", "s96", "s97", "s98", "s99", "s100", "s101", "vcc", "m0", "scc", "memory");
#endif
}
extern "C" void kernel_launch(void* const* d_in, const int* in_sizes, int n_in, void* d_out, int out_size, void* d_ws, size_t ws_size, hipStream_t stream) {
  (void)hipFuncSetAttribute((const void*)layers, hipFuncAttributeMaxDynamicSharedMemorySize, 160 * 1024);
  hipLaunchKernelGGL(layers, dim3(256), dim3(256), 160 * 1024, stream, d_in[0], d_in[1], d_in[2], d_in[4], d_in[5], d_in[6], d_in[7], d_out, d_ws);
}
```
